```python
import torch

def _softmax(x, axis=-1):
    return torch.softmax(x, dim=axis)
def _gelu(x):
    return torch.nn.functional.gelu(x, approximate="tanh")
def _silu(x):
    return torch.nn.functional.silu(x)
def _sigmoid(x):
    return torch.sigmoid(x)
def _layernorm(x, axis=-1, eps=1e-5):
    m = x.mean(dim=axis, keepdim=True)
    v = x.var(dim=axis, keepdim=True, unbiased=False)
    return (x - m) / torch.sqrt(v + eps)
def _scatter_add(tgt, idx, src, axis=0):
    return tgt.clone().index_add_(axis, idx, src)
def _segment_sum(src, seg, n):
    out = torch.zeros((n,) + src.shape[1:], dtype=src.dtype)
    return out.index_add_(0, seg, src)
def _pad(x, pw):
    flat = tuple(v for pair in reversed(pw) for v in pair)
    return torch.nn.functional.pad(x, flat)

kernel_name = 'interleave_layers__sliding_local_dilated__swiglu_ffn__prompt'

def setup_inputs():

    _w = (lambda a: (lambda t: t.to(torch.bfloat16) if t.is_floating_point() else t)(torch.as_tensor(a)))
    def _A_setup():
        B, S, D = 1, 2048, 1024
        return {
            "x": _w((lambda *s: torch.randn(*s, dtype=torch.bfloat16) * 0.05)(B, S, D)),
            "wqkv": _w((lambda *s: torch.randn(*s, dtype=torch.bfloat16) * 0.05)(D, 3 * D)),
            "wo": _w((lambda *s: torch.randn(*s, dtype=torch.bfloat16) * 0.05)(D, D)),
        }
    _A_in = _A_setup()

    def _B_setup():
        B, S, D, F = 1, 2048, 1024, 3584
        return {
            "x": _w((lambda *s: torch.randn(*s, dtype=torch.bfloat16) * 0.05)(B, S, D)),
            "w_gate": _w((lambda *s: torch.randn(*s, dtype=torch.bfloat16) * 0.05)(D, F)),
            "w_up": _w((lambda *s: torch.randn(*s, dtype=torch.bfloat16) * 0.05)(D, F)),
            "w_down": _w((lambda *s: torch.randn(*s, dtype=torch.bfloat16) * 0.05)(F, D)),
        }
    _B_in = _B_setup()

    return {
        "A_x": _A_in["x"],
        "A_wqkv": _A_in["wqkv"],
        "A_wo": _A_in["wo"],
        "B_x": _B_in["x"],
        "B_w_gate": _B_in["w_gate"],
        "B_w_up": _B_in["w_up"],
        "B_w_down": _B_in["w_down"],
    }

def reference(A_x, A_wqkv, A_wo, B_x, B_w_gate, B_w_up, B_w_down):
    def _A_ref(x, wqkv, wo):
        B, S, D = x.shape
        H = 32; Dh = D // H
        W, dil = 8, 2
        qkv = (lambda x, *d: x.permute(*d))((x @ wqkv).reshape(B, S, 3, H, Dh), 2, 0, 3, 1, 4)
        q, k, v = qkv[0], qkv[1], qkv[2]
        scale = Dh ** -0.5
        pos = torch.arange(S)
        off = torch.arange(W) * dil
        idx = pos[:, None] - off[None, :]
        mask = idx >= 0
        idx_c = torch.where(mask, idx, 0).reshape(-1)
        kg = k[:, :, idx_c, :].reshape(B, H, S, W, Dh)
        vg = v[:, :, idx_c, :].reshape(B, H, S, W, Dh)
        logits = torch.einsum("bhsd,bhswd->bhsw", q, kg) * scale
        logits = torch.where(mask[None, None, :, :], logits, -1e9)
        attn = _softmax(logits, axis=-1)
        o = torch.einsum("bhsw,bhswd->bhsd", attn, vg)
        return (lambda x, *d: x.permute(*d))(o, 0, 2, 1, 3).reshape(B, S, D) @ wo

    def _B_ref(x, w_gate, w_up, w_down):
        g = _silu(x @ w_gate)
        u = x @ w_up
        return (g * u) @ w_down

    x = A_x
    x = x + _A_ref(_layernorm(x, axis=-1), A_wqkv, A_wo)
    x = x + _B_ref(_layernorm(x, axis=-1), B_w_gate, B_w_up, B_w_down)
    x = x + _A_ref(_layernorm(x, axis=-1), A_wqkv, A_wo)
    x = x + _B_ref(_layernorm(x, axis=-1), B_w_gate, B_w_up, B_w_down)
    return x
```

```cpp
#include <hip/hip_runtime.h>
#pragma clang diagnostic ignored "-Winline-asm"
#define W_XI 4612096
#define W_O 4648960
#define W_XA 4681728
#define W_H 4718592
#define W_P 4833280
#define W_EB 296960
#define W_MAP 1
extern "C" __global__ __launch_bounds__(256, 1) void body_k(const void* x, const void* wq, const void* wo, const void* wg, const void* wu,
                                                            const void* wd, void* out, void* ws) {
    unsigned tid = threadIdx.x;
    asm volatile(
"s_load_dwordx2 s[52:53], s[22:23], 0x0\n"
"v_lshrrev_b32 v3, 6, v0\n"
"v_and_b32 v1, 63, v0\n"
"v_readfirstlane_b32 s26, v3\n"
"v_lshlrev_b32 v2, 4, v1\n"
"s_add_u32 s32, s22, 0x0\n"
"s_addc_u32 s33, s23, 0x0\n"
"s_and_b32 s33, s33, 0xffff\n"
"s_mov_b32 s34, 0x504c000\n"
"s_mov_b32 s35, 0x20000\n"
"s_mov_b32 s73, 0\n"
"s_lshr_b32 s49, s24, 3\n"
"s_cmp_lt_u32 s49, 16\n"
"s_cbranch_scc1 .L_nc\n"
"s_bfe_u32 s28, s24, 0x20001\n"
"s_and_b32 s27, s49, 7\n"
"s_bfe_u32 s29, s49, 0x10003\n"
"s_mov_b32 s62, 0\n"
"s_mov_b32 s74, 1\n"
"s_bitcmp1_b32 s24, 0\n"
"s_cbranch_scc0 .L_nt\n"
"s_add_u32 s36, s10, 0x0\n"
"s_addc_u32 s37, s11, 0x0\n"
"s_and_b32 s37, s37, 0xffff\n"
"s_mov_b32 s38, 0x600000\n"
"s_mov_b32 s39, 0x20000\n"
"s_lshl_b32 s48, s49, 8\n"
"s_sub_u32 s48, s48, 4096\n"
"v_add_u32 v4, s48, v0\n"
"s_lshl_b32 s48, s28, 9\n"
"v_mul_u32_u24 v5, 43691, v4\n"
"v_lshrrev_b32 v5, 19, v5\n"
"v_mul_u32_u24 v6, 12, v5\n"
"v_sub_u32 v6, v4, v6\n"
"v_mul_u32_u24 v5, 48, v5\n"
"v_lshrrev_b32 v7, 2, v6\n"
"v_mul_u32_u24 v7, 12, v7\n"
"v_add3_u32 v5, v5, v6, v7\n"
"v_lshlrev_b32 v5, 7, v5\n"
"v_add_u32 v4, 4096, v4\n"
"buffer_load_dword v10, v5, s[36:39], s48 offen\n"
"v_mul_u32_u24 v5, 43691, v4\n"
"v_lshrrev_b32 v5, 19, v5\n"
"v_mul_u32_u24 v6, 12, v5\n"
"v_sub_u32 v6, v4, v6\n"
"v_mul_u32_u24 v5, 48, v5\n"
"v_lshrrev_b32 v7, 2, v6\n"
"v_mul_u32_u24 v7, 12, v7\n"
"v_add3_u32 v5, v5, v6, v7\n"
"v_lshlrev_b32 v5, 7, v5\n"
"v_add_u32 v4, 4096, v4\n"
"buffer_load_dword v10, v5, s[36:39], s48 offen\n"
"v_mul_u32_u24 v5, 43691, v4\n"
"v_lshrrev_b32 v5, 19, v5\n"
"v_mul_u32_u24 v6, 12, v5\n"
"v_sub_u32 v6, v4, v6\n"
"v_mul_u32_u24 v5, 48, v5\n"
"v_lshrrev_b32 v7, 2, v6\n"
"v_mul_u32_u24 v7, 12, v7\n"
"v_add3_u32 v5, v5, v6, v7\n"
"v_lshlrev_b32 v5, 7, v5\n"
"v_add_u32 v4, 4096, v4\n"
"buffer_load_dword v10, v5, s[36:39], s48 offen\n"
"s_mov_b32 s62, 1\n"
"s_mov_b32 s74, 4\n"
".L_nt:\n"
"s_lshl2_add_u32 s50, s27, s26\n"
"s_mul_i32 s70, s50, 1152\n"
"v_and_b32 v20, 15, v1\n"
"v_lshlrev_b32 v20, 11, v20\n"
"v_and_b32 v21, 48, v1\n"
"s_lshl_b32 s48, s50, 6\n"
"v_add3_u32 v20, v20, v21, s48\n"
"s_add_u32 s36, s8, 0x0\n"
"s_addc_u32 s37, s9, 0x0\n"
"s_and_b32 s37, s37, 0xffff\n"
"s_mov_b32 s38, 0x400000\n"
"s_mov_b32 s39, 0x20000\n"
"s_lshl2_add_u32 s48, s28, s62\n"
"s_lshl_b32 s48, s48, 3\n"
"s_sub_u32 s48, s48, s29\n"
"s_add_u32 s48, s48, 127\n"
"s_lshl_b32 s66, s27, 7\n"
"s_add_u32 s66, s66, 286720\n"
"s_sub_u32 s66, s66, s70\n"
"s_add_u32 s49, s28, 3\n"
"s_and_b32 s49, s49, 3\n"
"s_mul_i32 s49, s49, 296960\n"
"s_add_u32 s69, s70, s49\n"
"s_add_u32 s69, s69, 41435136\n"
"s_mul_i32 s49, s28, 296960\n"
"s_add_u32 s70, s70, s49\n"
"s_and_b32 s51, s50, 6\n"
"s_lshl_b32 s51, s51, 1\n"
"v_add_u32 v3, s51, v1\n"
"v_and_b32 v3, 15, v3\n"
"v_lshlrev_b32 v3, 3, v3\n"
"v_cmp_gt_u32 vcc, 16, v1\n"
"v_mov_b32 v21, 0x7fff0000\n"
"s_nop 0\n"
"v_cndmask_b32 v21, v21, v3, vcc\n"
"s_mul_i32 s49, s29, 1187840\n"
"s_sub_u32 s71, s70, s49\n"
"s_add_u32 s71, s71, 4612096\n"
"v_mul_u32_u24 v24, 2375680, v1\n"
"v_add_u32 v24, s71, v24\n"
"s_cmp_eq_u32 s29, 0\n"
"s_cselect_b32 s68, s71, s69\n"
"v_writelane_b32 v24, s68, 0\n"
"s_mul_i32 s49, s62, 9502720\n"
"s_add_u32 s71, s71, s49\n"
"s_cmp_eq_u32 s62, 0\n"
"s_cselect_b32 s68, s68, s71\n"
"s_add_u32 s71, s71, 2375680\n"
"v_lshrrev_b32 v22, 2, v0\n"
"v_mov_b32 v23, 0x3f803f80\n"
".L_dc:\n"
"s_and_b32 s49, s48, 127\n"
"s_lshl_b32 s49, s49, 15\n"
"s_add_u32 s48, s48, 2\n"
"buffer_load_dwordx4 v[32:35], v20, s[36:39], s49 offen\n"
"s_and_b32 s49, s48, 127\n"
"s_lshl_b32 s49, s49, 15\n"
"s_add_u32 s48, s48, 2\n"
"buffer_load_dwordx4 v[36:39], v20, s[36:39], s49 offen\n"
"s_and_b32 s49, s48, 127\n"
"s_lshl_b32 s49, s49, 15\n"
"s_add_u32 s48, s48, 2\n"
"buffer_load_dwordx4 v[40:43], v20, s[36:39], s49 offen\n"
"s_and_b32 s49, s48, 127\n"
"s_lshl_b32 s49, s49, 15\n"
"s_add_u32 s48, s48, 2\n"
"buffer_load_dwordx4 v[44:47], v20, s[36:39], s49 offen\n"
"v_cmp_eq_u32 vcc, s62, v22\n"
"v_mov_b32 v25, 0x7fff0000\n"
"s_add_u32 s62, s62, 1\n"
"v_cndmask_b32 v25, v25, v24, vcc\n"
"s_waitcnt lgkmcnt(0)\n"
"s_add_u32 s54, s52, 0x7f4a7c15\n"
"s_addc_u32 s55, s53, 0x9e3779b9\n"
"v_mov_b32 v8, s54\n"
"v_mov_b32 v9, s55\n"
"s_waitcnt vmcnt(0)\n"
"v_dot2_f32_bf16 v48, v32, v23, 0\n"
"v_dot2_f32_bf16 v49, v32, v32, 0\n"
"v_dot2_f32_bf16 v50, v36, v23, 0\n"
"v_dot2_f32_bf16 v51, v36, v36, 0\n"
"v_dot2_f32_bf16 v52, v40, v23, 0\n"
"v_dot2_f32_bf16 v53, v40, v40, 0\n"
"v_dot2_f32_bf16 v54, v44, v23, 0\n"
"v_dot2_f32_bf16 v55, v44, v44, 0\n"
"v_dot2_f32_bf16 v48, v33, v23, v48\n"
"v_dot2_f32_bf16 v49, v33, v33, v49\n"
"v_dot2_f32_bf16 v50, v37, v23, v50\n"
"v_dot2_f32_bf16 v51, v37, v37, v51\n"
"v_dot2_f32_bf16 v52, v41, v23, v52\n"
"v_dot2_f32_bf16 v53, v41, v41, v53\n"
"v_dot2_f32_bf16 v54, v45, v23, v54\n"
"v_dot2_f32_bf16 v55, v45, v45, v55\n"
"v_dot2_f32_bf16 v48, v34, v23, v48\n"
"v_dot2_f32_bf16 v49, v34, v34, v49\n"
"v_dot2_f32_bf16 v50, v38, v23, v50\n"
"v_dot2_f32_bf16 v51, v38, v38, v51\n"
"v_dot2_f32_bf16 v52, v42, v23, v52\n"
"v_dot2_f32_bf16 v53, v42, v42, v53\n"
"v_dot2_f32_bf16 v54, v46, v23, v54\n"
"v_dot2_f32_bf16 v55, v46, v46, v55\n"
"v_dot2_f32_bf16 v48, v35, v23, v48\n"
"v_dot2_f32_bf16 v49, v35, v35, v49\n"
"v_dot2_f32_bf16 v50, v39, v23, v50\n"
"v_dot2_f32_bf16 v51, v39, v39, v51\n"
"v_dot2_f32_bf16 v52, v43, v23, v52\n"
"v_dot2_f32_bf16 v53, v43, v43, v53\n"
"v_dot2_f32_bf16 v54, v47, v23, v54\n"
"v_dot2_f32_bf16 v55, v47, v47, v55\n"
"v_permlane32_swap_b32 v48, v49\n"
"v_permlane32_swap_b32 v50, v51\n"
"v_permlane32_swap_b32 v52, v53\n"
"v_permlane32_swap_b32 v54, v55\n"
"v_add_f32 v48, v48, v49\n"
"v_add_f32 v50, v50, v51\n"
"v_add_f32 v52, v52, v53\n"
"v_add_f32 v54, v54, v55\n"
"v_permlane16_swap_b32 v48, v49\n"
"v_permlane16_swap_b32 v50, v51\n"
"v_permlane16_swap_b32 v52, v53\n"
"v_permlane16_swap_b32 v54, v55\n"
"v_add_f32 v48, v48, v49\n"
"v_add_f32 v50, v50, v51\n"
"v_add_f32 v52, v52, v53\n"
"v_add_f32 v54, v54, v55\n"
"v_permlane32_swap_b32 v48, v49\n"
"v_permlane32_swap_b32 v50, v51\n"
"v_permlane32_swap_b32 v52, v53\n"
"v_permlane32_swap_b32 v54, v55\n"
"buffer_store_dwordx4 v[32:35], v2, s[32:35], s68 offen sc1\n"
"buffer_store_dwordx2 v[48:49], v21, s[32:35], s68 offen offset:1024 sc1\n"
"s_mov_b32 s68, s71\n"
"s_add_u32 s71, s71, 2375680\n"
"buffer_store_dwordx4 v[36:39], v2, s[32:35], s68 offen sc1\n"
"buffer_store_dwordx2 v[50:51], v21, s[32:35], s68 offen offset:1024 sc1\n"
"s_mov_b32 s68, s71\n"
"s_add_u32 s71, s71, 2375680\n"
"buffer_store_dwordx4 v[40:43], v2, s[32:35], s68 offen sc1\n"
"buffer_store_dwordx2 v[52:53], v21, s[32:35], s68 offen offset:1024 sc1\n"
"s_mov_b32 s68, s71\n"
"s_add_u32 s71, s71, 2375680\n"
"buffer_store_dwordx4 v[44:47], v2, s[32:35], s68 offen sc1\n"
"buffer_store_dwordx2 v[54:55], v21, s[32:35], s68 offen offset:1024 sc1\n"
"s_mov_b32 s68, s71\n"
"s_add_u32 s71, s71, 2375680\n"
"s_waitcnt vmcnt(0)\n"
"s_barrier\n"
"buffer_store_dwordx2 v[8:9], v25, s[32:35], s66 offen sc1\n"
"s_cmp_lt_u32 s62, s74\n"
"s_cbranch_scc1 .L_dc\n"
"s_bitcmp1_b32 s24, 0\n"
"s_cbranch_scc1 .L_d\n"
"s_lshr_b32 s49, s24, 3\n"
".L_nc:\n"
"s_bfe_u32 s50, s24, 0x20001\n"
"s_and_b32 s27, s49, 3\n"
"s_lshl2_add_u32 s27, s50, s27\n"
"s_lshr_b32 s28, s49, 2\n"
"s_bitcmp1_b32 s24, 0\n"
"s_cbranch_scc1 .L_q\n"
"s_and_b32 s27, s49, 1\n"
"s_lshl1_add_u32 s27, s50, s27\n"
"s_lshr_b32 s28, s49, 1\n"
"s_cmp_lt_u32 s49, 4\n"
"s_cbranch_scc1 .L_w\n"
"s_cmp_ge_u32 s49, 16\n"
"s_cbranch_scc1 .L_ns\n"
"s_lshl2_add_u32 s48, s49, s50\n"
"s_lshl_b32 s48, s48, 8\n"
"s_sub_u32 s48, s48, 4096\n"
"v_add_u32 v4, s48, v0\n"
"v_mul_u32_u24 v5, 52429, v4\n"
"v_lshrrev_b32 v5, 22, v5\n"
"v_mul_u32_u24 v5, 2240, v5\n"
"v_add_u32 v5, v5, v4\n"
"v_lshlrev_b32 v5, 7, v5\n"
"v_add_u32 v5, 4898816, v5\n"
"v_add_u32 v4, 12288, v4\n"
"buffer_load_dword v120, v5, s[32:35], 0 offen sc1\n"
"v_mul_u32_u24 v5, 52429, v4\n"
"v_lshrrev_b32 v5, 22, v5\n"
"v_mul_u32_u24 v5, 2240, v5\n"
"v_add_u32 v5, v5, v4\n"
"v_lshlrev_b32 v5, 7, v5\n"
"v_add_u32 v5, 4898816, v5\n"
"v_add_u32 v4, 12288, v4\n"
"buffer_load_dword v120, v5, s[32:35], 0 offen sc1\n"
"s_sleep 30\n"
".L_ns:\n"
"s_lshr_b32 s27, s50, 1\n"
"s_and_b32 s28, s50, 1\n"
"s_cmp_eq_u32 s49, 4\n"
"s_cbranch_scc1 .L_h\n"
"s_sub_u32 s27, s49, 14\n"
"s_lshr_b32 s27, s27, 1\n"
"s_and_b32 s28, s49, 1\n"
"s_add_u32 s28, s28, 1\n"
"s_sub_u32 s48, s49, 5\n"
"s_cmp_lt_u32 s49, 14\n"
"s_cselect_b32 s27, s48, s27\n"
"s_cselect_b32 s28, 0, s28\n"
"s_mul_i32 s50, s50, 9\n"
"s_add_u32 s27, s27, s50\n"
".L_g:\n"
"v_mov_b32 v16, 0x3f803f80\n"
"v_mov_b32 v17, 0x3f803f80\n"
"v_mov_b32 v18, 0x3f803f80\n"
"v_mov_b32 v19, 0x3f803f80\n"
"s_add_u32 s36, s14, 0x0\n"
"s_addc_u32 s37, s15, 0x0\n"
"s_and_b32 s37, s37, 0xffff\n"
"s_mov_b32 s38, 0x700000\n"
"s_mov_b32 s39, 0x20000\n"
"s_add_u32 s40, s16, 0x0\n"
"s_addc_u32 s41, s17, 0x0\n"
"s_and_b32 s41, s41, 0xffff\n"
"s_mov_b32 s42, 0x700000\n"
"s_mov_b32 s43, 0x20000\n"
"v_add_u32 v8, 0, v1\n"
"v_mul_u32_u24 v9, 5462, v8\n"
"v_lshrrev_b32 v9, 16, v9\n"
"v_mul_u32_u24 v10, 12, v9\n"
"v_sub_u32 v10, v8, v10\n"
"v_mul_u32_u24 v9, 7168, v9\n"
"v_lshl_add_u32 v9, v10, 4, v9\n"
"v_subrev_u32 v4, 0, v9\n"
"v_add_u32 v8, 64, v1\n"
"v_mul_u32_u24 v9, 5462, v8\n"
"v_lshrrev_b32 v9, 16, v9\n"
"v_mul_u32_u24 v10, 12, v9\n"
"v_sub_u32 v10, v8, v10\n"
"v_mul_u32_u24 v9, 7168, v9\n"
"v_lshl_add_u32 v9, v10, 4, v9\n"
"v_subrev_u32 v5, 1024, v9\n"
"v_add_u32 v8, 128, v1\n"
"v_mul_u32_u24 v9, 5462, v8\n"
"v_lshrrev_b32 v9, 16, v9\n"
"v_mul_u32_u24 v10, 12, v9\n"
"v_sub_u32 v10, v8, v10\n"
"v_mul_u32_u24 v9, 7168, v9\n"
"v_lshl_add_u32 v9, v10, 4, v9\n"
"v_subrev_u32 v6, 2048, v9\n"
"s_mul_i32 s48, s26, 114688\n"
"s_mul_i32 s49, s27, 192\n"
"s_add_u32 s63, s48, s49\n"
"s_mul_i32 s64, s26, 3072\n"
"s_add_u32 m0, s64, 0\n"
"s_nop 0\n"
"buffer_load_dwordx4 v4, s[36:39], s63 offen offset:0 lds\n"
"buffer_load_dwordx4 v5, s[36:39], s63 offen offset:1024 lds\n"
"buffer_load_dwordx4 v6, s[36:39], s63 offen offset:2048 lds\n"
"s_add_u32 m0, s64, 12288\n"
"s_nop 0\n"
"buffer_load_dwordx4 v4, s[40:43], s63 offen offset:0 lds\n"
"buffer_load_dwordx4 v5, s[40:43], s63 offen offset:1024 lds\n"
"buffer_load_dwordx4 v6, s[40:43], s63 offen offset:2048 lds\n"
"s_add_u32 s63, s63, 458752\n"
"s_add_u32 m0, s64, 24576\n"
"s_nop 0\n"
"buffer_load_dwordx4 v4, s[36:39], s63 offen offset:0 lds\n"
"buffer_load_dwordx4 v5, s[36:39], s63 offen offset:1024 lds\n"
"buffer_load_dwordx4 v6, s[36:39], s63 offen offset:2048 lds\n"
"s_add_u32 m0, s64, 36864\n"
"s_nop 0\n"
"buffer_load_dwordx4 v4, s[40:43], s63 offen offset:0 lds\n"
"buffer_load_dwordx4 v5, s[40:43], s63 offen offset:1024 lds\n"
"buffer_load_dwordx4 v6, s[40:43], s63 offen offset:2048 lds\n"
"s_add_u32 s63, s63, 458752\n"
"s_add_u32 m0, s64, 49152\n"
"s_nop 0\n"
"buffer_load_dwordx4 v4, s[36:39], s63 offen offset:0 lds\n"
"buffer_load_dwordx4 v5, s[36:39], s63 offen offset:1024 lds\n"
"buffer_load_dwordx4 v6, s[36:39], s63 offen offset:2048 lds\n"
"s_add_u32 m0, s64, 61440\n"
"s_nop 0\n"
"buffer_load_dwordx4 v4, s[40:43], s63 offen offset:0 lds\n"
"buffer_load_dwordx4 v5, s[40:43], s63 offen offset:1024 lds\n"
"buffer_load_dwordx4 v6, s[40:43], s63 offen offset:2048 lds\n"
"s_add_u32 s63, s63, 458752\n"
"s_add_u32 m0, s64, 73728\n"
"s_nop 0\n"
"buffer_load_dwordx4 v4, s[36:39], s63 offen offset:0 lds\n"
"buffer_load_dwordx4 v5, s[36:39], s63 offen offset:1024 lds\n"
"buffer_load_dwordx4 v6, s[36:39], s63 offen offset:2048 lds\n"
"s_add_u32 m0, s64, 86016\n"
"s_nop 0\n"
"buffer_load_dwordx4 v4, s[40:43], s63 offen offset:0 lds\n"
"buffer_load_dwordx4 v5, s[40:43], s63 offen offset:1024 lds\n"
"buffer_load_dwordx4 v6, s[40:43], s63 offen offset:2048 lds\n"
"s_add_u32 s63, s63, 458752\n"
"s_add_u32 m0, s64, 98304\n"
"s_nop 0\n"
"buffer_load_dwordx4 v4, s[36:39], s63 offen offset:0 lds\n"
"buffer_load_dwordx4 v5, s[36:39], s63 offen offset:1024 lds\n"
"buffer_load_dwordx4 v6, s[36:39], s63 offen offset:2048 lds\n"
"s_add_u32 m0, s64, 110592\n"
"s_nop 0\n"
"buffer_load_dwordx4 v4, s[40:43], s63 offen offset:0 lds\n"
"buffer_load_dwordx4 v5, s[40:43], s63 offen offset:1024 lds\n"
"buffer_load_dwordx4 v6, s[40:43], s63 offen offset:2048 lds\n"
"s_add_u32 s63, s63, 458752\n"
"v_and_b32 v12, 15, v1\n"
"v_lshrrev_b32 v13, 4, v1\n"
"v_lshrrev_b32 v14, 2, v12\n"
"v_and_b32 v15, 3, v12\n"
"v_lshl_add_u32 v13, v13, 3, v14\n"
"v_mul_u32_u24 v13, 192, v13\n"
"s_mul_i32 s48, s26, 48\n"
"v_add_u32 v13, s48, v13\n"
"v_and_b32 v14, 1, v15\n"
"v_lshrrev_b32 v15, 1, v15\n"
"v_lshl_add_u32 v13, v14, 3, v13\n"
"v_lshl_add_u32 v8, v15, 5, v13\n"
"v_mul_u32_u24 v15, 12288, v15\n"
"v_add3_u32 v9, v13, v15, 16\n"
"v_add_u32 v10, 49152, v8\n"
"v_add_u32 v11, 49152, v9\n"
"v_add_u32 v12, 98304, v8\n"
"v_add_u32 v13, 98304, v9\n"
"s_waitcnt vmcnt(24)\n"
"s_barrier\n"
"s_add_u32 m0, s64, 122880\n"
"s_nop 0\n"
"buffer_load_dwordx4 v4, s[36:39], s63 offen offset:0 lds\n"
"buffer_load_dwordx4 v5, s[36:39], s63 offen offset:1024 lds\n"
"buffer_load_dwordx4 v6, s[36:39], s63 offen offset:2048 lds\n"
"s_add_u32 m0, s64, 135168\n"
"s_nop 0\n"
"buffer_load_dwordx4 v4, s[40:43], s63 offen offset:0 lds\n"
"buffer_load_dwordx4 v5, s[40:43], s63 offen offset:1024 lds\n"
"buffer_load_dwordx4 v6, s[40:43], s63 offen offset:2048 lds\n"
"s_add_u32 s63, s63, 458752\n"
"ds_read_b64_tr_b16 a[0:1], v8 offset:0\n"
"ds_read_b64_tr_b16 a[2:3], v8 offset:768\n"
"ds_read_b64_tr_b16 a[4:5], v8 offset:12288\n"
"ds_read_b64_tr_b16 a[6:7], v8 offset:13056\n"
"ds_read_b64_tr_b16 a[8:9], v9 offset:0\n"
"ds_read_b64_tr_b16 a[10:11], v9 offset:768\n"
"ds_read_b64_tr_b16 a[12:13], v8 offset:6144\n"
"ds_read_b64_tr_b16 a[14:15], v8 offset:6912\n"
"ds_read_b64_tr_b16 a[16:17], v8 offset:18432\n"
"ds_read_b64_tr_b16 a[18:19], v8 offset:19200\n"
"ds_read_b64_tr_b16 a[20:21], v9 offset:6144\n"
"ds_read_b64_tr_b16 a[22:23], v9 offset:6912\n"
"s_waitcnt lgkmcnt(0)\n"
"v_mfma_f32_16x16x32_bf16 v[20:23], a[0:3], v[16:19], 0\n"
"v_mfma_f32_16x16x32_bf16 v[24:27], a[4:7], v[16:19], 0\n"
"v_mfma_f32_16x16x32_bf16 v[28:31], a[8:11], v[16:19], 0\n"
"v_mfma_f32_16x16x32_bf16 v[20:23], a[12:15], v[16:19], v[20:23]\n"
"v_mfma_f32_16x16x32_bf16 v[24:27], a[16:19], v[16:19], v[24:27]\n"
"v_mfma_f32_16x16x32_bf16 v[28:31], a[20:23], v[16:19], v[28:31]\n"
"s_waitcnt vmcnt(24)\n"
"s_barrier\n"
"s_add_u32 m0, s64, 0\n"
"s_nop 0\n"
"buffer_load_dwordx4 v4, s[36:39], s63 offen offset:0 lds\n"
"buffer_load_dwordx4 v5, s[36:39], s63 offen offset:1024 lds\n"
"buffer_load_dwordx4 v6, s[36:39], s63 offen offset:2048 lds\n"
"s_add_u32 m0, s64, 12288\n"
"s_nop 0\n"
"buffer_load_dwordx4 v4, s[40:43], s63 offen offset:0 lds\n"
"buffer_load_dwordx4 v5, s[40:43], s63 offen offset:1024 lds\n"
"buffer_load_dwordx4 v6, s[40:43], s63 offen offset:2048 lds\n"
"s_add_u32 s63, s63, 458752\n"
"ds_read_b64_tr_b16 a[24:25], v8 offset:24576\n"
"ds_read_b64_tr_b16 a[26:27], v8 offset:25344\n"
"ds_read_b64_tr_b16 a[28:29], v8 offset:36864\n"
"ds_read_b64_tr_b16 a[30:31], v8 offset:37632\n"
"ds_read_b64_tr_b16 a[32:33], v9 offset:24576\n"
"ds_read_b64_tr_b16 a[34:35], v9 offset:25344\n"
"ds_read_b64_tr_b16 a[36:37], v8 offset:30720\n"
"ds_read_b64_tr_b16 a[38:39], v8 offset:31488\n"
"ds_read_b64_tr_b16 a[40:41], v8 offset:43008\n"
"ds_read_b64_tr_b16 a[42:43], v8 offset:43776\n"
"ds_read_b64_tr_b16 a[44:45], v9 offset:30720\n"
"ds_read_b64_tr_b16 a[46:47], v9 offset:31488\n"
"s_waitcnt lgkmcnt(0)\n"
"v_mfma_f32_16x16x32_bf16 v[20:23], a[24:27], v[16:19], v[20:23]\n"
"v_mfma_f32_16x16x32_bf16 v[24:27], a[28:31], v[16:19], v[24:27]\n"
"v_mfma_f32_16x16x32_bf16 v[28:31], a[32:35], v[16:19], v[28:31]\n"
"v_mfma_f32_16x16x32_bf16 v[20:23], a[36:39], v[16:19], v[20:23]\n"
"v_mfma_f32_16x16x32_bf16 v[24:27], a[40:43], v[16:19], v[24:27]\n"
"v_mfma_f32_16x16x32_bf16 v[28:31], a[44:47], v[16:19], v[28:31]\n"
"s_waitcnt vmcnt(24)\n"
"s_barrier\n"
"s_add_u32 m0, s64, 24576\n"
"s_nop 0\n"
"buffer_load_dwordx4 v4, s[36:39], s63 offen offset:0 lds\n"
"buffer_load_dwordx4 v5, s[36:39], s63 offen offset:1024 lds\n"
"buffer_load_dwordx4 v6, s[36:39], s63 offen offset:2048 lds\n"
"s_add_u32 m0, s64, 36864\n"
"s_nop 0\n"
"buffer_load_dwordx4 v4, s[40:43], s63 offen offset:0 lds\n"
"buffer_load_dwordx4 v5, s[40:43], s63 offen offset:1024 lds\n"
"buffer_load_dwordx4 v6, s[40:43], s63 offen offset:2048 lds\n"
"s_add_u32 s63, s63, 458752\n"
"ds_read_b64_tr_b16 a[48:49], v10 offset:0\n"
"ds_read_b64_tr_b16 a[50:51], v10 offset:768\n"
"ds_read_b64_tr_b16 a[52:53], v10 offset:12288\n"
"ds_read_b64_tr_b16 a[54:55], v10 offset:13056\n"
"ds_read_b64_tr_b16 a[56:57], v11 offset:0\n"
"ds_read_b64_tr_b16 a[58:59], v11 offset:768\n"
"ds_read_b64_tr_b16 a[60:61], v10 offset:6144\n"
"ds_read_b64_tr_b16 a[62:63], v10 offset:6912\n"
"ds_read_b64_tr_b16 a[64:65], v10 offset:18432\n"
"ds_read_b64_tr_b16 a[66:67], v10 offset:19200\n"
"ds_read_b64_tr_b16 a[68:69], v11 offset:6144\n"
"ds_read_b64_tr_b16 a[70:71], v11 offset:6912\n"
"s_waitcnt lgkmcnt(0)\n"
"v_mfma_f32_16x16x32_bf16 v[20:23], a[48:51], v[16:19], v[20:23]\n"
"v_mfma_f32_16x16x32_bf16 v[24:27], a[52:55], v[16:19], v[24:27]\n"
"v_mfma_f32_16x16x32_bf16 v[28:31], a[56:59], v[16:19], v[28:31]\n"
"v_mfma_f32_16x16x32_bf16 v[20:23], a[60:63], v[16:19], v[20:23]\n"
"v_mfma_f32_16x16x32_bf16 v[24:27], a[64:67], v[16:19], v[24:27]\n"
"v_mfma_f32_16x16x32_bf16 v[28:31], a[68:71], v[16:19], v[28:31]\n"
"s_waitcnt vmcnt(24)\n"
"s_barrier\n"
"s_add_u32 m0, s64, 49152\n"
"s_nop 0\n"
"buffer_load_dwordx4 v4, s[36:39], s63 offen offset:0 lds\n"
"buffer_load_dwordx4 v5, s[36:39], s63 offen offset:1024 lds\n"
"buffer_load_dwordx4 v6, s[36:39], s63 offen offset:2048 lds\n"
"s_add_u32 m0, s64, 61440\n"
"s_nop 0\n"
"buffer_load_dwordx4 v4, s[40:43], s63 offen offset:0 lds\n"
"buffer_load_dwordx4 v5, s[40:43], s63 offen offset:1024 lds\n"
"buffer_load_dwordx4 v6, s[40:43], s63 offen offset:2048 lds\n"
"s_add_u32 s63, s63, 458752\n"
"ds_read_b64_tr_b16 a[72:73], v10 offset:24576\n"
"ds_read_b64_tr_b16 a[74:75], v10 offset:25344\n"
"ds_read_b64_tr_b16 a[76:77], v10 offset:36864\n"
"ds_read_b64_tr_b16 a[78:79], v10 offset:37632\n"
"ds_read_b64_tr_b16 a[80:81], v11 offset:24576\n"
"ds_read_b64_tr_b16 a[82:83], v11 offset:25344\n"
"ds_read_b64_tr_b16 a[84:85], v10 offset:30720\n"
"ds_read_b64_tr_b16 a[86:87], v10 offset:31488\n"
"ds_read_b64_tr_b16 a[88:89], v10 offset:43008\n"
"ds_read_b64_tr_b16 a[90:91], v10 offset:43776\n"
"ds_read_b64_tr_b16 a[92:93], v11 offset:30720\n"
"ds_read_b64_tr_b16 a[94:95], v11 offset:31488\n"
"s_waitcnt lgkmcnt(0)\n"
"v_mfma_f32_16x16x32_bf16 v[20:23], a[72:75], v[16:19], v[20:23]\n"
"v_mfma_f32_16x16x32_bf16 v[24:27], a[76:79], v[16:19], v[24:27]\n"
"v_mfma_f32_16x16x32_bf16 v[28:31], a[80:83], v[16:19], v[28:31]\n"
"v_mfma_f32_16x16x32_bf16 v[20:23], a[84:87], v[16:19], v[20:23]\n"
"v_mfma_f32_16x16x32_bf16 v[24:27], a[88:91], v[16:19], v[24:27]\n"
"v_mfma_f32_16x16x32_bf16 v[28:31], a[92:95], v[16:19], v[28:31]\n"
"s_waitcnt vmcnt(24)\n"
"s_barrier\n"
"s_add_u32 m0, s64, 73728\n"
"s_nop 0\n"
"buffer_load_dwordx4 v4, s[36:39], s63 offen offset:0 lds\n"
"buffer_load_dwordx4 v5, s[36:39], s63 offen offset:1024 lds\n"
"buffer_load_dwordx4 v6, s[36:39], s63 offen offset:2048 lds\n"
"s_add_u32 m0, s64, 86016\n"
"s_nop 0\n"
"buffer_load_dwordx4 v4, s[40:43], s63 offen offset:0 lds\n"
"buffer_load_dwordx4 v5, s[40:43], s63 offen offset:1024 lds\n"
"buffer_load_dwordx4 v6, s[40:43], s63 offen offset:2048 lds\n"
"s_add_u32 s63, s63, 458752\n"
"ds_read_b64_tr_b16 a[96:97], v12 offset:0\n"
"ds_read_b64_tr_b16 a[98:99], v12 offset:768\n"
"ds_read_b64_tr_b16 a[100:101], v12 offset:12288\n"
"ds_read_b64_tr_b16 a[102:103], v12 offset:13056\n"
"ds_read_b64_tr_b16 a[104:105], v13 offset:0\n"
"ds_read_b64_tr_b16 a[106:107], v13 offset:768\n"
"ds_read_b64_tr_b16 a[108:109], v12 offset:6144\n"
"ds_read_b64_tr_b16 a[110:111], v12 offset:6912\n"
"ds_read_b64_tr_b16 a[112:113], v12 offset:18432\n"
"ds_read_b64_tr_b16 a[114:115], v12 offset:19200\n"
"ds_read_b64_tr_b16 a[116:117], v13 offset:6144\n"
"ds_read_b64_tr_b16 a[118:119], v13 offset:6912\n"
"s_waitcnt lgkmcnt(0)\n"
"v_mfma_f32_16x16x32_bf16 v[20:23], a[96:99], v[16:19], v[20:23]\n"
"v_mfma_f32_16x16x32_bf16 v[24:27], a[100:103], v[16:19], v[24:27]\n"
"v_mfma_f32_16x16x32_bf16 v[28:31], a[104:107], v[16:19], v[28:31]\n"
"v_mfma_f32_16x16x32_bf16 v[20:23], a[108:111], v[16:19], v[20:23]\n"
"v_mfma_f32_16x16x32_bf16 v[24:27], a[112:115], v[16:19], v[24:27]\n"
"v_mfma_f32_16x16x32_bf16 v[28:31], a[116:119], v[16:19], v[28:31]\n"
"s_waitcnt vmcnt(24)\n"
"s_barrier\n"
"s_add_u32 m0, s64, 98304\n"
"s_nop 0\n"
"buffer_load_dwordx4 v4, s[36:39], s63 offen offset:0 lds\n"
"buffer_load_dwordx4 v5, s[36:39], s63 offen offset:1024 lds\n"
"buffer_load_dwordx4 v6, s[36:39], s63 offen offset:2048 lds\n"
"s_add_u32 m0, s64, 110592\n"
"s_nop 0\n"
"buffer_load_dwordx4 v4, s[40:43], s63 offen offset:0 lds\n"
"buffer_load_dwordx4 v5, s[40:43], s63 offen offset:1024 lds\n"
"buffer_load_dwordx4 v6, s[40:43], s63 offen offset:2048 lds\n"
"s_add_u32 s63, s63, 458752\n"
"ds_read_b64_tr_b16 a[120:121], v12 offset:24576\n"
"ds_read_b64_tr_b16 a[122:123], v12 offset:25344\n"
"ds_read_b64_tr_b16 a[124:125], v12 offset:36864\n"
"ds_read_b64_tr_b16 a[126:127], v12 offset:37632\n"
"ds_read_b64_tr_b16 a[128:129], v13 offset:24576\n"
"ds_read_b64_tr_b16 a[130:131], v13 offset:25344\n"
"ds_read_b64_tr_b16 a[132:133], v12 offset:30720\n"
"ds_read_b64_tr_b16 a[134:135], v12 offset:31488\n"
"ds_read_b64_tr_b16 a[136:137], v12 offset:43008\n"
"ds_read_b64_tr_b16 a[138:139], v12 offset:43776\n"
"ds_read_b64_tr_b16 a[140:141], v13 offset:30720\n"
"ds_read_b64_tr_b16 a[142:143], v13 offset:31488\n"
"s_waitcnt lgkmcnt(0)\n"
"v_mfma_f32_16x16x32_bf16 v[20:23], a[120:123], v[16:19], v[20:23]\n"
"v_mfma_f32_16x16x32_bf16 v[24:27], a[124:127], v[16:19], v[24:27]\n"
"v_mfma_f32_16x16x32_bf16 v[28:31], a[128:131], v[16:19], v[28:31]\n"
"v_mfma_f32_16x16x32_bf16 v[20:23], a[132:135], v[16:19], v[20:23]\n"
"v_mfma_f32_16x16x32_bf16 v[24:27], a[136:139], v[16:19], v[24:27]\n"
"v_mfma_f32_16x16x32_bf16 v[28:31], a[140:143], v[16:19], v[28:31]\n"
"s_waitcnt vmcnt(24)\n"
"s_barrier\n"
"s_add_u32 m0, s64, 122880\n"
"s_nop 0\n"
"buffer_load_dwordx4 v4, s[36:39], s63 offen offset:0 lds\n"
"buffer_load_dwordx4 v5, s[36:39], s63 offen offset:1024 lds\n"
"buffer_load_dwordx4 v6, s[36:39], s63 offen offset:2048 lds\n"
"s_add_u32 m0, s64, 135168\n"
"s_nop 0\n"
"buffer_load_dwordx4 v4, s[40:43], s63 offen offset:0 lds\n"
"buffer_load_dwordx4 v5, s[40:43], s63 offen offset:1024 lds\n"
"buffer_load_dwordx4 v6, s[40:43], s63 offen offset:2048 lds\n"
"s_add_u32 s63, s63, 458752\n"
"ds_read_b64_tr_b16 a[144:145], v8 offset:0\n"
"ds_read_b64_tr_b16 a[146:147], v8 offset:768\n"
"ds_read_b64_tr_b16 a[148:149], v8 offset:12288\n"
"ds_read_b64_tr_b16 a[150:151], v8 offset:13056\n"
"ds_read_b64_tr_b16 a[152:153], v9 offset:0\n"
"ds_read_b64_tr_b16 a[154:155], v9 offset:768\n"
"ds_read_b64_tr_b16 a[156:157], v8 offset:6144\n"
"ds_read_b64_tr_b16 a[158:159], v8 offset:6912\n"
"ds_read_b64_tr_b16 a[160:161], v8 offset:18432\n"
"ds_read_b64_tr_b16 a[162:163], v8 offset:19200\n"
"ds_read_b64_tr_b16 a[164:165], v9 offset:6144\n"
"ds_read_b64_tr_b16 a[166:167], v9 offset:6912\n"
"s_waitcnt lgkmcnt(0)\n"
"v_mfma_f32_16x16x32_bf16 v[20:23], a[144:147], v[16:19], v[20:23]\n"
"v_mfma_f32_16x16x32_bf16 v[24:27], a[148:151], v[16:19], v[24:27]\n"
"v_mfma_f32_16x16x32_bf16 v[28:31], a[152:155], v[16:19], v[28:31]\n"
"v_mfma_f32_16x16x32_bf16 v[20:23], a[156:159], v[16:19], v[20:23]\n"
"v_mfma_f32_16x16x32_bf16 v[24:27], a[160:163], v[16:19], v[24:27]\n"
"v_mfma_f32_16x16x32_bf16 v[28:31], a[164:167], v[16:19], v[28:31]\n"
"s_waitcnt vmcnt(24)\n"
"s_barrier\n"
"s_add_u32 m0, s64, 0\n"
"s_nop 0\n"
"buffer_load_dwordx4 v4, s[36:39], s63 offen offset:0 lds\n"
"buffer_load_dwordx4 v5, s[36:39], s63 offen offset:1024 lds\n"
"buffer_load_dwordx4 v6, s[36:39], s63 offen offset:2048 lds\n"
"s_add_u32 m0, s64, 12288\n"
"s_nop 0\n"
"buffer_load_dwordx4 v4, s[40:43], s63 offen offset:0 lds\n"
"buffer_load_dwordx4 v5, s[40:43], s63 offen offset:1024 lds\n"
"buffer_load_dwordx4 v6, s[40:43], s63 offen offset:2048 lds\n"
"s_add_u32 s63, s63, 458752\n"
"ds_read_b64_tr_b16 a[168:169], v8 offset:24576\n"
"ds_read_b64_tr_b16 a[170:171], v8 offset:25344\n"
"ds_read_b64_tr_b16 a[172:173], v8 offset:36864\n"
"ds_read_b64_tr_b16 a[174:175], v8 offset:37632\n"
"ds_read_b64_tr_b16 a[176:177], v9 offset:24576\n"
"ds_read_b64_tr_b16 a[178:179], v9 offset:25344\n"
"ds_read_b64_tr_b16 a[180:181], v8 offset:30720\n"
"ds_read_b64_tr_b16 a[182:183], v8 offset:31488\n"
"ds_read_b64_tr_b16 a[184:185], v8 offset:43008\n"
"ds_read_b64_tr_b16 a[186:187], v8 offset:43776\n"
"ds_read_b64_tr_b16 a[188:189], v9 offset:30720\n"
"ds_read_b64_tr_b16 a[190:191], v9 offset:31488\n"
"s_waitcnt lgkmcnt(0)\n"
"v_mfma_f32_16x16x32_bf16 v[20:23], a[168:171], v[16:19], v[20:23]\n"
"v_mfma_f32_16x16x32_bf16 v[24:27], a[172:175], v[16:19], v[24:27]\n"
"v_mfma_f32_16x16x32_bf16 v[28:31], a[176:179], v[16:19], v[28:31]\n"
"v_mfma_f32_16x16x32_bf16 v[20:23], a[180:183], v[16:19], v[20:23]\n"
"v_mfma_f32_16x16x32_bf16 v[24:27], a[184:187], v[16:19], v[24:27]\n"
"v_mfma_f32_16x16x32_bf16 v[28:31], a[188:191], v[16:19], v[28:31]\n"
"s_waitcnt vmcnt(24)\n"
"s_barrier\n"
"s_add_u32 m0, s64, 24576\n"
"s_nop 0\n"
"buffer_load_dwordx4 v4, s[36:39], s63 offen offset:0 lds\n"
"buffer_load_dwordx4 v5, s[36:39], s63 offen offset:1024 lds\n"
"buffer_load_dwordx4 v6, s[36:39], s63 offen offset:2048 lds\n"
"s_add_u32 m0, s64, 36864\n"
"s_nop 0\n"
"buffer_load_dwordx4 v4, s[40:43], s63 offen offset:0 lds\n"
"buffer_load_dwordx4 v5, s[40:43], s63 offen offset:1024 lds\n"
"buffer_load_dwordx4 v6, s[40:43], s63 offen offset:2048 lds\n"
"s_add_u32 s63, s63, 458752\n"
"ds_read_b64_tr_b16 a[192:193], v10 offset:0\n"
"ds_read_b64_tr_b16 a[194:195], v10 offset:768\n"
"ds_read_b64_tr_b16 a[196:197], v10 offset:12288\n"
"ds_read_b64_tr_b16 a[198:199], v10 offset:13056\n"
"ds_read_b64_tr_b16 a[200:201], v11 offset:0\n"
"ds_read_b64_tr_b16 a[202:203], v11 offset:768\n"
"ds_read_b64_tr_b16 a[204:205], v10 offset:6144\n"
"ds_read_b64_tr_b16 a[206:207], v10 offset:6912\n"
"ds_read_b64_tr_b16 a[208:209], v10 offset:18432\n"
"ds_read_b64_tr_b16 a[210:211], v10 offset:19200\n"
"ds_read_b64_tr_b16 a[212:213], v11 offset:6144\n"
"ds_read_b64_tr_b16 a[214:215], v11 offset:6912\n"
"s_waitcnt lgkmcnt(0)\n"
"v_mfma_f32_16x16x32_bf16 v[20:23], a[192:195], v[16:19], v[20:23]\n"
"v_mfma_f32_16x16x32_bf16 v[24:27], a[196:199], v[16:19], v[24:27]\n"
"v_mfma_f32_16x16x32_bf16 v[28:31], a[200:203], v[16:19], v[28:31]\n"
"v_mfma_f32_16x16x32_bf16 v[20:23], a[204:207], v[16:19], v[20:23]\n"
"v_mfma_f32_16x16x32_bf16 v[24:27], a[208:211], v[16:19], v[24:27]\n"
"v_mfma_f32_16x16x32_bf16 v[28:31], a[212:215], v[16:19], v[28:31]\n"
"s_waitcnt vmcnt(24)\n"
"s_barrier\n"
"s_add_u32 m0, s64, 49152\n"
"s_nop 0\n"
"buffer_load_dwordx4 v4, s[36:39], s63 offen offset:0 lds\n"
"buffer_load_dwordx4 v5, s[36:39], s63 offen offset:1024 lds\n"
"buffer_load_dwordx4 v6, s[36:39], s63 offen offset:2048 lds\n"
"s_add_u32 m0, s64, 61440\n"
"s_nop 0\n"
"buffer_load_dwordx4 v4, s[40:43], s63 offen offset:0 lds\n"
"buffer_load_dwordx4 v5, s[40:43], s63 offen offset:1024 lds\n"
"buffer_load_dwordx4 v6, s[40:43], s63 offen offset:2048 lds\n"
"s_add_u32 s63, s63, 458752\n"
"ds_read_b64_tr_b16 a[216:217], v10 offset:24576\n"
"ds_read_b64_tr_b16 a[218:219], v10 offset:25344\n"
"ds_read_b64_tr_b16 a[220:221], v10 offset:36864\n"
"ds_read_b64_tr_b16 a[222:223], v10 offset:37632\n"
"ds_read_b64_tr_b16 a[224:225], v11 offset:24576\n"
"ds_read_b64_tr_b16 a[226:227], v11 offset:25344\n"
"ds_read_b64_tr_b16 a[228:229], v10 offset:30720\n"
"ds_read_b64_tr_b16 a[230:231], v10 offset:31488\n"
"ds_read_b64_tr_b16 a[232:233], v10 offset:43008\n"
"ds_read_b64_tr_b16 a[234:235], v10 offset:43776\n"
"ds_read_b64_tr_b16 a[236:237], v11 offset:30720\n"
"ds_read_b64_tr_b16 a[238:239], v11 offset:31488\n"
"s_waitcnt lgkmcnt(0)\n"
"v_mfma_f32_16x16x32_bf16 v[20:23], a[216:219], v[16:19], v[20:23]\n"
"v_mfma_f32_16x16x32_bf16 v[24:27], a[220:223], v[16:19], v[24:27]\n"
"v_mfma_f32_16x16x32_bf16 v[28:31], a[224:227], v[16:19], v[28:31]\n"
"v_mfma_f32_16x16x32_bf16 v[20:23], a[228:231], v[16:19], v[20:23]\n"
"v_mfma_f32_16x16x32_bf16 v[24:27], a[232:235], v[16:19], v[24:27]\n"
"v_mfma_f32_16x16x32_bf16 v[28:31], a[236:239], v[16:19], v[28:31]\n"
"s_waitcnt vmcnt(24)\n"
"s_barrier\n"
"s_add_u32 m0, s64, 73728\n"
"s_nop 0\n"
"buffer_load_dwordx4 v4, s[36:39], s63 offen offset:0 lds\n"
"buffer_load_dwordx4 v5, s[36:39], s63 offen offset:1024 lds\n"
"buffer_load_dwordx4 v6, s[36:39], s63 offen offset:2048 lds\n"
"s_add_u32 m0, s64, 86016\n"
"s_nop 0\n"
"buffer_load_dwordx4 v4, s[40:43], s63 offen offset:0 lds\n"
"buffer_load_dwordx4 v5, s[40:43], s63 offen offset:1024 lds\n"
"buffer_load_dwordx4 v6, s[40:43], s63 offen offset:2048 lds\n"
"s_add_u32 s63, s63, 458752\n"
"ds_read_b64_tr_b16 a[240:241], v12 offset:0\n"
"ds_read_b64_tr_b16 a[242:243], v12 offset:768\n"
"ds_read_b64_tr_b16 a[244:245], v12 offset:12288\n"
"ds_read_b64_tr_b16 a[246:247], v12 offset:13056\n"
"ds_read_b64_tr_b16 a[248:249], v13 offset:0\n"
"ds_read_b64_tr_b16 a[250:251], v13 offset:768\n"
"ds_read_b64_tr_b16 a[252:253], v12 offset:6144\n"
"ds_read_b64_tr_b16 a[254:255], v12 offset:6912\n"
"ds_read_b64_tr_b16 v[128:129], v12 offset:18432\n"
"ds_read_b64_tr_b16 v[130:131], v12 offset:19200\n"
"ds_read_b64_tr_b16 v[132:133], v13 offset:6144\n"
"ds_read_b64_tr_b16 v[134:135], v13 offset:6912\n"
"s_waitcnt lgkmcnt(0)\n"
"v_mfma_f32_16x16x32_bf16 v[20:23], a[240:243], v[16:19], v[20:23]\n"
"v_mfma_f32_16x16x32_bf16 v[24:27], a[244:247], v[16:19], v[24:27]\n"
"v_mfma_f32_16x16x32_bf16 v[28:31], a[248:251], v[16:19], v[28:31]\n"
"v_mfma_f32_16x16x32_bf16 v[20:23], a[252:255], v[16:19], v[20:23]\n"
"v_mfma_f32_16x16x32_bf16 v[24:27], v[128:131], v[16:19], v[24:27]\n"
"v_mfma_f32_16x16x32_bf16 v[28:31], v[132:135], v[16:19], v[28:31]\n"
"s_waitcnt vmcnt(24)\n"
"s_barrier\n"
"ds_read_b64_tr_b16 v[136:137], v12 offset:24576\n"
"ds_read_b64_tr_b16 v[138:139], v12 offset:25344\n"
"ds_read_b64_tr_b16 v[140:141], v12 offset:36864\n"
"ds_read_b64_tr_b16 v[142:143], v12 offset:37632\n"
"ds_read_b64_tr_b16 v[144:145], v13 offset:24576\n"
"ds_read_b64_tr_b16 v[146:147], v13 offset:25344\n"
"ds_read_b64_tr_b16 v[148:149], v12 offset:30720\n"
"ds_read_b64_tr_b16 v[150:151], v12 offset:31488\n"
"ds_read_b64_tr_b16 v[152:153], v12 offset:43008\n"
"ds_read_b64_tr_b16 v[154:155], v12 offset:43776\n"
"ds_read_b64_tr_b16 v[156:157], v13 offset:30720\n"
"ds_read_b64_tr_b16 v[158:159], v13 offset:31488\n"
"s_waitcnt lgkmcnt(0)\n"
"v_mfma_f32_16x16x32_bf16 v[20:23], v[136:139], v[16:19], v[20:23]\n"
"v_mfma_f32_16x16x32_bf16 v[24:27], v[140:143], v[16:19], v[24:27]\n"
"v_mfma_f32_16x16x32_bf16 v[28:31], v[144:147], v[16:19], v[28:31]\n"
"v_mfma_f32_16x16x32_bf16 v[20:23], v[148:151], v[16:19], v[20:23]\n"
"v_mfma_f32_16x16x32_bf16 v[24:27], v[152:155], v[16:19], v[24:27]\n"
"v_mfma_f32_16x16x32_bf16 v[28:31], v[156:159], v[16:19], v[28:31]\n"
"s_waitcnt vmcnt(18)\n"
"s_barrier\n"
"ds_read_b64_tr_b16 v[160:161], v8 offset:0\n"
"ds_read_b64_tr_b16 v[162:163], v8 offset:768\n"
"ds_read_b64_tr_b16 v[164:165], v8 offset:12288\n"
"ds_read_b64_tr_b16 v[166:167], v8 offset:13056\n"
"ds_read_b64_tr_b16 v[168:169], v9 offset:0\n"
"ds_read_b64_tr_b16 v[170:171], v9 offset:768\n"
"ds_read_b64_tr_b16 v[172:173], v8 offset:6144\n"
"ds_read_b64_tr_b16 v[174:175], v8 offset:6912\n"
"ds_read_b64_tr_b16 v[176:177], v8 offset:18432\n"
"ds_read_b64_tr_b16 v[178:179], v8 offset:19200\n"
"ds_read_b64_tr_b16 v[180:181], v9 offset:6144\n"
"ds_read_b64_tr_b16 v[182:183], v9 offset:6912\n"
"s_waitcnt lgkmcnt(0)\n"
"v_mfma_f32_16x16x32_bf16 v[20:23], v[160:163], v[16:19], v[20:23]\n"
"v_mfma_f32_16x16x32_bf16 v[24:27], v[164:167], v[16:19], v[24:27]\n"
"v_mfma_f32_16x16x32_bf16 v[28:31], v[168:171], v[16:19], v[28:31]\n"
"v_mfma_f32_16x16x32_bf16 v[20:23], v[172:175], v[16:19], v[20:23]\n"
"v_mfma_f32_16x16x32_bf16 v[24:27], v[176:179], v[16:19], v[24:27]\n"
"v_mfma_f32_16x16x32_bf16 v[28:31], v[180:183], v[16:19], v[28:31]\n"
"s_waitcnt vmcnt(12)\n"
"s_barrier\n"
"ds_read_b64_tr_b16 v[184:185], v8 offset:24576\n"
"ds_read_b64_tr_b16 v[186:187], v8 offset:25344\n"
"ds_read_b64_tr_b16 v[188:189], v8 offset:36864\n"
"ds_read_b64_tr_b16 v[190:191], v8 offset:37632\n"
"ds_read_b64_tr_b16 v[192:193], v9 offset:24576\n"
"ds_read_b64_tr_b16 v[194:195], v9 offset:25344\n"
"ds_read_b64_tr_b16 v[196:197], v8 offset:30720\n"
"ds_read_b64_tr_b16 v[198:199], v8 offset:31488\n"
"ds_read_b64_tr_b16 v[200:201], v8 offset:43008\n"
"ds_read_b64_tr_b16 v[202:203], v8 offset:43776\n"
"ds_read_b64_tr_b16 v[204:205], v9 offset:30720\n"
"ds_read_b64_tr_b16 v[206:207], v9 offset:31488\n"
"s_waitcnt lgkmcnt(0)\n"
"v_mfma_f32_16x16x32_bf16 v[20:23], v[184:187], v[16:19], v[20:23]\n"
"v_mfma_f32_16x16x32_bf16 v[24:27], v[188:191], v[16:19], v[24:27]\n"
"v_mfma_f32_16x16x32_bf16 v[28:31], v[192:195], v[16:19], v[28:31]\n"
"v_mfma_f32_16x16x32_bf16 v[20:23], v[196:199], v[16:19], v[20:23]\n"
"v_mfma_f32_16x16x32_bf16 v[24:27], v[200:203], v[16:19], v[24:27]\n"
"v_mfma_f32_16x16x32_bf16 v[28:31], v[204:207], v[16:19], v[28:31]\n"
"s_waitcnt vmcnt(6)\n"
"s_barrier\n"
"ds_read_b64_tr_b16 v[208:209], v10 offset:0\n"
"ds_read_b64_tr_b16 v[210:211], v10 offset:768\n"
"ds_read_b64_tr_b16 v[212:213], v10 offset:12288\n"
"ds_read_b64_tr_b16 v[214:215], v10 offset:13056\n"
"ds_read_b64_tr_b16 v[216:217], v11 offset:0\n"
"ds_read_b64_tr_b16 v[218:219], v11 offset:768\n"
"ds_read_b64_tr_b16 v[220:221], v10 offset:6144\n"
"ds_read_b64_tr_b16 v[222:223], v10 offset:6912\n"
"ds_read_b64_tr_b16 v[224:225], v10 offset:18432\n"
"ds_read_b64_tr_b16 v[226:227], v10 offset:19200\n"
"ds_read_b64_tr_b16 v[228:229], v11 offset:6144\n"
"ds_read_b64_tr_b16 v[230:231], v11 offset:6912\n"
"s_waitcnt lgkmcnt(0)\n"
"v_mfma_f32_16x16x32_bf16 v[20:23], v[208:211], v[16:19], v[20:23]\n"
"v_mfma_f32_16x16x32_bf16 v[24:27], v[212:215], v[16:19], v[24:27]\n"
"v_mfma_f32_16x16x32_bf16 v[28:31], v[216:219], v[16:19], v[28:31]\n"
"v_mfma_f32_16x16x32_bf16 v[20:23], v[220:223], v[16:19], v[20:23]\n"
"v_mfma_f32_16x16x32_bf16 v[24:27], v[224:227], v[16:19], v[24:27]\n"
"v_mfma_f32_16x16x32_bf16 v[28:31], v[228:231], v[16:19], v[28:31]\n"
"s_waitcnt vmcnt(0)\n"
"s_barrier\n"
"ds_read_b64_tr_b16 v[232:233], v10 offset:24576\n"
"ds_read_b64_tr_b16 v[234:235], v10 offset:25344\n"
"ds_read_b64_tr_b16 v[236:237], v10 offset:36864\n"
"ds_read_b64_tr_b16 v[238:239], v10 offset:37632\n"
"ds_read_b64_tr_b16 v[240:241], v11 offset:24576\n"
"ds_read_b64_tr_b16 v[242:243], v11 offset:25344\n"
"ds_read_b64_tr_b16 v[244:245], v10 offset:30720\n"
"ds_read_b64_tr_b16 v[246:247], v10 offset:31488\n"
"ds_read_b64_tr_b16 v[248:249], v10 offset:43008\n"
"ds_read_b64_tr_b16 v[250:251], v10 offset:43776\n"
"ds_read_b64_tr_b16 v[252:253], v11 offset:30720\n"
"ds_read_b64_tr_b16 v[254:255], v11 offset:31488\n"
"s_waitcnt lgkmcnt(0)\n"
"v_mfma_f32_16x16x32_bf16 v[20:23], v[232:235], v[16:19], v[20:23]\n"
"v_mfma_f32_16x16x32_bf16 v[24:27], v[236:239], v[16:19], v[24:27]\n"
"v_mfma_f32_16x16x32_bf16 v[28:31], v[240:243], v[16:19], v[28:31]\n"
"v_mfma_f32_16x16x32_bf16 v[20:23], v[244:247], v[16:19], v[20:23]\n"
"v_mfma_f32_16x16x32_bf16 v[24:27], v[248:251], v[16:19], v[24:27]\n"
"v_mfma_f32_16x16x32_bf16 v[28:31], v[252:255], v[16:19], v[28:31]\n"
"s_barrier\n"
"s_lshl2_add_u32 s49, s27, 128\n"
"s_mul_i32 s68, s27, 3072\n"
"s_mul_i32 s48, s26, 768\n"
"s_cmp_eq_u32 s28, 0\n"
"s_addc_u32 s62, 84, 0\n"
"v_cmp_gt_u32 vcc, 48, v1\n"
"v_mov_b32 v15, 0x7fff0000\n"
"s_nop 0\n"
"v_cndmask_b32 v15, v15, v2, vcc\n"
"s_add_u32 s68, s68, s48\n"
"s_add_u32 s68, s68, 4718592\n"
"s_waitcnt lgkmcnt(0)\n"
"s_add_u32 s54, s52, 0x7f4a7c15\n"
"s_addc_u32 s55, s53, 0x9e3779b9\n"
"s_lshl_b32 s67, s49, 5\n"
"s_add_u32 s67, s67, 4898816\n"
"s_sub_u32 s51, s67, s68\n"
"s_add_u32 s92, s32, s51\n"
"s_addc_u32 s93, s33, 0x10000\n"
"s_sub_u32 s92, s92, 890880\n"
"s_subb_u32 s93, s93, 0\n"
"s_cmp_eq_u32 s26, 0\n"
"s_cselect_b32 s94, 1, 0\n"
"s_mov_b32 s95, 0x20000\n"
"v_mov_b32 v16, s54\n"
"v_mov_b32 v17, s55\n"
"v_cmp_gt_u32 vcc, 8, v1\n"
"v_mul_u32_u24 v3, 296960, v1\n"
"v_mov_b32 v12, 0x7fff0000\n"
"v_cndmask_b32 v3, v12, v3, vcc\n"
"s_add_u32 s51, s67, 76021760\n"
"buffer_store_dwordx2 v[16:17], v3, s[32:35], s51 offen sc1\n"
"s_sub_u32 s67, s68, 296960\n"
"s_mul_i32 s48, s28, 296960\n"
"s_add_u32 s68, s68, s48\n"
"s_sub_u32 s68, s68, 1781760\n"
"s_add_u32 s63, s48, 4681728\n"
"s_add_u32 s66, s48, 4898816\n"
"s_mul_i32 s64, s26, 9216\n"
"s_add_u32 s65, s64, 73728\n"
"s_xor_b32 s65, s65, s64\n"
"s_add_u32 s63, s63, s64\n"
"v_mov_b32 v4, v2\n"
"v_add_u32 v5, 36864, v2\n"
"v_add_u32 v6, 4096, v2\n"
"v_add_u32 v7, 8192, v2\n"
"v_bfe_u32 v3, v1, 1, 3\n"
"v_mul_u32_u24 v8, 1152, v3\n"
"v_lshrrev_b32 v3, 1, v3\n"
"v_add_u32 v3, s26, v3\n"
"v_lshrrev_b32 v9, 4, v1\n"
"v_lshl_add_u32 v3, v3, 2, v9\n"
"v_and_b32 v3, 15, v3\n"
"v_lshl_add_u32 v8, v3, 3, v8\n"
"v_and_b32 v3, 1, v1\n"
"v_lshl_add_u32 v8, v3, 2, v8\n"
"v_add_u32 v8, 1024, v8\n"
"s_lshl_b32 s48, s26, 2\n"
"v_add_u32 v10, s48, v9\n"
"v_and_b32 v11, 15, v1\n"
"v_add_u32 v3, 64, v1\n"
"v_cmp_eq_u32 vcc, 0, v11\n"
"v_add_u32 v9, 73728, v8\n"
"v_xor_b32 v9, v9, v8\n"
"v_cndmask_b32 v10, v3, v10, vcc\n"
"v_lshlrev_b32 v10, 2, v10\n"
"v_add_u32 v10, 147456, v10\n"
"v_lshlrev_b32 v11, 2, v11\n"
"v_add_u32 v11, 147456, v11\n"
"s_lshl_b32 s48, s26, 3\n"
"s_add_u32 s48, s48, 96\n"
"v_add_u32 v14, s48, v1\n"
"v_lshrrev_b32 v14, 2, v14\n"
"v_lshlrev_b32 v14, 7, v14\n"
"v_cmp_gt_u32 vcc, 8, v1\n"
"v_mov_b32 v3, 0x7fff0000\n"
"s_nop 0\n"
"v_cndmask_b32 v14, v3, v14, vcc\n"
"v_cndmask_b32 v16, 0, v16, vcc\n"
"v_cndmask_b32 v17, 0, v17, vcc\n"
"s_mov_b32 s48, 20000\n"
"s_branch .L_rfga\n"
".L_rlga:\n"
"s_sleep 8\n"
".L_rfga:\n"
"buffer_load_dwordx2 v[12:13], v14, s[32:35], s66 offen sc1\n"
"s_sub_u32 s48, s48, 1\n"
"s_cbranch_scc1 .L_end\n"
"s_waitcnt vmcnt(0)\n"
"v_cmp_ne_u64 vcc, v[16:17], v[12:13]\n"
"s_cbranch_vccnz .L_rlga\n"
"s_add_u32 s66, s66, 890880\n"
"s_add_u32 m0, s64, 0\n"
"s_nop 0\n"
"buffer_load_dwordx4 v2, s[32:35], s63 offen offset:0 sc1 lds\n"
"buffer_load_dwordx4 v2, s[32:35], s63 offen offset:1024 sc1 lds\n"
"buffer_load_dwordx4 v2, s[32:35], s63 offen offset:2048 sc1 lds\n"
"buffer_load_dwordx4 v2, s[32:35], s63 offen offset:3072 sc1 lds\n"
"s_add_u32 m0, s64, 4096\n"
"s_nop 0\n"
"buffer_load_dwordx4 v6, s[32:35], s63 offen offset:0 sc1 lds\n"
"buffer_load_dwordx4 v6, s[32:35], s63 offen offset:1024 sc1 lds\n"
"buffer_load_dwordx4 v6, s[32:35], s63 offen offset:2048 sc1 lds\n"
"buffer_load_dwordx4 v6, s[32:35], s63 offen offset:3072 sc1 lds\n"
"s_add_u32 m0, s64, 8192\n"
"s_nop 0\n"
"buffer_load_dwordx4 v7, s[32:35], s63 offen offset:0 sc1 lds\n"
"s_add_u32 s63, s63, 890880\n"
"s_mov_b32 s48, 20000\n"
"s_branch .L_rfgb\n"
".L_rlgb:\n"
"s_sleep 8\n"
".L_rfgb:\n"
"buffer_load_dwordx2 v[12:13], v14, s[32:35], s66 offen sc1\n"
"s_sub_u32 s48, s48, 1\n"
"s_cbranch_scc1 .L_end\n"
"s_waitcnt vmcnt(0)\n"
"v_cmp_ne_u64 vcc, v[16:17], v[12:13]\n"
"s_cbranch_vccnz .L_rlgb\n"
"s_add_u32 s66, s66, 890880\n"
"s_add_u32 m0, s64, 36864\n"
"s_nop 0\n"
"buffer_load_dwordx4 v2, s[32:35], s63 offen offset:0 sc1 lds\n"
"buffer_load_dwordx4 v2, s[32:35], s63 offen offset:1024 sc1 lds\n"
"buffer_load_dwordx4 v2, s[32:35], s63 offen offset:2048 sc1 lds\n"
"buffer_load_dwordx4 v2, s[32:35], s63 offen offset:3072 sc1 lds\n"
"s_add_u32 m0, s64, 40960\n"
"s_nop 0\n"
"buffer_load_dwordx4 v6, s[32:35], s63 offen offset:0 sc1 lds\n"
"buffer_load_dwordx4 v6, s[32:35], s63 offen offset:1024 sc1 lds\n"
"buffer_load_dwordx4 v6, s[32:35], s63 offen offset:2048 sc1 lds\n"
"buffer_load_dwordx4 v6, s[32:35], s63 offen offset:3072 sc1 lds\n"
"s_add_u32 m0, s64, 45056\n"
"s_nop 0\n"
"buffer_load_dwordx4 v7, s[32:35], s63 offen offset:0 sc1 lds\n"
"s_add_u32 s63, s63, 890880\n"
"s_xor_b32 s64, s64, s65\n"
"buffer_load_dwordx2 v[12:13], v14, s[32:35], s66 offen sc1\n"
"s_waitcnt vmcnt(0)\n"
"s_barrier\n"
"ds_read_b128 v[76:79], v4 offset:0\n"
"ds_read_b128 v[80:83], v4 offset:1152\n"
"ds_read_b128 v[84:87], v4 offset:2304\n"
"ds_read_b128 v[88:91], v4 offset:3456\n"
"ds_read_b128 v[92:95], v4 offset:4608\n"
"ds_read_b128 v[96:99], v4 offset:5760\n"
".p2align 3\n"
".L_gl:\n"
"s_waitcnt lgkmcnt(2)\n"
"v_mfma_f32_16x16x32_bf16 v[32:35], a[0:3], v[76:79], 0\n"
"ds_read_b128 v[100:103], v4 offset:6912\n"
"ds_read_b32 v62, v11 offset:128\n"
"v_mfma_f32_16x16x32_bf16 v[36:39], a[4:7], v[76:79], 0\n"
"s_add_u32 s68, s68, 890880\n"
"v_mfma_f32_16x16x32_bf16 v[40:43], a[8:11], v[76:79], 0\n"
"v_mfma_f32_16x16x32_bf16 v[40:43], a[20:23], v[80:83], v[40:43]\n"
"ds_read_b128 v[104:107], v4 offset:8064\n"
"v_mfma_f32_16x16x32_bf16 v[36:39], a[16:19], v[80:83], v[36:39]\n"
"ds_read2st64_b32 v[56:57], v8 offset0:0 offset1:36\n"
"v_mfma_f32_16x16x32_bf16 v[32:35], a[12:15], v[80:83], v[32:35]\n"
"s_waitcnt vmcnt(5)\n"
"v_cmp_ne_u64 vcc, v[16:17], v[12:13]\n"
"v_mfma_f32_16x16x32_bf16 v[32:35], a[24:27], v[84:87], v[32:35]\n"
"ds_read_b128 v[76:79], v4 offset:9216\n"
"ds_read2st64_b32 v[58:59], v8 offset0:72 offset1:108\n"
"v_mfma_f32_16x16x32_bf16 v[36:39], a[28:31], v[84:87], v[36:39]\n"
"s_cbranch_vccnz .L_rtg0\n"
".L_bkg0:\n"
"s_add_u32 s66, s66, 890880\n"
"v_mfma_f32_16x16x32_bf16 v[40:43], a[32:35], v[84:87], v[40:43]\n"
"s_add_u32 m0, s64, 0\n"
"s_waitcnt lgkmcnt(4)\n"
"v_mfma_f32_16x16x32_bf16 v[40:43], a[44:47], v[88:91], v[40:43]\n"
"ds_read_b128 v[80:83], v4 offset:10368\n"
"buffer_load_dwordx4 v2, s[32:35], s63 offen offset:0 sc1 lds\n"
"v_mfma_f32_16x16x32_bf16 v[36:39], a[40:43], v[88:91], v[36:39]\n"
"v_lshlrev_b32 v60, 16, v62\n"
"v_and_b32 v62, 0xffff0000, v62\n"
"v_mfma_f32_16x16x32_bf16 v[32:35], a[36:39], v[88:91], v[32:35]\n"
"v_fma_f32 v52, -v60, v28, v52\n"
"s_waitcnt lgkmcnt(4)\n"
"v_mfma_f32_16x16x32_bf16 v[32:35], a[48:51], v[92:95], v[32:35]\n"
"ds_read_b128 v[84:87], v4 offset:11520\n"
"v_fma_f32 v53, -v60, v29, v53\n"
"v_mfma_f32_16x16x32_bf16 v[36:39], a[52:55], v[92:95], v[36:39]\n"
"v_rcp_f32 v61, v62\n"
"v_mfma_f32_16x16x32_bf16 v[40:43], a[56:59], v[92:95], v[40:43]\n"
"v_mul_f32 v62, v62, v62\n"
"v_mul_f32 v61, 0xbfb8aa3b, v61\n"
"v_mfma_f32_16x16x32_bf16 v[40:43], a[68:71], v[96:99], v[40:43]\n"
"ds_read_b128 v[88:91], v4 offset:12672\n"
"v_fma_f32 v54, -v60, v30, v54\n"
"v_mfma_f32_16x16x32_bf16 v[36:39], a[64:67], v[96:99], v[36:39]\n"
"buffer_load_dwordx4 v2, s[32:35], s63 offen offset:1024 sc1 lds\n"
"v_fma_f32 v55, -v60, v31, v55\n"
"v_mfma_f32_16x16x32_bf16 v[32:35], a[60:63], v[96:99], v[32:35]\n"
"v_permlane32_swap_b32 v52, v54\n"
"v_mfma_f32_16x16x32_bf16 v[32:35], a[72:75], v[100:103], v[32:35]\n"
"ds_read_b128 v[92:95], v4 offset:13824\n"
"v_fma_f32 v48, -v60, v24, v48\n"
"v_mfma_f32_16x16x32_bf16 v[36:39], a[76:79], v[100:103], v[36:39]\n"
"v_permlane32_swap_b32 v53, v55\n"
"v_mfma_f32_16x16x32_bf16 v[40:43], a[80:83], v[100:103], v[40:43]\n"
"v_fma_f32 v49, -v60, v25, v49\n"
"v_fma_f32 v50, -v60, v26, v50\n"
"v_mfma_f32_16x16x32_bf16 v[40:43], a[92:95], v[104:107], v[40:43]\n"
"ds_read_b128 v[96:99], v4 offset:14976\n"
"v_fma_f32 v51, -v60, v27, v51\n"
"v_mfma_f32_16x16x32_bf16 v[36:39], a[88:91], v[104:107], v[36:39]\n"
"v_fma_f32 v44, -v60, v20, v44\n"
"v_fma_f32 v45, -v60, v21, v45\n"
"v_mfma_f32_16x16x32_bf16 v[32:35], a[84:87], v[104:107], v[32:35]\n"
"v_fma_f32 v46, -v60, v22, v46\n"
"s_waitcnt lgkmcnt(2)\n"
"v_mfma_f32_16x16x32_bf16 v[32:35], a[96:99], v[76:79], v[32:35]\n"
"ds_read_b128 v[100:103], v4 offset:16128\n"
"buffer_load_dwordx4 v2, s[32:35], s63 offen offset:2048 sc1 lds\n"
"v_mfma_f32_16x16x32_bf16 v[36:39], a[100:103], v[76:79], v[36:39]\n"
"v_add_f32 v56, v56, v57\n"
"v_add_f32 v58, v58, v59\n"
"v_mfma_f32_16x16x32_bf16 v[40:43], a[104:107], v[76:79], v[40:43]\n"
"v_add_f32 v56, v56, v58\n"
"v_fma_f32 v47, -v60, v23, v47\n"
"v_mfma_f32_16x16x32_bf16 v[40:43], a[116:119], v[80:83], v[40:43]\n"
"ds_read_b128 v[104:107], v4 offset:17280\n"
"v_add_f32_dpp v56, v56, v56 row_ror:8 row_mask:0xf bank_mask:0xf\n"
"v_mfma_f32_16x16x32_bf16 v[36:39], a[112:115], v[80:83], v[36:39]\n"
"v_mul_f32 v64, v44, v61\n"
"v_add_f32_dpp v56, v56, v56 row_ror:4 row_mask:0xf bank_mask:0xf\n"
"v_mfma_f32_16x16x32_bf16 v[32:35], a[108:111], v[80:83], v[32:35]\n"
"v_exp_f32 v64, v64\n"
"v_mfma_f32_16x16x32_bf16 v[32:35], a[120:123], v[84:87], v[32:35]\n"
"ds_read_b128 v[76:79], v4 offset:18432\n"
"v_add_f32_dpp v56, v56, v56 row_ror:2 row_mask:0xf bank_mask:0xf\n"
"v_mfma_f32_16x16x32_bf16 v[36:39], a[124:127], v[84:87], v[36:39]\n"
"v_mul_f32 v56, 0x3a800000, v56\n"
"v_mul_f32 v65, v45, v61\n"
"v_mfma_f32_16x16x32_bf16 v[40:43], a[128:131], v[84:87], v[40:43]\n"
"buffer_load_dwordx4 v2, s[32:35], s63 offen offset:3072 sc1 lds\n"
"v_mul_f32 v57, v56, v56\n"
"v_mfma_f32_16x16x32_bf16 v[40:43], a[140:143], v[88:91], v[40:43]\n"
"ds_read_b128 v[80:83], v4 offset:19584\n"
"v_sub_f32_dpp v57, v56, v57 quad_perm:[1,1,3,3] row_mask:0xf bank_mask:0xf\n"
"v_mfma_f32_16x16x32_bf16 v[36:39], a[136:139], v[88:91], v[36:39]\n"
"v_exp_f32 v65, v65\n"
"v_mfma_f32_16x16x32_bf16 v[32:35], a[132:135], v[88:91], v[32:35]\n"
"v_max_f32 v57, 0, v57\n"
"s_waitcnt lgkmcnt(2)\n"
"v_mfma_f32_16x16x32_bf16 v[32:35], a[144:147], v[92:95], v[32:35]\n"
"ds_read_b128 v[84:87], v4 offset:20736\n"
"v_cvt_pk_bf16_f32 v57, 0, v57\n"
"v_mfma_f32_16x16x32_bf16 v[36:39], a[148:151], v[92:95], v[36:39]\n"
"v_add_f32 v57, 0x3727c5ac, v57\n"
"v_mul_f32 v66, v46, v61\n"
"v_mfma_f32_16x16x32_bf16 v[40:43], a[152:155], v[92:95], v[40:43]\n"
"v_exp_f32 v66, v66\n"
"v_mfma_f32_16x16x32_bf16 v[40:43], a[164:167], v[96:99], v[40:43]\n"
"ds_read_b128 v[88:91], v4 offset:21888\n"
"s_add_u32 m0, s64, 4096\n"
"v_mfma_f32_16x16x32_bf16 v[36:39], a[160:163], v[96:99], v[36:39]\n"
"buffer_load_dwordx4 v6, s[32:35], s63 offen offset:0 sc1 lds\n"
"v_cvt_pk_bf16_f32 v57, 0, v57\n"
"v_mfma_f32_16x16x32_bf16 v[32:35], a[156:159], v[96:99], v[32:35]\n"
"v_sqrt_f32 v57, v57\n"
"v_mfma_f32_16x16x32_bf16 v[32:35], a[168:171], v[100:103], v[32:35]\n"
"ds_read_b128 v[92:95], v4 offset:23040\n"
"v_cvt_pk_bf16_f32 v57, v56, v57\n"
"v_mfma_f32_16x16x32_bf16 v[36:39], a[172:175], v[100:103], v[36:39]\n"
"ds_write_b32 v10, v57 offset:0\n"
"v_mul_f32 v67, v47, v61\n"
"v_mfma_f32_16x16x32_bf16 v[40:43], a[176:179], v[100:103], v[40:43]\n"
"v_exp_f32 v67, v67\n"
"v_mfma_f32_16x16x32_bf16 v[40:43], a[188:191], v[104:107], v[40:43]\n"
"ds_read_b128 v[96:99], v4 offset:24192\n"
"v_mul_f32 v68, v52, v61\n"
"v_mfma_f32_16x16x32_bf16 v[36:39], a[184:187], v[104:107], v[36:39]\n"
"v_exp_f32 v68, v68\n"
"v_mfma_f32_16x16x32_bf16 v[32:35], a[180:183], v[104:107], v[32:35]\n"
"v_mul_f32 v69, v53, v61\n"
"s_waitcnt lgkmcnt(3)\n"
"v_mfma_f32_16x16x32_bf16 v[32:35], a[192:195], v[76:79], v[32:35]\n"
"ds_read_b128 v[100:103], v4 offset:25344\n"
"buffer_load_dwordx4 v6, s[32:35], s63 offen offset:1024 sc1 lds\n"
"v_mfma_f32_16x16x32_bf16 v[36:39], a[196:199], v[76:79], v[36:39]\n"
"v_exp_f32 v69, v69\n"
"v_mfma_f32_16x16x32_bf16 v[40:43], a[200:203], v[76:79], v[40:43]\n"
"buffer_load_dwordx2 v[12:13], v14, s[32:35], s66 offen sc1\n"
"v_mul_f32 v44, v44, v48\n"
"v_mfma_f32_16x16x32_bf16 v[40:43], a[212:215], v[80:83], v[40:43]\n"
"ds_read_b128 v[104:107], v4 offset:26496\n"
"v_mul_f32 v45, v45, v49\n"
"v_mfma_f32_16x16x32_bf16 v[36:39], a[208:211], v[80:83], v[36:39]\n"
"v_mul_f32 v46, v46, v50\n"
"v_mul_f32 v47, v47, v51\n"
"v_mfma_f32_16x16x32_bf16 v[32:35], a[204:207], v[80:83], v[32:35]\n"
"v_mul_f32 v52, v52, v54\n"
"v_mul_f32 v53, v53, v55\n"
"v_mfma_f32_16x16x32_bf16 v[32:35], a[216:219], v[84:87], v[32:35]\n"
"ds_read_b128 v[76:79], v4 offset:27648\n"
"v_fma_f32 v64, v64, v62, v62\n"
"v_mfma_f32_16x16x32_bf16 v[36:39], a[220:223], v[84:87], v[36:39]\n"
"v_rcp_f32 v64, v64\n"
"v_mfma_f32_16x16x32_bf16 v[40:43], a[224:227], v[84:87], v[40:43]\n"
"buffer_load_dwordx4 v6, s[32:35], s63 offen offset:2048 sc1 lds\n"
"v_fma_f32 v65, v65, v62, v62\n"
"v_mfma_f32_16x16x32_bf16 v[40:43], a[236:239], v[88:91], v[40:43]\n"
"ds_read_b128 v[80:83], v4 offset:28800\n"
"v_fma_f32 v66, v66, v62, v62\n"
"v_mfma_f32_16x16x32_bf16 v[36:39], a[232:235], v[88:91], v[36:39]\n"
"v_rcp_f32 v65, v65\n"
"v_mfma_f32_16x16x32_bf16 v[32:35], a[228:231], v[88:91], v[32:35]\n"
"v_fma_f32 v67, v67, v62, v62\n"
"s_waitcnt lgkmcnt(2)\n"
"v_mfma_f32_16x16x32_bf16 v[32:35], a[240:243], v[92:95], v[32:35]\n"
"ds_read_b128 v[84:87], v4 offset:29952\n"
"v_fma_f32 v68, v68, v62, v62\n"
"v_mfma_f32_16x16x32_bf16 v[36:39], a[244:247], v[92:95], v[36:39]\n"
"v_rcp_f32 v66, v66\n"
"v_mfma_f32_16x16x32_bf16 v[40:43], a[248:251], v[92:95], v[40:43]\n"
"v_rcp_f32 v67, v67\n"
"v_mfma_f32_16x16x32_bf16 v[40:43], v[132:135], v[96:99], v[40:43]\n"
"ds_read_b128 v[88:91], v4 offset:31104\n"
"v_fma_f32 v69, v69, v62, v62\n"
"v_mfma_f32_16x16x32_bf16 v[36:39], v[128:131], v[96:99], v[36:39]\n"
"buffer_load_dwordx4 v6, s[32:35], s63 offen offset:3072 sc1 lds\n"
"v_mul_f32 v44, v44, v64\n"
"v_mfma_f32_16x16x32_bf16 v[32:35], a[252:255], v[96:99], v[32:35]\n"
"v_rcp_f32 v68, v68\n"
"v_mfma_f32_16x16x32_bf16 v[32:35], v[136:139], v[100:103], v[32:35]\n"
"ds_read_b128 v[92:95], v4 offset:32256\n"
"v_mul_f32 v45, v45, v65\n"
"v_mfma_f32_16x16x32_bf16 v[36:39], v[140:143], v[100:103], v[36:39]\n"
"v_rcp_f32 v69, v69\n"
"v_mfma_f32_16x16x32_bf16 v[40:43], v[144:147], v[100:103], v[40:43]\n"
"v_mul_f32 v46, v46, v66\n"
"v_mul_f32 v47, v47, v67\n"
"v_mfma_f32_16x16x32_bf16 v[40:43], v[156:159], v[104:107], v[40:43]\n"
"ds_read_b128 v[96:99], v4 offset:33408\n"
"v_mul_f32 v52, v52, v68\n"
"v_mfma_f32_16x16x32_bf16 v[36:39], v[152:155], v[104:107], v[36:39]\n"
"v_mul_f32 v53, v53, v69\n"
"v_cvt_pk_bf16_f32 v72, v44, v45\n"
"v_mfma_f32_16x16x32_bf16 v[32:35], v[148:151], v[104:107], v[32:35]\n"
"s_add_u32 m0, s64, 8192\n"
"s_waitcnt lgkmcnt(2)\n"
"v_mfma_f32_16x16x32_bf16 v[32:35], v[160:163], v[76:79], v[32:35]\n"
"ds_read_b128 v[100:103], v4 offset:34560\n"
"buffer_load_dwordx4 v7, s[32:35], s63 offen offset:0 sc1 lds\n"
"v_mfma_f32_16x16x32_bf16 v[36:39], v[164:167], v[76:79], v[36:39]\n"
"s_add_u32 s63, s63, 890880\n"
"v_cvt_pk_bf16_f32 v74, v52, v53\n"
"v_mfma_f32_16x16x32_bf16 v[40:43], v[168:171], v[76:79], v[40:43]\n"
"v_cvt_pk_bf16_f32 v73, v46, v47\n"
"v_mfma_f32_16x16x32_bf16 v[40:43], v[180:183], v[80:83], v[40:43]\n"
"ds_read_b128 v[104:107], v4 offset:35712\n"
"v_mfma_f32_16x16x32_bf16 v[36:39], v[176:179], v[80:83], v[36:39]\n"
"v_permlane32_swap_b32 v74, v75\n"
"v_mfma_f32_16x16x32_bf16 v[32:35], v[172:175], v[80:83], v[32:35]\n"
"v_xor_b32 v4, 73728, v4\n"
"s_waitcnt vmcnt(10)\n"
"v_mfma_f32_16x16x32_bf16 v[32:35], v[184:187], v[84:87], v[32:35]\n"
"s_barrier\n"
"ds_read_b128 v[76:79], v5 offset:0\n"
"v_mfma_f32_16x16x32_bf16 v[36:39], v[188:191], v[84:87], v[36:39]\n"
"buffer_store_dwordx2 v[16:17], v2, s[92:95], s68 idxen sc1\n"
"v_mfma_f32_16x16x32_bf16 v[40:43], v[192:195], v[84:87], v[40:43]\n"
"v_permlane16_swap_b32 v72, v74\n"
"v_mfma_f32_16x16x32_bf16 v[40:43], v[204:207], v[88:91], v[40:43]\n"
"ds_read_b128 v[80:83], v5 offset:1152\n"
"v_mfma_f32_16x16x32_bf16 v[36:39], v[200:203], v[88:91], v[36:39]\n"
"v_permlane16_swap_b32 v73, v75\n"
"v_mfma_f32_16x16x32_bf16 v[32:35], v[196:199], v[88:91], v[32:35]\n"
"buffer_store_dwordx4 v[72:75], v15, s[32:35], s68 offen sc1\n"
"s_waitcnt lgkmcnt(2)\n"
"v_mfma_f32_16x16x32_bf16 v[32:35], v[208:211], v[92:95], v[32:35]\n"
"ds_read_b128 v[84:87], v5 offset:2304\n"
"v_mfma_f32_16x16x32_bf16 v[36:39], v[212:215], v[92:95], v[36:39]\n"
"v_mfma_f32_16x16x32_bf16 v[40:43], v[216:219], v[92:95], v[40:43]\n"
"v_mfma_f32_16x16x32_bf16 v[40:43], v[228:231], v[96:99], v[40:43]\n"
"ds_read_b128 v[88:91], v5 offset:3456\n"
"v_mfma_f32_16x16x32_bf16 v[36:39], v[224:227], v[96:99], v[36:39]\n"
"v_mfma_f32_16x16x32_bf16 v[32:35], v[220:223], v[96:99], v[32:35]\n"
"v_mfma_f32_16x16x32_bf16 v[32:35], v[232:235], v[100:103], v[32:35]\n"
"ds_read_b128 v[92:95], v5 offset:4608\n"
"v_mfma_f32_16x16x32_bf16 v[36:39], v[236:239], v[100:103], v[36:39]\n"
"v_mfma_f32_16x16x32_bf16 v[40:43], v[240:243], v[100:103], v[40:43]\n"
"v_mfma_f32_16x16x32_bf16 v[40:43], v[252:255], v[104:107], v[40:43]\n"
"ds_read_b128 v[96:99], v5 offset:5760\n"
"v_mfma_f32_16x16x32_bf16 v[36:39], v[248:251], v[104:107], v[36:39]\n"
"s_sub_u32 s62, s62, 1\n"
"v_mfma_f32_16x16x32_bf16 v[32:35], v[244:247], v[104:107], v[32:35]\n"
"s_cbranch_scc1 .L_gt0\n"
"s_waitcnt lgkmcnt(2)\n"
"v_mfma_f32_16x16x32_bf16 v[44:47], a[0:3], v[76:79], 0\n"
"ds_read_b128 v[100:103], v5 offset:6912\n"
"ds_read_b32 v62, v11 offset:0\n"
"v_mfma_f32_16x16x32_bf16 v[48:51], a[4:7], v[76:79], 0\n"
"s_add_u32 s68, s68, 890880\n"
"v_mfma_f32_16x16x32_bf16 v[52:55], a[8:11], v[76:79], 0\n"
"v_mfma_f32_16x16x32_bf16 v[52:55], a[20:23], v[80:83], v[52:55]\n"
"ds_read_b128 v[104:107], v5 offset:8064\n"
"v_mfma_f32_16x16x32_bf16 v[48:51], a[16:19], v[80:83], v[48:51]\n"
"ds_read2st64_b32 v[56:57], v8 offset0:144 offset1:180\n"
"v_mfma_f32_16x16x32_bf16 v[44:47], a[12:15], v[80:83], v[44:47]\n"
"s_waitcnt vmcnt(5)\n"
"v_cmp_ne_u64 vcc, v[16:17], v[12:13]\n"
"v_mfma_f32_16x16x32_bf16 v[44:47], a[24:27], v[84:87], v[44:47]\n"
"ds_read_b128 v[76:79], v5 offset:9216\n"
"ds_read2st64_b32 v[58:59], v8 offset0:216 offset1:252\n"
"v_mfma_f32_16x16x32_bf16 v[48:51], a[28:31], v[84:87], v[48:51]\n"
"s_cbranch_vccnz .L_rtg1\n"
".L_bkg1:\n"
"s_add_u32 s66, s66, 890880\n"
"v_mfma_f32_16x16x32_bf16 v[52:55], a[32:35], v[84:87], v[52:55]\n"
"s_add_u32 m0, s64, 36864\n"
"v_xor_b32 v8, v9, v8\n"
"v_mfma_f32_16x16x32_bf16 v[52:55], a[44:47], v[88:91], v[52:55]\n"
"ds_read_b128 v[80:83], v5 offset:10368\n"
"buffer_load_dwordx4 v2, s[32:35], s63 offen offset:0 sc1 lds\n"
"v_mfma_f32_16x16x32_bf16 v[48:51], a[40:43], v[88:91], v[48:51]\n"
"s_waitcnt lgkmcnt(5)\n"
"v_lshlrev_b32 v60, 16, v62\n"
"v_mfma_f32_16x16x32_bf16 v[44:47], a[36:39], v[88:91], v[44:47]\n"
"v_and_b32 v62, 0xffff0000, v62\n"
"s_waitcnt lgkmcnt(4)\n"
"v_mfma_f32_16x16x32_bf16 v[44:47], a[48:51], v[92:95], v[44:47]\n"
"ds_read_b128 v[84:87], v5 offset:11520\n"
"v_fma_f32 v40, -v60, v28, v40\n"
"v_mfma_f32_16x16x32_bf16 v[48:51], a[52:55], v[92:95], v[48:51]\n"
"v_rcp_f32 v61, v62\n"
"v_mfma_f32_16x16x32_bf16 v[52:55], a[56:59], v[92:95], v[52:55]\n"
"v_mul_f32 v62, v62, v62\n"
"v_mul_f32 v61, 0xbfb8aa3b, v61\n"
"v_mfma_f32_16x16x32_bf16 v[52:55], a[68:71], v[96:99], v[52:55]\n"
"ds_read_b128 v[88:91], v5 offset:12672\n"
"v_fma_f32 v41, -v60, v29, v41\n"
"v_mfma_f32_16x16x32_bf16 v[48:51], a[64:67], v[96:99], v[48:51]\n"
"buffer_load_dwordx4 v2, s[32:35], s63 offen offset:1024 sc1 lds\n"
"v_fma_f32 v42, -v60, v30, v42\n"
"v_mfma_f32_16x16x32_bf16 v[44:47], a[60:63], v[96:99], v[44:47]\n"
"v_fma_f32 v43, -v60, v31, v43\n"
"v_fma_f32 v36, -v60, v24, v36\n"
"v_mfma_f32_16x16x32_bf16 v[44:47], a[72:75], v[100:103], v[44:47]\n"
"ds_read_b128 v[92:95], v5 offset:13824\n"
"v_fma_f32 v37, -v60, v25, v37\n"
"v_mfma_f32_16x16x32_bf16 v[48:51], a[76:79], v[100:103], v[48:51]\n"
"v_permlane32_swap_b32 v40, v42\n"
"v_mfma_f32_16x16x32_bf16 v[52:55], a[80:83], v[100:103], v[52:55]\n"
"v_permlane32_swap_b32 v41, v43\n"
"v_mfma_f32_16x16x32_bf16 v[52:55], a[92:95], v[104:107], v[52:55]\n"
"ds_read_b128 v[96:99], v5 offset:14976\n"
"v_fma_f32 v38, -v60, v26, v38\n"
"v_mfma_f32_16x16x32_bf16 v[48:51], a[88:91], v[104:107], v[48:51]\n"
"v_fma_f32 v39, -v60, v27, v39\n"
"v_fma_f32 v32, -v60, v20, v32\n"
"v_mfma_f32_16x16x32_bf16 v[44:47], a[84:87], v[104:107], v[44:47]\n"
"v_fma_f32 v33, -v60, v21, v33\n"
"s_waitcnt lgkmcnt(2)\n"
"v_mfma_f32_16x16x32_bf16 v[44:47], a[96:99], v[76:79], v[44:47]\n"
"ds_read_b128 v[100:103], v5 offset:16128\n"
"buffer_load_dwordx4 v2, s[32:35], s63 offen offset:2048 sc1 lds\n"
"v_mfma_f32_16x16x32_bf16 v[48:51], a[100:103], v[76:79], v[48:51]\n"
"v_add_f32 v56, v56, v57\n"
"v_add_f32 v58, v58, v59\n"
"v_mfma_f32_16x16x32_bf16 v[52:55], a[104:107], v[76:79], v[52:55]\n"
"v_add_f32 v56, v56, v58\n"
"v_fma_f32 v34, -v60, v22, v34\n"
"v_mfma_f32_16x16x32_bf16 v[52:55], a[116:119], v[80:83], v[52:55]\n"
"ds_read_b128 v[104:107], v5 offset:17280\n"
"v_add_f32_dpp v56, v56, v56 row_ror:8 row_mask:0xf bank_mask:0xf\n"
"v_mfma_f32_16x16x32_bf16 v[48:51], a[112:115], v[80:83], v[48:51]\n"
"v_fma_f32 v35, -v60, v23, v35\n"
"v_add_f32_dpp v56, v56, v56 row_ror:4 row_mask:0xf bank_mask:0xf\n"
"v_mfma_f32_16x16x32_bf16 v[44:47], a[108:111], v[80:83], v[44:47]\n"
"v_mul_f32 v64, v32, v61\n"
"v_add_f32_dpp v56, v56, v56 row_ror:2 row_mask:0xf bank_mask:0xf\n"
"v_mfma_f32_16x16x32_bf16 v[44:47], a[120:123], v[84:87], v[44:47]\n"
"ds_read_b128 v[76:79], v5 offset:18432\n"
"v_mul_f32 v56, 0x3a800000, v56\n"
"v_mfma_f32_16x16x32_bf16 v[48:51], a[124:127], v[84:87], v[48:51]\n"
"v_exp_f32 v64, v64\n"
"v_mfma_f32_16x16x32_bf16 v[52:55], a[128:131], v[84:87], v[52:55]\n"
"buffer_load_dwordx4 v2, s[32:35], s63 offen offset:3072 sc1 lds\n"
"v_mul_f32 v57, v56, v56\n"
"v_mfma_f32_16x16x32_bf16 v[52:55], a[140:143], v[88:91], v[52:55]\n"
"ds_read_b128 v[80:83], v5 offset:19584\n"
"v_sub_f32_dpp v57, v56, v57 quad_perm:[1,1,3,3] row_mask:0xf bank_mask:0xf\n"
"v_mfma_f32_16x16x32_bf16 v[48:51], a[136:139], v[88:91], v[48:51]\n"
"v_max_f32 v57, 0, v57\n"
"v_mul_f32 v65, v33, v61\n"
"v_mfma_f32_16x16x32_bf16 v[44:47], a[132:135], v[88:91], v[44:47]\n"
"v_cvt_pk_bf16_f32 v57, 0, v57\n"
"s_waitcnt lgkmcnt(2)\n"
"v_mfma_f32_16x16x32_bf16 v[44:47], a[144:147], v[92:95], v[44:47]\n"
"ds_read_b128 v[84:87], v5 offset:20736\n"
"v_add_f32 v57, 0x3727c5ac, v57\n"
"v_mfma_f32_16x16x32_bf16 v[48:51], a[148:151], v[92:95], v[48:51]\n"
"v_exp_f32 v65, v65\n"
"v_mfma_f32_16x16x32_bf16 v[52:55], a[152:155], v[92:95], v[52:55]\n"
"v_cvt_pk_bf16_f32 v57, 0, v57\n"
"v_mul_f32 v66, v34, v61\n"
"v_mfma_f32_16x16x32_bf16 v[52:55], a[164:167], v[96:99], v[52:55]\n"
"ds_read_b128 v[88:91], v5 offset:21888\n"
"s_add_u32 m0, s64, 40960\n"
"v_mfma_f32_16x16x32_bf16 v[48:51], a[160:163], v[96:99], v[48:51]\n"
"buffer_load_dwordx4 v6, s[32:35], s63 offen offset:0 sc1 lds\n"
"v_mul_f32 v67, v35, v61\n"
"v_mfma_f32_16x16x32_bf16 v[44:47], a[156:159], v[96:99], v[44:47]\n"
"v_sqrt_f32 v57, v57\n"
"v_mfma_f32_16x16x32_bf16 v[44:47], a[168:171], v[100:103], v[44:47]\n"
"ds_read_b128 v[92:95], v5 offset:23040\n"
"v_cvt_pk_bf16_f32 v57, v56, v57\n"
"v_mfma_f32_16x16x32_bf16 v[48:51], a[172:175], v[100:103], v[48:51]\n"
"ds_write_b32 v10, v57 offset:128\n"
"v_mul_f32 v68, v40, v61\n"
"v_mfma_f32_16x16x32_bf16 v[52:55], a[176:179], v[100:103], v[52:55]\n"
"v_exp_f32 v66, v66\n"
"v_mfma_f32_16x16x32_bf16 v[52:55], a[188:191], v[104:107], v[52:55]\n"
"ds_read_b128 v[96:99], v5 offset:24192\n"
"v_mul_f32 v69, v41, v61\n"
"v_mfma_f32_16x16x32_bf16 v[48:51], a[184:187], v[104:107], v[48:51]\n"
"v_exp_f32 v67, v67\n"
"v_mfma_f32_16x16x32_bf16 v[44:47], a[180:183], v[104:107], v[44:47]\n"
"v_mul_f32 v32, v32, v36\n"
"s_waitcnt lgkmcnt(3)\n"
"v_mfma_f32_16x16x32_bf16 v[44:47], a[192:195], v[76:79], v[44:47]\n"
"ds_read_b128 v[100:103], v5 offset:25344\n"
"buffer_load_dwordx4 v6, s[32:35], s63 offen offset:1024 sc1 lds\n"
"v_mfma_f32_16x16x32_bf16 v[48:51], a[196:199], v[76:79], v[48:51]\n"
"v_exp_f32 v68, v68\n"
"v_mfma_f32_16x16x32_bf16 v[52:55], a[200:203], v[76:79], v[52:55]\n"
"buffer_load_dwordx2 v[12:13], v14, s[32:35], s66 offen sc1\n"
"v_mul_f32 v33, v33, v37\n"
"v_mfma_f32_16x16x32_bf16 v[52:55], a[212:215], v[80:83], v[52:55]\n"
"ds_read_b128 v[104:107], v5 offset:26496\n"
"v_mul_f32 v34, v34, v38\n"
"v_mfma_f32_16x16x32_bf16 v[48:51], a[208:211], v[80:83], v[48:51]\n"
"v_exp_f32 v69, v69\n"
"v_mfma_f32_16x16x32_bf16 v[44:47], a[204:207], v[80:83], v[44:47]\n"
"v_mul_f32 v35, v35, v39\n"
"v_mul_f32 v40, v40, v42\n"
"v_mfma_f32_16x16x32_bf16 v[44:47], a[216:219], v[84:87], v[44:47]\n"
"ds_read_b128 v[76:79], v5 offset:27648\n"
"v_mul_f32 v41, v41, v43\n"
"v_mfma_f32_16x16x32_bf16 v[48:51], a[220:223], v[84:87], v[48:51]\n"
"v_fma_f32 v64, v64, v62, v62\n"
"v_fma_f32 v65, v65, v62, v62\n"
"v_mfma_f32_16x16x32_bf16 v[52:55], a[224:227], v[84:87], v[52:55]\n"
"buffer_load_dwordx4 v6, s[32:35], s63 offen offset:2048 sc1 lds\n"
"v_fma_f32 v66, v66, v62, v62\n"
"v_mfma_f32_16x16x32_bf16 v[52:55], a[236:239], v[88:91], v[52:55]\n"
"ds_read_b128 v[80:83], v5 offset:28800\n"
"v_fma_f32 v67, v67, v62, v62\n"
"v_mfma_f32_16x16x32_bf16 v[48:51], a[232:235], v[88:91], v[48:51]\n"
"v_rcp_f32 v64, v64\n"
"v_mfma_f32_16x16x32_bf16 v[44:47], a[228:231], v[88:91], v[44:47]\n"
"v_fma_f32 v68, v68, v62, v62\n"
"s_waitcnt lgkmcnt(2)\n"
"v_mfma_f32_16x16x32_bf16 v[44:47], a[240:243], v[92:95], v[44:47]\n"
"ds_read_b128 v[84:87], v5 offset:29952\n"
"v_fma_f32 v69, v69, v62, v62\n"
"v_mfma_f32_16x16x32_bf16 v[48:51], a[244:247], v[92:95], v[48:51]\n"
"v_rcp_f32 v65, v65\n"
"v_mfma_f32_16x16x32_bf16 v[52:55], a[248:251], v[92:95], v[52:55]\n"
"v_rcp_f32 v66, v66\n"
"v_mfma_f32_16x16x32_bf16 v[52:55], v[132:135], v[96:99], v[52:55]\n"
"ds_read_b128 v[88:91], v5 offset:31104\n"
"v_mul_f32 v32, v32, v64\n"
"v_mfma_f32_16x16x32_bf16 v[48:51], v[128:131], v[96:99], v[48:51]\n"
"buffer_load_dwordx4 v6, s[32:35], s63 offen offset:3072 sc1 lds\n"
"v_mul_f32 v33, v33, v65\n"
"v_mfma_f32_16x16x32_bf16 v[44:47], a[252:255], v[96:99], v[44:47]\n"
"v_rcp_f32 v67, v67\n"
"v_mfma_f32_16x16x32_bf16 v[44:47], v[136:139], v[100:103], v[44:47]\n"
"ds_read_b128 v[92:95], v5 offset:32256\n"
"v_mul_f32 v34, v34, v66\n"
"v_mfma_f32_16x16x32_bf16 v[48:51], v[140:143], v[100:103], v[48:51]\n"
"v_rcp_f32 v68, v68\n"
"v_mfma_f32_16x16x32_bf16 v[52:55], v[144:147], v[100:103], v[52:55]\n"
"v_rcp_f32 v69, v69\n"
"v_mfma_f32_16x16x32_bf16 v[52:55], v[156:159], v[104:107], v[52:55]\n"
"ds_read_b128 v[96:99], v5 offset:33408\n"
"v_mul_f32 v35, v35, v67\n"
"v_mfma_f32_16x16x32_bf16 v[48:51], v[152:155], v[104:107], v[48:51]\n"
"v_mul_f32 v40, v40, v68\n"
"v_mul_f32 v41, v41, v69\n"
"v_mfma_f32_16x16x32_bf16 v[44:47], v[148:151], v[104:107], v[44:47]\n"
"s_add_u32 m0, s64, 45056\n"
"s_waitcnt lgkmcnt(2)\n"
"v_mfma_f32_16x16x32_bf16 v[44:47], v[160:163], v[76:79], v[44:47]\n"
"ds_read_b128 v[100:103], v5 offset:34560\n"
"buffer_load_dwordx4 v7, s[32:35], s63 offen offset:0 sc1 lds\n"
"v_mfma_f32_16x16x32_bf16 v[48:51], v[164:167], v[76:79], v[48:51]\n"
"s_add_u32 s63, s63, 890880\n"
"s_xor_b32 s64, s64, s65\n"
"v_mfma_f32_16x16x32_bf16 v[52:55], v[168:171], v[76:79], v[52:55]\n"
"v_cvt_pk_bf16_f32 v74, v40, v41\n"
"v_cvt_pk_bf16_f32 v72, v32, v33\n"
"v_mfma_f32_16x16x32_bf16 v[52:55], v[180:183], v[80:83], v[52:55]\n"
"ds_read_b128 v[104:107], v5 offset:35712\n"
"v_cvt_pk_bf16_f32 v73, v34, v35\n"
"v_mfma_f32_16x16x32_bf16 v[48:51], v[176:179], v[80:83], v[48:51]\n"
"v_permlane32_swap_b32 v74, v75\n"
"v_mfma_f32_16x16x32_bf16 v[44:47], v[172:175], v[80:83], v[44:47]\n"
"v_xor_b32 v5, 73728, v5\n"
"s_waitcnt vmcnt(10)\n"
"v_mfma_f32_16x16x32_bf16 v[44:47], v[184:187], v[84:87], v[44:47]\n"
"s_barrier\n"
"ds_read_b128 v[76:79], v4 offset:0\n"
"v_mfma_f32_16x16x32_bf16 v[48:51], v[188:191], v[84:87], v[48:51]\n"
"buffer_store_dwordx2 v[16:17], v2, s[92:95], s68 idxen sc1\n"
"v_mfma_f32_16x16x32_bf16 v[52:55], v[192:195], v[84:87], v[52:55]\n"
"v_permlane16_swap_b32 v72, v74\n"
"v_mfma_f32_16x16x32_bf16 v[52:55], v[204:207], v[88:91], v[52:55]\n"
"ds_read_b128 v[80:83], v4 offset:1152\n"
"v_mfma_f32_16x16x32_bf16 v[48:51], v[200:203], v[88:91], v[48:51]\n"
"v_permlane16_swap_b32 v73, v75\n"
"v_mfma_f32_16x16x32_bf16 v[44:47], v[196:199], v[88:91], v[44:47]\n"
"buffer_store_dwordx4 v[72:75], v15, s[32:35], s68 offen sc1\n"
"s_waitcnt lgkmcnt(2)\n"
"v_mfma_f32_16x16x32_bf16 v[44:47], v[208:211], v[92:95], v[44:47]\n"
"ds_read_b128 v[84:87], v4 offset:2304\n"
"v_mfma_f32_16x16x32_bf16 v[48:51], v[212:215], v[92:95], v[48:51]\n"
"v_mfma_f32_16x16x32_bf16 v[52:55], v[216:219], v[92:95], v[52:55]\n"
"v_mfma_f32_16x16x32_bf16 v[52:55], v[228:231], v[96:99], v[52:55]\n"
"ds_read_b128 v[88:91], v4 offset:3456\n"
"v_mfma_f32_16x16x32_bf16 v[48:51], v[224:227], v[96:99], v[48:51]\n"
"v_mfma_f32_16x16x32_bf16 v[44:47], v[220:223], v[96:99], v[44:47]\n"
"v_mfma_f32_16x16x32_bf16 v[44:47], v[232:235], v[100:103], v[44:47]\n"
"ds_read_b128 v[92:95], v4 offset:4608\n"
"v_mfma_f32_16x16x32_bf16 v[48:51], v[236:239], v[100:103], v[48:51]\n"
"v_mfma_f32_16x16x32_bf16 v[52:55], v[240:243], v[100:103], v[52:55]\n"
"v_mfma_f32_16x16x32_bf16 v[52:55], v[252:255], v[104:107], v[52:55]\n"
"ds_read_b128 v[96:99], v4 offset:5760\n"
"v_mfma_f32_16x16x32_bf16 v[48:51], v[248:251], v[104:107], v[48:51]\n"
"s_sub_u32 s62, s62, 1\n"
"v_mfma_f32_16x16x32_bf16 v[44:47], v[244:247], v[104:107], v[44:47]\n"
"s_cbranch_scc0 .L_gl\n"
"s_nop 1\n"
"ds_read_b32 v62, v11 offset:128\n"
"s_add_u32 s68, s68, 890880\n"
"s_waitcnt lgkmcnt(0)\n"
"v_lshlrev_b32 v60, 16, v62\n"
"v_and_b32 v62, 0xffff0000, v62\n"
"v_rcp_f32 v61, v62\n"
"v_mul_f32 v62, v62, v62\n"
"v_mul_f32 v61, 0xbfb8aa3b, v61\n"
"v_fma_f32 v52, -v60, v28, v52\n"
"v_fma_f32 v53, -v60, v29, v53\n"
"v_fma_f32 v54, -v60, v30, v54\n"
"v_fma_f32 v55, -v60, v31, v55\n"
"v_fma_f32 v48, -v60, v24, v48\n"
"v_fma_f32 v49, -v60, v25, v49\n"
"v_fma_f32 v50, -v60, v26, v50\n"
"v_fma_f32 v51, -v60, v27, v51\n"
"v_fma_f32 v44, -v60, v20, v44\n"
"v_fma_f32 v45, -v60, v21, v45\n"
"v_fma_f32 v46, -v60, v22, v46\n"
"v_fma_f32 v47, -v60, v23, v47\n"
"v_permlane32_swap_b32 v52, v54\n"
"v_permlane32_swap_b32 v53, v55\n"
"v_mul_f32 v64, v44, v61\n"
"v_mul_f32 v65, v45, v61\n"
"v_mul_f32 v66, v46, v61\n"
"v_mul_f32 v67, v47, v61\n"
"v_mul_f32 v68, v52, v61\n"
"v_mul_f32 v69, v53, v61\n"
"v_exp_f32 v64, v64\n"
"v_exp_f32 v65, v65\n"
"v_mul_f32 v44, v44, v48\n"
"v_mul_f32 v45, v45, v49\n"
"v_exp_f32 v66, v66\n"
"v_exp_f32 v67, v67\n"
"v_mul_f32 v46, v46, v50\n"
"v_mul_f32 v47, v47, v51\n"
"v_exp_f32 v68, v68\n"
"v_exp_f32 v69, v69\n"
"v_mul_f32 v52, v52, v54\n"
"v_mul_f32 v53, v53, v55\n"
"v_fma_f32 v64, v64, v62, v62\n"
"v_fma_f32 v65, v65, v62, v62\n"
"v_rcp_f32 v64, v64\n"
"v_rcp_f32 v65, v65\n"
"v_fma_f32 v66, v66, v62, v62\n"
"v_fma_f32 v67, v67, v62, v62\n"
"v_rcp_f32 v66, v66\n"
"v_rcp_f32 v67, v67\n"
"v_fma_f32 v68, v68, v62, v62\n"
"v_fma_f32 v69, v69, v62, v62\n"
"v_rcp_f32 v68, v68\n"
"v_rcp_f32 v69, v69\n"
"v_mul_f32 v44, v44, v64\n"
"v_mul_f32 v45, v45, v65\n"
"v_mul_f32 v46, v46, v66\n"
"v_mul_f32 v47, v47, v67\n"
"v_mul_f32 v52, v52, v68\n"
"v_mul_f32 v53, v53, v69\n"
"v_cvt_pk_bf16_f32 v74, v52, v53\n"
"v_cvt_pk_bf16_f32 v72, v44, v45\n"
"v_cvt_pk_bf16_f32 v73, v46, v47\n"
"v_permlane32_swap_b32 v74, v75\n"
"s_nop 1\n"
"v_permlane16_swap_b32 v72, v74\n"
"v_permlane16_swap_b32 v73, v75\n"
"buffer_store_dwordx4 v[72:75], v15, s[32:35], s68 offen sc1\n"
"s_waitcnt vmcnt(1)\n"
"s_barrier\n"
"buffer_store_dwordx2 v[16:17], v2, s[92:95], s68 idxen sc1\n"
"s_add_u32 s68, s68, 890880\n"
"s_waitcnt vmcnt(1)\n"
"s_barrier\n"
"buffer_store_dwordx2 v[16:17], v2, s[92:95], s68 idxen sc1\n"
"s_branch .L_end\n"
".L_gt0:\n"
"s_nop 1\n"
"ds_read_b32 v62, v11 offset:0\n"
"s_add_u32 s68, s68, 890880\n"
"s_waitcnt lgkmcnt(0)\n"
"v_lshlrev_b32 v60, 16, v62\n"
"v_and_b32 v62, 0xffff0000, v62\n"
"v_rcp_f32 v61, v62\n"
"v_mul_f32 v62, v62, v62\n"
"v_mul_f32 v61, 0xbfb8aa3b, v61\n"
"v_fma_f32 v40, -v60, v28, v40\n"
"v_fma_f32 v41, -v60, v29, v41\n"
"v_fma_f32 v42, -v60, v30, v42\n"
"v_fma_f32 v43, -v60, v31, v43\n"
"v_fma_f32 v36, -v60, v24, v36\n"
"v_fma_f32 v37, -v60, v25, v37\n"
"v_fma_f32 v38, -v60, v26, v38\n"
"v_fma_f32 v39, -v60, v27, v39\n"
"v_fma_f32 v32, -v60, v20, v32\n"
"v_fma_f32 v33, -v60, v21, v33\n"
"v_fma_f32 v34, -v60, v22, v34\n"
"v_fma_f32 v35, -v60, v23, v35\n"
"v_permlane32_swap_b32 v40, v42\n"
"v_permlane32_swap_b32 v41, v43\n"
"v_mul_f32 v64, v32, v61\n"
"v_mul_f32 v65, v33, v61\n"
"v_mul_f32 v66, v34, v61\n"
"v_mul_f32 v67, v35, v61\n"
"v_mul_f32 v68, v40, v61\n"
"v_mul_f32 v69, v41, v61\n"
"v_exp_f32 v64, v64\n"
"v_exp_f32 v65, v65\n"
"v_mul_f32 v32, v32, v36\n"
"v_mul_f32 v33, v33, v37\n"
"v_exp_f32 v66, v66\n"
"v_exp_f32 v67, v67\n"
"v_mul_f32 v34, v34, v38\n"
"v_mul_f32 v35, v35, v39\n"
"v_exp_f32 v68, v68\n"
"v_exp_f32 v69, v69\n"
"v_mul_f32 v40, v40, v42\n"
"v_mul_f32 v41, v41, v43\n"
"v_fma_f32 v64, v64, v62, v62\n"
"v_fma_f32 v65, v65, v62, v62\n"
"v_rcp_f32 v64, v64\n"
"v_rcp_f32 v65, v65\n"
"v_fma_f32 v66, v66, v62, v62\n"
"v_fma_f32 v67, v67, v62, v62\n"
"v_rcp_f32 v66, v66\n"
"v_rcp_f32 v67, v67\n"
"v_fma_f32 v68, v68, v62, v62\n"
"v_fma_f32 v69, v69, v62, v62\n"
"v_rcp_f32 v68, v68\n"
"v_rcp_f32 v69, v69\n"
"v_mul_f32 v32, v32, v64\n"
"v_mul_f32 v33, v33, v65\n"
"v_mul_f32 v34, v34, v66\n"
"v_mul_f32 v35, v35, v67\n"
"v_mul_f32 v40, v40, v68\n"
"v_mul_f32 v41, v41, v69\n"
"v_cvt_pk_bf16_f32 v74, v40, v41\n"
"v_cvt_pk_bf16_f32 v72, v32, v33\n"
"v_cvt_pk_bf16_f32 v73, v34, v35\n"
"v_permlane32_swap_b32 v74, v75\n"
"s_nop 1\n"
"v_permlane16_swap_b32 v72, v74\n"
"v_permlane16_swap_b32 v73, v75\n"
"buffer_store_dwordx4 v[72:75], v15, s[32:35], s68 offen sc1\n"
"s_waitcnt vmcnt(1)\n"
"s_barrier\n"
"buffer_store_dwordx2 v[16:17], v2, s[92:95], s68 idxen sc1\n"
"s_add_u32 s68, s68, 890880\n"
"s_waitcnt vmcnt(1)\n"
"s_barrier\n"
"buffer_store_dwordx2 v[16:17], v2, s[92:95], s68 idxen sc1\n"
"s_branch .L_end\n"
".L_q:\n"
"v_mov_b32 v16, 0x3f803f80\n"
"v_mov_b32 v17, 0x3f803f80\n"
"v_mov_b32 v18, 0x3f803f80\n"
"v_mov_b32 v19, 0x3f803f80\n"
"s_add_u32 s36, s10, 0x0\n"
"s_addc_u32 s37, s11, 0x0\n"
"s_and_b32 s37, s37, 0xffff\n"
"s_mov_b32 s38, 0x600000\n"
"s_mov_b32 s39, 0x20000\n"
"s_add_u32 s40, s10, 0x400\n"
"s_addc_u32 s41, s11, 0x0\n"
"s_and_b32 s41, s41, 0xffff\n"
"s_mov_b32 s42, 0x5ffc00\n"
"s_mov_b32 s43, 0x20000\n"
"s_add_u32 s44, s10, 0x800\n"
"s_addc_u32 s45, s11, 0x0\n"
"s_and_b32 s45, s45, 0xffff\n"
"s_mov_b32 s46, 0x5ff800\n"
"s_mov_b32 s47, 0x20000\n"
"s_lshl_b32 s96, s27, 7\n"
"v_and_b32 v68, 3, v1\n"
"v_mul_u32_u24 v68, 8, v68\n"
"v_and_b32 v71, 1, v1\n"
"v_bfe_u32 v3, v1, 1, 1\n"
"v_lshl_add_u32 v71, v71, 1, v3\n"
"v_mul_u32_u24 v71, 8, v71\n"
"s_lshl_b32 s48, s26, 5\n"
"v_add_u32 v68, s48, v68\n"
"v_add_u32 v71, s48, v71\n"
"v_lshrrev_b32 v64, 3, v1\n"
"v_bfe_u32 v65, v1, 4, 1\n"
"s_and_b32 s48, s26, 1\n"
"s_lshl_b32 s48, s48, 1\n"
"v_or_b32 v65, s48, v65\n"
"v_lshlrev_b32 v65, 1, v65\n"
"v_and_b32 v66, 7, v1\n"
"v_xor_b32 v65, v65, v66\n"
"v_mul_u32_u24 v64, 6144, v64\n"
"v_lshl_add_u32 v67, v65, 4, v64\n"
"s_mul_i32 s48, s26, 49152\n"
"s_add_u32 s96, s96, s48\n"
"s_mul_i32 s97, s26, 3072\n"
"s_add_u32 m0, s97, 49152\n"
"s_nop 0\n"
"buffer_load_dwordx4 v67, s[36:39], s96 offen offset:0 lds\n"
"buffer_load_dwordx4 v67, s[40:43], s96 offen offset:1024 lds\n"
"buffer_load_dwordx4 v67, s[44:47], s96 offen offset:2048 lds\n"
"s_add_u32 s96, s96, 196608\n"
"s_add_u32 m0, s97, 61440\n"
"s_nop 0\n"
"buffer_load_dwordx4 v67, s[36:39], s96 offen offset:0 lds\n"
"buffer_load_dwordx4 v67, s[40:43], s96 offen offset:1024 lds\n"
"buffer_load_dwordx4 v67, s[44:47], s96 offen offset:2048 lds\n"
"s_add_u32 s96, s96, 196608\n"
"s_add_u32 m0, s97, 73728\n"
"s_nop 0\n"
"buffer_load_dwordx4 v67, s[36:39], s96 offen offset:0 lds\n"
"buffer_load_dwordx4 v67, s[40:43], s96 offen offset:1024 lds\n"
"buffer_load_dwordx4 v67, s[44:47], s96 offen offset:2048 lds\n"
"s_add_u32 s96, s96, 196608\n"
"s_add_u32 m0, s97, 86016\n"
"s_nop 0\n"
"buffer_load_dwordx4 v67, s[36:39], s96 offen offset:0 lds\n"
"buffer_load_dwordx4 v67, s[40:43], s96 offen offset:1024 lds\n"
"buffer_load_dwordx4 v67, s[44:47], s96 offen offset:2048 lds\n"
"s_add_u32 s96, s96, 196608\n"
"s_add_u32 m0, s97, 98304\n"
"s_nop 0\n"
"buffer_load_dwordx4 v67, s[36:39], s96 offen offset:0 lds\n"
"buffer_load_dwordx4 v67, s[40:43], s96 offen offset:1024 lds\n"
"buffer_load_dwordx4 v67, s[44:47], s96 offen offset:2048 lds\n"
"s_add_u32 s96, s96, 196608\n"
"s_add_u32 m0, s97, 110592\n"
"s_nop 0\n"
"buffer_load_dwordx4 v67, s[36:39], s96 offen offset:0 lds\n"
"buffer_load_dwordx4 v67, s[40:43], s96 offen offset:1024 lds\n"
"buffer_load_dwordx4 v67, s[44:47], s96 offen offset:2048 lds\n"
"s_add_u32 s96, s96, 196608\n"
"s_add_u32 m0, s97, 122880\n"
"s_nop 0\n"
"buffer_load_dwordx4 v67, s[36:39], s96 offen offset:0 lds\n"
"buffer_load_dwordx4 v67, s[40:43], s96 offen offset:1024 lds\n"
"buffer_load_dwordx4 v67, s[44:47], s96 offen offset:2048 lds\n"
"s_add_u32 s96, s96, 196608\n"
"s_add_u32 m0, s97, 135168\n"
"s_nop 0\n"
"buffer_load_dwordx4 v67, s[36:39], s96 offen offset:0 lds\n"
"buffer_load_dwordx4 v67, s[40:43], s96 offen offset:1024 lds\n"
"buffer_load_dwordx4 v67, s[44:47], s96 offen offset:2048 lds\n"
"s_add_u32 s96, s96, 196608\n"
"s_add_u32 m0, s97, 0\n"
"s_nop 0\n"
"buffer_load_dwordx4 v67, s[36:39], s96 offen offset:0 lds\n"
"buffer_load_dwordx4 v67, s[40:43], s96 offen offset:1024 lds\n"
"buffer_load_dwordx4 v67, s[44:47], s96 offen offset:2048 lds\n"
"s_add_u32 s96, s96, 196608\n"
"s_add_u32 m0, s97, 12288\n"
"s_nop 0\n"
"buffer_load_dwordx4 v67, s[36:39], s96 offen offset:0 lds\n"
"buffer_load_dwordx4 v67, s[40:43], s96 offen offset:1024 lds\n"
"buffer_load_dwordx4 v67, s[44:47], s96 offen offset:2048 lds\n"
"s_add_u32 s96, s96, 196608\n"
"v_lshrrev_b32 v65, 4, v1\n"
"v_bfe_u32 v64, v1, 3, 1\n"
"v_and_b32 v66, 1, v65\n"
"v_lshl_add_u32 v64, v66, 1, v64\n"
"v_lshlrev_b32 v64, 5, v64\n"
"v_mul_u32_u24 v65, 3072, v65\n"
"v_bfe_u32 v66, v1, 2, 2\n"
"v_lshl_add_u32 v65, v66, 7, v65\n"
"v_xor_b32 v68, v64, v68\n"
"v_add_u32 v68, v65, v68\n"
"v_add_u32 v69, 49152, v68\n"
"v_add_u32 v70, 98304, v68\n"
"v_xor_b32 v71, v64, v71\n"
"v_add_u32 v71, v65, v71\n"
"v_add_u32 v72, 49152, v71\n"
"v_add_u32 v73, 98304, v71\n"
"s_lshl2_add_u32 s48, s27, s26\n"
"s_lshl_b32 s48, s48, 9\n"
"s_add_u32 s68, s48, 4648960\n"
"s_lshl2_add_u32 s49, s27, 32\n"
"s_waitcnt lgkmcnt(0)\n"
"s_add_u32 s54, s52, 0x7f4a7c15\n"
"s_addc_u32 s55, s53, 0x9e3779b9\n"
"s_lshl_b32 s67, s49, 5\n"
"s_add_u32 s67, s67, 4898816\n"
"s_sub_u32 s51, s67, s68\n"
"s_add_u32 s92, s32, s51\n"
"s_addc_u32 s93, s33, 0x10000\n"
"s_cmp_eq_u32 s26, 0\n"
"s_cselect_b32 s94, 1, 0\n"
"s_mov_b32 s95, 0x20000\n"
"v_mov_b32 v30, s54\n"
"v_mov_b32 v31, s55\n"
"v_cmp_gt_u32 vcc, 8, v1\n"
"v_mul_u32_u24 v3, 296960, v1\n"
"v_mov_b32 v12, 0x7fff0000\n"
"v_cndmask_b32 v3, v12, v3, vcc\n"
"s_add_u32 s51, s67, 76021760\n"
"buffer_store_dwordx2 v[30:31], v3, s[32:35], s51 offen sc1\n"
"s_sub_u32 s67, s68, 296960\n"
"s_mul_i32 s49, s28, 296960\n"
"s_add_u32 s68, s68, s49\n"
"s_sub_u32 s68, s68, 3563520\n"
"s_mov_b32 s62, 32\n"
"s_add_u32 s48, s28, 3\n"
"s_and_b32 s48, s48, 3\n"
"s_mul_i32 s48, s48, 296960\n"
"s_add_u32 s66, s48, 41721856\n"
"s_sub_u32 s48, s48, s49\n"
"s_add_u32 s69, s48, 38010880\n"
"s_sub_u32 s70, 0xfdce2000, s48\n"
"s_mov_b32 s72, s70\n"
"s_mov_b32 s63, 0xfffba000\n"
"s_mul_i32 s64, s26, 9216\n"
"s_add_u32 s65, s64, 73728\n"
"s_xor_b32 s65, s65, s64\n"
"s_add_u32 s63, s63, s64\n"
"v_mov_b32 v4, v2\n"
"v_add_u32 v5, 36864, v2\n"
"s_mov_b32 s71, s63\n"
"v_bfe_u32 v3, v1, 1, 3\n"
"v_mul_u32_u24 v6, 1152, v3\n"
"v_lshrrev_b32 v3, 1, v3\n"
"v_add_u32 v3, s26, v3\n"
"v_lshrrev_b32 v7, 4, v1\n"
"v_lshl_add_u32 v3, v3, 2, v7\n"
"v_and_b32 v3, 15, v3\n"
"v_lshl_add_u32 v6, v3, 3, v6\n"
"v_and_b32 v3, 1, v1\n"
"v_lshl_add_u32 v6, v3, 2, v6\n"
"v_add_u32 v6, 1024, v6\n"
"s_lshl_b32 s48, s26, 2\n"
"v_add_u32 v8, s48, v7\n"
"v_and_b32 v9, 15, v1\n"
"v_add_u32 v3, 64, v1\n"
"v_cmp_eq_u32 vcc, 0, v9\n"
"v_add_u32 v7, 73728, v6\n"
"v_xor_b32 v7, v7, v6\n"
"v_cndmask_b32 v8, v3, v8, vcc\n"
"v_lshlrev_b32 v8, 2, v8\n"
"v_add_u32 v8, 147456, v8\n"
"v_lshlrev_b32 v9, 2, v9\n"
"v_add_u32 v9, 147456, v9\n"
"v_lshrrev_b32 v10, 4, v1\n"
"v_and_b32 v32, 15, v1\n"
"v_and_b32 v33, 1, v1\n"
"v_lshl_add_u32 v34, v10, 2, v33\n"
"v_lshlrev_b32 v33, 4, v33\n"
"v_mov_b32 v35, 0xffff\n"
"v_lshlrev_b32 v33, v33, v35\n"
"v_cmp_le_u32 vcc, v34, v32\n"
"v_add_u32 v34, 2, v34\n"
"s_nop 0\n"
"v_cndmask_b32 v124, 0, v33, vcc\n"
"v_xor_b32 v126, v124, v33\n"
"v_cmp_le_u32 vcc, v34, v32\n"
"s_nop 1\n"
"v_cndmask_b32 v125, 0, v33, vcc\n"
"v_xor_b32 v127, v125, v33\n"
"v_lshlrev_b32 v10, 4, v10\n"
"v_add_u32 v10, 147456, v10\n"
"s_lshl_b32 s48, s26, 10\n"
"s_add_u32 s48, s48, 148480\n"
"v_lshlrev_b32 v11, 3, v1\n"
"v_add_u32 v11, s48, v11\n"
"s_lshl_b32 s48, s26, 3\n"
"s_add_u32 s48, s48, 0\n"
"v_add_u32 v0, s48, v1\n"
"v_lshrrev_b32 v0, 2, v0\n"
"v_lshlrev_b32 v0, 7, v0\n"
"v_cmp_gt_u32 vcc, 8, v1\n"
"v_mov_b32 v3, 0x7fff0000\n"
"s_nop 0\n"
"v_cndmask_b32 v0, v3, v0, vcc\n"
"v_cndmask_b32 v30, 0, v30, vcc\n"
"v_cndmask_b32 v31, 0, v31, vcc\n"
"v_mov_b32 v116, 0xff61b1e6\n"
"v_mov_b32 v117, 0xff61b1e6\n"
"v_mov_b32 v118, 0xff61b1e6\n"
"v_mov_b32 v119, 0xff61b1e6\n"
"v_mov_b32 v120, 0xff61b1e6\n"
"v_mov_b32 v121, 0xff61b1e6\n"
"v_mov_b32 v122, 0xff61b1e6\n"
"v_mov_b32 v123, 0xff61b1e6\n"
"v_cmp_ne_u32 vcc, 0xffff, v124\n"
"s_nop 1\n"
"v_cndmask_b32 v116, 0, v116, vcc\n"
"v_cmp_ne_u32 vcc, 0xffff0000, v124\n"
"s_nop 1\n"
"v_cndmask_b32 v117, 0, v117, vcc\n"
"v_cmp_ne_u32 vcc, 0xffff, v125\n"
"s_nop 1\n"
"v_cndmask_b32 v118, 0, v118, vcc\n"
"v_cmp_ne_u32 vcc, 0xffff0000, v125\n"
"s_nop 1\n"
"v_cndmask_b32 v119, 0, v119, vcc\n"
"v_cmp_ne_u32 vcc, 0xffff, v126\n"
"s_nop 1\n"
"v_cndmask_b32 v120, 0, v120, vcc\n"
"v_cmp_ne_u32 vcc, 0xffff0000, v126\n"
"s_nop 1\n"
"v_cndmask_b32 v121, 0, v121, vcc\n"
"v_cmp_ne_u32 vcc, 0xffff, v127\n"
"s_nop 1\n"
"v_cndmask_b32 v122, 0, v122, vcc\n"
"v_cmp_ne_u32 vcc, 0xffff0000, v127\n"
"s_nop 1\n"
"v_cndmask_b32 v123, 0, v123, vcc\n"
"v_cmp_gt_u32 vcc, 32, v1\n"
"v_mov_b32 v1, 0x7fff0000\n"
"s_nop 0\n"
"v_cndmask_b32 v1, v1, v2, vcc\n"
"s_mov_b32 s58, 0x4003c000\n"
"s_mov_b32 s59, 0x0\n"
"s_mov_b32 s60, 0x80028000\n"
"s_mov_b32 s61, 0x0\n"
"v_subrev_u32 v3, 148480, v11\n"
"v_xor_b32 v3, 1024, v3\n"
"v_add_u32 v3, 148480, v3\n"
"s_add_u32 s74, s66, s72\n"
"s_waitcnt vmcnt(25)\n"
"s_barrier\n"
"s_add_u32 m0, s97, 24576\n"
"s_nop 0\n"
"buffer_load_dwordx4 v67, s[36:39], s96 offen offset:0 lds\n"
"buffer_load_dwordx4 v67, s[40:43], s96 offen offset:1024 lds\n"
"buffer_load_dwordx4 v67, s[44:47], s96 offen offset:2048 lds\n"
"s_add_u32 s96, s96, 196608\n"
"s_add_u32 m0, s97, 36864\n"
"s_nop 0\n"
"buffer_load_dwordx4 v67, s[36:39], s96 offen offset:0 lds\n"
"buffer_load_dwordx4 v67, s[40:43], s96 offen offset:1024 lds\n"
"buffer_load_dwordx4 v67, s[44:47], s96 offen offset:2048 lds\n"
"s_add_u32 s96, s96, 196608\n"
"ds_read_b64_tr_b16 a[0:1], v69 offset:0\n"
"ds_read_b64_tr_b16 a[2:3], v69 offset:512\n"
"ds_read_b64_tr_b16 a[4:5], v69 offset:1024\n"
"ds_read_b64_tr_b16 a[6:7], v69 offset:1536\n"
"ds_read_b64_tr_b16 a[8:9], v72 offset:2048\n"
"ds_read_b64_tr_b16 a[10:11], v72 offset:2560\n"
"ds_read_b64_tr_b16 a[12:13], v69 offset:12288\n"
"ds_read_b64_tr_b16 a[14:15], v69 offset:12800\n"
"ds_read_b64_tr_b16 a[16:17], v69 offset:13312\n"
"ds_read_b64_tr_b16 a[18:19], v69 offset:13824\n"
"ds_read_b64_tr_b16 a[20:21], v72 offset:14336\n"
"ds_read_b64_tr_b16 a[22:23], v72 offset:14848\n"
"s_waitcnt lgkmcnt(0)\n"
"v_mfma_f32_16x16x32_bf16 v[20:23], a[0:3], v[16:19], 0\n"
"v_mfma_f32_16x16x32_bf16 v[24:27], a[4:7], v[16:19], 0\n"
"v_mfma_f32_16x16x32_bf16 v[32:35], v[16:19], a[8:11], 0\n"
"v_mfma_f32_16x16x32_bf16 v[20:23], a[12:15], v[16:19], v[20:23]\n"
"v_mfma_f32_16x16x32_bf16 v[24:27], a[16:19], v[16:19], v[24:27]\n"
"v_mfma_f32_16x16x32_bf16 v[32:35], v[16:19], a[20:23], v[32:35]\n"
"s_waitcnt vmcnt(25)\n"
"s_barrier\n"
"s_add_u32 m0, s97, 49152\n"
"s_nop 0\n"
"buffer_load_dwordx4 v67, s[36:39], s96 offen offset:0 lds\n"
"buffer_load_dwordx4 v67, s[40:43], s96 offen offset:1024 lds\n"
"buffer_load_dwordx4 v67, s[44:47], s96 offen offset:2048 lds\n"
"s_add_u32 s96, s96, 196608\n"
"s_add_u32 m0, s97, 61440\n"
"s_nop 0\n"
"buffer_load_dwordx4 v67, s[36:39], s96 offen offset:0 lds\n"
"buffer_load_dwordx4 v67, s[40:43], s96 offen offset:1024 lds\n"
"buffer_load_dwordx4 v67, s[44:47], s96 offen offset:2048 lds\n"
"s_add_u32 s96, s96, 196608\n"
"ds_read_b64_tr_b16 a[24:25], v69 offset:24576\n"
"ds_read_b64_tr_b16 a[26:27], v69 offset:25088\n"
"ds_read_b64_tr_b16 a[28:29], v69 offset:25600\n"
"ds_read_b64_tr_b16 a[30:31], v69 offset:26112\n"
"ds_read_b64_tr_b16 a[32:33], v72 offset:26624\n"
"ds_read_b64_tr_b16 a[34:35], v72 offset:27136\n"
"ds_read_b64_tr_b16 a[36:37], v69 offset:36864\n"
"ds_read_b64_tr_b16 a[38:39], v69 offset:37376\n"
"ds_read_b64_tr_b16 a[40:41], v69 offset:37888\n"
"ds_read_b64_tr_b16 a[42:43], v69 offset:38400\n"
"ds_read_b64_tr_b16 a[44:45], v72 offset:38912\n"
"ds_read_b64_tr_b16 a[46:47], v72 offset:39424\n"
"s_waitcnt lgkmcnt(0)\n"
"v_mfma_f32_16x16x32_bf16 v[20:23], a[24:27], v[16:19], v[20:23]\n"
"v_mfma_f32_16x16x32_bf16 v[24:27], a[28:31], v[16:19], v[24:27]\n"
"v_mfma_f32_16x16x32_bf16 v[32:35], v[16:19], a[32:35], v[32:35]\n"
"v_mfma_f32_16x16x32_bf16 v[20:23], a[36:39], v[16:19], v[20:23]\n"
"v_mfma_f32_16x16x32_bf16 v[24:27], a[40:43], v[16:19], v[24:27]\n"
"v_mfma_f32_16x16x32_bf16 v[32:35], v[16:19], a[44:47], v[32:35]\n"
"s_waitcnt vmcnt(25)\n"
"s_barrier\n"
"s_add_u32 m0, s97, 73728\n"
"s_nop 0\n"
"buffer_load_dwordx4 v67, s[36:39], s96 offen offset:0 lds\n"
"buffer_load_dwordx4 v67, s[40:43], s96 offen offset:1024 lds\n"
"buffer_load_dwordx4 v67, s[44:47], s96 offen offset:2048 lds\n"
"s_add_u32 s96, s96, 196608\n"
"s_add_u32 m0, s97, 86016\n"
"s_nop 0\n"
"buffer_load_dwordx4 v67, s[36:39], s96 offen offset:0 lds\n"
"buffer_load_dwordx4 v67, s[40:43], s96 offen offset:1024 lds\n"
"buffer_load_dwordx4 v67, s[44:47], s96 offen offset:2048 lds\n"
"s_add_u32 s96, s96, 196608\n"
"ds_read_b64_tr_b16 a[48:49], v70 offset:0\n"
"ds_read_b64_tr_b16 a[50:51], v70 offset:512\n"
"ds_read_b64_tr_b16 a[52:53], v70 offset:1024\n"
"ds_read_b64_tr_b16 a[54:55], v70 offset:1536\n"
"ds_read_b64_tr_b16 a[56:57], v73 offset:2048\n"
"ds_read_b64_tr_b16 a[58:59], v73 offset:2560\n"
"ds_read_b64_tr_b16 a[60:61], v70 offset:12288\n"
"ds_read_b64_tr_b16 a[62:63], v70 offset:12800\n"
"ds_read_b64_tr_b16 a[64:65], v70 offset:13312\n"
"ds_read_b64_tr_b16 a[66:67], v70 offset:13824\n"
"ds_read_b64_tr_b16 a[68:69], v73 offset:14336\n"
"ds_read_b64_tr_b16 a[70:71], v73 offset:14848\n"
"s_waitcnt lgkmcnt(0)\n"
"v_mfma_f32_16x16x32_bf16 v[20:23], a[48:51], v[16:19], v[20:23]\n"
"v_mfma_f32_16x16x32_bf16 v[24:27], a[52:55], v[16:19], v[24:27]\n"
"v_mfma_f32_16x16x32_bf16 v[32:35], v[16:19], a[56:59], v[32:35]\n"
"v_mfma_f32_16x16x32_bf16 v[20:23], a[60:63], v[16:19], v[20:23]\n"
"v_mfma_f32_16x16x32_bf16 v[24:27], a[64:67], v[16:19], v[24:27]\n"
"v_mfma_f32_16x16x32_bf16 v[32:35], v[16:19], a[68:71], v[32:35]\n"
"s_waitcnt vmcnt(25)\n"
"s_barrier\n"
"s_add_u32 m0, s97, 98304\n"
"s_nop 0\n"
"buffer_load_dwordx4 v67, s[36:39], s96 offen offset:0 lds\n"
"buffer_load_dwordx4 v67, s[40:43], s96 offen offset:1024 lds\n"
"buffer_load_dwordx4 v67, s[44:47], s96 offen offset:2048 lds\n"
"s_add_u32 s96, s96, 196608\n"
"s_add_u32 m0, s97, 110592\n"
"s_nop 0\n"
"buffer_load_dwordx4 v67, s[36:39], s96 offen offset:0 lds\n"
"buffer_load_dwordx4 v67, s[40:43], s96 offen offset:1024 lds\n"
"buffer_load_dwordx4 v67, s[44:47], s96 offen offset:2048 lds\n"
"s_add_u32 s96, s96, 196608\n"
"ds_read_b64_tr_b16 a[72:73], v70 offset:24576\n"
"ds_read_b64_tr_b16 a[74:75], v70 offset:25088\n"
"ds_read_b64_tr_b16 a[76:77], v70 offset:25600\n"
"ds_read_b64_tr_b16 a[78:79], v70 offset:26112\n"
"ds_read_b64_tr_b16 a[80:81], v73 offset:26624\n"
"ds_read_b64_tr_b16 a[82:83], v73 offset:27136\n"
"ds_read_b64_tr_b16 a[84:85], v70 offset:36864\n"
"ds_read_b64_tr_b16 a[86:87], v70 offset:37376\n"
"ds_read_b64_tr_b16 a[88:89], v70 offset:37888\n"
"ds_read_b64_tr_b16 a[90:91], v70 offset:38400\n"
"ds_read_b64_tr_b16 a[92:93], v73 offset:38912\n"
"ds_read_b64_tr_b16 a[94:95], v73 offset:39424\n"
"s_waitcnt lgkmcnt(0)\n"
"v_mfma_f32_16x16x32_bf16 v[20:23], a[72:75], v[16:19], v[20:23]\n"
"v_mfma_f32_16x16x32_bf16 v[24:27], a[76:79], v[16:19], v[24:27]\n"
"v_mfma_f32_16x16x32_bf16 v[32:35], v[16:19], a[80:83], v[32:35]\n"
"v_mfma_f32_16x16x32_bf16 v[20:23], a[84:87], v[16:19], v[20:23]\n"
"v_mfma_f32_16x16x32_bf16 v[24:27], a[88:91], v[16:19], v[24:27]\n"
"v_mfma_f32_16x16x32_bf16 v[32:35], v[16:19], a[92:95], v[32:35]\n"
"s_waitcnt vmcnt(25)\n"
"s_barrier\n"
"s_add_u32 m0, s97, 122880\n"
"s_nop 0\n"
"buffer_load_dwordx4 v67, s[36:39], s96 offen offset:0 lds\n"
"buffer_load_dwordx4 v67, s[40:43], s96 offen offset:1024 lds\n"
"buffer_load_dwordx4 v67, s[44:47], s96 offen offset:2048 lds\n"
"s_add_u32 s96, s96, 196608\n"
"s_add_u32 m0, s97, 135168\n"
"s_nop 0\n"
"buffer_load_dwordx4 v67, s[36:39], s96 offen offset:0 lds\n"
"buffer_load_dwordx4 v67, s[40:43], s96 offen offset:1024 lds\n"
"buffer_load_dwordx4 v67, s[44:47], s96 offen offset:2048 lds\n"
"s_add_u32 s96, s96, 196608\n"
"ds_read_b64_tr_b16 a[96:97], v68 offset:0\n"
"ds_read_b64_tr_b16 a[98:99], v68 offset:512\n"
"ds_read_b64_tr_b16 a[100:101], v68 offset:1024\n"
"ds_read_b64_tr_b16 a[102:103], v68 offset:1536\n"
"ds_read_b64_tr_b16 a[104:105], v71 offset:2048\n"
"ds_read_b64_tr_b16 a[106:107], v71 offset:2560\n"
"ds_read_b64_tr_b16 a[108:109], v68 offset:12288\n"
"ds_read_b64_tr_b16 a[110:111], v68 offset:12800\n"
"ds_read_b64_tr_b16 a[112:113], v68 offset:13312\n"
"ds_read_b64_tr_b16 a[114:115], v68 offset:13824\n"
"ds_read_b64_tr_b16 a[116:117], v71 offset:14336\n"
"ds_read_b64_tr_b16 a[118:119], v71 offset:14848\n"
"s_waitcnt lgkmcnt(0)\n"
"v_mfma_f32_16x16x32_bf16 v[20:23], a[96:99], v[16:19], v[20:23]\n"
"v_mfma_f32_16x16x32_bf16 v[24:27], a[100:103], v[16:19], v[24:27]\n"
"v_mfma_f32_16x16x32_bf16 v[32:35], v[16:19], a[104:107], v[32:35]\n"
"v_mfma_f32_16x16x32_bf16 v[20:23], a[108:111], v[16:19], v[20:23]\n"
"v_mfma_f32_16x16x32_bf16 v[24:27], a[112:115], v[16:19], v[24:27]\n"
"v_mfma_f32_16x16x32_bf16 v[32:35], v[16:19], a[116:119], v[32:35]\n"
"s_waitcnt vmcnt(24)\n"
"s_barrier\n"
"s_add_u32 m0, s97, 0\n"
"s_nop 0\n"
"buffer_load_dwordx4 v67, s[36:39], s96 offen offset:0 lds\n"
"buffer_load_dwordx4 v67, s[40:43], s96 offen offset:1024 lds\n"
"buffer_load_dwordx4 v67, s[44:47], s96 offen offset:2048 lds\n"
"s_add_u32 s96, s96, 196608\n"
"s_add_u32 m0, s97, 12288\n"
"s_nop 0\n"
"buffer_load_dwordx4 v67, s[36:39], s96 offen offset:0 lds\n"
"buffer_load_dwordx4 v67, s[40:43], s96 offen offset:1024 lds\n"
"buffer_load_dwordx4 v67, s[44:47], s96 offen offset:2048 lds\n"
"s_add_u32 s96, s96, 196608\n"
"ds_read_b64_tr_b16 a[120:121], v68 offset:24576\n"
"ds_read_b64_tr_b16 a[122:123], v68 offset:25088\n"
"ds_read_b64_tr_b16 a[124:125], v68 offset:25600\n"
"ds_read_b64_tr_b16 a[126:127], v68 offset:26112\n"
"ds_read_b64_tr_b16 a[128:129], v71 offset:26624\n"
"ds_read_b64_tr_b16 a[130:131], v71 offset:27136\n"
"ds_read_b64_tr_b16 a[132:133], v68 offset:36864\n"
"ds_read_b64_tr_b16 a[134:135], v68 offset:37376\n"
"ds_read_b64_tr_b16 a[136:137], v68 offset:37888\n"
"ds_read_b64_tr_b16 a[138:139], v68 offset:38400\n"
"ds_read_b64_tr_b16 a[140:141], v71 offset:38912\n"
"ds_read_b64_tr_b16 a[142:143], v71 offset:39424\n"
"s_waitcnt lgkmcnt(0)\n"
"v_mfma_f32_16x16x32_bf16 v[20:23], a[120:123], v[16:19], v[20:23]\n"
"v_mfma_f32_16x16x32_bf16 v[24:27], a[124:127], v[16:19], v[24:27]\n"
"v_mfma_f32_16x16x32_bf16 v[32:35], v[16:19], a[128:131], v[32:35]\n"
"v_mfma_f32_16x16x32_bf16 v[20:23], a[132:135], v[16:19], v[20:23]\n"
"v_mfma_f32_16x16x32_bf16 v[24:27], a[136:139], v[16:19], v[24:27]\n"
"v_mfma_f32_16x16x32_bf16 v[32:35], v[16:19], a[140:143], v[32:35]\n"
"s_waitcnt vmcnt(24)\n"
"s_barrier\n"
"s_add_u32 m0, s97, 24576\n"
"s_nop 0\n"
"buffer_load_dwordx4 v67, s[36:39], s96 offen offset:0 lds\n"
"buffer_load_dwordx4 v67, s[40:43], s96 offen offset:1024 lds\n"
"buffer_load_dwordx4 v67, s[44:47], s96 offen offset:2048 lds\n"
"s_add_u32 s96, s96, 196608\n"
"s_add_u32 m0, s97, 36864\n"
"s_nop 0\n"
"buffer_load_dwordx4 v67, s[36:39], s96 offen offset:0 lds\n"
"buffer_load_dwordx4 v67, s[40:43], s96 offen offset:1024 lds\n"
"buffer_load_dwordx4 v67, s[44:47], s96 offen offset:2048 lds\n"
"s_add_u32 s96, s96, 196608\n"
"ds_read_b64_tr_b16 a[144:145], v69 offset:0\n"
"ds_read_b64_tr_b16 a[146:147], v69 offset:512\n"
"ds_read_b64_tr_b16 a[148:149], v69 offset:1024\n"
"ds_read_b64_tr_b16 a[150:151], v69 offset:1536\n"
"ds_read_b64_tr_b16 a[152:153], v72 offset:2048\n"
"ds_read_b64_tr_b16 a[154:155], v72 offset:2560\n"
"ds_read_b64_tr_b16 a[156:157], v69 offset:12288\n"
"ds_read_b64_tr_b16 a[158:159], v69 offset:12800\n"
"ds_read_b64_tr_b16 a[160:161], v69 offset:13312\n"
"ds_read_b64_tr_b16 a[162:163], v69 offset:13824\n"
"ds_read_b64_tr_b16 a[164:165], v72 offset:14336\n"
"ds_read_b64_tr_b16 a[166:167], v72 offset:14848\n"
"s_waitcnt lgkmcnt(0)\n"
"v_mfma_f32_16x16x32_bf16 v[20:23], a[144:147], v[16:19], v[20:23]\n"
"v_mfma_f32_16x16x32_bf16 v[24:27], a[148:151], v[16:19], v[24:27]\n"
"v_mfma_f32_16x16x32_bf16 v[32:35], v[16:19], a[152:155], v[32:35]\n"
"v_mfma_f32_16x16x32_bf16 v[20:23], a[156:159], v[16:19], v[20:23]\n"
"v_mfma_f32_16x16x32_bf16 v[24:27], a[160:163], v[16:19], v[24:27]\n"
"v_mfma_f32_16x16x32_bf16 v[32:35], v[16:19], a[164:167], v[32:35]\n"
"s_waitcnt vmcnt(24)\n"
"s_barrier\n"
"s_add_u32 m0, s97, 49152\n"
"s_nop 0\n"
"buffer_load_dwordx4 v67, s[36:39], s96 offen offset:0 lds\n"
"buffer_load_dwordx4 v67, s[40:43], s96 offen offset:1024 lds\n"
"buffer_load_dwordx4 v67, s[44:47], s96 offen offset:2048 lds\n"
"s_add_u32 s96, s96, 196608\n"
"s_add_u32 m0, s97, 61440\n"
"s_nop 0\n"
"buffer_load_dwordx4 v67, s[36:39], s96 offen offset:0 lds\n"
"buffer_load_dwordx4 v67, s[40:43], s96 offen offset:1024 lds\n"
"buffer_load_dwordx4 v67, s[44:47], s96 offen offset:2048 lds\n"
"s_add_u32 s96, s96, 196608\n"
"ds_read_b64_tr_b16 a[168:169], v69 offset:24576\n"
"ds_read_b64_tr_b16 a[170:171], v69 offset:25088\n"
"ds_read_b64_tr_b16 a[172:173], v69 offset:25600\n"
"ds_read_b64_tr_b16 a[174:175], v69 offset:26112\n"
"ds_read_b64_tr_b16 a[176:177], v72 offset:26624\n"
"ds_read_b64_tr_b16 a[178:179], v72 offset:27136\n"
"ds_read_b64_tr_b16 a[180:181], v69 offset:36864\n"
"ds_read_b64_tr_b16 a[182:183], v69 offset:37376\n"
"ds_read_b64_tr_b16 a[184:185], v69 offset:37888\n"
"ds_read_b64_tr_b16 a[186:187], v69 offset:38400\n"
"ds_read_b64_tr_b16 a[188:189], v72 offset:38912\n"
"ds_read_b64_tr_b16 a[190:191], v72 offset:39424\n"
"s_waitcnt lgkmcnt(0)\n"
"v_mfma_f32_16x16x32_bf16 v[20:23], a[168:171], v[16:19], v[20:23]\n"
"v_mfma_f32_16x16x32_bf16 v[24:27], a[172:175], v[16:19], v[24:27]\n"
"v_mfma_f32_16x16x32_bf16 v[32:35], v[16:19], a[176:179], v[32:35]\n"
"v_mfma_f32_16x16x32_bf16 v[20:23], a[180:183], v[16:19], v[20:23]\n"
"v_mfma_f32_16x16x32_bf16 v[24:27], a[184:187], v[16:19], v[24:27]\n"
"v_mfma_f32_16x16x32_bf16 v[32:35], v[16:19], a[188:191], v[32:35]\n"
"s_waitcnt vmcnt(24)\n"
"s_barrier\n"
"s_add_u32 m0, s97, 73728\n"
"s_nop 0\n"
"buffer_load_dwordx4 v67, s[36:39], s96 offen offset:0 lds\n"
"buffer_load_dwordx4 v67, s[40:43], s96 offen offset:1024 lds\n"
"buffer_load_dwordx4 v67, s[44:47], s96 offen offset:2048 lds\n"
"s_add_u32 s96, s96, 196608\n"
"s_add_u32 m0, s97, 86016\n"
"s_nop 0\n"
"buffer_load_dwordx4 v67, s[36:39], s96 offen offset:0 lds\n"
"buffer_load_dwordx4 v67, s[40:43], s96 offen offset:1024 lds\n"
"buffer_load_dwordx4 v67, s[44:47], s96 offen offset:2048 lds\n"
"s_add_u32 s96, s96, 196608\n"
"ds_read_b64_tr_b16 a[192:193], v70 offset:0\n"
"ds_read_b64_tr_b16 a[194:195], v70 offset:512\n"
"ds_read_b64_tr_b16 a[196:197], v70 offset:1024\n"
"ds_read_b64_tr_b16 a[198:199], v70 offset:1536\n"
"ds_read_b64_tr_b16 a[200:201], v73 offset:2048\n"
"ds_read_b64_tr_b16 a[202:203], v73 offset:2560\n"
"ds_read_b64_tr_b16 a[204:205], v70 offset:12288\n"
"ds_read_b64_tr_b16 a[206:207], v70 offset:12800\n"
"ds_read_b64_tr_b16 a[208:209], v70 offset:13312\n"
"ds_read_b64_tr_b16 a[210:211], v70 offset:13824\n"
"ds_read_b64_tr_b16 a[212:213], v73 offset:14336\n"
"ds_read_b64_tr_b16 a[214:215], v73 offset:14848\n"
"s_waitcnt lgkmcnt(0)\n"
"v_mfma_f32_16x16x32_bf16 v[20:23], a[192:195], v[16:19], v[20:23]\n"
"v_mfma_f32_16x16x32_bf16 v[24:27], a[196:199], v[16:19], v[24:27]\n"
"v_mfma_f32_16x16x32_bf16 v[32:35], v[16:19], a[200:203], v[32:35]\n"
"v_mfma_f32_16x16x32_bf16 v[20:23], a[204:207], v[16:19], v[20:23]\n"
"v_mfma_f32_16x16x32_bf16 v[24:27], a[208:211], v[16:19], v[24:27]\n"
"v_mfma_f32_16x16x32_bf16 v[32:35], v[16:19], a[212:215], v[32:35]\n"
"s_waitcnt vmcnt(24)\n"
"s_barrier\n"
"s_add_u32 m0, s97, 98304\n"
"s_nop 0\n"
"buffer_load_dwordx4 v67, s[36:39], s96 offen offset:0 lds\n"
"buffer_load_dwordx4 v67, s[40:43], s96 offen offset:1024 lds\n"
"buffer_load_dwordx4 v67, s[44:47], s96 offen offset:2048 lds\n"
"s_add_u32 s96, s96, 196608\n"
"s_add_u32 m0, s97, 110592\n"
"s_nop 0\n"
"buffer_load_dwordx4 v67, s[36:39], s96 offen offset:0 lds\n"
"buffer_load_dwordx4 v67, s[40:43], s96 offen offset:1024 lds\n"
"buffer_load_dwordx4 v67, s[44:47], s96 offen offset:2048 lds\n"
"s_add_u32 s96, s96, 196608\n"
"buffer_load_dwordx2 v[76:77], v0, s[32:35], s66 offen sc1\n"
"buffer_load_dwordx2 v[78:79], v0, s[32:35], s74 offen sc1\n"
"ds_read_b64_tr_b16 a[216:217], v70 offset:24576\n"
"ds_read_b64_tr_b16 a[218:219], v70 offset:25088\n"
"ds_read_b64_tr_b16 a[220:221], v70 offset:25600\n"
"ds_read_b64_tr_b16 a[222:223], v70 offset:26112\n"
"ds_read_b64_tr_b16 a[224:225], v73 offset:26624\n"
"ds_read_b64_tr_b16 a[226:227], v73 offset:27136\n"
"ds_read_b64_tr_b16 a[228:229], v70 offset:36864\n"
"ds_read_b64_tr_b16 a[230:231], v70 offset:37376\n"
"ds_read_b64_tr_b16 a[232:233], v70 offset:37888\n"
"ds_read_b64_tr_b16 a[234:235], v70 offset:38400\n"
"ds_read_b64_tr_b16 a[236:237], v73 offset:38912\n"
"ds_read_b64_tr_b16 a[238:239], v73 offset:39424\n"
"s_waitcnt lgkmcnt(0)\n"
"v_mfma_f32_16x16x32_bf16 v[20:23], a[216:219], v[16:19], v[20:23]\n"
"v_mfma_f32_16x16x32_bf16 v[24:27], a[220:223], v[16:19], v[24:27]\n"
"v_mfma_f32_16x16x32_bf16 v[32:35], v[16:19], a[224:227], v[32:35]\n"
"v_mfma_f32_16x16x32_bf16 v[20:23], a[228:231], v[16:19], v[20:23]\n"
"v_mfma_f32_16x16x32_bf16 v[24:27], a[232:235], v[16:19], v[24:27]\n"
"v_mfma_f32_16x16x32_bf16 v[32:35], v[16:19], a[236:239], v[32:35]\n"
"s_waitcnt vmcnt(26)\n"
"s_barrier\n"
"s_add_u32 m0, s97, 122880\n"
"s_nop 0\n"
"buffer_load_dwordx4 v67, s[36:39], s96 offen offset:0 lds\n"
"buffer_load_dwordx4 v67, s[40:43], s96 offen offset:1024 lds\n"
"buffer_load_dwordx4 v67, s[44:47], s96 offen offset:2048 lds\n"
"s_add_u32 s96, s96, 196608\n"
"s_add_u32 m0, s97, 135168\n"
"s_nop 0\n"
"buffer_load_dwordx4 v67, s[36:39], s96 offen offset:0 lds\n"
"buffer_load_dwordx4 v67, s[40:43], s96 offen offset:1024 lds\n"
"buffer_load_dwordx4 v67, s[44:47], s96 offen offset:2048 lds\n"
"s_add_u32 s96, s96, 196608\n"
"ds_read_b64_tr_b16 a[240:241], v68 offset:0\n"
"ds_read_b64_tr_b16 a[242:243], v68 offset:512\n"
"ds_read_b64_tr_b16 a[244:245], v68 offset:1024\n"
"ds_read_b64_tr_b16 a[246:247], v68 offset:1536\n"
"ds_read_b64_tr_b16 a[248:249], v71 offset:2048\n"
"ds_read_b64_tr_b16 a[250:251], v71 offset:2560\n"
"ds_read_b64_tr_b16 a[252:253], v68 offset:12288\n"
"ds_read_b64_tr_b16 a[254:255], v68 offset:12800\n"
"ds_read_b64_tr_b16 v[128:129], v68 offset:13312\n"
"ds_read_b64_tr_b16 v[130:131], v68 offset:13824\n"
"ds_read_b64_tr_b16 v[132:133], v71 offset:14336\n"
"ds_read_b64_tr_b16 v[134:135], v71 offset:14848\n"
"s_waitcnt lgkmcnt(0)\n"
"v_mfma_f32_16x16x32_bf16 v[20:23], a[240:243], v[16:19], v[20:23]\n"
"v_mfma_f32_16x16x32_bf16 v[24:27], a[244:247], v[16:19], v[24:27]\n"
"v_mfma_f32_16x16x32_bf16 v[32:35], v[16:19], a[248:251], v[32:35]\n"
"v_mfma_f32_16x16x32_bf16 v[20:23], a[252:255], v[16:19], v[20:23]\n"
"v_mfma_f32_16x16x32_bf16 v[24:27], v[128:131], v[16:19], v[24:27]\n"
"v_mfma_f32_16x16x32_bf16 v[32:35], v[16:19], v[132:135], v[32:35]\n"
"s_waitcnt vmcnt(26)\n"
"s_barrier\n"
"ds_read_b64_tr_b16 v[136:137], v68 offset:24576\n"
"ds_read_b64_tr_b16 v[138:139], v68 offset:25088\n"
"ds_read_b64_tr_b16 v[140:141], v68 offset:25600\n"
"ds_read_b64_tr_b16 v[142:143], v68 offset:26112\n"
"ds_read_b64_tr_b16 v[144:145], v71 offset:26624\n"
"ds_read_b64_tr_b16 v[146:147], v71 offset:27136\n"
"ds_read_b64_tr_b16 v[148:149], v68 offset:36864\n"
"ds_read_b64_tr_b16 v[150:151], v68 offset:37376\n"
"ds_read_b64_tr_b16 v[152:153], v68 offset:37888\n"
"ds_read_b64_tr_b16 v[154:155], v68 offset:38400\n"
"ds_read_b64_tr_b16 v[156:157], v71 offset:38912\n"
"ds_read_b64_tr_b16 v[158:159], v71 offset:39424\n"
"s_waitcnt lgkmcnt(0)\n"
"v_mfma_f32_16x16x32_bf16 v[20:23], v[136:139], v[16:19], v[20:23]\n"
"v_mfma_f32_16x16x32_bf16 v[24:27], v[140:143], v[16:19], v[24:27]\n"
"v_mfma_f32_16x16x32_bf16 v[32:35], v[16:19], v[144:147], v[32:35]\n"
"v_mfma_f32_16x16x32_bf16 v[20:23], v[148:151], v[16:19], v[20:23]\n"
"v_mfma_f32_16x16x32_bf16 v[24:27], v[152:155], v[16:19], v[24:27]\n"
"v_mfma_f32_16x16x32_bf16 v[32:35], v[16:19], v[156:159], v[32:35]\n"
"s_waitcnt vmcnt(20)\n"
"s_barrier\n"
"ds_read_b64_tr_b16 v[160:161], v69 offset:0\n"
"ds_read_b64_tr_b16 v[162:163], v69 offset:512\n"
"ds_read_b64_tr_b16 v[164:165], v69 offset:1024\n"
"ds_read_b64_tr_b16 v[166:167], v69 offset:1536\n"
"ds_read_b64_tr_b16 v[168:169], v72 offset:2048\n"
"ds_read_b64_tr_b16 v[170:171], v72 offset:2560\n"
"ds_read_b64_tr_b16 v[172:173], v69 offset:12288\n"
"ds_read_b64_tr_b16 v[174:175], v69 offset:12800\n"
"ds_read_b64_tr_b16 v[176:177], v69 offset:13312\n"
"ds_read_b64_tr_b16 v[178:179], v69 offset:13824\n"
"ds_read_b64_tr_b16 v[180:181], v72 offset:14336\n"
"ds_read_b64_tr_b16 v[182:183], v72 offset:14848\n"
"s_waitcnt lgkmcnt(0)\n"
"v_mfma_f32_16x16x32_bf16 v[20:23], v[160:163], v[16:19], v[20:23]\n"
"v_mfma_f32_16x16x32_bf16 v[24:27], v[164:167], v[16:19], v[24:27]\n"
"v_mfma_f32_16x16x32_bf16 v[32:35], v[16:19], v[168:171], v[32:35]\n"
"v_mfma_f32_16x16x32_bf16 v[20:23], v[172:175], v[16:19], v[20:23]\n"
"v_mfma_f32_16x16x32_bf16 v[24:27], v[176:179], v[16:19], v[24:27]\n"
"v_mfma_f32_16x16x32_bf16 v[32:35], v[16:19], v[180:183], v[32:35]\n"
"s_waitcnt vmcnt(14)\n"
"s_barrier\n"
"ds_read_b64_tr_b16 v[184:185], v69 offset:24576\n"
"ds_read_b64_tr_b16 v[186:187], v69 offset:25088\n"
"ds_read_b64_tr_b16 v[188:189], v69 offset:25600\n"
"ds_read_b64_tr_b16 v[190:191], v69 offset:26112\n"
"ds_read_b64_tr_b16 v[192:193], v72 offset:26624\n"
"ds_read_b64_tr_b16 v[194:195], v72 offset:27136\n"
"ds_read_b64_tr_b16 v[196:197], v69 offset:36864\n"
"ds_read_b64_tr_b16 v[198:199], v69 offset:37376\n"
"ds_read_b64_tr_b16 v[200:201], v69 offset:37888\n"
"ds_read_b64_tr_b16 v[202:203], v69 offset:38400\n"
"ds_read_b64_tr_b16 v[204:205], v72 offset:38912\n"
"ds_read_b64_tr_b16 v[206:207], v72 offset:39424\n"
"s_waitcnt lgkmcnt(0)\n"
"v_mfma_f32_16x16x32_bf16 v[20:23], v[184:187], v[16:19], v[20:23]\n"
"v_mfma_f32_16x16x32_bf16 v[24:27], v[188:191], v[16:19], v[24:27]\n"
"v_mfma_f32_16x16x32_bf16 v[32:35], v[16:19], v[192:195], v[32:35]\n"
"v_mfma_f32_16x16x32_bf16 v[20:23], v[196:199], v[16:19], v[20:23]\n"
"v_mfma_f32_16x16x32_bf16 v[24:27], v[200:203], v[16:19], v[24:27]\n"
"v_mfma_f32_16x16x32_bf16 v[32:35], v[16:19], v[204:207], v[32:35]\n"
"s_waitcnt vmcnt(8)\n"
"s_barrier\n"
"s_waitcnt vmcnt(7)\n"
"v_cmp_ne_u64 vcc, v[30:31], v[76:77]\n"
"s_cbranch_vccnz .L_rtqa\n"
".L_bkqa:\n"
"s_add_u32 s63, s66, s71\n"
"s_add_u32 s66, s66, s72\n"
"s_add_u32 m0, s64, 0\n"
"s_nop 0\n"
"buffer_load_dwordx4 v2, s[32:35], s63 offen offset:0 sc1 lds\n"
"buffer_load_dwordx4 v2, s[32:35], s63 offen offset:1024 sc1 lds\n"
"buffer_load_dwordx4 v2, s[32:35], s63 offen offset:2048 sc1 lds\n"
"buffer_load_dwordx4 v2, s[32:35], s63 offen offset:3072 sc1 lds\n"
"s_add_u32 s63, s63, 4096\n"
"s_add_u32 m0, s64, 4096\n"
"s_nop 0\n"
"buffer_load_dwordx4 v2, s[32:35], s63 offen offset:0 sc1 lds\n"
"buffer_load_dwordx4 v2, s[32:35], s63 offen offset:1024 sc1 lds\n"
"buffer_load_dwordx4 v2, s[32:35], s63 offen offset:2048 sc1 lds\n"
"buffer_load_dwordx4 v2, s[32:35], s63 offen offset:3072 sc1 lds\n"
"s_add_u32 s63, s63, 4096\n"
"s_add_u32 m0, s64, 8192\n"
"s_nop 0\n"
"buffer_load_dwordx4 v2, s[32:35], s63 offen offset:0 sc1 lds\n"
"s_mov_b32 s72, 1187840\n"
"s_waitcnt vmcnt(15)\n"
"v_cmp_ne_u64 vcc, v[30:31], v[78:79]\n"
"s_cbranch_vccnz .L_rtqb\n"
".L_bkqb:\n"
"s_add_u32 s63, s66, s71\n"
"s_add_u32 s66, s66, s72\n"
"s_add_u32 m0, s64, 36864\n"
"s_nop 0\n"
"buffer_load_dwordx4 v2, s[32:35], s63 offen offset:0 sc1 lds\n"
"buffer_load_dwordx4 v2, s[32:35], s63 offen offset:1024 sc1 lds\n"
"buffer_load_dwordx4 v2, s[32:35], s63 offen offset:2048 sc1 lds\n"
"buffer_load_dwordx4 v2, s[32:35], s63 offen offset:3072 sc1 lds\n"
"s_add_u32 s63, s63, 4096\n"
"s_add_u32 m0, s64, 40960\n"
"s_nop 0\n"
"buffer_load_dwordx4 v2, s[32:35], s63 offen offset:0 sc1 lds\n"
"buffer_load_dwordx4 v2, s[32:35], s63 offen offset:1024 sc1 lds\n"
"buffer_load_dwordx4 v2, s[32:35], s63 offen offset:2048 sc1 lds\n"
"buffer_load_dwordx4 v2, s[32:35], s63 offen offset:3072 sc1 lds\n"
"s_add_u32 s63, s63, 4096\n"
"s_add_u32 m0, s64, 45056\n"
"s_nop 0\n"
"buffer_load_dwordx4 v2, s[32:35], s63 offen offset:0 sc1 lds\n"
"s_xor_b32 s64, s64, s65\n"
"buffer_load_dwordx2 v[12:13], v0, s[32:35], s66 offen sc1\n"
"ds_read_b64_tr_b16 v[208:209], v70 offset:0\n"
"ds_read_b64_tr_b16 v[210:211], v70 offset:512\n"
"ds_read_b64_tr_b16 v[212:213], v70 offset:1024\n"
"ds_read_b64_tr_b16 v[214:215], v70 offset:1536\n"
"ds_read_b64_tr_b16 v[216:217], v73 offset:2048\n"
"ds_read_b64_tr_b16 v[218:219], v73 offset:2560\n"
"ds_read_b64_tr_b16 v[220:221], v70 offset:12288\n"
"ds_read_b64_tr_b16 v[222:223], v70 offset:12800\n"
"ds_read_b64_tr_b16 v[224:225], v70 offset:13312\n"
"ds_read_b64_tr_b16 v[226:227], v70 offset:13824\n"
"ds_read_b64_tr_b16 v[228:229], v73 offset:14336\n"
"ds_read_b64_tr_b16 v[230:231], v73 offset:14848\n"
"s_waitcnt lgkmcnt(0)\n"
"v_mfma_f32_16x16x32_bf16 v[20:23], v[208:211], v[16:19], v[20:23]\n"
"v_mfma_f32_16x16x32_bf16 v[24:27], v[212:215], v[16:19], v[24:27]\n"
"v_mfma_f32_16x16x32_bf16 v[32:35], v[16:19], v[216:219], v[32:35]\n"
"v_mfma_f32_16x16x32_bf16 v[20:23], v[220:223], v[16:19], v[20:23]\n"
"v_mfma_f32_16x16x32_bf16 v[24:27], v[224:227], v[16:19], v[24:27]\n"
"v_mfma_f32_16x16x32_bf16 v[32:35], v[16:19], v[228:231], v[32:35]\n"
"s_waitcnt vmcnt(19)\n"
"s_barrier\n"
"ds_read_b64_tr_b16 v[232:233], v70 offset:24576\n"
"ds_read_b64_tr_b16 v[234:235], v70 offset:25088\n"
"ds_read_b64_tr_b16 v[236:237], v70 offset:25600\n"
"ds_read_b64_tr_b16 v[238:239], v70 offset:26112\n"
"ds_read_b64_tr_b16 v[240:241], v73 offset:26624\n"
"ds_read_b64_tr_b16 v[242:243], v73 offset:27136\n"
"ds_read_b64_tr_b16 v[244:245], v70 offset:36864\n"
"ds_read_b64_tr_b16 v[246:247], v70 offset:37376\n"
"ds_read_b64_tr_b16 v[248:249], v70 offset:37888\n"
"ds_read_b64_tr_b16 v[250:251], v70 offset:38400\n"
"ds_read_b64_tr_b16 v[252:253], v73 offset:38912\n"
"ds_read_b64_tr_b16 v[254:255], v73 offset:39424\n"
"s_waitcnt lgkmcnt(0)\n"
"v_mfma_f32_16x16x32_bf16 v[20:23], v[232:235], v[16:19], v[20:23]\n"
"v_mfma_f32_16x16x32_bf16 v[24:27], v[236:239], v[16:19], v[24:27]\n"
"v_mfma_f32_16x16x32_bf16 v[32:35], v[16:19], v[240:243], v[32:35]\n"
"v_mfma_f32_16x16x32_bf16 v[20:23], v[244:247], v[16:19], v[20:23]\n"
"v_mfma_f32_16x16x32_bf16 v[24:27], v[248:251], v[16:19], v[24:27]\n"
"v_mfma_f32_16x16x32_bf16 v[32:35], v[16:19], v[252:255], v[32:35]\n"
"s_nop 7\n"
"v_mov_b32 v28, v32\n"
"s_waitcnt vmcnt(0)\n"
"s_barrier\n"
"ds_read_b128 v[56:59], v4 offset:0\n"
"ds_read_b128 v[60:63], v4 offset:1152\n"
"ds_read_b128 v[64:67], v4 offset:2304\n"
"ds_read_b128 v[68:71], v4 offset:3456\n"
"ds_read_b128 v[72:75], v4 offset:4608\n"
"ds_read_b128 v[76:79], v4 offset:5760\n"
".p2align 3\n"
".L_ql:\n"
"s_bitcmp1_b64 s[58:59], s62\n"
"s_cbranch_scc1 .L_qs0\n"
".L_qb0:\n"
"s_waitcnt lgkmcnt(2)\n"
"v_mfma_f32_16x16x32_bf16 v[32:35], a[0:3], v[56:59], 0\n"
"ds_read_b128 v[80:83], v4 offset:6912\n"
"ds_read2_b32 v[14:15], v9 offset0:32 offset1:48\n"
"v_mfma_f32_16x16x32_bf16 v[36:39], a[4:7], v[56:59], 0\n"
"ds_read_b128 v[92:95], v10 offset:128\n"
"ds_read_b128 v[96:99], v10 offset:192\n"
"v_mfma_f32_16x16x32_bf16 v[40:43], v[56:59], a[8:11], 0\n"
"s_add_u32 s68, s68, 1187840\n"
"v_mfma_f32_16x16x32_bf16 v[40:43], v[60:63], a[20:23], v[40:43]\n"
"ds_read_b128 v[84:87], v4 offset:8064\n"
"v_mfma_f32_16x16x32_bf16 v[36:39], a[16:19], v[60:63], v[36:39]\n"
"ds_read2st64_b32 v[88:89], v6 offset0:0 offset1:36\n"
"v_mfma_f32_16x16x32_bf16 v[32:35], a[12:15], v[60:63], v[32:35]\n"
"s_waitcnt vmcnt(5)\n"
"v_cmp_ne_u64 vcc, v[30:31], v[12:13]\n"
"v_mfma_f32_16x16x32_bf16 v[32:35], a[24:27], v[64:67], v[32:35]\n"
"ds_read_b128 v[56:59], v4 offset:9216\n"
"ds_read2st64_b32 v[90:91], v6 offset0:72 offset1:108\n"
"v_mfma_f32_16x16x32_bf16 v[36:39], a[28:31], v[64:67], v[36:39]\n"
"s_cbranch_vccnz .L_rtq0\n"
".L_bkq0:\n"
"s_add_u32 s63, s66, s71\n"
"v_mfma_f32_16x16x32_bf16 v[40:43], v[64:67], a[32:35], v[40:43]\n"
"s_add_u32 s66, s66, s72\n"
"s_add_u32 m0, s64, 0\n"
"v_mfma_f32_16x16x32_bf16 v[40:43], v[68:71], a[44:47], v[40:43]\n"
"ds_read_b128 v[60:63], v4 offset:10368\n"
"buffer_load_dwordx4 v2, s[32:35], s63 offen offset:0 sc1 lds\n"
"v_mfma_f32_16x16x32_bf16 v[36:39], a[40:43], v[68:71], v[36:39]\n"
"s_waitcnt lgkmcnt(5)\n"
"v_mul_f32 v29, 0x3e8293ee, v15\n"
"v_mfma_f32_16x16x32_bf16 v[32:35], a[36:39], v[68:71], v[32:35]\n"
"v_fma_f32 v48, -v14, v24, v48\n"
"s_waitcnt lgkmcnt(4)\n"
"v_mfma_f32_16x16x32_bf16 v[32:35], a[48:51], v[72:75], v[32:35]\n"
"ds_read_b128 v[64:67], v4 offset:11520\n"
"v_fma_f32 v49, -v14, v25, v49\n"
"v_mfma_f32_16x16x32_bf16 v[36:39], a[52:55], v[72:75], v[36:39]\n"
"v_fma_f32 v50, -v14, v26, v50\n"
"v_fma_f32 v51, -v14, v27, v51\n"
"v_mfma_f32_16x16x32_bf16 v[40:43], v[72:75], a[56:59], v[40:43]\n"
"v_fma_f32 v44, -v14, v20, v44\n"
"v_fma_f32 v45, -v14, v21, v45\n"
"v_mfma_f32_16x16x32_bf16 v[40:43], v[76:79], a[68:71], v[40:43]\n"
"ds_read_b128 v[68:71], v4 offset:12672\n"
"v_fma_f32 v46, -v14, v22, v46\n"
"v_mfma_f32_16x16x32_bf16 v[36:39], a[64:67], v[76:79], v[36:39]\n"
"buffer_load_dwordx4 v2, s[32:35], s63 offen offset:1024 sc1 lds\n"
"v_fma_f32 v47, -v14, v23, v47\n"
"v_mfma_f32_16x16x32_bf16 v[32:35], a[60:63], v[76:79], v[32:35]\n"
"v_mul_f32 v48, v48, v15\n"
"v_mul_f32 v49, v49, v15\n"
"v_mfma_f32_16x16x32_bf16 v[32:35], a[72:75], v[80:83], v[32:35]\n"
"ds_read_b128 v[72:75], v4 offset:13824\n"
"v_mul_f32 v50, v50, v15\n"
"v_mfma_f32_16x16x32_bf16 v[36:39], a[76:79], v[80:83], v[36:39]\n"
"v_mul_f32 v51, v51, v15\n"
"v_mul_f32 v44, v44, v29\n"
"v_mfma_f32_16x16x32_bf16 v[40:43], v[80:83], a[80:83], v[40:43]\n"
"v_mul_f32 v45, v45, v29\n"
"v_mul_f32 v46, v46, v29\n"
"v_mfma_f32_16x16x32_bf16 v[40:43], v[84:87], a[92:95], v[40:43]\n"
"ds_read_b128 v[76:79], v4 offset:14976\n"
"v_mul_f32 v47, v47, v29\n"
"v_mfma_f32_16x16x32_bf16 v[36:39], a[88:91], v[84:87], v[36:39]\n"
"v_cvt_pk_bf16_f32 v108, v48, v49\n"
"v_cvt_pk_bf16_f32 v109, v50, v51\n"
"v_mfma_f32_16x16x32_bf16 v[32:35], a[84:87], v[84:87], v[32:35]\n"
"v_cvt_pk_bf16_f32 v100, v44, v45\n"
"s_waitcnt lgkmcnt(2)\n"
"v_mfma_f32_16x16x32_bf16 v[32:35], a[96:99], v[56:59], v[32:35]\n"
"ds_read_b128 v[80:83], v4 offset:16128\n"
"buffer_load_dwordx4 v2, s[32:35], s63 offen offset:2048 sc1 lds\n"
"v_mfma_f32_16x16x32_bf16 v[36:39], a[100:103], v[56:59], v[36:39]\n"
"v_cvt_pk_bf16_f32 v101, v46, v47\n"
"v_mfma_f32_16x16x32_bf16 v[40:43], v[56:59], a[104:107], v[40:43]\n"
"ds_write2st64_b64 v11, v[100:101], v[108:109] offset0:0 offset1:1\n"
"v_fma_f32 v52, -v92, v28, v52\n"
"v_mfma_f32_16x16x32_bf16 v[40:43], v[60:63], a[116:119], v[40:43]\n"
"ds_read_b128 v[84:87], v4 offset:17280\n"
"v_fma_f32 v53, -v93, v28, v53\n"
"v_mfma_f32_16x16x32_bf16 v[36:39], a[112:115], v[60:63], v[36:39]\n"
"v_fma_f32 v54, -v94, v28, v54\n"
"v_fma_f32 v55, -v95, v28, v55\n"
"v_mfma_f32_16x16x32_bf16 v[32:35], a[108:111], v[60:63], v[32:35]\n"
"v_mul_f32 v52, v52, v96\n"
"v_mul_f32 v53, v53, v97\n"
"v_mfma_f32_16x16x32_bf16 v[32:35], a[120:123], v[64:67], v[32:35]\n"
"ds_read_b128 v[56:59], v4 offset:18432\n"
"v_mul_f32 v54, v54, v98\n"
"v_mfma_f32_16x16x32_bf16 v[36:39], a[124:127], v[64:67], v[36:39]\n"
"v_mul_f32 v55, v55, v99\n"
"s_waitcnt lgkmcnt(2)\n"
"v_mfma_f32_16x16x32_bf16 v[40:43], v[64:67], a[128:131], v[40:43]\n"
"buffer_load_dwordx4 v2, s[32:35], s63 offen offset:3072 sc1 lds\n"
"s_barrier\n"
"v_mfma_f32_16x16x32_bf16 v[40:43], v[68:71], a[140:143], v[40:43]\n"
"ds_read_b128 v[60:63], v4 offset:19584\n"
"ds_read_b64 v[102:103], v3\n"
"v_mfma_f32_16x16x32_bf16 v[36:39], a[136:139], v[68:71], v[36:39]\n"
"ds_read_b64 v[110:111], v3 offset:512\n"
"v_cvt_pk_bf16_f32 v114, v52, v53\n"
"v_mfma_f32_16x16x32_bf16 v[32:35], a[132:135], v[68:71], v[32:35]\n"
"v_cvt_pk_bf16_f32 v115, v54, v55\n"
"s_waitcnt lgkmcnt(4)\n"
"v_mfma_f32_16x16x32_bf16 v[32:35], a[144:147], v[72:75], v[32:35]\n"
"ds_read_b128 v[64:67], v4 offset:20736\n"
"v_add_f32 v88, v88, v89\n"
"v_mfma_f32_16x16x32_bf16 v[36:39], a[148:151], v[72:75], v[36:39]\n"
"v_add_f32 v90, v90, v91\n"
"v_mfma_f32_16x16x32_bf16 v[40:43], v[72:75], a[152:155], v[40:43]\n"
"v_add_f32 v88, v88, v90\n"
"v_mfma_f32_16x16x32_bf16 v[40:43], v[76:79], a[164:167], v[40:43]\n"
"ds_read_b128 v[68:71], v4 offset:21888\n"
"s_add_u32 s63, s63, 4096\n"
"v_mfma_f32_16x16x32_bf16 v[36:39], a[160:163], v[76:79], v[36:39]\n"
"s_add_u32 m0, s64, 4096\n"
"s_waitcnt lgkmcnt(2)\n"
"v_mfma_f32_16x16x32_bf16 v[48:51], v[104:107], v[100:103], v[120:123]\n"
"buffer_load_dwordx4 v2, s[32:35], s63 offen offset:0 sc1 lds\n"
"v_add_f32_dpp v88, v88, v88 row_ror:8 row_mask:0xf bank_mask:0xf\n"
"v_mfma_f32_16x16x32_bf16 v[44:47], v[108:111], v[100:103], v[116:119]\n"
"v_mfma_f32_16x16x32_bf16 v[32:35], a[156:159], v[76:79], v[32:35]\n"
"v_add_f32_dpp v88, v88, v88 row_ror:4 row_mask:0xf bank_mask:0xf\n"
"v_mfma_f32_16x16x32_bf16 v[32:35], a[168:171], v[80:83], v[32:35]\n"
"ds_read_b128 v[72:75], v4 offset:23040\n"
"v_add_f32_dpp v88, v88, v88 row_ror:2 row_mask:0xf bank_mask:0xf\n"
"v_mfma_f32_16x16x32_bf16 v[36:39], a[172:175], v[80:83], v[36:39]\n"
"v_mul_f32 v88, 0x3a800000, v88\n"
"v_mfma_f32_16x16x32_bf16 v[40:43], v[80:83], a[176:179], v[40:43]\n"
"v_max3_f32 v44, v44, v45, v48\n"
"v_max3_f32 v46, v46, v47, v50\n"
"v_mfma_f32_16x16x32_bf16 v[40:43], v[84:87], a[188:191], v[40:43]\n"
"ds_read_b128 v[76:79], v4 offset:24192\n"
"v_max_f32 v44, v44, v49\n"
"v_mfma_f32_16x16x32_bf16 v[36:39], a[184:187], v[84:87], v[36:39]\n"
"v_max_f32 v46, v46, v51\n"
"v_mul_f32 v89, v88, v88\n"
"v_mfma_f32_16x16x32_bf16 v[32:35], a[180:183], v[84:87], v[32:35]\n"
"v_max_f32 v96, v44, v46\n"
"s_waitcnt lgkmcnt(2)\n"
"v_mfma_f32_16x16x32_bf16 v[32:35], a[192:195], v[56:59], v[32:35]\n"
"ds_read_b128 v[80:83], v4 offset:25344\n"
"buffer_load_dwordx4 v2, s[32:35], s63 offen offset:1024 sc1 lds\n"
"v_mfma_f32_16x16x32_bf16 v[36:39], a[196:199], v[56:59], v[36:39]\n"
"v_max_f32 v97, v44, v46\n"
"v_sub_f32_dpp v89, v88, v89 quad_perm:[1,1,3,3] row_mask:0xf bank_mask:0xf\n"
"v_mfma_f32_16x16x32_bf16 v[40:43], v[56:59], a[200:203], v[40:43]\n"
"buffer_load_dwordx2 v[12:13], v0, s[32:35], s66 offen sc1\n"
"v_max_f32 v89, 0, v89\n"
"v_mfma_f32_16x16x32_bf16 v[40:43], v[60:63], a[212:215], v[40:43]\n"
"ds_read_b128 v[84:87], v4 offset:26496\n"
"v_cvt_pk_bf16_f32 v89, 0, v89\n"
"v_mfma_f32_16x16x32_bf16 v[36:39], a[208:211], v[60:63], v[36:39]\n"
"v_permlane16_swap_b32 v96, v97\n"
"v_mfma_f32_16x16x32_bf16 v[32:35], a[204:207], v[60:63], v[32:35]\n"
"v_max_f32 v98, v96, v97\n"
"v_max_f32 v99, v96, v97\n"
"v_mfma_f32_16x16x32_bf16 v[32:35], a[216:219], v[64:67], v[32:35]\n"
"ds_read_b128 v[56:59], v4 offset:27648\n"
"v_add_f32 v89, 0x3727c5ac, v89\n"
"v_mfma_f32_16x16x32_bf16 v[36:39], a[220:223], v[64:67], v[36:39]\n"
"v_permlane32_swap_b32 v98, v99\n"
"v_mfma_f32_16x16x32_bf16 v[40:43], v[64:67], a[224:227], v[40:43]\n"
"buffer_load_dwordx4 v2, s[32:35], s63 offen offset:2048 sc1 lds\n"
"v_max_f32 v96, v98, v99\n"
"v_mfma_f32_16x16x32_bf16 v[40:43], v[68:71], a[236:239], v[40:43]\n"
"ds_read_b128 v[60:63], v4 offset:28800\n"
"v_sub_f32 v44, v44, v96\n"
"v_mfma_f32_16x16x32_bf16 v[36:39], a[232:235], v[68:71], v[36:39]\n"
"v_exp_f32 v44, v44\n"
"v_mfma_f32_16x16x32_bf16 v[32:35], a[228:231], v[68:71], v[32:35]\n"
"v_sub_f32 v46, v46, v96\n"
"s_waitcnt lgkmcnt(2)\n"
"v_mfma_f32_16x16x32_bf16 v[32:35], a[240:243], v[72:75], v[32:35]\n"
"ds_read_b128 v[64:67], v4 offset:29952\n"
"v_cvt_pk_bf16_f32 v44, v44, v44\n"
"v_mfma_f32_16x16x32_bf16 v[36:39], a[244:247], v[72:75], v[36:39]\n"
"v_exp_f32 v46, v46\n"
"v_mfma_f32_16x16x32_bf16 v[40:43], v[72:75], a[248:251], v[40:43]\n"
"v_cvt_pk_bf16_f32 v46, v46, v46\n"
"v_and_b32 v94, v44, v124\n"
"v_mfma_f32_16x16x32_bf16 v[40:43], v[76:79], v[132:135], v[40:43]\n"
"ds_read_b128 v[68:71], v4 offset:31104\n"
"v_and_b32 v92, v44, v126\n"
"v_mfma_f32_16x16x32_bf16 v[36:39], v[128:131], v[76:79], v[36:39]\n"
"buffer_load_dwordx4 v2, s[32:35], s63 offen offset:3072 sc1 lds\n"
"v_and_b32 v95, v46, v125\n"
"v_mfma_f32_16x16x32_bf16 v[32:35], a[252:255], v[76:79], v[32:35]\n"
"v_and_b32 v93, v46, v127\n"
"v_cvt_pk_bf16_f32 v89, 0, v89\n"
"v_mfma_f32_16x16x32_bf16 v[32:35], v[136:139], v[80:83], v[32:35]\n"
"ds_read_b128 v[72:75], v4 offset:32256\n"
"v_cvt_pk_bf16_f32 v88, 0, v88\n"
"v_mfma_f32_16x16x32_bf16 v[48:51], v[16:19], v[92:95], 0\n"
"v_sqrt_f32 v89, v89\n"
"v_mfma_f32_16x16x32_bf16 v[52:55], v[112:115], v[92:95], 0\n"
"v_cvt_pk_bf16_f32 v89, 0, v89\n"
"v_mfma_f32_16x16x32_bf16 v[36:39], v[140:143], v[80:83], v[36:39]\n"
"v_rcp_f32 v89, v89\n"
"v_mfma_f32_16x16x32_bf16 v[40:43], v[80:83], v[144:147], v[40:43]\n"
"ds_write2_b32 v8, v88, v89 offset0:0 offset1:16\n"
"v_mfma_f32_16x16x32_bf16 v[40:43], v[84:87], v[156:159], v[40:43]\n"
"ds_read_b128 v[76:79], v4 offset:33408\n"
"v_mfma_f32_16x16x32_bf16 v[36:39], v[152:155], v[84:87], v[36:39]\n"
"v_rcp_f32 v48, v48\n"
"v_mfma_f32_16x16x32_bf16 v[32:35], v[148:151], v[84:87], v[32:35]\n"
"s_add_u32 s63, s63, 4096\n"
"s_waitcnt lgkmcnt(3)\n"
"v_mfma_f32_16x16x32_bf16 v[32:35], v[160:163], v[56:59], v[32:35]\n"
"ds_read_b128 v[80:83], v4 offset:34560\n"
"s_add_u32 m0, s64, 8192\n"
"v_mfma_f32_16x16x32_bf16 v[36:39], v[164:167], v[56:59], v[36:39]\n"
"buffer_load_dwordx4 v2, s[32:35], s63 offen offset:0 sc1 lds\n"
"v_mul_f32 v52, v52, v48\n"
"v_mfma_f32_16x16x32_bf16 v[40:43], v[56:59], v[168:171], v[40:43]\n"
"v_mul_f32 v53, v53, v48\n"
"v_mul_f32 v54, v54, v48\n"
"v_mfma_f32_16x16x32_bf16 v[40:43], v[60:63], v[180:183], v[40:43]\n"
"ds_read_b128 v[84:87], v4 offset:35712\n"
"v_mul_f32 v55, v55, v48\n"
"v_mfma_f32_16x16x32_bf16 v[36:39], v[176:179], v[60:63], v[36:39]\n"
"v_xor_b32 v4, 73728, v4\n"
"v_cvt_pk_bf16_f32 v100, v52, v53\n"
"v_mfma_f32_16x16x32_bf16 v[32:35], v[172:175], v[60:63], v[32:35]\n"
"s_waitcnt vmcnt(10)\n"
"s_waitcnt lgkmcnt(3)\n"
"s_barrier\n"
"v_mfma_f32_16x16x32_bf16 v[32:35], v[184:187], v[64:67], v[32:35]\n"
"ds_read_b128 v[56:59], v5 offset:0\n"
"v_cvt_pk_bf16_f32 v101, v54, v55\n"
"v_mfma_f32_16x16x32_bf16 v[36:39], v[188:191], v[64:67], v[36:39]\n"
"buffer_store_dwordx2 v[30:31], v2, s[92:95], s67 idxen sc1\n"
"v_mfma_f32_16x16x32_bf16 v[40:43], v[64:67], v[192:195], v[40:43]\n"
"v_permlane32_swap_b32 v100, v102\n"
"v_mfma_f32_16x16x32_bf16 v[40:43], v[68:71], v[204:207], v[40:43]\n"
"ds_read_b128 v[60:63], v5 offset:1152\n"
"v_mfma_f32_16x16x32_bf16 v[36:39], v[200:203], v[68:71], v[36:39]\n"
"v_permlane32_swap_b32 v101, v103\n"
"v_mfma_f32_16x16x32_bf16 v[32:35], v[196:199], v[68:71], v[32:35]\n"
"buffer_store_dwordx4 v[100:103], v1, s[32:35], s68 offen sc1\n"
"s_waitcnt lgkmcnt(2)\n"
"v_mfma_f32_16x16x32_bf16 v[32:35], v[208:211], v[72:75], v[32:35]\n"
"ds_read_b128 v[64:67], v5 offset:2304\n"
"s_mov_b32 s67, s68\n"
"v_mfma_f32_16x16x32_bf16 v[36:39], v[212:215], v[72:75], v[36:39]\n"
"v_mfma_f32_16x16x32_bf16 v[40:43], v[72:75], v[216:219], v[40:43]\n"
"v_mfma_f32_16x16x32_bf16 v[40:43], v[76:79], v[228:231], v[40:43]\n"
"ds_read_b128 v[68:71], v5 offset:3456\n"
"v_mfma_f32_16x16x32_bf16 v[36:39], v[224:227], v[76:79], v[36:39]\n"
"v_mfma_f32_16x16x32_bf16 v[32:35], v[220:223], v[76:79], v[32:35]\n"
"v_mfma_f32_16x16x32_bf16 v[32:35], v[232:235], v[80:83], v[32:35]\n"
"ds_read_b128 v[72:75], v5 offset:4608\n"
"v_mfma_f32_16x16x32_bf16 v[36:39], v[236:239], v[80:83], v[36:39]\n"
"v_mfma_f32_16x16x32_bf16 v[40:43], v[80:83], v[240:243], v[40:43]\n"
"v_mfma_f32_16x16x32_bf16 v[40:43], v[84:87], v[252:255], v[40:43]\n"
"ds_read_b128 v[76:79], v5 offset:5760\n"
"v_mfma_f32_16x16x32_bf16 v[36:39], v[248:251], v[84:87], v[36:39]\n"
"v_mfma_f32_16x16x32_bf16 v[32:35], v[244:247], v[84:87], v[32:35]\n"
"s_bitcmp1_b64 s[60:61], s62\n"
"s_cbranch_scc1 .L_qs1\n"
".L_qb1:\n"
"s_waitcnt lgkmcnt(2)\n"
"v_mfma_f32_16x16x32_bf16 v[44:47], a[0:3], v[56:59], 0\n"
"ds_read_b128 v[80:83], v5 offset:6912\n"
"ds_read2_b32 v[14:15], v9 offset0:0 offset1:16\n"
"v_mfma_f32_16x16x32_bf16 v[48:51], a[4:7], v[56:59], 0\n"
"ds_read_b128 v[92:95], v10 offset:0\n"
"ds_read_b128 v[96:99], v10 offset:64\n"
"v_mfma_f32_16x16x32_bf16 v[52:55], v[56:59], a[8:11], 0\n"
"s_add_u32 s68, s68, 1187840\n"
"v_mfma_f32_16x16x32_bf16 v[52:55], v[60:63], a[20:23], v[52:55]\n"
"ds_read_b128 v[84:87], v5 offset:8064\n"
"v_mfma_f32_16x16x32_bf16 v[48:51], a[16:19], v[60:63], v[48:51]\n"
"ds_read2st64_b32 v[88:89], v6 offset0:144 offset1:180\n"
"v_mfma_f32_16x16x32_bf16 v[44:47], a[12:15], v[60:63], v[44:47]\n"
"s_waitcnt vmcnt(5)\n"
"v_cmp_ne_u64 vcc, v[30:31], v[12:13]\n"
"v_mfma_f32_16x16x32_bf16 v[44:47], a[24:27], v[64:67], v[44:47]\n"
"ds_read_b128 v[56:59], v5 offset:9216\n"
"ds_read2st64_b32 v[90:91], v6 offset0:216 offset1:252\n"
"v_mfma_f32_16x16x32_bf16 v[48:51], a[28:31], v[64:67], v[48:51]\n"
"s_cbranch_vccnz .L_rtq1\n"
".L_bkq1:\n"
"s_add_u32 s63, s66, s71\n"
"v_mfma_f32_16x16x32_bf16 v[52:55], v[64:67], a[32:35], v[52:55]\n"
"s_add_u32 s66, s66, s72\n"
"s_add_u32 m0, s64, 36864\n"
"v_mfma_f32_16x16x32_bf16 v[52:55], v[68:71], a[44:47], v[52:55]\n"
"ds_read_b128 v[60:63], v5 offset:10368\n"
"buffer_load_dwordx4 v2, s[32:35], s63 offen offset:0 sc1 lds\n"
"v_mfma_f32_16x16x32_bf16 v[48:51], a[40:43], v[68:71], v[48:51]\n"
"v_xor_b32 v6, v7, v6\n"
"s_waitcnt lgkmcnt(5)\n"
"v_mfma_f32_16x16x32_bf16 v[44:47], a[36:39], v[68:71], v[44:47]\n"
"v_mul_f32 v29, 0x3e8293ee, v15\n"
"s_waitcnt lgkmcnt(4)\n"
"v_mfma_f32_16x16x32_bf16 v[44:47], a[48:51], v[72:75], v[44:47]\n"
"ds_read_b128 v[64:67], v5 offset:11520\n"
"v_fma_f32 v36, -v14, v24, v36\n"
"v_mfma_f32_16x16x32_bf16 v[48:51], a[52:55], v[72:75], v[48:51]\n"
"v_fma_f32 v37, -v14, v25, v37\n"
"v_fma_f32 v38, -v14, v26, v38\n"
"v_mfma_f32_16x16x32_bf16 v[52:55], v[72:75], a[56:59], v[52:55]\n"
"v_fma_f32 v39, -v14, v27, v39\n"
"v_fma_f32 v32, -v14, v20, v32\n"
"v_mfma_f32_16x16x32_bf16 v[52:55], v[76:79], a[68:71], v[52:55]\n"
"ds_read_b128 v[68:71], v5 offset:12672\n"
"v_fma_f32 v33, -v14, v21, v33\n"
"v_mfma_f32_16x16x32_bf16 v[48:51], a[64:67], v[76:79], v[48:51]\n"
"buffer_load_dwordx4 v2, s[32:35], s63 offen offset:1024 sc1 lds\n"
"v_fma_f32 v34, -v14, v22, v34\n"
"v_mfma_f32_16x16x32_bf16 v[44:47], a[60:63], v[76:79], v[44:47]\n"
"v_fma_f32 v35, -v14, v23, v35\n"
"v_mul_f32 v36, v36, v15\n"
"v_mfma_f32_16x16x32_bf16 v[44:47], a[72:75], v[80:83], v[44:47]\n"
"ds_read_b128 v[72:75], v5 offset:13824\n"
"v_mul_f32 v37, v37, v15\n"
"v_mfma_f32_16x16x32_bf16 v[48:51], a[76:79], v[80:83], v[48:51]\n"
"v_mul_f32 v38, v38, v15\n"
"v_mul_f32 v39, v39, v15\n"
"v_mfma_f32_16x16x32_bf16 v[52:55], v[80:83], a[80:83], v[52:55]\n"
"v_mul_f32 v32, v32, v29\n"
"v_mul_f32 v33, v33, v29\n"
"v_mfma_f32_16x16x32_bf16 v[52:55], v[84:87], a[92:95], v[52:55]\n"
"ds_read_b128 v[76:79], v5 offset:14976\n"
"v_mul_f32 v34, v34, v29\n"
"v_mfma_f32_16x16x32_bf16 v[48:51], a[88:91], v[84:87], v[48:51]\n"
"v_mul_f32 v35, v35, v29\n"
"v_cvt_pk_bf16_f32 v104, v36, v37\n"
"v_mfma_f32_16x16x32_bf16 v[44:47], a[84:87], v[84:87], v[44:47]\n"
"v_cvt_pk_bf16_f32 v105, v38, v39\n"
"s_waitcnt lgkmcnt(2)\n"
"v_mfma_f32_16x16x32_bf16 v[44:47], a[96:99], v[56:59], v[44:47]\n"
"ds_read_b128 v[80:83], v5 offset:16128\n"
"buffer_load_dwordx4 v2, s[32:35], s63 offen offset:2048 sc1 lds\n"
"v_mfma_f32_16x16x32_bf16 v[48:51], a[100:103], v[56:59], v[48:51]\n"
"v_cvt_pk_bf16_f32 v100, v32, v33\n"
"v_cvt_pk_bf16_f32 v101, v34, v35\n"
"v_mfma_f32_16x16x32_bf16 v[52:55], v[56:59], a[104:107], v[52:55]\n"
"ds_write2st64_b64 v11, v[100:101], v[104:105] offset0:0 offset1:1\n"
"v_fma_f32 v40, -v92, v28, v40\n"
"v_mfma_f32_16x16x32_bf16 v[52:55], v[60:63], a[116:119], v[52:55]\n"
"ds_read_b128 v[84:87], v5 offset:17280\n"
"v_fma_f32 v41, -v93, v28, v41\n"
"v_mfma_f32_16x16x32_bf16 v[48:51], a[112:115], v[60:63], v[48:51]\n"
"v_fma_f32 v42, -v94, v28, v42\n"
"v_fma_f32 v43, -v95, v28, v43\n"
"v_mfma_f32_16x16x32_bf16 v[44:47], a[108:111], v[60:63], v[44:47]\n"
"v_mul_f32 v40, v40, v96\n"
"v_mul_f32 v41, v41, v97\n"
"v_mfma_f32_16x16x32_bf16 v[44:47], a[120:123], v[64:67], v[44:47]\n"
"ds_read_b128 v[56:59], v5 offset:18432\n"
"v_mul_f32 v42, v42, v98\n"
"v_mfma_f32_16x16x32_bf16 v[48:51], a[124:127], v[64:67], v[48:51]\n"
"v_mul_f32 v43, v43, v99\n"
"s_waitcnt lgkmcnt(2)\n"
"v_mfma_f32_16x16x32_bf16 v[52:55], v[64:67], a[128:131], v[52:55]\n"
"buffer_load_dwordx4 v2, s[32:35], s63 offen offset:3072 sc1 lds\n"
"s_barrier\n"
"v_mfma_f32_16x16x32_bf16 v[52:55], v[68:71], a[140:143], v[52:55]\n"
"ds_read_b128 v[60:63], v5 offset:19584\n"
"ds_read_b64 v[102:103], v3\n"
"v_mfma_f32_16x16x32_bf16 v[48:51], a[136:139], v[68:71], v[48:51]\n"
"ds_read_b64 v[106:107], v3 offset:512\n"
"v_cvt_pk_bf16_f32 v112, v40, v41\n"
"v_mfma_f32_16x16x32_bf16 v[44:47], a[132:135], v[68:71], v[44:47]\n"
"v_cvt_pk_bf16_f32 v113, v42, v43\n"
"s_waitcnt lgkmcnt(4)\n"
"v_mfma_f32_16x16x32_bf16 v[44:47], a[144:147], v[72:75], v[44:47]\n"
"ds_read_b128 v[64:67], v5 offset:20736\n"
"v_add_f32 v88, v88, v89\n"
"v_mfma_f32_16x16x32_bf16 v[48:51], a[148:151], v[72:75], v[48:51]\n"
"v_add_f32 v90, v90, v91\n"
"v_mfma_f32_16x16x32_bf16 v[52:55], v[72:75], a[152:155], v[52:55]\n"
"v_add_f32 v88, v88, v90\n"
"v_mfma_f32_16x16x32_bf16 v[52:55], v[76:79], a[164:167], v[52:55]\n"
"ds_read_b128 v[68:71], v5 offset:21888\n"
"s_add_u32 s63, s63, 4096\n"
"v_mfma_f32_16x16x32_bf16 v[48:51], a[160:163], v[76:79], v[48:51]\n"
"s_add_u32 m0, s64, 40960\n"
"s_waitcnt lgkmcnt(2)\n"
"v_mfma_f32_16x16x32_bf16 v[36:39], v[108:111], v[100:103], v[120:123]\n"
"buffer_load_dwordx4 v2, s[32:35], s63 offen offset:0 sc1 lds\n"
"v_add_f32_dpp v88, v88, v88 row_ror:8 row_mask:0xf bank_mask:0xf\n"
"v_mfma_f32_16x16x32_bf16 v[32:35], v[104:107], v[100:103], v[116:119]\n"
"v_mfma_f32_16x16x32_bf16 v[44:47], a[156:159], v[76:79], v[44:47]\n"
"v_add_f32_dpp v88, v88, v88 row_ror:4 row_mask:0xf bank_mask:0xf\n"
"v_mfma_f32_16x16x32_bf16 v[44:47], a[168:171], v[80:83], v[44:47]\n"
"ds_read_b128 v[72:75], v5 offset:23040\n"
"v_add_f32_dpp v88, v88, v88 row_ror:2 row_mask:0xf bank_mask:0xf\n"
"v_mfma_f32_16x16x32_bf16 v[48:51], a[172:175], v[80:83], v[48:51]\n"
"v_mul_f32 v88, 0x3a800000, v88\n"
"v_mfma_f32_16x16x32_bf16 v[52:55], v[80:83], a[176:179], v[52:55]\n"
"v_max3_f32 v32, v32, v33, v36\n"
"v_max3_f32 v34, v34, v35, v38\n"
"v_mfma_f32_16x16x32_bf16 v[52:55], v[84:87], a[188:191], v[52:55]\n"
"ds_read_b128 v[76:79], v5 offset:24192\n"
"v_max_f32 v32, v32, v37\n"
"v_mfma_f32_16x16x32_bf16 v[48:51], a[184:187], v[84:87], v[48:51]\n"
"v_max_f32 v34, v34, v39\n"
"v_mul_f32 v89, v88, v88\n"
"v_mfma_f32_16x16x32_bf16 v[44:47], a[180:183], v[84:87], v[44:47]\n"
"v_max_f32 v96, v32, v34\n"
"s_waitcnt lgkmcnt(2)\n"
"v_mfma_f32_16x16x32_bf16 v[44:47], a[192:195], v[56:59], v[44:47]\n"
"ds_read_b128 v[80:83], v5 offset:25344\n"
"buffer_load_dwordx4 v2, s[32:35], s63 offen offset:1024 sc1 lds\n"
"v_mfma_f32_16x16x32_bf16 v[48:51], a[196:199], v[56:59], v[48:51]\n"
"v_max_f32 v97, v32, v34\n"
"v_sub_f32_dpp v89, v88, v89 quad_perm:[1,1,3,3] row_mask:0xf bank_mask:0xf\n"
"v_mfma_f32_16x16x32_bf16 v[52:55], v[56:59], a[200:203], v[52:55]\n"
"buffer_load_dwordx2 v[12:13], v0, s[32:35], s66 offen sc1\n"
"v_max_f32 v89, 0, v89\n"
"v_mfma_f32_16x16x32_bf16 v[52:55], v[60:63], a[212:215], v[52:55]\n"
"ds_read_b128 v[84:87], v5 offset:26496\n"
"v_cvt_pk_bf16_f32 v89, 0, v89\n"
"v_mfma_f32_16x16x32_bf16 v[48:51], a[208:211], v[60:63], v[48:51]\n"
"v_permlane16_swap_b32 v96, v97\n"
"v_mfma_f32_16x16x32_bf16 v[44:47], a[204:207], v[60:63], v[44:47]\n"
"v_max_f32 v98, v96, v97\n"
"v_max_f32 v99, v96, v97\n"
"v_mfma_f32_16x16x32_bf16 v[44:47], a[216:219], v[64:67], v[44:47]\n"
"ds_read_b128 v[56:59], v5 offset:27648\n"
"v_add_f32 v89, 0x3727c5ac, v89\n"
"v_mfma_f32_16x16x32_bf16 v[48:51], a[220:223], v[64:67], v[48:51]\n"
"v_permlane32_swap_b32 v98, v99\n"
"v_mfma_f32_16x16x32_bf16 v[52:55], v[64:67], a[224:227], v[52:55]\n"
"buffer_load_dwordx4 v2, s[32:35], s63 offen offset:2048 sc1 lds\n"
"v_max_f32 v96, v98, v99\n"
"v_mfma_f32_16x16x32_bf16 v[52:55], v[68:71], a[236:239], v[52:55]\n"
"ds_read_b128 v[60:63], v5 offset:28800\n"
"v_sub_f32 v32, v32, v96\n"
"v_mfma_f32_16x16x32_bf16 v[48:51], a[232:235], v[68:71], v[48:51]\n"
"v_exp_f32 v32, v32\n"
"v_mfma_f32_16x16x32_bf16 v[44:47], a[228:231], v[68:71], v[44:47]\n"
"v_sub_f32 v34, v34, v96\n"
"s_waitcnt lgkmcnt(2)\n"
"v_mfma_f32_16x16x32_bf16 v[44:47], a[240:243], v[72:75], v[44:47]\n"
"ds_read_b128 v[64:67], v5 offset:29952\n"
"v_cvt_pk_bf16_f32 v32, v32, v32\n"
"v_mfma_f32_16x16x32_bf16 v[48:51], a[244:247], v[72:75], v[48:51]\n"
"v_exp_f32 v34, v34\n"
"v_mfma_f32_16x16x32_bf16 v[52:55], v[72:75], a[248:251], v[52:55]\n"
"v_cvt_pk_bf16_f32 v34, v34, v34\n"
"v_and_b32 v92, v32, v124\n"
"v_mfma_f32_16x16x32_bf16 v[52:55], v[76:79], v[132:135], v[52:55]\n"
"ds_read_b128 v[68:71], v5 offset:31104\n"
"v_and_b32 v94, v32, v126\n"
"v_mfma_f32_16x16x32_bf16 v[48:51], v[128:131], v[76:79], v[48:51]\n"
"buffer_load_dwordx4 v2, s[32:35], s63 offen offset:3072 sc1 lds\n"
"v_and_b32 v93, v34, v125\n"
"v_mfma_f32_16x16x32_bf16 v[44:47], a[252:255], v[76:79], v[44:47]\n"
"v_and_b32 v95, v34, v127\n"
"v_cvt_pk_bf16_f32 v89, 0, v89\n"
"v_mfma_f32_16x16x32_bf16 v[44:47], v[136:139], v[80:83], v[44:47]\n"
"ds_read_b128 v[72:75], v5 offset:32256\n"
"v_cvt_pk_bf16_f32 v88, 0, v88\n"
"v_mfma_f32_16x16x32_bf16 v[36:39], v[16:19], v[92:95], 0\n"
"v_sqrt_f32 v89, v89\n"
"v_mfma_f32_16x16x32_bf16 v[40:43], v[112:115], v[92:95], 0\n"
"v_cvt_pk_bf16_f32 v89, 0, v89\n"
"v_mfma_f32_16x16x32_bf16 v[48:51], v[140:143], v[80:83], v[48:51]\n"
"v_rcp_f32 v89, v89\n"
"v_mfma_f32_16x16x32_bf16 v[52:55], v[80:83], v[144:147], v[52:55]\n"
"ds_write2_b32 v8, v88, v89 offset0:32 offset1:48\n"
"v_mfma_f32_16x16x32_bf16 v[52:55], v[84:87], v[156:159], v[52:55]\n"
"ds_read_b128 v[76:79], v5 offset:33408\n"
"v_mfma_f32_16x16x32_bf16 v[48:51], v[152:155], v[84:87], v[48:51]\n"
"v_rcp_f32 v36, v36\n"
"v_mfma_f32_16x16x32_bf16 v[44:47], v[148:151], v[84:87], v[44:47]\n"
"s_add_u32 s63, s63, 4096\n"
"s_waitcnt lgkmcnt(3)\n"
"v_mfma_f32_16x16x32_bf16 v[44:47], v[160:163], v[56:59], v[44:47]\n"
"ds_read_b128 v[80:83], v5 offset:34560\n"
"s_add_u32 m0, s64, 45056\n"
"v_mfma_f32_16x16x32_bf16 v[48:51], v[164:167], v[56:59], v[48:51]\n"
"buffer_load_dwordx4 v2, s[32:35], s63 offen offset:0 sc1 lds\n"
"s_xor_b32 s64, s64, s65\n"
"v_mfma_f32_16x16x32_bf16 v[52:55], v[56:59], v[168:171], v[52:55]\n"
"v_mul_f32 v40, v40, v36\n"
"v_mul_f32 v41, v41, v36\n"
"v_mfma_f32_16x16x32_bf16 v[52:55], v[60:63], v[180:183], v[52:55]\n"
"ds_read_b128 v[84:87], v5 offset:35712\n"
"v_mul_f32 v42, v42, v36\n"
"v_mfma_f32_16x16x32_bf16 v[48:51], v[176:179], v[60:63], v[48:51]\n"
"v_xor_b32 v5, 73728, v5\n"
"v_mul_f32 v43, v43, v36\n"
"v_mfma_f32_16x16x32_bf16 v[44:47], v[172:175], v[60:63], v[44:47]\n"
"s_waitcnt vmcnt(10)\n"
"s_waitcnt lgkmcnt(3)\n"
"s_barrier\n"
"v_mfma_f32_16x16x32_bf16 v[44:47], v[184:187], v[64:67], v[44:47]\n"
"ds_read_b128 v[56:59], v4 offset:0\n"
"v_cvt_pk_bf16_f32 v100, v40, v41\n"
"v_mfma_f32_16x16x32_bf16 v[48:51], v[188:191], v[64:67], v[48:51]\n"
"buffer_store_dwordx2 v[30:31], v2, s[92:95], s67 idxen sc1\n"
"v_cvt_pk_bf16_f32 v101, v42, v43\n"
"v_mfma_f32_16x16x32_bf16 v[52:55], v[64:67], v[192:195], v[52:55]\n"
"v_permlane32_swap_b32 v100, v102\n"
"v_mfma_f32_16x16x32_bf16 v[52:55], v[68:71], v[204:207], v[52:55]\n"
"ds_read_b128 v[60:63], v4 offset:1152\n"
"v_mfma_f32_16x16x32_bf16 v[48:51], v[200:203], v[68:71], v[48:51]\n"
"v_permlane32_swap_b32 v101, v103\n"
"v_mfma_f32_16x16x32_bf16 v[44:47], v[196:199], v[68:71], v[44:47]\n"
"buffer_store_dwordx4 v[100:103], v1, s[32:35], s68 offen sc1\n"
"s_waitcnt lgkmcnt(2)\n"
"v_mfma_f32_16x16x32_bf16 v[44:47], v[208:211], v[72:75], v[44:47]\n"
"ds_read_b128 v[64:67], v4 offset:2304\n"
"s_mov_b32 s67, s68\n"
"v_mfma_f32_16x16x32_bf16 v[48:51], v[212:215], v[72:75], v[48:51]\n"
"v_mfma_f32_16x16x32_bf16 v[52:55], v[72:75], v[216:219], v[52:55]\n"
"v_mfma_f32_16x16x32_bf16 v[52:55], v[76:79], v[228:231], v[52:55]\n"
"ds_read_b128 v[68:71], v4 offset:3456\n"
"v_mfma_f32_16x16x32_bf16 v[48:51], v[224:227], v[76:79], v[48:51]\n"
"v_mfma_f32_16x16x32_bf16 v[44:47], v[220:223], v[76:79], v[44:47]\n"
"v_mfma_f32_16x16x32_bf16 v[44:47], v[232:235], v[80:83], v[44:47]\n"
"ds_read_b128 v[72:75], v4 offset:4608\n"
"v_mfma_f32_16x16x32_bf16 v[48:51], v[236:239], v[80:83], v[48:51]\n"
"v_mfma_f32_16x16x32_bf16 v[52:55], v[80:83], v[240:243], v[52:55]\n"
"v_mfma_f32_16x16x32_bf16 v[52:55], v[84:87], v[252:255], v[52:55]\n"
"ds_read_b128 v[76:79], v4 offset:5760\n"
"v_mfma_f32_16x16x32_bf16 v[48:51], v[248:251], v[84:87], v[48:51]\n"
"s_sub_u32 s62, s62, 1\n"
"v_mfma_f32_16x16x32_bf16 v[44:47], v[244:247], v[84:87], v[44:47]\n"
"s_cbranch_scc0 .L_ql\n"
"s_nop 1\n"
"ds_read2_b32 v[14:15], v9 offset0:32 offset1:48\n"
"ds_read_b128 v[92:95], v10 offset:128\n"
"ds_read_b128 v[96:99], v10 offset:192\n"
"s_add_u32 s68, s68, 1187840\n"
"s_waitcnt lgkmcnt(0)\n"
"v_mul_f32 v29, 0x3e8293ee, v15\n"
"v_fma_f32 v48, -v14, v24, v48\n"
"v_fma_f32 v49, -v14, v25, v49\n"
"v_fma_f32 v50, -v14, v26, v50\n"
"v_fma_f32 v51, -v14, v27, v51\n"
"v_fma_f32 v44, -v14, v20, v44\n"
"v_fma_f32 v45, -v14, v21, v45\n"
"v_fma_f32 v46, -v14, v22, v46\n"
"v_fma_f32 v47, -v14, v23, v47\n"
"v_mul_f32 v48, v48, v15\n"
"v_mul_f32 v49, v49, v15\n"
"v_mul_f32 v50, v50, v15\n"
"v_mul_f32 v51, v51, v15\n"
"v_mul_f32 v44, v44, v29\n"
"v_mul_f32 v45, v45, v29\n"
"v_mul_f32 v46, v46, v29\n"
"v_mul_f32 v47, v47, v29\n"
"v_cvt_pk_bf16_f32 v108, v48, v49\n"
"v_cvt_pk_bf16_f32 v109, v50, v51\n"
"v_cvt_pk_bf16_f32 v100, v44, v45\n"
"v_cvt_pk_bf16_f32 v101, v46, v47\n"
"ds_write2st64_b64 v11, v[100:101], v[108:109] offset0:0 offset1:1\n"
"v_fma_f32 v52, -v92, v28, v52\n"
"v_fma_f32 v53, -v93, v28, v53\n"
"v_fma_f32 v54, -v94, v28, v54\n"
"v_fma_f32 v55, -v95, v28, v55\n"
"v_mul_f32 v52, v52, v96\n"
"v_mul_f32 v53, v53, v97\n"
"v_mul_f32 v54, v54, v98\n"
"v_mul_f32 v55, v55, v99\n"
"s_waitcnt lgkmcnt(0)\n"
"s_barrier\n"
"ds_read_b64 v[102:103], v3\n"
"ds_read_b64 v[110:111], v3 offset:512\n"
"v_cvt_pk_bf16_f32 v114, v52, v53\n"
"v_cvt_pk_bf16_f32 v115, v54, v55\n"
"s_waitcnt lgkmcnt(0)\n"
"v_mfma_f32_16x16x32_bf16 v[48:51], v[104:107], v[100:103], v[120:123]\n"
"v_mfma_f32_16x16x32_bf16 v[44:47], v[108:111], v[100:103], v[116:119]\n"
"s_nop 7\n"
"v_max3_f32 v44, v44, v45, v48\n"
"v_max3_f32 v46, v46, v47, v50\n"
"v_max_f32 v44, v44, v49\n"
"v_max_f32 v46, v46, v51\n"
"v_max_f32 v96, v44, v46\n"
"v_max_f32 v97, v44, v46\n"
"s_nop 1\n"
"v_permlane16_swap_b32 v96, v97\n"
"v_max_f32 v98, v96, v97\n"
"v_max_f32 v99, v96, v97\n"
"s_nop 1\n"
"v_permlane32_swap_b32 v98, v99\n"
"v_max_f32 v96, v98, v99\n"
"v_sub_f32 v44, v44, v96\n"
"v_sub_f32 v46, v46, v96\n"
"v_exp_f32 v44, v44\n"
"v_exp_f32 v46, v46\n"
"v_cvt_pk_bf16_f32 v44, v44, v44\n"
"v_cvt_pk_bf16_f32 v46, v46, v46\n"
"v_and_b32 v94, v44, v124\n"
"v_and_b32 v92, v44, v126\n"
"v_and_b32 v95, v46, v125\n"
"v_and_b32 v93, v46, v127\n"
"s_nop 1\n"
"v_mfma_f32_16x16x32_bf16 v[48:51], v[16:19], v[92:95], 0\n"
"v_mfma_f32_16x16x32_bf16 v[52:55], v[112:115], v[92:95], 0\n"
"s_nop 6\n"
"v_rcp_f32 v48, v48\n"
"s_nop 0\n"
"v_mul_f32 v52, v52, v48\n"
"v_mul_f32 v53, v53, v48\n"
"v_mul_f32 v54, v54, v48\n"
"v_mul_f32 v55, v55, v48\n"
"v_cvt_pk_bf16_f32 v100, v52, v53\n"
"v_cvt_pk_bf16_f32 v101, v54, v55\n"
"s_nop 0\n"
"v_permlane32_swap_b32 v100, v102\n"
"v_permlane32_swap_b32 v101, v103\n"
"buffer_store_dwordx4 v[100:103], v1, s[32:35], s68 offen sc1\n"
"s_waitcnt vmcnt(1)\n"
"s_barrier\n"
"buffer_store_dwordx2 v[30:31], v2, s[92:95], s67 idxen sc1\n"
"s_mov_b32 s67, s68\n"
"s_waitcnt vmcnt(1)\n"
"s_barrier\n"
"buffer_store_dwordx2 v[30:31], v2, s[92:95], s67 idxen sc1\n"
"s_branch .L_end\n"
".L_w:\n"
"s_mov_b32 s96, 0\n"
"s_add_u32 s36, s12, 0x0\n"
"s_addc_u32 s37, s13, 0x0\n"
"s_and_b32 s37, s37, 0xffff\n"
"s_mov_b32 s38, 0x200000\n"
"s_mov_b32 s39, 0x20000\n"
"s_add_u32 s40, s12, 0xfffffc80\n"
"s_addc_u32 s41, s13, 0xffffffff\n"
"s_and_b32 s41, s41, 0xffff\n"
"s_mov_b32 s42, 0x200380\n"
"s_mov_b32 s43, 0x20000\n"
"s_lshl_b32 s48, s27, 8\n"
"s_add_u32 s96, s96, s48\n"
"v_and_b32 v8, 3, v1\n"
"v_mul_u32_u24 v8, 16, v8\n"
"s_and_b32 s48, s26, 1\n"
"s_lshl_b32 s48, s48, 6\n"
"s_lshr_b32 s49, s26, 1\n"
"s_lshl_b32 s49, s49, 10\n"
"s_add_u32 s48, s48, s49\n"
"v_add_u32 v8, s48, v8\n"
"v_lshrrev_b32 v4, 3, v1\n"
"v_bfe_u32 v5, v1, 4, 1\n"
"s_and_b32 s48, s26, 1\n"
"s_lshl_b32 s48, s48, 1\n"
"v_or_b32 v5, s48, v5\n"
"v_lshlrev_b32 v5, 1, v5\n"
"v_and_b32 v6, 7, v1\n"
"v_xor_b32 v5, v5, v6\n"
"v_mul_u32_u24 v4, 2048, v4\n"
"v_lshl_add_u32 v7, v5, 4, v4\n"
"s_mul_i32 s48, s26, 16384\n"
"s_add_u32 s96, s96, s48\n"
"s_mul_i32 s97, s26, 2048\n"
"s_add_u32 m0, s97, 0\n"
"s_nop 0\n"
"buffer_load_dwordx4 v7, s[36:39], s96 offen offset:0 lds\n"
"buffer_load_dwordx4 v7, s[40:43], s96 offen offset:1024 lds\n"
"s_add_u32 s96, s96, 65536\n"
"s_add_u32 m0, s97, 8192\n"
"s_nop 0\n"
"buffer_load_dwordx4 v7, s[36:39], s96 offen offset:0 lds\n"
"buffer_load_dwordx4 v7, s[40:43], s96 offen offset:1024 lds\n"
"s_add_u32 s96, s96, 65536\n"
"s_add_u32 m0, s97, 16384\n"
"s_nop 0\n"
"buffer_load_dwordx4 v7, s[36:39], s96 offen offset:0 lds\n"
"buffer_load_dwordx4 v7, s[40:43], s96 offen offset:1024 lds\n"
"s_add_u32 s96, s96, 65536\n"
"s_add_u32 m0, s97, 24576\n"
"s_nop 0\n"
"buffer_load_dwordx4 v7, s[36:39], s96 offen offset:0 lds\n"
"buffer_load_dwordx4 v7, s[40:43], s96 offen offset:1024 lds\n"
"s_add_u32 s96, s96, 65536\n"
"s_add_u32 m0, s97, 32768\n"
"s_nop 0\n"
"buffer_load_dwordx4 v7, s[36:39], s96 offen offset:0 lds\n"
"buffer_load_dwordx4 v7, s[40:43], s96 offen offset:1024 lds\n"
"s_add_u32 s96, s96, 65536\n"
"s_add_u32 m0, s97, 40960\n"
"s_nop 0\n"
"buffer_load_dwordx4 v7, s[36:39], s96 offen offset:0 lds\n"
"buffer_load_dwordx4 v7, s[40:43], s96 offen offset:1024 lds\n"
"s_add_u32 s96, s96, 65536\n"
"s_add_u32 m0, s97, 49152\n"
"s_nop 0\n"
"buffer_load_dwordx4 v7, s[36:39], s96 offen offset:0 lds\n"
"buffer_load_dwordx4 v7, s[40:43], s96 offen offset:1024 lds\n"
"s_add_u32 s96, s96, 65536\n"
"s_add_u32 m0, s97, 57344\n"
"s_nop 0\n"
"buffer_load_dwordx4 v7, s[36:39], s96 offen offset:0 lds\n"
"buffer_load_dwordx4 v7, s[40:43], s96 offen offset:1024 lds\n"
"s_add_u32 s96, s96, 65536\n"
"s_add_u32 m0, s97, 65536\n"
"s_nop 0\n"
"buffer_load_dwordx4 v7, s[36:39], s96 offen offset:0 lds\n"
"buffer_load_dwordx4 v7, s[40:43], s96 offen offset:1024 lds\n"
"s_add_u32 s96, s96, 65536\n"
"s_add_u32 m0, s97, 73728\n"
"s_nop 0\n"
"buffer_load_dwordx4 v7, s[36:39], s96 offen offset:0 lds\n"
"buffer_load_dwordx4 v7, s[40:43], s96 offen offset:1024 lds\n"
"s_add_u32 s96, s96, 65536\n"
"s_add_u32 m0, s97, 81920\n"
"s_nop 0\n"
"buffer_load_dwordx4 v7, s[36:39], s96 offen offset:0 lds\n"
"buffer_load_dwordx4 v7, s[40:43], s96 offen offset:1024 lds\n"
"s_add_u32 s96, s96, 65536\n"
"s_add_u32 m0, s97, 90112\n"
"s_nop 0\n"
"buffer_load_dwordx4 v7, s[36:39], s96 offen offset:0 lds\n"
"buffer_load_dwordx4 v7, s[40:43], s96 offen offset:1024 lds\n"
"s_add_u32 s96, s96, 65536\n"
"s_add_u32 m0, s97, 98304\n"
"s_nop 0\n"
"buffer_load_dwordx4 v7, s[36:39], s96 offen offset:0 lds\n"
"buffer_load_dwordx4 v7, s[40:43], s96 offen offset:1024 lds\n"
"s_add_u32 s96, s96, 65536\n"
"s_add_u32 m0, s97, 106496\n"
"s_nop 0\n"
"buffer_load_dwordx4 v7, s[36:39], s96 offen offset:0 lds\n"
"buffer_load_dwordx4 v7, s[40:43], s96 offen offset:1024 lds\n"
"s_add_u32 s96, s96, 65536\n"
"v_lshrrev_b32 v5, 4, v1\n"
"v_bfe_u32 v4, v1, 3, 1\n"
"v_and_b32 v6, 1, v5\n"
"v_lshl_add_u32 v4, v6, 1, v4\n"
"v_lshlrev_b32 v4, 5, v4\n"
"v_mul_u32_u24 v5, 2048, v5\n"
"v_bfe_u32 v6, v1, 2, 2\n"
"v_lshl_add_u32 v5, v6, 7, v5\n"
"v_xor_b32 v8, v4, v8\n"
"v_add_u32 v8, v5, v8\n"
"v_add_u32 v9, 65536, v8\n"
"s_waitcnt vmcnt(24)\n"
"s_barrier\n"
"s_add_u32 m0, s97, 114688\n"
"s_nop 0\n"
"buffer_load_dwordx4 v7, s[36:39], s96 offen offset:0 lds\n"
"buffer_load_dwordx4 v7, s[40:43], s96 offen offset:1024 lds\n"
"s_add_u32 s96, s96, 65536\n"
"s_add_u32 m0, s97, 122880\n"
"s_nop 0\n"
"buffer_load_dwordx4 v7, s[36:39], s96 offen offset:0 lds\n"
"buffer_load_dwordx4 v7, s[40:43], s96 offen offset:1024 lds\n"
"s_add_u32 s96, s96, 65536\n"
"ds_read_b64_tr_b16 a[0:1], v8 offset:0\n"
"ds_read_b64_tr_b16 a[2:3], v8 offset:512\n"
"ds_read_b64_tr_b16 a[4:5], v8 offset:8\n"
"ds_read_b64_tr_b16 a[6:7], v8 offset:520\n"
"ds_read_b64_tr_b16 a[8:9], v8 offset:8192\n"
"ds_read_b64_tr_b16 a[10:11], v8 offset:8704\n"
"ds_read_b64_tr_b16 a[12:13], v8 offset:8200\n"
"ds_read_b64_tr_b16 a[14:15], v8 offset:8712\n"
"s_waitcnt lgkmcnt(0)\n"
"s_waitcnt vmcnt(24)\n"
"s_barrier\n"
"s_add_u32 m0, s97, 0\n"
"s_nop 0\n"
"buffer_load_dwordx4 v7, s[36:39], s96 offen offset:0 lds\n"
"buffer_load_dwordx4 v7, s[40:43], s96 offen offset:1024 lds\n"
"s_add_u32 s96, s96, 65536\n"
"s_add_u32 m0, s97, 8192\n"
"s_nop 0\n"
"buffer_load_dwordx4 v7, s[36:39], s96 offen offset:0 lds\n"
"buffer_load_dwordx4 v7, s[40:43], s96 offen offset:1024 lds\n"
"s_add_u32 s96, s96, 65536\n"
"ds_read_b64_tr_b16 a[16:17], v8 offset:16384\n"
"ds_read_b64_tr_b16 a[18:19], v8 offset:16896\n"
"ds_read_b64_tr_b16 a[20:21], v8 offset:16392\n"
"ds_read_b64_tr_b16 a[22:23], v8 offset:16904\n"
"ds_read_b64_tr_b16 a[24:25], v8 offset:24576\n"
"ds_read_b64_tr_b16 a[26:27], v8 offset:25088\n"
"ds_read_b64_tr_b16 a[28:29], v8 offset:24584\n"
"ds_read_b64_tr_b16 a[30:31], v8 offset:25096\n"
"s_waitcnt lgkmcnt(0)\n"
"s_waitcnt vmcnt(24)\n"
"s_barrier\n"
"s_add_u32 m0, s97, 16384\n"
"s_nop 0\n"
"buffer_load_dwordx4 v7, s[36:39], s96 offen offset:0 lds\n"
"buffer_load_dwordx4 v7, s[40:43], s96 offen offset:1024 lds\n"
"s_add_u32 s96, s96, 65536\n"
"s_add_u32 m0, s97, 24576\n"
"s_nop 0\n"
"buffer_load_dwordx4 v7, s[36:39], s96 offen offset:0 lds\n"
"buffer_load_dwordx4 v7, s[40:43], s96 offen offset:1024 lds\n"
"s_add_u32 s96, s96, 65536\n"
"ds_read_b64_tr_b16 a[32:33], v8 offset:32768\n"
"ds_read_b64_tr_b16 a[34:35], v8 offset:33280\n"
"ds_read_b64_tr_b16 a[36:37], v8 offset:32776\n"
"ds_read_b64_tr_b16 a[38:39], v8 offset:33288\n"
"ds_read_b64_tr_b16 a[40:41], v8 offset:40960\n"
"ds_read_b64_tr_b16 a[42:43], v8 offset:41472\n"
"ds_read_b64_tr_b16 a[44:45], v8 offset:40968\n"
"ds_read_b64_tr_b16 a[46:47], v8 offset:41480\n"
"s_waitcnt lgkmcnt(0)\n"
"s_waitcnt vmcnt(24)\n"
"s_barrier\n"
"s_add_u32 m0, s97, 32768\n"
"s_nop 0\n"
"buffer_load_dwordx4 v7, s[36:39], s96 offen offset:0 lds\n"
"buffer_load_dwordx4 v7, s[40:43], s96 offen offset:1024 lds\n"
"s_add_u32 s96, s96, 65536\n"
"s_add_u32 m0, s97, 40960\n"
"s_nop 0\n"
"buffer_load_dwordx4 v7, s[36:39], s96 offen offset:0 lds\n"
"buffer_load_dwordx4 v7, s[40:43], s96 offen offset:1024 lds\n"
"s_add_u32 s96, s96, 65536\n"
"ds_read_b64_tr_b16 a[48:49], v8 offset:49152\n"
"ds_read_b64_tr_b16 a[50:51], v8 offset:49664\n"
"ds_read_b64_tr_b16 a[52:53], v8 offset:49160\n"
"ds_read_b64_tr_b16 a[54:55], v8 offset:49672\n"
"ds_read_b64_tr_b16 a[56:57], v8 offset:57344\n"
"ds_read_b64_tr_b16 a[58:59], v8 offset:57856\n"
"ds_read_b64_tr_b16 a[60:61], v8 offset:57352\n"
"ds_read_b64_tr_b16 a[62:63], v8 offset:57864\n"
"s_waitcnt lgkmcnt(0)\n"
"s_waitcnt vmcnt(24)\n"
"s_barrier\n"
"s_add_u32 m0, s97, 49152\n"
"s_nop 0\n"
"buffer_load_dwordx4 v7, s[36:39], s96 offen offset:0 lds\n"
"buffer_load_dwordx4 v7, s[40:43], s96 offen offset:1024 lds\n"
"s_add_u32 s96, s96, 65536\n"
"s_add_u32 m0, s97, 57344\n"
"s_nop 0\n"
"buffer_load_dwordx4 v7, s[36:39], s96 offen offset:0 lds\n"
"buffer_load_dwordx4 v7, s[40:43], s96 offen offset:1024 lds\n"
"s_add_u32 s96, s96, 65536\n"
"ds_read_b64_tr_b16 a[64:65], v9 offset:0\n"
"ds_read_b64_tr_b16 a[66:67], v9 offset:512\n"
"ds_read_b64_tr_b16 a[68:69], v9 offset:8\n"
"ds_read_b64_tr_b16 a[70:71], v9 offset:520\n"
"ds_read_b64_tr_b16 a[72:73], v9 offset:8192\n"
"ds_read_b64_tr_b16 a[74:75], v9 offset:8704\n"
"ds_read_b64_tr_b16 a[76:77], v9 offset:8200\n"
"ds_read_b64_tr_b16 a[78:79], v9 offset:8712\n"
"s_waitcnt lgkmcnt(0)\n"
"s_waitcnt vmcnt(24)\n"
"s_barrier\n"
"s_add_u32 m0, s97, 65536\n"
"s_nop 0\n"
"buffer_load_dwordx4 v7, s[36:39], s96 offen offset:0 lds\n"
"buffer_load_dwordx4 v7, s[40:43], s96 offen offset:1024 lds\n"
"s_add_u32 s96, s96, 65536\n"
"s_add_u32 m0, s97, 73728\n"
"s_nop 0\n"
"buffer_load_dwordx4 v7, s[36:39], s96 offen offset:0 lds\n"
"buffer_load_dwordx4 v7, s[40:43], s96 offen offset:1024 lds\n"
"s_add_u32 s96, s96, 65536\n"
"ds_read_b64_tr_b16 a[80:81], v9 offset:16384\n"
"ds_read_b64_tr_b16 a[82:83], v9 offset:16896\n"
"ds_read_b64_tr_b16 a[84:85], v9 offset:16392\n"
"ds_read_b64_tr_b16 a[86:87], v9 offset:16904\n"
"ds_read_b64_tr_b16 a[88:89], v9 offset:24576\n"
"ds_read_b64_tr_b16 a[90:91], v9 offset:25088\n"
"ds_read_b64_tr_b16 a[92:93], v9 offset:24584\n"
"ds_read_b64_tr_b16 a[94:95], v9 offset:25096\n"
"s_waitcnt lgkmcnt(0)\n"
"s_waitcnt vmcnt(24)\n"
"s_barrier\n"
"s_add_u32 m0, s97, 81920\n"
"s_nop 0\n"
"buffer_load_dwordx4 v7, s[36:39], s96 offen offset:0 lds\n"
"buffer_load_dwordx4 v7, s[40:43], s96 offen offset:1024 lds\n"
"s_add_u32 s96, s96, 65536\n"
"s_add_u32 m0, s97, 90112\n"
"s_nop 0\n"
"buffer_load_dwordx4 v7, s[36:39], s96 offen offset:0 lds\n"
"buffer_load_dwordx4 v7, s[40:43], s96 offen offset:1024 lds\n"
"s_add_u32 s96, s96, 65536\n"
"ds_read_b64_tr_b16 a[96:97], v9 offset:32768\n"
"ds_read_b64_tr_b16 a[98:99], v9 offset:33280\n"
"ds_read_b64_tr_b16 a[100:101], v9 offset:32776\n"
"ds_read_b64_tr_b16 a[102:103], v9 offset:33288\n"
"ds_read_b64_tr_b16 a[104:105], v9 offset:40960\n"
"ds_read_b64_tr_b16 a[106:107], v9 offset:41472\n"
"ds_read_b64_tr_b16 a[108:109], v9 offset:40968\n"
"ds_read_b64_tr_b16 a[110:111], v9 offset:41480\n"
"s_waitcnt lgkmcnt(0)\n"
"s_waitcnt vmcnt(24)\n"
"s_barrier\n"
"s_add_u32 m0, s97, 98304\n"
"s_nop 0\n"
"buffer_load_dwordx4 v7, s[36:39], s96 offen offset:0 lds\n"
"buffer_load_dwordx4 v7, s[40:43], s96 offen offset:1024 lds\n"
"s_add_u32 s96, s96, 65536\n"
"s_add_u32 m0, s97, 106496\n"
"s_nop 0\n"
"buffer_load_dwordx4 v7, s[36:39], s96 offen offset:0 lds\n"
"buffer_load_dwordx4 v7, s[40:43], s96 offen offset:1024 lds\n"
"s_add_u32 s96, s96, 65536\n"
"ds_read_b64_tr_b16 a[112:113], v9 offset:49152\n"
"ds_read_b64_tr_b16 a[114:115], v9 offset:49664\n"
"ds_read_b64_tr_b16 a[116:117], v9 offset:49160\n"
"ds_read_b64_tr_b16 a[118:119], v9 offset:49672\n"
"ds_read_b64_tr_b16 a[120:121], v9 offset:57344\n"
"ds_read_b64_tr_b16 a[122:123], v9 offset:57856\n"
"ds_read_b64_tr_b16 a[124:125], v9 offset:57352\n"
"ds_read_b64_tr_b16 a[126:127], v9 offset:57864\n"
"s_waitcnt lgkmcnt(0)\n"
"s_waitcnt vmcnt(24)\n"
"s_barrier\n"
"s_add_u32 m0, s97, 114688\n"
"s_nop 0\n"
"buffer_load_dwordx4 v7, s[36:39], s96 offen offset:0 lds\n"
"buffer_load_dwordx4 v7, s[40:43], s96 offen offset:1024 lds\n"
"s_add_u32 s96, s96, 65536\n"
"s_add_u32 m0, s97, 122880\n"
"s_nop 0\n"
"buffer_load_dwordx4 v7, s[36:39], s96 offen offset:0 lds\n"
"buffer_load_dwordx4 v7, s[40:43], s96 offen offset:1024 lds\n"
"s_add_u32 s96, s96, 65536\n"
"ds_read_b64_tr_b16 a[128:129], v8 offset:0\n"
"ds_read_b64_tr_b16 a[130:131], v8 offset:512\n"
"ds_read_b64_tr_b16 a[132:133], v8 offset:8\n"
"ds_read_b64_tr_b16 a[134:135], v8 offset:520\n"
"ds_read_b64_tr_b16 a[136:137], v8 offset:8192\n"
"ds_read_b64_tr_b16 a[138:139], v8 offset:8704\n"
"ds_read_b64_tr_b16 a[140:141], v8 offset:8200\n"
"ds_read_b64_tr_b16 a[142:143], v8 offset:8712\n"
"s_waitcnt lgkmcnt(0)\n"
"s_waitcnt vmcnt(24)\n"
"s_barrier\n"
"ds_read_b64_tr_b16 a[144:145], v8 offset:16384\n"
"ds_read_b64_tr_b16 a[146:147], v8 offset:16896\n"
"ds_read_b64_tr_b16 a[148:149], v8 offset:16392\n"
"ds_read_b64_tr_b16 a[150:151], v8 offset:16904\n"
"ds_read_b64_tr_b16 a[152:153], v8 offset:24576\n"
"ds_read_b64_tr_b16 a[154:155], v8 offset:25088\n"
"ds_read_b64_tr_b16 a[156:157], v8 offset:24584\n"
"ds_read_b64_tr_b16 a[158:159], v8 offset:25096\n"
"s_waitcnt lgkmcnt(0)\n"
"s_waitcnt vmcnt(20)\n"
"s_barrier\n"
"ds_read_b64_tr_b16 a[160:161], v8 offset:32768\n"
"ds_read_b64_tr_b16 a[162:163], v8 offset:33280\n"
"ds_read_b64_tr_b16 a[164:165], v8 offset:32776\n"
"ds_read_b64_tr_b16 a[166:167], v8 offset:33288\n"
"ds_read_b64_tr_b16 a[168:169], v8 offset:40960\n"
"ds_read_b64_tr_b16 a[170:171], v8 offset:41472\n"
"ds_read_b64_tr_b16 a[172:173], v8 offset:40968\n"
"ds_read_b64_tr_b16 a[174:175], v8 offset:41480\n"
"s_waitcnt lgkmcnt(0)\n"
"s_waitcnt vmcnt(16)\n"
"s_barrier\n"
"ds_read_b64_tr_b16 a[176:177], v8 offset:49152\n"
"ds_read_b64_tr_b16 a[178:179], v8 offset:49664\n"
"ds_read_b64_tr_b16 a[180:181], v8 offset:49160\n"
"ds_read_b64_tr_b16 a[182:183], v8 offset:49672\n"
"ds_read_b64_tr_b16 a[184:185], v8 offset:57344\n"
"ds_read_b64_tr_b16 a[186:187], v8 offset:57856\n"
"ds_read_b64_tr_b16 a[188:189], v8 offset:57352\n"
"ds_read_b64_tr_b16 a[190:191], v8 offset:57864\n"
"s_waitcnt lgkmcnt(0)\n"
"s_waitcnt vmcnt(12)\n"
"s_barrier\n"
"ds_read_b64_tr_b16 a[192:193], v9 offset:0\n"
"ds_read_b64_tr_b16 a[194:195], v9 offset:512\n"
"ds_read_b64_tr_b16 a[196:197], v9 offset:8\n"
"ds_read_b64_tr_b16 a[198:199], v9 offset:520\n"
"ds_read_b64_tr_b16 a[200:201], v9 offset:8192\n"
"ds_read_b64_tr_b16 a[202:203], v9 offset:8704\n"
"ds_read_b64_tr_b16 a[204:205], v9 offset:8200\n"
"ds_read_b64_tr_b16 a[206:207], v9 offset:8712\n"
"s_waitcnt lgkmcnt(0)\n"
"s_waitcnt vmcnt(8)\n"
"s_barrier\n"
"ds_read_b64_tr_b16 a[208:209], v9 offset:16384\n"
"ds_read_b64_tr_b16 a[210:211], v9 offset:16896\n"
"ds_read_b64_tr_b16 a[212:213], v9 offset:16392\n"
"ds_read_b64_tr_b16 a[214:215], v9 offset:16904\n"
"ds_read_b64_tr_b16 a[216:217], v9 offset:24576\n"
"ds_read_b64_tr_b16 a[218:219], v9 offset:25088\n"
"ds_read_b64_tr_b16 a[220:221], v9 offset:24584\n"
"ds_read_b64_tr_b16 a[222:223], v9 offset:25096\n"
"s_waitcnt lgkmcnt(0)\n"
"s_waitcnt vmcnt(4)\n"
"s_barrier\n"
"ds_read_b64_tr_b16 a[224:225], v9 offset:32768\n"
"ds_read_b64_tr_b16 a[226:227], v9 offset:33280\n"
"ds_read_b64_tr_b16 a[228:229], v9 offset:32776\n"
"ds_read_b64_tr_b16 a[230:231], v9 offset:33288\n"
"ds_read_b64_tr_b16 a[232:233], v9 offset:40960\n"
"ds_read_b64_tr_b16 a[234:235], v9 offset:41472\n"
"ds_read_b64_tr_b16 a[236:237], v9 offset:40968\n"
"ds_read_b64_tr_b16 a[238:239], v9 offset:41480\n"
"s_waitcnt lgkmcnt(0)\n"
"s_waitcnt vmcnt(0)\n"
"s_barrier\n"
"ds_read_b64_tr_b16 a[240:241], v9 offset:49152\n"
"ds_read_b64_tr_b16 a[242:243], v9 offset:49664\n"
"ds_read_b64_tr_b16 a[244:245], v9 offset:49160\n"
"ds_read_b64_tr_b16 a[246:247], v9 offset:49672\n"
"ds_read_b64_tr_b16 a[248:249], v9 offset:57344\n"
"ds_read_b64_tr_b16 a[250:251], v9 offset:57856\n"
"ds_read_b64_tr_b16 a[252:253], v9 offset:57352\n"
"ds_read_b64_tr_b16 a[254:255], v9 offset:57864\n"
"s_waitcnt lgkmcnt(0)\n"
"s_barrier\n"
"s_lshl2_add_u32 s50, s27, s26\n"
"s_and_b32 s51, s50, 6\n"
"s_lshl_b32 s51, s51, 1\n"
"v_add_u32 v3, s51, v1\n"
"v_and_b32 v3, 15, v3\n"
"v_lshlrev_b32 v3, 3, v3\n"
"v_cmp_gt_u32 vcc, 16, v1\n"
"v_mov_b32 v15, 0x7fff0000\n"
"s_nop 0\n"
"v_cndmask_b32 v15, v15, v3, vcc\n"
"s_mul_i32 s48, s50, 1152\n"
"s_add_u32 s68, s48, 4681728\n"
"s_lshl2_add_u32 s49, s27, 96\n"
"s_waitcnt lgkmcnt(0)\n"
"s_add_u32 s54, s52, 0x7f4a7c15\n"
"s_addc_u32 s55, s53, 0x9e3779b9\n"
"s_lshl_b32 s67, s49, 5\n"
"s_add_u32 s67, s67, 4898816\n"
"s_sub_u32 s51, s67, s68\n"
"s_add_u32 s92, s32, s51\n"
"s_addc_u32 s93, s33, 0x10000\n"
"s_sub_u32 s92, s92, 593920\n"
"s_subb_u32 s93, s93, 0\n"
"s_cmp_eq_u32 s26, 0\n"
"s_cselect_b32 s94, 1, 0\n"
"s_mov_b32 s95, 0x20000\n"
"v_mov_b32 v16, s54\n"
"v_mov_b32 v17, s55\n"
"v_cmp_gt_u32 vcc, 8, v1\n"
"v_mul_u32_u24 v3, 296960, v1\n"
"v_mov_b32 v12, 0x7fff0000\n"
"v_cndmask_b32 v3, v12, v3, vcc\n"
"s_add_u32 s51, s67, 76021760\n"
"buffer_store_dwordx2 v[16:17], v3, s[32:35], s51 offen sc1\n"
"s_sub_u32 s67, s68, 296960\n"
"s_mul_i32 s49, s28, 296960\n"
"s_add_u32 s48, s48, s49\n"
"s_add_u32 s71, s48, 4612096\n"
"s_add_u32 s68, s48, 3493888\n"
"s_mov_b32 s62, 63\n"
"s_add_u32 s63, s49, 4648960\n"
"s_add_u32 s66, s49, 4898816\n"
"s_mul_i32 s64, s26, 8192\n"
"s_add_u32 s65, s64, 65536\n"
"s_xor_b32 s65, s65, s64\n"
"s_add_u32 s63, s63, s64\n"
"v_mov_b32 v4, v2\n"
"v_add_u32 v6, 4096, v2\n"
"v_and_b32 v14, 15, v1\n"
"s_lshl4_add_u32 s48, s26, 32\n"
"v_add_u32 v14, s48, v14\n"
"v_lshrrev_b32 v14, 2, v14\n"
"v_lshlrev_b32 v14, 7, v14\n"
"v_lshrrev_b32 v3, 4, v1\n"
"v_mul_u32_u24 v3, 593920, v3\n"
"v_add_u32 v14, v3, v14\n"
"v_cmp_gt_u32 vcc, 32, v1\n"
"v_mov_b32 v3, 0x7fff0000\n"
"s_nop 0\n"
"v_cndmask_b32 v14, v3, v14, vcc\n"
"v_cndmask_b32 v16, 0, v16, vcc\n"
"v_cndmask_b32 v17, 0, v17, vcc\n"
"s_mov_b32 s48, 20000\n"
"s_branch .L_rfwa\n"
".L_rlwa:\n"
"s_sleep 8\n"
".L_rfwa:\n"
"buffer_load_dwordx2 v[12:13], v14, s[32:35], s66 offen sc1\n"
"s_sub_u32 s48, s48, 1\n"
"s_cbranch_scc1 .L_end\n"
"s_waitcnt vmcnt(0)\n"
"v_cmp_ne_u64 vcc, v[16:17], v[12:13]\n"
"s_cbranch_vccnz .L_rlwa\n"
"s_add_u32 s66, s66, 1187840\n"
"s_add_u32 m0, s64, 0\n"
"s_nop 0\n"
"buffer_load_dwordx4 v2, s[32:35], s63 offen offset:0 sc1 lds\n"
"buffer_load_dwordx4 v2, s[32:35], s63 offen offset:1024 sc1 lds\n"
"buffer_load_dwordx4 v2, s[32:35], s63 offen offset:2048 sc1 lds\n"
"buffer_load_dwordx4 v2, s[32:35], s63 offen offset:3072 sc1 lds\n"
"s_add_u32 m0, s64, 4096\n"
"s_nop 0\n"
"buffer_load_dwordx4 v6, s[32:35], s63 offen offset:0 sc1 lds\n"
"buffer_load_dwordx4 v6, s[32:35], s63 offen offset:1024 sc1 lds\n"
"buffer_load_dwordx4 v6, s[32:35], s63 offen offset:2048 sc1 lds\n"
"buffer_load_dwordx4 v6, s[32:35], s63 offen offset:3072 sc1 lds\n"
"s_add_u32 s63, s63, 593920\n"
"buffer_load_dwordx4 v[48:51], v2, s[32:35], s71 offen sc1\n"
"s_add_u32 s71, s71, 593920\n"
"buffer_load_dwordx4 v[52:55], v2, s[32:35], s71 offen sc1\n"
"s_add_u32 s71, s71, 593920\n"
"s_add_u32 m0, s64, 32768\n"
"s_nop 0\n"
"buffer_load_dwordx4 v2, s[32:35], s63 offen offset:0 sc1 lds\n"
"buffer_load_dwordx4 v2, s[32:35], s63 offen offset:1024 sc1 lds\n"
"buffer_load_dwordx4 v2, s[32:35], s63 offen offset:2048 sc1 lds\n"
"buffer_load_dwordx4 v2, s[32:35], s63 offen offset:3072 sc1 lds\n"
"s_add_u32 m0, s64, 36864\n"
"s_nop 0\n"
"buffer_load_dwordx4 v6, s[32:35], s63 offen offset:0 sc1 lds\n"
"buffer_load_dwordx4 v6, s[32:35], s63 offen offset:1024 sc1 lds\n"
"buffer_load_dwordx4 v6, s[32:35], s63 offen offset:2048 sc1 lds\n"
"buffer_load_dwordx4 v6, s[32:35], s63 offen offset:3072 sc1 lds\n"
"s_add_u32 s63, s63, 593920\n"
"s_xor_b32 s64, s64, s65\n"
"buffer_load_dwordx2 v[12:13], v14, s[32:35], s66 offen sc1\n"
"s_waitcnt vmcnt(0)\n"
"s_barrier\n"
"ds_read_b128 v[128:131], v4 offset:0\n"
"ds_read_b128 v[132:135], v4 offset:1024\n"
"ds_read_b128 v[136:139], v4 offset:2048\n"
"ds_read_b128 v[140:143], v4 offset:3072\n"
"ds_read_b128 v[144:147], v4 offset:4096\n"
"ds_read_b128 v[148:151], v4 offset:5120\n"
"ds_read_b128 v[152:155], v4 offset:6144\n"
"ds_read_b128 v[156:159], v4 offset:7168\n"
"ds_read_b128 v[160:163], v4 offset:8192\n"
"ds_read_b128 v[164:167], v4 offset:9216\n"
"ds_read_b128 v[168:171], v4 offset:10240\n"
"ds_read_b128 v[172:175], v4 offset:11264\n"
"v_lshlrev_b32 v32, 16, v48\n"
"v_and_b32 v33, 0xffff0000, v48\n"
"v_lshlrev_b32 v34, 16, v49\n"
"v_and_b32 v35, 0xffff0000, v49\n"
"v_lshlrev_b32 v36, 16, v50\n"
"v_and_b32 v37, 0xffff0000, v50\n"
"v_lshlrev_b32 v38, 16, v51\n"
"v_and_b32 v39, 0xffff0000, v51\n"
".p2align 3\n"
".L_wl:\n"
"s_waitcnt lgkmcnt(4)\n"
"v_mfma_f32_16x16x32_bf16 v[32:35], a[0:3], v[128:131], v[32:35]\n"
"ds_read_b128 v[176:179], v4 offset:12288\n"
"v_mfma_f32_16x16x32_bf16 v[36:39], a[4:7], v[128:131], v[36:39]\n"
"v_mfma_f32_16x16x32_bf16 v[36:39], a[12:15], v[132:135], v[36:39]\n"
"ds_read_b128 v[180:183], v4 offset:13312\n"
"s_add_u32 s68, s68, 593920\n"
"v_mfma_f32_16x16x32_bf16 v[32:35], a[8:11], v[132:135], v[32:35]\n"
"s_waitcnt vmcnt(10)\n"
"v_cmp_ne_u64 vcc, v[16:17], v[12:13]\n"
"v_mfma_f32_16x16x32_bf16 v[32:35], a[16:19], v[136:139], v[32:35]\n"
"ds_read_b128 v[184:187], v4 offset:14336\n"
"v_cvt_pk_bf16_f32 v56, v40, v41\n"
"v_mfma_f32_16x16x32_bf16 v[36:39], a[20:23], v[136:139], v[36:39]\n"
"s_cbranch_vccnz .L_rtw0\n"
".L_bkw0:\n"
"s_add_u32 s66, s66, 1187840\n"
"v_mfma_f32_16x16x32_bf16 v[36:39], a[28:31], v[140:143], v[36:39]\n"
"ds_read_b128 v[188:191], v4 offset:15360\n"
"v_cvt_pk_bf16_f32 v57, v42, v43\n"
"v_mfma_f32_16x16x32_bf16 v[32:35], a[24:27], v[140:143], v[32:35]\n"
"buffer_load_dwordx4 v[48:51], v2, s[32:35], s71 offen sc1\n"
"v_mfma_f32_16x16x32_bf16 v[32:35], a[32:35], v[144:147], v[32:35]\n"
"ds_read_b128 v[128:131], v4 offset:16384\n"
"s_add_u32 s71, s71, 593920\n"
"v_mfma_f32_16x16x32_bf16 v[36:39], a[36:39], v[144:147], v[36:39]\n"
"s_add_u32 m0, s64, 0\n"
"v_cvt_pk_bf16_f32 v58, v44, v45\n"
"v_mfma_f32_16x16x32_bf16 v[36:39], a[44:47], v[148:151], v[36:39]\n"
"ds_read_b128 v[132:135], v4 offset:17408\n"
"v_cvt_pk_bf16_f32 v59, v46, v47\n"
"v_mfma_f32_16x16x32_bf16 v[32:35], a[40:43], v[148:151], v[32:35]\n"
"buffer_load_dwordx4 v2, s[32:35], s63 offen offset:0 sc1 lds\n"
"v_mfma_f32_16x16x32_bf16 v[32:35], a[48:51], v[152:155], v[32:35]\n"
"ds_read_b128 v[136:139], v4 offset:18432\n"
"v_mfma_f32_16x16x32_bf16 v[36:39], a[52:55], v[152:155], v[36:39]\n"
"buffer_load_dwordx4 v2, s[32:35], s63 offen offset:1024 sc1 lds\n"
"v_mfma_f32_16x16x32_bf16 v[36:39], a[60:63], v[156:159], v[36:39]\n"
"ds_read_b128 v[140:143], v4 offset:19456\n"
"v_mfma_f32_16x16x32_bf16 v[32:35], a[56:59], v[156:159], v[32:35]\n"
"s_waitcnt lgkmcnt(4)\n"
"v_mfma_f32_16x16x32_bf16 v[32:35], a[64:67], v[160:163], v[32:35]\n"
"ds_read_b128 v[144:147], v4 offset:20480\n"
"v_mfma_f32_16x16x32_bf16 v[36:39], a[68:71], v[160:163], v[36:39]\n"
"buffer_store_dwordx4 v[56:59], v2, s[32:35], s68 offen sc1\n"
"v_mfma_f32_16x16x32_bf16 v[36:39], a[76:79], v[164:167], v[36:39]\n"
"ds_read_b128 v[148:151], v4 offset:21504\n"
"v_add_f32 v60, v40, v41\n"
"v_mfma_f32_16x16x32_bf16 v[32:35], a[72:75], v[164:167], v[32:35]\n"
"buffer_load_dwordx4 v2, s[32:35], s63 offen offset:2048 sc1 lds\n"
"v_mfma_f32_16x16x32_bf16 v[32:35], a[80:83], v[168:171], v[32:35]\n"
"ds_read_b128 v[152:155], v4 offset:22528\n"
"v_mul_f32 v61, v40, v40\n"
"v_mfma_f32_16x16x32_bf16 v[36:39], a[84:87], v[168:171], v[36:39]\n"
"v_fmac_f32 v61, v41, v41\n"
"v_add_f32 v60, v60, v42\n"
"v_mfma_f32_16x16x32_bf16 v[36:39], a[92:95], v[172:175], v[36:39]\n"
"ds_read_b128 v[156:159], v4 offset:23552\n"
"v_fmac_f32 v61, v42, v42\n"
"v_mfma_f32_16x16x32_bf16 v[32:35], a[88:91], v[172:175], v[32:35]\n"
"v_add_f32 v60, v60, v43\n"
"v_fmac_f32 v61, v43, v43\n"
"v_mfma_f32_16x16x32_bf16 v[32:35], a[96:99], v[176:179], v[32:35]\n"
"ds_read_b128 v[160:163], v4 offset:24576\n"
"v_add_f32 v60, v60, v44\n"
"v_mfma_f32_16x16x32_bf16 v[36:39], a[100:103], v[176:179], v[36:39]\n"
"buffer_load_dwordx4 v2, s[32:35], s63 offen offset:3072 sc1 lds\n"
"v_mfma_f32_16x16x32_bf16 v[36:39], a[108:111], v[180:183], v[36:39]\n"
"ds_read_b128 v[164:167], v4 offset:25600\n"
"v_fmac_f32 v61, v44, v44\n"
"v_mfma_f32_16x16x32_bf16 v[32:35], a[104:107], v[180:183], v[32:35]\n"
"v_add_f32 v60, v60, v45\n"
"v_fmac_f32 v61, v45, v45\n"
"v_mfma_f32_16x16x32_bf16 v[32:35], a[112:115], v[184:187], v[32:35]\n"
"ds_read_b128 v[168:171], v4 offset:26624\n"
"v_add_f32 v60, v60, v46\n"
"v_mfma_f32_16x16x32_bf16 v[36:39], a[116:119], v[184:187], v[36:39]\n"
"s_add_u32 m0, s64, 4096\n"
"v_fmac_f32 v61, v46, v46\n"
"v_mfma_f32_16x16x32_bf16 v[36:39], a[124:127], v[188:191], v[36:39]\n"
"ds_read_b128 v[172:175], v4 offset:27648\n"
"v_add_f32 v60, v60, v47\n"
"v_mfma_f32_16x16x32_bf16 v[32:35], a[120:123], v[188:191], v[32:35]\n"
"v_fmac_f32 v61, v47, v47\n"
"s_waitcnt lgkmcnt(4)\n"
"v_mfma_f32_16x16x32_bf16 v[32:35], a[128:131], v[128:131], v[32:35]\n"
"ds_read_b128 v[176:179], v4 offset:28672\n"
"v_mfma_f32_16x16x32_bf16 v[36:39], a[132:135], v[128:131], v[36:39]\n"
"buffer_load_dwordx4 v6, s[32:35], s63 offen offset:0 sc1 lds\n"
"v_mfma_f32_16x16x32_bf16 v[36:39], a[140:143], v[132:135], v[36:39]\n"
"ds_read_b128 v[180:183], v4 offset:29696\n"
"v_mfma_f32_16x16x32_bf16 v[32:35], a[136:139], v[132:135], v[32:35]\n"
"v_permlane32_swap_b32 v60, v61\n"
"v_mfma_f32_16x16x32_bf16 v[32:35], a[144:147], v[136:139], v[32:35]\n"
"ds_read_b128 v[184:187], v4 offset:30720\n"
"v_add_f32 v60, v60, v61\n"
"v_mfma_f32_16x16x32_bf16 v[36:39], a[148:151], v[136:139], v[36:39]\n"
"buffer_load_dwordx4 v6, s[32:35], s63 offen offset:1024 sc1 lds\n"
"v_mfma_f32_16x16x32_bf16 v[36:39], a[156:159], v[140:143], v[36:39]\n"
"ds_read_b128 v[188:191], v4 offset:31744\n"
"v_mfma_f32_16x16x32_bf16 v[32:35], a[152:155], v[140:143], v[32:35]\n"
"s_waitcnt vmcnt(9)\n"
"s_waitcnt vmcnt(8)\n"
"s_barrier\n"
"v_mfma_f32_16x16x32_bf16 v[32:35], a[160:163], v[144:147], v[32:35]\n"
"ds_read_b128 v[128:131], v4 offset:32768\n"
"v_mfma_f32_16x16x32_bf16 v[36:39], a[164:167], v[144:147], v[36:39]\n"
"buffer_store_dwordx2 v[16:17], v2, s[92:95], s68 idxen sc1\n"
"v_mfma_f32_16x16x32_bf16 v[36:39], a[172:175], v[148:151], v[36:39]\n"
"ds_read_b128 v[132:135], v4 offset:33792\n"
"v_mfma_f32_16x16x32_bf16 v[32:35], a[168:171], v[148:151], v[32:35]\n"
"buffer_load_dwordx4 v6, s[32:35], s63 offen offset:2048 sc1 lds\n"
"v_mfma_f32_16x16x32_bf16 v[32:35], a[176:179], v[152:155], v[32:35]\n"
"ds_read_b128 v[136:139], v4 offset:34816\n"
"v_mfma_f32_16x16x32_bf16 v[36:39], a[180:183], v[152:155], v[36:39]\n"
"v_permlane16_swap_b32 v60, v61\n"
"v_mfma_f32_16x16x32_bf16 v[36:39], a[188:191], v[156:159], v[36:39]\n"
"ds_read_b128 v[140:143], v4 offset:35840\n"
"v_add_f32 v60, v60, v61\n"
"v_mfma_f32_16x16x32_bf16 v[32:35], a[184:187], v[156:159], v[32:35]\n"
"s_waitcnt lgkmcnt(4)\n"
"v_mfma_f32_16x16x32_bf16 v[32:35], a[192:195], v[160:163], v[32:35]\n"
"ds_read_b128 v[144:147], v4 offset:36864\n"
"v_mfma_f32_16x16x32_bf16 v[36:39], a[196:199], v[160:163], v[36:39]\n"
"buffer_load_dwordx4 v6, s[32:35], s63 offen offset:3072 sc1 lds\n"
"v_mfma_f32_16x16x32_bf16 v[36:39], a[204:207], v[164:167], v[36:39]\n"
"ds_read_b128 v[148:151], v4 offset:37888\n"
"s_add_u32 s63, s63, 593920\n"
"v_mfma_f32_16x16x32_bf16 v[32:35], a[200:203], v[164:167], v[32:35]\n"
"v_permlane32_swap_b32 v60, v61\n"
"v_mfma_f32_16x16x32_bf16 v[32:35], a[208:211], v[168:171], v[32:35]\n"
"ds_read_b128 v[152:155], v4 offset:38912\n"
"v_mfma_f32_16x16x32_bf16 v[36:39], a[212:215], v[168:171], v[36:39]\n"
"buffer_store_dwordx2 v[60:61], v15, s[32:35], s68 offen offset:1024 sc1\n"
"v_mfma_f32_16x16x32_bf16 v[36:39], a[220:223], v[172:175], v[36:39]\n"
"ds_read_b128 v[156:159], v4 offset:39936\n"
"v_mfma_f32_16x16x32_bf16 v[32:35], a[216:219], v[172:175], v[32:35]\n"
"v_lshlrev_b32 v40, 16, v52\n"
"v_and_b32 v41, 0xffff0000, v52\n"
"v_mfma_f32_16x16x32_bf16 v[32:35], a[224:227], v[176:179], v[32:35]\n"
"ds_read_b128 v[160:163], v4 offset:40960\n"
"v_lshlrev_b32 v42, 16, v53\n"
"v_mfma_f32_16x16x32_bf16 v[36:39], a[228:231], v[176:179], v[36:39]\n"
"v_and_b32 v43, 0xffff0000, v53\n"
"v_lshlrev_b32 v44, 16, v54\n"
"v_mfma_f32_16x16x32_bf16 v[36:39], a[236:239], v[180:183], v[36:39]\n"
"ds_read_b128 v[164:167], v4 offset:41984\n"
"v_and_b32 v45, 0xffff0000, v54\n"
"v_mfma_f32_16x16x32_bf16 v[32:35], a[232:235], v[180:183], v[32:35]\n"
"v_lshlrev_b32 v46, 16, v55\n"
"v_and_b32 v47, 0xffff0000, v55\n"
"v_mfma_f32_16x16x32_bf16 v[32:35], a[240:243], v[184:187], v[32:35]\n"
"ds_read_b128 v[168:171], v4 offset:43008\n"
"v_mfma_f32_16x16x32_bf16 v[36:39], a[244:247], v[184:187], v[36:39]\n"
"v_mfma_f32_16x16x32_bf16 v[36:39], a[252:255], v[188:191], v[36:39]\n"
"ds_read_b128 v[172:175], v4 offset:44032\n"
"v_mfma_f32_16x16x32_bf16 v[32:35], a[248:251], v[188:191], v[32:35]\n"
"s_waitcnt lgkmcnt(4)\n"
"v_mfma_f32_16x16x32_bf16 v[40:43], a[0:3], v[128:131], v[40:43]\n"
"ds_read_b128 v[176:179], v4 offset:45056\n"
"v_mfma_f32_16x16x32_bf16 v[44:47], a[4:7], v[128:131], v[44:47]\n"
"buffer_load_dwordx4 v[52:55], v2, s[32:35], s71 offen sc1\n"
"v_mfma_f32_16x16x32_bf16 v[44:47], a[12:15], v[132:135], v[44:47]\n"
"ds_read_b128 v[180:183], v4 offset:46080\n"
"s_add_u32 s71, s71, 593920\n"
"v_mfma_f32_16x16x32_bf16 v[40:43], a[8:11], v[132:135], v[40:43]\n"
"s_add_u32 s68, s68, 593920\n"
"v_cvt_pk_bf16_f32 v56, v32, v33\n"
"v_mfma_f32_16x16x32_bf16 v[40:43], a[16:19], v[136:139], v[40:43]\n"
"ds_read_b128 v[184:187], v4 offset:47104\n"
"v_cvt_pk_bf16_f32 v57, v34, v35\n"
"v_mfma_f32_16x16x32_bf16 v[44:47], a[20:23], v[136:139], v[44:47]\n"
"s_add_u32 m0, s64, 32768\n"
"v_cvt_pk_bf16_f32 v58, v36, v37\n"
"v_mfma_f32_16x16x32_bf16 v[44:47], a[28:31], v[140:143], v[44:47]\n"
"ds_read_b128 v[188:191], v4 offset:48128\n"
"v_cvt_pk_bf16_f32 v59, v38, v39\n"
"v_mfma_f32_16x16x32_bf16 v[40:43], a[24:27], v[140:143], v[40:43]\n"
"buffer_load_dwordx4 v2, s[32:35], s63 offen offset:0 sc1 lds\n"
"v_mfma_f32_16x16x32_bf16 v[40:43], a[32:35], v[144:147], v[40:43]\n"
"ds_read_b128 v[128:131], v4 offset:49152\n"
"v_mfma_f32_16x16x32_bf16 v[44:47], a[36:39], v[144:147], v[44:47]\n"
"buffer_load_dwordx2 v[12:13], v14, s[32:35], s66 offen sc1\n"
"v_mfma_f32_16x16x32_bf16 v[44:47], a[44:47], v[148:151], v[44:47]\n"
"ds_read_b128 v[132:135], v4 offset:50176\n"
"v_mfma_f32_16x16x32_bf16 v[40:43], a[40:43], v[148:151], v[40:43]\n"
"buffer_store_dwordx4 v[56:59], v2, s[32:35], s68 offen sc1\n"
"v_mfma_f32_16x16x32_bf16 v[40:43], a[48:51], v[152:155], v[40:43]\n"
"ds_read_b128 v[136:139], v4 offset:51200\n"
"v_add_f32 v60, v32, v33\n"
"v_mfma_f32_16x16x32_bf16 v[44:47], a[52:55], v[152:155], v[44:47]\n"
"buffer_load_dwordx4 v2, s[32:35], s63 offen offset:1024 sc1 lds\n"
"v_mfma_f32_16x16x32_bf16 v[44:47], a[60:63], v[156:159], v[44:47]\n"
"ds_read_b128 v[140:143], v4 offset:52224\n"
"v_mul_f32 v61, v32, v32\n"
"v_mfma_f32_16x16x32_bf16 v[40:43], a[56:59], v[156:159], v[40:43]\n"
"v_fmac_f32 v61, v33, v33\n"
"s_waitcnt lgkmcnt(4)\n"
"v_mfma_f32_16x16x32_bf16 v[40:43], a[64:67], v[160:163], v[40:43]\n"
"ds_read_b128 v[144:147], v4 offset:53248\n"
"v_add_f32 v60, v60, v34\n"
"v_mfma_f32_16x16x32_bf16 v[44:47], a[68:71], v[160:163], v[44:47]\n"
"v_fmac_f32 v61, v34, v34\n"
"v_add_f32 v60, v60, v35\n"
"v_mfma_f32_16x16x32_bf16 v[44:47], a[76:79], v[164:167], v[44:47]\n"
"ds_read_b128 v[148:151], v4 offset:54272\n"
"v_fmac_f32 v61, v35, v35\n"
"v_mfma_f32_16x16x32_bf16 v[40:43], a[72:75], v[164:167], v[40:43]\n"
"buffer_load_dwordx4 v2, s[32:35], s63 offen offset:2048 sc1 lds\n"
"v_mfma_f32_16x16x32_bf16 v[40:43], a[80:83], v[168:171], v[40:43]\n"
"ds_read_b128 v[152:155], v4 offset:55296\n"
"v_add_f32 v60, v60, v36\n"
"v_mfma_f32_16x16x32_bf16 v[44:47], a[84:87], v[168:171], v[44:47]\n"
"v_fmac_f32 v61, v36, v36\n"
"v_add_f32 v60, v60, v37\n"
"v_mfma_f32_16x16x32_bf16 v[44:47], a[92:95], v[172:175], v[44:47]\n"
"ds_read_b128 v[156:159], v4 offset:56320\n"
"v_fmac_f32 v61, v37, v37\n"
"v_mfma_f32_16x16x32_bf16 v[40:43], a[88:91], v[172:175], v[40:43]\n"
"v_add_f32 v60, v60, v38\n"
"v_fmac_f32 v61, v38, v38\n"
"v_mfma_f32_16x16x32_bf16 v[40:43], a[96:99], v[176:179], v[40:43]\n"
"ds_read_b128 v[160:163], v4 offset:57344\n"
"v_add_f32 v60, v60, v39\n"
"v_mfma_f32_16x16x32_bf16 v[44:47], a[100:103], v[176:179], v[44:47]\n"
"buffer_load_dwordx4 v2, s[32:35], s63 offen offset:3072 sc1 lds\n"
"v_mfma_f32_16x16x32_bf16 v[44:47], a[108:111], v[180:183], v[44:47]\n"
"ds_read_b128 v[164:167], v4 offset:58368\n"
"v_fmac_f32 v61, v39, v39\n"
"v_mfma_f32_16x16x32_bf16 v[40:43], a[104:107], v[180:183], v[40:43]\n"
"v_mfma_f32_16x16x32_bf16 v[40:43], a[112:115], v[184:187], v[40:43]\n"
"ds_read_b128 v[168:171], v4 offset:59392\n"
"v_mfma_f32_16x16x32_bf16 v[44:47], a[116:119], v[184:187], v[44:47]\n"
"s_add_u32 m0, s64, 36864\n"
"v_mfma_f32_16x16x32_bf16 v[44:47], a[124:127], v[188:191], v[44:47]\n"
"ds_read_b128 v[172:175], v4 offset:60416\n"
"v_mfma_f32_16x16x32_bf16 v[40:43], a[120:123], v[188:191], v[40:43]\n"
"s_waitcnt lgkmcnt(4)\n"
"v_mfma_f32_16x16x32_bf16 v[40:43], a[128:131], v[128:131], v[40:43]\n"
"ds_read_b128 v[176:179], v4 offset:61440\n"
"v_mfma_f32_16x16x32_bf16 v[44:47], a[132:135], v[128:131], v[44:47]\n"
"buffer_load_dwordx4 v6, s[32:35], s63 offen offset:0 sc1 lds\n"
"v_mfma_f32_16x16x32_bf16 v[44:47], a[140:143], v[132:135], v[44:47]\n"
"ds_read_b128 v[180:183], v4 offset:62464\n"
"v_mfma_f32_16x16x32_bf16 v[40:43], a[136:139], v[132:135], v[40:43]\n"
"v_permlane32_swap_b32 v60, v61\n"
"v_mfma_f32_16x16x32_bf16 v[40:43], a[144:147], v[136:139], v[40:43]\n"
"ds_read_b128 v[184:187], v4 offset:63488\n"
"v_add_f32 v60, v60, v61\n"
"v_mfma_f32_16x16x32_bf16 v[44:47], a[148:151], v[136:139], v[44:47]\n"
"buffer_load_dwordx4 v6, s[32:35], s63 offen offset:1024 sc1 lds\n"
"v_mfma_f32_16x16x32_bf16 v[44:47], a[156:159], v[140:143], v[44:47]\n"
"ds_read_b128 v[188:191], v4 offset:64512\n"
"v_xor_b32 v4, 65536, v4\n"
"v_mfma_f32_16x16x32_bf16 v[40:43], a[152:155], v[140:143], v[40:43]\n"
"s_waitcnt vmcnt(10)\n"
"s_waitcnt vmcnt(9)\n"
"s_barrier\n"
"v_mfma_f32_16x16x32_bf16 v[40:43], a[160:163], v[144:147], v[40:43]\n"
"ds_read_b128 v[128:131], v4 offset:0\n"
"v_mfma_f32_16x16x32_bf16 v[44:47], a[164:167], v[144:147], v[44:47]\n"
"buffer_store_dwordx2 v[16:17], v2, s[92:95], s68 idxen sc1\n"
"v_mfma_f32_16x16x32_bf16 v[44:47], a[172:175], v[148:151], v[44:47]\n"
"ds_read_b128 v[132:135], v4 offset:1024\n"
"v_mfma_f32_16x16x32_bf16 v[40:43], a[168:171], v[148:151], v[40:43]\n"
"buffer_load_dwordx4 v6, s[32:35], s63 offen offset:2048 sc1 lds\n"
"v_mfma_f32_16x16x32_bf16 v[40:43], a[176:179], v[152:155], v[40:43]\n"
"ds_read_b128 v[136:139], v4 offset:2048\n"
"v_mfma_f32_16x16x32_bf16 v[44:47], a[180:183], v[152:155], v[44:47]\n"
"v_permlane16_swap_b32 v60, v61\n"
"v_mfma_f32_16x16x32_bf16 v[44:47], a[188:191], v[156:159], v[44:47]\n"
"ds_read_b128 v[140:143], v4 offset:3072\n"
"v_add_f32 v60, v60, v61\n"
"v_mfma_f32_16x16x32_bf16 v[40:43], a[184:187], v[156:159], v[40:43]\n"
"s_waitcnt lgkmcnt(4)\n"
"v_mfma_f32_16x16x32_bf16 v[40:43], a[192:195], v[160:163], v[40:43]\n"
"ds_read_b128 v[144:147], v4 offset:4096\n"
"v_mfma_f32_16x16x32_bf16 v[44:47], a[196:199], v[160:163], v[44:47]\n"
"buffer_load_dwordx4 v6, s[32:35], s63 offen offset:3072 sc1 lds\n"
"v_mfma_f32_16x16x32_bf16 v[44:47], a[204:207], v[164:167], v[44:47]\n"
"ds_read_b128 v[148:151], v4 offset:5120\n"
"s_add_u32 s63, s63, 593920\n"
"v_mfma_f32_16x16x32_bf16 v[40:43], a[200:203], v[164:167], v[40:43]\n"
"s_xor_b32 s64, s64, s65\n"
"v_mfma_f32_16x16x32_bf16 v[40:43], a[208:211], v[168:171], v[40:43]\n"
"ds_read_b128 v[152:155], v4 offset:6144\n"
"v_mfma_f32_16x16x32_bf16 v[44:47], a[212:215], v[168:171], v[44:47]\n"
"v_permlane32_swap_b32 v60, v61\n"
"v_mfma_f32_16x16x32_bf16 v[44:47], a[220:223], v[172:175], v[44:47]\n"
"ds_read_b128 v[156:159], v4 offset:7168\n"
"v_mfma_f32_16x16x32_bf16 v[40:43], a[216:219], v[172:175], v[40:43]\n"
"buffer_store_dwordx2 v[60:61], v15, s[32:35], s68 offen offset:1024 sc1\n"
"v_mfma_f32_16x16x32_bf16 v[40:43], a[224:227], v[176:179], v[40:43]\n"
"ds_read_b128 v[160:163], v4 offset:8192\n"
"v_mfma_f32_16x16x32_bf16 v[44:47], a[228:231], v[176:179], v[44:47]\n"
"v_lshlrev_b32 v32, 16, v48\n"
"v_and_b32 v33, 0xffff0000, v48\n"
"v_mfma_f32_16x16x32_bf16 v[44:47], a[236:239], v[180:183], v[44:47]\n"
"ds_read_b128 v[164:167], v4 offset:9216\n"
"v_lshlrev_b32 v34, 16, v49\n"
"v_mfma_f32_16x16x32_bf16 v[40:43], a[232:235], v[180:183], v[40:43]\n"
"v_and_b32 v35, 0xffff0000, v49\n"
"v_lshlrev_b32 v36, 16, v50\n"
"v_mfma_f32_16x16x32_bf16 v[40:43], a[240:243], v[184:187], v[40:43]\n"
"ds_read_b128 v[168:171], v4 offset:10240\n"
"v_and_b32 v37, 0xffff0000, v50\n"
"v_mfma_f32_16x16x32_bf16 v[44:47], a[244:247], v[184:187], v[44:47]\n"
"v_lshlrev_b32 v38, 16, v51\n"
"v_and_b32 v39, 0xffff0000, v51\n"
"v_mfma_f32_16x16x32_bf16 v[44:47], a[252:255], v[188:191], v[44:47]\n"
"ds_read_b128 v[172:175], v4 offset:11264\n"
"s_sub_u32 s62, s62, 1\n"
"v_mfma_f32_16x16x32_bf16 v[40:43], a[248:251], v[188:191], v[40:43]\n"
"s_cbranch_scc0 .L_wl\n"
"s_nop 1\n"
"s_add_u32 s68, s68, 593920\n"
"s_nop 3\n"
"v_cvt_pk_bf16_f32 v56, v40, v41\n"
"v_cvt_pk_bf16_f32 v57, v42, v43\n"
"v_cvt_pk_bf16_f32 v58, v44, v45\n"
"v_cvt_pk_bf16_f32 v59, v46, v47\n"
"buffer_store_dwordx4 v[56:59], v2, s[32:35], s68 offen sc1\n"
"v_add_f32 v60, v40, v41\n"
"v_mul_f32 v61, v40, v40\n"
"v_fmac_f32 v61, v41, v41\n"
"v_add_f32 v60, v60, v42\n"
"v_fmac_f32 v61, v42, v42\n"
"v_add_f32 v60, v60, v43\n"
"v_fmac_f32 v61, v43, v43\n"
"v_add_f32 v60, v60, v44\n"
"v_fmac_f32 v61, v44, v44\n"
"v_add_f32 v60, v60, v45\n"
"v_fmac_f32 v61, v45, v45\n"
"v_add_f32 v60, v60, v46\n"
"v_fmac_f32 v61, v46, v46\n"
"v_add_f32 v60, v60, v47\n"
"v_fmac_f32 v61, v47, v47\n"
"s_nop 1\n"
"v_permlane32_swap_b32 v60, v61\n"
"v_add_f32 v60, v60, v61\n"
"s_nop 1\n"
"v_permlane16_swap_b32 v60, v61\n"
"v_add_f32 v60, v60, v61\n"
"s_nop 1\n"
"v_permlane32_swap_b32 v60, v61\n"
"buffer_store_dwordx2 v[60:61], v15, s[32:35], s68 offen offset:1024 sc1\n"
"s_waitcnt vmcnt(2)\n"
"s_barrier\n"
"buffer_store_dwordx2 v[16:17], v2, s[92:95], s68 idxen sc1\n"
"s_add_u32 s68, s68, 593920\n"
"s_waitcnt vmcnt(1)\n"
"s_barrier\n"
"buffer_store_dwordx2 v[16:17], v2, s[92:95], s68 idxen sc1\n"
"s_branch .L_end\n"
".L_h:\n"
"v_mov_b32 v16, 0x3f803f80\n"
"v_mov_b32 v17, 0x3f803f80\n"
"v_mov_b32 v18, 0x3f803f80\n"
"v_mov_b32 v19, 0x3f803f80\n"
"s_add_u32 s36, s14, 0x0\n"
"s_addc_u32 s37, s15, 0x0\n"
"s_and_b32 s37, s37, 0xffff\n"
"s_mov_b32 s38, 0x700000\n"
"s_mov_b32 s39, 0x20000\n"
"s_add_u32 s40, s16, 0xfffffc00\n"
"s_addc_u32 s41, s17, 0xffffffff\n"
"s_and_b32 s41, s41, 0xffff\n"
"s_mov_b32 s42, 0x700400\n"
"s_mov_b32 s43, 0x20000\n"
"s_lshl_b32 s96, s27, 7\n"
"s_add_u32 s96, s96, 6912\n"
"v_and_b32 v8, 1, v1\n"
"v_bfe_u32 v3, v1, 1, 1\n"
"v_lshl_add_u32 v8, v8, 1, v3\n"
"v_mul_u32_u24 v8, 8, v8\n"
"s_lshl_b32 s48, s26, 5\n"
"v_add_u32 v8, s48, v8\n"
"v_bfe_u32 v3, v1, 4, 1\n"
"v_lshl_add_u32 v8, v3, 10, v8\n"
"v_lshrrev_b32 v4, 3, v1\n"
"v_bfe_u32 v5, v1, 4, 1\n"
"s_and_b32 s48, s26, 1\n"
"s_lshl_b32 s48, s48, 1\n"
"v_or_b32 v5, s48, v5\n"
"v_lshlrev_b32 v5, 1, v5\n"
"v_and_b32 v6, 7, v1\n"
"v_xor_b32 v5, v5, v6\n"
"v_mul_u32_u24 v4, 7168, v4\n"
"v_lshl_add_u32 v7, v5, 4, v4\n"
"s_mul_i32 s48, s26, 57344\n"
"s_add_u32 s96, s96, s48\n"
"s_mul_i32 s97, s26, 2048\n"
"s_add_u32 m0, s97, 0\n"
"s_nop 0\n"
"buffer_load_dwordx4 v7, s[36:39], s96 offen offset:0 lds\n"
"buffer_load_dwordx4 v7, s[40:43], s96 offen offset:1024 lds\n"
"s_add_u32 s96, s96, 229376\n"
"s_add_u32 m0, s97, 8192\n"
"s_nop 0\n"
"buffer_load_dwordx4 v7, s[36:39], s96 offen offset:0 lds\n"
"buffer_load_dwordx4 v7, s[40:43], s96 offen offset:1024 lds\n"
"s_add_u32 s96, s96, 229376\n"
"s_add_u32 m0, s97, 16384\n"
"s_nop 0\n"
"buffer_load_dwordx4 v7, s[36:39], s96 offen offset:0 lds\n"
"buffer_load_dwordx4 v7, s[40:43], s96 offen offset:1024 lds\n"
"s_add_u32 s96, s96, 229376\n"
"s_add_u32 m0, s97, 24576\n"
"s_nop 0\n"
"buffer_load_dwordx4 v7, s[36:39], s96 offen offset:0 lds\n"
"buffer_load_dwordx4 v7, s[40:43], s96 offen offset:1024 lds\n"
"s_add_u32 s96, s96, 229376\n"
"s_add_u32 m0, s97, 32768\n"
"s_nop 0\n"
"buffer_load_dwordx4 v7, s[36:39], s96 offen offset:0 lds\n"
"buffer_load_dwordx4 v7, s[40:43], s96 offen offset:1024 lds\n"
"s_add_u32 s96, s96, 229376\n"
"s_add_u32 m0, s97, 40960\n"
"s_nop 0\n"
"buffer_load_dwordx4 v7, s[36:39], s96 offen offset:0 lds\n"
"buffer_load_dwordx4 v7, s[40:43], s96 offen offset:1024 lds\n"
"s_add_u32 s96, s96, 229376\n"
"s_add_u32 m0, s97, 49152\n"
"s_nop 0\n"
"buffer_load_dwordx4 v7, s[36:39], s96 offen offset:0 lds\n"
"buffer_load_dwordx4 v7, s[40:43], s96 offen offset:1024 lds\n"
"s_add_u32 s96, s96, 229376\n"
"s_add_u32 m0, s97, 57344\n"
"s_nop 0\n"
"buffer_load_dwordx4 v7, s[36:39], s96 offen offset:0 lds\n"
"buffer_load_dwordx4 v7, s[40:43], s96 offen offset:1024 lds\n"
"s_add_u32 s96, s96, 229376\n"
"s_add_u32 m0, s97, 65536\n"
"s_nop 0\n"
"buffer_load_dwordx4 v7, s[36:39], s96 offen offset:0 lds\n"
"buffer_load_dwordx4 v7, s[40:43], s96 offen offset:1024 lds\n"
"s_add_u32 s96, s96, 229376\n"
"s_add_u32 m0, s97, 73728\n"
"s_nop 0\n"
"buffer_load_dwordx4 v7, s[36:39], s96 offen offset:0 lds\n"
"buffer_load_dwordx4 v7, s[40:43], s96 offen offset:1024 lds\n"
"s_add_u32 s96, s96, 229376\n"
"s_add_u32 m0, s97, 81920\n"
"s_nop 0\n"
"buffer_load_dwordx4 v7, s[36:39], s96 offen offset:0 lds\n"
"buffer_load_dwordx4 v7, s[40:43], s96 offen offset:1024 lds\n"
"s_add_u32 s96, s96, 229376\n"
"s_add_u32 m0, s97, 90112\n"
"s_nop 0\n"
"buffer_load_dwordx4 v7, s[36:39], s96 offen offset:0 lds\n"
"buffer_load_dwordx4 v7, s[40:43], s96 offen offset:1024 lds\n"
"s_add_u32 s96, s96, 229376\n"
"s_add_u32 m0, s97, 98304\n"
"s_nop 0\n"
"buffer_load_dwordx4 v7, s[36:39], s96 offen offset:0 lds\n"
"buffer_load_dwordx4 v7, s[40:43], s96 offen offset:1024 lds\n"
"s_add_u32 s96, s96, 229376\n"
"s_add_u32 m0, s97, 106496\n"
"s_nop 0\n"
"buffer_load_dwordx4 v7, s[36:39], s96 offen offset:0 lds\n"
"buffer_load_dwordx4 v7, s[40:43], s96 offen offset:1024 lds\n"
"s_add_u32 s96, s96, 229376\n"
"v_lshrrev_b32 v5, 5, v1\n"
"v_bfe_u32 v4, v1, 3, 1\n"
"v_and_b32 v6, 1, v5\n"
"v_lshl_add_u32 v4, v6, 1, v4\n"
"v_lshlrev_b32 v4, 5, v4\n"
"v_mul_u32_u24 v5, 2048, v5\n"
"v_bfe_u32 v6, v1, 2, 2\n"
"v_lshl_add_u32 v5, v6, 7, v5\n"
"v_xor_b32 v8, v4, v8\n"
"v_add_u32 v8, v5, v8\n"
"v_add_u32 v9, 65536, v8\n"
"s_waitcnt vmcnt(24)\n"
"s_barrier\n"
"s_add_u32 m0, s97, 114688\n"
"s_nop 0\n"
"buffer_load_dwordx4 v7, s[36:39], s96 offen offset:0 lds\n"
"buffer_load_dwordx4 v7, s[40:43], s96 offen offset:1024 lds\n"
"s_add_u32 s96, s96, 229376\n"
"s_add_u32 m0, s97, 122880\n"
"s_nop 0\n"
"buffer_load_dwordx4 v7, s[36:39], s96 offen offset:0 lds\n"
"buffer_load_dwordx4 v7, s[40:43], s96 offen offset:1024 lds\n"
"s_add_u32 s96, s96, 229376\n"
"ds_read_b64_tr_b16 a[0:1], v8 offset:0\n"
"ds_read_b64_tr_b16 a[2:3], v8 offset:512\n"
"ds_read_b64_tr_b16 a[4:5], v8 offset:4096\n"
"ds_read_b64_tr_b16 a[6:7], v8 offset:4608\n"
"ds_read_b64_tr_b16 a[32:33], v8 offset:8192\n"
"ds_read_b64_tr_b16 a[34:35], v8 offset:8704\n"
"ds_read_b64_tr_b16 a[36:37], v8 offset:12288\n"
"ds_read_b64_tr_b16 a[38:39], v8 offset:12800\n"
"s_waitcnt lgkmcnt(0)\n"
"v_mfma_f32_32x32x16_bf16 v[112:127], a[0:3], v[16:19], 0\n"
"v_mfma_f32_32x32x16_bf16 v[112:127], a[4:7], v[16:19], v[112:127]\n"
"v_mfma_f32_32x32x16_bf16 v[112:127], a[32:35], v[16:19], v[112:127]\n"
"v_mfma_f32_32x32x16_bf16 v[112:127], a[36:39], v[16:19], v[112:127]\n"
"s_waitcnt vmcnt(24)\n"
"s_barrier\n"
"s_add_u32 m0, s97, 0\n"
"s_nop 0\n"
"buffer_load_dwordx4 v7, s[36:39], s96 offen offset:0 lds\n"
"buffer_load_dwordx4 v7, s[40:43], s96 offen offset:1024 lds\n"
"s_add_u32 s96, s96, 229376\n"
"s_add_u32 m0, s97, 8192\n"
"s_nop 0\n"
"buffer_load_dwordx4 v7, s[36:39], s96 offen offset:0 lds\n"
"buffer_load_dwordx4 v7, s[40:43], s96 offen offset:1024 lds\n"
"s_add_u32 s96, s96, 229376\n"
"ds_read_b64_tr_b16 a[64:65], v8 offset:16384\n"
"ds_read_b64_tr_b16 a[66:67], v8 offset:16896\n"
"ds_read_b64_tr_b16 a[68:69], v8 offset:20480\n"
"ds_read_b64_tr_b16 a[70:71], v8 offset:20992\n"
"ds_read_b64_tr_b16 a[96:97], v8 offset:24576\n"
"ds_read_b64_tr_b16 a[98:99], v8 offset:25088\n"
"ds_read_b64_tr_b16 a[100:101], v8 offset:28672\n"
"ds_read_b64_tr_b16 a[102:103], v8 offset:29184\n"
"s_waitcnt lgkmcnt(0)\n"
"v_mfma_f32_32x32x16_bf16 v[112:127], a[64:67], v[16:19], v[112:127]\n"
"v_mfma_f32_32x32x16_bf16 v[112:127], a[68:71], v[16:19], v[112:127]\n"
"v_mfma_f32_32x32x16_bf16 v[112:127], a[96:99], v[16:19], v[112:127]\n"
"v_mfma_f32_32x32x16_bf16 v[112:127], a[100:103], v[16:19], v[112:127]\n"
"s_waitcnt vmcnt(24)\n"
"s_barrier\n"
"s_add_u32 m0, s97, 16384\n"
"s_nop 0\n"
"buffer_load_dwordx4 v7, s[36:39], s96 offen offset:0 lds\n"
"buffer_load_dwordx4 v7, s[40:43], s96 offen offset:1024 lds\n"
"s_add_u32 s96, s96, 229376\n"
"s_add_u32 m0, s97, 24576\n"
"s_nop 0\n"
"buffer_load_dwordx4 v7, s[36:39], s96 offen offset:0 lds\n"
"buffer_load_dwordx4 v7, s[40:43], s96 offen offset:1024 lds\n"
"s_add_u32 s96, s96, 229376\n"
"ds_read_b64_tr_b16 a[128:129], v8 offset:32768\n"
"ds_read_b64_tr_b16 a[130:131], v8 offset:33280\n"
"ds_read_b64_tr_b16 a[132:133], v8 offset:36864\n"
"ds_read_b64_tr_b16 a[134:135], v8 offset:37376\n"
"ds_read_b64_tr_b16 a[160:161], v8 offset:40960\n"
"ds_read_b64_tr_b16 a[162:163], v8 offset:41472\n"
"ds_read_b64_tr_b16 a[164:165], v8 offset:45056\n"
"ds_read_b64_tr_b16 a[166:167], v8 offset:45568\n"
"s_waitcnt lgkmcnt(0)\n"
"v_mfma_f32_32x32x16_bf16 v[112:127], a[128:131], v[16:19], v[112:127]\n"
"v_mfma_f32_32x32x16_bf16 v[112:127], a[132:135], v[16:19], v[112:127]\n"
"v_mfma_f32_32x32x16_bf16 v[112:127], a[160:163], v[16:19], v[112:127]\n"
"v_mfma_f32_32x32x16_bf16 v[112:127], a[164:167], v[16:19], v[112:127]\n"
"s_waitcnt vmcnt(24)\n"
"s_barrier\n"
"s_add_u32 m0, s97, 32768\n"
"s_nop 0\n"
"buffer_load_dwordx4 v7, s[36:39], s96 offen offset:0 lds\n"
"buffer_load_dwordx4 v7, s[40:43], s96 offen offset:1024 lds\n"
"s_add_u32 s96, s96, 229376\n"
"s_add_u32 m0, s97, 40960\n"
"s_nop 0\n"
"buffer_load_dwordx4 v7, s[36:39], s96 offen offset:0 lds\n"
"buffer_load_dwordx4 v7, s[40:43], s96 offen offset:1024 lds\n"
"s_add_u32 s96, s96, 229376\n"
"ds_read_b64_tr_b16 a[192:193], v8 offset:49152\n"
"ds_read_b64_tr_b16 a[194:195], v8 offset:49664\n"
"ds_read_b64_tr_b16 a[196:197], v8 offset:53248\n"
"ds_read_b64_tr_b16 a[198:199], v8 offset:53760\n"
"ds_read_b64_tr_b16 a[224:225], v8 offset:57344\n"
"ds_read_b64_tr_b16 a[226:227], v8 offset:57856\n"
"ds_read_b64_tr_b16 a[228:229], v8 offset:61440\n"
"ds_read_b64_tr_b16 a[230:231], v8 offset:61952\n"
"s_waitcnt lgkmcnt(0)\n"
"v_mfma_f32_32x32x16_bf16 v[112:127], a[192:195], v[16:19], v[112:127]\n"
"v_mfma_f32_32x32x16_bf16 v[112:127], a[196:199], v[16:19], v[112:127]\n"
"v_mfma_f32_32x32x16_bf16 v[112:127], a[224:227], v[16:19], v[112:127]\n"
"v_mfma_f32_32x32x16_bf16 v[112:127], a[228:231], v[16:19], v[112:127]\n"
"s_waitcnt vmcnt(24)\n"
"s_barrier\n"
"s_add_u32 m0, s97, 49152\n"
"s_nop 0\n"
"buffer_load_dwordx4 v7, s[36:39], s96 offen offset:0 lds\n"
"buffer_load_dwordx4 v7, s[40:43], s96 offen offset:1024 lds\n"
"s_add_u32 s96, s96, 229376\n"
"s_add_u32 m0, s97, 57344\n"
"s_nop 0\n"
"buffer_load_dwordx4 v7, s[36:39], s96 offen offset:0 lds\n"
"buffer_load_dwordx4 v7, s[40:43], s96 offen offset:1024 lds\n"
"s_add_u32 s96, s96, 229376\n"
"ds_read_b64_tr_b16 a[8:9], v9 offset:0\n"
"ds_read_b64_tr_b16 a[10:11], v9 offset:512\n"
"ds_read_b64_tr_b16 a[12:13], v9 offset:4096\n"
"ds_read_b64_tr_b16 a[14:15], v9 offset:4608\n"
"ds_read_b64_tr_b16 a[40:41], v9 offset:8192\n"
"ds_read_b64_tr_b16 a[42:43], v9 offset:8704\n"
"ds_read_b64_tr_b16 a[44:45], v9 offset:12288\n"
"ds_read_b64_tr_b16 a[46:47], v9 offset:12800\n"
"s_waitcnt lgkmcnt(0)\n"
"v_mfma_f32_32x32x16_bf16 v[112:127], a[8:11], v[16:19], v[112:127]\n"
"v_mfma_f32_32x32x16_bf16 v[112:127], a[12:15], v[16:19], v[112:127]\n"
"v_mfma_f32_32x32x16_bf16 v[112:127], a[40:43], v[16:19], v[112:127]\n"
"v_mfma_f32_32x32x16_bf16 v[112:127], a[44:47], v[16:19], v[112:127]\n"
"s_waitcnt vmcnt(24)\n"
"s_barrier\n"
"s_add_u32 m0, s97, 65536\n"
"s_nop 0\n"
"buffer_load_dwordx4 v7, s[36:39], s96 offen offset:0 lds\n"
"buffer_load_dwordx4 v7, s[40:43], s96 offen offset:1024 lds\n"
"s_add_u32 s96, s96, 229376\n"
"s_add_u32 m0, s97, 73728\n"
"s_nop 0\n"
"buffer_load_dwordx4 v7, s[36:39], s96 offen offset:0 lds\n"
"buffer_load_dwordx4 v7, s[40:43], s96 offen offset:1024 lds\n"
"s_add_u32 s96, s96, 229376\n"
"ds_read_b64_tr_b16 a[72:73], v9 offset:16384\n"
"ds_read_b64_tr_b16 a[74:75], v9 offset:16896\n"
"ds_read_b64_tr_b16 a[76:77], v9 offset:20480\n"
"ds_read_b64_tr_b16 a[78:79], v9 offset:20992\n"
"ds_read_b64_tr_b16 a[104:105], v9 offset:24576\n"
"ds_read_b64_tr_b16 a[106:107], v9 offset:25088\n"
"ds_read_b64_tr_b16 a[108:109], v9 offset:28672\n"
"ds_read_b64_tr_b16 a[110:111], v9 offset:29184\n"
"s_waitcnt lgkmcnt(0)\n"
"v_mfma_f32_32x32x16_bf16 v[112:127], a[72:75], v[16:19], v[112:127]\n"
"v_mfma_f32_32x32x16_bf16 v[112:127], a[76:79], v[16:19], v[112:127]\n"
"v_mfma_f32_32x32x16_bf16 v[112:127], a[104:107], v[16:19], v[112:127]\n"
"v_mfma_f32_32x32x16_bf16 v[112:127], a[108:111], v[16:19], v[112:127]\n"
"s_waitcnt vmcnt(24)\n"
"s_barrier\n"
"s_add_u32 m0, s97, 81920\n"
"s_nop 0\n"
"buffer_load_dwordx4 v7, s[36:39], s96 offen offset:0 lds\n"
"buffer_load_dwordx4 v7, s[40:43], s96 offen offset:1024 lds\n"
"s_add_u32 s96, s96, 229376\n"
"s_add_u32 m0, s97, 90112\n"
"s_nop 0\n"
"buffer_load_dwordx4 v7, s[36:39], s96 offen offset:0 lds\n"
"buffer_load_dwordx4 v7, s[40:43], s96 offen offset:1024 lds\n"
"s_add_u32 s96, s96, 229376\n"
"ds_read_b64_tr_b16 a[136:137], v9 offset:32768\n"
"ds_read_b64_tr_b16 a[138:139], v9 offset:33280\n"
"ds_read_b64_tr_b16 a[140:141], v9 offset:36864\n"
"ds_read_b64_tr_b16 a[142:143], v9 offset:37376\n"
"ds_read_b64_tr_b16 a[168:169], v9 offset:40960\n"
"ds_read_b64_tr_b16 a[170:171], v9 offset:41472\n"
"ds_read_b64_tr_b16 a[172:173], v9 offset:45056\n"
"ds_read_b64_tr_b16 a[174:175], v9 offset:45568\n"
"s_waitcnt lgkmcnt(0)\n"
"v_mfma_f32_32x32x16_bf16 v[112:127], a[136:139], v[16:19], v[112:127]\n"
"v_mfma_f32_32x32x16_bf16 v[112:127], a[140:143], v[16:19], v[112:127]\n"
"v_mfma_f32_32x32x16_bf16 v[112:127], a[168:171], v[16:19], v[112:127]\n"
"v_mfma_f32_32x32x16_bf16 v[112:127], a[172:175], v[16:19], v[112:127]\n"
"s_waitcnt vmcnt(24)\n"
"s_barrier\n"
"s_add_u32 m0, s97, 98304\n"
"s_nop 0\n"
"buffer_load_dwordx4 v7, s[36:39], s96 offen offset:0 lds\n"
"buffer_load_dwordx4 v7, s[40:43], s96 offen offset:1024 lds\n"
"s_add_u32 s96, s96, 229376\n"
"s_add_u32 m0, s97, 106496\n"
"s_nop 0\n"
"buffer_load_dwordx4 v7, s[36:39], s96 offen offset:0 lds\n"
"buffer_load_dwordx4 v7, s[40:43], s96 offen offset:1024 lds\n"
"s_add_u32 s96, s96, 229376\n"
"ds_read_b64_tr_b16 a[200:201], v9 offset:49152\n"
"ds_read_b64_tr_b16 a[202:203], v9 offset:49664\n"
"ds_read_b64_tr_b16 a[204:205], v9 offset:53248\n"
"ds_read_b64_tr_b16 a[206:207], v9 offset:53760\n"
"ds_read_b64_tr_b16 a[232:233], v9 offset:57344\n"
"ds_read_b64_tr_b16 a[234:235], v9 offset:57856\n"
"ds_read_b64_tr_b16 a[236:237], v9 offset:61440\n"
"ds_read_b64_tr_b16 a[238:239], v9 offset:61952\n"
"s_waitcnt lgkmcnt(0)\n"
"v_mfma_f32_32x32x16_bf16 v[112:127], a[200:203], v[16:19], v[112:127]\n"
"v_mfma_f32_32x32x16_bf16 v[112:127], a[204:207], v[16:19], v[112:127]\n"
"v_mfma_f32_32x32x16_bf16 v[112:127], a[232:235], v[16:19], v[112:127]\n"
"v_mfma_f32_32x32x16_bf16 v[112:127], a[236:239], v[16:19], v[112:127]\n"
"s_waitcnt vmcnt(24)\n"
"s_barrier\n"
"s_add_u32 m0, s97, 114688\n"
"s_nop 0\n"
"buffer_load_dwordx4 v7, s[36:39], s96 offen offset:0 lds\n"
"buffer_load_dwordx4 v7, s[40:43], s96 offen offset:1024 lds\n"
"s_add_u32 s96, s96, 229376\n"
"s_add_u32 m0, s97, 122880\n"
"s_nop 0\n"
"buffer_load_dwordx4 v7, s[36:39], s96 offen offset:0 lds\n"
"buffer_load_dwordx4 v7, s[40:43], s96 offen offset:1024 lds\n"
"s_add_u32 s96, s96, 229376\n"
"ds_read_b64_tr_b16 a[16:17], v8 offset:0\n"
"ds_read_b64_tr_b16 a[18:19], v8 offset:512\n"
"ds_read_b64_tr_b16 a[20:21], v8 offset:4096\n"
"ds_read_b64_tr_b16 a[22:23], v8 offset:4608\n"
"ds_read_b64_tr_b16 a[48:49], v8 offset:8192\n"
"ds_read_b64_tr_b16 a[50:51], v8 offset:8704\n"
"ds_read_b64_tr_b16 a[52:53], v8 offset:12288\n"
"ds_read_b64_tr_b16 a[54:55], v8 offset:12800\n"
"s_waitcnt lgkmcnt(0)\n"
"v_mfma_f32_32x32x16_bf16 v[112:127], a[16:19], v[16:19], v[112:127]\n"
"v_mfma_f32_32x32x16_bf16 v[112:127], a[20:23], v[16:19], v[112:127]\n"
"v_mfma_f32_32x32x16_bf16 v[112:127], a[48:51], v[16:19], v[112:127]\n"
"v_mfma_f32_32x32x16_bf16 v[112:127], a[52:55], v[16:19], v[112:127]\n"
"s_waitcnt vmcnt(24)\n"
"s_barrier\n"
"ds_read_b64_tr_b16 a[80:81], v8 offset:16384\n"
"ds_read_b64_tr_b16 a[82:83], v8 offset:16896\n"
"ds_read_b64_tr_b16 a[84:85], v8 offset:20480\n"
"ds_read_b64_tr_b16 a[86:87], v8 offset:20992\n"
"ds_read_b64_tr_b16 a[112:113], v8 offset:24576\n"
"ds_read_b64_tr_b16 a[114:115], v8 offset:25088\n"
"ds_read_b64_tr_b16 a[116:117], v8 offset:28672\n"
"ds_read_b64_tr_b16 a[118:119], v8 offset:29184\n"
"s_waitcnt lgkmcnt(0)\n"
"v_mfma_f32_32x32x16_bf16 v[112:127], a[80:83], v[16:19], v[112:127]\n"
"v_mfma_f32_32x32x16_bf16 v[112:127], a[84:87], v[16:19], v[112:127]\n"
"v_mfma_f32_32x32x16_bf16 v[112:127], a[112:115], v[16:19], v[112:127]\n"
"v_mfma_f32_32x32x16_bf16 v[112:127], a[116:119], v[16:19], v[112:127]\n"
"s_waitcnt vmcnt(20)\n"
"s_barrier\n"
"ds_read_b64_tr_b16 a[144:145], v8 offset:32768\n"
"ds_read_b64_tr_b16 a[146:147], v8 offset:33280\n"
"ds_read_b64_tr_b16 a[148:149], v8 offset:36864\n"
"ds_read_b64_tr_b16 a[150:151], v8 offset:37376\n"
"ds_read_b64_tr_b16 a[176:177], v8 offset:40960\n"
"ds_read_b64_tr_b16 a[178:179], v8 offset:41472\n"
"ds_read_b64_tr_b16 a[180:181], v8 offset:45056\n"
"ds_read_b64_tr_b16 a[182:183], v8 offset:45568\n"
"s_waitcnt lgkmcnt(0)\n"
"v_mfma_f32_32x32x16_bf16 v[112:127], a[144:147], v[16:19], v[112:127]\n"
"v_mfma_f32_32x32x16_bf16 v[112:127], a[148:151], v[16:19], v[112:127]\n"
"v_mfma_f32_32x32x16_bf16 v[112:127], a[176:179], v[16:19], v[112:127]\n"
"v_mfma_f32_32x32x16_bf16 v[112:127], a[180:183], v[16:19], v[112:127]\n"
"s_waitcnt vmcnt(16)\n"
"s_barrier\n"
"ds_read_b64_tr_b16 a[208:209], v8 offset:49152\n"
"ds_read_b64_tr_b16 a[210:211], v8 offset:49664\n"
"ds_read_b64_tr_b16 a[212:213], v8 offset:53248\n"
"ds_read_b64_tr_b16 a[214:215], v8 offset:53760\n"
"ds_read_b64_tr_b16 a[240:241], v8 offset:57344\n"
"ds_read_b64_tr_b16 a[242:243], v8 offset:57856\n"
"ds_read_b64_tr_b16 a[244:245], v8 offset:61440\n"
"ds_read_b64_tr_b16 a[246:247], v8 offset:61952\n"
"s_waitcnt lgkmcnt(0)\n"
"v_mfma_f32_32x32x16_bf16 v[112:127], a[208:211], v[16:19], v[112:127]\n"
"v_mfma_f32_32x32x16_bf16 v[112:127], a[212:215], v[16:19], v[112:127]\n"
"v_mfma_f32_32x32x16_bf16 v[112:127], a[240:243], v[16:19], v[112:127]\n"
"v_mfma_f32_32x32x16_bf16 v[112:127], a[244:247], v[16:19], v[112:127]\n"
"s_waitcnt vmcnt(12)\n"
"s_barrier\n"
"ds_read_b64_tr_b16 a[24:25], v9 offset:0\n"
"ds_read_b64_tr_b16 a[26:27], v9 offset:512\n"
"ds_read_b64_tr_b16 a[28:29], v9 offset:4096\n"
"ds_read_b64_tr_b16 a[30:31], v9 offset:4608\n"
"ds_read_b64_tr_b16 a[56:57], v9 offset:8192\n"
"ds_read_b64_tr_b16 a[58:59], v9 offset:8704\n"
"ds_read_b64_tr_b16 a[60:61], v9 offset:12288\n"
"ds_read_b64_tr_b16 a[62:63], v9 offset:12800\n"
"s_waitcnt lgkmcnt(0)\n"
"v_mfma_f32_32x32x16_bf16 v[112:127], a[24:27], v[16:19], v[112:127]\n"
"v_mfma_f32_32x32x16_bf16 v[112:127], a[28:31], v[16:19], v[112:127]\n"
"v_mfma_f32_32x32x16_bf16 v[112:127], a[56:59], v[16:19], v[112:127]\n"
"v_mfma_f32_32x32x16_bf16 v[112:127], a[60:63], v[16:19], v[112:127]\n"
"s_waitcnt vmcnt(8)\n"
"s_barrier\n"
"ds_read_b64_tr_b16 a[88:89], v9 offset:16384\n"
"ds_read_b64_tr_b16 a[90:91], v9 offset:16896\n"
"ds_read_b64_tr_b16 a[92:93], v9 offset:20480\n"
"ds_read_b64_tr_b16 a[94:95], v9 offset:20992\n"
"ds_read_b64_tr_b16 a[120:121], v9 offset:24576\n"
"ds_read_b64_tr_b16 a[122:123], v9 offset:25088\n"
"ds_read_b64_tr_b16 a[124:125], v9 offset:28672\n"
"ds_read_b64_tr_b16 a[126:127], v9 offset:29184\n"
"s_waitcnt lgkmcnt(0)\n"
"v_mfma_f32_32x32x16_bf16 v[112:127], a[88:91], v[16:19], v[112:127]\n"
"v_mfma_f32_32x32x16_bf16 v[112:127], a[92:95], v[16:19], v[112:127]\n"
"v_mfma_f32_32x32x16_bf16 v[112:127], a[120:123], v[16:19], v[112:127]\n"
"v_mfma_f32_32x32x16_bf16 v[112:127], a[124:127], v[16:19], v[112:127]\n"
"s_waitcnt vmcnt(4)\n"
"s_barrier\n"
"ds_read_b64_tr_b16 a[152:153], v9 offset:32768\n"
"ds_read_b64_tr_b16 a[154:155], v9 offset:33280\n"
"ds_read_b64_tr_b16 a[156:157], v9 offset:36864\n"
"ds_read_b64_tr_b16 a[158:159], v9 offset:37376\n"
"ds_read_b64_tr_b16 a[184:185], v9 offset:40960\n"
"ds_read_b64_tr_b16 a[186:187], v9 offset:41472\n"
"ds_read_b64_tr_b16 a[188:189], v9 offset:45056\n"
"ds_read_b64_tr_b16 a[190:191], v9 offset:45568\n"
"s_waitcnt lgkmcnt(0)\n"
"v_mfma_f32_32x32x16_bf16 v[112:127], a[152:155], v[16:19], v[112:127]\n"
"v_mfma_f32_32x32x16_bf16 v[112:127], a[156:159], v[16:19], v[112:127]\n"
"v_mfma_f32_32x32x16_bf16 v[112:127], a[184:187], v[16:19], v[112:127]\n"
"v_mfma_f32_32x32x16_bf16 v[112:127], a[188:191], v[16:19], v[112:127]\n"
"s_waitcnt vmcnt(0)\n"
"s_barrier\n"
"ds_read_b64_tr_b16 a[216:217], v9 offset:49152\n"
"ds_read_b64_tr_b16 a[218:219], v9 offset:49664\n"
"ds_read_b64_tr_b16 a[220:221], v9 offset:53248\n"
"ds_read_b64_tr_b16 a[222:223], v9 offset:53760\n"
"ds_read_b64_tr_b16 a[248:249], v9 offset:57344\n"
"ds_read_b64_tr_b16 a[250:251], v9 offset:57856\n"
"ds_read_b64_tr_b16 a[252:253], v9 offset:61440\n"
"ds_read_b64_tr_b16 a[254:255], v9 offset:61952\n"
"s_waitcnt lgkmcnt(0)\n"
"v_mfma_f32_32x32x16_bf16 v[112:127], a[216:219], v[16:19], v[112:127]\n"
"v_mfma_f32_32x32x16_bf16 v[112:127], a[220:223], v[16:19], v[112:127]\n"
"v_mfma_f32_32x32x16_bf16 v[112:127], a[248:251], v[16:19], v[112:127]\n"
"v_mfma_f32_32x32x16_bf16 v[112:127], a[252:255], v[16:19], v[112:127]\n"
"s_barrier\n"
"s_lshl2_add_u32 s49, s27, 272\n"
"s_lshl2_add_u32 s68, s27, s26\n"
"s_lshl_b32 s68, s68, 9\n"
"s_add_u32 s68, s68, 4829184\n"
"s_mov_b32 s63, 4681728\n"
"s_waitcnt lgkmcnt(0)\n"
"s_add_u32 s54, s52, 0x7f4a7c15\n"
"s_addc_u32 s55, s53, 0x9e3779b9\n"
"s_lshl_b32 s67, s49, 5\n"
"s_add_u32 s67, s67, 4898816\n"
"s_sub_u32 s51, s67, s68\n"
"s_add_u32 s92, s32, s51\n"
"s_addc_u32 s93, s33, 0x10000\n"
"s_cmp_eq_u32 s26, 0\n"
"s_cselect_b32 s94, 1, 0\n"
"s_mov_b32 s95, 0x20000\n"
"v_mov_b32 v16, s54\n"
"v_mov_b32 v17, s55\n"
"v_cmp_gt_u32 vcc, 8, v1\n"
"v_mul_u32_u24 v3, 296960, v1\n"
"v_mov_b32 v12, 0x7fff0000\n"
"v_cndmask_b32 v3, v12, v3, vcc\n"
"s_add_u32 s51, s67, 76021760\n"
"buffer_store_dwordx2 v[16:17], v3, s[32:35], s51 offen sc1\n"
"s_sub_u32 s67, s68, 296960\n"
"s_sub_u32 s51, s51, 76021760\n"
"s_sub_u32 s51, s51, s68\n"
"v_mul_u32_u24 v3, 296960, v1\n"
"v_add_u32 v3, s51, v3\n"
"v_cmp_gt_u32 vcc, 2, v0\n"
"v_mov_b32 v18, 0x7fff0000\n"
"s_nop 0\n"
"v_cndmask_b32 v18, v18, v3, vcc\n"
"s_mul_i32 s48, s28, 593920\n"
"s_add_u32 s68, s68, s48\n"
"s_sub_u32 s68, s68, 3563520\n"
"s_add_u32 s63, s63, s48\n"
"s_add_u32 s66, s48, 4898816\n"
"s_mul_i32 s64, s26, 9216\n"
"s_add_u32 s63, s63, s64\n"
"s_mov_b32 s62, 31\n"
"v_and_b32 v4, 0xf0, v2\n"
"v_lshrrev_b32 v3, 5, v1\n"
"v_lshl_add_u32 v4, v3, 8, v4\n"
"v_bfe_u32 v3, v1, 4, 1\n"
"v_mul_u32_u24 v15, 296960, v3\n"
"v_add_u32 v15, v15, v4\n"
"v_add_u32 v15, 1187840, v15\n"
"v_mul_u32_u24 v3, 36864, v3\n"
"v_add_u32 v4, v3, v4\n"
"v_add_u32 v5, 73728, v4\n"
"v_add_u32 v20, 0, v2\n"
"v_add_u32 v21, 296960, v2\n"
"v_add_u32 v22, 4096, v2\n"
"v_add_u32 v23, 301056, v2\n"
"v_add_u32 v24, 5120, v2\n"
"v_add_u32 v25, 302080, v2\n"
"v_and_b32 v14, 7, v1\n"
"s_mul_i32 s48, s26, 8\n"
"s_add_u32 s48, s48, 96\n"
"v_add_u32 v14, s48, v14\n"
"v_lshrrev_b32 v14, 2, v14\n"
"v_lshlrev_b32 v14, 7, v14\n"
"v_lshrrev_b32 v3, 4, v1\n"
"v_mul_u32_u24 v3, 296960, v3\n"
"v_add_u32 v14, v3, v14\n"
"v_cmp_gt_u32 vcc, 32, v1\n"
"v_mov_b32 v3, 0x7fff0000\n"
"s_nop 0\n"
"v_cndmask_b32 v14, v3, v14, vcc\n"
"v_cndmask_b32 v16, 0, v16, vcc\n"
"v_cndmask_b32 v17, 0, v17, vcc\n"
"v_bfe_u32 v3, v1, 1, 3\n"
"v_mul_u32_u24 v8, 1152, v3\n"
"v_lshrrev_b32 v3, 1, v3\n"
"v_add_u32 v3, s26, v3\n"
"v_lshrrev_b32 v9, 4, v1\n"
"v_lshl_add_u32 v3, v3, 2, v9\n"
"v_and_b32 v3, 15, v3\n"
"v_lshl_add_u32 v8, v3, 3, v8\n"
"v_and_b32 v3, 1, v1\n"
"v_lshl_add_u32 v8, v3, 2, v8\n"
"v_add_u32 v8, 1024, v8\n"
"s_lshl_b32 s48, s26, 2\n"
"v_add_u32 v10, s48, v9\n"
"v_and_b32 v11, 15, v1\n"
"v_add_u32 v3, 64, v1\n"
"v_cmp_eq_u32 vcc, 0, v11\n"
"v_add_u32 v9, 73728, v8\n"
"v_xor_b32 v9, v9, v8\n"
"v_cndmask_b32 v10, v3, v10, vcc\n"
"v_lshlrev_b32 v10, 2, v10\n"
"v_add_u32 v10, 147456, v10\n"
"v_lshlrev_b32 v11, 2, v11\n"
"v_add_u32 v11, 147456, v11\n"
"v_and_b32 v3, 15, v1\n"
"v_cmp_eq_u32 vcc, 8, v3\n"
"v_lshrrev_b32 v3, 4, v1\n"
"s_lshl2_add_u32 s48, s26, 36880\n"
"v_add_lshl_u32 v3, v3, s48, 2\n"
"v_cndmask_b32 v10, v10, v3, vcc\n"
"v_and_b32 v3, 16, v1\n"
"v_lshl_add_u32 v11, v3, 2, v11\n"
"s_mov_b32 s48, 20000\n"
"s_branch .L_rfha\n"
".L_rlha:\n"
"s_sleep 8\n"
".L_rfha:\n"
"buffer_load_dwordx2 v[12:13], v14, s[32:35], s66 offen sc1\n"
"s_sub_u32 s48, s48, 1\n"
"s_cbranch_scc1 .L_end\n"
"s_waitcnt vmcnt(0)\n"
"v_cmp_ne_u64 vcc, v[16:17], v[12:13]\n"
"s_cbranch_vccnz .L_rlha\n"
"s_add_u32 s66, s66, 1187840\n"
"s_add_u32 m0, s64, 0\n"
"s_nop 0\n"
"buffer_load_dwordx4 v20, s[32:35], s63 offen offset:0 sc1 lds\n"
"buffer_load_dwordx4 v20, s[32:35], s63 offen offset:1024 sc1 lds\n"
"buffer_load_dwordx4 v20, s[32:35], s63 offen offset:2048 sc1 lds\n"
"buffer_load_dwordx4 v20, s[32:35], s63 offen offset:3072 sc1 lds\n"
"s_add_u32 m0, s64, 36864\n"
"s_nop 0\n"
"buffer_load_dwordx4 v21, s[32:35], s63 offen offset:0 sc1 lds\n"
"buffer_load_dwordx4 v21, s[32:35], s63 offen offset:1024 sc1 lds\n"
"buffer_load_dwordx4 v21, s[32:35], s63 offen offset:2048 sc1 lds\n"
"buffer_load_dwordx4 v21, s[32:35], s63 offen offset:3072 sc1 lds\n"
"s_add_u32 m0, s64, 4096\n"
"s_nop 0\n"
"buffer_load_dwordx4 v22, s[32:35], s63 offen offset:0 sc1 lds\n"
"s_add_u32 m0, s64, 40960\n"
"s_nop 0\n"
"buffer_load_dwordx4 v23, s[32:35], s63 offen offset:0 sc1 lds\n"
"buffer_load_dwordx2 v[12:13], v14, s[32:35], s66 offen sc1\n"
"s_add_u32 m0, s64, 5120\n"
"s_nop 0\n"
"buffer_load_dwordx4 v24, s[32:35], s63 offen offset:0 sc1 lds\n"
"buffer_load_dwordx4 v24, s[32:35], s63 offen offset:1024 sc1 lds\n"
"buffer_load_dwordx4 v24, s[32:35], s63 offen offset:2048 sc1 lds\n"
"buffer_load_dwordx4 v24, s[32:35], s63 offen offset:3072 sc1 lds\n"
"s_add_u32 m0, s64, 41984\n"
"s_nop 0\n"
"buffer_load_dwordx4 v25, s[32:35], s63 offen offset:0 sc1 lds\n"
"buffer_load_dwordx4 v25, s[32:35], s63 offen offset:1024 sc1 lds\n"
"buffer_load_dwordx4 v25, s[32:35], s63 offen offset:2048 sc1 lds\n"
"buffer_load_dwordx4 v25, s[32:35], s63 offen offset:3072 sc1 lds\n"
"s_add_u32 s63, s63, 1187840\n"
"s_waitcnt vmcnt(9)\n"
"s_barrier\n"
"ds_read_b128 v[128:131], v4 offset:0\n"
"ds_read_b128 v[132:135], v4 offset:512\n"
"ds_read_b128 v[136:139], v4 offset:9216\n"
"ds_read_b128 v[140:143], v4 offset:9728\n"
"ds_read_b128 v[144:147], v4 offset:18432\n"
"ds_read_b128 v[148:151], v4 offset:18944\n"
"ds_read_b128 v[152:155], v4 offset:27648\n"
"ds_read_b128 v[156:159], v4 offset:28160\n"
".p2align 3\n"
".L_hl:\n"
"s_waitcnt lgkmcnt(4)\n"
"v_mfma_f32_32x32x16_bf16 v[32:47], a[0:3], v[128:131], 0\n"
"ds_read_b128 v[160:163], v4 offset:1152\n"
"s_waitcnt vmcnt(6)\n"
"v_cmp_ne_u64 vcc, v[16:17], v[12:13]\n"
"ds_read_b32 v74, v11 offset:128\n"
"s_add_u32 s68, s68, 1187840\n"
"v_mfma_f32_32x32x16_bf16 v[32:47], a[4:7], v[132:135], v[32:47]\n"
"ds_read_b128 v[164:167], v4 offset:1664\n"
"s_cbranch_vccnz .L_rth0\n"
".L_bkh0:\n"
"s_add_u32 s66, s66, 1187840\n"
"v_mfma_f32_32x32x16_bf16 v[32:47], a[8:11], v[136:139], v[32:47]\n"
"ds_read_b128 v[168:171], v4 offset:10368\n"
"s_add_u32 m0, s64, 73728\n"
"v_mfma_f32_32x32x16_bf16 v[32:47], a[12:15], v[140:143], v[32:47]\n"
"ds_read_b128 v[172:175], v4 offset:10880\n"
"buffer_load_dwordx4 v20, s[32:35], s63 offen offset:0 sc1 lds\n"
"s_waitcnt lgkmcnt(3)\n"
"v_mfma_f32_32x32x16_bf16 v[32:47], a[16:19], v[144:147], v[32:47]\n"
"ds_read_b128 v[176:179], v4 offset:19584\n"
"buffer_load_dwordx4 v20, s[32:35], s63 offen offset:1024 sc1 lds\n"
"v_lshlrev_b32 v72, 16, v74\n"
"v_and_b32 v74, 0xffff0000, v74\n"
"v_mfma_f32_32x32x16_bf16 v[32:47], a[20:23], v[148:151], v[32:47]\n"
"ds_read_b128 v[180:183], v4 offset:20096\n"
"v_rcp_f32 v73, v74\n"
"v_mul_f32 v74, v74, v74\n"
"v_fma_f32 v48, -v72, v112, v48\n"
"v_fma_f32 v49, -v72, v113, v49\n"
"v_mfma_f32_32x32x16_bf16 v[32:47], a[24:27], v[152:155], v[32:47]\n"
"ds_read_b128 v[184:187], v4 offset:28800\n"
"v_mul_f32 v73, 0xbfb8aa3b, v73\n"
"v_fma_f32 v50, -v72, v114, v50\n"
"v_fma_f32 v51, -v72, v115, v51\n"
"v_fma_f32 v52, -v72, v116, v52\n"
"v_fma_f32 v53, -v72, v117, v53\n"
"v_mfma_f32_32x32x16_bf16 v[32:47], a[28:31], v[156:159], v[32:47]\n"
"ds_read_b128 v[188:191], v4 offset:29312\n"
"buffer_load_dwordx4 v20, s[32:35], s63 offen offset:2048 sc1 lds\n"
"v_fma_f32 v54, -v72, v118, v54\n"
"s_waitcnt lgkmcnt(4)\n"
"v_mfma_f32_32x32x16_bf16 v[32:47], a[32:35], v[160:163], v[32:47]\n"
"ds_read_b128 v[128:131], v4 offset:2304\n"
"v_fma_f32 v55, -v72, v119, v55\n"
"v_fma_f32 v56, -v72, v120, v56\n"
"v_fma_f32 v57, -v72, v121, v57\n"
"v_fma_f32 v58, -v72, v122, v58\n"
"v_fma_f32 v59, -v72, v123, v59\n"
"v_mfma_f32_32x32x16_bf16 v[32:47], a[36:39], v[164:167], v[32:47]\n"
"ds_read_b128 v[132:135], v4 offset:2816\n"
"v_fma_f32 v60, -v72, v124, v60\n"
"v_fma_f32 v61, -v72, v125, v61\n"
"v_fma_f32 v62, -v72, v126, v62\n"
"v_fma_f32 v63, -v72, v127, v63\n"
"v_mul_f32 v76, v48, v73\n"
"v_mfma_f32_32x32x16_bf16 v[32:47], a[40:43], v[168:171], v[32:47]\n"
"ds_read_b128 v[136:139], v4 offset:11520\n"
"buffer_load_dwordx4 v20, s[32:35], s63 offen offset:3072 sc1 lds\n"
"v_exp_f32 v76, v76\n"
"v_mfma_f32_32x32x16_bf16 v[32:47], a[44:47], v[172:175], v[32:47]\n"
"ds_read_b128 v[140:143], v4 offset:12032\n"
"v_mul_f32 v77, v49, v73\n"
"v_mul_f32 v78, v50, v73\n"
"v_exp_f32 v77, v77\n"
"s_waitcnt lgkmcnt(4)\n"
"v_mfma_f32_32x32x16_bf16 v[32:47], a[48:51], v[176:179], v[32:47]\n"
"ds_read_b128 v[144:147], v4 offset:20736\n"
"v_exp_f32 v78, v78\n"
"v_mul_f32 v79, v51, v73\n"
"v_mul_f32 v80, v52, v73\n"
"v_mul_f32 v81, v53, v73\n"
"v_mfma_f32_32x32x16_bf16 v[32:47], a[52:55], v[180:183], v[32:47]\n"
"ds_read_b128 v[148:151], v4 offset:21248\n"
"s_add_u32 m0, s64, 110592\n"
"v_exp_f32 v79, v79\n"
"v_exp_f32 v80, v80\n"
"v_mfma_f32_32x32x16_bf16 v[32:47], a[56:59], v[184:187], v[32:47]\n"
"ds_read_b128 v[152:155], v4 offset:29952\n"
"buffer_load_dwordx4 v21, s[32:35], s63 offen offset:0 sc1 lds\n"
"v_exp_f32 v81, v81\n"
"v_mfma_f32_32x32x16_bf16 v[32:47], a[60:63], v[188:191], v[32:47]\n"
"ds_read_b128 v[156:159], v4 offset:30464\n"
"buffer_load_dwordx4 v21, s[32:35], s63 offen offset:1024 sc1 lds\n"
"v_mul_f32 v82, v54, v73\n"
"s_waitcnt lgkmcnt(4)\n"
"v_mfma_f32_32x32x16_bf16 v[32:47], a[64:67], v[128:131], v[32:47]\n"
"ds_read_b128 v[160:163], v4 offset:3456\n"
"v_exp_f32 v82, v82\n"
"v_mul_f32 v83, v55, v73\n"
"v_mul_f32 v48, v48, v56\n"
"v_mul_f32 v49, v49, v57\n"
"v_mfma_f32_32x32x16_bf16 v[32:47], a[68:71], v[132:135], v[32:47]\n"
"ds_read_b128 v[164:167], v4 offset:3968\n"
"v_exp_f32 v83, v83\n"
"v_mul_f32 v50, v50, v58\n"
"v_mul_f32 v51, v51, v59\n"
"v_mul_f32 v52, v52, v60\n"
"v_mfma_f32_32x32x16_bf16 v[32:47], a[72:75], v[136:139], v[32:47]\n"
"ds_read_b128 v[168:171], v4 offset:12672\n"
"buffer_load_dwordx4 v21, s[32:35], s63 offen offset:2048 sc1 lds\n"
"v_mul_f32 v53, v53, v61\n"
"v_mul_f32 v54, v54, v62\n"
"v_mfma_f32_32x32x16_bf16 v[32:47], a[76:79], v[140:143], v[32:47]\n"
"ds_read_b128 v[172:175], v4 offset:13184\n"
"v_mul_f32 v55, v55, v63\n"
"v_fma_f32 v76, v76, v74, v74\n"
"v_fma_f32 v77, v77, v74, v74\n"
"v_fma_f32 v78, v78, v74, v74\n"
"s_waitcnt lgkmcnt(4)\n"
"v_mfma_f32_32x32x16_bf16 v[32:47], a[80:83], v[144:147], v[32:47]\n"
"ds_read_b128 v[176:179], v4 offset:21888\n"
"v_rcp_f32 v76, v76\n"
"v_rcp_f32 v77, v77\n"
"v_fma_f32 v79, v79, v74, v74\n"
"v_mfma_f32_32x32x16_bf16 v[32:47], a[84:87], v[148:151], v[32:47]\n"
"ds_read_b128 v[180:183], v4 offset:22400\n"
"buffer_load_dwordx4 v21, s[32:35], s63 offen offset:3072 sc1 lds\n"
"v_rcp_f32 v78, v78\n"
"v_mfma_f32_32x32x16_bf16 v[32:47], a[88:91], v[152:155], v[32:47]\n"
"ds_read_b128 v[184:187], v4 offset:31104\n"
"v_rcp_f32 v79, v79\n"
"v_fma_f32 v80, v80, v74, v74\n"
"v_fma_f32 v81, v81, v74, v74\n"
"v_fma_f32 v82, v82, v74, v74\n"
"v_mfma_f32_32x32x16_bf16 v[32:47], a[92:95], v[156:159], v[32:47]\n"
"ds_read_b128 v[188:191], v4 offset:31616\n"
"v_rcp_f32 v80, v80\n"
"v_rcp_f32 v81, v81\n"
"s_waitcnt lgkmcnt(4)\n"
"v_mfma_f32_32x32x16_bf16 v[32:47], a[96:99], v[160:163], v[32:47]\n"
"s_waitcnt vmcnt(8)\n"
"s_barrier\n"
"ds_read_b128 v[128:131], v4 offset:4608\n"
"s_add_u32 m0, s64, 77824\n"
"v_rcp_f32 v82, v82\n"
"v_mfma_f32_32x32x16_bf16 v[32:47], a[100:103], v[164:167], v[32:47]\n"
"ds_read_b128 v[132:135], v4 offset:5120\n"
"buffer_load_dwordx4 v22, s[32:35], s63 offen offset:0 sc1 lds\n"
"v_fma_f32 v83, v83, v74, v74\n"
"v_mul_f32 v48, v48, v76\n"
"v_mfma_f32_32x32x16_bf16 v[32:47], a[104:107], v[168:171], v[32:47]\n"
"ds_read_b128 v[136:139], v4 offset:13824\n"
"buffer_store_dwordx2 v[16:17], v18, s[32:35], s68 offen sc1\n"
"v_rcp_f32 v83, v83\n"
"v_mfma_f32_32x32x16_bf16 v[32:47], a[108:111], v[172:175], v[32:47]\n"
"ds_read_b128 v[140:143], v4 offset:14336\n"
"s_add_u32 m0, s64, 114688\n"
"v_mul_f32 v49, v49, v77\n"
"v_mul_f32 v50, v50, v78\n"
"v_mul_f32 v51, v51, v79\n"
"s_waitcnt lgkmcnt(4)\n"
"v_mfma_f32_32x32x16_bf16 v[32:47], a[112:115], v[176:179], v[32:47]\n"
"ds_read_b128 v[144:147], v4 offset:23040\n"
"buffer_load_dwordx4 v23, s[32:35], s63 offen offset:0 sc1 lds\n"
"v_mul_f32 v52, v52, v80\n"
"v_mul_f32 v53, v53, v81\n"
"v_mfma_f32_32x32x16_bf16 v[32:47], a[116:119], v[180:183], v[32:47]\n"
"ds_read_b128 v[148:151], v4 offset:23552\n"
"v_mul_f32 v54, v54, v82\n"
"v_mul_f32 v55, v55, v83\n"
"v_cvt_pk_bf16_f32 v84, v48, v49\n"
"v_cvt_pk_bf16_f32 v85, v50, v51\n"
"v_cvt_pk_bf16_f32 v86, v52, v53\n"
"v_mfma_f32_32x32x16_bf16 v[32:47], a[120:123], v[184:187], v[32:47]\n"
"ds_read_b128 v[152:155], v4 offset:32256\n"
"s_add_u32 m0, s64, 78848\n"
"v_cvt_pk_bf16_f32 v87, v54, v55\n"
"buffer_load_dwordx4 v24, s[32:35], s63 offen offset:0 sc1 lds\n"
"v_mfma_f32_32x32x16_bf16 v[32:47], a[124:127], v[188:191], v[32:47]\n"
"ds_read_b128 v[156:159], v4 offset:32768\n"
"ds_read2st64_b32 v[64:65], v8 offset0:0 offset1:36\n"
"ds_read2st64_b32 v[66:67], v8 offset0:72 offset1:108\n"
"ds_read2st64_b32 v[68:69], v8 offset0:144 offset1:180\n"
"ds_read2st64_b32 v[70:71], v8 offset0:216 offset1:252\n"
"s_waitcnt lgkmcnt(8)\n"
"v_mfma_f32_32x32x16_bf16 v[32:47], a[128:131], v[128:131], v[32:47]\n"
"ds_read_b128 v[160:163], v4 offset:5760\n"
"buffer_store_dwordx4 v[84:87], v15, s[32:35], s68 offen sc1\n"
"v_mfma_f32_32x32x16_bf16 v[32:47], a[132:135], v[132:135], v[32:47]\n"
"ds_read_b128 v[164:167], v4 offset:6272\n"
"buffer_load_dwordx4 v24, s[32:35], s63 offen offset:1024 sc1 lds\n"
"v_xor_b32 v8, v9, v8\n"
"v_mfma_f32_32x32x16_bf16 v[32:47], a[136:139], v[136:139], v[32:47]\n"
"ds_read_b128 v[168:171], v4 offset:14976\n"
"v_mfma_f32_32x32x16_bf16 v[32:47], a[140:143], v[140:143], v[32:47]\n"
"ds_read_b128 v[172:175], v4 offset:15488\n"
"s_waitcnt lgkmcnt(8)\n"
"v_mfma_f32_32x32x16_bf16 v[32:47], a[144:147], v[144:147], v[32:47]\n"
"ds_read_b128 v[176:179], v4 offset:24192\n"
"buffer_load_dwordx2 v[12:13], v14, s[32:35], s66 offen sc1\n"
"v_mfma_f32_32x32x16_bf16 v[32:47], a[148:151], v[148:151], v[32:47]\n"
"ds_read_b128 v[180:183], v4 offset:24704\n"
"buffer_load_dwordx4 v24, s[32:35], s63 offen offset:2048 sc1 lds\n"
"s_waitcnt lgkmcnt(6)\n"
"v_add_f32 v64, v64, v65\n"
"v_mfma_f32_32x32x16_bf16 v[32:47], a[152:155], v[152:155], v[32:47]\n"
"ds_read_b128 v[184:187], v4 offset:33408\n"
"v_add_f32 v66, v66, v67\n"
"v_add_f32 v68, v68, v69\n"
"v_add_f32 v70, v70, v71\n"
"v_add_f32 v66, v64, v66\n"
"v_add_f32 v68, v68, v70\n"
"v_mfma_f32_32x32x16_bf16 v[32:47], a[156:159], v[156:159], v[32:47]\n"
"ds_read_b128 v[188:191], v4 offset:33920\n"
"v_add_f32_dpp v64, v66, v66 row_ror:8 row_mask:0xf bank_mask:0x3\n"
"s_waitcnt lgkmcnt(4)\n"
"v_mfma_f32_32x32x16_bf16 v[32:47], a[160:163], v[160:163], v[32:47]\n"
"ds_read_b128 v[128:131], v4 offset:6912\n"
"v_add_f32_dpp v64, v68, v68 row_ror:8 row_mask:0xf bank_mask:0xc\n"
"v_mfma_f32_32x32x16_bf16 v[32:47], a[164:167], v[164:167], v[32:47]\n"
"ds_read_b128 v[132:135], v4 offset:7424\n"
"buffer_load_dwordx4 v24, s[32:35], s63 offen offset:3072 sc1 lds\n"
"v_add_f32_dpp v64, v64, v64 row_ror:12 row_mask:0xf bank_mask:0xf\n"
"v_mfma_f32_32x32x16_bf16 v[32:47], a[168:171], v[168:171], v[32:47]\n"
"ds_read_b128 v[136:139], v4 offset:16128\n"
"v_add_f32_dpp v64, v64, v64 quad_perm:[2,3,0,1] row_mask:0xf bank_mask:0xf\n"
"v_mfma_f32_32x32x16_bf16 v[32:47], a[172:175], v[172:175], v[32:47]\n"
"ds_read_b128 v[140:143], v4 offset:16640\n"
"v_mul_f32 v64, 0x3a800000, v64\n"
"s_waitcnt lgkmcnt(4)\n"
"v_mfma_f32_32x32x16_bf16 v[32:47], a[176:179], v[176:179], v[32:47]\n"
"ds_read_b128 v[144:147], v4 offset:25344\n"
"v_mul_f32 v65, v64, v64\n"
"v_mfma_f32_32x32x16_bf16 v[32:47], a[180:183], v[180:183], v[32:47]\n"
"ds_read_b128 v[148:151], v4 offset:25856\n"
"s_add_u32 m0, s64, 115712\n"
"v_sub_f32_dpp v65, v64, v65 quad_perm:[1,1,3,3] row_mask:0xf bank_mask:0xf\n"
"buffer_load_dwordx4 v25, s[32:35], s63 offen offset:0 sc1 lds\n"
"v_mfma_f32_32x32x16_bf16 v[32:47], a[184:187], v[184:187], v[32:47]\n"
"ds_read_b128 v[152:155], v4 offset:34560\n"
"v_max_f32 v65, 0, v65\n"
"v_mfma_f32_32x32x16_bf16 v[32:47], a[188:191], v[188:191], v[32:47]\n"
"ds_read_b128 v[156:159], v4 offset:35072\n"
"v_cvt_pk_bf16_f32 v65, 0, v65\n"
"s_waitcnt lgkmcnt(4)\n"
"v_mfma_f32_32x32x16_bf16 v[32:47], a[192:195], v[128:131], v[32:47]\n"
"ds_read_b128 v[160:163], v4 offset:8064\n"
"v_add_f32 v65, 0x3727c5ac, v65\n"
"v_mfma_f32_32x32x16_bf16 v[32:47], a[196:199], v[132:135], v[32:47]\n"
"ds_read_b128 v[164:167], v4 offset:8576\n"
"buffer_load_dwordx4 v25, s[32:35], s63 offen offset:1024 sc1 lds\n"
"v_cvt_pk_bf16_f32 v65, 0, v65\n"
"v_mfma_f32_32x32x16_bf16 v[32:47], a[200:203], v[136:139], v[32:47]\n"
"ds_read_b128 v[168:171], v4 offset:17280\n"
"v_sqrt_f32 v65, v65\n"
"v_mfma_f32_32x32x16_bf16 v[32:47], a[204:207], v[140:143], v[32:47]\n"
"ds_read_b128 v[172:175], v4 offset:17792\n"
"v_cvt_pk_bf16_f32 v65, v64, v65\n"
"s_waitcnt lgkmcnt(4)\n"
"v_mfma_f32_32x32x16_bf16 v[32:47], a[208:211], v[144:147], v[32:47]\n"
"ds_read_b128 v[176:179], v4 offset:26496\n"
"ds_write_b32 v10, v65 offset:0\n"
"v_mfma_f32_32x32x16_bf16 v[32:47], a[212:215], v[148:151], v[32:47]\n"
"ds_read_b128 v[180:183], v4 offset:27008\n"
"buffer_load_dwordx4 v25, s[32:35], s63 offen offset:2048 sc1 lds\n"
"v_mfma_f32_32x32x16_bf16 v[32:47], a[216:219], v[152:155], v[32:47]\n"
"ds_read_b128 v[184:187], v4 offset:35712\n"
"v_mfma_f32_32x32x16_bf16 v[32:47], a[220:223], v[156:159], v[32:47]\n"
"ds_read_b128 v[188:191], v4 offset:36224\n"
"s_waitcnt lgkmcnt(5)\n"
"v_mfma_f32_32x32x16_bf16 v[32:47], a[224:227], v[160:163], v[32:47]\n"
"s_waitcnt vmcnt(9)\n"
"s_waitcnt lgkmcnt(3)\n"
"s_barrier\n"
"ds_read_b128 v[128:131], v5 offset:0\n"
"v_mfma_f32_32x32x16_bf16 v[32:47], a[228:231], v[164:167], v[32:47]\n"
"ds_read_b128 v[132:135], v5 offset:512\n"
"buffer_load_dwordx4 v25, s[32:35], s63 offen offset:3072 sc1 lds\n"
"s_add_u32 s63, s63, 1187840\n"
"v_mfma_f32_32x32x16_bf16 v[32:47], a[232:235], v[168:171], v[32:47]\n"
"ds_read_b128 v[136:139], v5 offset:9216\n"
"v_mfma_f32_32x32x16_bf16 v[32:47], a[236:239], v[172:175], v[32:47]\n"
"ds_read_b128 v[140:143], v5 offset:9728\n"
"s_waitcnt lgkmcnt(4)\n"
"v_mfma_f32_32x32x16_bf16 v[32:47], a[240:243], v[176:179], v[32:47]\n"
"ds_read_b128 v[144:147], v5 offset:18432\n"
"v_mfma_f32_32x32x16_bf16 v[32:47], a[244:247], v[180:183], v[32:47]\n"
"ds_read_b128 v[148:151], v5 offset:18944\n"
"v_mfma_f32_32x32x16_bf16 v[32:47], a[248:251], v[184:187], v[32:47]\n"
"ds_read_b128 v[152:155], v5 offset:27648\n"
"v_mfma_f32_32x32x16_bf16 v[32:47], a[252:255], v[188:191], v[32:47]\n"
"ds_read_b128 v[156:159], v5 offset:28160\n"
"s_waitcnt lgkmcnt(4)\n"
"v_mfma_f32_32x32x16_bf16 v[48:63], a[0:3], v[128:131], 0\n"
"ds_read_b128 v[160:163], v5 offset:1152\n"
"s_waitcnt vmcnt(6)\n"
"v_cmp_ne_u64 vcc, v[16:17], v[12:13]\n"
"ds_read_b32 v74, v11 offset:0\n"
"s_add_u32 s68, s68, 1187840\n"
"v_mfma_f32_32x32x16_bf16 v[48:63], a[4:7], v[132:135], v[48:63]\n"
"ds_read_b128 v[164:167], v5 offset:1664\n"
"s_cbranch_vccnz .L_rth1\n"
".L_bkh1:\n"
"s_add_u32 s66, s66, 1187840\n"
"v_mfma_f32_32x32x16_bf16 v[48:63], a[8:11], v[136:139], v[48:63]\n"
"ds_read_b128 v[168:171], v5 offset:10368\n"
"s_add_u32 m0, s64, 0\n"
"v_mfma_f32_32x32x16_bf16 v[48:63], a[12:15], v[140:143], v[48:63]\n"
"ds_read_b128 v[172:175], v5 offset:10880\n"
"buffer_load_dwordx4 v20, s[32:35], s63 offen offset:0 sc1 lds\n"
"s_waitcnt lgkmcnt(3)\n"
"v_mfma_f32_32x32x16_bf16 v[48:63], a[16:19], v[144:147], v[48:63]\n"
"ds_read_b128 v[176:179], v5 offset:19584\n"
"buffer_load_dwordx4 v20, s[32:35], s63 offen offset:1024 sc1 lds\n"
"v_lshlrev_b32 v72, 16, v74\n"
"v_and_b32 v74, 0xffff0000, v74\n"
"v_mfma_f32_32x32x16_bf16 v[48:63], a[20:23], v[148:151], v[48:63]\n"
"ds_read_b128 v[180:183], v5 offset:20096\n"
"v_rcp_f32 v73, v74\n"
"v_mul_f32 v74, v74, v74\n"
"v_fma_f32 v32, -v72, v112, v32\n"
"v_fma_f32 v33, -v72, v113, v33\n"
"v_mfma_f32_32x32x16_bf16 v[48:63], a[24:27], v[152:155], v[48:63]\n"
"ds_read_b128 v[184:187], v5 offset:28800\n"
"v_mul_f32 v73, 0xbfb8aa3b, v73\n"
"v_fma_f32 v34, -v72, v114, v34\n"
"v_fma_f32 v35, -v72, v115, v35\n"
"v_fma_f32 v36, -v72, v116, v36\n"
"v_fma_f32 v37, -v72, v117, v37\n"
"v_mfma_f32_32x32x16_bf16 v[48:63], a[28:31], v[156:159], v[48:63]\n"
"ds_read_b128 v[188:191], v5 offset:29312\n"
"buffer_load_dwordx4 v20, s[32:35], s63 offen offset:2048 sc1 lds\n"
"v_fma_f32 v38, -v72, v118, v38\n"
"s_waitcnt lgkmcnt(4)\n"
"v_mfma_f32_32x32x16_bf16 v[48:63], a[32:35], v[160:163], v[48:63]\n"
"ds_read_b128 v[128:131], v5 offset:2304\n"
"v_fma_f32 v39, -v72, v119, v39\n"
"v_fma_f32 v40, -v72, v120, v40\n"
"v_fma_f32 v41, -v72, v121, v41\n"
"v_fma_f32 v42, -v72, v122, v42\n"
"v_fma_f32 v43, -v72, v123, v43\n"
"v_mfma_f32_32x32x16_bf16 v[48:63], a[36:39], v[164:167], v[48:63]\n"
"ds_read_b128 v[132:135], v5 offset:2816\n"
"v_fma_f32 v44, -v72, v124, v44\n"
"v_fma_f32 v45, -v72, v125, v45\n"
"v_fma_f32 v46, -v72, v126, v46\n"
"v_fma_f32 v47, -v72, v127, v47\n"
"v_mul_f32 v76, v32, v73\n"
"v_mfma_f32_32x32x16_bf16 v[48:63], a[40:43], v[168:171], v[48:63]\n"
"ds_read_b128 v[136:139], v5 offset:11520\n"
"buffer_load_dwordx4 v20, s[32:35], s63 offen offset:3072 sc1 lds\n"
"v_exp_f32 v76, v76\n"
"v_mfma_f32_32x32x16_bf16 v[48:63], a[44:47], v[172:175], v[48:63]\n"
"ds_read_b128 v[140:143], v5 offset:12032\n"
"v_mul_f32 v77, v33, v73\n"
"v_mul_f32 v78, v34, v73\n"
"v_exp_f32 v77, v77\n"
"s_waitcnt lgkmcnt(4)\n"
"v_mfma_f32_32x32x16_bf16 v[48:63], a[48:51], v[176:179], v[48:63]\n"
"ds_read_b128 v[144:147], v5 offset:20736\n"
"v_exp_f32 v78, v78\n"
"v_mul_f32 v79, v35, v73\n"
"v_mul_f32 v80, v36, v73\n"
"v_mul_f32 v81, v37, v73\n"
"v_mfma_f32_32x32x16_bf16 v[48:63], a[52:55], v[180:183], v[48:63]\n"
"ds_read_b128 v[148:151], v5 offset:21248\n"
"s_add_u32 m0, s64, 36864\n"
"v_exp_f32 v79, v79\n"
"v_exp_f32 v80, v80\n"
"v_mfma_f32_32x32x16_bf16 v[48:63], a[56:59], v[184:187], v[48:63]\n"
"ds_read_b128 v[152:155], v5 offset:29952\n"
"buffer_load_dwordx4 v21, s[32:35], s63 offen offset:0 sc1 lds\n"
"v_exp_f32 v81, v81\n"
"v_mfma_f32_32x32x16_bf16 v[48:63], a[60:63], v[188:191], v[48:63]\n"
"ds_read_b128 v[156:159], v5 offset:30464\n"
"buffer_load_dwordx4 v21, s[32:35], s63 offen offset:1024 sc1 lds\n"
"v_mul_f32 v82, v38, v73\n"
"s_waitcnt lgkmcnt(4)\n"
"v_mfma_f32_32x32x16_bf16 v[48:63], a[64:67], v[128:131], v[48:63]\n"
"ds_read_b128 v[160:163], v5 offset:3456\n"
"v_exp_f32 v82, v82\n"
"v_mul_f32 v83, v39, v73\n"
"v_mul_f32 v32, v32, v40\n"
"v_mul_f32 v33, v33, v41\n"
"v_mfma_f32_32x32x16_bf16 v[48:63], a[68:71], v[132:135], v[48:63]\n"
"ds_read_b128 v[164:167], v5 offset:3968\n"
"v_exp_f32 v83, v83\n"
"v_mul_f32 v34, v34, v42\n"
"v_mul_f32 v35, v35, v43\n"
"v_mul_f32 v36, v36, v44\n"
"v_mfma_f32_32x32x16_bf16 v[48:63], a[72:75], v[136:139], v[48:63]\n"
"ds_read_b128 v[168:171], v5 offset:12672\n"
"buffer_load_dwordx4 v21, s[32:35], s63 offen offset:2048 sc1 lds\n"
"v_mul_f32 v37, v37, v45\n"
"v_mul_f32 v38, v38, v46\n"
"v_mfma_f32_32x32x16_bf16 v[48:63], a[76:79], v[140:143], v[48:63]\n"
"ds_read_b128 v[172:175], v5 offset:13184\n"
"v_mul_f32 v39, v39, v47\n"
"v_fma_f32 v76, v76, v74, v74\n"
"v_fma_f32 v77, v77, v74, v74\n"
"v_fma_f32 v78, v78, v74, v74\n"
"s_waitcnt lgkmcnt(4)\n"
"v_mfma_f32_32x32x16_bf16 v[48:63], a[80:83], v[144:147], v[48:63]\n"
"ds_read_b128 v[176:179], v5 offset:21888\n"
"v_rcp_f32 v76, v76\n"
"v_rcp_f32 v77, v77\n"
"v_fma_f32 v79, v79, v74, v74\n"
"v_mfma_f32_32x32x16_bf16 v[48:63], a[84:87], v[148:151], v[48:63]\n"
"ds_read_b128 v[180:183], v5 offset:22400\n"
"buffer_load_dwordx4 v21, s[32:35], s63 offen offset:3072 sc1 lds\n"
"v_rcp_f32 v78, v78\n"
"v_mfma_f32_32x32x16_bf16 v[48:63], a[88:91], v[152:155], v[48:63]\n"
"ds_read_b128 v[184:187], v5 offset:31104\n"
"v_rcp_f32 v79, v79\n"
"v_fma_f32 v80, v80, v74, v74\n"
"v_fma_f32 v81, v81, v74, v74\n"
"v_fma_f32 v82, v82, v74, v74\n"
"v_mfma_f32_32x32x16_bf16 v[48:63], a[92:95], v[156:159], v[48:63]\n"
"ds_read_b128 v[188:191], v5 offset:31616\n"
"v_rcp_f32 v80, v80\n"
"v_rcp_f32 v81, v81\n"
"s_waitcnt lgkmcnt(4)\n"
"v_mfma_f32_32x32x16_bf16 v[48:63], a[96:99], v[160:163], v[48:63]\n"
"s_waitcnt vmcnt(8)\n"
"s_barrier\n"
"ds_read_b128 v[128:131], v5 offset:4608\n"
"s_add_u32 m0, s64, 4096\n"
"v_rcp_f32 v82, v82\n"
"v_mfma_f32_32x32x16_bf16 v[48:63], a[100:103], v[164:167], v[48:63]\n"
"ds_read_b128 v[132:135], v5 offset:5120\n"
"buffer_load_dwordx4 v22, s[32:35], s63 offen offset:0 sc1 lds\n"
"v_fma_f32 v83, v83, v74, v74\n"
"v_mul_f32 v32, v32, v76\n"
"v_mfma_f32_32x32x16_bf16 v[48:63], a[104:107], v[168:171], v[48:63]\n"
"ds_read_b128 v[136:139], v5 offset:13824\n"
"buffer_store_dwordx2 v[16:17], v18, s[32:35], s68 offen sc1\n"
"v_rcp_f32 v83, v83\n"
"v_mfma_f32_32x32x16_bf16 v[48:63], a[108:111], v[172:175], v[48:63]\n"
"ds_read_b128 v[140:143], v5 offset:14336\n"
"s_add_u32 m0, s64, 40960\n"
"v_mul_f32 v33, v33, v77\n"
"v_mul_f32 v34, v34, v78\n"
"v_mul_f32 v35, v35, v79\n"
"s_waitcnt lgkmcnt(4)\n"
"v_mfma_f32_32x32x16_bf16 v[48:63], a[112:115], v[176:179], v[48:63]\n"
"ds_read_b128 v[144:147], v5 offset:23040\n"
"buffer_load_dwordx4 v23, s[32:35], s63 offen offset:0 sc1 lds\n"
"v_mul_f32 v36, v36, v80\n"
"v_mul_f32 v37, v37, v81\n"
"v_mfma_f32_32x32x16_bf16 v[48:63], a[116:119], v[180:183], v[48:63]\n"
"ds_read_b128 v[148:151], v5 offset:23552\n"
"v_mul_f32 v38, v38, v82\n"
"v_mul_f32 v39, v39, v83\n"
"v_cvt_pk_bf16_f32 v84, v32, v33\n"
"v_cvt_pk_bf16_f32 v85, v34, v35\n"
"v_cvt_pk_bf16_f32 v86, v36, v37\n"
"v_mfma_f32_32x32x16_bf16 v[48:63], a[120:123], v[184:187], v[48:63]\n"
"ds_read_b128 v[152:155], v5 offset:32256\n"
"s_add_u32 m0, s64, 5120\n"
"v_cvt_pk_bf16_f32 v87, v38, v39\n"
"buffer_load_dwordx4 v24, s[32:35], s63 offen offset:0 sc1 lds\n"
"v_mfma_f32_32x32x16_bf16 v[48:63], a[124:127], v[188:191], v[48:63]\n"
"ds_read_b128 v[156:159], v5 offset:32768\n"
"ds_read2st64_b32 v[64:65], v8 offset0:0 offset1:36\n"
"ds_read2st64_b32 v[66:67], v8 offset0:72 offset1:108\n"
"ds_read2st64_b32 v[68:69], v8 offset0:144 offset1:180\n"
"ds_read2st64_b32 v[70:71], v8 offset0:216 offset1:252\n"
"s_waitcnt lgkmcnt(8)\n"
"v_mfma_f32_32x32x16_bf16 v[48:63], a[128:131], v[128:131], v[48:63]\n"
"ds_read_b128 v[160:163], v5 offset:5760\n"
"buffer_store_dwordx4 v[84:87], v15, s[32:35], s68 offen sc1\n"
"v_mfma_f32_32x32x16_bf16 v[48:63], a[132:135], v[132:135], v[48:63]\n"
"ds_read_b128 v[164:167], v5 offset:6272\n"
"buffer_load_dwordx4 v24, s[32:35], s63 offen offset:1024 sc1 lds\n"
"v_xor_b32 v8, v9, v8\n"
"v_mfma_f32_32x32x16_bf16 v[48:63], a[136:139], v[136:139], v[48:63]\n"
"ds_read_b128 v[168:171], v5 offset:14976\n"
"v_mfma_f32_32x32x16_bf16 v[48:63], a[140:143], v[140:143], v[48:63]\n"
"ds_read_b128 v[172:175], v5 offset:15488\n"
"s_waitcnt lgkmcnt(8)\n"
"v_mfma_f32_32x32x16_bf16 v[48:63], a[144:147], v[144:147], v[48:63]\n"
"ds_read_b128 v[176:179], v5 offset:24192\n"
"buffer_load_dwordx2 v[12:13], v14, s[32:35], s66 offen sc1\n"
"v_mfma_f32_32x32x16_bf16 v[48:63], a[148:151], v[148:151], v[48:63]\n"
"ds_read_b128 v[180:183], v5 offset:24704\n"
"buffer_load_dwordx4 v24, s[32:35], s63 offen offset:2048 sc1 lds\n"
"s_waitcnt lgkmcnt(6)\n"
"v_add_f32 v64, v64, v65\n"
"v_mfma_f32_32x32x16_bf16 v[48:63], a[152:155], v[152:155], v[48:63]\n"
"ds_read_b128 v[184:187], v5 offset:33408\n"
"v_add_f32 v66, v66, v67\n"
"v_add_f32 v68, v68, v69\n"
"v_add_f32 v70, v70, v71\n"
"v_add_f32 v66, v64, v66\n"
"v_add_f32 v68, v68, v70\n"
"v_mfma_f32_32x32x16_bf16 v[48:63], a[156:159], v[156:159], v[48:63]\n"
"ds_read_b128 v[188:191], v5 offset:33920\n"
"v_add_f32_dpp v64, v66, v66 row_ror:8 row_mask:0xf bank_mask:0x3\n"
"s_waitcnt lgkmcnt(4)\n"
"v_mfma_f32_32x32x16_bf16 v[48:63], a[160:163], v[160:163], v[48:63]\n"
"ds_read_b128 v[128:131], v5 offset:6912\n"
"v_add_f32_dpp v64, v68, v68 row_ror:8 row_mask:0xf bank_mask:0xc\n"
"v_mfma_f32_32x32x16_bf16 v[48:63], a[164:167], v[164:167], v[48:63]\n"
"ds_read_b128 v[132:135], v5 offset:7424\n"
"buffer_load_dwordx4 v24, s[32:35], s63 offen offset:3072 sc1 lds\n"
"v_add_f32_dpp v64, v64, v64 row_ror:12 row_mask:0xf bank_mask:0xf\n"
"v_mfma_f32_32x32x16_bf16 v[48:63], a[168:171], v[168:171], v[48:63]\n"
"ds_read_b128 v[136:139], v5 offset:16128\n"
"v_add_f32_dpp v64, v64, v64 quad_perm:[2,3,0,1] row_mask:0xf bank_mask:0xf\n"
"v_mfma_f32_32x32x16_bf16 v[48:63], a[172:175], v[172:175], v[48:63]\n"
"ds_read_b128 v[140:143], v5 offset:16640\n"
"v_mul_f32 v64, 0x3a800000, v64\n"
"s_waitcnt lgkmcnt(4)\n"
"v_mfma_f32_32x32x16_bf16 v[48:63], a[176:179], v[176:179], v[48:63]\n"
"ds_read_b128 v[144:147], v5 offset:25344\n"
"v_mul_f32 v65, v64, v64\n"
"v_mfma_f32_32x32x16_bf16 v[48:63], a[180:183], v[180:183], v[48:63]\n"
"ds_read_b128 v[148:151], v5 offset:25856\n"
"s_add_u32 m0, s64, 41984\n"
"v_sub_f32_dpp v65, v64, v65 quad_perm:[1,1,3,3] row_mask:0xf bank_mask:0xf\n"
"buffer_load_dwordx4 v25, s[32:35], s63 offen offset:0 sc1 lds\n"
"v_mfma_f32_32x32x16_bf16 v[48:63], a[184:187], v[184:187], v[48:63]\n"
"ds_read_b128 v[152:155], v5 offset:34560\n"
"v_max_f32 v65, 0, v65\n"
"v_mfma_f32_32x32x16_bf16 v[48:63], a[188:191], v[188:191], v[48:63]\n"
"ds_read_b128 v[156:159], v5 offset:35072\n"
"v_cvt_pk_bf16_f32 v65, 0, v65\n"
"s_waitcnt lgkmcnt(4)\n"
"v_mfma_f32_32x32x16_bf16 v[48:63], a[192:195], v[128:131], v[48:63]\n"
"ds_read_b128 v[160:163], v5 offset:8064\n"
"v_add_f32 v65, 0x3727c5ac, v65\n"
"v_mfma_f32_32x32x16_bf16 v[48:63], a[196:199], v[132:135], v[48:63]\n"
"ds_read_b128 v[164:167], v5 offset:8576\n"
"buffer_load_dwordx4 v25, s[32:35], s63 offen offset:1024 sc1 lds\n"
"v_cvt_pk_bf16_f32 v65, 0, v65\n"
"v_mfma_f32_32x32x16_bf16 v[48:63], a[200:203], v[136:139], v[48:63]\n"
"ds_read_b128 v[168:171], v5 offset:17280\n"
"v_sqrt_f32 v65, v65\n"
"v_mfma_f32_32x32x16_bf16 v[48:63], a[204:207], v[140:143], v[48:63]\n"
"ds_read_b128 v[172:175], v5 offset:17792\n"
"v_cvt_pk_bf16_f32 v65, v64, v65\n"
"s_waitcnt lgkmcnt(4)\n"
"v_mfma_f32_32x32x16_bf16 v[48:63], a[208:211], v[144:147], v[48:63]\n"
"ds_read_b128 v[176:179], v5 offset:26496\n"
"ds_write_b32 v10, v65 offset:128\n"
"v_mfma_f32_32x32x16_bf16 v[48:63], a[212:215], v[148:151], v[48:63]\n"
"ds_read_b128 v[180:183], v5 offset:27008\n"
"buffer_load_dwordx4 v25, s[32:35], s63 offen offset:2048 sc1 lds\n"
"v_mfma_f32_32x32x16_bf16 v[48:63], a[216:219], v[152:155], v[48:63]\n"
"ds_read_b128 v[184:187], v5 offset:35712\n"
"v_mfma_f32_32x32x16_bf16 v[48:63], a[220:223], v[156:159], v[48:63]\n"
"ds_read_b128 v[188:191], v5 offset:36224\n"
"s_waitcnt lgkmcnt(5)\n"
"v_mfma_f32_32x32x16_bf16 v[48:63], a[224:227], v[160:163], v[48:63]\n"
"s_waitcnt vmcnt(9)\n"
"s_waitcnt lgkmcnt(3)\n"
"s_barrier\n"
"ds_read_b128 v[128:131], v4 offset:0\n"
"v_mfma_f32_32x32x16_bf16 v[48:63], a[228:231], v[164:167], v[48:63]\n"
"ds_read_b128 v[132:135], v4 offset:512\n"
"buffer_load_dwordx4 v25, s[32:35], s63 offen offset:3072 sc1 lds\n"
"s_add_u32 s63, s63, 1187840\n"
"v_mfma_f32_32x32x16_bf16 v[48:63], a[232:235], v[168:171], v[48:63]\n"
"ds_read_b128 v[136:139], v4 offset:9216\n"
"v_mfma_f32_32x32x16_bf16 v[48:63], a[236:239], v[172:175], v[48:63]\n"
"ds_read_b128 v[140:143], v4 offset:9728\n"
"s_waitcnt lgkmcnt(4)\n"
"v_mfma_f32_32x32x16_bf16 v[48:63], a[240:243], v[176:179], v[48:63]\n"
"ds_read_b128 v[144:147], v4 offset:18432\n"
"v_mfma_f32_32x32x16_bf16 v[48:63], a[244:247], v[180:183], v[48:63]\n"
"ds_read_b128 v[148:151], v4 offset:18944\n"
"v_mfma_f32_32x32x16_bf16 v[48:63], a[248:251], v[184:187], v[48:63]\n"
"ds_read_b128 v[152:155], v4 offset:27648\n"
"s_sub_u32 s62, s62, 1\n"
"v_mfma_f32_32x32x16_bf16 v[48:63], a[252:255], v[188:191], v[48:63]\n"
"ds_read_b128 v[156:159], v4 offset:28160\n"
"s_cbranch_scc0 .L_hl\n"
"s_nop 1\n"
"ds_read_b32 v74, v11 offset:128\n"
"s_add_u32 s68, s68, 1187840\n"
"s_waitcnt lgkmcnt(0)\n"
"v_lshlrev_b32 v72, 16, v74\n"
"v_and_b32 v74, 0xffff0000, v74\n"
"v_rcp_f32 v73, v74\n"
"v_mul_f32 v74, v74, v74\n"
"v_mul_f32 v73, 0xbfb8aa3b, v73\n"
"v_fma_f32 v48, -v72, v112, v48\n"
"v_fma_f32 v49, -v72, v113, v49\n"
"v_fma_f32 v50, -v72, v114, v50\n"
"v_fma_f32 v51, -v72, v115, v51\n"
"v_fma_f32 v52, -v72, v116, v52\n"
"v_fma_f32 v53, -v72, v117, v53\n"
"v_fma_f32 v54, -v72, v118, v54\n"
"v_fma_f32 v55, -v72, v119, v55\n"
"v_fma_f32 v56, -v72, v120, v56\n"
"v_fma_f32 v57, -v72, v121, v57\n"
"v_fma_f32 v58, -v72, v122, v58\n"
"v_fma_f32 v59, -v72, v123, v59\n"
"v_fma_f32 v60, -v72, v124, v60\n"
"v_fma_f32 v61, -v72, v125, v61\n"
"v_fma_f32 v62, -v72, v126, v62\n"
"v_fma_f32 v63, -v72, v127, v63\n"
"v_mul_f32 v76, v48, v73\n"
"v_mul_f32 v77, v49, v73\n"
"v_mul_f32 v78, v50, v73\n"
"v_mul_f32 v79, v51, v73\n"
"v_mul_f32 v80, v52, v73\n"
"v_mul_f32 v81, v53, v73\n"
"v_mul_f32 v82, v54, v73\n"
"v_mul_f32 v83, v55, v73\n"
"v_exp_f32 v76, v76\n"
"v_exp_f32 v77, v77\n"
"v_mul_f32 v48, v48, v56\n"
"v_mul_f32 v49, v49, v57\n"
"v_exp_f32 v78, v78\n"
"v_exp_f32 v79, v79\n"
"v_mul_f32 v50, v50, v58\n"
"v_mul_f32 v51, v51, v59\n"
"v_exp_f32 v80, v80\n"
"v_exp_f32 v81, v81\n"
"v_mul_f32 v52, v52, v60\n"
"v_mul_f32 v53, v53, v61\n"
"v_exp_f32 v82, v82\n"
"v_exp_f32 v83, v83\n"
"v_mul_f32 v54, v54, v62\n"
"v_mul_f32 v55, v55, v63\n"
"v_fma_f32 v76, v76, v74, v74\n"
"v_fma_f32 v77, v77, v74, v74\n"
"v_rcp_f32 v76, v76\n"
"v_rcp_f32 v77, v77\n"
"v_fma_f32 v78, v78, v74, v74\n"
"v_fma_f32 v79, v79, v74, v74\n"
"v_rcp_f32 v78, v78\n"
"v_rcp_f32 v79, v79\n"
"v_fma_f32 v80, v80, v74, v74\n"
"v_fma_f32 v81, v81, v74, v74\n"
"v_rcp_f32 v80, v80\n"
"v_rcp_f32 v81, v81\n"
"v_fma_f32 v82, v82, v74, v74\n"
"v_fma_f32 v83, v83, v74, v74\n"
"v_rcp_f32 v82, v82\n"
"v_rcp_f32 v83, v83\n"
"v_mul_f32 v48, v48, v76\n"
"v_mul_f32 v49, v49, v77\n"
"v_mul_f32 v50, v50, v78\n"
"v_mul_f32 v51, v51, v79\n"
"v_mul_f32 v52, v52, v80\n"
"v_mul_f32 v53, v53, v81\n"
"v_mul_f32 v54, v54, v82\n"
"v_mul_f32 v55, v55, v83\n"
"v_cvt_pk_bf16_f32 v84, v48, v49\n"
"v_cvt_pk_bf16_f32 v85, v50, v51\n"
"v_cvt_pk_bf16_f32 v86, v52, v53\n"
"v_cvt_pk_bf16_f32 v87, v54, v55\n"
"buffer_store_dwordx4 v[84:87], v15, s[32:35], s68 offen sc1\n"
"s_waitcnt vmcnt(9)\n"
"s_barrier\n"
"buffer_store_dwordx2 v[16:17], v18, s[32:35], s68 offen sc1\n"
"s_add_u32 s68, s68, 1187840\n"
"s_waitcnt vmcnt(1)\n"
"s_barrier\n"
"buffer_store_dwordx2 v[16:17], v18, s[32:35], s68 offen sc1\n"
"s_branch .L_end\n"
".L_d:\n"
"s_lshl_b32 s65, s50, 11\n"
"s_mul_i32 s48, s50, 1152\n"
"s_add_u32 s48, s48, 4612096\n"
"s_add_u32 s65, s65, 4833280\n"
"s_cmp_eq_u32 s29, 0\n"
"s_cselect_b32 s48, s65, s48\n"
"s_cselect_b32 s49, 280, 0\n"
"s_lshl2_add_u32 s49, s27, s49\n"
"s_waitcnt lgkmcnt(0)\n"
"s_add_u32 s54, s52, 0x7f4a7c15\n"
"s_addc_u32 s55, s53, 0x9e3779b9\n"
"s_lshl_b32 s67, s49, 5\n"
"s_add_u32 s67, s67, 4898816\n"
"s_sub_u32 s51, s67, s48\n"
"s_add_u32 s92, s32, s51\n"
"s_addc_u32 s93, s33, 0x10000\n"
"s_cmp_eq_u32 s26, 0\n"
"s_cselect_b32 s94, 1, 0\n"
"s_mov_b32 s95, 0x20000\n"
"v_mov_b32 v8, s54\n"
"v_mov_b32 v9, s55\n"
"v_cmp_gt_u32 vcc, 8, v1\n"
"v_mul_u32_u24 v3, 296960, v1\n"
"v_mov_b32 v6, 0x7fff0000\n"
"v_cndmask_b32 v3, v6, v3, vcc\n"
"s_add_u32 s51, s67, 76021760\n"
"buffer_store_dwordx2 v[8:9], v3, s[32:35], s51 offen sc1\n"
"s_sub_u32 s67, s48, 296960\n"
"s_mul_i32 s48, s28, 296960\n"
"s_add_u32 s65, s65, s48\n"
"s_mul_i32 s96, s29, 3670016\n"
"s_add_u32 s36, s18, 0x0\n"
"s_addc_u32 s37, s19, 0x0\n"
"s_and_b32 s37, s37, 0xffff\n"
"s_mov_b32 s38, 0x700000\n"
"s_mov_b32 s39, 0x20000\n"
"s_add_u32 s40, s18, 0xfffffc80\n"
"s_addc_u32 s41, s19, 0xffffffff\n"
"s_and_b32 s41, s41, 0xffff\n"
"s_mov_b32 s42, 0x700380\n"
"s_mov_b32 s43, 0x20000\n"
"s_lshl_b32 s48, s27, 8\n"
"s_add_u32 s96, s96, s48\n"
"v_and_b32 v8, 3, v1\n"
"v_mul_u32_u24 v8, 16, v8\n"
"s_and_b32 s48, s26, 1\n"
"s_lshl_b32 s48, s48, 6\n"
"s_lshr_b32 s49, s26, 1\n"
"s_lshl_b32 s49, s49, 10\n"
"s_add_u32 s48, s48, s49\n"
"v_add_u32 v8, s48, v8\n"
"v_lshrrev_b32 v4, 3, v1\n"
"v_bfe_u32 v5, v1, 4, 1\n"
"s_and_b32 s48, s26, 1\n"
"s_lshl_b32 s48, s48, 1\n"
"v_or_b32 v5, s48, v5\n"
"v_lshlrev_b32 v5, 1, v5\n"
"v_and_b32 v6, 7, v1\n"
"v_xor_b32 v5, v5, v6\n"
"v_mul_u32_u24 v4, 2048, v4\n"
"v_lshl_add_u32 v7, v5, 4, v4\n"
"s_mul_i32 s48, s26, 16384\n"
"s_add_u32 s96, s96, s48\n"
"s_mul_i32 s97, s26, 2048\n"
"s_add_u32 m0, s97, 0\n"
"s_nop 0\n"
"buffer_load_dwordx4 v7, s[36:39], s96 offen offset:0 lds\n"
"buffer_load_dwordx4 v7, s[40:43], s96 offen offset:1024 lds\n"
"s_add_u32 s96, s96, 65536\n"
"s_add_u32 m0, s97, 8192\n"
"s_nop 0\n"
"buffer_load_dwordx4 v7, s[36:39], s96 offen offset:0 lds\n"
"buffer_load_dwordx4 v7, s[40:43], s96 offen offset:1024 lds\n"
"s_add_u32 s96, s96, 65536\n"
"s_add_u32 m0, s97, 16384\n"
"s_nop 0\n"
"buffer_load_dwordx4 v7, s[36:39], s96 offen offset:0 lds\n"
"buffer_load_dwordx4 v7, s[40:43], s96 offen offset:1024 lds\n"
"s_add_u32 s96, s96, 65536\n"
"s_add_u32 m0, s97, 24576\n"
"s_nop 0\n"
"buffer_load_dwordx4 v7, s[36:39], s96 offen offset:0 lds\n"
"buffer_load_dwordx4 v7, s[40:43], s96 offen offset:1024 lds\n"
"s_add_u32 s96, s96, 65536\n"
"s_add_u32 m0, s97, 32768\n"
"s_nop 0\n"
"buffer_load_dwordx4 v7, s[36:39], s96 offen offset:0 lds\n"
"buffer_load_dwordx4 v7, s[40:43], s96 offen offset:1024 lds\n"
"s_add_u32 s96, s96, 65536\n"
"s_add_u32 m0, s97, 40960\n"
"s_nop 0\n"
"buffer_load_dwordx4 v7, s[36:39], s96 offen offset:0 lds\n"
"buffer_load_dwordx4 v7, s[40:43], s96 offen offset:1024 lds\n"
"s_add_u32 s96, s96, 65536\n"
"s_add_u32 m0, s97, 49152\n"
"s_nop 0\n"
"buffer_load_dwordx4 v7, s[36:39], s96 offen offset:0 lds\n"
"buffer_load_dwordx4 v7, s[40:43], s96 offen offset:1024 lds\n"
"s_add_u32 s96, s96, 65536\n"
"s_add_u32 m0, s97, 57344\n"
"s_nop 0\n"
"buffer_load_dwordx4 v7, s[36:39], s96 offen offset:0 lds\n"
"buffer_load_dwordx4 v7, s[40:43], s96 offen offset:1024 lds\n"
"s_add_u32 s96, s96, 65536\n"
"s_add_u32 m0, s97, 65536\n"
"s_nop 0\n"
"buffer_load_dwordx4 v7, s[36:39], s96 offen offset:0 lds\n"
"buffer_load_dwordx4 v7, s[40:43], s96 offen offset:1024 lds\n"
"s_add_u32 s96, s96, 65536\n"
"s_add_u32 m0, s97, 73728\n"
"s_nop 0\n"
"buffer_load_dwordx4 v7, s[36:39], s96 offen offset:0 lds\n"
"buffer_load_dwordx4 v7, s[40:43], s96 offen offset:1024 lds\n"
"s_add_u32 s96, s96, 65536\n"
"s_add_u32 m0, s97, 81920\n"
"s_nop 0\n"
"buffer_load_dwordx4 v7, s[36:39], s96 offen offset:0 lds\n"
"buffer_load_dwordx4 v7, s[40:43], s96 offen offset:1024 lds\n"
"s_add_u32 s96, s96, 65536\n"
"s_add_u32 m0, s97, 90112\n"
"s_nop 0\n"
"buffer_load_dwordx4 v7, s[36:39], s96 offen offset:0 lds\n"
"buffer_load_dwordx4 v7, s[40:43], s96 offen offset:1024 lds\n"
"s_add_u32 s96, s96, 65536\n"
"s_add_u32 m0, s97, 98304\n"
"s_nop 0\n"
"buffer_load_dwordx4 v7, s[36:39], s96 offen offset:0 lds\n"
"buffer_load_dwordx4 v7, s[40:43], s96 offen offset:1024 lds\n"
"s_add_u32 s96, s96, 65536\n"
"s_add_u32 m0, s97, 106496\n"
"s_nop 0\n"
"buffer_load_dwordx4 v7, s[36:39], s96 offen offset:0 lds\n"
"buffer_load_dwordx4 v7, s[40:43], s96 offen offset:1024 lds\n"
"s_add_u32 s96, s96, 65536\n"
"v_lshrrev_b32 v5, 4, v1\n"
"v_bfe_u32 v4, v1, 3, 1\n"
"v_and_b32 v6, 1, v5\n"
"v_lshl_add_u32 v4, v6, 1, v4\n"
"v_lshlrev_b32 v4, 5, v4\n"
"v_mul_u32_u24 v5, 2048, v5\n"
"v_bfe_u32 v6, v1, 2, 2\n"
"v_lshl_add_u32 v5, v6, 7, v5\n"
"v_xor_b32 v8, v4, v8\n"
"v_add_u32 v8, v5, v8\n"
"v_add_u32 v9, 65536, v8\n"
"s_waitcnt vmcnt(24)\n"
"s_barrier\n"
"s_add_u32 m0, s97, 114688\n"
"s_nop 0\n"
"buffer_load_dwordx4 v7, s[36:39], s96 offen offset:0 lds\n"
"buffer_load_dwordx4 v7, s[40:43], s96 offen offset:1024 lds\n"
"s_add_u32 s96, s96, 65536\n"
"s_add_u32 m0, s97, 122880\n"
"s_nop 0\n"
"buffer_load_dwordx4 v7, s[36:39], s96 offen offset:0 lds\n"
"buffer_load_dwordx4 v7, s[40:43], s96 offen offset:1024 lds\n"
"s_add_u32 s96, s96, 65536\n"
"ds_read_b64_tr_b16 a[0:1], v8 offset:0\n"
"ds_read_b64_tr_b16 a[2:3], v8 offset:512\n"
"ds_read_b64_tr_b16 a[4:5], v8 offset:8\n"
"ds_read_b64_tr_b16 a[6:7], v8 offset:520\n"
"ds_read_b64_tr_b16 a[8:9], v8 offset:8192\n"
"ds_read_b64_tr_b16 a[10:11], v8 offset:8704\n"
"ds_read_b64_tr_b16 a[12:13], v8 offset:8200\n"
"ds_read_b64_tr_b16 a[14:15], v8 offset:8712\n"
"s_waitcnt lgkmcnt(0)\n"
"s_waitcnt vmcnt(24)\n"
"s_barrier\n"
"s_add_u32 m0, s97, 0\n"
"s_nop 0\n"
"buffer_load_dwordx4 v7, s[36:39], s96 offen offset:0 lds\n"
"buffer_load_dwordx4 v7, s[40:43], s96 offen offset:1024 lds\n"
"s_add_u32 s96, s96, 65536\n"
"s_add_u32 m0, s97, 8192\n"
"s_nop 0\n"
"buffer_load_dwordx4 v7, s[36:39], s96 offen offset:0 lds\n"
"buffer_load_dwordx4 v7, s[40:43], s96 offen offset:1024 lds\n"
"s_add_u32 s96, s96, 65536\n"
"ds_read_b64_tr_b16 a[16:17], v8 offset:16384\n"
"ds_read_b64_tr_b16 a[18:19], v8 offset:16896\n"
"ds_read_b64_tr_b16 a[20:21], v8 offset:16392\n"
"ds_read_b64_tr_b16 a[22:23], v8 offset:16904\n"
"ds_read_b64_tr_b16 a[24:25], v8 offset:24576\n"
"ds_read_b64_tr_b16 a[26:27], v8 offset:25088\n"
"ds_read_b64_tr_b16 a[28:29], v8 offset:24584\n"
"ds_read_b64_tr_b16 a[30:31], v8 offset:25096\n"
"s_waitcnt lgkmcnt(0)\n"
"s_waitcnt vmcnt(24)\n"
"s_barrier\n"
"s_add_u32 m0, s97, 16384\n"
"s_nop 0\n"
"buffer_load_dwordx4 v7, s[36:39], s96 offen offset:0 lds\n"
"buffer_load_dwordx4 v7, s[40:43], s96 offen offset:1024 lds\n"
"s_add_u32 s96, s96, 65536\n"
"s_add_u32 m0, s97, 24576\n"
"s_nop 0\n"
"buffer_load_dwordx4 v7, s[36:39], s96 offen offset:0 lds\n"
"buffer_load_dwordx4 v7, s[40:43], s96 offen offset:1024 lds\n"
"s_add_u32 s96, s96, 65536\n"
"ds_read_b64_tr_b16 a[32:33], v8 offset:32768\n"
"ds_read_b64_tr_b16 a[34:35], v8 offset:33280\n"
"ds_read_b64_tr_b16 a[36:37], v8 offset:32776\n"
"ds_read_b64_tr_b16 a[38:39], v8 offset:33288\n"
"ds_read_b64_tr_b16 a[40:41], v8 offset:40960\n"
"ds_read_b64_tr_b16 a[42:43], v8 offset:41472\n"
"ds_read_b64_tr_b16 a[44:45], v8 offset:40968\n"
"ds_read_b64_tr_b16 a[46:47], v8 offset:41480\n"
"s_waitcnt lgkmcnt(0)\n"
"s_waitcnt vmcnt(24)\n"
"s_barrier\n"
"s_add_u32 m0, s97, 32768\n"
"s_nop 0\n"
"buffer_load_dwordx4 v7, s[36:39], s96 offen offset:0 lds\n"
"buffer_load_dwordx4 v7, s[40:43], s96 offen offset:1024 lds\n"
"s_add_u32 s96, s96, 65536\n"
"s_add_u32 m0, s97, 40960\n"
"s_nop 0\n"
"buffer_load_dwordx4 v7, s[36:39], s96 offen offset:0 lds\n"
"buffer_load_dwordx4 v7, s[40:43], s96 offen offset:1024 lds\n"
"s_add_u32 s96, s96, 65536\n"
"ds_read_b64_tr_b16 a[48:49], v8 offset:49152\n"
"ds_read_b64_tr_b16 a[50:51], v8 offset:49664\n"
"ds_read_b64_tr_b16 a[52:53], v8 offset:49160\n"
"ds_read_b64_tr_b16 a[54:55], v8 offset:49672\n"
"ds_read_b64_tr_b16 a[56:57], v8 offset:57344\n"
"ds_read_b64_tr_b16 a[58:59], v8 offset:57856\n"
"ds_read_b64_tr_b16 a[60:61], v8 offset:57352\n"
"ds_read_b64_tr_b16 a[62:63], v8 offset:57864\n"
"s_waitcnt lgkmcnt(0)\n"
"s_waitcnt vmcnt(24)\n"
"s_barrier\n"
"s_add_u32 m0, s97, 49152\n"
"s_nop 0\n"
"buffer_load_dwordx4 v7, s[36:39], s96 offen offset:0 lds\n"
"buffer_load_dwordx4 v7, s[40:43], s96 offen offset:1024 lds\n"
"s_add_u32 s96, s96, 65536\n"
"s_add_u32 m0, s97, 57344\n"
"s_nop 0\n"
"buffer_load_dwordx4 v7, s[36:39], s96 offen offset:0 lds\n"
"buffer_load_dwordx4 v7, s[40:43], s96 offen offset:1024 lds\n"
"s_add_u32 s96, s96, 65536\n"
"ds_read_b64_tr_b16 a[64:65], v9 offset:0\n"
"ds_read_b64_tr_b16 a[66:67], v9 offset:512\n"
"ds_read_b64_tr_b16 a[68:69], v9 offset:8\n"
"ds_read_b64_tr_b16 a[70:71], v9 offset:520\n"
"ds_read_b64_tr_b16 a[72:73], v9 offset:8192\n"
"ds_read_b64_tr_b16 a[74:75], v9 offset:8704\n"
"ds_read_b64_tr_b16 a[76:77], v9 offset:8200\n"
"ds_read_b64_tr_b16 a[78:79], v9 offset:8712\n"
"s_waitcnt lgkmcnt(0)\n"
"s_waitcnt vmcnt(24)\n"
"s_barrier\n"
"s_add_u32 m0, s97, 65536\n"
"s_nop 0\n"
"buffer_load_dwordx4 v7, s[36:39], s96 offen offset:0 lds\n"
"buffer_load_dwordx4 v7, s[40:43], s96 offen offset:1024 lds\n"
"s_add_u32 s96, s96, 65536\n"
"s_add_u32 m0, s97, 73728\n"
"s_nop 0\n"
"buffer_load_dwordx4 v7, s[36:39], s96 offen offset:0 lds\n"
"buffer_load_dwordx4 v7, s[40:43], s96 offen offset:1024 lds\n"
"s_add_u32 s96, s96, 65536\n"
"ds_read_b64_tr_b16 a[80:81], v9 offset:16384\n"
"ds_read_b64_tr_b16 a[82:83], v9 offset:16896\n"
"ds_read_b64_tr_b16 a[84:85], v9 offset:16392\n"
"ds_read_b64_tr_b16 a[86:87], v9 offset:16904\n"
"ds_read_b64_tr_b16 a[88:89], v9 offset:24576\n"
"ds_read_b64_tr_b16 a[90:91], v9 offset:25088\n"
"ds_read_b64_tr_b16 a[92:93], v9 offset:24584\n"
"ds_read_b64_tr_b16 a[94:95], v9 offset:25096\n"
"s_waitcnt lgkmcnt(0)\n"
"s_waitcnt vmcnt(24)\n"
"s_barrier\n"
"s_add_u32 m0, s97, 81920\n"
"s_nop 0\n"
"buffer_load_dwordx4 v7, s[36:39], s96 offen offset:0 lds\n"
"buffer_load_dwordx4 v7, s[40:43], s96 offen offset:1024 lds\n"
"s_add_u32 s96, s96, 65536\n"
"s_add_u32 m0, s97, 90112\n"
"s_nop 0\n"
"buffer_load_dwordx4 v7, s[36:39], s96 offen offset:0 lds\n"
"buffer_load_dwordx4 v7, s[40:43], s96 offen offset:1024 lds\n"
"s_add_u32 s96, s96, 65536\n"
"ds_read_b64_tr_b16 a[96:97], v9 offset:32768\n"
"ds_read_b64_tr_b16 a[98:99], v9 offset:33280\n"
"ds_read_b64_tr_b16 a[100:101], v9 offset:32776\n"
"ds_read_b64_tr_b16 a[102:103], v9 offset:33288\n"
"ds_read_b64_tr_b16 a[104:105], v9 offset:40960\n"
"ds_read_b64_tr_b16 a[106:107], v9 offset:41472\n"
"ds_read_b64_tr_b16 a[108:109], v9 offset:40968\n"
"ds_read_b64_tr_b16 a[110:111], v9 offset:41480\n"
"s_waitcnt lgkmcnt(0)\n"
"s_waitcnt vmcnt(24)\n"
"s_barrier\n"
"s_add_u32 m0, s97, 98304\n"
"s_nop 0\n"
"buffer_load_dwordx4 v7, s[36:39], s96 offen offset:0 lds\n"
"buffer_load_dwordx4 v7, s[40:43], s96 offen offset:1024 lds\n"
"s_add_u32 s96, s96, 65536\n"
"s_add_u32 m0, s97, 106496\n"
"s_nop 0\n"
"buffer_load_dwordx4 v7, s[36:39], s96 offen offset:0 lds\n"
"buffer_load_dwordx4 v7, s[40:43], s96 offen offset:1024 lds\n"
"s_add_u32 s96, s96, 65536\n"
"ds_read_b64_tr_b16 a[112:113], v9 offset:49152\n"
"ds_read_b64_tr_b16 a[114:115], v9 offset:49664\n"
"ds_read_b64_tr_b16 a[116:117], v9 offset:49160\n"
"ds_read_b64_tr_b16 a[118:119], v9 offset:49672\n"
"ds_read_b64_tr_b16 a[120:121], v9 offset:57344\n"
"ds_read_b64_tr_b16 a[122:123], v9 offset:57856\n"
"ds_read_b64_tr_b16 a[124:125], v9 offset:57352\n"
"ds_read_b64_tr_b16 a[126:127], v9 offset:57864\n"
"s_waitcnt lgkmcnt(0)\n"
"s_waitcnt vmcnt(24)\n"
"s_barrier\n"
"s_add_u32 m0, s97, 114688\n"
"s_nop 0\n"
"buffer_load_dwordx4 v7, s[36:39], s96 offen offset:0 lds\n"
"buffer_load_dwordx4 v7, s[40:43], s96 offen offset:1024 lds\n"
"s_add_u32 s96, s96, 65536\n"
"s_add_u32 m0, s97, 122880\n"
"s_nop 0\n"
"buffer_load_dwordx4 v7, s[36:39], s96 offen offset:0 lds\n"
"buffer_load_dwordx4 v7, s[40:43], s96 offen offset:1024 lds\n"
"s_add_u32 s96, s96, 65536\n"
"ds_read_b64_tr_b16 a[128:129], v8 offset:0\n"
"ds_read_b64_tr_b16 a[130:131], v8 offset:512\n"
"ds_read_b64_tr_b16 a[132:133], v8 offset:8\n"
"ds_read_b64_tr_b16 a[134:135], v8 offset:520\n"
"ds_read_b64_tr_b16 a[136:137], v8 offset:8192\n"
"ds_read_b64_tr_b16 a[138:139], v8 offset:8704\n"
"ds_read_b64_tr_b16 a[140:141], v8 offset:8200\n"
"ds_read_b64_tr_b16 a[142:143], v8 offset:8712\n"
"s_waitcnt lgkmcnt(0)\n"
"s_waitcnt vmcnt(24)\n"
"s_barrier\n"
"s_add_u32 m0, s97, 0\n"
"s_nop 0\n"
"buffer_load_dwordx4 v7, s[36:39], s96 offen offset:0 lds\n"
"buffer_load_dwordx4 v7, s[40:43], s96 offen offset:1024 lds\n"
"s_add_u32 s96, s96, 65536\n"
"s_add_u32 m0, s97, 8192\n"
"s_nop 0\n"
"buffer_load_dwordx4 v7, s[36:39], s96 offen offset:0 lds\n"
"buffer_load_dwordx4 v7, s[40:43], s96 offen offset:1024 lds\n"
"s_add_u32 s96, s96, 65536\n"
"ds_read_b64_tr_b16 a[144:145], v8 offset:16384\n"
"ds_read_b64_tr_b16 a[146:147], v8 offset:16896\n"
"ds_read_b64_tr_b16 a[148:149], v8 offset:16392\n"
"ds_read_b64_tr_b16 a[150:151], v8 offset:16904\n"
"ds_read_b64_tr_b16 a[152:153], v8 offset:24576\n"
"ds_read_b64_tr_b16 a[154:155], v8 offset:25088\n"
"ds_read_b64_tr_b16 a[156:157], v8 offset:24584\n"
"ds_read_b64_tr_b16 a[158:159], v8 offset:25096\n"
"s_waitcnt lgkmcnt(0)\n"
"s_waitcnt vmcnt(24)\n"
"s_barrier\n"
"s_add_u32 m0, s97, 16384\n"
"s_nop 0\n"
"buffer_load_dwordx4 v7, s[36:39], s96 offen offset:0 lds\n"
"buffer_load_dwordx4 v7, s[40:43], s96 offen offset:1024 lds\n"
"s_add_u32 s96, s96, 65536\n"
"s_add_u32 m0, s97, 24576\n"
"s_nop 0\n"
"buffer_load_dwordx4 v7, s[36:39], s96 offen offset:0 lds\n"
"buffer_load_dwordx4 v7, s[40:43], s96 offen offset:1024 lds\n"
"s_add_u32 s96, s96, 65536\n"
"ds_read_b64_tr_b16 a[160:161], v8 offset:32768\n"
"ds_read_b64_tr_b16 a[162:163], v8 offset:33280\n"
"ds_read_b64_tr_b16 a[164:165], v8 offset:32776\n"
"ds_read_b64_tr_b16 a[166:167], v8 offset:33288\n"
"ds_read_b64_tr_b16 a[168:169], v8 offset:40960\n"
"ds_read_b64_tr_b16 a[170:171], v8 offset:41472\n"
"ds_read_b64_tr_b16 a[172:173], v8 offset:40968\n"
"ds_read_b64_tr_b16 a[174:175], v8 offset:41480\n"
"s_waitcnt lgkmcnt(0)\n"
"s_waitcnt vmcnt(24)\n"
"s_barrier\n"
"s_add_u32 m0, s97, 32768\n"
"s_nop 0\n"
"buffer_load_dwordx4 v7, s[36:39], s96 offen offset:0 lds\n"
"buffer_load_dwordx4 v7, s[40:43], s96 offen offset:1024 lds\n"
"s_add_u32 s96, s96, 65536\n"
"s_add_u32 m0, s97, 40960\n"
"s_nop 0\n"
"buffer_load_dwordx4 v7, s[36:39], s96 offen offset:0 lds\n"
"buffer_load_dwordx4 v7, s[40:43], s96 offen offset:1024 lds\n"
"s_add_u32 s96, s96, 65536\n"
"ds_read_b64_tr_b16 a[176:177], v8 offset:49152\n"
"ds_read_b64_tr_b16 a[178:179], v8 offset:49664\n"
"ds_read_b64_tr_b16 a[180:181], v8 offset:49160\n"
"ds_read_b64_tr_b16 a[182:183], v8 offset:49672\n"
"ds_read_b64_tr_b16 a[184:185], v8 offset:57344\n"
"ds_read_b64_tr_b16 a[186:187], v8 offset:57856\n"
"ds_read_b64_tr_b16 a[188:189], v8 offset:57352\n"
"ds_read_b64_tr_b16 a[190:191], v8 offset:57864\n"
"s_waitcnt lgkmcnt(0)\n"
"s_waitcnt vmcnt(24)\n"
"s_barrier\n"
"s_add_u32 m0, s97, 49152\n"
"s_nop 0\n"
"buffer_load_dwordx4 v7, s[36:39], s96 offen offset:0 lds\n"
"buffer_load_dwordx4 v7, s[40:43], s96 offen offset:1024 lds\n"
"s_add_u32 s96, s96, 65536\n"
"s_add_u32 m0, s97, 57344\n"
"s_nop 0\n"
"buffer_load_dwordx4 v7, s[36:39], s96 offen offset:0 lds\n"
"buffer_load_dwordx4 v7, s[40:43], s96 offen offset:1024 lds\n"
"s_add_u32 s96, s96, 65536\n"
"ds_read_b64_tr_b16 a[192:193], v9 offset:0\n"
"ds_read_b64_tr_b16 a[194:195], v9 offset:512\n"
"ds_read_b64_tr_b16 a[196:197], v9 offset:8\n"
"ds_read_b64_tr_b16 a[198:199], v9 offset:520\n"
"ds_read_b64_tr_b16 a[200:201], v9 offset:8192\n"
"ds_read_b64_tr_b16 a[202:203], v9 offset:8704\n"
"ds_read_b64_tr_b16 a[204:205], v9 offset:8200\n"
"ds_read_b64_tr_b16 a[206:207], v9 offset:8712\n"
"s_waitcnt lgkmcnt(0)\n"
"s_waitcnt vmcnt(24)\n"
"s_barrier\n"
"s_add_u32 m0, s97, 65536\n"
"s_nop 0\n"
"buffer_load_dwordx4 v7, s[36:39], s96 offen offset:0 lds\n"
"buffer_load_dwordx4 v7, s[40:43], s96 offen offset:1024 lds\n"
"s_add_u32 s96, s96, 65536\n"
"s_add_u32 m0, s97, 73728\n"
"s_nop 0\n"
"buffer_load_dwordx4 v7, s[36:39], s96 offen offset:0 lds\n"
"buffer_load_dwordx4 v7, s[40:43], s96 offen offset:1024 lds\n"
"s_add_u32 s96, s96, 65536\n"
"ds_read_b64_tr_b16 a[208:209], v9 offset:16384\n"
"ds_read_b64_tr_b16 a[210:211], v9 offset:16896\n"
"ds_read_b64_tr_b16 a[212:213], v9 offset:16392\n"
"ds_read_b64_tr_b16 a[214:215], v9 offset:16904\n"
"ds_read_b64_tr_b16 a[216:217], v9 offset:24576\n"
"ds_read_b64_tr_b16 a[218:219], v9 offset:25088\n"
"ds_read_b64_tr_b16 a[220:221], v9 offset:24584\n"
"ds_read_b64_tr_b16 a[222:223], v9 offset:25096\n"
"s_waitcnt lgkmcnt(0)\n"
"s_waitcnt vmcnt(24)\n"
"s_barrier\n"
"s_add_u32 m0, s97, 81920\n"
"s_nop 0\n"
"buffer_load_dwordx4 v7, s[36:39], s96 offen offset:0 lds\n"
"buffer_load_dwordx4 v7, s[40:43], s96 offen offset:1024 lds\n"
"s_add_u32 s96, s96, 65536\n"
"s_add_u32 m0, s97, 90112\n"
"s_nop 0\n"
"buffer_load_dwordx4 v7, s[36:39], s96 offen offset:0 lds\n"
"buffer_load_dwordx4 v7, s[40:43], s96 offen offset:1024 lds\n"
"s_add_u32 s96, s96, 65536\n"
"ds_read_b64_tr_b16 a[224:225], v9 offset:32768\n"
"ds_read_b64_tr_b16 a[226:227], v9 offset:33280\n"
"ds_read_b64_tr_b16 a[228:229], v9 offset:32776\n"
"ds_read_b64_tr_b16 a[230:231], v9 offset:33288\n"
"ds_read_b64_tr_b16 a[232:233], v9 offset:40960\n"
"ds_read_b64_tr_b16 a[234:235], v9 offset:41472\n"
"ds_read_b64_tr_b16 a[236:237], v9 offset:40968\n"
"ds_read_b64_tr_b16 a[238:239], v9 offset:41480\n"
"s_waitcnt lgkmcnt(0)\n"
"s_waitcnt vmcnt(24)\n"
"s_barrier\n"
"s_add_u32 m0, s97, 98304\n"
"s_nop 0\n"
"buffer_load_dwordx4 v7, s[36:39], s96 offen offset:0 lds\n"
"buffer_load_dwordx4 v7, s[40:43], s96 offen offset:1024 lds\n"
"s_add_u32 s96, s96, 65536\n"
"s_add_u32 m0, s97, 106496\n"
"s_nop 0\n"
"buffer_load_dwordx4 v7, s[36:39], s96 offen offset:0 lds\n"
"buffer_load_dwordx4 v7, s[40:43], s96 offen offset:1024 lds\n"
"s_add_u32 s96, s96, 65536\n"
"ds_read_b64_tr_b16 a[240:241], v9 offset:49152\n"
"ds_read_b64_tr_b16 a[242:243], v9 offset:49664\n"
"ds_read_b64_tr_b16 a[244:245], v9 offset:49160\n"
"ds_read_b64_tr_b16 a[246:247], v9 offset:49672\n"
"ds_read_b64_tr_b16 a[248:249], v9 offset:57344\n"
"ds_read_b64_tr_b16 a[250:251], v9 offset:57856\n"
"ds_read_b64_tr_b16 a[252:253], v9 offset:57352\n"
"ds_read_b64_tr_b16 a[254:255], v9 offset:57864\n"
"s_waitcnt lgkmcnt(0)\n"
"s_waitcnt vmcnt(24)\n"
"s_barrier\n"
"s_add_u32 m0, s97, 114688\n"
"s_nop 0\n"
"buffer_load_dwordx4 v7, s[36:39], s96 offen offset:0 lds\n"
"buffer_load_dwordx4 v7, s[40:43], s96 offen offset:1024 lds\n"
"s_add_u32 s96, s96, 65536\n"
"s_add_u32 m0, s97, 122880\n"
"s_nop 0\n"
"buffer_load_dwordx4 v7, s[36:39], s96 offen offset:0 lds\n"
"buffer_load_dwordx4 v7, s[40:43], s96 offen offset:1024 lds\n"
"s_add_u32 s96, s96, 65536\n"
"ds_read_b64_tr_b16 v[64:65], v8 offset:0\n"
"ds_read_b64_tr_b16 v[66:67], v8 offset:512\n"
"ds_read_b64_tr_b16 v[68:69], v8 offset:8\n"
"ds_read_b64_tr_b16 v[70:71], v8 offset:520\n"
"ds_read_b64_tr_b16 v[72:73], v8 offset:8192\n"
"ds_read_b64_tr_b16 v[74:75], v8 offset:8704\n"
"ds_read_b64_tr_b16 v[76:77], v8 offset:8200\n"
"ds_read_b64_tr_b16 v[78:79], v8 offset:8712\n"
"s_waitcnt lgkmcnt(0)\n"
"s_waitcnt vmcnt(24)\n"
"s_barrier\n"
"s_add_u32 m0, s97, 0\n"
"s_nop 0\n"
"buffer_load_dwordx4 v7, s[36:39], s96 offen offset:0 lds\n"
"buffer_load_dwordx4 v7, s[40:43], s96 offen offset:1024 lds\n"
"s_add_u32 s96, s96, 65536\n"
"s_add_u32 m0, s97, 8192\n"
"s_nop 0\n"
"buffer_load_dwordx4 v7, s[36:39], s96 offen offset:0 lds\n"
"buffer_load_dwordx4 v7, s[40:43], s96 offen offset:1024 lds\n"
"s_add_u32 s96, s96, 65536\n"
"ds_read_b64_tr_b16 v[80:81], v8 offset:16384\n"
"ds_read_b64_tr_b16 v[82:83], v8 offset:16896\n"
"ds_read_b64_tr_b16 v[84:85], v8 offset:16392\n"
"ds_read_b64_tr_b16 v[86:87], v8 offset:16904\n"
"ds_read_b64_tr_b16 v[88:89], v8 offset:24576\n"
"ds_read_b64_tr_b16 v[90:91], v8 offset:25088\n"
"ds_read_b64_tr_b16 v[92:93], v8 offset:24584\n"
"ds_read_b64_tr_b16 v[94:95], v8 offset:25096\n"
"s_waitcnt lgkmcnt(0)\n"
"s_waitcnt vmcnt(24)\n"
"s_barrier\n"
"s_add_u32 m0, s97, 16384\n"
"s_nop 0\n"
"buffer_load_dwordx4 v7, s[36:39], s96 offen offset:0 lds\n"
"buffer_load_dwordx4 v7, s[40:43], s96 offen offset:1024 lds\n"
"s_add_u32 s96, s96, 65536\n"
"s_add_u32 m0, s97, 24576\n"
"s_nop 0\n"
"buffer_load_dwordx4 v7, s[36:39], s96 offen offset:0 lds\n"
"buffer_load_dwordx4 v7, s[40:43], s96 offen offset:1024 lds\n"
"s_add_u32 s96, s96, 65536\n"
"ds_read_b64_tr_b16 v[96:97], v8 offset:32768\n"
"ds_read_b64_tr_b16 v[98:99], v8 offset:33280\n"
"ds_read_b64_tr_b16 v[100:101], v8 offset:32776\n"
"ds_read_b64_tr_b16 v[102:103], v8 offset:33288\n"
"ds_read_b64_tr_b16 v[104:105], v8 offset:40960\n"
"ds_read_b64_tr_b16 v[106:107], v8 offset:41472\n"
"ds_read_b64_tr_b16 v[108:109], v8 offset:40968\n"
"ds_read_b64_tr_b16 v[110:111], v8 offset:41480\n"
"s_waitcnt lgkmcnt(0)\n"
"s_waitcnt vmcnt(24)\n"
"s_barrier\n"
"s_add_u32 m0, s97, 32768\n"
"s_nop 0\n"
"buffer_load_dwordx4 v7, s[36:39], s96 offen offset:0 lds\n"
"buffer_load_dwordx4 v7, s[40:43], s96 offen offset:1024 lds\n"
"s_add_u32 s96, s96, 65536\n"
"s_add_u32 m0, s97, 40960\n"
"s_nop 0\n"
"buffer_load_dwordx4 v7, s[36:39], s96 offen offset:0 lds\n"
"buffer_load_dwordx4 v7, s[40:43], s96 offen offset:1024 lds\n"
"s_add_u32 s96, s96, 65536\n"
"ds_read_b64_tr_b16 v[112:113], v8 offset:49152\n"
"ds_read_b64_tr_b16 v[114:115], v8 offset:49664\n"
"ds_read_b64_tr_b16 v[116:117], v8 offset:49160\n"
"ds_read_b64_tr_b16 v[118:119], v8 offset:49672\n"
"ds_read_b64_tr_b16 v[120:121], v8 offset:57344\n"
"ds_read_b64_tr_b16 v[122:123], v8 offset:57856\n"
"ds_read_b64_tr_b16 v[124:125], v8 offset:57352\n"
"ds_read_b64_tr_b16 v[126:127], v8 offset:57864\n"
"s_waitcnt lgkmcnt(0)\n"
"s_waitcnt vmcnt(24)\n"
"s_barrier\n"
"s_add_u32 m0, s97, 49152\n"
"s_nop 0\n"
"buffer_load_dwordx4 v7, s[36:39], s96 offen offset:0 lds\n"
"buffer_load_dwordx4 v7, s[40:43], s96 offen offset:1024 lds\n"
"s_add_u32 s96, s96, 65536\n"
"s_add_u32 m0, s97, 57344\n"
"s_nop 0\n"
"buffer_load_dwordx4 v7, s[36:39], s96 offen offset:0 lds\n"
"buffer_load_dwordx4 v7, s[40:43], s96 offen offset:1024 lds\n"
"s_add_u32 s96, s96, 65536\n"
"ds_read_b64_tr_b16 v[128:129], v9 offset:0\n"
"ds_read_b64_tr_b16 v[130:131], v9 offset:512\n"
"ds_read_b64_tr_b16 v[132:133], v9 offset:8\n"
"ds_read_b64_tr_b16 v[134:135], v9 offset:520\n"
"ds_read_b64_tr_b16 v[136:137], v9 offset:8192\n"
"ds_read_b64_tr_b16 v[138:139], v9 offset:8704\n"
"ds_read_b64_tr_b16 v[140:141], v9 offset:8200\n"
"ds_read_b64_tr_b16 v[142:143], v9 offset:8712\n"
"s_waitcnt lgkmcnt(0)\n"
"s_waitcnt vmcnt(24)\n"
"s_barrier\n"
"ds_read_b64_tr_b16 v[144:145], v9 offset:16384\n"
"ds_read_b64_tr_b16 v[146:147], v9 offset:16896\n"
"ds_read_b64_tr_b16 v[148:149], v9 offset:16392\n"
"ds_read_b64_tr_b16 v[150:151], v9 offset:16904\n"
"ds_read_b64_tr_b16 v[152:153], v9 offset:24576\n"
"ds_read_b64_tr_b16 v[154:155], v9 offset:25088\n"
"ds_read_b64_tr_b16 v[156:157], v9 offset:24584\n"
"ds_read_b64_tr_b16 v[158:159], v9 offset:25096\n"
"s_waitcnt lgkmcnt(0)\n"
"s_waitcnt vmcnt(20)\n"
"s_barrier\n"
"ds_read_b64_tr_b16 v[160:161], v9 offset:32768\n"
"ds_read_b64_tr_b16 v[162:163], v9 offset:33280\n"
"ds_read_b64_tr_b16 v[164:165], v9 offset:32776\n"
"ds_read_b64_tr_b16 v[166:167], v9 offset:33288\n"
"ds_read_b64_tr_b16 v[168:169], v9 offset:40960\n"
"ds_read_b64_tr_b16 v[170:171], v9 offset:41472\n"
"ds_read_b64_tr_b16 v[172:173], v9 offset:40968\n"
"ds_read_b64_tr_b16 v[174:175], v9 offset:41480\n"
"s_waitcnt lgkmcnt(0)\n"
"s_waitcnt vmcnt(16)\n"
"s_barrier\n"
"ds_read_b64_tr_b16 v[176:177], v9 offset:49152\n"
"ds_read_b64_tr_b16 v[178:179], v9 offset:49664\n"
"ds_read_b64_tr_b16 v[180:181], v9 offset:49160\n"
"ds_read_b64_tr_b16 v[182:183], v9 offset:49672\n"
"ds_read_b64_tr_b16 v[184:185], v9 offset:57344\n"
"ds_read_b64_tr_b16 v[186:187], v9 offset:57856\n"
"ds_read_b64_tr_b16 v[188:189], v9 offset:57352\n"
"ds_read_b64_tr_b16 v[190:191], v9 offset:57864\n"
"s_waitcnt lgkmcnt(0)\n"
"s_waitcnt vmcnt(12)\n"
"s_barrier\n"
"ds_read_b64_tr_b16 v[192:193], v8 offset:0\n"
"ds_read_b64_tr_b16 v[194:195], v8 offset:512\n"
"ds_read_b64_tr_b16 v[196:197], v8 offset:8\n"
"ds_read_b64_tr_b16 v[198:199], v8 offset:520\n"
"ds_read_b64_tr_b16 v[200:201], v8 offset:8192\n"
"ds_read_b64_tr_b16 v[202:203], v8 offset:8704\n"
"ds_read_b64_tr_b16 v[204:205], v8 offset:8200\n"
"ds_read_b64_tr_b16 v[206:207], v8 offset:8712\n"
"s_waitcnt lgkmcnt(0)\n"
"s_waitcnt vmcnt(8)\n"
"s_barrier\n"
"ds_read_b64_tr_b16 v[208:209], v8 offset:16384\n"
"ds_read_b64_tr_b16 v[210:211], v8 offset:16896\n"
"ds_read_b64_tr_b16 v[212:213], v8 offset:16392\n"
"ds_read_b64_tr_b16 v[214:215], v8 offset:16904\n"
"ds_read_b64_tr_b16 v[216:217], v8 offset:24576\n"
"ds_read_b64_tr_b16 v[218:219], v8 offset:25088\n"
"ds_read_b64_tr_b16 v[220:221], v8 offset:24584\n"
"ds_read_b64_tr_b16 v[222:223], v8 offset:25096\n"
"s_waitcnt lgkmcnt(0)\n"
"s_waitcnt vmcnt(4)\n"
"s_barrier\n"
"ds_read_b64_tr_b16 v[224:225], v8 offset:32768\n"
"ds_read_b64_tr_b16 v[226:227], v8 offset:33280\n"
"ds_read_b64_tr_b16 v[228:229], v8 offset:32776\n"
"ds_read_b64_tr_b16 v[230:231], v8 offset:33288\n"
"ds_read_b64_tr_b16 v[232:233], v8 offset:40960\n"
"ds_read_b64_tr_b16 v[234:235], v8 offset:41472\n"
"ds_read_b64_tr_b16 v[236:237], v8 offset:40968\n"
"ds_read_b64_tr_b16 v[238:239], v8 offset:41480\n"
"s_waitcnt lgkmcnt(0)\n"
"s_waitcnt vmcnt(0)\n"
"s_barrier\n"
"ds_read_b64_tr_b16 v[240:241], v8 offset:49152\n"
"ds_read_b64_tr_b16 v[242:243], v8 offset:49664\n"
"ds_read_b64_tr_b16 v[244:245], v8 offset:49160\n"
"ds_read_b64_tr_b16 v[246:247], v8 offset:49672\n"
"ds_read_b64_tr_b16 v[248:249], v8 offset:57344\n"
"ds_read_b64_tr_b16 v[250:251], v8 offset:57856\n"
"ds_read_b64_tr_b16 v[252:253], v8 offset:57352\n"
"ds_read_b64_tr_b16 v[254:255], v8 offset:57864\n"
"s_waitcnt lgkmcnt(0)\n"
"s_barrier\n"
"v_mov_b32 v8, s54\n"
"v_mov_b32 v9, s55\n"
"s_lshl2_add_u32 s48, s29, s26\n"
"s_mul_i32 s63, s48, 14336\n"
"s_mul_i32 s49, s48, 2446696\n"
"s_add_u32 s48, s49, 2403005\n"
"s_lshr_b32 s49, s49, 17\n"
"s_lshr_b32 s48, s48, 17\n"
"s_cmp_gt_u32 s48, 143\n"
"s_cselect_b32 s48, 151, s48\n"
"s_sub_u32 s48, s48, s49\n"
"s_add_u32 s49, s49, 128\n"
"v_add_u32 v0, s49, v1\n"
"s_add_u32 s49, s50, 280\n"
"v_writelane_b32 v0, s49, 63\n"
"v_lshrrev_b32 v0, 2, v0\n"
"v_lshlrev_b32 v0, 7, v0\n"
"v_cmp_ge_u32 vcc, s48, v1\n"
"s_lshl_b32 s49, s29, 31\n"
"s_nop 4\n"
"s_or_b32 vcc_hi, vcc_hi, s49\n"
"s_nop 4\n"
"v_mov_b32 v3, 0x7fff0000\n"
"v_cndmask_b32 v0, v3, v0, vcc\n"
"v_cndmask_b32 v8, 0, v8, vcc\n"
"v_cndmask_b32 v9, 0, v9, vcc\n"
"v_mov_b32 v3, v21\n"
"v_mov_b32 v1, v20\n"
"s_mul_i32 s48, s28, 296960\n"
"s_add_u32 s63, s63, s48\n"
"s_add_u32 s63, s63, 4718592\n"
"s_add_u32 s66, s48, 4898816\n"
"s_mul_i32 s64, s26, 14336\n"
"s_lshl_b32 s74, s26, 11\n"
"s_add_u32 s74, s74, 114688\n"
"v_add_u32 v5, s74, v2\n"
"v_add_u32 v4, 57344, v2\n"
"s_mov_b32 s48, 20000\n"
"s_branch .L_rfdx\n"
".L_rldx:\n"
"s_sleep 8\n"
".L_rfdx:\n"
"buffer_load_dwordx2 v[6:7], v0, s[32:35], s66 offen sc1\n"
"s_sub_u32 s48, s48, 1\n"
"s_cbranch_scc1 .L_end\n"
"s_waitcnt vmcnt(0)\n"
"v_cmp_ne_u64 vcc, v[8:9], v[6:7]\n"
"s_cbranch_vccnz .L_rldx\n"
"s_add_u32 s66, s66, 1187840\n"
"s_add_u32 m0, s64, 0\n"
"s_nop 0\n"
"buffer_load_dwordx4 v2, s[32:35], s63 offen offset:0 sc1 lds\n"
"buffer_load_dwordx4 v2, s[32:35], s63 offen offset:1024 sc1 lds\n"
"buffer_load_dwordx4 v2, s[32:35], s63 offen offset:2048 sc1 lds\n"
"buffer_load_dwordx4 v2, s[32:35], s63 offen offset:3072 sc1 lds\n"
"s_add_u32 s63, s63, 4096\n"
"s_add_u32 m0, s64, 4096\n"
"s_nop 0\n"
"buffer_load_dwordx4 v2, s[32:35], s63 offen offset:0 sc1 lds\n"
"buffer_load_dwordx4 v2, s[32:35], s63 offen offset:1024 sc1 lds\n"
"buffer_load_dwordx4 v2, s[32:35], s63 offen offset:2048 sc1 lds\n"
"buffer_load_dwordx4 v2, s[32:35], s63 offen offset:3072 sc1 lds\n"
"s_add_u32 s63, s63, 4096\n"
"s_add_u32 m0, s64, 8192\n"
"s_nop 0\n"
"buffer_load_dwordx4 v2, s[32:35], s63 offen offset:0 sc1 lds\n"
"buffer_load_dwordx4 v2, s[32:35], s63 offen offset:1024 sc1 lds\n"
"buffer_load_dwordx4 v2, s[32:35], s63 offen offset:2048 sc1 lds\n"
"buffer_load_dwordx4 v2, s[32:35], s63 offen offset:3072 sc1 lds\n"
"s_add_u32 s63, s63, 4096\n"
"s_add_u32 m0, s64, 12288\n"
"s_nop 0\n"
"buffer_load_dwordx4 v2, s[32:35], s63 offen offset:0 sc1 lds\n"
"buffer_load_dwordx4 v2, s[32:35], s63 offen offset:1024 sc1 lds\n"
"s_add_u32 s63, s63, 1175552\n"
"s_mov_b32 s48, 20000\n"
"s_branch .L_rfdy\n"
".L_rldy:\n"
"s_sleep 8\n"
".L_rfdy:\n"
"buffer_load_dwordx2 v[6:7], v0, s[32:35], s66 offen sc1\n"
"s_sub_u32 s48, s48, 1\n"
"s_cbranch_scc1 .L_end\n"
"s_waitcnt vmcnt(0)\n"
"v_cmp_ne_u64 vcc, v[8:9], v[6:7]\n"
"s_cbranch_vccnz .L_rldy\n"
"s_add_u32 s66, s66, 1187840\n"
"s_cmp_lg_u32 s29, 0\n"
"s_cbranch_scc1 .L_dh\n"
"s_add_u32 s71, s70, 4681728\n"
"s_sub_u32 s68, s65, 2375680\n"
"s_mov_b32 s62, 31\n"
"s_add_u32 m0, s74, 0\n"
"s_nop 0\n"
"buffer_load_dwordx4 v2, s[32:35], s71 offen offset:0 sc1 lds\n"
"s_add_u32 s71, s71, 1187840\n"
"s_add_u32 m0, s74, 8192\n"
"s_nop 0\n"
"buffer_load_dwordx4 v2, s[32:35], s71 offen offset:0 sc1 lds\n"
"s_add_u32 s71, s71, 1187840\n"
"s_add_u32 m0, s64, 57344\n"
"s_nop 0\n"
"buffer_load_dwordx4 v2, s[32:35], s63 offen offset:0 sc1 lds\n"
"buffer_load_dwordx4 v2, s[32:35], s63 offen offset:1024 sc1 lds\n"
"buffer_load_dwordx4 v2, s[32:35], s63 offen offset:2048 sc1 lds\n"
"buffer_load_dwordx4 v2, s[32:35], s63 offen offset:3072 sc1 lds\n"
"s_add_u32 s63, s63, 4096\n"
"s_add_u32 m0, s64, 61440\n"
"s_nop 0\n"
"buffer_load_dwordx4 v2, s[32:35], s63 offen offset:0 sc1 lds\n"
"s_waitcnt vmcnt(0)\n"
"s_barrier\n"
"ds_read_b128 v[12:15], v5 offset:0\n"
"s_waitcnt lgkmcnt(0)\n"
"v_lshlrev_b32 v16, 16, v12\n"
"v_and_b32 v17, 0xffff0000, v12\n"
"v_lshlrev_b32 v18, 16, v13\n"
"v_and_b32 v19, 0xffff0000, v13\n"
"v_lshlrev_b32 v20, 16, v14\n"
"v_and_b32 v21, 0xffff0000, v14\n"
"v_lshlrev_b32 v22, 16, v15\n"
"v_and_b32 v23, 0xffff0000, v15\n"
"ds_read_b128 v[32:35], v2 offset:0\n"
"ds_read_b128 v[36:39], v2 offset:14336\n"
"ds_read_b128 v[40:43], v2 offset:28672\n"
"ds_read_b128 v[44:47], v2 offset:43008\n"
"ds_read_b128 v[48:51], v2 offset:1024\n"
"ds_read_b128 v[52:55], v2 offset:15360\n"
".p2align 3\n"
".L_dll:\n"
"s_waitcnt lgkmcnt(4)\n"
"v_mfma_f32_16x16x32_bf16 v[16:19], a[0:3], v[32:35], v[16:19]\n"
"ds_read_b128 v[56:59], v2 offset:29696\n"
"v_mfma_f32_16x16x32_bf16 v[20:23], a[4:7], v[32:35], v[20:23]\n"
"buffer_load_dwordx4 v2, s[32:35], s63 offen offset:1024 sc1 lds\n"
"v_mfma_f32_16x16x32_bf16 v[20:23], a[116:119], v[36:39], v[20:23]\n"
"ds_read_b128 v[60:63], v2 offset:44032\n"
"v_mfma_f32_16x16x32_bf16 v[16:19], a[112:115], v[36:39], v[16:19]\n"
"s_waitcnt lgkmcnt(4)\n"
"v_mfma_f32_16x16x32_bf16 v[16:19], a[224:227], v[40:43], v[16:19]\n"
"ds_read_b128 v[32:35], v2 offset:2048\n"
"s_add_u32 s68, s68, 1187840\n"
"v_mfma_f32_16x16x32_bf16 v[20:23], a[228:231], v[40:43], v[20:23]\n"
"buffer_store_dwordx4 v[28:31], v2, s[32:35], s68 offen offset:1024 sc1\n"
"buffer_store_dwordx4 v[24:27], v2, s[32:35], s68 offen sc1\n"
"v_mfma_f32_16x16x32_bf16 v[20:23], v[148:151], v[44:47], v[20:23]\n"
"ds_read_b128 v[36:39], v2 offset:16384\n"
"s_mov_b32 s67, s68\n"
"v_mfma_f32_16x16x32_bf16 v[16:19], v[144:147], v[44:47], v[16:19]\n"
"buffer_load_dwordx4 v2, s[32:35], s63 offen offset:2048 sc1 lds\n"
"s_waitcnt lgkmcnt(4)\n"
"v_mfma_f32_16x16x32_bf16 v[16:19], a[8:11], v[48:51], v[16:19]\n"
"ds_read_b128 v[40:43], v2 offset:30720\n"
"v_mfma_f32_16x16x32_bf16 v[20:23], a[12:15], v[48:51], v[20:23]\n"
"v_mfma_f32_16x16x32_bf16 v[20:23], a[124:127], v[52:55], v[20:23]\n"
"ds_read_b128 v[44:47], v2 offset:45056\n"
"buffer_load_dwordx2 v[6:7], v0, s[32:35], s66 offen sc1\n"
"v_mfma_f32_16x16x32_bf16 v[16:19], a[120:123], v[52:55], v[16:19]\n"
"s_waitcnt lgkmcnt(4)\n"
"v_mfma_f32_16x16x32_bf16 v[16:19], a[232:235], v[56:59], v[16:19]\n"
"ds_read_b128 v[48:51], v2 offset:3072\n"
"v_mfma_f32_16x16x32_bf16 v[20:23], a[236:239], v[56:59], v[20:23]\n"
"buffer_load_dwordx4 v2, s[32:35], s63 offen offset:3072 sc1 lds\n"
"v_mfma_f32_16x16x32_bf16 v[20:23], v[156:159], v[60:63], v[20:23]\n"
"ds_read_b128 v[52:55], v2 offset:17408\n"
"v_mfma_f32_16x16x32_bf16 v[16:19], v[152:155], v[60:63], v[16:19]\n"
"s_waitcnt lgkmcnt(4)\n"
"v_mfma_f32_16x16x32_bf16 v[16:19], a[16:19], v[32:35], v[16:19]\n"
"ds_read_b128 v[56:59], v2 offset:31744\n"
"v_mfma_f32_16x16x32_bf16 v[20:23], a[20:23], v[32:35], v[20:23]\n"
"v_mfma_f32_16x16x32_bf16 v[20:23], a[132:135], v[36:39], v[20:23]\n"
"ds_read_b128 v[60:63], v2 offset:46080\n"
"v_mfma_f32_16x16x32_bf16 v[16:19], a[128:131], v[36:39], v[16:19]\n"
"s_add_u32 s63, s63, 4096\n"
"s_waitcnt lgkmcnt(4)\n"
"v_mfma_f32_16x16x32_bf16 v[16:19], a[240:243], v[40:43], v[16:19]\n"
"ds_read_b128 v[32:35], v2 offset:4096\n"
"s_add_u32 m0, s64, 65536\n"
"v_mfma_f32_16x16x32_bf16 v[20:23], a[244:247], v[40:43], v[20:23]\n"
"buffer_load_dwordx4 v2, s[32:35], s63 offen offset:0 sc1 lds\n"
"v_mfma_f32_16x16x32_bf16 v[20:23], v[164:167], v[44:47], v[20:23]\n"
"ds_read_b128 v[36:39], v2 offset:18432\n"
"v_mfma_f32_16x16x32_bf16 v[16:19], v[160:163], v[44:47], v[16:19]\n"
"s_waitcnt lgkmcnt(4)\n"
"v_mfma_f32_16x16x32_bf16 v[16:19], a[24:27], v[48:51], v[16:19]\n"
"ds_read_b128 v[40:43], v2 offset:32768\n"
"v_mfma_f32_16x16x32_bf16 v[20:23], a[28:31], v[48:51], v[20:23]\n"
"buffer_load_dwordx4 v2, s[32:35], s63 offen offset:1024 sc1 lds\n"
"v_mfma_f32_16x16x32_bf16 v[20:23], a[140:143], v[52:55], v[20:23]\n"
"ds_read_b128 v[44:47], v2 offset:47104\n"
"v_mfma_f32_16x16x32_bf16 v[16:19], a[136:139], v[52:55], v[16:19]\n"
"s_waitcnt lgkmcnt(4)\n"
"v_mfma_f32_16x16x32_bf16 v[16:19], a[248:251], v[56:59], v[16:19]\n"
"ds_read_b128 v[48:51], v2 offset:5120\n"
"v_mfma_f32_16x16x32_bf16 v[20:23], a[252:255], v[56:59], v[20:23]\n"
"v_mfma_f32_16x16x32_bf16 v[20:23], v[172:175], v[60:63], v[20:23]\n"
"ds_read_b128 v[52:55], v2 offset:19456\n"
"v_mfma_f32_16x16x32_bf16 v[16:19], v[168:171], v[60:63], v[16:19]\n"
"buffer_load_dwordx4 v2, s[32:35], s63 offen offset:2048 sc1 lds\n"
"s_waitcnt lgkmcnt(4)\n"
"v_mfma_f32_16x16x32_bf16 v[16:19], a[32:35], v[32:35], v[16:19]\n"
"ds_read_b128 v[56:59], v2 offset:33792\n"
"v_mfma_f32_16x16x32_bf16 v[20:23], a[36:39], v[32:35], v[20:23]\n"
"v_mfma_f32_16x16x32_bf16 v[20:23], a[148:151], v[36:39], v[20:23]\n"
"ds_read_b128 v[60:63], v2 offset:48128\n"
"v_mfma_f32_16x16x32_bf16 v[16:19], a[144:147], v[36:39], v[16:19]\n"
"s_waitcnt lgkmcnt(4)\n"
"v_mfma_f32_16x16x32_bf16 v[16:19], v[64:67], v[40:43], v[16:19]\n"
"ds_read_b128 v[32:35], v2 offset:6144\n"
"v_mfma_f32_16x16x32_bf16 v[20:23], v[68:71], v[40:43], v[20:23]\n"
"buffer_load_dwordx4 v2, s[32:35], s63 offen offset:3072 sc1 lds\n"
"v_mfma_f32_16x16x32_bf16 v[20:23], v[180:183], v[44:47], v[20:23]\n"
"ds_read_b128 v[36:39], v2 offset:20480\n"
"v_mfma_f32_16x16x32_bf16 v[16:19], v[176:179], v[44:47], v[16:19]\n"
"s_waitcnt lgkmcnt(4)\n"
"v_mfma_f32_16x16x32_bf16 v[16:19], a[40:43], v[48:51], v[16:19]\n"
"ds_read_b128 v[40:43], v2 offset:34816\n"
"v_mfma_f32_16x16x32_bf16 v[20:23], a[44:47], v[48:51], v[20:23]\n"
"v_mfma_f32_16x16x32_bf16 v[20:23], a[156:159], v[52:55], v[20:23]\n"
"ds_read_b128 v[44:47], v2 offset:49152\n"
"v_mfma_f32_16x16x32_bf16 v[16:19], a[152:155], v[52:55], v[16:19]\n"
"s_waitcnt vmcnt(17)\n"
"s_waitcnt lgkmcnt(4)\n"
"v_mfma_f32_16x16x32_bf16 v[16:19], v[72:75], v[56:59], v[16:19]\n"
"s_barrier\n"
"ds_read_b128 v[48:51], v2 offset:7168\n"
"v_mfma_f32_16x16x32_bf16 v[20:23], v[76:79], v[56:59], v[20:23]\n"
"v_mfma_f32_16x16x32_bf16 v[20:23], v[188:191], v[60:63], v[20:23]\n"
"ds_read_b128 v[52:55], v2 offset:21504\n"
"s_add_u32 s63, s63, 4096\n"
"v_mfma_f32_16x16x32_bf16 v[16:19], v[184:187], v[60:63], v[16:19]\n"
"s_add_u32 m0, s64, 69632\n"
"s_waitcnt lgkmcnt(4)\n"
"v_mfma_f32_16x16x32_bf16 v[16:19], a[48:51], v[32:35], v[16:19]\n"
"ds_read_b128 v[56:59], v2 offset:35840\n"
"buffer_load_dwordx4 v2, s[32:35], s63 offen offset:0 sc1 lds\n"
"v_mfma_f32_16x16x32_bf16 v[20:23], a[52:55], v[32:35], v[20:23]\n"
"v_mfma_f32_16x16x32_bf16 v[20:23], a[164:167], v[36:39], v[20:23]\n"
"ds_read_b128 v[60:63], v2 offset:50176\n"
"v_mfma_f32_16x16x32_bf16 v[16:19], a[160:163], v[36:39], v[16:19]\n"
"buffer_load_dwordx4 v2, s[32:35], s63 offen offset:1024 sc1 lds\n"
"s_waitcnt lgkmcnt(4)\n"
"v_mfma_f32_16x16x32_bf16 v[16:19], v[80:83], v[40:43], v[16:19]\n"
"ds_read_b128 v[32:35], v2 offset:8192\n"
"s_add_u32 s63, s63, 1175552\n"
"v_mfma_f32_16x16x32_bf16 v[20:23], v[84:87], v[40:43], v[20:23]\n"
"v_mfma_f32_16x16x32_bf16 v[20:23], v[196:199], v[44:47], v[20:23]\n"
"ds_read_b128 v[36:39], v2 offset:22528\n"
"s_waitcnt vmcnt(7)\n"
"v_mfma_f32_16x16x32_bf16 v[16:19], v[192:195], v[44:47], v[16:19]\n"
"v_cmp_ne_u64 vcc, v[8:9], v[6:7]\n"
"s_waitcnt lgkmcnt(4)\n"
"v_mfma_f32_16x16x32_bf16 v[16:19], a[56:59], v[48:51], v[16:19]\n"
"ds_read_b128 v[40:43], v2 offset:36864\n"
"s_cbranch_vccnz .L_rtl0\n"
".L_bkl0:\n"
"v_mfma_f32_16x16x32_bf16 v[20:23], a[60:63], v[48:51], v[20:23]\n"
"s_add_u32 s66, s66, 1187840\n"
"v_mfma_f32_16x16x32_bf16 v[20:23], a[172:175], v[52:55], v[20:23]\n"
"ds_read_b128 v[44:47], v2 offset:51200\n"
"s_add_u32 m0, s74, 0\n"
"v_mfma_f32_16x16x32_bf16 v[16:19], a[168:171], v[52:55], v[16:19]\n"
"buffer_load_dwordx4 v2, s[32:35], s71 offen offset:0 sc1 lds\n"
"s_waitcnt lgkmcnt(4)\n"
"v_mfma_f32_16x16x32_bf16 v[16:19], v[88:91], v[56:59], v[16:19]\n"
"ds_read_b128 v[48:51], v2 offset:9216\n"
"s_add_u32 s71, s71, 1187840\n"
"v_mfma_f32_16x16x32_bf16 v[20:23], v[92:95], v[56:59], v[20:23]\n"
"v_mfma_f32_16x16x32_bf16 v[20:23], v[204:207], v[60:63], v[20:23]\n"
"ds_read_b128 v[52:55], v2 offset:23552\n"
"v_mfma_f32_16x16x32_bf16 v[16:19], v[200:203], v[60:63], v[16:19]\n"
"s_waitcnt lgkmcnt(4)\n"
"v_mfma_f32_16x16x32_bf16 v[16:19], a[64:67], v[32:35], v[16:19]\n"
"ds_read_b128 v[56:59], v2 offset:37888\n"
"v_mfma_f32_16x16x32_bf16 v[20:23], a[68:71], v[32:35], v[20:23]\n"
"v_mfma_f32_16x16x32_bf16 v[20:23], a[180:183], v[36:39], v[20:23]\n"
"ds_read_b128 v[60:63], v2 offset:52224\n"
"s_add_u32 m0, s64, 0\n"
"v_mfma_f32_16x16x32_bf16 v[16:19], a[176:179], v[36:39], v[16:19]\n"
"buffer_load_dwordx4 v2, s[32:35], s63 offen offset:0 sc1 lds\n"
"s_waitcnt lgkmcnt(4)\n"
"v_mfma_f32_16x16x32_bf16 v[16:19], v[96:99], v[40:43], v[16:19]\n"
"ds_read_b128 v[32:35], v2 offset:10240\n"
"v_mfma_f32_16x16x32_bf16 v[20:23], v[100:103], v[40:43], v[20:23]\n"
"v_mfma_f32_16x16x32_bf16 v[20:23], v[212:215], v[44:47], v[20:23]\n"
"ds_read_b128 v[36:39], v2 offset:24576\n"
"v_mfma_f32_16x16x32_bf16 v[16:19], v[208:211], v[44:47], v[16:19]\n"
"s_waitcnt lgkmcnt(4)\n"
"v_mfma_f32_16x16x32_bf16 v[16:19], a[72:75], v[48:51], v[16:19]\n"
"ds_read_b128 v[40:43], v2 offset:38912\n"
"buffer_load_dwordx4 v2, s[32:35], s63 offen offset:1024 sc1 lds\n"
"v_mfma_f32_16x16x32_bf16 v[20:23], a[76:79], v[48:51], v[20:23]\n"
"ds_read_b128 v[12:15], v5 offset:8192\n"
"v_mfma_f32_16x16x32_bf16 v[20:23], a[188:191], v[52:55], v[20:23]\n"
"ds_read_b128 v[44:47], v2 offset:53248\n"
"v_mfma_f32_16x16x32_bf16 v[16:19], a[184:187], v[52:55], v[16:19]\n"
"s_waitcnt lgkmcnt(5)\n"
"v_mfma_f32_16x16x32_bf16 v[16:19], v[104:107], v[56:59], v[16:19]\n"
"ds_read_b128 v[48:51], v2 offset:11264\n"
"v_mfma_f32_16x16x32_bf16 v[20:23], v[108:111], v[56:59], v[20:23]\n"
"v_mfma_f32_16x16x32_bf16 v[20:23], v[220:223], v[60:63], v[20:23]\n"
"ds_read_b128 v[52:55], v2 offset:25600\n"
"buffer_load_dwordx4 v2, s[32:35], s63 offen offset:2048 sc1 lds\n"
"v_mfma_f32_16x16x32_bf16 v[16:19], v[216:219], v[60:63], v[16:19]\n"
"s_waitcnt lgkmcnt(3)\n"
"v_mfma_f32_16x16x32_bf16 v[16:19], a[80:83], v[32:35], v[16:19]\n"
"ds_read_b128 v[56:59], v2 offset:39936\n"
"v_lshlrev_b32 v24, 16, v12\n"
"v_mfma_f32_16x16x32_bf16 v[20:23], a[84:87], v[32:35], v[20:23]\n"
"v_and_b32 v25, 0xffff0000, v12\n"
"v_lshlrev_b32 v26, 16, v13\n"
"v_mfma_f32_16x16x32_bf16 v[20:23], a[196:199], v[36:39], v[20:23]\n"
"ds_read_b128 v[60:63], v2 offset:54272\n"
"v_and_b32 v27, 0xffff0000, v13\n"
"v_mfma_f32_16x16x32_bf16 v[16:19], a[192:195], v[36:39], v[16:19]\n"
"v_lshlrev_b32 v28, 16, v14\n"
"s_waitcnt lgkmcnt(4)\n"
"v_mfma_f32_16x16x32_bf16 v[16:19], v[112:115], v[40:43], v[16:19]\n"
"ds_read_b128 v[32:35], v2 offset:12288\n"
"buffer_load_dwordx4 v2, s[32:35], s63 offen offset:3072 sc1 lds\n"
"v_mfma_f32_16x16x32_bf16 v[20:23], v[116:119], v[40:43], v[20:23]\n"
"v_and_b32 v29, 0xffff0000, v14\n"
"v_lshlrev_b32 v30, 16, v15\n"
"v_mfma_f32_16x16x32_bf16 v[20:23], v[228:231], v[44:47], v[20:23]\n"
"ds_read_b128 v[36:39], v2 offset:26624\n"
"v_and_b32 v31, 0xffff0000, v15\n"
"v_mfma_f32_16x16x32_bf16 v[16:19], v[224:227], v[44:47], v[16:19]\n"
"s_waitcnt lgkmcnt(4)\n"
"v_mfma_f32_16x16x32_bf16 v[16:19], a[88:91], v[48:51], v[16:19]\n"
"ds_read_b128 v[40:43], v2 offset:40960\n"
"v_mfma_f32_16x16x32_bf16 v[20:23], a[92:95], v[48:51], v[20:23]\n"
"v_mfma_f32_16x16x32_bf16 v[20:23], a[204:207], v[52:55], v[20:23]\n"
"ds_read_b128 v[44:47], v2 offset:55296\n"
"s_add_u32 s63, s63, 4096\n"
"v_mfma_f32_16x16x32_bf16 v[16:19], a[200:203], v[52:55], v[16:19]\n"
"s_add_u32 m0, s64, 4096\n"
"s_waitcnt lgkmcnt(4)\n"
"v_mfma_f32_16x16x32_bf16 v[16:19], v[120:123], v[56:59], v[16:19]\n"
"ds_read_b128 v[48:51], v2 offset:13312\n"
"buffer_load_dwordx4 v2, s[32:35], s63 offen offset:0 sc1 lds\n"
"v_mfma_f32_16x16x32_bf16 v[20:23], v[124:127], v[56:59], v[20:23]\n"
"v_mfma_f32_16x16x32_bf16 v[20:23], v[236:239], v[60:63], v[20:23]\n"
"ds_read_b128 v[52:55], v2 offset:27648\n"
"v_mfma_f32_16x16x32_bf16 v[16:19], v[232:235], v[60:63], v[16:19]\n"
"s_waitcnt lgkmcnt(4)\n"
"v_mfma_f32_16x16x32_bf16 v[16:19], a[96:99], v[32:35], v[16:19]\n"
"ds_read_b128 v[56:59], v2 offset:41984\n"
"v_mfma_f32_16x16x32_bf16 v[20:23], a[100:103], v[32:35], v[20:23]\n"
"v_mfma_f32_16x16x32_bf16 v[20:23], a[212:215], v[36:39], v[20:23]\n"
"ds_read_b128 v[60:63], v2 offset:56320\n"
"v_mfma_f32_16x16x32_bf16 v[16:19], a[208:211], v[36:39], v[16:19]\n"
"s_waitcnt lgkmcnt(4)\n"
"v_mfma_f32_16x16x32_bf16 v[16:19], v[128:131], v[40:43], v[16:19]\n"
"s_barrier\n"
"ds_read_b128 v[32:35], v4 offset:0\n"
"v_mfma_f32_16x16x32_bf16 v[20:23], v[132:135], v[40:43], v[20:23]\n"
"buffer_store_dwordx2 v[8:9], v2, s[92:95], s67 idxen sc1\n"
"v_mfma_f32_16x16x32_bf16 v[20:23], v[244:247], v[44:47], v[20:23]\n"
"ds_read_b128 v[36:39], v4 offset:14336\n"
"v_mfma_f32_16x16x32_bf16 v[16:19], v[240:243], v[44:47], v[16:19]\n"
"s_waitcnt lgkmcnt(4)\n"
"v_mfma_f32_16x16x32_bf16 v[16:19], a[104:107], v[48:51], v[16:19]\n"
"ds_read_b128 v[40:43], v4 offset:28672\n"
"v_mfma_f32_16x16x32_bf16 v[20:23], a[108:111], v[48:51], v[20:23]\n"
"v_mfma_f32_16x16x32_bf16 v[20:23], a[220:223], v[52:55], v[20:23]\n"
"ds_read_b128 v[44:47], v4 offset:43008\n"
"v_mfma_f32_16x16x32_bf16 v[16:19], a[216:219], v[52:55], v[16:19]\n"
"s_waitcnt lgkmcnt(4)\n"
"v_mfma_f32_16x16x32_bf16 v[16:19], v[136:139], v[56:59], v[16:19]\n"
"ds_read_b128 v[48:51], v4 offset:1024\n"
"v_mfma_f32_16x16x32_bf16 v[20:23], v[140:143], v[56:59], v[20:23]\n"
"v_mfma_f32_16x16x32_bf16 v[20:23], v[252:255], v[60:63], v[20:23]\n"
"ds_read_b128 v[52:55], v4 offset:15360\n"
"v_mfma_f32_16x16x32_bf16 v[16:19], v[248:251], v[60:63], v[16:19]\n"
"s_waitcnt lgkmcnt(4)\n"
"v_mfma_f32_16x16x32_bf16 v[24:27], a[0:3], v[32:35], v[24:27]\n"
"ds_read_b128 v[56:59], v4 offset:29696\n"
"v_mfma_f32_16x16x32_bf16 v[28:31], a[4:7], v[32:35], v[28:31]\n"
"buffer_load_dwordx4 v2, s[32:35], s63 offen offset:1024 sc1 lds\n"
"v_mfma_f32_16x16x32_bf16 v[28:31], a[116:119], v[36:39], v[28:31]\n"
"ds_read_b128 v[60:63], v4 offset:44032\n"
"v_mfma_f32_16x16x32_bf16 v[24:27], a[112:115], v[36:39], v[24:27]\n"
"s_waitcnt lgkmcnt(4)\n"
"v_mfma_f32_16x16x32_bf16 v[24:27], a[224:227], v[40:43], v[24:27]\n"
"ds_read_b128 v[32:35], v4 offset:2048\n"
"s_add_u32 s68, s68, 1187840\n"
"v_mfma_f32_16x16x32_bf16 v[28:31], a[228:231], v[40:43], v[28:31]\n"
"buffer_store_dwordx4 v[20:23], v2, s[32:35], s68 offen offset:1024 sc1\n"
"buffer_store_dwordx4 v[16:19], v2, s[32:35], s68 offen sc1\n"
"v_mfma_f32_16x16x32_bf16 v[28:31], v[148:151], v[44:47], v[28:31]\n"
"ds_read_b128 v[36:39], v4 offset:16384\n"
"s_mov_b32 s67, s68\n"
"v_mfma_f32_16x16x32_bf16 v[24:27], v[144:147], v[44:47], v[24:27]\n"
"buffer_load_dwordx4 v2, s[32:35], s63 offen offset:2048 sc1 lds\n"
"s_waitcnt lgkmcnt(4)\n"
"v_mfma_f32_16x16x32_bf16 v[24:27], a[8:11], v[48:51], v[24:27]\n"
"ds_read_b128 v[40:43], v4 offset:30720\n"
"v_mfma_f32_16x16x32_bf16 v[28:31], a[12:15], v[48:51], v[28:31]\n"
"v_mfma_f32_16x16x32_bf16 v[28:31], a[124:127], v[52:55], v[28:31]\n"
"ds_read_b128 v[44:47], v4 offset:45056\n"
"buffer_load_dwordx2 v[6:7], v0, s[32:35], s66 offen sc1\n"
"v_mfma_f32_16x16x32_bf16 v[24:27], a[120:123], v[52:55], v[24:27]\n"
"s_waitcnt lgkmcnt(4)\n"
"v_mfma_f32_16x16x32_bf16 v[24:27], a[232:235], v[56:59], v[24:27]\n"
"ds_read_b128 v[48:51], v4 offset:3072\n"
"v_mfma_f32_16x16x32_bf16 v[28:31], a[236:239], v[56:59], v[28:31]\n"
"buffer_load_dwordx4 v2, s[32:35], s63 offen offset:3072 sc1 lds\n"
"v_mfma_f32_16x16x32_bf16 v[28:31], v[156:159], v[60:63], v[28:31]\n"
"ds_read_b128 v[52:55], v4 offset:17408\n"
"v_mfma_f32_16x16x32_bf16 v[24:27], v[152:155], v[60:63], v[24:27]\n"
"s_waitcnt lgkmcnt(4)\n"
"v_mfma_f32_16x16x32_bf16 v[24:27], a[16:19], v[32:35], v[24:27]\n"
"ds_read_b128 v[56:59], v4 offset:31744\n"
"v_mfma_f32_16x16x32_bf16 v[28:31], a[20:23], v[32:35], v[28:31]\n"
"v_mfma_f32_16x16x32_bf16 v[28:31], a[132:135], v[36:39], v[28:31]\n"
"ds_read_b128 v[60:63], v4 offset:46080\n"
"v_mfma_f32_16x16x32_bf16 v[24:27], a[128:131], v[36:39], v[24:27]\n"
"s_add_u32 s63, s63, 4096\n"
"s_waitcnt lgkmcnt(4)\n"
"v_mfma_f32_16x16x32_bf16 v[24:27], a[240:243], v[40:43], v[24:27]\n"
"ds_read_b128 v[32:35], v4 offset:4096\n"
"s_add_u32 m0, s64, 8192\n"
"v_mfma_f32_16x16x32_bf16 v[28:31], a[244:247], v[40:43], v[28:31]\n"
"buffer_load_dwordx4 v2, s[32:35], s63 offen offset:0 sc1 lds\n"
"v_mfma_f32_16x16x32_bf16 v[28:31], v[164:167], v[44:47], v[28:31]\n"
"ds_read_b128 v[36:39], v4 offset:18432\n"
"v_mfma_f32_16x16x32_bf16 v[24:27], v[160:163], v[44:47], v[24:27]\n"
"s_waitcnt lgkmcnt(4)\n"
"v_mfma_f32_16x16x32_bf16 v[24:27], a[24:27], v[48:51], v[24:27]\n"
"ds_read_b128 v[40:43], v4 offset:32768\n"
"v_mfma_f32_16x16x32_bf16 v[28:31], a[28:31], v[48:51], v[28:31]\n"
"buffer_load_dwordx4 v2, s[32:35], s63 offen offset:1024 sc1 lds\n"
"v_mfma_f32_16x16x32_bf16 v[28:31], a[140:143], v[52:55], v[28:31]\n"
"ds_read_b128 v[44:47], v4 offset:47104\n"
"v_mfma_f32_16x16x32_bf16 v[24:27], a[136:139], v[52:55], v[24:27]\n"
"s_waitcnt lgkmcnt(4)\n"
"v_mfma_f32_16x16x32_bf16 v[24:27], a[248:251], v[56:59], v[24:27]\n"
"ds_read_b128 v[48:51], v4 offset:5120\n"
"v_mfma_f32_16x16x32_bf16 v[28:31], a[252:255], v[56:59], v[28:31]\n"
"v_mfma_f32_16x16x32_bf16 v[28:31], v[172:175], v[60:63], v[28:31]\n"
"ds_read_b128 v[52:55], v4 offset:19456\n"
"v_mfma_f32_16x16x32_bf16 v[24:27], v[168:171], v[60:63], v[24:27]\n"
"buffer_load_dwordx4 v2, s[32:35], s63 offen offset:2048 sc1 lds\n"
"s_waitcnt lgkmcnt(4)\n"
"v_mfma_f32_16x16x32_bf16 v[24:27], a[32:35], v[32:35], v[24:27]\n"
"ds_read_b128 v[56:59], v4 offset:33792\n"
"v_mfma_f32_16x16x32_bf16 v[28:31], a[36:39], v[32:35], v[28:31]\n"
"v_mfma_f32_16x16x32_bf16 v[28:31], a[148:151], v[36:39], v[28:31]\n"
"ds_read_b128 v[60:63], v4 offset:48128\n"
"v_mfma_f32_16x16x32_bf16 v[24:27], a[144:147], v[36:39], v[24:27]\n"
"s_waitcnt lgkmcnt(4)\n"
"v_mfma_f32_16x16x32_bf16 v[24:27], v[64:67], v[40:43], v[24:27]\n"
"ds_read_b128 v[32:35], v4 offset:6144\n"
"v_mfma_f32_16x16x32_bf16 v[28:31], v[68:71], v[40:43], v[28:31]\n"
"buffer_load_dwordx4 v2, s[32:35], s63 offen offset:3072 sc1 lds\n"
"v_mfma_f32_16x16x32_bf16 v[28:31], v[180:183], v[44:47], v[28:31]\n"
"ds_read_b128 v[36:39], v4 offset:20480\n"
"v_mfma_f32_16x16x32_bf16 v[24:27], v[176:179], v[44:47], v[24:27]\n"
"s_waitcnt lgkmcnt(4)\n"
"v_mfma_f32_16x16x32_bf16 v[24:27], a[40:43], v[48:51], v[24:27]\n"
"ds_read_b128 v[40:43], v4 offset:34816\n"
"v_mfma_f32_16x16x32_bf16 v[28:31], a[44:47], v[48:51], v[28:31]\n"
"v_mfma_f32_16x16x32_bf16 v[28:31], a[156:159], v[52:55], v[28:31]\n"
"ds_read_b128 v[44:47], v4 offset:49152\n"
"v_mfma_f32_16x16x32_bf16 v[24:27], a[152:155], v[52:55], v[24:27]\n"
"s_waitcnt vmcnt(17)\n"
"s_waitcnt lgkmcnt(4)\n"
"v_mfma_f32_16x16x32_bf16 v[24:27], v[72:75], v[56:59], v[24:27]\n"
"s_barrier\n"
"ds_read_b128 v[48:51], v4 offset:7168\n"
"v_mfma_f32_16x16x32_bf16 v[28:31], v[76:79], v[56:59], v[28:31]\n"
"v_mfma_f32_16x16x32_bf16 v[28:31], v[188:191], v[60:63], v[28:31]\n"
"ds_read_b128 v[52:55], v4 offset:21504\n"
"s_add_u32 s63, s63, 4096\n"
"v_mfma_f32_16x16x32_bf16 v[24:27], v[184:187], v[60:63], v[24:27]\n"
"s_add_u32 m0, s64, 12288\n"
"s_waitcnt lgkmcnt(4)\n"
"v_mfma_f32_16x16x32_bf16 v[24:27], a[48:51], v[32:35], v[24:27]\n"
"ds_read_b128 v[56:59], v4 offset:35840\n"
"buffer_load_dwordx4 v2, s[32:35], s63 offen offset:0 sc1 lds\n"
"v_mfma_f32_16x16x32_bf16 v[28:31], a[52:55], v[32:35], v[28:31]\n"
"v_mfma_f32_16x16x32_bf16 v[28:31], a[164:167], v[36:39], v[28:31]\n"
"ds_read_b128 v[60:63], v4 offset:50176\n"
"v_mfma_f32_16x16x32_bf16 v[24:27], a[160:163], v[36:39], v[24:27]\n"
"buffer_load_dwordx4 v2, s[32:35], s63 offen offset:1024 sc1 lds\n"
"s_waitcnt lgkmcnt(4)\n"
"v_mfma_f32_16x16x32_bf16 v[24:27], v[80:83], v[40:43], v[24:27]\n"
"ds_read_b128 v[32:35], v4 offset:8192\n"
"s_add_u32 s63, s63, 1175552\n"
"v_mfma_f32_16x16x32_bf16 v[28:31], v[84:87], v[40:43], v[28:31]\n"
"v_mfma_f32_16x16x32_bf16 v[28:31], v[196:199], v[44:47], v[28:31]\n"
"ds_read_b128 v[36:39], v4 offset:22528\n"
"s_waitcnt vmcnt(7)\n"
"v_mfma_f32_16x16x32_bf16 v[24:27], v[192:195], v[44:47], v[24:27]\n"
"v_cmp_ne_u64 vcc, v[8:9], v[6:7]\n"
"s_waitcnt lgkmcnt(4)\n"
"v_mfma_f32_16x16x32_bf16 v[24:27], a[56:59], v[48:51], v[24:27]\n"
"ds_read_b128 v[40:43], v4 offset:36864\n"
"s_cbranch_vccnz .L_rtl1\n"
".L_bkl1:\n"
"v_mfma_f32_16x16x32_bf16 v[28:31], a[60:63], v[48:51], v[28:31]\n"
"s_add_u32 s66, s66, 1187840\n"
"v_mfma_f32_16x16x32_bf16 v[28:31], a[172:175], v[52:55], v[28:31]\n"
"ds_read_b128 v[44:47], v4 offset:51200\n"
"s_add_u32 m0, s74, 8192\n"
"v_mfma_f32_16x16x32_bf16 v[24:27], a[168:171], v[52:55], v[24:27]\n"
"buffer_load_dwordx4 v2, s[32:35], s71 offen offset:0 sc1 lds\n"
"s_waitcnt lgkmcnt(4)\n"
"v_mfma_f32_16x16x32_bf16 v[24:27], v[88:91], v[56:59], v[24:27]\n"
"ds_read_b128 v[48:51], v4 offset:9216\n"
"s_add_u32 s71, s71, 1187840\n"
"v_mfma_f32_16x16x32_bf16 v[28:31], v[92:95], v[56:59], v[28:31]\n"
"v_mfma_f32_16x16x32_bf16 v[28:31], v[204:207], v[60:63], v[28:31]\n"
"ds_read_b128 v[52:55], v4 offset:23552\n"
"v_mfma_f32_16x16x32_bf16 v[24:27], v[200:203], v[60:63], v[24:27]\n"
"s_waitcnt lgkmcnt(4)\n"
"v_mfma_f32_16x16x32_bf16 v[24:27], a[64:67], v[32:35], v[24:27]\n"
"ds_read_b128 v[56:59], v4 offset:37888\n"
"v_mfma_f32_16x16x32_bf16 v[28:31], a[68:71], v[32:35], v[28:31]\n"
"v_mfma_f32_16x16x32_bf16 v[28:31], a[180:183], v[36:39], v[28:31]\n"
"ds_read_b128 v[60:63], v4 offset:52224\n"
"s_add_u32 m0, s64, 57344\n"
"v_mfma_f32_16x16x32_bf16 v[24:27], a[176:179], v[36:39], v[24:27]\n"
"buffer_load_dwordx4 v2, s[32:35], s63 offen offset:0 sc1 lds\n"
"s_waitcnt lgkmcnt(4)\n"
"v_mfma_f32_16x16x32_bf16 v[24:27], v[96:99], v[40:43], v[24:27]\n"
"ds_read_b128 v[32:35], v4 offset:10240\n"
"v_mfma_f32_16x16x32_bf16 v[28:31], v[100:103], v[40:43], v[28:31]\n"
"v_mfma_f32_16x16x32_bf16 v[28:31], v[212:215], v[44:47], v[28:31]\n"
"ds_read_b128 v[36:39], v4 offset:24576\n"
"v_mfma_f32_16x16x32_bf16 v[24:27], v[208:211], v[44:47], v[24:27]\n"
"s_waitcnt lgkmcnt(4)\n"
"v_mfma_f32_16x16x32_bf16 v[24:27], a[72:75], v[48:51], v[24:27]\n"
"ds_read_b128 v[40:43], v4 offset:38912\n"
"buffer_load_dwordx4 v2, s[32:35], s63 offen offset:1024 sc1 lds\n"
"v_mfma_f32_16x16x32_bf16 v[28:31], a[76:79], v[48:51], v[28:31]\n"
"ds_read_b128 v[12:15], v5 offset:0\n"
"v_mfma_f32_16x16x32_bf16 v[28:31], a[188:191], v[52:55], v[28:31]\n"
"ds_read_b128 v[44:47], v4 offset:53248\n"
"v_mfma_f32_16x16x32_bf16 v[24:27], a[184:187], v[52:55], v[24:27]\n"
"s_waitcnt lgkmcnt(5)\n"
"v_mfma_f32_16x16x32_bf16 v[24:27], v[104:107], v[56:59], v[24:27]\n"
"ds_read_b128 v[48:51], v4 offset:11264\n"
"v_mfma_f32_16x16x32_bf16 v[28:31], v[108:111], v[56:59], v[28:31]\n"
"v_mfma_f32_16x16x32_bf16 v[28:31], v[220:223], v[60:63], v[28:31]\n"
"ds_read_b128 v[52:55], v4 offset:25600\n"
"buffer_load_dwordx4 v2, s[32:35], s63 offen offset:2048 sc1 lds\n"
"v_mfma_f32_16x16x32_bf16 v[24:27], v[216:219], v[60:63], v[24:27]\n"
"s_waitcnt lgkmcnt(3)\n"
"v_mfma_f32_16x16x32_bf16 v[24:27], a[80:83], v[32:35], v[24:27]\n"
"ds_read_b128 v[56:59], v4 offset:39936\n"
"v_lshlrev_b32 v16, 16, v12\n"
"v_mfma_f32_16x16x32_bf16 v[28:31], a[84:87], v[32:35], v[28:31]\n"
"v_and_b32 v17, 0xffff0000, v12\n"
"v_lshlrev_b32 v18, 16, v13\n"
"v_mfma_f32_16x16x32_bf16 v[28:31], a[196:199], v[36:39], v[28:31]\n"
"ds_read_b128 v[60:63], v4 offset:54272\n"
"v_and_b32 v19, 0xffff0000, v13\n"
"v_mfma_f32_16x16x32_bf16 v[24:27], a[192:195], v[36:39], v[24:27]\n"
"v_lshlrev_b32 v20, 16, v14\n"
"s_waitcnt lgkmcnt(4)\n"
"v_mfma_f32_16x16x32_bf16 v[24:27], v[112:115], v[40:43], v[24:27]\n"
"ds_read_b128 v[32:35], v4 offset:12288\n"
"buffer_load_dwordx4 v2, s[32:35], s63 offen offset:3072 sc1 lds\n"
"v_mfma_f32_16x16x32_bf16 v[28:31], v[116:119], v[40:43], v[28:31]\n"
"v_and_b32 v21, 0xffff0000, v14\n"
"v_lshlrev_b32 v22, 16, v15\n"
"v_mfma_f32_16x16x32_bf16 v[28:31], v[228:231], v[44:47], v[28:31]\n"
"ds_read_b128 v[36:39], v4 offset:26624\n"
"v_and_b32 v23, 0xffff0000, v15\n"
"v_mfma_f32_16x16x32_bf16 v[24:27], v[224:227], v[44:47], v[24:27]\n"
"s_waitcnt lgkmcnt(4)\n"
"v_mfma_f32_16x16x32_bf16 v[24:27], a[88:91], v[48:51], v[24:27]\n"
"ds_read_b128 v[40:43], v4 offset:40960\n"
"v_mfma_f32_16x16x32_bf16 v[28:31], a[92:95], v[48:51], v[28:31]\n"
"v_mfma_f32_16x16x32_bf16 v[28:31], a[204:207], v[52:55], v[28:31]\n"
"ds_read_b128 v[44:47], v4 offset:55296\n"
"s_add_u32 s63, s63, 4096\n"
"v_mfma_f32_16x16x32_bf16 v[24:27], a[200:203], v[52:55], v[24:27]\n"
"s_add_u32 m0, s64, 61440\n"
"s_waitcnt lgkmcnt(4)\n"
"v_mfma_f32_16x16x32_bf16 v[24:27], v[120:123], v[56:59], v[24:27]\n"
"ds_read_b128 v[48:51], v4 offset:13312\n"
"buffer_load_dwordx4 v2, s[32:35], s63 offen offset:0 sc1 lds\n"
"v_mfma_f32_16x16x32_bf16 v[28:31], v[124:127], v[56:59], v[28:31]\n"
"v_mfma_f32_16x16x32_bf16 v[28:31], v[236:239], v[60:63], v[28:31]\n"
"ds_read_b128 v[52:55], v4 offset:27648\n"
"v_mfma_f32_16x16x32_bf16 v[24:27], v[232:235], v[60:63], v[24:27]\n"
"s_waitcnt lgkmcnt(4)\n"
"v_mfma_f32_16x16x32_bf16 v[24:27], a[96:99], v[32:35], v[24:27]\n"
"ds_read_b128 v[56:59], v4 offset:41984\n"
"v_mfma_f32_16x16x32_bf16 v[28:31], a[100:103], v[32:35], v[28:31]\n"
"v_mfma_f32_16x16x32_bf16 v[28:31], a[212:215], v[36:39], v[28:31]\n"
"ds_read_b128 v[60:63], v4 offset:56320\n"
"v_mfma_f32_16x16x32_bf16 v[24:27], a[208:211], v[36:39], v[24:27]\n"
"s_waitcnt lgkmcnt(4)\n"
"v_mfma_f32_16x16x32_bf16 v[24:27], v[128:131], v[40:43], v[24:27]\n"
"s_barrier\n"
"ds_read_b128 v[32:35], v2 offset:0\n"
"v_mfma_f32_16x16x32_bf16 v[28:31], v[132:135], v[40:43], v[28:31]\n"
"buffer_store_dwordx2 v[8:9], v2, s[92:95], s67 idxen sc1\n"
"v_mfma_f32_16x16x32_bf16 v[28:31], v[244:247], v[44:47], v[28:31]\n"
"ds_read_b128 v[36:39], v2 offset:14336\n"
"v_mfma_f32_16x16x32_bf16 v[24:27], v[240:243], v[44:47], v[24:27]\n"
"s_waitcnt lgkmcnt(4)\n"
"v_mfma_f32_16x16x32_bf16 v[24:27], a[104:107], v[48:51], v[24:27]\n"
"ds_read_b128 v[40:43], v2 offset:28672\n"
"v_mfma_f32_16x16x32_bf16 v[28:31], a[108:111], v[48:51], v[28:31]\n"
"v_mfma_f32_16x16x32_bf16 v[28:31], a[220:223], v[52:55], v[28:31]\n"
"ds_read_b128 v[44:47], v2 offset:43008\n"
"v_mfma_f32_16x16x32_bf16 v[24:27], a[216:219], v[52:55], v[24:27]\n"
"s_waitcnt lgkmcnt(4)\n"
"v_mfma_f32_16x16x32_bf16 v[24:27], v[136:139], v[56:59], v[24:27]\n"
"ds_read_b128 v[48:51], v2 offset:1024\n"
"v_mfma_f32_16x16x32_bf16 v[28:31], v[140:143], v[56:59], v[28:31]\n"
"v_mfma_f32_16x16x32_bf16 v[28:31], v[252:255], v[60:63], v[28:31]\n"
"ds_read_b128 v[52:55], v2 offset:15360\n"
"s_sub_u32 s62, s62, 1\n"
"v_mfma_f32_16x16x32_bf16 v[24:27], v[248:251], v[60:63], v[24:27]\n"
"s_cbranch_scc0 .L_dll\n"
"s_nop 1\n"
"s_add_u32 s68, s68, 1187840\n"
"s_nop 0\n"
"buffer_store_dwordx4 v[28:31], v2, s[32:35], s68 offen offset:1024 sc1\n"
"s_nop 1\n"
"buffer_store_dwordx4 v[24:27], v2, s[32:35], s68 offen sc1\n"
"s_mov_b32 s67, s68\n"
"s_waitcnt vmcnt(0)\n"
"s_barrier\n"
"buffer_store_dwordx2 v[8:9], v2, s[92:95], s67 idxen sc1\n"
"s_branch .L_end\n"
".L_dh:\n"
"s_mov_b32 s71, s65\n"
"s_add_u32 s69, s69, 38010880\n"
"s_add_u32 s68, s70, 2236416\n"
"s_add_u32 s70, s70, 42622976\n"
"s_add_u32 s44, s20, 0x0\n"
"s_addc_u32 s45, s21, 0x0\n"
"s_and_b32 s45, s45, 0xffff\n"
"s_mov_b32 s46, 0x400000\n"
"s_mov_b32 s47, 0x20000\n"
"s_lshl_b32 s65, s28, 20\n"
"s_mov_b32 s62, 16\n"
"s_add_u32 m0, s74, 0\n"
"s_nop 0\n"
"buffer_load_dwordx4 v2, s[32:35], s71 offen offset:0 sc1 lds\n"
"buffer_load_dwordx4 v2, s[32:35], s71 offen offset:1024 sc1 lds\n"
"s_add_u32 s71, s71, 1187840\n"
"s_add_u32 m0, s74, 8192\n"
"s_nop 0\n"
"buffer_load_dwordx4 v2, s[32:35], s71 offen offset:0 sc1 lds\n"
"buffer_load_dwordx4 v2, s[32:35], s71 offen offset:1024 sc1 lds\n"
"s_add_u32 s71, s71, 1187840\n"
"s_add_u32 m0, s64, 57344\n"
"s_nop 0\n"
"buffer_load_dwordx4 v2, s[32:35], s63 offen offset:0 sc1 lds\n"
"buffer_load_dwordx4 v2, s[32:35], s63 offen offset:1024 sc1 lds\n"
"buffer_load_dwordx4 v2, s[32:35], s63 offen offset:2048 sc1 lds\n"
"buffer_load_dwordx4 v2, s[32:35], s63 offen offset:3072 sc1 lds\n"
"s_add_u32 s63, s63, 4096\n"
"s_add_u32 m0, s64, 61440\n"
"s_nop 0\n"
"buffer_load_dwordx4 v2, s[32:35], s63 offen offset:0 sc1 lds\n"
"s_waitcnt vmcnt(0)\n"
"s_barrier\n"
"ds_read_b128 v[16:19], v5 offset:0\n"
"ds_read_b128 v[20:23], v5 offset:1024\n"
"ds_read_b128 v[32:35], v2 offset:0\n"
"ds_read_b128 v[36:39], v2 offset:14336\n"
"ds_read_b128 v[40:43], v2 offset:28672\n"
"ds_read_b128 v[44:47], v2 offset:43008\n"
"ds_read_b128 v[48:51], v2 offset:1024\n"
"ds_read_b128 v[52:55], v2 offset:15360\n"
".p2align 3\n"
".L_dal:\n"
"s_waitcnt lgkmcnt(4)\n"
"v_mfma_f32_16x16x32_bf16 v[16:19], a[0:3], v[32:35], v[16:19]\n"
"ds_read_b128 v[56:59], v2 offset:29696\n"
"v_mfma_f32_16x16x32_bf16 v[20:23], a[4:7], v[32:35], v[20:23]\n"
"buffer_load_dwordx4 v2, s[32:35], s63 offen offset:1024 sc1 lds\n"
"v_mfma_f32_16x16x32_bf16 v[20:23], a[116:119], v[36:39], v[20:23]\n"
"ds_read_b128 v[60:63], v2 offset:44032\n"
"v_mfma_f32_16x16x32_bf16 v[16:19], a[112:115], v[36:39], v[16:19]\n"
"s_waitcnt lgkmcnt(4)\n"
"v_mfma_f32_16x16x32_bf16 v[16:19], a[224:227], v[40:43], v[16:19]\n"
"ds_read_b128 v[32:35], v2 offset:2048\n"
"v_cvt_pk_bf16_f32 v12, v24, v25\n"
"v_mfma_f32_16x16x32_bf16 v[20:23], a[228:231], v[40:43], v[20:23]\n"
"v_cvt_pk_bf16_f32 v13, v26, v27\n"
"v_cvt_pk_bf16_f32 v14, v28, v29\n"
"v_mfma_f32_16x16x32_bf16 v[20:23], v[148:151], v[44:47], v[20:23]\n"
"ds_read_b128 v[36:39], v2 offset:16384\n"
"v_cvt_pk_bf16_f32 v15, v30, v31\n"
"v_mfma_f32_16x16x32_bf16 v[16:19], v[144:147], v[44:47], v[16:19]\n"
"buffer_load_dwordx4 v2, s[32:35], s63 offen offset:2048 sc1 lds\n"
"s_waitcnt lgkmcnt(4)\n"
"v_mfma_f32_16x16x32_bf16 v[16:19], a[8:11], v[48:51], v[16:19]\n"
"ds_read_b128 v[40:43], v2 offset:30720\n"
"buffer_store_dwordx4 v[12:15], v2, s[32:35], s68 offen sc1\n"
"v_mfma_f32_16x16x32_bf16 v[20:23], a[12:15], v[48:51], v[20:23]\n"
"v_add_f32 v10, v24, v25\n"
"v_mul_f32 v11, v24, v24\n"
"v_mfma_f32_16x16x32_bf16 v[20:23], a[124:127], v[52:55], v[20:23]\n"
"ds_read_b128 v[44:47], v2 offset:45056\n"
"buffer_load_dwordx2 v[6:7], v0, s[32:35], s66 offen sc1\n"
"v_mfma_f32_16x16x32_bf16 v[16:19], a[120:123], v[52:55], v[16:19]\n"
"v_fmac_f32 v11, v25, v25\n"
"s_waitcnt lgkmcnt(4)\n"
"v_mfma_f32_16x16x32_bf16 v[16:19], a[232:235], v[56:59], v[16:19]\n"
"ds_read_b128 v[48:51], v2 offset:3072\n"
"v_add_f32 v10, v10, v26\n"
"v_mfma_f32_16x16x32_bf16 v[20:23], a[236:239], v[56:59], v[20:23]\n"
"buffer_load_dwordx4 v2, s[32:35], s63 offen offset:3072 sc1 lds\n"
"v_fmac_f32 v11, v26, v26\n"
"v_mfma_f32_16x16x32_bf16 v[20:23], v[156:159], v[60:63], v[20:23]\n"
"ds_read_b128 v[52:55], v2 offset:17408\n"
"v_add_f32 v10, v10, v27\n"
"v_mfma_f32_16x16x32_bf16 v[16:19], v[152:155], v[60:63], v[16:19]\n"
"v_fmac_f32 v11, v27, v27\n"
"s_waitcnt lgkmcnt(4)\n"
"v_mfma_f32_16x16x32_bf16 v[16:19], a[16:19], v[32:35], v[16:19]\n"
"ds_read_b128 v[56:59], v2 offset:31744\n"
"v_add_f32 v10, v10, v28\n"
"v_mfma_f32_16x16x32_bf16 v[20:23], a[20:23], v[32:35], v[20:23]\n"
"v_fmac_f32 v11, v28, v28\n"
"v_add_f32 v10, v10, v29\n"
"v_mfma_f32_16x16x32_bf16 v[20:23], a[132:135], v[36:39], v[20:23]\n"
"ds_read_b128 v[60:63], v2 offset:46080\n"
"v_fmac_f32 v11, v29, v29\n"
"v_mfma_f32_16x16x32_bf16 v[16:19], a[128:131], v[36:39], v[16:19]\n"
"s_add_u32 s63, s63, 4096\n"
"s_waitcnt lgkmcnt(4)\n"
"v_mfma_f32_16x16x32_bf16 v[16:19], a[240:243], v[40:43], v[16:19]\n"
"ds_read_b128 v[32:35], v2 offset:4096\n"
"s_add_u32 m0, s64, 65536\n"
"v_mfma_f32_16x16x32_bf16 v[20:23], a[244:247], v[40:43], v[20:23]\n"
"buffer_load_dwordx4 v2, s[32:35], s63 offen offset:0 sc1 lds\n"
"v_add_f32 v10, v10, v30\n"
"v_mfma_f32_16x16x32_bf16 v[20:23], v[164:167], v[44:47], v[20:23]\n"
"ds_read_b128 v[36:39], v2 offset:18432\n"
"v_fmac_f32 v11, v30, v30\n"
"v_mfma_f32_16x16x32_bf16 v[16:19], v[160:163], v[44:47], v[16:19]\n"
"v_add_f32 v10, v10, v31\n"
"s_waitcnt lgkmcnt(4)\n"
"v_mfma_f32_16x16x32_bf16 v[16:19], a[24:27], v[48:51], v[16:19]\n"
"ds_read_b128 v[40:43], v2 offset:32768\n"
"v_fmac_f32 v11, v31, v31\n"
"v_mfma_f32_16x16x32_bf16 v[20:23], a[28:31], v[48:51], v[20:23]\n"
"buffer_load_dwordx4 v2, s[32:35], s63 offen offset:1024 sc1 lds\n"
"v_mfma_f32_16x16x32_bf16 v[20:23], a[140:143], v[52:55], v[20:23]\n"
"ds_read_b128 v[44:47], v2 offset:47104\n"
"v_mfma_f32_16x16x32_bf16 v[16:19], a[136:139], v[52:55], v[16:19]\n"
"s_waitcnt lgkmcnt(4)\n"
"v_mfma_f32_16x16x32_bf16 v[16:19], a[248:251], v[56:59], v[16:19]\n"
"ds_read_b128 v[48:51], v2 offset:5120\n"
"v_mfma_f32_16x16x32_bf16 v[20:23], a[252:255], v[56:59], v[20:23]\n"
"v_permlane32_swap_b32 v10, v11\n"
"v_mfma_f32_16x16x32_bf16 v[20:23], v[172:175], v[60:63], v[20:23]\n"
"ds_read_b128 v[52:55], v2 offset:19456\n"
"v_add_f32 v10, v10, v11\n"
"v_mfma_f32_16x16x32_bf16 v[16:19], v[168:171], v[60:63], v[16:19]\n"
"buffer_load_dwordx4 v2, s[32:35], s63 offen offset:2048 sc1 lds\n"
"s_waitcnt lgkmcnt(4)\n"
"v_mfma_f32_16x16x32_bf16 v[16:19], a[32:35], v[32:35], v[16:19]\n"
"ds_read_b128 v[56:59], v2 offset:33792\n"
"v_mfma_f32_16x16x32_bf16 v[20:23], a[36:39], v[32:35], v[20:23]\n"
"v_permlane16_swap_b32 v10, v11\n"
"v_mfma_f32_16x16x32_bf16 v[20:23], a[148:151], v[36:39], v[20:23]\n"
"ds_read_b128 v[60:63], v2 offset:48128\n"
"v_add_f32 v10, v10, v11\n"
"v_mfma_f32_16x16x32_bf16 v[16:19], a[144:147], v[36:39], v[16:19]\n"
"s_waitcnt lgkmcnt(4)\n"
"v_mfma_f32_16x16x32_bf16 v[16:19], v[64:67], v[40:43], v[16:19]\n"
"ds_read_b128 v[32:35], v2 offset:6144\n"
"v_mfma_f32_16x16x32_bf16 v[20:23], v[68:71], v[40:43], v[20:23]\n"
"buffer_load_dwordx4 v2, s[32:35], s63 offen offset:3072 sc1 lds\n"
"v_mfma_f32_16x16x32_bf16 v[20:23], v[180:183], v[44:47], v[20:23]\n"
"ds_read_b128 v[36:39], v2 offset:20480\n"
"v_mfma_f32_16x16x32_bf16 v[16:19], v[176:179], v[44:47], v[16:19]\n"
"s_waitcnt lgkmcnt(4)\n"
"v_mfma_f32_16x16x32_bf16 v[16:19], a[40:43], v[48:51], v[16:19]\n"
"ds_read_b128 v[40:43], v2 offset:34816\n"
"v_mfma_f32_16x16x32_bf16 v[20:23], a[44:47], v[48:51], v[20:23]\n"
"v_permlane32_swap_b32 v10, v11\n"
"v_mfma_f32_16x16x32_bf16 v[20:23], a[156:159], v[52:55], v[20:23]\n"
"ds_read_b128 v[44:47], v2 offset:49152\n"
"buffer_store_dwordx2 v[10:11], v3, s[32:35], s68 offen offset:1024 sc1\n"
"v_mfma_f32_16x16x32_bf16 v[16:19], a[152:155], v[52:55], v[16:19]\n"
"s_waitcnt vmcnt(18)\n"
"s_waitcnt lgkmcnt(4)\n"
"v_mfma_f32_16x16x32_bf16 v[16:19], v[72:75], v[56:59], v[16:19]\n"
"s_barrier\n"
"ds_read_b128 v[48:51], v2 offset:7168\n"
"v_mfma_f32_16x16x32_bf16 v[20:23], v[76:79], v[56:59], v[20:23]\n"
"s_mov_b32 s67, s68\n"
"s_mov_b32 s68, s70\n"
"v_mfma_f32_16x16x32_bf16 v[20:23], v[188:191], v[60:63], v[20:23]\n"
"ds_read_b128 v[52:55], v2 offset:21504\n"
"s_add_u32 s63, s63, 4096\n"
"v_mfma_f32_16x16x32_bf16 v[16:19], v[184:187], v[60:63], v[16:19]\n"
"s_add_u32 m0, s64, 69632\n"
"s_waitcnt lgkmcnt(4)\n"
"v_mfma_f32_16x16x32_bf16 v[16:19], a[48:51], v[32:35], v[16:19]\n"
"ds_read_b128 v[56:59], v2 offset:35840\n"
"buffer_load_dwordx4 v2, s[32:35], s63 offen offset:0 sc1 lds\n"
"v_mfma_f32_16x16x32_bf16 v[20:23], a[52:55], v[32:35], v[20:23]\n"
"s_add_u32 s70, s70, 1187840\n"
"v_mfma_f32_16x16x32_bf16 v[20:23], a[164:167], v[36:39], v[20:23]\n"
"ds_read_b128 v[60:63], v2 offset:50176\n"
"v_mfma_f32_16x16x32_bf16 v[16:19], a[160:163], v[36:39], v[16:19]\n"
"buffer_load_dwordx4 v2, s[32:35], s63 offen offset:1024 sc1 lds\n"
"s_waitcnt lgkmcnt(4)\n"
"v_mfma_f32_16x16x32_bf16 v[16:19], v[80:83], v[40:43], v[16:19]\n"
"ds_read_b128 v[32:35], v2 offset:8192\n"
"s_add_u32 s63, s63, 1175552\n"
"v_mfma_f32_16x16x32_bf16 v[20:23], v[84:87], v[40:43], v[20:23]\n"
"v_mfma_f32_16x16x32_bf16 v[20:23], v[196:199], v[44:47], v[20:23]\n"
"ds_read_b128 v[36:39], v2 offset:22528\n"
"s_waitcnt vmcnt(8)\n"
"v_mfma_f32_16x16x32_bf16 v[16:19], v[192:195], v[44:47], v[16:19]\n"
"v_cmp_ne_u64 vcc, v[8:9], v[6:7]\n"
"s_waitcnt lgkmcnt(4)\n"
"v_mfma_f32_16x16x32_bf16 v[16:19], a[56:59], v[48:51], v[16:19]\n"
"ds_read_b128 v[40:43], v2 offset:36864\n"
"s_cbranch_vccnz .L_rta0\n"
".L_bka0:\n"
"v_mfma_f32_16x16x32_bf16 v[20:23], a[60:63], v[48:51], v[20:23]\n"
"s_add_u32 s66, s66, 1187840\n"
"v_mfma_f32_16x16x32_bf16 v[20:23], a[172:175], v[52:55], v[20:23]\n"
"ds_read_b128 v[44:47], v2 offset:51200\n"
"s_add_u32 m0, s74, 0\n"
"v_mfma_f32_16x16x32_bf16 v[16:19], a[168:171], v[52:55], v[16:19]\n"
"buffer_load_dwordx4 v2, s[32:35], s71 offen offset:0 sc1 lds\n"
"s_waitcnt lgkmcnt(4)\n"
"v_mfma_f32_16x16x32_bf16 v[16:19], v[88:91], v[56:59], v[16:19]\n"
"ds_read_b128 v[48:51], v2 offset:9216\n"
"buffer_load_dwordx4 v2, s[32:35], s71 offen offset:1024 sc1 lds\n"
"v_mfma_f32_16x16x32_bf16 v[20:23], v[92:95], v[56:59], v[20:23]\n"
"s_add_u32 s71, s71, 1187840\n"
"v_mfma_f32_16x16x32_bf16 v[20:23], v[204:207], v[60:63], v[20:23]\n"
"ds_read_b128 v[52:55], v2 offset:23552\n"
"v_mfma_f32_16x16x32_bf16 v[16:19], v[200:203], v[60:63], v[16:19]\n"
"s_waitcnt lgkmcnt(4)\n"
"v_mfma_f32_16x16x32_bf16 v[16:19], a[64:67], v[32:35], v[16:19]\n"
"ds_read_b128 v[56:59], v2 offset:37888\n"
"v_mfma_f32_16x16x32_bf16 v[20:23], a[68:71], v[32:35], v[20:23]\n"
"v_mfma_f32_16x16x32_bf16 v[20:23], a[180:183], v[36:39], v[20:23]\n"
"ds_read_b128 v[60:63], v2 offset:52224\n"
"s_add_u32 m0, s64, 0\n"
"v_mfma_f32_16x16x32_bf16 v[16:19], a[176:179], v[36:39], v[16:19]\n"
"buffer_load_dwordx4 v2, s[32:35], s63 offen offset:0 sc1 lds\n"
"s_waitcnt lgkmcnt(4)\n"
"v_mfma_f32_16x16x32_bf16 v[16:19], v[96:99], v[40:43], v[16:19]\n"
"ds_read_b128 v[32:35], v2 offset:10240\n"
"v_mfma_f32_16x16x32_bf16 v[20:23], v[100:103], v[40:43], v[20:23]\n"
"v_mfma_f32_16x16x32_bf16 v[20:23], v[212:215], v[44:47], v[20:23]\n"
"ds_read_b128 v[36:39], v2 offset:24576\n"
"v_mfma_f32_16x16x32_bf16 v[16:19], v[208:211], v[44:47], v[16:19]\n"
"s_waitcnt lgkmcnt(4)\n"
"v_mfma_f32_16x16x32_bf16 v[16:19], a[72:75], v[48:51], v[16:19]\n"
"ds_read_b128 v[40:43], v2 offset:38912\n"
"buffer_load_dwordx4 v2, s[32:35], s63 offen offset:1024 sc1 lds\n"
"v_mfma_f32_16x16x32_bf16 v[20:23], a[76:79], v[48:51], v[20:23]\n"
"ds_read_b128 v[24:27], v5 offset:8192\n"
"v_mfma_f32_16x16x32_bf16 v[20:23], a[188:191], v[52:55], v[20:23]\n"
"ds_read_b128 v[44:47], v2 offset:53248\n"
"ds_read_b128 v[28:31], v5 offset:9216\n"
"v_mfma_f32_16x16x32_bf16 v[16:19], a[184:187], v[52:55], v[16:19]\n"
"s_waitcnt lgkmcnt(6)\n"
"v_mfma_f32_16x16x32_bf16 v[16:19], v[104:107], v[56:59], v[16:19]\n"
"ds_read_b128 v[48:51], v2 offset:11264\n"
"v_mfma_f32_16x16x32_bf16 v[20:23], v[108:111], v[56:59], v[20:23]\n"
"v_mfma_f32_16x16x32_bf16 v[20:23], v[220:223], v[60:63], v[20:23]\n"
"ds_read_b128 v[52:55], v2 offset:25600\n"
"buffer_load_dwordx4 v2, s[32:35], s63 offen offset:2048 sc1 lds\n"
"v_mfma_f32_16x16x32_bf16 v[16:19], v[216:219], v[60:63], v[16:19]\n"
"s_waitcnt lgkmcnt(6)\n"
"v_mfma_f32_16x16x32_bf16 v[16:19], a[80:83], v[32:35], v[16:19]\n"
"ds_read_b128 v[56:59], v2 offset:39936\n"
"v_mfma_f32_16x16x32_bf16 v[20:23], a[84:87], v[32:35], v[20:23]\n"
"v_mfma_f32_16x16x32_bf16 v[20:23], a[196:199], v[36:39], v[20:23]\n"
"ds_read_b128 v[60:63], v2 offset:54272\n"
"v_mfma_f32_16x16x32_bf16 v[16:19], a[192:195], v[36:39], v[16:19]\n"
"s_waitcnt lgkmcnt(5)\n"
"v_mfma_f32_16x16x32_bf16 v[16:19], v[112:115], v[40:43], v[16:19]\n"
"ds_read_b128 v[32:35], v2 offset:12288\n"
"buffer_load_dwordx4 v2, s[32:35], s63 offen offset:3072 sc1 lds\n"
"v_mfma_f32_16x16x32_bf16 v[20:23], v[116:119], v[40:43], v[20:23]\n"
"v_mfma_f32_16x16x32_bf16 v[20:23], v[228:231], v[44:47], v[20:23]\n"
"ds_read_b128 v[36:39], v2 offset:26624\n"
"v_mfma_f32_16x16x32_bf16 v[16:19], v[224:227], v[44:47], v[16:19]\n"
"s_waitcnt lgkmcnt(4)\n"
"v_mfma_f32_16x16x32_bf16 v[16:19], a[88:91], v[48:51], v[16:19]\n"
"ds_read_b128 v[40:43], v2 offset:40960\n"
"v_mfma_f32_16x16x32_bf16 v[20:23], a[92:95], v[48:51], v[20:23]\n"
"v_mfma_f32_16x16x32_bf16 v[20:23], a[204:207], v[52:55], v[20:23]\n"
"ds_read_b128 v[44:47], v2 offset:55296\n"
"s_add_u32 s63, s63, 4096\n"
"v_mfma_f32_16x16x32_bf16 v[16:19], a[200:203], v[52:55], v[16:19]\n"
"s_add_u32 m0, s64, 4096\n"
"s_waitcnt lgkmcnt(4)\n"
"v_mfma_f32_16x16x32_bf16 v[16:19], v[120:123], v[56:59], v[16:19]\n"
"ds_read_b128 v[48:51], v2 offset:13312\n"
"buffer_load_dwordx4 v2, s[32:35], s63 offen offset:0 sc1 lds\n"
"v_mfma_f32_16x16x32_bf16 v[20:23], v[124:127], v[56:59], v[20:23]\n"
"v_mfma_f32_16x16x32_bf16 v[20:23], v[236:239], v[60:63], v[20:23]\n"
"ds_read_b128 v[52:55], v2 offset:27648\n"
"v_mfma_f32_16x16x32_bf16 v[16:19], v[232:235], v[60:63], v[16:19]\n"
"s_waitcnt lgkmcnt(4)\n"
"v_mfma_f32_16x16x32_bf16 v[16:19], a[96:99], v[32:35], v[16:19]\n"
"ds_read_b128 v[56:59], v2 offset:41984\n"
"v_mfma_f32_16x16x32_bf16 v[20:23], a[100:103], v[32:35], v[20:23]\n"
"v_mfma_f32_16x16x32_bf16 v[20:23], a[212:215], v[36:39], v[20:23]\n"
"ds_read_b128 v[60:63], v2 offset:56320\n"
"v_mfma_f32_16x16x32_bf16 v[16:19], a[208:211], v[36:39], v[16:19]\n"
"s_waitcnt vmcnt(9)\n"
"s_waitcnt lgkmcnt(4)\n"
"v_mfma_f32_16x16x32_bf16 v[16:19], v[128:131], v[40:43], v[16:19]\n"
"s_barrier\n"
"ds_read_b128 v[32:35], v4 offset:0\n"
"v_mfma_f32_16x16x32_bf16 v[20:23], v[132:135], v[40:43], v[20:23]\n"
"buffer_store_dwordx2 v[8:9], v2, s[92:95], s67 idxen sc1\n"
"v_mfma_f32_16x16x32_bf16 v[20:23], v[244:247], v[44:47], v[20:23]\n"
"ds_read_b128 v[36:39], v4 offset:14336\n"
"v_mfma_f32_16x16x32_bf16 v[16:19], v[240:243], v[44:47], v[16:19]\n"
"s_waitcnt lgkmcnt(4)\n"
"v_mfma_f32_16x16x32_bf16 v[16:19], a[104:107], v[48:51], v[16:19]\n"
"ds_read_b128 v[40:43], v4 offset:28672\n"
"v_mfma_f32_16x16x32_bf16 v[20:23], a[108:111], v[48:51], v[20:23]\n"
"v_mfma_f32_16x16x32_bf16 v[20:23], a[220:223], v[52:55], v[20:23]\n"
"ds_read_b128 v[44:47], v4 offset:43008\n"
"v_mfma_f32_16x16x32_bf16 v[16:19], a[216:219], v[52:55], v[16:19]\n"
"s_waitcnt lgkmcnt(4)\n"
"v_mfma_f32_16x16x32_bf16 v[16:19], v[136:139], v[56:59], v[16:19]\n"
"ds_read_b128 v[48:51], v4 offset:1024\n"
"v_mfma_f32_16x16x32_bf16 v[20:23], v[140:143], v[56:59], v[20:23]\n"
"v_mfma_f32_16x16x32_bf16 v[20:23], v[252:255], v[60:63], v[20:23]\n"
"ds_read_b128 v[52:55], v4 offset:15360\n"
"s_sub_u32 s62, s62, 1\n"
"v_mfma_f32_16x16x32_bf16 v[16:19], v[248:251], v[60:63], v[16:19]\n"
"s_cbranch_scc1 .L_dab\n"
"s_waitcnt lgkmcnt(4)\n"
"v_mfma_f32_16x16x32_bf16 v[24:27], a[0:3], v[32:35], v[24:27]\n"
"ds_read_b128 v[56:59], v4 offset:29696\n"
"v_mfma_f32_16x16x32_bf16 v[28:31], a[4:7], v[32:35], v[28:31]\n"
"buffer_load_dwordx4 v2, s[32:35], s63 offen offset:1024 sc1 lds\n"
"v_mfma_f32_16x16x32_bf16 v[28:31], a[116:119], v[36:39], v[28:31]\n"
"ds_read_b128 v[60:63], v4 offset:44032\n"
"v_mfma_f32_16x16x32_bf16 v[24:27], a[112:115], v[36:39], v[24:27]\n"
"s_waitcnt lgkmcnt(4)\n"
"v_mfma_f32_16x16x32_bf16 v[24:27], a[224:227], v[40:43], v[24:27]\n"
"ds_read_b128 v[32:35], v4 offset:2048\n"
"v_cvt_pk_bf16_f32 v12, v16, v17\n"
"v_mfma_f32_16x16x32_bf16 v[28:31], a[228:231], v[40:43], v[28:31]\n"
"v_cvt_pk_bf16_f32 v13, v18, v19\n"
"v_cvt_pk_bf16_f32 v14, v20, v21\n"
"v_mfma_f32_16x16x32_bf16 v[28:31], v[148:151], v[44:47], v[28:31]\n"
"ds_read_b128 v[36:39], v4 offset:16384\n"
"v_cvt_pk_bf16_f32 v15, v22, v23\n"
"v_mfma_f32_16x16x32_bf16 v[24:27], v[144:147], v[44:47], v[24:27]\n"
"buffer_load_dwordx4 v2, s[32:35], s63 offen offset:2048 sc1 lds\n"
"s_waitcnt lgkmcnt(4)\n"
"v_mfma_f32_16x16x32_bf16 v[24:27], a[8:11], v[48:51], v[24:27]\n"
"ds_read_b128 v[40:43], v4 offset:30720\n"
"buffer_store_dwordx4 v[12:15], v2, s[32:35], s69 offen sc1\n"
"v_mfma_f32_16x16x32_bf16 v[28:31], a[12:15], v[48:51], v[28:31]\n"
"v_add_f32 v10, v16, v17\n"
"v_mul_f32 v11, v16, v16\n"
"v_mfma_f32_16x16x32_bf16 v[28:31], a[124:127], v[52:55], v[28:31]\n"
"ds_read_b128 v[44:47], v4 offset:45056\n"
"buffer_load_dwordx2 v[6:7], v0, s[32:35], s66 offen sc1\n"
"v_mfma_f32_16x16x32_bf16 v[24:27], a[120:123], v[52:55], v[24:27]\n"
"v_fmac_f32 v11, v17, v17\n"
"s_waitcnt lgkmcnt(4)\n"
"v_mfma_f32_16x16x32_bf16 v[24:27], a[232:235], v[56:59], v[24:27]\n"
"ds_read_b128 v[48:51], v4 offset:3072\n"
"v_add_f32 v10, v10, v18\n"
"v_mfma_f32_16x16x32_bf16 v[28:31], a[236:239], v[56:59], v[28:31]\n"
"buffer_load_dwordx4 v2, s[32:35], s63 offen offset:3072 sc1 lds\n"
"v_fmac_f32 v11, v18, v18\n"
"v_mfma_f32_16x16x32_bf16 v[28:31], v[156:159], v[60:63], v[28:31]\n"
"ds_read_b128 v[52:55], v4 offset:17408\n"
"v_add_f32 v10, v10, v19\n"
"v_mfma_f32_16x16x32_bf16 v[24:27], v[152:155], v[60:63], v[24:27]\n"
"v_fmac_f32 v11, v19, v19\n"
"s_waitcnt lgkmcnt(4)\n"
"v_mfma_f32_16x16x32_bf16 v[24:27], a[16:19], v[32:35], v[24:27]\n"
"ds_read_b128 v[56:59], v4 offset:31744\n"
"v_add_f32 v10, v10, v20\n"
"v_mfma_f32_16x16x32_bf16 v[28:31], a[20:23], v[32:35], v[28:31]\n"
"v_fmac_f32 v11, v20, v20\n"
"v_add_f32 v10, v10, v21\n"
"v_mfma_f32_16x16x32_bf16 v[28:31], a[132:135], v[36:39], v[28:31]\n"
"ds_read_b128 v[60:63], v4 offset:46080\n"
"v_fmac_f32 v11, v21, v21\n"
"v_mfma_f32_16x16x32_bf16 v[24:27], a[128:131], v[36:39], v[24:27]\n"
"s_add_u32 s63, s63, 4096\n"
"s_waitcnt lgkmcnt(4)\n"
"v_mfma_f32_16x16x32_bf16 v[24:27], a[240:243], v[40:43], v[24:27]\n"
"ds_read_b128 v[32:35], v4 offset:4096\n"
"s_add_u32 m0, s64, 8192\n"
"v_mfma_f32_16x16x32_bf16 v[28:31], a[244:247], v[40:43], v[28:31]\n"
"buffer_load_dwordx4 v2, s[32:35], s63 offen offset:0 sc1 lds\n"
"v_add_f32 v10, v10, v22\n"
"v_mfma_f32_16x16x32_bf16 v[28:31], v[164:167], v[44:47], v[28:31]\n"
"ds_read_b128 v[36:39], v4 offset:18432\n"
"v_fmac_f32 v11, v22, v22\n"
"v_mfma_f32_16x16x32_bf16 v[24:27], v[160:163], v[44:47], v[24:27]\n"
"v_add_f32 v10, v10, v23\n"
"s_waitcnt lgkmcnt(4)\n"
"v_mfma_f32_16x16x32_bf16 v[24:27], a[24:27], v[48:51], v[24:27]\n"
"ds_read_b128 v[40:43], v4 offset:32768\n"
"v_fmac_f32 v11, v23, v23\n"
"v_mfma_f32_16x16x32_bf16 v[28:31], a[28:31], v[48:51], v[28:31]\n"
"buffer_load_dwordx4 v2, s[32:35], s63 offen offset:1024 sc1 lds\n"
"v_mfma_f32_16x16x32_bf16 v[28:31], a[140:143], v[52:55], v[28:31]\n"
"ds_read_b128 v[44:47], v4 offset:47104\n"
"v_mfma_f32_16x16x32_bf16 v[24:27], a[136:139], v[52:55], v[24:27]\n"
"s_waitcnt lgkmcnt(4)\n"
"v_mfma_f32_16x16x32_bf16 v[24:27], a[248:251], v[56:59], v[24:27]\n"
"ds_read_b128 v[48:51], v4 offset:5120\n"
"v_mfma_f32_16x16x32_bf16 v[28:31], a[252:255], v[56:59], v[28:31]\n"
"v_permlane32_swap_b32 v10, v11\n"
"v_mfma_f32_16x16x32_bf16 v[28:31], v[172:175], v[60:63], v[28:31]\n"
"ds_read_b128 v[52:55], v4 offset:19456\n"
"v_add_f32 v10, v10, v11\n"
"v_mfma_f32_16x16x32_bf16 v[24:27], v[168:171], v[60:63], v[24:27]\n"
"buffer_load_dwordx4 v2, s[32:35], s63 offen offset:2048 sc1 lds\n"
"s_waitcnt lgkmcnt(4)\n"
"v_mfma_f32_16x16x32_bf16 v[24:27], a[32:35], v[32:35], v[24:27]\n"
"ds_read_b128 v[56:59], v4 offset:33792\n"
"v_mfma_f32_16x16x32_bf16 v[28:31], a[36:39], v[32:35], v[28:31]\n"
"v_permlane16_swap_b32 v10, v11\n"
"v_mfma_f32_16x16x32_bf16 v[28:31], a[148:151], v[36:39], v[28:31]\n"
"ds_read_b128 v[60:63], v4 offset:48128\n"
"v_add_f32 v10, v10, v11\n"
"v_mfma_f32_16x16x32_bf16 v[24:27], a[144:147], v[36:39], v[24:27]\n"
"s_waitcnt lgkmcnt(4)\n"
"v_mfma_f32_16x16x32_bf16 v[24:27], v[64:67], v[40:43], v[24:27]\n"
"ds_read_b128 v[32:35], v4 offset:6144\n"
"v_mfma_f32_16x16x32_bf16 v[28:31], v[68:71], v[40:43], v[28:31]\n"
"buffer_load_dwordx4 v2, s[32:35], s63 offen offset:3072 sc1 lds\n"
"v_mfma_f32_16x16x32_bf16 v[28:31], v[180:183], v[44:47], v[28:31]\n"
"ds_read_b128 v[36:39], v4 offset:20480\n"
"v_mfma_f32_16x16x32_bf16 v[24:27], v[176:179], v[44:47], v[24:27]\n"
"s_waitcnt lgkmcnt(4)\n"
"v_mfma_f32_16x16x32_bf16 v[24:27], a[40:43], v[48:51], v[24:27]\n"
"ds_read_b128 v[40:43], v4 offset:34816\n"
"v_mfma_f32_16x16x32_bf16 v[28:31], a[44:47], v[48:51], v[28:31]\n"
"v_permlane32_swap_b32 v10, v11\n"
"v_mfma_f32_16x16x32_bf16 v[28:31], a[156:159], v[52:55], v[28:31]\n"
"ds_read_b128 v[44:47], v4 offset:49152\n"
"buffer_store_dwordx2 v[10:11], v3, s[32:35], s69 offen offset:1024 sc1\n"
"v_mfma_f32_16x16x32_bf16 v[24:27], a[152:155], v[52:55], v[24:27]\n"
"s_waitcnt vmcnt(18)\n"
"s_waitcnt lgkmcnt(4)\n"
"v_mfma_f32_16x16x32_bf16 v[24:27], v[72:75], v[56:59], v[24:27]\n"
"s_barrier\n"
"ds_read_b128 v[48:51], v4 offset:7168\n"
"v_mfma_f32_16x16x32_bf16 v[28:31], v[76:79], v[56:59], v[28:31]\n"
"s_mov_b32 s67, s69\n"
"s_mov_b32 s69, s70\n"
"v_mfma_f32_16x16x32_bf16 v[28:31], v[188:191], v[60:63], v[28:31]\n"
"ds_read_b128 v[52:55], v4 offset:21504\n"
"s_add_u32 s63, s63, 4096\n"
"v_mfma_f32_16x16x32_bf16 v[24:27], v[184:187], v[60:63], v[24:27]\n"
"s_add_u32 m0, s64, 12288\n"
"s_waitcnt lgkmcnt(4)\n"
"v_mfma_f32_16x16x32_bf16 v[24:27], a[48:51], v[32:35], v[24:27]\n"
"ds_read_b128 v[56:59], v4 offset:35840\n"
"buffer_load_dwordx4 v2, s[32:35], s63 offen offset:0 sc1 lds\n"
"v_mfma_f32_16x16x32_bf16 v[28:31], a[52:55], v[32:35], v[28:31]\n"
"s_add_u32 s70, s70, 1187840\n"
"v_mfma_f32_16x16x32_bf16 v[28:31], a[164:167], v[36:39], v[28:31]\n"
"ds_read_b128 v[60:63], v4 offset:50176\n"
"v_mfma_f32_16x16x32_bf16 v[24:27], a[160:163], v[36:39], v[24:27]\n"
"buffer_load_dwordx4 v2, s[32:35], s63 offen offset:1024 sc1 lds\n"
"s_waitcnt lgkmcnt(4)\n"
"v_mfma_f32_16x16x32_bf16 v[24:27], v[80:83], v[40:43], v[24:27]\n"
"ds_read_b128 v[32:35], v4 offset:8192\n"
"s_add_u32 s63, s63, 1175552\n"
"v_mfma_f32_16x16x32_bf16 v[28:31], v[84:87], v[40:43], v[28:31]\n"
"v_mfma_f32_16x16x32_bf16 v[28:31], v[196:199], v[44:47], v[28:31]\n"
"ds_read_b128 v[36:39], v4 offset:22528\n"
"s_waitcnt vmcnt(8)\n"
"v_mfma_f32_16x16x32_bf16 v[24:27], v[192:195], v[44:47], v[24:27]\n"
"v_cmp_ne_u64 vcc, v[8:9], v[6:7]\n"
"s_waitcnt lgkmcnt(4)\n"
"v_mfma_f32_16x16x32_bf16 v[24:27], a[56:59], v[48:51], v[24:27]\n"
"ds_read_b128 v[40:43], v4 offset:36864\n"
"s_cbranch_vccnz .L_rta1\n"
".L_bka1:\n"
"v_mfma_f32_16x16x32_bf16 v[28:31], a[60:63], v[48:51], v[28:31]\n"
"s_add_u32 s66, s66, 1187840\n"
"v_mfma_f32_16x16x32_bf16 v[28:31], a[172:175], v[52:55], v[28:31]\n"
"ds_read_b128 v[44:47], v4 offset:51200\n"
"s_add_u32 m0, s74, 8192\n"
"v_mfma_f32_16x16x32_bf16 v[24:27], a[168:171], v[52:55], v[24:27]\n"
"buffer_load_dwordx4 v2, s[32:35], s71 offen offset:0 sc1 lds\n"
"s_waitcnt lgkmcnt(4)\n"
"v_mfma_f32_16x16x32_bf16 v[24:27], v[88:91], v[56:59], v[24:27]\n"
"ds_read_b128 v[48:51], v4 offset:9216\n"
"buffer_load_dwordx4 v2, s[32:35], s71 offen offset:1024 sc1 lds\n"
"v_mfma_f32_16x16x32_bf16 v[28:31], v[92:95], v[56:59], v[28:31]\n"
"s_add_u32 s71, s71, 1187840\n"
"v_mfma_f32_16x16x32_bf16 v[28:31], v[204:207], v[60:63], v[28:31]\n"
"ds_read_b128 v[52:55], v4 offset:23552\n"
"v_mfma_f32_16x16x32_bf16 v[24:27], v[200:203], v[60:63], v[24:27]\n"
"s_waitcnt lgkmcnt(4)\n"
"v_mfma_f32_16x16x32_bf16 v[24:27], a[64:67], v[32:35], v[24:27]\n"
"ds_read_b128 v[56:59], v4 offset:37888\n"
"v_mfma_f32_16x16x32_bf16 v[28:31], a[68:71], v[32:35], v[28:31]\n"
"v_mfma_f32_16x16x32_bf16 v[28:31], a[180:183], v[36:39], v[28:31]\n"
"ds_read_b128 v[60:63], v4 offset:52224\n"
"s_add_u32 m0, s64, 57344\n"
"v_mfma_f32_16x16x32_bf16 v[24:27], a[176:179], v[36:39], v[24:27]\n"
"buffer_load_dwordx4 v2, s[32:35], s63 offen offset:0 sc1 lds\n"
"s_waitcnt lgkmcnt(4)\n"
"v_mfma_f32_16x16x32_bf16 v[24:27], v[96:99], v[40:43], v[24:27]\n"
"ds_read_b128 v[32:35], v4 offset:10240\n"
"v_mfma_f32_16x16x32_bf16 v[28:31], v[100:103], v[40:43], v[28:31]\n"
"v_mfma_f32_16x16x32_bf16 v[28:31], v[212:215], v[44:47], v[28:31]\n"
"ds_read_b128 v[36:39], v4 offset:24576\n"
"v_mfma_f32_16x16x32_bf16 v[24:27], v[208:211], v[44:47], v[24:27]\n"
"s_waitcnt lgkmcnt(4)\n"
"v_mfma_f32_16x16x32_bf16 v[24:27], a[72:75], v[48:51], v[24:27]\n"
"ds_read_b128 v[40:43], v4 offset:38912\n"
"buffer_load_dwordx4 v2, s[32:35], s63 offen offset:1024 sc1 lds\n"
"v_mfma_f32_16x16x32_bf16 v[28:31], a[76:79], v[48:51], v[28:31]\n"
"ds_read_b128 v[16:19], v5 offset:0\n"
"v_mfma_f32_16x16x32_bf16 v[28:31], a[188:191], v[52:55], v[28:31]\n"
"ds_read_b128 v[44:47], v4 offset:53248\n"
"ds_read_b128 v[20:23], v5 offset:1024\n"
"v_mfma_f32_16x16x32_bf16 v[24:27], a[184:187], v[52:55], v[24:27]\n"
"s_waitcnt lgkmcnt(6)\n"
"v_mfma_f32_16x16x32_bf16 v[24:27], v[104:107], v[56:59], v[24:27]\n"
"ds_read_b128 v[48:51], v4 offset:11264\n"
"v_mfma_f32_16x16x32_bf16 v[28:31], v[108:111], v[56:59], v[28:31]\n"
"v_mfma_f32_16x16x32_bf16 v[28:31], v[220:223], v[60:63], v[28:31]\n"
"ds_read_b128 v[52:55], v4 offset:25600\n"
"buffer_load_dwordx4 v2, s[32:35], s63 offen offset:2048 sc1 lds\n"
"v_mfma_f32_16x16x32_bf16 v[24:27], v[216:219], v[60:63], v[24:27]\n"
"s_waitcnt lgkmcnt(6)\n"
"v_mfma_f32_16x16x32_bf16 v[24:27], a[80:83], v[32:35], v[24:27]\n"
"ds_read_b128 v[56:59], v4 offset:39936\n"
"v_mfma_f32_16x16x32_bf16 v[28:31], a[84:87], v[32:35], v[28:31]\n"
"v_mfma_f32_16x16x32_bf16 v[28:31], a[196:199], v[36:39], v[28:31]\n"
"ds_read_b128 v[60:63], v4 offset:54272\n"
"v_mfma_f32_16x16x32_bf16 v[24:27], a[192:195], v[36:39], v[24:27]\n"
"s_waitcnt lgkmcnt(5)\n"
"v_mfma_f32_16x16x32_bf16 v[24:27], v[112:115], v[40:43], v[24:27]\n"
"ds_read_b128 v[32:35], v4 offset:12288\n"
"buffer_load_dwordx4 v2, s[32:35], s63 offen offset:3072 sc1 lds\n"
"v_mfma_f32_16x16x32_bf16 v[28:31], v[116:119], v[40:43], v[28:31]\n"
"v_mfma_f32_16x16x32_bf16 v[28:31], v[228:231], v[44:47], v[28:31]\n"
"ds_read_b128 v[36:39], v4 offset:26624\n"
"v_mfma_f32_16x16x32_bf16 v[24:27], v[224:227], v[44:47], v[24:27]\n"
"s_waitcnt lgkmcnt(4)\n"
"v_mfma_f32_16x16x32_bf16 v[24:27], a[88:91], v[48:51], v[24:27]\n"
"ds_read_b128 v[40:43], v4 offset:40960\n"
"v_mfma_f32_16x16x32_bf16 v[28:31], a[92:95], v[48:51], v[28:31]\n"
"v_mfma_f32_16x16x32_bf16 v[28:31], a[204:207], v[52:55], v[28:31]\n"
"ds_read_b128 v[44:47], v4 offset:55296\n"
"s_add_u32 s63, s63, 4096\n"
"v_mfma_f32_16x16x32_bf16 v[24:27], a[200:203], v[52:55], v[24:27]\n"
"s_add_u32 m0, s64, 61440\n"
"s_waitcnt lgkmcnt(4)\n"
"v_mfma_f32_16x16x32_bf16 v[24:27], v[120:123], v[56:59], v[24:27]\n"
"ds_read_b128 v[48:51], v4 offset:13312\n"
"buffer_load_dwordx4 v2, s[32:35], s63 offen offset:0 sc1 lds\n"
"v_mfma_f32_16x16x32_bf16 v[28:31], v[124:127], v[56:59], v[28:31]\n"
"v_mfma_f32_16x16x32_bf16 v[28:31], v[236:239], v[60:63], v[28:31]\n"
"ds_read_b128 v[52:55], v4 offset:27648\n"
"v_mfma_f32_16x16x32_bf16 v[24:27], v[232:235], v[60:63], v[24:27]\n"
"s_waitcnt lgkmcnt(4)\n"
"v_mfma_f32_16x16x32_bf16 v[24:27], a[96:99], v[32:35], v[24:27]\n"
"ds_read_b128 v[56:59], v4 offset:41984\n"
"v_mfma_f32_16x16x32_bf16 v[28:31], a[100:103], v[32:35], v[28:31]\n"
"v_mfma_f32_16x16x32_bf16 v[28:31], a[212:215], v[36:39], v[28:31]\n"
"ds_read_b128 v[60:63], v4 offset:56320\n"
"v_mfma_f32_16x16x32_bf16 v[24:27], a[208:211], v[36:39], v[24:27]\n"
"s_waitcnt vmcnt(9)\n"
"s_waitcnt lgkmcnt(4)\n"
"v_mfma_f32_16x16x32_bf16 v[24:27], v[128:131], v[40:43], v[24:27]\n"
"s_barrier\n"
"ds_read_b128 v[32:35], v2 offset:0\n"
"v_mfma_f32_16x16x32_bf16 v[28:31], v[132:135], v[40:43], v[28:31]\n"
"buffer_store_dwordx2 v[8:9], v2, s[92:95], s67 idxen sc1\n"
"v_mfma_f32_16x16x32_bf16 v[28:31], v[244:247], v[44:47], v[28:31]\n"
"ds_read_b128 v[36:39], v2 offset:14336\n"
"v_mfma_f32_16x16x32_bf16 v[24:27], v[240:243], v[44:47], v[24:27]\n"
"s_waitcnt lgkmcnt(4)\n"
"v_mfma_f32_16x16x32_bf16 v[24:27], a[104:107], v[48:51], v[24:27]\n"
"ds_read_b128 v[40:43], v2 offset:28672\n"
"v_mfma_f32_16x16x32_bf16 v[28:31], a[108:111], v[48:51], v[28:31]\n"
"v_mfma_f32_16x16x32_bf16 v[28:31], a[220:223], v[52:55], v[28:31]\n"
"ds_read_b128 v[44:47], v2 offset:43008\n"
"v_mfma_f32_16x16x32_bf16 v[24:27], a[216:219], v[52:55], v[24:27]\n"
"s_waitcnt lgkmcnt(4)\n"
"v_mfma_f32_16x16x32_bf16 v[24:27], v[136:139], v[56:59], v[24:27]\n"
"ds_read_b128 v[48:51], v2 offset:1024\n"
"v_mfma_f32_16x16x32_bf16 v[28:31], v[140:143], v[56:59], v[28:31]\n"
"v_mfma_f32_16x16x32_bf16 v[28:31], v[252:255], v[60:63], v[28:31]\n"
"ds_read_b128 v[52:55], v2 offset:15360\n"
"v_mfma_f32_16x16x32_bf16 v[24:27], v[248:251], v[60:63], v[24:27]\n"
"s_branch .L_dal\n"
".L_dab:\n"
"s_mov_b32 s62, 15\n"
".p2align 3\n"
".L_dbl:\n"
"s_waitcnt lgkmcnt(4)\n"
"v_mfma_f32_16x16x32_bf16 v[24:27], a[0:3], v[32:35], v[24:27]\n"
"ds_read_b128 v[56:59], v4 offset:29696\n"
"v_mfma_f32_16x16x32_bf16 v[28:31], a[4:7], v[32:35], v[28:31]\n"
"buffer_load_dwordx4 v2, s[32:35], s63 offen offset:1024 sc1 lds\n"
"v_mfma_f32_16x16x32_bf16 v[28:31], a[116:119], v[36:39], v[28:31]\n"
"ds_read_b128 v[60:63], v4 offset:44032\n"
"v_mfma_f32_16x16x32_bf16 v[24:27], a[112:115], v[36:39], v[24:27]\n"
"s_waitcnt lgkmcnt(4)\n"
"v_mfma_f32_16x16x32_bf16 v[24:27], a[224:227], v[40:43], v[24:27]\n"
"ds_read_b128 v[32:35], v4 offset:2048\n"
"v_cvt_pk_bf16_f32 v12, v16, v17\n"
"v_mfma_f32_16x16x32_bf16 v[28:31], a[228:231], v[40:43], v[28:31]\n"
"v_cvt_pk_bf16_f32 v13, v18, v19\n"
"v_cvt_pk_bf16_f32 v14, v20, v21\n"
"v_mfma_f32_16x16x32_bf16 v[28:31], v[148:151], v[44:47], v[28:31]\n"
"ds_read_b128 v[36:39], v4 offset:16384\n"
"v_cvt_pk_bf16_f32 v15, v22, v23\n"
"v_mfma_f32_16x16x32_bf16 v[24:27], v[144:147], v[44:47], v[24:27]\n"
"buffer_load_dwordx4 v2, s[32:35], s63 offen offset:2048 sc1 lds\n"
"s_waitcnt lgkmcnt(4)\n"
"v_mfma_f32_16x16x32_bf16 v[24:27], a[8:11], v[48:51], v[24:27]\n"
"ds_read_b128 v[40:43], v4 offset:30720\n"
"buffer_store_dwordx4 v[12:15], v1, s[44:47], s65 offen\n"
"v_mfma_f32_16x16x32_bf16 v[28:31], a[12:15], v[48:51], v[28:31]\n"
"s_add_u32 s65, s65, 32768\n"
"v_mfma_f32_16x16x32_bf16 v[28:31], a[124:127], v[52:55], v[28:31]\n"
"ds_read_b128 v[44:47], v4 offset:45056\n"
"buffer_load_dwordx2 v[6:7], v0, s[32:35], s66 offen sc1\n"
"v_mfma_f32_16x16x32_bf16 v[24:27], a[120:123], v[52:55], v[24:27]\n"
"s_waitcnt lgkmcnt(4)\n"
"v_mfma_f32_16x16x32_bf16 v[24:27], a[232:235], v[56:59], v[24:27]\n"
"ds_read_b128 v[48:51], v4 offset:3072\n"
"v_mfma_f32_16x16x32_bf16 v[28:31], a[236:239], v[56:59], v[28:31]\n"
"buffer_load_dwordx4 v2, s[32:35], s63 offen offset:3072 sc1 lds\n"
"v_mfma_f32_16x16x32_bf16 v[28:31], v[156:159], v[60:63], v[28:31]\n"
"ds_read_b128 v[52:55], v4 offset:17408\n"
"v_mfma_f32_16x16x32_bf16 v[24:27], v[152:155], v[60:63], v[24:27]\n"
"s_waitcnt lgkmcnt(4)\n"
"v_mfma_f32_16x16x32_bf16 v[24:27], a[16:19], v[32:35], v[24:27]\n"
"ds_read_b128 v[56:59], v4 offset:31744\n"
"v_mfma_f32_16x16x32_bf16 v[28:31], a[20:23], v[32:35], v[28:31]\n"
"v_mfma_f32_16x16x32_bf16 v[28:31], a[132:135], v[36:39], v[28:31]\n"
"ds_read_b128 v[60:63], v4 offset:46080\n"
"v_mfma_f32_16x16x32_bf16 v[24:27], a[128:131], v[36:39], v[24:27]\n"
"s_add_u32 s63, s63, 4096\n"
"s_waitcnt lgkmcnt(4)\n"
"v_mfma_f32_16x16x32_bf16 v[24:27], a[240:243], v[40:43], v[24:27]\n"
"ds_read_b128 v[32:35], v4 offset:4096\n"
"s_add_u32 m0, s64, 8192\n"
"v_mfma_f32_16x16x32_bf16 v[28:31], a[244:247], v[40:43], v[28:31]\n"
"buffer_load_dwordx4 v2, s[32:35], s63 offen offset:0 sc1 lds\n"
"v_mfma_f32_16x16x32_bf16 v[28:31], v[164:167], v[44:47], v[28:31]\n"
"ds_read_b128 v[36:39], v4 offset:18432\n"
"v_mfma_f32_16x16x32_bf16 v[24:27], v[160:163], v[44:47], v[24:27]\n"
"s_waitcnt lgkmcnt(4)\n"
"v_mfma_f32_16x16x32_bf16 v[24:27], a[24:27], v[48:51], v[24:27]\n"
"ds_read_b128 v[40:43], v4 offset:32768\n"
"v_mfma_f32_16x16x32_bf16 v[28:31], a[28:31], v[48:51], v[28:31]\n"
"buffer_load_dwordx4 v2, s[32:35], s63 offen offset:1024 sc1 lds\n"
"v_mfma_f32_16x16x32_bf16 v[28:31], a[140:143], v[52:55], v[28:31]\n"
"ds_read_b128 v[44:47], v4 offset:47104\n"
"v_mfma_f32_16x16x32_bf16 v[24:27], a[136:139], v[52:55], v[24:27]\n"
"s_waitcnt lgkmcnt(4)\n"
"v_mfma_f32_16x16x32_bf16 v[24:27], a[248:251], v[56:59], v[24:27]\n"
"ds_read_b128 v[48:51], v4 offset:5120\n"
"v_mfma_f32_16x16x32_bf16 v[28:31], a[252:255], v[56:59], v[28:31]\n"
"v_mfma_f32_16x16x32_bf16 v[28:31], v[172:175], v[60:63], v[28:31]\n"
"ds_read_b128 v[52:55], v4 offset:19456\n"
"v_mfma_f32_16x16x32_bf16 v[24:27], v[168:171], v[60:63], v[24:27]\n"
"buffer_load_dwordx4 v2, s[32:35], s63 offen offset:2048 sc1 lds\n"
"s_waitcnt lgkmcnt(4)\n"
"v_mfma_f32_16x16x32_bf16 v[24:27], a[32:35], v[32:35], v[24:27]\n"
"ds_read_b128 v[56:59], v4 offset:33792\n"
"v_mfma_f32_16x16x32_bf16 v[28:31], a[36:39], v[32:35], v[28:31]\n"
"v_mfma_f32_16x16x32_bf16 v[28:31], a[148:151], v[36:39], v[28:31]\n"
"ds_read_b128 v[60:63], v4 offset:48128\n"
"v_mfma_f32_16x16x32_bf16 v[24:27], a[144:147], v[36:39], v[24:27]\n"
"s_waitcnt lgkmcnt(4)\n"
"v_mfma_f32_16x16x32_bf16 v[24:27], v[64:67], v[40:43], v[24:27]\n"
"ds_read_b128 v[32:35], v4 offset:6144\n"
"v_mfma_f32_16x16x32_bf16 v[28:31], v[68:71], v[40:43], v[28:31]\n"
"buffer_load_dwordx4 v2, s[32:35], s63 offen offset:3072 sc1 lds\n"
"v_mfma_f32_16x16x32_bf16 v[28:31], v[180:183], v[44:47], v[28:31]\n"
"ds_read_b128 v[36:39], v4 offset:20480\n"
"v_mfma_f32_16x16x32_bf16 v[24:27], v[176:179], v[44:47], v[24:27]\n"
"s_waitcnt lgkmcnt(4)\n"
"v_mfma_f32_16x16x32_bf16 v[24:27], a[40:43], v[48:51], v[24:27]\n"
"ds_read_b128 v[40:43], v4 offset:34816\n"
"v_mfma_f32_16x16x32_bf16 v[28:31], a[44:47], v[48:51], v[28:31]\n"
"v_mfma_f32_16x16x32_bf16 v[28:31], a[156:159], v[52:55], v[28:31]\n"
"ds_read_b128 v[44:47], v4 offset:49152\n"
"v_mfma_f32_16x16x32_bf16 v[24:27], a[152:155], v[52:55], v[24:27]\n"
"s_waitcnt vmcnt(16)\n"
"s_waitcnt lgkmcnt(4)\n"
"v_mfma_f32_16x16x32_bf16 v[24:27], v[72:75], v[56:59], v[24:27]\n"
"s_barrier\n"
"ds_read_b128 v[48:51], v4 offset:7168\n"
"v_mfma_f32_16x16x32_bf16 v[28:31], v[76:79], v[56:59], v[28:31]\n"
"v_mfma_f32_16x16x32_bf16 v[28:31], v[188:191], v[60:63], v[28:31]\n"
"ds_read_b128 v[52:55], v4 offset:21504\n"
"s_add_u32 s63, s63, 4096\n"
"v_mfma_f32_16x16x32_bf16 v[24:27], v[184:187], v[60:63], v[24:27]\n"
"s_add_u32 m0, s64, 12288\n"
"s_waitcnt lgkmcnt(4)\n"
"v_mfma_f32_16x16x32_bf16 v[24:27], a[48:51], v[32:35], v[24:27]\n"
"ds_read_b128 v[56:59], v4 offset:35840\n"
"buffer_load_dwordx4 v2, s[32:35], s63 offen offset:0 sc1 lds\n"
"v_mfma_f32_16x16x32_bf16 v[28:31], a[52:55], v[32:35], v[28:31]\n"
"v_mfma_f32_16x16x32_bf16 v[28:31], a[164:167], v[36:39], v[28:31]\n"
"ds_read_b128 v[60:63], v4 offset:50176\n"
"v_mfma_f32_16x16x32_bf16 v[24:27], a[160:163], v[36:39], v[24:27]\n"
"buffer_load_dwordx4 v2, s[32:35], s63 offen offset:1024 sc1 lds\n"
"s_waitcnt lgkmcnt(4)\n"
"v_mfma_f32_16x16x32_bf16 v[24:27], v[80:83], v[40:43], v[24:27]\n"
"ds_read_b128 v[32:35], v4 offset:8192\n"
"s_add_u32 s63, s63, 1175552\n"
"v_mfma_f32_16x16x32_bf16 v[28:31], v[84:87], v[40:43], v[28:31]\n"
"v_mfma_f32_16x16x32_bf16 v[28:31], v[196:199], v[44:47], v[28:31]\n"
"ds_read_b128 v[36:39], v4 offset:22528\n"
"s_waitcnt vmcnt(7)\n"
"v_mfma_f32_16x16x32_bf16 v[24:27], v[192:195], v[44:47], v[24:27]\n"
"v_cmp_ne_u64 vcc, v[8:9], v[6:7]\n"
"s_waitcnt lgkmcnt(4)\n"
"v_mfma_f32_16x16x32_bf16 v[24:27], a[56:59], v[48:51], v[24:27]\n"
"ds_read_b128 v[40:43], v4 offset:36864\n"
"s_cbranch_vccnz .L_rtb1\n"
".L_bkb1:\n"
"v_mfma_f32_16x16x32_bf16 v[28:31], a[60:63], v[48:51], v[28:31]\n"
"s_add_u32 s66, s66, 1187840\n"
"v_mfma_f32_16x16x32_bf16 v[28:31], a[172:175], v[52:55], v[28:31]\n"
"ds_read_b128 v[44:47], v4 offset:51200\n"
"s_add_u32 m0, s74, 8192\n"
"v_mfma_f32_16x16x32_bf16 v[24:27], a[168:171], v[52:55], v[24:27]\n"
"buffer_load_dwordx4 v2, s[32:35], s71 offen offset:0 sc1 lds\n"
"s_waitcnt lgkmcnt(4)\n"
"v_mfma_f32_16x16x32_bf16 v[24:27], v[88:91], v[56:59], v[24:27]\n"
"ds_read_b128 v[48:51], v4 offset:9216\n"
"buffer_load_dwordx4 v2, s[32:35], s71 offen offset:1024 sc1 lds\n"
"v_mfma_f32_16x16x32_bf16 v[28:31], v[92:95], v[56:59], v[28:31]\n"
"s_add_u32 s71, s71, 1187840\n"
"v_mfma_f32_16x16x32_bf16 v[28:31], v[204:207], v[60:63], v[28:31]\n"
"ds_read_b128 v[52:55], v4 offset:23552\n"
"v_mfma_f32_16x16x32_bf16 v[24:27], v[200:203], v[60:63], v[24:27]\n"
"s_waitcnt lgkmcnt(4)\n"
"v_mfma_f32_16x16x32_bf16 v[24:27], a[64:67], v[32:35], v[24:27]\n"
"ds_read_b128 v[56:59], v4 offset:37888\n"
"v_mfma_f32_16x16x32_bf16 v[28:31], a[68:71], v[32:35], v[28:31]\n"
"v_mfma_f32_16x16x32_bf16 v[28:31], a[180:183], v[36:39], v[28:31]\n"
"ds_read_b128 v[60:63], v4 offset:52224\n"
"s_add_u32 m0, s64, 57344\n"
"v_mfma_f32_16x16x32_bf16 v[24:27], a[176:179], v[36:39], v[24:27]\n"
"buffer_load_dwordx4 v2, s[32:35], s63 offen offset:0 sc1 lds\n"
"s_waitcnt lgkmcnt(4)\n"
"v_mfma_f32_16x16x32_bf16 v[24:27], v[96:99], v[40:43], v[24:27]\n"
"ds_read_b128 v[32:35], v4 offset:10240\n"
"v_mfma_f32_16x16x32_bf16 v[28:31], v[100:103], v[40:43], v[28:31]\n"
"v_mfma_f32_16x16x32_bf16 v[28:31], v[212:215], v[44:47], v[28:31]\n"
"ds_read_b128 v[36:39], v4 offset:24576\n"
"v_mfma_f32_16x16x32_bf16 v[24:27], v[208:211], v[44:47], v[24:27]\n"
"s_waitcnt lgkmcnt(4)\n"
"v_mfma_f32_16x16x32_bf16 v[24:27], a[72:75], v[48:51], v[24:27]\n"
"ds_read_b128 v[40:43], v4 offset:38912\n"
"buffer_load_dwordx4 v2, s[32:35], s63 offen offset:1024 sc1 lds\n"
"v_mfma_f32_16x16x32_bf16 v[28:31], a[76:79], v[48:51], v[28:31]\n"
"ds_read_b128 v[16:19], v5 offset:0\n"
"v_mfma_f32_16x16x32_bf16 v[28:31], a[188:191], v[52:55], v[28:31]\n"
"ds_read_b128 v[44:47], v4 offset:53248\n"
"ds_read_b128 v[20:23], v5 offset:1024\n"
"v_mfma_f32_16x16x32_bf16 v[24:27], a[184:187], v[52:55], v[24:27]\n"
"s_waitcnt lgkmcnt(6)\n"
"v_mfma_f32_16x16x32_bf16 v[24:27], v[104:107], v[56:59], v[24:27]\n"
"ds_read_b128 v[48:51], v4 offset:11264\n"
"v_mfma_f32_16x16x32_bf16 v[28:31], v[108:111], v[56:59], v[28:31]\n"
"v_mfma_f32_16x16x32_bf16 v[28:31], v[220:223], v[60:63], v[28:31]\n"
"ds_read_b128 v[52:55], v4 offset:25600\n"
"buffer_load_dwordx4 v2, s[32:35], s63 offen offset:2048 sc1 lds\n"
"v_mfma_f32_16x16x32_bf16 v[24:27], v[216:219], v[60:63], v[24:27]\n"
"s_waitcnt lgkmcnt(6)\n"
"v_mfma_f32_16x16x32_bf16 v[24:27], a[80:83], v[32:35], v[24:27]\n"
"ds_read_b128 v[56:59], v4 offset:39936\n"
"v_mfma_f32_16x16x32_bf16 v[28:31], a[84:87], v[32:35], v[28:31]\n"
"v_mfma_f32_16x16x32_bf16 v[28:31], a[196:199], v[36:39], v[28:31]\n"
"ds_read_b128 v[60:63], v4 offset:54272\n"
"v_mfma_f32_16x16x32_bf16 v[24:27], a[192:195], v[36:39], v[24:27]\n"
"s_waitcnt lgkmcnt(5)\n"
"v_mfma_f32_16x16x32_bf16 v[24:27], v[112:115], v[40:43], v[24:27]\n"
"ds_read_b128 v[32:35], v4 offset:12288\n"
"buffer_load_dwordx4 v2, s[32:35], s63 offen offset:3072 sc1 lds\n"
"v_mfma_f32_16x16x32_bf16 v[28:31], v[116:119], v[40:43], v[28:31]\n"
"v_mfma_f32_16x16x32_bf16 v[28:31], v[228:231], v[44:47], v[28:31]\n"
"ds_read_b128 v[36:39], v4 offset:26624\n"
"v_mfma_f32_16x16x32_bf16 v[24:27], v[224:227], v[44:47], v[24:27]\n"
"s_waitcnt lgkmcnt(4)\n"
"v_mfma_f32_16x16x32_bf16 v[24:27], a[88:91], v[48:51], v[24:27]\n"
"ds_read_b128 v[40:43], v4 offset:40960\n"
"v_mfma_f32_16x16x32_bf16 v[28:31], a[92:95], v[48:51], v[28:31]\n"
"v_mfma_f32_16x16x32_bf16 v[28:31], a[204:207], v[52:55], v[28:31]\n"
"ds_read_b128 v[44:47], v4 offset:55296\n"
"s_add_u32 s63, s63, 4096\n"
"v_mfma_f32_16x16x32_bf16 v[24:27], a[200:203], v[52:55], v[24:27]\n"
"s_add_u32 m0, s64, 61440\n"
"s_waitcnt lgkmcnt(4)\n"
"v_mfma_f32_16x16x32_bf16 v[24:27], v[120:123], v[56:59], v[24:27]\n"
"ds_read_b128 v[48:51], v4 offset:13312\n"
"buffer_load_dwordx4 v2, s[32:35], s63 offen offset:0 sc1 lds\n"
"v_mfma_f32_16x16x32_bf16 v[28:31], v[124:127], v[56:59], v[28:31]\n"
"v_mfma_f32_16x16x32_bf16 v[28:31], v[236:239], v[60:63], v[28:31]\n"
"ds_read_b128 v[52:55], v4 offset:27648\n"
"v_mfma_f32_16x16x32_bf16 v[24:27], v[232:235], v[60:63], v[24:27]\n"
"s_waitcnt lgkmcnt(4)\n"
"v_mfma_f32_16x16x32_bf16 v[24:27], a[96:99], v[32:35], v[24:27]\n"
"ds_read_b128 v[56:59], v4 offset:41984\n"
"v_mfma_f32_16x16x32_bf16 v[28:31], a[100:103], v[32:35], v[28:31]\n"
"v_mfma_f32_16x16x32_bf16 v[28:31], a[212:215], v[36:39], v[28:31]\n"
"ds_read_b128 v[60:63], v4 offset:56320\n"
"v_mfma_f32_16x16x32_bf16 v[24:27], a[208:211], v[36:39], v[24:27]\n"
"s_waitcnt lgkmcnt(4)\n"
"v_mfma_f32_16x16x32_bf16 v[24:27], v[128:131], v[40:43], v[24:27]\n"
"s_barrier\n"
"ds_read_b128 v[32:35], v2 offset:0\n"
"v_mfma_f32_16x16x32_bf16 v[28:31], v[132:135], v[40:43], v[28:31]\n"
"v_mfma_f32_16x16x32_bf16 v[28:31], v[244:247], v[44:47], v[28:31]\n"
"ds_read_b128 v[36:39], v2 offset:14336\n"
"v_mfma_f32_16x16x32_bf16 v[24:27], v[240:243], v[44:47], v[24:27]\n"
"s_waitcnt lgkmcnt(4)\n"
"v_mfma_f32_16x16x32_bf16 v[24:27], a[104:107], v[48:51], v[24:27]\n"
"ds_read_b128 v[40:43], v2 offset:28672\n"
"v_mfma_f32_16x16x32_bf16 v[28:31], a[108:111], v[48:51], v[28:31]\n"
"v_mfma_f32_16x16x32_bf16 v[28:31], a[220:223], v[52:55], v[28:31]\n"
"ds_read_b128 v[44:47], v2 offset:43008\n"
"v_mfma_f32_16x16x32_bf16 v[24:27], a[216:219], v[52:55], v[24:27]\n"
"s_waitcnt lgkmcnt(4)\n"
"v_mfma_f32_16x16x32_bf16 v[24:27], v[136:139], v[56:59], v[24:27]\n"
"ds_read_b128 v[48:51], v2 offset:1024\n"
"v_mfma_f32_16x16x32_bf16 v[28:31], v[140:143], v[56:59], v[28:31]\n"
"v_mfma_f32_16x16x32_bf16 v[28:31], v[252:255], v[60:63], v[28:31]\n"
"ds_read_b128 v[52:55], v2 offset:15360\n"
"s_sub_u32 s62, s62, 1\n"
"v_mfma_f32_16x16x32_bf16 v[24:27], v[248:251], v[60:63], v[24:27]\n"
"s_cbranch_scc1 .L_dbt\n"
"s_waitcnt lgkmcnt(4)\n"
"v_mfma_f32_16x16x32_bf16 v[16:19], a[0:3], v[32:35], v[16:19]\n"
"ds_read_b128 v[56:59], v2 offset:29696\n"
"v_mfma_f32_16x16x32_bf16 v[20:23], a[4:7], v[32:35], v[20:23]\n"
"buffer_load_dwordx4 v2, s[32:35], s63 offen offset:1024 sc1 lds\n"
"v_mfma_f32_16x16x32_bf16 v[20:23], a[116:119], v[36:39], v[20:23]\n"
"ds_read_b128 v[60:63], v2 offset:44032\n"
"v_mfma_f32_16x16x32_bf16 v[16:19], a[112:115], v[36:39], v[16:19]\n"
"s_waitcnt lgkmcnt(4)\n"
"v_mfma_f32_16x16x32_bf16 v[16:19], a[224:227], v[40:43], v[16:19]\n"
"ds_read_b128 v[32:35], v2 offset:2048\n"
"v_cvt_pk_bf16_f32 v12, v24, v25\n"
"v_mfma_f32_16x16x32_bf16 v[20:23], a[228:231], v[40:43], v[20:23]\n"
"v_cvt_pk_bf16_f32 v13, v26, v27\n"
"v_cvt_pk_bf16_f32 v14, v28, v29\n"
"v_mfma_f32_16x16x32_bf16 v[20:23], v[148:151], v[44:47], v[20:23]\n"
"ds_read_b128 v[36:39], v2 offset:16384\n"
"v_cvt_pk_bf16_f32 v15, v30, v31\n"
"v_mfma_f32_16x16x32_bf16 v[16:19], v[144:147], v[44:47], v[16:19]\n"
"buffer_load_dwordx4 v2, s[32:35], s63 offen offset:2048 sc1 lds\n"
"s_waitcnt lgkmcnt(4)\n"
"v_mfma_f32_16x16x32_bf16 v[16:19], a[8:11], v[48:51], v[16:19]\n"
"ds_read_b128 v[40:43], v2 offset:30720\n"
"buffer_store_dwordx4 v[12:15], v1, s[44:47], s65 offen\n"
"v_mfma_f32_16x16x32_bf16 v[20:23], a[12:15], v[48:51], v[20:23]\n"
"s_add_u32 s65, s65, 32768\n"
"v_mfma_f32_16x16x32_bf16 v[20:23], a[124:127], v[52:55], v[20:23]\n"
"ds_read_b128 v[44:47], v2 offset:45056\n"
"buffer_load_dwordx2 v[6:7], v0, s[32:35], s66 offen sc1\n"
"v_mfma_f32_16x16x32_bf16 v[16:19], a[120:123], v[52:55], v[16:19]\n"
"s_waitcnt lgkmcnt(4)\n"
"v_mfma_f32_16x16x32_bf16 v[16:19], a[232:235], v[56:59], v[16:19]\n"
"ds_read_b128 v[48:51], v2 offset:3072\n"
"v_mfma_f32_16x16x32_bf16 v[20:23], a[236:239], v[56:59], v[20:23]\n"
"buffer_load_dwordx4 v2, s[32:35], s63 offen offset:3072 sc1 lds\n"
"v_mfma_f32_16x16x32_bf16 v[20:23], v[156:159], v[60:63], v[20:23]\n"
"ds_read_b128 v[52:55], v2 offset:17408\n"
"v_mfma_f32_16x16x32_bf16 v[16:19], v[152:155], v[60:63], v[16:19]\n"
"s_waitcnt lgkmcnt(4)\n"
"v_mfma_f32_16x16x32_bf16 v[16:19], a[16:19], v[32:35], v[16:19]\n"
"ds_read_b128 v[56:59], v2 offset:31744\n"
"v_mfma_f32_16x16x32_bf16 v[20:23], a[20:23], v[32:35], v[20:23]\n"
"v_mfma_f32_16x16x32_bf16 v[20:23], a[132:135], v[36:39], v[20:23]\n"
"ds_read_b128 v[60:63], v2 offset:46080\n"
"v_mfma_f32_16x16x32_bf16 v[16:19], a[128:131], v[36:39], v[16:19]\n"
"s_add_u32 s63, s63, 4096\n"
"s_waitcnt lgkmcnt(4)\n"
"v_mfma_f32_16x16x32_bf16 v[16:19], a[240:243], v[40:43], v[16:19]\n"
"ds_read_b128 v[32:35], v2 offset:4096\n"
"s_add_u32 m0, s64, 65536\n"
"v_mfma_f32_16x16x32_bf16 v[20:23], a[244:247], v[40:43], v[20:23]\n"
"buffer_load_dwordx4 v2, s[32:35], s63 offen offset:0 sc1 lds\n"
"v_mfma_f32_16x16x32_bf16 v[20:23], v[164:167], v[44:47], v[20:23]\n"
"ds_read_b128 v[36:39], v2 offset:18432\n"
"v_mfma_f32_16x16x32_bf16 v[16:19], v[160:163], v[44:47], v[16:19]\n"
"s_waitcnt lgkmcnt(4)\n"
"v_mfma_f32_16x16x32_bf16 v[16:19], a[24:27], v[48:51], v[16:19]\n"
"ds_read_b128 v[40:43], v2 offset:32768\n"
"v_mfma_f32_16x16x32_bf16 v[20:23], a[28:31], v[48:51], v[20:23]\n"
"buffer_load_dwordx4 v2, s[32:35], s63 offen offset:1024 sc1 lds\n"
"v_mfma_f32_16x16x32_bf16 v[20:23], a[140:143], v[52:55], v[20:23]\n"
"ds_read_b128 v[44:47], v2 offset:47104\n"
"v_mfma_f32_16x16x32_bf16 v[16:19], a[136:139], v[52:55], v[16:19]\n"
"s_waitcnt lgkmcnt(4)\n"
"v_mfma_f32_16x16x32_bf16 v[16:19], a[248:251], v[56:59], v[16:19]\n"
"ds_read_b128 v[48:51], v2 offset:5120\n"
"v_mfma_f32_16x16x32_bf16 v[20:23], a[252:255], v[56:59], v[20:23]\n"
"v_mfma_f32_16x16x32_bf16 v[20:23], v[172:175], v[60:63], v[20:23]\n"
"ds_read_b128 v[52:55], v2 offset:19456\n"
"v_mfma_f32_16x16x32_bf16 v[16:19], v[168:171], v[60:63], v[16:19]\n"
"buffer_load_dwordx4 v2, s[32:35], s63 offen offset:2048 sc1 lds\n"
"s_waitcnt lgkmcnt(4)\n"
"v_mfma_f32_16x16x32_bf16 v[16:19], a[32:35], v[32:35], v[16:19]\n"
"ds_read_b128 v[56:59], v2 offset:33792\n"
"v_mfma_f32_16x16x32_bf16 v[20:23], a[36:39], v[32:35], v[20:23]\n"
"v_mfma_f32_16x16x32_bf16 v[20:23], a[148:151], v[36:39], v[20:23]\n"
"ds_read_b128 v[60:63], v2 offset:48128\n"
"v_mfma_f32_16x16x32_bf16 v[16:19], a[144:147], v[36:39], v[16:19]\n"
"s_waitcnt lgkmcnt(4)\n"
"v_mfma_f32_16x16x32_bf16 v[16:19], v[64:67], v[40:43], v[16:19]\n"
"ds_read_b128 v[32:35], v2 offset:6144\n"
"v_mfma_f32_16x16x32_bf16 v[20:23], v[68:71], v[40:43], v[20:23]\n"
"buffer_load_dwordx4 v2, s[32:35], s63 offen offset:3072 sc1 lds\n"
"v_mfma_f32_16x16x32_bf16 v[20:23], v[180:183], v[44:47], v[20:23]\n"
"ds_read_b128 v[36:39], v2 offset:20480\n"
"v_mfma_f32_16x16x32_bf16 v[16:19], v[176:179], v[44:47], v[16:19]\n"
"s_waitcnt lgkmcnt(4)\n"
"v_mfma_f32_16x16x32_bf16 v[16:19], a[40:43], v[48:51], v[16:19]\n"
"ds_read_b128 v[40:43], v2 offset:34816\n"
"v_mfma_f32_16x16x32_bf16 v[20:23], a[44:47], v[48:51], v[20:23]\n"
"v_mfma_f32_16x16x32_bf16 v[20:23], a[156:159], v[52:55], v[20:23]\n"
"ds_read_b128 v[44:47], v2 offset:49152\n"
"v_mfma_f32_16x16x32_bf16 v[16:19], a[152:155], v[52:55], v[16:19]\n"
"s_waitcnt vmcnt(16)\n"
"s_waitcnt lgkmcnt(4)\n"
"v_mfma_f32_16x16x32_bf16 v[16:19], v[72:75], v[56:59], v[16:19]\n"
"s_barrier\n"
"ds_read_b128 v[48:51], v2 offset:7168\n"
"v_mfma_f32_16x16x32_bf16 v[20:23], v[76:79], v[56:59], v[20:23]\n"
"v_mfma_f32_16x16x32_bf16 v[20:23], v[188:191], v[60:63], v[20:23]\n"
"ds_read_b128 v[52:55], v2 offset:21504\n"
"s_add_u32 s63, s63, 4096\n"
"v_mfma_f32_16x16x32_bf16 v[16:19], v[184:187], v[60:63], v[16:19]\n"
"s_add_u32 m0, s64, 69632\n"
"s_waitcnt lgkmcnt(4)\n"
"v_mfma_f32_16x16x32_bf16 v[16:19], a[48:51], v[32:35], v[16:19]\n"
"ds_read_b128 v[56:59], v2 offset:35840\n"
"buffer_load_dwordx4 v2, s[32:35], s63 offen offset:0 sc1 lds\n"
"v_mfma_f32_16x16x32_bf16 v[20:23], a[52:55], v[32:35], v[20:23]\n"
"v_mfma_f32_16x16x32_bf16 v[20:23], a[164:167], v[36:39], v[20:23]\n"
"ds_read_b128 v[60:63], v2 offset:50176\n"
"v_mfma_f32_16x16x32_bf16 v[16:19], a[160:163], v[36:39], v[16:19]\n"
"buffer_load_dwordx4 v2, s[32:35], s63 offen offset:1024 sc1 lds\n"
"s_waitcnt lgkmcnt(4)\n"
"v_mfma_f32_16x16x32_bf16 v[16:19], v[80:83], v[40:43], v[16:19]\n"
"ds_read_b128 v[32:35], v2 offset:8192\n"
"s_add_u32 s63, s63, 1175552\n"
"v_mfma_f32_16x16x32_bf16 v[20:23], v[84:87], v[40:43], v[20:23]\n"
"v_mfma_f32_16x16x32_bf16 v[20:23], v[196:199], v[44:47], v[20:23]\n"
"ds_read_b128 v[36:39], v2 offset:22528\n"
"s_waitcnt vmcnt(7)\n"
"v_mfma_f32_16x16x32_bf16 v[16:19], v[192:195], v[44:47], v[16:19]\n"
"v_cmp_ne_u64 vcc, v[8:9], v[6:7]\n"
"s_waitcnt lgkmcnt(4)\n"
"v_mfma_f32_16x16x32_bf16 v[16:19], a[56:59], v[48:51], v[16:19]\n"
"ds_read_b128 v[40:43], v2 offset:36864\n"
"s_cbranch_vccnz .L_rtb0\n"
".L_bkb0:\n"
"v_mfma_f32_16x16x32_bf16 v[20:23], a[60:63], v[48:51], v[20:23]\n"
"s_add_u32 s66, s66, 1187840\n"
"v_mfma_f32_16x16x32_bf16 v[20:23], a[172:175], v[52:55], v[20:23]\n"
"ds_read_b128 v[44:47], v2 offset:51200\n"
"s_add_u32 m0, s74, 0\n"
"v_mfma_f32_16x16x32_bf16 v[16:19], a[168:171], v[52:55], v[16:19]\n"
"buffer_load_dwordx4 v2, s[32:35], s71 offen offset:0 sc1 lds\n"
"s_waitcnt lgkmcnt(4)\n"
"v_mfma_f32_16x16x32_bf16 v[16:19], v[88:91], v[56:59], v[16:19]\n"
"ds_read_b128 v[48:51], v2 offset:9216\n"
"buffer_load_dwordx4 v2, s[32:35], s71 offen offset:1024 sc1 lds\n"
"v_mfma_f32_16x16x32_bf16 v[20:23], v[92:95], v[56:59], v[20:23]\n"
"s_add_u32 s71, s71, 1187840\n"
"v_mfma_f32_16x16x32_bf16 v[20:23], v[204:207], v[60:63], v[20:23]\n"
"ds_read_b128 v[52:55], v2 offset:23552\n"
"v_mfma_f32_16x16x32_bf16 v[16:19], v[200:203], v[60:63], v[16:19]\n"
"s_waitcnt lgkmcnt(4)\n"
"v_mfma_f32_16x16x32_bf16 v[16:19], a[64:67], v[32:35], v[16:19]\n"
"ds_read_b128 v[56:59], v2 offset:37888\n"
"v_mfma_f32_16x16x32_bf16 v[20:23], a[68:71], v[32:35], v[20:23]\n"
"v_mfma_f32_16x16x32_bf16 v[20:23], a[180:183], v[36:39], v[20:23]\n"
"ds_read_b128 v[60:63], v2 offset:52224\n"
"s_add_u32 m0, s64, 0\n"
"v_mfma_f32_16x16x32_bf16 v[16:19], a[176:179], v[36:39], v[16:19]\n"
"buffer_load_dwordx4 v2, s[32:35], s63 offen offset:0 sc1 lds\n"
"s_waitcnt lgkmcnt(4)\n"
"v_mfma_f32_16x16x32_bf16 v[16:19], v[96:99], v[40:43], v[16:19]\n"
"ds_read_b128 v[32:35], v2 offset:10240\n"
"v_mfma_f32_16x16x32_bf16 v[20:23], v[100:103], v[40:43], v[20:23]\n"
"v_mfma_f32_16x16x32_bf16 v[20:23], v[212:215], v[44:47], v[20:23]\n"
"ds_read_b128 v[36:39], v2 offset:24576\n"
"v_mfma_f32_16x16x32_bf16 v[16:19], v[208:211], v[44:47], v[16:19]\n"
"s_waitcnt lgkmcnt(4)\n"
"v_mfma_f32_16x16x32_bf16 v[16:19], a[72:75], v[48:51], v[16:19]\n"
"ds_read_b128 v[40:43], v2 offset:38912\n"
"buffer_load_dwordx4 v2, s[32:35], s63 offen offset:1024 sc1 lds\n"
"v_mfma_f32_16x16x32_bf16 v[20:23], a[76:79], v[48:51], v[20:23]\n"
"ds_read_b128 v[24:27], v5 offset:8192\n"
"v_mfma_f32_16x16x32_bf16 v[20:23], a[188:191], v[52:55], v[20:23]\n"
"ds_read_b128 v[44:47], v2 offset:53248\n"
"ds_read_b128 v[28:31], v5 offset:9216\n"
"v_mfma_f32_16x16x32_bf16 v[16:19], a[184:187], v[52:55], v[16:19]\n"
"s_waitcnt lgkmcnt(6)\n"
"v_mfma_f32_16x16x32_bf16 v[16:19], v[104:107], v[56:59], v[16:19]\n"
"ds_read_b128 v[48:51], v2 offset:11264\n"
"v_mfma_f32_16x16x32_bf16 v[20:23], v[108:111], v[56:59], v[20:23]\n"
"v_mfma_f32_16x16x32_bf16 v[20:23], v[220:223], v[60:63], v[20:23]\n"
"ds_read_b128 v[52:55], v2 offset:25600\n"
"buffer_load_dwordx4 v2, s[32:35], s63 offen offset:2048 sc1 lds\n"
"v_mfma_f32_16x16x32_bf16 v[16:19], v[216:219], v[60:63], v[16:19]\n"
"s_waitcnt lgkmcnt(6)\n"
"v_mfma_f32_16x16x32_bf16 v[16:19], a[80:83], v[32:35], v[16:19]\n"
"ds_read_b128 v[56:59], v2 offset:39936\n"
"v_mfma_f32_16x16x32_bf16 v[20:23], a[84:87], v[32:35], v[20:23]\n"
"v_mfma_f32_16x16x32_bf16 v[20:23], a[196:199], v[36:39], v[20:23]\n"
"ds_read_b128 v[60:63], v2 offset:54272\n"
"v_mfma_f32_16x16x32_bf16 v[16:19], a[192:195], v[36:39], v[16:19]\n"
"s_waitcnt lgkmcnt(5)\n"
"v_mfma_f32_16x16x32_bf16 v[16:19], v[112:115], v[40:43], v[16:19]\n"
"ds_read_b128 v[32:35], v2 offset:12288\n"
"buffer_load_dwordx4 v2, s[32:35], s63 offen offset:3072 sc1 lds\n"
"v_mfma_f32_16x16x32_bf16 v[20:23], v[116:119], v[40:43], v[20:23]\n"
"v_mfma_f32_16x16x32_bf16 v[20:23], v[228:231], v[44:47], v[20:23]\n"
"ds_read_b128 v[36:39], v2 offset:26624\n"
"v_mfma_f32_16x16x32_bf16 v[16:19], v[224:227], v[44:47], v[16:19]\n"
"s_waitcnt lgkmcnt(4)\n"
"v_mfma_f32_16x16x32_bf16 v[16:19], a[88:91], v[48:51], v[16:19]\n"
"ds_read_b128 v[40:43], v2 offset:40960\n"
"v_mfma_f32_16x16x32_bf16 v[20:23], a[92:95], v[48:51], v[20:23]\n"
"v_mfma_f32_16x16x32_bf16 v[20:23], a[204:207], v[52:55], v[20:23]\n"
"ds_read_b128 v[44:47], v2 offset:55296\n"
"s_add_u32 s63, s63, 4096\n"
"v_mfma_f32_16x16x32_bf16 v[16:19], a[200:203], v[52:55], v[16:19]\n"
"s_add_u32 m0, s64, 4096\n"
"s_waitcnt lgkmcnt(4)\n"
"v_mfma_f32_16x16x32_bf16 v[16:19], v[120:123], v[56:59], v[16:19]\n"
"ds_read_b128 v[48:51], v2 offset:13312\n"
"buffer_load_dwordx4 v2, s[32:35], s63 offen offset:0 sc1 lds\n"
"v_mfma_f32_16x16x32_bf16 v[20:23], v[124:127], v[56:59], v[20:23]\n"
"v_mfma_f32_16x16x32_bf16 v[20:23], v[236:239], v[60:63], v[20:23]\n"
"ds_read_b128 v[52:55], v2 offset:27648\n"
"v_mfma_f32_16x16x32_bf16 v[16:19], v[232:235], v[60:63], v[16:19]\n"
"s_waitcnt lgkmcnt(4)\n"
"v_mfma_f32_16x16x32_bf16 v[16:19], a[96:99], v[32:35], v[16:19]\n"
"ds_read_b128 v[56:59], v2 offset:41984\n"
"v_mfma_f32_16x16x32_bf16 v[20:23], a[100:103], v[32:35], v[20:23]\n"
"v_mfma_f32_16x16x32_bf16 v[20:23], a[212:215], v[36:39], v[20:23]\n"
"ds_read_b128 v[60:63], v2 offset:56320\n"
"v_mfma_f32_16x16x32_bf16 v[16:19], a[208:211], v[36:39], v[16:19]\n"
"s_waitcnt lgkmcnt(4)\n"
"v_mfma_f32_16x16x32_bf16 v[16:19], v[128:131], v[40:43], v[16:19]\n"
"s_barrier\n"
"ds_read_b128 v[32:35], v4 offset:0\n"
"v_mfma_f32_16x16x32_bf16 v[20:23], v[132:135], v[40:43], v[20:23]\n"
"v_mfma_f32_16x16x32_bf16 v[20:23], v[244:247], v[44:47], v[20:23]\n"
"ds_read_b128 v[36:39], v4 offset:14336\n"
"v_mfma_f32_16x16x32_bf16 v[16:19], v[240:243], v[44:47], v[16:19]\n"
"s_waitcnt lgkmcnt(4)\n"
"v_mfma_f32_16x16x32_bf16 v[16:19], a[104:107], v[48:51], v[16:19]\n"
"ds_read_b128 v[40:43], v4 offset:28672\n"
"v_mfma_f32_16x16x32_bf16 v[20:23], a[108:111], v[48:51], v[20:23]\n"
"v_mfma_f32_16x16x32_bf16 v[20:23], a[220:223], v[52:55], v[20:23]\n"
"ds_read_b128 v[44:47], v4 offset:43008\n"
"v_mfma_f32_16x16x32_bf16 v[16:19], a[216:219], v[52:55], v[16:19]\n"
"s_waitcnt lgkmcnt(4)\n"
"v_mfma_f32_16x16x32_bf16 v[16:19], v[136:139], v[56:59], v[16:19]\n"
"ds_read_b128 v[48:51], v4 offset:1024\n"
"v_mfma_f32_16x16x32_bf16 v[20:23], v[140:143], v[56:59], v[20:23]\n"
"v_mfma_f32_16x16x32_bf16 v[20:23], v[252:255], v[60:63], v[20:23]\n"
"ds_read_b128 v[52:55], v4 offset:15360\n"
"v_mfma_f32_16x16x32_bf16 v[16:19], v[248:251], v[60:63], v[16:19]\n"
"s_branch .L_dbl\n"
".L_dbt:\n"
"s_nop 1\n"
"s_nop 4\n"
"v_cvt_pk_bf16_f32 v12, v24, v25\n"
"v_cvt_pk_bf16_f32 v13, v26, v27\n"
"v_cvt_pk_bf16_f32 v14, v28, v29\n"
"v_cvt_pk_bf16_f32 v15, v30, v31\n"
"buffer_store_dwordx4 v[12:15], v1, s[44:47], s65 offen\n"
"s_add_u32 s65, s65, 32768\n"
"s_or_b32 s48, s28, s27\n"
"s_or_b32 s48, s48, s26\n"
"s_cbranch_scc1 .L_end\n"
"v_mov_b32 v8, s54\n"
"v_mov_b32 v9, s55\n"
"buffer_store_dwordx2 v[8:9], off, s[32:35], 0 sc1\n"
"s_branch .L_end\n"
".L_rtg0:\n"
"s_mov_b32 s48, 20000\n"
".L_rlg0:\n"
"s_sleep 8\n"
".L_rfg0:\n"
"buffer_load_dwordx2 v[12:13], v14, s[32:35], s66 offen sc1\n"
"s_sub_u32 s48, s48, 1\n"
"s_cbranch_scc1 .L_end\n"
"s_waitcnt vmcnt(0)\n"
"v_cmp_ne_u64 vcc, v[16:17], v[12:13]\n"
"s_cbranch_vccnz .L_rlg0\n"
"s_branch .L_bkg0\n"
".L_rtg1:\n"
"s_mov_b32 s48, 20000\n"
".L_rlg1:\n"
"s_sleep 8\n"
".L_rfg1:\n"
"buffer_load_dwordx2 v[12:13], v14, s[32:35], s66 offen sc1\n"
"s_sub_u32 s48, s48, 1\n"
"s_cbranch_scc1 .L_end\n"
"s_waitcnt vmcnt(0)\n"
"v_cmp_ne_u64 vcc, v[16:17], v[12:13]\n"
"s_cbranch_vccnz .L_rlg1\n"
"s_branch .L_bkg1\n"
".L_rtqa:\n"
"s_mov_b32 s48, 20000\n"
"s_branch .L_rfqa\n"
".L_rlqa:\n"
"s_sleep 8\n"
".L_rfqa:\n"
"buffer_load_dwordx2 v[12:13], v0, s[32:35], s66 offen sc1\n"
"s_sub_u32 s48, s48, 1\n"
"s_cbranch_scc1 .L_end\n"
"s_waitcnt vmcnt(0)\n"
"v_cmp_ne_u64 vcc, v[30:31], v[12:13]\n"
"s_cbranch_vccnz .L_rlqa\n"
"s_branch .L_bkqa\n"
".L_rtqb:\n"
"s_mov_b32 s48, 20000\n"
"s_branch .L_rfqb\n"
".L_rlqb:\n"
"s_sleep 8\n"
".L_rfqb:\n"
"buffer_load_dwordx2 v[12:13], v0, s[32:35], s66 offen sc1\n"
"s_sub_u32 s48, s48, 1\n"
"s_cbranch_scc1 .L_end\n"
"s_waitcnt vmcnt(0)\n"
"v_cmp_ne_u64 vcc, v[30:31], v[12:13]\n"
"s_cbranch_vccnz .L_rlqb\n"
"s_branch .L_bkqb\n"
".L_rtq0:\n"
"s_mov_b32 s48, 20000\n"
".L_rlq0:\n"
"s_sleep 8\n"
".L_rfq0:\n"
"buffer_load_dwordx2 v[12:13], v0, s[32:35], s66 offen sc1\n"
"s_sub_u32 s48, s48, 1\n"
"s_cbranch_scc1 .L_end\n"
"s_waitcnt vmcnt(0)\n"
"v_cmp_ne_u64 vcc, v[30:31], v[12:13]\n"
"s_cbranch_vccnz .L_rlq0\n"
"s_branch .L_bkq0\n"
".L_rtq1:\n"
"s_mov_b32 s48, 20000\n"
".L_rlq1:\n"
"s_sleep 8\n"
".L_rfq1:\n"
"buffer_load_dwordx2 v[12:13], v0, s[32:35], s66 offen sc1\n"
"s_sub_u32 s48, s48, 1\n"
"s_cbranch_scc1 .L_end\n"
"s_waitcnt vmcnt(0)\n"
"v_cmp_ne_u64 vcc, v[30:31], v[12:13]\n"
"s_cbranch_vccnz .L_rlq1\n"
"s_branch .L_bkq1\n"
".L_qs0:\n"
"s_cmp_lg_u32 s62, 30\n"
"s_cbranch_scc1 .L_qn4\n"
"v_mov_b32 v120, 0xff61b1e6\n"
"v_mov_b32 v121, 0xff61b1e6\n"
"v_mov_b32 v122, 0xff61b1e6\n"
"v_mov_b32 v123, 0xff61b1e6\n"
"v_cmp_ne_u32 vcc, 0xffff, v126\n"
"s_nop 1\n"
"v_cndmask_b32 v120, 0, v120, vcc\n"
"v_cmp_ne_u32 vcc, 0xffff0000, v126\n"
"s_nop 1\n"
"v_cndmask_b32 v121, 0, v121, vcc\n"
"v_cmp_ne_u32 vcc, 0xffff, v127\n"
"s_nop 1\n"
"v_cndmask_b32 v122, 0, v122, vcc\n"
"v_cmp_ne_u32 vcc, 0xffff0000, v127\n"
"s_nop 1\n"
"v_cndmask_b32 v123, 0, v123, vcc\n"
"s_nop 1\n"
"s_branch .L_qb0\n"
".L_qn4:\n"
"s_cmp_lg_u32 s62, 17\n"
"s_cbranch_scc1 .L_qn30\n"
"s_mov_b32 s72, s69\n"
"s_nop 1\n"
"s_branch .L_qb0\n"
".L_qn30:\n"
"s_cmp_lg_u32 s62, 16\n"
"s_cbranch_scc1 .L_qn32\n"
"s_mov_b32 s72, 1187840\n"
"s_nop 1\n"
"s_branch .L_qb0\n"
".L_qn32:\n"
"s_cmp_lg_u32 s62, 15\n"
"s_cbranch_scc1 .L_qn34\n"
"s_sub_u32 s68, s68, 39198720\n"
"s_nop 1\n"
"s_branch .L_qb0\n"
".L_qn34:\n"
"s_cmp_lg_u32 s62, 14\n"
"s_cbranch_scc1 .L_qn36\n"
"v_mov_b32 v120, 0xff61b1e6\n"
"v_mov_b32 v121, 0xff61b1e6\n"
"v_mov_b32 v122, 0xff61b1e6\n"
"v_mov_b32 v123, 0xff61b1e6\n"
"v_cmp_ne_u32 vcc, 0xffff, v126\n"
"s_nop 1\n"
"v_cndmask_b32 v120, 0, v120, vcc\n"
"v_cmp_ne_u32 vcc, 0xffff0000, v126\n"
"s_nop 1\n"
"v_cndmask_b32 v121, 0, v121, vcc\n"
"v_cmp_ne_u32 vcc, 0xffff, v127\n"
"s_nop 1\n"
"v_cndmask_b32 v122, 0, v122, vcc\n"
"v_cmp_ne_u32 vcc, 0xffff0000, v127\n"
"s_nop 1\n"
"v_cndmask_b32 v123, 0, v123, vcc\n"
"s_nop 1\n"
"s_branch .L_qb0\n"
".L_qn36:\n"
"s_branch .L_qb0\n"
".L_qs1:\n"
"s_cmp_lg_u32 s62, 31\n"
"s_cbranch_scc1 .L_qn3\n"
"s_cmp_lg_u32 s28, 0\n"
"s_cbranch_scc1 .L_qb1\n"
"v_mov_b32 v120, 0xff61b1e6\n"
"v_mov_b32 v121, 0xff61b1e6\n"
"v_mov_b32 v122, 0xff61b1e6\n"
"v_mov_b32 v123, 0xff61b1e6\n"
"s_nop 1\n"
"s_branch .L_qb1\n"
".L_qn3:\n"
"s_cmp_lg_u32 s62, 17\n"
"s_cbranch_scc1 .L_qn31\n"
"s_mov_b32 s72, s70\n"
"s_nop 1\n"
"s_branch .L_qb1\n"
".L_qn31:\n"
"s_cmp_lg_u32 s62, 15\n"
"s_cbranch_scc1 .L_qn35\n"
"s_add_u32 s68, s68, 38010880\n"
"s_cmp_lg_u32 s28, 0\n"
"s_cbranch_scc1 .L_qb1\n"
"v_mov_b32 v120, 0xff61b1e6\n"
"v_mov_b32 v121, 0xff61b1e6\n"
"v_mov_b32 v122, 0xff61b1e6\n"
"v_mov_b32 v123, 0xff61b1e6\n"
"s_nop 1\n"
"s_branch .L_qb1\n"
".L_qn35:\n"
"s_branch .L_qb1\n"
".L_rtw0:\n"
"s_mov_b32 s48, 20000\n"
".L_rlw0:\n"
"s_sleep 8\n"
".L_rfw0:\n"
"buffer_load_dwordx2 v[12:13], v14, s[32:35], s66 offen sc1\n"
"s_sub_u32 s48, s48, 1\n"
"s_cbranch_scc1 .L_end\n"
"s_waitcnt vmcnt(0)\n"
"v_cmp_ne_u64 vcc, v[16:17], v[12:13]\n"
"s_cbranch_vccnz .L_rlw0\n"
"s_branch .L_bkw0\n"
".L_rth0:\n"
"s_mov_b32 s48, 20000\n"
".L_rlh0:\n"
"s_sleep 8\n"
".L_rfh0:\n"
"buffer_load_dwordx2 v[12:13], v14, s[32:35], s66 offen sc1\n"
"s_sub_u32 s48, s48, 1\n"
"s_cbranch_scc1 .L_end\n"
"s_waitcnt vmcnt(0)\n"
"v_cmp_ne_u64 vcc, v[16:17], v[12:13]\n"
"s_cbranch_vccnz .L_rlh0\n"
"s_branch .L_bkh0\n"
".L_rth1:\n"
"s_mov_b32 s48, 20000\n"
".L_rlh1:\n"
"s_sleep 8\n"
".L_rfh1:\n"
"buffer_load_dwordx2 v[12:13], v14, s[32:35], s66 offen sc1\n"
"s_sub_u32 s48, s48, 1\n"
"s_cbranch_scc1 .L_end\n"
"s_waitcnt vmcnt(0)\n"
"v_cmp_ne_u64 vcc, v[16:17], v[12:13]\n"
"s_cbranch_vccnz .L_rlh1\n"
"s_branch .L_bkh1\n"
".L_rtl0:\n"
"s_mov_b32 s48, 20000\n"
".L_rll0:\n"
"s_sleep 8\n"
".L_rfl0:\n"
"buffer_load_dwordx2 v[6:7], v0, s[32:35], s66 offen sc1\n"
"s_sub_u32 s48, s48, 1\n"
"s_cbranch_scc1 .L_end\n"
"s_waitcnt vmcnt(0)\n"
"v_cmp_ne_u64 vcc, v[8:9], v[6:7]\n"
"s_cbranch_vccnz .L_rll0\n"
"s_branch .L_bkl0\n"
".L_rtl1:\n"
"s_mov_b32 s48, 20000\n"
".L_rll1:\n"
"s_sleep 8\n"
".L_rfl1:\n"
"buffer_load_dwordx2 v[6:7], v0, s[32:35], s66 offen sc1\n"
"s_sub_u32 s48, s48, 1\n"
"s_cbranch_scc1 .L_end\n"
"s_waitcnt vmcnt(0)\n"
"v_cmp_ne_u64 vcc, v[8:9], v[6:7]\n"
"s_cbranch_vccnz .L_rll1\n"
"s_branch .L_bkl1\n"
".L_rta0:\n"
"s_mov_b32 s48, 20000\n"
".L_rla0:\n"
"s_sleep 8\n"
".L_rfa0:\n"
"buffer_load_dwordx2 v[6:7], v0, s[32:35], s66 offen sc1\n"
"s_sub_u32 s48, s48, 1\n"
"s_cbranch_scc1 .L_end\n"
"s_waitcnt vmcnt(0)\n"
"v_cmp_ne_u64 vcc, v[8:9], v[6:7]\n"
"s_cbranch_vccnz .L_rla0\n"
"s_branch .L_bka0\n"
".L_rta1:\n"
"s_mov_b32 s48, 20000\n"
".L_rla1:\n"
"s_sleep 8\n"
".L_rfa1:\n"
"buffer_load_dwordx2 v[6:7], v0, s[32:35], s66 offen sc1\n"
"s_sub_u32 s48, s48, 1\n"
"s_cbranch_scc1 .L_end\n"
"s_waitcnt vmcnt(0)\n"
"v_cmp_ne_u64 vcc, v[8:9], v[6:7]\n"
"s_cbranch_vccnz .L_rla1\n"
"s_branch .L_bka1\n"
".L_rtb1:\n"
"s_mov_b32 s48, 20000\n"
".L_rlb1:\n"
"s_sleep 8\n"
".L_rfb1:\n"
"buffer_load_dwordx2 v[6:7], v0, s[32:35], s66 offen sc1\n"
"s_sub_u32 s48, s48, 1\n"
"s_cbranch_scc1 .L_end\n"
"s_waitcnt vmcnt(0)\n"
"v_cmp_ne_u64 vcc, v[8:9], v[6:7]\n"
"s_cbranch_vccnz .L_rlb1\n"
"s_branch .L_bkb1\n"
".L_rtb0:\n"
"s_mov_b32 s48, 20000\n"
".L_rlb0:\n"
"s_sleep 8\n"
".L_rfb0:\n"
"buffer_load_dwordx2 v[6:7], v0, s[32:35], s66 offen sc1\n"
"s_sub_u32 s48, s48, 1\n"
"s_cbranch_scc1 .L_end\n"
"s_waitcnt vmcnt(0)\n"
"v_cmp_ne_u64 vcc, v[8:9], v[6:7]\n"
"s_cbranch_vccnz .L_rlb0\n"
"s_branch .L_bkb0\n"
".L_end:\n"
"s_waitcnt vmcnt(0) lgkmcnt(0)\n"
    : "+{v0}"(tid)
    : "{s[8:9]}"(x), "{s[10:11]}"(wq), "{s[12:13]}"(wo), "{s[14:15]}"(wg), "{s[16:17]}"(wu), "{s[18:19]}"(wd),
      "{s[20:21]}"(out), "{s[22:23]}"(ws), "{s24}"(blockIdx.x)
    : "v1", "v2", "v3", "v4", "v5", "v6", "v7", "v8", "v9", "v10", "v11", "v12", "v13", "v14", "v15", "v16", "v17", "v18", "v19", "v20", "v21", "v22", "v23", "v24", "v25", "v26", "v27", "v28", "v29", "v30", "v31", "v32", "v33", "v34", "v35", "v36", "v37", "v38", "v39", "v40", "v41", "v42", "v43", "v44", "v45", "v46", "v47", "v48", "v49", "v50", "v51", "v52", "v53", "v54", "v55", "v56", "v57", "v58", "v59", "v60", "v61", "v62", "v63", "v64", "v65", "v66", "v67", "v68", "v69", "v70", "v71", "v72", "v73", "v74", "v75", "v76", "v77", "v78", "v79", "v80", "v81", "v82", "v83", "v84", "v85", "v86", "v87", "v88", "v89", "v90", "v91", "v92", "v93", "v94", "v95", "v96", "v97", "v98", "v99", "v100", "v101", "v102", "v103", "v104", "v105", "v106", "v107", "v108", "v109", "v110", "v111", "v112", "v113", "v114", "v115", "v116", "v117", "v118", "v119", "v120", "v121", "v122", "v123", "v124", "v125", "v126", "v127", "v128", "v129", "v130", "v131", "v132", "v133", "v134", "v135", "v136", "v137", "v138", "v139", "v140", "v141", "v142", "v143", "v144", "v145", "v146", "v147", "v148", "v149", "v150", "v151", "v152", "v153", "v154", "v155", "v156", "v157", "v158", "v159", "v160", "v161", "v162", "v163", "v164", "v165", "v166", "v167", "v168", "v169", "v170", "v171", "v172", "v173", "v174", "v175", "v176", "v177", "v178", "v179", "v180", "v181", "v182", "v183", "v184", "v185", "v186", "v187", "v188", "v189", "v190", "v191", "v192", "v193", "v194", "v195", "v196", "v197", "v198", "v199", "v200", "v201", "v202", "v203", "v204", "v205", "v206", "v207", "v208", "v209", "v210", "v211", "v212", "v213", "v214", "v215", "v216", "v217", "v218", "v219", "v220", "v221", "v222", "v223", "v224", "v225", "v226", "v227", "v228", "v229", "v230", "v231", "v232", "v233", "v234", "v235", "v236", "v237", "v238", "v239", "v240", "v241", "v242", "v243", "v244", "v245", "v246", "v247", "v248", "v249", "v250", "v251", "v252", "v253", "v254", "v255", "a0", "a1", "a2", "a3", "a4", "a5", "a6", "a7", "a8", "a9", "a10", "a11", "a12", "a13", "a14", "a15", "a16", "a17", "a18", "a19", "a20", "a21", "a22", "a23", "a24", "a25", "a26", "a27", "a28", "a29", "a30", "a31", "a32", "a33", "a34", "a35", "a36", "a37", "a38", "a39", "a40", "a41", "a42", "a43", "a44", "a45", "a46", "a47", "a48", "a49", "a50", "a51", "a52", "a53", "a54", "a55", "a56", "a57", "a58", "a59", "a60", "a61", "a62", "a63", "a64", "a65", "a66", "a67", "a68", "a69", "a70", "a71", "a72", "a73", "a74", "a75", "a76", "a77", "a78", "a79", "a80", "a81", "a82", "a83", "a84", "a85", "a86", "a87", "a88", "a89", "a90", "a91", "a92", "a93", "a94", "a95", "a96", "a97", "a98", "a99", "a100", "a101", "a102", "a103", "a104", "a105", "a106", "a107", "a108", "a109", "a110", "a111", "a112", "a113", "a114", "a115", "a116", "a117", "a118", "a119", "a120", "a121", "a122", "a123", "a124", "a125", "a126", "a127", "a128", "a129", "a130", "a131", "a132", "a133", "a134", "a135", "a136", "a137", "a138", "a139", "a140", "a141", "a142", "a143", "a144", "a145", "a146", "a147", "a148", "a149", "a150", "a151", "a152", "a153", "a154", "a155", "a156", "a157", "a158", "a159", "a160", "a161", "a162", "a163", "a164", "a165", "a166", "a167", "a168", "a169", "a170", "a171", "a172", "a173", "a174", "a175", "a176", "a177", "a178", "a179", "a180", "a181", "a182", "a183", "a184", "a185", "a186", "a187", "a188", "a189", "a190", "a191", "a192", "a193", "a194", "a195", "a196", "a197", "a198", "a199", "a200", "a201", "a202", "a203", "a204", "a205", "a206", "a207", "a208", "a209", "a210", "a211", "a212", "a213", "a214", "a215", "a216", "a217", "a218", "a219", "a220", "a221", "a222", "a223", "a224", "a225", "a226", "a227", "a228", "a229", "a230", "a231", "a232", "a233", "a234", "a235", "a236", "a237", "a238", "a239", "a240", "a241", "a242", "a243", "a244", "a245", "a246", "a247", "a248", "a249", "a250", "a251", "a252", "a253", "a254", "a255", "s25", "s26", "s27", "s28", "s29", "s30", "s31", "s32", "s33", "s34", "s35", "s36", "s37", "s38", "s39", "s40", "s41", "s42", "s43", "s44", "s45", "s46", "s47", "s48", "s49", "s50", "s51", "s52", "s53", "s54", "s55", "s56", "s57", "s58", "s59", "s60", "s61", "s62", "s63", "s64", "s65", "s66", "s67", "s68", "s69", "s70", "s71", "s72", "s73", "s74", "s75", "s76", "s77", "s78", "s79", "s80", "s81", "s82", "s83", "s84", "s85", "s86", "s87", "s88", "s89", "s90", "s91", "s92", "s93", "s94", "s95", "s96", "s97", "s98", "s99", "vcc", "scc", "m0", "memory");
}

extern "C" void kernel_launch(void* const* d_in, const int* in_sizes, int n_in, void* d_out, int out_size,
                              void* d_ws, size_t ws_size, hipStream_t stream) {
    constexpr int smem = 163840;
    (void)hipFuncSetAttribute((const void*)body_k, hipFuncAttributeMaxDynamicSharedMemorySize, smem);
    body_k<<<256, 256, smem, stream>>>(d_in[0], d_in[1], d_in[2], d_in[4], d_in[5], d_in[6], d_out, d_ws);
}
```

```cpp
#include <hip/hip_runtime.h>
#pragma clang diagnostic ignored "-Winline-asm"
#define W_XI 4612096
#define W_O 4648960
#define W_XA 4681728
#define W_H 4718592
#define W_P 4833280
#define W_EB 296960
#define W_MAP 1
extern "C" __global__ __launch_bounds__(256, 1) void body_k(const void* x, const void* wq, const void* wo, const void* wg, const void* wu,
                                                            const void* wd, void* out, void* ws) {
    unsigned tid = threadIdx.x;
    asm volatile(
"s_load_dwordx2 s[52:53], s[22:23], 0x0\n"
"v_lshrrev_b32 v3, 6, v0\n"
"v_and_b32 v1, 63, v0\n"
"v_readfirstlane_b32 s26, v3\n"
"v_lshlrev_b32 v2, 4, v1\n"
"s_add_u32 s32, s22, 0x0\n"
"s_addc_u32 s33, s23, 0x0\n"
"s_and_b32 s33, s33, 0xffff\n"
"s_mov_b32 s34, 0x504c000\n"
"s_mov_b32 s35, 0x20000\n"
"s_mov_b32 s73, 0\n"
"s_lshr_b32 s49, s24, 3\n"
"s_cmp_lt_u32 s49, 16\n"
"s_cbranch_scc1 .L_nc\n"
"s_bfe_u32 s28, s24, 0x20001\n"
"s_and_b32 s27, s49, 7\n"
"s_bfe_u32 s29, s49, 0x10003\n"
"s_mov_b32 s62, 0\n"
"s_mov_b32 s74, 1\n"
"s_bitcmp1_b32 s24, 0\n"
"s_cbranch_scc0 .L_nt\n"
"s_getpc_b64 s[40:41]\n"
".L_ct16:\n"
"s_and_b32 s41, s41, 0xffff\n"
"s_mov_b32 s42, .L_last-.L_ct16\n"
"s_mov_b32 s43, 0x20000\n"
"s_sub_u32 s48, s49, 16\n"
"s_lshl_b32 s48, s48, 8\n"
"v_add_lshl_u32 v5, v0, s48, 7\n"
"buffer_load_dword v10, v5, s[40:43], 0 offen\n"
"s_add_u32 s36, s10, 0x0\n"
"s_addc_u32 s37, s11, 0x0\n"
"s_and_b32 s37, s37, 0xffff\n"
"s_mov_b32 s38, 0x600000\n"
"s_mov_b32 s39, 0x20000\n"
"s_lshl_b32 s48, s49, 8\n"
"s_sub_u32 s48, s48, 4096\n"
"v_add_u32 v4, s48, v0\n"
"s_lshl_b32 s48, s28, 9\n"
"v_mul_u32_u24 v5, 43691, v4\n"
"v_lshrrev_b32 v5, 19, v5\n"
"v_mul_u32_u24 v6, 12, v5\n"
"v_sub_u32 v6, v4, v6\n"
"v_mul_u32_u24 v5, 48, v5\n"
"v_lshrrev_b32 v7, 2, v6\n"
"v_mul_u32_u24 v7, 12, v7\n"
"v_add3_u32 v5, v5, v6, v7\n"
"v_lshlrev_b32 v5, 7, v5\n"
"v_add_u32 v4, 4096, v4\n"
"buffer_load_dword v10, v5, s[36:39], s48 offen\n"
"v_mul_u32_u24 v5, 43691, v4\n"
"v_lshrrev_b32 v5, 19, v5\n"
"v_mul_u32_u24 v6, 12, v5\n"
"v_sub_u32 v6, v4, v6\n"
"v_mul_u32_u24 v5, 48, v5\n"
"v_lshrrev_b32 v7, 2, v6\n"
"v_mul_u32_u24 v7, 12, v7\n"
"v_add3_u32 v5, v5, v6, v7\n"
"v_lshlrev_b32 v5, 7, v5\n"
"v_add_u32 v4, 4096, v4\n"
"buffer_load_dword v10, v5, s[36:39], s48 offen\n"
"v_mul_u32_u24 v5, 43691, v4\n"
"v_lshrrev_b32 v5, 19, v5\n"
"v_mul_u32_u24 v6, 12, v5\n"
"v_sub_u32 v6, v4, v6\n"
"v_mul_u32_u24 v5, 48, v5\n"
"v_lshrrev_b32 v7, 2, v6\n"
"v_mul_u32_u24 v7, 12, v7\n"
"v_add3_u32 v5, v5, v6, v7\n"
"v_lshlrev_b32 v5, 7, v5\n"
"v_add_u32 v4, 4096, v4\n"
"buffer_load_dword v10, v5, s[36:39], s48 offen\n"
"s_mov_b32 s62, 1\n"
"s_mov_b32 s74, 4\n"
".L_nt:\n"
"s_lshl2_add_u32 s50, s27, s26\n"
"s_mul_i32 s70, s50, 1152\n"
"v_and_b32 v20, 15, v1\n"
"v_lshlrev_b32 v20, 11, v20\n"
"v_and_b32 v21, 48, v1\n"
"s_lshl_b32 s48, s50, 6\n"
"v_add3_u32 v20, v20, v21, s48\n"
"s_add_u32 s36, s8, 0x0\n"
"s_addc_u32 s37, s9, 0x0\n"
"s_and_b32 s37, s37, 0xffff\n"
"s_mov_b32 s38, 0x400000\n"
"s_mov_b32 s39, 0x20000\n"
"s_lshl2_add_u32 s48, s28, s62\n"
"s_lshl_b32 s48, s48, 3\n"
"s_sub_u32 s48, s48, s29\n"
"s_add_u32 s48, s48, 127\n"
"s_lshl_b32 s66, s27, 7\n"
"s_add_u32 s66, s66, 286720\n"
"s_sub_u32 s66, s66, s70\n"
"s_add_u32 s49, s28, 3\n"
"s_and_b32 s49, s49, 3\n"
"s_mul_i32 s49, s49, 296960\n"
"s_add_u32 s69, s70, s49\n"
"s_add_u32 s69, s69, 41435136\n"
"s_mul_i32 s49, s28, 296960\n"
"s_add_u32 s70, s70, s49\n"
"s_and_b32 s51, s50, 6\n"
"s_lshl_b32 s51, s51, 1\n"
"v_add_u32 v3, s51, v1\n"
"v_bfi_b32 v3, 15, v3, v1\n"
"v_lshlrev_b32 v3, 2, v3\n"
"v_cmp_gt_u32 vcc, 32, v1\n"
"v_mov_b32 v21, 0x7fff0000\n"
"s_nop 0\n"
"v_cndmask_b32 v21, v21, v3, vcc\n"
"s_mul_i32 s49, s29, 1187840\n"
"s_sub_u32 s71, s70, s49\n"
"s_add_u32 s71, s71, 4612096\n"
"v_mul_u32_u24 v24, 2375680, v1\n"
"v_add_u32 v24, s71, v24\n"
"s_cmp_eq_u32 s29, 0\n"
"s_cselect_b32 s68, s71, s69\n"
"v_writelane_b32 v24, s68, 0\n"
"s_mul_i32 s49, s62, 9502720\n"
"s_add_u32 s71, s71, s49\n"
"s_cmp_eq_u32 s62, 0\n"
"s_cselect_b32 s68, s68, s71\n"
"s_add_u32 s71, s71, 2375680\n"
"v_lshrrev_b32 v22, 2, v0\n"
"v_mov_b32 v23, 0x3f803f80\n"
".L_dc:\n"
"s_and_b32 s49, s48, 127\n"
"s_lshl_b32 s49, s49, 15\n"
"s_add_u32 s48, s48, 2\n"
"buffer_load_dwordx4 v[32:35], v20, s[36:39], s49 offen\n"
"s_and_b32 s49, s48, 127\n"
"s_lshl_b32 s49, s49, 15\n"
"s_add_u32 s48, s48, 2\n"
"buffer_load_dwordx4 v[36:39], v20, s[36:39], s49 offen\n"
"s_and_b32 s49, s48, 127\n"
"s_lshl_b32 s49, s49, 15\n"
"s_add_u32 s48, s48, 2\n"
"buffer_load_dwordx4 v[40:43], v20, s[36:39], s49 offen\n"
"s_and_b32 s49, s48, 127\n"
"s_lshl_b32 s49, s49, 15\n"
"s_add_u32 s48, s48, 2\n"
"buffer_load_dwordx4 v[44:47], v20, s[36:39], s49 offen\n"
"v_cmp_eq_u32 vcc, s62, v22\n"
"v_mov_b32 v25, 0x7fff0000\n"
"s_add_u32 s62, s62, 1\n"
"v_cndmask_b32 v25, v25, v24, vcc\n"
"s_waitcnt lgkmcnt(0)\n"
"s_add_u32 s54, s52, 0x7f4a7c15\n"
"s_addc_u32 s55, s53, 0x9e3779b9\n"
"v_mov_b32 v8, s54\n"
"v_mov_b32 v9, s55\n"
"s_waitcnt vmcnt(0)\n"
"v_dot2_f32_bf16 v48, v32, v23, 0\n"
"v_dot2_f32_bf16 v49, v32, v32, 0\n"
"v_dot2_f32_bf16 v50, v36, v23, 0\n"
"v_dot2_f32_bf16 v51, v36, v36, 0\n"
"v_dot2_f32_bf16 v52, v40, v23, 0\n"
"v_dot2_f32_bf16 v53, v40, v40, 0\n"
"v_dot2_f32_bf16 v54, v44, v23, 0\n"
"v_dot2_f32_bf16 v55, v44, v44, 0\n"
"v_dot2_f32_bf16 v48, v33, v23, v48\n"
"v_dot2_f32_bf16 v49, v33, v33, v49\n"
"v_dot2_f32_bf16 v50, v37, v23, v50\n"
"v_dot2_f32_bf16 v51, v37, v37, v51\n"
"v_dot2_f32_bf16 v52, v41, v23, v52\n"
"v_dot2_f32_bf16 v53, v41, v41, v53\n"
"v_dot2_f32_bf16 v54, v45, v23, v54\n"
"v_dot2_f32_bf16 v55, v45, v45, v55\n"
"v_dot2_f32_bf16 v48, v34, v23, v48\n"
"v_dot2_f32_bf16 v49, v34, v34, v49\n"
"v_dot2_f32_bf16 v50, v38, v23, v50\n"
"v_dot2_f32_bf16 v51, v38, v38, v51\n"
"v_dot2_f32_bf16 v52, v42, v23, v52\n"
"v_dot2_f32_bf16 v53, v42, v42, v53\n"
"v_dot2_f32_bf16 v54, v46, v23, v54\n"
"v_dot2_f32_bf16 v55, v46, v46, v55\n"
"v_dot2_f32_bf16 v48, v35, v23, v48\n"
"v_dot2_f32_bf16 v49, v35, v35, v49\n"
"v_dot2_f32_bf16 v50, v39, v23, v50\n"
"v_dot2_f32_bf16 v51, v39, v39, v51\n"
"v_dot2_f32_bf16 v52, v43, v23, v52\n"
"v_dot2_f32_bf16 v53, v43, v43, v53\n"
"v_dot2_f32_bf16 v54, v47, v23, v54\n"
"v_dot2_f32_bf16 v55, v47, v47, v55\n"
"v_permlane16_swap_b32 v48, v49\n"
"v_permlane16_swap_b32 v50, v51\n"
"v_permlane16_swap_b32 v52, v53\n"
"v_permlane16_swap_b32 v54, v55\n"
"v_add_f32 v48, v48, v49\n"
"v_add_f32 v50, v50, v51\n"
"v_add_f32 v52, v52, v53\n"
"v_add_f32 v54, v54, v55\n"
"v_permlane32_swap_b32 v48, v49\n"
"v_permlane32_swap_b32 v50, v51\n"
"v_permlane32_swap_b32 v52, v53\n"
"v_permlane32_swap_b32 v54, v55\n"
"v_add_f32 v48, v48, v49\n"
"v_add_f32 v50, v50, v51\n"
"v_add_f32 v52, v52, v53\n"
"v_add_f32 v54, v54, v55\n"
"buffer_store_dwordx4 v[32:35], v2, s[32:35], s68 offen sc1\n"
"buffer_store_dword v48, v21, s[32:35], s68 offen offset:1024 sc1\n"
"s_mov_b32 s68, s71\n"
"s_add_u32 s71, s71, 2375680\n"
"buffer_store_dwordx4 v[36:39], v2, s[32:35], s68 offen sc1\n"
"buffer_store_dword v50, v21, s[32:35], s68 offen offset:1024 sc1\n"
"s_mov_b32 s68, s71\n"
"s_add_u32 s71, s71, 2375680\n"
"buffer_store_dwordx4 v[40:43], v2, s[32:35], s68 offen sc1\n"
"buffer_store_dword v52, v21, s[32:35], s68 offen offset:1024 sc1\n"
"s_mov_b32 s68, s71\n"
"s_add_u32 s71, s71, 2375680\n"
"buffer_store_dwordx4 v[44:47], v2, s[32:35], s68 offen sc1\n"
"buffer_store_dword v54, v21, s[32:35], s68 offen offset:1024 sc1\n"
"s_mov_b32 s68, s71\n"
"s_add_u32 s71, s71, 2375680\n"
"s_waitcnt vmcnt(0)\n"
"s_barrier\n"
"buffer_store_dwordx2 v[8:9], v25, s[32:35], s66 offen sc1\n"
"s_cmp_lt_u32 s62, s74\n"
"s_cbranch_scc1 .L_dc\n"
"s_bitcmp1_b32 s24, 0\n"
"s_cbranch_scc1 .L_d\n"
"s_lshr_b32 s49, s24, 3\n"
".L_nc:\n"
"s_bfe_u32 s50, s24, 0x20001\n"
"s_and_b32 s27, s49, 3\n"
"s_lshl2_add_u32 s27, s50, s27\n"
"s_lshr_b32 s28, s49, 2\n"
"s_bitcmp1_b32 s24, 0\n"
"s_cbranch_scc1 .L_q\n"
"s_and_b32 s27, s49, 1\n"
"s_lshl1_add_u32 s27, s50, s27\n"
"s_lshr_b32 s28, s49, 1\n"
"s_cmp_lt_u32 s49, 4\n"
"s_cbranch_scc1 .L_w\n"
"s_cmp_ge_u32 s49, 16\n"
"s_cbranch_scc1 .L_ns\n"
"s_getpc_b64 s[40:41]\n"
".L_ct5:\n"
"s_and_b32 s41, s41, 0xffff\n"
"s_mov_b32 s42, .L_last-.L_ct5\n"
"s_mov_b32 s43, 0x20000\n"
"s_sub_u32 s48, s49, 5\n"
"s_lshl_b32 s48, s48, 8\n"
"v_add_lshl_u32 v5, v0, s48, 7\n"
"buffer_load_dword v120, v5, s[40:43], 0 offen\n"
"s_lshl2_add_u32 s48, s49, s50\n"
"s_lshl_b32 s48, s48, 8\n"
"s_sub_u32 s48, s48, 4096\n"
"v_add_u32 v4, s48, v0\n"
"v_mul_u32_u24 v5, 52429, v4\n"
"v_lshrrev_b32 v5, 22, v5\n"
"v_mul_u32_u24 v5, 2240, v5\n"
"v_add_u32 v5, v5, v4\n"
"v_lshlrev_b32 v5, 7, v5\n"
"v_add_u32 v5, 4898816, v5\n"
"v_add_u32 v4, 12288, v4\n"
"buffer_load_dword v120, v5, s[32:35], 0 offen sc1\n"
"v_mul_u32_u24 v5, 52429, v4\n"
"v_lshrrev_b32 v5, 22, v5\n"
"v_mul_u32_u24 v5, 2240, v5\n"
"v_add_u32 v5, v5, v4\n"
"v_lshlrev_b32 v5, 7, v5\n"
"v_add_u32 v5, 4898816, v5\n"
"v_add_u32 v4, 12288, v4\n"
"buffer_load_dword v120, v5, s[32:35], 0 offen sc1\n"
"s_sleep 30\n"
".L_ns:\n"
"s_lshr_b32 s27, s50, 1\n"
"s_and_b32 s28, s50, 1\n"
"s_cmp_eq_u32 s49, 4\n"
"s_cbranch_scc1 .L_h\n"
"s_sub_u32 s27, s49, 14\n"
"s_lshr_b32 s27, s27, 1\n"
"s_and_b32 s28, s49, 1\n"
"s_add_u32 s28, s28, 1\n"
"s_sub_u32 s48, s49, 5\n"
"s_cmp_lt_u32 s49, 14\n"
"s_cselect_b32 s27, s48, s27\n"
"s_cselect_b32 s28, 0, s28\n"
"s_mul_i32 s50, s50, 9\n"
"s_add_u32 s27, s27, s50\n"
".L_g:\n"
"v_mov_b32 v16, 0x3f803f80\n"
"v_mov_b32 v17, 0x3f803f80\n"
"v_mov_b32 v18, 0x3f803f80\n"
"v_mov_b32 v19, 0x3f803f80\n"
"s_add_u32 s36, s14, 0x0\n"
"s_addc_u32 s37, s15, 0x0\n"
"s_and_b32 s37, s37, 0xffff\n"
"s_mov_b32 s38, 0x700000\n"
"s_mov_b32 s39, 0x20000\n"
"s_add_u32 s40, s16, 0x0\n"
"s_addc_u32 s41, s17, 0x0\n"
"s_and_b32 s41, s41, 0xffff\n"
"s_mov_b32 s42, 0x700000\n"
"s_mov_b32 s43, 0x20000\n"
"v_add_u32 v8, 0, v1\n"
"v_mul_u32_u24 v9, 5462, v8\n"
"v_lshrrev_b32 v9, 16, v9\n"
"v_mul_u32_u24 v10, 12, v9\n"
"v_sub_u32 v10, v8, v10\n"
"v_mul_u32_u24 v9, 7168, v9\n"
"v_lshl_add_u32 v9, v10, 4, v9\n"
"v_subrev_u32 v4, 0, v9\n"
"v_add_u32 v8, 64, v1\n"
"v_mul_u32_u24 v9, 5462, v8\n"
"v_lshrrev_b32 v9, 16, v9\n"
"v_mul_u32_u24 v10, 12, v9\n"
"v_sub_u32 v10, v8, v10\n"
"v_mul_u32_u24 v9, 7168, v9\n"
"v_lshl_add_u32 v9, v10, 4, v9\n"
"v_subrev_u32 v5, 1024, v9\n"
"v_add_u32 v8, 128, v1\n"
"v_mul_u32_u24 v9, 5462, v8\n"
"v_lshrrev_b32 v9, 16, v9\n"
"v_mul_u32_u24 v10, 12, v9\n"
"v_sub_u32 v10, v8, v10\n"
"v_mul_u32_u24 v9, 7168, v9\n"
"v_lshl_add_u32 v9, v10, 4, v9\n"
"v_subrev_u32 v6, 2048, v9\n"
"s_mul_i32 s48, s26, 114688\n"
"s_mul_i32 s49, s27, 192\n"
"s_add_u32 s63, s48, s49\n"
"s_mul_i32 s64, s26, 3072\n"
"s_add_u32 m0, s64, 0\n"
"s_nop 0\n"
"buffer_load_dwordx4 v4, s[36:39], s63 offen offset:0 lds\n"
"buffer_load_dwordx4 v5, s[36:39], s63 offen offset:1024 lds\n"
"buffer_load_dwordx4 v6, s[36:39], s63 offen offset:2048 lds\n"
"s_add_u32 m0, s64, 12288\n"
"s_nop 0\n"
"buffer_load_dwordx4 v4, s[40:43], s63 offen offset:0 lds\n"
"buffer_load_dwordx4 v5, s[40:43], s63 offen offset:1024 lds\n"
"buffer_load_dwordx4 v6, s[40:43], s63 offen offset:2048 lds\n"
"s_add_u32 s63, s63, 458752\n"
"s_add_u32 m0, s64, 24576\n"
"s_nop 0\n"
"buffer_load_dwordx4 v4, s[36:39], s63 offen offset:0 lds\n"
"buffer_load_dwordx4 v5, s[36:39], s63 offen offset:1024 lds\n"
"buffer_load_dwordx4 v6, s[36:39], s63 offen offset:2048 lds\n"
"s_add_u32 m0, s64, 36864\n"
"s_nop 0\n"
"buffer_load_dwordx4 v4, s[40:43], s63 offen offset:0 lds\n"
"buffer_load_dwordx4 v5, s[40:43], s63 offen offset:1024 lds\n"
"buffer_load_dwordx4 v6, s[40:43], s63 offen offset:2048 lds\n"
"s_add_u32 s63, s63, 458752\n"
"s_add_u32 m0, s64, 49152\n"
"s_nop 0\n"
"buffer_load_dwordx4 v4, s[36:39], s63 offen offset:0 lds\n"
"buffer_load_dwordx4 v5, s[36:39], s63 offen offset:1024 lds\n"
"buffer_load_dwordx4 v6, s[36:39], s63 offen offset:2048 lds\n"
"s_add_u32 m0, s64, 61440\n"
"s_nop 0\n"
"buffer_load_dwordx4 v4, s[40:43], s63 offen offset:0 lds\n"
"buffer_load_dwordx4 v5, s[40:43], s63 offen offset:1024 lds\n"
"buffer_load_dwordx4 v6, s[40:43], s63 offen offset:2048 lds\n"
"s_add_u32 s63, s63, 458752\n"
"s_add_u32 m0, s64, 73728\n"
"s_nop 0\n"
"buffer_load_dwordx4 v4, s[36:39], s63 offen offset:0 lds\n"
"buffer_load_dwordx4 v5, s[36:39], s63 offen offset:1024 lds\n"
"buffer_load_dwordx4 v6, s[36:39], s63 offen offset:2048 lds\n"
"s_add_u32 m0, s64, 86016\n"
"s_nop 0\n"
"buffer_load_dwordx4 v4, s[40:43], s63 offen offset:0 lds\n"
"buffer_load_dwordx4 v5, s[40:43], s63 offen offset:1024 lds\n"
"buffer_load_dwordx4 v6, s[40:43], s63 offen offset:2048 lds\n"
"s_add_u32 s63, s63, 458752\n"
"s_add_u32 m0, s64, 98304\n"
"s_nop 0\n"
"buffer_load_dwordx4 v4, s[36:39], s63 offen offset:0 lds\n"
"buffer_load_dwordx4 v5, s[36:39], s63 offen offset:1024 lds\n"
"buffer_load_dwordx4 v6, s[36:39], s63 offen offset:2048 lds\n"
"s_add_u32 m0, s64, 110592\n"
"s_nop 0\n"
"buffer_load_dwordx4 v4, s[40:43], s63 offen offset:0 lds\n"
"buffer_load_dwordx4 v5, s[40:43], s63 offen offset:1024 lds\n"
"buffer_load_dwordx4 v6, s[40:43], s63 offen offset:2048 lds\n"
"s_add_u32 s63, s63, 458752\n"
"v_and_b32 v12, 15, v1\n"
"v_lshrrev_b32 v13, 4, v1\n"
"v_lshrrev_b32 v14, 2, v12\n"
"v_and_b32 v15, 3, v12\n"
"v_lshl_add_u32 v13, v13, 3, v14\n"
"v_mul_u32_u24 v13, 192, v13\n"
"s_mul_i32 s48, s26, 48\n"
"v_add_u32 v13, s48, v13\n"
"v_and_b32 v14, 1, v15\n"
"v_lshrrev_b32 v15, 1, v15\n"
"v_lshl_add_u32 v13, v14, 3, v13\n"
"v_lshl_add_u32 v8, v15, 5, v13\n"
"v_mul_u32_u24 v15, 12288, v15\n"
"v_add3_u32 v9, v13, v15, 16\n"
"v_add_u32 v10, 49152, v8\n"
"v_add_u32 v11, 49152, v9\n"
"v_add_u32 v12, 98304, v8\n"
"v_add_u32 v13, 98304, v9\n"
"s_waitcnt vmcnt(24)\n"
"s_barrier\n"
"s_add_u32 m0, s64, 122880\n"
"s_nop 0\n"
"buffer_load_dwordx4 v4, s[36:39], s63 offen offset:0 lds\n"
"buffer_load_dwordx4 v5, s[36:39], s63 offen offset:1024 lds\n"
"buffer_load_dwordx4 v6, s[36:39], s63 offen offset:2048 lds\n"
"s_add_u32 m0, s64, 135168\n"
"s_nop 0\n"
"buffer_load_dwordx4 v4, s[40:43], s63 offen offset:0 lds\n"
"buffer_load_dwordx4 v5, s[40:43], s63 offen offset:1024 lds\n"
"buffer_load_dwordx4 v6, s[40:43], s63 offen offset:2048 lds\n"
"s_add_u32 s63, s63, 458752\n"
"ds_read_b64_tr_b16 a[0:1], v8 offset:0\n"
"ds_read_b64_tr_b16 a[2:3], v8 offset:768\n"
"ds_read_b64_tr_b16 a[4:5], v8 offset:12288\n"
"ds_read_b64_tr_b16 a[6:7], v8 offset:13056\n"
"ds_read_b64_tr_b16 a[8:9], v9 offset:0\n"
"ds_read_b64_tr_b16 a[10:11], v9 offset:768\n"
"ds_read_b64_tr_b16 a[12:13], v8 offset:6144\n"
"ds_read_b64_tr_b16 a[14:15], v8 offset:6912\n"
"ds_read_b64_tr_b16 a[16:17], v8 offset:18432\n"
"ds_read_b64_tr_b16 a[18:19], v8 offset:19200\n"
"ds_read_b64_tr_b16 a[20:21], v9 offset:6144\n"
"ds_read_b64_tr_b16 a[22:23], v9 offset:6912\n"
"s_waitcnt lgkmcnt(0)\n"
"v_mfma_f32_16x16x32_bf16 v[20:23], a[0:3], v[16:19], 0\n"
"v_mfma_f32_16x16x32_bf16 v[24:27], a[4:7], v[16:19], 0\n"
"v_mfma_f32_16x16x32_bf16 v[28:31], a[8:11], v[16:19], 0\n"
"v_mfma_f32_16x16x32_bf16 v[20:23], a[12:15], v[16:19], v[20:23]\n"
"v_mfma_f32_16x16x32_bf16 v[24:27], a[16:19], v[16:19], v[24:27]\n"
"v_mfma_f32_16x16x32_bf16 v[28:31], a[20:23], v[16:19], v[28:31]\n"
"s_waitcnt vmcnt(24)\n"
"s_barrier\n"
"s_add_u32 m0, s64, 0\n"
"s_nop 0\n"
"buffer_load_dwordx4 v4, s[36:39], s63 offen offset:0 lds\n"
"buffer_load_dwordx4 v5, s[36:39], s63 offen offset:1024 lds\n"
"buffer_load_dwordx4 v6, s[36:39], s63 offen offset:2048 lds\n"
"s_add_u32 m0, s64, 12288\n"
"s_nop 0\n"
"buffer_load_dwordx4 v4, s[40:43], s63 offen offset:0 lds\n"
"buffer_load_dwordx4 v5, s[40:43], s63 offen offset:1024 lds\n"
"buffer_load_dwordx4 v6, s[40:43], s63 offen offset:2048 lds\n"
"s_add_u32 s63, s63, 458752\n"
"ds_read_b64_tr_b16 a[24:25], v8 offset:24576\n"
"ds_read_b64_tr_b16 a[26:27], v8 offset:25344\n"
"ds_read_b64_tr_b16 a[28:29], v8 offset:36864\n"
"ds_read_b64_tr_b16 a[30:31], v8 offset:37632\n"
"ds_read_b64_tr_b16 a[32:33], v9 offset:24576\n"
"ds_read_b64_tr_b16 a[34:35], v9 offset:25344\n"
"ds_read_b64_tr_b16 a[36:37], v8 offset:30720\n"
"ds_read_b64_tr_b16 a[38:39], v8 offset:31488\n"
"ds_read_b64_tr_b16 a[40:41], v8 offset:43008\n"
"ds_read_b64_tr_b16 a[42:43], v8 offset:43776\n"
"ds_read_b64_tr_b16 a[44:45], v9 offset:30720\n"
"ds_read_b64_tr_b16 a[46:47], v9 offset:31488\n"
"s_waitcnt lgkmcnt(0)\n"
"v_mfma_f32_16x16x32_bf16 v[20:23], a[24:27], v[16:19], v[20:23]\n"
"v_mfma_f32_16x16x32_bf16 v[24:27], a[28:31], v[16:19], v[24:27]\n"
"v_mfma_f32_16x16x32_bf16 v[28:31], a[32:35], v[16:19], v[28:31]\n"
"v_mfma_f32_16x16x32_bf16 v[20:23], a[36:39], v[16:19], v[20:23]\n"
"v_mfma_f32_16x16x32_bf16 v[24:27], a[40:43], v[16:19], v[24:27]\n"
"v_mfma_f32_16x16x32_bf16 v[28:31], a[44:47], v[16:19], v[28:31]\n"
"s_waitcnt vmcnt(24)\n"
"s_barrier\n"
"s_add_u32 m0, s64, 24576\n"
"s_nop 0\n"
"buffer_load_dwordx4 v4, s[36:39], s63 offen offset:0 lds\n"
"buffer_load_dwordx4 v5, s[36:39], s63 offen offset:1024 lds\n"
"buffer_load_dwordx4 v6, s[36:39], s63 offen offset:2048 lds\n"
"s_add_u32 m0, s64, 36864\n"
"s_nop 0\n"
"buffer_load_dwordx4 v4, s[40:43], s63 offen offset:0 lds\n"
"buffer_load_dwordx4 v5, s[40:43], s63 offen offset:1024 lds\n"
"buffer_load_dwordx4 v6, s[40:43], s63 offen offset:2048 lds\n"
"s_add_u32 s63, s63, 458752\n"
"ds_read_b64_tr_b16 a[48:49], v10 offset:0\n"
"ds_read_b64_tr_b16 a[50:51], v10 offset:768\n"
"ds_read_b64_tr_b16 a[52:53], v10 offset:12288\n"
"ds_read_b64_tr_b16 a[54:55], v10 offset:13056\n"
"ds_read_b64_tr_b16 a[56:57], v11 offset:0\n"
"ds_read_b64_tr_b16 a[58:59], v11 offset:768\n"
"ds_read_b64_tr_b16 a[60:61], v10 offset:6144\n"
"ds_read_b64_tr_b16 a[62:63], v10 offset:6912\n"
"ds_read_b64_tr_b16 a[64:65], v10 offset:18432\n"
"ds_read_b64_tr_b16 a[66:67], v10 offset:19200\n"
"ds_read_b64_tr_b16 a[68:69], v11 offset:6144\n"
"ds_read_b64_tr_b16 a[70:71], v11 offset:6912\n"
"s_waitcnt lgkmcnt(0)\n"
"v_mfma_f32_16x16x32_bf16 v[20:23], a[48:51], v[16:19], v[20:23]\n"
"v_mfma_f32_16x16x32_bf16 v[24:27], a[52:55], v[16:19], v[24:27]\n"
"v_mfma_f32_16x16x32_bf16 v[28:31], a[56:59], v[16:19], v[28:31]\n"
"v_mfma_f32_16x16x32_bf16 v[20:23], a[60:63], v[16:19], v[20:23]\n"
"v_mfma_f32_16x16x32_bf16 v[24:27], a[64:67], v[16:19], v[24:27]\n"
"v_mfma_f32_16x16x32_bf16 v[28:31], a[68:71], v[16:19], v[28:31]\n"
"s_waitcnt vmcnt(24)\n"
"s_barrier\n"
"s_add_u32 m0, s64, 49152\n"
"s_nop 0\n"
"buffer_load_dwordx4 v4, s[36:39], s63 offen offset:0 lds\n"
"buffer_load_dwordx4 v5, s[36:39], s63 offen offset:1024 lds\n"
"buffer_load_dwordx4 v6, s[36:39], s63 offen offset:2048 lds\n"
"s_add_u32 m0, s64, 61440\n"
"s_nop 0\n"
"buffer_load_dwordx4 v4, s[40:43], s63 offen offset:0 lds\n"
"buffer_load_dwordx4 v5, s[40:43], s63 offen offset:1024 lds\n"
"buffer_load_dwordx4 v6, s[40:43], s63 offen offset:2048 lds\n"
"s_add_u32 s63, s63, 458752\n"
"ds_read_b64_tr_b16 a[72:73], v10 offset:24576\n"
"ds_read_b64_tr_b16 a[74:75], v10 offset:25344\n"
"ds_read_b64_tr_b16 a[76:77], v10 offset:36864\n"
"ds_read_b64_tr_b16 a[78:79], v10 offset:37632\n"
"ds_read_b64_tr_b16 a[80:81], v11 offset:24576\n"
"ds_read_b64_tr_b16 a[82:83], v11 offset:25344\n"
"ds_read_b64_tr_b16 a[84:85], v10 offset:30720\n"
"ds_read_b64_tr_b16 a[86:87], v10 offset:31488\n"
"ds_read_b64_tr_b16 a[88:89], v10 offset:43008\n"
"ds_read_b64_tr_b16 a[90:91], v10 offset:43776\n"
"ds_read_b64_tr_b16 a[92:93], v11 offset:30720\n"
"ds_read_b64_tr_b16 a[94:95], v11 offset:31488\n"
"s_waitcnt lgkmcnt(0)\n"
"v_mfma_f32_16x16x32_bf16 v[20:23], a[72:75], v[16:19], v[20:23]\n"
"v_mfma_f32_16x16x32_bf16 v[24:27], a[76:79], v[16:19], v[24:27]\n"
"v_mfma_f32_16x16x32_bf16 v[28:31], a[80:83], v[16:19], v[28:31]\n"
"v_mfma_f32_16x16x32_bf16 v[20:23], a[84:87], v[16:19], v[20:23]\n"
"v_mfma_f32_16x16x32_bf16 v[24:27], a[88:91], v[16:19], v[24:27]\n"
"v_mfma_f32_16x16x32_bf16 v[28:31], a[92:95], v[16:19], v[28:31]\n"
"s_waitcnt vmcnt(24)\n"
"s_barrier\n"
"s_add_u32 m0, s64, 73728\n"
"s_nop 0\n"
"buffer_load_dwordx4 v4, s[36:39], s63 offen offset:0 lds\n"
"buffer_load_dwordx4 v5, s[36:39], s63 offen offset:1024 lds\n"
"buffer_load_dwordx4 v6, s[36:39], s63 offen offset:2048 lds\n"
"s_add_u32 m0, s64, 86016\n"
"s_nop 0\n"
"buffer_load_dwordx4 v4, s[40:43], s63 offen offset:0 lds\n"
"buffer_load_dwordx4 v5, s[40:43], s63 offen offset:1024 lds\n"
"buffer_load_dwordx4 v6, s[40:43], s63 offen offset:2048 lds\n"
"s_add_u32 s63, s63, 458752\n"
"ds_read_b64_tr_b16 a[96:97], v12 offset:0\n"
"ds_read_b64_tr_b16 a[98:99], v12 offset:768\n"
"ds_read_b64_tr_b16 a[100:101], v12 offset:12288\n"
"ds_read_b64_tr_b16 a[102:103], v12 offset:13056\n"
"ds_read_b64_tr_b16 a[104:105], v13 offset:0\n"
"ds_read_b64_tr_b16 a[106:107], v13 offset:768\n"
"ds_read_b64_tr_b16 a[108:109], v12 offset:6144\n"
"ds_read_b64_tr_b16 a[110:111], v12 offset:6912\n"
"ds_read_b64_tr_b16 a[112:113], v12 offset:18432\n"
"ds_read_b64_tr_b16 a[114:115], v12 offset:19200\n"
"ds_read_b64_tr_b16 a[116:117], v13 offset:6144\n"
"ds_read_b64_tr_b16 a[118:119], v13 offset:6912\n"
"s_waitcnt lgkmcnt(0)\n"
"v_mfma_f32_16x16x32_bf16 v[20:23], a[96:99], v[16:19], v[20:23]\n"
"v_mfma_f32_16x16x32_bf16 v[24:27], a[100:103], v[16:19], v[24:27]\n"
"v_mfma_f32_16x16x32_bf16 v[28:31], a[104:107], v[16:19], v[28:31]\n"
"v_mfma_f32_16x16x32_bf16 v[20:23], a[108:111], v[16:19], v[20:23]\n"
"v_mfma_f32_16x16x32_bf16 v[24:27], a[112:115], v[16:19], v[24:27]\n"
"v_mfma_f32_16x16x32_bf16 v[28:31], a[116:119], v[16:19], v[28:31]\n"
"s_waitcnt vmcnt(24)\n"
"s_barrier\n"
"s_add_u32 m0, s64, 98304\n"
"s_nop 0\n"
"buffer_load_dwordx4 v4, s[36:39], s63 offen offset:0 lds\n"
"buffer_load_dwordx4 v5, s[36:39], s63 offen offset:1024 lds\n"
"buffer_load_dwordx4 v6, s[36:39], s63 offen offset:2048 lds\n"
"s_add_u32 m0, s64, 110592\n"
"s_nop 0\n"
"buffer_load_dwordx4 v4, s[40:43], s63 offen offset:0 lds\n"
"buffer_load_dwordx4 v5, s[40:43], s63 offen offset:1024 lds\n"
"buffer_load_dwordx4 v6, s[40:43], s63 offen offset:2048 lds\n"
"s_add_u32 s63, s63, 458752\n"
"ds_read_b64_tr_b16 a[120:121], v12 offset:24576\n"
"ds_read_b64_tr_b16 a[122:123], v12 offset:25344\n"
"ds_read_b64_tr_b16 a[124:125], v12 offset:36864\n"
"ds_read_b64_tr_b16 a[126:127], v12 offset:37632\n"
"ds_read_b64_tr_b16 a[128:129], v13 offset:24576\n"
"ds_read_b64_tr_b16 a[130:131], v13 offset:25344\n"
"ds_read_b64_tr_b16 a[132:133], v12 offset:30720\n"
"ds_read_b64_tr_b16 a[134:135], v12 offset:31488\n"
"ds_read_b64_tr_b16 a[136:137], v12 offset:43008\n"
"ds_read_b64_tr_b16 a[138:139], v12 offset:43776\n"
"ds_read_b64_tr_b16 a[140:141], v13 offset:30720\n"
"ds_read_b64_tr_b16 a[142:143], v13 offset:31488\n"
"s_waitcnt lgkmcnt(0)\n"
"v_mfma_f32_16x16x32_bf16 v[20:23], a[120:123], v[16:19], v[20:23]\n"
"v_mfma_f32_16x16x32_bf16 v[24:27], a[124:127], v[16:19], v[24:27]\n"
"v_mfma_f32_16x16x32_bf16 v[28:31], a[128:131], v[16:19], v[28:31]\n"
"v_mfma_f32_16x16x32_bf16 v[20:23], a[132:135], v[16:19], v[20:23]\n"
"v_mfma_f32_16x16x32_bf16 v[24:27], a[136:139], v[16:19], v[24:27]\n"
"v_mfma_f32_16x16x32_bf16 v[28:31], a[140:143], v[16:19], v[28:31]\n"
"s_waitcnt vmcnt(24)\n"
"s_barrier\n"
"s_add_u32 m0, s64, 122880\n"
"s_nop 0\n"
"buffer_load_dwordx4 v4, s[36:39], s63 offen offset:0 lds\n"
"buffer_load_dwordx4 v5, s[36:39], s63 offen offset:1024 lds\n"
"buffer_load_dwordx4 v6, s[36:39], s63 offen offset:2048 lds\n"
"s_add_u32 m0, s64, 135168\n"
"s_nop 0\n"
"buffer_load_dwordx4 v4, s[40:43], s63 offen offset:0 lds\n"
"buffer_load_dwordx4 v5, s[40:43], s63 offen offset:1024 lds\n"
"buffer_load_dwordx4 v6, s[40:43], s63 offen offset:2048 lds\n"
"s_add_u32 s63, s63, 458752\n"
"ds_read_b64_tr_b16 a[144:145], v8 offset:0\n"
"ds_read_b64_tr_b16 a[146:147], v8 offset:768\n"
"ds_read_b64_tr_b16 a[148:149], v8 offset:12288\n"
"ds_read_b64_tr_b16 a[150:151], v8 offset:13056\n"
"ds_read_b64_tr_b16 a[152:153], v9 offset:0\n"
"ds_read_b64_tr_b16 a[154:155], v9 offset:768\n"
"ds_read_b64_tr_b16 a[156:157], v8 offset:6144\n"
"ds_read_b64_tr_b16 a[158:159], v8 offset:6912\n"
"ds_read_b64_tr_b16 a[160:161], v8 offset:18432\n"
"ds_read_b64_tr_b16 a[162:163], v8 offset:19200\n"
"ds_read_b64_tr_b16 a[164:165], v9 offset:6144\n"
"ds_read_b64_tr_b16 a[166:167], v9 offset:6912\n"
"s_waitcnt lgkmcnt(0)\n"
"v_mfma_f32_16x16x32_bf16 v[20:23], a[144:147], v[16:19], v[20:23]\n"
"v_mfma_f32_16x16x32_bf16 v[24:27], a[148:151], v[16:19], v[24:27]\n"
"v_mfma_f32_16x16x32_bf16 v[28:31], a[152:155], v[16:19], v[28:31]\n"
"v_mfma_f32_16x16x32_bf16 v[20:23], a[156:159], v[16:19], v[20:23]\n"
"v_mfma_f32_16x16x32_bf16 v[24:27], a[160:163], v[16:19], v[24:27]\n"
"v_mfma_f32_16x16x32_bf16 v[28:31], a[164:167], v[16:19], v[28:31]\n"
"s_waitcnt vmcnt(24)\n"
"s_barrier\n"
"s_add_u32 m0, s64, 0\n"
"s_nop 0\n"
"buffer_load_dwordx4 v4, s[36:39], s63 offen offset:0 lds\n"
"buffer_load_dwordx4 v5, s[36:39], s63 offen offset:1024 lds\n"
"buffer_load_dwordx4 v6, s[36:39], s63 offen offset:2048 lds\n"
"s_add_u32 m0, s64, 12288\n"
"s_nop 0\n"
"buffer_load_dwordx4 v4, s[40:43], s63 offen offset:0 lds\n"
"buffer_load_dwordx4 v5, s[40:43], s63 offen offset:1024 lds\n"
"buffer_load_dwordx4 v6, s[40:43], s63 offen offset:2048 lds\n"
"s_add_u32 s63, s63, 458752\n"
"ds_read_b64_tr_b16 a[168:169], v8 offset:24576\n"
"ds_read_b64_tr_b16 a[170:171], v8 offset:25344\n"
"ds_read_b64_tr_b16 a[172:173], v8 offset:36864\n"
"ds_read_b64_tr_b16 a[174:175], v8 offset:37632\n"
"ds_read_b64_tr_b16 a[176:177], v9 offset:24576\n"
"ds_read_b64_tr_b16 a[178:179], v9 offset:25344\n"
"ds_read_b64_tr_b16 a[180:181], v8 offset:30720\n"
"ds_read_b64_tr_b16 a[182:183], v8 offset:31488\n"
"ds_read_b64_tr_b16 a[184:185], v8 offset:43008\n"
"ds_read_b64_tr_b16 a[186:187], v8 offset:43776\n"
"ds_read_b64_tr_b16 a[188:189], v9 offset:30720\n"
"ds_read_b64_tr_b16 a[190:191], v9 offset:31488\n"
"s_waitcnt lgkmcnt(0)\n"
"v_mfma_f32_16x16x32_bf16 v[20:23], a[168:171], v[16:19], v[20:23]\n"
"v_mfma_f32_16x16x32_bf16 v[24:27], a[172:175], v[16:19], v[24:27]\n"
"v_mfma_f32_16x16x32_bf16 v[28:31], a[176:179], v[16:19], v[28:31]\n"
"v_mfma_f32_16x16x32_bf16 v[20:23], a[180:183], v[16:19], v[20:23]\n"
"v_mfma_f32_16x16x32_bf16 v[24:27], a[184:187], v[16:19], v[24:27]\n"
"v_mfma_f32_16x16x32_bf16 v[28:31], a[188:191], v[16:19], v[28:31]\n"
"s_waitcnt vmcnt(24)\n"
"s_barrier\n"
"s_add_u32 m0, s64, 24576\n"
"s_nop 0\n"
"buffer_load_dwordx4 v4, s[36:39], s63 offen offset:0 lds\n"
"buffer_load_dwordx4 v5, s[36:39], s63 offen offset:1024 lds\n"
"buffer_load_dwordx4 v6, s[36:39], s63 offen offset:2048 lds\n"
"s_add_u32 m0, s64, 36864\n"
"s_nop 0\n"
"buffer_load_dwordx4 v4, s[40:43], s63 offen offset:0 lds\n"
"buffer_load_dwordx4 v5, s[40:43], s63 offen offset:1024 lds\n"
"buffer_load_dwordx4 v6, s[40:43], s63 offen offset:2048 lds\n"
"s_add_u32 s63, s63, 458752\n"
"ds_read_b64_tr_b16 a[192:193], v10 offset:0\n"
"ds_read_b64_tr_b16 a[194:195], v10 offset:768\n"
"ds_read_b64_tr_b16 a[196:197], v10 offset:12288\n"
"ds_read_b64_tr_b16 a[198:199], v10 offset:13056\n"
"ds_read_b64_tr_b16 a[200:201], v11 offset:0\n"
"ds_read_b64_tr_b16 a[202:203], v11 offset:768\n"
"ds_read_b64_tr_b16 a[204:205], v10 offset:6144\n"
"ds_read_b64_tr_b16 a[206:207], v10 offset:6912\n"
"ds_read_b64_tr_b16 a[208:209], v10 offset:18432\n"
"ds_read_b64_tr_b16 a[210:211], v10 offset:19200\n"
"ds_read_b64_tr_b16 a[212:213], v11 offset:6144\n"
"ds_read_b64_tr_b16 a[214:215], v11 offset:6912\n"
"s_waitcnt lgkmcnt(0)\n"
"v_mfma_f32_16x16x32_bf16 v[20:23], a[192:195], v[16:19], v[20:23]\n"
"v_mfma_f32_16x16x32_bf16 v[24:27], a[196:199], v[16:19], v[24:27]\n"
"v_mfma_f32_16x16x32_bf16 v[28:31], a[200:203], v[16:19], v[28:31]\n"
"v_mfma_f32_16x16x32_bf16 v[20:23], a[204:207], v[16:19], v[20:23]\n"
"v_mfma_f32_16x16x32_bf16 v[24:27], a[208:211], v[16:19], v[24:27]\n"
"v_mfma_f32_16x16x32_bf16 v[28:31], a[212:215], v[16:19], v[28:31]\n"
"s_waitcnt vmcnt(24)\n"
"s_barrier\n"
"s_add_u32 m0, s64, 49152\n"
"s_nop 0\n"
"buffer_load_dwordx4 v4, s[36:39], s63 offen offset:0 lds\n"
"buffer_load_dwordx4 v5, s[36:39], s63 offen offset:1024 lds\n"
"buffer_load_dwordx4 v6, s[36:39], s63 offen offset:2048 lds\n"
"s_add_u32 m0, s64, 61440\n"
"s_nop 0\n"
"buffer_load_dwordx4 v4, s[40:43], s63 offen offset:0 lds\n"
"buffer_load_dwordx4 v5, s[40:43], s63 offen offset:1024 lds\n"
"buffer_load_dwordx4 v6, s[40:43], s63 offen offset:2048 lds\n"
"s_add_u32 s63, s63, 458752\n"
"ds_read_b64_tr_b16 a[216:217], v10 offset:24576\n"
"ds_read_b64_tr_b16 a[218:219], v10 offset:25344\n"
"ds_read_b64_tr_b16 a[220:221], v10 offset:36864\n"
"ds_read_b64_tr_b16 a[222:223], v10 offset:37632\n"
"ds_read_b64_tr_b16 a[224:225], v11 offset:24576\n"
"ds_read_b64_tr_b16 a[226:227], v11 offset:25344\n"
"ds_read_b64_tr_b16 a[228:229], v10 offset:30720\n"
"ds_read_b64_tr_b16 a[230:231], v10 offset:31488\n"
"ds_read_b64_tr_b16 a[232:233], v10 offset:43008\n"
"ds_read_b64_tr_b16 a[234:235], v10 offset:43776\n"
"ds_read_b64_tr_b16 a[236:237], v11 offset:30720\n"
"ds_read_b64_tr_b16 a[238:239], v11 offset:31488\n"
"s_waitcnt lgkmcnt(0)\n"
"v_mfma_f32_16x16x32_bf16 v[20:23], a[216:219], v[16:19], v[20:23]\n"
"v_mfma_f32_16x16x32_bf16 v[24:27], a[220:223], v[16:19], v[24:27]\n"
"v_mfma_f32_16x16x32_bf16 v[28:31], a[224:227], v[16:19], v[28:31]\n"
"v_mfma_f32_16x16x32_bf16 v[20:23], a[228:231], v[16:19], v[20:23]\n"
"v_mfma_f32_16x16x32_bf16 v[24:27], a[232:235], v[16:19], v[24:27]\n"
"v_mfma_f32_16x16x32_bf16 v[28:31], a[236:239], v[16:19], v[28:31]\n"
"s_waitcnt vmcnt(24)\n"
"s_barrier\n"
"s_add_u32 m0, s64, 73728\n"
"s_nop 0\n"
"buffer_load_dwordx4 v4, s[36:39], s63 offen offset:0 lds\n"
"buffer_load_dwordx4 v5, s[36:39], s63 offen offset:1024 lds\n"
"buffer_load_dwordx4 v6, s[36:39], s63 offen offset:2048 lds\n"
"s_add_u32 m0, s64, 86016\n"
"s_nop 0\n"
"buffer_load_dwordx4 v4, s[40:43], s63 offen offset:0 lds\n"
"buffer_load_dwordx4 v5, s[40:43], s63 offen offset:1024 lds\n"
"buffer_load_dwordx4 v6, s[40:43], s63 offen offset:2048 lds\n"
"s_add_u32 s63, s63, 458752\n"
"ds_read_b64_tr_b16 a[240:241], v12 offset:0\n"
"ds_read_b64_tr_b16 a[242:243], v12 offset:768\n"
"ds_read_b64_tr_b16 a[244:245], v12 offset:12288\n"
"ds_read_b64_tr_b16 a[246:247], v12 offset:13056\n"
"ds_read_b64_tr_b16 a[248:249], v13 offset:0\n"
"ds_read_b64_tr_b16 a[250:251], v13 offset:768\n"
"ds_read_b64_tr_b16 a[252:253], v12 offset:6144\n"
"ds_read_b64_tr_b16 a[254:255], v12 offset:6912\n"
"ds_read_b64_tr_b16 v[128:129], v12 offset:18432\n"
"ds_read_b64_tr_b16 v[130:131], v12 offset:19200\n"
"ds_read_b64_tr_b16 v[132:133], v13 offset:6144\n"
"ds_read_b64_tr_b16 v[134:135], v13 offset:6912\n"
"s_waitcnt lgkmcnt(0)\n"
"v_mfma_f32_16x16x32_bf16 v[20:23], a[240:243], v[16:19], v[20:23]\n"
"v_mfma_f32_16x16x32_bf16 v[24:27], a[244:247], v[16:19], v[24:27]\n"
"v_mfma_f32_16x16x32_bf16 v[28:31], a[248:251], v[16:19], v[28:31]\n"
"v_mfma_f32_16x16x32_bf16 v[20:23], a[252:255], v[16:19], v[20:23]\n"
"v_mfma_f32_16x16x32_bf16 v[24:27], v[128:131], v[16:19], v[24:27]\n"
"v_mfma_f32_16x16x32_bf16 v[28:31], v[132:135], v[16:19], v[28:31]\n"
"s_waitcnt vmcnt(24)\n"
"s_barrier\n"
"ds_read_b64_tr_b16 v[136:137], v12 offset:24576\n"
"ds_read_b64_tr_b16 v[138:139], v12 offset:25344\n"
"ds_read_b64_tr_b16 v[140:141], v12 offset:36864\n"
"ds_read_b64_tr_b16 v[142:143], v12 offset:37632\n"
"ds_read_b64_tr_b16 v[144:145], v13 offset:24576\n"
"ds_read_b64_tr_b16 v[146:147], v13 offset:25344\n"
"ds_read_b64_tr_b16 v[148:149], v12 offset:30720\n"
"ds_read_b64_tr_b16 v[150:151], v12 offset:31488\n"
"ds_read_b64_tr_b16 v[152:153], v12 offset:43008\n"
"ds_read_b64_tr_b16 v[154:155], v12 offset:43776\n"
"ds_read_b64_tr_b16 v[156:157], v13 offset:30720\n"
"ds_read_b64_tr_b16 v[158:159], v13 offset:31488\n"
"s_waitcnt lgkmcnt(0)\n"
"v_mfma_f32_16x16x32_bf16 v[20:23], v[136:139], v[16:19], v[20:23]\n"
"v_mfma_f32_16x16x32_bf16 v[24:27], v[140:143], v[16:19], v[24:27]\n"
"v_mfma_f32_16x16x32_bf16 v[28:31], v[144:147], v[16:19], v[28:31]\n"
"v_mfma_f32_16x16x32_bf16 v[20:23], v[148:151], v[16:19], v[20:23]\n"
"v_mfma_f32_16x16x32_bf16 v[24:27], v[152:155], v[16:19], v[24:27]\n"
"v_mfma_f32_16x16x32_bf16 v[28:31], v[156:159], v[16:19], v[28:31]\n"
"s_waitcnt vmcnt(18)\n"
"s_barrier\n"
"ds_read_b64_tr_b16 v[160:161], v8 offset:0\n"
"ds_read_b64_tr_b16 v[162:163], v8 offset:768\n"
"ds_read_b64_tr_b16 v[164:165], v8 offset:12288\n"
"ds_read_b64_tr_b16 v[166:167], v8 offset:13056\n"
"ds_read_b64_tr_b16 v[168:169], v9 offset:0\n"
"ds_read_b64_tr_b16 v[170:171], v9 offset:768\n"
"ds_read_b64_tr_b16 v[172:173], v8 offset:6144\n"
"ds_read_b64_tr_b16 v[174:175], v8 offset:6912\n"
"ds_read_b64_tr_b16 v[176:177], v8 offset:18432\n"
"ds_read_b64_tr_b16 v[178:179], v8 offset:19200\n"
"ds_read_b64_tr_b16 v[180:181], v9 offset:6144\n"
"ds_read_b64_tr_b16 v[182:183], v9 offset:6912\n"
"s_waitcnt lgkmcnt(0)\n"
"v_mfma_f32_16x16x32_bf16 v[20:23], v[160:163], v[16:19], v[20:23]\n"
"v_mfma_f32_16x16x32_bf16 v[24:27], v[164:167], v[16:19], v[24:27]\n"
"v_mfma_f32_16x16x32_bf16 v[28:31], v[168:171], v[16:19], v[28:31]\n"
"v_mfma_f32_16x16x32_bf16 v[20:23], v[172:175], v[16:19], v[20:23]\n"
"v_mfma_f32_16x16x32_bf16 v[24:27], v[176:179], v[16:19], v[24:27]\n"
"v_mfma_f32_16x16x32_bf16 v[28:31], v[180:183], v[16:19], v[28:31]\n"
"s_waitcnt vmcnt(12)\n"
"s_barrier\n"
"ds_read_b64_tr_b16 v[184:185], v8 offset:24576\n"
"ds_read_b64_tr_b16 v[186:187], v8 offset:25344\n"
"ds_read_b64_tr_b16 v[188:189], v8 offset:36864\n"
"ds_read_b64_tr_b16 v[190:191], v8 offset:37632\n"
"ds_read_b64_tr_b16 v[192:193], v9 offset:24576\n"
"ds_read_b64_tr_b16 v[194:195], v9 offset:25344\n"
"ds_read_b64_tr_b16 v[196:197], v8 offset:30720\n"
"ds_read_b64_tr_b16 v[198:199], v8 offset:31488\n"
"ds_read_b64_tr_b16 v[200:201], v8 offset:43008\n"
"ds_read_b64_tr_b16 v[202:203], v8 offset:43776\n"
"ds_read_b64_tr_b16 v[204:205], v9 offset:30720\n"
"ds_read_b64_tr_b16 v[206:207], v9 offset:31488\n"
"s_waitcnt lgkmcnt(0)\n"
"v_mfma_f32_16x16x32_bf16 v[20:23], v[184:187], v[16:19], v[20:23]\n"
"v_mfma_f32_16x16x32_bf16 v[24:27], v[188:191], v[16:19], v[24:27]\n"
"v_mfma_f32_16x16x32_bf16 v[28:31], v[192:195], v[16:19], v[28:31]\n"
"v_mfma_f32_16x16x32_bf16 v[20:23], v[196:199], v[16:19], v[20:23]\n"
"v_mfma_f32_16x16x32_bf16 v[24:27], v[200:203], v[16:19], v[24:27]\n"
"v_mfma_f32_16x16x32_bf16 v[28:31], v[204:207], v[16:19], v[28:31]\n"
"s_waitcnt vmcnt(6)\n"
"s_barrier\n"
"ds_read_b64_tr_b16 v[208:209], v10 offset:0\n"
"ds_read_b64_tr_b16 v[210:211], v10 offset:768\n"
"ds_read_b64_tr_b16 v[212:213], v10 offset:12288\n"
"ds_read_b64_tr_b16 v[214:215], v10 offset:13056\n"
"ds_read_b64_tr_b16 v[216:217], v11 offset:0\n"
"ds_read_b64_tr_b16 v[218:219], v11 offset:768\n"
"ds_read_b64_tr_b16 v[220:221], v10 offset:6144\n"
"ds_read_b64_tr_b16 v[222:223], v10 offset:6912\n"
"ds_read_b64_tr_b16 v[224:225], v10 offset:18432\n"
"ds_read_b64_tr_b16 v[226:227], v10 offset:19200\n"
"ds_read_b64_tr_b16 v[228:229], v11 offset:6144\n"
"ds_read_b64_tr_b16 v[230:231], v11 offset:6912\n"
"s_waitcnt lgkmcnt(0)\n"
"v_mfma_f32_16x16x32_bf16 v[20:23], v[208:211], v[16:19], v[20:23]\n"
"v_mfma_f32_16x16x32_bf16 v[24:27], v[212:215], v[16:19], v[24:27]\n"
"v_mfma_f32_16x16x32_bf16 v[28:31], v[216:219], v[16:19], v[28:31]\n"
"v_mfma_f32_16x16x32_bf16 v[20:23], v[220:223], v[16:19], v[20:23]\n"
"v_mfma_f32_16x16x32_bf16 v[24:27], v[224:227], v[16:19], v[24:27]\n"
"v_mfma_f32_16x16x32_bf16 v[28:31], v[228:231], v[16:19], v[28:31]\n"
"s_waitcnt vmcnt(0)\n"
"s_barrier\n"
"ds_read_b64_tr_b16 v[232:233], v10 offset:24576\n"
"ds_read_b64_tr_b16 v[234:235], v10 offset:25344\n"
"ds_read_b64_tr_b16 v[236:237], v10 offset:36864\n"
"ds_read_b64_tr_b16 v[238:239], v10 offset:37632\n"
"ds_read_b64_tr_b16 v[240:241], v11 offset:24576\n"
"ds_read_b64_tr_b16 v[242:243], v11 offset:25344\n"
"ds_read_b64_tr_b16 v[244:245], v10 offset:30720\n"
"ds_read_b64_tr_b16 v[246:247], v10 offset:31488\n"
"ds_read_b64_tr_b16 v[248:249], v10 offset:43008\n"
"ds_read_b64_tr_b16 v[250:251], v10 offset:43776\n"
"ds_read_b64_tr_b16 v[252:253], v11 offset:30720\n"
"ds_read_b64_tr_b16 v[254:255], v11 offset:31488\n"
"s_waitcnt lgkmcnt(0)\n"
"v_mfma_f32_16x16x32_bf16 v[20:23], v[232:235], v[16:19], v[20:23]\n"
"v_mfma_f32_16x16x32_bf16 v[24:27], v[236:239], v[16:19], v[24:27]\n"
"v_mfma_f32_16x16x32_bf16 v[28:31], v[240:243], v[16:19], v[28:31]\n"
"v_mfma_f32_16x16x32_bf16 v[20:23], v[244:247], v[16:19], v[20:23]\n"
"v_mfma_f32_16x16x32_bf16 v[24:27], v[248:251], v[16:19], v[24:27]\n"
"v_mfma_f32_16x16x32_bf16 v[28:31], v[252:255], v[16:19], v[28:31]\n"
"s_barrier\n"
"s_lshl2_add_u32 s49, s27, 128\n"
"s_mul_i32 s68, s27, 3072\n"
"s_mul_i32 s48, s26, 768\n"
"s_cmp_eq_u32 s28, 0\n"
"s_addc_u32 s62, 84, 0\n"
"v_cmp_gt_u32 vcc, 48, v1\n"
"v_mov_b32 v15, 0x7fff0000\n"
"s_nop 0\n"
"v_cndmask_b32 v15, v15, v2, vcc\n"
"s_add_u32 s68, s68, s48\n"
"s_add_u32 s68, s68, 4718592\n"
"s_waitcnt lgkmcnt(0)\n"
"s_add_u32 s54, s52, 0x7f4a7c15\n"
"s_addc_u32 s55, s53, 0x9e3779b9\n"
"s_lshl_b32 s67, s49, 5\n"
"s_add_u32 s67, s67, 4898816\n"
"s_sub_u32 s51, s67, s68\n"
"s_add_u32 s92, s32, s51\n"
"s_addc_u32 s93, s33, 0x10000\n"
"s_sub_u32 s92, s92, 890880\n"
"s_subb_u32 s93, s93, 0\n"
"s_cmp_eq_u32 s26, 3\n"
"s_cselect_b32 s94, 1, 0\n"
"s_mov_b32 s95, 0x20000\n"
"v_mov_b32 v16, s54\n"
"v_mov_b32 v17, s55\n"
"v_cmp_gt_u32 vcc, 12, v1\n"
"v_mul_u32_u24 v3, 296960, v1\n"
"v_mov_b32 v12, 0x7fff0000\n"
"v_cndmask_b32 v3, v12, v3, vcc\n"
"s_add_u32 s51, s67, 76021760\n"
"buffer_store_dwordx2 v[16:17], v3, s[32:35], s51 offen sc1\n"
"s_sub_u32 s67, s68, 296960\n"
"s_mul_i32 s48, s28, 296960\n"
"s_add_u32 s68, s68, s48\n"
"s_sub_u32 s68, s68, 1781760\n"
"s_add_u32 s63, s48, 4681728\n"
"s_add_u32 s66, s48, 4898816\n"
"s_mul_i32 s64, s26, 9216\n"
"s_add_u32 s65, s64, 73728\n"
"s_xor_b32 s65, s65, s64\n"
"s_add_u32 s63, s63, s64\n"
"v_mov_b32 v4, v2\n"
"v_add_u32 v5, 36864, v2\n"
"v_add_u32 v6, 4096, v2\n"
"v_add_u32 v7, 8192, v2\n"
"v_bfe_u32 v3, v1, 1, 3\n"
"v_mul_u32_u24 v8, 1152, v3\n"
"v_lshrrev_b32 v3, 1, v3\n"
"v_add_u32 v3, s26, v3\n"
"v_lshrrev_b32 v9, 4, v1\n"
"v_lshl_add_u32 v3, v3, 2, v9\n"
"v_and_b32 v3, 15, v3\n"
"v_lshl_add_u32 v8, v3, 2, v8\n"
"v_and_b32 v3, 1, v1\n"
"v_lshl_add_u32 v8, v3, 6, v8\n"
"v_add_u32 v8, 1024, v8\n"
"s_lshl_b32 s48, s26, 2\n"
"v_add_u32 v10, s48, v9\n"
"v_and_b32 v11, 15, v1\n"
"v_add_u32 v3, 64, v1\n"
"v_cmp_eq_u32 vcc, 0, v11\n"
"v_add_u32 v9, 73728, v8\n"
"v_xor_b32 v9, v9, v8\n"
"v_cndmask_b32 v10, v3, v10, vcc\n"
"v_lshlrev_b32 v10, 2, v10\n"
"v_add_u32 v10, 147456, v10\n"
"v_lshlrev_b32 v11, 2, v11\n"
"v_add_u32 v11, 147456, v11\n"
"s_lshl_b32 s48, s26, 3\n"
"s_add_u32 s48, s48, 96\n"
"v_add_u32 v14, s48, v1\n"
"v_lshrrev_b32 v14, 2, v14\n"
"v_lshlrev_b32 v14, 7, v14\n"
"v_cmp_gt_u32 vcc, 8, v1\n"
"v_mov_b32 v3, 0x7fff0000\n"
"s_nop 0\n"
"v_cndmask_b32 v14, v3, v14, vcc\n"
"v_cndmask_b32 v16, 0, v16, vcc\n"
"v_cndmask_b32 v17, 0, v17, vcc\n"
"s_mov_b32 s48, 20000\n"
"s_branch .L_rfga\n"
".L_rlga:\n"
"s_sleep 8\n"
".L_rfga:\n"
"buffer_load_dwordx2 v[12:13], v14, s[32:35], s66 offen sc1\n"
"s_sub_u32 s48, s48, 1\n"
"s_cbranch_scc1 .L_end\n"
"s_waitcnt vmcnt(0)\n"
"v_cmp_ne_u64 vcc, v[16:17], v[12:13]\n"
"s_cbranch_vccnz .L_rlga\n"
"s_add_u32 s66, s66, 890880\n"
"s_add_u32 m0, s64, 0\n"
"s_nop 0\n"
"buffer_load_dwordx4 v2, s[32:35], s63 offen offset:0 sc1 lds\n"
"buffer_load_dwordx4 v2, s[32:35], s63 offen offset:1024 sc1 lds\n"
"buffer_load_dwordx4 v2, s[32:35], s63 offen offset:2048 sc1 lds\n"
"buffer_load_dwordx4 v2, s[32:35], s63 offen offset:3072 sc1 lds\n"
"s_add_u32 m0, s64, 4096\n"
"s_nop 0\n"
"buffer_load_dwordx4 v6, s[32:35], s63 offen offset:0 sc1 lds\n"
"buffer_load_dwordx4 v6, s[32:35], s63 offen offset:1024 sc1 lds\n"
"buffer_load_dwordx4 v6, s[32:35], s63 offen offset:2048 sc1 lds\n"
"buffer_load_dwordx4 v6, s[32:35], s63 offen offset:3072 sc1 lds\n"
"s_add_u32 m0, s64, 8192\n"
"s_nop 0\n"
"buffer_load_dwordx4 v7, s[32:35], s63 offen offset:0 sc1 lds\n"
"s_add_u32 s63, s63, 890880\n"
"s_mov_b32 s48, 20000\n"
"s_branch .L_rfgb\n"
".L_rlgb:\n"
"s_sleep 8\n"
".L_rfgb:\n"
"buffer_load_dwordx2 v[12:13], v14, s[32:35], s66 offen sc1\n"
"s_sub_u32 s48, s48, 1\n"
"s_cbranch_scc1 .L_end\n"
"s_waitcnt vmcnt(0)\n"
"v_cmp_ne_u64 vcc, v[16:17], v[12:13]\n"
"s_cbranch_vccnz .L_rlgb\n"
"s_add_u32 s66, s66, 890880\n"
"s_add_u32 m0, s64, 36864\n"
"s_nop 0\n"
"buffer_load_dwordx4 v2, s[32:35], s63 offen offset:0 sc1 lds\n"
"buffer_load_dwordx4 v2, s[32:35], s63 offen offset:1024 sc1 lds\n"
"buffer_load_dwordx4 v2, s[32:35], s63 offen offset:2048 sc1 lds\n"
"buffer_load_dwordx4 v2, s[32:35], s63 offen offset:3072 sc1 lds\n"
"s_add_u32 m0, s64, 40960\n"
"s_nop 0\n"
"buffer_load_dwordx4 v6, s[32:35], s63 offen offset:0 sc1 lds\n"
"buffer_load_dwordx4 v6, s[32:35], s63 offen offset:1024 sc1 lds\n"
"buffer_load_dwordx4 v6, s[32:35], s63 offen offset:2048 sc1 lds\n"
"buffer_load_dwordx4 v6, s[32:35], s63 offen offset:3072 sc1 lds\n"
"s_add_u32 m0, s64, 45056\n"
"s_nop 0\n"
"buffer_load_dwordx4 v7, s[32:35], s63 offen offset:0 sc1 lds\n"
"s_add_u32 s63, s63, 890880\n"
"s_xor_b32 s64, s64, s65\n"
"buffer_load_dwordx2 v[12:13], v14, s[32:35], s66 offen sc1\n"
"s_waitcnt vmcnt(0)\n"
"s_barrier\n"
"ds_read_b128 v[76:79], v4 offset:0\n"
"ds_read_b128 v[80:83], v4 offset:1152\n"
"ds_read_b128 v[84:87], v4 offset:2304\n"
"ds_read_b128 v[88:91], v4 offset:3456\n"
"ds_read_b128 v[92:95], v4 offset:4608\n"
"ds_read_b128 v[96:99], v4 offset:5760\n"
"ds_read_b128 v[100:103], v4 offset:6912\n"
"ds_read_b128 v[104:107], v4 offset:8064\n"
"s_cmp_eq_u32 s26, 1\n"
"s_cbranch_scc1 .L_gv1\n"
"s_cmp_eq_u32 s26, 2\n"
"s_cbranch_scc1 .L_gv2\n"
"s_cmp_eq_u32 s26, 3\n"
"s_cbranch_scc1 .L_gv3\n"
".p2align 6\n"
".L_gv0:\n"
".L_gl_0:\n"
"s_waitcnt lgkmcnt(4)\n"
"v_mfma_f32_16x16x32_bf16 v[32:35], a[0:3], v[76:79], 0\n"
"ds_read_b128 v[108:111], v4 offset:9216\n"
"ds_read_b32 v62, v11 offset:128\n"
"v_mfma_f32_16x16x32_bf16 v[32:35], a[12:15], v[80:83], v[32:35]\n"
"s_add_u32 s68, s68, 890880\n"
"v_mfma_f32_16x16x32_bf16 v[32:35], a[24:27], v[84:87], v[32:35]\n"
"v_mfma_f32_16x16x32_bf16 v[32:35], a[36:39], v[88:91], v[32:35]\n"
"ds_read_b128 v[112:115], v4 offset:10368\n"
"v_mfma_f32_16x16x32_bf16 v[36:39], a[40:43], v[88:91], 0\n"
"ds_read2st64_b32 v[56:57], v8 offset0:0 offset1:36\n"
"v_mfma_f32_16x16x32_bf16 v[36:39], a[28:31], v[84:87], v[36:39]\n"
"s_waitcnt vmcnt(4)\n"
"v_cmp_ne_u64 vcc, v[16:17], v[12:13]\n"
"v_mfma_f32_16x16x32_bf16 v[36:39], a[16:19], v[80:83], v[36:39]\n"
"ds_read_b128 v[116:119], v4 offset:11520\n"
"ds_read2st64_b32 v[58:59], v8 offset0:72 offset1:108\n"
"v_mfma_f32_16x16x32_bf16 v[36:39], a[4:7], v[76:79], v[36:39]\n"
"s_cbranch_vccnz .L_rtg0_0\n"
".L_bkg0_0:\n"
"s_add_u32 s66, s66, 890880\n"
"v_mfma_f32_16x16x32_bf16 v[40:43], a[8:11], v[76:79], 0\n"
"s_add_u32 m0, s64, 0\n"
"s_waitcnt lgkmcnt(4)\n"
"v_mfma_f32_16x16x32_bf16 v[40:43], a[20:23], v[80:83], v[40:43]\n"
"buffer_load_dwordx4 v2, s[32:35], s63 offen offset:0 sc1 lds\n"
"ds_read_b128 v[120:123], v4 offset:12672\n"
"v_mfma_f32_16x16x32_bf16 v[40:43], a[32:35], v[84:87], v[40:43]\n"
"v_lshlrev_b32 v60, 16, v62\n"
"v_and_b32 v62, 0xffff0000, v62\n"
"v_mfma_f32_16x16x32_bf16 v[40:43], a[44:47], v[88:91], v[40:43]\n"
"v_fma_f32 v52, -v60, v28, v52\n"
"v_mfma_f32_16x16x32_bf16 v[40:43], a[56:59], v[92:95], v[40:43]\n"
"ds_read_b128 v[124:127], v4 offset:13824\n"
"v_fma_f32 v53, -v60, v29, v53\n"
"v_mfma_f32_16x16x32_bf16 v[40:43], a[68:71], v[96:99], v[40:43]\n"
"v_rcp_f32 v61, v62\n"
"v_mfma_f32_16x16x32_bf16 v[40:43], a[80:83], v[100:103], v[40:43]\n"
"v_mul_f32 v62, v62, v62\n"
"v_mul_f32 v61, 0xbfb8aa3b, v61\n"
"v_mfma_f32_16x16x32_bf16 v[40:43], a[92:95], v[104:107], v[40:43]\n"
"ds_read_b128 v[76:79], v4 offset:14976\n"
"v_fma_f32 v54, -v60, v30, v54\n"
"v_mfma_f32_16x16x32_bf16 v[36:39], a[88:91], v[104:107], v[36:39]\n"
"v_fma_f32 v55, -v60, v31, v55\n"
"v_fma_f32 v48, -v60, v24, v48\n"
"v_mfma_f32_16x16x32_bf16 v[36:39], a[76:79], v[100:103], v[36:39]\n"
"buffer_load_dwordx4 v2, s[32:35], s63 offen offset:1024 sc1 lds\n"
"v_fma_f32 v49, -v60, v25, v49\n"
"v_mfma_f32_16x16x32_bf16 v[36:39], a[64:67], v[96:99], v[36:39]\n"
"ds_read_b128 v[80:83], v4 offset:16128\n"
"v_fma_f32 v50, -v60, v26, v50\n"
"v_mfma_f32_16x16x32_bf16 v[36:39], a[52:55], v[92:95], v[36:39]\n"
"v_permlane32_swap_b32 v52, v54\n"
"v_mfma_f32_16x16x32_bf16 v[32:35], a[48:51], v[92:95], v[32:35]\n"
"v_permlane32_swap_b32 v53, v55\n"
"v_mfma_f32_16x16x32_bf16 v[32:35], a[60:63], v[96:99], v[32:35]\n"
"ds_read_b128 v[84:87], v4 offset:17280\n"
"v_fma_f32 v51, -v60, v27, v51\n"
"v_mfma_f32_16x16x32_bf16 v[32:35], a[72:75], v[100:103], v[32:35]\n"
"v_fma_f32 v44, -v60, v20, v44\n"
"v_fma_f32 v45, -v60, v21, v45\n"
"v_mfma_f32_16x16x32_bf16 v[32:35], a[84:87], v[104:107], v[32:35]\n"
"v_fma_f32 v46, -v60, v22, v46\n"
"s_waitcnt lgkmcnt(4)\n"
"v_mfma_f32_16x16x32_bf16 v[32:35], a[96:99], v[108:111], v[32:35]\n"
"ds_read_b128 v[88:91], v4 offset:18432\n"
"v_add_f32 v56, v56, v57\n"
"v_mfma_f32_16x16x32_bf16 v[32:35], a[108:111], v[112:115], v[32:35]\n"
"buffer_load_dwordx4 v2, s[32:35], s63 offen offset:2048 sc1 lds\n"
"v_add_f32 v58, v58, v59\n"
"v_mfma_f32_16x16x32_bf16 v[32:35], a[120:123], v[116:119], v[32:35]\n"
"v_add_f32 v56, v56, v58\n"
"v_fma_f32 v47, -v60, v23, v47\n"
"v_mfma_f32_16x16x32_bf16 v[32:35], a[132:135], v[120:123], v[32:35]\n"
"ds_read_b128 v[92:95], v4 offset:19584\n"
"v_add_f32_dpp v56, v56, v56 row_ror:8 row_mask:0xf bank_mask:0xf\n"
"v_mfma_f32_16x16x32_bf16 v[36:39], a[136:139], v[120:123], v[36:39]\n"
"v_mul_f32 v64, v44, v61\n"
"v_add_f32_dpp v56, v56, v56 row_ror:4 row_mask:0xf bank_mask:0xf\n"
"v_mfma_f32_16x16x32_bf16 v[36:39], a[124:127], v[116:119], v[36:39]\n"
"v_exp_f32 v64, v64\n"
"v_mfma_f32_16x16x32_bf16 v[36:39], a[112:115], v[112:115], v[36:39]\n"
"ds_read_b128 v[96:99], v4 offset:20736\n"
"v_add_f32_dpp v56, v56, v56 row_ror:2 row_mask:0xf bank_mask:0xf\n"
"v_mfma_f32_16x16x32_bf16 v[36:39], a[100:103], v[108:111], v[36:39]\n"
"v_mul_f32 v56, 0x3a800000, v56\n"
"v_mul_f32 v65, v45, v61\n"
"v_mfma_f32_16x16x32_bf16 v[40:43], a[104:107], v[108:111], v[40:43]\n"
"v_exp_f32 v65, v65\n"
"v_mfma_f32_16x16x32_bf16 v[40:43], a[116:119], v[112:115], v[40:43]\n"
"buffer_load_dwordx4 v2, s[32:35], s63 offen offset:3072 sc1 lds\n"
"ds_read_b128 v[100:103], v4 offset:21888\n"
"v_mfma_f32_16x16x32_bf16 v[40:43], a[128:131], v[116:119], v[40:43]\n"
"v_mul_f32 v57, v56, v56\n"
"v_mul_f32 v66, v46, v61\n"
"v_mfma_f32_16x16x32_bf16 v[40:43], a[140:143], v[120:123], v[40:43]\n"
"v_sub_f32_dpp v57, v56, v57 quad_perm:[1,1,3,3] row_mask:0xf bank_mask:0xf\n"
"s_waitcnt lgkmcnt(4)\n"
"v_mfma_f32_16x16x32_bf16 v[40:43], a[152:155], v[124:127], v[40:43]\n"
"ds_read_b128 v[104:107], v4 offset:23040\n"
"v_max_f32 v57, 0, v57\n"
"v_mfma_f32_16x16x32_bf16 v[40:43], a[164:167], v[76:79], v[40:43]\n"
"v_exp_f32 v66, v66\n"
"v_mfma_f32_16x16x32_bf16 v[40:43], a[176:179], v[80:83], v[40:43]\n"
"v_cvt_pk_bf16_f32 v57, 0, v57\n"
"v_mul_f32 v67, v47, v61\n"
"v_mfma_f32_16x16x32_bf16 v[40:43], a[188:191], v[84:87], v[40:43]\n"
"ds_read_b128 v[108:111], v4 offset:24192\n"
"s_add_u32 m0, s64, 4096\n"
"v_mfma_f32_16x16x32_bf16 v[36:39], a[184:187], v[84:87], v[36:39]\n"
"v_exp_f32 v67, v67\n"
"v_mfma_f32_16x16x32_bf16 v[36:39], a[172:175], v[80:83], v[36:39]\n"
"buffer_load_dwordx4 v6, s[32:35], s63 offen offset:0 sc1 lds\n"
"v_add_f32 v57, 0x3727c5ac, v57\n"
"v_mfma_f32_16x16x32_bf16 v[36:39], a[160:163], v[76:79], v[36:39]\n"
"ds_read_b128 v[112:115], v4 offset:25344\n"
"v_cvt_pk_bf16_f32 v57, 0, v57\n"
"v_mfma_f32_16x16x32_bf16 v[36:39], a[148:151], v[124:127], v[36:39]\n"
"v_sqrt_f32 v57, v57\n"
"v_mfma_f32_16x16x32_bf16 v[32:35], a[144:147], v[124:127], v[32:35]\n"
"v_cvt_pk_bf16_f32 v57, v56, v57\n"
"v_mul_f32 v68, v52, v61\n"
"v_mfma_f32_16x16x32_bf16 v[32:35], a[156:159], v[76:79], v[32:35]\n"
"ds_read_b128 v[116:119], v4 offset:26496\n"
"ds_write_b32 v10, v57 offset:0\n"
"v_mfma_f32_16x16x32_bf16 v[32:35], a[168:171], v[80:83], v[32:35]\n"
"v_exp_f32 v68, v68\n"
"v_mfma_f32_16x16x32_bf16 v[32:35], a[180:183], v[84:87], v[32:35]\n"
"v_mul_f32 v69, v53, v61\n"
"s_waitcnt lgkmcnt(5)\n"
"v_mfma_f32_16x16x32_bf16 v[32:35], a[192:195], v[88:91], v[32:35]\n"
"ds_read_b128 v[120:123], v4 offset:27648\n"
"v_mul_f32 v44, v44, v48\n"
"v_mfma_f32_16x16x32_bf16 v[32:35], a[204:207], v[92:95], v[32:35]\n"
"buffer_load_dwordx4 v6, s[32:35], s63 offen offset:1024 sc1 lds\n"
"v_mul_f32 v45, v45, v49\n"
"v_mfma_f32_16x16x32_bf16 v[32:35], a[216:219], v[96:99], v[32:35]\n"
"v_exp_f32 v69, v69\n"
"v_mfma_f32_16x16x32_bf16 v[32:35], a[228:231], v[100:103], v[32:35]\n"
"ds_read_b128 v[124:127], v4 offset:28800\n"
"v_mul_f32 v46, v46, v50\n"
"v_mfma_f32_16x16x32_bf16 v[36:39], a[232:235], v[100:103], v[36:39]\n"
"v_mul_f32 v47, v47, v51\n"
"v_mul_f32 v52, v52, v54\n"
"v_mfma_f32_16x16x32_bf16 v[36:39], a[220:223], v[96:99], v[36:39]\n"
"buffer_load_dwordx2 v[12:13], v14, s[32:35], s66 offen sc1\n"
"v_mul_f32 v53, v53, v55\n"
"v_mfma_f32_16x16x32_bf16 v[36:39], a[208:211], v[92:95], v[36:39]\n"
"ds_read_b128 v[76:79], v4 offset:29952\n"
"v_fma_f32 v64, v64, v62, v62\n"
"v_mfma_f32_16x16x32_bf16 v[36:39], a[196:199], v[88:91], v[36:39]\n"
"v_rcp_f32 v64, v64\n"
"v_mfma_f32_16x16x32_bf16 v[40:43], a[200:203], v[88:91], v[40:43]\n"
"v_fma_f32 v65, v65, v62, v62\n"
"v_fma_f32 v66, v66, v62, v62\n"
"v_mfma_f32_16x16x32_bf16 v[40:43], a[212:215], v[92:95], v[40:43]\n"
"buffer_load_dwordx4 v6, s[32:35], s63 offen offset:2048 sc1 lds\n"
"ds_read_b128 v[80:83], v4 offset:31104\n"
"v_mfma_f32_16x16x32_bf16 v[40:43], a[224:227], v[96:99], v[40:43]\n"
"v_rcp_f32 v65, v65\n"
"v_mfma_f32_16x16x32_bf16 v[40:43], a[236:239], v[100:103], v[40:43]\n"
"v_fma_f32 v67, v67, v62, v62\n"
"s_waitcnt lgkmcnt(5)\n"
"v_mfma_f32_16x16x32_bf16 v[40:43], a[248:251], v[104:107], v[40:43]\n"
"ds_read_b128 v[84:87], v4 offset:32256\n"
"v_fma_f32 v68, v68, v62, v62\n"
"v_mfma_f32_16x16x32_bf16 v[40:43], v[132:135], v[108:111], v[40:43]\n"
"v_rcp_f32 v66, v66\n"
"v_mfma_f32_16x16x32_bf16 v[40:43], v[144:147], v[112:115], v[40:43]\n"
"v_rcp_f32 v67, v67\n"
"v_mfma_f32_16x16x32_bf16 v[40:43], v[156:159], v[116:119], v[40:43]\n"
"ds_read_b128 v[88:91], v4 offset:33408\n"
"v_fma_f32 v69, v69, v62, v62\n"
"v_mfma_f32_16x16x32_bf16 v[36:39], v[152:155], v[116:119], v[36:39]\n"
"v_rcp_f32 v68, v68\n"
"v_mfma_f32_16x16x32_bf16 v[36:39], v[140:143], v[112:115], v[36:39]\n"
"buffer_load_dwordx4 v6, s[32:35], s63 offen offset:3072 sc1 lds\n"
"v_mul_f32 v44, v44, v64\n"
"v_mfma_f32_16x16x32_bf16 v[36:39], v[128:131], v[108:111], v[36:39]\n"
"ds_read_b128 v[92:95], v4 offset:34560\n"
"v_mul_f32 v45, v45, v65\n"
"v_mfma_f32_16x16x32_bf16 v[36:39], a[244:247], v[104:107], v[36:39]\n"
"v_rcp_f32 v69, v69\n"
"v_mfma_f32_16x16x32_bf16 v[32:35], a[240:243], v[104:107], v[32:35]\n"
"v_mul_f32 v46, v46, v66\n"
"v_mul_f32 v47, v47, v67\n"
"v_mfma_f32_16x16x32_bf16 v[32:35], a[252:255], v[108:111], v[32:35]\n"
"ds_read_b128 v[96:99], v4 offset:35712\n"
"v_mul_f32 v52, v52, v68\n"
"v_mfma_f32_16x16x32_bf16 v[32:35], v[136:139], v[112:115], v[32:35]\n"
"v_xor_b32 v4, 73728, v4\n"
"v_mul_f32 v53, v53, v69\n"
"v_mfma_f32_16x16x32_bf16 v[32:35], v[148:151], v[116:119], v[32:35]\n"
"s_waitcnt lgkmcnt(4)\n"
"s_waitcnt vmcnt(9)\n"
"s_barrier\n"
"v_mfma_f32_16x16x32_bf16 v[32:35], v[160:163], v[120:123], v[32:35]\n"
"ds_read_b128 v[100:103], v5 offset:0\n"
"s_add_u32 m0, s64, 8192\n"
"v_mfma_f32_16x16x32_bf16 v[32:35], v[172:175], v[124:127], v[32:35]\n"
"buffer_load_dwordx4 v7, s[32:35], s63 offen offset:0 sc1 lds\n"
"s_add_u32 s63, s63, 890880\n"
"v_mfma_f32_16x16x32_bf16 v[32:35], v[184:187], v[76:79], v[32:35]\n"
"v_cvt_pk_bf16_f32 v74, v52, v53\n"
"v_cvt_pk_bf16_f32 v72, v44, v45\n"
"v_mfma_f32_16x16x32_bf16 v[32:35], v[196:199], v[80:83], v[32:35]\n"
"ds_read_b128 v[104:107], v5 offset:1152\n"
"v_cvt_pk_bf16_f32 v73, v46, v47\n"
"v_mfma_f32_16x16x32_bf16 v[36:39], v[200:203], v[80:83], v[36:39]\n"
"v_permlane32_swap_b32 v74, v75\n"
"v_mfma_f32_16x16x32_bf16 v[36:39], v[188:191], v[76:79], v[36:39]\n"
"v_mfma_f32_16x16x32_bf16 v[36:39], v[176:179], v[124:127], v[36:39]\n"
"ds_read_b128 v[108:111], v5 offset:2304\n"
"v_mfma_f32_16x16x32_bf16 v[36:39], v[164:167], v[120:123], v[36:39]\n"
"v_permlane16_swap_b32 v72, v74\n"
"v_mfma_f32_16x16x32_bf16 v[40:43], v[168:171], v[120:123], v[40:43]\n"
"v_permlane16_swap_b32 v73, v75\n"
"v_mfma_f32_16x16x32_bf16 v[40:43], v[180:183], v[124:127], v[40:43]\n"
"buffer_store_dwordx4 v[72:75], v15, s[32:35], s68 offen sc1\n"
"ds_read_b128 v[112:115], v5 offset:3456\n"
"v_mfma_f32_16x16x32_bf16 v[40:43], v[192:195], v[76:79], v[40:43]\n"
"v_mfma_f32_16x16x32_bf16 v[40:43], v[204:207], v[80:83], v[40:43]\n"
"s_waitcnt lgkmcnt(4)\n"
"v_mfma_f32_16x16x32_bf16 v[40:43], v[216:219], v[84:87], v[40:43]\n"
"ds_read_b128 v[116:119], v5 offset:4608\n"
"v_mfma_f32_16x16x32_bf16 v[40:43], v[228:231], v[88:91], v[40:43]\n"
"v_mfma_f32_16x16x32_bf16 v[40:43], v[240:243], v[92:95], v[40:43]\n"
"v_mfma_f32_16x16x32_bf16 v[40:43], v[252:255], v[96:99], v[40:43]\n"
"ds_read_b128 v[120:123], v5 offset:5760\n"
"v_mfma_f32_16x16x32_bf16 v[36:39], v[248:251], v[96:99], v[36:39]\n"
"v_mfma_f32_16x16x32_bf16 v[36:39], v[236:239], v[92:95], v[36:39]\n"
"v_mfma_f32_16x16x32_bf16 v[36:39], v[224:227], v[88:91], v[36:39]\n"
"ds_read_b128 v[124:127], v5 offset:6912\n"
"v_mfma_f32_16x16x32_bf16 v[36:39], v[212:215], v[84:87], v[36:39]\n"
"v_mfma_f32_16x16x32_bf16 v[32:35], v[208:211], v[84:87], v[32:35]\n"
"v_mfma_f32_16x16x32_bf16 v[32:35], v[220:223], v[88:91], v[32:35]\n"
"ds_read_b128 v[76:79], v5 offset:8064\n"
"v_mfma_f32_16x16x32_bf16 v[32:35], v[232:235], v[92:95], v[32:35]\n"
"s_sub_u32 s62, s62, 1\n"
"v_mfma_f32_16x16x32_bf16 v[32:35], v[244:247], v[96:99], v[32:35]\n"
"s_cbranch_scc1 .L_gt0_0\n"
"s_waitcnt lgkmcnt(4)\n"
"v_mfma_f32_16x16x32_bf16 v[44:47], a[0:3], v[100:103], 0\n"
"ds_read_b128 v[80:83], v5 offset:9216\n"
"ds_read_b32 v62, v11 offset:0\n"
"v_mfma_f32_16x16x32_bf16 v[44:47], a[12:15], v[104:107], v[44:47]\n"
"s_add_u32 s68, s68, 890880\n"
"v_mfma_f32_16x16x32_bf16 v[44:47], a[24:27], v[108:111], v[44:47]\n"
"v_mfma_f32_16x16x32_bf16 v[44:47], a[36:39], v[112:115], v[44:47]\n"
"ds_read_b128 v[84:87], v5 offset:10368\n"
"v_mfma_f32_16x16x32_bf16 v[48:51], a[40:43], v[112:115], 0\n"
"ds_read2st64_b32 v[56:57], v8 offset0:144 offset1:180\n"
"v_mfma_f32_16x16x32_bf16 v[48:51], a[28:31], v[108:111], v[48:51]\n"
"s_waitcnt vmcnt(4)\n"
"v_cmp_ne_u64 vcc, v[16:17], v[12:13]\n"
"v_mfma_f32_16x16x32_bf16 v[48:51], a[16:19], v[104:107], v[48:51]\n"
"ds_read_b128 v[88:91], v5 offset:11520\n"
"ds_read2st64_b32 v[58:59], v8 offset0:216 offset1:252\n"
"v_mfma_f32_16x16x32_bf16 v[48:51], a[4:7], v[100:103], v[48:51]\n"
"s_cbranch_vccnz .L_rtg1_0\n"
".L_bkg1_0:\n"
"s_add_u32 s66, s66, 890880\n"
"v_mfma_f32_16x16x32_bf16 v[52:55], a[8:11], v[100:103], 0\n"
"s_add_u32 m0, s64, 36864\n"
"v_xor_b32 v8, v9, v8\n"
"v_mfma_f32_16x16x32_bf16 v[52:55], a[20:23], v[104:107], v[52:55]\n"
"buffer_load_dwordx4 v2, s[32:35], s63 offen offset:0 sc1 lds\n"
"ds_read_b128 v[92:95], v5 offset:12672\n"
"v_mfma_f32_16x16x32_bf16 v[52:55], a[32:35], v[108:111], v[52:55]\n"
"s_waitcnt lgkmcnt(5)\n"
"v_lshlrev_b32 v60, 16, v62\n"
"v_mfma_f32_16x16x32_bf16 v[52:55], a[44:47], v[112:115], v[52:55]\n"
"v_and_b32 v62, 0xffff0000, v62\n"
"v_mfma_f32_16x16x32_bf16 v[52:55], a[56:59], v[116:119], v[52:55]\n"
"ds_read_b128 v[96:99], v5 offset:13824\n"
"v_fma_f32 v40, -v60, v28, v40\n"
"v_mfma_f32_16x16x32_bf16 v[52:55], a[68:71], v[120:123], v[52:55]\n"
"v_rcp_f32 v61, v62\n"
"v_mfma_f32_16x16x32_bf16 v[52:55], a[80:83], v[124:127], v[52:55]\n"
"v_mul_f32 v62, v62, v62\n"
"v_mul_f32 v61, 0xbfb8aa3b, v61\n"
"v_mfma_f32_16x16x32_bf16 v[52:55], a[92:95], v[76:79], v[52:55]\n"
"ds_read_b128 v[100:103], v5 offset:14976\n"
"v_fma_f32 v41, -v60, v29, v41\n"
"v_mfma_f32_16x16x32_bf16 v[48:51], a[88:91], v[76:79], v[48:51]\n"
"v_fma_f32 v42, -v60, v30, v42\n"
"v_fma_f32 v43, -v60, v31, v43\n"
"v_mfma_f32_16x16x32_bf16 v[48:51], a[76:79], v[124:127], v[48:51]\n"
"buffer_load_dwordx4 v2, s[32:35], s63 offen offset:1024 sc1 lds\n"
"v_fma_f32 v36, -v60, v24, v36\n"
"v_mfma_f32_16x16x32_bf16 v[48:51], a[64:67], v[120:123], v[48:51]\n"
"ds_read_b128 v[104:107], v5 offset:16128\n"
"v_fma_f32 v37, -v60, v25, v37\n"
"v_mfma_f32_16x16x32_bf16 v[48:51], a[52:55], v[116:119], v[48:51]\n"
"v_permlane32_swap_b32 v40, v42\n"
"v_mfma_f32_16x16x32_bf16 v[44:47], a[48:51], v[116:119], v[44:47]\n"
"v_permlane32_swap_b32 v41, v43\n"
"v_mfma_f32_16x16x32_bf16 v[44:47], a[60:63], v[120:123], v[44:47]\n"
"ds_read_b128 v[108:111], v5 offset:17280\n"
"v_fma_f32 v38, -v60, v26, v38\n"
"v_mfma_f32_16x16x32_bf16 v[44:47], a[72:75], v[124:127], v[44:47]\n"
"v_fma_f32 v39, -v60, v27, v39\n"
"v_fma_f32 v32, -v60, v20, v32\n"
"v_mfma_f32_16x16x32_bf16 v[44:47], a[84:87], v[76:79], v[44:47]\n"
"v_fma_f32 v33, -v60, v21, v33\n"
"s_waitcnt lgkmcnt(4)\n"
"v_mfma_f32_16x16x32_bf16 v[44:47], a[96:99], v[80:83], v[44:47]\n"
"ds_read_b128 v[112:115], v5 offset:18432\n"
"v_add_f32 v56, v56, v57\n"
"v_mfma_f32_16x16x32_bf16 v[44:47], a[108:111], v[84:87], v[44:47]\n"
"buffer_load_dwordx4 v2, s[32:35], s63 offen offset:2048 sc1 lds\n"
"v_add_f32 v58, v58, v59\n"
"v_mfma_f32_16x16x32_bf16 v[44:47], a[120:123], v[88:91], v[44:47]\n"
"v_add_f32 v56, v56, v58\n"
"v_fma_f32 v34, -v60, v22, v34\n"
"v_mfma_f32_16x16x32_bf16 v[44:47], a[132:135], v[92:95], v[44:47]\n"
"ds_read_b128 v[116:119], v5 offset:19584\n"
"v_add_f32_dpp v56, v56, v56 row_ror:8 row_mask:0xf bank_mask:0xf\n"
"v_mfma_f32_16x16x32_bf16 v[48:51], a[136:139], v[92:95], v[48:51]\n"
"v_fma_f32 v35, -v60, v23, v35\n"
"v_add_f32_dpp v56, v56, v56 row_ror:4 row_mask:0xf bank_mask:0xf\n"
"v_mfma_f32_16x16x32_bf16 v[48:51], a[124:127], v[88:91], v[48:51]\n"
"v_mul_f32 v64, v32, v61\n"
"v_add_f32_dpp v56, v56, v56 row_ror:2 row_mask:0xf bank_mask:0xf\n"
"v_mfma_f32_16x16x32_bf16 v[48:51], a[112:115], v[84:87], v[48:51]\n"
"ds_read_b128 v[120:123], v5 offset:20736\n"
"v_mul_f32 v56, 0x3a800000, v56\n"
"v_mfma_f32_16x16x32_bf16 v[48:51], a[100:103], v[80:83], v[48:51]\n"
"v_exp_f32 v64, v64\n"
"v_mfma_f32_16x16x32_bf16 v[52:55], a[104:107], v[80:83], v[52:55]\n"
"v_mul_f32 v57, v56, v56\n"
"v_mul_f32 v65, v33, v61\n"
"v_mfma_f32_16x16x32_bf16 v[52:55], a[116:119], v[84:87], v[52:55]\n"
"buffer_load_dwordx4 v2, s[32:35], s63 offen offset:3072 sc1 lds\n"
"ds_read_b128 v[124:127], v5 offset:21888\n"
"v_mfma_f32_16x16x32_bf16 v[52:55], a[128:131], v[88:91], v[52:55]\n"
"v_exp_f32 v65, v65\n"
"v_mfma_f32_16x16x32_bf16 v[52:55], a[140:143], v[92:95], v[52:55]\n"
"v_sub_f32_dpp v57, v56, v57 quad_perm:[1,1,3,3] row_mask:0xf bank_mask:0xf\n"
"s_waitcnt lgkmcnt(4)\n"
"v_mfma_f32_16x16x32_bf16 v[52:55], a[152:155], v[96:99], v[52:55]\n"
"ds_read_b128 v[76:79], v5 offset:23040\n"
"v_max_f32 v57, 0, v57\n"
"v_mfma_f32_16x16x32_bf16 v[52:55], a[164:167], v[100:103], v[52:55]\n"
"v_cvt_pk_bf16_f32 v57, 0, v57\n"
"v_mul_f32 v66, v34, v61\n"
"v_mfma_f32_16x16x32_bf16 v[52:55], a[176:179], v[104:107], v[52:55]\n"
"v_exp_f32 v66, v66\n"
"v_mfma_f32_16x16x32_bf16 v[52:55], a[188:191], v[108:111], v[52:55]\n"
"ds_read_b128 v[80:83], v5 offset:24192\n"
"s_add_u32 m0, s64, 40960\n"
"v_mfma_f32_16x16x32_bf16 v[48:51], a[184:187], v[108:111], v[48:51]\n"
"v_add_f32 v57, 0x3727c5ac, v57\n"
"v_mul_f32 v67, v35, v61\n"
"v_mfma_f32_16x16x32_bf16 v[48:51], a[172:175], v[104:107], v[48:51]\n"
"buffer_load_dwordx4 v6, s[32:35], s63 offen offset:0 sc1 lds\n"
"v_cvt_pk_bf16_f32 v57, 0, v57\n"
"v_mfma_f32_16x16x32_bf16 v[48:51], a[160:163], v[100:103], v[48:51]\n"
"ds_read_b128 v[84:87], v5 offset:25344\n"
"v_mul_f32 v68, v40, v61\n"
"v_mfma_f32_16x16x32_bf16 v[48:51], a[148:151], v[96:99], v[48:51]\n"
"v_sqrt_f32 v57, v57\n"
"v_mfma_f32_16x16x32_bf16 v[44:47], a[144:147], v[96:99], v[44:47]\n"
"v_exp_f32 v67, v67\n"
"v_mfma_f32_16x16x32_bf16 v[44:47], a[156:159], v[100:103], v[44:47]\n"
"ds_read_b128 v[88:91], v5 offset:26496\n"
"v_cvt_pk_bf16_f32 v57, v56, v57\n"
"v_mfma_f32_16x16x32_bf16 v[44:47], a[168:171], v[104:107], v[44:47]\n"
"ds_write_b32 v10, v57 offset:128\n"
"v_mul_f32 v69, v41, v61\n"
"v_mfma_f32_16x16x32_bf16 v[44:47], a[180:183], v[108:111], v[44:47]\n"
"v_mul_f32 v32, v32, v36\n"
"s_waitcnt lgkmcnt(5)\n"
"v_mfma_f32_16x16x32_bf16 v[44:47], a[192:195], v[112:115], v[44:47]\n"
"ds_read_b128 v[92:95], v5 offset:27648\n"
"v_mul_f32 v33, v33, v37\n"
"v_mfma_f32_16x16x32_bf16 v[44:47], a[204:207], v[116:119], v[44:47]\n"
"buffer_load_dwordx4 v6, s[32:35], s63 offen offset:1024 sc1 lds\n"
"v_mul_f32 v34, v34, v38\n"
"v_mfma_f32_16x16x32_bf16 v[44:47], a[216:219], v[120:123], v[44:47]\n"
"v_exp_f32 v68, v68\n"
"v_mfma_f32_16x16x32_bf16 v[44:47], a[228:231], v[124:127], v[44:47]\n"
"ds_read_b128 v[96:99], v5 offset:28800\n"
"v_mul_f32 v35, v35, v39\n"
"v_mfma_f32_16x16x32_bf16 v[48:51], a[232:235], v[124:127], v[48:51]\n"
"v_exp_f32 v69, v69\n"
"v_mfma_f32_16x16x32_bf16 v[48:51], a[220:223], v[120:123], v[48:51]\n"
"buffer_load_dwordx2 v[12:13], v14, s[32:35], s66 offen sc1\n"
"v_mul_f32 v40, v40, v42\n"
"v_mfma_f32_16x16x32_bf16 v[48:51], a[208:211], v[116:119], v[48:51]\n"
"ds_read_b128 v[100:103], v5 offset:29952\n"
"v_mul_f32 v41, v41, v43\n"
"v_mfma_f32_16x16x32_bf16 v[48:51], a[196:199], v[112:115], v[48:51]\n"
"v_fma_f32 v64, v64, v62, v62\n"
"v_fma_f32 v65, v65, v62, v62\n"
"v_mfma_f32_16x16x32_bf16 v[52:55], a[200:203], v[112:115], v[52:55]\n"
"v_rcp_f32 v64, v64\n"
"v_mfma_f32_16x16x32_bf16 v[52:55], a[212:215], v[116:119], v[52:55]\n"
"buffer_load_dwordx4 v6, s[32:35], s63 offen offset:2048 sc1 lds\n"
"ds_read_b128 v[104:107], v5 offset:31104\n"
"v_mfma_f32_16x16x32_bf16 v[52:55], a[224:227], v[120:123], v[52:55]\n"
"v_rcp_f32 v65, v65\n"
"v_mfma_f32_16x16x32_bf16 v[52:55], a[236:239], v[124:127], v[52:55]\n"
"v_fma_f32 v66, v66, v62, v62\n"
"s_waitcnt lgkmcnt(5)\n"
"v_mfma_f32_16x16x32_bf16 v[52:55], a[248:251], v[76:79], v[52:55]\n"
"ds_read_b128 v[108:111], v5 offset:32256\n"
"v_fma_f32 v67, v67, v62, v62\n"
"v_mfma_f32_16x16x32_bf16 v[52:55], v[132:135], v[80:83], v[52:55]\n"
"v_rcp_f32 v66, v66\n"
"v_mfma_f32_16x16x32_bf16 v[52:55], v[144:147], v[84:87], v[52:55]\n"
"v_rcp_f32 v67, v67\n"
"v_mfma_f32_16x16x32_bf16 v[52:55], v[156:159], v[88:91], v[52:55]\n"
"ds_read_b128 v[112:115], v5 offset:33408\n"
"v_fma_f32 v68, v68, v62, v62\n"
"v_mfma_f32_16x16x32_bf16 v[48:51], v[152:155], v[88:91], v[48:51]\n"
"v_rcp_f32 v68, v68\n"
"v_mfma_f32_16x16x32_bf16 v[48:51], v[140:143], v[84:87], v[48:51]\n"
"buffer_load_dwordx4 v6, s[32:35], s63 offen offset:3072 sc1 lds\n"
"v_fma_f32 v69, v69, v62, v62\n"
"v_mfma_f32_16x16x32_bf16 v[48:51], v[128:131], v[80:83], v[48:51]\n"
"ds_read_b128 v[116:119], v5 offset:34560\n"
"v_mul_f32 v32, v32, v64\n"
"v_mfma_f32_16x16x32_bf16 v[48:51], a[244:247], v[76:79], v[48:51]\n"
"v_rcp_f32 v69, v69\n"
"v_mfma_f32_16x16x32_bf16 v[44:47], a[240:243], v[76:79], v[44:47]\n"
"v_mul_f32 v33, v33, v65\n"
"v_mul_f32 v34, v34, v66\n"
"v_mfma_f32_16x16x32_bf16 v[44:47], a[252:255], v[80:83], v[44:47]\n"
"ds_read_b128 v[120:123], v5 offset:35712\n"
"v_mul_f32 v35, v35, v67\n"
"v_mfma_f32_16x16x32_bf16 v[44:47], v[136:139], v[84:87], v[44:47]\n"
"v_xor_b32 v5, 73728, v5\n"
"v_mul_f32 v40, v40, v68\n"
"v_mfma_f32_16x16x32_bf16 v[44:47], v[148:151], v[88:91], v[44:47]\n"
"s_waitcnt lgkmcnt(4)\n"
"s_waitcnt vmcnt(9)\n"
"s_barrier\n"
"v_mfma_f32_16x16x32_bf16 v[44:47], v[160:163], v[92:95], v[44:47]\n"
"ds_read_b128 v[76:79], v4 offset:0\n"
"s_add_u32 m0, s64, 45056\n"
"v_mfma_f32_16x16x32_bf16 v[44:47], v[172:175], v[96:99], v[44:47]\n"
"buffer_load_dwordx4 v7, s[32:35], s63 offen offset:0 sc1 lds\n"
"s_add_u32 s63, s63, 890880\n"
"v_mfma_f32_16x16x32_bf16 v[44:47], v[184:187], v[100:103], v[44:47]\n"
"s_xor_b32 s64, s64, s65\n"
"v_mul_f32 v41, v41, v69\n"
"v_mfma_f32_16x16x32_bf16 v[44:47], v[196:199], v[104:107], v[44:47]\n"
"ds_read_b128 v[80:83], v4 offset:1152\n"
"v_cvt_pk_bf16_f32 v74, v40, v41\n"
"v_mfma_f32_16x16x32_bf16 v[48:51], v[200:203], v[104:107], v[48:51]\n"
"v_cvt_pk_bf16_f32 v72, v32, v33\n"
"v_cvt_pk_bf16_f32 v73, v34, v35\n"
"v_mfma_f32_16x16x32_bf16 v[48:51], v[188:191], v[100:103], v[48:51]\n"
"v_permlane32_swap_b32 v74, v75\n"
"v_mfma_f32_16x16x32_bf16 v[48:51], v[176:179], v[96:99], v[48:51]\n"
"ds_read_b128 v[84:87], v4 offset:2304\n"
"v_mfma_f32_16x16x32_bf16 v[48:51], v[164:167], v[92:95], v[48:51]\n"
"v_permlane16_swap_b32 v72, v74\n"
"v_mfma_f32_16x16x32_bf16 v[52:55], v[168:171], v[92:95], v[52:55]\n"
"v_permlane16_swap_b32 v73, v75\n"
"v_mfma_f32_16x16x32_bf16 v[52:55], v[180:183], v[96:99], v[52:55]\n"
"buffer_store_dwordx4 v[72:75], v15, s[32:35], s68 offen sc1\n"
"ds_read_b128 v[88:91], v4 offset:3456\n"
"v_mfma_f32_16x16x32_bf16 v[52:55], v[192:195], v[100:103], v[52:55]\n"
"v_mfma_f32_16x16x32_bf16 v[52:55], v[204:207], v[104:107], v[52:55]\n"
"s_waitcnt lgkmcnt(4)\n"
"v_mfma_f32_16x16x32_bf16 v[52:55], v[216:219], v[108:111], v[52:55]\n"
"ds_read_b128 v[92:95], v4 offset:4608\n"
"v_mfma_f32_16x16x32_bf16 v[52:55], v[228:231], v[112:115], v[52:55]\n"
"v_mfma_f32_16x16x32_bf16 v[52:55], v[240:243], v[116:119], v[52:55]\n"
"v_mfma_f32_16x16x32_bf16 v[52:55], v[252:255], v[120:123], v[52:55]\n"
"ds_read_b128 v[96:99], v4 offset:5760\n"
"v_mfma_f32_16x16x32_bf16 v[48:51], v[248:251], v[120:123], v[48:51]\n"
"v_mfma_f32_16x16x32_bf16 v[48:51], v[236:239], v[116:119], v[48:51]\n"
"v_mfma_f32_16x16x32_bf16 v[48:51], v[224:227], v[112:115], v[48:51]\n"
"ds_read_b128 v[100:103], v4 offset:6912\n"
"v_mfma_f32_16x16x32_bf16 v[48:51], v[212:215], v[108:111], v[48:51]\n"
"v_mfma_f32_16x16x32_bf16 v[44:47], v[208:211], v[108:111], v[44:47]\n"
"v_mfma_f32_16x16x32_bf16 v[44:47], v[220:223], v[112:115], v[44:47]\n"
"ds_read_b128 v[104:107], v4 offset:8064\n"
"v_mfma_f32_16x16x32_bf16 v[44:47], v[232:235], v[116:119], v[44:47]\n"
"s_sub_u32 s62, s62, 1\n"
"v_mfma_f32_16x16x32_bf16 v[44:47], v[244:247], v[120:123], v[44:47]\n"
"s_cbranch_scc0 .L_gl_0\n"
"s_nop 1\n"
"ds_read_b32 v62, v11 offset:128\n"
"s_add_u32 s68, s68, 890880\n"
"s_waitcnt lgkmcnt(0)\n"
"v_lshlrev_b32 v60, 16, v62\n"
"v_and_b32 v62, 0xffff0000, v62\n"
"v_rcp_f32 v61, v62\n"
"v_mul_f32 v62, v62, v62\n"
"v_mul_f32 v61, 0xbfb8aa3b, v61\n"
"v_fma_f32 v52, -v60, v28, v52\n"
"v_fma_f32 v53, -v60, v29, v53\n"
"v_fma_f32 v54, -v60, v30, v54\n"
"v_fma_f32 v55, -v60, v31, v55\n"
"v_fma_f32 v48, -v60, v24, v48\n"
"v_fma_f32 v49, -v60, v25, v49\n"
"v_fma_f32 v50, -v60, v26, v50\n"
"v_fma_f32 v51, -v60, v27, v51\n"
"v_fma_f32 v44, -v60, v20, v44\n"
"v_fma_f32 v45, -v60, v21, v45\n"
"v_fma_f32 v46, -v60, v22, v46\n"
"v_fma_f32 v47, -v60, v23, v47\n"
"v_permlane32_swap_b32 v52, v54\n"
"v_permlane32_swap_b32 v53, v55\n"
"v_mul_f32 v64, v44, v61\n"
"v_mul_f32 v65, v45, v61\n"
"v_mul_f32 v66, v46, v61\n"
"v_mul_f32 v67, v47, v61\n"
"v_mul_f32 v68, v52, v61\n"
"v_mul_f32 v69, v53, v61\n"
"v_exp_f32 v64, v64\n"
"v_exp_f32 v65, v65\n"
"v_mul_f32 v44, v44, v48\n"
"v_mul_f32 v45, v45, v49\n"
"v_exp_f32 v66, v66\n"
"v_exp_f32 v67, v67\n"
"v_mul_f32 v46, v46, v50\n"
"v_mul_f32 v47, v47, v51\n"
"v_exp_f32 v68, v68\n"
"v_exp_f32 v69, v69\n"
"v_mul_f32 v52, v52, v54\n"
"v_mul_f32 v53, v53, v55\n"
"v_fma_f32 v64, v64, v62, v62\n"
"v_fma_f32 v65, v65, v62, v62\n"
"v_rcp_f32 v64, v64\n"
"v_rcp_f32 v65, v65\n"
"v_fma_f32 v66, v66, v62, v62\n"
"v_fma_f32 v67, v67, v62, v62\n"
"v_rcp_f32 v66, v66\n"
"v_rcp_f32 v67, v67\n"
"v_fma_f32 v68, v68, v62, v62\n"
"v_fma_f32 v69, v69, v62, v62\n"
"v_rcp_f32 v68, v68\n"
"v_rcp_f32 v69, v69\n"
"v_mul_f32 v44, v44, v64\n"
"v_mul_f32 v45, v45, v65\n"
"v_mul_f32 v46, v46, v66\n"
"v_mul_f32 v47, v47, v67\n"
"v_mul_f32 v52, v52, v68\n"
"v_mul_f32 v53, v53, v69\n"
"v_cvt_pk_bf16_f32 v74, v52, v53\n"
"v_cvt_pk_bf16_f32 v72, v44, v45\n"
"v_cvt_pk_bf16_f32 v73, v46, v47\n"
"v_permlane32_swap_b32 v74, v75\n"
"s_nop 1\n"
"v_permlane16_swap_b32 v72, v74\n"
"v_permlane16_swap_b32 v73, v75\n"
"buffer_store_dwordx4 v[72:75], v15, s[32:35], s68 offen sc1\n"
"s_waitcnt vmcnt(1)\n"
"s_barrier\n"
"buffer_store_dwordx2 v[16:17], v2, s[92:95], s68 idxen sc1\n"
"s_add_u32 s68, s68, 890880\n"
"s_waitcnt vmcnt(1)\n"
"s_barrier\n"
"buffer_store_dwordx2 v[16:17], v2, s[92:95], s68 idxen sc1\n"
"s_branch .L_end\n"
".L_gt0_0:\n"
"s_nop 1\n"
"ds_read_b32 v62, v11 offset:0\n"
"s_add_u32 s68, s68, 890880\n"
"s_waitcnt lgkmcnt(0)\n"
"v_lshlrev_b32 v60, 16, v62\n"
"v_and_b32 v62, 0xffff0000, v62\n"
"v_rcp_f32 v61, v62\n"
"v_mul_f32 v62, v62, v62\n"
"v_mul_f32 v61, 0xbfb8aa3b, v61\n"
"v_fma_f32 v40, -v60, v28, v40\n"
"v_fma_f32 v41, -v60, v29, v41\n"
"v_fma_f32 v42, -v60, v30, v42\n"
"v_fma_f32 v43, -v60, v31, v43\n"
"v_fma_f32 v36, -v60, v24, v36\n"
"v_fma_f32 v37, -v60, v25, v37\n"
"v_fma_f32 v38, -v60, v26, v38\n"
"v_fma_f32 v39, -v60, v27, v39\n"
"v_fma_f32 v32, -v60, v20, v32\n"
"v_fma_f32 v33, -v60, v21, v33\n"
"v_fma_f32 v34, -v60, v22, v34\n"
"v_fma_f32 v35, -v60, v23, v35\n"
"v_permlane32_swap_b32 v40, v42\n"
"v_permlane32_swap_b32 v41, v43\n"
"v_mul_f32 v64, v32, v61\n"
"v_mul_f32 v65, v33, v61\n"
"v_mul_f32 v66, v34, v61\n"
"v_mul_f32 v67, v35, v61\n"
"v_mul_f32 v68, v40, v61\n"
"v_mul_f32 v69, v41, v61\n"
"v_exp_f32 v64, v64\n"
"v_exp_f32 v65, v65\n"
"v_mul_f32 v32, v32, v36\n"
"v_mul_f32 v33, v33, v37\n"
"v_exp_f32 v66, v66\n"
"v_exp_f32 v67, v67\n"
"v_mul_f32 v34, v34, v38\n"
"v_mul_f32 v35, v35, v39\n"
"v_exp_f32 v68, v68\n"
"v_exp_f32 v69, v69\n"
"v_mul_f32 v40, v40, v42\n"
"v_mul_f32 v41, v41, v43\n"
"v_fma_f32 v64, v64, v62, v62\n"
"v_fma_f32 v65, v65, v62, v62\n"
"v_rcp_f32 v64, v64\n"
"v_rcp_f32 v65, v65\n"
"v_fma_f32 v66, v66, v62, v62\n"
"v_fma_f32 v67, v67, v62, v62\n"
"v_rcp_f32 v66, v66\n"
"v_rcp_f32 v67, v67\n"
"v_fma_f32 v68, v68, v62, v62\n"
"v_fma_f32 v69, v69, v62, v62\n"
"v_rcp_f32 v68, v68\n"
"v_rcp_f32 v69, v69\n"
"v_mul_f32 v32, v32, v64\n"
"v_mul_f32 v33, v33, v65\n"
"v_mul_f32 v34, v34, v66\n"
"v_mul_f32 v35, v35, v67\n"
"v_mul_f32 v40, v40, v68\n"
"v_mul_f32 v41, v41, v69\n"
"v_cvt_pk_bf16_f32 v74, v40, v41\n"
"v_cvt_pk_bf16_f32 v72, v32, v33\n"
"v_cvt_pk_bf16_f32 v73, v34, v35\n"
"v_permlane32_swap_b32 v74, v75\n"
"s_nop 1\n"
"v_permlane16_swap_b32 v72, v74\n"
"v_permlane16_swap_b32 v73, v75\n"
"buffer_store_dwordx4 v[72:75], v15, s[32:35], s68 offen sc1\n"
"s_waitcnt vmcnt(1)\n"
"s_barrier\n"
"buffer_store_dwordx2 v[16:17], v2, s[92:95], s68 idxen sc1\n"
"s_add_u32 s68, s68, 890880\n"
"s_waitcnt vmcnt(1)\n"
"s_barrier\n"
"buffer_store_dwordx2 v[16:17], v2, s[92:95], s68 idxen sc1\n"
"s_branch .L_end\n"
".p2align 6\n"
".L_gv1:\n"
".L_gl_1:\n"
"s_waitcnt lgkmcnt(4)\n"
"v_mfma_f32_16x16x32_bf16 v[32:35], a[0:3], v[76:79], 0\n"
"ds_read_b128 v[108:111], v4 offset:9216\n"
"ds_read_b32 v62, v11 offset:128\n"
"v_mfma_f32_16x16x32_bf16 v[32:35], a[12:15], v[80:83], v[32:35]\n"
"s_add_u32 s68, s68, 890880\n"
"v_mfma_f32_16x16x32_bf16 v[32:35], a[24:27], v[84:87], v[32:35]\n"
"v_mfma_f32_16x16x32_bf16 v[32:35], a[36:39], v[88:91], v[32:35]\n"
"ds_read_b128 v[112:115], v4 offset:10368\n"
"v_mfma_f32_16x16x32_bf16 v[36:39], a[40:43], v[88:91], 0\n"
"ds_read2st64_b32 v[56:57], v8 offset0:0 offset1:36\n"
"v_mfma_f32_16x16x32_bf16 v[36:39], a[28:31], v[84:87], v[36:39]\n"
"s_waitcnt vmcnt(4)\n"
"v_cmp_ne_u64 vcc, v[16:17], v[12:13]\n"
"v_mfma_f32_16x16x32_bf16 v[36:39], a[16:19], v[80:83], v[36:39]\n"
"ds_read_b128 v[116:119], v4 offset:11520\n"
"ds_read2st64_b32 v[58:59], v8 offset0:72 offset1:108\n"
"v_mfma_f32_16x16x32_bf16 v[36:39], a[4:7], v[76:79], v[36:39]\n"
"s_cbranch_vccnz .L_rtg0_1\n"
".L_bkg0_1:\n"
"s_add_u32 s66, s66, 890880\n"
"v_mfma_f32_16x16x32_bf16 v[40:43], a[8:11], v[76:79], 0\n"
"s_add_u32 m0, s64, 0\n"
"s_waitcnt lgkmcnt(4)\n"
"v_mfma_f32_16x16x32_bf16 v[40:43], a[20:23], v[80:83], v[40:43]\n"
"buffer_load_dwordx4 v2, s[32:35], s63 offen offset:0 sc1 lds\n"
"ds_read_b128 v[120:123], v4 offset:12672\n"
"v_mfma_f32_16x16x32_bf16 v[40:43], a[32:35], v[84:87], v[40:43]\n"
"v_lshlrev_b32 v60, 16, v62\n"
"v_and_b32 v62, 0xffff0000, v62\n"
"v_mfma_f32_16x16x32_bf16 v[40:43], a[44:47], v[88:91], v[40:43]\n"
"v_fma_f32 v52, -v60, v28, v52\n"
"v_mfma_f32_16x16x32_bf16 v[40:43], a[56:59], v[92:95], v[40:43]\n"
"ds_read_b128 v[124:127], v4 offset:13824\n"
"v_fma_f32 v53, -v60, v29, v53\n"
"v_mfma_f32_16x16x32_bf16 v[40:43], a[68:71], v[96:99], v[40:43]\n"
"v_rcp_f32 v61, v62\n"
"v_mfma_f32_16x16x32_bf16 v[40:43], a[80:83], v[100:103], v[40:43]\n"
"v_mul_f32 v62, v62, v62\n"
"v_mul_f32 v61, 0xbfb8aa3b, v61\n"
"v_mfma_f32_16x16x32_bf16 v[40:43], a[92:95], v[104:107], v[40:43]\n"
"ds_read_b128 v[76:79], v4 offset:14976\n"
"v_fma_f32 v54, -v60, v30, v54\n"
"v_mfma_f32_16x16x32_bf16 v[36:39], a[88:91], v[104:107], v[36:39]\n"
"v_fma_f32 v55, -v60, v31, v55\n"
"v_fma_f32 v48, -v60, v24, v48\n"
"v_mfma_f32_16x16x32_bf16 v[36:39], a[76:79], v[100:103], v[36:39]\n"
"buffer_load_dwordx4 v2, s[32:35], s63 offen offset:1024 sc1 lds\n"
"v_fma_f32 v49, -v60, v25, v49\n"
"v_mfma_f32_16x16x32_bf16 v[36:39], a[64:67], v[96:99], v[36:39]\n"
"ds_read_b128 v[80:83], v4 offset:16128\n"
"v_fma_f32 v50, -v60, v26, v50\n"
"v_mfma_f32_16x16x32_bf16 v[36:39], a[52:55], v[92:95], v[36:39]\n"
"v_permlane32_swap_b32 v52, v54\n"
"v_mfma_f32_16x16x32_bf16 v[32:35], a[48:51], v[92:95], v[32:35]\n"
"v_permlane32_swap_b32 v53, v55\n"
"v_mfma_f32_16x16x32_bf16 v[32:35], a[60:63], v[96:99], v[32:35]\n"
"ds_read_b128 v[84:87], v4 offset:17280\n"
"v_fma_f32 v51, -v60, v27, v51\n"
"v_mfma_f32_16x16x32_bf16 v[32:35], a[72:75], v[100:103], v[32:35]\n"
"v_fma_f32 v44, -v60, v20, v44\n"
"v_fma_f32 v45, -v60, v21, v45\n"
"v_mfma_f32_16x16x32_bf16 v[32:35], a[84:87], v[104:107], v[32:35]\n"
"v_fma_f32 v46, -v60, v22, v46\n"
"s_waitcnt lgkmcnt(4)\n"
"v_mfma_f32_16x16x32_bf16 v[32:35], a[96:99], v[108:111], v[32:35]\n"
"ds_read_b128 v[88:91], v4 offset:18432\n"
"v_add_f32 v56, v56, v57\n"
"v_mfma_f32_16x16x32_bf16 v[32:35], a[108:111], v[112:115], v[32:35]\n"
"buffer_load_dwordx4 v2, s[32:35], s63 offen offset:2048 sc1 lds\n"
"v_add_f32 v58, v58, v59\n"
"v_mfma_f32_16x16x32_bf16 v[32:35], a[120:123], v[116:119], v[32:35]\n"
"v_add_f32 v56, v56, v58\n"
"v_fma_f32 v47, -v60, v23, v47\n"
"v_mfma_f32_16x16x32_bf16 v[32:35], a[132:135], v[120:123], v[32:35]\n"
"ds_read_b128 v[92:95], v4 offset:19584\n"
"v_add_f32_dpp v56, v56, v56 row_ror:8 row_mask:0xf bank_mask:0xf\n"
"v_mfma_f32_16x16x32_bf16 v[36:39], a[136:139], v[120:123], v[36:39]\n"
"v_mul_f32 v64, v44, v61\n"
"v_add_f32_dpp v56, v56, v56 row_ror:4 row_mask:0xf bank_mask:0xf\n"
"v_mfma_f32_16x16x32_bf16 v[36:39], a[124:127], v[116:119], v[36:39]\n"
"v_exp_f32 v64, v64\n"
"v_mfma_f32_16x16x32_bf16 v[36:39], a[112:115], v[112:115], v[36:39]\n"
"ds_read_b128 v[96:99], v4 offset:20736\n"
"v_add_f32_dpp v56, v56, v56 row_ror:2 row_mask:0xf bank_mask:0xf\n"
"v_mfma_f32_16x16x32_bf16 v[36:39], a[100:103], v[108:111], v[36:39]\n"
"v_mul_f32 v56, 0x3a800000, v56\n"
"v_mul_f32 v65, v45, v61\n"
"v_mfma_f32_16x16x32_bf16 v[40:43], a[104:107], v[108:111], v[40:43]\n"
"v_exp_f32 v65, v65\n"
"v_mfma_f32_16x16x32_bf16 v[40:43], a[116:119], v[112:115], v[40:43]\n"
"buffer_load_dwordx4 v2, s[32:35], s63 offen offset:3072 sc1 lds\n"
"ds_read_b128 v[100:103], v4 offset:21888\n"
"v_mfma_f32_16x16x32_bf16 v[40:43], a[128:131], v[116:119], v[40:43]\n"
"v_mul_f32 v57, v56, v56\n"
"v_mul_f32 v66, v46, v61\n"
"v_mfma_f32_16x16x32_bf16 v[40:43], a[140:143], v[120:123], v[40:43]\n"
"v_sub_f32_dpp v57, v56, v57 quad_perm:[1,1,3,3] row_mask:0xf bank_mask:0xf\n"
"s_waitcnt lgkmcnt(4)\n"
"v_mfma_f32_16x16x32_bf16 v[40:43], a[152:155], v[124:127], v[40:43]\n"
"ds_read_b128 v[104:107], v4 offset:23040\n"
"v_max_f32 v57, 0, v57\n"
"v_mfma_f32_16x16x32_bf16 v[40:43], a[164:167], v[76:79], v[40:43]\n"
"v_exp_f32 v66, v66\n"
"v_mfma_f32_16x16x32_bf16 v[40:43], a[176:179], v[80:83], v[40:43]\n"
"v_cvt_pk_bf16_f32 v57, 0, v57\n"
"v_mul_f32 v67, v47, v61\n"
"v_mfma_f32_16x16x32_bf16 v[40:43], a[188:191], v[84:87], v[40:43]\n"
"ds_read_b128 v[108:111], v4 offset:24192\n"
"s_add_u32 m0, s64, 4096\n"
"v_mfma_f32_16x16x32_bf16 v[36:39], a[184:187], v[84:87], v[36:39]\n"
"v_exp_f32 v67, v67\n"
"v_mfma_f32_16x16x32_bf16 v[36:39], a[172:175], v[80:83], v[36:39]\n"
"buffer_load_dwordx4 v6, s[32:35], s63 offen offset:0 sc1 lds\n"
"v_add_f32 v57, 0x3727c5ac, v57\n"
"v_mfma_f32_16x16x32_bf16 v[36:39], a[160:163], v[76:79], v[36:39]\n"
"ds_read_b128 v[112:115], v4 offset:25344\n"
"v_cvt_pk_bf16_f32 v57, 0, v57\n"
"v_mfma_f32_16x16x32_bf16 v[36:39], a[148:151], v[124:127], v[36:39]\n"
"v_sqrt_f32 v57, v57\n"
"v_mfma_f32_16x16x32_bf16 v[32:35], a[144:147], v[124:127], v[32:35]\n"
"v_cvt_pk_bf16_f32 v57, v56, v57\n"
"v_mul_f32 v68, v52, v61\n"
"v_mfma_f32_16x16x32_bf16 v[32:35], a[156:159], v[76:79], v[32:35]\n"
"ds_read_b128 v[116:119], v4 offset:26496\n"
"ds_write_b32 v10, v57 offset:0\n"
"v_mfma_f32_16x16x32_bf16 v[32:35], a[168:171], v[80:83], v[32:35]\n"
"v_exp_f32 v68, v68\n"
"v_mfma_f32_16x16x32_bf16 v[32:35], a[180:183], v[84:87], v[32:35]\n"
"v_mul_f32 v69, v53, v61\n"
"s_waitcnt lgkmcnt(5)\n"
"v_mfma_f32_16x16x32_bf16 v[32:35], a[192:195], v[88:91], v[32:35]\n"
"ds_read_b128 v[120:123], v4 offset:27648\n"
"v_mul_f32 v44, v44, v48\n"
"v_mfma_f32_16x16x32_bf16 v[32:35], a[204:207], v[92:95], v[32:35]\n"
"buffer_load_dwordx4 v6, s[32:35], s63 offen offset:1024 sc1 lds\n"
"v_mul_f32 v45, v45, v49\n"
"v_mfma_f32_16x16x32_bf16 v[32:35], a[216:219], v[96:99], v[32:35]\n"
"v_exp_f32 v69, v69\n"
"v_mfma_f32_16x16x32_bf16 v[32:35], a[228:231], v[100:103], v[32:35]\n"
"ds_read_b128 v[124:127], v4 offset:28800\n"
"v_mul_f32 v46, v46, v50\n"
"v_mfma_f32_16x16x32_bf16 v[36:39], a[232:235], v[100:103], v[36:39]\n"
"v_mul_f32 v47, v47, v51\n"
"v_mul_f32 v52, v52, v54\n"
"v_mfma_f32_16x16x32_bf16 v[36:39], a[220:223], v[96:99], v[36:39]\n"
"buffer_load_dwordx2 v[12:13], v14, s[32:35], s66 offen sc1\n"
"v_mul_f32 v53, v53, v55\n"
"v_mfma_f32_16x16x32_bf16 v[36:39], a[208:211], v[92:95], v[36:39]\n"
"ds_read_b128 v[76:79], v4 offset:29952\n"
"v_fma_f32 v64, v64, v62, v62\n"
"v_mfma_f32_16x16x32_bf16 v[36:39], a[196:199], v[88:91], v[36:39]\n"
"v_rcp_f32 v64, v64\n"
"v_mfma_f32_16x16x32_bf16 v[40:43], a[200:203], v[88:91], v[40:43]\n"
"v_fma_f32 v65, v65, v62, v62\n"
"v_fma_f32 v66, v66, v62, v62\n"
"v_mfma_f32_16x16x32_bf16 v[40:43], a[212:215], v[92:95], v[40:43]\n"
"buffer_load_dwordx4 v6, s[32:35], s63 offen offset:2048 sc1 lds\n"
"ds_read_b128 v[80:83], v4 offset:31104\n"
"v_mfma_f32_16x16x32_bf16 v[40:43], a[224:227], v[96:99], v[40:43]\n"
"v_rcp_f32 v65, v65\n"
"v_mfma_f32_16x16x32_bf16 v[40:43], a[236:239], v[100:103], v[40:43]\n"
"v_fma_f32 v67, v67, v62, v62\n"
"s_waitcnt lgkmcnt(5)\n"
"v_mfma_f32_16x16x32_bf16 v[40:43], a[248:251], v[104:107], v[40:43]\n"
"ds_read_b128 v[84:87], v4 offset:32256\n"
"v_fma_f32 v68, v68, v62, v62\n"
"v_mfma_f32_16x16x32_bf16 v[40:43], v[132:135], v[108:111], v[40:43]\n"
"v_rcp_f32 v66, v66\n"
"v_mfma_f32_16x16x32_bf16 v[40:43], v[144:147], v[112:115], v[40:43]\n"
"v_rcp_f32 v67, v67\n"
"v_mfma_f32_16x16x32_bf16 v[40:43], v[156:159], v[116:119], v[40:43]\n"
"ds_read_b128 v[88:91], v4 offset:33408\n"
"v_fma_f32 v69, v69, v62, v62\n"
"v_mfma_f32_16x16x32_bf16 v[36:39], v[152:155], v[116:119], v[36:39]\n"
"v_rcp_f32 v68, v68\n"
"v_mfma_f32_16x16x32_bf16 v[36:39], v[140:143], v[112:115], v[36:39]\n"
"buffer_load_dwordx4 v6, s[32:35], s63 offen offset:3072 sc1 lds\n"
"v_mul_f32 v44, v44, v64\n"
"v_mfma_f32_16x16x32_bf16 v[36:39], v[128:131], v[108:111], v[36:39]\n"
"ds_read_b128 v[92:95], v4 offset:34560\n"
"v_mul_f32 v45, v45, v65\n"
"v_mfma_f32_16x16x32_bf16 v[36:39], a[244:247], v[104:107], v[36:39]\n"
"v_rcp_f32 v69, v69\n"
"v_mfma_f32_16x16x32_bf16 v[32:35], a[240:243], v[104:107], v[32:35]\n"
"v_mul_f32 v46, v46, v66\n"
"v_mul_f32 v47, v47, v67\n"
"v_mfma_f32_16x16x32_bf16 v[32:35], a[252:255], v[108:111], v[32:35]\n"
"ds_read_b128 v[96:99], v4 offset:35712\n"
"v_mul_f32 v52, v52, v68\n"
"v_mfma_f32_16x16x32_bf16 v[32:35], v[136:139], v[112:115], v[32:35]\n"
"s_waitcnt vmcnt(9)\n"
"s_waitcnt lgkmcnt(8)\n"
"s_barrier\n"
"v_mfma_f32_16x16x32_bf16 v[32:35], v[148:151], v[116:119], v[32:35]\n"
"v_xor_b32 v4, 73728, v4\n"
"s_waitcnt lgkmcnt(4)\n"
"v_mfma_f32_16x16x32_bf16 v[32:35], v[160:163], v[120:123], v[32:35]\n"
"ds_read_b128 v[100:103], v5 offset:0\n"
"s_add_u32 m0, s64, 8192\n"
"v_mfma_f32_16x16x32_bf16 v[32:35], v[172:175], v[124:127], v[32:35]\n"
"buffer_load_dwordx4 v7, s[32:35], s63 offen offset:0 sc1 lds\n"
"s_add_u32 s63, s63, 890880\n"
"v_mfma_f32_16x16x32_bf16 v[32:35], v[184:187], v[76:79], v[32:35]\n"
"v_mul_f32 v53, v53, v69\n"
"v_cvt_pk_bf16_f32 v72, v44, v45\n"
"v_mfma_f32_16x16x32_bf16 v[32:35], v[196:199], v[80:83], v[32:35]\n"
"ds_read_b128 v[104:107], v5 offset:1152\n"
"v_cvt_pk_bf16_f32 v74, v52, v53\n"
"v_mfma_f32_16x16x32_bf16 v[36:39], v[200:203], v[80:83], v[36:39]\n"
"v_cvt_pk_bf16_f32 v73, v46, v47\n"
"v_mfma_f32_16x16x32_bf16 v[36:39], v[188:191], v[76:79], v[36:39]\n"
"v_permlane32_swap_b32 v74, v75\n"
"v_mfma_f32_16x16x32_bf16 v[36:39], v[176:179], v[124:127], v[36:39]\n"
"ds_read_b128 v[108:111], v5 offset:2304\n"
"v_mfma_f32_16x16x32_bf16 v[36:39], v[164:167], v[120:123], v[36:39]\n"
"v_permlane16_swap_b32 v72, v74\n"
"v_mfma_f32_16x16x32_bf16 v[40:43], v[168:171], v[120:123], v[40:43]\n"
"v_permlane16_swap_b32 v73, v75\n"
"v_mfma_f32_16x16x32_bf16 v[40:43], v[180:183], v[124:127], v[40:43]\n"
"buffer_store_dwordx4 v[72:75], v15, s[32:35], s68 offen sc1\n"
"ds_read_b128 v[112:115], v5 offset:3456\n"
"v_mfma_f32_16x16x32_bf16 v[40:43], v[192:195], v[76:79], v[40:43]\n"
"v_mfma_f32_16x16x32_bf16 v[40:43], v[204:207], v[80:83], v[40:43]\n"
"s_waitcnt lgkmcnt(4)\n"
"v_mfma_f32_16x16x32_bf16 v[40:43], v[216:219], v[84:87], v[40:43]\n"
"ds_read_b128 v[116:119], v5 offset:4608\n"
"v_mfma_f32_16x16x32_bf16 v[40:43], v[228:231], v[88:91], v[40:43]\n"
"v_mfma_f32_16x16x32_bf16 v[40:43], v[240:243], v[92:95], v[40:43]\n"
"v_mfma_f32_16x16x32_bf16 v[40:43], v[252:255], v[96:99], v[40:43]\n"
"ds_read_b128 v[120:123], v5 offset:5760\n"
"v_mfma_f32_16x16x32_bf16 v[36:39], v[248:251], v[96:99], v[36:39]\n"
"v_mfma_f32_16x16x32_bf16 v[36:39], v[236:239], v[92:95], v[36:39]\n"
"v_mfma_f32_16x16x32_bf16 v[36:39], v[224:227], v[88:91], v[36:39]\n"
"ds_read_b128 v[124:127], v5 offset:6912\n"
"v_mfma_f32_16x16x32_bf16 v[36:39], v[212:215], v[84:87], v[36:39]\n"
"v_mfma_f32_16x16x32_bf16 v[32:35], v[208:211], v[84:87], v[32:35]\n"
"v_mfma_f32_16x16x32_bf16 v[32:35], v[220:223], v[88:91], v[32:35]\n"
"ds_read_b128 v[76:79], v5 offset:8064\n"
"v_mfma_f32_16x16x32_bf16 v[32:35], v[232:235], v[92:95], v[32:35]\n"
"s_sub_u32 s62, s62, 1\n"
"v_mfma_f32_16x16x32_bf16 v[32:35], v[244:247], v[96:99], v[32:35]\n"
"s_cbranch_scc1 .L_gt0_1\n"
"s_waitcnt lgkmcnt(4)\n"
"v_mfma_f32_16x16x32_bf16 v[44:47], a[0:3], v[100:103], 0\n"
"ds_read_b128 v[80:83], v5 offset:9216\n"
"ds_read_b32 v62, v11 offset:0\n"
"v_mfma_f32_16x16x32_bf16 v[44:47], a[12:15], v[104:107], v[44:47]\n"
"s_add_u32 s68, s68, 890880\n"
"v_mfma_f32_16x16x32_bf16 v[44:47], a[24:27], v[108:111], v[44:47]\n"
"v_mfma_f32_16x16x32_bf16 v[44:47], a[36:39], v[112:115], v[44:47]\n"
"ds_read_b128 v[84:87], v5 offset:10368\n"
"v_mfma_f32_16x16x32_bf16 v[48:51], a[40:43], v[112:115], 0\n"
"ds_read2st64_b32 v[56:57], v8 offset0:144 offset1:180\n"
"v_mfma_f32_16x16x32_bf16 v[48:51], a[28:31], v[108:111], v[48:51]\n"
"s_waitcnt vmcnt(4)\n"
"v_cmp_ne_u64 vcc, v[16:17], v[12:13]\n"
"v_mfma_f32_16x16x32_bf16 v[48:51], a[16:19], v[104:107], v[48:51]\n"
"ds_read_b128 v[88:91], v5 offset:11520\n"
"ds_read2st64_b32 v[58:59], v8 offset0:216 offset1:252\n"
"v_mfma_f32_16x16x32_bf16 v[48:51], a[4:7], v[100:103], v[48:51]\n"
"s_cbranch_vccnz .L_rtg1_1\n"
".L_bkg1_1:\n"
"s_add_u32 s66, s66, 890880\n"
"v_mfma_f32_16x16x32_bf16 v[52:55], a[8:11], v[100:103], 0\n"
"s_add_u32 m0, s64, 36864\n"
"v_xor_b32 v8, v9, v8\n"
"v_mfma_f32_16x16x32_bf16 v[52:55], a[20:23], v[104:107], v[52:55]\n"
"buffer_load_dwordx4 v2, s[32:35], s63 offen offset:0 sc1 lds\n"
"ds_read_b128 v[92:95], v5 offset:12672\n"
"v_mfma_f32_16x16x32_bf16 v[52:55], a[32:35], v[108:111], v[52:55]\n"
"s_waitcnt lgkmcnt(5)\n"
"v_lshlrev_b32 v60, 16, v62\n"
"v_mfma_f32_16x16x32_bf16 v[52:55], a[44:47], v[112:115], v[52:55]\n"
"v_and_b32 v62, 0xffff0000, v62\n"
"v_mfma_f32_16x16x32_bf16 v[52:55], a[56:59], v[116:119], v[52:55]\n"
"ds_read_b128 v[96:99], v5 offset:13824\n"
"v_fma_f32 v40, -v60, v28, v40\n"
"v_mfma_f32_16x16x32_bf16 v[52:55], a[68:71], v[120:123], v[52:55]\n"
"v_rcp_f32 v61, v62\n"
"v_mfma_f32_16x16x32_bf16 v[52:55], a[80:83], v[124:127], v[52:55]\n"
"v_mul_f32 v62, v62, v62\n"
"v_mul_f32 v61, 0xbfb8aa3b, v61\n"
"v_mfma_f32_16x16x32_bf16 v[52:55], a[92:95], v[76:79], v[52:55]\n"
"ds_read_b128 v[100:103], v5 offset:14976\n"
"v_fma_f32 v41, -v60, v29, v41\n"
"v_mfma_f32_16x16x32_bf16 v[48:51], a[88:91], v[76:79], v[48:51]\n"
"v_fma_f32 v42, -v60, v30, v42\n"
"v_fma_f32 v43, -v60, v31, v43\n"
"v_mfma_f32_16x16x32_bf16 v[48:51], a[76:79], v[124:127], v[48:51]\n"
"buffer_load_dwordx4 v2, s[32:35], s63 offen offset:1024 sc1 lds\n"
"v_fma_f32 v36, -v60, v24, v36\n"
"v_mfma_f32_16x16x32_bf16 v[48:51], a[64:67], v[120:123], v[48:51]\n"
"ds_read_b128 v[104:107], v5 offset:16128\n"
"v_fma_f32 v37, -v60, v25, v37\n"
"v_mfma_f32_16x16x32_bf16 v[48:51], a[52:55], v[116:119], v[48:51]\n"
"v_permlane32_swap_b32 v40, v42\n"
"v_mfma_f32_16x16x32_bf16 v[44:47], a[48:51], v[116:119], v[44:47]\n"
"v_permlane32_swap_b32 v41, v43\n"
"v_mfma_f32_16x16x32_bf16 v[44:47], a[60:63], v[120:123], v[44:47]\n"
"ds_read_b128 v[108:111], v5 offset:17280\n"
"v_fma_f32 v38, -v60, v26, v38\n"
"v_mfma_f32_16x16x32_bf16 v[44:47], a[72:75], v[124:127], v[44:47]\n"
"v_fma_f32 v39, -v60, v27, v39\n"
"v_fma_f32 v32, -v60, v20, v32\n"
"v_mfma_f32_16x16x32_bf16 v[44:47], a[84:87], v[76:79], v[44:47]\n"
"v_fma_f32 v33, -v60, v21, v33\n"
"s_waitcnt lgkmcnt(4)\n"
"v_mfma_f32_16x16x32_bf16 v[44:47], a[96:99], v[80:83], v[44:47]\n"
"ds_read_b128 v[112:115], v5 offset:18432\n"
"v_add_f32 v56, v56, v57\n"
"v_mfma_f32_16x16x32_bf16 v[44:47], a[108:111], v[84:87], v[44:47]\n"
"buffer_load_dwordx4 v2, s[32:35], s63 offen offset:2048 sc1 lds\n"
"v_add_f32 v58, v58, v59\n"
"v_mfma_f32_16x16x32_bf16 v[44:47], a[120:123], v[88:91], v[44:47]\n"
"v_add_f32 v56, v56, v58\n"
"v_fma_f32 v34, -v60, v22, v34\n"
"v_mfma_f32_16x16x32_bf16 v[44:47], a[132:135], v[92:95], v[44:47]\n"
"ds_read_b128 v[116:119], v5 offset:19584\n"
"v_add_f32_dpp v56, v56, v56 row_ror:8 row_mask:0xf bank_mask:0xf\n"
"v_mfma_f32_16x16x32_bf16 v[48:51], a[136:139], v[92:95], v[48:51]\n"
"v_fma_f32 v35, -v60, v23, v35\n"
"v_add_f32_dpp v56, v56, v56 row_ror:4 row_mask:0xf bank_mask:0xf\n"
"v_mfma_f32_16x16x32_bf16 v[48:51], a[124:127], v[88:91], v[48:51]\n"
"v_mul_f32 v64, v32, v61\n"
"v_add_f32_dpp v56, v56, v56 row_ror:2 row_mask:0xf bank_mask:0xf\n"
"v_mfma_f32_16x16x32_bf16 v[48:51], a[112:115], v[84:87], v[48:51]\n"
"ds_read_b128 v[120:123], v5 offset:20736\n"
"v_mul_f32 v56, 0x3a800000, v56\n"
"v_mfma_f32_16x16x32_bf16 v[48:51], a[100:103], v[80:83], v[48:51]\n"
"v_exp_f32 v64, v64\n"
"v_mfma_f32_16x16x32_bf16 v[52:55], a[104:107], v[80:83], v[52:55]\n"
"v_mul_f32 v57, v56, v56\n"
"v_mul_f32 v65, v33, v61\n"
"v_mfma_f32_16x16x32_bf16 v[52:55], a[116:119], v[84:87], v[52:55]\n"
"buffer_load_dwordx4 v2, s[32:35], s63 offen offset:3072 sc1 lds\n"
"ds_read_b128 v[124:127], v5 offset:21888\n"
"v_mfma_f32_16x16x32_bf16 v[52:55], a[128:131], v[88:91], v[52:55]\n"
"v_exp_f32 v65, v65\n"
"v_mfma_f32_16x16x32_bf16 v[52:55], a[140:143], v[92:95], v[52:55]\n"
"v_sub_f32_dpp v57, v56, v57 quad_perm:[1,1,3,3] row_mask:0xf bank_mask:0xf\n"
"s_waitcnt lgkmcnt(4)\n"
"v_mfma_f32_16x16x32_bf16 v[52:55], a[152:155], v[96:99], v[52:55]\n"
"ds_read_b128 v[76:79], v5 offset:23040\n"
"v_max_f32 v57, 0, v57\n"
"v_mfma_f32_16x16x32_bf16 v[52:55], a[164:167], v[100:103], v[52:55]\n"
"v_cvt_pk_bf16_f32 v57, 0, v57\n"
"v_mul_f32 v66, v34, v61\n"
"v_mfma_f32_16x16x32_bf16 v[52:55], a[176:179], v[104:107], v[52:55]\n"
"v_exp_f32 v66, v66\n"
"v_mfma_f32_16x16x32_bf16 v[52:55], a[188:191], v[108:111], v[52:55]\n"
"ds_read_b128 v[80:83], v5 offset:24192\n"
"s_add_u32 m0, s64, 40960\n"
"v_mfma_f32_16x16x32_bf16 v[48:51], a[184:187], v[108:111], v[48:51]\n"
"v_add_f32 v57, 0x3727c5ac, v57\n"
"v_mul_f32 v67, v35, v61\n"
"v_mfma_f32_16x16x32_bf16 v[48:51], a[172:175], v[104:107], v[48:51]\n"
"buffer_load_dwordx4 v6, s[32:35], s63 offen offset:0 sc1 lds\n"
"v_cvt_pk_bf16_f32 v57, 0, v57\n"
"v_mfma_f32_16x16x32_bf16 v[48:51], a[160:163], v[100:103], v[48:51]\n"
"ds_read_b128 v[84:87], v5 offset:25344\n"
"v_mul_f32 v68, v40, v61\n"
"v_mfma_f32_16x16x32_bf16 v[48:51], a[148:151], v[96:99], v[48:51]\n"
"v_sqrt_f32 v57, v57\n"
"v_mfma_f32_16x16x32_bf16 v[44:47], a[144:147], v[96:99], v[44:47]\n"
"v_exp_f32 v67, v67\n"
"v_mfma_f32_16x16x32_bf16 v[44:47], a[156:159], v[100:103], v[44:47]\n"
"ds_read_b128 v[88:91], v5 offset:26496\n"
"v_cvt_pk_bf16_f32 v57, v56, v57\n"
"v_mfma_f32_16x16x32_bf16 v[44:47], a[168:171], v[104:107], v[44:47]\n"
"ds_write_b32 v10, v57 offset:128\n"
"v_mul_f32 v69, v41, v61\n"
"v_mfma_f32_16x16x32_bf16 v[44:47], a[180:183], v[108:111], v[44:47]\n"
"v_mul_f32 v32, v32, v36\n"
"s_waitcnt lgkmcnt(5)\n"
"v_mfma_f32_16x16x32_bf16 v[44:47], a[192:195], v[112:115], v[44:47]\n"
"ds_read_b128 v[92:95], v5 offset:27648\n"
"v_mul_f32 v33, v33, v37\n"
"v_mfma_f32_16x16x32_bf16 v[44:47], a[204:207], v[116:119], v[44:47]\n"
"buffer_load_dwordx4 v6, s[32:35], s63 offen offset:1024 sc1 lds\n"
"v_mul_f32 v34, v34, v38\n"
"v_mfma_f32_16x16x32_bf16 v[44:47], a[216:219], v[120:123], v[44:47]\n"
"v_exp_f32 v68, v68\n"
"v_mfma_f32_16x16x32_bf16 v[44:47], a[228:231], v[124:127], v[44:47]\n"
"ds_read_b128 v[96:99], v5 offset:28800\n"
"v_mul_f32 v35, v35, v39\n"
"v_mfma_f32_16x16x32_bf16 v[48:51], a[232:235], v[124:127], v[48:51]\n"
"v_exp_f32 v69, v69\n"
"v_mfma_f32_16x16x32_bf16 v[48:51], a[220:223], v[120:123], v[48:51]\n"
"buffer_load_dwordx2 v[12:13], v14, s[32:35], s66 offen sc1\n"
"v_mul_f32 v40, v40, v42\n"
"v_mfma_f32_16x16x32_bf16 v[48:51], a[208:211], v[116:119], v[48:51]\n"
"ds_read_b128 v[100:103], v5 offset:29952\n"
"v_mul_f32 v41, v41, v43\n"
"v_mfma_f32_16x16x32_bf16 v[48:51], a[196:199], v[112:115], v[48:51]\n"
"v_fma_f32 v64, v64, v62, v62\n"
"v_fma_f32 v65, v65, v62, v62\n"
"v_mfma_f32_16x16x32_bf16 v[52:55], a[200:203], v[112:115], v[52:55]\n"
"v_rcp_f32 v64, v64\n"
"v_mfma_f32_16x16x32_bf16 v[52:55], a[212:215], v[116:119], v[52:55]\n"
"buffer_load_dwordx4 v6, s[32:35], s63 offen offset:2048 sc1 lds\n"
"ds_read_b128 v[104:107], v5 offset:31104\n"
"v_mfma_f32_16x16x32_bf16 v[52:55], a[224:227], v[120:123], v[52:55]\n"
"v_rcp_f32 v65, v65\n"
"v_mfma_f32_16x16x32_bf16 v[52:55], a[236:239], v[124:127], v[52:55]\n"
"v_fma_f32 v66, v66, v62, v62\n"
"s_waitcnt lgkmcnt(5)\n"
"v_mfma_f32_16x16x32_bf16 v[52:55], a[248:251], v[76:79], v[52:55]\n"
"ds_read_b128 v[108:111], v5 offset:32256\n"
"v_fma_f32 v67, v67, v62, v62\n"
"v_mfma_f32_16x16x32_bf16 v[52:55], v[132:135], v[80:83], v[52:55]\n"
"v_rcp_f32 v66, v66\n"
"v_mfma_f32_16x16x32_bf16 v[52:55], v[144:147], v[84:87], v[52:55]\n"
"v_rcp_f32 v67, v67\n"
"v_mfma_f32_16x16x32_bf16 v[52:55], v[156:159], v[88:91], v[52:55]\n"
"ds_read_b128 v[112:115], v5 offset:33408\n"
"v_fma_f32 v68, v68, v62, v62\n"
"v_mfma_f32_16x16x32_bf16 v[48:51], v[152:155], v[88:91], v[48:51]\n"
"v_rcp_f32 v68, v68\n"
"v_mfma_f32_16x16x32_bf16 v[48:51], v[140:143], v[84:87], v[48:51]\n"
"buffer_load_dwordx4 v6, s[32:35], s63 offen offset:3072 sc1 lds\n"
"v_fma_f32 v69, v69, v62, v62\n"
"v_mfma_f32_16x16x32_bf16 v[48:51], v[128:131], v[80:83], v[48:51]\n"
"ds_read_b128 v[116:119], v5 offset:34560\n"
"v_mul_f32 v32, v32, v64\n"
"v_mfma_f32_16x16x32_bf16 v[48:51], a[244:247], v[76:79], v[48:51]\n"
"v_rcp_f32 v69, v69\n"
"v_mfma_f32_16x16x32_bf16 v[44:47], a[240:243], v[76:79], v[44:47]\n"
"v_mul_f32 v33, v33, v65\n"
"v_mul_f32 v34, v34, v66\n"
"v_mfma_f32_16x16x32_bf16 v[44:47], a[252:255], v[80:83], v[44:47]\n"
"ds_read_b128 v[120:123], v5 offset:35712\n"
"v_mul_f32 v35, v35, v67\n"
"v_mfma_f32_16x16x32_bf16 v[44:47], v[136:139], v[84:87], v[44:47]\n"
"s_waitcnt vmcnt(9)\n"
"s_waitcnt lgkmcnt(8)\n"
"s_barrier\n"
"v_mfma_f32_16x16x32_bf16 v[44:47], v[148:151], v[88:91], v[44:47]\n"
"v_xor_b32 v5, 73728, v5\n"
"s_waitcnt lgkmcnt(4)\n"
"v_mfma_f32_16x16x32_bf16 v[44:47], v[160:163], v[92:95], v[44:47]\n"
"ds_read_b128 v[76:79], v4 offset:0\n"
"s_add_u32 m0, s64, 45056\n"
"v_mfma_f32_16x16x32_bf16 v[44:47], v[172:175], v[96:99], v[44:47]\n"
"buffer_load_dwordx4 v7, s[32:35], s63 offen offset:0 sc1 lds\n"
"s_add_u32 s63, s63, 890880\n"
"v_mfma_f32_16x16x32_bf16 v[44:47], v[184:187], v[100:103], v[44:47]\n"
"s_xor_b32 s64, s64, s65\n"
"v_mul_f32 v40, v40, v68\n"
"v_mfma_f32_16x16x32_bf16 v[44:47], v[196:199], v[104:107], v[44:47]\n"
"ds_read_b128 v[80:83], v4 offset:1152\n"
"v_mul_f32 v41, v41, v69\n"
"v_mfma_f32_16x16x32_bf16 v[48:51], v[200:203], v[104:107], v[48:51]\n"
"v_cvt_pk_bf16_f32 v74, v40, v41\n"
"v_cvt_pk_bf16_f32 v72, v32, v33\n"
"v_mfma_f32_16x16x32_bf16 v[48:51], v[188:191], v[100:103], v[48:51]\n"
"v_permlane32_swap_b32 v74, v75\n"
"v_mfma_f32_16x16x32_bf16 v[48:51], v[176:179], v[96:99], v[48:51]\n"
"ds_read_b128 v[84:87], v4 offset:2304\n"
"v_cvt_pk_bf16_f32 v73, v34, v35\n"
"v_mfma_f32_16x16x32_bf16 v[48:51], v[164:167], v[92:95], v[48:51]\n"
"v_permlane16_swap_b32 v72, v74\n"
"v_mfma_f32_16x16x32_bf16 v[52:55], v[168:171], v[92:95], v[52:55]\n"
"v_permlane16_swap_b32 v73, v75\n"
"v_mfma_f32_16x16x32_bf16 v[52:55], v[180:183], v[96:99], v[52:55]\n"
"buffer_store_dwordx4 v[72:75], v15, s[32:35], s68 offen sc1\n"
"ds_read_b128 v[88:91], v4 offset:3456\n"
"v_mfma_f32_16x16x32_bf16 v[52:55], v[192:195], v[100:103], v[52:55]\n"
"v_mfma_f32_16x16x32_bf16 v[52:55], v[204:207], v[104:107], v[52:55]\n"
"s_waitcnt lgkmcnt(4)\n"
"v_mfma_f32_16x16x32_bf16 v[52:55], v[216:219], v[108:111], v[52:55]\n"
"ds_read_b128 v[92:95], v4 offset:4608\n"
"v_mfma_f32_16x16x32_bf16 v[52:55], v[228:231], v[112:115], v[52:55]\n"
"v_mfma_f32_16x16x32_bf16 v[52:55], v[240:243], v[116:119], v[52:55]\n"
"v_mfma_f32_16x16x32_bf16 v[52:55], v[252:255], v[120:123], v[52:55]\n"
"ds_read_b128 v[96:99], v4 offset:5760\n"
"v_mfma_f32_16x16x32_bf16 v[48:51], v[248:251], v[120:123], v[48:51]\n"
"v_mfma_f32_16x16x32_bf16 v[48:51], v[236:239], v[116:119], v[48:51]\n"
"v_mfma_f32_16x16x32_bf16 v[48:51], v[224:227], v[112:115], v[48:51]\n"
"ds_read_b128 v[100:103], v4 offset:6912\n"
"v_mfma_f32_16x16x32_bf16 v[48:51], v[212:215], v[108:111], v[48:51]\n"
"v_mfma_f32_16x16x32_bf16 v[44:47], v[208:211], v[108:111], v[44:47]\n"
"v_mfma_f32_16x16x32_bf16 v[44:47], v[220:223], v[112:115], v[44:47]\n"
"ds_read_b128 v[104:107], v4 offset:8064\n"
"v_mfma_f32_16x16x32_bf16 v[44:47], v[232:235], v[116:119], v[44:47]\n"
"s_sub_u32 s62, s62, 1\n"
"v_mfma_f32_16x16x32_bf16 v[44:47], v[244:247], v[120:123], v[44:47]\n"
"s_cbranch_scc0 .L_gl_1\n"
"s_nop 1\n"
"ds_read_b32 v62, v11 offset:128\n"
"s_add_u32 s68, s68, 890880\n"
"s_waitcnt lgkmcnt(0)\n"
"v_lshlrev_b32 v60, 16, v62\n"
"v_and_b32 v62, 0xffff0000, v62\n"
"v_rcp_f32 v61, v62\n"
"v_mul_f32 v62, v62, v62\n"
"v_mul_f32 v61, 0xbfb8aa3b, v61\n"
"v_fma_f32 v52, -v60, v28, v52\n"
"v_fma_f32 v53, -v60, v29, v53\n"
"v_fma_f32 v54, -v60, v30, v54\n"
"v_fma_f32 v55, -v60, v31, v55\n"
"v_fma_f32 v48, -v60, v24, v48\n"
"v_fma_f32 v49, -v60, v25, v49\n"
"v_fma_f32 v50, -v60, v26, v50\n"
"v_fma_f32 v51, -v60, v27, v51\n"
"v_fma_f32 v44, -v60, v20, v44\n"
"v_fma_f32 v45, -v60, v21, v45\n"
"v_fma_f32 v46, -v60, v22, v46\n"
"v_fma_f32 v47, -v60, v23, v47\n"
"v_permlane32_swap_b32 v52, v54\n"
"v_permlane32_swap_b32 v53, v55\n"
"v_mul_f32 v64, v44, v61\n"
"v_mul_f32 v65, v45, v61\n"
"v_mul_f32 v66, v46, v61\n"
"v_mul_f32 v67, v47, v61\n"
"v_mul_f32 v68, v52, v61\n"
"v_mul_f32 v69, v53, v61\n"
"v_exp_f32 v64, v64\n"
"v_exp_f32 v65, v65\n"
"v_mul_f32 v44, v44, v48\n"
"v_mul_f32 v45, v45, v49\n"
"v_exp_f32 v66, v66\n"
"v_exp_f32 v67, v67\n"
"v_mul_f32 v46, v46, v50\n"
"v_mul_f32 v47, v47, v51\n"
"v_exp_f32 v68, v68\n"
"v_exp_f32 v69, v69\n"
"v_mul_f32 v52, v52, v54\n"
"v_mul_f32 v53, v53, v55\n"
"v_fma_f32 v64, v64, v62, v62\n"
"v_fma_f32 v65, v65, v62, v62\n"
"v_rcp_f32 v64, v64\n"
"v_rcp_f32 v65, v65\n"
"v_fma_f32 v66, v66, v62, v62\n"
"v_fma_f32 v67, v67, v62, v62\n"
"v_rcp_f32 v66, v66\n"
"v_rcp_f32 v67, v67\n"
"v_fma_f32 v68, v68, v62, v62\n"
"v_fma_f32 v69, v69, v62, v62\n"
"v_rcp_f32 v68, v68\n"
"v_rcp_f32 v69, v69\n"
"v_mul_f32 v44, v44, v64\n"
"v_mul_f32 v45, v45, v65\n"
"v_mul_f32 v46, v46, v66\n"
"v_mul_f32 v47, v47, v67\n"
"v_mul_f32 v52, v52, v68\n"
"v_mul_f32 v53, v53, v69\n"
"v_cvt_pk_bf16_f32 v74, v52, v53\n"
"v_cvt_pk_bf16_f32 v72, v44, v45\n"
"v_cvt_pk_bf16_f32 v73, v46, v47\n"
"v_permlane32_swap_b32 v74, v75\n"
"s_nop 1\n"
"v_permlane16_swap_b32 v72, v74\n"
"v_permlane16_swap_b32 v73, v75\n"
"buffer_store_dwordx4 v[72:75], v15, s[32:35], s68 offen sc1\n"
"s_waitcnt vmcnt(1)\n"
"s_barrier\n"
"buffer_store_dwordx2 v[16:17], v2, s[92:95], s68 idxen sc1\n"
"s_add_u32 s68, s68, 890880\n"
"s_waitcnt vmcnt(1)\n"
"s_barrier\n"
"buffer_store_dwordx2 v[16:17], v2, s[92:95], s68 idxen sc1\n"
"s_branch .L_end\n"
".L_gt0_1:\n"
"s_nop 1\n"
"ds_read_b32 v62, v11 offset:0\n"
"s_add_u32 s68, s68, 890880\n"
"s_waitcnt lgkmcnt(0)\n"
"v_lshlrev_b32 v60, 16, v62\n"
"v_and_b32 v62, 0xffff0000, v62\n"
"v_rcp_f32 v61, v62\n"
"v_mul_f32 v62, v62, v62\n"
"v_mul_f32 v61, 0xbfb8aa3b, v61\n"
"v_fma_f32 v40, -v60, v28, v40\n"
"v_fma_f32 v41, -v60, v29, v41\n"
"v_fma_f32 v42, -v60, v30, v42\n"
"v_fma_f32 v43, -v60, v31, v43\n"
"v_fma_f32 v36, -v60, v24, v36\n"
"v_fma_f32 v37, -v60, v25, v37\n"
"v_fma_f32 v38, -v60, v26, v38\n"
"v_fma_f32 v39, -v60, v27, v39\n"
"v_fma_f32 v32, -v60, v20, v32\n"
"v_fma_f32 v33, -v60, v21, v33\n"
"v_fma_f32 v34, -v60, v22, v34\n"
"v_fma_f32 v35, -v60, v23, v35\n"
"v_permlane32_swap_b32 v40, v42\n"
"v_permlane32_swap_b32 v41, v43\n"
"v_mul_f32 v64, v32, v61\n"
"v_mul_f32 v65, v33, v61\n"
"v_mul_f32 v66, v34, v61\n"
"v_mul_f32 v67, v35, v61\n"
"v_mul_f32 v68, v40, v61\n"
"v_mul_f32 v69, v41, v61\n"
"v_exp_f32 v64, v64\n"
"v_exp_f32 v65, v65\n"
"v_mul_f32 v32, v32, v36\n"
"v_mul_f32 v33, v33, v37\n"
"v_exp_f32 v66, v66\n"
"v_exp_f32 v67, v67\n"
"v_mul_f32 v34, v34, v38\n"
"v_mul_f32 v35, v35, v39\n"
"v_exp_f32 v68, v68\n"
"v_exp_f32 v69, v69\n"
"v_mul_f32 v40, v40, v42\n"
"v_mul_f32 v41, v41, v43\n"
"v_fma_f32 v64, v64, v62, v62\n"
"v_fma_f32 v65, v65, v62, v62\n"
"v_rcp_f32 v64, v64\n"
"v_rcp_f32 v65, v65\n"
"v_fma_f32 v66, v66, v62, v62\n"
"v_fma_f32 v67, v67, v62, v62\n"
"v_rcp_f32 v66, v66\n"
"v_rcp_f32 v67, v67\n"
"v_fma_f32 v68, v68, v62, v62\n"
"v_fma_f32 v69, v69, v62, v62\n"
"v_rcp_f32 v68, v68\n"
"v_rcp_f32 v69, v69\n"
"v_mul_f32 v32, v32, v64\n"
"v_mul_f32 v33, v33, v65\n"
"v_mul_f32 v34, v34, v66\n"
"v_mul_f32 v35, v35, v67\n"
"v_mul_f32 v40, v40, v68\n"
"v_mul_f32 v41, v41, v69\n"
"v_cvt_pk_bf16_f32 v74, v40, v41\n"
"v_cvt_pk_bf16_f32 v72, v32, v33\n"
"v_cvt_pk_bf16_f32 v73, v34, v35\n"
"v_permlane32_swap_b32 v74, v75\n"
"s_nop 1\n"
"v_permlane16_swap_b32 v72, v74\n"
"v_permlane16_swap_b32 v73, v75\n"
"buffer_store_dwordx4 v[72:75], v15, s[32:35], s68 offen sc1\n"
"s_waitcnt vmcnt(1)\n"
"s_barrier\n"
"buffer_store_dwordx2 v[16:17], v2, s[92:95], s68 idxen sc1\n"
"s_add_u32 s68, s68, 890880\n"
"s_waitcnt vmcnt(1)\n"
"s_barrier\n"
"buffer_store_dwordx2 v[16:17], v2, s[92:95], s68 idxen sc1\n"
"s_branch .L_end\n"
".p2align 6\n"
".L_gv2:\n"
".L_gl_2:\n"
"s_waitcnt lgkmcnt(4)\n"
"v_mfma_f32_16x16x32_bf16 v[32:35], a[0:3], v[76:79], 0\n"
"ds_read_b128 v[108:111], v4 offset:9216\n"
"ds_read_b32 v62, v11 offset:128\n"
"v_mfma_f32_16x16x32_bf16 v[32:35], a[12:15], v[80:83], v[32:35]\n"
"s_add_u32 s68, s68, 890880\n"
"v_mfma_f32_16x16x32_bf16 v[32:35], a[24:27], v[84:87], v[32:35]\n"
"v_mfma_f32_16x16x32_bf16 v[32:35], a[36:39], v[88:91], v[32:35]\n"
"ds_read_b128 v[112:115], v4 offset:10368\n"
"v_mfma_f32_16x16x32_bf16 v[36:39], a[40:43], v[88:91], 0\n"
"ds_read2st64_b32 v[56:57], v8 offset0:0 offset1:36\n"
"v_mfma_f32_16x16x32_bf16 v[36:39], a[28:31], v[84:87], v[36:39]\n"
"s_waitcnt vmcnt(4)\n"
"v_cmp_ne_u64 vcc, v[16:17], v[12:13]\n"
"v_mfma_f32_16x16x32_bf16 v[36:39], a[16:19], v[80:83], v[36:39]\n"
"ds_read_b128 v[116:119], v4 offset:11520\n"
"ds_read2st64_b32 v[58:59], v8 offset0:72 offset1:108\n"
"v_mfma_f32_16x16x32_bf16 v[36:39], a[4:7], v[76:79], v[36:39]\n"
"s_cbranch_vccnz .L_rtg0_2\n"
".L_bkg0_2:\n"
"s_add_u32 s66, s66, 890880\n"
"v_mfma_f32_16x16x32_bf16 v[40:43], a[8:11], v[76:79], 0\n"
"s_add_u32 m0, s64, 0\n"
"s_waitcnt lgkmcnt(4)\n"
"v_mfma_f32_16x16x32_bf16 v[40:43], a[20:23], v[80:83], v[40:43]\n"
"buffer_load_dwordx4 v2, s[32:35], s63 offen offset:0 sc1 lds\n"
"ds_read_b128 v[120:123], v4 offset:12672\n"
"v_mfma_f32_16x16x32_bf16 v[40:43], a[32:35], v[84:87], v[40:43]\n"
"v_lshlrev_b32 v60, 16, v62\n"
"v_and_b32 v62, 0xffff0000, v62\n"
"v_mfma_f32_16x16x32_bf16 v[40:43], a[44:47], v[88:91], v[40:43]\n"
"v_fma_f32 v52, -v60, v28, v52\n"
"v_mfma_f32_16x16x32_bf16 v[40:43], a[56:59], v[92:95], v[40:43]\n"
"ds_read_b128 v[124:127], v4 offset:13824\n"
"v_fma_f32 v53, -v60, v29, v53\n"
"v_mfma_f32_16x16x32_bf16 v[40:43], a[68:71], v[96:99], v[40:43]\n"
"v_rcp_f32 v61, v62\n"
"v_mfma_f32_16x16x32_bf16 v[40:43], a[80:83], v[100:103], v[40:43]\n"
"v_mul_f32 v62, v62, v62\n"
"v_mul_f32 v61, 0xbfb8aa3b, v61\n"
"v_mfma_f32_16x16x32_bf16 v[40:43], a[92:95], v[104:107], v[40:43]\n"
"ds_read_b128 v[76:79], v4 offset:14976\n"
"v_fma_f32 v54, -v60, v30, v54\n"
"v_mfma_f32_16x16x32_bf16 v[36:39], a[88:91], v[104:107], v[36:39]\n"
"v_fma_f32 v55, -v60, v31, v55\n"
"v_fma_f32 v48, -v60, v24, v48\n"
"v_mfma_f32_16x16x32_bf16 v[36:39], a[76:79], v[100:103], v[36:39]\n"
"buffer_load_dwordx4 v2, s[32:35], s63 offen offset:1024 sc1 lds\n"
"v_fma_f32 v49, -v60, v25, v49\n"
"v_mfma_f32_16x16x32_bf16 v[36:39], a[64:67], v[96:99], v[36:39]\n"
"ds_read_b128 v[80:83], v4 offset:16128\n"
"v_fma_f32 v50, -v60, v26, v50\n"
"v_mfma_f32_16x16x32_bf16 v[36:39], a[52:55], v[92:95], v[36:39]\n"
"v_permlane32_swap_b32 v52, v54\n"
"v_mfma_f32_16x16x32_bf16 v[32:35], a[48:51], v[92:95], v[32:35]\n"
"v_permlane32_swap_b32 v53, v55\n"
"v_mfma_f32_16x16x32_bf16 v[32:35], a[60:63], v[96:99], v[32:35]\n"
"ds_read_b128 v[84:87], v4 offset:17280\n"
"v_fma_f32 v51, -v60, v27, v51\n"
"v_mfma_f32_16x16x32_bf16 v[32:35], a[72:75], v[100:103], v[32:35]\n"
"v_fma_f32 v44, -v60, v20, v44\n"
"v_fma_f32 v45, -v60, v21, v45\n"
"v_mfma_f32_16x16x32_bf16 v[32:35], a[84:87], v[104:107], v[32:35]\n"
"v_fma_f32 v46, -v60, v22, v46\n"
"s_waitcnt lgkmcnt(4)\n"
"v_mfma_f32_16x16x32_bf16 v[32:35], a[96:99], v[108:111], v[32:35]\n"
"ds_read_b128 v[88:91], v4 offset:18432\n"
"v_add_f32 v56, v56, v57\n"
"v_mfma_f32_16x16x32_bf16 v[32:35], a[108:111], v[112:115], v[32:35]\n"
"buffer_load_dwordx4 v2, s[32:35], s63 offen offset:2048 sc1 lds\n"
"v_add_f32 v58, v58, v59\n"
"v_mfma_f32_16x16x32_bf16 v[32:35], a[120:123], v[116:119], v[32:35]\n"
"v_add_f32 v56, v56, v58\n"
"v_fma_f32 v47, -v60, v23, v47\n"
"v_mfma_f32_16x16x32_bf16 v[32:35], a[132:135], v[120:123], v[32:35]\n"
"ds_read_b128 v[92:95], v4 offset:19584\n"
"v_add_f32_dpp v56, v56, v56 row_ror:8 row_mask:0xf bank_mask:0xf\n"
"v_mfma_f32_16x16x32_bf16 v[36:39], a[136:139], v[120:123], v[36:39]\n"
"v_mul_f32 v64, v44, v61\n"
"v_add_f32_dpp v56, v56, v56 row_ror:4 row_mask:0xf bank_mask:0xf\n"
"v_mfma_f32_16x16x32_bf16 v[36:39], a[124:127], v[116:119], v[36:39]\n"
"v_exp_f32 v64, v64\n"
"v_mfma_f32_16x16x32_bf16 v[36:39], a[112:115], v[112:115], v[36:39]\n"
"ds_read_b128 v[96:99], v4 offset:20736\n"
"v_add_f32_dpp v56, v56, v56 row_ror:2 row_mask:0xf bank_mask:0xf\n"
"v_mfma_f32_16x16x32_bf16 v[36:39], a[100:103], v[108:111], v[36:39]\n"
"v_mul_f32 v56, 0x3a800000, v56\n"
"v_mul_f32 v65, v45, v61\n"
"v_mfma_f32_16x16x32_bf16 v[40:43], a[104:107], v[108:111], v[40:43]\n"
"v_exp_f32 v65, v65\n"
"v_mfma_f32_16x16x32_bf16 v[40:43], a[116:119], v[112:115], v[40:43]\n"
"buffer_load_dwordx4 v2, s[32:35], s63 offen offset:3072 sc1 lds\n"
"ds_read_b128 v[100:103], v4 offset:21888\n"
"v_mfma_f32_16x16x32_bf16 v[40:43], a[128:131], v[116:119], v[40:43]\n"
"v_mul_f32 v57, v56, v56\n"
"v_mul_f32 v66, v46, v61\n"
"v_mfma_f32_16x16x32_bf16 v[40:43], a[140:143], v[120:123], v[40:43]\n"
"v_sub_f32_dpp v57, v56, v57 quad_perm:[1,1,3,3] row_mask:0xf bank_mask:0xf\n"
"s_waitcnt lgkmcnt(4)\n"
"v_mfma_f32_16x16x32_bf16 v[40:43], a[152:155], v[124:127], v[40:43]\n"
"ds_read_b128 v[104:107], v4 offset:23040\n"
"v_max_f32 v57, 0, v57\n"
"v_mfma_f32_16x16x32_bf16 v[40:43], a[164:167], v[76:79], v[40:43]\n"
"v_exp_f32 v66, v66\n"
"v_mfma_f32_16x16x32_bf16 v[40:43], a[176:179], v[80:83], v[40:43]\n"
"v_cvt_pk_bf16_f32 v57, 0, v57\n"
"v_mul_f32 v67, v47, v61\n"
"v_mfma_f32_16x16x32_bf16 v[40:43], a[188:191], v[84:87], v[40:43]\n"
"ds_read_b128 v[108:111], v4 offset:24192\n"
"s_add_u32 m0, s64, 4096\n"
"v_mfma_f32_16x16x32_bf16 v[36:39], a[184:187], v[84:87], v[36:39]\n"
"v_exp_f32 v67, v67\n"
"v_mfma_f32_16x16x32_bf16 v[36:39], a[172:175], v[80:83], v[36:39]\n"
"buffer_load_dwordx4 v6, s[32:35], s63 offen offset:0 sc1 lds\n"
"v_add_f32 v57, 0x3727c5ac, v57\n"
"v_mfma_f32_16x16x32_bf16 v[36:39], a[160:163], v[76:79], v[36:39]\n"
"ds_read_b128 v[112:115], v4 offset:25344\n"
"v_cvt_pk_bf16_f32 v57, 0, v57\n"
"v_mfma_f32_16x16x32_bf16 v[36:39], a[148:151], v[124:127], v[36:39]\n"
"v_sqrt_f32 v57, v57\n"
"v_mfma_f32_16x16x32_bf16 v[32:35], a[144:147], v[124:127], v[32:35]\n"
"v_cvt_pk_bf16_f32 v57, v56, v57\n"
"v_mul_f32 v68, v52, v61\n"
"v_mfma_f32_16x16x32_bf16 v[32:35], a[156:159], v[76:79], v[32:35]\n"
"ds_read_b128 v[116:119], v4 offset:26496\n"
"ds_write_b32 v10, v57 offset:0\n"
"v_mfma_f32_16x16x32_bf16 v[32:35], a[168:171], v[80:83], v[32:35]\n"
"v_exp_f32 v68, v68\n"
"v_mfma_f32_16x16x32_bf16 v[32:35], a[180:183], v[84:87], v[32:35]\n"
"v_mul_f32 v69, v53, v61\n"
"s_waitcnt lgkmcnt(5)\n"
"v_mfma_f32_16x16x32_bf16 v[32:35], a[192:195], v[88:91], v[32:35]\n"
"ds_read_b128 v[120:123], v4 offset:27648\n"
"v_mul_f32 v44, v44, v48\n"
"v_mfma_f32_16x16x32_bf16 v[32:35], a[204:207], v[92:95], v[32:35]\n"
"buffer_load_dwordx4 v6, s[32:35], s63 offen offset:1024 sc1 lds\n"
"v_mul_f32 v45, v45, v49\n"
"v_mfma_f32_16x16x32_bf16 v[32:35], a[216:219], v[96:99], v[32:35]\n"
"v_exp_f32 v69, v69\n"
"v_mfma_f32_16x16x32_bf16 v[32:35], a[228:231], v[100:103], v[32:35]\n"
"ds_read_b128 v[124:127], v4 offset:28800\n"
"v_mul_f32 v46, v46, v50\n"
"v_mfma_f32_16x16x32_bf16 v[36:39], a[232:235], v[100:103], v[36:39]\n"
"v_mul_f32 v47, v47, v51\n"
"v_mul_f32 v52, v52, v54\n"
"v_mfma_f32_16x16x32_bf16 v[36:39], a[220:223], v[96:99], v[36:39]\n"
"buffer_load_dwordx2 v[12:13], v14, s[32:35], s66 offen sc1\n"
"v_mul_f32 v53, v53, v55\n"
"v_mfma_f32_16x16x32_bf16 v[36:39], a[208:211], v[92:95], v[36:39]\n"
"ds_read_b128 v[76:79], v4 offset:29952\n"
"v_fma_f32 v64, v64, v62, v62\n"
"v_mfma_f32_16x16x32_bf16 v[36:39], a[196:199], v[88:91], v[36:39]\n"
"v_rcp_f32 v64, v64\n"
"v_mfma_f32_16x16x32_bf16 v[40:43], a[200:203], v[88:91], v[40:43]\n"
"v_fma_f32 v65, v65, v62, v62\n"
"v_fma_f32 v66, v66, v62, v62\n"
"v_mfma_f32_16x16x32_bf16 v[40:43], a[212:215], v[92:95], v[40:43]\n"
"buffer_load_dwordx4 v6, s[32:35], s63 offen offset:2048 sc1 lds\n"
"ds_read_b128 v[80:83], v4 offset:31104\n"
"v_mfma_f32_16x16x32_bf16 v[40:43], a[224:227], v[96:99], v[40:43]\n"
"v_rcp_f32 v65, v65\n"
"v_mfma_f32_16x16x32_bf16 v[40:43], a[236:239], v[100:103], v[40:43]\n"
"v_fma_f32 v67, v67, v62, v62\n"
"s_waitcnt lgkmcnt(5)\n"
"v_mfma_f32_16x16x32_bf16 v[40:43], a[248:251], v[104:107], v[40:43]\n"
"ds_read_b128 v[84:87], v4 offset:32256\n"
"v_fma_f32 v68, v68, v62, v62\n"
"v_mfma_f32_16x16x32_bf16 v[40:43], v[132:135], v[108:111], v[40:43]\n"
"v_rcp_f32 v66, v66\n"
"v_mfma_f32_16x16x32_bf16 v[40:43], v[144:147], v[112:115], v[40:43]\n"
"v_rcp_f32 v67, v67\n"
"v_mfma_f32_16x16x32_bf16 v[40:43], v[156:159], v[116:119], v[40:43]\n"
"ds_read_b128 v[88:91], v4 offset:33408\n"
"v_fma_f32 v69, v69, v62, v62\n"
"v_mfma_f32_16x16x32_bf16 v[36:39], v[152:155], v[116:119], v[36:39]\n"
"v_rcp_f32 v68, v68\n"
"v_mfma_f32_16x16x32_bf16 v[36:39], v[140:143], v[112:115], v[36:39]\n"
"buffer_load_dwordx4 v6, s[32:35], s63 offen offset:3072 sc1 lds\n"
"v_mul_f32 v44, v44, v64\n"
"v_mfma_f32_16x16x32_bf16 v[36:39], v[128:131], v[108:111], v[36:39]\n"
"ds_read_b128 v[92:95], v4 offset:34560\n"
"v_mul_f32 v45, v45, v65\n"
"v_mfma_f32_16x16x32_bf16 v[36:39], a[244:247], v[104:107], v[36:39]\n"
"v_rcp_f32 v69, v69\n"
"v_mfma_f32_16x16x32_bf16 v[32:35], a[240:243], v[104:107], v[32:35]\n"
"v_mul_f32 v46, v46, v66\n"
"v_mul_f32 v47, v47, v67\n"
"v_mfma_f32_16x16x32_bf16 v[32:35], a[252:255], v[108:111], v[32:35]\n"
"s_waitcnt vmcnt(9)\n"
"s_waitcnt lgkmcnt(7)\n"
"s_barrier\n"
"v_mfma_f32_16x16x32_bf16 v[32:35], v[136:139], v[112:115], v[32:35]\n"
"ds_read_b128 v[96:99], v4 offset:35712\n"
"v_xor_b32 v4, 73728, v4\n"
"v_mfma_f32_16x16x32_bf16 v[32:35], v[148:151], v[116:119], v[32:35]\n"
"s_add_u32 m0, s64, 8192\n"
"s_waitcnt lgkmcnt(4)\n"
"v_mfma_f32_16x16x32_bf16 v[32:35], v[160:163], v[120:123], v[32:35]\n"
"ds_read_b128 v[100:103], v5 offset:0\n"
"v_mul_f32 v52, v52, v68\n"
"v_mfma_f32_16x16x32_bf16 v[32:35], v[172:175], v[124:127], v[32:35]\n"
"buffer_load_dwordx4 v7, s[32:35], s63 offen offset:0 sc1 lds\n"
"s_add_u32 s63, s63, 890880\n"
"v_mfma_f32_16x16x32_bf16 v[32:35], v[184:187], v[76:79], v[32:35]\n"
"v_mul_f32 v53, v53, v69\n"
"v_cvt_pk_bf16_f32 v72, v44, v45\n"
"v_mfma_f32_16x16x32_bf16 v[32:35], v[196:199], v[80:83], v[32:35]\n"
"ds_read_b128 v[104:107], v5 offset:1152\n"
"v_cvt_pk_bf16_f32 v74, v52, v53\n"
"v_mfma_f32_16x16x32_bf16 v[36:39], v[200:203], v[80:83], v[36:39]\n"
"v_cvt_pk_bf16_f32 v73, v46, v47\n"
"v_mfma_f32_16x16x32_bf16 v[36:39], v[188:191], v[76:79], v[36:39]\n"
"v_permlane32_swap_b32 v74, v75\n"
"v_mfma_f32_16x16x32_bf16 v[36:39], v[176:179], v[124:127], v[36:39]\n"
"ds_read_b128 v[108:111], v5 offset:2304\n"
"v_mfma_f32_16x16x32_bf16 v[36:39], v[164:167], v[120:123], v[36:39]\n"
"v_permlane16_swap_b32 v72, v74\n"
"v_mfma_f32_16x16x32_bf16 v[40:43], v[168:171], v[120:123], v[40:43]\n"
"v_permlane16_swap_b32 v73, v75\n"
"v_mfma_f32_16x16x32_bf16 v[40:43], v[180:183], v[124:127], v[40:43]\n"
"buffer_store_dwordx4 v[72:75], v15, s[32:35], s68 offen sc1\n"
"ds_read_b128 v[112:115], v5 offset:3456\n"
"v_mfma_f32_16x16x32_bf16 v[40:43], v[192:195], v[76:79], v[40:43]\n"
"v_mfma_f32_16x16x32_bf16 v[40:43], v[204:207], v[80:83], v[40:43]\n"
"s_waitcnt lgkmcnt(4)\n"
"v_mfma_f32_16x16x32_bf16 v[40:43], v[216:219], v[84:87], v[40:43]\n"
"ds_read_b128 v[116:119], v5 offset:4608\n"
"v_mfma_f32_16x16x32_bf16 v[40:43], v[228:231], v[88:91], v[40:43]\n"
"v_mfma_f32_16x16x32_bf16 v[40:43], v[240:243], v[92:95], v[40:43]\n"
"v_mfma_f32_16x16x32_bf16 v[40:43], v[252:255], v[96:99], v[40:43]\n"
"ds_read_b128 v[120:123], v5 offset:5760\n"
"v_mfma_f32_16x16x32_bf16 v[36:39], v[248:251], v[96:99], v[36:39]\n"
"v_mfma_f32_16x16x32_bf16 v[36:39], v[236:239], v[92:95], v[36:39]\n"
"v_mfma_f32_16x16x32_bf16 v[36:39], v[224:227], v[88:91], v[36:39]\n"
"ds_read_b128 v[124:127], v5 offset:6912\n"
"v_mfma_f32_16x16x32_bf16 v[36:39], v[212:215], v[84:87], v[36:39]\n"
"v_mfma_f32_16x16x32_bf16 v[32:35], v[208:211], v[84:87], v[32:35]\n"
"v_mfma_f32_16x16x32_bf16 v[32:35], v[220:223], v[88:91], v[32:35]\n"
"ds_read_b128 v[76:79], v5 offset:8064\n"
"v_mfma_f32_16x16x32_bf16 v[32:35], v[232:235], v[92:95], v[32:35]\n"
"s_sub_u32 s62, s62, 1\n"
"v_mfma_f32_16x16x32_bf16 v[32:35], v[244:247], v[96:99], v[32:35]\n"
"s_cbranch_scc1 .L_gt0_2\n"
"s_waitcnt lgkmcnt(4)\n"
"v_mfma_f32_16x16x32_bf16 v[44:47], a[0:3], v[100:103], 0\n"
"ds_read_b128 v[80:83], v5 offset:9216\n"
"ds_read_b32 v62, v11 offset:0\n"
"v_mfma_f32_16x16x32_bf16 v[44:47], a[12:15], v[104:107], v[44:47]\n"
"s_add_u32 s68, s68, 890880\n"
"v_mfma_f32_16x16x32_bf16 v[44:47], a[24:27], v[108:111], v[44:47]\n"
"v_mfma_f32_16x16x32_bf16 v[44:47], a[36:39], v[112:115], v[44:47]\n"
"ds_read_b128 v[84:87], v5 offset:10368\n"
"v_mfma_f32_16x16x32_bf16 v[48:51], a[40:43], v[112:115], 0\n"
"ds_read2st64_b32 v[56:57], v8 offset0:144 offset1:180\n"
"v_mfma_f32_16x16x32_bf16 v[48:51], a[28:31], v[108:111], v[48:51]\n"
"s_waitcnt vmcnt(4)\n"
"v_cmp_ne_u64 vcc, v[16:17], v[12:13]\n"
"v_mfma_f32_16x16x32_bf16 v[48:51], a[16:19], v[104:107], v[48:51]\n"
"ds_read_b128 v[88:91], v5 offset:11520\n"
"ds_read2st64_b32 v[58:59], v8 offset0:216 offset1:252\n"
"v_mfma_f32_16x16x32_bf16 v[48:51], a[4:7], v[100:103], v[48:51]\n"
"s_cbranch_vccnz .L_rtg1_2\n"
".L_bkg1_2:\n"
"s_add_u32 s66, s66, 890880\n"
"v_mfma_f32_16x16x32_bf16 v[52:55], a[8:11], v[100:103], 0\n"
"s_add_u32 m0, s64, 36864\n"
"v_xor_b32 v8, v9, v8\n"
"v_mfma_f32_16x16x32_bf16 v[52:55], a[20:23], v[104:107], v[52:55]\n"
"buffer_load_dwordx4 v2, s[32:35], s63 offen offset:0 sc1 lds\n"
"ds_read_b128 v[92:95], v5 offset:12672\n"
"v_mfma_f32_16x16x32_bf16 v[52:55], a[32:35], v[108:111], v[52:55]\n"
"s_waitcnt lgkmcnt(5)\n"
"v_lshlrev_b32 v60, 16, v62\n"
"v_mfma_f32_16x16x32_bf16 v[52:55], a[44:47], v[112:115], v[52:55]\n"
"v_and_b32 v62, 0xffff0000, v62\n"
"v_mfma_f32_16x16x32_bf16 v[52:55], a[56:59], v[116:119], v[52:55]\n"
"ds_read_b128 v[96:99], v5 offset:13824\n"
"v_fma_f32 v40, -v60, v28, v40\n"
"v_mfma_f32_16x16x32_bf16 v[52:55], a[68:71], v[120:123], v[52:55]\n"
"v_rcp_f32 v61, v62\n"
"v_mfma_f32_16x16x32_bf16 v[52:55], a[80:83], v[124:127], v[52:55]\n"
"v_mul_f32 v62, v62, v62\n"
"v_mul_f32 v61, 0xbfb8aa3b, v61\n"
"v_mfma_f32_16x16x32_bf16 v[52:55], a[92:95], v[76:79], v[52:55]\n"
"ds_read_b128 v[100:103], v5 offset:14976\n"
"v_fma_f32 v41, -v60, v29, v41\n"
"v_mfma_f32_16x16x32_bf16 v[48:51], a[88:91], v[76:79], v[48:51]\n"
"v_fma_f32 v42, -v60, v30, v42\n"
"v_fma_f32 v43, -v60, v31, v43\n"
"v_mfma_f32_16x16x32_bf16 v[48:51], a[76:79], v[124:127], v[48:51]\n"
"buffer_load_dwordx4 v2, s[32:35], s63 offen offset:1024 sc1 lds\n"
"v_fma_f32 v36, -v60, v24, v36\n"
"v_mfma_f32_16x16x32_bf16 v[48:51], a[64:67], v[120:123], v[48:51]\n"
"ds_read_b128 v[104:107], v5 offset:16128\n"
"v_fma_f32 v37, -v60, v25, v37\n"
"v_mfma_f32_16x16x32_bf16 v[48:51], a[52:55], v[116:119], v[48:51]\n"
"v_permlane32_swap_b32 v40, v42\n"
"v_mfma_f32_16x16x32_bf16 v[44:47], a[48:51], v[116:119], v[44:47]\n"
"v_permlane32_swap_b32 v41, v43\n"
"v_mfma_f32_16x16x32_bf16 v[44:47], a[60:63], v[120:123], v[44:47]\n"
"ds_read_b128 v[108:111], v5 offset:17280\n"
"v_fma_f32 v38, -v60, v26, v38\n"
"v_mfma_f32_16x16x32_bf16 v[44:47], a[72:75], v[124:127], v[44:47]\n"
"v_fma_f32 v39, -v60, v27, v39\n"
"v_fma_f32 v32, -v60, v20, v32\n"
"v_mfma_f32_16x16x32_bf16 v[44:47], a[84:87], v[76:79], v[44:47]\n"
"v_fma_f32 v33, -v60, v21, v33\n"
"s_waitcnt lgkmcnt(4)\n"
"v_mfma_f32_16x16x32_bf16 v[44:47], a[96:99], v[80:83], v[44:47]\n"
"ds_read_b128 v[112:115], v5 offset:18432\n"
"v_add_f32 v56, v56, v57\n"
"v_mfma_f32_16x16x32_bf16 v[44:47], a[108:111], v[84:87], v[44:47]\n"
"buffer_load_dwordx4 v2, s[32:35], s63 offen offset:2048 sc1 lds\n"
"v_add_f32 v58, v58, v59\n"
"v_mfma_f32_16x16x32_bf16 v[44:47], a[120:123], v[88:91], v[44:47]\n"
"v_add_f32 v56, v56, v58\n"
"v_fma_f32 v34, -v60, v22, v34\n"
"v_mfma_f32_16x16x32_bf16 v[44:47], a[132:135], v[92:95], v[44:47]\n"
"ds_read_b128 v[116:119], v5 offset:19584\n"
"v_add_f32_dpp v56, v56, v56 row_ror:8 row_mask:0xf bank_mask:0xf\n"
"v_mfma_f32_16x16x32_bf16 v[48:51], a[136:139], v[92:95], v[48:51]\n"
"v_fma_f32 v35, -v60, v23, v35\n"
"v_add_f32_dpp v56, v56, v56 row_ror:4 row_mask:0xf bank_mask:0xf\n"
"v_mfma_f32_16x16x32_bf16 v[48:51], a[124:127], v[88:91], v[48:51]\n"
"v_mul_f32 v64, v32, v61\n"
"v_add_f32_dpp v56, v56, v56 row_ror:2 row_mask:0xf bank_mask:0xf\n"
"v_mfma_f32_16x16x32_bf16 v[48:51], a[112:115], v[84:87], v[48:51]\n"
"ds_read_b128 v[120:123], v5 offset:20736\n"
"v_mul_f32 v56, 0x3a800000, v56\n"
"v_mfma_f32_16x16x32_bf16 v[48:51], a[100:103], v[80:83], v[48:51]\n"
"v_exp_f32 v64, v64\n"
"v_mfma_f32_16x16x32_bf16 v[52:55], a[104:107], v[80:83], v[52:55]\n"
"v_mul_f32 v57, v56, v56\n"
"v_mul_f32 v65, v33, v61\n"
"v_mfma_f32_16x16x32_bf16 v[52:55], a[116:119], v[84:87], v[52:55]\n"
"buffer_load_dwordx4 v2, s[32:35], s63 offen offset:3072 sc1 lds\n"
"ds_read_b128 v[124:127], v5 offset:21888\n"
"v_mfma_f32_16x16x32_bf16 v[52:55], a[128:131], v[88:91], v[52:55]\n"
"v_exp_f32 v65, v65\n"
"v_mfma_f32_16x16x32_bf16 v[52:55], a[140:143], v[92:95], v[52:55]\n"
"v_sub_f32_dpp v57, v56, v57 quad_perm:[1,1,3,3] row_mask:0xf bank_mask:0xf\n"
"s_waitcnt lgkmcnt(4)\n"
"v_mfma_f32_16x16x32_bf16 v[52:55], a[152:155], v[96:99], v[52:55]\n"
"ds_read_b128 v[76:79], v5 offset:23040\n"
"v_max_f32 v57, 0, v57\n"
"v_mfma_f32_16x16x32_bf16 v[52:55], a[164:167], v[100:103], v[52:55]\n"
"v_cvt_pk_bf16_f32 v57, 0, v57\n"
"v_mul_f32 v66, v34, v61\n"
"v_mfma_f32_16x16x32_bf16 v[52:55], a[176:179], v[104:107], v[52:55]\n"
"v_exp_f32 v66, v66\n"
"v_mfma_f32_16x16x32_bf16 v[52:55], a[188:191], v[108:111], v[52:55]\n"
"ds_read_b128 v[80:83], v5 offset:24192\n"
"s_add_u32 m0, s64, 40960\n"
"v_mfma_f32_16x16x32_bf16 v[48:51], a[184:187], v[108:111], v[48:51]\n"
"v_add_f32 v57, 0x3727c5ac, v57\n"
"v_mul_f32 v67, v35, v61\n"
"v_mfma_f32_16x16x32_bf16 v[48:51], a[172:175], v[104:107], v[48:51]\n"
"buffer_load_dwordx4 v6, s[32:35], s63 offen offset:0 sc1 lds\n"
"v_cvt_pk_bf16_f32 v57, 0, v57\n"
"v_mfma_f32_16x16x32_bf16 v[48:51], a[160:163], v[100:103], v[48:51]\n"
"ds_read_b128 v[84:87], v5 offset:25344\n"
"v_mul_f32 v68, v40, v61\n"
"v_mfma_f32_16x16x32_bf16 v[48:51], a[148:151], v[96:99], v[48:51]\n"
"v_sqrt_f32 v57, v57\n"
"v_mfma_f32_16x16x32_bf16 v[44:47], a[144:147], v[96:99], v[44:47]\n"
"v_exp_f32 v67, v67\n"
"v_mfma_f32_16x16x32_bf16 v[44:47], a[156:159], v[100:103], v[44:47]\n"
"ds_read_b128 v[88:91], v5 offset:26496\n"
"v_cvt_pk_bf16_f32 v57, v56, v57\n"
"v_mfma_f32_16x16x32_bf16 v[44:47], a[168:171], v[104:107], v[44:47]\n"
"ds_write_b32 v10, v57 offset:128\n"
"v_mul_f32 v69, v41, v61\n"
"v_mfma_f32_16x16x32_bf16 v[44:47], a[180:183], v[108:111], v[44:47]\n"
"v_mul_f32 v32, v32, v36\n"
"s_waitcnt lgkmcnt(5)\n"
"v_mfma_f32_16x16x32_bf16 v[44:47], a[192:195], v[112:115], v[44:47]\n"
"ds_read_b128 v[92:95], v5 offset:27648\n"
"v_mul_f32 v33, v33, v37\n"
"v_mfma_f32_16x16x32_bf16 v[44:47], a[204:207], v[116:119], v[44:47]\n"
"buffer_load_dwordx4 v6, s[32:35], s63 offen offset:1024 sc1 lds\n"
"v_mul_f32 v34, v34, v38\n"
"v_mfma_f32_16x16x32_bf16 v[44:47], a[216:219], v[120:123], v[44:47]\n"
"v_exp_f32 v68, v68\n"
"v_mfma_f32_16x16x32_bf16 v[44:47], a[228:231], v[124:127], v[44:47]\n"
"ds_read_b128 v[96:99], v5 offset:28800\n"
"v_mul_f32 v35, v35, v39\n"
"v_mfma_f32_16x16x32_bf16 v[48:51], a[232:235], v[124:127], v[48:51]\n"
"v_exp_f32 v69, v69\n"
"v_mfma_f32_16x16x32_bf16 v[48:51], a[220:223], v[120:123], v[48:51]\n"
"buffer_load_dwordx2 v[12:13], v14, s[32:35], s66 offen sc1\n"
"v_mul_f32 v40, v40, v42\n"
"v_mfma_f32_16x16x32_bf16 v[48:51], a[208:211], v[116:119], v[48:51]\n"
"ds_read_b128 v[100:103], v5 offset:29952\n"
"v_mul_f32 v41, v41, v43\n"
"v_mfma_f32_16x16x32_bf16 v[48:51], a[196:199], v[112:115], v[48:51]\n"
"v_fma_f32 v64, v64, v62, v62\n"
"v_fma_f32 v65, v65, v62, v62\n"
"v_mfma_f32_16x16x32_bf16 v[52:55], a[200:203], v[112:115], v[52:55]\n"
"v_rcp_f32 v64, v64\n"
"v_mfma_f32_16x16x32_bf16 v[52:55], a[212:215], v[116:119], v[52:55]\n"
"buffer_load_dwordx4 v6, s[32:35], s63 offen offset:2048 sc1 lds\n"
"ds_read_b128 v[104:107], v5 offset:31104\n"
"v_mfma_f32_16x16x32_bf16 v[52:55], a[224:227], v[120:123], v[52:55]\n"
"v_rcp_f32 v65, v65\n"
"v_mfma_f32_16x16x32_bf16 v[52:55], a[236:239], v[124:127], v[52:55]\n"
"v_fma_f32 v66, v66, v62, v62\n"
"s_waitcnt lgkmcnt(5)\n"
"v_mfma_f32_16x16x32_bf16 v[52:55], a[248:251], v[76:79], v[52:55]\n"
"ds_read_b128 v[108:111], v5 offset:32256\n"
"v_fma_f32 v67, v67, v62, v62\n"
"v_mfma_f32_16x16x32_bf16 v[52:55], v[132:135], v[80:83], v[52:55]\n"
"v_rcp_f32 v66, v66\n"
"v_mfma_f32_16x16x32_bf16 v[52:55], v[144:147], v[84:87], v[52:55]\n"
"v_rcp_f32 v67, v67\n"
"v_mfma_f32_16x16x32_bf16 v[52:55], v[156:159], v[88:91], v[52:55]\n"
"ds_read_b128 v[112:115], v5 offset:33408\n"
"v_fma_f32 v68, v68, v62, v62\n"
"v_mfma_f32_16x16x32_bf16 v[48:51], v[152:155], v[88:91], v[48:51]\n"
"v_rcp_f32 v68, v68\n"
"v_mfma_f32_16x16x32_bf16 v[48:51], v[140:143], v[84:87], v[48:51]\n"
"buffer_load_dwordx4 v6, s[32:35], s63 offen offset:3072 sc1 lds\n"
"v_fma_f32 v69, v69, v62, v62\n"
"v_mfma_f32_16x16x32_bf16 v[48:51], v[128:131], v[80:83], v[48:51]\n"
"ds_read_b128 v[116:119], v5 offset:34560\n"
"v_mul_f32 v32, v32, v64\n"
"v_mfma_f32_16x16x32_bf16 v[48:51], a[244:247], v[76:79], v[48:51]\n"
"v_rcp_f32 v69, v69\n"
"v_mfma_f32_16x16x32_bf16 v[44:47], a[240:243], v[76:79], v[44:47]\n"
"v_mul_f32 v33, v33, v65\n"
"v_mul_f32 v34, v34, v66\n"
"v_mfma_f32_16x16x32_bf16 v[44:47], a[252:255], v[80:83], v[44:47]\n"
"s_waitcnt vmcnt(9)\n"
"s_waitcnt lgkmcnt(7)\n"
"s_barrier\n"
"v_mfma_f32_16x16x32_bf16 v[44:47], v[136:139], v[84:87], v[44:47]\n"
"ds_read_b128 v[120:123], v5 offset:35712\n"
"v_xor_b32 v5, 73728, v5\n"
"v_mfma_f32_16x16x32_bf16 v[44:47], v[148:151], v[88:91], v[44:47]\n"
"s_add_u32 m0, s64, 45056\n"
"s_waitcnt lgkmcnt(4)\n"
"v_mfma_f32_16x16x32_bf16 v[44:47], v[160:163], v[92:95], v[44:47]\n"
"ds_read_b128 v[76:79], v4 offset:0\n"
"v_mul_f32 v35, v35, v67\n"
"v_mfma_f32_16x16x32_bf16 v[44:47], v[172:175], v[96:99], v[44:47]\n"
"buffer_load_dwordx4 v7, s[32:35], s63 offen offset:0 sc1 lds\n"
"s_add_u32 s63, s63, 890880\n"
"v_mfma_f32_16x16x32_bf16 v[44:47], v[184:187], v[100:103], v[44:47]\n"
"s_xor_b32 s64, s64, s65\n"
"v_mul_f32 v40, v40, v68\n"
"v_mfma_f32_16x16x32_bf16 v[44:47], v[196:199], v[104:107], v[44:47]\n"
"ds_read_b128 v[80:83], v4 offset:1152\n"
"v_mul_f32 v41, v41, v69\n"
"v_mfma_f32_16x16x32_bf16 v[48:51], v[200:203], v[104:107], v[48:51]\n"
"v_cvt_pk_bf16_f32 v74, v40, v41\n"
"v_cvt_pk_bf16_f32 v72, v32, v33\n"
"v_mfma_f32_16x16x32_bf16 v[48:51], v[188:191], v[100:103], v[48:51]\n"
"v_permlane32_swap_b32 v74, v75\n"
"v_mfma_f32_16x16x32_bf16 v[48:51], v[176:179], v[96:99], v[48:51]\n"
"ds_read_b128 v[84:87], v4 offset:2304\n"
"v_cvt_pk_bf16_f32 v73, v34, v35\n"
"v_mfma_f32_16x16x32_bf16 v[48:51], v[164:167], v[92:95], v[48:51]\n"
"v_permlane16_swap_b32 v72, v74\n"
"v_mfma_f32_16x16x32_bf16 v[52:55], v[168:171], v[92:95], v[52:55]\n"
"v_permlane16_swap_b32 v73, v75\n"
"v_mfma_f32_16x16x32_bf16 v[52:55], v[180:183], v[96:99], v[52:55]\n"
"buffer_store_dwordx4 v[72:75], v15, s[32:35], s68 offen sc1\n"
"ds_read_b128 v[88:91], v4 offset:3456\n"
"v_mfma_f32_16x16x32_bf16 v[52:55], v[192:195], v[100:103], v[52:55]\n"
"v_mfma_f32_16x16x32_bf16 v[52:55], v[204:207], v[104:107], v[52:55]\n"
"s_waitcnt lgkmcnt(4)\n"
"v_mfma_f32_16x16x32_bf16 v[52:55], v[216:219], v[108:111], v[52:55]\n"
"ds_read_b128 v[92:95], v4 offset:4608\n"
"v_mfma_f32_16x16x32_bf16 v[52:55], v[228:231], v[112:115], v[52:55]\n"
"v_mfma_f32_16x16x32_bf16 v[52:55], v[240:243], v[116:119], v[52:55]\n"
"v_mfma_f32_16x16x32_bf16 v[52:55], v[252:255], v[120:123], v[52:55]\n"
"ds_read_b128 v[96:99], v4 offset:5760\n"
"v_mfma_f32_16x16x32_bf16 v[48:51], v[248:251], v[120:123], v[48:51]\n"
"v_mfma_f32_16x16x32_bf16 v[48:51], v[236:239], v[116:119], v[48:51]\n"
"v_mfma_f32_16x16x32_bf16 v[48:51], v[224:227], v[112:115], v[48:51]\n"
"ds_read_b128 v[100:103], v4 offset:6912\n"
"v_mfma_f32_16x16x32_bf16 v[48:51], v[212:215], v[108:111], v[48:51]\n"
"v_mfma_f32_16x16x32_bf16 v[44:47], v[208:211], v[108:111], v[44:47]\n"
"v_mfma_f32_16x16x32_bf16 v[44:47], v[220:223], v[112:115], v[44:47]\n"
"ds_read_b128 v[104:107], v4 offset:8064\n"
"v_mfma_f32_16x16x32_bf16 v[44:47], v[232:235], v[116:119], v[44:47]\n"
"s_sub_u32 s62, s62, 1\n"
"v_mfma_f32_16x16x32_bf16 v[44:47], v[244:247], v[120:123], v[44:47]\n"
"s_cbranch_scc0 .L_gl_2\n"
"s_nop 1\n"
"ds_read_b32 v62, v11 offset:128\n"
"s_add_u32 s68, s68, 890880\n"
"s_waitcnt lgkmcnt(0)\n"
"v_lshlrev_b32 v60, 16, v62\n"
"v_and_b32 v62, 0xffff0000, v62\n"
"v_rcp_f32 v61, v62\n"
"v_mul_f32 v62, v62, v62\n"
"v_mul_f32 v61, 0xbfb8aa3b, v61\n"
"v_fma_f32 v52, -v60, v28, v52\n"
"v_fma_f32 v53, -v60, v29, v53\n"
"v_fma_f32 v54, -v60, v30, v54\n"
"v_fma_f32 v55, -v60, v31, v55\n"
"v_fma_f32 v48, -v60, v24, v48\n"
"v_fma_f32 v49, -v60, v25, v49\n"
"v_fma_f32 v50, -v60, v26, v50\n"
"v_fma_f32 v51, -v60, v27, v51\n"
"v_fma_f32 v44, -v60, v20, v44\n"
"v_fma_f32 v45, -v60, v21, v45\n"
"v_fma_f32 v46, -v60, v22, v46\n"
"v_fma_f32 v47, -v60, v23, v47\n"
"v_permlane32_swap_b32 v52, v54\n"
"v_permlane32_swap_b32 v53, v55\n"
"v_mul_f32 v64, v44, v61\n"
"v_mul_f32 v65, v45, v61\n"
"v_mul_f32 v66, v46, v61\n"
"v_mul_f32 v67, v47, v61\n"
"v_mul_f32 v68, v52, v61\n"
"v_mul_f32 v69, v53, v61\n"
"v_exp_f32 v64, v64\n"
"v_exp_f32 v65, v65\n"
"v_mul_f32 v44, v44, v48\n"
"v_mul_f32 v45, v45, v49\n"
"v_exp_f32 v66, v66\n"
"v_exp_f32 v67, v67\n"
"v_mul_f32 v46, v46, v50\n"
"v_mul_f32 v47, v47, v51\n"
"v_exp_f32 v68, v68\n"
"v_exp_f32 v69, v69\n"
"v_mul_f32 v52, v52, v54\n"
"v_mul_f32 v53, v53, v55\n"
"v_fma_f32 v64, v64, v62, v62\n"
"v_fma_f32 v65, v65, v62, v62\n"
"v_rcp_f32 v64, v64\n"
"v_rcp_f32 v65, v65\n"
"v_fma_f32 v66, v66, v62, v62\n"
"v_fma_f32 v67, v67, v62, v62\n"
"v_rcp_f32 v66, v66\n"
"v_rcp_f32 v67, v67\n"
"v_fma_f32 v68, v68, v62, v62\n"
"v_fma_f32 v69, v69, v62, v62\n"
"v_rcp_f32 v68, v68\n"
"v_rcp_f32 v69, v69\n"
"v_mul_f32 v44, v44, v64\n"
"v_mul_f32 v45, v45, v65\n"
"v_mul_f32 v46, v46, v66\n"
"v_mul_f32 v47, v47, v67\n"
"v_mul_f32 v52, v52, v68\n"
"v_mul_f32 v53, v53, v69\n"
"v_cvt_pk_bf16_f32 v74, v52, v53\n"
"v_cvt_pk_bf16_f32 v72, v44, v45\n"
"v_cvt_pk_bf16_f32 v73, v46, v47\n"
"v_permlane32_swap_b32 v74, v75\n"
"s_nop 1\n"
"v_permlane16_swap_b32 v72, v74\n"
"v_permlane16_swap_b32 v73, v75\n"
"buffer_store_dwordx4 v[72:75], v15, s[32:35], s68 offen sc1\n"
"s_waitcnt vmcnt(1)\n"
"s_barrier\n"
"buffer_store_dwordx2 v[16:17], v2, s[92:95], s68 idxen sc1\n"
"s_add_u32 s68, s68, 890880\n"
"s_waitcnt vmcnt(1)\n"
"s_barrier\n"
"buffer_store_dwordx2 v[16:17], v2, s[92:95], s68 idxen sc1\n"
"s_branch .L_end\n"
".L_gt0_2:\n"
"s_nop 1\n"
"ds_read_b32 v62, v11 offset:0\n"
"s_add_u32 s68, s68, 890880\n"
"s_waitcnt lgkmcnt(0)\n"
"v_lshlrev_b32 v60, 16, v62\n"
"v_and_b32 v62, 0xffff0000, v62\n"
"v_rcp_f32 v61, v62\n"
"v_mul_f32 v62, v62, v62\n"
"v_mul_f32 v61, 0xbfb8aa3b, v61\n"
"v_fma_f32 v40, -v60, v28, v40\n"
"v_fma_f32 v41, -v60, v29, v41\n"
"v_fma_f32 v42, -v60, v30, v42\n"
"v_fma_f32 v43, -v60, v31, v43\n"
"v_fma_f32 v36, -v60, v24, v36\n"
"v_fma_f32 v37, -v60, v25, v37\n"
"v_fma_f32 v38, -v60, v26, v38\n"
"v_fma_f32 v39, -v60, v27, v39\n"
"v_fma_f32 v32, -v60, v20, v32\n"
"v_fma_f32 v33, -v60, v21, v33\n"
"v_fma_f32 v34, -v60, v22, v34\n"
"v_fma_f32 v35, -v60, v23, v35\n"
"v_permlane32_swap_b32 v40, v42\n"
"v_permlane32_swap_b32 v41, v43\n"
"v_mul_f32 v64, v32, v61\n"
"v_mul_f32 v65, v33, v61\n"
"v_mul_f32 v66, v34, v61\n"
"v_mul_f32 v67, v35, v61\n"
"v_mul_f32 v68, v40, v61\n"
"v_mul_f32 v69, v41, v61\n"
"v_exp_f32 v64, v64\n"
"v_exp_f32 v65, v65\n"
"v_mul_f32 v32, v32, v36\n"
"v_mul_f32 v33, v33, v37\n"
"v_exp_f32 v66, v66\n"
"v_exp_f32 v67, v67\n"
"v_mul_f32 v34, v34, v38\n"
"v_mul_f32 v35, v35, v39\n"
"v_exp_f32 v68, v68\n"
"v_exp_f32 v69, v69\n"
"v_mul_f32 v40, v40, v42\n"
"v_mul_f32 v41, v41, v43\n"
"v_fma_f32 v64, v64, v62, v62\n"
"v_fma_f32 v65, v65, v62, v62\n"
"v_rcp_f32 v64, v64\n"
"v_rcp_f32 v65, v65\n"
"v_fma_f32 v66, v66, v62, v62\n"
"v_fma_f32 v67, v67, v62, v62\n"
"v_rcp_f32 v66, v66\n"
"v_rcp_f32 v67, v67\n"
"v_fma_f32 v68, v68, v62, v62\n"
"v_fma_f32 v69, v69, v62, v62\n"
"v_rcp_f32 v68, v68\n"
"v_rcp_f32 v69, v69\n"
"v_mul_f32 v32, v32, v64\n"
"v_mul_f32 v33, v33, v65\n"
"v_mul_f32 v34, v34, v66\n"
"v_mul_f32 v35, v35, v67\n"
"v_mul_f32 v40, v40, v68\n"
"v_mul_f32 v41, v41, v69\n"
"v_cvt_pk_bf16_f32 v74, v40, v41\n"
"v_cvt_pk_bf16_f32 v72, v32, v33\n"
"v_cvt_pk_bf16_f32 v73, v34, v35\n"
"v_permlane32_swap_b32 v74, v75\n"
"s_nop 1\n"
"v_permlane16_swap_b32 v72, v74\n"
"v_permlane16_swap_b32 v73, v75\n"
"buffer_store_dwordx4 v[72:75], v15, s[32:35], s68 offen sc1\n"
"s_waitcnt vmcnt(1)\n"
"s_barrier\n"
"buffer_store_dwordx2 v[16:17], v2, s[92:95], s68 idxen sc1\n"
"s_add_u32 s68, s68, 890880\n"
"s_waitcnt vmcnt(1)\n"
"s_barrier\n"
"buffer_store_dwordx2 v[16:17], v2, s[92:95], s68 idxen sc1\n"
"s_branch .L_end\n"
".p2align 6\n"
".L_gv3:\n"
".L_gl_3:\n"
"s_waitcnt lgkmcnt(4)\n"
"v_mfma_f32_16x16x32_bf16 v[32:35], a[0:3], v[76:79], 0\n"
"ds_read_b128 v[108:111], v4 offset:9216\n"
"ds_read_b32 v62, v11 offset:128\n"
"v_mfma_f32_16x16x32_bf16 v[32:35], a[12:15], v[80:83], v[32:35]\n"
"s_add_u32 s68, s68, 890880\n"
"v_mfma_f32_16x16x32_bf16 v[32:35], a[24:27], v[84:87], v[32:35]\n"
"v_mfma_f32_16x16x32_bf16 v[32:35], a[36:39], v[88:91], v[32:35]\n"
"ds_read_b128 v[112:115], v4 offset:10368\n"
"v_mfma_f32_16x16x32_bf16 v[36:39], a[40:43], v[88:91], 0\n"
"ds_read2st64_b32 v[56:57], v8 offset0:0 offset1:36\n"
"v_mfma_f32_16x16x32_bf16 v[36:39], a[28:31], v[84:87], v[36:39]\n"
"s_waitcnt vmcnt(5)\n"
"v_cmp_ne_u64 vcc, v[16:17], v[12:13]\n"
"v_mfma_f32_16x16x32_bf16 v[36:39], a[16:19], v[80:83], v[36:39]\n"
"ds_read_b128 v[116:119], v4 offset:11520\n"
"ds_read2st64_b32 v[58:59], v8 offset0:72 offset1:108\n"
"v_mfma_f32_16x16x32_bf16 v[36:39], a[4:7], v[76:79], v[36:39]\n"
"s_cbranch_vccnz .L_rtg0_3\n"
".L_bkg0_3:\n"
"s_add_u32 s66, s66, 890880\n"
"v_mfma_f32_16x16x32_bf16 v[40:43], a[8:11], v[76:79], 0\n"
"s_add_u32 m0, s64, 0\n"
"s_waitcnt lgkmcnt(4)\n"
"v_mfma_f32_16x16x32_bf16 v[40:43], a[20:23], v[80:83], v[40:43]\n"
"buffer_load_dwordx4 v2, s[32:35], s63 offen offset:0 sc1 lds\n"
"ds_read_b128 v[120:123], v4 offset:12672\n"
"v_mfma_f32_16x16x32_bf16 v[40:43], a[32:35], v[84:87], v[40:43]\n"
"v_lshlrev_b32 v60, 16, v62\n"
"v_and_b32 v62, 0xffff0000, v62\n"
"v_mfma_f32_16x16x32_bf16 v[40:43], a[44:47], v[88:91], v[40:43]\n"
"v_fma_f32 v52, -v60, v28, v52\n"
"v_mfma_f32_16x16x32_bf16 v[40:43], a[56:59], v[92:95], v[40:43]\n"
"ds_read_b128 v[124:127], v4 offset:13824\n"
"v_fma_f32 v53, -v60, v29, v53\n"
"v_mfma_f32_16x16x32_bf16 v[40:43], a[68:71], v[96:99], v[40:43]\n"
"v_rcp_f32 v61, v62\n"
"v_mfma_f32_16x16x32_bf16 v[40:43], a[80:83], v[100:103], v[40:43]\n"
"v_mul_f32 v62, v62, v62\n"
"v_mul_f32 v61, 0xbfb8aa3b, v61\n"
"v_mfma_f32_16x16x32_bf16 v[40:43], a[92:95], v[104:107], v[40:43]\n"
"ds_read_b128 v[76:79], v4 offset:14976\n"
"v_fma_f32 v54, -v60, v30, v54\n"
"v_mfma_f32_16x16x32_bf16 v[36:39], a[88:91], v[104:107], v[36:39]\n"
"v_fma_f32 v55, -v60, v31, v55\n"
"v_fma_f32 v48, -v60, v24, v48\n"
"v_mfma_f32_16x16x32_bf16 v[36:39], a[76:79], v[100:103], v[36:39]\n"
"buffer_load_dwordx4 v2, s[32:35], s63 offen offset:1024 sc1 lds\n"
"v_fma_f32 v49, -v60, v25, v49\n"
"v_mfma_f32_16x16x32_bf16 v[36:39], a[64:67], v[96:99], v[36:39]\n"
"ds_read_b128 v[80:83], v4 offset:16128\n"
"v_fma_f32 v50, -v60, v26, v50\n"
"v_mfma_f32_16x16x32_bf16 v[36:39], a[52:55], v[92:95], v[36:39]\n"
"v_permlane32_swap_b32 v52, v54\n"
"v_mfma_f32_16x16x32_bf16 v[32:35], a[48:51], v[92:95], v[32:35]\n"
"v_permlane32_swap_b32 v53, v55\n"
"v_mfma_f32_16x16x32_bf16 v[32:35], a[60:63], v[96:99], v[32:35]\n"
"ds_read_b128 v[84:87], v4 offset:17280\n"
"v_fma_f32 v51, -v60, v27, v51\n"
"v_mfma_f32_16x16x32_bf16 v[32:35], a[72:75], v[100:103], v[32:35]\n"
"v_fma_f32 v44, -v60, v20, v44\n"
"v_fma_f32 v45, -v60, v21, v45\n"
"v_mfma_f32_16x16x32_bf16 v[32:35], a[84:87], v[104:107], v[32:35]\n"
"v_fma_f32 v46, -v60, v22, v46\n"
"s_waitcnt lgkmcnt(4)\n"
"v_mfma_f32_16x16x32_bf16 v[32:35], a[96:99], v[108:111], v[32:35]\n"
"ds_read_b128 v[88:91], v4 offset:18432\n"
"v_add_f32 v56, v56, v57\n"
"v_mfma_f32_16x16x32_bf16 v[32:35], a[108:111], v[112:115], v[32:35]\n"
"buffer_load_dwordx4 v2, s[32:35], s63 offen offset:2048 sc1 lds\n"
"v_add_f32 v58, v58, v59\n"
"v_mfma_f32_16x16x32_bf16 v[32:35], a[120:123], v[116:119], v[32:35]\n"
"v_add_f32 v56, v56, v58\n"
"v_fma_f32 v47, -v60, v23, v47\n"
"v_mfma_f32_16x16x32_bf16 v[32:35], a[132:135], v[120:123], v[32:35]\n"
"ds_read_b128 v[92:95], v4 offset:19584\n"
"v_add_f32_dpp v56, v56, v56 row_ror:8 row_mask:0xf bank_mask:0xf\n"
"v_mfma_f32_16x16x32_bf16 v[36:39], a[136:139], v[120:123], v[36:39]\n"
"v_mul_f32 v64, v44, v61\n"
"v_add_f32_dpp v56, v56, v56 row_ror:4 row_mask:0xf bank_mask:0xf\n"
"v_mfma_f32_16x16x32_bf16 v[36:39], a[124:127], v[116:119], v[36:39]\n"
"v_exp_f32 v64, v64\n"
"v_mfma_f32_16x16x32_bf16 v[36:39], a[112:115], v[112:115], v[36:39]\n"
"ds_read_b128 v[96:99], v4 offset:20736\n"
"v_add_f32_dpp v56, v56, v56 row_ror:2 row_mask:0xf bank_mask:0xf\n"
"v_mfma_f32_16x16x32_bf16 v[36:39], a[100:103], v[108:111], v[36:39]\n"
"v_mul_f32 v56, 0x3a800000, v56\n"
"v_mul_f32 v65, v45, v61\n"
"v_mfma_f32_16x16x32_bf16 v[40:43], a[104:107], v[108:111], v[40:43]\n"
"v_exp_f32 v65, v65\n"
"v_mfma_f32_16x16x32_bf16 v[40:43], a[116:119], v[112:115], v[40:43]\n"
"buffer_load_dwordx4 v2, s[32:35], s63 offen offset:3072 sc1 lds\n"
"ds_read_b128 v[100:103], v4 offset:21888\n"
"v_mfma_f32_16x16x32_bf16 v[40:43], a[128:131], v[116:119], v[40:43]\n"
"v_mul_f32 v57, v56, v56\n"
"v_mul_f32 v66, v46, v61\n"
"v_mfma_f32_16x16x32_bf16 v[40:43], a[140:143], v[120:123], v[40:43]\n"
"v_sub_f32_dpp v57, v56, v57 quad_perm:[1,1,3,3] row_mask:0xf bank_mask:0xf\n"
"s_waitcnt lgkmcnt(4)\n"
"v_mfma_f32_16x16x32_bf16 v[40:43], a[152:155], v[124:127], v[40:43]\n"
"ds_read_b128 v[104:107], v4 offset:23040\n"
"v_max_f32 v57, 0, v57\n"
"v_mfma_f32_16x16x32_bf16 v[40:43], a[164:167], v[76:79], v[40:43]\n"
"v_exp_f32 v66, v66\n"
"v_mfma_f32_16x16x32_bf16 v[40:43], a[176:179], v[80:83], v[40:43]\n"
"v_cvt_pk_bf16_f32 v57, 0, v57\n"
"v_mul_f32 v67, v47, v61\n"
"v_mfma_f32_16x16x32_bf16 v[40:43], a[188:191], v[84:87], v[40:43]\n"
"ds_read_b128 v[108:111], v4 offset:24192\n"
"s_add_u32 m0, s64, 4096\n"
"v_mfma_f32_16x16x32_bf16 v[36:39], a[184:187], v[84:87], v[36:39]\n"
"v_exp_f32 v67, v67\n"
"v_mfma_f32_16x16x32_bf16 v[36:39], a[172:175], v[80:83], v[36:39]\n"
"buffer_load_dwordx4 v6, s[32:35], s63 offen offset:0 sc1 lds\n"
"v_add_f32 v57, 0x3727c5ac, v57\n"
"v_mfma_f32_16x16x32_bf16 v[36:39], a[160:163], v[76:79], v[36:39]\n"
"ds_read_b128 v[112:115], v4 offset:25344\n"
"v_cvt_pk_bf16_f32 v57, 0, v57\n"
"v_mfma_f32_16x16x32_bf16 v[36:39], a[148:151], v[124:127], v[36:39]\n"
"v_sqrt_f32 v57, v57\n"
"v_mfma_f32_16x16x32_bf16 v[32:35], a[144:147], v[124:127], v[32:35]\n"
"v_cvt_pk_bf16_f32 v57, v56, v57\n"
"v_mul_f32 v68, v52, v61\n"
"v_mfma_f32_16x16x32_bf16 v[32:35], a[156:159], v[76:79], v[32:35]\n"
"ds_read_b128 v[116:119], v4 offset:26496\n"
"ds_write_b32 v10, v57 offset:0\n"
"v_mfma_f32_16x16x32_bf16 v[32:35], a[168:171], v[80:83], v[32:35]\n"
"v_exp_f32 v68, v68\n"
"v_mfma_f32_16x16x32_bf16 v[32:35], a[180:183], v[84:87], v[32:35]\n"
"v_mul_f32 v69, v53, v61\n"
"s_waitcnt lgkmcnt(5)\n"
"v_mfma_f32_16x16x32_bf16 v[32:35], a[192:195], v[88:91], v[32:35]\n"
"ds_read_b128 v[120:123], v4 offset:27648\n"
"v_mul_f32 v44, v44, v48\n"
"v_mfma_f32_16x16x32_bf16 v[32:35], a[204:207], v[92:95], v[32:35]\n"
"buffer_load_dwordx4 v6, s[32:35], s63 offen offset:1024 sc1 lds\n"
"v_mul_f32 v45, v45, v49\n"
"v_mfma_f32_16x16x32_bf16 v[32:35], a[216:219], v[96:99], v[32:35]\n"
"v_exp_f32 v69, v69\n"
"v_mfma_f32_16x16x32_bf16 v[32:35], a[228:231], v[100:103], v[32:35]\n"
"ds_read_b128 v[124:127], v4 offset:28800\n"
"v_mul_f32 v46, v46, v50\n"
"v_mfma_f32_16x16x32_bf16 v[36:39], a[232:235], v[100:103], v[36:39]\n"
"v_mul_f32 v47, v47, v51\n"
"v_mul_f32 v52, v52, v54\n"
"v_mfma_f32_16x16x32_bf16 v[36:39], a[220:223], v[96:99], v[36:39]\n"
"buffer_load_dwordx2 v[12:13], v14, s[32:35], s66 offen sc1\n"
"v_mul_f32 v53, v53, v55\n"
"v_mfma_f32_16x16x32_bf16 v[36:39], a[208:211], v[92:95], v[36:39]\n"
"ds_read_b128 v[76:79], v4 offset:29952\n"
"v_fma_f32 v64, v64, v62, v62\n"
"v_mfma_f32_16x16x32_bf16 v[36:39], a[196:199], v[88:91], v[36:39]\n"
"v_rcp_f32 v64, v64\n"
"v_mfma_f32_16x16x32_bf16 v[40:43], a[200:203], v[88:91], v[40:43]\n"
"v_fma_f32 v65, v65, v62, v62\n"
"v_fma_f32 v66, v66, v62, v62\n"
"v_mfma_f32_16x16x32_bf16 v[40:43], a[212:215], v[92:95], v[40:43]\n"
"buffer_load_dwordx4 v6, s[32:35], s63 offen offset:2048 sc1 lds\n"
"ds_read_b128 v[80:83], v4 offset:31104\n"
"v_mfma_f32_16x16x32_bf16 v[40:43], a[224:227], v[96:99], v[40:43]\n"
"v_rcp_f32 v65, v65\n"
"v_mfma_f32_16x16x32_bf16 v[40:43], a[236:239], v[100:103], v[40:43]\n"
"v_fma_f32 v67, v67, v62, v62\n"
"s_waitcnt lgkmcnt(5)\n"
"v_mfma_f32_16x16x32_bf16 v[40:43], a[248:251], v[104:107], v[40:43]\n"
"ds_read_b128 v[84:87], v4 offset:32256\n"
"v_fma_f32 v68, v68, v62, v62\n"
"v_mfma_f32_16x16x32_bf16 v[40:43], v[132:135], v[108:111], v[40:43]\n"
"v_rcp_f32 v66, v66\n"
"v_mfma_f32_16x16x32_bf16 v[40:43], v[144:147], v[112:115], v[40:43]\n"
"v_rcp_f32 v67, v67\n"
"v_mfma_f32_16x16x32_bf16 v[40:43], v[156:159], v[116:119], v[40:43]\n"
"ds_read_b128 v[88:91], v4 offset:33408\n"
"v_fma_f32 v69, v69, v62, v62\n"
"v_mfma_f32_16x16x32_bf16 v[36:39], v[152:155], v[116:119], v[36:39]\n"
"v_rcp_f32 v68, v68\n"
"v_mfma_f32_16x16x32_bf16 v[36:39], v[140:143], v[112:115], v[36:39]\n"
"buffer_load_dwordx4 v6, s[32:35], s63 offen offset:3072 sc1 lds\n"
"v_mul_f32 v44, v44, v64\n"
"v_mfma_f32_16x16x32_bf16 v[36:39], v[128:131], v[108:111], v[36:39]\n"
"ds_read_b128 v[92:95], v4 offset:34560\n"
"v_mul_f32 v45, v45, v65\n"
"v_mfma_f32_16x16x32_bf16 v[36:39], a[244:247], v[104:107], v[36:39]\n"
"v_rcp_f32 v69, v69\n"
"v_mfma_f32_16x16x32_bf16 v[32:35], a[240:243], v[104:107], v[32:35]\n"
"s_waitcnt vmcnt(9)\n"
"s_waitcnt lgkmcnt(7)\n"
"s_barrier\n"
"v_mfma_f32_16x16x32_bf16 v[32:35], a[252:255], v[108:111], v[32:35]\n"
"ds_read_b128 v[96:99], v4 offset:35712\n"
"v_mul_f32 v46, v46, v66\n"
"v_mfma_f32_16x16x32_bf16 v[32:35], v[136:139], v[112:115], v[32:35]\n"
"v_xor_b32 v4, 73728, v4\n"
"v_mul_f32 v47, v47, v67\n"
"v_mfma_f32_16x16x32_bf16 v[32:35], v[148:151], v[116:119], v[32:35]\n"
"s_add_u32 m0, s64, 8192\n"
"s_waitcnt lgkmcnt(4)\n"
"v_mfma_f32_16x16x32_bf16 v[32:35], v[160:163], v[120:123], v[32:35]\n"
"ds_read_b128 v[100:103], v5 offset:0\n"
"v_mul_f32 v52, v52, v68\n"
"v_mfma_f32_16x16x32_bf16 v[32:35], v[172:175], v[124:127], v[32:35]\n"
"buffer_store_dwordx2 v[16:17], v2, s[92:95], s68 idxen sc1\n"
"v_mul_f32 v53, v53, v69\n"
"v_mfma_f32_16x16x32_bf16 v[32:35], v[184:187], v[76:79], v[32:35]\n"
"v_cvt_pk_bf16_f32 v74, v52, v53\n"
"v_cvt_pk_bf16_f32 v72, v44, v45\n"
"v_mfma_f32_16x16x32_bf16 v[32:35], v[196:199], v[80:83], v[32:35]\n"
"ds_read_b128 v[104:107], v5 offset:1152\n"
"v_cvt_pk_bf16_f32 v73, v46, v47\n"
"v_mfma_f32_16x16x32_bf16 v[36:39], v[200:203], v[80:83], v[36:39]\n"
"v_permlane32_swap_b32 v74, v75\n"
"v_mfma_f32_16x16x32_bf16 v[36:39], v[188:191], v[76:79], v[36:39]\n"
"buffer_load_dwordx4 v7, s[32:35], s63 offen offset:0 sc1 lds\n"
"s_add_u32 s63, s63, 890880\n"
"v_mfma_f32_16x16x32_bf16 v[36:39], v[176:179], v[124:127], v[36:39]\n"
"ds_read_b128 v[108:111], v5 offset:2304\n"
"v_mfma_f32_16x16x32_bf16 v[36:39], v[164:167], v[120:123], v[36:39]\n"
"v_permlane16_swap_b32 v72, v74\n"
"v_mfma_f32_16x16x32_bf16 v[40:43], v[168:171], v[120:123], v[40:43]\n"
"v_permlane16_swap_b32 v73, v75\n"
"v_mfma_f32_16x16x32_bf16 v[40:43], v[180:183], v[124:127], v[40:43]\n"
"buffer_store_dwordx4 v[72:75], v15, s[32:35], s68 offen sc1\n"
"ds_read_b128 v[112:115], v5 offset:3456\n"
"v_mfma_f32_16x16x32_bf16 v[40:43], v[192:195], v[76:79], v[40:43]\n"
"v_mfma_f32_16x16x32_bf16 v[40:43], v[204:207], v[80:83], v[40:43]\n"
"s_waitcnt lgkmcnt(4)\n"
"v_mfma_f32_16x16x32_bf16 v[40:43], v[216:219], v[84:87], v[40:43]\n"
"ds_read_b128 v[116:119], v5 offset:4608\n"
"v_mfma_f32_16x16x32_bf16 v[40:43], v[228:231], v[88:91], v[40:43]\n"
"v_mfma_f32_16x16x32_bf16 v[40:43], v[240:243], v[92:95], v[40:43]\n"
"v_mfma_f32_16x16x32_bf16 v[40:43], v[252:255], v[96:99], v[40:43]\n"
"ds_read_b128 v[120:123], v5 offset:5760\n"
"v_mfma_f32_16x16x32_bf16 v[36:39], v[248:251], v[96:99], v[36:39]\n"
"v_mfma_f32_16x16x32_bf16 v[36:39], v[236:239], v[92:95], v[36:39]\n"
"v_mfma_f32_16x16x32_bf16 v[36:39], v[224:227], v[88:91], v[36:39]\n"
"ds_read_b128 v[124:127], v5 offset:6912\n"
"v_mfma_f32_16x16x32_bf16 v[36:39], v[212:215], v[84:87], v[36:39]\n"
"v_mfma_f32_16x16x32_bf16 v[32:35], v[208:211], v[84:87], v[32:35]\n"
"v_mfma_f32_16x16x32_bf16 v[32:35], v[220:223], v[88:91], v[32:35]\n"
"ds_read_b128 v[76:79], v5 offset:8064\n"
"v_mfma_f32_16x16x32_bf16 v[32:35], v[232:235], v[92:95], v[32:35]\n"
"s_sub_u32 s62, s62, 1\n"
"v_mfma_f32_16x16x32_bf16 v[32:35], v[244:247], v[96:99], v[32:35]\n"
"s_cbranch_scc1 .L_gt0_3\n"
"s_waitcnt lgkmcnt(4)\n"
"v_mfma_f32_16x16x32_bf16 v[44:47], a[0:3], v[100:103], 0\n"
"ds_read_b128 v[80:83], v5 offset:9216\n"
"ds_read_b32 v62, v11 offset:0\n"
"v_mfma_f32_16x16x32_bf16 v[44:47], a[12:15], v[104:107], v[44:47]\n"
"s_add_u32 s68, s68, 890880\n"
"v_mfma_f32_16x16x32_bf16 v[44:47], a[24:27], v[108:111], v[44:47]\n"
"v_mfma_f32_16x16x32_bf16 v[44:47], a[36:39], v[112:115], v[44:47]\n"
"ds_read_b128 v[84:87], v5 offset:10368\n"
"v_mfma_f32_16x16x32_bf16 v[48:51], a[40:43], v[112:115], 0\n"
"ds_read2st64_b32 v[56:57], v8 offset0:144 offset1:180\n"
"v_mfma_f32_16x16x32_bf16 v[48:51], a[28:31], v[108:111], v[48:51]\n"
"s_waitcnt vmcnt(5)\n"
"v_cmp_ne_u64 vcc, v[16:17], v[12:13]\n"
"v_mfma_f32_16x16x32_bf16 v[48:51], a[16:19], v[104:107], v[48:51]\n"
"ds_read_b128 v[88:91], v5 offset:11520\n"
"ds_read2st64_b32 v[58:59], v8 offset0:216 offset1:252\n"
"v_mfma_f32_16x16x32_bf16 v[48:51], a[4:7], v[100:103], v[48:51]\n"
"s_cbranch_vccnz .L_rtg1_3\n"
".L_bkg1_3:\n"
"s_add_u32 s66, s66, 890880\n"
"v_mfma_f32_16x16x32_bf16 v[52:55], a[8:11], v[100:103], 0\n"
"s_add_u32 m0, s64, 36864\n"
"v_xor_b32 v8, v9, v8\n"
"v_mfma_f32_16x16x32_bf16 v[52:55], a[20:23], v[104:107], v[52:55]\n"
"buffer_load_dwordx4 v2, s[32:35], s63 offen offset:0 sc1 lds\n"
"ds_read_b128 v[92:95], v5 offset:12672\n"
"v_mfma_f32_16x16x32_bf16 v[52:55], a[32:35], v[108:111], v[52:55]\n"
"s_waitcnt lgkmcnt(5)\n"
"v_lshlrev_b32 v60, 16, v62\n"
"v_mfma_f32_16x16x32_bf16 v[52:55], a[44:47], v[112:115], v[52:55]\n"
"v_and_b32 v62, 0xffff0000, v62\n"
"v_mfma_f32_16x16x32_bf16 v[52:55], a[56:59], v[116:119], v[52:55]\n"
"ds_read_b128 v[96:99], v5 offset:13824\n"
"v_fma_f32 v40, -v60, v28, v40\n"
"v_mfma_f32_16x16x32_bf16 v[52:55], a[68:71], v[120:123], v[52:55]\n"
"v_rcp_f32 v61, v62\n"
"v_mfma_f32_16x16x32_bf16 v[52:55], a[80:83], v[124:127], v[52:55]\n"
"v_mul_f32 v62, v62, v62\n"
"v_mul_f32 v61, 0xbfb8aa3b, v61\n"
"v_mfma_f32_16x16x32_bf16 v[52:55], a[92:95], v[76:79], v[52:55]\n"
"ds_read_b128 v[100:103], v5 offset:14976\n"
"v_fma_f32 v41, -v60, v29, v41\n"
"v_mfma_f32_16x16x32_bf16 v[48:51], a[88:91], v[76:79], v[48:51]\n"
"v_fma_f32 v42, -v60, v30, v42\n"
"v_fma_f32 v43, -v60, v31, v43\n"
"v_mfma_f32_16x16x32_bf16 v[48:51], a[76:79], v[124:127], v[48:51]\n"
"buffer_load_dwordx4 v2, s[32:35], s63 offen offset:1024 sc1 lds\n"
"v_fma_f32 v36, -v60, v24, v36\n"
"v_mfma_f32_16x16x32_bf16 v[48:51], a[64:67], v[120:123], v[48:51]\n"
"ds_read_b128 v[104:107], v5 offset:16128\n"
"v_fma_f32 v37, -v60, v25, v37\n"
"v_mfma_f32_16x16x32_bf16 v[48:51], a[52:55], v[116:119], v[48:51]\n"
"v_permlane32_swap_b32 v40, v42\n"
"v_mfma_f32_16x16x32_bf16 v[44:47], a[48:51], v[116:119], v[44:47]\n"
"v_permlane32_swap_b32 v41, v43\n"
"v_mfma_f32_16x16x32_bf16 v[44:47], a[60:63], v[120:123], v[44:47]\n"
"ds_read_b128 v[108:111], v5 offset:17280\n"
"v_fma_f32 v38, -v60, v26, v38\n"
"v_mfma_f32_16x16x32_bf16 v[44:47], a[72:75], v[124:127], v[44:47]\n"
"v_fma_f32 v39, -v60, v27, v39\n"
"v_fma_f32 v32, -v60, v20, v32\n"
"v_mfma_f32_16x16x32_bf16 v[44:47], a[84:87], v[76:79], v[44:47]\n"
"v_fma_f32 v33, -v60, v21, v33\n"
"s_waitcnt lgkmcnt(4)\n"
"v_mfma_f32_16x16x32_bf16 v[44:47], a[96:99], v[80:83], v[44:47]\n"
"ds_read_b128 v[112:115], v5 offset:18432\n"
"v_add_f32 v56, v56, v57\n"
"v_mfma_f32_16x16x32_bf16 v[44:47], a[108:111], v[84:87], v[44:47]\n"
"buffer_load_dwordx4 v2, s[32:35], s63 offen offset:2048 sc1 lds\n"
"v_add_f32 v58, v58, v59\n"
"v_mfma_f32_16x16x32_bf16 v[44:47], a[120:123], v[88:91], v[44:47]\n"
"v_add_f32 v56, v56, v58\n"
"v_fma_f32 v34, -v60, v22, v34\n"
"v_mfma_f32_16x16x32_bf16 v[44:47], a[132:135], v[92:95], v[44:47]\n"
"ds_read_b128 v[116:119], v5 offset:19584\n"
"v_add_f32_dpp v56, v56, v56 row_ror:8 row_mask:0xf bank_mask:0xf\n"
"v_mfma_f32_16x16x32_bf16 v[48:51], a[136:139], v[92:95], v[48:51]\n"
"v_fma_f32 v35, -v60, v23, v35\n"
"v_add_f32_dpp v56, v56, v56 row_ror:4 row_mask:0xf bank_mask:0xf\n"
"v_mfma_f32_16x16x32_bf16 v[48:51], a[124:127], v[88:91], v[48:51]\n"
"v_mul_f32 v64, v32, v61\n"
"v_add_f32_dpp v56, v56, v56 row_ror:2 row_mask:0xf bank_mask:0xf\n"
"v_mfma_f32_16x16x32_bf16 v[48:51], a[112:115], v[84:87], v[48:51]\n"
"ds_read_b128 v[120:123], v5 offset:20736\n"
"v_mul_f32 v56, 0x3a800000, v56\n"
"v_mfma_f32_16x16x32_bf16 v[48:51], a[100:103], v[80:83], v[48:51]\n"
"v_exp_f32 v64, v64\n"
"v_mfma_f32_16x16x32_bf16 v[52:55], a[104:107], v[80:83], v[52:55]\n"
"v_mul_f32 v57, v56, v56\n"
"v_mul_f32 v65, v33, v61\n"
"v_mfma_f32_16x16x32_bf16 v[52:55], a[116:119], v[84:87], v[52:55]\n"
"buffer_load_dwordx4 v2, s[32:35], s63 offen offset:3072 sc1 lds\n"
"ds_read_b128 v[124:127], v5 offset:21888\n"
"v_mfma_f32_16x16x32_bf16 v[52:55], a[128:131], v[88:91], v[52:55]\n"
"v_exp_f32 v65, v65\n"
"v_mfma_f32_16x16x32_bf16 v[52:55], a[140:143], v[92:95], v[52:55]\n"
"v_sub_f32_dpp v57, v56, v57 quad_perm:[1,1,3,3] row_mask:0xf bank_mask:0xf\n"
"s_waitcnt lgkmcnt(4)\n"
"v_mfma_f32_16x16x32_bf16 v[52:55], a[152:155], v[96:99], v[52:55]\n"
"ds_read_b128 v[76:79], v5 offset:23040\n"
"v_max_f32 v57, 0, v57\n"
"v_mfma_f32_16x16x32_bf16 v[52:55], a[164:167], v[100:103], v[52:55]\n"
"v_cvt_pk_bf16_f32 v57, 0, v57\n"
"v_mul_f32 v66, v34, v61\n"
"v_mfma_f32_16x16x32_bf16 v[52:55], a[176:179], v[104:107], v[52:55]\n"
"v_exp_f32 v66, v66\n"
"v_mfma_f32_16x16x32_bf16 v[52:55], a[188:191], v[108:111], v[52:55]\n"
"ds_read_b128 v[80:83], v5 offset:24192\n"
"s_add_u32 m0, s64, 40960\n"
"v_mfma_f32_16x16x32_bf16 v[48:51], a[184:187], v[108:111], v[48:51]\n"
"v_add_f32 v57, 0x3727c5ac, v57\n"
"v_mul_f32 v67, v35, v61\n"
"v_mfma_f32_16x16x32_bf16 v[48:51], a[172:175], v[104:107], v[48:51]\n"
"buffer_load_dwordx4 v6, s[32:35], s63 offen offset:0 sc1 lds\n"
"v_cvt_pk_bf16_f32 v57, 0, v57\n"
"v_mfma_f32_16x16x32_bf16 v[48:51], a[160:163], v[100:103], v[48:51]\n"
"ds_read_b128 v[84:87], v5 offset:25344\n"
"v_mul_f32 v68, v40, v61\n"
"v_mfma_f32_16x16x32_bf16 v[48:51], a[148:151], v[96:99], v[48:51]\n"
"v_sqrt_f32 v57, v57\n"
"v_mfma_f32_16x16x32_bf16 v[44:47], a[144:147], v[96:99], v[44:47]\n"
"v_exp_f32 v67, v67\n"
"v_mfma_f32_16x16x32_bf16 v[44:47], a[156:159], v[100:103], v[44:47]\n"
"ds_read_b128 v[88:91], v5 offset:26496\n"
"v_cvt_pk_bf16_f32 v57, v56, v57\n"
"v_mfma_f32_16x16x32_bf16 v[44:47], a[168:171], v[104:107], v[44:47]\n"
"ds_write_b32 v10, v57 offset:128\n"
"v_mul_f32 v69, v41, v61\n"
"v_mfma_f32_16x16x32_bf16 v[44:47], a[180:183], v[108:111], v[44:47]\n"
"v_mul_f32 v32, v32, v36\n"
"s_waitcnt lgkmcnt(5)\n"
"v_mfma_f32_16x16x32_bf16 v[44:47], a[192:195], v[112:115], v[44:47]\n"
"ds_read_b128 v[92:95], v5 offset:27648\n"
"v_mul_f32 v33, v33, v37\n"
"v_mfma_f32_16x16x32_bf16 v[44:47], a[204:207], v[116:119], v[44:47]\n"
"buffer_load_dwordx4 v6, s[32:35], s63 offen offset:1024 sc1 lds\n"
"v_mul_f32 v34, v34, v38\n"
"v_mfma_f32_16x16x32_bf16 v[44:47], a[216:219], v[120:123], v[44:47]\n"
"v_exp_f32 v68, v68\n"
"v_mfma_f32_16x16x32_bf16 v[44:47], a[228:231], v[124:127], v[44:47]\n"
"ds_read_b128 v[96:99], v5 offset:28800\n"
"v_mul_f32 v35, v35, v39\n"
"v_mfma_f32_16x16x32_bf16 v[48:51], a[232:235], v[124:127], v[48:51]\n"
"v_exp_f32 v69, v69\n"
"v_mfma_f32_16x16x32_bf16 v[48:51], a[220:223], v[120:123], v[48:51]\n"
"buffer_load_dwordx2 v[12:13], v14, s[32:35], s66 offen sc1\n"
"v_mul_f32 v40, v40, v42\n"
"v_mfma_f32_16x16x32_bf16 v[48:51], a[208:211], v[116:119], v[48:51]\n"
"ds_read_b128 v[100:103], v5 offset:29952\n"
"v_mul_f32 v41, v41, v43\n"
"v_mfma_f32_16x16x32_bf16 v[48:51], a[196:199], v[112:115], v[48:51]\n"
"v_fma_f32 v64, v64, v62, v62\n"
"v_fma_f32 v65, v65, v62, v62\n"
"v_mfma_f32_16x16x32_bf16 v[52:55], a[200:203], v[112:115], v[52:55]\n"
"v_rcp_f32 v64, v64\n"
"v_mfma_f32_16x16x32_bf16 v[52:55], a[212:215], v[116:119], v[52:55]\n"
"buffer_load_dwordx4 v6, s[32:35], s63 offen offset:2048 sc1 lds\n"
"ds_read_b128 v[104:107], v5 offset:31104\n"
"v_mfma_f32_16x16x32_bf16 v[52:55], a[224:227], v[120:123], v[52:55]\n"
"v_rcp_f32 v65, v65\n"
"v_mfma_f32_16x16x32_bf16 v[52:55], a[236:239], v[124:127], v[52:55]\n"
"v_fma_f32 v66, v66, v62, v62\n"
"s_waitcnt lgkmcnt(5)\n"
"v_mfma_f32_16x16x32_bf16 v[52:55], a[248:251], v[76:79], v[52:55]\n"
"ds_read_b128 v[108:111], v5 offset:32256\n"
"v_fma_f32 v67, v67, v62, v62\n"
"v_mfma_f32_16x16x32_bf16 v[52:55], v[132:135], v[80:83], v[52:55]\n"
"v_rcp_f32 v66, v66\n"
"v_mfma_f32_16x16x32_bf16 v[52:55], v[144:147], v[84:87], v[52:55]\n"
"v_rcp_f32 v67, v67\n"
"v_mfma_f32_16x16x32_bf16 v[52:55], v[156:159], v[88:91], v[52:55]\n"
"ds_read_b128 v[112:115], v5 offset:33408\n"
"v_fma_f32 v68, v68, v62, v62\n"
"v_mfma_f32_16x16x32_bf16 v[48:51], v[152:155], v[88:91], v[48:51]\n"
"v_rcp_f32 v68, v68\n"
"v_mfma_f32_16x16x32_bf16 v[48:51], v[140:143], v[84:87], v[48:51]\n"
"buffer_load_dwordx4 v6, s[32:35], s63 offen offset:3072 sc1 lds\n"
"v_fma_f32 v69, v69, v62, v62\n"
"v_mfma_f32_16x16x32_bf16 v[48:51], v[128:131], v[80:83], v[48:51]\n"
"ds_read_b128 v[116:119], v5 offset:34560\n"
"v_mul_f32 v32, v32, v64\n"
"v_mfma_f32_16x16x32_bf16 v[48:51], a[244:247], v[76:79], v[48:51]\n"
"v_rcp_f32 v69, v69\n"
"v_mfma_f32_16x16x32_bf16 v[44:47], a[240:243], v[76:79], v[44:47]\n"
"s_waitcnt vmcnt(9)\n"
"s_waitcnt lgkmcnt(7)\n"
"s_barrier\n"
"v_mfma_f32_16x16x32_bf16 v[44:47], a[252:255], v[80:83], v[44:47]\n"
"ds_read_b128 v[120:123], v5 offset:35712\n"
"v_mul_f32 v33, v33, v65\n"
"v_mfma_f32_16x16x32_bf16 v[44:47], v[136:139], v[84:87], v[44:47]\n"
"v_xor_b32 v5, 73728, v5\n"
"v_mul_f32 v34, v34, v66\n"
"v_mfma_f32_16x16x32_bf16 v[44:47], v[148:151], v[88:91], v[44:47]\n"
"s_add_u32 m0, s64, 45056\n"
"s_waitcnt lgkmcnt(4)\n"
"v_mfma_f32_16x16x32_bf16 v[44:47], v[160:163], v[92:95], v[44:47]\n"
"ds_read_b128 v[76:79], v4 offset:0\n"
"v_mul_f32 v35, v35, v67\n"
"v_mfma_f32_16x16x32_bf16 v[44:47], v[172:175], v[96:99], v[44:47]\n"
"buffer_store_dwordx2 v[16:17], v2, s[92:95], s68 idxen sc1\n"
"v_mul_f32 v40, v40, v68\n"
"v_mfma_f32_16x16x32_bf16 v[44:47], v[184:187], v[100:103], v[44:47]\n"
"v_mul_f32 v41, v41, v69\n"
"v_cvt_pk_bf16_f32 v72, v32, v33\n"
"v_mfma_f32_16x16x32_bf16 v[44:47], v[196:199], v[104:107], v[44:47]\n"
"ds_read_b128 v[80:83], v4 offset:1152\n"
"v_cvt_pk_bf16_f32 v74, v40, v41\n"
"v_mfma_f32_16x16x32_bf16 v[48:51], v[200:203], v[104:107], v[48:51]\n"
"v_cvt_pk_bf16_f32 v73, v34, v35\n"
"v_mfma_f32_16x16x32_bf16 v[48:51], v[188:191], v[100:103], v[48:51]\n"
"buffer_load_dwordx4 v7, s[32:35], s63 offen offset:0 sc1 lds\n"
"s_add_u32 s63, s63, 890880\n"
"v_mfma_f32_16x16x32_bf16 v[48:51], v[176:179], v[96:99], v[48:51]\n"
"ds_read_b128 v[84:87], v4 offset:2304\n"
"s_xor_b32 s64, s64, s65\n"
"v_mfma_f32_16x16x32_bf16 v[48:51], v[164:167], v[92:95], v[48:51]\n"
"v_permlane32_swap_b32 v74, v75\n"
"v_mfma_f32_16x16x32_bf16 v[52:55], v[168:171], v[92:95], v[52:55]\n"
"v_mfma_f32_16x16x32_bf16 v[52:55], v[180:183], v[96:99], v[52:55]\n"
"ds_read_b128 v[88:91], v4 offset:3456\n"
"v_mfma_f32_16x16x32_bf16 v[52:55], v[192:195], v[100:103], v[52:55]\n"
"v_permlane16_swap_b32 v72, v74\n"
"v_mfma_f32_16x16x32_bf16 v[52:55], v[204:207], v[104:107], v[52:55]\n"
"s_waitcnt lgkmcnt(4)\n"
"v_mfma_f32_16x16x32_bf16 v[52:55], v[216:219], v[108:111], v[52:55]\n"
"ds_read_b128 v[92:95], v4 offset:4608\n"
"v_mfma_f32_16x16x32_bf16 v[52:55], v[228:231], v[112:115], v[52:55]\n"
"v_permlane16_swap_b32 v73, v75\n"
"v_mfma_f32_16x16x32_bf16 v[52:55], v[240:243], v[116:119], v[52:55]\n"
"v_mfma_f32_16x16x32_bf16 v[52:55], v[252:255], v[120:123], v[52:55]\n"
"ds_read_b128 v[96:99], v4 offset:5760\n"
"v_mfma_f32_16x16x32_bf16 v[48:51], v[248:251], v[120:123], v[48:51]\n"
"v_mfma_f32_16x16x32_bf16 v[48:51], v[236:239], v[116:119], v[48:51]\n"
"buffer_store_dwordx4 v[72:75], v15, s[32:35], s68 offen sc1\n"
"v_mfma_f32_16x16x32_bf16 v[48:51], v[224:227], v[112:115], v[48:51]\n"
"ds_read_b128 v[100:103], v4 offset:6912\n"
"v_mfma_f32_16x16x32_bf16 v[48:51], v[212:215], v[108:111], v[48:51]\n"
"v_mfma_f32_16x16x32_bf16 v[44:47], v[208:211], v[108:111], v[44:47]\n"
"v_mfma_f32_16x16x32_bf16 v[44:47], v[220:223], v[112:115], v[44:47]\n"
"ds_read_b128 v[104:107], v4 offset:8064\n"
"v_mfma_f32_16x16x32_bf16 v[44:47], v[232:235], v[116:119], v[44:47]\n"
"s_sub_u32 s62, s62, 1\n"
"v_mfma_f32_16x16x32_bf16 v[44:47], v[244:247], v[120:123], v[44:47]\n"
"s_cbranch_scc0 .L_gl_3\n"
"s_nop 1\n"
"ds_read_b32 v62, v11 offset:128\n"
"s_add_u32 s68, s68, 890880\n"
"s_waitcnt lgkmcnt(0)\n"
"v_lshlrev_b32 v60, 16, v62\n"
"v_and_b32 v62, 0xffff0000, v62\n"
"v_rcp_f32 v61, v62\n"
"v_mul_f32 v62, v62, v62\n"
"v_mul_f32 v61, 0xbfb8aa3b, v61\n"
"v_fma_f32 v52, -v60, v28, v52\n"
"v_fma_f32 v53, -v60, v29, v53\n"
"v_fma_f32 v54, -v60, v30, v54\n"
"v_fma_f32 v55, -v60, v31, v55\n"
"v_fma_f32 v48, -v60, v24, v48\n"
"v_fma_f32 v49, -v60, v25, v49\n"
"v_fma_f32 v50, -v60, v26, v50\n"
"v_fma_f32 v51, -v60, v27, v51\n"
"v_fma_f32 v44, -v60, v20, v44\n"
"v_fma_f32 v45, -v60, v21, v45\n"
"v_fma_f32 v46, -v60, v22, v46\n"
"v_fma_f32 v47, -v60, v23, v47\n"
"v_permlane32_swap_b32 v52, v54\n"
"v_permlane32_swap_b32 v53, v55\n"
"v_mul_f32 v64, v44, v61\n"
"v_mul_f32 v65, v45, v61\n"
"v_mul_f32 v66, v46, v61\n"
"v_mul_f32 v67, v47, v61\n"
"v_mul_f32 v68, v52, v61\n"
"v_mul_f32 v69, v53, v61\n"
"v_exp_f32 v64, v64\n"
"v_exp_f32 v65, v65\n"
"v_mul_f32 v44, v44, v48\n"
"v_mul_f32 v45, v45, v49\n"
"v_exp_f32 v66, v66\n"
"v_exp_f32 v67, v67\n"
"v_mul_f32 v46, v46, v50\n"
"v_mul_f32 v47, v47, v51\n"
"v_exp_f32 v68, v68\n"
"v_exp_f32 v69, v69\n"
"v_mul_f32 v52, v52, v54\n"
"v_mul_f32 v53, v53, v55\n"
"v_fma_f32 v64, v64, v62, v62\n"
"v_fma_f32 v65, v65, v62, v62\n"
"v_rcp_f32 v64, v64\n"
"v_rcp_f32 v65, v65\n"
"v_fma_f32 v66, v66, v62, v62\n"
"v_fma_f32 v67, v67, v62, v62\n"
"v_rcp_f32 v66, v66\n"
"v_rcp_f32 v67, v67\n"
"v_fma_f32 v68, v68, v62, v62\n"
"v_fma_f32 v69, v69, v62, v62\n"
"v_rcp_f32 v68, v68\n"
"v_rcp_f32 v69, v69\n"
"v_mul_f32 v44, v44, v64\n"
"v_mul_f32 v45, v45, v65\n"
"v_mul_f32 v46, v46, v66\n"
"v_mul_f32 v47, v47, v67\n"
"v_mul_f32 v52, v52, v68\n"
"v_mul_f32 v53, v53, v69\n"
"v_cvt_pk_bf16_f32 v74, v52, v53\n"
"v_cvt_pk_bf16_f32 v72, v44, v45\n"
"v_cvt_pk_bf16_f32 v73, v46, v47\n"
"v_permlane32_swap_b32 v74, v75\n"
"s_nop 1\n"
"v_permlane16_swap_b32 v72, v74\n"
"v_permlane16_swap_b32 v73, v75\n"
"buffer_store_dwordx4 v[72:75], v15, s[32:35], s68 offen sc1\n"
"s_waitcnt vmcnt(1)\n"
"s_barrier\n"
"buffer_store_dwordx2 v[16:17], v2, s[92:95], s68 idxen sc1\n"
"s_add_u32 s68, s68, 890880\n"
"s_waitcnt vmcnt(1)\n"
"s_barrier\n"
"buffer_store_dwordx2 v[16:17], v2, s[92:95], s68 idxen sc1\n"
"s_branch .L_end\n"
".L_gt0_3:\n"
"s_nop 1\n"
"ds_read_b32 v62, v11 offset:0\n"
"s_add_u32 s68, s68, 890880\n"
"s_waitcnt lgkmcnt(0)\n"
"v_lshlrev_b32 v60, 16, v62\n"
"v_and_b32 v62, 0xffff0000, v62\n"
"v_rcp_f32 v61, v62\n"
"v_mul_f32 v62, v62, v62\n"
"v_mul_f32 v61, 0xbfb8aa3b, v61\n"
"v_fma_f32 v40, -v60, v28, v40\n"
"v_fma_f32 v41, -v60, v29, v41\n"
"v_fma_f32 v42, -v60, v30, v42\n"
"v_fma_f32 v43, -v60, v31, v43\n"
"v_fma_f32 v36, -v60, v24, v36\n"
"v_fma_f32 v37, -v60, v25, v37\n"
"v_fma_f32 v38, -v60, v26, v38\n"
"v_fma_f32 v39, -v60, v27, v39\n"
"v_fma_f32 v32, -v60, v20, v32\n"
"v_fma_f32 v33, -v60, v21, v33\n"
"v_fma_f32 v34, -v60, v22, v34\n"
"v_fma_f32 v35, -v60, v23, v35\n"
"v_permlane32_swap_b32 v40, v42\n"
"v_permlane32_swap_b32 v41, v43\n"
"v_mul_f32 v64, v32, v61\n"
"v_mul_f32 v65, v33, v61\n"
"v_mul_f32 v66, v34, v61\n"
"v_mul_f32 v67, v35, v61\n"
"v_mul_f32 v68, v40, v61\n"
"v_mul_f32 v69, v41, v61\n"
"v_exp_f32 v64, v64\n"
"v_exp_f32 v65, v65\n"
"v_mul_f32 v32, v32, v36\n"
"v_mul_f32 v33, v33, v37\n"
"v_exp_f32 v66, v66\n"
"v_exp_f32 v67, v67\n"
"v_mul_f32 v34, v34, v38\n"
"v_mul_f32 v35, v35, v39\n"
"v_exp_f32 v68, v68\n"
"v_exp_f32 v69, v69\n"
"v_mul_f32 v40, v40, v42\n"
"v_mul_f32 v41, v41, v43\n"
"v_fma_f32 v64, v64, v62, v62\n"
"v_fma_f32 v65, v65, v62, v62\n"
"v_rcp_f32 v64, v64\n"
"v_rcp_f32 v65, v65\n"
"v_fma_f32 v66, v66, v62, v62\n"
"v_fma_f32 v67, v67, v62, v62\n"
"v_rcp_f32 v66, v66\n"
"v_rcp_f32 v67, v67\n"
"v_fma_f32 v68, v68, v62, v62\n"
"v_fma_f32 v69, v69, v62, v62\n"
"v_rcp_f32 v68, v68\n"
"v_rcp_f32 v69, v69\n"
"v_mul_f32 v32, v32, v64\n"
"v_mul_f32 v33, v33, v65\n"
"v_mul_f32 v34, v34, v66\n"
"v_mul_f32 v35, v35, v67\n"
"v_mul_f32 v40, v40, v68\n"
"v_mul_f32 v41, v41, v69\n"
"v_cvt_pk_bf16_f32 v74, v40, v41\n"
"v_cvt_pk_bf16_f32 v72, v32, v33\n"
"v_cvt_pk_bf16_f32 v73, v34, v35\n"
"v_permlane32_swap_b32 v74, v75\n"
"s_nop 1\n"
"v_permlane16_swap_b32 v72, v74\n"
"v_permlane16_swap_b32 v73, v75\n"
"buffer_store_dwordx4 v[72:75], v15, s[32:35], s68 offen sc1\n"
"s_waitcnt vmcnt(1)\n"
"s_barrier\n"
"buffer_store_dwordx2 v[16:17], v2, s[92:95], s68 idxen sc1\n"
"s_add_u32 s68, s68, 890880\n"
"s_waitcnt vmcnt(1)\n"
"s_barrier\n"
"buffer_store_dwordx2 v[16:17], v2, s[92:95], s68 idxen sc1\n"
"s_branch .L_end\n"
".L_q:\n"
"v_mov_b32 v16, 0x3f803f80\n"
"v_mov_b32 v17, 0x3f803f80\n"
"v_mov_b32 v18, 0x3f803f80\n"
"v_mov_b32 v19, 0x3f803f80\n"
"s_add_u32 s36, s10, 0x0\n"
"s_addc_u32 s37, s11, 0x0\n"
"s_and_b32 s37, s37, 0xffff\n"
"s_mov_b32 s38, 0x600000\n"
"s_mov_b32 s39, 0x20000\n"
"s_add_u32 s40, s10, 0x400\n"
"s_addc_u32 s41, s11, 0x0\n"
"s_and_b32 s41, s41, 0xffff\n"
"s_mov_b32 s42, 0x5ffc00\n"
"s_mov_b32 s43, 0x20000\n"
"s_add_u32 s44, s10, 0x800\n"
"s_addc_u32 s45, s11, 0x0\n"
"s_and_b32 s45, s45, 0xffff\n"
"s_mov_b32 s46, 0x5ff800\n"
"s_mov_b32 s47, 0x20000\n"
"s_lshl_b32 s96, s27, 7\n"
"v_and_b32 v68, 3, v1\n"
"v_mul_u32_u24 v68, 8, v68\n"
"v_and_b32 v71, 1, v1\n"
"v_bfe_u32 v3, v1, 1, 1\n"
"v_lshl_add_u32 v71, v71, 1, v3\n"
"v_mul_u32_u24 v71, 8, v71\n"
"s_lshl_b32 s48, s26, 5\n"
"v_add_u32 v68, s48, v68\n"
"v_add_u32 v71, s48, v71\n"
"v_lshrrev_b32 v64, 3, v1\n"
"v_bfe_u32 v65, v1, 4, 1\n"
"s_and_b32 s48, s26, 1\n"
"s_lshl_b32 s48, s48, 1\n"
"v_or_b32 v65, s48, v65\n"
"v_lshlrev_b32 v65, 1, v65\n"
"v_and_b32 v66, 7, v1\n"
"v_xor_b32 v65, v65, v66\n"
"v_mul_u32_u24 v64, 6144, v64\n"
"v_lshl_add_u32 v67, v65, 4, v64\n"
"s_mul_i32 s48, s26, 49152\n"
"s_add_u32 s96, s96, s48\n"
"s_mul_i32 s97, s26, 3072\n"
"s_add_u32 m0, s97, 49152\n"
"s_nop 0\n"
"buffer_load_dwordx4 v67, s[36:39], s96 offen offset:0 lds\n"
"buffer_load_dwordx4 v67, s[40:43], s96 offen offset:1024 lds\n"
"buffer_load_dwordx4 v67, s[44:47], s96 offen offset:2048 lds\n"
"s_add_u32 s96, s96, 196608\n"
"s_add_u32 m0, s97, 61440\n"
"s_nop 0\n"
"buffer_load_dwordx4 v67, s[36:39], s96 offen offset:0 lds\n"
"buffer_load_dwordx4 v67, s[40:43], s96 offen offset:1024 lds\n"
"buffer_load_dwordx4 v67, s[44:47], s96 offen offset:2048 lds\n"
"s_add_u32 s96, s96, 196608\n"
"s_add_u32 m0, s97, 73728\n"
"s_nop 0\n"
"buffer_load_dwordx4 v67, s[36:39], s96 offen offset:0 lds\n"
"buffer_load_dwordx4 v67, s[40:43], s96 offen offset:1024 lds\n"
"buffer_load_dwordx4 v67, s[44:47], s96 offen offset:2048 lds\n"
"s_add_u32 s96, s96, 196608\n"
"s_add_u32 m0, s97, 86016\n"
"s_nop 0\n"
"buffer_load_dwordx4 v67, s[36:39], s96 offen offset:0 lds\n"
"buffer_load_dwordx4 v67, s[40:43], s96 offen offset:1024 lds\n"
"buffer_load_dwordx4 v67, s[44:47], s96 offen offset:2048 lds\n"
"s_add_u32 s96, s96, 196608\n"
"s_add_u32 m0, s97, 98304\n"
"s_nop 0\n"
"buffer_load_dwordx4 v67, s[36:39], s96 offen offset:0 lds\n"
"buffer_load_dwordx4 v67, s[40:43], s96 offen offset:1024 lds\n"
"buffer_load_dwordx4 v67, s[44:47], s96 offen offset:2048 lds\n"
"s_add_u32 s96, s96, 196608\n"
"s_add_u32 m0, s97, 110592\n"
"s_nop 0\n"
"buffer_load_dwordx4 v67, s[36:39], s96 offen offset:0 lds\n"
"buffer_load_dwordx4 v67, s[40:43], s96 offen offset:1024 lds\n"
"buffer_load_dwordx4 v67, s[44:47], s96 offen offset:2048 lds\n"
"s_add_u32 s96, s96, 196608\n"
"s_add_u32 m0, s97, 122880\n"
"s_nop 0\n"
"buffer_load_dwordx4 v67, s[36:39], s96 offen offset:0 lds\n"
"buffer_load_dwordx4 v67, s[40:43], s96 offen offset:1024 lds\n"
"buffer_load_dwordx4 v67, s[44:47], s96 offen offset:2048 lds\n"
"s_add_u32 s96, s96, 196608\n"
"s_add_u32 m0, s97, 135168\n"
"s_nop 0\n"
"buffer_load_dwordx4 v67, s[36:39], s96 offen offset:0 lds\n"
"buffer_load_dwordx4 v67, s[40:43], s96 offen offset:1024 lds\n"
"buffer_load_dwordx4 v67, s[44:47], s96 offen offset:2048 lds\n"
"s_add_u32 s96, s96, 196608\n"
"s_add_u32 m0, s97, 0\n"
"s_nop 0\n"
"buffer_load_dwordx4 v67, s[36:39], s96 offen offset:0 lds\n"
"buffer_load_dwordx4 v67, s[40:43], s96 offen offset:1024 lds\n"
"buffer_load_dwordx4 v67, s[44:47], s96 offen offset:2048 lds\n"
"s_add_u32 s96, s96, 196608\n"
"s_add_u32 m0, s97, 12288\n"
"s_nop 0\n"
"buffer_load_dwordx4 v67, s[36:39], s96 offen offset:0 lds\n"
"buffer_load_dwordx4 v67, s[40:43], s96 offen offset:1024 lds\n"
"buffer_load_dwordx4 v67, s[44:47], s96 offen offset:2048 lds\n"
"s_add_u32 s96, s96, 196608\n"
"v_lshrrev_b32 v65, 4, v1\n"
"v_bfe_u32 v64, v1, 3, 1\n"
"v_and_b32 v66, 1, v65\n"
"v_lshl_add_u32 v64, v66, 1, v64\n"
"v_lshlrev_b32 v64, 5, v64\n"
"v_mul_u32_u24 v65, 3072, v65\n"
"v_bfe_u32 v66, v1, 2, 2\n"
"v_lshl_add_u32 v65, v66, 7, v65\n"
"v_xor_b32 v68, v64, v68\n"
"v_add_u32 v68, v65, v68\n"
"v_add_u32 v69, 49152, v68\n"
"v_add_u32 v70, 98304, v68\n"
"v_xor_b32 v71, v64, v71\n"
"v_add_u32 v71, v65, v71\n"
"v_add_u32 v72, 49152, v71\n"
"v_add_u32 v73, 98304, v71\n"
"s_lshl2_add_u32 s48, s27, s26\n"
"s_lshl_b32 s48, s48, 9\n"
"s_add_u32 s68, s48, 4648960\n"
"s_lshl2_add_u32 s49, s27, 32\n"
"s_waitcnt lgkmcnt(0)\n"
"s_add_u32 s54, s52, 0x7f4a7c15\n"
"s_addc_u32 s55, s53, 0x9e3779b9\n"
"s_lshl_b32 s67, s49, 5\n"
"s_add_u32 s67, s67, 4898816\n"
"s_sub_u32 s51, s67, s68\n"
"s_add_u32 s92, s32, s51\n"
"s_addc_u32 s93, s33, 0x10000\n"
"s_cmp_eq_u32 s26, 0\n"
"s_cselect_b32 s94, 1, 0\n"
"s_mov_b32 s95, 0x20000\n"
"v_mov_b32 v30, s54\n"
"v_mov_b32 v31, s55\n"
"v_cmp_gt_u32 vcc, 12, v1\n"
"v_mul_u32_u24 v3, 296960, v1\n"
"v_mov_b32 v12, 0x7fff0000\n"
"v_cndmask_b32 v3, v12, v3, vcc\n"
"s_add_u32 s51, s67, 76021760\n"
"buffer_store_dwordx2 v[30:31], v3, s[32:35], s51 offen sc1\n"
"s_sub_u32 s67, s68, 296960\n"
"s_mul_i32 s49, s28, 296960\n"
"s_add_u32 s68, s68, s49\n"
"s_sub_u32 s68, s68, 3563520\n"
"s_mov_b32 s62, 32\n"
"s_add_u32 s48, s28, 3\n"
"s_and_b32 s48, s48, 3\n"
"s_mul_i32 s48, s48, 296960\n"
"s_add_u32 s66, s48, 41721856\n"
"s_sub_u32 s48, s48, s49\n"
"s_add_u32 s69, s48, 38010880\n"
"s_sub_u32 s70, 0xfdce2000, s48\n"
"s_mov_b32 s72, s70\n"
"s_mov_b32 s63, 0xfffba000\n"
"s_mul_i32 s64, s26, 9216\n"
"s_add_u32 s65, s64, 73728\n"
"s_xor_b32 s65, s65, s64\n"
"s_add_u32 s63, s63, s64\n"
"v_mov_b32 v4, v2\n"
"v_add_u32 v5, 36864, v2\n"
"s_mov_b32 s71, s63\n"
"v_bfe_u32 v3, v1, 1, 3\n"
"v_mul_u32_u24 v6, 1152, v3\n"
"v_lshrrev_b32 v3, 1, v3\n"
"v_add_u32 v3, s26, v3\n"
"v_lshrrev_b32 v7, 4, v1\n"
"v_lshl_add_u32 v3, v3, 2, v7\n"
"v_and_b32 v3, 15, v3\n"
"v_lshl_add_u32 v6, v3, 2, v6\n"
"v_and_b32 v3, 1, v1\n"
"v_lshl_add_u32 v6, v3, 6, v6\n"
"v_add_u32 v6, 1024, v6\n"
"s_lshl_b32 s48, s26, 2\n"
"v_add_u32 v8, s48, v7\n"
"v_and_b32 v9, 15, v1\n"
"v_add_u32 v3, 64, v1\n"
"v_cmp_eq_u32 vcc, 0, v9\n"
"v_add_u32 v7, 73728, v6\n"
"v_xor_b32 v7, v7, v6\n"
"v_cndmask_b32 v8, v3, v8, vcc\n"
"v_lshlrev_b32 v8, 2, v8\n"
"v_add_u32 v8, 147456, v8\n"
"v_lshlrev_b32 v9, 2, v9\n"
"v_add_u32 v9, 147456, v9\n"
"v_lshrrev_b32 v10, 4, v1\n"
"v_and_b32 v32, 15, v1\n"
"v_and_b32 v33, 1, v1\n"
"v_lshl_add_u32 v34, v10, 2, v33\n"
"v_lshlrev_b32 v33, 4, v33\n"
"v_mov_b32 v35, 0xffff\n"
"v_lshlrev_b32 v33, v33, v35\n"
"v_cmp_le_u32 vcc, v34, v32\n"
"v_add_u32 v34, 2, v34\n"
"s_nop 0\n"
"v_cndmask_b32 v124, 0, v33, vcc\n"
"v_xor_b32 v126, v124, v33\n"
"v_cmp_le_u32 vcc, v34, v32\n"
"s_nop 1\n"
"v_cndmask_b32 v125, 0, v33, vcc\n"
"v_xor_b32 v127, v125, v33\n"
"v_lshlrev_b32 v10, 4, v10\n"
"v_add_u32 v10, 147456, v10\n"
"s_lshl_b32 s48, s26, 10\n"
"s_add_u32 s48, s48, 148480\n"
"v_lshlrev_b32 v11, 3, v1\n"
"v_add_u32 v11, s48, v11\n"
"s_lshl_b32 s48, s26, 3\n"
"s_add_u32 s48, s48, 0\n"
"v_add_u32 v0, s48, v1\n"
"v_lshrrev_b32 v0, 2, v0\n"
"v_lshlrev_b32 v0, 7, v0\n"
"v_cmp_gt_u32 vcc, 8, v1\n"
"v_mov_b32 v3, 0x7fff0000\n"
"s_nop 0\n"
"v_cndmask_b32 v0, v3, v0, vcc\n"
"v_cndmask_b32 v30, 0, v30, vcc\n"
"v_cndmask_b32 v31, 0, v31, vcc\n"
"v_mov_b32 v116, 0xff61b1e6\n"
"v_mov_b32 v117, 0xff61b1e6\n"
"v_mov_b32 v118, 0xff61b1e6\n"
"v_mov_b32 v119, 0xff61b1e6\n"
"v_mov_b32 v120, 0xff61b1e6\n"
"v_mov_b32 v121, 0xff61b1e6\n"
"v_mov_b32 v122, 0xff61b1e6\n"
"v_mov_b32 v123, 0xff61b1e6\n"
"v_cmp_ne_u32 vcc, 0xffff, v124\n"
"s_nop 1\n"
"v_cndmask_b32 v116, 0, v116, vcc\n"
"v_cmp_ne_u32 vcc, 0xffff0000, v124\n"
"s_nop 1\n"
"v_cndmask_b32 v117, 0, v117, vcc\n"
"v_cmp_ne_u32 vcc, 0xffff, v125\n"
"s_nop 1\n"
"v_cndmask_b32 v118, 0, v118, vcc\n"
"v_cmp_ne_u32 vcc, 0xffff0000, v125\n"
"s_nop 1\n"
"v_cndmask_b32 v119, 0, v119, vcc\n"
"v_cmp_ne_u32 vcc, 0xffff, v126\n"
"s_nop 1\n"
"v_cndmask_b32 v120, 0, v120, vcc\n"
"v_cmp_ne_u32 vcc, 0xffff0000, v126\n"
"s_nop 1\n"
"v_cndmask_b32 v121, 0, v121, vcc\n"
"v_cmp_ne_u32 vcc, 0xffff, v127\n"
"s_nop 1\n"
"v_cndmask_b32 v122, 0, v122, vcc\n"
"v_cmp_ne_u32 vcc, 0xffff0000, v127\n"
"s_nop 1\n"
"v_cndmask_b32 v123, 0, v123, vcc\n"
"v_cmp_gt_u32 vcc, 32, v1\n"
"v_mov_b32 v1, 0x7fff0000\n"
"s_nop 0\n"
"v_cndmask_b32 v1, v1, v2, vcc\n"
"s_mov_b32 s58, 0x4003c000\n"
"s_mov_b32 s59, 0x0\n"
"s_mov_b32 s60, 0x80028000\n"
"s_mov_b32 s61, 0x0\n"
"v_subrev_u32 v3, 148480, v11\n"
"v_xor_b32 v3, 1024, v3\n"
"v_add_u32 v3, 148480, v3\n"
"s_add_u32 s74, s66, s72\n"
"s_waitcnt vmcnt(25)\n"
"s_barrier\n"
"s_add_u32 m0, s97, 24576\n"
"s_nop 0\n"
"buffer_load_dwordx4 v67, s[36:39], s96 offen offset:0 lds\n"
"buffer_load_dwordx4 v67, s[40:43], s96 offen offset:1024 lds\n"
"buffer_load_dwordx4 v67, s[44:47], s96 offen offset:2048 lds\n"
"s_add_u32 s96, s96, 196608\n"
"s_add_u32 m0, s97, 36864\n"
"s_nop 0\n"
"buffer_load_dwordx4 v67, s[36:39], s96 offen offset:0 lds\n"
"buffer_load_dwordx4 v67, s[40:43], s96 offen offset:1024 lds\n"
"buffer_load_dwordx4 v67, s[44:47], s96 offen offset:2048 lds\n"
"s_add_u32 s96, s96, 196608\n"
"ds_read_b64_tr_b16 a[0:1], v69 offset:0\n"
"ds_read_b64_tr_b16 a[2:3], v69 offset:512\n"
"ds_read_b64_tr_b16 a[4:5], v69 offset:1024\n"
"ds_read_b64_tr_b16 a[6:7], v69 offset:1536\n"
"ds_read_b64_tr_b16 a[8:9], v72 offset:2048\n"
"ds_read_b64_tr_b16 a[10:11], v72 offset:2560\n"
"ds_read_b64_tr_b16 a[12:13], v69 offset:12288\n"
"ds_read_b64_tr_b16 a[14:15], v69 offset:12800\n"
"ds_read_b64_tr_b16 a[16:17], v69 offset:13312\n"
"ds_read_b64_tr_b16 a[18:19], v69 offset:13824\n"
"ds_read_b64_tr_b16 a[20:21], v72 offset:14336\n"
"ds_read_b64_tr_b16 a[22:23], v72 offset:14848\n"
"s_waitcnt lgkmcnt(0)\n"
"v_mfma_f32_16x16x32_bf16 v[20:23], a[0:3], v[16:19], 0\n"
"v_mfma_f32_16x16x32_bf16 v[24:27], a[4:7], v[16:19], 0\n"
"v_mfma_f32_16x16x32_bf16 v[32:35], v[16:19], a[8:11], 0\n"
"v_mfma_f32_16x16x32_bf16 v[20:23], a[12:15], v[16:19], v[20:23]\n"
"v_mfma_f32_16x16x32_bf16 v[24:27], a[16:19], v[16:19], v[24:27]\n"
"v_mfma_f32_16x16x32_bf16 v[32:35], v[16:19], a[20:23], v[32:35]\n"
"s_waitcnt vmcnt(25)\n"
"s_barrier\n"
"s_add_u32 m0, s97, 49152\n"
"s_nop 0\n"
"buffer_load_dwordx4 v67, s[36:39], s96 offen offset:0 lds\n"
"buffer_load_dwordx4 v67, s[40:43], s96 offen offset:1024 lds\n"
"buffer_load_dwordx4 v67, s[44:47], s96 offen offset:2048 lds\n"
"s_add_u32 s96, s96, 196608\n"
"s_add_u32 m0, s97, 61440\n"
"s_nop 0\n"
"buffer_load_dwordx4 v67, s[36:39], s96 offen offset:0 lds\n"
"buffer_load_dwordx4 v67, s[40:43], s96 offen offset:1024 lds\n"
"buffer_load_dwordx4 v67, s[44:47], s96 offen offset:2048 lds\n"
"s_add_u32 s96, s96, 196608\n"
"ds_read_b64_tr_b16 a[24:25], v69 offset:24576\n"
"ds_read_b64_tr_b16 a[26:27], v69 offset:25088\n"
"ds_read_b64_tr_b16 a[28:29], v69 offset:25600\n"
"ds_read_b64_tr_b16 a[30:31], v69 offset:26112\n"
"ds_read_b64_tr_b16 a[32:33], v72 offset:26624\n"
"ds_read_b64_tr_b16 a[34:35], v72 offset:27136\n"
"ds_read_b64_tr_b16 a[36:37], v69 offset:36864\n"
"ds_read_b64_tr_b16 a[38:39], v69 offset:37376\n"
"ds_read_b64_tr_b16 a[40:41], v69 offset:37888\n"
"ds_read_b64_tr_b16 a[42:43], v69 offset:38400\n"
"ds_read_b64_tr_b16 a[44:45], v72 offset:38912\n"
"ds_read_b64_tr_b16 a[46:47], v72 offset:39424\n"
"s_waitcnt lgkmcnt(0)\n"
"v_mfma_f32_16x16x32_bf16 v[20:23], a[24:27], v[16:19], v[20:23]\n"
"v_mfma_f32_16x16x32_bf16 v[24:27], a[28:31], v[16:19], v[24:27]\n"
"v_mfma_f32_16x16x32_bf16 v[32:35], v[16:19], a[32:35], v[32:35]\n"
"v_mfma_f32_16x16x32_bf16 v[20:23], a[36:39], v[16:19], v[20:23]\n"
"v_mfma_f32_16x16x32_bf16 v[24:27], a[40:43], v[16:19], v[24:27]\n"
"v_mfma_f32_16x16x32_bf16 v[32:35], v[16:19], a[44:47], v[32:35]\n"
"s_waitcnt vmcnt(25)\n"
"s_barrier\n"
"s_add_u32 m0, s97, 73728\n"
"s_nop 0\n"
"buffer_load_dwordx4 v67, s[36:39], s96 offen offset:0 lds\n"
"buffer_load_dwordx4 v67, s[40:43], s96 offen offset:1024 lds\n"
"buffer_load_dwordx4 v67, s[44:47], s96 offen offset:2048 lds\n"
"s_add_u32 s96, s96, 196608\n"
"s_add_u32 m0, s97, 86016\n"
"s_nop 0\n"
"buffer_load_dwordx4 v67, s[36:39], s96 offen offset:0 lds\n"
"buffer_load_dwordx4 v67, s[40:43], s96 offen offset:1024 lds\n"
"buffer_load_dwordx4 v67, s[44:47], s96 offen offset:2048 lds\n"
"s_add_u32 s96, s96, 196608\n"
"ds_read_b64_tr_b16 a[48:49], v70 offset:0\n"
"ds_read_b64_tr_b16 a[50:51], v70 offset:512\n"
"ds_read_b64_tr_b16 a[52:53], v70 offset:1024\n"
"ds_read_b64_tr_b16 a[54:55], v70 offset:1536\n"
"ds_read_b64_tr_b16 a[56:57], v73 offset:2048\n"
"ds_read_b64_tr_b16 a[58:59], v73 offset:2560\n"
"ds_read_b64_tr_b16 a[60:61], v70 offset:12288\n"
"ds_read_b64_tr_b16 a[62:63], v70 offset:12800\n"
"ds_read_b64_tr_b16 a[64:65], v70 offset:13312\n"
"ds_read_b64_tr_b16 a[66:67], v70 offset:13824\n"
"ds_read_b64_tr_b16 a[68:69], v73 offset:14336\n"
"ds_read_b64_tr_b16 a[70:71], v73 offset:14848\n"
"s_waitcnt lgkmcnt(0)\n"
"v_mfma_f32_16x16x32_bf16 v[20:23], a[48:51], v[16:19], v[20:23]\n"
"v_mfma_f32_16x16x32_bf16 v[24:27], a[52:55], v[16:19], v[24:27]\n"
"v_mfma_f32_16x16x32_bf16 v[32:35], v[16:19], a[56:59], v[32:35]\n"
"v_mfma_f32_16x16x32_bf16 v[20:23], a[60:63], v[16:19], v[20:23]\n"
"v_mfma_f32_16x16x32_bf16 v[24:27], a[64:67], v[16:19], v[24:27]\n"
"v_mfma_f32_16x16x32_bf16 v[32:35], v[16:19], a[68:71], v[32:35]\n"
"s_waitcnt vmcnt(25)\n"
"s_barrier\n"
"s_add_u32 m0, s97, 98304\n"
"s_nop 0\n"
"buffer_load_dwordx4 v67, s[36:39], s96 offen offset:0 lds\n"
"buffer_load_dwordx4 v67, s[40:43], s96 offen offset:1024 lds\n"
"buffer_load_dwordx4 v67, s[44:47], s96 offen offset:2048 lds\n"
"s_add_u32 s96, s96, 196608\n"
"s_add_u32 m0, s97, 110592\n"
"s_nop 0\n"
"buffer_load_dwordx4 v67, s[36:39], s96 offen offset:0 lds\n"
"buffer_load_dwordx4 v67, s[40:43], s96 offen offset:1024 lds\n"
"buffer_load_dwordx4 v67, s[44:47], s96 offen offset:2048 lds\n"
"s_add_u32 s96, s96, 196608\n"
"ds_read_b64_tr_b16 a[72:73], v70 offset:24576\n"
"ds_read_b64_tr_b16 a[74:75], v70 offset:25088\n"
"ds_read_b64_tr_b16 a[76:77], v70 offset:25600\n"
"ds_read_b64_tr_b16 a[78:79], v70 offset:26112\n"
"ds_read_b64_tr_b16 a[80:81], v73 offset:26624\n"
"ds_read_b64_tr_b16 a[82:83], v73 offset:27136\n"
"ds_read_b64_tr_b16 a[84:85], v70 offset:36864\n"
"ds_read_b64_tr_b16 a[86:87], v70 offset:37376\n"
"ds_read_b64_tr_b16 a[88:89], v70 offset:37888\n"
"ds_read_b64_tr_b16 a[90:91], v70 offset:38400\n"
"ds_read_b64_tr_b16 a[92:93], v73 offset:38912\n"
"ds_read_b64_tr_b16 a[94:95], v73 offset:39424\n"
"s_waitcnt lgkmcnt(0)\n"
"v_mfma_f32_16x16x32_bf16 v[20:23], a[72:75], v[16:19], v[20:23]\n"
"v_mfma_f32_16x16x32_bf16 v[24:27], a[76:79], v[16:19], v[24:27]\n"
"v_mfma_f32_16x16x32_bf16 v[32:35], v[16:19], a[80:83], v[32:35]\n"
"v_mfma_f32_16x16x32_bf16 v[20:23], a[84:87], v[16:19], v[20:23]\n"
"v_mfma_f32_16x16x32_bf16 v[24:27], a[88:91], v[16:19], v[24:27]\n"
"v_mfma_f32_16x16x32_bf16 v[32:35], v[16:19], a[92:95], v[32:35]\n"
"s_waitcnt vmcnt(25)\n"
"s_barrier\n"
"s_add_u32 m0, s97, 122880\n"
"s_nop 0\n"
"buffer_load_dwordx4 v67, s[36:39], s96 offen offset:0 lds\n"
"buffer_load_dwordx4 v67, s[40:43], s96 offen offset:1024 lds\n"
"buffer_load_dwordx4 v67, s[44:47], s96 offen offset:2048 lds\n"
"s_add_u32 s96, s96, 196608\n"
"s_add_u32 m0, s97, 135168\n"
"s_nop 0\n"
"buffer_load_dwordx4 v67, s[36:39], s96 offen offset:0 lds\n"
"buffer_load_dwordx4 v67, s[40:43], s96 offen offset:1024 lds\n"
"buffer_load_dwordx4 v67, s[44:47], s96 offen offset:2048 lds\n"
"s_add_u32 s96, s96, 196608\n"
"ds_read_b64_tr_b16 a[96:97], v68 offset:0\n"
"ds_read_b64_tr_b16 a[98:99], v68 offset:512\n"
"ds_read_b64_tr_b16 a[100:101], v68 offset:1024\n"
"ds_read_b64_tr_b16 a[102:103], v68 offset:1536\n"
"ds_read_b64_tr_b16 a[104:105], v71 offset:2048\n"
"ds_read_b64_tr_b16 a[106:107], v71 offset:2560\n"
"ds_read_b64_tr_b16 a[108:109], v68 offset:12288\n"
"ds_read_b64_tr_b16 a[110:111], v68 offset:12800\n"
"ds_read_b64_tr_b16 a[112:113], v68 offset:13312\n"
"ds_read_b64_tr_b16 a[114:115], v68 offset:13824\n"
"ds_read_b64_tr_b16 a[116:117], v71 offset:14336\n"
"ds_read_b64_tr_b16 a[118:119], v71 offset:14848\n"
"s_waitcnt lgkmcnt(0)\n"
"v_mfma_f32_16x16x32_bf16 v[20:23], a[96:99], v[16:19], v[20:23]\n"
"v_mfma_f32_16x16x32_bf16 v[24:27], a[100:103], v[16:19], v[24:27]\n"
"v_mfma_f32_16x16x32_bf16 v[32:35], v[16:19], a[104:107], v[32:35]\n"
"v_mfma_f32_16x16x32_bf16 v[20:23], a[108:111], v[16:19], v[20:23]\n"
"v_mfma_f32_16x16x32_bf16 v[24:27], a[112:115], v[16:19], v[24:27]\n"
"v_mfma_f32_16x16x32_bf16 v[32:35], v[16:19], a[116:119], v[32:35]\n"
"s_waitcnt vmcnt(24)\n"
"s_barrier\n"
"s_add_u32 m0, s97, 0\n"
"s_nop 0\n"
"buffer_load_dwordx4 v67, s[36:39], s96 offen offset:0 lds\n"
"buffer_load_dwordx4 v67, s[40:43], s96 offen offset:1024 lds\n"
"buffer_load_dwordx4 v67, s[44:47], s96 offen offset:2048 lds\n"
"s_add_u32 s96, s96, 196608\n"
"s_add_u32 m0, s97, 12288\n"
"s_nop 0\n"
"buffer_load_dwordx4 v67, s[36:39], s96 offen offset:0 lds\n"
"buffer_load_dwordx4 v67, s[40:43], s96 offen offset:1024 lds\n"
"buffer_load_dwordx4 v67, s[44:47], s96 offen offset:2048 lds\n"
"s_add_u32 s96, s96, 196608\n"
"ds_read_b64_tr_b16 a[120:121], v68 offset:24576\n"
"ds_read_b64_tr_b16 a[122:123], v68 offset:25088\n"
"ds_read_b64_tr_b16 a[124:125], v68 offset:25600\n"
"ds_read_b64_tr_b16 a[126:127], v68 offset:26112\n"
"ds_read_b64_tr_b16 a[128:129], v71 offset:26624\n"
"ds_read_b64_tr_b16 a[130:131], v71 offset:27136\n"
"ds_read_b64_tr_b16 a[132:133], v68 offset:36864\n"
"ds_read_b64_tr_b16 a[134:135], v68 offset:37376\n"
"ds_read_b64_tr_b16 a[136:137], v68 offset:37888\n"
"ds_read_b64_tr_b16 a[138:139], v68 offset:38400\n"
"ds_read_b64_tr_b16 a[140:141], v71 offset:38912\n"
"ds_read_b64_tr_b16 a[142:143], v71 offset:39424\n"
"s_waitcnt lgkmcnt(0)\n"
"v_mfma_f32_16x16x32_bf16 v[20:23], a[120:123], v[16:19], v[20:23]\n"
"v_mfma_f32_16x16x32_bf16 v[24:27], a[124:127], v[16:19], v[24:27]\n"
"v_mfma_f32_16x16x32_bf16 v[32:35], v[16:19], a[128:131], v[32:35]\n"
"v_mfma_f32_16x16x32_bf16 v[20:23], a[132:135], v[16:19], v[20:23]\n"
"v_mfma_f32_16x16x32_bf16 v[24:27], a[136:139], v[16:19], v[24:27]\n"
"v_mfma_f32_16x16x32_bf16 v[32:35], v[16:19], a[140:143], v[32:35]\n"
"s_waitcnt vmcnt(24)\n"
"s_barrier\n"
"s_add_u32 m0, s97, 24576\n"
"s_nop 0\n"
"buffer_load_dwordx4 v67, s[36:39], s96 offen offset:0 lds\n"
"buffer_load_dwordx4 v67, s[40:43], s96 offen offset:1024 lds\n"
"buffer_load_dwordx4 v67, s[44:47], s96 offen offset:2048 lds\n"
"s_add_u32 s96, s96, 196608\n"
"s_add_u32 m0, s97, 36864\n"
"s_nop 0\n"
"buffer_load_dwordx4 v67, s[36:39], s96 offen offset:0 lds\n"
"buffer_load_dwordx4 v67, s[40:43], s96 offen offset:1024 lds\n"
"buffer_load_dwordx4 v67, s[44:47], s96 offen offset:2048 lds\n"
"s_add_u32 s96, s96, 196608\n"
"ds_read_b64_tr_b16 a[144:145], v69 offset:0\n"
"ds_read_b64_tr_b16 a[146:147], v69 offset:512\n"
"ds_read_b64_tr_b16 a[148:149], v69 offset:1024\n"
"ds_read_b64_tr_b16 a[150:151], v69 offset:1536\n"
"ds_read_b64_tr_b16 a[152:153], v72 offset:2048\n"
"ds_read_b64_tr_b16 a[154:155], v72 offset:2560\n"
"ds_read_b64_tr_b16 a[156:157], v69 offset:12288\n"
"ds_read_b64_tr_b16 a[158:159], v69 offset:12800\n"
"ds_read_b64_tr_b16 a[160:161], v69 offset:13312\n"
"ds_read_b64_tr_b16 a[162:163], v69 offset:13824\n"
"ds_read_b64_tr_b16 a[164:165], v72 offset:14336\n"
"ds_read_b64_tr_b16 a[166:167], v72 offset:14848\n"
"s_waitcnt lgkmcnt(0)\n"
"v_mfma_f32_16x16x32_bf16 v[20:23], a[144:147], v[16:19], v[20:23]\n"
"v_mfma_f32_16x16x32_bf16 v[24:27], a[148:151], v[16:19], v[24:27]\n"
"v_mfma_f32_16x16x32_bf16 v[32:35], v[16:19], a[152:155], v[32:35]\n"
"v_mfma_f32_16x16x32_bf16 v[20:23], a[156:159], v[16:19], v[20:23]\n"
"v_mfma_f32_16x16x32_bf16 v[24:27], a[160:163], v[16:19], v[24:27]\n"
"v_mfma_f32_16x16x32_bf16 v[32:35], v[16:19], a[164:167], v[32:35]\n"
"s_waitcnt vmcnt(24)\n"
"s_barrier\n"
"s_add_u32 m0, s97, 49152\n"
"s_nop 0\n"
"buffer_load_dwordx4 v67, s[36:39], s96 offen offset:0 lds\n"
"buffer_load_dwordx4 v67, s[40:43], s96 offen offset:1024 lds\n"
"buffer_load_dwordx4 v67, s[44:47], s96 offen offset:2048 lds\n"
"s_add_u32 s96, s96, 196608\n"
"s_add_u32 m0, s97, 61440\n"
"s_nop 0\n"
"buffer_load_dwordx4 v67, s[36:39], s96 offen offset:0 lds\n"
"buffer_load_dwordx4 v67, s[40:43], s96 offen offset:1024 lds\n"
"buffer_load_dwordx4 v67, s[44:47], s96 offen offset:2048 lds\n"
"s_add_u32 s96, s96, 196608\n"
"ds_read_b64_tr_b16 a[168:169], v69 offset:24576\n"
"ds_read_b64_tr_b16 a[170:171], v69 offset:25088\n"
"ds_read_b64_tr_b16 a[172:173], v69 offset:25600\n"
"ds_read_b64_tr_b16 a[174:175], v69 offset:26112\n"
"ds_read_b64_tr_b16 a[176:177], v72 offset:26624\n"
"ds_read_b64_tr_b16 a[178:179], v72 offset:27136\n"
"ds_read_b64_tr_b16 a[180:181], v69 offset:36864\n"
"ds_read_b64_tr_b16 a[182:183], v69 offset:37376\n"
"ds_read_b64_tr_b16 a[184:185], v69 offset:37888\n"
"ds_read_b64_tr_b16 a[186:187], v69 offset:38400\n"
"ds_read_b64_tr_b16 a[188:189], v72 offset:38912\n"
"ds_read_b64_tr_b16 a[190:191], v72 offset:39424\n"
"s_waitcnt lgkmcnt(0)\n"
"v_mfma_f32_16x16x32_bf16 v[20:23], a[168:171], v[16:19], v[20:23]\n"
"v_mfma_f32_16x16x32_bf16 v[24:27], a[172:175], v[16:19], v[24:27]\n"
"v_mfma_f32_16x16x32_bf16 v[32:35], v[16:19], a[176:179], v[32:35]\n"
"v_mfma_f32_16x16x32_bf16 v[20:23], a[180:183], v[16:19], v[20:23]\n"
"v_mfma_f32_16x16x32_bf16 v[24:27], a[184:187], v[16:19], v[24:27]\n"
"v_mfma_f32_16x16x32_bf16 v[32:35], v[16:19], a[188:191], v[32:35]\n"
"s_waitcnt vmcnt(24)\n"
"s_barrier\n"
"s_add_u32 m0, s97, 73728\n"
"s_nop 0\n"
"buffer_load_dwordx4 v67, s[36:39], s96 offen offset:0 lds\n"
"buffer_load_dwordx4 v67, s[40:43], s96 offen offset:1024 lds\n"
"buffer_load_dwordx4 v67, s[44:47], s96 offen offset:2048 lds\n"
"s_add_u32 s96, s96, 196608\n"
"s_add_u32 m0, s97, 86016\n"
"s_nop 0\n"
"buffer_load_dwordx4 v67, s[36:39], s96 offen offset:0 lds\n"
"buffer_load_dwordx4 v67, s[40:43], s96 offen offset:1024 lds\n"
"buffer_load_dwordx4 v67, s[44:47], s96 offen offset:2048 lds\n"
"s_add_u32 s96, s96, 196608\n"
"ds_read_b64_tr_b16 a[192:193], v70 offset:0\n"
"ds_read_b64_tr_b16 a[194:195], v70 offset:512\n"
"ds_read_b64_tr_b16 a[196:197], v70 offset:1024\n"
"ds_read_b64_tr_b16 a[198:199], v70 offset:1536\n"
"ds_read_b64_tr_b16 a[200:201], v73 offset:2048\n"
"ds_read_b64_tr_b16 a[202:203], v73 offset:2560\n"
"ds_read_b64_tr_b16 a[204:205], v70 offset:12288\n"
"ds_read_b64_tr_b16 a[206:207], v70 offset:12800\n"
"ds_read_b64_tr_b16 a[208:209], v70 offset:13312\n"
"ds_read_b64_tr_b16 a[210:211], v70 offset:13824\n"
"ds_read_b64_tr_b16 a[212:213], v73 offset:14336\n"
"ds_read_b64_tr_b16 a[214:215], v73 offset:14848\n"
"s_waitcnt lgkmcnt(0)\n"
"v_mfma_f32_16x16x32_bf16 v[20:23], a[192:195], v[16:19], v[20:23]\n"
"v_mfma_f32_16x16x32_bf16 v[24:27], a[196:199], v[16:19], v[24:27]\n"
"v_mfma_f32_16x16x32_bf16 v[32:35], v[16:19], a[200:203], v[32:35]\n"
"v_mfma_f32_16x16x32_bf16 v[20:23], a[204:207], v[16:19], v[20:23]\n"
"v_mfma_f32_16x16x32_bf16 v[24:27], a[208:211], v[16:19], v[24:27]\n"
"v_mfma_f32_16x16x32_bf16 v[32:35], v[16:19], a[212:215], v[32:35]\n"
"s_waitcnt vmcnt(24)\n"
"s_barrier\n"
"s_add_u32 m0, s97, 98304\n"
"s_nop 0\n"
"buffer_load_dwordx4 v67, s[36:39], s96 offen offset:0 lds\n"
"buffer_load_dwordx4 v67, s[40:43], s96 offen offset:1024 lds\n"
"buffer_load_dwordx4 v67, s[44:47], s96 offen offset:2048 lds\n"
"s_add_u32 s96, s96, 196608\n"
"s_add_u32 m0, s97, 110592\n"
"s_nop 0\n"
"buffer_load_dwordx4 v67, s[36:39], s96 offen offset:0 lds\n"
"buffer_load_dwordx4 v67, s[40:43], s96 offen offset:1024 lds\n"
"buffer_load_dwordx4 v67, s[44:47], s96 offen offset:2048 lds\n"
"s_add_u32 s96, s96, 196608\n"
"buffer_load_dwordx2 v[76:77], v0, s[32:35], s66 offen sc1\n"
"buffer_load_dwordx2 v[78:79], v0, s[32:35], s74 offen sc1\n"
"ds_read_b64_tr_b16 a[216:217], v70 offset:24576\n"
"ds_read_b64_tr_b16 a[218:219], v70 offset:25088\n"
"ds_read_b64_tr_b16 a[220:221], v70 offset:25600\n"
"ds_read_b64_tr_b16 a[222:223], v70 offset:26112\n"
"ds_read_b64_tr_b16 a[224:225], v73 offset:26624\n"
"ds_read_b64_tr_b16 a[226:227], v73 offset:27136\n"
"ds_read_b64_tr_b16 a[228:229], v70 offset:36864\n"
"ds_read_b64_tr_b16 a[230:231], v70 offset:37376\n"
"ds_read_b64_tr_b16 a[232:233], v70 offset:37888\n"
"ds_read_b64_tr_b16 a[234:235], v70 offset:38400\n"
"ds_read_b64_tr_b16 a[236:237], v73 offset:38912\n"
"ds_read_b64_tr_b16 a[238:239], v73 offset:39424\n"
"s_waitcnt lgkmcnt(0)\n"
"v_mfma_f32_16x16x32_bf16 v[20:23], a[216:219], v[16:19], v[20:23]\n"
"v_mfma_f32_16x16x32_bf16 v[24:27], a[220:223], v[16:19], v[24:27]\n"
"v_mfma_f32_16x16x32_bf16 v[32:35], v[16:19], a[224:227], v[32:35]\n"
"v_mfma_f32_16x16x32_bf16 v[20:23], a[228:231], v[16:19], v[20:23]\n"
"v_mfma_f32_16x16x32_bf16 v[24:27], a[232:235], v[16:19], v[24:27]\n"
"v_mfma_f32_16x16x32_bf16 v[32:35], v[16:19], a[236:239], v[32:35]\n"
"s_waitcnt vmcnt(26)\n"
"s_barrier\n"
"s_add_u32 m0, s97, 122880\n"
"s_nop 0\n"
"buffer_load_dwordx4 v67, s[36:39], s96 offen offset:0 lds\n"
"buffer_load_dwordx4 v67, s[40:43], s96 offen offset:1024 lds\n"
"buffer_load_dwordx4 v67, s[44:47], s96 offen offset:2048 lds\n"
"s_add_u32 s96, s96, 196608\n"
"s_add_u32 m0, s97, 135168\n"
"s_nop 0\n"
"buffer_load_dwordx4 v67, s[36:39], s96 offen offset:0 lds\n"
"buffer_load_dwordx4 v67, s[40:43], s96 offen offset:1024 lds\n"
"buffer_load_dwordx4 v67, s[44:47], s96 offen offset:2048 lds\n"
"s_add_u32 s96, s96, 196608\n"
"ds_read_b64_tr_b16 a[240:241], v68 offset:0\n"
"ds_read_b64_tr_b16 a[242:243], v68 offset:512\n"
"ds_read_b64_tr_b16 a[244:245], v68 offset:1024\n"
"ds_read_b64_tr_b16 a[246:247], v68 offset:1536\n"
"ds_read_b64_tr_b16 a[248:249], v71 offset:2048\n"
"ds_read_b64_tr_b16 a[250:251], v71 offset:2560\n"
"ds_read_b64_tr_b16 a[252:253], v68 offset:12288\n"
"ds_read_b64_tr_b16 a[254:255], v68 offset:12800\n"
"ds_read_b64_tr_b16 v[128:129], v68 offset:13312\n"
"ds_read_b64_tr_b16 v[130:131], v68 offset:13824\n"
"ds_read_b64_tr_b16 v[132:133], v71 offset:14336\n"
"ds_read_b64_tr_b16 v[134:135], v71 offset:14848\n"
"s_waitcnt lgkmcnt(0)\n"
"v_mfma_f32_16x16x32_bf16 v[20:23], a[240:243], v[16:19], v[20:23]\n"
"v_mfma_f32_16x16x32_bf16 v[24:27], a[244:247], v[16:19], v[24:27]\n"
"v_mfma_f32_16x16x32_bf16 v[32:35], v[16:19], a[248:251], v[32:35]\n"
"v_mfma_f32_16x16x32_bf16 v[20:23], a[252:255], v[16:19], v[20:23]\n"
"v_mfma_f32_16x16x32_bf16 v[24:27], v[128:131], v[16:19], v[24:27]\n"
"v_mfma_f32_16x16x32_bf16 v[32:35], v[16:19], v[132:135], v[32:35]\n"
"s_waitcnt vmcnt(26)\n"
"s_barrier\n"
"ds_read_b64_tr_b16 v[136:137], v68 offset:24576\n"
"ds_read_b64_tr_b16 v[138:139], v68 offset:25088\n"
"ds_read_b64_tr_b16 v[140:141], v68 offset:25600\n"
"ds_read_b64_tr_b16 v[142:143], v68 offset:26112\n"
"ds_read_b64_tr_b16 v[144:145], v71 offset:26624\n"
"ds_read_b64_tr_b16 v[146:147], v71 offset:27136\n"
"ds_read_b64_tr_b16 v[148:149], v68 offset:36864\n"
"ds_read_b64_tr_b16 v[150:151], v68 offset:37376\n"
"ds_read_b64_tr_b16 v[152:153], v68 offset:37888\n"
"ds_read_b64_tr_b16 v[154:155], v68 offset:38400\n"
"ds_read_b64_tr_b16 v[156:157], v71 offset:38912\n"
"ds_read_b64_tr_b16 v[158:159], v71 offset:39424\n"
"s_waitcnt lgkmcnt(0)\n"
"v_mfma_f32_16x16x32_bf16 v[20:23], v[136:139], v[16:19], v[20:23]\n"
"v_mfma_f32_16x16x32_bf16 v[24:27], v[140:143], v[16:19], v[24:27]\n"
"v_mfma_f32_16x16x32_bf16 v[32:35], v[16:19], v[144:147], v[32:35]\n"
"v_mfma_f32_16x16x32_bf16 v[20:23], v[148:151], v[16:19], v[20:23]\n"
"v_mfma_f32_16x16x32_bf16 v[24:27], v[152:155], v[16:19], v[24:27]\n"
"v_mfma_f32_16x16x32_bf16 v[32:35], v[16:19], v[156:159], v[32:35]\n"
"s_waitcnt vmcnt(20)\n"
"s_barrier\n"
"ds_read_b64_tr_b16 v[160:161], v69 offset:0\n"
"ds_read_b64_tr_b16 v[162:163], v69 offset:512\n"
"ds_read_b64_tr_b16 v[164:165], v69 offset:1024\n"
"ds_read_b64_tr_b16 v[166:167], v69 offset:1536\n"
"ds_read_b64_tr_b16 v[168:169], v72 offset:2048\n"
"ds_read_b64_tr_b16 v[170:171], v72 offset:2560\n"
"ds_read_b64_tr_b16 v[172:173], v69 offset:12288\n"
"ds_read_b64_tr_b16 v[174:175], v69 offset:12800\n"
"ds_read_b64_tr_b16 v[176:177], v69 offset:13312\n"
"ds_read_b64_tr_b16 v[178:179], v69 offset:13824\n"
"ds_read_b64_tr_b16 v[180:181], v72 offset:14336\n"
"ds_read_b64_tr_b16 v[182:183], v72 offset:14848\n"
"s_waitcnt lgkmcnt(0)\n"
"v_mfma_f32_16x16x32_bf16 v[20:23], v[160:163], v[16:19], v[20:23]\n"
"v_mfma_f32_16x16x32_bf16 v[24:27], v[164:167], v[16:19], v[24:27]\n"
"v_mfma_f32_16x16x32_bf16 v[32:35], v[16:19], v[168:171], v[32:35]\n"
"v_mfma_f32_16x16x32_bf16 v[20:23], v[172:175], v[16:19], v[20:23]\n"
"v_mfma_f32_16x16x32_bf16 v[24:27], v[176:179], v[16:19], v[24:27]\n"
"v_mfma_f32_16x16x32_bf16 v[32:35], v[16:19], v[180:183], v[32:35]\n"
"s_waitcnt vmcnt(14)\n"
"s_barrier\n"
"ds_read_b64_tr_b16 v[184:185], v69 offset:24576\n"
"ds_read_b64_tr_b16 v[186:187], v69 offset:25088\n"
"ds_read_b64_tr_b16 v[188:189], v69 offset:25600\n"
"ds_read_b64_tr_b16 v[190:191], v69 offset:26112\n"
"ds_read_b64_tr_b16 v[192:193], v72 offset:26624\n"
"ds_read_b64_tr_b16 v[194:195], v72 offset:27136\n"
"ds_read_b64_tr_b16 v[196:197], v69 offset:36864\n"
"ds_read_b64_tr_b16 v[198:199], v69 offset:37376\n"
"ds_read_b64_tr_b16 v[200:201], v69 offset:37888\n"
"ds_read_b64_tr_b16 v[202:203], v69 offset:38400\n"
"ds_read_b64_tr_b16 v[204:205], v72 offset:38912\n"
"ds_read_b64_tr_b16 v[206:207], v72 offset:39424\n"
"s_waitcnt lgkmcnt(0)\n"
"v_mfma_f32_16x16x32_bf16 v[20:23], v[184:187], v[16:19], v[20:23]\n"
"v_mfma_f32_16x16x32_bf16 v[24:27], v[188:191], v[16:19], v[24:27]\n"
"v_mfma_f32_16x16x32_bf16 v[32:35], v[16:19], v[192:195], v[32:35]\n"
"v_mfma_f32_16x16x32_bf16 v[20:23], v[196:199], v[16:19], v[20:23]\n"
"v_mfma_f32_16x16x32_bf16 v[24:27], v[200:203], v[16:19], v[24:27]\n"
"v_mfma_f32_16x16x32_bf16 v[32:35], v[16:19], v[204:207], v[32:35]\n"
"s_waitcnt vmcnt(8)\n"
"s_barrier\n"
"s_waitcnt vmcnt(7)\n"
"v_cmp_ne_u64 vcc, v[30:31], v[76:77]\n"
"s_cbranch_vccnz .L_rtqa\n"
".L_bkqa:\n"
"s_add_u32 s63, s66, s71\n"
"s_add_u32 s66, s66, s72\n"
"s_add_u32 m0, s64, 0\n"
"s_nop 0\n"
"buffer_load_dwordx4 v2, s[32:35], s63 offen offset:0 sc1 lds\n"
"buffer_load_dwordx4 v2, s[32:35], s63 offen offset:1024 sc1 lds\n"
"buffer_load_dwordx4 v2, s[32:35], s63 offen offset:2048 sc1 lds\n"
"buffer_load_dwordx4 v2, s[32:35], s63 offen offset:3072 sc1 lds\n"
"s_add_u32 s63, s63, 4096\n"
"s_add_u32 m0, s64, 4096\n"
"s_nop 0\n"
"buffer_load_dwordx4 v2, s[32:35], s63 offen offset:0 sc1 lds\n"
"buffer_load_dwordx4 v2, s[32:35], s63 offen offset:1024 sc1 lds\n"
"buffer_load_dwordx4 v2, s[32:35], s63 offen offset:2048 sc1 lds\n"
"buffer_load_dwordx4 v2, s[32:35], s63 offen offset:3072 sc1 lds\n"
"s_add_u32 s63, s63, 4096\n"
"s_add_u32 m0, s64, 8192\n"
"s_nop 0\n"
"buffer_load_dwordx4 v2, s[32:35], s63 offen offset:0 sc1 lds\n"
"s_mov_b32 s72, 1187840\n"
"s_waitcnt vmcnt(15)\n"
"v_cmp_ne_u64 vcc, v[30:31], v[78:79]\n"
"s_cbranch_vccnz .L_rtqb\n"
".L_bkqb:\n"
"s_add_u32 s63, s66, s71\n"
"s_add_u32 s66, s66, s72\n"
"s_add_u32 m0, s64, 36864\n"
"s_nop 0\n"
"buffer_load_dwordx4 v2, s[32:35], s63 offen offset:0 sc1 lds\n"
"buffer_load_dwordx4 v2, s[32:35], s63 offen offset:1024 sc1 lds\n"
"buffer_load_dwordx4 v2, s[32:35], s63 offen offset:2048 sc1 lds\n"
"buffer_load_dwordx4 v2, s[32:35], s63 offen offset:3072 sc1 lds\n"
"s_add_u32 s63, s63, 4096\n"
"s_add_u32 m0, s64, 40960\n"
"s_nop 0\n"
"buffer_load_dwordx4 v2, s[32:35], s63 offen offset:0 sc1 lds\n"
"buffer_load_dwordx4 v2, s[32:35], s63 offen offset:1024 sc1 lds\n"
"buffer_load_dwordx4 v2, s[32:35], s63 offen offset:2048 sc1 lds\n"
"buffer_load_dwordx4 v2, s[32:35], s63 offen offset:3072 sc1 lds\n"
"s_add_u32 s63, s63, 4096\n"
"s_add_u32 m0, s64, 45056\n"
"s_nop 0\n"
"buffer_load_dwordx4 v2, s[32:35], s63 offen offset:0 sc1 lds\n"
"s_xor_b32 s64, s64, s65\n"
"buffer_load_dwordx2 v[12:13], v0, s[32:35], s66 offen sc1\n"
"ds_read_b64_tr_b16 v[208:209], v70 offset:0\n"
"ds_read_b64_tr_b16 v[210:211], v70 offset:512\n"
"ds_read_b64_tr_b16 v[212:213], v70 offset:1024\n"
"ds_read_b64_tr_b16 v[214:215], v70 offset:1536\n"
"ds_read_b64_tr_b16 v[216:217], v73 offset:2048\n"
"ds_read_b64_tr_b16 v[218:219], v73 offset:2560\n"
"ds_read_b64_tr_b16 v[220:221], v70 offset:12288\n"
"ds_read_b64_tr_b16 v[222:223], v70 offset:12800\n"
"ds_read_b64_tr_b16 v[224:225], v70 offset:13312\n"
"ds_read_b64_tr_b16 v[226:227], v70 offset:13824\n"
"ds_read_b64_tr_b16 v[228:229], v73 offset:14336\n"
"ds_read_b64_tr_b16 v[230:231], v73 offset:14848\n"
"s_waitcnt lgkmcnt(0)\n"
"v_mfma_f32_16x16x32_bf16 v[20:23], v[208:211], v[16:19], v[20:23]\n"
"v_mfma_f32_16x16x32_bf16 v[24:27], v[212:215], v[16:19], v[24:27]\n"
"v_mfma_f32_16x16x32_bf16 v[32:35], v[16:19], v[216:219], v[32:35]\n"
"v_mfma_f32_16x16x32_bf16 v[20:23], v[220:223], v[16:19], v[20:23]\n"
"v_mfma_f32_16x16x32_bf16 v[24:27], v[224:227], v[16:19], v[24:27]\n"
"v_mfma_f32_16x16x32_bf16 v[32:35], v[16:19], v[228:231], v[32:35]\n"
"s_waitcnt vmcnt(19)\n"
"s_barrier\n"
"ds_read_b64_tr_b16 v[232:233], v70 offset:24576\n"
"ds_read_b64_tr_b16 v[234:235], v70 offset:25088\n"
"ds_read_b64_tr_b16 v[236:237], v70 offset:25600\n"
"ds_read_b64_tr_b16 v[238:239], v70 offset:26112\n"
"ds_read_b64_tr_b16 v[240:241], v73 offset:26624\n"
"ds_read_b64_tr_b16 v[242:243], v73 offset:27136\n"
"ds_read_b64_tr_b16 v[244:245], v70 offset:36864\n"
"ds_read_b64_tr_b16 v[246:247], v70 offset:37376\n"
"ds_read_b64_tr_b16 v[248:249], v70 offset:37888\n"
"ds_read_b64_tr_b16 v[250:251], v70 offset:38400\n"
"ds_read_b64_tr_b16 v[252:253], v73 offset:38912\n"
"ds_read_b64_tr_b16 v[254:255], v73 offset:39424\n"
"s_waitcnt lgkmcnt(0)\n"
"v_mfma_f32_16x16x32_bf16 v[20:23], v[232:235], v[16:19], v[20:23]\n"
"v_mfma_f32_16x16x32_bf16 v[24:27], v[236:239], v[16:19], v[24:27]\n"
"v_mfma_f32_16x16x32_bf16 v[32:35], v[16:19], v[240:243], v[32:35]\n"
"v_mfma_f32_16x16x32_bf16 v[20:23], v[244:247], v[16:19], v[20:23]\n"
"v_mfma_f32_16x16x32_bf16 v[24:27], v[248:251], v[16:19], v[24:27]\n"
"v_mfma_f32_16x16x32_bf16 v[32:35], v[16:19], v[252:255], v[32:35]\n"
"s_nop 7\n"
"v_mov_b32 v28, v32\n"
"s_waitcnt vmcnt(0)\n"
"s_barrier\n"
"ds_read_b128 v[56:59], v4 offset:0\n"
"ds_read_b128 v[60:63], v4 offset:1152\n"
"ds_read_b128 v[64:67], v4 offset:2304\n"
"ds_read_b128 v[68:71], v4 offset:3456\n"
"ds_read_b128 v[72:75], v4 offset:4608\n"
"ds_read_b128 v[76:79], v4 offset:5760\n"
".p2align 6\n"
".L_ql:\n"
"s_bitcmp1_b64 s[58:59], s62\n"
"s_cbranch_scc1 .L_qs0\n"
".L_qb0:\n"
"s_waitcnt lgkmcnt(2)\n"
"v_mfma_f32_16x16x32_bf16 v[32:35], a[0:3], v[56:59], 0\n"
"ds_read_b128 v[80:83], v4 offset:6912\n"
"ds_read2_b32 v[14:15], v9 offset0:32 offset1:48\n"
"v_mfma_f32_16x16x32_bf16 v[32:35], a[12:15], v[60:63], v[32:35]\n"
"ds_read_b128 v[92:95], v10 offset:128\n"
"ds_read_b128 v[96:99], v10 offset:192\n"
"v_mfma_f32_16x16x32_bf16 v[36:39], a[16:19], v[60:63], 0\n"
"s_add_u32 s68, s68, 1187840\n"
"v_mfma_f32_16x16x32_bf16 v[36:39], a[4:7], v[56:59], v[36:39]\n"
"ds_read_b128 v[84:87], v4 offset:8064\n"
"v_mfma_f32_16x16x32_bf16 v[40:43], v[56:59], a[8:11], 0\n"
"ds_read2st64_b32 v[88:89], v6 offset0:0 offset1:36\n"
"v_mfma_f32_16x16x32_bf16 v[40:43], v[60:63], a[20:23], v[40:43]\n"
"s_waitcnt vmcnt(5)\n"
"v_cmp_ne_u64 vcc, v[30:31], v[12:13]\n"
"v_mfma_f32_16x16x32_bf16 v[40:43], v[64:67], a[32:35], v[40:43]\n"
"ds_read_b128 v[56:59], v4 offset:9216\n"
"ds_read2st64_b32 v[90:91], v6 offset0:72 offset1:108\n"
"v_mfma_f32_16x16x32_bf16 v[40:43], v[68:71], a[44:47], v[40:43]\n"
"s_cbranch_vccnz .L_rtq0\n"
".L_bkq0:\n"
"s_add_u32 s63, s66, s71\n"
"v_mfma_f32_16x16x32_bf16 v[36:39], a[40:43], v[68:71], v[36:39]\n"
"s_add_u32 s66, s66, s72\n"
"s_add_u32 m0, s64, 0\n"
"v_mfma_f32_16x16x32_bf16 v[36:39], a[28:31], v[64:67], v[36:39]\n"
"ds_read_b128 v[60:63], v4 offset:10368\n"
"buffer_load_dwordx4 v2, s[32:35], s63 offen offset:0 sc1 lds\n"
"v_mfma_f32_16x16x32_bf16 v[32:35], a[24:27], v[64:67], v[32:35]\n"
"s_waitcnt lgkmcnt(5)\n"
"v_mul_f32 v29, 0x3e8293ee, v15\n"
"v_mfma_f32_16x16x32_bf16 v[32:35], a[36:39], v[68:71], v[32:35]\n"
"v_fma_f32 v48, -v14, v24, v48\n"
"s_waitcnt lgkmcnt(4)\n"
"v_mfma_f32_16x16x32_bf16 v[32:35], a[48:51], v[72:75], v[32:35]\n"
"ds_read_b128 v[64:67], v4 offset:11520\n"
"v_fma_f32 v49, -v14, v25, v49\n"
"v_mfma_f32_16x16x32_bf16 v[32:35], a[60:63], v[76:79], v[32:35]\n"
"v_fma_f32 v50, -v14, v26, v50\n"
"v_fma_f32 v51, -v14, v27, v51\n"
"v_mfma_f32_16x16x32_bf16 v[36:39], a[64:67], v[76:79], v[36:39]\n"
"v_fma_f32 v44, -v14, v20, v44\n"
"v_fma_f32 v45, -v14, v21, v45\n"
"v_mfma_f32_16x16x32_bf16 v[36:39], a[52:55], v[72:75], v[36:39]\n"
"ds_read_b128 v[68:71], v4 offset:12672\n"
"v_fma_f32 v46, -v14, v22, v46\n"
"v_mfma_f32_16x16x32_bf16 v[40:43], v[72:75], a[56:59], v[40:43]\n"
"buffer_load_dwordx4 v2, s[32:35], s63 offen offset:1024 sc1 lds\n"
"v_fma_f32 v47, -v14, v23, v47\n"
"v_mfma_f32_16x16x32_bf16 v[40:43], v[76:79], a[68:71], v[40:43]\n"
"v_mul_f32 v48, v48, v15\n"
"v_mul_f32 v49, v49, v15\n"
"v_mfma_f32_16x16x32_bf16 v[40:43], v[80:83], a[80:83], v[40:43]\n"
"ds_read_b128 v[72:75], v4 offset:13824\n"
"v_mul_f32 v50, v50, v15\n"
"v_mfma_f32_16x16x32_bf16 v[40:43], v[84:87], a[92:95], v[40:43]\n"
"v_mul_f32 v51, v51, v15\n"
"v_mul_f32 v44, v44, v29\n"
"v_mfma_f32_16x16x32_bf16 v[36:39], a[88:91], v[84:87], v[36:39]\n"
"v_mul_f32 v45, v45, v29\n"
"v_mul_f32 v46, v46, v29\n"
"v_mfma_f32_16x16x32_bf16 v[36:39], a[76:79], v[80:83], v[36:39]\n"
"ds_read_b128 v[76:79], v4 offset:14976\n"
"v_mul_f32 v47, v47, v29\n"
"v_mfma_f32_16x16x32_bf16 v[32:35], a[72:75], v[80:83], v[32:35]\n"
"v_cvt_pk_bf16_f32 v108, v48, v49\n"
"v_cvt_pk_bf16_f32 v109, v50, v51\n"
"v_mfma_f32_16x16x32_bf16 v[32:35], a[84:87], v[84:87], v[32:35]\n"
"v_cvt_pk_bf16_f32 v100, v44, v45\n"
"s_waitcnt lgkmcnt(2)\n"
"v_mfma_f32_16x16x32_bf16 v[32:35], a[96:99], v[56:59], v[32:35]\n"
"ds_read_b128 v[80:83], v4 offset:16128\n"
"buffer_load_dwordx4 v2, s[32:35], s63 offen offset:2048 sc1 lds\n"
"v_mfma_f32_16x16x32_bf16 v[32:35], a[108:111], v[60:63], v[32:35]\n"
"v_cvt_pk_bf16_f32 v101, v46, v47\n"
"v_mfma_f32_16x16x32_bf16 v[36:39], a[112:115], v[60:63], v[36:39]\n"
"ds_write2st64_b64 v11, v[100:101], v[108:109] offset0:0 offset1:1\n"
"v_fma_f32 v52, -v92, v28, v52\n"
"v_mfma_f32_16x16x32_bf16 v[36:39], a[100:103], v[56:59], v[36:39]\n"
"ds_read_b128 v[84:87], v4 offset:17280\n"
"v_fma_f32 v53, -v93, v28, v53\n"
"v_mfma_f32_16x16x32_bf16 v[40:43], v[56:59], a[104:107], v[40:43]\n"
"v_fma_f32 v54, -v94, v28, v54\n"
"v_fma_f32 v55, -v95, v28, v55\n"
"v_mfma_f32_16x16x32_bf16 v[40:43], v[60:63], a[116:119], v[40:43]\n"
"v_mul_f32 v52, v52, v96\n"
"v_mul_f32 v53, v53, v97\n"
"v_mfma_f32_16x16x32_bf16 v[40:43], v[64:67], a[128:131], v[40:43]\n"
"ds_read_b128 v[56:59], v4 offset:18432\n"
"v_mul_f32 v54, v54, v98\n"
"v_mfma_f32_16x16x32_bf16 v[40:43], v[68:71], a[140:143], v[40:43]\n"
"v_mul_f32 v55, v55, v99\n"
"s_waitcnt lgkmcnt(2)\n"
"v_mfma_f32_16x16x32_bf16 v[36:39], a[136:139], v[68:71], v[36:39]\n"
"buffer_load_dwordx4 v2, s[32:35], s63 offen offset:3072 sc1 lds\n"
"s_barrier\n"
"v_mfma_f32_16x16x32_bf16 v[36:39], a[124:127], v[64:67], v[36:39]\n"
"ds_read_b128 v[60:63], v4 offset:19584\n"
"ds_read_b64 v[102:103], v3\n"
"v_mfma_f32_16x16x32_bf16 v[32:35], a[120:123], v[64:67], v[32:35]\n"
"ds_read_b64 v[110:111], v3 offset:512\n"
"v_cvt_pk_bf16_f32 v114, v52, v53\n"
"v_mfma_f32_16x16x32_bf16 v[32:35], a[132:135], v[68:71], v[32:35]\n"
"v_cvt_pk_bf16_f32 v115, v54, v55\n"
"s_waitcnt lgkmcnt(4)\n"
"v_mfma_f32_16x16x32_bf16 v[32:35], a[144:147], v[72:75], v[32:35]\n"
"ds_read_b128 v[64:67], v4 offset:20736\n"
"v_add_f32 v88, v88, v89\n"
"v_mfma_f32_16x16x32_bf16 v[32:35], a[156:159], v[76:79], v[32:35]\n"
"v_add_f32 v90, v90, v91\n"
"v_mfma_f32_16x16x32_bf16 v[36:39], a[160:163], v[76:79], v[36:39]\n"
"v_add_f32 v88, v88, v90\n"
"v_mfma_f32_16x16x32_bf16 v[36:39], a[148:151], v[72:75], v[36:39]\n"
"ds_read_b128 v[68:71], v4 offset:21888\n"
"s_add_u32 s63, s63, 4096\n"
"v_mfma_f32_16x16x32_bf16 v[40:43], v[72:75], a[152:155], v[40:43]\n"
"s_add_u32 m0, s64, 4096\n"
"s_waitcnt lgkmcnt(2)\n"
"v_mfma_f32_16x16x32_bf16 v[48:51], v[104:107], v[100:103], v[120:123]\n"
"buffer_load_dwordx4 v2, s[32:35], s63 offen offset:0 sc1 lds\n"
"v_add_f32_dpp v88, v88, v88 row_ror:8 row_mask:0xf bank_mask:0xf\n"
"v_mfma_f32_16x16x32_bf16 v[44:47], v[108:111], v[100:103], v[116:119]\n"
"v_mfma_f32_16x16x32_bf16 v[40:43], v[76:79], a[164:167], v[40:43]\n"
"v_add_f32_dpp v88, v88, v88 row_ror:4 row_mask:0xf bank_mask:0xf\n"
"v_mfma_f32_16x16x32_bf16 v[40:43], v[80:83], a[176:179], v[40:43]\n"
"ds_read_b128 v[72:75], v4 offset:23040\n"
"v_add_f32_dpp v88, v88, v88 row_ror:2 row_mask:0xf bank_mask:0xf\n"
"v_mfma_f32_16x16x32_bf16 v[40:43], v[84:87], a[188:191], v[40:43]\n"
"v_mul_f32 v88, 0x3a800000, v88\n"
"v_mfma_f32_16x16x32_bf16 v[36:39], a[184:187], v[84:87], v[36:39]\n"
"v_max3_f32 v44, v44, v45, v48\n"
"v_max3_f32 v46, v46, v47, v50\n"
"v_mfma_f32_16x16x32_bf16 v[36:39], a[172:175], v[80:83], v[36:39]\n"
"ds_read_b128 v[76:79], v4 offset:24192\n"
"v_max_f32 v44, v44, v49\n"
"v_mfma_f32_16x16x32_bf16 v[32:35], a[168:171], v[80:83], v[32:35]\n"
"v_max_f32 v46, v46, v51\n"
"v_mul_f32 v89, v88, v88\n"
"v_mfma_f32_16x16x32_bf16 v[32:35], a[180:183], v[84:87], v[32:35]\n"
"v_max_f32 v96, v44, v46\n"
"s_waitcnt lgkmcnt(2)\n"
"v_mfma_f32_16x16x32_bf16 v[32:35], a[192:195], v[56:59], v[32:35]\n"
"ds_read_b128 v[80:83], v4 offset:25344\n"
"buffer_load_dwordx4 v2, s[32:35], s63 offen offset:1024 sc1 lds\n"
"v_mfma_f32_16x16x32_bf16 v[32:35], a[204:207], v[60:63], v[32:35]\n"
"v_max_f32 v97, v44, v46\n"
"v_sub_f32_dpp v89, v88, v89 quad_perm:[1,1,3,3] row_mask:0xf bank_mask:0xf\n"
"v_mfma_f32_16x16x32_bf16 v[36:39], a[208:211], v[60:63], v[36:39]\n"
"buffer_load_dwordx2 v[12:13], v0, s[32:35], s66 offen sc1\n"
"v_max_f32 v89, 0, v89\n"
"v_mfma_f32_16x16x32_bf16 v[36:39], a[196:199], v[56:59], v[36:39]\n"
"ds_read_b128 v[84:87], v4 offset:26496\n"
"v_cvt_pk_bf16_f32 v89, 0, v89\n"
"v_mfma_f32_16x16x32_bf16 v[40:43], v[56:59], a[200:203], v[40:43]\n"
"v_permlane16_swap_b32 v96, v97\n"
"v_mfma_f32_16x16x32_bf16 v[40:43], v[60:63], a[212:215], v[40:43]\n"
"v_max_f32 v98, v96, v97\n"
"v_max_f32 v99, v96, v97\n"
"v_mfma_f32_16x16x32_bf16 v[40:43], v[64:67], a[224:227], v[40:43]\n"
"ds_read_b128 v[56:59], v4 offset:27648\n"
"v_add_f32 v89, 0x3727c5ac, v89\n"
"v_mfma_f32_16x16x32_bf16 v[40:43], v[68:71], a[236:239], v[40:43]\n"
"v_permlane32_swap_b32 v98, v99\n"
"v_mfma_f32_16x16x32_bf16 v[36:39], a[232:235], v[68:71], v[36:39]\n"
"buffer_load_dwordx4 v2, s[32:35], s63 offen offset:2048 sc1 lds\n"
"v_max_f32 v96, v98, v99\n"
"v_mfma_f32_16x16x32_bf16 v[36:39], a[220:223], v[64:67], v[36:39]\n"
"ds_read_b128 v[60:63], v4 offset:28800\n"
"v_sub_f32 v44, v44, v96\n"
"v_mfma_f32_16x16x32_bf16 v[32:35], a[216:219], v[64:67], v[32:35]\n"
"v_exp_f32 v44, v44\n"
"v_mfma_f32_16x16x32_bf16 v[32:35], a[228:231], v[68:71], v[32:35]\n"
"v_sub_f32 v46, v46, v96\n"
"s_waitcnt lgkmcnt(2)\n"
"v_mfma_f32_16x16x32_bf16 v[32:35], a[240:243], v[72:75], v[32:35]\n"
"ds_read_b128 v[64:67], v4 offset:29952\n"
"v_cvt_pk_bf16_f32 v44, v44, v44\n"
"v_mfma_f32_16x16x32_bf16 v[32:35], a[252:255], v[76:79], v[32:35]\n"
"v_exp_f32 v46, v46\n"
"v_mfma_f32_16x16x32_bf16 v[36:39], v[128:131], v[76:79], v[36:39]\n"
"v_cvt_pk_bf16_f32 v46, v46, v46\n"
"v_and_b32 v94, v44, v124\n"
"v_mfma_f32_16x16x32_bf16 v[36:39], a[244:247], v[72:75], v[36:39]\n"
"ds_read_b128 v[68:71], v4 offset:31104\n"
"v_and_b32 v92, v44, v126\n"
"v_mfma_f32_16x16x32_bf16 v[40:43], v[72:75], a[248:251], v[40:43]\n"
"buffer_load_dwordx4 v2, s[32:35], s63 offen offset:3072 sc1 lds\n"
"v_and_b32 v95, v46, v125\n"
"v_mfma_f32_16x16x32_bf16 v[40:43], v[76:79], v[132:135], v[40:43]\n"
"v_and_b32 v93, v46, v127\n"
"v_cvt_pk_bf16_f32 v89, 0, v89\n"
"v_mfma_f32_16x16x32_bf16 v[40:43], v[80:83], v[144:147], v[40:43]\n"
"ds_read_b128 v[72:75], v4 offset:32256\n"
"v_cvt_pk_bf16_f32 v88, 0, v88\n"
"v_mfma_f32_16x16x32_bf16 v[48:51], v[16:19], v[92:95], 0\n"
"v_sqrt_f32 v89, v89\n"
"v_mfma_f32_16x16x32_bf16 v[52:55], v[112:115], v[92:95], 0\n"
"v_cvt_pk_bf16_f32 v89, 0, v89\n"
"v_mfma_f32_16x16x32_bf16 v[40:43], v[84:87], v[156:159], v[40:43]\n"
"v_rcp_f32 v89, v89\n"
"v_mfma_f32_16x16x32_bf16 v[36:39], v[152:155], v[84:87], v[36:39]\n"
"ds_write2_b32 v8, v88, v89 offset0:0 offset1:16\n"
"v_mfma_f32_16x16x32_bf16 v[36:39], v[140:143], v[80:83], v[36:39]\n"
"ds_read_b128 v[76:79], v4 offset:33408\n"
"v_mfma_f32_16x16x32_bf16 v[32:35], v[136:139], v[80:83], v[32:35]\n"
"v_rcp_f32 v48, v48\n"
"v_mfma_f32_16x16x32_bf16 v[32:35], v[148:151], v[84:87], v[32:35]\n"
"s_add_u32 s63, s63, 4096\n"
"s_waitcnt lgkmcnt(3)\n"
"v_mfma_f32_16x16x32_bf16 v[32:35], v[160:163], v[56:59], v[32:35]\n"
"ds_read_b128 v[80:83], v4 offset:34560\n"
"s_add_u32 m0, s64, 8192\n"
"v_mfma_f32_16x16x32_bf16 v[32:35], v[172:175], v[60:63], v[32:35]\n"
"buffer_load_dwordx4 v2, s[32:35], s63 offen offset:0 sc1 lds\n"
"v_mul_f32 v52, v52, v48\n"
"v_mfma_f32_16x16x32_bf16 v[36:39], v[176:179], v[60:63], v[36:39]\n"
"v_mul_f32 v53, v53, v48\n"
"v_mul_f32 v54, v54, v48\n"
"v_mfma_f32_16x16x32_bf16 v[36:39], v[164:167], v[56:59], v[36:39]\n"
"ds_read_b128 v[84:87], v4 offset:35712\n"
"v_mul_f32 v55, v55, v48\n"
"v_mfma_f32_16x16x32_bf16 v[40:43], v[56:59], v[168:171], v[40:43]\n"
"v_xor_b32 v4, 73728, v4\n"
"v_cvt_pk_bf16_f32 v100, v52, v53\n"
"v_mfma_f32_16x16x32_bf16 v[40:43], v[60:63], v[180:183], v[40:43]\n"
"s_waitcnt vmcnt(10)\n"
"s_waitcnt lgkmcnt(3)\n"
"s_barrier\n"
"v_mfma_f32_16x16x32_bf16 v[40:43], v[64:67], v[192:195], v[40:43]\n"
"ds_read_b128 v[56:59], v5 offset:0\n"
"v_cvt_pk_bf16_f32 v101, v54, v55\n"
"v_mfma_f32_16x16x32_bf16 v[40:43], v[68:71], v[204:207], v[40:43]\n"
"buffer_store_dwordx2 v[30:31], v2, s[92:95], s67 idxen sc1\n"
"v_mfma_f32_16x16x32_bf16 v[36:39], v[200:203], v[68:71], v[36:39]\n"
"v_permlane32_swap_b32 v100, v102\n"
"v_mfma_f32_16x16x32_bf16 v[36:39], v[188:191], v[64:67], v[36:39]\n"
"ds_read_b128 v[60:63], v5 offset:1152\n"
"v_mfma_f32_16x16x32_bf16 v[32:35], v[184:187], v[64:67], v[32:35]\n"
"v_permlane32_swap_b32 v101, v103\n"
"v_mfma_f32_16x16x32_bf16 v[32:35], v[196:199], v[68:71], v[32:35]\n"
"buffer_store_dwordx4 v[100:103], v1, s[32:35], s68 offen sc1\n"
"s_waitcnt lgkmcnt(2)\n"
"v_mfma_f32_16x16x32_bf16 v[32:35], v[208:211], v[72:75], v[32:35]\n"
"ds_read_b128 v[64:67], v5 offset:2304\n"
"s_mov_b32 s67, s68\n"
"v_mfma_f32_16x16x32_bf16 v[32:35], v[220:223], v[76:79], v[32:35]\n"
"v_mfma_f32_16x16x32_bf16 v[36:39], v[224:227], v[76:79], v[36:39]\n"
"v_mfma_f32_16x16x32_bf16 v[36:39], v[212:215], v[72:75], v[36:39]\n"
"ds_read_b128 v[68:71], v5 offset:3456\n"
"v_mfma_f32_16x16x32_bf16 v[40:43], v[72:75], v[216:219], v[40:43]\n"
"v_mfma_f32_16x16x32_bf16 v[40:43], v[76:79], v[228:231], v[40:43]\n"
"v_mfma_f32_16x16x32_bf16 v[40:43], v[80:83], v[240:243], v[40:43]\n"
"ds_read_b128 v[72:75], v5 offset:4608\n"
"v_mfma_f32_16x16x32_bf16 v[40:43], v[84:87], v[252:255], v[40:43]\n"
"v_mfma_f32_16x16x32_bf16 v[36:39], v[248:251], v[84:87], v[36:39]\n"
"v_mfma_f32_16x16x32_bf16 v[36:39], v[236:239], v[80:83], v[36:39]\n"
"ds_read_b128 v[76:79], v5 offset:5760\n"
"v_mfma_f32_16x16x32_bf16 v[32:35], v[232:235], v[80:83], v[32:35]\n"
"v_mfma_f32_16x16x32_bf16 v[32:35], v[244:247], v[84:87], v[32:35]\n"
"s_bitcmp1_b64 s[60:61], s62\n"
"s_cbranch_scc1 .L_qs1\n"
".L_qb1:\n"
"s_waitcnt lgkmcnt(2)\n"
"v_mfma_f32_16x16x32_bf16 v[44:47], a[0:3], v[56:59], 0\n"
"ds_read_b128 v[80:83], v5 offset:6912\n"
"ds_read2_b32 v[14:15], v9 offset0:0 offset1:16\n"
"v_mfma_f32_16x16x32_bf16 v[44:47], a[12:15], v[60:63], v[44:47]\n"
"ds_read_b128 v[92:95], v10 offset:0\n"
"ds_read_b128 v[96:99], v10 offset:64\n"
"v_mfma_f32_16x16x32_bf16 v[48:51], a[16:19], v[60:63], 0\n"
"s_add_u32 s68, s68, 1187840\n"
"v_mfma_f32_16x16x32_bf16 v[48:51], a[4:7], v[56:59], v[48:51]\n"
"ds_read_b128 v[84:87], v5 offset:8064\n"
"v_mfma_f32_16x16x32_bf16 v[52:55], v[56:59], a[8:11], 0\n"
"ds_read2st64_b32 v[88:89], v6 offset0:144 offset1:180\n"
"v_mfma_f32_16x16x32_bf16 v[52:55], v[60:63], a[20:23], v[52:55]\n"
"s_waitcnt vmcnt(5)\n"
"v_cmp_ne_u64 vcc, v[30:31], v[12:13]\n"
"v_mfma_f32_16x16x32_bf16 v[52:55], v[64:67], a[32:35], v[52:55]\n"
"ds_read_b128 v[56:59], v5 offset:9216\n"
"ds_read2st64_b32 v[90:91], v6 offset0:216 offset1:252\n"
"v_mfma_f32_16x16x32_bf16 v[52:55], v[68:71], a[44:47], v[52:55]\n"
"s_cbranch_vccnz .L_rtq1\n"
".L_bkq1:\n"
"s_add_u32 s63, s66, s71\n"
"v_mfma_f32_16x16x32_bf16 v[48:51], a[40:43], v[68:71], v[48:51]\n"
"s_add_u32 s66, s66, s72\n"
"s_add_u32 m0, s64, 36864\n"
"v_mfma_f32_16x16x32_bf16 v[48:51], a[28:31], v[64:67], v[48:51]\n"
"ds_read_b128 v[60:63], v5 offset:10368\n"
"buffer_load_dwordx4 v2, s[32:35], s63 offen offset:0 sc1 lds\n"
"v_mfma_f32_16x16x32_bf16 v[44:47], a[24:27], v[64:67], v[44:47]\n"
"v_xor_b32 v6, v7, v6\n"
"s_waitcnt lgkmcnt(5)\n"
"v_mfma_f32_16x16x32_bf16 v[44:47], a[36:39], v[68:71], v[44:47]\n"
"v_mul_f32 v29, 0x3e8293ee, v15\n"
"s_waitcnt lgkmcnt(4)\n"
"v_mfma_f32_16x16x32_bf16 v[44:47], a[48:51], v[72:75], v[44:47]\n"
"ds_read_b128 v[64:67], v5 offset:11520\n"
"v_fma_f32 v36, -v14, v24, v36\n"
"v_mfma_f32_16x16x32_bf16 v[44:47], a[60:63], v[76:79], v[44:47]\n"
"v_fma_f32 v37, -v14, v25, v37\n"
"v_fma_f32 v38, -v14, v26, v38\n"
"v_mfma_f32_16x16x32_bf16 v[48:51], a[64:67], v[76:79], v[48:51]\n"
"v_fma_f32 v39, -v14, v27, v39\n"
"v_fma_f32 v32, -v14, v20, v32\n"
"v_mfma_f32_16x16x32_bf16 v[48:51], a[52:55], v[72:75], v[48:51]\n"
"ds_read_b128 v[68:71], v5 offset:12672\n"
"v_fma_f32 v33, -v14, v21, v33\n"
"v_mfma_f32_16x16x32_bf16 v[52:55], v[72:75], a[56:59], v[52:55]\n"
"buffer_load_dwordx4 v2, s[32:35], s63 offen offset:1024 sc1 lds\n"
"v_fma_f32 v34, -v14, v22, v34\n"
"v_mfma_f32_16x16x32_bf16 v[52:55], v[76:79], a[68:71], v[52:55]\n"
"v_fma_f32 v35, -v14, v23, v35\n"
"v_mul_f32 v36, v36, v15\n"
"v_mfma_f32_16x16x32_bf16 v[52:55], v[80:83], a[80:83], v[52:55]\n"
"ds_read_b128 v[72:75], v5 offset:13824\n"
"v_mul_f32 v37, v37, v15\n"
"v_mfma_f32_16x16x32_bf16 v[52:55], v[84:87], a[92:95], v[52:55]\n"
"v_mul_f32 v38, v38, v15\n"
"v_mul_f32 v39, v39, v15\n"
"v_mfma_f32_16x16x32_bf16 v[48:51], a[88:91], v[84:87], v[48:51]\n"
"v_mul_f32 v32, v32, v29\n"
"v_mul_f32 v33, v33, v29\n"
"v_mfma_f32_16x16x32_bf16 v[48:51], a[76:79], v[80:83], v[48:51]\n"
"ds_read_b128 v[76:79], v5 offset:14976\n"
"v_mul_f32 v34, v34, v29\n"
"v_mfma_f32_16x16x32_bf16 v[44:47], a[72:75], v[80:83], v[44:47]\n"
"v_mul_f32 v35, v35, v29\n"
"v_cvt_pk_bf16_f32 v104, v36, v37\n"
"v_mfma_f32_16x16x32_bf16 v[44:47], a[84:87], v[84:87], v[44:47]\n"
"v_cvt_pk_bf16_f32 v105, v38, v39\n"
"s_waitcnt lgkmcnt(2)\n"
"v_mfma_f32_16x16x32_bf16 v[44:47], a[96:99], v[56:59], v[44:47]\n"
"ds_read_b128 v[80:83], v5 offset:16128\n"
"buffer_load_dwordx4 v2, s[32:35], s63 offen offset:2048 sc1 lds\n"
"v_mfma_f32_16x16x32_bf16 v[44:47], a[108:111], v[60:63], v[44:47]\n"
"v_cvt_pk_bf16_f32 v100, v32, v33\n"
"v_cvt_pk_bf16_f32 v101, v34, v35\n"
"v_mfma_f32_16x16x32_bf16 v[48:51], a[112:115], v[60:63], v[48:51]\n"
"ds_write2st64_b64 v11, v[100:101], v[104:105] offset0:0 offset1:1\n"
"v_fma_f32 v40, -v92, v28, v40\n"
"v_mfma_f32_16x16x32_bf16 v[48:51], a[100:103], v[56:59], v[48:51]\n"
"ds_read_b128 v[84:87], v5 offset:17280\n"
"v_fma_f32 v41, -v93, v28, v41\n"
"v_mfma_f32_16x16x32_bf16 v[52:55], v[56:59], a[104:107], v[52:55]\n"
"v_fma_f32 v42, -v94, v28, v42\n"
"v_fma_f32 v43, -v95, v28, v43\n"
"v_mfma_f32_16x16x32_bf16 v[52:55], v[60:63], a[116:119], v[52:55]\n"
"v_mul_f32 v40, v40, v96\n"
"v_mul_f32 v41, v41, v97\n"
"v_mfma_f32_16x16x32_bf16 v[52:55], v[64:67], a[128:131], v[52:55]\n"
"ds_read_b128 v[56:59], v5 offset:18432\n"
"v_mul_f32 v42, v42, v98\n"
"v_mfma_f32_16x16x32_bf16 v[52:55], v[68:71], a[140:143], v[52:55]\n"
"v_mul_f32 v43, v43, v99\n"
"s_waitcnt lgkmcnt(2)\n"
"v_mfma_f32_16x16x32_bf16 v[48:51], a[136:139], v[68:71], v[48:51]\n"
"buffer_load_dwordx4 v2, s[32:35], s63 offen offset:3072 sc1 lds\n"
"s_barrier\n"
"v_mfma_f32_16x16x32_bf16 v[48:51], a[124:127], v[64:67], v[48:51]\n"
"ds_read_b128 v[60:63], v5 offset:19584\n"
"ds_read_b64 v[102:103], v3\n"
"v_mfma_f32_16x16x32_bf16 v[44:47], a[120:123], v[64:67], v[44:47]\n"
"ds_read_b64 v[106:107], v3 offset:512\n"
"v_cvt_pk_bf16_f32 v112, v40, v41\n"
"v_mfma_f32_16x16x32_bf16 v[44:47], a[132:135], v[68:71], v[44:47]\n"
"v_cvt_pk_bf16_f32 v113, v42, v43\n"
"s_waitcnt lgkmcnt(4)\n"
"v_mfma_f32_16x16x32_bf16 v[44:47], a[144:147], v[72:75], v[44:47]\n"
"ds_read_b128 v[64:67], v5 offset:20736\n"
"v_add_f32 v88, v88, v89\n"
"v_mfma_f32_16x16x32_bf16 v[44:47], a[156:159], v[76:79], v[44:47]\n"
"v_add_f32 v90, v90, v91\n"
"v_mfma_f32_16x16x32_bf16 v[48:51], a[160:163], v[76:79], v[48:51]\n"
"v_add_f32 v88, v88, v90\n"
"v_mfma_f32_16x16x32_bf16 v[48:51], a[148:151], v[72:75], v[48:51]\n"
"ds_read_b128 v[68:71], v5 offset:21888\n"
"s_add_u32 s63, s63, 4096\n"
"v_mfma_f32_16x16x32_bf16 v[52:55], v[72:75], a[152:155], v[52:55]\n"
"s_add_u32 m0, s64, 40960\n"
"s_waitcnt lgkmcnt(2)\n"
"v_mfma_f32_16x16x32_bf16 v[36:39], v[108:111], v[100:103], v[120:123]\n"
"buffer_load_dwordx4 v2, s[32:35], s63 offen offset:0 sc1 lds\n"
"v_add_f32_dpp v88, v88, v88 row_ror:8 row_mask:0xf bank_mask:0xf\n"
"v_mfma_f32_16x16x32_bf16 v[32:35], v[104:107], v[100:103], v[116:119]\n"
"v_mfma_f32_16x16x32_bf16 v[52:55], v[76:79], a[164:167], v[52:55]\n"
"v_add_f32_dpp v88, v88, v88 row_ror:4 row_mask:0xf bank_mask:0xf\n"
"v_mfma_f32_16x16x32_bf16 v[52:55], v[80:83], a[176:179], v[52:55]\n"
"ds_read_b128 v[72:75], v5 offset:23040\n"
"v_add_f32_dpp v88, v88, v88 row_ror:2 row_mask:0xf bank_mask:0xf\n"
"v_mfma_f32_16x16x32_bf16 v[52:55], v[84:87], a[188:191], v[52:55]\n"
"v_mul_f32 v88, 0x3a800000, v88\n"
"v_mfma_f32_16x16x32_bf16 v[48:51], a[184:187], v[84:87], v[48:51]\n"
"v_max3_f32 v32, v32, v33, v36\n"
"v_max3_f32 v34, v34, v35, v38\n"
"v_mfma_f32_16x16x32_bf16 v[48:51], a[172:175], v[80:83], v[48:51]\n"
"ds_read_b128 v[76:79], v5 offset:24192\n"
"v_max_f32 v32, v32, v37\n"
"v_mfma_f32_16x16x32_bf16 v[44:47], a[168:171], v[80:83], v[44:47]\n"
"v_max_f32 v34, v34, v39\n"
"v_mul_f32 v89, v88, v88\n"
"v_mfma_f32_16x16x32_bf16 v[44:47], a[180:183], v[84:87], v[44:47]\n"
"v_max_f32 v96, v32, v34\n"
"s_waitcnt lgkmcnt(2)\n"
"v_mfma_f32_16x16x32_bf16 v[44:47], a[192:195], v[56:59], v[44:47]\n"
"ds_read_b128 v[80:83], v5 offset:25344\n"
"buffer_load_dwordx4 v2, s[32:35], s63 offen offset:1024 sc1 lds\n"
"v_mfma_f32_16x16x32_bf16 v[44:47], a[204:207], v[60:63], v[44:47]\n"
"v_max_f32 v97, v32, v34\n"
"v_sub_f32_dpp v89, v88, v89 quad_perm:[1,1,3,3] row_mask:0xf bank_mask:0xf\n"
"v_mfma_f32_16x16x32_bf16 v[48:51], a[208:211], v[60:63], v[48:51]\n"
"buffer_load_dwordx2 v[12:13], v0, s[32:35], s66 offen sc1\n"
"v_max_f32 v89, 0, v89\n"
"v_mfma_f32_16x16x32_bf16 v[48:51], a[196:199], v[56:59], v[48:51]\n"
"ds_read_b128 v[84:87], v5 offset:26496\n"
"v_cvt_pk_bf16_f32 v89, 0, v89\n"
"v_mfma_f32_16x16x32_bf16 v[52:55], v[56:59], a[200:203], v[52:55]\n"
"v_permlane16_swap_b32 v96, v97\n"
"v_mfma_f32_16x16x32_bf16 v[52:55], v[60:63], a[212:215], v[52:55]\n"
"v_max_f32 v98, v96, v97\n"
"v_max_f32 v99, v96, v97\n"
"v_mfma_f32_16x16x32_bf16 v[52:55], v[64:67], a[224:227], v[52:55]\n"
"ds_read_b128 v[56:59], v5 offset:27648\n"
"v_add_f32 v89, 0x3727c5ac, v89\n"
"v_mfma_f32_16x16x32_bf16 v[52:55], v[68:71], a[236:239], v[52:55]\n"
"v_permlane32_swap_b32 v98, v99\n"
"v_mfma_f32_16x16x32_bf16 v[48:51], a[232:235], v[68:71], v[48:51]\n"
"buffer_load_dwordx4 v2, s[32:35], s63 offen offset:2048 sc1 lds\n"
"v_max_f32 v96, v98, v99\n"
"v_mfma_f32_16x16x32_bf16 v[48:51], a[220:223], v[64:67], v[48:51]\n"
"ds_read_b128 v[60:63], v5 offset:28800\n"
"v_sub_f32 v32, v32, v96\n"
"v_mfma_f32_16x16x32_bf16 v[44:47], a[216:219], v[64:67], v[44:47]\n"
"v_exp_f32 v32, v32\n"
"v_mfma_f32_16x16x32_bf16 v[44:47], a[228:231], v[68:71], v[44:47]\n"
"v_sub_f32 v34, v34, v96\n"
"s_waitcnt lgkmcnt(2)\n"
"v_mfma_f32_16x16x32_bf16 v[44:47], a[240:243], v[72:75], v[44:47]\n"
"ds_read_b128 v[64:67], v5 offset:29952\n"
"v_cvt_pk_bf16_f32 v32, v32, v32\n"
"v_mfma_f32_16x16x32_bf16 v[44:47], a[252:255], v[76:79], v[44:47]\n"
"v_exp_f32 v34, v34\n"
"v_mfma_f32_16x16x32_bf16 v[48:51], v[128:131], v[76:79], v[48:51]\n"
"v_cvt_pk_bf16_f32 v34, v34, v34\n"
"v_and_b32 v92, v32, v124\n"
"v_mfma_f32_16x16x32_bf16 v[48:51], a[244:247], v[72:75], v[48:51]\n"
"ds_read_b128 v[68:71], v5 offset:31104\n"
"v_and_b32 v94, v32, v126\n"
"v_mfma_f32_16x16x32_bf16 v[52:55], v[72:75], a[248:251], v[52:55]\n"
"buffer_load_dwordx4 v2, s[32:35], s63 offen offset:3072 sc1 lds\n"
"v_and_b32 v93, v34, v125\n"
"v_mfma_f32_16x16x32_bf16 v[52:55], v[76:79], v[132:135], v[52:55]\n"
"v_and_b32 v95, v34, v127\n"
"v_cvt_pk_bf16_f32 v89, 0, v89\n"
"v_mfma_f32_16x16x32_bf16 v[52:55], v[80:83], v[144:147], v[52:55]\n"
"ds_read_b128 v[72:75], v5 offset:32256\n"
"v_cvt_pk_bf16_f32 v88, 0, v88\n"
"v_mfma_f32_16x16x32_bf16 v[36:39], v[16:19], v[92:95], 0\n"
"v_sqrt_f32 v89, v89\n"
"v_mfma_f32_16x16x32_bf16 v[40:43], v[112:115], v[92:95], 0\n"
"v_cvt_pk_bf16_f32 v89, 0, v89\n"
"v_mfma_f32_16x16x32_bf16 v[52:55], v[84:87], v[156:159], v[52:55]\n"
"v_rcp_f32 v89, v89\n"
"v_mfma_f32_16x16x32_bf16 v[48:51], v[152:155], v[84:87], v[48:51]\n"
"ds_write2_b32 v8, v88, v89 offset0:32 offset1:48\n"
"v_mfma_f32_16x16x32_bf16 v[48:51], v[140:143], v[80:83], v[48:51]\n"
"ds_read_b128 v[76:79], v5 offset:33408\n"
"v_mfma_f32_16x16x32_bf16 v[44:47], v[136:139], v[80:83], v[44:47]\n"
"v_rcp_f32 v36, v36\n"
"v_mfma_f32_16x16x32_bf16 v[44:47], v[148:151], v[84:87], v[44:47]\n"
"s_add_u32 s63, s63, 4096\n"
"s_waitcnt lgkmcnt(3)\n"
"v_mfma_f32_16x16x32_bf16 v[44:47], v[160:163], v[56:59], v[44:47]\n"
"ds_read_b128 v[80:83], v5 offset:34560\n"
"s_add_u32 m0, s64, 45056\n"
"v_mfma_f32_16x16x32_bf16 v[44:47], v[172:175], v[60:63], v[44:47]\n"
"buffer_load_dwordx4 v2, s[32:35], s63 offen offset:0 sc1 lds\n"
"s_xor_b32 s64, s64, s65\n"
"v_mfma_f32_16x16x32_bf16 v[48:51], v[176:179], v[60:63], v[48:51]\n"
"v_mul_f32 v40, v40, v36\n"
"v_mul_f32 v41, v41, v36\n"
"v_mfma_f32_16x16x32_bf16 v[48:51], v[164:167], v[56:59], v[48:51]\n"
"ds_read_b128 v[84:87], v5 offset:35712\n"
"v_mul_f32 v42, v42, v36\n"
"v_mfma_f32_16x16x32_bf16 v[52:55], v[56:59], v[168:171], v[52:55]\n"
"v_xor_b32 v5, 73728, v5\n"
"v_mul_f32 v43, v43, v36\n"
"v_mfma_f32_16x16x32_bf16 v[52:55], v[60:63], v[180:183], v[52:55]\n"
"s_waitcnt vmcnt(10)\n"
"s_waitcnt lgkmcnt(3)\n"
"s_barrier\n"
"v_mfma_f32_16x16x32_bf16 v[52:55], v[64:67], v[192:195], v[52:55]\n"
"ds_read_b128 v[56:59], v4 offset:0\n"
"v_cvt_pk_bf16_f32 v100, v40, v41\n"
"v_mfma_f32_16x16x32_bf16 v[52:55], v[68:71], v[204:207], v[52:55]\n"
"buffer_store_dwordx2 v[30:31], v2, s[92:95], s67 idxen sc1\n"
"v_cvt_pk_bf16_f32 v101, v42, v43\n"
"v_mfma_f32_16x16x32_bf16 v[48:51], v[200:203], v[68:71], v[48:51]\n"
"v_permlane32_swap_b32 v100, v102\n"
"v_mfma_f32_16x16x32_bf16 v[48:51], v[188:191], v[64:67], v[48:51]\n"
"ds_read_b128 v[60:63], v4 offset:1152\n"
"v_mfma_f32_16x16x32_bf16 v[44:47], v[184:187], v[64:67], v[44:47]\n"
"v_permlane32_swap_b32 v101, v103\n"
"v_mfma_f32_16x16x32_bf16 v[44:47], v[196:199], v[68:71], v[44:47]\n"
"buffer_store_dwordx4 v[100:103], v1, s[32:35], s68 offen sc1\n"
"s_waitcnt lgkmcnt(2)\n"
"v_mfma_f32_16x16x32_bf16 v[44:47], v[208:211], v[72:75], v[44:47]\n"
"ds_read_b128 v[64:67], v4 offset:2304\n"
"s_mov_b32 s67, s68\n"
"v_mfma_f32_16x16x32_bf16 v[44:47], v[220:223], v[76:79], v[44:47]\n"
"v_mfma_f32_16x16x32_bf16 v[48:51], v[224:227], v[76:79], v[48:51]\n"
"v_mfma_f32_16x16x32_bf16 v[48:51], v[212:215], v[72:75], v[48:51]\n"
"ds_read_b128 v[68:71], v4 offset:3456\n"
"v_mfma_f32_16x16x32_bf16 v[52:55], v[72:75], v[216:219], v[52:55]\n"
"v_mfma_f32_16x16x32_bf16 v[52:55], v[76:79], v[228:231], v[52:55]\n"
"v_mfma_f32_16x16x32_bf16 v[52:55], v[80:83], v[240:243], v[52:55]\n"
"ds_read_b128 v[72:75], v4 offset:4608\n"
"v_mfma_f32_16x16x32_bf16 v[52:55], v[84:87], v[252:255], v[52:55]\n"
"v_mfma_f32_16x16x32_bf16 v[48:51], v[248:251], v[84:87], v[48:51]\n"
"v_mfma_f32_16x16x32_bf16 v[48:51], v[236:239], v[80:83], v[48:51]\n"
"ds_read_b128 v[76:79], v4 offset:5760\n"
"v_mfma_f32_16x16x32_bf16 v[44:47], v[232:235], v[80:83], v[44:47]\n"
"s_sub_u32 s62, s62, 1\n"
"v_mfma_f32_16x16x32_bf16 v[44:47], v[244:247], v[84:87], v[44:47]\n"
"s_cbranch_scc0 .L_ql\n"
"s_nop 1\n"
"ds_read2_b32 v[14:15], v9 offset0:32 offset1:48\n"
"ds_read_b128 v[92:95], v10 offset:128\n"
"ds_read_b128 v[96:99], v10 offset:192\n"
"s_add_u32 s68, s68, 1187840\n"
"s_waitcnt lgkmcnt(0)\n"
"v_mul_f32 v29, 0x3e8293ee, v15\n"
"v_fma_f32 v48, -v14, v24, v48\n"
"v_fma_f32 v49, -v14, v25, v49\n"
"v_fma_f32 v50, -v14, v26, v50\n"
"v_fma_f32 v51, -v14, v27, v51\n"
"v_fma_f32 v44, -v14, v20, v44\n"
"v_fma_f32 v45, -v14, v21, v45\n"
"v_fma_f32 v46, -v14, v22, v46\n"
"v_fma_f32 v47, -v14, v23, v47\n"
"v_mul_f32 v48, v48, v15\n"
"v_mul_f32 v49, v49, v15\n"
"v_mul_f32 v50, v50, v15\n"
"v_mul_f32 v51, v51, v15\n"
"v_mul_f32 v44, v44, v29\n"
"v_mul_f32 v45, v45, v29\n"
"v_mul_f32 v46, v46, v29\n"
"v_mul_f32 v47, v47, v29\n"
"v_cvt_pk_bf16_f32 v108, v48, v49\n"
"v_cvt_pk_bf16_f32 v109, v50, v51\n"
"v_cvt_pk_bf16_f32 v100, v44, v45\n"
"v_cvt_pk_bf16_f32 v101, v46, v47\n"
"ds_write2st64_b64 v11, v[100:101], v[108:109] offset0:0 offset1:1\n"
"v_fma_f32 v52, -v92, v28, v52\n"
"v_fma_f32 v53, -v93, v28, v53\n"
"v_fma_f32 v54, -v94, v28, v54\n"
"v_fma_f32 v55, -v95, v28, v55\n"
"v_mul_f32 v52, v52, v96\n"
"v_mul_f32 v53, v53, v97\n"
"v_mul_f32 v54, v54, v98\n"
"v_mul_f32 v55, v55, v99\n"
"s_waitcnt lgkmcnt(0)\n"
"s_barrier\n"
"ds_read_b64 v[102:103], v3\n"
"ds_read_b64 v[110:111], v3 offset:512\n"
"v_cvt_pk_bf16_f32 v114, v52, v53\n"
"v_cvt_pk_bf16_f32 v115, v54, v55\n"
"s_waitcnt lgkmcnt(0)\n"
"v_mfma_f32_16x16x32_bf16 v[48:51], v[104:107], v[100:103], v[120:123]\n"
"v_mfma_f32_16x16x32_bf16 v[44:47], v[108:111], v[100:103], v[116:119]\n"
"s_nop 7\n"
"v_max3_f32 v44, v44, v45, v48\n"
"v_max3_f32 v46, v46, v47, v50\n"
"v_max_f32 v44, v44, v49\n"
"v_max_f32 v46, v46, v51\n"
"v_max_f32 v96, v44, v46\n"
"v_max_f32 v97, v44, v46\n"
"s_nop 1\n"
"v_permlane16_swap_b32 v96, v97\n"
"v_max_f32 v98, v96, v97\n"
"v_max_f32 v99, v96, v97\n"
"s_nop 1\n"
"v_permlane32_swap_b32 v98, v99\n"
"v_max_f32 v96, v98, v99\n"
"v_sub_f32 v44, v44, v96\n"
"v_sub_f32 v46, v46, v96\n"
"v_exp_f32 v44, v44\n"
"v_exp_f32 v46, v46\n"
"v_cvt_pk_bf16_f32 v44, v44, v44\n"
"v_cvt_pk_bf16_f32 v46, v46, v46\n"
"v_and_b32 v94, v44, v124\n"
"v_and_b32 v92, v44, v126\n"
"v_and_b32 v95, v46, v125\n"
"v_and_b32 v93, v46, v127\n"
"s_nop 1\n"
"v_mfma_f32_16x16x32_bf16 v[48:51], v[16:19], v[92:95], 0\n"
"v_mfma_f32_16x16x32_bf16 v[52:55], v[112:115], v[92:95], 0\n"
"s_nop 6\n"
"v_rcp_f32 v48, v48\n"
"s_nop 0\n"
"v_mul_f32 v52, v52, v48\n"
"v_mul_f32 v53, v53, v48\n"
"v_mul_f32 v54, v54, v48\n"
"v_mul_f32 v55, v55, v48\n"
"v_cvt_pk_bf16_f32 v100, v52, v53\n"
"v_cvt_pk_bf16_f32 v101, v54, v55\n"
"s_nop 0\n"
"v_permlane32_swap_b32 v100, v102\n"
"v_permlane32_swap_b32 v101, v103\n"
"buffer_store_dwordx4 v[100:103], v1, s[32:35], s68 offen sc1\n"
"s_waitcnt vmcnt(1)\n"
"s_barrier\n"
"buffer_store_dwordx2 v[30:31], v2, s[92:95], s67 idxen sc1\n"
"s_mov_b32 s67, s68\n"
"s_waitcnt vmcnt(1)\n"
"s_barrier\n"
"buffer_store_dwordx2 v[30:31], v2, s[92:95], s67 idxen sc1\n"
"s_branch .L_end\n"
".L_w:\n"
"s_mov_b32 s96, 0\n"
"s_add_u32 s36, s12, 0x0\n"
"s_addc_u32 s37, s13, 0x0\n"
"s_and_b32 s37, s37, 0xffff\n"
"s_mov_b32 s38, 0x200000\n"
"s_mov_b32 s39, 0x20000\n"
"s_add_u32 s40, s12, 0xfffffc80\n"
"s_addc_u32 s41, s13, 0xffffffff\n"
"s_and_b32 s41, s41, 0xffff\n"
"s_mov_b32 s42, 0x200380\n"
"s_mov_b32 s43, 0x20000\n"
"s_lshl_b32 s48, s27, 8\n"
"s_add_u32 s96, s96, s48\n"
"v_and_b32 v8, 3, v1\n"
"v_mul_u32_u24 v8, 16, v8\n"
"s_and_b32 s48, s26, 1\n"
"s_lshl_b32 s48, s48, 6\n"
"s_lshr_b32 s49, s26, 1\n"
"s_lshl_b32 s49, s49, 10\n"
"s_add_u32 s48, s48, s49\n"
"v_add_u32 v8, s48, v8\n"
"v_lshrrev_b32 v4, 3, v1\n"
"v_bfe_u32 v5, v1, 4, 1\n"
"s_and_b32 s48, s26, 1\n"
"s_lshl_b32 s48, s48, 1\n"
"v_or_b32 v5, s48, v5\n"
"v_lshlrev_b32 v5, 1, v5\n"
"v_and_b32 v6, 7, v1\n"
"v_xor_b32 v5, v5, v6\n"
"v_mul_u32_u24 v4, 2048, v4\n"
"v_lshl_add_u32 v7, v5, 4, v4\n"
"s_mul_i32 s48, s26, 16384\n"
"s_add_u32 s96, s96, s48\n"
"s_mul_i32 s97, s26, 2048\n"
"s_add_u32 m0, s97, 0\n"
"s_nop 0\n"
"buffer_load_dwordx4 v7, s[36:39], s96 offen offset:0 lds\n"
"buffer_load_dwordx4 v7, s[40:43], s96 offen offset:1024 lds\n"
"s_add_u32 s96, s96, 65536\n"
"s_add_u32 m0, s97, 8192\n"
"s_nop 0\n"
"buffer_load_dwordx4 v7, s[36:39], s96 offen offset:0 lds\n"
"buffer_load_dwordx4 v7, s[40:43], s96 offen offset:1024 lds\n"
"s_add_u32 s96, s96, 65536\n"
"s_add_u32 m0, s97, 16384\n"
"s_nop 0\n"
"buffer_load_dwordx4 v7, s[36:39], s96 offen offset:0 lds\n"
"buffer_load_dwordx4 v7, s[40:43], s96 offen offset:1024 lds\n"
"s_add_u32 s96, s96, 65536\n"
"s_add_u32 m0, s97, 24576\n"
"s_nop 0\n"
"buffer_load_dwordx4 v7, s[36:39], s96 offen offset:0 lds\n"
"buffer_load_dwordx4 v7, s[40:43], s96 offen offset:1024 lds\n"
"s_add_u32 s96, s96, 65536\n"
"s_add_u32 m0, s97, 32768\n"
"s_nop 0\n"
"buffer_load_dwordx4 v7, s[36:39], s96 offen offset:0 lds\n"
"buffer_load_dwordx4 v7, s[40:43], s96 offen offset:1024 lds\n"
"s_add_u32 s96, s96, 65536\n"
"s_add_u32 m0, s97, 40960\n"
"s_nop 0\n"
"buffer_load_dwordx4 v7, s[36:39], s96 offen offset:0 lds\n"
"buffer_load_dwordx4 v7, s[40:43], s96 offen offset:1024 lds\n"
"s_add_u32 s96, s96, 65536\n"
"s_add_u32 m0, s97, 49152\n"
"s_nop 0\n"
"buffer_load_dwordx4 v7, s[36:39], s96 offen offset:0 lds\n"
"buffer_load_dwordx4 v7, s[40:43], s96 offen offset:1024 lds\n"
"s_add_u32 s96, s96, 65536\n"
"s_add_u32 m0, s97, 57344\n"
"s_nop 0\n"
"buffer_load_dwordx4 v7, s[36:39], s96 offen offset:0 lds\n"
"buffer_load_dwordx4 v7, s[40:43], s96 offen offset:1024 lds\n"
"s_add_u32 s96, s96, 65536\n"
"s_add_u32 m0, s97, 65536\n"
"s_nop 0\n"
"buffer_load_dwordx4 v7, s[36:39], s96 offen offset:0 lds\n"
"buffer_load_dwordx4 v7, s[40:43], s96 offen offset:1024 lds\n"
"s_add_u32 s96, s96, 65536\n"
"s_add_u32 m0, s97, 73728\n"
"s_nop 0\n"
"buffer_load_dwordx4 v7, s[36:39], s96 offen offset:0 lds\n"
"buffer_load_dwordx4 v7, s[40:43], s96 offen offset:1024 lds\n"
"s_add_u32 s96, s96, 65536\n"
"s_add_u32 m0, s97, 81920\n"
"s_nop 0\n"
"buffer_load_dwordx4 v7, s[36:39], s96 offen offset:0 lds\n"
"buffer_load_dwordx4 v7, s[40:43], s96 offen offset:1024 lds\n"
"s_add_u32 s96, s96, 65536\n"
"s_add_u32 m0, s97, 90112\n"
"s_nop 0\n"
"buffer_load_dwordx4 v7, s[36:39], s96 offen offset:0 lds\n"
"buffer_load_dwordx4 v7, s[40:43], s96 offen offset:1024 lds\n"
"s_add_u32 s96, s96, 65536\n"
"s_add_u32 m0, s97, 98304\n"
"s_nop 0\n"
"buffer_load_dwordx4 v7, s[36:39], s96 offen offset:0 lds\n"
"buffer_load_dwordx4 v7, s[40:43], s96 offen offset:1024 lds\n"
"s_add_u32 s96, s96, 65536\n"
"s_add_u32 m0, s97, 106496\n"
"s_nop 0\n"
"buffer_load_dwordx4 v7, s[36:39], s96 offen offset:0 lds\n"
"buffer_load_dwordx4 v7, s[40:43], s96 offen offset:1024 lds\n"
"s_add_u32 s96, s96, 65536\n"
"v_lshrrev_b32 v5, 4, v1\n"
"v_bfe_u32 v4, v1, 3, 1\n"
"v_and_b32 v6, 1, v5\n"
"v_lshl_add_u32 v4, v6, 1, v4\n"
"v_lshlrev_b32 v4, 5, v4\n"
"v_mul_u32_u24 v5, 2048, v5\n"
"v_bfe_u32 v6, v1, 2, 2\n"
"v_lshl_add_u32 v5, v6, 7, v5\n"
"v_xor_b32 v8, v4, v8\n"
"v_add_u32 v8, v5, v8\n"
"v_add_u32 v9, 65536, v8\n"
"s_waitcnt vmcnt(24)\n"
"s_barrier\n"
"s_add_u32 m0, s97, 114688\n"
"s_nop 0\n"
"buffer_load_dwordx4 v7, s[36:39], s96 offen offset:0 lds\n"
"buffer_load_dwordx4 v7, s[40:43], s96 offen offset:1024 lds\n"
"s_add_u32 s96, s96, 65536\n"
"s_add_u32 m0, s97, 122880\n"
"s_nop 0\n"
"buffer_load_dwordx4 v7, s[36:39], s96 offen offset:0 lds\n"
"buffer_load_dwordx4 v7, s[40:43], s96 offen offset:1024 lds\n"
"s_add_u32 s96, s96, 65536\n"
"ds_read_b64_tr_b16 a[0:1], v8 offset:0\n"
"ds_read_b64_tr_b16 a[2:3], v8 offset:512\n"
"ds_read_b64_tr_b16 a[4:5], v8 offset:8\n"
"ds_read_b64_tr_b16 a[6:7], v8 offset:520\n"
"ds_read_b64_tr_b16 a[8:9], v8 offset:8192\n"
"ds_read_b64_tr_b16 a[10:11], v8 offset:8704\n"
"ds_read_b64_tr_b16 a[12:13], v8 offset:8200\n"
"ds_read_b64_tr_b16 a[14:15], v8 offset:8712\n"
"s_waitcnt lgkmcnt(0)\n"
"s_waitcnt vmcnt(24)\n"
"s_barrier\n"
"s_add_u32 m0, s97, 0\n"
"s_nop 0\n"
"buffer_load_dwordx4 v7, s[36:39], s96 offen offset:0 lds\n"
"buffer_load_dwordx4 v7, s[40:43], s96 offen offset:1024 lds\n"
"s_add_u32 s96, s96, 65536\n"
"s_add_u32 m0, s97, 8192\n"
"s_nop 0\n"
"buffer_load_dwordx4 v7, s[36:39], s96 offen offset:0 lds\n"
"buffer_load_dwordx4 v7, s[40:43], s96 offen offset:1024 lds\n"
"s_add_u32 s96, s96, 65536\n"
"ds_read_b64_tr_b16 a[16:17], v8 offset:16384\n"
"ds_read_b64_tr_b16 a[18:19], v8 offset:16896\n"
"ds_read_b64_tr_b16 a[20:21], v8 offset:16392\n"
"ds_read_b64_tr_b16 a[22:23], v8 offset:16904\n"
"ds_read_b64_tr_b16 a[24:25], v8 offset:24576\n"
"ds_read_b64_tr_b16 a[26:27], v8 offset:25088\n"
"ds_read_b64_tr_b16 a[28:29], v8 offset:24584\n"
"ds_read_b64_tr_b16 a[30:31], v8 offset:25096\n"
"s_waitcnt lgkmcnt(0)\n"
"s_waitcnt vmcnt(24)\n"
"s_barrier\n"
"s_add_u32 m0, s97, 16384\n"
"s_nop 0\n"
"buffer_load_dwordx4 v7, s[36:39], s96 offen offset:0 lds\n"
"buffer_load_dwordx4 v7, s[40:43], s96 offen offset:1024 lds\n"
"s_add_u32 s96, s96, 65536\n"
"s_add_u32 m0, s97, 24576\n"
"s_nop 0\n"
"buffer_load_dwordx4 v7, s[36:39], s96 offen offset:0 lds\n"
"buffer_load_dwordx4 v7, s[40:43], s96 offen offset:1024 lds\n"
"s_add_u32 s96, s96, 65536\n"
"ds_read_b64_tr_b16 a[32:33], v8 offset:32768\n"
"ds_read_b64_tr_b16 a[34:35], v8 offset:33280\n"
"ds_read_b64_tr_b16 a[36:37], v8 offset:32776\n"
"ds_read_b64_tr_b16 a[38:39], v8 offset:33288\n"
"ds_read_b64_tr_b16 a[40:41], v8 offset:40960\n"
"ds_read_b64_tr_b16 a[42:43], v8 offset:41472\n"
"ds_read_b64_tr_b16 a[44:45], v8 offset:40968\n"
"ds_read_b64_tr_b16 a[46:47], v8 offset:41480\n"
"s_waitcnt lgkmcnt(0)\n"
"s_waitcnt vmcnt(24)\n"
"s_barrier\n"
"s_add_u32 m0, s97, 32768\n"
"s_nop 0\n"
"buffer_load_dwordx4 v7, s[36:39], s96 offen offset:0 lds\n"
"buffer_load_dwordx4 v7, s[40:43], s96 offen offset:1024 lds\n"
"s_add_u32 s96, s96, 65536\n"
"s_add_u32 m0, s97, 40960\n"
"s_nop 0\n"
"buffer_load_dwordx4 v7, s[36:39], s96 offen offset:0 lds\n"
"buffer_load_dwordx4 v7, s[40:43], s96 offen offset:1024 lds\n"
"s_add_u32 s96, s96, 65536\n"
"ds_read_b64_tr_b16 a[48:49], v8 offset:49152\n"
"ds_read_b64_tr_b16 a[50:51], v8 offset:49664\n"
"ds_read_b64_tr_b16 a[52:53], v8 offset:49160\n"
"ds_read_b64_tr_b16 a[54:55], v8 offset:49672\n"
"ds_read_b64_tr_b16 a[56:57], v8 offset:57344\n"
"ds_read_b64_tr_b16 a[58:59], v8 offset:57856\n"
"ds_read_b64_tr_b16 a[60:61], v8 offset:57352\n"
"ds_read_b64_tr_b16 a[62:63], v8 offset:57864\n"
"s_waitcnt lgkmcnt(0)\n"
"s_waitcnt vmcnt(24)\n"
"s_barrier\n"
"s_add_u32 m0, s97, 49152\n"
"s_nop 0\n"
"buffer_load_dwordx4 v7, s[36:39], s96 offen offset:0 lds\n"
"buffer_load_dwordx4 v7, s[40:43], s96 offen offset:1024 lds\n"
"s_add_u32 s96, s96, 65536\n"
"s_add_u32 m0, s97, 57344\n"
"s_nop 0\n"
"buffer_load_dwordx4 v7, s[36:39], s96 offen offset:0 lds\n"
"buffer_load_dwordx4 v7, s[40:43], s96 offen offset:1024 lds\n"
"s_add_u32 s96, s96, 65536\n"
"ds_read_b64_tr_b16 a[64:65], v9 offset:0\n"
"ds_read_b64_tr_b16 a[66:67], v9 offset:512\n"
"ds_read_b64_tr_b16 a[68:69], v9 offset:8\n"
"ds_read_b64_tr_b16 a[70:71], v9 offset:520\n"
"ds_read_b64_tr_b16 a[72:73], v9 offset:8192\n"
"ds_read_b64_tr_b16 a[74:75], v9 offset:8704\n"
"ds_read_b64_tr_b16 a[76:77], v9 offset:8200\n"
"ds_read_b64_tr_b16 a[78:79], v9 offset:8712\n"
"s_waitcnt lgkmcnt(0)\n"
"s_waitcnt vmcnt(24)\n"
"s_barrier\n"
"s_add_u32 m0, s97, 65536\n"
"s_nop 0\n"
"buffer_load_dwordx4 v7, s[36:39], s96 offen offset:0 lds\n"
"buffer_load_dwordx4 v7, s[40:43], s96 offen offset:1024 lds\n"
"s_add_u32 s96, s96, 65536\n"
"s_add_u32 m0, s97, 73728\n"
"s_nop 0\n"
"buffer_load_dwordx4 v7, s[36:39], s96 offen offset:0 lds\n"
"buffer_load_dwordx4 v7, s[40:43], s96 offen offset:1024 lds\n"
"s_add_u32 s96, s96, 65536\n"
"ds_read_b64_tr_b16 a[80:81], v9 offset:16384\n"
"ds_read_b64_tr_b16 a[82:83], v9 offset:16896\n"
"ds_read_b64_tr_b16 a[84:85], v9 offset:16392\n"
"ds_read_b64_tr_b16 a[86:87], v9 offset:16904\n"
"ds_read_b64_tr_b16 a[88:89], v9 offset:24576\n"
"ds_read_b64_tr_b16 a[90:91], v9 offset:25088\n"
"ds_read_b64_tr_b16 a[92:93], v9 offset:24584\n"
"ds_read_b64_tr_b16 a[94:95], v9 offset:25096\n"
"s_waitcnt lgkmcnt(0)\n"
"s_waitcnt vmcnt(24)\n"
"s_barrier\n"
"s_add_u32 m0, s97, 81920\n"
"s_nop 0\n"
"buffer_load_dwordx4 v7, s[36:39], s96 offen offset:0 lds\n"
"buffer_load_dwordx4 v7, s[40:43], s96 offen offset:1024 lds\n"
"s_add_u32 s96, s96, 65536\n"
"s_add_u32 m0, s97, 90112\n"
"s_nop 0\n"
"buffer_load_dwordx4 v7, s[36:39], s96 offen offset:0 lds\n"
"buffer_load_dwordx4 v7, s[40:43], s96 offen offset:1024 lds\n"
"s_add_u32 s96, s96, 65536\n"
"ds_read_b64_tr_b16 a[96:97], v9 offset:32768\n"
"ds_read_b64_tr_b16 a[98:99], v9 offset:33280\n"
"ds_read_b64_tr_b16 a[100:101], v9 offset:32776\n"
"ds_read_b64_tr_b16 a[102:103], v9 offset:33288\n"
"ds_read_b64_tr_b16 a[104:105], v9 offset:40960\n"
"ds_read_b64_tr_b16 a[106:107], v9 offset:41472\n"
"ds_read_b64_tr_b16 a[108:109], v9 offset:40968\n"
"ds_read_b64_tr_b16 a[110:111], v9 offset:41480\n"
"s_waitcnt lgkmcnt(0)\n"
"s_waitcnt vmcnt(24)\n"
"s_barrier\n"
"s_add_u32 m0, s97, 98304\n"
"s_nop 0\n"
"buffer_load_dwordx4 v7, s[36:39], s96 offen offset:0 lds\n"
"buffer_load_dwordx4 v7, s[40:43], s96 offen offset:1024 lds\n"
"s_add_u32 s96, s96, 65536\n"
"s_add_u32 m0, s97, 106496\n"
"s_nop 0\n"
"buffer_load_dwordx4 v7, s[36:39], s96 offen offset:0 lds\n"
"buffer_load_dwordx4 v7, s[40:43], s96 offen offset:1024 lds\n"
"s_add_u32 s96, s96, 65536\n"
"ds_read_b64_tr_b16 a[112:113], v9 offset:49152\n"
"ds_read_b64_tr_b16 a[114:115], v9 offset:49664\n"
"ds_read_b64_tr_b16 a[116:117], v9 offset:49160\n"
"ds_read_b64_tr_b16 a[118:119], v9 offset:49672\n"
"ds_read_b64_tr_b16 a[120:121], v9 offset:57344\n"
"ds_read_b64_tr_b16 a[122:123], v9 offset:57856\n"
"ds_read_b64_tr_b16 a[124:125], v9 offset:57352\n"
"ds_read_b64_tr_b16 a[126:127], v9 offset:57864\n"
"s_waitcnt lgkmcnt(0)\n"
"s_waitcnt vmcnt(24)\n"
"s_barrier\n"
"s_add_u32 m0, s97, 114688\n"
"s_nop 0\n"
"buffer_load_dwordx4 v7, s[36:39], s96 offen offset:0 lds\n"
"buffer_load_dwordx4 v7, s[40:43], s96 offen offset:1024 lds\n"
"s_add_u32 s96, s96, 65536\n"
"s_add_u32 m0, s97, 122880\n"
"s_nop 0\n"
"buffer_load_dwordx4 v7, s[36:39], s96 offen offset:0 lds\n"
"buffer_load_dwordx4 v7, s[40:43], s96 offen offset:1024 lds\n"
"s_add_u32 s96, s96, 65536\n"
"ds_read_b64_tr_b16 a[128:129], v8 offset:0\n"
"ds_read_b64_tr_b16 a[130:131], v8 offset:512\n"
"ds_read_b64_tr_b16 a[132:133], v8 offset:8\n"
"ds_read_b64_tr_b16 a[134:135], v8 offset:520\n"
"ds_read_b64_tr_b16 a[136:137], v8 offset:8192\n"
"ds_read_b64_tr_b16 a[138:139], v8 offset:8704\n"
"ds_read_b64_tr_b16 a[140:141], v8 offset:8200\n"
"ds_read_b64_tr_b16 a[142:143], v8 offset:8712\n"
"s_waitcnt lgkmcnt(0)\n"
"s_waitcnt vmcnt(24)\n"
"s_barrier\n"
"ds_read_b64_tr_b16 a[144:145], v8 offset:16384\n"
"ds_read_b64_tr_b16 a[146:147], v8 offset:16896\n"
"ds_read_b64_tr_b16 a[148:149], v8 offset:16392\n"
"ds_read_b64_tr_b16 a[150:151], v8 offset:16904\n"
"ds_read_b64_tr_b16 a[152:153], v8 offset:24576\n"
"ds_read_b64_tr_b16 a[154:155], v8 offset:25088\n"
"ds_read_b64_tr_b16 a[156:157], v8 offset:24584\n"
"ds_read_b64_tr_b16 a[158:159], v8 offset:25096\n"
"s_waitcnt lgkmcnt(0)\n"
"s_waitcnt vmcnt(20)\n"
"s_barrier\n"
"ds_read_b64_tr_b16 a[160:161], v8 offset:32768\n"
"ds_read_b64_tr_b16 a[162:163], v8 offset:33280\n"
"ds_read_b64_tr_b16 a[164:165], v8 offset:32776\n"
"ds_read_b64_tr_b16 a[166:167], v8 offset:33288\n"
"ds_read_b64_tr_b16 a[168:169], v8 offset:40960\n"
"ds_read_b64_tr_b16 a[170:171], v8 offset:41472\n"
"ds_read_b64_tr_b16 a[172:173], v8 offset:40968\n"
"ds_read_b64_tr_b16 a[174:175], v8 offset:41480\n"
"s_waitcnt lgkmcnt(0)\n"
"s_waitcnt vmcnt(16)\n"
"s_barrier\n"
"ds_read_b64_tr_b16 a[176:177], v8 offset:49152\n"
"ds_read_b64_tr_b16 a[178:179], v8 offset:49664\n"
"ds_read_b64_tr_b16 a[180:181], v8 offset:49160\n"
"ds_read_b64_tr_b16 a[182:183], v8 offset:49672\n"
"ds_read_b64_tr_b16 a[184:185], v8 offset:57344\n"
"ds_read_b64_tr_b16 a[186:187], v8 offset:57856\n"
"ds_read_b64_tr_b16 a[188:189], v8 offset:57352\n"
"ds_read_b64_tr_b16 a[190:191], v8 offset:57864\n"
"s_waitcnt lgkmcnt(0)\n"
"s_waitcnt vmcnt(12)\n"
"s_barrier\n"
"ds_read_b64_tr_b16 a[192:193], v9 offset:0\n"
"ds_read_b64_tr_b16 a[194:195], v9 offset:512\n"
"ds_read_b64_tr_b16 a[196:197], v9 offset:8\n"
"ds_read_b64_tr_b16 a[198:199], v9 offset:520\n"
"ds_read_b64_tr_b16 a[200:201], v9 offset:8192\n"
"ds_read_b64_tr_b16 a[202:203], v9 offset:8704\n"
"ds_read_b64_tr_b16 a[204:205], v9 offset:8200\n"
"ds_read_b64_tr_b16 a[206:207], v9 offset:8712\n"
"s_waitcnt lgkmcnt(0)\n"
"s_waitcnt vmcnt(8)\n"
"s_barrier\n"
"ds_read_b64_tr_b16 a[208:209], v9 offset:16384\n"
"ds_read_b64_tr_b16 a[210:211], v9 offset:16896\n"
"ds_read_b64_tr_b16 a[212:213], v9 offset:16392\n"
"ds_read_b64_tr_b16 a[214:215], v9 offset:16904\n"
"ds_read_b64_tr_b16 a[216:217], v9 offset:24576\n"
"ds_read_b64_tr_b16 a[218:219], v9 offset:25088\n"
"ds_read_b64_tr_b16 a[220:221], v9 offset:24584\n"
"ds_read_b64_tr_b16 a[222:223], v9 offset:25096\n"
"s_waitcnt lgkmcnt(0)\n"
"s_waitcnt vmcnt(4)\n"
"s_barrier\n"
"ds_read_b64_tr_b16 a[224:225], v9 offset:32768\n"
"ds_read_b64_tr_b16 a[226:227], v9 offset:33280\n"
"ds_read_b64_tr_b16 a[228:229], v9 offset:32776\n"
"ds_read_b64_tr_b16 a[230:231], v9 offset:33288\n"
"ds_read_b64_tr_b16 a[232:233], v9 offset:40960\n"
"ds_read_b64_tr_b16 a[234:235], v9 offset:41472\n"
"ds_read_b64_tr_b16 a[236:237], v9 offset:40968\n"
"ds_read_b64_tr_b16 a[238:239], v9 offset:41480\n"
"s_waitcnt lgkmcnt(0)\n"
"s_waitcnt vmcnt(0)\n"
"s_barrier\n"
"ds_read_b64_tr_b16 a[240:241], v9 offset:49152\n"
"ds_read_b64_tr_b16 a[242:243], v9 offset:49664\n"
"ds_read_b64_tr_b16 a[244:245], v9 offset:49160\n"
"ds_read_b64_tr_b16 a[246:247], v9 offset:49672\n"
"ds_read_b64_tr_b16 a[248:249], v9 offset:57344\n"
"ds_read_b64_tr_b16 a[250:251], v9 offset:57856\n"
"ds_read_b64_tr_b16 a[252:253], v9 offset:57352\n"
"ds_read_b64_tr_b16 a[254:255], v9 offset:57864\n"
"s_waitcnt lgkmcnt(0)\n"
"s_barrier\n"
"s_lshl2_add_u32 s50, s27, s26\n"
"s_and_b32 s51, s50, 6\n"
"s_lshl_b32 s51, s51, 1\n"
"v_add_u32 v3, s51, v1\n"
"v_bfi_b32 v3, 15, v3, v1\n"
"v_lshlrev_b32 v3, 2, v3\n"
"v_cmp_gt_u32 vcc, 32, v1\n"
"v_mov_b32 v15, 0x7fff0000\n"
"s_nop 0\n"
"v_cndmask_b32 v15, v15, v3, vcc\n"
"s_mul_i32 s48, s50, 1152\n"
"s_add_u32 s68, s48, 4681728\n"
"s_lshl2_add_u32 s49, s27, 96\n"
"s_waitcnt lgkmcnt(0)\n"
"s_add_u32 s54, s52, 0x7f4a7c15\n"
"s_addc_u32 s55, s53, 0x9e3779b9\n"
"s_lshl_b32 s67, s49, 5\n"
"s_add_u32 s67, s67, 4898816\n"
"s_sub_u32 s51, s67, s68\n"
"s_add_u32 s92, s32, s51\n"
"s_addc_u32 s93, s33, 0x10000\n"
"s_sub_u32 s92, s92, 593920\n"
"s_subb_u32 s93, s93, 0\n"
"s_cmp_eq_u32 s26, 0\n"
"s_cselect_b32 s94, 1, 0\n"
"s_mov_b32 s95, 0x20000\n"
"v_mov_b32 v16, s54\n"
"v_mov_b32 v17, s55\n"
"v_cmp_gt_u32 vcc, 12, v1\n"
"v_mul_u32_u24 v3, 296960, v1\n"
"v_mov_b32 v12, 0x7fff0000\n"
"v_cndmask_b32 v3, v12, v3, vcc\n"
"s_add_u32 s51, s67, 76021760\n"
"buffer_store_dwordx2 v[16:17], v3, s[32:35], s51 offen sc1\n"
"s_sub_u32 s67, s68, 296960\n"
"s_mul_i32 s49, s28, 296960\n"
"s_add_u32 s48, s48, s49\n"
"s_add_u32 s71, s48, 4612096\n"
"s_add_u32 s68, s48, 3493888\n"
"s_mov_b32 s62, 63\n"
"s_add_u32 s63, s49, 4648960\n"
"s_add_u32 s66, s49, 4898816\n"
"s_mul_i32 s64, s26, 8192\n"
"s_add_u32 s65, s64, 65536\n"
"s_xor_b32 s65, s65, s64\n"
"s_add_u32 s63, s63, s64\n"
"v_mov_b32 v4, v2\n"
"v_add_u32 v6, 4096, v2\n"
"v_and_b32 v14, 15, v1\n"
"v_lshlrev_b32 v14, 2, v14\n"
"s_mov_b32 s48, 32\n"
"v_add_u32 v14, s48, v14\n"
"v_lshrrev_b32 v14, 2, v14\n"
"v_lshlrev_b32 v14, 7, v14\n"
"v_lshrrev_b32 v3, 4, v1\n"
"v_mul_u32_u24 v3, 593920, v3\n"
"v_add_u32 v14, v3, v14\n"
"v_cmp_gt_u32 vcc, 32, v1\n"
"v_mov_b32 v3, 0x7fff0000\n"
"s_nop 0\n"
"v_cndmask_b32 v14, v3, v14, vcc\n"
"v_cndmask_b32 v16, 0, v16, vcc\n"
"v_cndmask_b32 v17, 0, v17, vcc\n"
"s_mov_b32 s48, 20000\n"
"s_branch .L_rfwa\n"
".L_rlwa:\n"
"s_sleep 8\n"
".L_rfwa:\n"
"buffer_load_dwordx2 v[12:13], v14, s[32:35], s66 offen sc1\n"
"s_sub_u32 s48, s48, 1\n"
"s_cbranch_scc1 .L_end\n"
"s_waitcnt vmcnt(0)\n"
"v_cmp_ne_u64 vcc, v[16:17], v[12:13]\n"
"s_cbranch_vccnz .L_rlwa\n"
"s_add_u32 s66, s66, 1187840\n"
"s_add_u32 m0, s64, 0\n"
"s_nop 0\n"
"buffer_load_dwordx4 v2, s[32:35], s63 offen offset:0 sc1 lds\n"
"buffer_load_dwordx4 v2, s[32:35], s63 offen offset:1024 sc1 lds\n"
"buffer_load_dwordx4 v2, s[32:35], s63 offen offset:2048 sc1 lds\n"
"buffer_load_dwordx4 v2, s[32:35], s63 offen offset:3072 sc1 lds\n"
"s_add_u32 m0, s64, 4096\n"
"s_nop 0\n"
"buffer_load_dwordx4 v6, s[32:35], s63 offen offset:0 sc1 lds\n"
"buffer_load_dwordx4 v6, s[32:35], s63 offen offset:1024 sc1 lds\n"
"buffer_load_dwordx4 v6, s[32:35], s63 offen offset:2048 sc1 lds\n"
"buffer_load_dwordx4 v6, s[32:35], s63 offen offset:3072 sc1 lds\n"
"s_add_u32 s63, s63, 593920\n"
"buffer_load_dwordx4 v[48:51], v2, s[32:35], s71 offen sc1\n"
"s_add_u32 s71, s71, 593920\n"
"buffer_load_dwordx4 v[52:55], v2, s[32:35], s71 offen sc1\n"
"s_add_u32 s71, s71, 593920\n"
"s_add_u32 m0, s64, 32768\n"
"s_nop 0\n"
"buffer_load_dwordx4 v2, s[32:35], s63 offen offset:0 sc1 lds\n"
"buffer_load_dwordx4 v2, s[32:35], s63 offen offset:1024 sc1 lds\n"
"buffer_load_dwordx4 v2, s[32:35], s63 offen offset:2048 sc1 lds\n"
"buffer_load_dwordx4 v2, s[32:35], s63 offen offset:3072 sc1 lds\n"
"s_add_u32 m0, s64, 36864\n"
"s_nop 0\n"
"buffer_load_dwordx4 v6, s[32:35], s63 offen offset:0 sc1 lds\n"
"buffer_load_dwordx4 v6, s[32:35], s63 offen offset:1024 sc1 lds\n"
"buffer_load_dwordx4 v6, s[32:35], s63 offen offset:2048 sc1 lds\n"
"buffer_load_dwordx4 v6, s[32:35], s63 offen offset:3072 sc1 lds\n"
"s_add_u32 s63, s63, 593920\n"
"s_xor_b32 s64, s64, s65\n"
"s_mov_b32 s48, 20000\n"
"s_branch .L_rfwc\n"
".L_rlwc:\n"
"s_sleep 8\n"
".L_rfwc:\n"
"buffer_load_dwordx2 v[12:13], v14, s[32:35], s66 offen sc1\n"
"s_sub_u32 s48, s48, 1\n"
"s_cbranch_scc1 .L_end\n"
"s_waitcnt vmcnt(0)\n"
"v_cmp_ne_u64 vcc, v[16:17], v[12:13]\n"
"s_cbranch_vccnz .L_rlwc\n"
"s_add_u32 s66, s66, 1187840\n"
"s_add_u32 m0, s64, 0\n"
"s_nop 0\n"
"buffer_load_dwordx4 v2, s[32:35], s63 offen offset:0 sc1 lds\n"
"buffer_load_dwordx4 v2, s[32:35], s63 offen offset:1024 sc1 lds\n"
"buffer_load_dwordx4 v2, s[32:35], s63 offen offset:2048 sc1 lds\n"
"buffer_load_dwordx4 v2, s[32:35], s63 offen offset:3072 sc1 lds\n"
"s_add_u32 m0, s64, 4096\n"
"s_nop 0\n"
"buffer_load_dwordx4 v6, s[32:35], s63 offen offset:0 sc1 lds\n"
"s_barrier\n"
"ds_read_b128 v[128:131], v4 offset:0\n"
"ds_read_b128 v[132:135], v4 offset:1024\n"
"ds_read_b128 v[136:139], v4 offset:2048\n"
"ds_read_b128 v[140:143], v4 offset:3072\n"
"ds_read_b128 v[144:147], v4 offset:4096\n"
"ds_read_b128 v[148:151], v4 offset:5120\n"
"ds_read_b128 v[152:155], v4 offset:6144\n"
"ds_read_b128 v[156:159], v4 offset:7168\n"
"ds_read_b128 v[160:163], v4 offset:8192\n"
"ds_read_b128 v[164:167], v4 offset:9216\n"
"ds_read_b128 v[168:171], v4 offset:10240\n"
"ds_read_b128 v[172:175], v4 offset:11264\n"
"v_lshlrev_b32 v32, 16, v48\n"
"v_and_b32 v33, 0xffff0000, v48\n"
"v_lshlrev_b32 v34, 16, v49\n"
"v_and_b32 v35, 0xffff0000, v49\n"
"v_lshlrev_b32 v36, 16, v50\n"
"v_and_b32 v37, 0xffff0000, v50\n"
"v_lshlrev_b32 v38, 16, v51\n"
"v_and_b32 v39, 0xffff0000, v51\n"
"s_cmp_eq_u32 s26, 1\n"
"s_cbranch_scc1 .L_wv1\n"
"s_cmp_eq_u32 s26, 2\n"
"s_cbranch_scc1 .L_wv2\n"
"s_cmp_eq_u32 s26, 3\n"
"s_cbranch_scc1 .L_wv3\n"
".p2align 6\n"
".L_wv0:\n"
".L_wl_0:\n"
"s_waitcnt lgkmcnt(4)\n"
"v_mfma_f32_16x16x32_bf16 v[32:35], a[0:3], v[128:131], v[32:35]\n"
"ds_read_b128 v[176:179], v4 offset:12288\n"
"v_mfma_f32_16x16x32_bf16 v[32:35], a[8:11], v[132:135], v[32:35]\n"
"buffer_load_dwordx4 v6, s[32:35], s63 offen offset:1024 sc1 lds\n"
"v_mfma_f32_16x16x32_bf16 v[32:35], a[16:19], v[136:139], v[32:35]\n"
"ds_read_b128 v[180:183], v4 offset:13312\n"
"s_add_u32 s68, s68, 593920\n"
"v_mfma_f32_16x16x32_bf16 v[32:35], a[24:27], v[140:143], v[32:35]\n"
"v_cvt_pk_bf16_f32 v56, v40, v41\n"
"v_cvt_pk_bf16_f32 v57, v42, v43\n"
"v_mfma_f32_16x16x32_bf16 v[36:39], a[28:31], v[140:143], v[36:39]\n"
"ds_read_b128 v[184:187], v4 offset:14336\n"
"v_cvt_pk_bf16_f32 v58, v44, v45\n"
"v_mfma_f32_16x16x32_bf16 v[36:39], a[20:23], v[136:139], v[36:39]\n"
"v_cvt_pk_bf16_f32 v59, v46, v47\n"
"v_add_f32 v60, v40, v41\n"
"v_mfma_f32_16x16x32_bf16 v[36:39], a[12:15], v[132:135], v[36:39]\n"
"ds_read_b128 v[188:191], v4 offset:15360\n"
"v_mul_f32 v61, v40, v40\n"
"v_mfma_f32_16x16x32_bf16 v[36:39], a[4:7], v[128:131], v[36:39]\n"
"v_fmac_f32 v61, v41, v41\n"
"v_add_f32 v60, v60, v42\n"
"v_mfma_f32_16x16x32_bf16 v[36:39], a[36:39], v[144:147], v[36:39]\n"
"ds_read_b128 v[128:131], v4 offset:16384\n"
"v_fmac_f32 v61, v42, v42\n"
"v_mfma_f32_16x16x32_bf16 v[36:39], a[44:47], v[148:151], v[36:39]\n"
"buffer_store_dwordx4 v[56:59], v2, s[32:35], s68 offen sc1\n"
"v_mfma_f32_16x16x32_bf16 v[36:39], a[52:55], v[152:155], v[36:39]\n"
"ds_read_b128 v[132:135], v4 offset:17408\n"
"v_add_f32 v60, v60, v43\n"
"v_mfma_f32_16x16x32_bf16 v[36:39], a[60:63], v[156:159], v[36:39]\n"
"v_fmac_f32 v61, v43, v43\n"
"v_add_f32 v60, v60, v44\n"
"v_mfma_f32_16x16x32_bf16 v[32:35], a[56:59], v[156:159], v[32:35]\n"
"ds_read_b128 v[136:139], v4 offset:18432\n"
"v_fmac_f32 v61, v44, v44\n"
"v_mfma_f32_16x16x32_bf16 v[32:35], a[48:51], v[152:155], v[32:35]\n"
"v_add_f32 v60, v60, v45\n"
"v_fmac_f32 v61, v45, v45\n"
"v_mfma_f32_16x16x32_bf16 v[32:35], a[40:43], v[148:151], v[32:35]\n"
"ds_read_b128 v[140:143], v4 offset:19456\n"
"v_add_f32 v60, v60, v46\n"
"v_mfma_f32_16x16x32_bf16 v[32:35], a[32:35], v[144:147], v[32:35]\n"
"v_fmac_f32 v61, v46, v46\n"
"s_waitcnt lgkmcnt(4)\n"
"v_mfma_f32_16x16x32_bf16 v[32:35], a[64:67], v[160:163], v[32:35]\n"
"ds_read_b128 v[144:147], v4 offset:20480\n"
"v_add_f32 v60, v60, v47\n"
"v_mfma_f32_16x16x32_bf16 v[32:35], a[72:75], v[164:167], v[32:35]\n"
"v_fmac_f32 v61, v47, v47\n"
"v_mfma_f32_16x16x32_bf16 v[32:35], a[80:83], v[168:171], v[32:35]\n"
"ds_read_b128 v[148:151], v4 offset:21504\n"
"v_mfma_f32_16x16x32_bf16 v[32:35], a[88:91], v[172:175], v[32:35]\n"
"v_permlane16_swap_b32 v60, v61\n"
"v_mfma_f32_16x16x32_bf16 v[36:39], a[92:95], v[172:175], v[36:39]\n"
"ds_read_b128 v[152:155], v4 offset:22528\n"
"v_add_f32 v60, v60, v61\n"
"v_mfma_f32_16x16x32_bf16 v[36:39], a[84:87], v[168:171], v[36:39]\n"
"buffer_load_dwordx4 v[48:51], v2, s[32:35], s71 offen sc1\n"
"v_mfma_f32_16x16x32_bf16 v[36:39], a[76:79], v[164:167], v[36:39]\n"
"ds_read_b128 v[156:159], v4 offset:23552\n"
"s_add_u32 s71, s71, 593920\n"
"v_mfma_f32_16x16x32_bf16 v[36:39], a[68:71], v[160:163], v[36:39]\n"
"v_permlane32_swap_b32 v60, v61\n"
"v_mfma_f32_16x16x32_bf16 v[36:39], a[100:103], v[176:179], v[36:39]\n"
"ds_read_b128 v[160:163], v4 offset:24576\n"
"v_add_f32 v60, v60, v61\n"
"v_mfma_f32_16x16x32_bf16 v[36:39], a[108:111], v[180:183], v[36:39]\n"
"buffer_store_dword v60, v15, s[32:35], s68 offen offset:1024 sc1\n"
"v_mfma_f32_16x16x32_bf16 v[36:39], a[116:119], v[184:187], v[36:39]\n"
"ds_read_b128 v[164:167], v4 offset:25600\n"
"v_mfma_f32_16x16x32_bf16 v[36:39], a[124:127], v[188:191], v[36:39]\n"
"v_mfma_f32_16x16x32_bf16 v[32:35], a[120:123], v[188:191], v[32:35]\n"
"ds_read_b128 v[168:171], v4 offset:26624\n"
"v_mfma_f32_16x16x32_bf16 v[32:35], a[112:115], v[184:187], v[32:35]\n"
"buffer_load_dwordx4 v6, s[32:35], s63 offen offset:2048 sc1 lds\n"
"v_mfma_f32_16x16x32_bf16 v[32:35], a[104:107], v[180:183], v[32:35]\n"
"ds_read_b128 v[172:175], v4 offset:27648\n"
"v_mfma_f32_16x16x32_bf16 v[32:35], a[96:99], v[176:179], v[32:35]\n"
"s_waitcnt lgkmcnt(4)\n"
"v_mfma_f32_16x16x32_bf16 v[32:35], a[128:131], v[128:131], v[32:35]\n"
"ds_read_b128 v[176:179], v4 offset:28672\n"
"v_mfma_f32_16x16x32_bf16 v[32:35], a[136:139], v[132:135], v[32:35]\n"
"buffer_load_dwordx4 v6, s[32:35], s63 offen offset:3072 sc1 lds\n"
"v_mfma_f32_16x16x32_bf16 v[32:35], a[144:147], v[136:139], v[32:35]\n"
"ds_read_b128 v[180:183], v4 offset:29696\n"
"s_add_u32 s63, s63, 593920\n"
"v_mfma_f32_16x16x32_bf16 v[32:35], a[152:155], v[140:143], v[32:35]\n"
"v_mfma_f32_16x16x32_bf16 v[36:39], a[156:159], v[140:143], v[36:39]\n"
"ds_read_b128 v[184:187], v4 offset:30720\n"
"v_mfma_f32_16x16x32_bf16 v[36:39], a[148:151], v[136:139], v[36:39]\n"
"v_mfma_f32_16x16x32_bf16 v[36:39], a[140:143], v[132:135], v[36:39]\n"
"ds_read_b128 v[188:191], v4 offset:31744\n"
"v_mfma_f32_16x16x32_bf16 v[36:39], a[132:135], v[128:131], v[36:39]\n"
"s_waitcnt vmcnt(12)\n"
"s_barrier\n"
"v_mfma_f32_16x16x32_bf16 v[36:39], a[164:167], v[144:147], v[36:39]\n"
"ds_read_b128 v[128:131], v4 offset:32768\n"
"v_lshlrev_b32 v40, 16, v52\n"
"v_mfma_f32_16x16x32_bf16 v[36:39], a[172:175], v[148:151], v[36:39]\n"
"buffer_store_dwordx2 v[16:17], v2, s[92:95], s68 idxen sc1\n"
"v_mfma_f32_16x16x32_bf16 v[36:39], a[180:183], v[152:155], v[36:39]\n"
"ds_read_b128 v[132:135], v4 offset:33792\n"
"v_and_b32 v41, 0xffff0000, v52\n"
"v_mfma_f32_16x16x32_bf16 v[36:39], a[188:191], v[156:159], v[36:39]\n"
"v_lshlrev_b32 v42, 16, v53\n"
"v_and_b32 v43, 0xffff0000, v53\n"
"v_mfma_f32_16x16x32_bf16 v[32:35], a[184:187], v[156:159], v[32:35]\n"
"ds_read_b128 v[136:139], v4 offset:34816\n"
"s_add_u32 m0, s64, 32768\n"
"v_mfma_f32_16x16x32_bf16 v[32:35], a[176:179], v[152:155], v[32:35]\n"
"buffer_load_dwordx4 v2, s[32:35], s63 offen offset:0 sc1 lds\n"
"v_mfma_f32_16x16x32_bf16 v[32:35], a[168:171], v[148:151], v[32:35]\n"
"ds_read_b128 v[140:143], v4 offset:35840\n"
"v_lshlrev_b32 v44, 16, v54\n"
"v_mfma_f32_16x16x32_bf16 v[32:35], a[160:163], v[144:147], v[32:35]\n"
"v_and_b32 v45, 0xffff0000, v54\n"
"s_waitcnt lgkmcnt(4)\n"
"v_mfma_f32_16x16x32_bf16 v[32:35], a[192:195], v[160:163], v[32:35]\n"
"ds_read_b128 v[144:147], v4 offset:36864\n"
"v_lshlrev_b32 v46, 16, v55\n"
"v_mfma_f32_16x16x32_bf16 v[32:35], a[200:203], v[164:167], v[32:35]\n"
"buffer_load_dwordx4 v2, s[32:35], s63 offen offset:1024 sc1 lds\n"
"v_mfma_f32_16x16x32_bf16 v[32:35], a[208:211], v[168:171], v[32:35]\n"
"ds_read_b128 v[148:151], v4 offset:37888\n"
"v_and_b32 v47, 0xffff0000, v55\n"
"v_mfma_f32_16x16x32_bf16 v[32:35], a[216:219], v[172:175], v[32:35]\n"
"v_mfma_f32_16x16x32_bf16 v[36:39], a[220:223], v[172:175], v[36:39]\n"
"ds_read_b128 v[152:155], v4 offset:38912\n"
"v_mfma_f32_16x16x32_bf16 v[36:39], a[212:215], v[168:171], v[36:39]\n"
"buffer_load_dwordx4 v2, s[32:35], s63 offen offset:2048 sc1 lds\n"
"v_mfma_f32_16x16x32_bf16 v[36:39], a[204:207], v[164:167], v[36:39]\n"
"ds_read_b128 v[156:159], v4 offset:39936\n"
"v_mfma_f32_16x16x32_bf16 v[36:39], a[196:199], v[160:163], v[36:39]\n"
"v_mfma_f32_16x16x32_bf16 v[36:39], a[228:231], v[176:179], v[36:39]\n"
"ds_read_b128 v[160:163], v4 offset:40960\n"
"v_mfma_f32_16x16x32_bf16 v[36:39], a[236:239], v[180:183], v[36:39]\n"
"buffer_load_dwordx4 v2, s[32:35], s63 offen offset:3072 sc1 lds\n"
"v_mfma_f32_16x16x32_bf16 v[36:39], a[244:247], v[184:187], v[36:39]\n"
"ds_read_b128 v[164:167], v4 offset:41984\n"
"v_mfma_f32_16x16x32_bf16 v[36:39], a[252:255], v[188:191], v[36:39]\n"
"v_mfma_f32_16x16x32_bf16 v[32:35], a[248:251], v[188:191], v[32:35]\n"
"ds_read_b128 v[168:171], v4 offset:43008\n"
"s_add_u32 m0, s64, 36864\n"
"v_mfma_f32_16x16x32_bf16 v[32:35], a[240:243], v[184:187], v[32:35]\n"
"buffer_load_dwordx4 v6, s[32:35], s63 offen offset:0 sc1 lds\n"
"v_mfma_f32_16x16x32_bf16 v[32:35], a[232:235], v[180:183], v[32:35]\n"
"ds_read_b128 v[172:175], v4 offset:44032\n"
"v_mfma_f32_16x16x32_bf16 v[32:35], a[224:227], v[176:179], v[32:35]\n"
"s_waitcnt lgkmcnt(4)\n"
"v_mfma_f32_16x16x32_bf16 v[40:43], a[0:3], v[128:131], v[40:43]\n"
"ds_read_b128 v[176:179], v4 offset:45056\n"
"v_mfma_f32_16x16x32_bf16 v[40:43], a[8:11], v[132:135], v[40:43]\n"
"buffer_load_dwordx4 v6, s[32:35], s63 offen offset:1024 sc1 lds\n"
"v_mfma_f32_16x16x32_bf16 v[40:43], a[16:19], v[136:139], v[40:43]\n"
"ds_read_b128 v[180:183], v4 offset:46080\n"
"s_add_u32 s68, s68, 593920\n"
"v_mfma_f32_16x16x32_bf16 v[40:43], a[24:27], v[140:143], v[40:43]\n"
"v_cvt_pk_bf16_f32 v56, v32, v33\n"
"v_cvt_pk_bf16_f32 v57, v34, v35\n"
"v_mfma_f32_16x16x32_bf16 v[44:47], a[28:31], v[140:143], v[44:47]\n"
"ds_read_b128 v[184:187], v4 offset:47104\n"
"v_cvt_pk_bf16_f32 v58, v36, v37\n"
"v_mfma_f32_16x16x32_bf16 v[44:47], a[20:23], v[136:139], v[44:47]\n"
"v_cvt_pk_bf16_f32 v59, v38, v39\n"
"v_add_f32 v60, v32, v33\n"
"v_mfma_f32_16x16x32_bf16 v[44:47], a[12:15], v[132:135], v[44:47]\n"
"ds_read_b128 v[188:191], v4 offset:48128\n"
"v_mul_f32 v61, v32, v32\n"
"v_mfma_f32_16x16x32_bf16 v[44:47], a[4:7], v[128:131], v[44:47]\n"
"v_fmac_f32 v61, v33, v33\n"
"v_add_f32 v60, v60, v34\n"
"v_mfma_f32_16x16x32_bf16 v[44:47], a[36:39], v[144:147], v[44:47]\n"
"ds_read_b128 v[128:131], v4 offset:49152\n"
"v_fmac_f32 v61, v34, v34\n"
"v_mfma_f32_16x16x32_bf16 v[44:47], a[44:47], v[148:151], v[44:47]\n"
"buffer_store_dwordx4 v[56:59], v2, s[32:35], s68 offen sc1\n"
"v_mfma_f32_16x16x32_bf16 v[44:47], a[52:55], v[152:155], v[44:47]\n"
"ds_read_b128 v[132:135], v4 offset:50176\n"
"v_add_f32 v60, v60, v35\n"
"v_mfma_f32_16x16x32_bf16 v[44:47], a[60:63], v[156:159], v[44:47]\n"
"v_fmac_f32 v61, v35, v35\n"
"v_add_f32 v60, v60, v36\n"
"v_mfma_f32_16x16x32_bf16 v[40:43], a[56:59], v[156:159], v[40:43]\n"
"ds_read_b128 v[136:139], v4 offset:51200\n"
"v_fmac_f32 v61, v36, v36\n"
"v_mfma_f32_16x16x32_bf16 v[40:43], a[48:51], v[152:155], v[40:43]\n"
"v_add_f32 v60, v60, v37\n"
"v_fmac_f32 v61, v37, v37\n"
"v_mfma_f32_16x16x32_bf16 v[40:43], a[40:43], v[148:151], v[40:43]\n"
"ds_read_b128 v[140:143], v4 offset:52224\n"
"v_add_f32 v60, v60, v38\n"
"v_mfma_f32_16x16x32_bf16 v[40:43], a[32:35], v[144:147], v[40:43]\n"
"v_fmac_f32 v61, v38, v38\n"
"s_waitcnt lgkmcnt(4)\n"
"v_mfma_f32_16x16x32_bf16 v[40:43], a[64:67], v[160:163], v[40:43]\n"
"ds_read_b128 v[144:147], v4 offset:53248\n"
"v_add_f32 v60, v60, v39\n"
"v_mfma_f32_16x16x32_bf16 v[40:43], a[72:75], v[164:167], v[40:43]\n"
"v_fmac_f32 v61, v39, v39\n"
"v_mfma_f32_16x16x32_bf16 v[40:43], a[80:83], v[168:171], v[40:43]\n"
"ds_read_b128 v[148:151], v4 offset:54272\n"
"v_mfma_f32_16x16x32_bf16 v[40:43], a[88:91], v[172:175], v[40:43]\n"
"v_permlane16_swap_b32 v60, v61\n"
"v_mfma_f32_16x16x32_bf16 v[44:47], a[92:95], v[172:175], v[44:47]\n"
"ds_read_b128 v[152:155], v4 offset:55296\n"
"v_add_f32 v60, v60, v61\n"
"v_mfma_f32_16x16x32_bf16 v[44:47], a[84:87], v[168:171], v[44:47]\n"
"buffer_load_dwordx4 v[52:55], v2, s[32:35], s71 offen sc1\n"
"v_mfma_f32_16x16x32_bf16 v[44:47], a[76:79], v[164:167], v[44:47]\n"
"ds_read_b128 v[156:159], v4 offset:56320\n"
"s_add_u32 s71, s71, 593920\n"
"v_mfma_f32_16x16x32_bf16 v[44:47], a[68:71], v[160:163], v[44:47]\n"
"v_permlane32_swap_b32 v60, v61\n"
"v_mfma_f32_16x16x32_bf16 v[44:47], a[100:103], v[176:179], v[44:47]\n"
"ds_read_b128 v[160:163], v4 offset:57344\n"
"v_add_f32 v60, v60, v61\n"
"v_mfma_f32_16x16x32_bf16 v[44:47], a[108:111], v[180:183], v[44:47]\n"
"buffer_store_dword v60, v15, s[32:35], s68 offen offset:1024 sc1\n"
"v_mfma_f32_16x16x32_bf16 v[44:47], a[116:119], v[184:187], v[44:47]\n"
"ds_read_b128 v[164:167], v4 offset:58368\n"
"v_mfma_f32_16x16x32_bf16 v[44:47], a[124:127], v[188:191], v[44:47]\n"
"v_mfma_f32_16x16x32_bf16 v[40:43], a[120:123], v[188:191], v[40:43]\n"
"ds_read_b128 v[168:171], v4 offset:59392\n"
"v_mfma_f32_16x16x32_bf16 v[40:43], a[112:115], v[184:187], v[40:43]\n"
"buffer_load_dwordx4 v6, s[32:35], s63 offen offset:2048 sc1 lds\n"
"v_mfma_f32_16x16x32_bf16 v[40:43], a[104:107], v[180:183], v[40:43]\n"
"ds_read_b128 v[172:175], v4 offset:60416\n"
"v_mfma_f32_16x16x32_bf16 v[40:43], a[96:99], v[176:179], v[40:43]\n"
"s_waitcnt lgkmcnt(4)\n"
"v_mfma_f32_16x16x32_bf16 v[40:43], a[128:131], v[128:131], v[40:43]\n"
"ds_read_b128 v[176:179], v4 offset:61440\n"
"v_mfma_f32_16x16x32_bf16 v[40:43], a[136:139], v[132:135], v[40:43]\n"
"buffer_load_dwordx4 v6, s[32:35], s63 offen offset:3072 sc1 lds\n"
"v_mfma_f32_16x16x32_bf16 v[40:43], a[144:147], v[136:139], v[40:43]\n"
"ds_read_b128 v[180:183], v4 offset:62464\n"
"s_add_u32 s63, s63, 593920\n"
"v_mfma_f32_16x16x32_bf16 v[40:43], a[152:155], v[140:143], v[40:43]\n"
"s_xor_b32 s64, s64, s65\n"
"v_mfma_f32_16x16x32_bf16 v[44:47], a[156:159], v[140:143], v[44:47]\n"
"ds_read_b128 v[184:187], v4 offset:63488\n"
"v_mfma_f32_16x16x32_bf16 v[44:47], a[148:151], v[136:139], v[44:47]\n"
"v_mfma_f32_16x16x32_bf16 v[44:47], a[140:143], v[132:135], v[44:47]\n"
"ds_read_b128 v[188:191], v4 offset:64512\n"
"v_xor_b32 v4, 65536, v4\n"
"v_mfma_f32_16x16x32_bf16 v[44:47], a[132:135], v[128:131], v[44:47]\n"
"s_waitcnt vmcnt(12)\n"
"s_barrier\n"
"v_mfma_f32_16x16x32_bf16 v[44:47], a[164:167], v[144:147], v[44:47]\n"
"ds_read_b128 v[128:131], v4 offset:0\n"
"v_lshlrev_b32 v32, 16, v48\n"
"v_mfma_f32_16x16x32_bf16 v[44:47], a[172:175], v[148:151], v[44:47]\n"
"buffer_store_dwordx2 v[16:17], v2, s[92:95], s68 idxen sc1\n"
"v_mfma_f32_16x16x32_bf16 v[44:47], a[180:183], v[152:155], v[44:47]\n"
"ds_read_b128 v[132:135], v4 offset:1024\n"
"v_and_b32 v33, 0xffff0000, v48\n"
"v_mfma_f32_16x16x32_bf16 v[44:47], a[188:191], v[156:159], v[44:47]\n"
"v_lshlrev_b32 v34, 16, v49\n"
"v_and_b32 v35, 0xffff0000, v49\n"
"v_mfma_f32_16x16x32_bf16 v[40:43], a[184:187], v[156:159], v[40:43]\n"
"ds_read_b128 v[136:139], v4 offset:2048\n"
"s_add_u32 m0, s64, 0\n"
"v_mfma_f32_16x16x32_bf16 v[40:43], a[176:179], v[152:155], v[40:43]\n"
"buffer_load_dwordx4 v2, s[32:35], s63 offen offset:0 sc1 lds\n"
"v_mfma_f32_16x16x32_bf16 v[40:43], a[168:171], v[148:151], v[40:43]\n"
"ds_read_b128 v[140:143], v4 offset:3072\n"
"v_lshlrev_b32 v36, 16, v50\n"
"v_mfma_f32_16x16x32_bf16 v[40:43], a[160:163], v[144:147], v[40:43]\n"
"v_and_b32 v37, 0xffff0000, v50\n"
"s_waitcnt lgkmcnt(4)\n"
"v_mfma_f32_16x16x32_bf16 v[40:43], a[192:195], v[160:163], v[40:43]\n"
"ds_read_b128 v[144:147], v4 offset:4096\n"
"v_lshlrev_b32 v38, 16, v51\n"
"v_mfma_f32_16x16x32_bf16 v[40:43], a[200:203], v[164:167], v[40:43]\n"
"buffer_load_dwordx4 v2, s[32:35], s63 offen offset:1024 sc1 lds\n"
"v_mfma_f32_16x16x32_bf16 v[40:43], a[208:211], v[168:171], v[40:43]\n"
"ds_read_b128 v[148:151], v4 offset:5120\n"
"v_and_b32 v39, 0xffff0000, v51\n"
"v_mfma_f32_16x16x32_bf16 v[40:43], a[216:219], v[172:175], v[40:43]\n"
"v_mfma_f32_16x16x32_bf16 v[44:47], a[220:223], v[172:175], v[44:47]\n"
"ds_read_b128 v[152:155], v4 offset:6144\n"
"v_mfma_f32_16x16x32_bf16 v[44:47], a[212:215], v[168:171], v[44:47]\n"
"buffer_load_dwordx4 v2, s[32:35], s63 offen offset:2048 sc1 lds\n"
"v_mfma_f32_16x16x32_bf16 v[44:47], a[204:207], v[164:167], v[44:47]\n"
"ds_read_b128 v[156:159], v4 offset:7168\n"
"v_mfma_f32_16x16x32_bf16 v[44:47], a[196:199], v[160:163], v[44:47]\n"
"v_mfma_f32_16x16x32_bf16 v[44:47], a[228:231], v[176:179], v[44:47]\n"
"ds_read_b128 v[160:163], v4 offset:8192\n"
"v_mfma_f32_16x16x32_bf16 v[44:47], a[236:239], v[180:183], v[44:47]\n"
"buffer_load_dwordx4 v2, s[32:35], s63 offen offset:3072 sc1 lds\n"
"v_mfma_f32_16x16x32_bf16 v[44:47], a[244:247], v[184:187], v[44:47]\n"
"ds_read_b128 v[164:167], v4 offset:9216\n"
"v_mfma_f32_16x16x32_bf16 v[44:47], a[252:255], v[188:191], v[44:47]\n"
"v_mfma_f32_16x16x32_bf16 v[40:43], a[248:251], v[188:191], v[40:43]\n"
"ds_read_b128 v[168:171], v4 offset:10240\n"
"s_add_u32 m0, s64, 4096\n"
"v_mfma_f32_16x16x32_bf16 v[40:43], a[240:243], v[184:187], v[40:43]\n"
"buffer_load_dwordx4 v6, s[32:35], s63 offen offset:0 sc1 lds\n"
"v_mfma_f32_16x16x32_bf16 v[40:43], a[232:235], v[180:183], v[40:43]\n"
"ds_read_b128 v[172:175], v4 offset:11264\n"
"s_sub_u32 s62, s62, 1\n"
"v_mfma_f32_16x16x32_bf16 v[40:43], a[224:227], v[176:179], v[40:43]\n"
"s_cbranch_scc0 .L_wl_0\n"
"s_nop 1\n"
"s_add_u32 s68, s68, 593920\n"
"s_nop 3\n"
"v_cvt_pk_bf16_f32 v56, v40, v41\n"
"v_cvt_pk_bf16_f32 v57, v42, v43\n"
"v_cvt_pk_bf16_f32 v58, v44, v45\n"
"v_cvt_pk_bf16_f32 v59, v46, v47\n"
"buffer_store_dwordx4 v[56:59], v2, s[32:35], s68 offen sc1\n"
"v_add_f32 v60, v40, v41\n"
"v_mul_f32 v61, v40, v40\n"
"v_fmac_f32 v61, v41, v41\n"
"v_add_f32 v60, v60, v42\n"
"v_fmac_f32 v61, v42, v42\n"
"v_add_f32 v60, v60, v43\n"
"v_fmac_f32 v61, v43, v43\n"
"v_add_f32 v60, v60, v44\n"
"v_fmac_f32 v61, v44, v44\n"
"v_add_f32 v60, v60, v45\n"
"v_fmac_f32 v61, v45, v45\n"
"v_add_f32 v60, v60, v46\n"
"v_fmac_f32 v61, v46, v46\n"
"v_add_f32 v60, v60, v47\n"
"v_fmac_f32 v61, v47, v47\n"
"s_nop 1\n"
"v_permlane16_swap_b32 v60, v61\n"
"v_add_f32 v60, v60, v61\n"
"s_nop 1\n"
"v_permlane32_swap_b32 v60, v61\n"
"v_add_f32 v60, v60, v61\n"
"buffer_store_dword v60, v15, s[32:35], s68 offen offset:1024 sc1\n"
"s_waitcnt vmcnt(10)\n"
"s_barrier\n"
"buffer_store_dwordx2 v[16:17], v2, s[92:95], s68 idxen sc1\n"
"s_add_u32 s68, s68, 593920\n"
"s_waitcnt vmcnt(1)\n"
"s_barrier\n"
"buffer_store_dwordx2 v[16:17], v2, s[92:95], s68 idxen sc1\n"
"s_branch .L_end\n"
".p2align 6\n"
".L_wv1:\n"
".L_wl_1:\n"
"s_waitcnt lgkmcnt(4)\n"
"v_mfma_f32_16x16x32_bf16 v[32:35], a[0:3], v[128:131], v[32:35]\n"
"ds_read_b128 v[176:179], v4 offset:12288\n"
"v_mfma_f32_16x16x32_bf16 v[32:35], a[8:11], v[132:135], v[32:35]\n"
"buffer_load_dwordx4 v6, s[32:35], s63 offen offset:1024 sc1 lds\n"
"v_mfma_f32_16x16x32_bf16 v[32:35], a[16:19], v[136:139], v[32:35]\n"
"ds_read_b128 v[180:183], v4 offset:13312\n"
"s_add_u32 s68, s68, 593920\n"
"v_mfma_f32_16x16x32_bf16 v[32:35], a[24:27], v[140:143], v[32:35]\n"
"v_cvt_pk_bf16_f32 v56, v40, v41\n"
"v_cvt_pk_bf16_f32 v57, v42, v43\n"
"v_mfma_f32_16x16x32_bf16 v[36:39], a[28:31], v[140:143], v[36:39]\n"
"ds_read_b128 v[184:187], v4 offset:14336\n"
"v_cvt_pk_bf16_f32 v58, v44, v45\n"
"v_mfma_f32_16x16x32_bf16 v[36:39], a[20:23], v[136:139], v[36:39]\n"
"v_cvt_pk_bf16_f32 v59, v46, v47\n"
"v_add_f32 v60, v40, v41\n"
"v_mfma_f32_16x16x32_bf16 v[36:39], a[12:15], v[132:135], v[36:39]\n"
"ds_read_b128 v[188:191], v4 offset:15360\n"
"v_mul_f32 v61, v40, v40\n"
"v_mfma_f32_16x16x32_bf16 v[36:39], a[4:7], v[128:131], v[36:39]\n"
"v_fmac_f32 v61, v41, v41\n"
"v_add_f32 v60, v60, v42\n"
"v_mfma_f32_16x16x32_bf16 v[36:39], a[36:39], v[144:147], v[36:39]\n"
"ds_read_b128 v[128:131], v4 offset:16384\n"
"v_fmac_f32 v61, v42, v42\n"
"v_mfma_f32_16x16x32_bf16 v[36:39], a[44:47], v[148:151], v[36:39]\n"
"buffer_store_dwordx4 v[56:59], v2, s[32:35], s68 offen sc1\n"
"v_mfma_f32_16x16x32_bf16 v[36:39], a[52:55], v[152:155], v[36:39]\n"
"ds_read_b128 v[132:135], v4 offset:17408\n"
"v_add_f32 v60, v60, v43\n"
"v_mfma_f32_16x16x32_bf16 v[36:39], a[60:63], v[156:159], v[36:39]\n"
"v_fmac_f32 v61, v43, v43\n"
"v_add_f32 v60, v60, v44\n"
"v_mfma_f32_16x16x32_bf16 v[32:35], a[56:59], v[156:159], v[32:35]\n"
"ds_read_b128 v[136:139], v4 offset:18432\n"
"v_fmac_f32 v61, v44, v44\n"
"v_mfma_f32_16x16x32_bf16 v[32:35], a[48:51], v[152:155], v[32:35]\n"
"v_add_f32 v60, v60, v45\n"
"v_fmac_f32 v61, v45, v45\n"
"v_mfma_f32_16x16x32_bf16 v[32:35], a[40:43], v[148:151], v[32:35]\n"
"ds_read_b128 v[140:143], v4 offset:19456\n"
"v_add_f32 v60, v60, v46\n"
"v_mfma_f32_16x16x32_bf16 v[32:35], a[32:35], v[144:147], v[32:35]\n"
"v_fmac_f32 v61, v46, v46\n"
"s_waitcnt lgkmcnt(4)\n"
"v_mfma_f32_16x16x32_bf16 v[32:35], a[64:67], v[160:163], v[32:35]\n"
"ds_read_b128 v[144:147], v4 offset:20480\n"
"v_add_f32 v60, v60, v47\n"
"v_mfma_f32_16x16x32_bf16 v[32:35], a[72:75], v[164:167], v[32:35]\n"
"v_fmac_f32 v61, v47, v47\n"
"v_mfma_f32_16x16x32_bf16 v[32:35], a[80:83], v[168:171], v[32:35]\n"
"ds_read_b128 v[148:151], v4 offset:21504\n"
"v_mfma_f32_16x16x32_bf16 v[32:35], a[88:91], v[172:175], v[32:35]\n"
"v_permlane16_swap_b32 v60, v61\n"
"v_mfma_f32_16x16x32_bf16 v[36:39], a[92:95], v[172:175], v[36:39]\n"
"ds_read_b128 v[152:155], v4 offset:22528\n"
"v_add_f32 v60, v60, v61\n"
"v_mfma_f32_16x16x32_bf16 v[36:39], a[84:87], v[168:171], v[36:39]\n"
"buffer_load_dwordx4 v[48:51], v2, s[32:35], s71 offen sc1\n"
"v_mfma_f32_16x16x32_bf16 v[36:39], a[76:79], v[164:167], v[36:39]\n"
"ds_read_b128 v[156:159], v4 offset:23552\n"
"s_add_u32 s71, s71, 593920\n"
"v_mfma_f32_16x16x32_bf16 v[36:39], a[68:71], v[160:163], v[36:39]\n"
"v_permlane32_swap_b32 v60, v61\n"
"v_mfma_f32_16x16x32_bf16 v[36:39], a[100:103], v[176:179], v[36:39]\n"
"ds_read_b128 v[160:163], v4 offset:24576\n"
"v_add_f32 v60, v60, v61\n"
"v_mfma_f32_16x16x32_bf16 v[36:39], a[108:111], v[180:183], v[36:39]\n"
"buffer_store_dword v60, v15, s[32:35], s68 offen offset:1024 sc1\n"
"v_mfma_f32_16x16x32_bf16 v[36:39], a[116:119], v[184:187], v[36:39]\n"
"ds_read_b128 v[164:167], v4 offset:25600\n"
"v_mfma_f32_16x16x32_bf16 v[36:39], a[124:127], v[188:191], v[36:39]\n"
"v_mfma_f32_16x16x32_bf16 v[32:35], a[120:123], v[188:191], v[32:35]\n"
"ds_read_b128 v[168:171], v4 offset:26624\n"
"v_mfma_f32_16x16x32_bf16 v[32:35], a[112:115], v[184:187], v[32:35]\n"
"buffer_load_dwordx4 v6, s[32:35], s63 offen offset:2048 sc1 lds\n"
"v_mfma_f32_16x16x32_bf16 v[32:35], a[104:107], v[180:183], v[32:35]\n"
"ds_read_b128 v[172:175], v4 offset:27648\n"
"v_mfma_f32_16x16x32_bf16 v[32:35], a[96:99], v[176:179], v[32:35]\n"
"s_waitcnt lgkmcnt(4)\n"
"v_mfma_f32_16x16x32_bf16 v[32:35], a[128:131], v[128:131], v[32:35]\n"
"ds_read_b128 v[176:179], v4 offset:28672\n"
"v_mfma_f32_16x16x32_bf16 v[32:35], a[136:139], v[132:135], v[32:35]\n"
"buffer_load_dwordx4 v6, s[32:35], s63 offen offset:3072 sc1 lds\n"
"v_mfma_f32_16x16x32_bf16 v[32:35], a[144:147], v[136:139], v[32:35]\n"
"ds_read_b128 v[180:183], v4 offset:29696\n"
"s_add_u32 s63, s63, 593920\n"
"v_mfma_f32_16x16x32_bf16 v[32:35], a[152:155], v[140:143], v[32:35]\n"
"v_mfma_f32_16x16x32_bf16 v[36:39], a[156:159], v[140:143], v[36:39]\n"
"ds_read_b128 v[184:187], v4 offset:30720\n"
"v_mfma_f32_16x16x32_bf16 v[36:39], a[148:151], v[136:139], v[36:39]\n"
"buffer_load_dwordx2 v[12:13], v14, s[32:35], s66 offen sc1\n"
"v_mfma_f32_16x16x32_bf16 v[36:39], a[140:143], v[132:135], v[36:39]\n"
"s_waitcnt vmcnt(12)\n"
"s_barrier\n"
"v_mfma_f32_16x16x32_bf16 v[36:39], a[132:135], v[128:131], v[36:39]\n"
"ds_read_b128 v[188:191], v4 offset:31744\n"
"v_mfma_f32_16x16x32_bf16 v[36:39], a[164:167], v[144:147], v[36:39]\n"
"ds_read_b128 v[128:131], v4 offset:32768\n"
"v_lshlrev_b32 v40, 16, v52\n"
"v_mfma_f32_16x16x32_bf16 v[36:39], a[172:175], v[148:151], v[36:39]\n"
"v_and_b32 v41, 0xffff0000, v52\n"
"v_lshlrev_b32 v42, 16, v53\n"
"v_mfma_f32_16x16x32_bf16 v[36:39], a[180:183], v[152:155], v[36:39]\n"
"ds_read_b128 v[132:135], v4 offset:33792\n"
"v_and_b32 v43, 0xffff0000, v53\n"
"v_mfma_f32_16x16x32_bf16 v[36:39], a[188:191], v[156:159], v[36:39]\n"
"v_lshlrev_b32 v44, 16, v54\n"
"v_and_b32 v45, 0xffff0000, v54\n"
"v_mfma_f32_16x16x32_bf16 v[32:35], a[184:187], v[156:159], v[32:35]\n"
"ds_read_b128 v[136:139], v4 offset:34816\n"
"s_add_u32 m0, s64, 32768\n"
"v_mfma_f32_16x16x32_bf16 v[32:35], a[176:179], v[152:155], v[32:35]\n"
"buffer_load_dwordx4 v2, s[32:35], s63 offen offset:0 sc1 lds\n"
"v_mfma_f32_16x16x32_bf16 v[32:35], a[168:171], v[148:151], v[32:35]\n"
"ds_read_b128 v[140:143], v4 offset:35840\n"
"v_lshlrev_b32 v46, 16, v55\n"
"v_mfma_f32_16x16x32_bf16 v[32:35], a[160:163], v[144:147], v[32:35]\n"
"v_and_b32 v47, 0xffff0000, v55\n"
"s_waitcnt lgkmcnt(4)\n"
"v_mfma_f32_16x16x32_bf16 v[32:35], a[192:195], v[160:163], v[32:35]\n"
"ds_read_b128 v[144:147], v4 offset:36864\n"
"v_mfma_f32_16x16x32_bf16 v[32:35], a[200:203], v[164:167], v[32:35]\n"
"buffer_load_dwordx4 v2, s[32:35], s63 offen offset:1024 sc1 lds\n"
"v_mfma_f32_16x16x32_bf16 v[32:35], a[208:211], v[168:171], v[32:35]\n"
"ds_read_b128 v[148:151], v4 offset:37888\n"
"v_mfma_f32_16x16x32_bf16 v[32:35], a[216:219], v[172:175], v[32:35]\n"
"v_mfma_f32_16x16x32_bf16 v[36:39], a[220:223], v[172:175], v[36:39]\n"
"ds_read_b128 v[152:155], v4 offset:38912\n"
"v_mfma_f32_16x16x32_bf16 v[36:39], a[212:215], v[168:171], v[36:39]\n"
"buffer_load_dwordx4 v2, s[32:35], s63 offen offset:2048 sc1 lds\n"
"v_mfma_f32_16x16x32_bf16 v[36:39], a[204:207], v[164:167], v[36:39]\n"
"ds_read_b128 v[156:159], v4 offset:39936\n"
"v_mfma_f32_16x16x32_bf16 v[36:39], a[196:199], v[160:163], v[36:39]\n"
"v_mfma_f32_16x16x32_bf16 v[36:39], a[228:231], v[176:179], v[36:39]\n"
"ds_read_b128 v[160:163], v4 offset:40960\n"
"v_mfma_f32_16x16x32_bf16 v[36:39], a[236:239], v[180:183], v[36:39]\n"
"buffer_load_dwordx4 v2, s[32:35], s63 offen offset:3072 sc1 lds\n"
"v_mfma_f32_16x16x32_bf16 v[36:39], a[244:247], v[184:187], v[36:39]\n"
"ds_read_b128 v[164:167], v4 offset:41984\n"
"v_mfma_f32_16x16x32_bf16 v[36:39], a[252:255], v[188:191], v[36:39]\n"
"v_mfma_f32_16x16x32_bf16 v[32:35], a[248:251], v[188:191], v[32:35]\n"
"ds_read_b128 v[168:171], v4 offset:43008\n"
"s_add_u32 m0, s64, 36864\n"
"v_mfma_f32_16x16x32_bf16 v[32:35], a[240:243], v[184:187], v[32:35]\n"
"buffer_load_dwordx4 v6, s[32:35], s63 offen offset:0 sc1 lds\n"
"v_mfma_f32_16x16x32_bf16 v[32:35], a[232:235], v[180:183], v[32:35]\n"
"ds_read_b128 v[172:175], v4 offset:44032\n"
"v_mfma_f32_16x16x32_bf16 v[32:35], a[224:227], v[176:179], v[32:35]\n"
"s_waitcnt lgkmcnt(4)\n"
"v_mfma_f32_16x16x32_bf16 v[40:43], a[0:3], v[128:131], v[40:43]\n"
"ds_read_b128 v[176:179], v4 offset:45056\n"
"v_mfma_f32_16x16x32_bf16 v[40:43], a[8:11], v[132:135], v[40:43]\n"
"buffer_load_dwordx4 v6, s[32:35], s63 offen offset:1024 sc1 lds\n"
"v_mfma_f32_16x16x32_bf16 v[40:43], a[16:19], v[136:139], v[40:43]\n"
"ds_read_b128 v[180:183], v4 offset:46080\n"
"s_add_u32 s68, s68, 593920\n"
"v_mfma_f32_16x16x32_bf16 v[40:43], a[24:27], v[140:143], v[40:43]\n"
"v_cvt_pk_bf16_f32 v56, v32, v33\n"
"v_cvt_pk_bf16_f32 v57, v34, v35\n"
"v_mfma_f32_16x16x32_bf16 v[44:47], a[28:31], v[140:143], v[44:47]\n"
"ds_read_b128 v[184:187], v4 offset:47104\n"
"v_cvt_pk_bf16_f32 v58, v36, v37\n"
"v_mfma_f32_16x16x32_bf16 v[44:47], a[20:23], v[136:139], v[44:47]\n"
"v_cvt_pk_bf16_f32 v59, v38, v39\n"
"v_add_f32 v60, v32, v33\n"
"v_mfma_f32_16x16x32_bf16 v[44:47], a[12:15], v[132:135], v[44:47]\n"
"ds_read_b128 v[188:191], v4 offset:48128\n"
"v_mul_f32 v61, v32, v32\n"
"v_mfma_f32_16x16x32_bf16 v[44:47], a[4:7], v[128:131], v[44:47]\n"
"v_fmac_f32 v61, v33, v33\n"
"v_add_f32 v60, v60, v34\n"
"v_mfma_f32_16x16x32_bf16 v[44:47], a[36:39], v[144:147], v[44:47]\n"
"ds_read_b128 v[128:131], v4 offset:49152\n"
"v_fmac_f32 v61, v34, v34\n"
"v_mfma_f32_16x16x32_bf16 v[44:47], a[44:47], v[148:151], v[44:47]\n"
"buffer_store_dwordx4 v[56:59], v2, s[32:35], s68 offen sc1\n"
"v_mfma_f32_16x16x32_bf16 v[44:47], a[52:55], v[152:155], v[44:47]\n"
"ds_read_b128 v[132:135], v4 offset:50176\n"
"v_add_f32 v60, v60, v35\n"
"v_mfma_f32_16x16x32_bf16 v[44:47], a[60:63], v[156:159], v[44:47]\n"
"v_fmac_f32 v61, v35, v35\n"
"v_add_f32 v60, v60, v36\n"
"v_mfma_f32_16x16x32_bf16 v[40:43], a[56:59], v[156:159], v[40:43]\n"
"ds_read_b128 v[136:139], v4 offset:51200\n"
"v_fmac_f32 v61, v36, v36\n"
"v_mfma_f32_16x16x32_bf16 v[40:43], a[48:51], v[152:155], v[40:43]\n"
"v_add_f32 v60, v60, v37\n"
"v_fmac_f32 v61, v37, v37\n"
"v_mfma_f32_16x16x32_bf16 v[40:43], a[40:43], v[148:151], v[40:43]\n"
"ds_read_b128 v[140:143], v4 offset:52224\n"
"v_add_f32 v60, v60, v38\n"
"v_mfma_f32_16x16x32_bf16 v[40:43], a[32:35], v[144:147], v[40:43]\n"
"v_fmac_f32 v61, v38, v38\n"
"s_waitcnt lgkmcnt(4)\n"
"v_mfma_f32_16x16x32_bf16 v[40:43], a[64:67], v[160:163], v[40:43]\n"
"ds_read_b128 v[144:147], v4 offset:53248\n"
"v_add_f32 v60, v60, v39\n"
"v_mfma_f32_16x16x32_bf16 v[40:43], a[72:75], v[164:167], v[40:43]\n"
"v_fmac_f32 v61, v39, v39\n"
"v_mfma_f32_16x16x32_bf16 v[40:43], a[80:83], v[168:171], v[40:43]\n"
"ds_read_b128 v[148:151], v4 offset:54272\n"
"v_mfma_f32_16x16x32_bf16 v[40:43], a[88:91], v[172:175], v[40:43]\n"
"v_permlane16_swap_b32 v60, v61\n"
"v_mfma_f32_16x16x32_bf16 v[44:47], a[92:95], v[172:175], v[44:47]\n"
"ds_read_b128 v[152:155], v4 offset:55296\n"
"v_add_f32 v60, v60, v61\n"
"v_mfma_f32_16x16x32_bf16 v[44:47], a[84:87], v[168:171], v[44:47]\n"
"buffer_load_dwordx4 v[52:55], v2, s[32:35], s71 offen sc1\n"
"v_mfma_f32_16x16x32_bf16 v[44:47], a[76:79], v[164:167], v[44:47]\n"
"ds_read_b128 v[156:159], v4 offset:56320\n"
"s_add_u32 s71, s71, 593920\n"
"v_mfma_f32_16x16x32_bf16 v[44:47], a[68:71], v[160:163], v[44:47]\n"
"v_permlane32_swap_b32 v60, v61\n"
"v_mfma_f32_16x16x32_bf16 v[44:47], a[100:103], v[176:179], v[44:47]\n"
"ds_read_b128 v[160:163], v4 offset:57344\n"
"v_add_f32 v60, v60, v61\n"
"v_mfma_f32_16x16x32_bf16 v[44:47], a[108:111], v[180:183], v[44:47]\n"
"buffer_store_dword v60, v15, s[32:35], s68 offen offset:1024 sc1\n"
"v_mfma_f32_16x16x32_bf16 v[44:47], a[116:119], v[184:187], v[44:47]\n"
"ds_read_b128 v[164:167], v4 offset:58368\n"
"v_mfma_f32_16x16x32_bf16 v[44:47], a[124:127], v[188:191], v[44:47]\n"
"v_mfma_f32_16x16x32_bf16 v[40:43], a[120:123], v[188:191], v[40:43]\n"
"ds_read_b128 v[168:171], v4 offset:59392\n"
"v_mfma_f32_16x16x32_bf16 v[40:43], a[112:115], v[184:187], v[40:43]\n"
"buffer_load_dwordx4 v6, s[32:35], s63 offen offset:2048 sc1 lds\n"
"v_mfma_f32_16x16x32_bf16 v[40:43], a[104:107], v[180:183], v[40:43]\n"
"ds_read_b128 v[172:175], v4 offset:60416\n"
"v_mfma_f32_16x16x32_bf16 v[40:43], a[96:99], v[176:179], v[40:43]\n"
"s_waitcnt lgkmcnt(4)\n"
"v_mfma_f32_16x16x32_bf16 v[40:43], a[128:131], v[128:131], v[40:43]\n"
"ds_read_b128 v[176:179], v4 offset:61440\n"
"v_mfma_f32_16x16x32_bf16 v[40:43], a[136:139], v[132:135], v[40:43]\n"
"buffer_load_dwordx4 v6, s[32:35], s63 offen offset:3072 sc1 lds\n"
"v_mfma_f32_16x16x32_bf16 v[40:43], a[144:147], v[136:139], v[40:43]\n"
"ds_read_b128 v[180:183], v4 offset:62464\n"
"s_waitcnt vmcnt(11)\n"
"v_mfma_f32_16x16x32_bf16 v[40:43], a[152:155], v[140:143], v[40:43]\n"
"v_cmp_ne_u64 vcc, v[16:17], v[12:13]\n"
"s_add_u32 s63, s63, 593920\n"
"v_mfma_f32_16x16x32_bf16 v[44:47], a[156:159], v[140:143], v[44:47]\n"
"ds_read_b128 v[184:187], v4 offset:63488\n"
"s_cbranch_vccnz .L_rtw1_1\n"
".L_bkw1_1:\n"
"v_mfma_f32_16x16x32_bf16 v[44:47], a[148:151], v[136:139], v[44:47]\n"
"s_add_u32 s66, s66, 1187840\n"
"s_xor_b32 s64, s64, s65\n"
"v_mfma_f32_16x16x32_bf16 v[44:47], a[140:143], v[132:135], v[44:47]\n"
"ds_read_b128 v[188:191], v4 offset:64512\n"
"s_barrier\n"
"v_mfma_f32_16x16x32_bf16 v[44:47], a[132:135], v[128:131], v[44:47]\n"
"v_xor_b32 v4, 65536, v4\n"
"v_mfma_f32_16x16x32_bf16 v[44:47], a[164:167], v[144:147], v[44:47]\n"
"ds_read_b128 v[128:131], v4 offset:0\n"
"v_lshlrev_b32 v32, 16, v48\n"
"v_mfma_f32_16x16x32_bf16 v[44:47], a[172:175], v[148:151], v[44:47]\n"
"v_and_b32 v33, 0xffff0000, v48\n"
"v_lshlrev_b32 v34, 16, v49\n"
"v_mfma_f32_16x16x32_bf16 v[44:47], a[180:183], v[152:155], v[44:47]\n"
"ds_read_b128 v[132:135], v4 offset:1024\n"
"v_and_b32 v35, 0xffff0000, v49\n"
"v_mfma_f32_16x16x32_bf16 v[44:47], a[188:191], v[156:159], v[44:47]\n"
"v_lshlrev_b32 v36, 16, v50\n"
"v_and_b32 v37, 0xffff0000, v50\n"
"v_mfma_f32_16x16x32_bf16 v[40:43], a[184:187], v[156:159], v[40:43]\n"
"ds_read_b128 v[136:139], v4 offset:2048\n"
"s_add_u32 m0, s64, 0\n"
"v_mfma_f32_16x16x32_bf16 v[40:43], a[176:179], v[152:155], v[40:43]\n"
"buffer_load_dwordx4 v2, s[32:35], s63 offen offset:0 sc1 lds\n"
"v_mfma_f32_16x16x32_bf16 v[40:43], a[168:171], v[148:151], v[40:43]\n"
"ds_read_b128 v[140:143], v4 offset:3072\n"
"v_lshlrev_b32 v38, 16, v51\n"
"v_mfma_f32_16x16x32_bf16 v[40:43], a[160:163], v[144:147], v[40:43]\n"
"v_and_b32 v39, 0xffff0000, v51\n"
"s_waitcnt lgkmcnt(4)\n"
"v_mfma_f32_16x16x32_bf16 v[40:43], a[192:195], v[160:163], v[40:43]\n"
"ds_read_b128 v[144:147], v4 offset:4096\n"
"v_mfma_f32_16x16x32_bf16 v[40:43], a[200:203], v[164:167], v[40:43]\n"
"buffer_load_dwordx4 v2, s[32:35], s63 offen offset:1024 sc1 lds\n"
"v_mfma_f32_16x16x32_bf16 v[40:43], a[208:211], v[168:171], v[40:43]\n"
"ds_read_b128 v[148:151], v4 offset:5120\n"
"v_mfma_f32_16x16x32_bf16 v[40:43], a[216:219], v[172:175], v[40:43]\n"
"v_mfma_f32_16x16x32_bf16 v[44:47], a[220:223], v[172:175], v[44:47]\n"
"ds_read_b128 v[152:155], v4 offset:6144\n"
"v_mfma_f32_16x16x32_bf16 v[44:47], a[212:215], v[168:171], v[44:47]\n"
"buffer_load_dwordx4 v2, s[32:35], s63 offen offset:2048 sc1 lds\n"
"v_mfma_f32_16x16x32_bf16 v[44:47], a[204:207], v[164:167], v[44:47]\n"
"ds_read_b128 v[156:159], v4 offset:7168\n"
"v_mfma_f32_16x16x32_bf16 v[44:47], a[196:199], v[160:163], v[44:47]\n"
"v_mfma_f32_16x16x32_bf16 v[44:47], a[228:231], v[176:179], v[44:47]\n"
"ds_read_b128 v[160:163], v4 offset:8192\n"
"v_mfma_f32_16x16x32_bf16 v[44:47], a[236:239], v[180:183], v[44:47]\n"
"buffer_load_dwordx4 v2, s[32:35], s63 offen offset:3072 sc1 lds\n"
"v_mfma_f32_16x16x32_bf16 v[44:47], a[244:247], v[184:187], v[44:47]\n"
"ds_read_b128 v[164:167], v4 offset:9216\n"
"v_mfma_f32_16x16x32_bf16 v[44:47], a[252:255], v[188:191], v[44:47]\n"
"v_mfma_f32_16x16x32_bf16 v[40:43], a[248:251], v[188:191], v[40:43]\n"
"ds_read_b128 v[168:171], v4 offset:10240\n"
"s_add_u32 m0, s64, 4096\n"
"v_mfma_f32_16x16x32_bf16 v[40:43], a[240:243], v[184:187], v[40:43]\n"
"buffer_load_dwordx4 v6, s[32:35], s63 offen offset:0 sc1 lds\n"
"v_mfma_f32_16x16x32_bf16 v[40:43], a[232:235], v[180:183], v[40:43]\n"
"ds_read_b128 v[172:175], v4 offset:11264\n"
"s_sub_u32 s62, s62, 1\n"
"v_mfma_f32_16x16x32_bf16 v[40:43], a[224:227], v[176:179], v[40:43]\n"
"s_cbranch_scc0 .L_wl_1\n"
"s_nop 1\n"
"s_add_u32 s68, s68, 593920\n"
"s_nop 3\n"
"v_cvt_pk_bf16_f32 v56, v40, v41\n"
"v_cvt_pk_bf16_f32 v57, v42, v43\n"
"v_cvt_pk_bf16_f32 v58, v44, v45\n"
"v_cvt_pk_bf16_f32 v59, v46, v47\n"
"buffer_store_dwordx4 v[56:59], v2, s[32:35], s68 offen sc1\n"
"v_add_f32 v60, v40, v41\n"
"v_mul_f32 v61, v40, v40\n"
"v_fmac_f32 v61, v41, v41\n"
"v_add_f32 v60, v60, v42\n"
"v_fmac_f32 v61, v42, v42\n"
"v_add_f32 v60, v60, v43\n"
"v_fmac_f32 v61, v43, v43\n"
"v_add_f32 v60, v60, v44\n"
"v_fmac_f32 v61, v44, v44\n"
"v_add_f32 v60, v60, v45\n"
"v_fmac_f32 v61, v45, v45\n"
"v_add_f32 v60, v60, v46\n"
"v_fmac_f32 v61, v46, v46\n"
"v_add_f32 v60, v60, v47\n"
"v_fmac_f32 v61, v47, v47\n"
"s_nop 1\n"
"v_permlane16_swap_b32 v60, v61\n"
"v_add_f32 v60, v60, v61\n"
"s_nop 1\n"
"v_permlane32_swap_b32 v60, v61\n"
"v_add_f32 v60, v60, v61\n"
"buffer_store_dword v60, v15, s[32:35], s68 offen offset:1024 sc1\n"
"s_waitcnt vmcnt(9)\n"
"s_barrier\n"
"buffer_store_dwordx2 v[16:17], v2, s[92:95], s68 idxen sc1\n"
"s_add_u32 s68, s68, 593920\n"
"s_waitcnt vmcnt(1)\n"
"s_barrier\n"
"buffer_store_dwordx2 v[16:17], v2, s[92:95], s68 idxen sc1\n"
"s_branch .L_end\n"
".p2align 6\n"
".L_wv2:\n"
".L_wl_2:\n"
"s_waitcnt lgkmcnt(4)\n"
"v_mfma_f32_16x16x32_bf16 v[32:35], a[0:3], v[128:131], v[32:35]\n"
"ds_read_b128 v[176:179], v4 offset:12288\n"
"v_mfma_f32_16x16x32_bf16 v[32:35], a[8:11], v[132:135], v[32:35]\n"
"buffer_load_dwordx4 v6, s[32:35], s63 offen offset:1024 sc1 lds\n"
"v_mfma_f32_16x16x32_bf16 v[32:35], a[16:19], v[136:139], v[32:35]\n"
"ds_read_b128 v[180:183], v4 offset:13312\n"
"s_add_u32 s68, s68, 593920\n"
"v_mfma_f32_16x16x32_bf16 v[32:35], a[24:27], v[140:143], v[32:35]\n"
"v_cvt_pk_bf16_f32 v56, v40, v41\n"
"v_cvt_pk_bf16_f32 v57, v42, v43\n"
"v_mfma_f32_16x16x32_bf16 v[36:39], a[28:31], v[140:143], v[36:39]\n"
"ds_read_b128 v[184:187], v4 offset:14336\n"
"v_cvt_pk_bf16_f32 v58, v44, v45\n"
"v_mfma_f32_16x16x32_bf16 v[36:39], a[20:23], v[136:139], v[36:39]\n"
"v_cvt_pk_bf16_f32 v59, v46, v47\n"
"v_add_f32 v60, v40, v41\n"
"v_mfma_f32_16x16x32_bf16 v[36:39], a[12:15], v[132:135], v[36:39]\n"
"ds_read_b128 v[188:191], v4 offset:15360\n"
"v_mul_f32 v61, v40, v40\n"
"v_mfma_f32_16x16x32_bf16 v[36:39], a[4:7], v[128:131], v[36:39]\n"
"v_fmac_f32 v61, v41, v41\n"
"v_add_f32 v60, v60, v42\n"
"v_mfma_f32_16x16x32_bf16 v[36:39], a[36:39], v[144:147], v[36:39]\n"
"ds_read_b128 v[128:131], v4 offset:16384\n"
"v_fmac_f32 v61, v42, v42\n"
"v_mfma_f32_16x16x32_bf16 v[36:39], a[44:47], v[148:151], v[36:39]\n"
"buffer_store_dwordx4 v[56:59], v2, s[32:35], s68 offen sc1\n"
"v_mfma_f32_16x16x32_bf16 v[36:39], a[52:55], v[152:155], v[36:39]\n"
"ds_read_b128 v[132:135], v4 offset:17408\n"
"v_add_f32 v60, v60, v43\n"
"v_mfma_f32_16x16x32_bf16 v[36:39], a[60:63], v[156:159], v[36:39]\n"
"v_fmac_f32 v61, v43, v43\n"
"v_add_f32 v60, v60, v44\n"
"v_mfma_f32_16x16x32_bf16 v[32:35], a[56:59], v[156:159], v[32:35]\n"
"ds_read_b128 v[136:139], v4 offset:18432\n"
"v_fmac_f32 v61, v44, v44\n"
"v_mfma_f32_16x16x32_bf16 v[32:35], a[48:51], v[152:155], v[32:35]\n"
"v_add_f32 v60, v60, v45\n"
"v_fmac_f32 v61, v45, v45\n"
"v_mfma_f32_16x16x32_bf16 v[32:35], a[40:43], v[148:151], v[32:35]\n"
"ds_read_b128 v[140:143], v4 offset:19456\n"
"v_add_f32 v60, v60, v46\n"
"v_mfma_f32_16x16x32_bf16 v[32:35], a[32:35], v[144:147], v[32:35]\n"
"v_fmac_f32 v61, v46, v46\n"
"s_waitcnt lgkmcnt(4)\n"
"v_mfma_f32_16x16x32_bf16 v[32:35], a[64:67], v[160:163], v[32:35]\n"
"ds_read_b128 v[144:147], v4 offset:20480\n"
"v_add_f32 v60, v60, v47\n"
"v_mfma_f32_16x16x32_bf16 v[32:35], a[72:75], v[164:167], v[32:35]\n"
"v_fmac_f32 v61, v47, v47\n"
"v_mfma_f32_16x16x32_bf16 v[32:35], a[80:83], v[168:171], v[32:35]\n"
"ds_read_b128 v[148:151], v4 offset:21504\n"
"v_mfma_f32_16x16x32_bf16 v[32:35], a[88:91], v[172:175], v[32:35]\n"
"v_permlane16_swap_b32 v60, v61\n"
"v_mfma_f32_16x16x32_bf16 v[36:39], a[92:95], v[172:175], v[36:39]\n"
"ds_read_b128 v[152:155], v4 offset:22528\n"
"v_add_f32 v60, v60, v61\n"
"v_mfma_f32_16x16x32_bf16 v[36:39], a[84:87], v[168:171], v[36:39]\n"
"buffer_load_dwordx4 v[48:51], v2, s[32:35], s71 offen sc1\n"
"v_mfma_f32_16x16x32_bf16 v[36:39], a[76:79], v[164:167], v[36:39]\n"
"ds_read_b128 v[156:159], v4 offset:23552\n"
"s_add_u32 s71, s71, 593920\n"
"v_mfma_f32_16x16x32_bf16 v[36:39], a[68:71], v[160:163], v[36:39]\n"
"v_permlane32_swap_b32 v60, v61\n"
"v_mfma_f32_16x16x32_bf16 v[36:39], a[100:103], v[176:179], v[36:39]\n"
"ds_read_b128 v[160:163], v4 offset:24576\n"
"v_add_f32 v60, v60, v61\n"
"v_mfma_f32_16x16x32_bf16 v[36:39], a[108:111], v[180:183], v[36:39]\n"
"buffer_store_dword v60, v15, s[32:35], s68 offen offset:1024 sc1\n"
"v_mfma_f32_16x16x32_bf16 v[36:39], a[116:119], v[184:187], v[36:39]\n"
"ds_read_b128 v[164:167], v4 offset:25600\n"
"v_mfma_f32_16x16x32_bf16 v[36:39], a[124:127], v[188:191], v[36:39]\n"
"v_mfma_f32_16x16x32_bf16 v[32:35], a[120:123], v[188:191], v[32:35]\n"
"ds_read_b128 v[168:171], v4 offset:26624\n"
"v_mfma_f32_16x16x32_bf16 v[32:35], a[112:115], v[184:187], v[32:35]\n"
"buffer_load_dwordx4 v6, s[32:35], s63 offen offset:2048 sc1 lds\n"
"v_mfma_f32_16x16x32_bf16 v[32:35], a[104:107], v[180:183], v[32:35]\n"
"ds_read_b128 v[172:175], v4 offset:27648\n"
"v_mfma_f32_16x16x32_bf16 v[32:35], a[96:99], v[176:179], v[32:35]\n"
"s_waitcnt lgkmcnt(4)\n"
"v_mfma_f32_16x16x32_bf16 v[32:35], a[128:131], v[128:131], v[32:35]\n"
"ds_read_b128 v[176:179], v4 offset:28672\n"
"v_mfma_f32_16x16x32_bf16 v[32:35], a[136:139], v[132:135], v[32:35]\n"
"buffer_load_dwordx4 v6, s[32:35], s63 offen offset:3072 sc1 lds\n"
"v_mfma_f32_16x16x32_bf16 v[32:35], a[144:147], v[136:139], v[32:35]\n"
"ds_read_b128 v[180:183], v4 offset:29696\n"
"s_add_u32 s63, s63, 593920\n"
"v_mfma_f32_16x16x32_bf16 v[32:35], a[152:155], v[140:143], v[32:35]\n"
"v_mfma_f32_16x16x32_bf16 v[36:39], a[156:159], v[140:143], v[36:39]\n"
"ds_read_b128 v[184:187], v4 offset:30720\n"
"v_mfma_f32_16x16x32_bf16 v[36:39], a[148:151], v[136:139], v[36:39]\n"
"s_waitcnt vmcnt(11)\n"
"s_barrier\n"
"v_mfma_f32_16x16x32_bf16 v[36:39], a[140:143], v[132:135], v[36:39]\n"
"ds_read_b128 v[188:191], v4 offset:31744\n"
"v_mfma_f32_16x16x32_bf16 v[36:39], a[132:135], v[128:131], v[36:39]\n"
"v_mfma_f32_16x16x32_bf16 v[36:39], a[164:167], v[144:147], v[36:39]\n"
"ds_read_b128 v[128:131], v4 offset:32768\n"
"v_lshlrev_b32 v40, 16, v52\n"
"v_mfma_f32_16x16x32_bf16 v[36:39], a[172:175], v[148:151], v[36:39]\n"
"v_and_b32 v41, 0xffff0000, v52\n"
"v_lshlrev_b32 v42, 16, v53\n"
"v_mfma_f32_16x16x32_bf16 v[36:39], a[180:183], v[152:155], v[36:39]\n"
"ds_read_b128 v[132:135], v4 offset:33792\n"
"v_and_b32 v43, 0xffff0000, v53\n"
"v_mfma_f32_16x16x32_bf16 v[36:39], a[188:191], v[156:159], v[36:39]\n"
"v_lshlrev_b32 v44, 16, v54\n"
"v_and_b32 v45, 0xffff0000, v54\n"
"v_mfma_f32_16x16x32_bf16 v[32:35], a[184:187], v[156:159], v[32:35]\n"
"ds_read_b128 v[136:139], v4 offset:34816\n"
"s_add_u32 m0, s64, 32768\n"
"v_mfma_f32_16x16x32_bf16 v[32:35], a[176:179], v[152:155], v[32:35]\n"
"buffer_load_dwordx4 v2, s[32:35], s63 offen offset:0 sc1 lds\n"
"v_mfma_f32_16x16x32_bf16 v[32:35], a[168:171], v[148:151], v[32:35]\n"
"ds_read_b128 v[140:143], v4 offset:35840\n"
"v_lshlrev_b32 v46, 16, v55\n"
"v_mfma_f32_16x16x32_bf16 v[32:35], a[160:163], v[144:147], v[32:35]\n"
"v_and_b32 v47, 0xffff0000, v55\n"
"s_waitcnt lgkmcnt(4)\n"
"v_mfma_f32_16x16x32_bf16 v[32:35], a[192:195], v[160:163], v[32:35]\n"
"ds_read_b128 v[144:147], v4 offset:36864\n"
"v_mfma_f32_16x16x32_bf16 v[32:35], a[200:203], v[164:167], v[32:35]\n"
"buffer_load_dwordx4 v2, s[32:35], s63 offen offset:1024 sc1 lds\n"
"v_mfma_f32_16x16x32_bf16 v[32:35], a[208:211], v[168:171], v[32:35]\n"
"ds_read_b128 v[148:151], v4 offset:37888\n"
"v_mfma_f32_16x16x32_bf16 v[32:35], a[216:219], v[172:175], v[32:35]\n"
"v_mfma_f32_16x16x32_bf16 v[36:39], a[220:223], v[172:175], v[36:39]\n"
"ds_read_b128 v[152:155], v4 offset:38912\n"
"v_mfma_f32_16x16x32_bf16 v[36:39], a[212:215], v[168:171], v[36:39]\n"
"buffer_load_dwordx4 v2, s[32:35], s63 offen offset:2048 sc1 lds\n"
"v_mfma_f32_16x16x32_bf16 v[36:39], a[204:207], v[164:167], v[36:39]\n"
"ds_read_b128 v[156:159], v4 offset:39936\n"
"v_mfma_f32_16x16x32_bf16 v[36:39], a[196:199], v[160:163], v[36:39]\n"
"v_mfma_f32_16x16x32_bf16 v[36:39], a[228:231], v[176:179], v[36:39]\n"
"ds_read_b128 v[160:163], v4 offset:40960\n"
"v_mfma_f32_16x16x32_bf16 v[36:39], a[236:239], v[180:183], v[36:39]\n"
"buffer_load_dwordx4 v2, s[32:35], s63 offen offset:3072 sc1 lds\n"
"v_mfma_f32_16x16x32_bf16 v[36:39], a[244:247], v[184:187], v[36:39]\n"
"ds_read_b128 v[164:167], v4 offset:41984\n"
"v_mfma_f32_16x16x32_bf16 v[36:39], a[252:255], v[188:191], v[36:39]\n"
"v_mfma_f32_16x16x32_bf16 v[32:35], a[248:251], v[188:191], v[32:35]\n"
"ds_read_b128 v[168:171], v4 offset:43008\n"
"s_add_u32 m0, s64, 36864\n"
"v_mfma_f32_16x16x32_bf16 v[32:35], a[240:243], v[184:187], v[32:35]\n"
"buffer_load_dwordx4 v6, s[32:35], s63 offen offset:0 sc1 lds\n"
"v_mfma_f32_16x16x32_bf16 v[32:35], a[232:235], v[180:183], v[32:35]\n"
"ds_read_b128 v[172:175], v4 offset:44032\n"
"v_mfma_f32_16x16x32_bf16 v[32:35], a[224:227], v[176:179], v[32:35]\n"
"s_waitcnt lgkmcnt(4)\n"
"v_mfma_f32_16x16x32_bf16 v[40:43], a[0:3], v[128:131], v[40:43]\n"
"ds_read_b128 v[176:179], v4 offset:45056\n"
"v_mfma_f32_16x16x32_bf16 v[40:43], a[8:11], v[132:135], v[40:43]\n"
"buffer_load_dwordx4 v6, s[32:35], s63 offen offset:1024 sc1 lds\n"
"v_mfma_f32_16x16x32_bf16 v[40:43], a[16:19], v[136:139], v[40:43]\n"
"ds_read_b128 v[180:183], v4 offset:46080\n"
"s_add_u32 s68, s68, 593920\n"
"v_mfma_f32_16x16x32_bf16 v[40:43], a[24:27], v[140:143], v[40:43]\n"
"v_cvt_pk_bf16_f32 v56, v32, v33\n"
"v_cvt_pk_bf16_f32 v57, v34, v35\n"
"v_mfma_f32_16x16x32_bf16 v[44:47], a[28:31], v[140:143], v[44:47]\n"
"ds_read_b128 v[184:187], v4 offset:47104\n"
"v_cvt_pk_bf16_f32 v58, v36, v37\n"
"v_mfma_f32_16x16x32_bf16 v[44:47], a[20:23], v[136:139], v[44:47]\n"
"v_cvt_pk_bf16_f32 v59, v38, v39\n"
"v_add_f32 v60, v32, v33\n"
"v_mfma_f32_16x16x32_bf16 v[44:47], a[12:15], v[132:135], v[44:47]\n"
"ds_read_b128 v[188:191], v4 offset:48128\n"
"v_mul_f32 v61, v32, v32\n"
"v_mfma_f32_16x16x32_bf16 v[44:47], a[4:7], v[128:131], v[44:47]\n"
"v_fmac_f32 v61, v33, v33\n"
"v_add_f32 v60, v60, v34\n"
"v_mfma_f32_16x16x32_bf16 v[44:47], a[36:39], v[144:147], v[44:47]\n"
"ds_read_b128 v[128:131], v4 offset:49152\n"
"v_fmac_f32 v61, v34, v34\n"
"v_mfma_f32_16x16x32_bf16 v[44:47], a[44:47], v[148:151], v[44:47]\n"
"buffer_store_dwordx4 v[56:59], v2, s[32:35], s68 offen sc1\n"
"v_mfma_f32_16x16x32_bf16 v[44:47], a[52:55], v[152:155], v[44:47]\n"
"ds_read_b128 v[132:135], v4 offset:50176\n"
"v_add_f32 v60, v60, v35\n"
"v_mfma_f32_16x16x32_bf16 v[44:47], a[60:63], v[156:159], v[44:47]\n"
"v_fmac_f32 v61, v35, v35\n"
"v_add_f32 v60, v60, v36\n"
"v_mfma_f32_16x16x32_bf16 v[40:43], a[56:59], v[156:159], v[40:43]\n"
"ds_read_b128 v[136:139], v4 offset:51200\n"
"v_fmac_f32 v61, v36, v36\n"
"v_mfma_f32_16x16x32_bf16 v[40:43], a[48:51], v[152:155], v[40:43]\n"
"v_add_f32 v60, v60, v37\n"
"v_fmac_f32 v61, v37, v37\n"
"v_mfma_f32_16x16x32_bf16 v[40:43], a[40:43], v[148:151], v[40:43]\n"
"ds_read_b128 v[140:143], v4 offset:52224\n"
"v_add_f32 v60, v60, v38\n"
"v_mfma_f32_16x16x32_bf16 v[40:43], a[32:35], v[144:147], v[40:43]\n"
"v_fmac_f32 v61, v38, v38\n"
"s_waitcnt lgkmcnt(4)\n"
"v_mfma_f32_16x16x32_bf16 v[40:43], a[64:67], v[160:163], v[40:43]\n"
"ds_read_b128 v[144:147], v4 offset:53248\n"
"v_add_f32 v60, v60, v39\n"
"v_mfma_f32_16x16x32_bf16 v[40:43], a[72:75], v[164:167], v[40:43]\n"
"v_fmac_f32 v61, v39, v39\n"
"v_mfma_f32_16x16x32_bf16 v[40:43], a[80:83], v[168:171], v[40:43]\n"
"ds_read_b128 v[148:151], v4 offset:54272\n"
"v_mfma_f32_16x16x32_bf16 v[40:43], a[88:91], v[172:175], v[40:43]\n"
"v_permlane16_swap_b32 v60, v61\n"
"v_mfma_f32_16x16x32_bf16 v[44:47], a[92:95], v[172:175], v[44:47]\n"
"ds_read_b128 v[152:155], v4 offset:55296\n"
"v_add_f32 v60, v60, v61\n"
"v_mfma_f32_16x16x32_bf16 v[44:47], a[84:87], v[168:171], v[44:47]\n"
"buffer_load_dwordx4 v[52:55], v2, s[32:35], s71 offen sc1\n"
"v_mfma_f32_16x16x32_bf16 v[44:47], a[76:79], v[164:167], v[44:47]\n"
"ds_read_b128 v[156:159], v4 offset:56320\n"
"s_add_u32 s71, s71, 593920\n"
"v_mfma_f32_16x16x32_bf16 v[44:47], a[68:71], v[160:163], v[44:47]\n"
"v_permlane32_swap_b32 v60, v61\n"
"v_mfma_f32_16x16x32_bf16 v[44:47], a[100:103], v[176:179], v[44:47]\n"
"ds_read_b128 v[160:163], v4 offset:57344\n"
"v_add_f32 v60, v60, v61\n"
"v_mfma_f32_16x16x32_bf16 v[44:47], a[108:111], v[180:183], v[44:47]\n"
"buffer_store_dword v60, v15, s[32:35], s68 offen offset:1024 sc1\n"
"v_mfma_f32_16x16x32_bf16 v[44:47], a[116:119], v[184:187], v[44:47]\n"
"ds_read_b128 v[164:167], v4 offset:58368\n"
"v_mfma_f32_16x16x32_bf16 v[44:47], a[124:127], v[188:191], v[44:47]\n"
"v_mfma_f32_16x16x32_bf16 v[40:43], a[120:123], v[188:191], v[40:43]\n"
"ds_read_b128 v[168:171], v4 offset:59392\n"
"v_mfma_f32_16x16x32_bf16 v[40:43], a[112:115], v[184:187], v[40:43]\n"
"buffer_load_dwordx4 v6, s[32:35], s63 offen offset:2048 sc1 lds\n"
"v_mfma_f32_16x16x32_bf16 v[40:43], a[104:107], v[180:183], v[40:43]\n"
"ds_read_b128 v[172:175], v4 offset:60416\n"
"v_mfma_f32_16x16x32_bf16 v[40:43], a[96:99], v[176:179], v[40:43]\n"
"s_waitcnt lgkmcnt(4)\n"
"v_mfma_f32_16x16x32_bf16 v[40:43], a[128:131], v[128:131], v[40:43]\n"
"ds_read_b128 v[176:179], v4 offset:61440\n"
"v_mfma_f32_16x16x32_bf16 v[40:43], a[136:139], v[132:135], v[40:43]\n"
"buffer_load_dwordx4 v6, s[32:35], s63 offen offset:3072 sc1 lds\n"
"v_mfma_f32_16x16x32_bf16 v[40:43], a[144:147], v[136:139], v[40:43]\n"
"ds_read_b128 v[180:183], v4 offset:62464\n"
"s_add_u32 s63, s63, 593920\n"
"v_mfma_f32_16x16x32_bf16 v[40:43], a[152:155], v[140:143], v[40:43]\n"
"s_xor_b32 s64, s64, s65\n"
"v_mfma_f32_16x16x32_bf16 v[44:47], a[156:159], v[140:143], v[44:47]\n"
"ds_read_b128 v[184:187], v4 offset:63488\n"
"v_mfma_f32_16x16x32_bf16 v[44:47], a[148:151], v[136:139], v[44:47]\n"
"s_waitcnt vmcnt(11)\n"
"s_barrier\n"
"v_mfma_f32_16x16x32_bf16 v[44:47], a[140:143], v[132:135], v[44:47]\n"
"ds_read_b128 v[188:191], v4 offset:64512\n"
"v_xor_b32 v4, 65536, v4\n"
"v_mfma_f32_16x16x32_bf16 v[44:47], a[132:135], v[128:131], v[44:47]\n"
"v_mfma_f32_16x16x32_bf16 v[44:47], a[164:167], v[144:147], v[44:47]\n"
"ds_read_b128 v[128:131], v4 offset:0\n"
"v_lshlrev_b32 v32, 16, v48\n"
"v_mfma_f32_16x16x32_bf16 v[44:47], a[172:175], v[148:151], v[44:47]\n"
"v_and_b32 v33, 0xffff0000, v48\n"
"v_lshlrev_b32 v34, 16, v49\n"
"v_mfma_f32_16x16x32_bf16 v[44:47], a[180:183], v[152:155], v[44:47]\n"
"ds_read_b128 v[132:135], v4 offset:1024\n"
"v_and_b32 v35, 0xffff0000, v49\n"
"v_mfma_f32_16x16x32_bf16 v[44:47], a[188:191], v[156:159], v[44:47]\n"
"v_lshlrev_b32 v36, 16, v50\n"
"v_and_b32 v37, 0xffff0000, v50\n"
"v_mfma_f32_16x16x32_bf16 v[40:43], a[184:187], v[156:159], v[40:43]\n"
"ds_read_b128 v[136:139], v4 offset:2048\n"
"s_add_u32 m0, s64, 0\n"
"v_mfma_f32_16x16x32_bf16 v[40:43], a[176:179], v[152:155], v[40:43]\n"
"buffer_load_dwordx4 v2, s[32:35], s63 offen offset:0 sc1 lds\n"
"v_mfma_f32_16x16x32_bf16 v[40:43], a[168:171], v[148:151], v[40:43]\n"
"ds_read_b128 v[140:143], v4 offset:3072\n"
"v_lshlrev_b32 v38, 16, v51\n"
"v_mfma_f32_16x16x32_bf16 v[40:43], a[160:163], v[144:147], v[40:43]\n"
"v_and_b32 v39, 0xffff0000, v51\n"
"s_waitcnt lgkmcnt(4)\n"
"v_mfma_f32_16x16x32_bf16 v[40:43], a[192:195], v[160:163], v[40:43]\n"
"ds_read_b128 v[144:147], v4 offset:4096\n"
"v_mfma_f32_16x16x32_bf16 v[40:43], a[200:203], v[164:167], v[40:43]\n"
"buffer_load_dwordx4 v2, s[32:35], s63 offen offset:1024 sc1 lds\n"
"v_mfma_f32_16x16x32_bf16 v[40:43], a[208:211], v[168:171], v[40:43]\n"
"ds_read_b128 v[148:151], v4 offset:5120\n"
"v_mfma_f32_16x16x32_bf16 v[40:43], a[216:219], v[172:175], v[40:43]\n"
"v_mfma_f32_16x16x32_bf16 v[44:47], a[220:223], v[172:175], v[44:47]\n"
"ds_read_b128 v[152:155], v4 offset:6144\n"
"v_mfma_f32_16x16x32_bf16 v[44:47], a[212:215], v[168:171], v[44:47]\n"
"buffer_load_dwordx4 v2, s[32:35], s63 offen offset:2048 sc1 lds\n"
"v_mfma_f32_16x16x32_bf16 v[44:47], a[204:207], v[164:167], v[44:47]\n"
"ds_read_b128 v[156:159], v4 offset:7168\n"
"v_mfma_f32_16x16x32_bf16 v[44:47], a[196:199], v[160:163], v[44:47]\n"
"v_mfma_f32_16x16x32_bf16 v[44:47], a[228:231], v[176:179], v[44:47]\n"
"ds_read_b128 v[160:163], v4 offset:8192\n"
"v_mfma_f32_16x16x32_bf16 v[44:47], a[236:239], v[180:183], v[44:47]\n"
"buffer_load_dwordx4 v2, s[32:35], s63 offen offset:3072 sc1 lds\n"
"v_mfma_f32_16x16x32_bf16 v[44:47], a[244:247], v[184:187], v[44:47]\n"
"ds_read_b128 v[164:167], v4 offset:9216\n"
"v_mfma_f32_16x16x32_bf16 v[44:47], a[252:255], v[188:191], v[44:47]\n"
"v_mfma_f32_16x16x32_bf16 v[40:43], a[248:251], v[188:191], v[40:43]\n"
"ds_read_b128 v[168:171], v4 offset:10240\n"
"s_add_u32 m0, s64, 4096\n"
"v_mfma_f32_16x16x32_bf16 v[40:43], a[240:243], v[184:187], v[40:43]\n"
"buffer_load_dwordx4 v6, s[32:35], s63 offen offset:0 sc1 lds\n"
"v_mfma_f32_16x16x32_bf16 v[40:43], a[232:235], v[180:183], v[40:43]\n"
"ds_read_b128 v[172:175], v4 offset:11264\n"
"s_sub_u32 s62, s62, 1\n"
"v_mfma_f32_16x16x32_bf16 v[40:43], a[224:227], v[176:179], v[40:43]\n"
"s_cbranch_scc0 .L_wl_2\n"
"s_nop 1\n"
"s_add_u32 s68, s68, 593920\n"
"s_nop 3\n"
"v_cvt_pk_bf16_f32 v56, v40, v41\n"
"v_cvt_pk_bf16_f32 v57, v42, v43\n"
"v_cvt_pk_bf16_f32 v58, v44, v45\n"
"v_cvt_pk_bf16_f32 v59, v46, v47\n"
"buffer_store_dwordx4 v[56:59], v2, s[32:35], s68 offen sc1\n"
"v_add_f32 v60, v40, v41\n"
"v_mul_f32 v61, v40, v40\n"
"v_fmac_f32 v61, v41, v41\n"
"v_add_f32 v60, v60, v42\n"
"v_fmac_f32 v61, v42, v42\n"
"v_add_f32 v60, v60, v43\n"
"v_fmac_f32 v61, v43, v43\n"
"v_add_f32 v60, v60, v44\n"
"v_fmac_f32 v61, v44, v44\n"
"v_add_f32 v60, v60, v45\n"
"v_fmac_f32 v61, v45, v45\n"
"v_add_f32 v60, v60, v46\n"
"v_fmac_f32 v61, v46, v46\n"
"v_add_f32 v60, v60, v47\n"
"v_fmac_f32 v61, v47, v47\n"
"s_nop 1\n"
"v_permlane16_swap_b32 v60, v61\n"
"v_add_f32 v60, v60, v61\n"
"s_nop 1\n"
"v_permlane32_swap_b32 v60, v61\n"
"v_add_f32 v60, v60, v61\n"
"buffer_store_dword v60, v15, s[32:35], s68 offen offset:1024 sc1\n"
"s_waitcnt vmcnt(9)\n"
"s_barrier\n"
"buffer_store_dwordx2 v[16:17], v2, s[92:95], s68 idxen sc1\n"
"s_add_u32 s68, s68, 593920\n"
"s_waitcnt vmcnt(1)\n"
"s_barrier\n"
"buffer_store_dwordx2 v[16:17], v2, s[92:95], s68 idxen sc1\n"
"s_branch .L_end\n"
".p2align 6\n"
".L_wv3:\n"
".L_wl_3:\n"
"s_waitcnt lgkmcnt(4)\n"
"v_mfma_f32_16x16x32_bf16 v[32:35], a[0:3], v[128:131], v[32:35]\n"
"ds_read_b128 v[176:179], v4 offset:12288\n"
"v_mfma_f32_16x16x32_bf16 v[32:35], a[8:11], v[132:135], v[32:35]\n"
"buffer_load_dwordx4 v6, s[32:35], s63 offen offset:1024 sc1 lds\n"
"v_mfma_f32_16x16x32_bf16 v[32:35], a[16:19], v[136:139], v[32:35]\n"
"ds_read_b128 v[180:183], v4 offset:13312\n"
"s_add_u32 s68, s68, 593920\n"
"v_mfma_f32_16x16x32_bf16 v[32:35], a[24:27], v[140:143], v[32:35]\n"
"v_cvt_pk_bf16_f32 v56, v40, v41\n"
"v_cvt_pk_bf16_f32 v57, v42, v43\n"
"v_mfma_f32_16x16x32_bf16 v[36:39], a[28:31], v[140:143], v[36:39]\n"
"ds_read_b128 v[184:187], v4 offset:14336\n"
"v_cvt_pk_bf16_f32 v58, v44, v45\n"
"v_mfma_f32_16x16x32_bf16 v[36:39], a[20:23], v[136:139], v[36:39]\n"
"v_cvt_pk_bf16_f32 v59, v46, v47\n"
"v_add_f32 v60, v40, v41\n"
"v_mfma_f32_16x16x32_bf16 v[36:39], a[12:15], v[132:135], v[36:39]\n"
"ds_read_b128 v[188:191], v4 offset:15360\n"
"v_mul_f32 v61, v40, v40\n"
"v_mfma_f32_16x16x32_bf16 v[36:39], a[4:7], v[128:131], v[36:39]\n"
"v_fmac_f32 v61, v41, v41\n"
"v_add_f32 v60, v60, v42\n"
"v_mfma_f32_16x16x32_bf16 v[36:39], a[36:39], v[144:147], v[36:39]\n"
"ds_read_b128 v[128:131], v4 offset:16384\n"
"v_fmac_f32 v61, v42, v42\n"
"v_mfma_f32_16x16x32_bf16 v[36:39], a[44:47], v[148:151], v[36:39]\n"
"buffer_store_dwordx4 v[56:59], v2, s[32:35], s68 offen sc1\n"
"v_mfma_f32_16x16x32_bf16 v[36:39], a[52:55], v[152:155], v[36:39]\n"
"ds_read_b128 v[132:135], v4 offset:17408\n"
"v_add_f32 v60, v60, v43\n"
"v_mfma_f32_16x16x32_bf16 v[36:39], a[60:63], v[156:159], v[36:39]\n"
"v_fmac_f32 v61, v43, v43\n"
"v_add_f32 v60, v60, v44\n"
"v_mfma_f32_16x16x32_bf16 v[32:35], a[56:59], v[156:159], v[32:35]\n"
"ds_read_b128 v[136:139], v4 offset:18432\n"
"v_fmac_f32 v61, v44, v44\n"
"v_mfma_f32_16x16x32_bf16 v[32:35], a[48:51], v[152:155], v[32:35]\n"
"v_add_f32 v60, v60, v45\n"
"v_fmac_f32 v61, v45, v45\n"
"v_mfma_f32_16x16x32_bf16 v[32:35], a[40:43], v[148:151], v[32:35]\n"
"ds_read_b128 v[140:143], v4 offset:19456\n"
"v_add_f32 v60, v60, v46\n"
"v_mfma_f32_16x16x32_bf16 v[32:35], a[32:35], v[144:147], v[32:35]\n"
"v_fmac_f32 v61, v46, v46\n"
"s_waitcnt lgkmcnt(4)\n"
"v_mfma_f32_16x16x32_bf16 v[32:35], a[64:67], v[160:163], v[32:35]\n"
"ds_read_b128 v[144:147], v4 offset:20480\n"
"v_add_f32 v60, v60, v47\n"
"v_mfma_f32_16x16x32_bf16 v[32:35], a[72:75], v[164:167], v[32:35]\n"
"v_fmac_f32 v61, v47, v47\n"
"v_mfma_f32_16x16x32_bf16 v[32:35], a[80:83], v[168:171], v[32:35]\n"
"ds_read_b128 v[148:151], v4 offset:21504\n"
"v_mfma_f32_16x16x32_bf16 v[32:35], a[88:91], v[172:175], v[32:35]\n"
"v_permlane16_swap_b32 v60, v61\n"
"v_mfma_f32_16x16x32_bf16 v[36:39], a[92:95], v[172:175], v[36:39]\n"
"ds_read_b128 v[152:155], v4 offset:22528\n"
"v_add_f32 v60, v60, v61\n"
"v_mfma_f32_16x16x32_bf16 v[36:39], a[84:87], v[168:171], v[36:39]\n"
"buffer_load_dwordx4 v[48:51], v2, s[32:35], s71 offen sc1\n"
"v_mfma_f32_16x16x32_bf16 v[36:39], a[76:79], v[164:167], v[36:39]\n"
"ds_read_b128 v[156:159], v4 offset:23552\n"
"s_add_u32 s71, s71, 593920\n"
"v_mfma_f32_16x16x32_bf16 v[36:39], a[68:71], v[160:163], v[36:39]\n"
"v_permlane32_swap_b32 v60, v61\n"
"v_mfma_f32_16x16x32_bf16 v[36:39], a[100:103], v[176:179], v[36:39]\n"
"ds_read_b128 v[160:163], v4 offset:24576\n"
"v_add_f32 v60, v60, v61\n"
"v_mfma_f32_16x16x32_bf16 v[36:39], a[108:111], v[180:183], v[36:39]\n"
"buffer_store_dword v60, v15, s[32:35], s68 offen offset:1024 sc1\n"
"v_mfma_f32_16x16x32_bf16 v[36:39], a[116:119], v[184:187], v[36:39]\n"
"ds_read_b128 v[164:167], v4 offset:25600\n"
"v_mfma_f32_16x16x32_bf16 v[36:39], a[124:127], v[188:191], v[36:39]\n"
"v_mfma_f32_16x16x32_bf16 v[32:35], a[120:123], v[188:191], v[32:35]\n"
"ds_read_b128 v[168:171], v4 offset:26624\n"
"v_mfma_f32_16x16x32_bf16 v[32:35], a[112:115], v[184:187], v[32:35]\n"
"buffer_load_dwordx4 v6, s[32:35], s63 offen offset:2048 sc1 lds\n"
"v_mfma_f32_16x16x32_bf16 v[32:35], a[104:107], v[180:183], v[32:35]\n"
"ds_read_b128 v[172:175], v4 offset:27648\n"
"v_mfma_f32_16x16x32_bf16 v[32:35], a[96:99], v[176:179], v[32:35]\n"
"s_waitcnt lgkmcnt(4)\n"
"v_mfma_f32_16x16x32_bf16 v[32:35], a[128:131], v[128:131], v[32:35]\n"
"ds_read_b128 v[176:179], v4 offset:28672\n"
"v_mfma_f32_16x16x32_bf16 v[32:35], a[136:139], v[132:135], v[32:35]\n"
"buffer_load_dwordx4 v6, s[32:35], s63 offen offset:3072 sc1 lds\n"
"v_mfma_f32_16x16x32_bf16 v[32:35], a[144:147], v[136:139], v[32:35]\n"
"ds_read_b128 v[180:183], v4 offset:29696\n"
"s_add_u32 s63, s63, 593920\n"
"v_mfma_f32_16x16x32_bf16 v[32:35], a[152:155], v[140:143], v[32:35]\n"
"v_mfma_f32_16x16x32_bf16 v[36:39], a[156:159], v[140:143], v[36:39]\n"
"s_waitcnt vmcnt(11)\n"
"s_barrier\n"
"v_mfma_f32_16x16x32_bf16 v[36:39], a[148:151], v[136:139], v[36:39]\n"
"ds_read_b128 v[184:187], v4 offset:30720\n"
"v_mfma_f32_16x16x32_bf16 v[36:39], a[140:143], v[132:135], v[36:39]\n"
"ds_read_b128 v[188:191], v4 offset:31744\n"
"v_mfma_f32_16x16x32_bf16 v[36:39], a[132:135], v[128:131], v[36:39]\n"
"v_mfma_f32_16x16x32_bf16 v[36:39], a[164:167], v[144:147], v[36:39]\n"
"ds_read_b128 v[128:131], v4 offset:32768\n"
"v_lshlrev_b32 v40, 16, v52\n"
"v_mfma_f32_16x16x32_bf16 v[36:39], a[172:175], v[148:151], v[36:39]\n"
"v_and_b32 v41, 0xffff0000, v52\n"
"v_lshlrev_b32 v42, 16, v53\n"
"v_mfma_f32_16x16x32_bf16 v[36:39], a[180:183], v[152:155], v[36:39]\n"
"ds_read_b128 v[132:135], v4 offset:33792\n"
"v_and_b32 v43, 0xffff0000, v53\n"
"v_mfma_f32_16x16x32_bf16 v[36:39], a[188:191], v[156:159], v[36:39]\n"
"v_lshlrev_b32 v44, 16, v54\n"
"v_and_b32 v45, 0xffff0000, v54\n"
"v_mfma_f32_16x16x32_bf16 v[32:35], a[184:187], v[156:159], v[32:35]\n"
"ds_read_b128 v[136:139], v4 offset:34816\n"
"s_add_u32 m0, s64, 32768\n"
"v_mfma_f32_16x16x32_bf16 v[32:35], a[176:179], v[152:155], v[32:35]\n"
"buffer_load_dwordx4 v2, s[32:35], s63 offen offset:0 sc1 lds\n"
"v_mfma_f32_16x16x32_bf16 v[32:35], a[168:171], v[148:151], v[32:35]\n"
"ds_read_b128 v[140:143], v4 offset:35840\n"
"v_lshlrev_b32 v46, 16, v55\n"
"v_mfma_f32_16x16x32_bf16 v[32:35], a[160:163], v[144:147], v[32:35]\n"
"v_and_b32 v47, 0xffff0000, v55\n"
"s_waitcnt lgkmcnt(4)\n"
"v_mfma_f32_16x16x32_bf16 v[32:35], a[192:195], v[160:163], v[32:35]\n"
"ds_read_b128 v[144:147], v4 offset:36864\n"
"v_mfma_f32_16x16x32_bf16 v[32:35], a[200:203], v[164:167], v[32:35]\n"
"buffer_load_dwordx4 v2, s[32:35], s63 offen offset:1024 sc1 lds\n"
"v_mfma_f32_16x16x32_bf16 v[32:35], a[208:211], v[168:171], v[32:35]\n"
"ds_read_b128 v[148:151], v4 offset:37888\n"
"v_mfma_f32_16x16x32_bf16 v[32:35], a[216:219], v[172:175], v[32:35]\n"
"v_mfma_f32_16x16x32_bf16 v[36:39], a[220:223], v[172:175], v[36:39]\n"
"ds_read_b128 v[152:155], v4 offset:38912\n"
"v_mfma_f32_16x16x32_bf16 v[36:39], a[212:215], v[168:171], v[36:39]\n"
"buffer_load_dwordx4 v2, s[32:35], s63 offen offset:2048 sc1 lds\n"
"v_mfma_f32_16x16x32_bf16 v[36:39], a[204:207], v[164:167], v[36:39]\n"
"ds_read_b128 v[156:159], v4 offset:39936\n"
"v_mfma_f32_16x16x32_bf16 v[36:39], a[196:199], v[160:163], v[36:39]\n"
"v_mfma_f32_16x16x32_bf16 v[36:39], a[228:231], v[176:179], v[36:39]\n"
"ds_read_b128 v[160:163], v4 offset:40960\n"
"v_mfma_f32_16x16x32_bf16 v[36:39], a[236:239], v[180:183], v[36:39]\n"
"buffer_load_dwordx4 v2, s[32:35], s63 offen offset:3072 sc1 lds\n"
"v_mfma_f32_16x16x32_bf16 v[36:39], a[244:247], v[184:187], v[36:39]\n"
"ds_read_b128 v[164:167], v4 offset:41984\n"
"v_mfma_f32_16x16x32_bf16 v[36:39], a[252:255], v[188:191], v[36:39]\n"
"v_mfma_f32_16x16x32_bf16 v[32:35], a[248:251], v[188:191], v[32:35]\n"
"ds_read_b128 v[168:171], v4 offset:43008\n"
"s_add_u32 m0, s64, 36864\n"
"v_mfma_f32_16x16x32_bf16 v[32:35], a[240:243], v[184:187], v[32:35]\n"
"buffer_load_dwordx4 v6, s[32:35], s63 offen offset:0 sc1 lds\n"
"v_mfma_f32_16x16x32_bf16 v[32:35], a[232:235], v[180:183], v[32:35]\n"
"ds_read_b128 v[172:175], v4 offset:44032\n"
"v_mfma_f32_16x16x32_bf16 v[32:35], a[224:227], v[176:179], v[32:35]\n"
"s_waitcnt lgkmcnt(4)\n"
"v_mfma_f32_16x16x32_bf16 v[40:43], a[0:3], v[128:131], v[40:43]\n"
"ds_read_b128 v[176:179], v4 offset:45056\n"
"v_mfma_f32_16x16x32_bf16 v[40:43], a[8:11], v[132:135], v[40:43]\n"
"buffer_load_dwordx4 v6, s[32:35], s63 offen offset:1024 sc1 lds\n"
"v_mfma_f32_16x16x32_bf16 v[40:43], a[16:19], v[136:139], v[40:43]\n"
"ds_read_b128 v[180:183], v4 offset:46080\n"
"s_add_u32 s68, s68, 593920\n"
"v_mfma_f32_16x16x32_bf16 v[40:43], a[24:27], v[140:143], v[40:43]\n"
"v_cvt_pk_bf16_f32 v56, v32, v33\n"
"v_cvt_pk_bf16_f32 v57, v34, v35\n"
"v_mfma_f32_16x16x32_bf16 v[44:47], a[28:31], v[140:143], v[44:47]\n"
"ds_read_b128 v[184:187], v4 offset:47104\n"
"v_cvt_pk_bf16_f32 v58, v36, v37\n"
"v_mfma_f32_16x16x32_bf16 v[44:47], a[20:23], v[136:139], v[44:47]\n"
"v_cvt_pk_bf16_f32 v59, v38, v39\n"
"v_add_f32 v60, v32, v33\n"
"v_mfma_f32_16x16x32_bf16 v[44:47], a[12:15], v[132:135], v[44:47]\n"
"ds_read_b128 v[188:191], v4 offset:48128\n"
"v_mul_f32 v61, v32, v32\n"
"v_mfma_f32_16x16x32_bf16 v[44:47], a[4:7], v[128:131], v[44:47]\n"
"v_fmac_f32 v61, v33, v33\n"
"v_add_f32 v60, v60, v34\n"
"v_mfma_f32_16x16x32_bf16 v[44:47], a[36:39], v[144:147], v[44:47]\n"
"ds_read_b128 v[128:131], v4 offset:49152\n"
"v_fmac_f32 v61, v34, v34\n"
"v_mfma_f32_16x16x32_bf16 v[44:47], a[44:47], v[148:151], v[44:47]\n"
"buffer_store_dwordx4 v[56:59], v2, s[32:35], s68 offen sc1\n"
"v_mfma_f32_16x16x32_bf16 v[44:47], a[52:55], v[152:155], v[44:47]\n"
"ds_read_b128 v[132:135], v4 offset:50176\n"
"v_add_f32 v60, v60, v35\n"
"v_mfma_f32_16x16x32_bf16 v[44:47], a[60:63], v[156:159], v[44:47]\n"
"v_fmac_f32 v61, v35, v35\n"
"v_add_f32 v60, v60, v36\n"
"v_mfma_f32_16x16x32_bf16 v[40:43], a[56:59], v[156:159], v[40:43]\n"
"ds_read_b128 v[136:139], v4 offset:51200\n"
"v_fmac_f32 v61, v36, v36\n"
"v_mfma_f32_16x16x32_bf16 v[40:43], a[48:51], v[152:155], v[40:43]\n"
"v_add_f32 v60, v60, v37\n"
"v_fmac_f32 v61, v37, v37\n"
"v_mfma_f32_16x16x32_bf16 v[40:43], a[40:43], v[148:151], v[40:43]\n"
"ds_read_b128 v[140:143], v4 offset:52224\n"
"v_add_f32 v60, v60, v38\n"
"v_mfma_f32_16x16x32_bf16 v[40:43], a[32:35], v[144:147], v[40:43]\n"
"v_fmac_f32 v61, v38, v38\n"
"s_waitcnt lgkmcnt(4)\n"
"v_mfma_f32_16x16x32_bf16 v[40:43], a[64:67], v[160:163], v[40:43]\n"
"ds_read_b128 v[144:147], v4 offset:53248\n"
"v_add_f32 v60, v60, v39\n"
"v_mfma_f32_16x16x32_bf16 v[40:43], a[72:75], v[164:167], v[40:43]\n"
"v_fmac_f32 v61, v39, v39\n"
"v_mfma_f32_16x16x32_bf16 v[40:43], a[80:83], v[168:171], v[40:43]\n"
"ds_read_b128 v[148:151], v4 offset:54272\n"
"v_mfma_f32_16x16x32_bf16 v[40:43], a[88:91], v[172:175], v[40:43]\n"
"v_permlane16_swap_b32 v60, v61\n"
"v_mfma_f32_16x16x32_bf16 v[44:47], a[92:95], v[172:175], v[44:47]\n"
"ds_read_b128 v[152:155], v4 offset:55296\n"
"v_add_f32 v60, v60, v61\n"
"v_mfma_f32_16x16x32_bf16 v[44:47], a[84:87], v[168:171], v[44:47]\n"
"buffer_load_dwordx4 v[52:55], v2, s[32:35], s71 offen sc1\n"
"v_mfma_f32_16x16x32_bf16 v[44:47], a[76:79], v[164:167], v[44:47]\n"
"ds_read_b128 v[156:159], v4 offset:56320\n"
"s_add_u32 s71, s71, 593920\n"
"v_mfma_f32_16x16x32_bf16 v[44:47], a[68:71], v[160:163], v[44:47]\n"
"v_permlane32_swap_b32 v60, v61\n"
"v_mfma_f32_16x16x32_bf16 v[44:47], a[100:103], v[176:179], v[44:47]\n"
"ds_read_b128 v[160:163], v4 offset:57344\n"
"v_add_f32 v60, v60, v61\n"
"v_mfma_f32_16x16x32_bf16 v[44:47], a[108:111], v[180:183], v[44:47]\n"
"buffer_store_dword v60, v15, s[32:35], s68 offen offset:1024 sc1\n"
"v_mfma_f32_16x16x32_bf16 v[44:47], a[116:119], v[184:187], v[44:47]\n"
"ds_read_b128 v[164:167], v4 offset:58368\n"
"v_mfma_f32_16x16x32_bf16 v[44:47], a[124:127], v[188:191], v[44:47]\n"
"v_mfma_f32_16x16x32_bf16 v[40:43], a[120:123], v[188:191], v[40:43]\n"
"ds_read_b128 v[168:171], v4 offset:59392\n"
"v_mfma_f32_16x16x32_bf16 v[40:43], a[112:115], v[184:187], v[40:43]\n"
"buffer_load_dwordx4 v6, s[32:35], s63 offen offset:2048 sc1 lds\n"
"v_mfma_f32_16x16x32_bf16 v[40:43], a[104:107], v[180:183], v[40:43]\n"
"ds_read_b128 v[172:175], v4 offset:60416\n"
"v_mfma_f32_16x16x32_bf16 v[40:43], a[96:99], v[176:179], v[40:43]\n"
"s_waitcnt lgkmcnt(4)\n"
"v_mfma_f32_16x16x32_bf16 v[40:43], a[128:131], v[128:131], v[40:43]\n"
"ds_read_b128 v[176:179], v4 offset:61440\n"
"v_mfma_f32_16x16x32_bf16 v[40:43], a[136:139], v[132:135], v[40:43]\n"
"buffer_load_dwordx4 v6, s[32:35], s63 offen offset:3072 sc1 lds\n"
"v_mfma_f32_16x16x32_bf16 v[40:43], a[144:147], v[136:139], v[40:43]\n"
"ds_read_b128 v[180:183], v4 offset:62464\n"
"s_add_u32 s63, s63, 593920\n"
"v_mfma_f32_16x16x32_bf16 v[40:43], a[152:155], v[140:143], v[40:43]\n"
"s_xor_b32 s64, s64, s65\n"
"v_mfma_f32_16x16x32_bf16 v[44:47], a[156:159], v[140:143], v[44:47]\n"
"s_waitcnt vmcnt(11)\n"
"s_barrier\n"
"v_mfma_f32_16x16x32_bf16 v[44:47], a[148:151], v[136:139], v[44:47]\n"
"ds_read_b128 v[184:187], v4 offset:63488\n"
"v_mfma_f32_16x16x32_bf16 v[44:47], a[140:143], v[132:135], v[44:47]\n"
"ds_read_b128 v[188:191], v4 offset:64512\n"
"v_xor_b32 v4, 65536, v4\n"
"v_mfma_f32_16x16x32_bf16 v[44:47], a[132:135], v[128:131], v[44:47]\n"
"v_mfma_f32_16x16x32_bf16 v[44:47], a[164:167], v[144:147], v[44:47]\n"
"ds_read_b128 v[128:131], v4 offset:0\n"
"v_lshlrev_b32 v32, 16, v48\n"
"v_mfma_f32_16x16x32_bf16 v[44:47], a[172:175], v[148:151], v[44:47]\n"
"v_and_b32 v33, 0xffff0000, v48\n"
"v_lshlrev_b32 v34, 16, v49\n"
"v_mfma_f32_16x16x32_bf16 v[44:47], a[180:183], v[152:155], v[44:47]\n"
"ds_read_b128 v[132:135], v4 offset:1024\n"
"v_and_b32 v35, 0xffff0000, v49\n"
"v_mfma_f32_16x16x32_bf16 v[44:47], a[188:191], v[156:159], v[44:47]\n"
"v_lshlrev_b32 v36, 16, v50\n"
"v_and_b32 v37, 0xffff0000, v50\n"
"v_mfma_f32_16x16x32_bf16 v[40:43], a[184:187], v[156:159], v[40:43]\n"
"ds_read_b128 v[136:139], v4 offset:2048\n"
"s_add_u32 m0, s64, 0\n"
"v_mfma_f32_16x16x32_bf16 v[40:43], a[176:179], v[152:155], v[40:43]\n"
"buffer_load_dwordx4 v2, s[32:35], s63 offen offset:0 sc1 lds\n"
"v_mfma_f32_16x16x32_bf16 v[40:43], a[168:171], v[148:151], v[40:43]\n"
"ds_read_b128 v[140:143], v4 offset:3072\n"
"v_lshlrev_b32 v38, 16, v51\n"
"v_mfma_f32_16x16x32_bf16 v[40:43], a[160:163], v[144:147], v[40:43]\n"
"v_and_b32 v39, 0xffff0000, v51\n"
"s_waitcnt lgkmcnt(4)\n"
"v_mfma_f32_16x16x32_bf16 v[40:43], a[192:195], v[160:163], v[40:43]\n"
"ds_read_b128 v[144:147], v4 offset:4096\n"
"v_mfma_f32_16x16x32_bf16 v[40:43], a[200:203], v[164:167], v[40:43]\n"
"buffer_load_dwordx4 v2, s[32:35], s63 offen offset:1024 sc1 lds\n"
"v_mfma_f32_16x16x32_bf16 v[40:43], a[208:211], v[168:171], v[40:43]\n"
"ds_read_b128 v[148:151], v4 offset:5120\n"
"v_mfma_f32_16x16x32_bf16 v[40:43], a[216:219], v[172:175], v[40:43]\n"
"v_mfma_f32_16x16x32_bf16 v[44:47], a[220:223], v[172:175], v[44:47]\n"
"ds_read_b128 v[152:155], v4 offset:6144\n"
"v_mfma_f32_16x16x32_bf16 v[44:47], a[212:215], v[168:171], v[44:47]\n"
"buffer_load_dwordx4 v2, s[32:35], s63 offen offset:2048 sc1 lds\n"
"v_mfma_f32_16x16x32_bf16 v[44:47], a[204:207], v[164:167], v[44:47]\n"
"ds_read_b128 v[156:159], v4 offset:7168\n"
"v_mfma_f32_16x16x32_bf16 v[44:47], a[196:199], v[160:163], v[44:47]\n"
"v_mfma_f32_16x16x32_bf16 v[44:47], a[228:231], v[176:179], v[44:47]\n"
"ds_read_b128 v[160:163], v4 offset:8192\n"
"v_mfma_f32_16x16x32_bf16 v[44:47], a[236:239], v[180:183], v[44:47]\n"
"buffer_load_dwordx4 v2, s[32:35], s63 offen offset:3072 sc1 lds\n"
"v_mfma_f32_16x16x32_bf16 v[44:47], a[244:247], v[184:187], v[44:47]\n"
"ds_read_b128 v[164:167], v4 offset:9216\n"
"v_mfma_f32_16x16x32_bf16 v[44:47], a[252:255], v[188:191], v[44:47]\n"
"v_mfma_f32_16x16x32_bf16 v[40:43], a[248:251], v[188:191], v[40:43]\n"
"ds_read_b128 v[168:171], v4 offset:10240\n"
"s_add_u32 m0, s64, 4096\n"
"v_mfma_f32_16x16x32_bf16 v[40:43], a[240:243], v[184:187], v[40:43]\n"
"buffer_load_dwordx4 v6, s[32:35], s63 offen offset:0 sc1 lds\n"
"v_mfma_f32_16x16x32_bf16 v[40:43], a[232:235], v[180:183], v[40:43]\n"
"ds_read_b128 v[172:175], v4 offset:11264\n"
"s_sub_u32 s62, s62, 1\n"
"v_mfma_f32_16x16x32_bf16 v[40:43], a[224:227], v[176:179], v[40:43]\n"
"s_cbranch_scc0 .L_wl_3\n"
"s_nop 1\n"
"s_add_u32 s68, s68, 593920\n"
"s_nop 3\n"
"v_cvt_pk_bf16_f32 v56, v40, v41\n"
"v_cvt_pk_bf16_f32 v57, v42, v43\n"
"v_cvt_pk_bf16_f32 v58, v44, v45\n"
"v_cvt_pk_bf16_f32 v59, v46, v47\n"
"buffer_store_dwordx4 v[56:59], v2, s[32:35], s68 offen sc1\n"
"v_add_f32 v60, v40, v41\n"
"v_mul_f32 v61, v40, v40\n"
"v_fmac_f32 v61, v41, v41\n"
"v_add_f32 v60, v60, v42\n"
"v_fmac_f32 v61, v42, v42\n"
"v_add_f32 v60, v60, v43\n"
"v_fmac_f32 v61, v43, v43\n"
"v_add_f32 v60, v60, v44\n"
"v_fmac_f32 v61, v44, v44\n"
"v_add_f32 v60, v60, v45\n"
"v_fmac_f32 v61, v45, v45\n"
"v_add_f32 v60, v60, v46\n"
"v_fmac_f32 v61, v46, v46\n"
"v_add_f32 v60, v60, v47\n"
"v_fmac_f32 v61, v47, v47\n"
"s_nop 1\n"
"v_permlane16_swap_b32 v60, v61\n"
"v_add_f32 v60, v60, v61\n"
"s_nop 1\n"
"v_permlane32_swap_b32 v60, v61\n"
"v_add_f32 v60, v60, v61\n"
"buffer_store_dword v60, v15, s[32:35], s68 offen offset:1024 sc1\n"
"s_waitcnt vmcnt(9)\n"
"s_barrier\n"
"buffer_store_dwordx2 v[16:17], v2, s[92:95], s68 idxen sc1\n"
"s_add_u32 s68, s68, 593920\n"
"s_waitcnt vmcnt(1)\n"
"s_barrier\n"
"buffer_store_dwordx2 v[16:17], v2, s[92:95], s68 idxen sc1\n"
"s_branch .L_end\n"
".L_h:\n"
"v_mov_b32 v16, 0x3f803f80\n"
"v_mov_b32 v17, 0x3f803f80\n"
"v_mov_b32 v18, 0x3f803f80\n"
"v_mov_b32 v19, 0x3f803f80\n"
"s_add_u32 s36, s14, 0x0\n"
"s_addc_u32 s37, s15, 0x0\n"
"s_and_b32 s37, s37, 0xffff\n"
"s_mov_b32 s38, 0x700000\n"
"s_mov_b32 s39, 0x20000\n"
"s_add_u32 s40, s16, 0xfffffc00\n"
"s_addc_u32 s41, s17, 0xffffffff\n"
"s_and_b32 s41, s41, 0xffff\n"
"s_mov_b32 s42, 0x700400\n"
"s_mov_b32 s43, 0x20000\n"
"s_lshl_b32 s96, s27, 7\n"
"s_add_u32 s96, s96, 6912\n"
"v_and_b32 v8, 1, v1\n"
"v_bfe_u32 v3, v1, 1, 1\n"
"v_lshl_add_u32 v8, v8, 1, v3\n"
"v_mul_u32_u24 v8, 8, v8\n"
"s_lshl_b32 s48, s26, 5\n"
"v_add_u32 v8, s48, v8\n"
"v_bfe_u32 v3, v1, 4, 1\n"
"v_lshl_add_u32 v8, v3, 10, v8\n"
"v_lshrrev_b32 v4, 3, v1\n"
"v_bfe_u32 v5, v1, 4, 1\n"
"s_and_b32 s48, s26, 1\n"
"s_lshl_b32 s48, s48, 1\n"
"v_or_b32 v5, s48, v5\n"
"v_lshlrev_b32 v5, 1, v5\n"
"v_and_b32 v6, 7, v1\n"
"v_xor_b32 v5, v5, v6\n"
"v_mul_u32_u24 v4, 7168, v4\n"
"v_lshl_add_u32 v7, v5, 4, v4\n"
"s_mul_i32 s48, s26, 57344\n"
"s_add_u32 s96, s96, s48\n"
"s_mul_i32 s97, s26, 2048\n"
"s_add_u32 m0, s97, 0\n"
"s_nop 0\n"
"buffer_load_dwordx4 v7, s[36:39], s96 offen offset:0 lds\n"
"buffer_load_dwordx4 v7, s[40:43], s96 offen offset:1024 lds\n"
"s_add_u32 s96, s96, 229376\n"
"s_add_u32 m0, s97, 8192\n"
"s_nop 0\n"
"buffer_load_dwordx4 v7, s[36:39], s96 offen offset:0 lds\n"
"buffer_load_dwordx4 v7, s[40:43], s96 offen offset:1024 lds\n"
"s_add_u32 s96, s96, 229376\n"
"s_add_u32 m0, s97, 16384\n"
"s_nop 0\n"
"buffer_load_dwordx4 v7, s[36:39], s96 offen offset:0 lds\n"
"buffer_load_dwordx4 v7, s[40:43], s96 offen offset:1024 lds\n"
"s_add_u32 s96, s96, 229376\n"
"s_add_u32 m0, s97, 24576\n"
"s_nop 0\n"
"buffer_load_dwordx4 v7, s[36:39], s96 offen offset:0 lds\n"
"buffer_load_dwordx4 v7, s[40:43], s96 offen offset:1024 lds\n"
"s_add_u32 s96, s96, 229376\n"
"s_add_u32 m0, s97, 32768\n"
"s_nop 0\n"
"buffer_load_dwordx4 v7, s[36:39], s96 offen offset:0 lds\n"
"buffer_load_dwordx4 v7, s[40:43], s96 offen offset:1024 lds\n"
"s_add_u32 s96, s96, 229376\n"
"s_add_u32 m0, s97, 40960\n"
"s_nop 0\n"
"buffer_load_dwordx4 v7, s[36:39], s96 offen offset:0 lds\n"
"buffer_load_dwordx4 v7, s[40:43], s96 offen offset:1024 lds\n"
"s_add_u32 s96, s96, 229376\n"
"s_add_u32 m0, s97, 49152\n"
"s_nop 0\n"
"buffer_load_dwordx4 v7, s[36:39], s96 offen offset:0 lds\n"
"buffer_load_dwordx4 v7, s[40:43], s96 offen offset:1024 lds\n"
"s_add_u32 s96, s96, 229376\n"
"s_add_u32 m0, s97, 57344\n"
"s_nop 0\n"
"buffer_load_dwordx4 v7, s[36:39], s96 offen offset:0 lds\n"
"buffer_load_dwordx4 v7, s[40:43], s96 offen offset:1024 lds\n"
"s_add_u32 s96, s96, 229376\n"
"s_add_u32 m0, s97, 65536\n"
"s_nop 0\n"
"buffer_load_dwordx4 v7, s[36:39], s96 offen offset:0 lds\n"
"buffer_load_dwordx4 v7, s[40:43], s96 offen offset:1024 lds\n"
"s_add_u32 s96, s96, 229376\n"
"s_add_u32 m0, s97, 73728\n"
"s_nop 0\n"
"buffer_load_dwordx4 v7, s[36:39], s96 offen offset:0 lds\n"
"buffer_load_dwordx4 v7, s[40:43], s96 offen offset:1024 lds\n"
"s_add_u32 s96, s96, 229376\n"
"s_add_u32 m0, s97, 81920\n"
"s_nop 0\n"
"buffer_load_dwordx4 v7, s[36:39], s96 offen offset:0 lds\n"
"buffer_load_dwordx4 v7, s[40:43], s96 offen offset:1024 lds\n"
"s_add_u32 s96, s96, 229376\n"
"s_add_u32 m0, s97, 90112\n"
"s_nop 0\n"
"buffer_load_dwordx4 v7, s[36:39], s96 offen offset:0 lds\n"
"buffer_load_dwordx4 v7, s[40:43], s96 offen offset:1024 lds\n"
"s_add_u32 s96, s96, 229376\n"
"s_add_u32 m0, s97, 98304\n"
"s_nop 0\n"
"buffer_load_dwordx4 v7, s[36:39], s96 offen offset:0 lds\n"
"buffer_load_dwordx4 v7, s[40:43], s96 offen offset:1024 lds\n"
"s_add_u32 s96, s96, 229376\n"
"s_add_u32 m0, s97, 106496\n"
"s_nop 0\n"
"buffer_load_dwordx4 v7, s[36:39], s96 offen offset:0 lds\n"
"buffer_load_dwordx4 v7, s[40:43], s96 offen offset:1024 lds\n"
"s_add_u32 s96, s96, 229376\n"
"v_lshrrev_b32 v5, 5, v1\n"
"v_bfe_u32 v4, v1, 3, 1\n"
"v_and_b32 v6, 1, v5\n"
"v_lshl_add_u32 v4, v6, 1, v4\n"
"v_lshlrev_b32 v4, 5, v4\n"
"v_mul_u32_u24 v5, 2048, v5\n"
"v_bfe_u32 v6, v1, 2, 2\n"
"v_lshl_add_u32 v5, v6, 7, v5\n"
"v_xor_b32 v8, v4, v8\n"
"v_add_u32 v8, v5, v8\n"
"v_add_u32 v9, 65536, v8\n"
"s_waitcnt vmcnt(24)\n"
"s_barrier\n"
"s_add_u32 m0, s97, 114688\n"
"s_nop 0\n"
"buffer_load_dwordx4 v7, s[36:39], s96 offen offset:0 lds\n"
"buffer_load_dwordx4 v7, s[40:43], s96 offen offset:1024 lds\n"
"s_add_u32 s96, s96, 229376\n"
"s_add_u32 m0, s97, 122880\n"
"s_nop 0\n"
"buffer_load_dwordx4 v7, s[36:39], s96 offen offset:0 lds\n"
"buffer_load_dwordx4 v7, s[40:43], s96 offen offset:1024 lds\n"
"s_add_u32 s96, s96, 229376\n"
"ds_read_b64_tr_b16 a[0:1], v8 offset:0\n"
"ds_read_b64_tr_b16 a[2:3], v8 offset:512\n"
"ds_read_b64_tr_b16 a[4:5], v8 offset:4096\n"
"ds_read_b64_tr_b16 a[6:7], v8 offset:4608\n"
"ds_read_b64_tr_b16 a[32:33], v8 offset:8192\n"
"ds_read_b64_tr_b16 a[34:35], v8 offset:8704\n"
"ds_read_b64_tr_b16 a[36:37], v8 offset:12288\n"
"ds_read_b64_tr_b16 a[38:39], v8 offset:12800\n"
"s_waitcnt lgkmcnt(0)\n"
"v_mfma_f32_32x32x16_bf16 v[112:127], a[0:3], v[16:19], 0\n"
"v_mfma_f32_32x32x16_bf16 v[112:127], a[4:7], v[16:19], v[112:127]\n"
"v_mfma_f32_32x32x16_bf16 v[112:127], a[32:35], v[16:19], v[112:127]\n"
"v_mfma_f32_32x32x16_bf16 v[112:127], a[36:39], v[16:19], v[112:127]\n"
"s_waitcnt vmcnt(24)\n"
"s_barrier\n"
"s_add_u32 m0, s97, 0\n"
"s_nop 0\n"
"buffer_load_dwordx4 v7, s[36:39], s96 offen offset:0 lds\n"
"buffer_load_dwordx4 v7, s[40:43], s96 offen offset:1024 lds\n"
"s_add_u32 s96, s96, 229376\n"
"s_add_u32 m0, s97, 8192\n"
"s_nop 0\n"
"buffer_load_dwordx4 v7, s[36:39], s96 offen offset:0 lds\n"
"buffer_load_dwordx4 v7, s[40:43], s96 offen offset:1024 lds\n"
"s_add_u32 s96, s96, 229376\n"
"ds_read_b64_tr_b16 a[64:65], v8 offset:16384\n"
"ds_read_b64_tr_b16 a[66:67], v8 offset:16896\n"
"ds_read_b64_tr_b16 a[68:69], v8 offset:20480\n"
"ds_read_b64_tr_b16 a[70:71], v8 offset:20992\n"
"ds_read_b64_tr_b16 a[96:97], v8 offset:24576\n"
"ds_read_b64_tr_b16 a[98:99], v8 offset:25088\n"
"ds_read_b64_tr_b16 a[100:101], v8 offset:28672\n"
"ds_read_b64_tr_b16 a[102:103], v8 offset:29184\n"
"s_waitcnt lgkmcnt(0)\n"
"v_mfma_f32_32x32x16_bf16 v[112:127], a[64:67], v[16:19], v[112:127]\n"
"v_mfma_f32_32x32x16_bf16 v[112:127], a[68:71], v[16:19], v[112:127]\n"
"v_mfma_f32_32x32x16_bf16 v[112:127], a[96:99], v[16:19], v[112:127]\n"
"v_mfma_f32_32x32x16_bf16 v[112:127], a[100:103], v[16:19], v[112:127]\n"
"s_waitcnt vmcnt(24)\n"
"s_barrier\n"
"s_add_u32 m0, s97, 16384\n"
"s_nop 0\n"
"buffer_load_dwordx4 v7, s[36:39], s96 offen offset:0 lds\n"
"buffer_load_dwordx4 v7, s[40:43], s96 offen offset:1024 lds\n"
"s_add_u32 s96, s96, 229376\n"
"s_add_u32 m0, s97, 24576\n"
"s_nop 0\n"
"buffer_load_dwordx4 v7, s[36:39], s96 offen offset:0 lds\n"
"buffer_load_dwordx4 v7, s[40:43], s96 offen offset:1024 lds\n"
"s_add_u32 s96, s96, 229376\n"
"ds_read_b64_tr_b16 a[128:129], v8 offset:32768\n"
"ds_read_b64_tr_b16 a[130:131], v8 offset:33280\n"
"ds_read_b64_tr_b16 a[132:133], v8 offset:36864\n"
"ds_read_b64_tr_b16 a[134:135], v8 offset:37376\n"
"ds_read_b64_tr_b16 a[160:161], v8 offset:40960\n"
"ds_read_b64_tr_b16 a[162:163], v8 offset:41472\n"
"ds_read_b64_tr_b16 a[164:165], v8 offset:45056\n"
"ds_read_b64_tr_b16 a[166:167], v8 offset:45568\n"
"s_waitcnt lgkmcnt(0)\n"
"v_mfma_f32_32x32x16_bf16 v[112:127], a[128:131], v[16:19], v[112:127]\n"
"v_mfma_f32_32x32x16_bf16 v[112:127], a[132:135], v[16:19], v[112:127]\n"
"v_mfma_f32_32x32x16_bf16 v[112:127], a[160:163], v[16:19], v[112:127]\n"
"v_mfma_f32_32x32x16_bf16 v[112:127], a[164:167], v[16:19], v[112:127]\n"
"s_waitcnt vmcnt(24)\n"
"s_barrier\n"
"s_add_u32 m0, s97, 32768\n"
"s_nop 0\n"
"buffer_load_dwordx4 v7, s[36:39], s96 offen offset:0 lds\n"
"buffer_load_dwordx4 v7, s[40:43], s96 offen offset:1024 lds\n"
"s_add_u32 s96, s96, 229376\n"
"s_add_u32 m0, s97, 40960\n"
"s_nop 0\n"
"buffer_load_dwordx4 v7, s[36:39], s96 offen offset:0 lds\n"
"buffer_load_dwordx4 v7, s[40:43], s96 offen offset:1024 lds\n"
"s_add_u32 s96, s96, 229376\n"
"ds_read_b64_tr_b16 a[192:193], v8 offset:49152\n"
"ds_read_b64_tr_b16 a[194:195], v8 offset:49664\n"
"ds_read_b64_tr_b16 a[196:197], v8 offset:53248\n"
"ds_read_b64_tr_b16 a[198:199], v8 offset:53760\n"
"ds_read_b64_tr_b16 a[224:225], v8 offset:57344\n"
"ds_read_b64_tr_b16 a[226:227], v8 offset:57856\n"
"ds_read_b64_tr_b16 a[228:229], v8 offset:61440\n"
"ds_read_b64_tr_b16 a[230:231], v8 offset:61952\n"
"s_waitcnt lgkmcnt(0)\n"
"v_mfma_f32_32x32x16_bf16 v[112:127], a[192:195], v[16:19], v[112:127]\n"
"v_mfma_f32_32x32x16_bf16 v[112:127], a[196:199], v[16:19], v[112:127]\n"
"v_mfma_f32_32x32x16_bf16 v[112:127], a[224:227], v[16:19], v[112:127]\n"
"v_mfma_f32_32x32x16_bf16 v[112:127], a[228:231], v[16:19], v[112:127]\n"
"s_waitcnt vmcnt(24)\n"
"s_barrier\n"
"s_add_u32 m0, s97, 49152\n"
"s_nop 0\n"
"buffer_load_dwordx4 v7, s[36:39], s96 offen offset:0 lds\n"
"buffer_load_dwordx4 v7, s[40:43], s96 offen offset:1024 lds\n"
"s_add_u32 s96, s96, 229376\n"
"s_add_u32 m0, s97, 57344\n"
"s_nop 0\n"
"buffer_load_dwordx4 v7, s[36:39], s96 offen offset:0 lds\n"
"buffer_load_dwordx4 v7, s[40:43], s96 offen offset:1024 lds\n"
"s_add_u32 s96, s96, 229376\n"
"ds_read_b64_tr_b16 a[8:9], v9 offset:0\n"
"ds_read_b64_tr_b16 a[10:11], v9 offset:512\n"
"ds_read_b64_tr_b16 a[12:13], v9 offset:4096\n"
"ds_read_b64_tr_b16 a[14:15], v9 offset:4608\n"
"ds_read_b64_tr_b16 a[40:41], v9 offset:8192\n"
"ds_read_b64_tr_b16 a[42:43], v9 offset:8704\n"
"ds_read_b64_tr_b16 a[44:45], v9 offset:12288\n"
"ds_read_b64_tr_b16 a[46:47], v9 offset:12800\n"
"s_waitcnt lgkmcnt(0)\n"
"v_mfma_f32_32x32x16_bf16 v[112:127], a[8:11], v[16:19], v[112:127]\n"
"v_mfma_f32_32x32x16_bf16 v[112:127], a[12:15], v[16:19], v[112:127]\n"
"v_mfma_f32_32x32x16_bf16 v[112:127], a[40:43], v[16:19], v[112:127]\n"
"v_mfma_f32_32x32x16_bf16 v[112:127], a[44:47], v[16:19], v[112:127]\n"
"s_waitcnt vmcnt(24)\n"
"s_barrier\n"
"s_add_u32 m0, s97, 65536\n"
"s_nop 0\n"
"buffer_load_dwordx4 v7, s[36:39], s96 offen offset:0 lds\n"
"buffer_load_dwordx4 v7, s[40:43], s96 offen offset:1024 lds\n"
"s_add_u32 s96, s96, 229376\n"
"s_add_u32 m0, s97, 73728\n"
"s_nop 0\n"
"buffer_load_dwordx4 v7, s[36:39], s96 offen offset:0 lds\n"
"buffer_load_dwordx4 v7, s[40:43], s96 offen offset:1024 lds\n"
"s_add_u32 s96, s96, 229376\n"
"ds_read_b64_tr_b16 a[72:73], v9 offset:16384\n"
"ds_read_b64_tr_b16 a[74:75], v9 offset:16896\n"
"ds_read_b64_tr_b16 a[76:77], v9 offset:20480\n"
"ds_read_b64_tr_b16 a[78:79], v9 offset:20992\n"
"ds_read_b64_tr_b16 a[104:105], v9 offset:24576\n"
"ds_read_b64_tr_b16 a[106:107], v9 offset:25088\n"
"ds_read_b64_tr_b16 a[108:109], v9 offset:28672\n"
"ds_read_b64_tr_b16 a[110:111], v9 offset:29184\n"
"s_waitcnt lgkmcnt(0)\n"
"v_mfma_f32_32x32x16_bf16 v[112:127], a[72:75], v[16:19], v[112:127]\n"
"v_mfma_f32_32x32x16_bf16 v[112:127], a[76:79], v[16:19], v[112:127]\n"
"v_mfma_f32_32x32x16_bf16 v[112:127], a[104:107], v[16:19], v[112:127]\n"
"v_mfma_f32_32x32x16_bf16 v[112:127], a[108:111], v[16:19], v[112:127]\n"
"s_waitcnt vmcnt(24)\n"
"s_barrier\n"
"s_add_u32 m0, s97, 81920\n"
"s_nop 0\n"
"buffer_load_dwordx4 v7, s[36:39], s96 offen offset:0 lds\n"
"buffer_load_dwordx4 v7, s[40:43], s96 offen offset:1024 lds\n"
"s_add_u32 s96, s96, 229376\n"
"s_add_u32 m0, s97, 90112\n"
"s_nop 0\n"
"buffer_load_dwordx4 v7, s[36:39], s96 offen offset:0 lds\n"
"buffer_load_dwordx4 v7, s[40:43], s96 offen offset:1024 lds\n"
"s_add_u32 s96, s96, 229376\n"
"ds_read_b64_tr_b16 a[136:137], v9 offset:32768\n"
"ds_read_b64_tr_b16 a[138:139], v9 offset:33280\n"
"ds_read_b64_tr_b16 a[140:141], v9 offset:36864\n"
"ds_read_b64_tr_b16 a[142:143], v9 offset:37376\n"
"ds_read_b64_tr_b16 a[168:169], v9 offset:40960\n"
"ds_read_b64_tr_b16 a[170:171], v9 offset:41472\n"
"ds_read_b64_tr_b16 a[172:173], v9 offset:45056\n"
"ds_read_b64_tr_b16 a[174:175], v9 offset:45568\n"
"s_waitcnt lgkmcnt(0)\n"
"v_mfma_f32_32x32x16_bf16 v[112:127], a[136:139], v[16:19], v[112:127]\n"
"v_mfma_f32_32x32x16_bf16 v[112:127], a[140:143], v[16:19], v[112:127]\n"
"v_mfma_f32_32x32x16_bf16 v[112:127], a[168:171], v[16:19], v[112:127]\n"
"v_mfma_f32_32x32x16_bf16 v[112:127], a[172:175], v[16:19], v[112:127]\n"
"s_waitcnt vmcnt(24)\n"
"s_barrier\n"
"s_add_u32 m0, s97, 98304\n"
"s_nop 0\n"
"buffer_load_dwordx4 v7, s[36:39], s96 offen offset:0 lds\n"
"buffer_load_dwordx4 v7, s[40:43], s96 offen offset:1024 lds\n"
"s_add_u32 s96, s96, 229376\n"
"s_add_u32 m0, s97, 106496\n"
"s_nop 0\n"
"buffer_load_dwordx4 v7, s[36:39], s96 offen offset:0 lds\n"
"buffer_load_dwordx4 v7, s[40:43], s96 offen offset:1024 lds\n"
"s_add_u32 s96, s96, 229376\n"
"ds_read_b64_tr_b16 a[200:201], v9 offset:49152\n"
"ds_read_b64_tr_b16 a[202:203], v9 offset:49664\n"
"ds_read_b64_tr_b16 a[204:205], v9 offset:53248\n"
"ds_read_b64_tr_b16 a[206:207], v9 offset:53760\n"
"ds_read_b64_tr_b16 a[232:233], v9 offset:57344\n"
"ds_read_b64_tr_b16 a[234:235], v9 offset:57856\n"
"ds_read_b64_tr_b16 a[236:237], v9 offset:61440\n"
"ds_read_b64_tr_b16 a[238:239], v9 offset:61952\n"
"s_waitcnt lgkmcnt(0)\n"
"v_mfma_f32_32x32x16_bf16 v[112:127], a[200:203], v[16:19], v[112:127]\n"
"v_mfma_f32_32x32x16_bf16 v[112:127], a[204:207], v[16:19], v[112:127]\n"
"v_mfma_f32_32x32x16_bf16 v[112:127], a[232:235], v[16:19], v[112:127]\n"
"v_mfma_f32_32x32x16_bf16 v[112:127], a[236:239], v[16:19], v[112:127]\n"
"s_waitcnt vmcnt(24)\n"
"s_barrier\n"
"s_add_u32 m0, s97, 114688\n"
"s_nop 0\n"
"buffer_load_dwordx4 v7, s[36:39], s96 offen offset:0 lds\n"
"buffer_load_dwordx4 v7, s[40:43], s96 offen offset:1024 lds\n"
"s_add_u32 s96, s96, 229376\n"
"s_add_u32 m0, s97, 122880\n"
"s_nop 0\n"
"buffer_load_dwordx4 v7, s[36:39], s96 offen offset:0 lds\n"
"buffer_load_dwordx4 v7, s[40:43], s96 offen offset:1024 lds\n"
"s_add_u32 s96, s96, 229376\n"
"ds_read_b64_tr_b16 a[16:17], v8 offset:0\n"
"ds_read_b64_tr_b16 a[18:19], v8 offset:512\n"
"ds_read_b64_tr_b16 a[20:21], v8 offset:4096\n"
"ds_read_b64_tr_b16 a[22:23], v8 offset:4608\n"
"ds_read_b64_tr_b16 a[48:49], v8 offset:8192\n"
"ds_read_b64_tr_b16 a[50:51], v8 offset:8704\n"
"ds_read_b64_tr_b16 a[52:53], v8 offset:12288\n"
"ds_read_b64_tr_b16 a[54:55], v8 offset:12800\n"
"s_waitcnt lgkmcnt(0)\n"
"v_mfma_f32_32x32x16_bf16 v[112:127], a[16:19], v[16:19], v[112:127]\n"
"v_mfma_f32_32x32x16_bf16 v[112:127], a[20:23], v[16:19], v[112:127]\n"
"v_mfma_f32_32x32x16_bf16 v[112:127], a[48:51], v[16:19], v[112:127]\n"
"v_mfma_f32_32x32x16_bf16 v[112:127], a[52:55], v[16:19], v[112:127]\n"
"s_waitcnt vmcnt(24)\n"
"s_barrier\n"
"ds_read_b64_tr_b16 a[80:81], v8 offset:16384\n"
"ds_read_b64_tr_b16 a[82:83], v8 offset:16896\n"
"ds_read_b64_tr_b16 a[84:85], v8 offset:20480\n"
"ds_read_b64_tr_b16 a[86:87], v8 offset:20992\n"
"ds_read_b64_tr_b16 a[112:113], v8 offset:24576\n"
"ds_read_b64_tr_b16 a[114:115], v8 offset:25088\n"
"ds_read_b64_tr_b16 a[116:117], v8 offset:28672\n"
"ds_read_b64_tr_b16 a[118:119], v8 offset:29184\n"
"s_waitcnt lgkmcnt(0)\n"
"v_mfma_f32_32x32x16_bf16 v[112:127], a[80:83], v[16:19], v[112:127]\n"
"v_mfma_f32_32x32x16_bf16 v[112:127], a[84:87], v[16:19], v[112:127]\n"
"v_mfma_f32_32x32x16_bf16 v[112:127], a[112:115], v[16:19], v[112:127]\n"
"v_mfma_f32_32x32x16_bf16 v[112:127], a[116:119], v[16:19], v[112:127]\n"
"s_waitcnt vmcnt(20)\n"
"s_barrier\n"
"ds_read_b64_tr_b16 a[144:145], v8 offset:32768\n"
"ds_read_b64_tr_b16 a[146:147], v8 offset:33280\n"
"ds_read_b64_tr_b16 a[148:149], v8 offset:36864\n"
"ds_read_b64_tr_b16 a[150:151], v8 offset:37376\n"
"ds_read_b64_tr_b16 a[176:177], v8 offset:40960\n"
"ds_read_b64_tr_b16 a[178:179], v8 offset:41472\n"
"ds_read_b64_tr_b16 a[180:181], v8 offset:45056\n"
"ds_read_b64_tr_b16 a[182:183], v8 offset:45568\n"
"s_waitcnt lgkmcnt(0)\n"
"v_mfma_f32_32x32x16_bf16 v[112:127], a[144:147], v[16:19], v[112:127]\n"
"v_mfma_f32_32x32x16_bf16 v[112:127], a[148:151], v[16:19], v[112:127]\n"
"v_mfma_f32_32x32x16_bf16 v[112:127], a[176:179], v[16:19], v[112:127]\n"
"v_mfma_f32_32x32x16_bf16 v[112:127], a[180:183], v[16:19], v[112:127]\n"
"s_waitcnt vmcnt(16)\n"
"s_barrier\n"
"ds_read_b64_tr_b16 a[208:209], v8 offset:49152\n"
"ds_read_b64_tr_b16 a[210:211], v8 offset:49664\n"
"ds_read_b64_tr_b16 a[212:213], v8 offset:53248\n"
"ds_read_b64_tr_b16 a[214:215], v8 offset:53760\n"
"ds_read_b64_tr_b16 a[240:241], v8 offset:57344\n"
"ds_read_b64_tr_b16 a[242:243], v8 offset:57856\n"
"ds_read_b64_tr_b16 a[244:245], v8 offset:61440\n"
"ds_read_b64_tr_b16 a[246:247], v8 offset:61952\n"
"s_waitcnt lgkmcnt(0)\n"
"v_mfma_f32_32x32x16_bf16 v[112:127], a[208:211], v[16:19], v[112:127]\n"
"v_mfma_f32_32x32x16_bf16 v[112:127], a[212:215], v[16:19], v[112:127]\n"
"v_mfma_f32_32x32x16_bf16 v[112:127], a[240:243], v[16:19], v[112:127]\n"
"v_mfma_f32_32x32x16_bf16 v[112:127], a[244:247], v[16:19], v[112:127]\n"
"s_waitcnt vmcnt(12)\n"
"s_barrier\n"
"ds_read_b64_tr_b16 a[24:25], v9 offset:0\n"
"ds_read_b64_tr_b16 a[26:27], v9 offset:512\n"
"ds_read_b64_tr_b16 a[28:29], v9 offset:4096\n"
"ds_read_b64_tr_b16 a[30:31], v9 offset:4608\n"
"ds_read_b64_tr_b16 a[56:57], v9 offset:8192\n"
"ds_read_b64_tr_b16 a[58:59], v9 offset:8704\n"
"ds_read_b64_tr_b16 a[60:61], v9 offset:12288\n"
"ds_read_b64_tr_b16 a[62:63], v9 offset:12800\n"
"s_waitcnt lgkmcnt(0)\n"
"v_mfma_f32_32x32x16_bf16 v[112:127], a[24:27], v[16:19], v[112:127]\n"
"v_mfma_f32_32x32x16_bf16 v[112:127], a[28:31], v[16:19], v[112:127]\n"
"v_mfma_f32_32x32x16_bf16 v[112:127], a[56:59], v[16:19], v[112:127]\n"
"v_mfma_f32_32x32x16_bf16 v[112:127], a[60:63], v[16:19], v[112:127]\n"
"s_waitcnt vmcnt(8)\n"
"s_barrier\n"
"ds_read_b64_tr_b16 a[88:89], v9 offset:16384\n"
"ds_read_b64_tr_b16 a[90:91], v9 offset:16896\n"
"ds_read_b64_tr_b16 a[92:93], v9 offset:20480\n"
"ds_read_b64_tr_b16 a[94:95], v9 offset:20992\n"
"ds_read_b64_tr_b16 a[120:121], v9 offset:24576\n"
"ds_read_b64_tr_b16 a[122:123], v9 offset:25088\n"
"ds_read_b64_tr_b16 a[124:125], v9 offset:28672\n"
"ds_read_b64_tr_b16 a[126:127], v9 offset:29184\n"
"s_waitcnt lgkmcnt(0)\n"
"v_mfma_f32_32x32x16_bf16 v[112:127], a[88:91], v[16:19], v[112:127]\n"
"v_mfma_f32_32x32x16_bf16 v[112:127], a[92:95], v[16:19], v[112:127]\n"
"v_mfma_f32_32x32x16_bf16 v[112:127], a[120:123], v[16:19], v[112:127]\n"
"v_mfma_f32_32x32x16_bf16 v[112:127], a[124:127], v[16:19], v[112:127]\n"
"s_waitcnt vmcnt(4)\n"
"s_barrier\n"
"ds_read_b64_tr_b16 a[152:153], v9 offset:32768\n"
"ds_read_b64_tr_b16 a[154:155], v9 offset:33280\n"
"ds_read_b64_tr_b16 a[156:157], v9 offset:36864\n"
"ds_read_b64_tr_b16 a[158:159], v9 offset:37376\n"
"ds_read_b64_tr_b16 a[184:185], v9 offset:40960\n"
"ds_read_b64_tr_b16 a[186:187], v9 offset:41472\n"
"ds_read_b64_tr_b16 a[188:189], v9 offset:45056\n"
"ds_read_b64_tr_b16 a[190:191], v9 offset:45568\n"
"s_waitcnt lgkmcnt(0)\n"
"v_mfma_f32_32x32x16_bf16 v[112:127], a[152:155], v[16:19], v[112:127]\n"
"v_mfma_f32_32x32x16_bf16 v[112:127], a[156:159], v[16:19], v[112:127]\n"
"v_mfma_f32_32x32x16_bf16 v[112:127], a[184:187], v[16:19], v[112:127]\n"
"v_mfma_f32_32x32x16_bf16 v[112:127], a[188:191], v[16:19], v[112:127]\n"
"s_waitcnt vmcnt(0)\n"
"s_barrier\n"
"ds_read_b64_tr_b16 a[216:217], v9 offset:49152\n"
"ds_read_b64_tr_b16 a[218:219], v9 offset:49664\n"
"ds_read_b64_tr_b16 a[220:221], v9 offset:53248\n"
"ds_read_b64_tr_b16 a[222:223], v9 offset:53760\n"
"ds_read_b64_tr_b16 a[248:249], v9 offset:57344\n"
"ds_read_b64_tr_b16 a[250:251], v9 offset:57856\n"
"ds_read_b64_tr_b16 a[252:253], v9 offset:61440\n"
"ds_read_b64_tr_b16 a[254:255], v9 offset:61952\n"
"s_waitcnt lgkmcnt(0)\n"
"v_mfma_f32_32x32x16_bf16 v[112:127], a[216:219], v[16:19], v[112:127]\n"
"v_mfma_f32_32x32x16_bf16 v[112:127], a[220:223], v[16:19], v[112:127]\n"
"v_mfma_f32_32x32x16_bf16 v[112:127], a[248:251], v[16:19], v[112:127]\n"
"v_mfma_f32_32x32x16_bf16 v[112:127], a[252:255], v[16:19], v[112:127]\n"
"s_barrier\n"
"s_lshl2_add_u32 s49, s27, 272\n"
"s_lshl2_add_u32 s68, s27, s26\n"
"s_lshl_b32 s68, s68, 9\n"
"s_add_u32 s68, s68, 4829184\n"
"s_mov_b32 s63, 4681728\n"
"s_waitcnt lgkmcnt(0)\n"
"s_add_u32 s54, s52, 0x7f4a7c15\n"
"s_addc_u32 s55, s53, 0x9e3779b9\n"
"s_lshl_b32 s67, s49, 5\n"
"s_add_u32 s67, s67, 4898816\n"
"s_sub_u32 s51, s67, s68\n"
"s_add_u32 s92, s32, s51\n"
"s_addc_u32 s93, s33, 0x10000\n"
"s_cmp_eq_u32 s26, 0\n"
"s_cselect_b32 s94, 1, 0\n"
"s_mov_b32 s95, 0x20000\n"
"v_mov_b32 v16, s54\n"
"v_mov_b32 v17, s55\n"
"v_cmp_gt_u32 vcc, 12, v1\n"
"v_mul_u32_u24 v3, 296960, v1\n"
"v_mov_b32 v12, 0x7fff0000\n"
"v_cndmask_b32 v3, v12, v3, vcc\n"
"s_add_u32 s51, s67, 76021760\n"
"buffer_store_dwordx2 v[16:17], v3, s[32:35], s51 offen sc1\n"
"s_sub_u32 s67, s68, 296960\n"
"s_sub_u32 s51, s51, 76021760\n"
"s_sub_u32 s51, s51, s68\n"
"v_mul_u32_u24 v3, 296960, v1\n"
"v_add_u32 v3, s51, v3\n"
"v_cmp_gt_u32 vcc, 2, v0\n"
"v_mov_b32 v18, 0x7fff0000\n"
"s_nop 0\n"
"v_cndmask_b32 v18, v18, v3, vcc\n"
"s_mul_i32 s48, s28, 593920\n"
"s_add_u32 s68, s68, s48\n"
"s_sub_u32 s68, s68, 3563520\n"
"s_add_u32 s63, s63, s48\n"
"s_add_u32 s66, s48, 4898816\n"
"s_mul_i32 s64, s26, 9216\n"
"s_add_u32 s63, s63, s64\n"
"s_mov_b32 s62, 31\n"
"v_and_b32 v4, 0xf0, v2\n"
"v_lshrrev_b32 v3, 5, v1\n"
"v_lshl_add_u32 v4, v3, 8, v4\n"
"v_bfe_u32 v3, v1, 4, 1\n"
"v_mul_u32_u24 v15, 296960, v3\n"
"v_add_u32 v15, v15, v4\n"
"v_add_u32 v15, 1187840, v15\n"
"v_mul_u32_u24 v3, 36864, v3\n"
"v_add_u32 v4, v3, v4\n"
"v_add_u32 v5, 73728, v4\n"
"v_add_u32 v20, 0, v2\n"
"v_add_u32 v21, 296960, v2\n"
"v_add_u32 v22, 4096, v2\n"
"v_add_u32 v23, 301056, v2\n"
"v_add_u32 v24, 5120, v2\n"
"v_add_u32 v25, 302080, v2\n"
"v_and_b32 v14, 7, v1\n"
"s_mul_i32 s48, s26, 8\n"
"s_add_u32 s48, s48, 96\n"
"v_add_u32 v14, s48, v14\n"
"v_lshrrev_b32 v14, 2, v14\n"
"v_lshlrev_b32 v14, 7, v14\n"
"v_lshrrev_b32 v3, 4, v1\n"
"v_mul_u32_u24 v3, 296960, v3\n"
"v_add_u32 v14, v3, v14\n"
"v_cmp_gt_u32 vcc, 32, v1\n"
"v_mov_b32 v3, 0x7fff0000\n"
"s_nop 0\n"
"v_cndmask_b32 v14, v3, v14, vcc\n"
"v_cndmask_b32 v16, 0, v16, vcc\n"
"v_cndmask_b32 v17, 0, v17, vcc\n"
"v_bfe_u32 v3, v1, 1, 3\n"
"v_mul_u32_u24 v8, 1152, v3\n"
"v_lshrrev_b32 v3, 1, v3\n"
"v_add_u32 v3, s26, v3\n"
"v_lshrrev_b32 v9, 4, v1\n"
"v_lshl_add_u32 v3, v3, 2, v9\n"
"v_and_b32 v3, 15, v3\n"
"v_lshl_add_u32 v8, v3, 2, v8\n"
"v_and_b32 v3, 1, v1\n"
"v_lshl_add_u32 v8, v3, 6, v8\n"
"v_add_u32 v8, 1024, v8\n"
"s_lshl_b32 s48, s26, 2\n"
"v_add_u32 v10, s48, v9\n"
"v_and_b32 v11, 15, v1\n"
"v_add_u32 v3, 64, v1\n"
"v_cmp_eq_u32 vcc, 0, v11\n"
"v_add_u32 v9, 73728, v8\n"
"v_xor_b32 v9, v9, v8\n"
"v_cndmask_b32 v10, v3, v10, vcc\n"
"v_lshlrev_b32 v10, 2, v10\n"
"v_add_u32 v10, 147456, v10\n"
"v_lshlrev_b32 v11, 2, v11\n"
"v_add_u32 v11, 147456, v11\n"
"v_and_b32 v3, 15, v1\n"
"v_cmp_eq_u32 vcc, 8, v3\n"
"v_lshrrev_b32 v3, 4, v1\n"
"s_lshl2_add_u32 s48, s26, 36880\n"
"v_add_lshl_u32 v3, v3, s48, 2\n"
"v_cndmask_b32 v10, v10, v3, vcc\n"
"v_and_b32 v3, 16, v1\n"
"v_lshl_add_u32 v11, v3, 2, v11\n"
"s_mov_b32 s48, 20000\n"
"s_branch .L_rfha\n"
".L_rlha:\n"
"s_sleep 8\n"
".L_rfha:\n"
"buffer_load_dwordx2 v[12:13], v14, s[32:35], s66 offen sc1\n"
"s_sub_u32 s48, s48, 1\n"
"s_cbranch_scc1 .L_end\n"
"s_waitcnt vmcnt(0)\n"
"v_cmp_ne_u64 vcc, v[16:17], v[12:13]\n"
"s_cbranch_vccnz .L_rlha\n"
"s_add_u32 s66, s66, 1187840\n"
"s_add_u32 m0, s64, 0\n"
"s_nop 0\n"
"buffer_load_dwordx4 v20, s[32:35], s63 offen offset:0 sc1 lds\n"
"buffer_load_dwordx4 v20, s[32:35], s63 offen offset:1024 sc1 lds\n"
"buffer_load_dwordx4 v20, s[32:35], s63 offen offset:2048 sc1 lds\n"
"buffer_load_dwordx4 v20, s[32:35], s63 offen offset:3072 sc1 lds\n"
"s_add_u32 m0, s64, 36864\n"
"s_nop 0\n"
"buffer_load_dwordx4 v21, s[32:35], s63 offen offset:0 sc1 lds\n"
"buffer_load_dwordx4 v21, s[32:35], s63 offen offset:1024 sc1 lds\n"
"buffer_load_dwordx4 v21, s[32:35], s63 offen offset:2048 sc1 lds\n"
"buffer_load_dwordx4 v21, s[32:35], s63 offen offset:3072 sc1 lds\n"
"s_add_u32 m0, s64, 4096\n"
"s_nop 0\n"
"buffer_load_dwordx4 v22, s[32:35], s63 offen offset:0 sc1 lds\n"
"s_add_u32 m0, s64, 40960\n"
"s_nop 0\n"
"buffer_load_dwordx4 v23, s[32:35], s63 offen offset:0 sc1 lds\n"
"buffer_load_dwordx2 v[12:13], v14, s[32:35], s66 offen sc1\n"
"s_add_u32 m0, s64, 5120\n"
"s_nop 0\n"
"buffer_load_dwordx4 v24, s[32:35], s63 offen offset:0 sc1 lds\n"
"buffer_load_dwordx4 v24, s[32:35], s63 offen offset:1024 sc1 lds\n"
"buffer_load_dwordx4 v24, s[32:35], s63 offen offset:2048 sc1 lds\n"
"buffer_load_dwordx4 v24, s[32:35], s63 offen offset:3072 sc1 lds\n"
"s_add_u32 m0, s64, 41984\n"
"s_nop 0\n"
"buffer_load_dwordx4 v25, s[32:35], s63 offen offset:0 sc1 lds\n"
"buffer_load_dwordx4 v25, s[32:35], s63 offen offset:1024 sc1 lds\n"
"buffer_load_dwordx4 v25, s[32:35], s63 offen offset:2048 sc1 lds\n"
"buffer_load_dwordx4 v25, s[32:35], s63 offen offset:3072 sc1 lds\n"
"s_add_u32 s63, s63, 1187840\n"
"s_waitcnt vmcnt(9)\n"
"s_barrier\n"
"ds_read_b128 v[128:131], v4 offset:0\n"
"ds_read_b128 v[132:135], v4 offset:512\n"
"ds_read_b128 v[136:139], v4 offset:9216\n"
"ds_read_b128 v[140:143], v4 offset:9728\n"
"ds_read_b128 v[144:147], v4 offset:18432\n"
"ds_read_b128 v[148:151], v4 offset:18944\n"
"ds_read_b128 v[152:155], v4 offset:27648\n"
"ds_read_b128 v[156:159], v4 offset:28160\n"
".p2align 6\n"
".L_hl:\n"
"s_waitcnt lgkmcnt(4)\n"
"v_mfma_f32_32x32x16_bf16 v[32:47], a[0:3], v[128:131], 0\n"
"ds_read_b128 v[160:163], v4 offset:1152\n"
"s_waitcnt vmcnt(6)\n"
"v_cmp_ne_u64 vcc, v[16:17], v[12:13]\n"
"ds_read_b32 v74, v11 offset:128\n"
"s_add_u32 s68, s68, 1187840\n"
"v_mfma_f32_32x32x16_bf16 v[32:47], a[4:7], v[132:135], v[32:47]\n"
"ds_read_b128 v[164:167], v4 offset:1664\n"
"s_cbranch_vccnz .L_rth0\n"
".L_bkh0:\n"
"s_add_u32 s66, s66, 1187840\n"
"v_mfma_f32_32x32x16_bf16 v[32:47], a[8:11], v[136:139], v[32:47]\n"
"ds_read_b128 v[168:171], v4 offset:10368\n"
"s_add_u32 m0, s64, 73728\n"
"v_mfma_f32_32x32x16_bf16 v[32:47], a[12:15], v[140:143], v[32:47]\n"
"ds_read_b128 v[172:175], v4 offset:10880\n"
"buffer_load_dwordx4 v20, s[32:35], s63 offen offset:0 sc1 lds\n"
"s_waitcnt lgkmcnt(3)\n"
"v_mfma_f32_32x32x16_bf16 v[32:47], a[16:19], v[144:147], v[32:47]\n"
"ds_read_b128 v[176:179], v4 offset:19584\n"
"buffer_load_dwordx4 v20, s[32:35], s63 offen offset:1024 sc1 lds\n"
"v_lshlrev_b32 v72, 16, v74\n"
"v_and_b32 v74, 0xffff0000, v74\n"
"v_mfma_f32_32x32x16_bf16 v[32:47], a[20:23], v[148:151], v[32:47]\n"
"ds_read_b128 v[180:183], v4 offset:20096\n"
"v_rcp_f32 v73, v74\n"
"v_mul_f32 v74, v74, v74\n"
"v_fma_f32 v48, -v72, v112, v48\n"
"v_fma_f32 v49, -v72, v113, v49\n"
"v_mfma_f32_32x32x16_bf16 v[32:47], a[24:27], v[152:155], v[32:47]\n"
"ds_read_b128 v[184:187], v4 offset:28800\n"
"v_mul_f32 v73, 0xbfb8aa3b, v73\n"
"v_fma_f32 v50, -v72, v114, v50\n"
"v_fma_f32 v51, -v72, v115, v51\n"
"v_fma_f32 v52, -v72, v116, v52\n"
"v_fma_f32 v53, -v72, v117, v53\n"
"v_mfma_f32_32x32x16_bf16 v[32:47], a[28:31], v[156:159], v[32:47]\n"
"ds_read_b128 v[188:191], v4 offset:29312\n"
"buffer_load_dwordx4 v20, s[32:35], s63 offen offset:2048 sc1 lds\n"
"v_fma_f32 v54, -v72, v118, v54\n"
"s_waitcnt lgkmcnt(4)\n"
"v_mfma_f32_32x32x16_bf16 v[32:47], a[32:35], v[160:163], v[32:47]\n"
"ds_read_b128 v[128:131], v4 offset:2304\n"
"v_fma_f32 v55, -v72, v119, v55\n"
"v_fma_f32 v56, -v72, v120, v56\n"
"v_fma_f32 v57, -v72, v121, v57\n"
"v_fma_f32 v58, -v72, v122, v58\n"
"v_fma_f32 v59, -v72, v123, v59\n"
"v_mfma_f32_32x32x16_bf16 v[32:47], a[36:39], v[164:167], v[32:47]\n"
"ds_read_b128 v[132:135], v4 offset:2816\n"
"v_fma_f32 v60, -v72, v124, v60\n"
"v_fma_f32 v61, -v72, v125, v61\n"
"v_fma_f32 v62, -v72, v126, v62\n"
"v_fma_f32 v63, -v72, v127, v63\n"
"v_mul_f32 v76, v48, v73\n"
"v_mfma_f32_32x32x16_bf16 v[32:47], a[40:43], v[168:171], v[32:47]\n"
"ds_read_b128 v[136:139], v4 offset:11520\n"
"buffer_load_dwordx4 v20, s[32:35], s63 offen offset:3072 sc1 lds\n"
"v_exp_f32 v76, v76\n"
"v_mfma_f32_32x32x16_bf16 v[32:47], a[44:47], v[172:175], v[32:47]\n"
"ds_read_b128 v[140:143], v4 offset:12032\n"
"v_mul_f32 v77, v49, v73\n"
"v_mul_f32 v78, v50, v73\n"
"v_exp_f32 v77, v77\n"
"s_waitcnt lgkmcnt(4)\n"
"v_mfma_f32_32x32x16_bf16 v[32:47], a[48:51], v[176:179], v[32:47]\n"
"ds_read_b128 v[144:147], v4 offset:20736\n"
"v_exp_f32 v78, v78\n"
"v_mul_f32 v79, v51, v73\n"
"v_mul_f32 v80, v52, v73\n"
"v_mul_f32 v81, v53, v73\n"
"v_mfma_f32_32x32x16_bf16 v[32:47], a[52:55], v[180:183], v[32:47]\n"
"ds_read_b128 v[148:151], v4 offset:21248\n"
"s_add_u32 m0, s64, 110592\n"
"v_exp_f32 v79, v79\n"
"v_exp_f32 v80, v80\n"
"v_mfma_f32_32x32x16_bf16 v[32:47], a[56:59], v[184:187], v[32:47]\n"
"ds_read_b128 v[152:155], v4 offset:29952\n"
"buffer_load_dwordx4 v21, s[32:35], s63 offen offset:0 sc1 lds\n"
"v_exp_f32 v81, v81\n"
"v_mfma_f32_32x32x16_bf16 v[32:47], a[60:63], v[188:191], v[32:47]\n"
"ds_read_b128 v[156:159], v4 offset:30464\n"
"buffer_load_dwordx4 v21, s[32:35], s63 offen offset:1024 sc1 lds\n"
"v_mul_f32 v82, v54, v73\n"
"s_waitcnt lgkmcnt(4)\n"
"v_mfma_f32_32x32x16_bf16 v[32:47], a[64:67], v[128:131], v[32:47]\n"
"ds_read_b128 v[160:163], v4 offset:3456\n"
"v_exp_f32 v82, v82\n"
"v_mul_f32 v83, v55, v73\n"
"v_mul_f32 v48, v48, v56\n"
"v_mul_f32 v49, v49, v57\n"
"v_mfma_f32_32x32x16_bf16 v[32:47], a[68:71], v[132:135], v[32:47]\n"
"ds_read_b128 v[164:167], v4 offset:3968\n"
"v_exp_f32 v83, v83\n"
"v_mul_f32 v50, v50, v58\n"
"v_mul_f32 v51, v51, v59\n"
"v_mul_f32 v52, v52, v60\n"
"v_mfma_f32_32x32x16_bf16 v[32:47], a[72:75], v[136:139], v[32:47]\n"
"ds_read_b128 v[168:171], v4 offset:12672\n"
"buffer_load_dwordx4 v21, s[32:35], s63 offen offset:2048 sc1 lds\n"
"v_mul_f32 v53, v53, v61\n"
"v_mul_f32 v54, v54, v62\n"
"v_mfma_f32_32x32x16_bf16 v[32:47], a[76:79], v[140:143], v[32:47]\n"
"ds_read_b128 v[172:175], v4 offset:13184\n"
"v_mul_f32 v55, v55, v63\n"
"v_fma_f32 v76, v76, v74, v74\n"
"v_fma_f32 v77, v77, v74, v74\n"
"v_fma_f32 v78, v78, v74, v74\n"
"s_waitcnt lgkmcnt(4)\n"
"v_mfma_f32_32x32x16_bf16 v[32:47], a[80:83], v[144:147], v[32:47]\n"
"ds_read_b128 v[176:179], v4 offset:21888\n"
"v_rcp_f32 v76, v76\n"
"v_rcp_f32 v77, v77\n"
"v_fma_f32 v79, v79, v74, v74\n"
"v_mfma_f32_32x32x16_bf16 v[32:47], a[84:87], v[148:151], v[32:47]\n"
"ds_read_b128 v[180:183], v4 offset:22400\n"
"buffer_load_dwordx4 v21, s[32:35], s63 offen offset:3072 sc1 lds\n"
"v_rcp_f32 v78, v78\n"
"v_mfma_f32_32x32x16_bf16 v[32:47], a[88:91], v[152:155], v[32:47]\n"
"ds_read_b128 v[184:187], v4 offset:31104\n"
"v_rcp_f32 v79, v79\n"
"v_fma_f32 v80, v80, v74, v74\n"
"v_fma_f32 v81, v81, v74, v74\n"
"v_fma_f32 v82, v82, v74, v74\n"
"v_mfma_f32_32x32x16_bf16 v[32:47], a[92:95], v[156:159], v[32:47]\n"
"ds_read_b128 v[188:191], v4 offset:31616\n"
"v_rcp_f32 v80, v80\n"
"v_rcp_f32 v81, v81\n"
"s_waitcnt lgkmcnt(4)\n"
"v_mfma_f32_32x32x16_bf16 v[32:47], a[96:99], v[160:163], v[32:47]\n"
"s_waitcnt vmcnt(8)\n"
"s_barrier\n"
"ds_read_b128 v[128:131], v4 offset:4608\n"
"s_add_u32 m0, s64, 77824\n"
"v_rcp_f32 v82, v82\n"
"v_mfma_f32_32x32x16_bf16 v[32:47], a[100:103], v[164:167], v[32:47]\n"
"ds_read_b128 v[132:135], v4 offset:5120\n"
"buffer_load_dwordx4 v22, s[32:35], s63 offen offset:0 sc1 lds\n"
"v_fma_f32 v83, v83, v74, v74\n"
"v_mul_f32 v48, v48, v76\n"
"v_mfma_f32_32x32x16_bf16 v[32:47], a[104:107], v[168:171], v[32:47]\n"
"ds_read_b128 v[136:139], v4 offset:13824\n"
"buffer_store_dwordx2 v[16:17], v18, s[32:35], s68 offen sc1\n"
"v_rcp_f32 v83, v83\n"
"v_mfma_f32_32x32x16_bf16 v[32:47], a[108:111], v[172:175], v[32:47]\n"
"ds_read_b128 v[140:143], v4 offset:14336\n"
"s_add_u32 m0, s64, 114688\n"
"v_mul_f32 v49, v49, v77\n"
"v_mul_f32 v50, v50, v78\n"
"v_mul_f32 v51, v51, v79\n"
"s_waitcnt lgkmcnt(4)\n"
"v_mfma_f32_32x32x16_bf16 v[32:47], a[112:115], v[176:179], v[32:47]\n"
"ds_read_b128 v[144:147], v4 offset:23040\n"
"buffer_load_dwordx4 v23, s[32:35], s63 offen offset:0 sc1 lds\n"
"v_mul_f32 v52, v52, v80\n"
"v_mul_f32 v53, v53, v81\n"
"v_mfma_f32_32x32x16_bf16 v[32:47], a[116:119], v[180:183], v[32:47]\n"
"ds_read_b128 v[148:151], v4 offset:23552\n"
"v_mul_f32 v54, v54, v82\n"
"v_mul_f32 v55, v55, v83\n"
"v_cvt_pk_bf16_f32 v84, v48, v49\n"
"v_cvt_pk_bf16_f32 v85, v50, v51\n"
"v_cvt_pk_bf16_f32 v86, v52, v53\n"
"v_mfma_f32_32x32x16_bf16 v[32:47], a[120:123], v[184:187], v[32:47]\n"
"ds_read_b128 v[152:155], v4 offset:32256\n"
"s_add_u32 m0, s64, 78848\n"
"v_cvt_pk_bf16_f32 v87, v54, v55\n"
"buffer_load_dwordx4 v24, s[32:35], s63 offen offset:0 sc1 lds\n"
"v_mfma_f32_32x32x16_bf16 v[32:47], a[124:127], v[188:191], v[32:47]\n"
"ds_read_b128 v[156:159], v4 offset:32768\n"
"ds_read2st64_b32 v[64:65], v8 offset0:0 offset1:36\n"
"ds_read2st64_b32 v[66:67], v8 offset0:72 offset1:108\n"
"ds_read2st64_b32 v[68:69], v8 offset0:144 offset1:180\n"
"ds_read2st64_b32 v[70:71], v8 offset0:216 offset1:252\n"
"s_waitcnt lgkmcnt(8)\n"
"v_mfma_f32_32x32x16_bf16 v[32:47], a[128:131], v[128:131], v[32:47]\n"
"ds_read_b128 v[160:163], v4 offset:5760\n"
"buffer_store_dwordx4 v[84:87], v15, s[32:35], s68 offen sc1\n"
"v_mfma_f32_32x32x16_bf16 v[32:47], a[132:135], v[132:135], v[32:47]\n"
"ds_read_b128 v[164:167], v4 offset:6272\n"
"buffer_load_dwordx4 v24, s[32:35], s63 offen offset:1024 sc1 lds\n"
"v_xor_b32 v8, v9, v8\n"
"v_mfma_f32_32x32x16_bf16 v[32:47], a[136:139], v[136:139], v[32:47]\n"
"ds_read_b128 v[168:171], v4 offset:14976\n"
"v_mfma_f32_32x32x16_bf16 v[32:47], a[140:143], v[140:143], v[32:47]\n"
"ds_read_b128 v[172:175], v4 offset:15488\n"
"s_waitcnt lgkmcnt(8)\n"
"v_mfma_f32_32x32x16_bf16 v[32:47], a[144:147], v[144:147], v[32:47]\n"
"ds_read_b128 v[176:179], v4 offset:24192\n"
"buffer_load_dwordx2 v[12:13], v14, s[32:35], s66 offen sc1\n"
"v_mfma_f32_32x32x16_bf16 v[32:47], a[148:151], v[148:151], v[32:47]\n"
"ds_read_b128 v[180:183], v4 offset:24704\n"
"buffer_load_dwordx4 v24, s[32:35], s63 offen offset:2048 sc1 lds\n"
"s_waitcnt lgkmcnt(6)\n"
"v_add_f32 v64, v64, v65\n"
"v_mfma_f32_32x32x16_bf16 v[32:47], a[152:155], v[152:155], v[32:47]\n"
"ds_read_b128 v[184:187], v4 offset:33408\n"
"v_add_f32 v66, v66, v67\n"
"v_add_f32 v68, v68, v69\n"
"v_add_f32 v70, v70, v71\n"
"v_add_f32 v66, v64, v66\n"
"v_add_f32 v68, v68, v70\n"
"v_mfma_f32_32x32x16_bf16 v[32:47], a[156:159], v[156:159], v[32:47]\n"
"ds_read_b128 v[188:191], v4 offset:33920\n"
"v_add_f32_dpp v64, v66, v66 row_ror:8 row_mask:0xf bank_mask:0x3\n"
"s_waitcnt lgkmcnt(4)\n"
"v_mfma_f32_32x32x16_bf16 v[32:47], a[160:163], v[160:163], v[32:47]\n"
"ds_read_b128 v[128:131], v4 offset:6912\n"
"v_add_f32_dpp v64, v68, v68 row_ror:8 row_mask:0xf bank_mask:0xc\n"
"v_mfma_f32_32x32x16_bf16 v[32:47], a[164:167], v[164:167], v[32:47]\n"
"ds_read_b128 v[132:135], v4 offset:7424\n"
"buffer_load_dwordx4 v24, s[32:35], s63 offen offset:3072 sc1 lds\n"
"v_add_f32_dpp v64, v64, v64 row_ror:12 row_mask:0xf bank_mask:0xf\n"
"v_mfma_f32_32x32x16_bf16 v[32:47], a[168:171], v[168:171], v[32:47]\n"
"ds_read_b128 v[136:139], v4 offset:16128\n"
"v_add_f32_dpp v64, v64, v64 quad_perm:[2,3,0,1] row_mask:0xf bank_mask:0xf\n"
"v_mfma_f32_32x32x16_bf16 v[32:47], a[172:175], v[172:175], v[32:47]\n"
"ds_read_b128 v[140:143], v4 offset:16640\n"
"v_mul_f32 v64, 0x3a800000, v64\n"
"s_waitcnt lgkmcnt(4)\n"
"v_mfma_f32_32x32x16_bf16 v[32:47], a[176:179], v[176:179], v[32:47]\n"
"ds_read_b128 v[144:147], v4 offset:25344\n"
"v_mul_f32 v65, v64, v64\n"
"v_mfma_f32_32x32x16_bf16 v[32:47], a[180:183], v[180:183], v[32:47]\n"
"ds_read_b128 v[148:151], v4 offset:25856\n"
"s_add_u32 m0, s64, 115712\n"
"v_sub_f32_dpp v65, v64, v65 quad_perm:[1,1,3,3] row_mask:0xf bank_mask:0xf\n"
"buffer_load_dwordx4 v25, s[32:35], s63 offen offset:0 sc1 lds\n"
"v_mfma_f32_32x32x16_bf16 v[32:47], a[184:187], v[184:187], v[32:47]\n"
"ds_read_b128 v[152:155], v4 offset:34560\n"
"v_max_f32 v65, 0, v65\n"
"v_mfma_f32_32x32x16_bf16 v[32:47], a[188:191], v[188:191], v[32:47]\n"
"ds_read_b128 v[156:159], v4 offset:35072\n"
"v_cvt_pk_bf16_f32 v65, 0, v65\n"
"s_waitcnt lgkmcnt(4)\n"
"v_mfma_f32_32x32x16_bf16 v[32:47], a[192:195], v[128:131], v[32:47]\n"
"ds_read_b128 v[160:163], v4 offset:8064\n"
"v_add_f32 v65, 0x3727c5ac, v65\n"
"v_mfma_f32_32x32x16_bf16 v[32:47], a[196:199], v[132:135], v[32:47]\n"
"ds_read_b128 v[164:167], v4 offset:8576\n"
"buffer_load_dwordx4 v25, s[32:35], s63 offen offset:1024 sc1 lds\n"
"v_cvt_pk_bf16_f32 v65, 0, v65\n"
"v_mfma_f32_32x32x16_bf16 v[32:47], a[200:203], v[136:139], v[32:47]\n"
"ds_read_b128 v[168:171], v4 offset:17280\n"
"v_sqrt_f32 v65, v65\n"
"v_mfma_f32_32x32x16_bf16 v[32:47], a[204:207], v[140:143], v[32:47]\n"
"ds_read_b128 v[172:175], v4 offset:17792\n"
"v_cvt_pk_bf16_f32 v65, v64, v65\n"
"s_waitcnt lgkmcnt(4)\n"
"v_mfma_f32_32x32x16_bf16 v[32:47], a[208:211], v[144:147], v[32:47]\n"
"ds_read_b128 v[176:179], v4 offset:26496\n"
"ds_write_b32 v10, v65 offset:0\n"
"v_mfma_f32_32x32x16_bf16 v[32:47], a[212:215], v[148:151], v[32:47]\n"
"ds_read_b128 v[180:183], v4 offset:27008\n"
"buffer_load_dwordx4 v25, s[32:35], s63 offen offset:2048 sc1 lds\n"
"v_mfma_f32_32x32x16_bf16 v[32:47], a[216:219], v[152:155], v[32:47]\n"
"ds_read_b128 v[184:187], v4 offset:35712\n"
"v_mfma_f32_32x32x16_bf16 v[32:47], a[220:223], v[156:159], v[32:47]\n"
"ds_read_b128 v[188:191], v4 offset:36224\n"
"s_waitcnt lgkmcnt(5)\n"
"v_mfma_f32_32x32x16_bf16 v[32:47], a[224:227], v[160:163], v[32:47]\n"
"s_waitcnt vmcnt(9)\n"
"s_waitcnt lgkmcnt(3)\n"
"s_barrier\n"
"ds_read_b128 v[128:131], v5 offset:0\n"
"v_mfma_f32_32x32x16_bf16 v[32:47], a[228:231], v[164:167], v[32:47]\n"
"ds_read_b128 v[132:135], v5 offset:512\n"
"buffer_load_dwordx4 v25, s[32:35], s63 offen offset:3072 sc1 lds\n"
"s_add_u32 s63, s63, 1187840\n"
"v_mfma_f32_32x32x16_bf16 v[32:47], a[232:235], v[168:171], v[32:47]\n"
"ds_read_b128 v[136:139], v5 offset:9216\n"
"v_mfma_f32_32x32x16_bf16 v[32:47], a[236:239], v[172:175], v[32:47]\n"
"ds_read_b128 v[140:143], v5 offset:9728\n"
"s_waitcnt lgkmcnt(4)\n"
"v_mfma_f32_32x32x16_bf16 v[32:47], a[240:243], v[176:179], v[32:47]\n"
"ds_read_b128 v[144:147], v5 offset:18432\n"
"v_mfma_f32_32x32x16_bf16 v[32:47], a[244:247], v[180:183], v[32:47]\n"
"ds_read_b128 v[148:151], v5 offset:18944\n"
"v_mfma_f32_32x32x16_bf16 v[32:47], a[248:251], v[184:187], v[32:47]\n"
"ds_read_b128 v[152:155], v5 offset:27648\n"
"v_mfma_f32_32x32x16_bf16 v[32:47], a[252:255], v[188:191], v[32:47]\n"
"ds_read_b128 v[156:159], v5 offset:28160\n"
"s_waitcnt lgkmcnt(4)\n"
"v_mfma_f32_32x32x16_bf16 v[48:63], a[0:3], v[128:131], 0\n"
"ds_read_b128 v[160:163], v5 offset:1152\n"
"s_waitcnt vmcnt(6)\n"
"v_cmp_ne_u64 vcc, v[16:17], v[12:13]\n"
"ds_read_b32 v74, v11 offset:0\n"
"s_add_u32 s68, s68, 1187840\n"
"v_mfma_f32_32x32x16_bf16 v[48:63], a[4:7], v[132:135], v[48:63]\n"
"ds_read_b128 v[164:167], v5 offset:1664\n"
"s_cbranch_vccnz .L_rth1\n"
".L_bkh1:\n"
"s_add_u32 s66, s66, 1187840\n"
"v_mfma_f32_32x32x16_bf16 v[48:63], a[8:11], v[136:139], v[48:63]\n"
"ds_read_b128 v[168:171], v5 offset:10368\n"
"s_add_u32 m0, s64, 0\n"
"v_mfma_f32_32x32x16_bf16 v[48:63], a[12:15], v[140:143], v[48:63]\n"
"ds_read_b128 v[172:175], v5 offset:10880\n"
"buffer_load_dwordx4 v20, s[32:35], s63 offen offset:0 sc1 lds\n"
"s_waitcnt lgkmcnt(3)\n"
"v_mfma_f32_32x32x16_bf16 v[48:63], a[16:19], v[144:147], v[48:63]\n"
"ds_read_b128 v[176:179], v5 offset:19584\n"
"buffer_load_dwordx4 v20, s[32:35], s63 offen offset:1024 sc1 lds\n"
"v_lshlrev_b32 v72, 16, v74\n"
"v_and_b32 v74, 0xffff0000, v74\n"
"v_mfma_f32_32x32x16_bf16 v[48:63], a[20:23], v[148:151], v[48:63]\n"
"ds_read_b128 v[180:183], v5 offset:20096\n"
"v_rcp_f32 v73, v74\n"
"v_mul_f32 v74, v74, v74\n"
"v_fma_f32 v32, -v72, v112, v32\n"
"v_fma_f32 v33, -v72, v113, v33\n"
"v_mfma_f32_32x32x16_bf16 v[48:63], a[24:27], v[152:155], v[48:63]\n"
"ds_read_b128 v[184:187], v5 offset:28800\n"
"v_mul_f32 v73, 0xbfb8aa3b, v73\n"
"v_fma_f32 v34, -v72, v114, v34\n"
"v_fma_f32 v35, -v72, v115, v35\n"
"v_fma_f32 v36, -v72, v116, v36\n"
"v_fma_f32 v37, -v72, v117, v37\n"
"v_mfma_f32_32x32x16_bf16 v[48:63], a[28:31], v[156:159], v[48:63]\n"
"ds_read_b128 v[188:191], v5 offset:29312\n"
"buffer_load_dwordx4 v20, s[32:35], s63 offen offset:2048 sc1 lds\n"
"v_fma_f32 v38, -v72, v118, v38\n"
"s_waitcnt lgkmcnt(4)\n"
"v_mfma_f32_32x32x16_bf16 v[48:63], a[32:35], v[160:163], v[48:63]\n"
"ds_read_b128 v[128:131], v5 offset:2304\n"
"v_fma_f32 v39, -v72, v119, v39\n"
"v_fma_f32 v40, -v72, v120, v40\n"
"v_fma_f32 v41, -v72, v121, v41\n"
"v_fma_f32 v42, -v72, v122, v42\n"
"v_fma_f32 v43, -v72, v123, v43\n"
"v_mfma_f32_32x32x16_bf16 v[48:63], a[36:39], v[164:167], v[48:63]\n"
"ds_read_b128 v[132:135], v5 offset:2816\n"
"v_fma_f32 v44, -v72, v124, v44\n"
"v_fma_f32 v45, -v72, v125, v45\n"
"v_fma_f32 v46, -v72, v126, v46\n"
"v_fma_f32 v47, -v72, v127, v47\n"
"v_mul_f32 v76, v32, v73\n"
"v_mfma_f32_32x32x16_bf16 v[48:63], a[40:43], v[168:171], v[48:63]\n"
"ds_read_b128 v[136:139], v5 offset:11520\n"
"buffer_load_dwordx4 v20, s[32:35], s63 offen offset:3072 sc1 lds\n"
"v_exp_f32 v76, v76\n"
"v_mfma_f32_32x32x16_bf16 v[48:63], a[44:47], v[172:175], v[48:63]\n"
"ds_read_b128 v[140:143], v5 offset:12032\n"
"v_mul_f32 v77, v33, v73\n"
"v_mul_f32 v78, v34, v73\n"
"v_exp_f32 v77, v77\n"
"s_waitcnt lgkmcnt(4)\n"
"v_mfma_f32_32x32x16_bf16 v[48:63], a[48:51], v[176:179], v[48:63]\n"
"ds_read_b128 v[144:147], v5 offset:20736\n"
"v_exp_f32 v78, v78\n"
"v_mul_f32 v79, v35, v73\n"
"v_mul_f32 v80, v36, v73\n"
"v_mul_f32 v81, v37, v73\n"
"v_mfma_f32_32x32x16_bf16 v[48:63], a[52:55], v[180:183], v[48:63]\n"
"ds_read_b128 v[148:151], v5 offset:21248\n"
"s_add_u32 m0, s64, 36864\n"
"v_exp_f32 v79, v79\n"
"v_exp_f32 v80, v80\n"
"v_mfma_f32_32x32x16_bf16 v[48:63], a[56:59], v[184:187], v[48:63]\n"
"ds_read_b128 v[152:155], v5 offset:29952\n"
"buffer_load_dwordx4 v21, s[32:35], s63 offen offset:0 sc1 lds\n"
"v_exp_f32 v81, v81\n"
"v_mfma_f32_32x32x16_bf16 v[48:63], a[60:63], v[188:191], v[48:63]\n"
"ds_read_b128 v[156:159], v5 offset:30464\n"
"buffer_load_dwordx4 v21, s[32:35], s63 offen offset:1024 sc1 lds\n"
"v_mul_f32 v82, v38, v73\n"
"s_waitcnt lgkmcnt(4)\n"
"v_mfma_f32_32x32x16_bf16 v[48:63], a[64:67], v[128:131], v[48:63]\n"
"ds_read_b128 v[160:163], v5 offset:3456\n"
"v_exp_f32 v82, v82\n"
"v_mul_f32 v83, v39, v73\n"
"v_mul_f32 v32, v32, v40\n"
"v_mul_f32 v33, v33, v41\n"
"v_mfma_f32_32x32x16_bf16 v[48:63], a[68:71], v[132:135], v[48:63]\n"
"ds_read_b128 v[164:167], v5 offset:3968\n"
"v_exp_f32 v83, v83\n"
"v_mul_f32 v34, v34, v42\n"
"v_mul_f32 v35, v35, v43\n"
"v_mul_f32 v36, v36, v44\n"
"v_mfma_f32_32x32x16_bf16 v[48:63], a[72:75], v[136:139], v[48:63]\n"
"ds_read_b128 v[168:171], v5 offset:12672\n"
"buffer_load_dwordx4 v21, s[32:35], s63 offen offset:2048 sc1 lds\n"
"v_mul_f32 v37, v37, v45\n"
"v_mul_f32 v38, v38, v46\n"
"v_mfma_f32_32x32x16_bf16 v[48:63], a[76:79], v[140:143], v[48:63]\n"
"ds_read_b128 v[172:175], v5 offset:13184\n"
"v_mul_f32 v39, v39, v47\n"
"v_fma_f32 v76, v76, v74, v74\n"
"v_fma_f32 v77, v77, v74, v74\n"
"v_fma_f32 v78, v78, v74, v74\n"
"s_waitcnt lgkmcnt(4)\n"
"v_mfma_f32_32x32x16_bf16 v[48:63], a[80:83], v[144:147], v[48:63]\n"
"ds_read_b128 v[176:179], v5 offset:21888\n"
"v_rcp_f32 v76, v76\n"
"v_rcp_f32 v77, v77\n"
"v_fma_f32 v79, v79, v74, v74\n"
"v_mfma_f32_32x32x16_bf16 v[48:63], a[84:87], v[148:151], v[48:63]\n"
"ds_read_b128 v[180:183], v5 offset:22400\n"
"buffer_load_dwordx4 v21, s[32:35], s63 offen offset:3072 sc1 lds\n"
"v_rcp_f32 v78, v78\n"
"v_mfma_f32_32x32x16_bf16 v[48:63], a[88:91], v[152:155], v[48:63]\n"
"ds_read_b128 v[184:187], v5 offset:31104\n"
"v_rcp_f32 v79, v79\n"
"v_fma_f32 v80, v80, v74, v74\n"
"v_fma_f32 v81, v81, v74, v74\n"
"v_fma_f32 v82, v82, v74, v74\n"
"v_mfma_f32_32x32x16_bf16 v[48:63], a[92:95], v[156:159], v[48:63]\n"
"ds_read_b128 v[188:191], v5 offset:31616\n"
"v_rcp_f32 v80, v80\n"
"v_rcp_f32 v81, v81\n"
"s_waitcnt lgkmcnt(4)\n"
"v_mfma_f32_32x32x16_bf16 v[48:63], a[96:99], v[160:163], v[48:63]\n"
"s_waitcnt vmcnt(8)\n"
"s_barrier\n"
"ds_read_b128 v[128:131], v5 offset:4608\n"
"s_add_u32 m0, s64, 4096\n"
"v_rcp_f32 v82, v82\n"
"v_mfma_f32_32x32x16_bf16 v[48:63], a[100:103], v[164:167], v[48:63]\n"
"ds_read_b128 v[132:135], v5 offset:5120\n"
"buffer_load_dwordx4 v22, s[32:35], s63 offen offset:0 sc1 lds\n"
"v_fma_f32 v83, v83, v74, v74\n"
"v_mul_f32 v32, v32, v76\n"
"v_mfma_f32_32x32x16_bf16 v[48:63], a[104:107], v[168:171], v[48:63]\n"
"ds_read_b128 v[136:139], v5 offset:13824\n"
"buffer_store_dwordx2 v[16:17], v18, s[32:35], s68 offen sc1\n"
"v_rcp_f32 v83, v83\n"
"v_mfma_f32_32x32x16_bf16 v[48:63], a[108:111], v[172:175], v[48:63]\n"
"ds_read_b128 v[140:143], v5 offset:14336\n"
"s_add_u32 m0, s64, 40960\n"
"v_mul_f32 v33, v33, v77\n"
"v_mul_f32 v34, v34, v78\n"
"v_mul_f32 v35, v35, v79\n"
"s_waitcnt lgkmcnt(4)\n"
"v_mfma_f32_32x32x16_bf16 v[48:63], a[112:115], v[176:179], v[48:63]\n"
"ds_read_b128 v[144:147], v5 offset:23040\n"
"buffer_load_dwordx4 v23, s[32:35], s63 offen offset:0 sc1 lds\n"
"v_mul_f32 v36, v36, v80\n"
"v_mul_f32 v37, v37, v81\n"
"v_mfma_f32_32x32x16_bf16 v[48:63], a[116:119], v[180:183], v[48:63]\n"
"ds_read_b128 v[148:151], v5 offset:23552\n"
"v_mul_f32 v38, v38, v82\n"
"v_mul_f32 v39, v39, v83\n"
"v_cvt_pk_bf16_f32 v84, v32, v33\n"
"v_cvt_pk_bf16_f32 v85, v34, v35\n"
"v_cvt_pk_bf16_f32 v86, v36, v37\n"
"v_mfma_f32_32x32x16_bf16 v[48:63], a[120:123], v[184:187], v[48:63]\n"
"ds_read_b128 v[152:155], v5 offset:32256\n"
"s_add_u32 m0, s64, 5120\n"
"v_cvt_pk_bf16_f32 v87, v38, v39\n"
"buffer_load_dwordx4 v24, s[32:35], s63 offen offset:0 sc1 lds\n"
"v_mfma_f32_32x32x16_bf16 v[48:63], a[124:127], v[188:191], v[48:63]\n"
"ds_read_b128 v[156:159], v5 offset:32768\n"
"ds_read2st64_b32 v[64:65], v8 offset0:0 offset1:36\n"
"ds_read2st64_b32 v[66:67], v8 offset0:72 offset1:108\n"
"ds_read2st64_b32 v[68:69], v8 offset0:144 offset1:180\n"
"ds_read2st64_b32 v[70:71], v8 offset0:216 offset1:252\n"
"s_waitcnt lgkmcnt(8)\n"
"v_mfma_f32_32x32x16_bf16 v[48:63], a[128:131], v[128:131], v[48:63]\n"
"ds_read_b128 v[160:163], v5 offset:5760\n"
"buffer_store_dwordx4 v[84:87], v15, s[32:35], s68 offen sc1\n"
"v_mfma_f32_32x32x16_bf16 v[48:63], a[132:135], v[132:135], v[48:63]\n"
"ds_read_b128 v[164:167], v5 offset:6272\n"
"buffer_load_dwordx4 v24, s[32:35], s63 offen offset:1024 sc1 lds\n"
"v_xor_b32 v8, v9, v8\n"
"v_mfma_f32_32x32x16_bf16 v[48:63], a[136:139], v[136:139], v[48:63]\n"
"ds_read_b128 v[168:171], v5 offset:14976\n"
"v_mfma_f32_32x32x16_bf16 v[48:63], a[140:143], v[140:143], v[48:63]\n"
"ds_read_b128 v[172:175], v5 offset:15488\n"
"s_waitcnt lgkmcnt(8)\n"
"v_mfma_f32_32x32x16_bf16 v[48:63], a[144:147], v[144:147], v[48:63]\n"
"ds_read_b128 v[176:179], v5 offset:24192\n"
"buffer_load_dwordx2 v[12:13], v14, s[32:35], s66 offen sc1\n"
"v_mfma_f32_32x32x16_bf16 v[48:63], a[148:151], v[148:151], v[48:63]\n"
"ds_read_b128 v[180:183], v5 offset:24704\n"
"buffer_load_dwordx4 v24, s[32:35], s63 offen offset:2048 sc1 lds\n"
"s_waitcnt lgkmcnt(6)\n"
"v_add_f32 v64, v64, v65\n"
"v_mfma_f32_32x32x16_bf16 v[48:63], a[152:155], v[152:155], v[48:63]\n"
"ds_read_b128 v[184:187], v5 offset:33408\n"
"v_add_f32 v66, v66, v67\n"
"v_add_f32 v68, v68, v69\n"
"v_add_f32 v70, v70, v71\n"
"v_add_f32 v66, v64, v66\n"
"v_add_f32 v68, v68, v70\n"
"v_mfma_f32_32x32x16_bf16 v[48:63], a[156:159], v[156:159], v[48:63]\n"
"ds_read_b128 v[188:191], v5 offset:33920\n"
"v_add_f32_dpp v64, v66, v66 row_ror:8 row_mask:0xf bank_mask:0x3\n"
"s_waitcnt lgkmcnt(4)\n"
"v_mfma_f32_32x32x16_bf16 v[48:63], a[160:163], v[160:163], v[48:63]\n"
"ds_read_b128 v[128:131], v5 offset:6912\n"
"v_add_f32_dpp v64, v68, v68 row_ror:8 row_mask:0xf bank_mask:0xc\n"
"v_mfma_f32_32x32x16_bf16 v[48:63], a[164:167], v[164:167], v[48:63]\n"
"ds_read_b128 v[132:135], v5 offset:7424\n"
"buffer_load_dwordx4 v24, s[32:35], s63 offen offset:3072 sc1 lds\n"
"v_add_f32_dpp v64, v64, v64 row_ror:12 row_mask:0xf bank_mask:0xf\n"
"v_mfma_f32_32x32x16_bf16 v[48:63], a[168:171], v[168:171], v[48:63]\n"
"ds_read_b128 v[136:139], v5 offset:16128\n"
"v_add_f32_dpp v64, v64, v64 quad_perm:[2,3,0,1] row_mask:0xf bank_mask:0xf\n"
"v_mfma_f32_32x32x16_bf16 v[48:63], a[172:175], v[172:175], v[48:63]\n"
"ds_read_b128 v[140:143], v5 offset:16640\n"
"v_mul_f32 v64, 0x3a800000, v64\n"
"s_waitcnt lgkmcnt(4)\n"
"v_mfma_f32_32x32x16_bf16 v[48:63], a[176:179], v[176:179], v[48:63]\n"
"ds_read_b128 v[144:147], v5 offset:25344\n"
"v_mul_f32 v65, v64, v64\n"
"v_mfma_f32_32x32x16_bf16 v[48:63], a[180:183], v[180:183], v[48:63]\n"
"ds_read_b128 v[148:151], v5 offset:25856\n"
"s_add_u32 m0, s64, 41984\n"
"v_sub_f32_dpp v65, v64, v65 quad_perm:[1,1,3,3] row_mask:0xf bank_mask:0xf\n"
"buffer_load_dwordx4 v25, s[32:35], s63 offen offset:0 sc1 lds\n"
"v_mfma_f32_32x32x16_bf16 v[48:63], a[184:187], v[184:187], v[48:63]\n"
"ds_read_b128 v[152:155], v5 offset:34560\n"
"v_max_f32 v65, 0, v65\n"
"v_mfma_f32_32x32x16_bf16 v[48:63], a[188:191], v[188:191], v[48:63]\n"
"ds_read_b128 v[156:159], v5 offset:35072\n"
"v_cvt_pk_bf16_f32 v65, 0, v65\n"
"s_waitcnt lgkmcnt(4)\n"
"v_mfma_f32_32x32x16_bf16 v[48:63], a[192:195], v[128:131], v[48:63]\n"
"ds_read_b128 v[160:163], v5 offset:8064\n"
"v_add_f32 v65, 0x3727c5ac, v65\n"
"v_mfma_f32_32x32x16_bf16 v[48:63], a[196:199], v[132:135], v[48:63]\n"
"ds_read_b128 v[164:167], v5 offset:8576\n"
"buffer_load_dwordx4 v25, s[32:35], s63 offen offset:1024 sc1 lds\n"
"v_cvt_pk_bf16_f32 v65, 0, v65\n"
"v_mfma_f32_32x32x16_bf16 v[48:63], a[200:203], v[136:139], v[48:63]\n"
"ds_read_b128 v[168:171], v5 offset:17280\n"
"v_sqrt_f32 v65, v65\n"
"v_mfma_f32_32x32x16_bf16 v[48:63], a[204:207], v[140:143], v[48:63]\n"
"ds_read_b128 v[172:175], v5 offset:17792\n"
"v_cvt_pk_bf16_f32 v65, v64, v65\n"
"s_waitcnt lgkmcnt(4)\n"
"v_mfma_f32_32x32x16_bf16 v[48:63], a[208:211], v[144:147], v[48:63]\n"
"ds_read_b128 v[176:179], v5 offset:26496\n"
"ds_write_b32 v10, v65 offset:128\n"
"v_mfma_f32_32x32x16_bf16 v[48:63], a[212:215], v[148:151], v[48:63]\n"
"ds_read_b128 v[180:183], v5 offset:27008\n"
"buffer_load_dwordx4 v25, s[32:35], s63 offen offset:2048 sc1 lds\n"
"v_mfma_f32_32x32x16_bf16 v[48:63], a[216:219], v[152:155], v[48:63]\n"
"ds_read_b128 v[184:187], v5 offset:35712\n"
"v_mfma_f32_32x32x16_bf16 v[48:63], a[220:223], v[156:159], v[48:63]\n"
"ds_read_b128 v[188:191], v5 offset:36224\n"
"s_waitcnt lgkmcnt(5)\n"
"v_mfma_f32_32x32x16_bf16 v[48:63], a[224:227], v[160:163], v[48:63]\n"
"s_waitcnt vmcnt(9)\n"
"s_waitcnt lgkmcnt(3)\n"
"s_barrier\n"
"ds_read_b128 v[128:131], v4 offset:0\n"
"v_mfma_f32_32x32x16_bf16 v[48:63], a[228:231], v[164:167], v[48:63]\n"
"ds_read_b128 v[132:135], v4 offset:512\n"
"buffer_load_dwordx4 v25, s[32:35], s63 offen offset:3072 sc1 lds\n"
"s_add_u32 s63, s63, 1187840\n"
"v_mfma_f32_32x32x16_bf16 v[48:63], a[232:235], v[168:171], v[48:63]\n"
"ds_read_b128 v[136:139], v4 offset:9216\n"
"v_mfma_f32_32x32x16_bf16 v[48:63], a[236:239], v[172:175], v[48:63]\n"
"ds_read_b128 v[140:143], v4 offset:9728\n"
"s_waitcnt lgkmcnt(4)\n"
"v_mfma_f32_32x32x16_bf16 v[48:63], a[240:243], v[176:179], v[48:63]\n"
"ds_read_b128 v[144:147], v4 offset:18432\n"
"v_mfma_f32_32x32x16_bf16 v[48:63], a[244:247], v[180:183], v[48:63]\n"
"ds_read_b128 v[148:151], v4 offset:18944\n"
"v_mfma_f32_32x32x16_bf16 v[48:63], a[248:251], v[184:187], v[48:63]\n"
"ds_read_b128 v[152:155], v4 offset:27648\n"
"s_sub_u32 s62, s62, 1\n"
"v_mfma_f32_32x32x16_bf16 v[48:63], a[252:255], v[188:191], v[48:63]\n"
"ds_read_b128 v[156:159], v4 offset:28160\n"
"s_cbranch_scc0 .L_hl\n"
"s_nop 1\n"
"ds_read_b32 v74, v11 offset:128\n"
"s_add_u32 s68, s68, 1187840\n"
"s_waitcnt lgkmcnt(0)\n"
"v_lshlrev_b32 v72, 16, v74\n"
"v_and_b32 v74, 0xffff0000, v74\n"
"v_rcp_f32 v73, v74\n"
"v_mul_f32 v74, v74, v74\n"
"v_mul_f32 v73, 0xbfb8aa3b, v73\n"
"v_fma_f32 v48, -v72, v112, v48\n"
"v_fma_f32 v49, -v72, v113, v49\n"
"v_fma_f32 v50, -v72, v114, v50\n"
"v_fma_f32 v51, -v72, v115, v51\n"
"v_fma_f32 v52, -v72, v116, v52\n"
"v_fma_f32 v53, -v72, v117, v53\n"
"v_fma_f32 v54, -v72, v118, v54\n"
"v_fma_f32 v55, -v72, v119, v55\n"
"v_fma_f32 v56, -v72, v120, v56\n"
"v_fma_f32 v57, -v72, v121, v57\n"
"v_fma_f32 v58, -v72, v122, v58\n"
"v_fma_f32 v59, -v72, v123, v59\n"
"v_fma_f32 v60, -v72, v124, v60\n"
"v_fma_f32 v61, -v72, v125, v61\n"
"v_fma_f32 v62, -v72, v126, v62\n"
"v_fma_f32 v63, -v72, v127, v63\n"
"v_mul_f32 v76, v48, v73\n"
"v_mul_f32 v77, v49, v73\n"
"v_mul_f32 v78, v50, v73\n"
"v_mul_f32 v79, v51, v73\n"
"v_mul_f32 v80, v52, v73\n"
"v_mul_f32 v81, v53, v73\n"
"v_mul_f32 v82, v54, v73\n"
"v_mul_f32 v83, v55, v73\n"
"v_exp_f32 v76, v76\n"
"v_exp_f32 v77, v77\n"
"v_mul_f32 v48, v48, v56\n"
"v_mul_f32 v49, v49, v57\n"
"v_exp_f32 v78, v78\n"
"v_exp_f32 v79, v79\n"
"v_mul_f32 v50, v50, v58\n"
"v_mul_f32 v51, v51, v59\n"
"v_exp_f32 v80, v80\n"
"v_exp_f32 v81, v81\n"
"v_mul_f32 v52, v52, v60\n"
"v_mul_f32 v53, v53, v61\n"
"v_exp_f32 v82, v82\n"
"v_exp_f32 v83, v83\n"
"v_mul_f32 v54, v54, v62\n"
"v_mul_f32 v55, v55, v63\n"
"v_fma_f32 v76, v76, v74, v74\n"
"v_fma_f32 v77, v77, v74, v74\n"
"v_rcp_f32 v76, v76\n"
"v_rcp_f32 v77, v77\n"
"v_fma_f32 v78, v78, v74, v74\n"
"v_fma_f32 v79, v79, v74, v74\n"
"v_rcp_f32 v78, v78\n"
"v_rcp_f32 v79, v79\n"
"v_fma_f32 v80, v80, v74, v74\n"
"v_fma_f32 v81, v81, v74, v74\n"
"v_rcp_f32 v80, v80\n"
"v_rcp_f32 v81, v81\n"
"v_fma_f32 v82, v82, v74, v74\n"
"v_fma_f32 v83, v83, v74, v74\n"
"v_rcp_f32 v82, v82\n"
"v_rcp_f32 v83, v83\n"
"v_mul_f32 v48, v48, v76\n"
"v_mul_f32 v49, v49, v77\n"
"v_mul_f32 v50, v50, v78\n"
"v_mul_f32 v51, v51, v79\n"
"v_mul_f32 v52, v52, v80\n"
"v_mul_f32 v53, v53, v81\n"
"v_mul_f32 v54, v54, v82\n"
"v_mul_f32 v55, v55, v83\n"
"v_cvt_pk_bf16_f32 v84, v48, v49\n"
"v_cvt_pk_bf16_f32 v85, v50, v51\n"
"v_cvt_pk_bf16_f32 v86, v52, v53\n"
"v_cvt_pk_bf16_f32 v87, v54, v55\n"
"buffer_store_dwordx4 v[84:87], v15, s[32:35], s68 offen sc1\n"
"s_waitcnt vmcnt(9)\n"
"s_barrier\n"
"buffer_store_dwordx2 v[16:17], v18, s[32:35], s68 offen sc1\n"
"s_add_u32 s68, s68, 1187840\n"
"s_waitcnt vmcnt(1)\n"
"s_barrier\n"
"buffer_store_dwordx2 v[16:17], v18, s[32:35], s68 offen sc1\n"
"s_branch .L_end\n"
".L_d:\n"
"s_lshl_b32 s65, s50, 11\n"
"s_mul_i32 s48, s50, 1152\n"
"s_add_u32 s48, s48, 4612096\n"
"s_add_u32 s65, s65, 4833280\n"
"s_cmp_eq_u32 s29, 0\n"
"s_cselect_b32 s48, s65, s48\n"
"s_cselect_b32 s49, 280, 0\n"
"s_lshl2_add_u32 s49, s27, s49\n"
"s_waitcnt lgkmcnt(0)\n"
"s_add_u32 s54, s52, 0x7f4a7c15\n"
"s_addc_u32 s55, s53, 0x9e3779b9\n"
"s_lshl_b32 s67, s49, 5\n"
"s_add_u32 s67, s67, 4898816\n"
"s_sub_u32 s51, s67, s48\n"
"s_add_u32 s92, s32, s51\n"
"s_addc_u32 s93, s33, 0x10000\n"
"s_cmp_eq_u32 s26, 0\n"
"s_cselect_b32 s94, 1, 0\n"
"s_mov_b32 s95, 0x20000\n"
"v_mov_b32 v8, s54\n"
"v_mov_b32 v9, s55\n"
"v_cmp_gt_u32 vcc, 12, v1\n"
"v_mul_u32_u24 v3, 296960, v1\n"
"v_mov_b32 v6, 0x7fff0000\n"
"v_cndmask_b32 v3, v6, v3, vcc\n"
"s_add_u32 s51, s67, 76021760\n"
"buffer_store_dwordx2 v[8:9], v3, s[32:35], s51 offen sc1\n"
"s_sub_u32 s67, s48, 296960\n"
"s_mul_i32 s48, s28, 296960\n"
"s_add_u32 s65, s65, s48\n"
"s_mul_i32 s96, s29, 3670016\n"
"s_add_u32 s36, s18, 0x0\n"
"s_addc_u32 s37, s19, 0x0\n"
"s_and_b32 s37, s37, 0xffff\n"
"s_mov_b32 s38, 0x700000\n"
"s_mov_b32 s39, 0x20000\n"
"s_add_u32 s40, s18, 0xfffffc80\n"
"s_addc_u32 s41, s19, 0xffffffff\n"
"s_and_b32 s41, s41, 0xffff\n"
"s_mov_b32 s42, 0x700380\n"
"s_mov_b32 s43, 0x20000\n"
"s_lshl_b32 s48, s27, 8\n"
"s_add_u32 s96, s96, s48\n"
"v_and_b32 v8, 3, v1\n"
"v_mul_u32_u24 v8, 16, v8\n"
"s_and_b32 s48, s26, 1\n"
"s_lshl_b32 s48, s48, 6\n"
"s_lshr_b32 s49, s26, 1\n"
"s_lshl_b32 s49, s49, 10\n"
"s_add_u32 s48, s48, s49\n"
"v_add_u32 v8, s48, v8\n"
"v_lshrrev_b32 v4, 3, v1\n"
"v_bfe_u32 v5, v1, 4, 1\n"
"s_and_b32 s48, s26, 1\n"
"s_lshl_b32 s48, s48, 1\n"
"v_or_b32 v5, s48, v5\n"
"v_lshlrev_b32 v5, 1, v5\n"
"v_and_b32 v6, 7, v1\n"
"v_xor_b32 v5, v5, v6\n"
"v_mul_u32_u24 v4, 2048, v4\n"
"v_lshl_add_u32 v7, v5, 4, v4\n"
"s_mul_i32 s48, s26, 16384\n"
"s_add_u32 s96, s96, s48\n"
"s_mul_i32 s97, s26, 2048\n"
"s_add_u32 m0, s97, 0\n"
"s_nop 0\n"
"buffer_load_dwordx4 v7, s[36:39], s96 offen offset:0 lds\n"
"buffer_load_dwordx4 v7, s[40:43], s96 offen offset:1024 lds\n"
"s_add_u32 s96, s96, 65536\n"
"s_add_u32 m0, s97, 8192\n"
"s_nop 0\n"
"buffer_load_dwordx4 v7, s[36:39], s96 offen offset:0 lds\n"
"buffer_load_dwordx4 v7, s[40:43], s96 offen offset:1024 lds\n"
"s_add_u32 s96, s96, 65536\n"
"s_add_u32 m0, s97, 16384\n"
"s_nop 0\n"
"buffer_load_dwordx4 v7, s[36:39], s96 offen offset:0 lds\n"
"buffer_load_dwordx4 v7, s[40:43], s96 offen offset:1024 lds\n"
"s_add_u32 s96, s96, 65536\n"
"s_add_u32 m0, s97, 24576\n"
"s_nop 0\n"
"buffer_load_dwordx4 v7, s[36:39], s96 offen offset:0 lds\n"
"buffer_load_dwordx4 v7, s[40:43], s96 offen offset:1024 lds\n"
"s_add_u32 s96, s96, 65536\n"
"s_add_u32 m0, s97, 32768\n"
"s_nop 0\n"
"buffer_load_dwordx4 v7, s[36:39], s96 offen offset:0 lds\n"
"buffer_load_dwordx4 v7, s[40:43], s96 offen offset:1024 lds\n"
"s_add_u32 s96, s96, 65536\n"
"s_add_u32 m0, s97, 40960\n"
"s_nop 0\n"
"buffer_load_dwordx4 v7, s[36:39], s96 offen offset:0 lds\n"
"buffer_load_dwordx4 v7, s[40:43], s96 offen offset:1024 lds\n"
"s_add_u32 s96, s96, 65536\n"
"s_add_u32 m0, s97, 49152\n"
"s_nop 0\n"
"buffer_load_dwordx4 v7, s[36:39], s96 offen offset:0 lds\n"
"buffer_load_dwordx4 v7, s[40:43], s96 offen offset:1024 lds\n"
"s_add_u32 s96, s96, 65536\n"
"s_add_u32 m0, s97, 57344\n"
"s_nop 0\n"
"buffer_load_dwordx4 v7, s[36:39], s96 offen offset:0 lds\n"
"buffer_load_dwordx4 v7, s[40:43], s96 offen offset:1024 lds\n"
"s_add_u32 s96, s96, 65536\n"
"s_add_u32 m0, s97, 65536\n"
"s_nop 0\n"
"buffer_load_dwordx4 v7, s[36:39], s96 offen offset:0 lds\n"
"buffer_load_dwordx4 v7, s[40:43], s96 offen offset:1024 lds\n"
"s_add_u32 s96, s96, 65536\n"
"s_add_u32 m0, s97, 73728\n"
"s_nop 0\n"
"buffer_load_dwordx4 v7, s[36:39], s96 offen offset:0 lds\n"
"buffer_load_dwordx4 v7, s[40:43], s96 offen offset:1024 lds\n"
"s_add_u32 s96, s96, 65536\n"
"s_add_u32 m0, s97, 81920\n"
"s_nop 0\n"
"buffer_load_dwordx4 v7, s[36:39], s96 offen offset:0 lds\n"
"buffer_load_dwordx4 v7, s[40:43], s96 offen offset:1024 lds\n"
"s_add_u32 s96, s96, 65536\n"
"s_add_u32 m0, s97, 90112\n"
"s_nop 0\n"
"buffer_load_dwordx4 v7, s[36:39], s96 offen offset:0 lds\n"
"buffer_load_dwordx4 v7, s[40:43], s96 offen offset:1024 lds\n"
"s_add_u32 s96, s96, 65536\n"
"s_add_u32 m0, s97, 98304\n"
"s_nop 0\n"
"buffer_load_dwordx4 v7, s[36:39], s96 offen offset:0 lds\n"
"buffer_load_dwordx4 v7, s[40:43], s96 offen offset:1024 lds\n"
"s_add_u32 s96, s96, 65536\n"
"s_add_u32 m0, s97, 106496\n"
"s_nop 0\n"
"buffer_load_dwordx4 v7, s[36:39], s96 offen offset:0 lds\n"
"buffer_load_dwordx4 v7, s[40:43], s96 offen offset:1024 lds\n"
"s_add_u32 s96, s96, 65536\n"
"v_lshrrev_b32 v5, 4, v1\n"
"v_bfe_u32 v4, v1, 3, 1\n"
"v_and_b32 v6, 1, v5\n"
"v_lshl_add_u32 v4, v6, 1, v4\n"
"v_lshlrev_b32 v4, 5, v4\n"
"v_mul_u32_u24 v5, 2048, v5\n"
"v_bfe_u32 v6, v1, 2, 2\n"
"v_lshl_add_u32 v5, v6, 7, v5\n"
"v_xor_b32 v8, v4, v8\n"
"v_add_u32 v8, v5, v8\n"
"v_add_u32 v9, 65536, v8\n"
"s_waitcnt vmcnt(24)\n"
"s_barrier\n"
"s_add_u32 m0, s97, 114688\n"
"s_nop 0\n"
"buffer_load_dwordx4 v7, s[36:39], s96 offen offset:0 lds\n"
"buffer_load_dwordx4 v7, s[40:43], s96 offen offset:1024 lds\n"
"s_add_u32 s96, s96, 65536\n"
"s_add_u32 m0, s97, 122880\n"
"s_nop 0\n"
"buffer_load_dwordx4 v7, s[36:39], s96 offen offset:0 lds\n"
"buffer_load_dwordx4 v7, s[40:43], s96 offen offset:1024 lds\n"
"s_add_u32 s96, s96, 65536\n"
"ds_read_b64_tr_b16 a[0:1], v8 offset:0\n"
"ds_read_b64_tr_b16 a[2:3], v8 offset:512\n"
"ds_read_b64_tr_b16 a[4:5], v8 offset:8\n"
"ds_read_b64_tr_b16 a[6:7], v8 offset:520\n"
"ds_read_b64_tr_b16 a[8:9], v8 offset:8192\n"
"ds_read_b64_tr_b16 a[10:11], v8 offset:8704\n"
"ds_read_b64_tr_b16 a[12:13], v8 offset:8200\n"
"ds_read_b64_tr_b16 a[14:15], v8 offset:8712\n"
"s_waitcnt lgkmcnt(0)\n"
"s_waitcnt vmcnt(24)\n"
"s_barrier\n"
"s_add_u32 m0, s97, 0\n"
"s_nop 0\n"
"buffer_load_dwordx4 v7, s[36:39], s96 offen offset:0 lds\n"
"buffer_load_dwordx4 v7, s[40:43], s96 offen offset:1024 lds\n"
"s_add_u32 s96, s96, 65536\n"
"s_add_u32 m0, s97, 8192\n"
"s_nop 0\n"
"buffer_load_dwordx4 v7, s[36:39], s96 offen offset:0 lds\n"
"buffer_load_dwordx4 v7, s[40:43], s96 offen offset:1024 lds\n"
"s_add_u32 s96, s96, 65536\n"
"ds_read_b64_tr_b16 a[16:17], v8 offset:16384\n"
"ds_read_b64_tr_b16 a[18:19], v8 offset:16896\n"
"ds_read_b64_tr_b16 a[20:21], v8 offset:16392\n"
"ds_read_b64_tr_b16 a[22:23], v8 offset:16904\n"
"ds_read_b64_tr_b16 a[24:25], v8 offset:24576\n"
"ds_read_b64_tr_b16 a[26:27], v8 offset:25088\n"
"ds_read_b64_tr_b16 a[28:29], v8 offset:24584\n"
"ds_read_b64_tr_b16 a[30:31], v8 offset:25096\n"
"s_waitcnt lgkmcnt(0)\n"
"s_waitcnt vmcnt(24)\n"
"s_barrier\n"
"s_add_u32 m0, s97, 16384\n"
"s_nop 0\n"
"buffer_load_dwordx4 v7, s[36:39], s96 offen offset:0 lds\n"
"buffer_load_dwordx4 v7, s[40:43], s96 offen offset:1024 lds\n"
"s_add_u32 s96, s96, 65536\n"
"s_add_u32 m0, s97, 24576\n"
"s_nop 0\n"
"buffer_load_dwordx4 v7, s[36:39], s96 offen offset:0 lds\n"
"buffer_load_dwordx4 v7, s[40:43], s96 offen offset:1024 lds\n"
"s_add_u32 s96, s96, 65536\n"
"ds_read_b64_tr_b16 a[32:33], v8 offset:32768\n"
"ds_read_b64_tr_b16 a[34:35], v8 offset:33280\n"
"ds_read_b64_tr_b16 a[36:37], v8 offset:32776\n"
"ds_read_b64_tr_b16 a[38:39], v8 offset:33288\n"
"ds_read_b64_tr_b16 a[40:41], v8 offset:40960\n"
"ds_read_b64_tr_b16 a[42:43], v8 offset:41472\n"
"ds_read_b64_tr_b16 a[44:45], v8 offset:40968\n"
"ds_read_b64_tr_b16 a[46:47], v8 offset:41480\n"
"s_waitcnt lgkmcnt(0)\n"
"s_waitcnt vmcnt(24)\n"
"s_barrier\n"
"s_add_u32 m0, s97, 32768\n"
"s_nop 0\n"
"buffer_load_dwordx4 v7, s[36:39], s96 offen offset:0 lds\n"
"buffer_load_dwordx4 v7, s[40:43], s96 offen offset:1024 lds\n"
"s_add_u32 s96, s96, 65536\n"
"s_add_u32 m0, s97, 40960\n"
"s_nop 0\n"
"buffer_load_dwordx4 v7, s[36:39], s96 offen offset:0 lds\n"
"buffer_load_dwordx4 v7, s[40:43], s96 offen offset:1024 lds\n"
"s_add_u32 s96, s96, 65536\n"
"ds_read_b64_tr_b16 a[48:49], v8 offset:49152\n"
"ds_read_b64_tr_b16 a[50:51], v8 offset:49664\n"
"ds_read_b64_tr_b16 a[52:53], v8 offset:49160\n"
"ds_read_b64_tr_b16 a[54:55], v8 offset:49672\n"
"ds_read_b64_tr_b16 a[56:57], v8 offset:57344\n"
"ds_read_b64_tr_b16 a[58:59], v8 offset:57856\n"
"ds_read_b64_tr_b16 a[60:61], v8 offset:57352\n"
"ds_read_b64_tr_b16 a[62:63], v8 offset:57864\n"
"s_waitcnt lgkmcnt(0)\n"
"s_waitcnt vmcnt(24)\n"
"s_barrier\n"
"s_add_u32 m0, s97, 49152\n"
"s_nop 0\n"
"buffer_load_dwordx4 v7, s[36:39], s96 offen offset:0 lds\n"
"buffer_load_dwordx4 v7, s[40:43], s96 offen offset:1024 lds\n"
"s_add_u32 s96, s96, 65536\n"
"s_add_u32 m0, s97, 57344\n"
"s_nop 0\n"
"buffer_load_dwordx4 v7, s[36:39], s96 offen offset:0 lds\n"
"buffer_load_dwordx4 v7, s[40:43], s96 offen offset:1024 lds\n"
"s_add_u32 s96, s96, 65536\n"
"ds_read_b64_tr_b16 a[64:65], v9 offset:0\n"
"ds_read_b64_tr_b16 a[66:67], v9 offset:512\n"
"ds_read_b64_tr_b16 a[68:69], v9 offset:8\n"
"ds_read_b64_tr_b16 a[70:71], v9 offset:520\n"
"ds_read_b64_tr_b16 a[72:73], v9 offset:8192\n"
"ds_read_b64_tr_b16 a[74:75], v9 offset:8704\n"
"ds_read_b64_tr_b16 a[76:77], v9 offset:8200\n"
"ds_read_b64_tr_b16 a[78:79], v9 offset:8712\n"
"s_waitcnt lgkmcnt(0)\n"
"s_waitcnt vmcnt(24)\n"
"s_barrier\n"
"s_add_u32 m0, s97, 65536\n"
"s_nop 0\n"
"buffer_load_dwordx4 v7, s[36:39], s96 offen offset:0 lds\n"
"buffer_load_dwordx4 v7, s[40:43], s96 offen offset:1024 lds\n"
"s_add_u32 s96, s96, 65536\n"
"s_add_u32 m0, s97, 73728\n"
"s_nop 0\n"
"buffer_load_dwordx4 v7, s[36:39], s96 offen offset:0 lds\n"
"buffer_load_dwordx4 v7, s[40:43], s96 offen offset:1024 lds\n"
"s_add_u32 s96, s96, 65536\n"
"ds_read_b64_tr_b16 a[80:81], v9 offset:16384\n"
"ds_read_b64_tr_b16 a[82:83], v9 offset:16896\n"
"ds_read_b64_tr_b16 a[84:85], v9 offset:16392\n"
"ds_read_b64_tr_b16 a[86:87], v9 offset:16904\n"
"ds_read_b64_tr_b16 a[88:89], v9 offset:24576\n"
"ds_read_b64_tr_b16 a[90:91], v9 offset:25088\n"
"ds_read_b64_tr_b16 a[92:93], v9 offset:24584\n"
"ds_read_b64_tr_b16 a[94:95], v9 offset:25096\n"
"s_waitcnt lgkmcnt(0)\n"
"s_waitcnt vmcnt(24)\n"
"s_barrier\n"
"s_add_u32 m0, s97, 81920\n"
"s_nop 0\n"
"buffer_load_dwordx4 v7, s[36:39], s96 offen offset:0 lds\n"
"buffer_load_dwordx4 v7, s[40:43], s96 offen offset:1024 lds\n"
"s_add_u32 s96, s96, 65536\n"
"s_add_u32 m0, s97, 90112\n"
"s_nop 0\n"
"buffer_load_dwordx4 v7, s[36:39], s96 offen offset:0 lds\n"
"buffer_load_dwordx4 v7, s[40:43], s96 offen offset:1024 lds\n"
"s_add_u32 s96, s96, 65536\n"
"ds_read_b64_tr_b16 a[96:97], v9 offset:32768\n"
"ds_read_b64_tr_b16 a[98:99], v9 offset:33280\n"
"ds_read_b64_tr_b16 a[100:101], v9 offset:32776\n"
"ds_read_b64_tr_b16 a[102:103], v9 offset:33288\n"
"ds_read_b64_tr_b16 a[104:105], v9 offset:40960\n"
"ds_read_b64_tr_b16 a[106:107], v9 offset:41472\n"
"ds_read_b64_tr_b16 a[108:109], v9 offset:40968\n"
"ds_read_b64_tr_b16 a[110:111], v9 offset:41480\n"
"s_waitcnt lgkmcnt(0)\n"
"s_waitcnt vmcnt(24)\n"
"s_barrier\n"
"s_add_u32 m0, s97, 98304\n"
"s_nop 0\n"
"buffer_load_dwordx4 v7, s[36:39], s96 offen offset:0 lds\n"
"buffer_load_dwordx4 v7, s[40:43], s96 offen offset:1024 lds\n"
"s_add_u32 s96, s96, 65536\n"
"s_add_u32 m0, s97, 106496\n"
"s_nop 0\n"
"buffer_load_dwordx4 v7, s[36:39], s96 offen offset:0 lds\n"
"buffer_load_dwordx4 v7, s[40:43], s96 offen offset:1024 lds\n"
"s_add_u32 s96, s96, 65536\n"
"ds_read_b64_tr_b16 a[112:113], v9 offset:49152\n"
"ds_read_b64_tr_b16 a[114:115], v9 offset:49664\n"
"ds_read_b64_tr_b16 a[116:117], v9 offset:49160\n"
"ds_read_b64_tr_b16 a[118:119], v9 offset:49672\n"
"ds_read_b64_tr_b16 a[120:121], v9 offset:57344\n"
"ds_read_b64_tr_b16 a[122:123], v9 offset:57856\n"
"ds_read_b64_tr_b16 a[124:125], v9 offset:57352\n"
"ds_read_b64_tr_b16 a[126:127], v9 offset:57864\n"
"s_waitcnt lgkmcnt(0)\n"
"s_waitcnt vmcnt(24)\n"
"s_barrier\n"
"s_add_u32 m0, s97, 114688\n"
"s_nop 0\n"
"buffer_load_dwordx4 v7, s[36:39], s96 offen offset:0 lds\n"
"buffer_load_dwordx4 v7, s[40:43], s96 offen offset:1024 lds\n"
"s_add_u32 s96, s96, 65536\n"
"s_add_u32 m0, s97, 122880\n"
"s_nop 0\n"
"buffer_load_dwordx4 v7, s[36:39], s96 offen offset:0 lds\n"
"buffer_load_dwordx4 v7, s[40:43], s96 offen offset:1024 lds\n"
"s_add_u32 s96, s96, 65536\n"
"ds_read_b64_tr_b16 a[128:129], v8 offset:0\n"
"ds_read_b64_tr_b16 a[130:131], v8 offset:512\n"
"ds_read_b64_tr_b16 a[132:133], v8 offset:8\n"
"ds_read_b64_tr_b16 a[134:135], v8 offset:520\n"
"ds_read_b64_tr_b16 a[136:137], v8 offset:8192\n"
"ds_read_b64_tr_b16 a[138:139], v8 offset:8704\n"
"ds_read_b64_tr_b16 a[140:141], v8 offset:8200\n"
"ds_read_b64_tr_b16 a[142:143], v8 offset:8712\n"
"s_waitcnt lgkmcnt(0)\n"
"s_waitcnt vmcnt(24)\n"
"s_barrier\n"
"s_add_u32 m0, s97, 0\n"
"s_nop 0\n"
"buffer_load_dwordx4 v7, s[36:39], s96 offen offset:0 lds\n"
"buffer_load_dwordx4 v7, s[40:43], s96 offen offset:1024 lds\n"
"s_add_u32 s96, s96, 65536\n"
"s_add_u32 m0, s97, 8192\n"
"s_nop 0\n"
"buffer_load_dwordx4 v7, s[36:39], s96 offen offset:0 lds\n"
"buffer_load_dwordx4 v7, s[40:43], s96 offen offset:1024 lds\n"
"s_add_u32 s96, s96, 65536\n"
"ds_read_b64_tr_b16 a[144:145], v8 offset:16384\n"
"ds_read_b64_tr_b16 a[146:147], v8 offset:16896\n"
"ds_read_b64_tr_b16 a[148:149], v8 offset:16392\n"
"ds_read_b64_tr_b16 a[150:151], v8 offset:16904\n"
"ds_read_b64_tr_b16 a[152:153], v8 offset:24576\n"
"ds_read_b64_tr_b16 a[154:155], v8 offset:25088\n"
"ds_read_b64_tr_b16 a[156:157], v8 offset:24584\n"
"ds_read_b64_tr_b16 a[158:159], v8 offset:25096\n"
"s_waitcnt lgkmcnt(0)\n"
"s_waitcnt vmcnt(24)\n"
"s_barrier\n"
"s_add_u32 m0, s97, 16384\n"
"s_nop 0\n"
"buffer_load_dwordx4 v7, s[36:39], s96 offen offset:0 lds\n"
"buffer_load_dwordx4 v7, s[40:43], s96 offen offset:1024 lds\n"
"s_add_u32 s96, s96, 65536\n"
"s_add_u32 m0, s97, 24576\n"
"s_nop 0\n"
"buffer_load_dwordx4 v7, s[36:39], s96 offen offset:0 lds\n"
"buffer_load_dwordx4 v7, s[40:43], s96 offen offset:1024 lds\n"
"s_add_u32 s96, s96, 65536\n"
"ds_read_b64_tr_b16 a[160:161], v8 offset:32768\n"
"ds_read_b64_tr_b16 a[162:163], v8 offset:33280\n"
"ds_read_b64_tr_b16 a[164:165], v8 offset:32776\n"
"ds_read_b64_tr_b16 a[166:167], v8 offset:33288\n"
"ds_read_b64_tr_b16 a[168:169], v8 offset:40960\n"
"ds_read_b64_tr_b16 a[170:171], v8 offset:41472\n"
"ds_read_b64_tr_b16 a[172:173], v8 offset:40968\n"
"ds_read_b64_tr_b16 a[174:175], v8 offset:41480\n"
"s_waitcnt lgkmcnt(0)\n"
"s_waitcnt vmcnt(24)\n"
"s_barrier\n"
"s_add_u32 m0, s97, 32768\n"
"s_nop 0\n"
"buffer_load_dwordx4 v7, s[36:39], s96 offen offset:0 lds\n"
"buffer_load_dwordx4 v7, s[40:43], s96 offen offset:1024 lds\n"
"s_add_u32 s96, s96, 65536\n"
"s_add_u32 m0, s97, 40960\n"
"s_nop 0\n"
"buffer_load_dwordx4 v7, s[36:39], s96 offen offset:0 lds\n"
"buffer_load_dwordx4 v7, s[40:43], s96 offen offset:1024 lds\n"
"s_add_u32 s96, s96, 65536\n"
"ds_read_b64_tr_b16 a[176:177], v8 offset:49152\n"
"ds_read_b64_tr_b16 a[178:179], v8 offset:49664\n"
"ds_read_b64_tr_b16 a[180:181], v8 offset:49160\n"
"ds_read_b64_tr_b16 a[182:183], v8 offset:49672\n"
"ds_read_b64_tr_b16 a[184:185], v8 offset:57344\n"
"ds_read_b64_tr_b16 a[186:187], v8 offset:57856\n"
"ds_read_b64_tr_b16 a[188:189], v8 offset:57352\n"
"ds_read_b64_tr_b16 a[190:191], v8 offset:57864\n"
"s_waitcnt lgkmcnt(0)\n"
"s_waitcnt vmcnt(24)\n"
"s_barrier\n"
"s_add_u32 m0, s97, 49152\n"
"s_nop 0\n"
"buffer_load_dwordx4 v7, s[36:39], s96 offen offset:0 lds\n"
"buffer_load_dwordx4 v7, s[40:43], s96 offen offset:1024 lds\n"
"s_add_u32 s96, s96, 65536\n"
"s_add_u32 m0, s97, 57344\n"
"s_nop 0\n"
"buffer_load_dwordx4 v7, s[36:39], s96 offen offset:0 lds\n"
"buffer_load_dwordx4 v7, s[40:43], s96 offen offset:1024 lds\n"
"s_add_u32 s96, s96, 65536\n"
"ds_read_b64_tr_b16 a[192:193], v9 offset:0\n"
"ds_read_b64_tr_b16 a[194:195], v9 offset:512\n"
"ds_read_b64_tr_b16 a[196:197], v9 offset:8\n"
"ds_read_b64_tr_b16 a[198:199], v9 offset:520\n"
"ds_read_b64_tr_b16 a[200:201], v9 offset:8192\n"
"ds_read_b64_tr_b16 a[202:203], v9 offset:8704\n"
"ds_read_b64_tr_b16 a[204:205], v9 offset:8200\n"
"ds_read_b64_tr_b16 a[206:207], v9 offset:8712\n"
"s_waitcnt lgkmcnt(0)\n"
"s_waitcnt vmcnt(24)\n"
"s_barrier\n"
"s_add_u32 m0, s97, 65536\n"
"s_nop 0\n"
"buffer_load_dwordx4 v7, s[36:39], s96 offen offset:0 lds\n"
"buffer_load_dwordx4 v7, s[40:43], s96 offen offset:1024 lds\n"
"s_add_u32 s96, s96, 65536\n"
"s_add_u32 m0, s97, 73728\n"
"s_nop 0\n"
"buffer_load_dwordx4 v7, s[36:39], s96 offen offset:0 lds\n"
"buffer_load_dwordx4 v7, s[40:43], s96 offen offset:1024 lds\n"
"s_add_u32 s96, s96, 65536\n"
"ds_read_b64_tr_b16 a[208:209], v9 offset:16384\n"
"ds_read_b64_tr_b16 a[210:211], v9 offset:16896\n"
"ds_read_b64_tr_b16 a[212:213], v9 offset:16392\n"
"ds_read_b64_tr_b16 a[214:215], v9 offset:16904\n"
"ds_read_b64_tr_b16 a[216:217], v9 offset:24576\n"
"ds_read_b64_tr_b16 a[218:219], v9 offset:25088\n"
"ds_read_b64_tr_b16 a[220:221], v9 offset:24584\n"
"ds_read_b64_tr_b16 a[222:223], v9 offset:25096\n"
"s_waitcnt lgkmcnt(0)\n"
"s_waitcnt vmcnt(24)\n"
"s_barrier\n"
"s_add_u32 m0, s97, 81920\n"
"s_nop 0\n"
"buffer_load_dwordx4 v7, s[36:39], s96 offen offset:0 lds\n"
"buffer_load_dwordx4 v7, s[40:43], s96 offen offset:1024 lds\n"
"s_add_u32 s96, s96, 65536\n"
"s_add_u32 m0, s97, 90112\n"
"s_nop 0\n"
"buffer_load_dwordx4 v7, s[36:39], s96 offen offset:0 lds\n"
"buffer_load_dwordx4 v7, s[40:43], s96 offen offset:1024 lds\n"
"s_add_u32 s96, s96, 65536\n"
"ds_read_b64_tr_b16 a[224:225], v9 offset:32768\n"
"ds_read_b64_tr_b16 a[226:227], v9 offset:33280\n"
"ds_read_b64_tr_b16 a[228:229], v9 offset:32776\n"
"ds_read_b64_tr_b16 a[230:231], v9 offset:33288\n"
"ds_read_b64_tr_b16 a[232:233], v9 offset:40960\n"
"ds_read_b64_tr_b16 a[234:235], v9 offset:41472\n"
"ds_read_b64_tr_b16 a[236:237], v9 offset:40968\n"
"ds_read_b64_tr_b16 a[238:239], v9 offset:41480\n"
"s_waitcnt lgkmcnt(0)\n"
"s_waitcnt vmcnt(24)\n"
"s_barrier\n"
"s_add_u32 m0, s97, 98304\n"
"s_nop 0\n"
"buffer_load_dwordx4 v7, s[36:39], s96 offen offset:0 lds\n"
"buffer_load_dwordx4 v7, s[40:43], s96 offen offset:1024 lds\n"
"s_add_u32 s96, s96, 65536\n"
"s_add_u32 m0, s97, 106496\n"
"s_nop 0\n"
"buffer_load_dwordx4 v7, s[36:39], s96 offen offset:0 lds\n"
"buffer_load_dwordx4 v7, s[40:43], s96 offen offset:1024 lds\n"
"s_add_u32 s96, s96, 65536\n"
"ds_read_b64_tr_b16 a[240:241], v9 offset:49152\n"
"ds_read_b64_tr_b16 a[242:243], v9 offset:49664\n"
"ds_read_b64_tr_b16 a[244:245], v9 offset:49160\n"
"ds_read_b64_tr_b16 a[246:247], v9 offset:49672\n"
"ds_read_b64_tr_b16 a[248:249], v9 offset:57344\n"
"ds_read_b64_tr_b16 a[250:251], v9 offset:57856\n"
"ds_read_b64_tr_b16 a[252:253], v9 offset:57352\n"
"ds_read_b64_tr_b16 a[254:255], v9 offset:57864\n"
"s_waitcnt lgkmcnt(0)\n"
"s_waitcnt vmcnt(24)\n"
"s_barrier\n"
"s_add_u32 m0, s97, 114688\n"
"s_nop 0\n"
"buffer_load_dwordx4 v7, s[36:39], s96 offen offset:0 lds\n"
"buffer_load_dwordx4 v7, s[40:43], s96 offen offset:1024 lds\n"
"s_add_u32 s96, s96, 65536\n"
"s_add_u32 m0, s97, 122880\n"
"s_nop 0\n"
"buffer_load_dwordx4 v7, s[36:39], s96 offen offset:0 lds\n"
"buffer_load_dwordx4 v7, s[40:43], s96 offen offset:1024 lds\n"
"s_add_u32 s96, s96, 65536\n"
"ds_read_b64_tr_b16 v[64:65], v8 offset:0\n"
"ds_read_b64_tr_b16 v[66:67], v8 offset:512\n"
"ds_read_b64_tr_b16 v[68:69], v8 offset:8\n"
"ds_read_b64_tr_b16 v[70:71], v8 offset:520\n"
"ds_read_b64_tr_b16 v[72:73], v8 offset:8192\n"
"ds_read_b64_tr_b16 v[74:75], v8 offset:8704\n"
"ds_read_b64_tr_b16 v[76:77], v8 offset:8200\n"
"ds_read_b64_tr_b16 v[78:79], v8 offset:8712\n"
"s_waitcnt lgkmcnt(0)\n"
"s_waitcnt vmcnt(24)\n"
"s_barrier\n"
"s_add_u32 m0, s97, 0\n"
"s_nop 0\n"
"buffer_load_dwordx4 v7, s[36:39], s96 offen offset:0 lds\n"
"buffer_load_dwordx4 v7, s[40:43], s96 offen offset:1024 lds\n"
"s_add_u32 s96, s96, 65536\n"
"s_add_u32 m0, s97, 8192\n"
"s_nop 0\n"
"buffer_load_dwordx4 v7, s[36:39], s96 offen offset:0 lds\n"
"buffer_load_dwordx4 v7, s[40:43], s96 offen offset:1024 lds\n"
"s_add_u32 s96, s96, 65536\n"
"ds_read_b64_tr_b16 v[80:81], v8 offset:16384\n"
"ds_read_b64_tr_b16 v[82:83], v8 offset:16896\n"
"ds_read_b64_tr_b16 v[84:85], v8 offset:16392\n"
"ds_read_b64_tr_b16 v[86:87], v8 offset:16904\n"
"ds_read_b64_tr_b16 v[88:89], v8 offset:24576\n"
"ds_read_b64_tr_b16 v[90:91], v8 offset:25088\n"
"ds_read_b64_tr_b16 v[92:93], v8 offset:24584\n"
"ds_read_b64_tr_b16 v[94:95], v8 offset:25096\n"
"s_waitcnt lgkmcnt(0)\n"
"s_waitcnt vmcnt(24)\n"
"s_barrier\n"
"s_add_u32 m0, s97, 16384\n"
"s_nop 0\n"
"buffer_load_dwordx4 v7, s[36:39], s96 offen offset:0 lds\n"
"buffer_load_dwordx4 v7, s[40:43], s96 offen offset:1024 lds\n"
"s_add_u32 s96, s96, 65536\n"
"s_add_u32 m0, s97, 24576\n"
"s_nop 0\n"
"buffer_load_dwordx4 v7, s[36:39], s96 offen offset:0 lds\n"
"buffer_load_dwordx4 v7, s[40:43], s96 offen offset:1024 lds\n"
"s_add_u32 s96, s96, 65536\n"
"ds_read_b64_tr_b16 v[96:97], v8 offset:32768\n"
"ds_read_b64_tr_b16 v[98:99], v8 offset:33280\n"
"ds_read_b64_tr_b16 v[100:101], v8 offset:32776\n"
"ds_read_b64_tr_b16 v[102:103], v8 offset:33288\n"
"ds_read_b64_tr_b16 v[104:105], v8 offset:40960\n"
"ds_read_b64_tr_b16 v[106:107], v8 offset:41472\n"
"ds_read_b64_tr_b16 v[108:109], v8 offset:40968\n"
"ds_read_b64_tr_b16 v[110:111], v8 offset:41480\n"
"s_waitcnt lgkmcnt(0)\n"
"s_waitcnt vmcnt(24)\n"
"s_barrier\n"
"s_add_u32 m0, s97, 32768\n"
"s_nop 0\n"
"buffer_load_dwordx4 v7, s[36:39], s96 offen offset:0 lds\n"
"buffer_load_dwordx4 v7, s[40:43], s96 offen offset:1024 lds\n"
"s_add_u32 s96, s96, 65536\n"
"s_add_u32 m0, s97, 40960\n"
"s_nop 0\n"
"buffer_load_dwordx4 v7, s[36:39], s96 offen offset:0 lds\n"
"buffer_load_dwordx4 v7, s[40:43], s96 offen offset:1024 lds\n"
"s_add_u32 s96, s96, 65536\n"
"ds_read_b64_tr_b16 v[112:113], v8 offset:49152\n"
"ds_read_b64_tr_b16 v[114:115], v8 offset:49664\n"
"ds_read_b64_tr_b16 v[116:117], v8 offset:49160\n"
"ds_read_b64_tr_b16 v[118:119], v8 offset:49672\n"
"ds_read_b64_tr_b16 v[120:121], v8 offset:57344\n"
"ds_read_b64_tr_b16 v[122:123], v8 offset:57856\n"
"ds_read_b64_tr_b16 v[124:125], v8 offset:57352\n"
"ds_read_b64_tr_b16 v[126:127], v8 offset:57864\n"
"s_waitcnt lgkmcnt(0)\n"
"s_waitcnt vmcnt(24)\n"
"s_barrier\n"
"s_add_u32 m0, s97, 49152\n"
"s_nop 0\n"
"buffer_load_dwordx4 v7, s[36:39], s96 offen offset:0 lds\n"
"buffer_load_dwordx4 v7, s[40:43], s96 offen offset:1024 lds\n"
"s_add_u32 s96, s96, 65536\n"
"s_add_u32 m0, s97, 57344\n"
"s_nop 0\n"
"buffer_load_dwordx4 v7, s[36:39], s96 offen offset:0 lds\n"
"buffer_load_dwordx4 v7, s[40:43], s96 offen offset:1024 lds\n"
"s_add_u32 s96, s96, 65536\n"
"ds_read_b64_tr_b16 v[128:129], v9 offset:0\n"
"ds_read_b64_tr_b16 v[130:131], v9 offset:512\n"
"ds_read_b64_tr_b16 v[132:133], v9 offset:8\n"
"ds_read_b64_tr_b16 v[134:135], v9 offset:520\n"
"ds_read_b64_tr_b16 v[136:137], v9 offset:8192\n"
"ds_read_b64_tr_b16 v[138:139], v9 offset:8704\n"
"ds_read_b64_tr_b16 v[140:141], v9 offset:8200\n"
"ds_read_b64_tr_b16 v[142:143], v9 offset:8712\n"
"s_waitcnt lgkmcnt(0)\n"
"s_waitcnt vmcnt(24)\n"
"s_barrier\n"
"ds_read_b64_tr_b16 v[144:145], v9 offset:16384\n"
"ds_read_b64_tr_b16 v[146:147], v9 offset:16896\n"
"ds_read_b64_tr_b16 v[148:149], v9 offset:16392\n"
"ds_read_b64_tr_b16 v[150:151], v9 offset:16904\n"
"ds_read_b64_tr_b16 v[152:153], v9 offset:24576\n"
"ds_read_b64_tr_b16 v[154:155], v9 offset:25088\n"
"ds_read_b64_tr_b16 v[156:157], v9 offset:24584\n"
"ds_read_b64_tr_b16 v[158:159], v9 offset:25096\n"
"s_waitcnt lgkmcnt(0)\n"
"s_waitcnt vmcnt(20)\n"
"s_barrier\n"
"ds_read_b64_tr_b16 v[160:161], v9 offset:32768\n"
"ds_read_b64_tr_b16 v[162:163], v9 offset:33280\n"
"ds_read_b64_tr_b16 v[164:165], v9 offset:32776\n"
"ds_read_b64_tr_b16 v[166:167], v9 offset:33288\n"
"ds_read_b64_tr_b16 v[168:169], v9 offset:40960\n"
"ds_read_b64_tr_b16 v[170:171], v9 offset:41472\n"
"ds_read_b64_tr_b16 v[172:173], v9 offset:40968\n"
"ds_read_b64_tr_b16 v[174:175], v9 offset:41480\n"
"s_waitcnt lgkmcnt(0)\n"
"s_waitcnt vmcnt(16)\n"
"s_barrier\n"
"ds_read_b64_tr_b16 v[176:177], v9 offset:49152\n"
"ds_read_b64_tr_b16 v[178:179], v9 offset:49664\n"
"ds_read_b64_tr_b16 v[180:181], v9 offset:49160\n"
"ds_read_b64_tr_b16 v[182:183], v9 offset:49672\n"
"ds_read_b64_tr_b16 v[184:185], v9 offset:57344\n"
"ds_read_b64_tr_b16 v[186:187], v9 offset:57856\n"
"ds_read_b64_tr_b16 v[188:189], v9 offset:57352\n"
"ds_read_b64_tr_b16 v[190:191], v9 offset:57864\n"
"s_waitcnt lgkmcnt(0)\n"
"s_waitcnt vmcnt(12)\n"
"s_barrier\n"
"ds_read_b64_tr_b16 v[192:193], v8 offset:0\n"
"ds_read_b64_tr_b16 v[194:195], v8 offset:512\n"
"ds_read_b64_tr_b16 v[196:197], v8 offset:8\n"
"ds_read_b64_tr_b16 v[198:199], v8 offset:520\n"
"ds_read_b64_tr_b16 v[200:201], v8 offset:8192\n"
"ds_read_b64_tr_b16 v[202:203], v8 offset:8704\n"
"ds_read_b64_tr_b16 v[204:205], v8 offset:8200\n"
"ds_read_b64_tr_b16 v[206:207], v8 offset:8712\n"
"s_waitcnt lgkmcnt(0)\n"
"s_waitcnt vmcnt(8)\n"
"s_barrier\n"
"ds_read_b64_tr_b16 v[208:209], v8 offset:16384\n"
"ds_read_b64_tr_b16 v[210:211], v8 offset:16896\n"
"ds_read_b64_tr_b16 v[212:213], v8 offset:16392\n"
"ds_read_b64_tr_b16 v[214:215], v8 offset:16904\n"
"ds_read_b64_tr_b16 v[216:217], v8 offset:24576\n"
"ds_read_b64_tr_b16 v[218:219], v8 offset:25088\n"
"ds_read_b64_tr_b16 v[220:221], v8 offset:24584\n"
"ds_read_b64_tr_b16 v[222:223], v8 offset:25096\n"
"s_waitcnt lgkmcnt(0)\n"
"s_waitcnt vmcnt(4)\n"
"s_barrier\n"
"ds_read_b64_tr_b16 v[224:225], v8 offset:32768\n"
"ds_read_b64_tr_b16 v[226:227], v8 offset:33280\n"
"ds_read_b64_tr_b16 v[228:229], v8 offset:32776\n"
"ds_read_b64_tr_b16 v[230:231], v8 offset:33288\n"
"ds_read_b64_tr_b16 v[232:233], v8 offset:40960\n"
"ds_read_b64_tr_b16 v[234:235], v8 offset:41472\n"
"ds_read_b64_tr_b16 v[236:237], v8 offset:40968\n"
"ds_read_b64_tr_b16 v[238:239], v8 offset:41480\n"
"s_waitcnt lgkmcnt(0)\n"
"s_waitcnt vmcnt(0)\n"
"s_barrier\n"
"ds_read_b64_tr_b16 v[240:241], v8 offset:49152\n"
"ds_read_b64_tr_b16 v[242:243], v8 offset:49664\n"
"ds_read_b64_tr_b16 v[244:245], v8 offset:49160\n"
"ds_read_b64_tr_b16 v[246:247], v8 offset:49672\n"
"ds_read_b64_tr_b16 v[248:249], v8 offset:57344\n"
"ds_read_b64_tr_b16 v[250:251], v8 offset:57856\n"
"ds_read_b64_tr_b16 v[252:253], v8 offset:57352\n"
"ds_read_b64_tr_b16 v[254:255], v8 offset:57864\n"
"s_waitcnt lgkmcnt(0)\n"
"s_barrier\n"
"v_mov_b32 v8, s54\n"
"v_mov_b32 v9, s55\n"
"s_lshl2_add_u32 s48, s29, s26\n"
"s_mul_i32 s63, s48, 14336\n"
"s_mul_i32 s49, s48, 2446696\n"
"s_add_u32 s48, s49, 2403005\n"
"s_lshr_b32 s49, s49, 17\n"
"s_lshr_b32 s48, s48, 17\n"
"s_cmp_gt_u32 s48, 143\n"
"s_cselect_b32 s48, 151, s48\n"
"s_sub_u32 s48, s48, s49\n"
"s_add_u32 s49, s49, 128\n"
"v_add_u32 v0, s49, v1\n"
"s_add_u32 s49, s50, 280\n"
"v_writelane_b32 v0, s49, 63\n"
"v_lshrrev_b32 v0, 2, v0\n"
"v_lshlrev_b32 v0, 7, v0\n"
"v_cmp_ge_u32 vcc, s48, v1\n"
"s_lshl_b32 s49, s29, 31\n"
"s_nop 4\n"
"s_or_b32 vcc_hi, vcc_hi, s49\n"
"s_nop 4\n"
"v_mov_b32 v3, 0x7fff0000\n"
"v_cndmask_b32 v0, v3, v0, vcc\n"
"v_cndmask_b32 v8, 0, v8, vcc\n"
"v_cndmask_b32 v9, 0, v9, vcc\n"
"v_mov_b32 v3, v21\n"
"v_mov_b32 v1, v20\n"
"s_mul_i32 s48, s28, 296960\n"
"s_add_u32 s63, s63, s48\n"
"s_add_u32 s63, s63, 4718592\n"
"s_add_u32 s66, s48, 4898816\n"
"s_mul_i32 s64, s26, 14336\n"
"s_lshl_b32 s74, s26, 11\n"
"s_add_u32 s74, s74, 114688\n"
"v_add_u32 v5, s74, v2\n"
"v_add_u32 v4, 57344, v2\n"
"s_mov_b32 s48, 20000\n"
"s_branch .L_rfdx\n"
".L_rldx:\n"
"s_sleep 8\n"
".L_rfdx:\n"
"buffer_load_dwordx2 v[6:7], v0, s[32:35], s66 offen sc1\n"
"s_sub_u32 s48, s48, 1\n"
"s_cbranch_scc1 .L_end\n"
"s_waitcnt vmcnt(0)\n"
"v_cmp_ne_u64 vcc, v[8:9], v[6:7]\n"
"s_cbranch_vccnz .L_rldx\n"
"s_add_u32 s66, s66, 1187840\n"
"s_add_u32 m0, s64, 0\n"
"s_nop 0\n"
"buffer_load_dwordx4 v2, s[32:35], s63 offen offset:0 sc1 lds\n"
"buffer_load_dwordx4 v2, s[32:35], s63 offen offset:1024 sc1 lds\n"
"buffer_load_dwordx4 v2, s[32:35], s63 offen offset:2048 sc1 lds\n"
"buffer_load_dwordx4 v2, s[32:35], s63 offen offset:3072 sc1 lds\n"
"s_add_u32 s63, s63, 4096\n"
"s_add_u32 m0, s64, 4096\n"
"s_nop 0\n"
"buffer_load_dwordx4 v2, s[32:35], s63 offen offset:0 sc1 lds\n"
"buffer_load_dwordx4 v2, s[32:35], s63 offen offset:1024 sc1 lds\n"
"buffer_load_dwordx4 v2, s[32:35], s63 offen offset:2048 sc1 lds\n"
"buffer_load_dwordx4 v2, s[32:35], s63 offen offset:3072 sc1 lds\n"
"s_add_u32 s63, s63, 4096\n"
"s_add_u32 m0, s64, 8192\n"
"s_nop 0\n"
"buffer_load_dwordx4 v2, s[32:35], s63 offen offset:0 sc1 lds\n"
"buffer_load_dwordx4 v2, s[32:35], s63 offen offset:1024 sc1 lds\n"
"buffer_load_dwordx4 v2, s[32:35], s63 offen offset:2048 sc1 lds\n"
"buffer_load_dwordx4 v2, s[32:35], s63 offen offset:3072 sc1 lds\n"
"s_add_u32 s63, s63, 4096\n"
"s_add_u32 m0, s64, 12288\n"
"s_nop 0\n"
"buffer_load_dwordx4 v2, s[32:35], s63 offen offset:0 sc1 lds\n"
"buffer_load_dwordx4 v2, s[32:35], s63 offen offset:1024 sc1 lds\n"
"s_add_u32 s63, s63, 1175552\n"
"s_mov_b32 s48, 20000\n"
"s_branch .L_rfdy\n"
".L_rldy:\n"
"s_sleep 8\n"
".L_rfdy:\n"
"buffer_load_dwordx2 v[6:7], v0, s[32:35], s66 offen sc1\n"
"s_sub_u32 s48, s48, 1\n"
"s_cbranch_scc1 .L_end\n"
"s_waitcnt vmcnt(0)\n"
"v_cmp_ne_u64 vcc, v[8:9], v[6:7]\n"
"s_cbranch_vccnz .L_rldy\n"
"s_add_u32 s66, s66, 1187840\n"
"s_cmp_lg_u32 s29, 0\n"
"s_cbranch_scc1 .L_dh\n"
"s_add_u32 s71, s70, 4681728\n"
"s_sub_u32 s68, s65, 2375680\n"
"s_mov_b32 s62, 31\n"
"s_add_u32 m0, s74, 0\n"
"s_nop 0\n"
"buffer_load_dwordx4 v2, s[32:35], s71 offen offset:0 sc1 lds\n"
"s_add_u32 s71, s71, 1187840\n"
"s_add_u32 m0, s74, 8192\n"
"s_nop 0\n"
"buffer_load_dwordx4 v2, s[32:35], s71 offen offset:0 sc1 lds\n"
"s_add_u32 s71, s71, 1187840\n"
"s_add_u32 m0, s64, 57344\n"
"s_nop 0\n"
"buffer_load_dwordx4 v2, s[32:35], s63 offen offset:0 sc1 lds\n"
"buffer_load_dwordx4 v2, s[32:35], s63 offen offset:1024 sc1 lds\n"
"buffer_load_dwordx4 v2, s[32:35], s63 offen offset:2048 sc1 lds\n"
"buffer_load_dwordx4 v2, s[32:35], s63 offen offset:3072 sc1 lds\n"
"s_add_u32 s63, s63, 4096\n"
"s_add_u32 m0, s64, 61440\n"
"s_nop 0\n"
"buffer_load_dwordx4 v2, s[32:35], s63 offen offset:0 sc1 lds\n"
"s_waitcnt vmcnt(0)\n"
"s_barrier\n"
"ds_read_b128 v[12:15], v5 offset:0\n"
"s_waitcnt lgkmcnt(0)\n"
"v_lshlrev_b32 v16, 16, v12\n"
"v_and_b32 v17, 0xffff0000, v12\n"
"v_lshlrev_b32 v18, 16, v13\n"
"v_and_b32 v19, 0xffff0000, v13\n"
"v_lshlrev_b32 v20, 16, v14\n"
"v_and_b32 v21, 0xffff0000, v14\n"
"v_lshlrev_b32 v22, 16, v15\n"
"v_and_b32 v23, 0xffff0000, v15\n"
"ds_read_b128 v[32:35], v2 offset:0\n"
"ds_read_b128 v[36:39], v2 offset:14336\n"
"ds_read_b128 v[40:43], v2 offset:28672\n"
"ds_read_b128 v[44:47], v2 offset:43008\n"
"ds_read_b128 v[48:51], v2 offset:1024\n"
"ds_read_b128 v[52:55], v2 offset:15360\n"
".p2align 6\n"
".L_dll:\n"
"s_waitcnt lgkmcnt(4)\n"
"v_mfma_f32_16x16x32_bf16 v[16:19], a[0:3], v[32:35], v[16:19]\n"
"ds_read_b128 v[56:59], v2 offset:29696\n"
"v_mfma_f32_16x16x32_bf16 v[16:19], a[112:115], v[36:39], v[16:19]\n"
"buffer_load_dwordx4 v2, s[32:35], s63 offen offset:1024 sc1 lds\n"
"v_mfma_f32_16x16x32_bf16 v[20:23], a[116:119], v[36:39], v[20:23]\n"
"ds_read_b128 v[60:63], v2 offset:44032\n"
"v_mfma_f32_16x16x32_bf16 v[20:23], a[4:7], v[32:35], v[20:23]\n"
"s_waitcnt lgkmcnt(4)\n"
"v_mfma_f32_16x16x32_bf16 v[20:23], a[228:231], v[40:43], v[20:23]\n"
"ds_read_b128 v[32:35], v2 offset:2048\n"
"s_add_u32 s68, s68, 1187840\n"
"v_mfma_f32_16x16x32_bf16 v[20:23], v[148:151], v[44:47], v[20:23]\n"
"buffer_store_dwordx4 v[28:31], v2, s[32:35], s68 offen offset:1024 sc1\n"
"buffer_store_dwordx4 v[24:27], v2, s[32:35], s68 offen sc1\n"
"v_mfma_f32_16x16x32_bf16 v[16:19], v[144:147], v[44:47], v[16:19]\n"
"ds_read_b128 v[36:39], v2 offset:16384\n"
"s_mov_b32 s67, s68\n"
"v_mfma_f32_16x16x32_bf16 v[16:19], a[224:227], v[40:43], v[16:19]\n"
"buffer_load_dwordx4 v2, s[32:35], s63 offen offset:2048 sc1 lds\n"
"s_waitcnt lgkmcnt(4)\n"
"v_mfma_f32_16x16x32_bf16 v[16:19], a[8:11], v[48:51], v[16:19]\n"
"ds_read_b128 v[40:43], v2 offset:30720\n"
"v_mfma_f32_16x16x32_bf16 v[16:19], a[120:123], v[52:55], v[16:19]\n"
"v_mfma_f32_16x16x32_bf16 v[20:23], a[124:127], v[52:55], v[20:23]\n"
"ds_read_b128 v[44:47], v2 offset:45056\n"
"buffer_load_dwordx2 v[6:7], v0, s[32:35], s66 offen sc1\n"
"v_mfma_f32_16x16x32_bf16 v[20:23], a[12:15], v[48:51], v[20:23]\n"
"s_waitcnt lgkmcnt(4)\n"
"v_mfma_f32_16x16x32_bf16 v[20:23], a[236:239], v[56:59], v[20:23]\n"
"ds_read_b128 v[48:51], v2 offset:3072\n"
"v_mfma_f32_16x16x32_bf16 v[20:23], v[156:159], v[60:63], v[20:23]\n"
"buffer_load_dwordx4 v2, s[32:35], s63 offen offset:3072 sc1 lds\n"
"v_mfma_f32_16x16x32_bf16 v[16:19], v[152:155], v[60:63], v[16:19]\n"
"ds_read_b128 v[52:55], v2 offset:17408\n"
"v_mfma_f32_16x16x32_bf16 v[16:19], a[232:235], v[56:59], v[16:19]\n"
"s_waitcnt lgkmcnt(4)\n"
"v_mfma_f32_16x16x32_bf16 v[16:19], a[16:19], v[32:35], v[16:19]\n"
"ds_read_b128 v[56:59], v2 offset:31744\n"
"v_mfma_f32_16x16x32_bf16 v[16:19], a[128:131], v[36:39], v[16:19]\n"
"v_mfma_f32_16x16x32_bf16 v[20:23], a[132:135], v[36:39], v[20:23]\n"
"ds_read_b128 v[60:63], v2 offset:46080\n"
"v_mfma_f32_16x16x32_bf16 v[20:23], a[20:23], v[32:35], v[20:23]\n"
"s_add_u32 s63, s63, 4096\n"
"s_waitcnt lgkmcnt(4)\n"
"v_mfma_f32_16x16x32_bf16 v[20:23], a[244:247], v[40:43], v[20:23]\n"
"ds_read_b128 v[32:35], v2 offset:4096\n"
"s_add_u32 m0, s64, 65536\n"
"v_mfma_f32_16x16x32_bf16 v[20:23], v[164:167], v[44:47], v[20:23]\n"
"buffer_load_dwordx4 v2, s[32:35], s63 offen offset:0 sc1 lds\n"
"v_mfma_f32_16x16x32_bf16 v[16:19], v[160:163], v[44:47], v[16:19]\n"
"ds_read_b128 v[36:39], v2 offset:18432\n"
"v_mfma_f32_16x16x32_bf16 v[16:19], a[240:243], v[40:43], v[16:19]\n"
"s_waitcnt lgkmcnt(4)\n"
"v_mfma_f32_16x16x32_bf16 v[16:19], a[24:27], v[48:51], v[16:19]\n"
"ds_read_b128 v[40:43], v2 offset:32768\n"
"v_mfma_f32_16x16x32_bf16 v[16:19], a[136:139], v[52:55], v[16:19]\n"
"buffer_load_dwordx4 v2, s[32:35], s63 offen offset:1024 sc1 lds\n"
"v_mfma_f32_16x16x32_bf16 v[20:23], a[140:143], v[52:55], v[20:23]\n"
"ds_read_b128 v[44:47], v2 offset:47104\n"
"v_mfma_f32_16x16x32_bf16 v[20:23], a[28:31], v[48:51], v[20:23]\n"
"s_waitcnt lgkmcnt(4)\n"
"v_mfma_f32_16x16x32_bf16 v[20:23], a[252:255], v[56:59], v[20:23]\n"
"ds_read_b128 v[48:51], v2 offset:5120\n"
"v_mfma_f32_16x16x32_bf16 v[20:23], v[172:175], v[60:63], v[20:23]\n"
"v_mfma_f32_16x16x32_bf16 v[16:19], v[168:171], v[60:63], v[16:19]\n"
"ds_read_b128 v[52:55], v2 offset:19456\n"
"v_mfma_f32_16x16x32_bf16 v[16:19], a[248:251], v[56:59], v[16:19]\n"
"buffer_load_dwordx4 v2, s[32:35], s63 offen offset:2048 sc1 lds\n"
"s_waitcnt lgkmcnt(4)\n"
"v_mfma_f32_16x16x32_bf16 v[16:19], a[32:35], v[32:35], v[16:19]\n"
"ds_read_b128 v[56:59], v2 offset:33792\n"
"v_mfma_f32_16x16x32_bf16 v[16:19], a[144:147], v[36:39], v[16:19]\n"
"v_mfma_f32_16x16x32_bf16 v[20:23], a[148:151], v[36:39], v[20:23]\n"
"ds_read_b128 v[60:63], v2 offset:48128\n"
"v_mfma_f32_16x16x32_bf16 v[20:23], a[36:39], v[32:35], v[20:23]\n"
"s_waitcnt lgkmcnt(4)\n"
"v_mfma_f32_16x16x32_bf16 v[20:23], v[68:71], v[40:43], v[20:23]\n"
"ds_read_b128 v[32:35], v2 offset:6144\n"
"v_mfma_f32_16x16x32_bf16 v[20:23], v[180:183], v[44:47], v[20:23]\n"
"buffer_load_dwordx4 v2, s[32:35], s63 offen offset:3072 sc1 lds\n"
"v_mfma_f32_16x16x32_bf16 v[16:19], v[176:179], v[44:47], v[16:19]\n"
"ds_read_b128 v[36:39], v2 offset:20480\n"
"v_mfma_f32_16x16x32_bf16 v[16:19], v[64:67], v[40:43], v[16:19]\n"
"s_waitcnt lgkmcnt(4)\n"
"v_mfma_f32_16x16x32_bf16 v[16:19], a[40:43], v[48:51], v[16:19]\n"
"ds_read_b128 v[40:43], v2 offset:34816\n"
"v_mfma_f32_16x16x32_bf16 v[16:19], a[152:155], v[52:55], v[16:19]\n"
"v_mfma_f32_16x16x32_bf16 v[20:23], a[156:159], v[52:55], v[20:23]\n"
"ds_read_b128 v[44:47], v2 offset:49152\n"
"v_mfma_f32_16x16x32_bf16 v[20:23], a[44:47], v[48:51], v[20:23]\n"
"s_waitcnt vmcnt(17)\n"
"s_waitcnt lgkmcnt(4)\n"
"v_mfma_f32_16x16x32_bf16 v[20:23], v[76:79], v[56:59], v[20:23]\n"
"s_barrier\n"
"ds_read_b128 v[48:51], v2 offset:7168\n"
"v_mfma_f32_16x16x32_bf16 v[20:23], v[188:191], v[60:63], v[20:23]\n"
"v_mfma_f32_16x16x32_bf16 v[16:19], v[184:187], v[60:63], v[16:19]\n"
"ds_read_b128 v[52:55], v2 offset:21504\n"
"s_add_u32 s63, s63, 4096\n"
"v_mfma_f32_16x16x32_bf16 v[16:19], v[72:75], v[56:59], v[16:19]\n"
"s_add_u32 m0, s64, 69632\n"
"s_waitcnt lgkmcnt(4)\n"
"v_mfma_f32_16x16x32_bf16 v[16:19], a[48:51], v[32:35], v[16:19]\n"
"ds_read_b128 v[56:59], v2 offset:35840\n"
"buffer_load_dwordx4 v2, s[32:35], s63 offen offset:0 sc1 lds\n"
"v_mfma_f32_16x16x32_bf16 v[16:19], a[160:163], v[36:39], v[16:19]\n"
"v_mfma_f32_16x16x32_bf16 v[20:23], a[164:167], v[36:39], v[20:23]\n"
"ds_read_b128 v[60:63], v2 offset:50176\n"
"v_mfma_f32_16x16x32_bf16 v[20:23], a[52:55], v[32:35], v[20:23]\n"
"buffer_load_dwordx4 v2, s[32:35], s63 offen offset:1024 sc1 lds\n"
"s_waitcnt lgkmcnt(4)\n"
"v_mfma_f32_16x16x32_bf16 v[20:23], v[84:87], v[40:43], v[20:23]\n"
"ds_read_b128 v[32:35], v2 offset:8192\n"
"s_add_u32 s63, s63, 1175552\n"
"v_mfma_f32_16x16x32_bf16 v[20:23], v[196:199], v[44:47], v[20:23]\n"
"v_mfma_f32_16x16x32_bf16 v[16:19], v[192:195], v[44:47], v[16:19]\n"
"ds_read_b128 v[36:39], v2 offset:22528\n"
"s_waitcnt vmcnt(7)\n"
"v_mfma_f32_16x16x32_bf16 v[16:19], v[80:83], v[40:43], v[16:19]\n"
"v_cmp_ne_u64 vcc, v[8:9], v[6:7]\n"
"s_waitcnt lgkmcnt(4)\n"
"v_mfma_f32_16x16x32_bf16 v[16:19], a[56:59], v[48:51], v[16:19]\n"
"ds_read_b128 v[40:43], v2 offset:36864\n"
"s_cbranch_vccnz .L_rtl0\n"
".L_bkl0:\n"
"v_mfma_f32_16x16x32_bf16 v[16:19], a[168:171], v[52:55], v[16:19]\n"
"s_add_u32 s66, s66, 1187840\n"
"v_mfma_f32_16x16x32_bf16 v[20:23], a[172:175], v[52:55], v[20:23]\n"
"ds_read_b128 v[44:47], v2 offset:51200\n"
"s_add_u32 m0, s74, 0\n"
"v_mfma_f32_16x16x32_bf16 v[20:23], a[60:63], v[48:51], v[20:23]\n"
"buffer_load_dwordx4 v2, s[32:35], s71 offen offset:0 sc1 lds\n"
"s_waitcnt lgkmcnt(4)\n"
"v_mfma_f32_16x16x32_bf16 v[20:23], v[92:95], v[56:59], v[20:23]\n"
"ds_read_b128 v[48:51], v2 offset:9216\n"
"s_add_u32 s71, s71, 1187840\n"
"v_mfma_f32_16x16x32_bf16 v[20:23], v[204:207], v[60:63], v[20:23]\n"
"v_mfma_f32_16x16x32_bf16 v[16:19], v[200:203], v[60:63], v[16:19]\n"
"ds_read_b128 v[52:55], v2 offset:23552\n"
"v_mfma_f32_16x16x32_bf16 v[16:19], v[88:91], v[56:59], v[16:19]\n"
"s_waitcnt lgkmcnt(4)\n"
"v_mfma_f32_16x16x32_bf16 v[16:19], a[64:67], v[32:35], v[16:19]\n"
"ds_read_b128 v[56:59], v2 offset:37888\n"
"v_mfma_f32_16x16x32_bf16 v[16:19], a[176:179], v[36:39], v[16:19]\n"
"v_mfma_f32_16x16x32_bf16 v[20:23], a[180:183], v[36:39], v[20:23]\n"
"ds_read_b128 v[60:63], v2 offset:52224\n"
"s_add_u32 m0, s64, 0\n"
"v_mfma_f32_16x16x32_bf16 v[20:23], a[68:71], v[32:35], v[20:23]\n"
"buffer_load_dwordx4 v2, s[32:35], s63 offen offset:0 sc1 lds\n"
"s_waitcnt lgkmcnt(4)\n"
"v_mfma_f32_16x16x32_bf16 v[20:23], v[100:103], v[40:43], v[20:23]\n"
"ds_read_b128 v[32:35], v2 offset:10240\n"
"v_mfma_f32_16x16x32_bf16 v[20:23], v[212:215], v[44:47], v[20:23]\n"
"v_mfma_f32_16x16x32_bf16 v[16:19], v[208:211], v[44:47], v[16:19]\n"
"ds_read_b128 v[36:39], v2 offset:24576\n"
"v_mfma_f32_16x16x32_bf16 v[16:19], v[96:99], v[40:43], v[16:19]\n"
"s_waitcnt lgkmcnt(4)\n"
"v_mfma_f32_16x16x32_bf16 v[16:19], a[72:75], v[48:51], v[16:19]\n"
"ds_read_b128 v[40:43], v2 offset:38912\n"
"buffer_load_dwordx4 v2, s[32:35], s63 offen offset:1024 sc1 lds\n"
"v_mfma_f32_16x16x32_bf16 v[16:19], a[184:187], v[52:55], v[16:19]\n"
"ds_read_b128 v[12:15], v5 offset:8192\n"
"v_mfma_f32_16x16x32_bf16 v[20:23], a[188:191], v[52:55], v[20:23]\n"
"ds_read_b128 v[44:47], v2 offset:53248\n"
"v_mfma_f32_16x16x32_bf16 v[20:23], a[76:79], v[48:51], v[20:23]\n"
"s_waitcnt lgkmcnt(5)\n"
"v_mfma_f32_16x16x32_bf16 v[20:23], v[108:111], v[56:59], v[20:23]\n"
"ds_read_b128 v[48:51], v2 offset:11264\n"
"v_mfma_f32_16x16x32_bf16 v[20:23], v[220:223], v[60:63], v[20:23]\n"
"v_mfma_f32_16x16x32_bf16 v[16:19], v[216:219], v[60:63], v[16:19]\n"
"ds_read_b128 v[52:55], v2 offset:25600\n"
"buffer_load_dwordx4 v2, s[32:35], s63 offen offset:2048 sc1 lds\n"
"v_mfma_f32_16x16x32_bf16 v[16:19], v[104:107], v[56:59], v[16:19]\n"
"s_waitcnt lgkmcnt(3)\n"
"v_mfma_f32_16x16x32_bf16 v[16:19], a[80:83], v[32:35], v[16:19]\n"
"ds_read_b128 v[56:59], v2 offset:39936\n"
"v_lshlrev_b32 v24, 16, v12\n"
"v_mfma_f32_16x16x32_bf16 v[16:19], a[192:195], v[36:39], v[16:19]\n"
"v_and_b32 v25, 0xffff0000, v12\n"
"v_lshlrev_b32 v26, 16, v13\n"
"v_mfma_f32_16x16x32_bf16 v[20:23], a[196:199], v[36:39], v[20:23]\n"
"ds_read_b128 v[60:63], v2 offset:54272\n"
"v_and_b32 v27, 0xffff0000, v13\n"
"v_mfma_f32_16x16x32_bf16 v[20:23], a[84:87], v[32:35], v[20:23]\n"
"v_lshlrev_b32 v28, 16, v14\n"
"s_waitcnt lgkmcnt(4)\n"
"v_mfma_f32_16x16x32_bf16 v[20:23], v[116:119], v[40:43], v[20:23]\n"
"ds_read_b128 v[32:35], v2 offset:12288\n"
"buffer_load_dwordx4 v2, s[32:35], s63 offen offset:3072 sc1 lds\n"
"v_mfma_f32_16x16x32_bf16 v[20:23], v[228:231], v[44:47], v[20:23]\n"
"v_and_b32 v29, 0xffff0000, v14\n"
"v_lshlrev_b32 v30, 16, v15\n"
"v_mfma_f32_16x16x32_bf16 v[16:19], v[224:227], v[44:47], v[16:19]\n"
"ds_read_b128 v[36:39], v2 offset:26624\n"
"v_and_b32 v31, 0xffff0000, v15\n"
"v_mfma_f32_16x16x32_bf16 v[16:19], v[112:115], v[40:43], v[16:19]\n"
"s_waitcnt lgkmcnt(4)\n"
"v_mfma_f32_16x16x32_bf16 v[16:19], a[88:91], v[48:51], v[16:19]\n"
"ds_read_b128 v[40:43], v2 offset:40960\n"
"v_mfma_f32_16x16x32_bf16 v[16:19], a[200:203], v[52:55], v[16:19]\n"
"v_mfma_f32_16x16x32_bf16 v[20:23], a[204:207], v[52:55], v[20:23]\n"
"ds_read_b128 v[44:47], v2 offset:55296\n"
"s_add_u32 s63, s63, 4096\n"
"v_mfma_f32_16x16x32_bf16 v[20:23], a[92:95], v[48:51], v[20:23]\n"
"s_add_u32 m0, s64, 4096\n"
"s_waitcnt lgkmcnt(4)\n"
"v_mfma_f32_16x16x32_bf16 v[20:23], v[124:127], v[56:59], v[20:23]\n"
"ds_read_b128 v[48:51], v2 offset:13312\n"
"buffer_load_dwordx4 v2, s[32:35], s63 offen offset:0 sc1 lds\n"
"v_mfma_f32_16x16x32_bf16 v[20:23], v[236:239], v[60:63], v[20:23]\n"
"v_mfma_f32_16x16x32_bf16 v[16:19], v[232:235], v[60:63], v[16:19]\n"
"ds_read_b128 v[52:55], v2 offset:27648\n"
"v_mfma_f32_16x16x32_bf16 v[16:19], v[120:123], v[56:59], v[16:19]\n"
"s_waitcnt lgkmcnt(4)\n"
"v_mfma_f32_16x16x32_bf16 v[16:19], a[96:99], v[32:35], v[16:19]\n"
"ds_read_b128 v[56:59], v2 offset:41984\n"
"v_mfma_f32_16x16x32_bf16 v[16:19], a[208:211], v[36:39], v[16:19]\n"
"v_mfma_f32_16x16x32_bf16 v[20:23], a[212:215], v[36:39], v[20:23]\n"
"ds_read_b128 v[60:63], v2 offset:56320\n"
"v_mfma_f32_16x16x32_bf16 v[20:23], a[100:103], v[32:35], v[20:23]\n"
"s_waitcnt lgkmcnt(4)\n"
"v_mfma_f32_16x16x32_bf16 v[20:23], v[132:135], v[40:43], v[20:23]\n"
"s_barrier\n"
"ds_read_b128 v[32:35], v4 offset:0\n"
"v_mfma_f32_16x16x32_bf16 v[20:23], v[244:247], v[44:47], v[20:23]\n"
"buffer_store_dwordx2 v[8:9], v2, s[92:95], s67 idxen sc1\n"
"v_mfma_f32_16x16x32_bf16 v[16:19], v[240:243], v[44:47], v[16:19]\n"
"ds_read_b128 v[36:39], v4 offset:14336\n"
"v_mfma_f32_16x16x32_bf16 v[16:19], v[128:131], v[40:43], v[16:19]\n"
"s_waitcnt lgkmcnt(4)\n"
"v_mfma_f32_16x16x32_bf16 v[16:19], a[104:107], v[48:51], v[16:19]\n"
"ds_read_b128 v[40:43], v4 offset:28672\n"
"v_mfma_f32_16x16x32_bf16 v[16:19], a[216:219], v[52:55], v[16:19]\n"
"v_mfma_f32_16x16x32_bf16 v[20:23], a[220:223], v[52:55], v[20:23]\n"
"ds_read_b128 v[44:47], v4 offset:43008\n"
"v_mfma_f32_16x16x32_bf16 v[20:23], a[108:111], v[48:51], v[20:23]\n"
"s_waitcnt lgkmcnt(4)\n"
"v_mfma_f32_16x16x32_bf16 v[20:23], v[140:143], v[56:59], v[20:23]\n"
"ds_read_b128 v[48:51], v4 offset:1024\n"
"v_mfma_f32_16x16x32_bf16 v[20:23], v[252:255], v[60:63], v[20:23]\n"
"v_mfma_f32_16x16x32_bf16 v[16:19], v[248:251], v[60:63], v[16:19]\n"
"ds_read_b128 v[52:55], v4 offset:15360\n"
"v_mfma_f32_16x16x32_bf16 v[16:19], v[136:139], v[56:59], v[16:19]\n"
"s_waitcnt lgkmcnt(4)\n"
"v_mfma_f32_16x16x32_bf16 v[24:27], a[0:3], v[32:35], v[24:27]\n"
"ds_read_b128 v[56:59], v4 offset:29696\n"
"v_mfma_f32_16x16x32_bf16 v[24:27], a[112:115], v[36:39], v[24:27]\n"
"buffer_load_dwordx4 v2, s[32:35], s63 offen offset:1024 sc1 lds\n"
"v_mfma_f32_16x16x32_bf16 v[28:31], a[116:119], v[36:39], v[28:31]\n"
"ds_read_b128 v[60:63], v4 offset:44032\n"
"v_mfma_f32_16x16x32_bf16 v[28:31], a[4:7], v[32:35], v[28:31]\n"
"s_waitcnt lgkmcnt(4)\n"
"v_mfma_f32_16x16x32_bf16 v[28:31], a[228:231], v[40:43], v[28:31]\n"
"ds_read_b128 v[32:35], v4 offset:2048\n"
"s_add_u32 s68, s68, 1187840\n"
"v_mfma_f32_16x16x32_bf16 v[28:31], v[148:151], v[44:47], v[28:31]\n"
"buffer_store_dwordx4 v[20:23], v2, s[32:35], s68 offen offset:1024 sc1\n"
"buffer_store_dwordx4 v[16:19], v2, s[32:35], s68 offen sc1\n"
"v_mfma_f32_16x16x32_bf16 v[24:27], v[144:147], v[44:47], v[24:27]\n"
"ds_read_b128 v[36:39], v4 offset:16384\n"
"s_mov_b32 s67, s68\n"
"v_mfma_f32_16x16x32_bf16 v[24:27], a[224:227], v[40:43], v[24:27]\n"
"buffer_load_dwordx4 v2, s[32:35], s63 offen offset:2048 sc1 lds\n"
"s_waitcnt lgkmcnt(4)\n"
"v_mfma_f32_16x16x32_bf16 v[24:27], a[8:11], v[48:51], v[24:27]\n"
"ds_read_b128 v[40:43], v4 offset:30720\n"
"v_mfma_f32_16x16x32_bf16 v[24:27], a[120:123], v[52:55], v[24:27]\n"
"v_mfma_f32_16x16x32_bf16 v[28:31], a[124:127], v[52:55], v[28:31]\n"
"ds_read_b128 v[44:47], v4 offset:45056\n"
"buffer_load_dwordx2 v[6:7], v0, s[32:35], s66 offen sc1\n"
"v_mfma_f32_16x16x32_bf16 v[28:31], a[12:15], v[48:51], v[28:31]\n"
"s_waitcnt lgkmcnt(4)\n"
"v_mfma_f32_16x16x32_bf16 v[28:31], a[236:239], v[56:59], v[28:31]\n"
"ds_read_b128 v[48:51], v4 offset:3072\n"
"v_mfma_f32_16x16x32_bf16 v[28:31], v[156:159], v[60:63], v[28:31]\n"
"buffer_load_dwordx4 v2, s[32:35], s63 offen offset:3072 sc1 lds\n"
"v_mfma_f32_16x16x32_bf16 v[24:27], v[152:155], v[60:63], v[24:27]\n"
"ds_read_b128 v[52:55], v4 offset:17408\n"
"v_mfma_f32_16x16x32_bf16 v[24:27], a[232:235], v[56:59], v[24:27]\n"
"s_waitcnt lgkmcnt(4)\n"
"v_mfma_f32_16x16x32_bf16 v[24:27], a[16:19], v[32:35], v[24:27]\n"
"ds_read_b128 v[56:59], v4 offset:31744\n"
"v_mfma_f32_16x16x32_bf16 v[24:27], a[128:131], v[36:39], v[24:27]\n"
"v_mfma_f32_16x16x32_bf16 v[28:31], a[132:135], v[36:39], v[28:31]\n"
"ds_read_b128 v[60:63], v4 offset:46080\n"
"v_mfma_f32_16x16x32_bf16 v[28:31], a[20:23], v[32:35], v[28:31]\n"
"s_add_u32 s63, s63, 4096\n"
"s_waitcnt lgkmcnt(4)\n"
"v_mfma_f32_16x16x32_bf16 v[28:31], a[244:247], v[40:43], v[28:31]\n"
"ds_read_b128 v[32:35], v4 offset:4096\n"
"s_add_u32 m0, s64, 8192\n"
"v_mfma_f32_16x16x32_bf16 v[28:31], v[164:167], v[44:47], v[28:31]\n"
"buffer_load_dwordx4 v2, s[32:35], s63 offen offset:0 sc1 lds\n"
"v_mfma_f32_16x16x32_bf16 v[24:27], v[160:163], v[44:47], v[24:27]\n"
"ds_read_b128 v[36:39], v4 offset:18432\n"
"v_mfma_f32_16x16x32_bf16 v[24:27], a[240:243], v[40:43], v[24:27]\n"
"s_waitcnt lgkmcnt(4)\n"
"v_mfma_f32_16x16x32_bf16 v[24:27], a[24:27], v[48:51], v[24:27]\n"
"ds_read_b128 v[40:43], v4 offset:32768\n"
"v_mfma_f32_16x16x32_bf16 v[24:27], a[136:139], v[52:55], v[24:27]\n"
"buffer_load_dwordx4 v2, s[32:35], s63 offen offset:1024 sc1 lds\n"
"v_mfma_f32_16x16x32_bf16 v[28:31], a[140:143], v[52:55], v[28:31]\n"
"ds_read_b128 v[44:47], v4 offset:47104\n"
"v_mfma_f32_16x16x32_bf16 v[28:31], a[28:31], v[48:51], v[28:31]\n"
"s_waitcnt lgkmcnt(4)\n"
"v_mfma_f32_16x16x32_bf16 v[28:31], a[252:255], v[56:59], v[28:31]\n"
"ds_read_b128 v[48:51], v4 offset:5120\n"
"v_mfma_f32_16x16x32_bf16 v[28:31], v[172:175], v[60:63], v[28:31]\n"
"v_mfma_f32_16x16x32_bf16 v[24:27], v[168:171], v[60:63], v[24:27]\n"
"ds_read_b128 v[52:55], v4 offset:19456\n"
"v_mfma_f32_16x16x32_bf16 v[24:27], a[248:251], v[56:59], v[24:27]\n"
"buffer_load_dwordx4 v2, s[32:35], s63 offen offset:2048 sc1 lds\n"
"s_waitcnt lgkmcnt(4)\n"
"v_mfma_f32_16x16x32_bf16 v[24:27], a[32:35], v[32:35], v[24:27]\n"
"ds_read_b128 v[56:59], v4 offset:33792\n"
"v_mfma_f32_16x16x32_bf16 v[24:27], a[144:147], v[36:39], v[24:27]\n"
"v_mfma_f32_16x16x32_bf16 v[28:31], a[148:151], v[36:39], v[28:31]\n"
"ds_read_b128 v[60:63], v4 offset:48128\n"
"v_mfma_f32_16x16x32_bf16 v[28:31], a[36:39], v[32:35], v[28:31]\n"
"s_waitcnt lgkmcnt(4)\n"
"v_mfma_f32_16x16x32_bf16 v[28:31], v[68:71], v[40:43], v[28:31]\n"
"ds_read_b128 v[32:35], v4 offset:6144\n"
"v_mfma_f32_16x16x32_bf16 v[28:31], v[180:183], v[44:47], v[28:31]\n"
"buffer_load_dwordx4 v2, s[32:35], s63 offen offset:3072 sc1 lds\n"
"v_mfma_f32_16x16x32_bf16 v[24:27], v[176:179], v[44:47], v[24:27]\n"
"ds_read_b128 v[36:39], v4 offset:20480\n"
"v_mfma_f32_16x16x32_bf16 v[24:27], v[64:67], v[40:43], v[24:27]\n"
"s_waitcnt lgkmcnt(4)\n"
"v_mfma_f32_16x16x32_bf16 v[24:27], a[40:43], v[48:51], v[24:27]\n"
"ds_read_b128 v[40:43], v4 offset:34816\n"
"v_mfma_f32_16x16x32_bf16 v[24:27], a[152:155], v[52:55], v[24:27]\n"
"v_mfma_f32_16x16x32_bf16 v[28:31], a[156:159], v[52:55], v[28:31]\n"
"ds_read_b128 v[44:47], v4 offset:49152\n"
"v_mfma_f32_16x16x32_bf16 v[28:31], a[44:47], v[48:51], v[28:31]\n"
"s_waitcnt vmcnt(17)\n"
"s_waitcnt lgkmcnt(4)\n"
"v_mfma_f32_16x16x32_bf16 v[28:31], v[76:79], v[56:59], v[28:31]\n"
"s_barrier\n"
"ds_read_b128 v[48:51], v4 offset:7168\n"
"v_mfma_f32_16x16x32_bf16 v[28:31], v[188:191], v[60:63], v[28:31]\n"
"v_mfma_f32_16x16x32_bf16 v[24:27], v[184:187], v[60:63], v[24:27]\n"
"ds_read_b128 v[52:55], v4 offset:21504\n"
"s_add_u32 s63, s63, 4096\n"
"v_mfma_f32_16x16x32_bf16 v[24:27], v[72:75], v[56:59], v[24:27]\n"
"s_add_u32 m0, s64, 12288\n"
"s_waitcnt lgkmcnt(4)\n"
"v_mfma_f32_16x16x32_bf16 v[24:27], a[48:51], v[32:35], v[24:27]\n"
"ds_read_b128 v[56:59], v4 offset:35840\n"
"buffer_load_dwordx4 v2, s[32:35], s63 offen offset:0 sc1 lds\n"
"v_mfma_f32_16x16x32_bf16 v[24:27], a[160:163], v[36:39], v[24:27]\n"
"v_mfma_f32_16x16x32_bf16 v[28:31], a[164:167], v[36:39], v[28:31]\n"
"ds_read_b128 v[60:63], v4 offset:50176\n"
"v_mfma_f32_16x16x32_bf16 v[28:31], a[52:55], v[32:35], v[28:31]\n"
"buffer_load_dwordx4 v2, s[32:35], s63 offen offset:1024 sc1 lds\n"
"s_waitcnt lgkmcnt(4)\n"
"v_mfma_f32_16x16x32_bf16 v[28:31], v[84:87], v[40:43], v[28:31]\n"
"ds_read_b128 v[32:35], v4 offset:8192\n"
"s_add_u32 s63, s63, 1175552\n"
"v_mfma_f32_16x16x32_bf16 v[28:31], v[196:199], v[44:47], v[28:31]\n"
"v_mfma_f32_16x16x32_bf16 v[24:27], v[192:195], v[44:47], v[24:27]\n"
"ds_read_b128 v[36:39], v4 offset:22528\n"
"s_waitcnt vmcnt(7)\n"
"v_mfma_f32_16x16x32_bf16 v[24:27], v[80:83], v[40:43], v[24:27]\n"
"v_cmp_ne_u64 vcc, v[8:9], v[6:7]\n"
"s_waitcnt lgkmcnt(4)\n"
"v_mfma_f32_16x16x32_bf16 v[24:27], a[56:59], v[48:51], v[24:27]\n"
"ds_read_b128 v[40:43], v4 offset:36864\n"
"s_cbranch_vccnz .L_rtl1\n"
".L_bkl1:\n"
"v_mfma_f32_16x16x32_bf16 v[24:27], a[168:171], v[52:55], v[24:27]\n"
"s_add_u32 s66, s66, 1187840\n"
"v_mfma_f32_16x16x32_bf16 v[28:31], a[172:175], v[52:55], v[28:31]\n"
"ds_read_b128 v[44:47], v4 offset:51200\n"
"s_add_u32 m0, s74, 8192\n"
"v_mfma_f32_16x16x32_bf16 v[28:31], a[60:63], v[48:51], v[28:31]\n"
"buffer_load_dwordx4 v2, s[32:35], s71 offen offset:0 sc1 lds\n"
"s_waitcnt lgkmcnt(4)\n"
"v_mfma_f32_16x16x32_bf16 v[28:31], v[92:95], v[56:59], v[28:31]\n"
"ds_read_b128 v[48:51], v4 offset:9216\n"
"s_add_u32 s71, s71, 1187840\n"
"v_mfma_f32_16x16x32_bf16 v[28:31], v[204:207], v[60:63], v[28:31]\n"
"v_mfma_f32_16x16x32_bf16 v[24:27], v[200:203], v[60:63], v[24:27]\n"
"ds_read_b128 v[52:55], v4 offset:23552\n"
"v_mfma_f32_16x16x32_bf16 v[24:27], v[88:91], v[56:59], v[24:27]\n"
"s_waitcnt lgkmcnt(4)\n"
"v_mfma_f32_16x16x32_bf16 v[24:27], a[64:67], v[32:35], v[24:27]\n"
"ds_read_b128 v[56:59], v4 offset:37888\n"
"v_mfma_f32_16x16x32_bf16 v[24:27], a[176:179], v[36:39], v[24:27]\n"
"v_mfma_f32_16x16x32_bf16 v[28:31], a[180:183], v[36:39], v[28:31]\n"
"ds_read_b128 v[60:63], v4 offset:52224\n"
"s_add_u32 m0, s64, 57344\n"
"v_mfma_f32_16x16x32_bf16 v[28:31], a[68:71], v[32:35], v[28:31]\n"
"buffer_load_dwordx4 v2, s[32:35], s63 offen offset:0 sc1 lds\n"
"s_waitcnt lgkmcnt(4)\n"
"v_mfma_f32_16x16x32_bf16 v[28:31], v[100:103], v[40:43], v[28:31]\n"
"ds_read_b128 v[32:35], v4 offset:10240\n"
"v_mfma_f32_16x16x32_bf16 v[28:31], v[212:215], v[44:47], v[28:31]\n"
"v_mfma_f32_16x16x32_bf16 v[24:27], v[208:211], v[44:47], v[24:27]\n"
"ds_read_b128 v[36:39], v4 offset:24576\n"
"v_mfma_f32_16x16x32_bf16 v[24:27], v[96:99], v[40:43], v[24:27]\n"
"s_waitcnt lgkmcnt(4)\n"
"v_mfma_f32_16x16x32_bf16 v[24:27], a[72:75], v[48:51], v[24:27]\n"
"ds_read_b128 v[40:43], v4 offset:38912\n"
"buffer_load_dwordx4 v2, s[32:35], s63 offen offset:1024 sc1 lds\n"
"v_mfma_f32_16x16x32_bf16 v[24:27], a[184:187], v[52:55], v[24:27]\n"
"ds_read_b128 v[12:15], v5 offset:0\n"
"v_mfma_f32_16x16x32_bf16 v[28:31], a[188:191], v[52:55], v[28:31]\n"
"ds_read_b128 v[44:47], v4 offset:53248\n"
"v_mfma_f32_16x16x32_bf16 v[28:31], a[76:79], v[48:51], v[28:31]\n"
"s_waitcnt lgkmcnt(5)\n"
"v_mfma_f32_16x16x32_bf16 v[28:31], v[108:111], v[56:59], v[28:31]\n"
"ds_read_b128 v[48:51], v4 offset:11264\n"
"v_mfma_f32_16x16x32_bf16 v[28:31], v[220:223], v[60:63], v[28:31]\n"
"v_mfma_f32_16x16x32_bf16 v[24:27], v[216:219], v[60:63], v[24:27]\n"
"ds_read_b128 v[52:55], v4 offset:25600\n"
"buffer_load_dwordx4 v2, s[32:35], s63 offen offset:2048 sc1 lds\n"
"v_mfma_f32_16x16x32_bf16 v[24:27], v[104:107], v[56:59], v[24:27]\n"
"s_waitcnt lgkmcnt(3)\n"
"v_mfma_f32_16x16x32_bf16 v[24:27], a[80:83], v[32:35], v[24:27]\n"
"ds_read_b128 v[56:59], v4 offset:39936\n"
"v_lshlrev_b32 v16, 16, v12\n"
"v_mfma_f32_16x16x32_bf16 v[24:27], a[192:195], v[36:39], v[24:27]\n"
"v_and_b32 v17, 0xffff0000, v12\n"
"v_lshlrev_b32 v18, 16, v13\n"
"v_mfma_f32_16x16x32_bf16 v[28:31], a[196:199], v[36:39], v[28:31]\n"
"ds_read_b128 v[60:63], v4 offset:54272\n"
"v_and_b32 v19, 0xffff0000, v13\n"
"v_mfma_f32_16x16x32_bf16 v[28:31], a[84:87], v[32:35], v[28:31]\n"
"v_lshlrev_b32 v20, 16, v14\n"
"s_waitcnt lgkmcnt(4)\n"
"v_mfma_f32_16x16x32_bf16 v[28:31], v[116:119], v[40:43], v[28:31]\n"
"ds_read_b128 v[32:35], v4 offset:12288\n"
"buffer_load_dwordx4 v2, s[32:35], s63 offen offset:3072 sc1 lds\n"
"v_mfma_f32_16x16x32_bf16 v[28:31], v[228:231], v[44:47], v[28:31]\n"
"v_and_b32 v21, 0xffff0000, v14\n"
"v_lshlrev_b32 v22, 16, v15\n"
"v_mfma_f32_16x16x32_bf16 v[24:27], v[224:227], v[44:47], v[24:27]\n"
"ds_read_b128 v[36:39], v4 offset:26624\n"
"v_and_b32 v23, 0xffff0000, v15\n"
"v_mfma_f32_16x16x32_bf16 v[24:27], v[112:115], v[40:43], v[24:27]\n"
"s_waitcnt lgkmcnt(4)\n"
"v_mfma_f32_16x16x32_bf16 v[24:27], a[88:91], v[48:51], v[24:27]\n"
"ds_read_b128 v[40:43], v4 offset:40960\n"
"v_mfma_f32_16x16x32_bf16 v[24:27], a[200:203], v[52:55], v[24:27]\n"
"v_mfma_f32_16x16x32_bf16 v[28:31], a[204:207], v[52:55], v[28:31]\n"
"ds_read_b128 v[44:47], v4 offset:55296\n"
"s_add_u32 s63, s63, 4096\n"
"v_mfma_f32_16x16x32_bf16 v[28:31], a[92:95], v[48:51], v[28:31]\n"
"s_add_u32 m0, s64, 61440\n"
"s_waitcnt lgkmcnt(4)\n"
"v_mfma_f32_16x16x32_bf16 v[28:31], v[124:127], v[56:59], v[28:31]\n"
"ds_read_b128 v[48:51], v4 offset:13312\n"
"buffer_load_dwordx4 v2, s[32:35], s63 offen offset:0 sc1 lds\n"
"v_mfma_f32_16x16x32_bf16 v[28:31], v[236:239], v[60:63], v[28:31]\n"
"v_mfma_f32_16x16x32_bf16 v[24:27], v[232:235], v[60:63], v[24:27]\n"
"ds_read_b128 v[52:55], v4 offset:27648\n"
"v_mfma_f32_16x16x32_bf16 v[24:27], v[120:123], v[56:59], v[24:27]\n"
"s_waitcnt lgkmcnt(4)\n"
"v_mfma_f32_16x16x32_bf16 v[24:27], a[96:99], v[32:35], v[24:27]\n"
"ds_read_b128 v[56:59], v4 offset:41984\n"
"v_mfma_f32_16x16x32_bf16 v[24:27], a[208:211], v[36:39], v[24:27]\n"
"v_mfma_f32_16x16x32_bf16 v[28:31], a[212:215], v[36:39], v[28:31]\n"
"ds_read_b128 v[60:63], v4 offset:56320\n"
"v_mfma_f32_16x16x32_bf16 v[28:31], a[100:103], v[32:35], v[28:31]\n"
"s_waitcnt lgkmcnt(4)\n"
"v_mfma_f32_16x16x32_bf16 v[28:31], v[132:135], v[40:43], v[28:31]\n"
"s_barrier\n"
"ds_read_b128 v[32:35], v2 offset:0\n"
"v_mfma_f32_16x16x32_bf16 v[28:31], v[244:247], v[44:47], v[28:31]\n"
"buffer_store_dwordx2 v[8:9], v2, s[92:95], s67 idxen sc1\n"
"v_mfma_f32_16x16x32_bf16 v[24:27], v[240:243], v[44:47], v[24:27]\n"
"ds_read_b128 v[36:39], v2 offset:14336\n"
"v_mfma_f32_16x16x32_bf16 v[24:27], v[128:131], v[40:43], v[24:27]\n"
"s_waitcnt lgkmcnt(4)\n"
"v_mfma_f32_16x16x32_bf16 v[24:27], a[104:107], v[48:51], v[24:27]\n"
"ds_read_b128 v[40:43], v2 offset:28672\n"
"v_mfma_f32_16x16x32_bf16 v[24:27], a[216:219], v[52:55], v[24:27]\n"
"v_mfma_f32_16x16x32_bf16 v[28:31], a[220:223], v[52:55], v[28:31]\n"
"ds_read_b128 v[44:47], v2 offset:43008\n"
"v_mfma_f32_16x16x32_bf16 v[28:31], a[108:111], v[48:51], v[28:31]\n"
"s_waitcnt lgkmcnt(4)\n"
"v_mfma_f32_16x16x32_bf16 v[28:31], v[140:143], v[56:59], v[28:31]\n"
"ds_read_b128 v[48:51], v2 offset:1024\n"
"v_mfma_f32_16x16x32_bf16 v[28:31], v[252:255], v[60:63], v[28:31]\n"
"v_mfma_f32_16x16x32_bf16 v[24:27], v[248:251], v[60:63], v[24:27]\n"
"ds_read_b128 v[52:55], v2 offset:15360\n"
"s_sub_u32 s62, s62, 1\n"
"v_mfma_f32_16x16x32_bf16 v[24:27], v[136:139], v[56:59], v[24:27]\n"
"s_cbranch_scc0 .L_dll\n"
"s_nop 1\n"
"s_add_u32 s68, s68, 1187840\n"
"buffer_store_dwordx4 v[28:31], v2, s[32:35], s68 offen offset:1024 sc1\n"
"s_nop 2\n"
"buffer_store_dwordx4 v[24:27], v2, s[32:35], s68 offen sc1\n"
"s_mov_b32 s67, s68\n"
"s_waitcnt vmcnt(0)\n"
"s_barrier\n"
"buffer_store_dwordx2 v[8:9], v2, s[92:95], s67 idxen sc1\n"
"s_branch .L_end\n"
".L_dh:\n"
"s_mov_b32 s71, s65\n"
"s_add_u32 s69, s69, 38010880\n"
"s_add_u32 s68, s70, 2236416\n"
"s_add_u32 s70, s70, 42622976\n"
"s_add_u32 s44, s20, 0x0\n"
"s_addc_u32 s45, s21, 0x0\n"
"s_and_b32 s45, s45, 0xffff\n"
"s_mov_b32 s46, 0x400000\n"
"s_mov_b32 s47, 0x20000\n"
"s_lshl_b32 s65, s28, 20\n"
"s_mov_b32 s62, 16\n"
"s_add_u32 m0, s74, 0\n"
"s_nop 0\n"
"buffer_load_dwordx4 v2, s[32:35], s71 offen offset:0 sc1 lds\n"
"buffer_load_dwordx4 v2, s[32:35], s71 offen offset:1024 sc1 lds\n"
"s_add_u32 s71, s71, 1187840\n"
"s_add_u32 m0, s74, 8192\n"
"s_nop 0\n"
"buffer_load_dwordx4 v2, s[32:35], s71 offen offset:0 sc1 lds\n"
"buffer_load_dwordx4 v2, s[32:35], s71 offen offset:1024 sc1 lds\n"
"s_add_u32 s71, s71, 1187840\n"
"s_add_u32 m0, s64, 57344\n"
"s_nop 0\n"
"buffer_load_dwordx4 v2, s[32:35], s63 offen offset:0 sc1 lds\n"
"buffer_load_dwordx4 v2, s[32:35], s63 offen offset:1024 sc1 lds\n"
"buffer_load_dwordx4 v2, s[32:35], s63 offen offset:2048 sc1 lds\n"
"buffer_load_dwordx4 v2, s[32:35], s63 offen offset:3072 sc1 lds\n"
"s_add_u32 s63, s63, 4096\n"
"s_add_u32 m0, s64, 61440\n"
"s_nop 0\n"
"buffer_load_dwordx4 v2, s[32:35], s63 offen offset:0 sc1 lds\n"
"s_waitcnt vmcnt(0)\n"
"s_barrier\n"
"ds_read_b128 v[16:19], v5 offset:0\n"
"ds_read_b128 v[20:23], v5 offset:1024\n"
"ds_read_b128 v[32:35], v2 offset:0\n"
"ds_read_b128 v[36:39], v2 offset:14336\n"
"ds_read_b128 v[40:43], v2 offset:28672\n"
"ds_read_b128 v[44:47], v2 offset:43008\n"
"ds_read_b128 v[48:51], v2 offset:1024\n"
"ds_read_b128 v[52:55], v2 offset:15360\n"
".p2align 6\n"
".L_dal:\n"
"s_waitcnt lgkmcnt(4)\n"
"v_mfma_f32_16x16x32_bf16 v[16:19], a[0:3], v[32:35], v[16:19]\n"
"ds_read_b128 v[56:59], v2 offset:29696\n"
"v_mfma_f32_16x16x32_bf16 v[16:19], a[112:115], v[36:39], v[16:19]\n"
"buffer_load_dwordx4 v2, s[32:35], s63 offen offset:1024 sc1 lds\n"
"v_mfma_f32_16x16x32_bf16 v[20:23], a[116:119], v[36:39], v[20:23]\n"
"ds_read_b128 v[60:63], v2 offset:44032\n"
"v_mfma_f32_16x16x32_bf16 v[20:23], a[4:7], v[32:35], v[20:23]\n"
"s_waitcnt lgkmcnt(4)\n"
"v_mfma_f32_16x16x32_bf16 v[20:23], a[228:231], v[40:43], v[20:23]\n"
"ds_read_b128 v[32:35], v2 offset:2048\n"
"v_cvt_pk_bf16_f32 v12, v24, v25\n"
"v_mfma_f32_16x16x32_bf16 v[20:23], v[148:151], v[44:47], v[20:23]\n"
"v_cvt_pk_bf16_f32 v13, v26, v27\n"
"v_cvt_pk_bf16_f32 v14, v28, v29\n"
"v_mfma_f32_16x16x32_bf16 v[16:19], v[144:147], v[44:47], v[16:19]\n"
"ds_read_b128 v[36:39], v2 offset:16384\n"
"v_cvt_pk_bf16_f32 v15, v30, v31\n"
"v_mfma_f32_16x16x32_bf16 v[16:19], a[224:227], v[40:43], v[16:19]\n"
"buffer_load_dwordx4 v2, s[32:35], s63 offen offset:2048 sc1 lds\n"
"s_waitcnt lgkmcnt(4)\n"
"v_mfma_f32_16x16x32_bf16 v[16:19], a[8:11], v[48:51], v[16:19]\n"
"ds_read_b128 v[40:43], v2 offset:30720\n"
"buffer_store_dwordx4 v[12:15], v2, s[32:35], s68 offen sc1\n"
"v_mfma_f32_16x16x32_bf16 v[16:19], a[120:123], v[52:55], v[16:19]\n"
"v_add_f32 v10, v24, v25\n"
"v_mul_f32 v11, v24, v24\n"
"v_mfma_f32_16x16x32_bf16 v[20:23], a[124:127], v[52:55], v[20:23]\n"
"ds_read_b128 v[44:47], v2 offset:45056\n"
"buffer_load_dwordx2 v[6:7], v0, s[32:35], s66 offen sc1\n"
"v_mfma_f32_16x16x32_bf16 v[20:23], a[12:15], v[48:51], v[20:23]\n"
"v_fmac_f32 v11, v25, v25\n"
"s_waitcnt lgkmcnt(4)\n"
"v_mfma_f32_16x16x32_bf16 v[20:23], a[236:239], v[56:59], v[20:23]\n"
"ds_read_b128 v[48:51], v2 offset:3072\n"
"v_add_f32 v10, v10, v26\n"
"v_mfma_f32_16x16x32_bf16 v[20:23], v[156:159], v[60:63], v[20:23]\n"
"buffer_load_dwordx4 v2, s[32:35], s63 offen offset:3072 sc1 lds\n"
"v_fmac_f32 v11, v26, v26\n"
"v_mfma_f32_16x16x32_bf16 v[16:19], v[152:155], v[60:63], v[16:19]\n"
"ds_read_b128 v[52:55], v2 offset:17408\n"
"v_add_f32 v10, v10, v27\n"
"v_mfma_f32_16x16x32_bf16 v[16:19], a[232:235], v[56:59], v[16:19]\n"
"v_fmac_f32 v11, v27, v27\n"
"s_waitcnt lgkmcnt(4)\n"
"v_mfma_f32_16x16x32_bf16 v[16:19], a[16:19], v[32:35], v[16:19]\n"
"ds_read_b128 v[56:59], v2 offset:31744\n"
"v_add_f32 v10, v10, v28\n"
"v_mfma_f32_16x16x32_bf16 v[16:19], a[128:131], v[36:39], v[16:19]\n"
"v_fmac_f32 v11, v28, v28\n"
"v_add_f32 v10, v10, v29\n"
"v_mfma_f32_16x16x32_bf16 v[20:23], a[132:135], v[36:39], v[20:23]\n"
"ds_read_b128 v[60:63], v2 offset:46080\n"
"v_fmac_f32 v11, v29, v29\n"
"v_mfma_f32_16x16x32_bf16 v[20:23], a[20:23], v[32:35], v[20:23]\n"
"s_add_u32 s63, s63, 4096\n"
"s_waitcnt lgkmcnt(4)\n"
"v_mfma_f32_16x16x32_bf16 v[20:23], a[244:247], v[40:43], v[20:23]\n"
"ds_read_b128 v[32:35], v2 offset:4096\n"
"s_add_u32 m0, s64, 65536\n"
"v_mfma_f32_16x16x32_bf16 v[20:23], v[164:167], v[44:47], v[20:23]\n"
"buffer_load_dwordx4 v2, s[32:35], s63 offen offset:0 sc1 lds\n"
"v_add_f32 v10, v10, v30\n"
"v_mfma_f32_16x16x32_bf16 v[16:19], v[160:163], v[44:47], v[16:19]\n"
"ds_read_b128 v[36:39], v2 offset:18432\n"
"v_fmac_f32 v11, v30, v30\n"
"v_mfma_f32_16x16x32_bf16 v[16:19], a[240:243], v[40:43], v[16:19]\n"
"v_add_f32 v10, v10, v31\n"
"s_waitcnt lgkmcnt(4)\n"
"v_mfma_f32_16x16x32_bf16 v[16:19], a[24:27], v[48:51], v[16:19]\n"
"ds_read_b128 v[40:43], v2 offset:32768\n"
"v_fmac_f32 v11, v31, v31\n"
"v_mfma_f32_16x16x32_bf16 v[16:19], a[136:139], v[52:55], v[16:19]\n"
"buffer_load_dwordx4 v2, s[32:35], s63 offen offset:1024 sc1 lds\n"
"v_mfma_f32_16x16x32_bf16 v[20:23], a[140:143], v[52:55], v[20:23]\n"
"ds_read_b128 v[44:47], v2 offset:47104\n"
"v_mfma_f32_16x16x32_bf16 v[20:23], a[28:31], v[48:51], v[20:23]\n"
"s_waitcnt lgkmcnt(4)\n"
"v_mfma_f32_16x16x32_bf16 v[20:23], a[252:255], v[56:59], v[20:23]\n"
"ds_read_b128 v[48:51], v2 offset:5120\n"
"v_mfma_f32_16x16x32_bf16 v[20:23], v[172:175], v[60:63], v[20:23]\n"
"v_permlane16_swap_b32 v10, v11\n"
"v_mfma_f32_16x16x32_bf16 v[16:19], v[168:171], v[60:63], v[16:19]\n"
"ds_read_b128 v[52:55], v2 offset:19456\n"
"v_add_f32 v10, v10, v11\n"
"v_mfma_f32_16x16x32_bf16 v[16:19], a[248:251], v[56:59], v[16:19]\n"
"buffer_load_dwordx4 v2, s[32:35], s63 offen offset:2048 sc1 lds\n"
"s_waitcnt lgkmcnt(4)\n"
"v_mfma_f32_16x16x32_bf16 v[16:19], a[32:35], v[32:35], v[16:19]\n"
"ds_read_b128 v[56:59], v2 offset:33792\n"
"v_mfma_f32_16x16x32_bf16 v[16:19], a[144:147], v[36:39], v[16:19]\n"
"v_permlane32_swap_b32 v10, v11\n"
"v_mfma_f32_16x16x32_bf16 v[20:23], a[148:151], v[36:39], v[20:23]\n"
"ds_read_b128 v[60:63], v2 offset:48128\n"
"v_add_f32 v10, v10, v11\n"
"v_mfma_f32_16x16x32_bf16 v[20:23], a[36:39], v[32:35], v[20:23]\n"
"buffer_store_dword v10, v3, s[32:35], s68 offen offset:1024 sc1\n"
"s_waitcnt lgkmcnt(4)\n"
"v_mfma_f32_16x16x32_bf16 v[20:23], v[68:71], v[40:43], v[20:23]\n"
"ds_read_b128 v[32:35], v2 offset:6144\n"
"s_mov_b32 s67, s68\n"
"v_mfma_f32_16x16x32_bf16 v[20:23], v[180:183], v[44:47], v[20:23]\n"
"buffer_load_dwordx4 v2, s[32:35], s63 offen offset:3072 sc1 lds\n"
"s_mov_b32 s68, s70\n"
"v_mfma_f32_16x16x32_bf16 v[16:19], v[176:179], v[44:47], v[16:19]\n"
"ds_read_b128 v[36:39], v2 offset:20480\n"
"s_add_u32 s70, s70, 1187840\n"
"v_mfma_f32_16x16x32_bf16 v[16:19], v[64:67], v[40:43], v[16:19]\n"
"s_waitcnt lgkmcnt(4)\n"
"v_mfma_f32_16x16x32_bf16 v[16:19], a[40:43], v[48:51], v[16:19]\n"
"ds_read_b128 v[40:43], v2 offset:34816\n"
"v_mfma_f32_16x16x32_bf16 v[16:19], a[152:155], v[52:55], v[16:19]\n"
"v_mfma_f32_16x16x32_bf16 v[20:23], a[156:159], v[52:55], v[20:23]\n"
"ds_read_b128 v[44:47], v2 offset:49152\n"
"v_mfma_f32_16x16x32_bf16 v[20:23], a[44:47], v[48:51], v[20:23]\n"
"s_waitcnt vmcnt(18)\n"
"s_waitcnt lgkmcnt(4)\n"
"v_mfma_f32_16x16x32_bf16 v[20:23], v[76:79], v[56:59], v[20:23]\n"
"s_barrier\n"
"ds_read_b128 v[48:51], v2 offset:7168\n"
"v_mfma_f32_16x16x32_bf16 v[20:23], v[188:191], v[60:63], v[20:23]\n"
"v_mfma_f32_16x16x32_bf16 v[16:19], v[184:187], v[60:63], v[16:19]\n"
"ds_read_b128 v[52:55], v2 offset:21504\n"
"s_add_u32 s63, s63, 4096\n"
"v_mfma_f32_16x16x32_bf16 v[16:19], v[72:75], v[56:59], v[16:19]\n"
"s_add_u32 m0, s64, 69632\n"
"s_waitcnt lgkmcnt(4)\n"
"v_mfma_f32_16x16x32_bf16 v[16:19], a[48:51], v[32:35], v[16:19]\n"
"ds_read_b128 v[56:59], v2 offset:35840\n"
"buffer_load_dwordx4 v2, s[32:35], s63 offen offset:0 sc1 lds\n"
"v_mfma_f32_16x16x32_bf16 v[16:19], a[160:163], v[36:39], v[16:19]\n"
"v_mfma_f32_16x16x32_bf16 v[20:23], a[164:167], v[36:39], v[20:23]\n"
"ds_read_b128 v[60:63], v2 offset:50176\n"
"v_mfma_f32_16x16x32_bf16 v[20:23], a[52:55], v[32:35], v[20:23]\n"
"buffer_load_dwordx4 v2, s[32:35], s63 offen offset:1024 sc1 lds\n"
"s_waitcnt lgkmcnt(4)\n"
"v_mfma_f32_16x16x32_bf16 v[20:23], v[84:87], v[40:43], v[20:23]\n"
"ds_read_b128 v[32:35], v2 offset:8192\n"
"s_add_u32 s63, s63, 1175552\n"
"v_mfma_f32_16x16x32_bf16 v[20:23], v[196:199], v[44:47], v[20:23]\n"
"v_mfma_f32_16x16x32_bf16 v[16:19], v[192:195], v[44:47], v[16:19]\n"
"ds_read_b128 v[36:39], v2 offset:22528\n"
"s_waitcnt vmcnt(8)\n"
"v_mfma_f32_16x16x32_bf16 v[16:19], v[80:83], v[40:43], v[16:19]\n"
"v_cmp_ne_u64 vcc, v[8:9], v[6:7]\n"
"s_waitcnt lgkmcnt(4)\n"
"v_mfma_f32_16x16x32_bf16 v[16:19], a[56:59], v[48:51], v[16:19]\n"
"ds_read_b128 v[40:43], v2 offset:36864\n"
"s_cbranch_vccnz .L_rta0\n"
".L_bka0:\n"
"v_mfma_f32_16x16x32_bf16 v[16:19], a[168:171], v[52:55], v[16:19]\n"
"s_add_u32 s66, s66, 1187840\n"
"v_mfma_f32_16x16x32_bf16 v[20:23], a[172:175], v[52:55], v[20:23]\n"
"ds_read_b128 v[44:47], v2 offset:51200\n"
"s_add_u32 m0, s74, 0\n"
"v_mfma_f32_16x16x32_bf16 v[20:23], a[60:63], v[48:51], v[20:23]\n"
"buffer_load_dwordx4 v2, s[32:35], s71 offen offset:0 sc1 lds\n"
"s_waitcnt lgkmcnt(4)\n"
"v_mfma_f32_16x16x32_bf16 v[20:23], v[92:95], v[56:59], v[20:23]\n"
"ds_read_b128 v[48:51], v2 offset:9216\n"
"buffer_load_dwordx4 v2, s[32:35], s71 offen offset:1024 sc1 lds\n"
"v_mfma_f32_16x16x32_bf16 v[20:23], v[204:207], v[60:63], v[20:23]\n"
"s_add_u32 s71, s71, 1187840\n"
"v_mfma_f32_16x16x32_bf16 v[16:19], v[200:203], v[60:63], v[16:19]\n"
"ds_read_b128 v[52:55], v2 offset:23552\n"
"v_mfma_f32_16x16x32_bf16 v[16:19], v[88:91], v[56:59], v[16:19]\n"
"s_waitcnt lgkmcnt(4)\n"
"v_mfma_f32_16x16x32_bf16 v[16:19], a[64:67], v[32:35], v[16:19]\n"
"ds_read_b128 v[56:59], v2 offset:37888\n"
"v_mfma_f32_16x16x32_bf16 v[16:19], a[176:179], v[36:39], v[16:19]\n"
"v_mfma_f32_16x16x32_bf16 v[20:23], a[180:183], v[36:39], v[20:23]\n"
"ds_read_b128 v[60:63], v2 offset:52224\n"
"s_add_u32 m0, s64, 0\n"
"v_mfma_f32_16x16x32_bf16 v[20:23], a[68:71], v[32:35], v[20:23]\n"
"buffer_load_dwordx4 v2, s[32:35], s63 offen offset:0 sc1 lds\n"
"s_waitcnt lgkmcnt(4)\n"
"v_mfma_f32_16x16x32_bf16 v[20:23], v[100:103], v[40:43], v[20:23]\n"
"ds_read_b128 v[32:35], v2 offset:10240\n"
"v_mfma_f32_16x16x32_bf16 v[20:23], v[212:215], v[44:47], v[20:23]\n"
"v_mfma_f32_16x16x32_bf16 v[16:19], v[208:211], v[44:47], v[16:19]\n"
"ds_read_b128 v[36:39], v2 offset:24576\n"
"v_mfma_f32_16x16x32_bf16 v[16:19], v[96:99], v[40:43], v[16:19]\n"
"s_waitcnt lgkmcnt(4)\n"
"v_mfma_f32_16x16x32_bf16 v[16:19], a[72:75], v[48:51], v[16:19]\n"
"ds_read_b128 v[40:43], v2 offset:38912\n"
"buffer_load_dwordx4 v2, s[32:35], s63 offen offset:1024 sc1 lds\n"
"v_mfma_f32_16x16x32_bf16 v[16:19], a[184:187], v[52:55], v[16:19]\n"
"ds_read_b128 v[24:27], v5 offset:8192\n"
"v_mfma_f32_16x16x32_bf16 v[20:23], a[188:191], v[52:55], v[20:23]\n"
"ds_read_b128 v[44:47], v2 offset:53248\n"
"ds_read_b128 v[28:31], v5 offset:9216\n"
"v_mfma_f32_16x16x32_bf16 v[20:23], a[76:79], v[48:51], v[20:23]\n"
"s_waitcnt lgkmcnt(6)\n"
"v_mfma_f32_16x16x32_bf16 v[20:23], v[108:111], v[56:59], v[20:23]\n"
"ds_read_b128 v[48:51], v2 offset:11264\n"
"v_mfma_f32_16x16x32_bf16 v[20:23], v[220:223], v[60:63], v[20:23]\n"
"v_mfma_f32_16x16x32_bf16 v[16:19], v[216:219], v[60:63], v[16:19]\n"
"ds_read_b128 v[52:55], v2 offset:25600\n"
"buffer_load_dwordx4 v2, s[32:35], s63 offen offset:2048 sc1 lds\n"
"v_mfma_f32_16x16x32_bf16 v[16:19], v[104:107], v[56:59], v[16:19]\n"
"s_waitcnt lgkmcnt(6)\n"
"v_mfma_f32_16x16x32_bf16 v[16:19], a[80:83], v[32:35], v[16:19]\n"
"ds_read_b128 v[56:59], v2 offset:39936\n"
"v_mfma_f32_16x16x32_bf16 v[16:19], a[192:195], v[36:39], v[16:19]\n"
"v_mfma_f32_16x16x32_bf16 v[20:23], a[196:199], v[36:39], v[20:23]\n"
"ds_read_b128 v[60:63], v2 offset:54272\n"
"v_mfma_f32_16x16x32_bf16 v[20:23], a[84:87], v[32:35], v[20:23]\n"
"s_waitcnt lgkmcnt(5)\n"
"v_mfma_f32_16x16x32_bf16 v[20:23], v[116:119], v[40:43], v[20:23]\n"
"ds_read_b128 v[32:35], v2 offset:12288\n"
"buffer_load_dwordx4 v2, s[32:35], s63 offen offset:3072 sc1 lds\n"
"v_mfma_f32_16x16x32_bf16 v[20:23], v[228:231], v[44:47], v[20:23]\n"
"v_mfma_f32_16x16x32_bf16 v[16:19], v[224:227], v[44:47], v[16:19]\n"
"ds_read_b128 v[36:39], v2 offset:26624\n"
"v_mfma_f32_16x16x32_bf16 v[16:19], v[112:115], v[40:43], v[16:19]\n"
"s_waitcnt lgkmcnt(4)\n"
"v_mfma_f32_16x16x32_bf16 v[16:19], a[88:91], v[48:51], v[16:19]\n"
"ds_read_b128 v[40:43], v2 offset:40960\n"
"v_mfma_f32_16x16x32_bf16 v[16:19], a[200:203], v[52:55], v[16:19]\n"
"v_mfma_f32_16x16x32_bf16 v[20:23], a[204:207], v[52:55], v[20:23]\n"
"ds_read_b128 v[44:47], v2 offset:55296\n"
"s_add_u32 s63, s63, 4096\n"
"v_mfma_f32_16x16x32_bf16 v[20:23], a[92:95], v[48:51], v[20:23]\n"
"s_add_u32 m0, s64, 4096\n"
"s_waitcnt lgkmcnt(4)\n"
"v_mfma_f32_16x16x32_bf16 v[20:23], v[124:127], v[56:59], v[20:23]\n"
"ds_read_b128 v[48:51], v2 offset:13312\n"
"buffer_load_dwordx4 v2, s[32:35], s63 offen offset:0 sc1 lds\n"
"v_mfma_f32_16x16x32_bf16 v[20:23], v[236:239], v[60:63], v[20:23]\n"
"v_mfma_f32_16x16x32_bf16 v[16:19], v[232:235], v[60:63], v[16:19]\n"
"ds_read_b128 v[52:55], v2 offset:27648\n"
"v_mfma_f32_16x16x32_bf16 v[16:19], v[120:123], v[56:59], v[16:19]\n"
"s_waitcnt lgkmcnt(4)\n"
"v_mfma_f32_16x16x32_bf16 v[16:19], a[96:99], v[32:35], v[16:19]\n"
"ds_read_b128 v[56:59], v2 offset:41984\n"
"v_mfma_f32_16x16x32_bf16 v[16:19], a[208:211], v[36:39], v[16:19]\n"
"v_mfma_f32_16x16x32_bf16 v[20:23], a[212:215], v[36:39], v[20:23]\n"
"ds_read_b128 v[60:63], v2 offset:56320\n"
"v_mfma_f32_16x16x32_bf16 v[20:23], a[100:103], v[32:35], v[20:23]\n"
"s_waitcnt vmcnt(10)\n"
"s_waitcnt lgkmcnt(4)\n"
"v_mfma_f32_16x16x32_bf16 v[20:23], v[132:135], v[40:43], v[20:23]\n"
"s_barrier\n"
"ds_read_b128 v[32:35], v4 offset:0\n"
"v_mfma_f32_16x16x32_bf16 v[20:23], v[244:247], v[44:47], v[20:23]\n"
"buffer_store_dwordx2 v[8:9], v2, s[92:95], s67 idxen sc1\n"
"v_mfma_f32_16x16x32_bf16 v[16:19], v[240:243], v[44:47], v[16:19]\n"
"ds_read_b128 v[36:39], v4 offset:14336\n"
"v_mfma_f32_16x16x32_bf16 v[16:19], v[128:131], v[40:43], v[16:19]\n"
"s_waitcnt lgkmcnt(4)\n"
"v_mfma_f32_16x16x32_bf16 v[16:19], a[104:107], v[48:51], v[16:19]\n"
"ds_read_b128 v[40:43], v4 offset:28672\n"
"v_mfma_f32_16x16x32_bf16 v[16:19], a[216:219], v[52:55], v[16:19]\n"
"v_mfma_f32_16x16x32_bf16 v[20:23], a[220:223], v[52:55], v[20:23]\n"
"ds_read_b128 v[44:47], v4 offset:43008\n"
"v_mfma_f32_16x16x32_bf16 v[20:23], a[108:111], v[48:51], v[20:23]\n"
"s_waitcnt lgkmcnt(4)\n"
"v_mfma_f32_16x16x32_bf16 v[20:23], v[140:143], v[56:59], v[20:23]\n"
"ds_read_b128 v[48:51], v4 offset:1024\n"
"v_mfma_f32_16x16x32_bf16 v[20:23], v[252:255], v[60:63], v[20:23]\n"
"v_mfma_f32_16x16x32_bf16 v[16:19], v[248:251], v[60:63], v[16:19]\n"
"ds_read_b128 v[52:55], v4 offset:15360\n"
"s_sub_u32 s62, s62, 1\n"
"v_mfma_f32_16x16x32_bf16 v[16:19], v[136:139], v[56:59], v[16:19]\n"
"s_cbranch_scc1 .L_dab\n"
"s_waitcnt lgkmcnt(4)\n"
"v_mfma_f32_16x16x32_bf16 v[24:27], a[0:3], v[32:35], v[24:27]\n"
"ds_read_b128 v[56:59], v4 offset:29696\n"
"v_mfma_f32_16x16x32_bf16 v[24:27], a[112:115], v[36:39], v[24:27]\n"
"buffer_load_dwordx4 v2, s[32:35], s63 offen offset:1024 sc1 lds\n"
"v_mfma_f32_16x16x32_bf16 v[28:31], a[116:119], v[36:39], v[28:31]\n"
"ds_read_b128 v[60:63], v4 offset:44032\n"
"v_mfma_f32_16x16x32_bf16 v[28:31], a[4:7], v[32:35], v[28:31]\n"
"s_waitcnt lgkmcnt(4)\n"
"v_mfma_f32_16x16x32_bf16 v[28:31], a[228:231], v[40:43], v[28:31]\n"
"ds_read_b128 v[32:35], v4 offset:2048\n"
"v_cvt_pk_bf16_f32 v12, v16, v17\n"
"v_mfma_f32_16x16x32_bf16 v[28:31], v[148:151], v[44:47], v[28:31]\n"
"v_cvt_pk_bf16_f32 v13, v18, v19\n"
"v_cvt_pk_bf16_f32 v14, v20, v21\n"
"v_mfma_f32_16x16x32_bf16 v[24:27], v[144:147], v[44:47], v[24:27]\n"
"ds_read_b128 v[36:39], v4 offset:16384\n"
"v_cvt_pk_bf16_f32 v15, v22, v23\n"
"v_mfma_f32_16x16x32_bf16 v[24:27], a[224:227], v[40:43], v[24:27]\n"
"buffer_load_dwordx4 v2, s[32:35], s63 offen offset:2048 sc1 lds\n"
"s_waitcnt lgkmcnt(4)\n"
"v_mfma_f32_16x16x32_bf16 v[24:27], a[8:11], v[48:51], v[24:27]\n"
"ds_read_b128 v[40:43], v4 offset:30720\n"
"buffer_store_dwordx4 v[12:15], v2, s[32:35], s69 offen sc1\n"
"v_mfma_f32_16x16x32_bf16 v[24:27], a[120:123], v[52:55], v[24:27]\n"
"v_add_f32 v10, v16, v17\n"
"v_mul_f32 v11, v16, v16\n"
"v_mfma_f32_16x16x32_bf16 v[28:31], a[124:127], v[52:55], v[28:31]\n"
"ds_read_b128 v[44:47], v4 offset:45056\n"
"buffer_load_dwordx2 v[6:7], v0, s[32:35], s66 offen sc1\n"
"v_mfma_f32_16x16x32_bf16 v[28:31], a[12:15], v[48:51], v[28:31]\n"
"v_fmac_f32 v11, v17, v17\n"
"s_waitcnt lgkmcnt(4)\n"
"v_mfma_f32_16x16x32_bf16 v[28:31], a[236:239], v[56:59], v[28:31]\n"
"ds_read_b128 v[48:51], v4 offset:3072\n"
"v_add_f32 v10, v10, v18\n"
"v_mfma_f32_16x16x32_bf16 v[28:31], v[156:159], v[60:63], v[28:31]\n"
"buffer_load_dwordx4 v2, s[32:35], s63 offen offset:3072 sc1 lds\n"
"v_fmac_f32 v11, v18, v18\n"
"v_mfma_f32_16x16x32_bf16 v[24:27], v[152:155], v[60:63], v[24:27]\n"
"ds_read_b128 v[52:55], v4 offset:17408\n"
"v_add_f32 v10, v10, v19\n"
"v_mfma_f32_16x16x32_bf16 v[24:27], a[232:235], v[56:59], v[24:27]\n"
"v_fmac_f32 v11, v19, v19\n"
"s_waitcnt lgkmcnt(4)\n"
"v_mfma_f32_16x16x32_bf16 v[24:27], a[16:19], v[32:35], v[24:27]\n"
"ds_read_b128 v[56:59], v4 offset:31744\n"
"v_add_f32 v10, v10, v20\n"
"v_mfma_f32_16x16x32_bf16 v[24:27], a[128:131], v[36:39], v[24:27]\n"
"v_fmac_f32 v11, v20, v20\n"
"v_add_f32 v10, v10, v21\n"
"v_mfma_f32_16x16x32_bf16 v[28:31], a[132:135], v[36:39], v[28:31]\n"
"ds_read_b128 v[60:63], v4 offset:46080\n"
"v_fmac_f32 v11, v21, v21\n"
"v_mfma_f32_16x16x32_bf16 v[28:31], a[20:23], v[32:35], v[28:31]\n"
"s_add_u32 s63, s63, 4096\n"
"s_waitcnt lgkmcnt(4)\n"
"v_mfma_f32_16x16x32_bf16 v[28:31], a[244:247], v[40:43], v[28:31]\n"
"ds_read_b128 v[32:35], v4 offset:4096\n"
"s_add_u32 m0, s64, 8192\n"
"v_mfma_f32_16x16x32_bf16 v[28:31], v[164:167], v[44:47], v[28:31]\n"
"buffer_load_dwordx4 v2, s[32:35], s63 offen offset:0 sc1 lds\n"
"v_add_f32 v10, v10, v22\n"
"v_mfma_f32_16x16x32_bf16 v[24:27], v[160:163], v[44:47], v[24:27]\n"
"ds_read_b128 v[36:39], v4 offset:18432\n"
"v_fmac_f32 v11, v22, v22\n"
"v_mfma_f32_16x16x32_bf16 v[24:27], a[240:243], v[40:43], v[24:27]\n"
"v_add_f32 v10, v10, v23\n"
"s_waitcnt lgkmcnt(4)\n"
"v_mfma_f32_16x16x32_bf16 v[24:27], a[24:27], v[48:51], v[24:27]\n"
"ds_read_b128 v[40:43], v4 offset:32768\n"
"v_fmac_f32 v11, v23, v23\n"
"v_mfma_f32_16x16x32_bf16 v[24:27], a[136:139], v[52:55], v[24:27]\n"
"buffer_load_dwordx4 v2, s[32:35], s63 offen offset:1024 sc1 lds\n"
"v_mfma_f32_16x16x32_bf16 v[28:31], a[140:143], v[52:55], v[28:31]\n"
"ds_read_b128 v[44:47], v4 offset:47104\n"
"v_mfma_f32_16x16x32_bf16 v[28:31], a[28:31], v[48:51], v[28:31]\n"
"s_waitcnt lgkmcnt(4)\n"
"v_mfma_f32_16x16x32_bf16 v[28:31], a[252:255], v[56:59], v[28:31]\n"
"ds_read_b128 v[48:51], v4 offset:5120\n"
"v_mfma_f32_16x16x32_bf16 v[28:31], v[172:175], v[60:63], v[28:31]\n"
"v_permlane16_swap_b32 v10, v11\n"
"v_mfma_f32_16x16x32_bf16 v[24:27], v[168:171], v[60:63], v[24:27]\n"
"ds_read_b128 v[52:55], v4 offset:19456\n"
"v_add_f32 v10, v10, v11\n"
"v_mfma_f32_16x16x32_bf16 v[24:27], a[248:251], v[56:59], v[24:27]\n"
"buffer_load_dwordx4 v2, s[32:35], s63 offen offset:2048 sc1 lds\n"
"s_waitcnt lgkmcnt(4)\n"
"v_mfma_f32_16x16x32_bf16 v[24:27], a[32:35], v[32:35], v[24:27]\n"
"ds_read_b128 v[56:59], v4 offset:33792\n"
"v_mfma_f32_16x16x32_bf16 v[24:27], a[144:147], v[36:39], v[24:27]\n"
"v_permlane32_swap_b32 v10, v11\n"
"v_mfma_f32_16x16x32_bf16 v[28:31], a[148:151], v[36:39], v[28:31]\n"
"ds_read_b128 v[60:63], v4 offset:48128\n"
"v_add_f32 v10, v10, v11\n"
"v_mfma_f32_16x16x32_bf16 v[28:31], a[36:39], v[32:35], v[28:31]\n"
"buffer_store_dword v10, v3, s[32:35], s69 offen offset:1024 sc1\n"
"s_waitcnt lgkmcnt(4)\n"
"v_mfma_f32_16x16x32_bf16 v[28:31], v[68:71], v[40:43], v[28:31]\n"
"ds_read_b128 v[32:35], v4 offset:6144\n"
"s_mov_b32 s67, s69\n"
"v_mfma_f32_16x16x32_bf16 v[28:31], v[180:183], v[44:47], v[28:31]\n"
"buffer_load_dwordx4 v2, s[32:35], s63 offen offset:3072 sc1 lds\n"
"s_mov_b32 s69, s70\n"
"v_mfma_f32_16x16x32_bf16 v[24:27], v[176:179], v[44:47], v[24:27]\n"
"ds_read_b128 v[36:39], v4 offset:20480\n"
"s_add_u32 s70, s70, 1187840\n"
"v_mfma_f32_16x16x32_bf16 v[24:27], v[64:67], v[40:43], v[24:27]\n"
"s_waitcnt lgkmcnt(4)\n"
"v_mfma_f32_16x16x32_bf16 v[24:27], a[40:43], v[48:51], v[24:27]\n"
"ds_read_b128 v[40:43], v4 offset:34816\n"
"v_mfma_f32_16x16x32_bf16 v[24:27], a[152:155], v[52:55], v[24:27]\n"
"v_mfma_f32_16x16x32_bf16 v[28:31], a[156:159], v[52:55], v[28:31]\n"
"ds_read_b128 v[44:47], v4 offset:49152\n"
"v_mfma_f32_16x16x32_bf16 v[28:31], a[44:47], v[48:51], v[28:31]\n"
"s_waitcnt vmcnt(18)\n"
"s_waitcnt lgkmcnt(4)\n"
"v_mfma_f32_16x16x32_bf16 v[28:31], v[76:79], v[56:59], v[28:31]\n"
"s_barrier\n"
"ds_read_b128 v[48:51], v4 offset:7168\n"
"v_mfma_f32_16x16x32_bf16 v[28:31], v[188:191], v[60:63], v[28:31]\n"
"v_mfma_f32_16x16x32_bf16 v[24:27], v[184:187], v[60:63], v[24:27]\n"
"ds_read_b128 v[52:55], v4 offset:21504\n"
"s_add_u32 s63, s63, 4096\n"
"v_mfma_f32_16x16x32_bf16 v[24:27], v[72:75], v[56:59], v[24:27]\n"
"s_add_u32 m0, s64, 12288\n"
"s_waitcnt lgkmcnt(4)\n"
"v_mfma_f32_16x16x32_bf16 v[24:27], a[48:51], v[32:35], v[24:27]\n"
"ds_read_b128 v[56:59], v4 offset:35840\n"
"buffer_load_dwordx4 v2, s[32:35], s63 offen offset:0 sc1 lds\n"
"v_mfma_f32_16x16x32_bf16 v[24:27], a[160:163], v[36:39], v[24:27]\n"
"v_mfma_f32_16x16x32_bf16 v[28:31], a[164:167], v[36:39], v[28:31]\n"
"ds_read_b128 v[60:63], v4 offset:50176\n"
"v_mfma_f32_16x16x32_bf16 v[28:31], a[52:55], v[32:35], v[28:31]\n"
"buffer_load_dwordx4 v2, s[32:35], s63 offen offset:1024 sc1 lds\n"
"s_waitcnt lgkmcnt(4)\n"
"v_mfma_f32_16x16x32_bf16 v[28:31], v[84:87], v[40:43], v[28:31]\n"
"ds_read_b128 v[32:35], v4 offset:8192\n"
"s_add_u32 s63, s63, 1175552\n"
"v_mfma_f32_16x16x32_bf16 v[28:31], v[196:199], v[44:47], v[28:31]\n"
"v_mfma_f32_16x16x32_bf16 v[24:27], v[192:195], v[44:47], v[24:27]\n"
"ds_read_b128 v[36:39], v4 offset:22528\n"
"s_waitcnt vmcnt(8)\n"
"v_mfma_f32_16x16x32_bf16 v[24:27], v[80:83], v[40:43], v[24:27]\n"
"v_cmp_ne_u64 vcc, v[8:9], v[6:7]\n"
"s_waitcnt lgkmcnt(4)\n"
"v_mfma_f32_16x16x32_bf16 v[24:27], a[56:59], v[48:51], v[24:27]\n"
"ds_read_b128 v[40:43], v4 offset:36864\n"
"s_cbranch_vccnz .L_rta1\n"
".L_bka1:\n"
"v_mfma_f32_16x16x32_bf16 v[24:27], a[168:171], v[52:55], v[24:27]\n"
"s_add_u32 s66, s66, 1187840\n"
"v_mfma_f32_16x16x32_bf16 v[28:31], a[172:175], v[52:55], v[28:31]\n"
"ds_read_b128 v[44:47], v4 offset:51200\n"
"s_add_u32 m0, s74, 8192\n"
"v_mfma_f32_16x16x32_bf16 v[28:31], a[60:63], v[48:51], v[28:31]\n"
"buffer_load_dwordx4 v2, s[32:35], s71 offen offset:0 sc1 lds\n"
"s_waitcnt lgkmcnt(4)\n"
"v_mfma_f32_16x16x32_bf16 v[28:31], v[92:95], v[56:59], v[28:31]\n"
"ds_read_b128 v[48:51], v4 offset:9216\n"
"buffer_load_dwordx4 v2, s[32:35], s71 offen offset:1024 sc1 lds\n"
"v_mfma_f32_16x16x32_bf16 v[28:31], v[204:207], v[60:63], v[28:31]\n"
"s_add_u32 s71, s71, 1187840\n"
"v_mfma_f32_16x16x32_bf16 v[24:27], v[200:203], v[60:63], v[24:27]\n"
"ds_read_b128 v[52:55], v4 offset:23552\n"
"v_mfma_f32_16x16x32_bf16 v[24:27], v[88:91], v[56:59], v[24:27]\n"
"s_waitcnt lgkmcnt(4)\n"
"v_mfma_f32_16x16x32_bf16 v[24:27], a[64:67], v[32:35], v[24:27]\n"
"ds_read_b128 v[56:59], v4 offset:37888\n"
"v_mfma_f32_16x16x32_bf16 v[24:27], a[176:179], v[36:39], v[24:27]\n"
"v_mfma_f32_16x16x32_bf16 v[28:31], a[180:183], v[36:39], v[28:31]\n"
"ds_read_b128 v[60:63], v4 offset:52224\n"
"s_add_u32 m0, s64, 57344\n"
"v_mfma_f32_16x16x32_bf16 v[28:31], a[68:71], v[32:35], v[28:31]\n"
"buffer_load_dwordx4 v2, s[32:35], s63 offen offset:0 sc1 lds\n"
"s_waitcnt lgkmcnt(4)\n"
"v_mfma_f32_16x16x32_bf16 v[28:31], v[100:103], v[40:43], v[28:31]\n"
"ds_read_b128 v[32:35], v4 offset:10240\n"
"v_mfma_f32_16x16x32_bf16 v[28:31], v[212:215], v[44:47], v[28:31]\n"
"v_mfma_f32_16x16x32_bf16 v[24:27], v[208:211], v[44:47], v[24:27]\n"
"ds_read_b128 v[36:39], v4 offset:24576\n"
"v_mfma_f32_16x16x32_bf16 v[24:27], v[96:99], v[40:43], v[24:27]\n"
"s_waitcnt lgkmcnt(4)\n"
"v_mfma_f32_16x16x32_bf16 v[24:27], a[72:75], v[48:51], v[24:27]\n"
"ds_read_b128 v[40:43], v4 offset:38912\n"
"buffer_load_dwordx4 v2, s[32:35], s63 offen offset:1024 sc1 lds\n"
"v_mfma_f32_16x16x32_bf16 v[24:27], a[184:187], v[52:55], v[24:27]\n"
"ds_read_b128 v[16:19], v5 offset:0\n"
"v_mfma_f32_16x16x32_bf16 v[28:31], a[188:191], v[52:55], v[28:31]\n"
"ds_read_b128 v[44:47], v4 offset:53248\n"
"ds_read_b128 v[20:23], v5 offset:1024\n"
"v_mfma_f32_16x16x32_bf16 v[28:31], a[76:79], v[48:51], v[28:31]\n"
"s_waitcnt lgkmcnt(6)\n"
"v_mfma_f32_16x16x32_bf16 v[28:31], v[108:111], v[56:59], v[28:31]\n"
"ds_read_b128 v[48:51], v4 offset:11264\n"
"v_mfma_f32_16x16x32_bf16 v[28:31], v[220:223], v[60:63], v[28:31]\n"
"v_mfma_f32_16x16x32_bf16 v[24:27], v[216:219], v[60:63], v[24:27]\n"
"ds_read_b128 v[52:55], v4 offset:25600\n"
"buffer_load_dwordx4 v2, s[32:35], s63 offen offset:2048 sc1 lds\n"
"v_mfma_f32_16x16x32_bf16 v[24:27], v[104:107], v[56:59], v[24:27]\n"
"s_waitcnt lgkmcnt(6)\n"
"v_mfma_f32_16x16x32_bf16 v[24:27], a[80:83], v[32:35], v[24:27]\n"
"ds_read_b128 v[56:59], v4 offset:39936\n"
"v_mfma_f32_16x16x32_bf16 v[24:27], a[192:195], v[36:39], v[24:27]\n"
"v_mfma_f32_16x16x32_bf16 v[28:31], a[196:199], v[36:39], v[28:31]\n"
"ds_read_b128 v[60:63], v4 offset:54272\n"
"v_mfma_f32_16x16x32_bf16 v[28:31], a[84:87], v[32:35], v[28:31]\n"
"s_waitcnt lgkmcnt(5)\n"
"v_mfma_f32_16x16x32_bf16 v[28:31], v[116:119], v[40:43], v[28:31]\n"
"ds_read_b128 v[32:35], v4 offset:12288\n"
"buffer_load_dwordx4 v2, s[32:35], s63 offen offset:3072 sc1 lds\n"
"v_mfma_f32_16x16x32_bf16 v[28:31], v[228:231], v[44:47], v[28:31]\n"
"v_mfma_f32_16x16x32_bf16 v[24:27], v[224:227], v[44:47], v[24:27]\n"
"ds_read_b128 v[36:39], v4 offset:26624\n"
"v_mfma_f32_16x16x32_bf16 v[24:27], v[112:115], v[40:43], v[24:27]\n"
"s_waitcnt lgkmcnt(4)\n"
"v_mfma_f32_16x16x32_bf16 v[24:27], a[88:91], v[48:51], v[24:27]\n"
"ds_read_b128 v[40:43], v4 offset:40960\n"
"v_mfma_f32_16x16x32_bf16 v[24:27], a[200:203], v[52:55], v[24:27]\n"
"v_mfma_f32_16x16x32_bf16 v[28:31], a[204:207], v[52:55], v[28:31]\n"
"ds_read_b128 v[44:47], v4 offset:55296\n"
"s_add_u32 s63, s63, 4096\n"
"v_mfma_f32_16x16x32_bf16 v[28:31], a[92:95], v[48:51], v[28:31]\n"
"s_add_u32 m0, s64, 61440\n"
"s_waitcnt lgkmcnt(4)\n"
"v_mfma_f32_16x16x32_bf16 v[28:31], v[124:127], v[56:59], v[28:31]\n"
"ds_read_b128 v[48:51], v4 offset:13312\n"
"buffer_load_dwordx4 v2, s[32:35], s63 offen offset:0 sc1 lds\n"
"v_mfma_f32_16x16x32_bf16 v[28:31], v[236:239], v[60:63], v[28:31]\n"
"v_mfma_f32_16x16x32_bf16 v[24:27], v[232:235], v[60:63], v[24:27]\n"
"ds_read_b128 v[52:55], v4 offset:27648\n"
"v_mfma_f32_16x16x32_bf16 v[24:27], v[120:123], v[56:59], v[24:27]\n"
"s_waitcnt lgkmcnt(4)\n"
"v_mfma_f32_16x16x32_bf16 v[24:27], a[96:99], v[32:35], v[24:27]\n"
"ds_read_b128 v[56:59], v4 offset:41984\n"
"v_mfma_f32_16x16x32_bf16 v[24:27], a[208:211], v[36:39], v[24:27]\n"
"v_mfma_f32_16x16x32_bf16 v[28:31], a[212:215], v[36:39], v[28:31]\n"
"ds_read_b128 v[60:63], v4 offset:56320\n"
"v_mfma_f32_16x16x32_bf16 v[28:31], a[100:103], v[32:35], v[28:31]\n"
"s_waitcnt vmcnt(10)\n"
"s_waitcnt lgkmcnt(4)\n"
"v_mfma_f32_16x16x32_bf16 v[28:31], v[132:135], v[40:43], v[28:31]\n"
"s_barrier\n"
"ds_read_b128 v[32:35], v2 offset:0\n"
"v_mfma_f32_16x16x32_bf16 v[28:31], v[244:247], v[44:47], v[28:31]\n"
"buffer_store_dwordx2 v[8:9], v2, s[92:95], s67 idxen sc1\n"
"v_mfma_f32_16x16x32_bf16 v[24:27], v[240:243], v[44:47], v[24:27]\n"
"ds_read_b128 v[36:39], v2 offset:14336\n"
"v_mfma_f32_16x16x32_bf16 v[24:27], v[128:131], v[40:43], v[24:27]\n"
"s_waitcnt lgkmcnt(4)\n"
"v_mfma_f32_16x16x32_bf16 v[24:27], a[104:107], v[48:51], v[24:27]\n"
"ds_read_b128 v[40:43], v2 offset:28672\n"
"v_mfma_f32_16x16x32_bf16 v[24:27], a[216:219], v[52:55], v[24:27]\n"
"v_mfma_f32_16x16x32_bf16 v[28:31], a[220:223], v[52:55], v[28:31]\n"
"ds_read_b128 v[44:47], v2 offset:43008\n"
"v_mfma_f32_16x16x32_bf16 v[28:31], a[108:111], v[48:51], v[28:31]\n"
"s_waitcnt lgkmcnt(4)\n"
"v_mfma_f32_16x16x32_bf16 v[28:31], v[140:143], v[56:59], v[28:31]\n"
"ds_read_b128 v[48:51], v2 offset:1024\n"
"v_mfma_f32_16x16x32_bf16 v[28:31], v[252:255], v[60:63], v[28:31]\n"
"v_mfma_f32_16x16x32_bf16 v[24:27], v[248:251], v[60:63], v[24:27]\n"
"ds_read_b128 v[52:55], v2 offset:15360\n"
"v_mfma_f32_16x16x32_bf16 v[24:27], v[136:139], v[56:59], v[24:27]\n"
"s_branch .L_dal\n"
".L_dab:\n"
"s_mov_b32 s62, 15\n"
".p2align 6\n"
".L_dbl:\n"
"s_waitcnt lgkmcnt(4)\n"
"v_mfma_f32_16x16x32_bf16 v[24:27], a[0:3], v[32:35], v[24:27]\n"
"ds_read_b128 v[56:59], v4 offset:29696\n"
"v_mfma_f32_16x16x32_bf16 v[24:27], a[112:115], v[36:39], v[24:27]\n"
"buffer_load_dwordx4 v2, s[32:35], s63 offen offset:1024 sc1 lds\n"
"v_mfma_f32_16x16x32_bf16 v[28:31], a[116:119], v[36:39], v[28:31]\n"
"ds_read_b128 v[60:63], v4 offset:44032\n"
"v_mfma_f32_16x16x32_bf16 v[28:31], a[4:7], v[32:35], v[28:31]\n"
"s_waitcnt lgkmcnt(4)\n"
"v_mfma_f32_16x16x32_bf16 v[28:31], a[228:231], v[40:43], v[28:31]\n"
"ds_read_b128 v[32:35], v4 offset:2048\n"
"v_cvt_pk_bf16_f32 v12, v16, v17\n"
"v_mfma_f32_16x16x32_bf16 v[28:31], v[148:151], v[44:47], v[28:31]\n"
"v_cvt_pk_bf16_f32 v13, v18, v19\n"
"v_cvt_pk_bf16_f32 v14, v20, v21\n"
"v_mfma_f32_16x16x32_bf16 v[24:27], v[144:147], v[44:47], v[24:27]\n"
"ds_read_b128 v[36:39], v4 offset:16384\n"
"v_cvt_pk_bf16_f32 v15, v22, v23\n"
"v_mfma_f32_16x16x32_bf16 v[24:27], a[224:227], v[40:43], v[24:27]\n"
"buffer_load_dwordx4 v2, s[32:35], s63 offen offset:2048 sc1 lds\n"
"s_waitcnt lgkmcnt(4)\n"
"v_mfma_f32_16x16x32_bf16 v[24:27], a[8:11], v[48:51], v[24:27]\n"
"ds_read_b128 v[40:43], v4 offset:30720\n"
"buffer_store_dwordx4 v[12:15], v1, s[44:47], s65 offen\n"
"v_mfma_f32_16x16x32_bf16 v[24:27], a[120:123], v[52:55], v[24:27]\n"
"s_add_u32 s65, s65, 32768\n"
"v_mfma_f32_16x16x32_bf16 v[28:31], a[124:127], v[52:55], v[28:31]\n"
"ds_read_b128 v[44:47], v4 offset:45056\n"
"buffer_load_dwordx2 v[6:7], v0, s[32:35], s66 offen sc1\n"
"v_mfma_f32_16x16x32_bf16 v[28:31], a[12:15], v[48:51], v[28:31]\n"
"s_waitcnt lgkmcnt(4)\n"
"v_mfma_f32_16x16x32_bf16 v[28:31], a[236:239], v[56:59], v[28:31]\n"
"ds_read_b128 v[48:51], v4 offset:3072\n"
"v_mfma_f32_16x16x32_bf16 v[28:31], v[156:159], v[60:63], v[28:31]\n"
"buffer_load_dwordx4 v2, s[32:35], s63 offen offset:3072 sc1 lds\n"
"v_mfma_f32_16x16x32_bf16 v[24:27], v[152:155], v[60:63], v[24:27]\n"
"ds_read_b128 v[52:55], v4 offset:17408\n"
"v_mfma_f32_16x16x32_bf16 v[24:27], a[232:235], v[56:59], v[24:27]\n"
"s_waitcnt lgkmcnt(4)\n"
"v_mfma_f32_16x16x32_bf16 v[24:27], a[16:19], v[32:35], v[24:27]\n"
"ds_read_b128 v[56:59], v4 offset:31744\n"
"v_mfma_f32_16x16x32_bf16 v[24:27], a[128:131], v[36:39], v[24:27]\n"
"v_mfma_f32_16x16x32_bf16 v[28:31], a[132:135], v[36:39], v[28:31]\n"
"ds_read_b128 v[60:63], v4 offset:46080\n"
"v_mfma_f32_16x16x32_bf16 v[28:31], a[20:23], v[32:35], v[28:31]\n"
"s_add_u32 s63, s63, 4096\n"
"s_waitcnt lgkmcnt(4)\n"
"v_mfma_f32_16x16x32_bf16 v[28:31], a[244:247], v[40:43], v[28:31]\n"
"ds_read_b128 v[32:35], v4 offset:4096\n"
"s_add_u32 m0, s64, 8192\n"
"v_mfma_f32_16x16x32_bf16 v[28:31], v[164:167], v[44:47], v[28:31]\n"
"buffer_load_dwordx4 v2, s[32:35], s63 offen offset:0 sc1 lds\n"
"v_mfma_f32_16x16x32_bf16 v[24:27], v[160:163], v[44:47], v[24:27]\n"
"ds_read_b128 v[36:39], v4 offset:18432\n"
"v_mfma_f32_16x16x32_bf16 v[24:27], a[240:243], v[40:43], v[24:27]\n"
"s_waitcnt lgkmcnt(4)\n"
"v_mfma_f32_16x16x32_bf16 v[24:27], a[24:27], v[48:51], v[24:27]\n"
"ds_read_b128 v[40:43], v4 offset:32768\n"
"v_mfma_f32_16x16x32_bf16 v[24:27], a[136:139], v[52:55], v[24:27]\n"
"buffer_load_dwordx4 v2, s[32:35], s63 offen offset:1024 sc1 lds\n"
"v_mfma_f32_16x16x32_bf16 v[28:31], a[140:143], v[52:55], v[28:31]\n"
"ds_read_b128 v[44:47], v4 offset:47104\n"
"v_mfma_f32_16x16x32_bf16 v[28:31], a[28:31], v[48:51], v[28:31]\n"
"s_waitcnt lgkmcnt(4)\n"
"v_mfma_f32_16x16x32_bf16 v[28:31], a[252:255], v[56:59], v[28:31]\n"
"ds_read_b128 v[48:51], v4 offset:5120\n"
"v_mfma_f32_16x16x32_bf16 v[28:31], v[172:175], v[60:63], v[28:31]\n"
"v_mfma_f32_16x16x32_bf16 v[24:27], v[168:171], v[60:63], v[24:27]\n"
"ds_read_b128 v[52:55], v4 offset:19456\n"
"v_mfma_f32_16x16x32_bf16 v[24:27], a[248:251], v[56:59], v[24:27]\n"
"buffer_load_dwordx4 v2, s[32:35], s63 offen offset:2048 sc1 lds\n"
"s_waitcnt lgkmcnt(4)\n"
"v_mfma_f32_16x16x32_bf16 v[24:27], a[32:35], v[32:35], v[24:27]\n"
"ds_read_b128 v[56:59], v4 offset:33792\n"
"v_mfma_f32_16x16x32_bf16 v[24:27], a[144:147], v[36:39], v[24:27]\n"
"v_mfma_f32_16x16x32_bf16 v[28:31], a[148:151], v[36:39], v[28:31]\n"
"ds_read_b128 v[60:63], v4 offset:48128\n"
"v_mfma_f32_16x16x32_bf16 v[28:31], a[36:39], v[32:35], v[28:31]\n"
"s_waitcnt lgkmcnt(4)\n"
"v_mfma_f32_16x16x32_bf16 v[28:31], v[68:71], v[40:43], v[28:31]\n"
"ds_read_b128 v[32:35], v4 offset:6144\n"
"v_mfma_f32_16x16x32_bf16 v[28:31], v[180:183], v[44:47], v[28:31]\n"
"buffer_load_dwordx4 v2, s[32:35], s63 offen offset:3072 sc1 lds\n"
"v_mfma_f32_16x16x32_bf16 v[24:27], v[176:179], v[44:47], v[24:27]\n"
"ds_read_b128 v[36:39], v4 offset:20480\n"
"v_mfma_f32_16x16x32_bf16 v[24:27], v[64:67], v[40:43], v[24:27]\n"
"s_waitcnt lgkmcnt(4)\n"
"v_mfma_f32_16x16x32_bf16 v[24:27], a[40:43], v[48:51], v[24:27]\n"
"ds_read_b128 v[40:43], v4 offset:34816\n"
"v_mfma_f32_16x16x32_bf16 v[24:27], a[152:155], v[52:55], v[24:27]\n"
"v_mfma_f32_16x16x32_bf16 v[28:31], a[156:159], v[52:55], v[28:31]\n"
"ds_read_b128 v[44:47], v4 offset:49152\n"
"v_mfma_f32_16x16x32_bf16 v[28:31], a[44:47], v[48:51], v[28:31]\n"
"s_waitcnt vmcnt(16)\n"
"s_waitcnt lgkmcnt(4)\n"
"v_mfma_f32_16x16x32_bf16 v[28:31], v[76:79], v[56:59], v[28:31]\n"
"s_barrier\n"
"ds_read_b128 v[48:51], v4 offset:7168\n"
"v_mfma_f32_16x16x32_bf16 v[28:31], v[188:191], v[60:63], v[28:31]\n"
"v_mfma_f32_16x16x32_bf16 v[24:27], v[184:187], v[60:63], v[24:27]\n"
"ds_read_b128 v[52:55], v4 offset:21504\n"
"s_add_u32 s63, s63, 4096\n"
"v_mfma_f32_16x16x32_bf16 v[24:27], v[72:75], v[56:59], v[24:27]\n"
"s_add_u32 m0, s64, 12288\n"
"s_waitcnt lgkmcnt(4)\n"
"v_mfma_f32_16x16x32_bf16 v[24:27], a[48:51], v[32:35], v[24:27]\n"
"ds_read_b128 v[56:59], v4 offset:35840\n"
"buffer_load_dwordx4 v2, s[32:35], s63 offen offset:0 sc1 lds\n"
"v_mfma_f32_16x16x32_bf16 v[24:27], a[160:163], v[36:39], v[24:27]\n"
"v_mfma_f32_16x16x32_bf16 v[28:31], a[164:167], v[36:39], v[28:31]\n"
"ds_read_b128 v[60:63], v4 offset:50176\n"
"v_mfma_f32_16x16x32_bf16 v[28:31], a[52:55], v[32:35], v[28:31]\n"
"buffer_load_dwordx4 v2, s[32:35], s63 offen offset:1024 sc1 lds\n"
"s_waitcnt lgkmcnt(4)\n"
"v_mfma_f32_16x16x32_bf16 v[28:31], v[84:87], v[40:43], v[28:31]\n"
"ds_read_b128 v[32:35], v4 offset:8192\n"
"s_add_u32 s63, s63, 1175552\n"
"v_mfma_f32_16x16x32_bf16 v[28:31], v[196:199], v[44:47], v[28:31]\n"
"v_mfma_f32_16x16x32_bf16 v[24:27], v[192:195], v[44:47], v[24:27]\n"
"ds_read_b128 v[36:39], v4 offset:22528\n"
"s_waitcnt vmcnt(7)\n"
"v_mfma_f32_16x16x32_bf16 v[24:27], v[80:83], v[40:43], v[24:27]\n"
"v_cmp_ne_u64 vcc, v[8:9], v[6:7]\n"
"s_waitcnt lgkmcnt(4)\n"
"v_mfma_f32_16x16x32_bf16 v[24:27], a[56:59], v[48:51], v[24:27]\n"
"ds_read_b128 v[40:43], v4 offset:36864\n"
"s_cbranch_vccnz .L_rtb1\n"
".L_bkb1:\n"
"v_mfma_f32_16x16x32_bf16 v[24:27], a[168:171], v[52:55], v[24:27]\n"
"s_add_u32 s66, s66, 1187840\n"
"v_mfma_f32_16x16x32_bf16 v[28:31], a[172:175], v[52:55], v[28:31]\n"
"ds_read_b128 v[44:47], v4 offset:51200\n"
"s_add_u32 m0, s74, 8192\n"
"v_mfma_f32_16x16x32_bf16 v[28:31], a[60:63], v[48:51], v[28:31]\n"
"buffer_load_dwordx4 v2, s[32:35], s71 offen offset:0 sc1 lds\n"
"s_waitcnt lgkmcnt(4)\n"
"v_mfma_f32_16x16x32_bf16 v[28:31], v[92:95], v[56:59], v[28:31]\n"
"ds_read_b128 v[48:51], v4 offset:9216\n"
"buffer_load_dwordx4 v2, s[32:35], s71 offen offset:1024 sc1 lds\n"
"v_mfma_f32_16x16x32_bf16 v[28:31], v[204:207], v[60:63], v[28:31]\n"
"s_add_u32 s71, s71, 1187840\n"
"v_mfma_f32_16x16x32_bf16 v[24:27], v[200:203], v[60:63], v[24:27]\n"
"ds_read_b128 v[52:55], v4 offset:23552\n"
"v_mfma_f32_16x16x32_bf16 v[24:27], v[88:91], v[56:59], v[24:27]\n"
"s_waitcnt lgkmcnt(4)\n"
"v_mfma_f32_16x16x32_bf16 v[24:27], a[64:67], v[32:35], v[24:27]\n"
"ds_read_b128 v[56:59], v4 offset:37888\n"
"v_mfma_f32_16x16x32_bf16 v[24:27], a[176:179], v[36:39], v[24:27]\n"
"v_mfma_f32_16x16x32_bf16 v[28:31], a[180:183], v[36:39], v[28:31]\n"
"ds_read_b128 v[60:63], v4 offset:52224\n"
"s_add_u32 m0, s64, 57344\n"
"v_mfma_f32_16x16x32_bf16 v[28:31], a[68:71], v[32:35], v[28:31]\n"
"buffer_load_dwordx4 v2, s[32:35], s63 offen offset:0 sc1 lds\n"
"s_waitcnt lgkmcnt(4)\n"
"v_mfma_f32_16x16x32_bf16 v[28:31], v[100:103], v[40:43], v[28:31]\n"
"ds_read_b128 v[32:35], v4 offset:10240\n"
"v_mfma_f32_16x16x32_bf16 v[28:31], v[212:215], v[44:47], v[28:31]\n"
"v_mfma_f32_16x16x32_bf16 v[24:27], v[208:211], v[44:47], v[24:27]\n"
"ds_read_b128 v[36:39], v4 offset:24576\n"
"v_mfma_f32_16x16x32_bf16 v[24:27], v[96:99], v[40:43], v[24:27]\n"
"s_waitcnt lgkmcnt(4)\n"
"v_mfma_f32_16x16x32_bf16 v[24:27], a[72:75], v[48:51], v[24:27]\n"
"ds_read_b128 v[40:43], v4 offset:38912\n"
"buffer_load_dwordx4 v2, s[32:35], s63 offen offset:1024 sc1 lds\n"
"v_mfma_f32_16x16x32_bf16 v[24:27], a[184:187], v[52:55], v[24:27]\n"
"ds_read_b128 v[16:19], v5 offset:0\n"
"v_mfma_f32_16x16x32_bf16 v[28:31], a[188:191], v[52:55], v[28:31]\n"
"ds_read_b128 v[44:47], v4 offset:53248\n"
"ds_read_b128 v[20:23], v5 offset:1024\n"
"v_mfma_f32_16x16x32_bf16 v[28:31], a[76:79], v[48:51], v[28:31]\n"
"s_waitcnt lgkmcnt(6)\n"
"v_mfma_f32_16x16x32_bf16 v[28:31], v[108:111], v[56:59], v[28:31]\n"
"ds_read_b128 v[48:51], v4 offset:11264\n"
"v_mfma_f32_16x16x32_bf16 v[28:31], v[220:223], v[60:63], v[28:31]\n"
"v_mfma_f32_16x16x32_bf16 v[24:27], v[216:219], v[60:63], v[24:27]\n"
"ds_read_b128 v[52:55], v4 offset:25600\n"
"buffer_load_dwordx4 v2, s[32:35], s63 offen offset:2048 sc1 lds\n"
"v_mfma_f32_16x16x32_bf16 v[24:27], v[104:107], v[56:59], v[24:27]\n"
"s_waitcnt lgkmcnt(6)\n"
"v_mfma_f32_16x16x32_bf16 v[24:27], a[80:83], v[32:35], v[24:27]\n"
"ds_read_b128 v[56:59], v4 offset:39936\n"
"v_mfma_f32_16x16x32_bf16 v[24:27], a[192:195], v[36:39], v[24:27]\n"
"v_mfma_f32_16x16x32_bf16 v[28:31], a[196:199], v[36:39], v[28:31]\n"
"ds_read_b128 v[60:63], v4 offset:54272\n"
"v_mfma_f32_16x16x32_bf16 v[28:31], a[84:87], v[32:35], v[28:31]\n"
"s_waitcnt lgkmcnt(5)\n"
"v_mfma_f32_16x16x32_bf16 v[28:31], v[116:119], v[40:43], v[28:31]\n"
"ds_read_b128 v[32:35], v4 offset:12288\n"
"buffer_load_dwordx4 v2, s[32:35], s63 offen offset:3072 sc1 lds\n"
"v_mfma_f32_16x16x32_bf16 v[28:31], v[228:231], v[44:47], v[28:31]\n"
"v_mfma_f32_16x16x32_bf16 v[24:27], v[224:227], v[44:47], v[24:27]\n"
"ds_read_b128 v[36:39], v4 offset:26624\n"
"v_mfma_f32_16x16x32_bf16 v[24:27], v[112:115], v[40:43], v[24:27]\n"
"s_waitcnt lgkmcnt(4)\n"
"v_mfma_f32_16x16x32_bf16 v[24:27], a[88:91], v[48:51], v[24:27]\n"
"ds_read_b128 v[40:43], v4 offset:40960\n"
"v_mfma_f32_16x16x32_bf16 v[24:27], a[200:203], v[52:55], v[24:27]\n"
"v_mfma_f32_16x16x32_bf16 v[28:31], a[204:207], v[52:55], v[28:31]\n"
"ds_read_b128 v[44:47], v4 offset:55296\n"
"s_add_u32 s63, s63, 4096\n"
"v_mfma_f32_16x16x32_bf16 v[28:31], a[92:95], v[48:51], v[28:31]\n"
"s_add_u32 m0, s64, 61440\n"
"s_waitcnt lgkmcnt(4)\n"
"v_mfma_f32_16x16x32_bf16 v[28:31], v[124:127], v[56:59], v[28:31]\n"
"ds_read_b128 v[48:51], v4 offset:13312\n"
"buffer_load_dwordx4 v2, s[32:35], s63 offen offset:0 sc1 lds\n"
"v_mfma_f32_16x16x32_bf16 v[28:31], v[236:239], v[60:63], v[28:31]\n"
"v_mfma_f32_16x16x32_bf16 v[24:27], v[232:235], v[60:63], v[24:27]\n"
"ds_read_b128 v[52:55], v4 offset:27648\n"
"v_mfma_f32_16x16x32_bf16 v[24:27], v[120:123], v[56:59], v[24:27]\n"
"s_waitcnt lgkmcnt(4)\n"
"v_mfma_f32_16x16x32_bf16 v[24:27], a[96:99], v[32:35], v[24:27]\n"
"ds_read_b128 v[56:59], v4 offset:41984\n"
"v_mfma_f32_16x16x32_bf16 v[24:27], a[208:211], v[36:39], v[24:27]\n"
"v_mfma_f32_16x16x32_bf16 v[28:31], a[212:215], v[36:39], v[28:31]\n"
"ds_read_b128 v[60:63], v4 offset:56320\n"
"v_mfma_f32_16x16x32_bf16 v[28:31], a[100:103], v[32:35], v[28:31]\n"
"s_waitcnt lgkmcnt(4)\n"
"v_mfma_f32_16x16x32_bf16 v[28:31], v[132:135], v[40:43], v[28:31]\n"
"s_barrier\n"
"ds_read_b128 v[32:35], v2 offset:0\n"
"v_mfma_f32_16x16x32_bf16 v[28:31], v[244:247], v[44:47], v[28:31]\n"
"v_mfma_f32_16x16x32_bf16 v[24:27], v[240:243], v[44:47], v[24:27]\n"
"ds_read_b128 v[36:39], v2 offset:14336\n"
"v_mfma_f32_16x16x32_bf16 v[24:27], v[128:131], v[40:43], v[24:27]\n"
"s_waitcnt lgkmcnt(4)\n"
"v_mfma_f32_16x16x32_bf16 v[24:27], a[104:107], v[48:51], v[24:27]\n"
"ds_read_b128 v[40:43], v2 offset:28672\n"
"v_mfma_f32_16x16x32_bf16 v[24:27], a[216:219], v[52:55], v[24:27]\n"
"v_mfma_f32_16x16x32_bf16 v[28:31], a[220:223], v[52:55], v[28:31]\n"
"ds_read_b128 v[44:47], v2 offset:43008\n"
"v_mfma_f32_16x16x32_bf16 v[28:31], a[108:111], v[48:51], v[28:31]\n"
"s_waitcnt lgkmcnt(4)\n"
"v_mfma_f32_16x16x32_bf16 v[28:31], v[140:143], v[56:59], v[28:31]\n"
"ds_read_b128 v[48:51], v2 offset:1024\n"
"v_mfma_f32_16x16x32_bf16 v[28:31], v[252:255], v[60:63], v[28:31]\n"
"v_mfma_f32_16x16x32_bf16 v[24:27], v[248:251], v[60:63], v[24:27]\n"
"ds_read_b128 v[52:55], v2 offset:15360\n"
"s_sub_u32 s62, s62, 1\n"
"v_mfma_f32_16x16x32_bf16 v[24:27], v[136:139], v[56:59], v[24:27]\n"
"s_cbranch_scc1 .L_dbt\n"
"s_waitcnt lgkmcnt(4)\n"
"v_mfma_f32_16x16x32_bf16 v[16:19], a[0:3], v[32:35], v[16:19]\n"
"ds_read_b128 v[56:59], v2 offset:29696\n"
"v_mfma_f32_16x16x32_bf16 v[16:19], a[112:115], v[36:39], v[16:19]\n"
"buffer_load_dwordx4 v2, s[32:35], s63 offen offset:1024 sc1 lds\n"
"v_mfma_f32_16x16x32_bf16 v[20:23], a[116:119], v[36:39], v[20:23]\n"
"ds_read_b128 v[60:63], v2 offset:44032\n"
"v_mfma_f32_16x16x32_bf16 v[20:23], a[4:7], v[32:35], v[20:23]\n"
"s_waitcnt lgkmcnt(4)\n"
"v_mfma_f32_16x16x32_bf16 v[20:23], a[228:231], v[40:43], v[20:23]\n"
"ds_read_b128 v[32:35], v2 offset:2048\n"
"v_cvt_pk_bf16_f32 v12, v24, v25\n"
"v_mfma_f32_16x16x32_bf16 v[20:23], v[148:151], v[44:47], v[20:23]\n"
"v_cvt_pk_bf16_f32 v13, v26, v27\n"
"v_cvt_pk_bf16_f32 v14, v28, v29\n"
"v_mfma_f32_16x16x32_bf16 v[16:19], v[144:147], v[44:47], v[16:19]\n"
"ds_read_b128 v[36:39], v2 offset:16384\n"
"v_cvt_pk_bf16_f32 v15, v30, v31\n"
"v_mfma_f32_16x16x32_bf16 v[16:19], a[224:227], v[40:43], v[16:19]\n"
"buffer_load_dwordx4 v2, s[32:35], s63 offen offset:2048 sc1 lds\n"
"s_waitcnt lgkmcnt(4)\n"
"v_mfma_f32_16x16x32_bf16 v[16:19], a[8:11], v[48:51], v[16:19]\n"
"ds_read_b128 v[40:43], v2 offset:30720\n"
"buffer_store_dwordx4 v[12:15], v1, s[44:47], s65 offen\n"
"v_mfma_f32_16x16x32_bf16 v[16:19], a[120:123], v[52:55], v[16:19]\n"
"s_add_u32 s65, s65, 32768\n"
"v_mfma_f32_16x16x32_bf16 v[20:23], a[124:127], v[52:55], v[20:23]\n"
"ds_read_b128 v[44:47], v2 offset:45056\n"
"buffer_load_dwordx2 v[6:7], v0, s[32:35], s66 offen sc1\n"
"v_mfma_f32_16x16x32_bf16 v[20:23], a[12:15], v[48:51], v[20:23]\n"
"s_waitcnt lgkmcnt(4)\n"
"v_mfma_f32_16x16x32_bf16 v[20:23], a[236:239], v[56:59], v[20:23]\n"
"ds_read_b128 v[48:51], v2 offset:3072\n"
"v_mfma_f32_16x16x32_bf16 v[20:23], v[156:159], v[60:63], v[20:23]\n"
"buffer_load_dwordx4 v2, s[32:35], s63 offen offset:3072 sc1 lds\n"
"v_mfma_f32_16x16x32_bf16 v[16:19], v[152:155], v[60:63], v[16:19]\n"
"ds_read_b128 v[52:55], v2 offset:17408\n"
"v_mfma_f32_16x16x32_bf16 v[16:19], a[232:235], v[56:59], v[16:19]\n"
"s_waitcnt lgkmcnt(4)\n"
"v_mfma_f32_16x16x32_bf16 v[16:19], a[16:19], v[32:35], v[16:19]\n"
"ds_read_b128 v[56:59], v2 offset:31744\n"
"v_mfma_f32_16x16x32_bf16 v[16:19], a[128:131], v[36:39], v[16:19]\n"
"v_mfma_f32_16x16x32_bf16 v[20:23], a[132:135], v[36:39], v[20:23]\n"
"ds_read_b128 v[60:63], v2 offset:46080\n"
"v_mfma_f32_16x16x32_bf16 v[20:23], a[20:23], v[32:35], v[20:23]\n"
"s_add_u32 s63, s63, 4096\n"
"s_waitcnt lgkmcnt(4)\n"
"v_mfma_f32_16x16x32_bf16 v[20:23], a[244:247], v[40:43], v[20:23]\n"
"ds_read_b128 v[32:35], v2 offset:4096\n"
"s_add_u32 m0, s64, 65536\n"
"v_mfma_f32_16x16x32_bf16 v[20:23], v[164:167], v[44:47], v[20:23]\n"
"buffer_load_dwordx4 v2, s[32:35], s63 offen offset:0 sc1 lds\n"
"v_mfma_f32_16x16x32_bf16 v[16:19], v[160:163], v[44:47], v[16:19]\n"
"ds_read_b128 v[36:39], v2 offset:18432\n"
"v_mfma_f32_16x16x32_bf16 v[16:19], a[240:243], v[40:43], v[16:19]\n"
"s_waitcnt lgkmcnt(4)\n"
"v_mfma_f32_16x16x32_bf16 v[16:19], a[24:27], v[48:51], v[16:19]\n"
"ds_read_b128 v[40:43], v2 offset:32768\n"
"v_mfma_f32_16x16x32_bf16 v[16:19], a[136:139], v[52:55], v[16:19]\n"
"buffer_load_dwordx4 v2, s[32:35], s63 offen offset:1024 sc1 lds\n"
"v_mfma_f32_16x16x32_bf16 v[20:23], a[140:143], v[52:55], v[20:23]\n"
"ds_read_b128 v[44:47], v2 offset:47104\n"
"v_mfma_f32_16x16x32_bf16 v[20:23], a[28:31], v[48:51], v[20:23]\n"
"s_waitcnt lgkmcnt(4)\n"
"v_mfma_f32_16x16x32_bf16 v[20:23], a[252:255], v[56:59], v[20:23]\n"
"ds_read_b128 v[48:51], v2 offset:5120\n"
"v_mfma_f32_16x16x32_bf16 v[20:23], v[172:175], v[60:63], v[20:23]\n"
"v_mfma_f32_16x16x32_bf16 v[16:19], v[168:171], v[60:63], v[16:19]\n"
"ds_read_b128 v[52:55], v2 offset:19456\n"
"v_mfma_f32_16x16x32_bf16 v[16:19], a[248:251], v[56:59], v[16:19]\n"
"buffer_load_dwordx4 v2, s[32:35], s63 offen offset:2048 sc1 lds\n"
"s_waitcnt lgkmcnt(4)\n"
"v_mfma_f32_16x16x32_bf16 v[16:19], a[32:35], v[32:35], v[16:19]\n"
"ds_read_b128 v[56:59], v2 offset:33792\n"
"v_mfma_f32_16x16x32_bf16 v[16:19], a[144:147], v[36:39], v[16:19]\n"
"v_mfma_f32_16x16x32_bf16 v[20:23], a[148:151], v[36:39], v[20:23]\n"
"ds_read_b128 v[60:63], v2 offset:48128\n"
"v_mfma_f32_16x16x32_bf16 v[20:23], a[36:39], v[32:35], v[20:23]\n"
"s_waitcnt lgkmcnt(4)\n"
"v_mfma_f32_16x16x32_bf16 v[20:23], v[68:71], v[40:43], v[20:23]\n"
"ds_read_b128 v[32:35], v2 offset:6144\n"
"v_mfma_f32_16x16x32_bf16 v[20:23], v[180:183], v[44:47], v[20:23]\n"
"buffer_load_dwordx4 v2, s[32:35], s63 offen offset:3072 sc1 lds\n"
"v_mfma_f32_16x16x32_bf16 v[16:19], v[176:179], v[44:47], v[16:19]\n"
"ds_read_b128 v[36:39], v2 offset:20480\n"
"v_mfma_f32_16x16x32_bf16 v[16:19], v[64:67], v[40:43], v[16:19]\n"
"s_waitcnt lgkmcnt(4)\n"
"v_mfma_f32_16x16x32_bf16 v[16:19], a[40:43], v[48:51], v[16:19]\n"
"ds_read_b128 v[40:43], v2 offset:34816\n"
"v_mfma_f32_16x16x32_bf16 v[16:19], a[152:155], v[52:55], v[16:19]\n"
"v_mfma_f32_16x16x32_bf16 v[20:23], a[156:159], v[52:55], v[20:23]\n"
"ds_read_b128 v[44:47], v2 offset:49152\n"
"v_mfma_f32_16x16x32_bf16 v[20:23], a[44:47], v[48:51], v[20:23]\n"
"s_waitcnt vmcnt(16)\n"
"s_waitcnt lgkmcnt(4)\n"
"v_mfma_f32_16x16x32_bf16 v[20:23], v[76:79], v[56:59], v[20:23]\n"
"s_barrier\n"
"ds_read_b128 v[48:51], v2 offset:7168\n"
"v_mfma_f32_16x16x32_bf16 v[20:23], v[188:191], v[60:63], v[20:23]\n"
"v_mfma_f32_16x16x32_bf16 v[16:19], v[184:187], v[60:63], v[16:19]\n"
"ds_read_b128 v[52:55], v2 offset:21504\n"
"s_add_u32 s63, s63, 4096\n"
"v_mfma_f32_16x16x32_bf16 v[16:19], v[72:75], v[56:59], v[16:19]\n"
"s_add_u32 m0, s64, 69632\n"
"s_waitcnt lgkmcnt(4)\n"
"v_mfma_f32_16x16x32_bf16 v[16:19], a[48:51], v[32:35], v[16:19]\n"
"ds_read_b128 v[56:59], v2 offset:35840\n"
"buffer_load_dwordx4 v2, s[32:35], s63 offen offset:0 sc1 lds\n"
"v_mfma_f32_16x16x32_bf16 v[16:19], a[160:163], v[36:39], v[16:19]\n"
"v_mfma_f32_16x16x32_bf16 v[20:23], a[164:167], v[36:39], v[20:23]\n"
"ds_read_b128 v[60:63], v2 offset:50176\n"
"v_mfma_f32_16x16x32_bf16 v[20:23], a[52:55], v[32:35], v[20:23]\n"
"buffer_load_dwordx4 v2, s[32:35], s63 offen offset:1024 sc1 lds\n"
"s_waitcnt lgkmcnt(4)\n"
"v_mfma_f32_16x16x32_bf16 v[20:23], v[84:87], v[40:43], v[20:23]\n"
"ds_read_b128 v[32:35], v2 offset:8192\n"
"s_add_u32 s63, s63, 1175552\n"
"v_mfma_f32_16x16x32_bf16 v[20:23], v[196:199], v[44:47], v[20:23]\n"
"v_mfma_f32_16x16x32_bf16 v[16:19], v[192:195], v[44:47], v[16:19]\n"
"ds_read_b128 v[36:39], v2 offset:22528\n"
"s_waitcnt vmcnt(7)\n"
"v_mfma_f32_16x16x32_bf16 v[16:19], v[80:83], v[40:43], v[16:19]\n"
"v_cmp_ne_u64 vcc, v[8:9], v[6:7]\n"
"s_waitcnt lgkmcnt(4)\n"
"v_mfma_f32_16x16x32_bf16 v[16:19], a[56:59], v[48:51], v[16:19]\n"
"ds_read_b128 v[40:43], v2 offset:36864\n"
"s_cbranch_vccnz .L_rtb0\n"
".L_bkb0:\n"
"v_mfma_f32_16x16x32_bf16 v[16:19], a[168:171], v[52:55], v[16:19]\n"
"s_add_u32 s66, s66, 1187840\n"
"v_mfma_f32_16x16x32_bf16 v[20:23], a[172:175], v[52:55], v[20:23]\n"
"ds_read_b128 v[44:47], v2 offset:51200\n"
"s_add_u32 m0, s74, 0\n"
"v_mfma_f32_16x16x32_bf16 v[20:23], a[60:63], v[48:51], v[20:23]\n"
"buffer_load_dwordx4 v2, s[32:35], s71 offen offset:0 sc1 lds\n"
"s_waitcnt lgkmcnt(4)\n"
"v_mfma_f32_16x16x32_bf16 v[20:23], v[92:95], v[56:59], v[20:23]\n"
"ds_read_b128 v[48:51], v2 offset:9216\n"
"buffer_load_dwordx4 v2, s[32:35], s71 offen offset:1024 sc1 lds\n"
"v_mfma_f32_16x16x32_bf16 v[20:23], v[204:207], v[60:63], v[20:23]\n"
"s_add_u32 s71, s71, 1187840\n"
"v_mfma_f32_16x16x32_bf16 v[16:19], v[200:203], v[60:63], v[16:19]\n"
"ds_read_b128 v[52:55], v2 offset:23552\n"
"v_mfma_f32_16x16x32_bf16 v[16:19], v[88:91], v[56:59], v[16:19]\n"
"s_waitcnt lgkmcnt(4)\n"
"v_mfma_f32_16x16x32_bf16 v[16:19], a[64:67], v[32:35], v[16:19]\n"
"ds_read_b128 v[56:59], v2 offset:37888\n"
"v_mfma_f32_16x16x32_bf16 v[16:19], a[176:179], v[36:39], v[16:19]\n"
"v_mfma_f32_16x16x32_bf16 v[20:23], a[180:183], v[36:39], v[20:23]\n"
"ds_read_b128 v[60:63], v2 offset:52224\n"
"s_add_u32 m0, s64, 0\n"
"v_mfma_f32_16x16x32_bf16 v[20:23], a[68:71], v[32:35], v[20:23]\n"
"buffer_load_dwordx4 v2, s[32:35], s63 offen offset:0 sc1 lds\n"
"s_waitcnt lgkmcnt(4)\n"
"v_mfma_f32_16x16x32_bf16 v[20:23], v[100:103], v[40:43], v[20:23]\n"
"ds_read_b128 v[32:35], v2 offset:10240\n"
"v_mfma_f32_16x16x32_bf16 v[20:23], v[212:215], v[44:47], v[20:23]\n"
"v_mfma_f32_16x16x32_bf16 v[16:19], v[208:211], v[44:47], v[16:19]\n"
"ds_read_b128 v[36:39], v2 offset:24576\n"
"v_mfma_f32_16x16x32_bf16 v[16:19], v[96:99], v[40:43], v[16:19]\n"
"s_waitcnt lgkmcnt(4)\n"
"v_mfma_f32_16x16x32_bf16 v[16:19], a[72:75], v[48:51], v[16:19]\n"
"ds_read_b128 v[40:43], v2 offset:38912\n"
"buffer_load_dwordx4 v2, s[32:35], s63 offen offset:1024 sc1 lds\n"
"v_mfma_f32_16x16x32_bf16 v[16:19], a[184:187], v[52:55], v[16:19]\n"
"ds_read_b128 v[24:27], v5 offset:8192\n"
"v_mfma_f32_16x16x32_bf16 v[20:23], a[188:191], v[52:55], v[20:23]\n"
"ds_read_b128 v[44:47], v2 offset:53248\n"
"ds_read_b128 v[28:31], v5 offset:9216\n"
"v_mfma_f32_16x16x32_bf16 v[20:23], a[76:79], v[48:51], v[20:23]\n"
"s_waitcnt lgkmcnt(6)\n"
"v_mfma_f32_16x16x32_bf16 v[20:23], v[108:111], v[56:59], v[20:23]\n"
"ds_read_b128 v[48:51], v2 offset:11264\n"
"v_mfma_f32_16x16x32_bf16 v[20:23], v[220:223], v[60:63], v[20:23]\n"
"v_mfma_f32_16x16x32_bf16 v[16:19], v[216:219], v[60:63], v[16:19]\n"
"ds_read_b128 v[52:55], v2 offset:25600\n"
"buffer_load_dwordx4 v2, s[32:35], s63 offen offset:2048 sc1 lds\n"
"v_mfma_f32_16x16x32_bf16 v[16:19], v[104:107], v[56:59], v[16:19]\n"
"s_waitcnt lgkmcnt(6)\n"
"v_mfma_f32_16x16x32_bf16 v[16:19], a[80:83], v[32:35], v[16:19]\n"
"ds_read_b128 v[56:59], v2 offset:39936\n"
"v_mfma_f32_16x16x32_bf16 v[16:19], a[192:195], v[36:39], v[16:19]\n"
"v_mfma_f32_16x16x32_bf16 v[20:23], a[196:199], v[36:39], v[20:23]\n"
"ds_read_b128 v[60:63], v2 offset:54272\n"
"v_mfma_f32_16x16x32_bf16 v[20:23], a[84:87], v[32:35], v[20:23]\n"
"s_waitcnt lgkmcnt(5)\n"
"v_mfma_f32_16x16x32_bf16 v[20:23], v[116:119], v[40:43], v[20:23]\n"
"ds_read_b128 v[32:35], v2 offset:12288\n"
"buffer_load_dwordx4 v2, s[32:35], s63 offen offset:3072 sc1 lds\n"
"v_mfma_f32_16x16x32_bf16 v[20:23], v[228:231], v[44:47], v[20:23]\n"
"v_mfma_f32_16x16x32_bf16 v[16:19], v[224:227], v[44:47], v[16:19]\n"
"ds_read_b128 v[36:39], v2 offset:26624\n"
"v_mfma_f32_16x16x32_bf16 v[16:19], v[112:115], v[40:43], v[16:19]\n"
"s_waitcnt lgkmcnt(4)\n"
"v_mfma_f32_16x16x32_bf16 v[16:19], a[88:91], v[48:51], v[16:19]\n"
"ds_read_b128 v[40:43], v2 offset:40960\n"
"v_mfma_f32_16x16x32_bf16 v[16:19], a[200:203], v[52:55], v[16:19]\n"
"v_mfma_f32_16x16x32_bf16 v[20:23], a[204:207], v[52:55], v[20:23]\n"
"ds_read_b128 v[44:47], v2 offset:55296\n"
"s_add_u32 s63, s63, 4096\n"
"v_mfma_f32_16x16x32_bf16 v[20:23], a[92:95], v[48:51], v[20:23]\n"
"s_add_u32 m0, s64, 4096\n"
"s_waitcnt lgkmcnt(4)\n"
"v_mfma_f32_16x16x32_bf16 v[20:23], v[124:127], v[56:59], v[20:23]\n"
"ds_read_b128 v[48:51], v2 offset:13312\n"
"buffer_load_dwordx4 v2, s[32:35], s63 offen offset:0 sc1 lds\n"
"v_mfma_f32_16x16x32_bf16 v[20:23], v[236:239], v[60:63], v[20:23]\n"
"v_mfma_f32_16x16x32_bf16 v[16:19], v[232:235], v[60:63], v[16:19]\n"
"ds_read_b128 v[52:55], v2 offset:27648\n"
"v_mfma_f32_16x16x32_bf16 v[16:19], v[120:123], v[56:59], v[16:19]\n"
"s_waitcnt lgkmcnt(4)\n"
"v_mfma_f32_16x16x32_bf16 v[16:19], a[96:99], v[32:35], v[16:19]\n"
"ds_read_b128 v[56:59], v2 offset:41984\n"
"v_mfma_f32_16x16x32_bf16 v[16:19], a[208:211], v[36:39], v[16:19]\n"
"v_mfma_f32_16x16x32_bf16 v[20:23], a[212:215], v[36:39], v[20:23]\n"
"ds_read_b128 v[60:63], v2 offset:56320\n"
"v_mfma_f32_16x16x32_bf16 v[20:23], a[100:103], v[32:35], v[20:23]\n"
"s_waitcnt lgkmcnt(4)\n"
"v_mfma_f32_16x16x32_bf16 v[20:23], v[132:135], v[40:43], v[20:23]\n"
"s_barrier\n"
"ds_read_b128 v[32:35], v4 offset:0\n"
"v_mfma_f32_16x16x32_bf16 v[20:23], v[244:247], v[44:47], v[20:23]\n"
"v_mfma_f32_16x16x32_bf16 v[16:19], v[240:243], v[44:47], v[16:19]\n"
"ds_read_b128 v[36:39], v4 offset:14336\n"
"v_mfma_f32_16x16x32_bf16 v[16:19], v[128:131], v[40:43], v[16:19]\n"
"s_waitcnt lgkmcnt(4)\n"
"v_mfma_f32_16x16x32_bf16 v[16:19], a[104:107], v[48:51], v[16:19]\n"
"ds_read_b128 v[40:43], v4 offset:28672\n"
"v_mfma_f32_16x16x32_bf16 v[16:19], a[216:219], v[52:55], v[16:19]\n"
"v_mfma_f32_16x16x32_bf16 v[20:23], a[220:223], v[52:55], v[20:23]\n"
"ds_read_b128 v[44:47], v4 offset:43008\n"
"v_mfma_f32_16x16x32_bf16 v[20:23], a[108:111], v[48:51], v[20:23]\n"
"s_waitcnt lgkmcnt(4)\n"
"v_mfma_f32_16x16x32_bf16 v[20:23], v[140:143], v[56:59], v[20:23]\n"
"ds_read_b128 v[48:51], v4 offset:1024\n"
"v_mfma_f32_16x16x32_bf16 v[20:23], v[252:255], v[60:63], v[20:23]\n"
"v_mfma_f32_16x16x32_bf16 v[16:19], v[248:251], v[60:63], v[16:19]\n"
"ds_read_b128 v[52:55], v4 offset:15360\n"
"v_mfma_f32_16x16x32_bf16 v[16:19], v[136:139], v[56:59], v[16:19]\n"
"s_branch .L_dbl\n"
".L_dbt:\n"
"s_nop 1\n"
"s_nop 4\n"
"v_cvt_pk_bf16_f32 v12, v24, v25\n"
"v_cvt_pk_bf16_f32 v13, v26, v27\n"
"v_cvt_pk_bf16_f32 v14, v28, v29\n"
"v_cvt_pk_bf16_f32 v15, v30, v31\n"
"buffer_store_dwordx4 v[12:15], v1, s[44:47], s65 offen\n"
"s_add_u32 s65, s65, 32768\n"
"s_or_b32 s48, s28, s27\n"
"s_or_b32 s48, s48, s26\n"
"s_cbranch_scc1 .L_end\n"
"v_mov_b32 v8, s54\n"
"v_mov_b32 v9, s55\n"
"buffer_store_dwordx2 v[8:9], off, s[32:35], 0 sc1\n"
"s_branch .L_end\n"
".L_rtg0_0:\n"
"s_mov_b32 s48, 20000\n"
".L_rlg0_0:\n"
"s_sleep 8\n"
".L_rfg0_0:\n"
"buffer_load_dwordx2 v[12:13], v14, s[32:35], s66 offen sc1\n"
"s_sub_u32 s48, s48, 1\n"
"s_cbranch_scc1 .L_end\n"
"s_waitcnt vmcnt(0)\n"
"v_cmp_ne_u64 vcc, v[16:17], v[12:13]\n"
"s_cbranch_vccnz .L_rlg0_0\n"
"s_branch .L_bkg0_0\n"
".L_rtg1_0:\n"
"s_mov_b32 s48, 20000\n"
".L_rlg1_0:\n"
"s_sleep 8\n"
".L_rfg1_0:\n"
"buffer_load_dwordx2 v[12:13], v14, s[32:35], s66 offen sc1\n"
"s_sub_u32 s48, s48, 1\n"
"s_cbranch_scc1 .L_end\n"
"s_waitcnt vmcnt(0)\n"
"v_cmp_ne_u64 vcc, v[16:17], v[12:13]\n"
"s_cbranch_vccnz .L_rlg1_0\n"
"s_branch .L_bkg1_0\n"
".L_rtg0_1:\n"
"s_mov_b32 s48, 20000\n"
".L_rlg0_1:\n"
"s_sleep 8\n"
".L_rfg0_1:\n"
"buffer_load_dwordx2 v[12:13], v14, s[32:35], s66 offen sc1\n"
"s_sub_u32 s48, s48, 1\n"
"s_cbranch_scc1 .L_end\n"
"s_waitcnt vmcnt(0)\n"
"v_cmp_ne_u64 vcc, v[16:17], v[12:13]\n"
"s_cbranch_vccnz .L_rlg0_1\n"
"s_branch .L_bkg0_1\n"
".L_rtg1_1:\n"
"s_mov_b32 s48, 20000\n"
".L_rlg1_1:\n"
"s_sleep 8\n"
".L_rfg1_1:\n"
"buffer_load_dwordx2 v[12:13], v14, s[32:35], s66 offen sc1\n"
"s_sub_u32 s48, s48, 1\n"
"s_cbranch_scc1 .L_end\n"
"s_waitcnt vmcnt(0)\n"
"v_cmp_ne_u64 vcc, v[16:17], v[12:13]\n"
"s_cbranch_vccnz .L_rlg1_1\n"
"s_branch .L_bkg1_1\n"
".L_rtg0_2:\n"
"s_mov_b32 s48, 20000\n"
".L_rlg0_2:\n"
"s_sleep 8\n"
".L_rfg0_2:\n"
"buffer_load_dwordx2 v[12:13], v14, s[32:35], s66 offen sc1\n"
"s_sub_u32 s48, s48, 1\n"
"s_cbranch_scc1 .L_end\n"
"s_waitcnt vmcnt(0)\n"
"v_cmp_ne_u64 vcc, v[16:17], v[12:13]\n"
"s_cbranch_vccnz .L_rlg0_2\n"
"s_branch .L_bkg0_2\n"
".L_rtg1_2:\n"
"s_mov_b32 s48, 20000\n"
".L_rlg1_2:\n"
"s_sleep 8\n"
".L_rfg1_2:\n"
"buffer_load_dwordx2 v[12:13], v14, s[32:35], s66 offen sc1\n"
"s_sub_u32 s48, s48, 1\n"
"s_cbranch_scc1 .L_end\n"
"s_waitcnt vmcnt(0)\n"
"v_cmp_ne_u64 vcc, v[16:17], v[12:13]\n"
"s_cbranch_vccnz .L_rlg1_2\n"
"s_branch .L_bkg1_2\n"
".L_rtg0_3:\n"
"s_mov_b32 s48, 20000\n"
".L_rlg0_3:\n"
"s_sleep 8\n"
".L_rfg0_3:\n"
"buffer_load_dwordx2 v[12:13], v14, s[32:35], s66 offen sc1\n"
"s_sub_u32 s48, s48, 1\n"
"s_cbranch_scc1 .L_end\n"
"s_waitcnt vmcnt(0)\n"
"v_cmp_ne_u64 vcc, v[16:17], v[12:13]\n"
"s_cbranch_vccnz .L_rlg0_3\n"
"s_branch .L_bkg0_3\n"
".L_rtg1_3:\n"
"s_mov_b32 s48, 20000\n"
".L_rlg1_3:\n"
"s_sleep 8\n"
".L_rfg1_3:\n"
"buffer_load_dwordx2 v[12:13], v14, s[32:35], s66 offen sc1\n"
"s_sub_u32 s48, s48, 1\n"
"s_cbranch_scc1 .L_end\n"
"s_waitcnt vmcnt(0)\n"
"v_cmp_ne_u64 vcc, v[16:17], v[12:13]\n"
"s_cbranch_vccnz .L_rlg1_3\n"
"s_branch .L_bkg1_3\n"
".L_rtqa:\n"
"s_mov_b32 s48, 20000\n"
"s_branch .L_rfqa\n"
".L_rlqa:\n"
"s_sleep 8\n"
".L_rfqa:\n"
"buffer_load_dwordx2 v[12:13], v0, s[32:35], s66 offen sc1\n"
"s_sub_u32 s48, s48, 1\n"
"s_cbranch_scc1 .L_end\n"
"s_waitcnt vmcnt(0)\n"
"v_cmp_ne_u64 vcc, v[30:31], v[12:13]\n"
"s_cbranch_vccnz .L_rlqa\n"
"s_branch .L_bkqa\n"
".L_rtqb:\n"
"s_mov_b32 s48, 20000\n"
"s_branch .L_rfqb\n"
".L_rlqb:\n"
"s_sleep 8\n"
".L_rfqb:\n"
"buffer_load_dwordx2 v[12:13], v0, s[32:35], s66 offen sc1\n"
"s_sub_u32 s48, s48, 1\n"
"s_cbranch_scc1 .L_end\n"
"s_waitcnt vmcnt(0)\n"
"v_cmp_ne_u64 vcc, v[30:31], v[12:13]\n"
"s_cbranch_vccnz .L_rlqb\n"
"s_branch .L_bkqb\n"
".L_rtq0:\n"
"s_mov_b32 s48, 20000\n"
".L_rlq0:\n"
"s_sleep 8\n"
".L_rfq0:\n"
"buffer_load_dwordx2 v[12:13], v0, s[32:35], s66 offen sc1\n"
"s_sub_u32 s48, s48, 1\n"
"s_cbranch_scc1 .L_end\n"
"s_waitcnt vmcnt(0)\n"
"v_cmp_ne_u64 vcc, v[30:31], v[12:13]\n"
"s_cbranch_vccnz .L_rlq0\n"
"s_branch .L_bkq0\n"
".L_rtq1:\n"
"s_mov_b32 s48, 20000\n"
".L_rlq1:\n"
"s_sleep 8\n"
".L_rfq1:\n"
"buffer_load_dwordx2 v[12:13], v0, s[32:35], s66 offen sc1\n"
"s_sub_u32 s48, s48, 1\n"
"s_cbranch_scc1 .L_end\n"
"s_waitcnt vmcnt(0)\n"
"v_cmp_ne_u64 vcc, v[30:31], v[12:13]\n"
"s_cbranch_vccnz .L_rlq1\n"
"s_branch .L_bkq1\n"
".L_qs0:\n"
"s_cmp_lg_u32 s62, 30\n"
"s_cbranch_scc1 .L_qn4\n"
"v_mov_b32 v120, 0xff61b1e6\n"
"v_mov_b32 v121, 0xff61b1e6\n"
"v_mov_b32 v122, 0xff61b1e6\n"
"v_mov_b32 v123, 0xff61b1e6\n"
"v_cmp_ne_u32 vcc, 0xffff, v126\n"
"s_nop 1\n"
"v_cndmask_b32 v120, 0, v120, vcc\n"
"v_cmp_ne_u32 vcc, 0xffff0000, v126\n"
"s_nop 1\n"
"v_cndmask_b32 v121, 0, v121, vcc\n"
"v_cmp_ne_u32 vcc, 0xffff, v127\n"
"s_nop 1\n"
"v_cndmask_b32 v122, 0, v122, vcc\n"
"v_cmp_ne_u32 vcc, 0xffff0000, v127\n"
"s_nop 1\n"
"v_cndmask_b32 v123, 0, v123, vcc\n"
"s_nop 1\n"
"s_branch .L_qb0\n"
".L_qn4:\n"
"s_cmp_lg_u32 s62, 17\n"
"s_cbranch_scc1 .L_qn30\n"
"s_mov_b32 s72, s69\n"
"s_nop 1\n"
"s_branch .L_qb0\n"
".L_qn30:\n"
"s_cmp_lg_u32 s62, 16\n"
"s_cbranch_scc1 .L_qn32\n"
"s_mov_b32 s72, 1187840\n"
"s_nop 1\n"
"s_branch .L_qb0\n"
".L_qn32:\n"
"s_cmp_lg_u32 s62, 15\n"
"s_cbranch_scc1 .L_qn34\n"
"s_sub_u32 s68, s68, 39198720\n"
"s_nop 1\n"
"s_branch .L_qb0\n"
".L_qn34:\n"
"s_cmp_lg_u32 s62, 14\n"
"s_cbranch_scc1 .L_qn36\n"
"v_mov_b32 v120, 0xff61b1e6\n"
"v_mov_b32 v121, 0xff61b1e6\n"
"v_mov_b32 v122, 0xff61b1e6\n"
"v_mov_b32 v123, 0xff61b1e6\n"
"v_cmp_ne_u32 vcc, 0xffff, v126\n"
"s_nop 1\n"
"v_cndmask_b32 v120, 0, v120, vcc\n"
"v_cmp_ne_u32 vcc, 0xffff0000, v126\n"
"s_nop 1\n"
"v_cndmask_b32 v121, 0, v121, vcc\n"
"v_cmp_ne_u32 vcc, 0xffff, v127\n"
"s_nop 1\n"
"v_cndmask_b32 v122, 0, v122, vcc\n"
"v_cmp_ne_u32 vcc, 0xffff0000, v127\n"
"s_nop 1\n"
"v_cndmask_b32 v123, 0, v123, vcc\n"
"s_nop 1\n"
"s_branch .L_qb0\n"
".L_qn36:\n"
"s_branch .L_qb0\n"
".L_qs1:\n"
"s_cmp_lg_u32 s62, 31\n"
"s_cbranch_scc1 .L_qn3\n"
"s_cmp_lg_u32 s28, 0\n"
"s_cbranch_scc1 .L_qb1\n"
"v_mov_b32 v120, 0xff61b1e6\n"
"v_mov_b32 v121, 0xff61b1e6\n"
"v_mov_b32 v122, 0xff61b1e6\n"
"v_mov_b32 v123, 0xff61b1e6\n"
"s_nop 1\n"
"s_branch .L_qb1\n"
".L_qn3:\n"
"s_cmp_lg_u32 s62, 17\n"
"s_cbranch_scc1 .L_qn31\n"
"s_mov_b32 s72, s70\n"
"s_nop 1\n"
"s_branch .L_qb1\n"
".L_qn31:\n"
"s_cmp_lg_u32 s62, 15\n"
"s_cbranch_scc1 .L_qn35\n"
"s_add_u32 s68, s68, 38010880\n"
"s_cmp_lg_u32 s28, 0\n"
"s_cbranch_scc1 .L_qb1\n"
"v_mov_b32 v120, 0xff61b1e6\n"
"v_mov_b32 v121, 0xff61b1e6\n"
"v_mov_b32 v122, 0xff61b1e6\n"
"v_mov_b32 v123, 0xff61b1e6\n"
"s_nop 1\n"
"s_branch .L_qb1\n"
".L_qn35:\n"
"s_branch .L_qb1\n"
".L_rtw1_1:\n"
"s_mov_b32 s48, 20000\n"
".L_rlw1_1:\n"
"s_sleep 8\n"
".L_rfw1_1:\n"
"buffer_load_dwordx2 v[12:13], v14, s[32:35], s66 offen sc1\n"
"s_sub_u32 s48, s48, 1\n"
"s_cbranch_scc1 .L_end\n"
"s_waitcnt vmcnt(0)\n"
"v_cmp_ne_u64 vcc, v[16:17], v[12:13]\n"
"s_cbranch_vccnz .L_rlw1_1\n"
"s_branch .L_bkw1_1\n"
".L_rth0:\n"
"s_mov_b32 s48, 20000\n"
".L_rlh0:\n"
"s_sleep 8\n"
".L_rfh0:\n"
"buffer_load_dwordx2 v[12:13], v14, s[32:35], s66 offen sc1\n"
"s_sub_u32 s48, s48, 1\n"
"s_cbranch_scc1 .L_end\n"
"s_waitcnt vmcnt(0)\n"
"v_cmp_ne_u64 vcc, v[16:17], v[12:13]\n"
"s_cbranch_vccnz .L_rlh0\n"
"s_branch .L_bkh0\n"
".L_rth1:\n"
"s_mov_b32 s48, 20000\n"
".L_rlh1:\n"
"s_sleep 8\n"
".L_rfh1:\n"
"buffer_load_dwordx2 v[12:13], v14, s[32:35], s66 offen sc1\n"
"s_sub_u32 s48, s48, 1\n"
"s_cbranch_scc1 .L_end\n"
"s_waitcnt vmcnt(0)\n"
"v_cmp_ne_u64 vcc, v[16:17], v[12:13]\n"
"s_cbranch_vccnz .L_rlh1\n"
"s_branch .L_bkh1\n"
".L_rtl0:\n"
"s_mov_b32 s48, 20000\n"
".L_rll0:\n"
"s_sleep 8\n"
".L_rfl0:\n"
"buffer_load_dwordx2 v[6:7], v0, s[32:35], s66 offen sc1\n"
"s_sub_u32 s48, s48, 1\n"
"s_cbranch_scc1 .L_end\n"
"s_waitcnt vmcnt(0)\n"
"v_cmp_ne_u64 vcc, v[8:9], v[6:7]\n"
"s_cbranch_vccnz .L_rll0\n"
"s_branch .L_bkl0\n"
".L_rtl1:\n"
"s_mov_b32 s48, 20000\n"
".L_rll1:\n"
"s_sleep 8\n"
".L_rfl1:\n"
"buffer_load_dwordx2 v[6:7], v0, s[32:35], s66 offen sc1\n"
"s_sub_u32 s48, s48, 1\n"
"s_cbranch_scc1 .L_end\n"
"s_waitcnt vmcnt(0)\n"
"v_cmp_ne_u64 vcc, v[8:9], v[6:7]\n"
"s_cbranch_vccnz .L_rll1\n"
"s_branch .L_bkl1\n"
".L_rta0:\n"
"s_mov_b32 s48, 20000\n"
".L_rla0:\n"
"s_sleep 8\n"
".L_rfa0:\n"
"buffer_load_dwordx2 v[6:7], v0, s[32:35], s66 offen sc1\n"
"s_sub_u32 s48, s48, 1\n"
"s_cbranch_scc1 .L_end\n"
"s_waitcnt vmcnt(0)\n"
"v_cmp_ne_u64 vcc, v[8:9], v[6:7]\n"
"s_cbranch_vccnz .L_rla0\n"
"s_branch .L_bka0\n"
".L_rta1:\n"
"s_mov_b32 s48, 20000\n"
".L_rla1:\n"
"s_sleep 8\n"
".L_rfa1:\n"
"buffer_load_dwordx2 v[6:7], v0, s[32:35], s66 offen sc1\n"
"s_sub_u32 s48, s48, 1\n"
"s_cbranch_scc1 .L_end\n"
"s_waitcnt vmcnt(0)\n"
"v_cmp_ne_u64 vcc, v[8:9], v[6:7]\n"
"s_cbranch_vccnz .L_rla1\n"
"s_branch .L_bka1\n"
".L_rtb1:\n"
"s_mov_b32 s48, 20000\n"
".L_rlb1:\n"
"s_sleep 8\n"
".L_rfb1:\n"
"buffer_load_dwordx2 v[6:7], v0, s[32:35], s66 offen sc1\n"
"s_sub_u32 s48, s48, 1\n"
"s_cbranch_scc1 .L_end\n"
"s_waitcnt vmcnt(0)\n"
"v_cmp_ne_u64 vcc, v[8:9], v[6:7]\n"
"s_cbranch_vccnz .L_rlb1\n"
"s_branch .L_bkb1\n"
".L_rtb0:\n"
"s_mov_b32 s48, 20000\n"
".L_rlb0:\n"
"s_sleep 8\n"
".L_rfb0:\n"
"buffer_load_dwordx2 v[6:7], v0, s[32:35], s66 offen sc1\n"
"s_sub_u32 s48, s48, 1\n"
"s_cbranch_scc1 .L_end\n"
"s_waitcnt vmcnt(0)\n"
"v_cmp_ne_u64 vcc, v[8:9], v[6:7]\n"
"s_cbranch_vccnz .L_rlb0\n"
"s_branch .L_bkb0\n"
".L_end:\n"
"s_waitcnt vmcnt(0) lgkmcnt(0)\n"
".L_last:\n"
    : "+{v0}"(tid)
    : "{s[8:9]}"(x), "{s[10:11]}"(wq), "{s[12:13]}"(wo), "{s[14:15]}"(wg), "{s[16:17]}"(wu), "{s[18:19]}"(wd),
      "{s[20:21]}"(out), "{s[22:23]}"(ws), "{s24}"(blockIdx.x)
    : "v1", "v2", "v3", "v4", "v5", "v6", "v7", "v8", "v9", "v10", "v11", "v12", "v13", "v14", "v15", "v16", "v17", "v18", "v19", "v20", "v21", "v22", "v23", "v24", "v25", "v26", "v27", "v28", "v29", "v30", "v31", "v32", "v33", "v34", "v35", "v36", "v37", "v38", "v39", "v40", "v41", "v42", "v43", "v44", "v45", "v46", "v47", "v48", "v49", "v50", "v51", "v52", "v53", "v54", "v55", "v56", "v57", "v58", "v59", "v60", "v61", "v62", "v63", "v64", "v65", "v66", "v67", "v68", "v69", "v70", "v71", "v72", "v73", "v74", "v75", "v76", "v77", "v78", "v79", "v80", "v81", "v82", "v83", "v84", "v85", "v86", "v87", "v88", "v89", "v90", "v91", "v92", "v93", "v94", "v95", "v96", "v97", "v98", "v99", "v100", "v101", "v102", "v103", "v104", "v105", "v106", "v107", "v108", "v109", "v110", "v111", "v112", "v113", "v114", "v115", "v116", "v117", "v118", "v119", "v120", "v121", "v122", "v123", "v124", "v125", "v126", "v127", "v128", "v129", "v130", "v131", "v132", "v133", "v134", "v135", "v136", "v137", "v138", "v139", "v140", "v141", "v142", "v143", "v144", "v145", "v146", "v147", "v148", "v149", "v150", "v151", "v152", "v153", "v154", "v155", "v156", "v157", "v158", "v159", "v160", "v161", "v162", "v163", "v164", "v165", "v166", "v167", "v168", "v169", "v170", "v171", "v172", "v173", "v174", "v175", "v176", "v177", "v178", "v179", "v180", "v181", "v182", "v183", "v184", "v185", "v186", "v187", "v188", "v189", "v190", "v191", "v192", "v193", "v194", "v195", "v196", "v197", "v198", "v199", "v200", "v201", "v202", "v203", "v204", "v205", "v206", "v207", "v208", "v209", "v210", "v211", "v212", "v213", "v214", "v215", "v216", "v217", "v218", "v219", "v220", "v221", "v222", "v223", "v224", "v225", "v226", "v227", "v228", "v229", "v230", "v231", "v232", "v233", "v234", "v235", "v236", "v237", "v238", "v239", "v240", "v241", "v242", "v243", "v244", "v245", "v246", "v247", "v248", "v249", "v250", "v251", "v252", "v253", "v254", "v255", "a0", "a1", "a2", "a3", "a4", "a5", "a6", "a7", "a8", "a9", "a10", "a11", "a12", "a13", "a14", "a15", "a16", "a17", "a18", "a19", "a20", "a21", "a22", "a23", "a24", "a25", "a26", "a27", "a28", "a29", "a30", "a31", "a32", "a33", "a34", "a35", "a36", "a37", "a38", "a39", "a40", "a41", "a42", "a43", "a44", "a45", "a46", "a47", "a48", "a49", "a50", "a51", "a52", "a53", "a54", "a55", "a56", "a57", "a58", "a59", "a60", "a61", "a62", "a63", "a64", "a65", "a66", "a67", "a68", "a69", "a70", "a71", "a72", "a73", "a74", "a75", "a76", "a77", "a78", "a79", "a80", "a81", "a82", "a83", "a84", "a85", "a86", "a87", "a88", "a89", "a90", "a91", "a92", "a93", "a94", "a95", "a96", "a97", "a98", "a99", "a100", "a101", "a102", "a103", "a104", "a105", "a106", "a107", "a108", "a109", "a110", "a111", "a112", "a113", "a114", "a115", "a116", "a117", "a118", "a119", "a120", "a121", "a122", "a123", "a124", "a125", "a126", "a127", "a128", "a129", "a130", "a131", "a132", "a133", "a134", "a135", "a136", "a137", "a138", "a139", "a140", "a141", "a142", "a143", "a144", "a145", "a146", "a147", "a148", "a149", "a150", "a151", "a152", "a153", "a154", "a155", "a156", "a157", "a158", "a159", "a160", "a161", "a162", "a163", "a164", "a165", "a166", "a167", "a168", "a169", "a170", "a171", "a172", "a173", "a174", "a175", "a176", "a177", "a178", "a179", "a180", "a181", "a182", "a183", "a184", "a185", "a186", "a187", "a188", "a189", "a190", "a191", "a192", "a193", "a194", "a195", "a196", "a197", "a198", "a199", "a200", "a201", "a202", "a203", "a204", "a205", "a206", "a207", "a208", "a209", "a210", "a211", "a212", "a213", "a214", "a215", "a216", "a217", "a218", "a219", "a220", "a221", "a222", "a223", "a224", "a225", "a226", "a227", "a228", "a229", "a230", "a231", "a232", "a233", "a234", "a235", "a236", "a237", "a238", "a239", "a240", "a241", "a242", "a243", "a244", "a245", "a246", "a247", "a248", "a249", "a250", "a251", "a252", "a253", "a254", "a255", "s25", "s26", "s27", "s28", "s29", "s30", "s31", "s32", "s33", "s34", "s35", "s36", "s37", "s38", "s39", "s40", "s41", "s42", "s43", "s44", "s45", "s46", "s47", "s48", "s49", "s50", "s51", "s52", "s53", "s54", "s55", "s56", "s57", "s58", "s59", "s60", "s61", "s62", "s63", "s64", "s65", "s66", "s67", "s68", "s69", "s70", "s71", "s72", "s73", "s74", "s75", "s76", "s77", "s78", "s79", "s80", "s81", "s82", "s83", "s84", "s85", "s86", "s87", "s88", "s89", "s90", "s91", "s92", "s93", "s94", "s95", "s96", "s97", "s98", "s99", "vcc", "scc", "m0", "memory");
}

extern "C" void kernel_launch(void* const* d_in, const int* in_sizes, int n_in, void* d_out, int out_size,
                              void* d_ws, size_t ws_size, hipStream_t stream) {
    constexpr int smem = 163840;
    (void)hipFuncSetAttribute((const void*)body_k, hipFuncAttributeMaxDynamicSharedMemorySize, smem);
    body_k<<<256, 256, smem, stream>>>(d_in[0], d_in[1], d_in[2], d_in[4], d_in[5], d_in[6], d_out, d_ws);
}
```

```cpp
#include <hip/hip_runtime.h>
#pragma clang diagnostic ignored "-Winline-asm"
#define W_XI 4612096
#define W_O 4648960
#define W_XA 4681728
#define W_H 4718592
#define W_P 4833280
#define W_EB 296960
#define W_MAP 1
extern "C" __global__ __launch_bounds__(256, 1) void body_k(const void* x, const void* wq, const void* wo, const void* wg, const void* wu,
                                                            const void* wd, void* out, void* ws) {
    unsigned tid = threadIdx.x;
    asm volatile(
"s_load_dwordx2 s[52:53], s[22:23], 0x0\n"
"v_lshrrev_b32 v3, 6, v0\n"
"v_and_b32 v1, 63, v0\n"
"v_readfirstlane_b32 s26, v3\n"
"v_lshlrev_b32 v2, 4, v1\n"
"s_add_u32 s32, s22, 0x0\n"
"s_addc_u32 s33, s23, 0x0\n"
"s_and_b32 s33, s33, 0xffff\n"
"s_mov_b32 s34, 0x504c000\n"
"s_mov_b32 s35, 0x20000\n"
"s_mov_b32 s73, 0\n"
"s_lshr_b32 s49, s24, 3\n"
"s_cmp_lt_u32 s49, 16\n"
"s_cbranch_scc1 .L_nc\n"
"s_bfe_u32 s28, s24, 0x20001\n"
"s_and_b32 s27, s49, 7\n"
"s_bfe_u32 s29, s49, 0x10003\n"
"s_mov_b32 s62, 0\n"
"s_mov_b32 s74, 1\n"
"s_bitcmp1_b32 s24, 0\n"
"s_cbranch_scc0 .L_nt\n"
"s_getpc_b64 s[40:41]\n"
".L_ct16:\n"
"s_and_b32 s41, s41, 0xffff\n"
"s_mov_b32 s42, .L_last-.L_ct16\n"
"s_mov_b32 s43, 0x20000\n"
"s_sub_u32 s48, s49, 16\n"
"s_lshl_b32 s48, s48, 8\n"
"v_add_lshl_u32 v5, v0, s48, 7\n"
"buffer_load_dword v10, v5, s[40:43], 0 offen\n"
"s_add_u32 s36, s10, 0x0\n"
"s_addc_u32 s37, s11, 0x0\n"
"s_and_b32 s37, s37, 0xffff\n"
"s_mov_b32 s38, 0x600000\n"
"s_mov_b32 s39, 0x20000\n"
"s_lshl_b32 s48, s49, 8\n"
"s_sub_u32 s48, s48, 4096\n"
"v_add_u32 v4, s48, v0\n"
"s_lshl_b32 s48, s28, 9\n"
"v_mul_u32_u24 v5, 43691, v4\n"
"v_lshrrev_b32 v5, 19, v5\n"
"v_mul_u32_u24 v6, 12, v5\n"
"v_sub_u32 v6, v4, v6\n"
"v_mul_u32_u24 v5, 48, v5\n"
"v_lshrrev_b32 v7, 2, v6\n"
"v_mul_u32_u24 v7, 12, v7\n"
"v_add3_u32 v5, v5, v6, v7\n"
"v_lshlrev_b32 v5, 7, v5\n"
"v_add_u32 v4, 4096, v4\n"
"buffer_load_dword v10, v5, s[36:39], s48 offen\n"
"v_mul_u32_u24 v5, 43691, v4\n"
"v_lshrrev_b32 v5, 19, v5\n"
"v_mul_u32_u24 v6, 12, v5\n"
"v_sub_u32 v6, v4, v6\n"
"v_mul_u32_u24 v5, 48, v5\n"
"v_lshrrev_b32 v7, 2, v6\n"
"v_mul_u32_u24 v7, 12, v7\n"
"v_add3_u32 v5, v5, v6, v7\n"
"v_lshlrev_b32 v5, 7, v5\n"
"v_add_u32 v4, 4096, v4\n"
"buffer_load_dword v10, v5, s[36:39], s48 offen\n"
"v_mul_u32_u24 v5, 43691, v4\n"
"v_lshrrev_b32 v5, 19, v5\n"
"v_mul_u32_u24 v6, 12, v5\n"
"v_sub_u32 v6, v4, v6\n"
"v_mul_u32_u24 v5, 48, v5\n"
"v_lshrrev_b32 v7, 2, v6\n"
"v_mul_u32_u24 v7, 12, v7\n"
"v_add3_u32 v5, v5, v6, v7\n"
"v_lshlrev_b32 v5, 7, v5\n"
"v_add_u32 v4, 4096, v4\n"
"buffer_load_dword v10, v5, s[36:39], s48 offen\n"
"s_mov_b32 s62, 1\n"
"s_mov_b32 s74, 4\n"
".L_nt:\n"
"s_lshl2_add_u32 s50, s27, s26\n"
"s_mul_i32 s70, s50, 1152\n"
"v_and_b32 v20, 15, v1\n"
"v_lshlrev_b32 v20, 11, v20\n"
"v_and_b32 v21, 48, v1\n"
"s_lshl_b32 s48, s50, 6\n"
"v_add3_u32 v20, v20, v21, s48\n"
"s_add_u32 s36, s8, 0x0\n"
"s_addc_u32 s37, s9, 0x0\n"
"s_and_b32 s37, s37, 0xffff\n"
"s_mov_b32 s38, 0x400000\n"
"s_mov_b32 s39, 0x20000\n"
"s_lshl2_add_u32 s48, s28, s62\n"
"s_lshl_b32 s48, s48, 3\n"
"s_sub_u32 s48, s48, s29\n"
"s_add_u32 s48, s48, 127\n"
"s_lshl_b32 s66, s27, 7\n"
"s_add_u32 s66, s66, 286720\n"
"s_sub_u32 s66, s66, s70\n"
"s_add_u32 s49, s28, 3\n"
"s_and_b32 s49, s49, 3\n"
"s_mul_i32 s49, s49, 296960\n"
"s_add_u32 s69, s70, s49\n"
"s_add_u32 s69, s69, 41435136\n"
"s_mul_i32 s49, s28, 296960\n"
"s_add_u32 s70, s70, s49\n"
"s_and_b32 s51, s50, 6\n"
"s_lshl_b32 s51, s51, 1\n"
"v_add_u32 v3, s51, v1\n"
"v_bfi_b32 v3, 15, v3, v1\n"
"v_lshlrev_b32 v3, 2, v3\n"
"v_cmp_gt_u32 vcc, 32, v1\n"
"v_mov_b32 v21, 0x7fff0000\n"
"s_nop 0\n"
"v_cndmask_b32 v21, v21, v3, vcc\n"
"s_mul_i32 s49, s29, 1187840\n"
"s_sub_u32 s71, s70, s49\n"
"s_add_u32 s71, s71, 4612096\n"
"v_mul_u32_u24 v24, 2375680, v1\n"
"v_add_u32 v24, s71, v24\n"
"s_cmp_eq_u32 s29, 0\n"
"s_cselect_b32 s68, s71, s69\n"
"v_writelane_b32 v24, s68, 0\n"
"s_mul_i32 s49, s62, 9502720\n"
"s_add_u32 s71, s71, s49\n"
"s_cmp_eq_u32 s62, 0\n"
"s_cselect_b32 s68, s68, s71\n"
"s_add_u32 s71, s71, 2375680\n"
"v_lshrrev_b32 v22, 2, v0\n"
"v_mov_b32 v23, 0x3f803f80\n"
".L_dc:\n"
"s_and_b32 s49, s48, 127\n"
"s_lshl_b32 s49, s49, 15\n"
"s_add_u32 s48, s48, 2\n"
"buffer_load_dwordx4 v[32:35], v20, s[36:39], s49 offen\n"
"s_and_b32 s49, s48, 127\n"
"s_lshl_b32 s49, s49, 15\n"
"s_add_u32 s48, s48, 2\n"
"buffer_load_dwordx4 v[36:39], v20, s[36:39], s49 offen\n"
"s_and_b32 s49, s48, 127\n"
"s_lshl_b32 s49, s49, 15\n"
"s_add_u32 s48, s48, 2\n"
"buffer_load_dwordx4 v[40:43], v20, s[36:39], s49 offen\n"
"s_and_b32 s49, s48, 127\n"
"s_lshl_b32 s49, s49, 15\n"
"s_add_u32 s48, s48, 2\n"
"buffer_load_dwordx4 v[44:47], v20, s[36:39], s49 offen\n"
"v_cmp_eq_u32 vcc, s62, v22\n"
"v_mov_b32 v25, 0x7fff0000\n"
"s_add_u32 s62, s62, 1\n"
"v_cndmask_b32 v25, v25, v24, vcc\n"
"s_waitcnt lgkmcnt(0)\n"
"s_add_u32 s54, s52, 0x7f4a7c15\n"
"s_addc_u32 s55, s53, 0x9e3779b9\n"
"v_mov_b32 v8, s54\n"
"v_mov_b32 v9, s55\n"
"s_waitcnt vmcnt(0)\n"
"v_dot2_f32_bf16 v48, v32, v23, 0\n"
"v_dot2_f32_bf16 v49, v32, v32, 0\n"
"v_dot2_f32_bf16 v50, v36, v23, 0\n"
"v_dot2_f32_bf16 v51, v36, v36, 0\n"
"v_dot2_f32_bf16 v52, v40, v23, 0\n"
"v_dot2_f32_bf16 v53, v40, v40, 0\n"
"v_dot2_f32_bf16 v54, v44, v23, 0\n"
"v_dot2_f32_bf16 v55, v44, v44, 0\n"
"v_dot2_f32_bf16 v48, v33, v23, v48\n"
"v_dot2_f32_bf16 v49, v33, v33, v49\n"
"v_dot2_f32_bf16 v50, v37, v23, v50\n"
"v_dot2_f32_bf16 v51, v37, v37, v51\n"
"v_dot2_f32_bf16 v52, v41, v23, v52\n"
"v_dot2_f32_bf16 v53, v41, v41, v53\n"
"v_dot2_f32_bf16 v54, v45, v23, v54\n"
"v_dot2_f32_bf16 v55, v45, v45, v55\n"
"v_dot2_f32_bf16 v48, v34, v23, v48\n"
"v_dot2_f32_bf16 v49, v34, v34, v49\n"
"v_dot2_f32_bf16 v50, v38, v23, v50\n"
"v_dot2_f32_bf16 v51, v38, v38, v51\n"
"v_dot2_f32_bf16 v52, v42, v23, v52\n"
"v_dot2_f32_bf16 v53, v42, v42, v53\n"
"v_dot2_f32_bf16 v54, v46, v23, v54\n"
"v_dot2_f32_bf16 v55, v46, v46, v55\n"
"v_dot2_f32_bf16 v48, v35, v23, v48\n"
"v_dot2_f32_bf16 v49, v35, v35, v49\n"
"v_dot2_f32_bf16 v50, v39, v23, v50\n"
"v_dot2_f32_bf16 v51, v39, v39, v51\n"
"v_dot2_f32_bf16 v52, v43, v23, v52\n"
"v_dot2_f32_bf16 v53, v43, v43, v53\n"
"v_dot2_f32_bf16 v54, v47, v23, v54\n"
"v_dot2_f32_bf16 v55, v47, v47, v55\n"
"v_permlane16_swap_b32 v48, v49\n"
"v_permlane16_swap_b32 v50, v51\n"
"v_permlane16_swap_b32 v52, v53\n"
"v_permlane16_swap_b32 v54, v55\n"
"v_add_f32 v48, v48, v49\n"
"v_add_f32 v50, v50, v51\n"
"v_add_f32 v52, v52, v53\n"
"v_add_f32 v54, v54, v55\n"
"v_permlane32_swap_b32 v48, v49\n"
"v_permlane32_swap_b32 v50, v51\n"
"v_permlane32_swap_b32 v52, v53\n"
"v_permlane32_swap_b32 v54, v55\n"
"v_add_f32 v48, v48, v49\n"
"v_add_f32 v50, v50, v51\n"
"v_add_f32 v52, v52, v53\n"
"v_add_f32 v54, v54, v55\n"
"buffer_store_dwordx4 v[32:35], v2, s[32:35], s68 offen sc1\n"
"buffer_store_dword v48, v21, s[32:35], s68 offen offset:1024 sc1\n"
"s_mov_b32 s68, s71\n"
"s_add_u32 s71, s71, 2375680\n"
"buffer_store_dwordx4 v[36:39], v2, s[32:35], s68 offen sc1\n"
"buffer_store_dword v50, v21, s[32:35], s68 offen offset:1024 sc1\n"
"s_mov_b32 s68, s71\n"
"s_add_u32 s71, s71, 2375680\n"
"buffer_store_dwordx4 v[40:43], v2, s[32:35], s68 offen sc1\n"
"buffer_store_dword v52, v21, s[32:35], s68 offen offset:1024 sc1\n"
"s_mov_b32 s68, s71\n"
"s_add_u32 s71, s71, 2375680\n"
"buffer_store_dwordx4 v[44:47], v2, s[32:35], s68 offen sc1\n"
"buffer_store_dword v54, v21, s[32:35], s68 offen offset:1024 sc1\n"
"s_mov_b32 s68, s71\n"
"s_add_u32 s71, s71, 2375680\n"
"s_waitcnt vmcnt(0)\n"
"s_barrier\n"
"buffer_store_dwordx2 v[8:9], v25, s[32:35], s66 offen sc1\n"
"s_cmp_lt_u32 s62, s74\n"
"s_cbranch_scc1 .L_dc\n"
"s_bitcmp1_b32 s24, 0\n"
"s_cbranch_scc1 .L_d\n"
"s_lshr_b32 s49, s24, 3\n"
".L_nc:\n"
"s_bfe_u32 s50, s24, 0x20001\n"
"s_and_b32 s27, s49, 3\n"
"s_lshl2_add_u32 s27, s50, s27\n"
"s_lshr_b32 s28, s49, 2\n"
"s_bitcmp1_b32 s24, 0\n"
"s_cbranch_scc1 .L_q\n"
"s_and_b32 s27, s49, 1\n"
"s_lshl1_add_u32 s27, s50, s27\n"
"s_lshr_b32 s28, s49, 1\n"
"s_cmp_lt_u32 s49, 4\n"
"s_cbranch_scc1 .L_w\n"
"s_cmp_ge_u32 s49, 16\n"
"s_cbranch_scc1 .L_ns\n"
"s_getpc_b64 s[40:41]\n"
".L_ct5:\n"
"s_and_b32 s41, s41, 0xffff\n"
"s_mov_b32 s42, .L_last-.L_ct5\n"
"s_mov_b32 s43, 0x20000\n"
"s_sub_u32 s48, s49, 5\n"
"s_lshl_b32 s48, s48, 8\n"
"v_add_lshl_u32 v5, v0, s48, 7\n"
"buffer_load_dword v120, v5, s[40:43], 0 offen\n"
"s_lshl2_add_u32 s48, s49, s50\n"
"s_lshl_b32 s48, s48, 8\n"
"s_sub_u32 s48, s48, 4096\n"
"v_add_u32 v4, s48, v0\n"
"v_mul_u32_u24 v5, 52429, v4\n"
"v_lshrrev_b32 v5, 22, v5\n"
"v_mul_u32_u24 v5, 2240, v5\n"
"v_add_u32 v5, v5, v4\n"
"v_lshlrev_b32 v5, 7, v5\n"
"v_add_u32 v5, 4898816, v5\n"
"v_add_u32 v4, 12288, v4\n"
"buffer_load_dword v120, v5, s[32:35], 0 offen sc1\n"
"v_mul_u32_u24 v5, 52429, v4\n"
"v_lshrrev_b32 v5, 22, v5\n"
"v_mul_u32_u24 v5, 2240, v5\n"
"v_add_u32 v5, v5, v4\n"
"v_lshlrev_b32 v5, 7, v5\n"
"v_add_u32 v5, 4898816, v5\n"
"v_add_u32 v4, 12288, v4\n"
"buffer_load_dword v120, v5, s[32:35], 0 offen sc1\n"
"s_sleep 30\n"
".L_ns:\n"
"s_lshr_b32 s27, s50, 1\n"
"s_and_b32 s28, s50, 1\n"
"s_cmp_eq_u32 s49, 4\n"
"s_cbranch_scc1 .L_h\n"
"s_sub_u32 s27, s49, 14\n"
"s_lshr_b32 s27, s27, 1\n"
"s_and_b32 s28, s49, 1\n"
"s_add_u32 s28, s28, 1\n"
"s_sub_u32 s48, s49, 5\n"
"s_cmp_lt_u32 s49, 14\n"
"s_cselect_b32 s27, s48, s27\n"
"s_cselect_b32 s28, 0, s28\n"
"s_mul_i32 s50, s50, 9\n"
"s_add_u32 s27, s27, s50\n"
".L_g:\n"
"v_mov_b32 v16, 0x3f803f80\n"
"v_mov_b32 v17, 0x3f803f80\n"
"v_mov_b32 v18, 0x3f803f80\n"
"v_mov_b32 v19, 0x3f803f80\n"
"s_add_u32 s36, s14, 0x0\n"
"s_addc_u32 s37, s15, 0x0\n"
"s_and_b32 s37, s37, 0xffff\n"
"s_mov_b32 s38, 0x700000\n"
"s_mov_b32 s39, 0x20000\n"
"s_add_u32 s40, s16, 0x0\n"
"s_addc_u32 s41, s17, 0x0\n"
"s_and_b32 s41, s41, 0xffff\n"
"s_mov_b32 s42, 0x700000\n"
"s_mov_b32 s43, 0x20000\n"
"v_add_u32 v8, 0, v1\n"
"v_mul_u32_u24 v9, 5462, v8\n"
"v_lshrrev_b32 v9, 16, v9\n"
"v_mul_u32_u24 v10, 12, v9\n"
"v_sub_u32 v10, v8, v10\n"
"v_mul_u32_u24 v9, 7168, v9\n"
"v_lshl_add_u32 v9, v10, 4, v9\n"
"v_subrev_u32 v4, 0, v9\n"
"v_add_u32 v8, 64, v1\n"
"v_mul_u32_u24 v9, 5462, v8\n"
"v_lshrrev_b32 v9, 16, v9\n"
"v_mul_u32_u24 v10, 12, v9\n"
"v_sub_u32 v10, v8, v10\n"
"v_mul_u32_u24 v9, 7168, v9\n"
"v_lshl_add_u32 v9, v10, 4, v9\n"
"v_subrev_u32 v5, 1024, v9\n"
"v_add_u32 v8, 128, v1\n"
"v_mul_u32_u24 v9, 5462, v8\n"
"v_lshrrev_b32 v9, 16, v9\n"
"v_mul_u32_u24 v10, 12, v9\n"
"v_sub_u32 v10, v8, v10\n"
"v_mul_u32_u24 v9, 7168, v9\n"
"v_lshl_add_u32 v9, v10, 4, v9\n"
"v_subrev_u32 v6, 2048, v9\n"
"s_mul_i32 s48, s26, 114688\n"
"s_mul_i32 s49, s27, 192\n"
"s_add_u32 s63, s48, s49\n"
"s_mul_i32 s64, s26, 3072\n"
"s_add_u32 m0, s64, 0\n"
"s_nop 0\n"
"buffer_load_dwordx4 v4, s[36:39], s63 offen offset:0 lds\n"
"buffer_load_dwordx4 v5, s[36:39], s63 offen offset:1024 lds\n"
"buffer_load_dwordx4 v6, s[36:39], s63 offen offset:2048 lds\n"
"s_add_u32 m0, s64, 12288\n"
"s_nop 0\n"
"buffer_load_dwordx4 v4, s[40:43], s63 offen offset:0 lds\n"
"buffer_load_dwordx4 v5, s[40:43], s63 offen offset:1024 lds\n"
"buffer_load_dwordx4 v6, s[40:43], s63 offen offset:2048 lds\n"
"s_add_u32 s63, s63, 458752\n"
"s_add_u32 m0, s64, 24576\n"
"s_nop 0\n"
"buffer_load_dwordx4 v4, s[36:39], s63 offen offset:0 lds\n"
"buffer_load_dwordx4 v5, s[36:39], s63 offen offset:1024 lds\n"
"buffer_load_dwordx4 v6, s[36:39], s63 offen offset:2048 lds\n"
"s_add_u32 m0, s64, 36864\n"
"s_nop 0\n"
"buffer_load_dwordx4 v4, s[40:43], s63 offen offset:0 lds\n"
"buffer_load_dwordx4 v5, s[40:43], s63 offen offset:1024 lds\n"
"buffer_load_dwordx4 v6, s[40:43], s63 offen offset:2048 lds\n"
"s_add_u32 s63, s63, 458752\n"
"s_add_u32 m0, s64, 49152\n"
"s_nop 0\n"
"buffer_load_dwordx4 v4, s[36:39], s63 offen offset:0 lds\n"
"buffer_load_dwordx4 v5, s[36:39], s63 offen offset:1024 lds\n"
"buffer_load_dwordx4 v6, s[36:39], s63 offen offset:2048 lds\n"
"s_add_u32 m0, s64, 61440\n"
"s_nop 0\n"
"buffer_load_dwordx4 v4, s[40:43], s63 offen offset:0 lds\n"
"buffer_load_dwordx4 v5, s[40:43], s63 offen offset:1024 lds\n"
"buffer_load_dwordx4 v6, s[40:43], s63 offen offset:2048 lds\n"
"s_add_u32 s63, s63, 458752\n"
"s_add_u32 m0, s64, 73728\n"
"s_nop 0\n"
"buffer_load_dwordx4 v4, s[36:39], s63 offen offset:0 lds\n"
"buffer_load_dwordx4 v5, s[36:39], s63 offen offset:1024 lds\n"
"buffer_load_dwordx4 v6, s[36:39], s63 offen offset:2048 lds\n"
"s_add_u32 m0, s64, 86016\n"
"s_nop 0\n"
"buffer_load_dwordx4 v4, s[40:43], s63 offen offset:0 lds\n"
"buffer_load_dwordx4 v5, s[40:43], s63 offen offset:1024 lds\n"
"buffer_load_dwordx4 v6, s[40:43], s63 offen offset:2048 lds\n"
"s_add_u32 s63, s63, 458752\n"
"s_add_u32 m0, s64, 98304\n"
"s_nop 0\n"
"buffer_load_dwordx4 v4, s[36:39], s63 offen offset:0 lds\n"
"buffer_load_dwordx4 v5, s[36:39], s63 offen offset:1024 lds\n"
"buffer_load_dwordx4 v6, s[36:39], s63 offen offset:2048 lds\n"
"s_add_u32 m0, s64, 110592\n"
"s_nop 0\n"
"buffer_load_dwordx4 v4, s[40:43], s63 offen offset:0 lds\n"
"buffer_load_dwordx4 v5, s[40:43], s63 offen offset:1024 lds\n"
"buffer_load_dwordx4 v6, s[40:43], s63 offen offset:2048 lds\n"
"s_add_u32 s63, s63, 458752\n"
"v_and_b32 v12, 15, v1\n"
"v_lshrrev_b32 v13, 4, v1\n"
"v_lshrrev_b32 v14, 2, v12\n"
"v_and_b32 v15, 3, v12\n"
"v_lshl_add_u32 v13, v13, 3, v14\n"
"v_mul_u32_u24 v13, 192, v13\n"
"s_mul_i32 s48, s26, 48\n"
"v_add_u32 v13, s48, v13\n"
"v_and_b32 v14, 1, v15\n"
"v_lshrrev_b32 v15, 1, v15\n"
"v_lshl_add_u32 v13, v14, 3, v13\n"
"v_lshl_add_u32 v8, v15, 5, v13\n"
"v_mul_u32_u24 v15, 12288, v15\n"
"v_add3_u32 v9, v13, v15, 16\n"
"v_add_u32 v10, 49152, v8\n"
"v_add_u32 v11, 49152, v9\n"
"v_add_u32 v12, 98304, v8\n"
"v_add_u32 v13, 98304, v9\n"
"s_waitcnt vmcnt(24)\n"
"s_barrier\n"
"s_add_u32 m0, s64, 122880\n"
"s_nop 0\n"
"buffer_load_dwordx4 v4, s[36:39], s63 offen offset:0 lds\n"
"buffer_load_dwordx4 v5, s[36:39], s63 offen offset:1024 lds\n"
"buffer_load_dwordx4 v6, s[36:39], s63 offen offset:2048 lds\n"
"s_add_u32 m0, s64, 135168\n"
"s_nop 0\n"
"buffer_load_dwordx4 v4, s[40:43], s63 offen offset:0 lds\n"
"buffer_load_dwordx4 v5, s[40:43], s63 offen offset:1024 lds\n"
"buffer_load_dwordx4 v6, s[40:43], s63 offen offset:2048 lds\n"
"s_add_u32 s63, s63, 458752\n"
"ds_read_b64_tr_b16 a[0:1], v8 offset:0\n"
"ds_read_b64_tr_b16 a[2:3], v8 offset:768\n"
"ds_read_b64_tr_b16 a[4:5], v8 offset:12288\n"
"ds_read_b64_tr_b16 a[6:7], v8 offset:13056\n"
"ds_read_b64_tr_b16 a[8:9], v9 offset:0\n"
"ds_read_b64_tr_b16 a[10:11], v9 offset:768\n"
"ds_read_b64_tr_b16 a[12:13], v8 offset:6144\n"
"ds_read_b64_tr_b16 a[14:15], v8 offset:6912\n"
"ds_read_b64_tr_b16 a[16:17], v8 offset:18432\n"
"ds_read_b64_tr_b16 a[18:19], v8 offset:19200\n"
"ds_read_b64_tr_b16 a[20:21], v9 offset:6144\n"
"ds_read_b64_tr_b16 a[22:23], v9 offset:6912\n"
"s_waitcnt lgkmcnt(0)\n"
"v_mfma_f32_16x16x32_bf16 v[20:23], a[0:3], v[16:19], 0\n"
"v_mfma_f32_16x16x32_bf16 v[24:27], a[4:7], v[16:19], 0\n"
"v_mfma_f32_16x16x32_bf16 v[28:31], a[8:11], v[16:19], 0\n"
"v_mfma_f32_16x16x32_bf16 v[20:23], a[12:15], v[16:19], v[20:23]\n"
"v_mfma_f32_16x16x32_bf16 v[24:27], a[16:19], v[16:19], v[24:27]\n"
"v_mfma_f32_16x16x32_bf16 v[28:31], a[20:23], v[16:19], v[28:31]\n"
"s_waitcnt vmcnt(24)\n"
"s_barrier\n"
"s_add_u32 m0, s64, 0\n"
"s_nop 0\n"
"buffer_load_dwordx4 v4, s[36:39], s63 offen offset:0 lds\n"
"buffer_load_dwordx4 v5, s[36:39], s63 offen offset:1024 lds\n"
"buffer_load_dwordx4 v6, s[36:39], s63 offen offset:2048 lds\n"
"s_add_u32 m0, s64, 12288\n"
"s_nop 0\n"
"buffer_load_dwordx4 v4, s[40:43], s63 offen offset:0 lds\n"
"buffer_load_dwordx4 v5, s[40:43], s63 offen offset:1024 lds\n"
"buffer_load_dwordx4 v6, s[40:43], s63 offen offset:2048 lds\n"
"s_add_u32 s63, s63, 458752\n"
"ds_read_b64_tr_b16 a[24:25], v8 offset:24576\n"
"ds_read_b64_tr_b16 a[26:27], v8 offset:25344\n"
"ds_read_b64_tr_b16 a[28:29], v8 offset:36864\n"
"ds_read_b64_tr_b16 a[30:31], v8 offset:37632\n"
"ds_read_b64_tr_b16 a[32:33], v9 offset:24576\n"
"ds_read_b64_tr_b16 a[34:35], v9 offset:25344\n"
"ds_read_b64_tr_b16 a[36:37], v8 offset:30720\n"
"ds_read_b64_tr_b16 a[38:39], v8 offset:31488\n"
"ds_read_b64_tr_b16 a[40:41], v8 offset:43008\n"
"ds_read_b64_tr_b16 a[42:43], v8 offset:43776\n"
"ds_read_b64_tr_b16 a[44:45], v9 offset:30720\n"
"ds_read_b64_tr_b16 a[46:47], v9 offset:31488\n"
"s_waitcnt lgkmcnt(0)\n"
"v_mfma_f32_16x16x32_bf16 v[20:23], a[24:27], v[16:19], v[20:23]\n"
"v_mfma_f32_16x16x32_bf16 v[24:27], a[28:31], v[16:19], v[24:27]\n"
"v_mfma_f32_16x16x32_bf16 v[28:31], a[32:35], v[16:19], v[28:31]\n"
"v_mfma_f32_16x16x32_bf16 v[20:23], a[36:39], v[16:19], v[20:23]\n"
"v_mfma_f32_16x16x32_bf16 v[24:27], a[40:43], v[16:19], v[24:27]\n"
"v_mfma_f32_16x16x32_bf16 v[28:31], a[44:47], v[16:19], v[28:31]\n"
"s_waitcnt vmcnt(24)\n"
"s_barrier\n"
"s_add_u32 m0, s64, 24576\n"
"s_nop 0\n"
"buffer_load_dwordx4 v4, s[36:39], s63 offen offset:0 lds\n"
"buffer_load_dwordx4 v5, s[36:39], s63 offen offset:1024 lds\n"
"buffer_load_dwordx4 v6, s[36:39], s63 offen offset:2048 lds\n"
"s_add_u32 m0, s64, 36864\n"
"s_nop 0\n"
"buffer_load_dwordx4 v4, s[40:43], s63 offen offset:0 lds\n"
"buffer_load_dwordx4 v5, s[40:43], s63 offen offset:1024 lds\n"
"buffer_load_dwordx4 v6, s[40:43], s63 offen offset:2048 lds\n"
"s_add_u32 s63, s63, 458752\n"
"ds_read_b64_tr_b16 a[48:49], v10 offset:0\n"
"ds_read_b64_tr_b16 a[50:51], v10 offset:768\n"
"ds_read_b64_tr_b16 a[52:53], v10 offset:12288\n"
"ds_read_b64_tr_b16 a[54:55], v10 offset:13056\n"
"ds_read_b64_tr_b16 a[56:57], v11 offset:0\n"
"ds_read_b64_tr_b16 a[58:59], v11 offset:768\n"
"ds_read_b64_tr_b16 a[60:61], v10 offset:6144\n"
"ds_read_b64_tr_b16 a[62:63], v10 offset:6912\n"
"ds_read_b64_tr_b16 a[64:65], v10 offset:18432\n"
"ds_read_b64_tr_b16 a[66:67], v10 offset:19200\n"
"ds_read_b64_tr_b16 a[68:69], v11 offset:6144\n"
"ds_read_b64_tr_b16 a[70:71], v11 offset:6912\n"
"s_waitcnt lgkmcnt(0)\n"
"v_mfma_f32_16x16x32_bf16 v[20:23], a[48:51], v[16:19], v[20:23]\n"
"v_mfma_f32_16x16x32_bf16 v[24:27], a[52:55], v[16:19], v[24:27]\n"
"v_mfma_f32_16x16x32_bf16 v[28:31], a[56:59], v[16:19], v[28:31]\n"
"v_mfma_f32_16x16x32_bf16 v[20:23], a[60:63], v[16:19], v[20:23]\n"
"v_mfma_f32_16x16x32_bf16 v[24:27], a[64:67], v[16:19], v[24:27]\n"
"v_mfma_f32_16x16x32_bf16 v[28:31], a[68:71], v[16:19], v[28:31]\n"
"s_waitcnt vmcnt(24)\n"
"s_barrier\n"
"s_add_u32 m0, s64, 49152\n"
"s_nop 0\n"
"buffer_load_dwordx4 v4, s[36:39], s63 offen offset:0 lds\n"
"buffer_load_dwordx4 v5, s[36:39], s63 offen offset:1024 lds\n"
"buffer_load_dwordx4 v6, s[36:39], s63 offen offset:2048 lds\n"
"s_add_u32 m0, s64, 61440\n"
"s_nop 0\n"
"buffer_load_dwordx4 v4, s[40:43], s63 offen offset:0 lds\n"
"buffer_load_dwordx4 v5, s[40:43], s63 offen offset:1024 lds\n"
"buffer_load_dwordx4 v6, s[40:43], s63 offen offset:2048 lds\n"
"s_add_u32 s63, s63, 458752\n"
"ds_read_b64_tr_b16 a[72:73], v10 offset:24576\n"
"ds_read_b64_tr_b16 a[74:75], v10 offset:25344\n"
"ds_read_b64_tr_b16 a[76:77], v10 offset:36864\n"
"ds_read_b64_tr_b16 a[78:79], v10 offset:37632\n"
"ds_read_b64_tr_b16 a[80:81], v11 offset:24576\n"
"ds_read_b64_tr_b16 a[82:83], v11 offset:25344\n"
"ds_read_b64_tr_b16 a[84:85], v10 offset:30720\n"
"ds_read_b64_tr_b16 a[86:87], v10 offset:31488\n"
"ds_read_b64_tr_b16 a[88:89], v10 offset:43008\n"
"ds_read_b64_tr_b16 a[90:91], v10 offset:43776\n"
"ds_read_b64_tr_b16 a[92:93], v11 offset:30720\n"
"ds_read_b64_tr_b16 a[94:95], v11 offset:31488\n"
"s_waitcnt lgkmcnt(0)\n"
"v_mfma_f32_16x16x32_bf16 v[20:23], a[72:75], v[16:19], v[20:23]\n"
"v_mfma_f32_16x16x32_bf16 v[24:27], a[76:79], v[16:19], v[24:27]\n"
"v_mfma_f32_16x16x32_bf16 v[28:31], a[80:83], v[16:19], v[28:31]\n"
"v_mfma_f32_16x16x32_bf16 v[20:23], a[84:87], v[16:19], v[20:23]\n"
"v_mfma_f32_16x16x32_bf16 v[24:27], a[88:91], v[16:19], v[24:27]\n"
"v_mfma_f32_16x16x32_bf16 v[28:31], a[92:95], v[16:19], v[28:31]\n"
"s_waitcnt vmcnt(24)\n"
"s_barrier\n"
"s_add_u32 m0, s64, 73728\n"
"s_nop 0\n"
"buffer_load_dwordx4 v4, s[36:39], s63 offen offset:0 lds\n"
"buffer_load_dwordx4 v5, s[36:39], s63 offen offset:1024 lds\n"
"buffer_load_dwordx4 v6, s[36:39], s63 offen offset:2048 lds\n"
"s_add_u32 m0, s64, 86016\n"
"s_nop 0\n"
"buffer_load_dwordx4 v4, s[40:43], s63 offen offset:0 lds\n"
"buffer_load_dwordx4 v5, s[40:43], s63 offen offset:1024 lds\n"
"buffer_load_dwordx4 v6, s[40:43], s63 offen offset:2048 lds\n"
"s_add_u32 s63, s63, 458752\n"
"ds_read_b64_tr_b16 a[96:97], v12 offset:0\n"
"ds_read_b64_tr_b16 a[98:99], v12 offset:768\n"
"ds_read_b64_tr_b16 a[100:101], v12 offset:12288\n"
"ds_read_b64_tr_b16 a[102:103], v12 offset:13056\n"
"ds_read_b64_tr_b16 a[104:105], v13 offset:0\n"
"ds_read_b64_tr_b16 a[106:107], v13 offset:768\n"
"ds_read_b64_tr_b16 a[108:109], v12 offset:6144\n"
"ds_read_b64_tr_b16 a[110:111], v12 offset:6912\n"
"ds_read_b64_tr_b16 a[112:113], v12 offset:18432\n"
"ds_read_b64_tr_b16 a[114:115], v12 offset:19200\n"
"ds_read_b64_tr_b16 a[116:117], v13 offset:6144\n"
"ds_read_b64_tr_b16 a[118:119], v13 offset:6912\n"
"s_waitcnt lgkmcnt(0)\n"
"v_mfma_f32_16x16x32_bf16 v[20:23], a[96:99], v[16:19], v[20:23]\n"
"v_mfma_f32_16x16x32_bf16 v[24:27], a[100:103], v[16:19], v[24:27]\n"
"v_mfma_f32_16x16x32_bf16 v[28:31], a[104:107], v[16:19], v[28:31]\n"
"v_mfma_f32_16x16x32_bf16 v[20:23], a[108:111], v[16:19], v[20:23]\n"
"v_mfma_f32_16x16x32_bf16 v[24:27], a[112:115], v[16:19], v[24:27]\n"
"v_mfma_f32_16x16x32_bf16 v[28:31], a[116:119], v[16:19], v[28:31]\n"
"s_waitcnt vmcnt(24)\n"
"s_barrier\n"
"s_add_u32 m0, s64, 98304\n"
"s_nop 0\n"
"buffer_load_dwordx4 v4, s[36:39], s63 offen offset:0 lds\n"
"buffer_load_dwordx4 v5, s[36:39], s63 offen offset:1024 lds\n"
"buffer_load_dwordx4 v6, s[36:39], s63 offen offset:2048 lds\n"
"s_add_u32 m0, s64, 110592\n"
"s_nop 0\n"
"buffer_load_dwordx4 v4, s[40:43], s63 offen offset:0 lds\n"
"buffer_load_dwordx4 v5, s[40:43], s63 offen offset:1024 lds\n"
"buffer_load_dwordx4 v6, s[40:43], s63 offen offset:2048 lds\n"
"s_add_u32 s63, s63, 458752\n"
"ds_read_b64_tr_b16 a[120:121], v12 offset:24576\n"
"ds_read_b64_tr_b16 a[122:123], v12 offset:25344\n"
"ds_read_b64_tr_b16 a[124:125], v12 offset:36864\n"
"ds_read_b64_tr_b16 a[126:127], v12 offset:37632\n"
"ds_read_b64_tr_b16 a[128:129], v13 offset:24576\n"
"ds_read_b64_tr_b16 a[130:131], v13 offset:25344\n"
"ds_read_b64_tr_b16 a[132:133], v12 offset:30720\n"
"ds_read_b64_tr_b16 a[134:135], v12 offset:31488\n"
"ds_read_b64_tr_b16 a[136:137], v12 offset:43008\n"
"ds_read_b64_tr_b16 a[138:139], v12 offset:43776\n"
"ds_read_b64_tr_b16 a[140:141], v13 offset:30720\n"
"ds_read_b64_tr_b16 a[142:143], v13 offset:31488\n"
"s_waitcnt lgkmcnt(0)\n"
"v_mfma_f32_16x16x32_bf16 v[20:23], a[120:123], v[16:19], v[20:23]\n"
"v_mfma_f32_16x16x32_bf16 v[24:27], a[124:127], v[16:19], v[24:27]\n"
"v_mfma_f32_16x16x32_bf16 v[28:31], a[128:131], v[16:19], v[28:31]\n"
"v_mfma_f32_16x16x32_bf16 v[20:23], a[132:135], v[16:19], v[20:23]\n"
"v_mfma_f32_16x16x32_bf16 v[24:27], a[136:139], v[16:19], v[24:27]\n"
"v_mfma_f32_16x16x32_bf16 v[28:31], a[140:143], v[16:19], v[28:31]\n"
"s_waitcnt vmcnt(24)\n"
"s_barrier\n"
"s_add_u32 m0, s64, 122880\n"
"s_nop 0\n"
"buffer_load_dwordx4 v4, s[36:39], s63 offen offset:0 lds\n"
"buffer_load_dwordx4 v5, s[36:39], s63 offen offset:1024 lds\n"
"buffer_load_dwordx4 v6, s[36:39], s63 offen offset:2048 lds\n"
"s_add_u32 m0, s64, 135168\n"
"s_nop 0\n"
"buffer_load_dwordx4 v4, s[40:43], s63 offen offset:0 lds\n"
"buffer_load_dwordx4 v5, s[40:43], s63 offen offset:1024 lds\n"
"buffer_load_dwordx4 v6, s[40:43], s63 offen offset:2048 lds\n"
"s_add_u32 s63, s63, 458752\n"
"ds_read_b64_tr_b16 a[144:145], v8 offset:0\n"
"ds_read_b64_tr_b16 a[146:147], v8 offset:768\n"
"ds_read_b64_tr_b16 a[148:149], v8 offset:12288\n"
"ds_read_b64_tr_b16 a[150:151], v8 offset:13056\n"
"ds_read_b64_tr_b16 a[152:153], v9 offset:0\n"
"ds_read_b64_tr_b16 a[154:155], v9 offset:768\n"
"ds_read_b64_tr_b16 a[156:157], v8 offset:6144\n"
"ds_read_b64_tr_b16 a[158:159], v8 offset:6912\n"
"ds_read_b64_tr_b16 a[160:161], v8 offset:18432\n"
"ds_read_b64_tr_b16 a[162:163], v8 offset:19200\n"
"ds_read_b64_tr_b16 a[164:165], v9 offset:6144\n"
"ds_read_b64_tr_b16 a[166:167], v9 offset:6912\n"
"s_waitcnt lgkmcnt(0)\n"
"v_mfma_f32_16x16x32_bf16 v[20:23], a[144:147], v[16:19], v[20:23]\n"
"v_mfma_f32_16x16x32_bf16 v[24:27], a[148:151], v[16:19], v[24:27]\n"
"v_mfma_f32_16x16x32_bf16 v[28:31], a[152:155], v[16:19], v[28:31]\n"
"v_mfma_f32_16x16x32_bf16 v[20:23], a[156:159], v[16:19], v[20:23]\n"
"v_mfma_f32_16x16x32_bf16 v[24:27], a[160:163], v[16:19], v[24:27]\n"
"v_mfma_f32_16x16x32_bf16 v[28:31], a[164:167], v[16:19], v[28:31]\n"
"s_waitcnt vmcnt(24)\n"
"s_barrier\n"
"s_add_u32 m0, s64, 0\n"
"s_nop 0\n"
"buffer_load_dwordx4 v4, s[36:39], s63 offen offset:0 lds\n"
"buffer_load_dwordx4 v5, s[36:39], s63 offen offset:1024 lds\n"
"buffer_load_dwordx4 v6, s[36:39], s63 offen offset:2048 lds\n"
"s_add_u32 m0, s64, 12288\n"
"s_nop 0\n"
"buffer_load_dwordx4 v4, s[40:43], s63 offen offset:0 lds\n"
"buffer_load_dwordx4 v5, s[40:43], s63 offen offset:1024 lds\n"
"buffer_load_dwordx4 v6, s[40:43], s63 offen offset:2048 lds\n"
"s_add_u32 s63, s63, 458752\n"
"ds_read_b64_tr_b16 a[168:169], v8 offset:24576\n"
"ds_read_b64_tr_b16 a[170:171], v8 offset:25344\n"
"ds_read_b64_tr_b16 a[172:173], v8 offset:36864\n"
"ds_read_b64_tr_b16 a[174:175], v8 offset:37632\n"
"ds_read_b64_tr_b16 a[176:177], v9 offset:24576\n"
"ds_read_b64_tr_b16 a[178:179], v9 offset:25344\n"
"ds_read_b64_tr_b16 a[180:181], v8 offset:30720\n"
"ds_read_b64_tr_b16 a[182:183], v8 offset:31488\n"
"ds_read_b64_tr_b16 a[184:185], v8 offset:43008\n"
"ds_read_b64_tr_b16 a[186:187], v8 offset:43776\n"
"ds_read_b64_tr_b16 a[188:189], v9 offset:30720\n"
"ds_read_b64_tr_b16 a[190:191], v9 offset:31488\n"
"s_waitcnt lgkmcnt(0)\n"
"v_mfma_f32_16x16x32_bf16 v[20:23], a[168:171], v[16:19], v[20:23]\n"
"v_mfma_f32_16x16x32_bf16 v[24:27], a[172:175], v[16:19], v[24:27]\n"
"v_mfma_f32_16x16x32_bf16 v[28:31], a[176:179], v[16:19], v[28:31]\n"
"v_mfma_f32_16x16x32_bf16 v[20:23], a[180:183], v[16:19], v[20:23]\n"
"v_mfma_f32_16x16x32_bf16 v[24:27], a[184:187], v[16:19], v[24:27]\n"
"v_mfma_f32_16x16x32_bf16 v[28:31], a[188:191], v[16:19], v[28:31]\n"
"s_waitcnt vmcnt(24)\n"
"s_barrier\n"
"s_add_u32 m0, s64, 24576\n"
"s_nop 0\n"
"buffer_load_dwordx4 v4, s[36:39], s63 offen offset:0 lds\n"
"buffer_load_dwordx4 v5, s[36:39], s63 offen offset:1024 lds\n"
"buffer_load_dwordx4 v6, s[36:39], s63 offen offset:2048 lds\n"
"s_add_u32 m0, s64, 36864\n"
"s_nop 0\n"
"buffer_load_dwordx4 v4, s[40:43], s63 offen offset:0 lds\n"
"buffer_load_dwordx4 v5, s[40:43], s63 offen offset:1024 lds\n"
"buffer_load_dwordx4 v6, s[40:43], s63 offen offset:2048 lds\n"
"s_add_u32 s63, s63, 458752\n"
"ds_read_b64_tr_b16 a[192:193], v10 offset:0\n"
"ds_read_b64_tr_b16 a[194:195], v10 offset:768\n"
"ds_read_b64_tr_b16 a[196:197], v10 offset:12288\n"
"ds_read_b64_tr_b16 a[198:199], v10 offset:13056\n"
"ds_read_b64_tr_b16 a[200:201], v11 offset:0\n"
"ds_read_b64_tr_b16 a[202:203], v11 offset:768\n"
"ds_read_b64_tr_b16 a[204:205], v10 offset:6144\n"
"ds_read_b64_tr_b16 a[206:207], v10 offset:6912\n"
"ds_read_b64_tr_b16 a[208:209], v10 offset:18432\n"
"ds_read_b64_tr_b16 a[210:211], v10 offset:19200\n"
"ds_read_b64_tr_b16 a[212:213], v11 offset:6144\n"
"ds_read_b64_tr_b16 a[214:215], v11 offset:6912\n"
"s_waitcnt lgkmcnt(0)\n"
"v_mfma_f32_16x16x32_bf16 v[20:23], a[192:195], v[16:19], v[20:23]\n"
"v_mfma_f32_16x16x32_bf16 v[24:27], a[196:199], v[16:19], v[24:27]\n"
"v_mfma_f32_16x16x32_bf16 v[28:31], a[200:203], v[16:19], v[28:31]\n"
"v_mfma_f32_16x16x32_bf16 v[20:23], a[204:207], v[16:19], v[20:23]\n"
"v_mfma_f32_16x16x32_bf16 v[24:27], a[208:211], v[16:19], v[24:27]\n"
"v_mfma_f32_16x16x32_bf16 v[28:31], a[212:215], v[16:19], v[28:31]\n"
"s_waitcnt vmcnt(24)\n"
"s_barrier\n"
"s_add_u32 m0, s64, 49152\n"
"s_nop 0\n"
"buffer_load_dwordx4 v4, s[36:39], s63 offen offset:0 lds\n"
"buffer_load_dwordx4 v5, s[36:39], s63 offen offset:1024 lds\n"
"buffer_load_dwordx4 v6, s[36:39], s63 offen offset:2048 lds\n"
"s_add_u32 m0, s64, 61440\n"
"s_nop 0\n"
"buffer_load_dwordx4 v4, s[40:43], s63 offen offset:0 lds\n"
"buffer_load_dwordx4 v5, s[40:43], s63 offen offset:1024 lds\n"
"buffer_load_dwordx4 v6, s[40:43], s63 offen offset:2048 lds\n"
"s_add_u32 s63, s63, 458752\n"
"ds_read_b64_tr_b16 a[216:217], v10 offset:24576\n"
"ds_read_b64_tr_b16 a[218:219], v10 offset:25344\n"
"ds_read_b64_tr_b16 a[220:221], v10 offset:36864\n"
"ds_read_b64_tr_b16 a[222:223], v10 offset:37632\n"
"ds_read_b64_tr_b16 a[224:225], v11 offset:24576\n"
"ds_read_b64_tr_b16 a[226:227], v11 offset:25344\n"
"ds_read_b64_tr_b16 a[228:229], v10 offset:30720\n"
"ds_read_b64_tr_b16 a[230:231], v10 offset:31488\n"
"ds_read_b64_tr_b16 a[232:233], v10 offset:43008\n"
"ds_read_b64_tr_b16 a[234:235], v10 offset:43776\n"
"ds_read_b64_tr_b16 a[236:237], v11 offset:30720\n"
"ds_read_b64_tr_b16 a[238:239], v11 offset:31488\n"
"s_waitcnt lgkmcnt(0)\n"
"v_mfma_f32_16x16x32_bf16 v[20:23], a[216:219], v[16:19], v[20:23]\n"
"v_mfma_f32_16x16x32_bf16 v[24:27], a[220:223], v[16:19], v[24:27]\n"
"v_mfma_f32_16x16x32_bf16 v[28:31], a[224:227], v[16:19], v[28:31]\n"
"v_mfma_f32_16x16x32_bf16 v[20:23], a[228:231], v[16:19], v[20:23]\n"
"v_mfma_f32_16x16x32_bf16 v[24:27], a[232:235], v[16:19], v[24:27]\n"
"v_mfma_f32_16x16x32_bf16 v[28:31], a[236:239], v[16:19], v[28:31]\n"
"s_waitcnt vmcnt(24)\n"
"s_barrier\n"
"s_add_u32 m0, s64, 73728\n"
"s_nop 0\n"
"buffer_load_dwordx4 v4, s[36:39], s63 offen offset:0 lds\n"
"buffer_load_dwordx4 v5, s[36:39], s63 offen offset:1024 lds\n"
"buffer_load_dwordx4 v6, s[36:39], s63 offen offset:2048 lds\n"
"s_add_u32 m0, s64, 86016\n"
"s_nop 0\n"
"buffer_load_dwordx4 v4, s[40:43], s63 offen offset:0 lds\n"
"buffer_load_dwordx4 v5, s[40:43], s63 offen offset:1024 lds\n"
"buffer_load_dwordx4 v6, s[40:43], s63 offen offset:2048 lds\n"
"s_add_u32 s63, s63, 458752\n"
"ds_read_b64_tr_b16 a[240:241], v12 offset:0\n"
"ds_read_b64_tr_b16 a[242:243], v12 offset:768\n"
"ds_read_b64_tr_b16 a[244:245], v12 offset:12288\n"
"ds_read_b64_tr_b16 a[246:247], v12 offset:13056\n"
"ds_read_b64_tr_b16 a[248:249], v13 offset:0\n"
"ds_read_b64_tr_b16 a[250:251], v13 offset:768\n"
"ds_read_b64_tr_b16 a[252:253], v12 offset:6144\n"
"ds_read_b64_tr_b16 a[254:255], v12 offset:6912\n"
"ds_read_b64_tr_b16 v[128:129], v12 offset:18432\n"
"ds_read_b64_tr_b16 v[130:131], v12 offset:19200\n"
"ds_read_b64_tr_b16 v[132:133], v13 offset:6144\n"
"ds_read_b64_tr_b16 v[134:135], v13 offset:6912\n"
"s_waitcnt lgkmcnt(0)\n"
"v_mfma_f32_16x16x32_bf16 v[20:23], a[240:243], v[16:19], v[20:23]\n"
"v_mfma_f32_16x16x32_bf16 v[24:27], a[244:247], v[16:19], v[24:27]\n"
"v_mfma_f32_16x16x32_bf16 v[28:31], a[248:251], v[16:19], v[28:31]\n"
"v_mfma_f32_16x16x32_bf16 v[20:23], a[252:255], v[16:19], v[20:23]\n"
"v_mfma_f32_16x16x32_bf16 v[24:27], v[128:131], v[16:19], v[24:27]\n"
"v_mfma_f32_16x16x32_bf16 v[28:31], v[132:135], v[16:19], v[28:31]\n"
"s_waitcnt vmcnt(24)\n"
"s_barrier\n"
"ds_read_b64_tr_b16 v[136:137], v12 offset:24576\n"
"ds_read_b64_tr_b16 v[138:139], v12 offset:25344\n"
"ds_read_b64_tr_b16 v[140:141], v12 offset:36864\n"
"ds_read_b64_tr_b16 v[142:143], v12 offset:37632\n"
"ds_read_b64_tr_b16 v[144:145], v13 offset:24576\n"
"ds_read_b64_tr_b16 v[146:147], v13 offset:25344\n"
"ds_read_b64_tr_b16 v[148:149], v12 offset:30720\n"
"ds_read_b64_tr_b16 v[150:151], v12 offset:31488\n"
"ds_read_b64_tr_b16 v[152:153], v12 offset:43008\n"
"ds_read_b64_tr_b16 v[154:155], v12 offset:43776\n"
"ds_read_b64_tr_b16 v[156:157], v13 offset:30720\n"
"ds_read_b64_tr_b16 v[158:159], v13 offset:31488\n"
"s_waitcnt lgkmcnt(0)\n"
"v_mfma_f32_16x16x32_bf16 v[20:23], v[136:139], v[16:19], v[20:23]\n"
"v_mfma_f32_16x16x32_bf16 v[24:27], v[140:143], v[16:19], v[24:27]\n"
"v_mfma_f32_16x16x32_bf16 v[28:31], v[144:147], v[16:19], v[28:31]\n"
"v_mfma_f32_16x16x32_bf16 v[20:23], v[148:151], v[16:19], v[20:23]\n"
"v_mfma_f32_16x16x32_bf16 v[24:27], v[152:155], v[16:19], v[24:27]\n"
"v_mfma_f32_16x16x32_bf16 v[28:31], v[156:159], v[16:19], v[28:31]\n"
"s_waitcnt vmcnt(18)\n"
"s_barrier\n"
"ds_read_b64_tr_b16 v[160:161], v8 offset:0\n"
"ds_read_b64_tr_b16 v[162:163], v8 offset:768\n"
"ds_read_b64_tr_b16 v[164:165], v8 offset:12288\n"
"ds_read_b64_tr_b16 v[166:167], v8 offset:13056\n"
"ds_read_b64_tr_b16 v[168:169], v9 offset:0\n"
"ds_read_b64_tr_b16 v[170:171], v9 offset:768\n"
"ds_read_b64_tr_b16 v[172:173], v8 offset:6144\n"
"ds_read_b64_tr_b16 v[174:175], v8 offset:6912\n"
"ds_read_b64_tr_b16 v[176:177], v8 offset:18432\n"
"ds_read_b64_tr_b16 v[178:179], v8 offset:19200\n"
"ds_read_b64_tr_b16 v[180:181], v9 offset:6144\n"
"ds_read_b64_tr_b16 v[182:183], v9 offset:6912\n"
"s_waitcnt lgkmcnt(0)\n"
"v_mfma_f32_16x16x32_bf16 v[20:23], v[160:163], v[16:19], v[20:23]\n"
"v_mfma_f32_16x16x32_bf16 v[24:27], v[164:167], v[16:19], v[24:27]\n"
"v_mfma_f32_16x16x32_bf16 v[28:31], v[168:171], v[16:19], v[28:31]\n"
"v_mfma_f32_16x16x32_bf16 v[20:23], v[172:175], v[16:19], v[20:23]\n"
"v_mfma_f32_16x16x32_bf16 v[24:27], v[176:179], v[16:19], v[24:27]\n"
"v_mfma_f32_16x16x32_bf16 v[28:31], v[180:183], v[16:19], v[28:31]\n"
"s_waitcnt vmcnt(12)\n"
"s_barrier\n"
"ds_read_b64_tr_b16 v[184:185], v8 offset:24576\n"
"ds_read_b64_tr_b16 v[186:187], v8 offset:25344\n"
"ds_read_b64_tr_b16 v[188:189], v8 offset:36864\n"
"ds_read_b64_tr_b16 v[190:191], v8 offset:37632\n"
"ds_read_b64_tr_b16 v[192:193], v9 offset:24576\n"
"ds_read_b64_tr_b16 v[194:195], v9 offset:25344\n"
"ds_read_b64_tr_b16 v[196:197], v8 offset:30720\n"
"ds_read_b64_tr_b16 v[198:199], v8 offset:31488\n"
"ds_read_b64_tr_b16 v[200:201], v8 offset:43008\n"
"ds_read_b64_tr_b16 v[202:203], v8 offset:43776\n"
"ds_read_b64_tr_b16 v[204:205], v9 offset:30720\n"
"ds_read_b64_tr_b16 v[206:207], v9 offset:31488\n"
"s_waitcnt lgkmcnt(0)\n"
"v_mfma_f32_16x16x32_bf16 v[20:23], v[184:187], v[16:19], v[20:23]\n"
"v_mfma_f32_16x16x32_bf16 v[24:27], v[188:191], v[16:19], v[24:27]\n"
"v_mfma_f32_16x16x32_bf16 v[28:31], v[192:195], v[16:19], v[28:31]\n"
"v_mfma_f32_16x16x32_bf16 v[20:23], v[196:199], v[16:19], v[20:23]\n"
"v_mfma_f32_16x16x32_bf16 v[24:27], v[200:203], v[16:19], v[24:27]\n"
"v_mfma_f32_16x16x32_bf16 v[28:31], v[204:207], v[16:19], v[28:31]\n"
"s_waitcnt vmcnt(6)\n"
"s_barrier\n"
"ds_read_b64_tr_b16 v[208:209], v10 offset:0\n"
"ds_read_b64_tr_b16 v[210:211], v10 offset:768\n"
"ds_read_b64_tr_b16 v[212:213], v10 offset:12288\n"
"ds_read_b64_tr_b16 v[214:215], v10 offset:13056\n"
"ds_read_b64_tr_b16 v[216:217], v11 offset:0\n"
"ds_read_b64_tr_b16 v[218:219], v11 offset:768\n"
"ds_read_b64_tr_b16 v[220:221], v10 offset:6144\n"
"ds_read_b64_tr_b16 v[222:223], v10 offset:6912\n"
"ds_read_b64_tr_b16 v[224:225], v10 offset:18432\n"
"ds_read_b64_tr_b16 v[226:227], v10 offset:19200\n"
"ds_read_b64_tr_b16 v[228:229], v11 offset:6144\n"
"ds_read_b64_tr_b16 v[230:231], v11 offset:6912\n"
"s_waitcnt lgkmcnt(0)\n"
"v_mfma_f32_16x16x32_bf16 v[20:23], v[208:211], v[16:19], v[20:23]\n"
"v_mfma_f32_16x16x32_bf16 v[24:27], v[212:215], v[16:19], v[24:27]\n"
"v_mfma_f32_16x16x32_bf16 v[28:31], v[216:219], v[16:19], v[28:31]\n"
"v_mfma_f32_16x16x32_bf16 v[20:23], v[220:223], v[16:19], v[20:23]\n"
"v_mfma_f32_16x16x32_bf16 v[24:27], v[224:227], v[16:19], v[24:27]\n"
"v_mfma_f32_16x16x32_bf16 v[28:31], v[228:231], v[16:19], v[28:31]\n"
"s_waitcnt vmcnt(0)\n"
"s_barrier\n"
"ds_read_b64_tr_b16 v[232:233], v10 offset:24576\n"
"ds_read_b64_tr_b16 v[234:235], v10 offset:25344\n"
"ds_read_b64_tr_b16 v[236:237], v10 offset:36864\n"
"ds_read_b64_tr_b16 v[238:239], v10 offset:37632\n"
"ds_read_b64_tr_b16 v[240:241], v11 offset:24576\n"
"ds_read_b64_tr_b16 v[242:243], v11 offset:25344\n"
"ds_read_b64_tr_b16 v[244:245], v10 offset:30720\n"
"ds_read_b64_tr_b16 v[246:247], v10 offset:31488\n"
"ds_read_b64_tr_b16 v[248:249], v10 offset:43008\n"
"ds_read_b64_tr_b16 v[250:251], v10 offset:43776\n"
"ds_read_b64_tr_b16 v[252:253], v11 offset:30720\n"
"ds_read_b64_tr_b16 v[254:255], v11 offset:31488\n"
"s_waitcnt lgkmcnt(0)\n"
"v_mfma_f32_16x16x32_bf16 v[20:23], v[232:235], v[16:19], v[20:23]\n"
"v_mfma_f32_16x16x32_bf16 v[24:27], v[236:239], v[16:19], v[24:27]\n"
"v_mfma_f32_16x16x32_bf16 v[28:31], v[240:243], v[16:19], v[28:31]\n"
"v_mfma_f32_16x16x32_bf16 v[20:23], v[244:247], v[16:19], v[20:23]\n"
"v_mfma_f32_16x16x32_bf16 v[24:27], v[248:251], v[16:19], v[24:27]\n"
"v_mfma_f32_16x16x32_bf16 v[28:31], v[252:255], v[16:19], v[28:31]\n"
"s_barrier\n"
"s_lshl2_add_u32 s49, s27, 128\n"
"s_mul_i32 s68, s27, 3072\n"
"s_mul_i32 s48, s26, 768\n"
"s_cmp_eq_u32 s28, 0\n"
"s_addc_u32 s62, 84, 0\n"
"v_cmp_gt_u32 vcc, 48, v1\n"
"v_mov_b32 v15, 0x7fff0000\n"
"s_nop 0\n"
"v_cndmask_b32 v15, v15, v2, vcc\n"
"s_add_u32 s68, s68, s48\n"
"s_add_u32 s68, s68, 4718592\n"
"s_waitcnt lgkmcnt(0)\n"
"s_add_u32 s54, s52, 0x7f4a7c15\n"
"s_addc_u32 s55, s53, 0x9e3779b9\n"
"s_lshl_b32 s67, s49, 5\n"
"s_add_u32 s67, s67, 4898816\n"
"s_sub_u32 s51, s67, s68\n"
"s_add_u32 s92, s32, s51\n"
"s_addc_u32 s93, s33, 0x10000\n"
"s_sub_u32 s92, s92, 890880\n"
"s_subb_u32 s93, s93, 0\n"
"s_cmp_eq_u32 s26, 3\n"
"s_cselect_b32 s94, 1, 0\n"
"s_mov_b32 s95, 0x20000\n"
"v_mov_b32 v16, s54\n"
"v_mov_b32 v17, s55\n"
"v_cmp_gt_u32 vcc, 12, v1\n"
"v_mul_u32_u24 v3, 296960, v1\n"
"v_mov_b32 v12, 0x7fff0000\n"
"v_cndmask_b32 v3, v12, v3, vcc\n"
"s_add_u32 s51, s67, 76021760\n"
"buffer_store_dwordx2 v[16:17], v3, s[32:35], s51 offen sc1\n"
"s_sub_u32 s67, s68, 296960\n"
"s_mul_i32 s48, s28, 296960\n"
"s_add_u32 s68, s68, s48\n"
"s_sub_u32 s68, s68, 1781760\n"
"s_add_u32 s63, s48, 4681728\n"
"s_add_u32 s66, s48, 4898816\n"
"s_mul_i32 s64, s26, 9216\n"
"s_add_u32 s65, s64, 73728\n"
"s_xor_b32 s65, s65, s64\n"
"s_add_u32 s63, s63, s64\n"
"v_mov_b32 v4, v2\n"
"v_add_u32 v5, 36864, v2\n"
"v_add_u32 v6, 4096, v2\n"
"v_add_u32 v7, 8192, v2\n"
"v_bfe_u32 v3, v1, 1, 3\n"
"v_mul_u32_u24 v8, 1152, v3\n"
"v_lshrrev_b32 v3, 1, v3\n"
"v_add_u32 v3, s26, v3\n"
"v_lshrrev_b32 v9, 4, v1\n"
"v_lshl_add_u32 v3, v3, 2, v9\n"
"v_and_b32 v3, 15, v3\n"
"v_lshl_add_u32 v8, v3, 2, v8\n"
"v_and_b32 v3, 1, v1\n"
"v_lshl_add_u32 v8, v3, 6, v8\n"
"v_add_u32 v8, 1024, v8\n"
"s_lshl_b32 s48, s26, 2\n"
"v_add_u32 v10, s48, v9\n"
"v_and_b32 v11, 15, v1\n"
"v_add_u32 v3, 64, v1\n"
"v_cmp_eq_u32 vcc, 0, v11\n"
"v_add_u32 v9, 73728, v8\n"
"v_xor_b32 v9, v9, v8\n"
"v_cndmask_b32 v10, v3, v10, vcc\n"
"v_lshlrev_b32 v10, 2, v10\n"
"v_add_u32 v10, 147456, v10\n"
"v_lshlrev_b32 v11, 2, v11\n"
"v_add_u32 v11, 147456, v11\n"
"s_lshl_b32 s48, s26, 3\n"
"s_add_u32 s48, s48, 96\n"
"v_add_u32 v14, s48, v1\n"
"v_lshrrev_b32 v14, 2, v14\n"
"v_lshlrev_b32 v14, 7, v14\n"
"v_cmp_gt_u32 vcc, 8, v1\n"
"v_mov_b32 v3, 0x7fff0000\n"
"s_nop 0\n"
"v_cndmask_b32 v14, v3, v14, vcc\n"
"v_cndmask_b32 v16, 0, v16, vcc\n"
"v_cndmask_b32 v17, 0, v17, vcc\n"
"s_mov_b32 s48, 20000\n"
"s_branch .L_rfga\n"
".L_rlga:\n"
"s_sleep 8\n"
".L_rfga:\n"
"buffer_load_dwordx2 v[12:13], v14, s[32:35], s66 offen sc1\n"
"s_sub_u32 s48, s48, 1\n"
"s_cbranch_scc1 .L_end\n"
"s_waitcnt vmcnt(0)\n"
"v_cmp_ne_u64 vcc, v[16:17], v[12:13]\n"
"s_cbranch_vccnz .L_rlga\n"
"s_add_u32 s66, s66, 890880\n"
"s_add_u32 m0, s64, 0\n"
"s_nop 0\n"
"buffer_load_dwordx4 v2, s[32:35], s63 offen offset:0 sc1 lds\n"
"buffer_load_dwordx4 v2, s[32:35], s63 offen offset:1024 sc1 lds\n"
"buffer_load_dwordx4 v2, s[32:35], s63 offen offset:2048 sc1 lds\n"
"buffer_load_dwordx4 v2, s[32:35], s63 offen offset:3072 sc1 lds\n"
"s_add_u32 m0, s64, 4096\n"
"s_nop 0\n"
"buffer_load_dwordx4 v6, s[32:35], s63 offen offset:0 sc1 lds\n"
"buffer_load_dwordx4 v6, s[32:35], s63 offen offset:1024 sc1 lds\n"
"buffer_load_dwordx4 v6, s[32:35], s63 offen offset:2048 sc1 lds\n"
"buffer_load_dwordx4 v6, s[32:35], s63 offen offset:3072 sc1 lds\n"
"s_add_u32 m0, s64, 8192\n"
"s_nop 0\n"
"buffer_load_dwordx4 v7, s[32:35], s63 offen offset:0 sc1 lds\n"
"s_add_u32 s63, s63, 890880\n"
"s_mov_b32 s48, 20000\n"
"s_branch .L_rfgb\n"
".L_rlgb:\n"
"s_sleep 8\n"
".L_rfgb:\n"
"buffer_load_dwordx2 v[12:13], v14, s[32:35], s66 offen sc1\n"
"s_sub_u32 s48, s48, 1\n"
"s_cbranch_scc1 .L_end\n"
"s_waitcnt vmcnt(0)\n"
"v_cmp_ne_u64 vcc, v[16:17], v[12:13]\n"
"s_cbranch_vccnz .L_rlgb\n"
"s_add_u32 s66, s66, 890880\n"
"s_add_u32 m0, s64, 36864\n"
"s_nop 0\n"
"buffer_load_dwordx4 v2, s[32:35], s63 offen offset:0 sc1 lds\n"
"buffer_load_dwordx4 v2, s[32:35], s63 offen offset:1024 sc1 lds\n"
"buffer_load_dwordx4 v2, s[32:35], s63 offen offset:2048 sc1 lds\n"
"buffer_load_dwordx4 v2, s[32:35], s63 offen offset:3072 sc1 lds\n"
"s_add_u32 m0, s64, 40960\n"
"s_nop 0\n"
"buffer_load_dwordx4 v6, s[32:35], s63 offen offset:0 sc1 lds\n"
"buffer_load_dwordx4 v6, s[32:35], s63 offen offset:1024 sc1 lds\n"
"buffer_load_dwordx4 v6, s[32:35], s63 offen offset:2048 sc1 lds\n"
"buffer_load_dwordx4 v6, s[32:35], s63 offen offset:3072 sc1 lds\n"
"s_add_u32 m0, s64, 45056\n"
"s_nop 0\n"
"buffer_load_dwordx4 v7, s[32:35], s63 offen offset:0 sc1 lds\n"
"s_add_u32 s63, s63, 890880\n"
"s_xor_b32 s64, s64, s65\n"
"buffer_load_dwordx2 v[12:13], v14, s[32:35], s66 offen sc1\n"
"s_waitcnt vmcnt(0)\n"
"s_barrier\n"
"ds_read_b128 v[76:79], v4 offset:0\n"
"ds_read_b128 v[80:83], v4 offset:1152\n"
"ds_read_b128 v[84:87], v4 offset:2304\n"
"ds_read_b128 v[88:91], v4 offset:3456\n"
"ds_read_b128 v[92:95], v4 offset:4608\n"
"ds_read_b128 v[96:99], v4 offset:5760\n"
"ds_read_b128 v[100:103], v4 offset:6912\n"
"ds_read_b128 v[104:107], v4 offset:8064\n"
"s_cmp_eq_u32 s26, 1\n"
"s_cbranch_scc1 .L_gv1\n"
"s_cmp_eq_u32 s26, 2\n"
"s_cbranch_scc1 .L_gv2\n"
"s_cmp_eq_u32 s26, 3\n"
"s_cbranch_scc1 .L_gv3\n"
".p2align 6\n"
".L_gv0:\n"
".L_gl_0:\n"
"s_waitcnt lgkmcnt(4)\n"
"v_mfma_f32_16x16x32_bf16 v[32:35], a[0:3], v[76:79], 0\n"
"ds_read_b128 v[108:111], v4 offset:9216\n"
"ds_read_b32 v62, v11 offset:128\n"
"v_mfma_f32_16x16x32_bf16 v[32:35], a[12:15], v[80:83], v[32:35]\n"
"s_add_u32 s68, s68, 890880\n"
"v_mfma_f32_16x16x32_bf16 v[32:35], a[24:27], v[84:87], v[32:35]\n"
"v_mfma_f32_16x16x32_bf16 v[32:35], a[36:39], v[88:91], v[32:35]\n"
"ds_read_b128 v[112:115], v4 offset:10368\n"
"v_mfma_f32_16x16x32_bf16 v[36:39], a[40:43], v[88:91], 0\n"
"ds_read2st64_b32 v[56:57], v8 offset0:0 offset1:36\n"
"v_mfma_f32_16x16x32_bf16 v[36:39], a[28:31], v[84:87], v[36:39]\n"
"s_waitcnt vmcnt(4)\n"
"v_cmp_ne_u64 vcc, v[16:17], v[12:13]\n"
"v_mfma_f32_16x16x32_bf16 v[36:39], a[16:19], v[80:83], v[36:39]\n"
"ds_read_b128 v[116:119], v4 offset:11520\n"
"ds_read2st64_b32 v[58:59], v8 offset0:72 offset1:108\n"
"v_mfma_f32_16x16x32_bf16 v[36:39], a[4:7], v[76:79], v[36:39]\n"
"s_cbranch_vccnz .L_rtg0_0\n"
".L_bkg0_0:\n"
"s_add_u32 s66, s66, 890880\n"
"v_mfma_f32_16x16x32_bf16 v[40:43], a[8:11], v[76:79], 0\n"
"s_add_u32 m0, s64, 0\n"
"s_waitcnt lgkmcnt(4)\n"
"v_mfma_f32_16x16x32_bf16 v[40:43], a[20:23], v[80:83], v[40:43]\n"
"buffer_load_dwordx4 v2, s[32:35], s63 offen offset:0 sc1 lds\n"
"ds_read_b128 v[120:123], v4 offset:12672\n"
"v_mfma_f32_16x16x32_bf16 v[40:43], a[32:35], v[84:87], v[40:43]\n"
"v_lshlrev_b32 v60, 16, v62\n"
"v_and_b32 v62, 0xffff0000, v62\n"
"v_mfma_f32_16x16x32_bf16 v[40:43], a[44:47], v[88:91], v[40:43]\n"
"v_fma_f32 v52, -v60, v28, v52\n"
"v_mfma_f32_16x16x32_bf16 v[40:43], a[56:59], v[92:95], v[40:43]\n"
"ds_read_b128 v[124:127], v4 offset:13824\n"
"v_fma_f32 v53, -v60, v29, v53\n"
"v_mfma_f32_16x16x32_bf16 v[40:43], a[68:71], v[96:99], v[40:43]\n"
"v_rcp_f32 v61, v62\n"
"v_mfma_f32_16x16x32_bf16 v[40:43], a[80:83], v[100:103], v[40:43]\n"
"v_mul_f32 v62, v62, v62\n"
"v_mul_f32 v61, 0xbfb8aa3b, v61\n"
"v_mfma_f32_16x16x32_bf16 v[40:43], a[92:95], v[104:107], v[40:43]\n"
"ds_read_b128 v[76:79], v4 offset:14976\n"
"v_fma_f32 v54, -v60, v30, v54\n"
"v_mfma_f32_16x16x32_bf16 v[36:39], a[88:91], v[104:107], v[36:39]\n"
"v_fma_f32 v55, -v60, v31, v55\n"
"v_fma_f32 v48, -v60, v24, v48\n"
"v_mfma_f32_16x16x32_bf16 v[36:39], a[76:79], v[100:103], v[36:39]\n"
"buffer_load_dwordx4 v2, s[32:35], s63 offen offset:1024 sc1 lds\n"
"v_fma_f32 v49, -v60, v25, v49\n"
"v_mfma_f32_16x16x32_bf16 v[36:39], a[64:67], v[96:99], v[36:39]\n"
"ds_read_b128 v[80:83], v4 offset:16128\n"
"v_fma_f32 v50, -v60, v26, v50\n"
"v_mfma_f32_16x16x32_bf16 v[36:39], a[52:55], v[92:95], v[36:39]\n"
"v_permlane32_swap_b32 v52, v54\n"
"v_mfma_f32_16x16x32_bf16 v[32:35], a[48:51], v[92:95], v[32:35]\n"
"v_permlane32_swap_b32 v53, v55\n"
"v_mfma_f32_16x16x32_bf16 v[32:35], a[60:63], v[96:99], v[32:35]\n"
"ds_read_b128 v[84:87], v4 offset:17280\n"
"v_fma_f32 v51, -v60, v27, v51\n"
"v_mfma_f32_16x16x32_bf16 v[32:35], a[72:75], v[100:103], v[32:35]\n"
"v_fma_f32 v44, -v60, v20, v44\n"
"v_fma_f32 v45, -v60, v21, v45\n"
"v_mfma_f32_16x16x32_bf16 v[32:35], a[84:87], v[104:107], v[32:35]\n"
"v_fma_f32 v46, -v60, v22, v46\n"
"s_waitcnt lgkmcnt(4)\n"
"v_mfma_f32_16x16x32_bf16 v[32:35], a[96:99], v[108:111], v[32:35]\n"
"ds_read_b128 v[88:91], v4 offset:18432\n"
"v_add_f32 v56, v56, v57\n"
"v_mfma_f32_16x16x32_bf16 v[32:35], a[108:111], v[112:115], v[32:35]\n"
"buffer_load_dwordx4 v2, s[32:35], s63 offen offset:2048 sc1 lds\n"
"v_add_f32 v58, v58, v59\n"
"v_mfma_f32_16x16x32_bf16 v[32:35], a[120:123], v[116:119], v[32:35]\n"
"v_add_f32 v56, v56, v58\n"
"v_fma_f32 v47, -v60, v23, v47\n"
"v_mfma_f32_16x16x32_bf16 v[32:35], a[132:135], v[120:123], v[32:35]\n"
"ds_read_b128 v[92:95], v4 offset:19584\n"
"v_add_f32_dpp v56, v56, v56 row_ror:8 row_mask:0xf bank_mask:0xf\n"
"v_mfma_f32_16x16x32_bf16 v[36:39], a[136:139], v[120:123], v[36:39]\n"
"v_mul_f32 v64, v44, v61\n"
"v_add_f32_dpp v56, v56, v56 row_ror:4 row_mask:0xf bank_mask:0xf\n"
"v_mfma_f32_16x16x32_bf16 v[36:39], a[124:127], v[116:119], v[36:39]\n"
"v_exp_f32 v64, v64\n"
"v_mfma_f32_16x16x32_bf16 v[36:39], a[112:115], v[112:115], v[36:39]\n"
"ds_read_b128 v[96:99], v4 offset:20736\n"
"v_add_f32_dpp v56, v56, v56 row_ror:2 row_mask:0xf bank_mask:0xf\n"
"v_mfma_f32_16x16x32_bf16 v[36:39], a[100:103], v[108:111], v[36:39]\n"
"v_mul_f32 v56, 0x3a800000, v56\n"
"v_mul_f32 v65, v45, v61\n"
"v_mfma_f32_16x16x32_bf16 v[40:43], a[104:107], v[108:111], v[40:43]\n"
"v_exp_f32 v65, v65\n"
"v_mfma_f32_16x16x32_bf16 v[40:43], a[116:119], v[112:115], v[40:43]\n"
"buffer_load_dwordx4 v2, s[32:35], s63 offen offset:3072 sc1 lds\n"
"ds_read_b128 v[100:103], v4 offset:21888\n"
"v_mfma_f32_16x16x32_bf16 v[40:43], a[128:131], v[116:119], v[40:43]\n"
"v_mul_f32 v57, v56, v56\n"
"v_mul_f32 v66, v46, v61\n"
"v_mfma_f32_16x16x32_bf16 v[40:43], a[140:143], v[120:123], v[40:43]\n"
"v_sub_f32_dpp v57, v56, v57 quad_perm:[1,1,3,3] row_mask:0xf bank_mask:0xf\n"
"s_waitcnt lgkmcnt(4)\n"
"v_mfma_f32_16x16x32_bf16 v[40:43], a[152:155], v[124:127], v[40:43]\n"
"ds_read_b128 v[104:107], v4 offset:23040\n"
"v_max_f32 v57, 0, v57\n"
"v_mfma_f32_16x16x32_bf16 v[40:43], a[164:167], v[76:79], v[40:43]\n"
"v_exp_f32 v66, v66\n"
"v_mfma_f32_16x16x32_bf16 v[40:43], a[176:179], v[80:83], v[40:43]\n"
"v_cvt_pk_bf16_f32 v57, 0, v57\n"
"v_mul_f32 v67, v47, v61\n"
"v_mfma_f32_16x16x32_bf16 v[40:43], a[188:191], v[84:87], v[40:43]\n"
"ds_read_b128 v[108:111], v4 offset:24192\n"
"s_add_u32 m0, s64, 4096\n"
"v_mfma_f32_16x16x32_bf16 v[36:39], a[184:187], v[84:87], v[36:39]\n"
"v_exp_f32 v67, v67\n"
"v_mfma_f32_16x16x32_bf16 v[36:39], a[172:175], v[80:83], v[36:39]\n"
"buffer_load_dwordx4 v6, s[32:35], s63 offen offset:0 sc1 lds\n"
"v_add_f32 v57, 0x3727c5ac, v57\n"
"v_mfma_f32_16x16x32_bf16 v[36:39], a[160:163], v[76:79], v[36:39]\n"
"ds_read_b128 v[112:115], v4 offset:25344\n"
"v_cvt_pk_bf16_f32 v57, 0, v57\n"
"v_mfma_f32_16x16x32_bf16 v[36:39], a[148:151], v[124:127], v[36:39]\n"
"v_sqrt_f32 v57, v57\n"
"v_mfma_f32_16x16x32_bf16 v[32:35], a[144:147], v[124:127], v[32:35]\n"
"v_cvt_pk_bf16_f32 v57, v56, v57\n"
"v_mul_f32 v68, v52, v61\n"
"v_mfma_f32_16x16x32_bf16 v[32:35], a[156:159], v[76:79], v[32:35]\n"
"ds_read_b128 v[116:119], v4 offset:26496\n"
"ds_write_b32 v10, v57 offset:0\n"
"v_mfma_f32_16x16x32_bf16 v[32:35], a[168:171], v[80:83], v[32:35]\n"
"v_exp_f32 v68, v68\n"
"v_mfma_f32_16x16x32_bf16 v[32:35], a[180:183], v[84:87], v[32:35]\n"
"v_mul_f32 v69, v53, v61\n"
"s_waitcnt lgkmcnt(5)\n"
"v_mfma_f32_16x16x32_bf16 v[32:35], a[192:195], v[88:91], v[32:35]\n"
"ds_read_b128 v[120:123], v4 offset:27648\n"
"v_mul_f32 v44, v44, v48\n"
"v_mfma_f32_16x16x32_bf16 v[32:35], a[204:207], v[92:95], v[32:35]\n"
"buffer_load_dwordx4 v6, s[32:35], s63 offen offset:1024 sc1 lds\n"
"v_mul_f32 v45, v45, v49\n"
"v_mfma_f32_16x16x32_bf16 v[32:35], a[216:219], v[96:99], v[32:35]\n"
"v_exp_f32 v69, v69\n"
"v_mfma_f32_16x16x32_bf16 v[32:35], a[228:231], v[100:103], v[32:35]\n"
"ds_read_b128 v[124:127], v4 offset:28800\n"
"v_mul_f32 v46, v46, v50\n"
"v_mfma_f32_16x16x32_bf16 v[36:39], a[232:235], v[100:103], v[36:39]\n"
"v_mul_f32 v47, v47, v51\n"
"v_mul_f32 v52, v52, v54\n"
"v_mfma_f32_16x16x32_bf16 v[36:39], a[220:223], v[96:99], v[36:39]\n"
"buffer_load_dwordx2 v[12:13], v14, s[32:35], s66 offen sc1\n"
"v_mul_f32 v53, v53, v55\n"
"v_mfma_f32_16x16x32_bf16 v[36:39], a[208:211], v[92:95], v[36:39]\n"
"ds_read_b128 v[76:79], v4 offset:29952\n"
"v_fma_f32 v64, v64, v62, v62\n"
"v_mfma_f32_16x16x32_bf16 v[36:39], a[196:199], v[88:91], v[36:39]\n"
"v_rcp_f32 v64, v64\n"
"v_mfma_f32_16x16x32_bf16 v[40:43], a[200:203], v[88:91], v[40:43]\n"
"v_fma_f32 v65, v65, v62, v62\n"
"v_fma_f32 v66, v66, v62, v62\n"
"v_mfma_f32_16x16x32_bf16 v[40:43], a[212:215], v[92:95], v[40:43]\n"
"buffer_load_dwordx4 v6, s[32:35], s63 offen offset:2048 sc1 lds\n"
"ds_read_b128 v[80:83], v4 offset:31104\n"
"v_mfma_f32_16x16x32_bf16 v[40:43], a[224:227], v[96:99], v[40:43]\n"
"v_rcp_f32 v65, v65\n"
"v_mfma_f32_16x16x32_bf16 v[40:43], a[236:239], v[100:103], v[40:43]\n"
"v_fma_f32 v67, v67, v62, v62\n"
"s_waitcnt lgkmcnt(5)\n"
"v_mfma_f32_16x16x32_bf16 v[40:43], a[248:251], v[104:107], v[40:43]\n"
"ds_read_b128 v[84:87], v4 offset:32256\n"
"v_fma_f32 v68, v68, v62, v62\n"
"v_mfma_f32_16x16x32_bf16 v[40:43], v[132:135], v[108:111], v[40:43]\n"
"v_rcp_f32 v66, v66\n"
"v_mfma_f32_16x16x32_bf16 v[40:43], v[144:147], v[112:115], v[40:43]\n"
"v_rcp_f32 v67, v67\n"
"v_mfma_f32_16x16x32_bf16 v[40:43], v[156:159], v[116:119], v[40:43]\n"
"ds_read_b128 v[88:91], v4 offset:33408\n"
"v_fma_f32 v69, v69, v62, v62\n"
"v_mfma_f32_16x16x32_bf16 v[36:39], v[152:155], v[116:119], v[36:39]\n"
"v_rcp_f32 v68, v68\n"
"v_mfma_f32_16x16x32_bf16 v[36:39], v[140:143], v[112:115], v[36:39]\n"
"buffer_load_dwordx4 v6, s[32:35], s63 offen offset:3072 sc1 lds\n"
"v_mul_f32 v44, v44, v64\n"
"v_mfma_f32_16x16x32_bf16 v[36:39], v[128:131], v[108:111], v[36:39]\n"
"ds_read_b128 v[92:95], v4 offset:34560\n"
"v_mul_f32 v45, v45, v65\n"
"v_mfma_f32_16x16x32_bf16 v[36:39], a[244:247], v[104:107], v[36:39]\n"
"v_rcp_f32 v69, v69\n"
"v_mfma_f32_16x16x32_bf16 v[32:35], a[240:243], v[104:107], v[32:35]\n"
"v_mul_f32 v46, v46, v66\n"
"v_mul_f32 v47, v47, v67\n"
"v_mfma_f32_16x16x32_bf16 v[32:35], a[252:255], v[108:111], v[32:35]\n"
"ds_read_b128 v[96:99], v4 offset:35712\n"
"v_mul_f32 v52, v52, v68\n"
"v_mfma_f32_16x16x32_bf16 v[32:35], v[136:139], v[112:115], v[32:35]\n"
"v_xor_b32 v4, 73728, v4\n"
"v_mul_f32 v53, v53, v69\n"
"v_mfma_f32_16x16x32_bf16 v[32:35], v[148:151], v[116:119], v[32:35]\n"
"s_waitcnt lgkmcnt(4)\n"
"s_waitcnt vmcnt(9)\n"
"s_barrier\n"
"v_mfma_f32_16x16x32_bf16 v[32:35], v[160:163], v[120:123], v[32:35]\n"
"ds_read_b128 v[100:103], v5 offset:0\n"
"s_add_u32 m0, s64, 8192\n"
"v_mfma_f32_16x16x32_bf16 v[32:35], v[172:175], v[124:127], v[32:35]\n"
"buffer_load_dwordx4 v7, s[32:35], s63 offen offset:0 sc1 lds\n"
"s_add_u32 s63, s63, 890880\n"
"v_mfma_f32_16x16x32_bf16 v[32:35], v[184:187], v[76:79], v[32:35]\n"
"v_cvt_pk_bf16_f32 v74, v52, v53\n"
"v_cvt_pk_bf16_f32 v72, v44, v45\n"
"v_mfma_f32_16x16x32_bf16 v[32:35], v[196:199], v[80:83], v[32:35]\n"
"ds_read_b128 v[104:107], v5 offset:1152\n"
"v_cvt_pk_bf16_f32 v73, v46, v47\n"
"v_mfma_f32_16x16x32_bf16 v[36:39], v[200:203], v[80:83], v[36:39]\n"
"v_permlane32_swap_b32 v74, v75\n"
"v_mfma_f32_16x16x32_bf16 v[36:39], v[188:191], v[76:79], v[36:39]\n"
"v_mfma_f32_16x16x32_bf16 v[36:39], v[176:179], v[124:127], v[36:39]\n"
"ds_read_b128 v[108:111], v5 offset:2304\n"
"v_mfma_f32_16x16x32_bf16 v[36:39], v[164:167], v[120:123], v[36:39]\n"
"v_permlane16_swap_b32 v72, v74\n"
"v_mfma_f32_16x16x32_bf16 v[40:43], v[168:171], v[120:123], v[40:43]\n"
"v_permlane16_swap_b32 v73, v75\n"
"v_mfma_f32_16x16x32_bf16 v[40:43], v[180:183], v[124:127], v[40:43]\n"
"buffer_store_dwordx4 v[72:75], v15, s[32:35], s68 offen sc1\n"
"ds_read_b128 v[112:115], v5 offset:3456\n"
"v_mfma_f32_16x16x32_bf16 v[40:43], v[192:195], v[76:79], v[40:43]\n"
"v_mfma_f32_16x16x32_bf16 v[40:43], v[204:207], v[80:83], v[40:43]\n"
"s_waitcnt lgkmcnt(4)\n"
"v_mfma_f32_16x16x32_bf16 v[40:43], v[216:219], v[84:87], v[40:43]\n"
"ds_read_b128 v[116:119], v5 offset:4608\n"
"v_mfma_f32_16x16x32_bf16 v[40:43], v[228:231], v[88:91], v[40:43]\n"
"v_mfma_f32_16x16x32_bf16 v[40:43], v[240:243], v[92:95], v[40:43]\n"
"v_mfma_f32_16x16x32_bf16 v[40:43], v[252:255], v[96:99], v[40:43]\n"
"ds_read_b128 v[120:123], v5 offset:5760\n"
"v_mfma_f32_16x16x32_bf16 v[36:39], v[248:251], v[96:99], v[36:39]\n"
"v_mfma_f32_16x16x32_bf16 v[36:39], v[236:239], v[92:95], v[36:39]\n"
"v_mfma_f32_16x16x32_bf16 v[36:39], v[224:227], v[88:91], v[36:39]\n"
"ds_read_b128 v[124:127], v5 offset:6912\n"
"v_mfma_f32_16x16x32_bf16 v[36:39], v[212:215], v[84:87], v[36:39]\n"
"v_mfma_f32_16x16x32_bf16 v[32:35], v[208:211], v[84:87], v[32:35]\n"
"v_mfma_f32_16x16x32_bf16 v[32:35], v[220:223], v[88:91], v[32:35]\n"
"ds_read_b128 v[76:79], v5 offset:8064\n"
"v_mfma_f32_16x16x32_bf16 v[32:35], v[232:235], v[92:95], v[32:35]\n"
"s_sub_u32 s62, s62, 1\n"
"v_mfma_f32_16x16x32_bf16 v[32:35], v[244:247], v[96:99], v[32:35]\n"
"s_cbranch_scc1 .L_gt0_0\n"
"s_waitcnt lgkmcnt(4)\n"
"v_mfma_f32_16x16x32_bf16 v[44:47], a[0:3], v[100:103], 0\n"
"ds_read_b128 v[80:83], v5 offset:9216\n"
"ds_read_b32 v62, v11 offset:0\n"
"v_mfma_f32_16x16x32_bf16 v[44:47], a[12:15], v[104:107], v[44:47]\n"
"s_add_u32 s68, s68, 890880\n"
"v_mfma_f32_16x16x32_bf16 v[44:47], a[24:27], v[108:111], v[44:47]\n"
"v_mfma_f32_16x16x32_bf16 v[44:47], a[36:39], v[112:115], v[44:47]\n"
"ds_read_b128 v[84:87], v5 offset:10368\n"
"v_mfma_f32_16x16x32_bf16 v[48:51], a[40:43], v[112:115], 0\n"
"ds_read2st64_b32 v[56:57], v8 offset0:144 offset1:180\n"
"v_mfma_f32_16x16x32_bf16 v[48:51], a[28:31], v[108:111], v[48:51]\n"
"s_waitcnt vmcnt(4)\n"
"v_cmp_ne_u64 vcc, v[16:17], v[12:13]\n"
"v_mfma_f32_16x16x32_bf16 v[48:51], a[16:19], v[104:107], v[48:51]\n"
"ds_read_b128 v[88:91], v5 offset:11520\n"
"ds_read2st64_b32 v[58:59], v8 offset0:216 offset1:252\n"
"v_mfma_f32_16x16x32_bf16 v[48:51], a[4:7], v[100:103], v[48:51]\n"
"s_cbranch_vccnz .L_rtg1_0\n"
".L_bkg1_0:\n"
"s_add_u32 s66, s66, 890880\n"
"v_mfma_f32_16x16x32_bf16 v[52:55], a[8:11], v[100:103], 0\n"
"s_add_u32 m0, s64, 36864\n"
"v_xor_b32 v8, v9, v8\n"
"v_mfma_f32_16x16x32_bf16 v[52:55], a[20:23], v[104:107], v[52:55]\n"
"buffer_load_dwordx4 v2, s[32:35], s63 offen offset:0 sc1 lds\n"
"ds_read_b128 v[92:95], v5 offset:12672\n"
"v_mfma_f32_16x16x32_bf16 v[52:55], a[32:35], v[108:111], v[52:55]\n"
"s_waitcnt lgkmcnt(5)\n"
"v_lshlrev_b32 v60, 16, v62\n"
"v_mfma_f32_16x16x32_bf16 v[52:55], a[44:47], v[112:115], v[52:55]\n"
"v_and_b32 v62, 0xffff0000, v62\n"
"v_mfma_f32_16x16x32_bf16 v[52:55], a[56:59], v[116:119], v[52:55]\n"
"ds_read_b128 v[96:99], v5 offset:13824\n"
"v_fma_f32 v40, -v60, v28, v40\n"
"v_mfma_f32_16x16x32_bf16 v[52:55], a[68:71], v[120:123], v[52:55]\n"
"v_rcp_f32 v61, v62\n"
"v_mfma_f32_16x16x32_bf16 v[52:55], a[80:83], v[124:127], v[52:55]\n"
"v_mul_f32 v62, v62, v62\n"
"v_mul_f32 v61, 0xbfb8aa3b, v61\n"
"v_mfma_f32_16x16x32_bf16 v[52:55], a[92:95], v[76:79], v[52:55]\n"
"ds_read_b128 v[100:103], v5 offset:14976\n"
"v_fma_f32 v41, -v60, v29, v41\n"
"v_mfma_f32_16x16x32_bf16 v[48:51], a[88:91], v[76:79], v[48:51]\n"
"v_fma_f32 v42, -v60, v30, v42\n"
"v_fma_f32 v43, -v60, v31, v43\n"
"v_mfma_f32_16x16x32_bf16 v[48:51], a[76:79], v[124:127], v[48:51]\n"
"buffer_load_dwordx4 v2, s[32:35], s63 offen offset:1024 sc1 lds\n"
"v_fma_f32 v36, -v60, v24, v36\n"
"v_mfma_f32_16x16x32_bf16 v[48:51], a[64:67], v[120:123], v[48:51]\n"
"ds_read_b128 v[104:107], v5 offset:16128\n"
"v_fma_f32 v37, -v60, v25, v37\n"
"v_mfma_f32_16x16x32_bf16 v[48:51], a[52:55], v[116:119], v[48:51]\n"
"v_permlane32_swap_b32 v40, v42\n"
"v_mfma_f32_16x16x32_bf16 v[44:47], a[48:51], v[116:119], v[44:47]\n"
"v_permlane32_swap_b32 v41, v43\n"
"v_mfma_f32_16x16x32_bf16 v[44:47], a[60:63], v[120:123], v[44:47]\n"
"ds_read_b128 v[108:111], v5 offset:17280\n"
"v_fma_f32 v38, -v60, v26, v38\n"
"v_mfma_f32_16x16x32_bf16 v[44:47], a[72:75], v[124:127], v[44:47]\n"
"v_fma_f32 v39, -v60, v27, v39\n"
"v_fma_f32 v32, -v60, v20, v32\n"
"v_mfma_f32_16x16x32_bf16 v[44:47], a[84:87], v[76:79], v[44:47]\n"
"v_fma_f32 v33, -v60, v21, v33\n"
"s_waitcnt lgkmcnt(4)\n"
"v_mfma_f32_16x16x32_bf16 v[44:47], a[96:99], v[80:83], v[44:47]\n"
"ds_read_b128 v[112:115], v5 offset:18432\n"
"v_add_f32 v56, v56, v57\n"
"v_mfma_f32_16x16x32_bf16 v[44:47], a[108:111], v[84:87], v[44:47]\n"
"buffer_load_dwordx4 v2, s[32:35], s63 offen offset:2048 sc1 lds\n"
"v_add_f32 v58, v58, v59\n"
"v_mfma_f32_16x16x32_bf16 v[44:47], a[120:123], v[88:91], v[44:47]\n"
"v_add_f32 v56, v56, v58\n"
"v_fma_f32 v34, -v60, v22, v34\n"
"v_mfma_f32_16x16x32_bf16 v[44:47], a[132:135], v[92:95], v[44:47]\n"
"ds_read_b128 v[116:119], v5 offset:19584\n"
"v_add_f32_dpp v56, v56, v56 row_ror:8 row_mask:0xf bank_mask:0xf\n"
"v_mfma_f32_16x16x32_bf16 v[48:51], a[136:139], v[92:95], v[48:51]\n"
"v_fma_f32 v35, -v60, v23, v35\n"
"v_add_f32_dpp v56, v56, v56 row_ror:4 row_mask:0xf bank_mask:0xf\n"
"v_mfma_f32_16x16x32_bf16 v[48:51], a[124:127], v[88:91], v[48:51]\n"
"v_mul_f32 v64, v32, v61\n"
"v_add_f32_dpp v56, v56, v56 row_ror:2 row_mask:0xf bank_mask:0xf\n"
"v_mfma_f32_16x16x32_bf16 v[48:51], a[112:115], v[84:87], v[48:51]\n"
"ds_read_b128 v[120:123], v5 offset:20736\n"
"v_mul_f32 v56, 0x3a800000, v56\n"
"v_mfma_f32_16x16x32_bf16 v[48:51], a[100:103], v[80:83], v[48:51]\n"
"v_exp_f32 v64, v64\n"
"v_mfma_f32_16x16x32_bf16 v[52:55], a[104:107], v[80:83], v[52:55]\n"
"v_mul_f32 v57, v56, v56\n"
"v_mul_f32 v65, v33, v61\n"
"v_mfma_f32_16x16x32_bf16 v[52:55], a[116:119], v[84:87], v[52:55]\n"
"buffer_load_dwordx4 v2, s[32:35], s63 offen offset:3072 sc1 lds\n"
"ds_read_b128 v[124:127], v5 offset:21888\n"
"v_mfma_f32_16x16x32_bf16 v[52:55], a[128:131], v[88:91], v[52:55]\n"
"v_exp_f32 v65, v65\n"
"v_mfma_f32_16x16x32_bf16 v[52:55], a[140:143], v[92:95], v[52:55]\n"
"v_sub_f32_dpp v57, v56, v57 quad_perm:[1,1,3,3] row_mask:0xf bank_mask:0xf\n"
"s_waitcnt lgkmcnt(4)\n"
"v_mfma_f32_16x16x32_bf16 v[52:55], a[152:155], v[96:99], v[52:55]\n"
"ds_read_b128 v[76:79], v5 offset:23040\n"
"v_max_f32 v57, 0, v57\n"
"v_mfma_f32_16x16x32_bf16 v[52:55], a[164:167], v[100:103], v[52:55]\n"
"v_cvt_pk_bf16_f32 v57, 0, v57\n"
"v_mul_f32 v66, v34, v61\n"
"v_mfma_f32_16x16x32_bf16 v[52:55], a[176:179], v[104:107], v[52:55]\n"
"v_exp_f32 v66, v66\n"
"v_mfma_f32_16x16x32_bf16 v[52:55], a[188:191], v[108:111], v[52:55]\n"
"ds_read_b128 v[80:83], v5 offset:24192\n"
"s_add_u32 m0, s64, 40960\n"
"v_mfma_f32_16x16x32_bf16 v[48:51], a[184:187], v[108:111], v[48:51]\n"
"v_add_f32 v57, 0x3727c5ac, v57\n"
"v_mul_f32 v67, v35, v61\n"
"v_mfma_f32_16x16x32_bf16 v[48:51], a[172:175], v[104:107], v[48:51]\n"
"buffer_load_dwordx4 v6, s[32:35], s63 offen offset:0 sc1 lds\n"
"v_cvt_pk_bf16_f32 v57, 0, v57\n"
"v_mfma_f32_16x16x32_bf16 v[48:51], a[160:163], v[100:103], v[48:51]\n"
"ds_read_b128 v[84:87], v5 offset:25344\n"
"v_mul_f32 v68, v40, v61\n"
"v_mfma_f32_16x16x32_bf16 v[48:51], a[148:151], v[96:99], v[48:51]\n"
"v_sqrt_f32 v57, v57\n"
"v_mfma_f32_16x16x32_bf16 v[44:47], a[144:147], v[96:99], v[44:47]\n"
"v_exp_f32 v67, v67\n"
"v_mfma_f32_16x16x32_bf16 v[44:47], a[156:159], v[100:103], v[44:47]\n"
"ds_read_b128 v[88:91], v5 offset:26496\n"
"v_cvt_pk_bf16_f32 v57, v56, v57\n"
"v_mfma_f32_16x16x32_bf16 v[44:47], a[168:171], v[104:107], v[44:47]\n"
"ds_write_b32 v10, v57 offset:128\n"
"v_mul_f32 v69, v41, v61\n"
"v_mfma_f32_16x16x32_bf16 v[44:47], a[180:183], v[108:111], v[44:47]\n"
"v_mul_f32 v32, v32, v36\n"
"s_waitcnt lgkmcnt(5)\n"
"v_mfma_f32_16x16x32_bf16 v[44:47], a[192:195], v[112:115], v[44:47]\n"
"ds_read_b128 v[92:95], v5 offset:27648\n"
"v_mul_f32 v33, v33, v37\n"
"v_mfma_f32_16x16x32_bf16 v[44:47], a[204:207], v[116:119], v[44:47]\n"
"buffer_load_dwordx4 v6, s[32:35], s63 offen offset:1024 sc1 lds\n"
"v_mul_f32 v34, v34, v38\n"
"v_mfma_f32_16x16x32_bf16 v[44:47], a[216:219], v[120:123], v[44:47]\n"
"v_exp_f32 v68, v68\n"
"v_mfma_f32_16x16x32_bf16 v[44:47], a[228:231], v[124:127], v[44:47]\n"
"ds_read_b128 v[96:99], v5 offset:28800\n"
"v_mul_f32 v35, v35, v39\n"
"v_mfma_f32_16x16x32_bf16 v[48:51], a[232:235], v[124:127], v[48:51]\n"
"v_exp_f32 v69, v69\n"
"v_mfma_f32_16x16x32_bf16 v[48:51], a[220:223], v[120:123], v[48:51]\n"
"buffer_load_dwordx2 v[12:13], v14, s[32:35], s66 offen sc1\n"
"v_mul_f32 v40, v40, v42\n"
"v_mfma_f32_16x16x32_bf16 v[48:51], a[208:211], v[116:119], v[48:51]\n"
"ds_read_b128 v[100:103], v5 offset:29952\n"
"v_mul_f32 v41, v41, v43\n"
"v_mfma_f32_16x16x32_bf16 v[48:51], a[196:199], v[112:115], v[48:51]\n"
"v_fma_f32 v64, v64, v62, v62\n"
"v_fma_f32 v65, v65, v62, v62\n"
"v_mfma_f32_16x16x32_bf16 v[52:55], a[200:203], v[112:115], v[52:55]\n"
"v_rcp_f32 v64, v64\n"
"v_mfma_f32_16x16x32_bf16 v[52:55], a[212:215], v[116:119], v[52:55]\n"
"buffer_load_dwordx4 v6, s[32:35], s63 offen offset:2048 sc1 lds\n"
"ds_read_b128 v[104:107], v5 offset:31104\n"
"v_mfma_f32_16x16x32_bf16 v[52:55], a[224:227], v[120:123], v[52:55]\n"
"v_rcp_f32 v65, v65\n"
"v_mfma_f32_16x16x32_bf16 v[52:55], a[236:239], v[124:127], v[52:55]\n"
"v_fma_f32 v66, v66, v62, v62\n"
"s_waitcnt lgkmcnt(5)\n"
"v_mfma_f32_16x16x32_bf16 v[52:55], a[248:251], v[76:79], v[52:55]\n"
"ds_read_b128 v[108:111], v5 offset:32256\n"
"v_fma_f32 v67, v67, v62, v62\n"
"v_mfma_f32_16x16x32_bf16 v[52:55], v[132:135], v[80:83], v[52:55]\n"
"v_rcp_f32 v66, v66\n"
"v_mfma_f32_16x16x32_bf16 v[52:55], v[144:147], v[84:87], v[52:55]\n"
"v_rcp_f32 v67, v67\n"
"v_mfma_f32_16x16x32_bf16 v[52:55], v[156:159], v[88:91], v[52:55]\n"
"ds_read_b128 v[112:115], v5 offset:33408\n"
"v_fma_f32 v68, v68, v62, v62\n"
"v_mfma_f32_16x16x32_bf16 v[48:51], v[152:155], v[88:91], v[48:51]\n"
"v_rcp_f32 v68, v68\n"
"v_mfma_f32_16x16x32_bf16 v[48:51], v[140:143], v[84:87], v[48:51]\n"
"buffer_load_dwordx4 v6, s[32:35], s63 offen offset:3072 sc1 lds\n"
"v_fma_f32 v69, v69, v62, v62\n"
"v_mfma_f32_16x16x32_bf16 v[48:51], v[128:131], v[80:83], v[48:51]\n"
"ds_read_b128 v[116:119], v5 offset:34560\n"
"v_mul_f32 v32, v32, v64\n"
"v_mfma_f32_16x16x32_bf16 v[48:51], a[244:247], v[76:79], v[48:51]\n"
"v_rcp_f32 v69, v69\n"
"v_mfma_f32_16x16x32_bf16 v[44:47], a[240:243], v[76:79], v[44:47]\n"
"v_mul_f32 v33, v33, v65\n"
"v_mul_f32 v34, v34, v66\n"
"v_mfma_f32_16x16x32_bf16 v[44:47], a[252:255], v[80:83], v[44:47]\n"
"ds_read_b128 v[120:123], v5 offset:35712\n"
"v_mul_f32 v35, v35, v67\n"
"v_mfma_f32_16x16x32_bf16 v[44:47], v[136:139], v[84:87], v[44:47]\n"
"v_xor_b32 v5, 73728, v5\n"
"v_mul_f32 v40, v40, v68\n"
"v_mfma_f32_16x16x32_bf16 v[44:47], v[148:151], v[88:91], v[44:47]\n"
"s_waitcnt lgkmcnt(4)\n"
"s_waitcnt vmcnt(9)\n"
"s_barrier\n"
"v_mfma_f32_16x16x32_bf16 v[44:47], v[160:163], v[92:95], v[44:47]\n"
"ds_read_b128 v[76:79], v4 offset:0\n"
"s_add_u32 m0, s64, 45056\n"
"v_mfma_f32_16x16x32_bf16 v[44:47], v[172:175], v[96:99], v[44:47]\n"
"buffer_load_dwordx4 v7, s[32:35], s63 offen offset:0 sc1 lds\n"
"s_add_u32 s63, s63, 890880\n"
"v_mfma_f32_16x16x32_bf16 v[44:47], v[184:187], v[100:103], v[44:47]\n"
"s_xor_b32 s64, s64, s65\n"
"v_mul_f32 v41, v41, v69\n"
"v_mfma_f32_16x16x32_bf16 v[44:47], v[196:199], v[104:107], v[44:47]\n"
"ds_read_b128 v[80:83], v4 offset:1152\n"
"v_cvt_pk_bf16_f32 v74, v40, v41\n"
"v_mfma_f32_16x16x32_bf16 v[48:51], v[200:203], v[104:107], v[48:51]\n"
"v_cvt_pk_bf16_f32 v72, v32, v33\n"
"v_cvt_pk_bf16_f32 v73, v34, v35\n"
"v_mfma_f32_16x16x32_bf16 v[48:51], v[188:191], v[100:103], v[48:51]\n"
"v_permlane32_swap_b32 v74, v75\n"
"v_mfma_f32_16x16x32_bf16 v[48:51], v[176:179], v[96:99], v[48:51]\n"
"ds_read_b128 v[84:87], v4 offset:2304\n"
"v_mfma_f32_16x16x32_bf16 v[48:51], v[164:167], v[92:95], v[48:51]\n"
"v_permlane16_swap_b32 v72, v74\n"
"v_mfma_f32_16x16x32_bf16 v[52:55], v[168:171], v[92:95], v[52:55]\n"
"v_permlane16_swap_b32 v73, v75\n"
"v_mfma_f32_16x16x32_bf16 v[52:55], v[180:183], v[96:99], v[52:55]\n"
"buffer_store_dwordx4 v[72:75], v15, s[32:35], s68 offen sc1\n"
"ds_read_b128 v[88:91], v4 offset:3456\n"
"v_mfma_f32_16x16x32_bf16 v[52:55], v[192:195], v[100:103], v[52:55]\n"
"v_mfma_f32_16x16x32_bf16 v[52:55], v[204:207], v[104:107], v[52:55]\n"
"s_waitcnt lgkmcnt(4)\n"
"v_mfma_f32_16x16x32_bf16 v[52:55], v[216:219], v[108:111], v[52:55]\n"
"ds_read_b128 v[92:95], v4 offset:4608\n"
"v_mfma_f32_16x16x32_bf16 v[52:55], v[228:231], v[112:115], v[52:55]\n"
"v_mfma_f32_16x16x32_bf16 v[52:55], v[240:243], v[116:119], v[52:55]\n"
"v_mfma_f32_16x16x32_bf16 v[52:55], v[252:255], v[120:123], v[52:55]\n"
"ds_read_b128 v[96:99], v4 offset:5760\n"
"v_mfma_f32_16x16x32_bf16 v[48:51], v[248:251], v[120:123], v[48:51]\n"
"v_mfma_f32_16x16x32_bf16 v[48:51], v[236:239], v[116:119], v[48:51]\n"
"v_mfma_f32_16x16x32_bf16 v[48:51], v[224:227], v[112:115], v[48:51]\n"
"ds_read_b128 v[100:103], v4 offset:6912\n"
"v_mfma_f32_16x16x32_bf16 v[48:51], v[212:215], v[108:111], v[48:51]\n"
"v_mfma_f32_16x16x32_bf16 v[44:47], v[208:211], v[108:111], v[44:47]\n"
"v_mfma_f32_16x16x32_bf16 v[44:47], v[220:223], v[112:115], v[44:47]\n"
"ds_read_b128 v[104:107], v4 offset:8064\n"
"v_mfma_f32_16x16x32_bf16 v[44:47], v[232:235], v[116:119], v[44:47]\n"
"s_sub_u32 s62, s62, 1\n"
"v_mfma_f32_16x16x32_bf16 v[44:47], v[244:247], v[120:123], v[44:47]\n"
"s_cbranch_scc0 .L_gl_0\n"
"s_nop 1\n"
"ds_read_b32 v62, v11 offset:128\n"
"s_add_u32 s68, s68, 890880\n"
"s_waitcnt lgkmcnt(0)\n"
"v_lshlrev_b32 v60, 16, v62\n"
"v_and_b32 v62, 0xffff0000, v62\n"
"v_rcp_f32 v61, v62\n"
"v_mul_f32 v62, v62, v62\n"
"v_mul_f32 v61, 0xbfb8aa3b, v61\n"
"v_fma_f32 v52, -v60, v28, v52\n"
"v_fma_f32 v53, -v60, v29, v53\n"
"v_fma_f32 v54, -v60, v30, v54\n"
"v_fma_f32 v55, -v60, v31, v55\n"
"v_fma_f32 v48, -v60, v24, v48\n"
"v_fma_f32 v49, -v60, v25, v49\n"
"v_fma_f32 v50, -v60, v26, v50\n"
"v_fma_f32 v51, -v60, v27, v51\n"
"v_fma_f32 v44, -v60, v20, v44\n"
"v_fma_f32 v45, -v60, v21, v45\n"
"v_fma_f32 v46, -v60, v22, v46\n"
"v_fma_f32 v47, -v60, v23, v47\n"
"v_permlane32_swap_b32 v52, v54\n"
"v_permlane32_swap_b32 v53, v55\n"
"v_mul_f32 v64, v44, v61\n"
"v_mul_f32 v65, v45, v61\n"
"v_mul_f32 v66, v46, v61\n"
"v_mul_f32 v67, v47, v61\n"
"v_mul_f32 v68, v52, v61\n"
"v_mul_f32 v69, v53, v61\n"
"v_exp_f32 v64, v64\n"
"v_exp_f32 v65, v65\n"
"v_mul_f32 v44, v44, v48\n"
"v_mul_f32 v45, v45, v49\n"
"v_exp_f32 v66, v66\n"
"v_exp_f32 v67, v67\n"
"v_mul_f32 v46, v46, v50\n"
"v_mul_f32 v47, v47, v51\n"
"v_exp_f32 v68, v68\n"
"v_exp_f32 v69, v69\n"
"v_mul_f32 v52, v52, v54\n"
"v_mul_f32 v53, v53, v55\n"
"v_fma_f32 v64, v64, v62, v62\n"
"v_fma_f32 v65, v65, v62, v62\n"
"v_rcp_f32 v64, v64\n"
"v_rcp_f32 v65, v65\n"
"v_fma_f32 v66, v66, v62, v62\n"
"v_fma_f32 v67, v67, v62, v62\n"
"v_rcp_f32 v66, v66\n"
"v_rcp_f32 v67, v67\n"
"v_fma_f32 v68, v68, v62, v62\n"
"v_fma_f32 v69, v69, v62, v62\n"
"v_rcp_f32 v68, v68\n"
"v_rcp_f32 v69, v69\n"
"v_mul_f32 v44, v44, v64\n"
"v_mul_f32 v45, v45, v65\n"
"v_mul_f32 v46, v46, v66\n"
"v_mul_f32 v47, v47, v67\n"
"v_mul_f32 v52, v52, v68\n"
"v_mul_f32 v53, v53, v69\n"
"v_cvt_pk_bf16_f32 v74, v52, v53\n"
"v_cvt_pk_bf16_f32 v72, v44, v45\n"
"v_cvt_pk_bf16_f32 v73, v46, v47\n"
"v_permlane32_swap_b32 v74, v75\n"
"s_nop 1\n"
"v_permlane16_swap_b32 v72, v74\n"
"v_permlane16_swap_b32 v73, v75\n"
"buffer_store_dwordx4 v[72:75], v15, s[32:35], s68 offen sc1\n"
"s_waitcnt vmcnt(1)\n"
"s_barrier\n"
"buffer_store_dwordx2 v[16:17], v2, s[92:95], s68 idxen sc1\n"
"s_add_u32 s68, s68, 890880\n"
"s_waitcnt vmcnt(1)\n"
"s_barrier\n"
"buffer_store_dwordx2 v[16:17], v2, s[92:95], s68 idxen sc1\n"
"s_branch .L_end\n"
".L_gt0_0:\n"
"s_nop 1\n"
"ds_read_b32 v62, v11 offset:0\n"
"s_add_u32 s68, s68, 890880\n"
"s_waitcnt lgkmcnt(0)\n"
"v_lshlrev_b32 v60, 16, v62\n"
"v_and_b32 v62, 0xffff0000, v62\n"
"v_rcp_f32 v61, v62\n"
"v_mul_f32 v62, v62, v62\n"
"v_mul_f32 v61, 0xbfb8aa3b, v61\n"
"v_fma_f32 v40, -v60, v28, v40\n"
"v_fma_f32 v41, -v60, v29, v41\n"
"v_fma_f32 v42, -v60, v30, v42\n"
"v_fma_f32 v43, -v60, v31, v43\n"
"v_fma_f32 v36, -v60, v24, v36\n"
"v_fma_f32 v37, -v60, v25, v37\n"
"v_fma_f32 v38, -v60, v26, v38\n"
"v_fma_f32 v39, -v60, v27, v39\n"
"v_fma_f32 v32, -v60, v20, v32\n"
"v_fma_f32 v33, -v60, v21, v33\n"
"v_fma_f32 v34, -v60, v22, v34\n"
"v_fma_f32 v35, -v60, v23, v35\n"
"v_permlane32_swap_b32 v40, v42\n"
"v_permlane32_swap_b32 v41, v43\n"
"v_mul_f32 v64, v32, v61\n"
"v_mul_f32 v65, v33, v61\n"
"v_mul_f32 v66, v34, v61\n"
"v_mul_f32 v67, v35, v61\n"
"v_mul_f32 v68, v40, v61\n"
"v_mul_f32 v69, v41, v61\n"
"v_exp_f32 v64, v64\n"
"v_exp_f32 v65, v65\n"
"v_mul_f32 v32, v32, v36\n"
"v_mul_f32 v33, v33, v37\n"
"v_exp_f32 v66, v66\n"
"v_exp_f32 v67, v67\n"
"v_mul_f32 v34, v34, v38\n"
"v_mul_f32 v35, v35, v39\n"
"v_exp_f32 v68, v68\n"
"v_exp_f32 v69, v69\n"
"v_mul_f32 v40, v40, v42\n"
"v_mul_f32 v41, v41, v43\n"
"v_fma_f32 v64, v64, v62, v62\n"
"v_fma_f32 v65, v65, v62, v62\n"
"v_rcp_f32 v64, v64\n"
"v_rcp_f32 v65, v65\n"
"v_fma_f32 v66, v66, v62, v62\n"
"v_fma_f32 v67, v67, v62, v62\n"
"v_rcp_f32 v66, v66\n"
"v_rcp_f32 v67, v67\n"
"v_fma_f32 v68, v68, v62, v62\n"
"v_fma_f32 v69, v69, v62, v62\n"
"v_rcp_f32 v68, v68\n"
"v_rcp_f32 v69, v69\n"
"v_mul_f32 v32, v32, v64\n"
"v_mul_f32 v33, v33, v65\n"
"v_mul_f32 v34, v34, v66\n"
"v_mul_f32 v35, v35, v67\n"
"v_mul_f32 v40, v40, v68\n"
"v_mul_f32 v41, v41, v69\n"
"v_cvt_pk_bf16_f32 v74, v40, v41\n"
"v_cvt_pk_bf16_f32 v72, v32, v33\n"
"v_cvt_pk_bf16_f32 v73, v34, v35\n"
"v_permlane32_swap_b32 v74, v75\n"
"s_nop 1\n"
"v_permlane16_swap_b32 v72, v74\n"
"v_permlane16_swap_b32 v73, v75\n"
"buffer_store_dwordx4 v[72:75], v15, s[32:35], s68 offen sc1\n"
"s_waitcnt vmcnt(1)\n"
"s_barrier\n"
"buffer_store_dwordx2 v[16:17], v2, s[92:95], s68 idxen sc1\n"
"s_add_u32 s68, s68, 890880\n"
"s_waitcnt vmcnt(1)\n"
"s_barrier\n"
"buffer_store_dwordx2 v[16:17], v2, s[92:95], s68 idxen sc1\n"
"s_branch .L_end\n"
".p2align 6\n"
".L_gv1:\n"
".L_gl_1:\n"
"s_waitcnt lgkmcnt(4)\n"
"v_mfma_f32_16x16x32_bf16 v[32:35], a[0:3], v[76:79], 0\n"
"ds_read_b128 v[108:111], v4 offset:9216\n"
"ds_read_b32 v62, v11 offset:128\n"
"v_mfma_f32_16x16x32_bf16 v[32:35], a[12:15], v[80:83], v[32:35]\n"
"s_add_u32 s68, s68, 890880\n"
"v_mfma_f32_16x16x32_bf16 v[32:35], a[24:27], v[84:87], v[32:35]\n"
"v_mfma_f32_16x16x32_bf16 v[32:35], a[36:39], v[88:91], v[32:35]\n"
"ds_read_b128 v[112:115], v4 offset:10368\n"
"v_mfma_f32_16x16x32_bf16 v[36:39], a[40:43], v[88:91], 0\n"
"ds_read2st64_b32 v[56:57], v8 offset0:0 offset1:36\n"
"v_mfma_f32_16x16x32_bf16 v[36:39], a[28:31], v[84:87], v[36:39]\n"
"s_waitcnt vmcnt(4)\n"
"v_cmp_ne_u64 vcc, v[16:17], v[12:13]\n"
"v_mfma_f32_16x16x32_bf16 v[36:39], a[16:19], v[80:83], v[36:39]\n"
"ds_read_b128 v[116:119], v4 offset:11520\n"
"ds_read2st64_b32 v[58:59], v8 offset0:72 offset1:108\n"
"v_mfma_f32_16x16x32_bf16 v[36:39], a[4:7], v[76:79], v[36:39]\n"
"s_cbranch_vccnz .L_rtg0_1\n"
".L_bkg0_1:\n"
"s_add_u32 s66, s66, 890880\n"
"v_mfma_f32_16x16x32_bf16 v[40:43], a[8:11], v[76:79], 0\n"
"s_add_u32 m0, s64, 0\n"
"s_waitcnt lgkmcnt(4)\n"
"v_mfma_f32_16x16x32_bf16 v[40:43], a[20:23], v[80:83], v[40:43]\n"
"buffer_load_dwordx4 v2, s[32:35], s63 offen offset:0 sc1 lds\n"
"ds_read_b128 v[120:123], v4 offset:12672\n"
"v_mfma_f32_16x16x32_bf16 v[40:43], a[32:35], v[84:87], v[40:43]\n"
"v_lshlrev_b32 v60, 16, v62\n"
"v_and_b32 v62, 0xffff0000, v62\n"
"v_mfma_f32_16x16x32_bf16 v[40:43], a[44:47], v[88:91], v[40:43]\n"
"v_fma_f32 v52, -v60, v28, v52\n"
"v_mfma_f32_16x16x32_bf16 v[40:43], a[56:59], v[92:95], v[40:43]\n"
"ds_read_b128 v[124:127], v4 offset:13824\n"
"v_fma_f32 v53, -v60, v29, v53\n"
"v_mfma_f32_16x16x32_bf16 v[40:43], a[68:71], v[96:99], v[40:43]\n"
"v_rcp_f32 v61, v62\n"
"v_mfma_f32_16x16x32_bf16 v[40:43], a[80:83], v[100:103], v[40:43]\n"
"v_mul_f32 v62, v62, v62\n"
"v_mul_f32 v61, 0xbfb8aa3b, v61\n"
"v_mfma_f32_16x16x32_bf16 v[40:43], a[92:95], v[104:107], v[40:43]\n"
"ds_read_b128 v[76:79], v4 offset:14976\n"
"v_fma_f32 v54, -v60, v30, v54\n"
"v_mfma_f32_16x16x32_bf16 v[36:39], a[88:91], v[104:107], v[36:39]\n"
"v_fma_f32 v55, -v60, v31, v55\n"
"v_fma_f32 v48, -v60, v24, v48\n"
"v_mfma_f32_16x16x32_bf16 v[36:39], a[76:79], v[100:103], v[36:39]\n"
"buffer_load_dwordx4 v2, s[32:35], s63 offen offset:1024 sc1 lds\n"
"v_fma_f32 v49, -v60, v25, v49\n"
"v_mfma_f32_16x16x32_bf16 v[36:39], a[64:67], v[96:99], v[36:39]\n"
"ds_read_b128 v[80:83], v4 offset:16128\n"
"v_fma_f32 v50, -v60, v26, v50\n"
"v_mfma_f32_16x16x32_bf16 v[36:39], a[52:55], v[92:95], v[36:39]\n"
"v_permlane32_swap_b32 v52, v54\n"
"v_mfma_f32_16x16x32_bf16 v[32:35], a[48:51], v[92:95], v[32:35]\n"
"v_permlane32_swap_b32 v53, v55\n"
"v_mfma_f32_16x16x32_bf16 v[32:35], a[60:63], v[96:99], v[32:35]\n"
"ds_read_b128 v[84:87], v4 offset:17280\n"
"v_fma_f32 v51, -v60, v27, v51\n"
"v_mfma_f32_16x16x32_bf16 v[32:35], a[72:75], v[100:103], v[32:35]\n"
"v_fma_f32 v44, -v60, v20, v44\n"
"v_fma_f32 v45, -v60, v21, v45\n"
"v_mfma_f32_16x16x32_bf16 v[32:35], a[84:87], v[104:107], v[32:35]\n"
"v_fma_f32 v46, -v60, v22, v46\n"
"s_waitcnt lgkmcnt(4)\n"
"v_mfma_f32_16x16x32_bf16 v[32:35], a[96:99], v[108:111], v[32:35]\n"
"ds_read_b128 v[88:91], v4 offset:18432\n"
"v_add_f32 v56, v56, v57\n"
"v_mfma_f32_16x16x32_bf16 v[32:35], a[108:111], v[112:115], v[32:35]\n"
"buffer_load_dwordx4 v2, s[32:35], s63 offen offset:2048 sc1 lds\n"
"v_add_f32 v58, v58, v59\n"
"v_mfma_f32_16x16x32_bf16 v[32:35], a[120:123], v[116:119], v[32:35]\n"
"v_add_f32 v56, v56, v58\n"
"v_fma_f32 v47, -v60, v23, v47\n"
"v_mfma_f32_16x16x32_bf16 v[32:35], a[132:135], v[120:123], v[32:35]\n"
"ds_read_b128 v[92:95], v4 offset:19584\n"
"v_add_f32_dpp v56, v56, v56 row_ror:8 row_mask:0xf bank_mask:0xf\n"
"v_mfma_f32_16x16x32_bf16 v[36:39], a[136:139], v[120:123], v[36:39]\n"
"v_mul_f32 v64, v44, v61\n"
"v_add_f32_dpp v56, v56, v56 row_ror:4 row_mask:0xf bank_mask:0xf\n"
"v_mfma_f32_16x16x32_bf16 v[36:39], a[124:127], v[116:119], v[36:39]\n"
"v_exp_f32 v64, v64\n"
"v_mfma_f32_16x16x32_bf16 v[36:39], a[112:115], v[112:115], v[36:39]\n"
"ds_read_b128 v[96:99], v4 offset:20736\n"
"v_add_f32_dpp v56, v56, v56 row_ror:2 row_mask:0xf bank_mask:0xf\n"
"v_mfma_f32_16x16x32_bf16 v[36:39], a[100:103], v[108:111], v[36:39]\n"
"v_mul_f32 v56, 0x3a800000, v56\n"
"v_mul_f32 v65, v45, v61\n"
"v_mfma_f32_16x16x32_bf16 v[40:43], a[104:107], v[108:111], v[40:43]\n"
"v_exp_f32 v65, v65\n"
"v_mfma_f32_16x16x32_bf16 v[40:43], a[116:119], v[112:115], v[40:43]\n"
"buffer_load_dwordx4 v2, s[32:35], s63 offen offset:3072 sc1 lds\n"
"ds_read_b128 v[100:103], v4 offset:21888\n"
"v_mfma_f32_16x16x32_bf16 v[40:43], a[128:131], v[116:119], v[40:43]\n"
"v_mul_f32 v57, v56, v56\n"
"v_mul_f32 v66, v46, v61\n"
"v_mfma_f32_16x16x32_bf16 v[40:43], a[140:143], v[120:123], v[40:43]\n"
"v_sub_f32_dpp v57, v56, v57 quad_perm:[1,1,3,3] row_mask:0xf bank_mask:0xf\n"
"s_waitcnt lgkmcnt(4)\n"
"v_mfma_f32_16x16x32_bf16 v[40:43], a[152:155], v[124:127], v[40:43]\n"
"ds_read_b128 v[104:107], v4 offset:23040\n"
"v_max_f32 v57, 0, v57\n"
"v_mfma_f32_16x16x32_bf16 v[40:43], a[164:167], v[76:79], v[40:43]\n"
"v_exp_f32 v66, v66\n"
"v_mfma_f32_16x16x32_bf16 v[40:43], a[176:179], v[80:83], v[40:43]\n"
"v_cvt_pk_bf16_f32 v57, 0, v57\n"
"v_mul_f32 v67, v47, v61\n"
"v_mfma_f32_16x16x32_bf16 v[40:43], a[188:191], v[84:87], v[40:43]\n"
"ds_read_b128 v[108:111], v4 offset:24192\n"
"s_add_u32 m0, s64, 4096\n"
"v_mfma_f32_16x16x32_bf16 v[36:39], a[184:187], v[84:87], v[36:39]\n"
"v_exp_f32 v67, v67\n"
"v_mfma_f32_16x16x32_bf16 v[36:39], a[172:175], v[80:83], v[36:39]\n"
"buffer_load_dwordx4 v6, s[32:35], s63 offen offset:0 sc1 lds\n"
"v_add_f32 v57, 0x3727c5ac, v57\n"
"v_mfma_f32_16x16x32_bf16 v[36:39], a[160:163], v[76:79], v[36:39]\n"
"ds_read_b128 v[112:115], v4 offset:25344\n"
"v_cvt_pk_bf16_f32 v57, 0, v57\n"
"v_mfma_f32_16x16x32_bf16 v[36:39], a[148:151], v[124:127], v[36:39]\n"
"v_sqrt_f32 v57, v57\n"
"v_mfma_f32_16x16x32_bf16 v[32:35], a[144:147], v[124:127], v[32:35]\n"
"v_cvt_pk_bf16_f32 v57, v56, v57\n"
"v_mul_f32 v68, v52, v61\n"
"v_mfma_f32_16x16x32_bf16 v[32:35], a[156:159], v[76:79], v[32:35]\n"
"ds_read_b128 v[116:119], v4 offset:26496\n"
"ds_write_b32 v10, v57 offset:0\n"
"v_mfma_f32_16x16x32_bf16 v[32:35], a[168:171], v[80:83], v[32:35]\n"
"v_exp_f32 v68, v68\n"
"v_mfma_f32_16x16x32_bf16 v[32:35], a[180:183], v[84:87], v[32:35]\n"
"v_mul_f32 v69, v53, v61\n"
"s_waitcnt lgkmcnt(5)\n"
"v_mfma_f32_16x16x32_bf16 v[32:35], a[192:195], v[88:91], v[32:35]\n"
"ds_read_b128 v[120:123], v4 offset:27648\n"
"v_mul_f32 v44, v44, v48\n"
"v_mfma_f32_16x16x32_bf16 v[32:35], a[204:207], v[92:95], v[32:35]\n"
"buffer_load_dwordx4 v6, s[32:35], s63 offen offset:1024 sc1 lds\n"
"v_mul_f32 v45, v45, v49\n"
"v_mfma_f32_16x16x32_bf16 v[32:35], a[216:219], v[96:99], v[32:35]\n"
"v_exp_f32 v69, v69\n"
"v_mfma_f32_16x16x32_bf16 v[32:35], a[228:231], v[100:103], v[32:35]\n"
"ds_read_b128 v[124:127], v4 offset:28800\n"
"v_mul_f32 v46, v46, v50\n"
"v_mfma_f32_16x16x32_bf16 v[36:39], a[232:235], v[100:103], v[36:39]\n"
"v_mul_f32 v47, v47, v51\n"
"v_mul_f32 v52, v52, v54\n"
"v_mfma_f32_16x16x32_bf16 v[36:39], a[220:223], v[96:99], v[36:39]\n"
"buffer_load_dwordx2 v[12:13], v14, s[32:35], s66 offen sc1\n"
"v_mul_f32 v53, v53, v55\n"
"v_mfma_f32_16x16x32_bf16 v[36:39], a[208:211], v[92:95], v[36:39]\n"
"ds_read_b128 v[76:79], v4 offset:29952\n"
"v_fma_f32 v64, v64, v62, v62\n"
"v_mfma_f32_16x16x32_bf16 v[36:39], a[196:199], v[88:91], v[36:39]\n"
"v_rcp_f32 v64, v64\n"
"v_mfma_f32_16x16x32_bf16 v[40:43], a[200:203], v[88:91], v[40:43]\n"
"v_fma_f32 v65, v65, v62, v62\n"
"v_fma_f32 v66, v66, v62, v62\n"
"v_mfma_f32_16x16x32_bf16 v[40:43], a[212:215], v[92:95], v[40:43]\n"
"buffer_load_dwordx4 v6, s[32:35], s63 offen offset:2048 sc1 lds\n"
"ds_read_b128 v[80:83], v4 offset:31104\n"
"v_mfma_f32_16x16x32_bf16 v[40:43], a[224:227], v[96:99], v[40:43]\n"
"v_rcp_f32 v65, v65\n"
"v_mfma_f32_16x16x32_bf16 v[40:43], a[236:239], v[100:103], v[40:43]\n"
"v_fma_f32 v67, v67, v62, v62\n"
"s_waitcnt lgkmcnt(5)\n"
"v_mfma_f32_16x16x32_bf16 v[40:43], a[248:251], v[104:107], v[40:43]\n"
"ds_read_b128 v[84:87], v4 offset:32256\n"
"v_fma_f32 v68, v68, v62, v62\n"
"v_mfma_f32_16x16x32_bf16 v[40:43], v[132:135], v[108:111], v[40:43]\n"
"v_rcp_f32 v66, v66\n"
"v_mfma_f32_16x16x32_bf16 v[40:43], v[144:147], v[112:115], v[40:43]\n"
"v_rcp_f32 v67, v67\n"
"v_mfma_f32_16x16x32_bf16 v[40:43], v[156:159], v[116:119], v[40:43]\n"
"ds_read_b128 v[88:91], v4 offset:33408\n"
"v_fma_f32 v69, v69, v62, v62\n"
"v_mfma_f32_16x16x32_bf16 v[36:39], v[152:155], v[116:119], v[36:39]\n"
"v_rcp_f32 v68, v68\n"
"v_mfma_f32_16x16x32_bf16 v[36:39], v[140:143], v[112:115], v[36:39]\n"
"buffer_load_dwordx4 v6, s[32:35], s63 offen offset:3072 sc1 lds\n"
"v_mul_f32 v44, v44, v64\n"
"v_mfma_f32_16x16x32_bf16 v[36:39], v[128:131], v[108:111], v[36:39]\n"
"ds_read_b128 v[92:95], v4 offset:34560\n"
"v_mul_f32 v45, v45, v65\n"
"v_mfma_f32_16x16x32_bf16 v[36:39], a[244:247], v[104:107], v[36:39]\n"
"v_rcp_f32 v69, v69\n"
"v_mfma_f32_16x16x32_bf16 v[32:35], a[240:243], v[104:107], v[32:35]\n"
"v_mul_f32 v46, v46, v66\n"
"v_mul_f32 v47, v47, v67\n"
"v_mfma_f32_16x16x32_bf16 v[32:35], a[252:255], v[108:111], v[32:35]\n"
"ds_read_b128 v[96:99], v4 offset:35712\n"
"v_mul_f32 v52, v52, v68\n"
"v_mfma_f32_16x16x32_bf16 v[32:35], v[136:139], v[112:115], v[32:35]\n"
"s_waitcnt vmcnt(9)\n"
"s_waitcnt lgkmcnt(8)\n"
"s_barrier\n"
"v_mfma_f32_16x16x32_bf16 v[32:35], v[148:151], v[116:119], v[32:35]\n"
"v_xor_b32 v4, 73728, v4\n"
"s_waitcnt lgkmcnt(4)\n"
"v_mfma_f32_16x16x32_bf16 v[32:35], v[160:163], v[120:123], v[32:35]\n"
"ds_read_b128 v[100:103], v5 offset:0\n"
"s_add_u32 m0, s64, 8192\n"
"v_mfma_f32_16x16x32_bf16 v[32:35], v[172:175], v[124:127], v[32:35]\n"
"buffer_load_dwordx4 v7, s[32:35], s63 offen offset:0 sc1 lds\n"
"s_add_u32 s63, s63, 890880\n"
"v_mfma_f32_16x16x32_bf16 v[32:35], v[184:187], v[76:79], v[32:35]\n"
"v_mul_f32 v53, v53, v69\n"
"v_cvt_pk_bf16_f32 v72, v44, v45\n"
"v_mfma_f32_16x16x32_bf16 v[32:35], v[196:199], v[80:83], v[32:35]\n"
"ds_read_b128 v[104:107], v5 offset:1152\n"
"v_cvt_pk_bf16_f32 v74, v52, v53\n"
"v_mfma_f32_16x16x32_bf16 v[36:39], v[200:203], v[80:83], v[36:39]\n"
"v_cvt_pk_bf16_f32 v73, v46, v47\n"
"v_mfma_f32_16x16x32_bf16 v[36:39], v[188:191], v[76:79], v[36:39]\n"
"v_permlane32_swap_b32 v74, v75\n"
"v_mfma_f32_16x16x32_bf16 v[36:39], v[176:179], v[124:127], v[36:39]\n"
"ds_read_b128 v[108:111], v5 offset:2304\n"
"v_mfma_f32_16x16x32_bf16 v[36:39], v[164:167], v[120:123], v[36:39]\n"
"v_permlane16_swap_b32 v72, v74\n"
"v_mfma_f32_16x16x32_bf16 v[40:43], v[168:171], v[120:123], v[40:43]\n"
"v_permlane16_swap_b32 v73, v75\n"
"v_mfma_f32_16x16x32_bf16 v[40:43], v[180:183], v[124:127], v[40:43]\n"
"buffer_store_dwordx4 v[72:75], v15, s[32:35], s68 offen sc1\n"
"ds_read_b128 v[112:115], v5 offset:3456\n"
"v_mfma_f32_16x16x32_bf16 v[40:43], v[192:195], v[76:79], v[40:43]\n"
"v_mfma_f32_16x16x32_bf16 v[40:43], v[204:207], v[80:83], v[40:43]\n"
"s_waitcnt lgkmcnt(4)\n"
"v_mfma_f32_16x16x32_bf16 v[40:43], v[216:219], v[84:87], v[40:43]\n"
"ds_read_b128 v[116:119], v5 offset:4608\n"
"v_mfma_f32_16x16x32_bf16 v[40:43], v[228:231], v[88:91], v[40:43]\n"
"v_mfma_f32_16x16x32_bf16 v[40:43], v[240:243], v[92:95], v[40:43]\n"
"v_mfma_f32_16x16x32_bf16 v[40:43], v[252:255], v[96:99], v[40:43]\n"
"ds_read_b128 v[120:123], v5 offset:5760\n"
"v_mfma_f32_16x16x32_bf16 v[36:39], v[248:251], v[96:99], v[36:39]\n"
"v_mfma_f32_16x16x32_bf16 v[36:39], v[236:239], v[92:95], v[36:39]\n"
"v_mfma_f32_16x16x32_bf16 v[36:39], v[224:227], v[88:91], v[36:39]\n"
"ds_read_b128 v[124:127], v5 offset:6912\n"
"v_mfma_f32_16x16x32_bf16 v[36:39], v[212:215], v[84:87], v[36:39]\n"
"v_mfma_f32_16x16x32_bf16 v[32:35], v[208:211], v[84:87], v[32:35]\n"
"v_mfma_f32_16x16x32_bf16 v[32:35], v[220:223], v[88:91], v[32:35]\n"
"ds_read_b128 v[76:79], v5 offset:8064\n"
"v_mfma_f32_16x16x32_bf16 v[32:35], v[232:235], v[92:95], v[32:35]\n"
"s_sub_u32 s62, s62, 1\n"
"v_mfma_f32_16x16x32_bf16 v[32:35], v[244:247], v[96:99], v[32:35]\n"
"s_cbranch_scc1 .L_gt0_1\n"
"s_waitcnt lgkmcnt(4)\n"
"v_mfma_f32_16x16x32_bf16 v[44:47], a[0:3], v[100:103], 0\n"
"ds_read_b128 v[80:83], v5 offset:9216\n"
"ds_read_b32 v62, v11 offset:0\n"
"v_mfma_f32_16x16x32_bf16 v[44:47], a[12:15], v[104:107], v[44:47]\n"
"s_add_u32 s68, s68, 890880\n"
"v_mfma_f32_16x16x32_bf16 v[44:47], a[24:27], v[108:111], v[44:47]\n"
"v_mfma_f32_16x16x32_bf16 v[44:47], a[36:39], v[112:115], v[44:47]\n"
"ds_read_b128 v[84:87], v5 offset:10368\n"
"v_mfma_f32_16x16x32_bf16 v[48:51], a[40:43], v[112:115], 0\n"
"ds_read2st64_b32 v[56:57], v8 offset0:144 offset1:180\n"
"v_mfma_f32_16x16x32_bf16 v[48:51], a[28:31], v[108:111], v[48:51]\n"
"s_waitcnt vmcnt(4)\n"
"v_cmp_ne_u64 vcc, v[16:17], v[12:13]\n"
"v_mfma_f32_16x16x32_bf16 v[48:51], a[16:19], v[104:107], v[48:51]\n"
"ds_read_b128 v[88:91], v5 offset:11520\n"
"ds_read2st64_b32 v[58:59], v8 offset0:216 offset1:252\n"
"v_mfma_f32_16x16x32_bf16 v[48:51], a[4:7], v[100:103], v[48:51]\n"
"s_cbranch_vccnz .L_rtg1_1\n"
".L_bkg1_1:\n"
"s_add_u32 s66, s66, 890880\n"
"v_mfma_f32_16x16x32_bf16 v[52:55], a[8:11], v[100:103], 0\n"
"s_add_u32 m0, s64, 36864\n"
"v_xor_b32 v8, v9, v8\n"
"v_mfma_f32_16x16x32_bf16 v[52:55], a[20:23], v[104:107], v[52:55]\n"
"buffer_load_dwordx4 v2, s[32:35], s63 offen offset:0 sc1 lds\n"
"ds_read_b128 v[92:95], v5 offset:12672\n"
"v_mfma_f32_16x16x32_bf16 v[52:55], a[32:35], v[108:111], v[52:55]\n"
"s_waitcnt lgkmcnt(5)\n"
"v_lshlrev_b32 v60, 16, v62\n"
"v_mfma_f32_16x16x32_bf16 v[52:55], a[44:47], v[112:115], v[52:55]\n"
"v_and_b32 v62, 0xffff0000, v62\n"
"v_mfma_f32_16x16x32_bf16 v[52:55], a[56:59], v[116:119], v[52:55]\n"
"ds_read_b128 v[96:99], v5 offset:13824\n"
"v_fma_f32 v40, -v60, v28, v40\n"
"v_mfma_f32_16x16x32_bf16 v[52:55], a[68:71], v[120:123], v[52:55]\n"
"v_rcp_f32 v61, v62\n"
"v_mfma_f32_16x16x32_bf16 v[52:55], a[80:83], v[124:127], v[52:55]\n"
"v_mul_f32 v62, v62, v62\n"
"v_mul_f32 v61, 0xbfb8aa3b, v61\n"
"v_mfma_f32_16x16x32_bf16 v[52:55], a[92:95], v[76:79], v[52:55]\n"
"ds_read_b128 v[100:103], v5 offset:14976\n"
"v_fma_f32 v41, -v60, v29, v41\n"
"v_mfma_f32_16x16x32_bf16 v[48:51], a[88:91], v[76:79], v[48:51]\n"
"v_fma_f32 v42, -v60, v30, v42\n"
"v_fma_f32 v43, -v60, v31, v43\n"
"v_mfma_f32_16x16x32_bf16 v[48:51], a[76:79], v[124:127], v[48:51]\n"
"buffer_load_dwordx4 v2, s[32:35], s63 offen offset:1024 sc1 lds\n"
"v_fma_f32 v36, -v60, v24, v36\n"
"v_mfma_f32_16x16x32_bf16 v[48:51], a[64:67], v[120:123], v[48:51]\n"
"ds_read_b128 v[104:107], v5 offset:16128\n"
"v_fma_f32 v37, -v60, v25, v37\n"
"v_mfma_f32_16x16x32_bf16 v[48:51], a[52:55], v[116:119], v[48:51]\n"
"v_permlane32_swap_b32 v40, v42\n"
"v_mfma_f32_16x16x32_bf16 v[44:47], a[48:51], v[116:119], v[44:47]\n"
"v_permlane32_swap_b32 v41, v43\n"
"v_mfma_f32_16x16x32_bf16 v[44:47], a[60:63], v[120:123], v[44:47]\n"
"ds_read_b128 v[108:111], v5 offset:17280\n"
"v_fma_f32 v38, -v60, v26, v38\n"
"v_mfma_f32_16x16x32_bf16 v[44:47], a[72:75], v[124:127], v[44:47]\n"
"v_fma_f32 v39, -v60, v27, v39\n"
"v_fma_f32 v32, -v60, v20, v32\n"
"v_mfma_f32_16x16x32_bf16 v[44:47], a[84:87], v[76:79], v[44:47]\n"
"v_fma_f32 v33, -v60, v21, v33\n"
"s_waitcnt lgkmcnt(4)\n"
"v_mfma_f32_16x16x32_bf16 v[44:47], a[96:99], v[80:83], v[44:47]\n"
"ds_read_b128 v[112:115], v5 offset:18432\n"
"v_add_f32 v56, v56, v57\n"
"v_mfma_f32_16x16x32_bf16 v[44:47], a[108:111], v[84:87], v[44:47]\n"
"buffer_load_dwordx4 v2, s[32:35], s63 offen offset:2048 sc1 lds\n"
"v_add_f32 v58, v58, v59\n"
"v_mfma_f32_16x16x32_bf16 v[44:47], a[120:123], v[88:91], v[44:47]\n"
"v_add_f32 v56, v56, v58\n"
"v_fma_f32 v34, -v60, v22, v34\n"
"v_mfma_f32_16x16x32_bf16 v[44:47], a[132:135], v[92:95], v[44:47]\n"
"ds_read_b128 v[116:119], v5 offset:19584\n"
"v_add_f32_dpp v56, v56, v56 row_ror:8 row_mask:0xf bank_mask:0xf\n"
"v_mfma_f32_16x16x32_bf16 v[48:51], a[136:139], v[92:95], v[48:51]\n"
"v_fma_f32 v35, -v60, v23, v35\n"
"v_add_f32_dpp v56, v56, v56 row_ror:4 row_mask:0xf bank_mask:0xf\n"
"v_mfma_f32_16x16x32_bf16 v[48:51], a[124:127], v[88:91], v[48:51]\n"
"v_mul_f32 v64, v32, v61\n"
"v_add_f32_dpp v56, v56, v56 row_ror:2 row_mask:0xf bank_mask:0xf\n"
"v_mfma_f32_16x16x32_bf16 v[48:51], a[112:115], v[84:87], v[48:51]\n"
"ds_read_b128 v[120:123], v5 offset:20736\n"
"v_mul_f32 v56, 0x3a800000, v56\n"
"v_mfma_f32_16x16x32_bf16 v[48:51], a[100:103], v[80:83], v[48:51]\n"
"v_exp_f32 v64, v64\n"
"v_mfma_f32_16x16x32_bf16 v[52:55], a[104:107], v[80:83], v[52:55]\n"
"v_mul_f32 v57, v56, v56\n"
"v_mul_f32 v65, v33, v61\n"
"v_mfma_f32_16x16x32_bf16 v[52:55], a[116:119], v[84:87], v[52:55]\n"
"buffer_load_dwordx4 v2, s[32:35], s63 offen offset:3072 sc1 lds\n"
"ds_read_b128 v[124:127], v5 offset:21888\n"
"v_mfma_f32_16x16x32_bf16 v[52:55], a[128:131], v[88:91], v[52:55]\n"
"v_exp_f32 v65, v65\n"
"v_mfma_f32_16x16x32_bf16 v[52:55], a[140:143], v[92:95], v[52:55]\n"
"v_sub_f32_dpp v57, v56, v57 quad_perm:[1,1,3,3] row_mask:0xf bank_mask:0xf\n"
"s_waitcnt lgkmcnt(4)\n"
"v_mfma_f32_16x16x32_bf16 v[52:55], a[152:155], v[96:99], v[52:55]\n"
"ds_read_b128 v[76:79], v5 offset:23040\n"
"v_max_f32 v57, 0, v57\n"
"v_mfma_f32_16x16x32_bf16 v[52:55], a[164:167], v[100:103], v[52:55]\n"
"v_cvt_pk_bf16_f32 v57, 0, v57\n"
"v_mul_f32 v66, v34, v61\n"
"v_mfma_f32_16x16x32_bf16 v[52:55], a[176:179], v[104:107], v[52:55]\n"
"v_exp_f32 v66, v66\n"
"v_mfma_f32_16x16x32_bf16 v[52:55], a[188:191], v[108:111], v[52:55]\n"
"ds_read_b128 v[80:83], v5 offset:24192\n"
"s_add_u32 m0, s64, 40960\n"
"v_mfma_f32_16x16x32_bf16 v[48:51], a[184:187], v[108:111], v[48:51]\n"
"v_add_f32 v57, 0x3727c5ac, v57\n"
"v_mul_f32 v67, v35, v61\n"
"v_mfma_f32_16x16x32_bf16 v[48:51], a[172:175], v[104:107], v[48:51]\n"
"buffer_load_dwordx4 v6, s[32:35], s63 offen offset:0 sc1 lds\n"
"v_cvt_pk_bf16_f32 v57, 0, v57\n"
"v_mfma_f32_16x16x32_bf16 v[48:51], a[160:163], v[100:103], v[48:51]\n"
"ds_read_b128 v[84:87], v5 offset:25344\n"
"v_mul_f32 v68, v40, v61\n"
"v_mfma_f32_16x16x32_bf16 v[48:51], a[148:151], v[96:99], v[48:51]\n"
"v_sqrt_f32 v57, v57\n"
"v_mfma_f32_16x16x32_bf16 v[44:47], a[144:147], v[96:99], v[44:47]\n"
"v_exp_f32 v67, v67\n"
"v_mfma_f32_16x16x32_bf16 v[44:47], a[156:159], v[100:103], v[44:47]\n"
"ds_read_b128 v[88:91], v5 offset:26496\n"
"v_cvt_pk_bf16_f32 v57, v56, v57\n"
"v_mfma_f32_16x16x32_bf16 v[44:47], a[168:171], v[104:107], v[44:47]\n"
"ds_write_b32 v10, v57 offset:128\n"
"v_mul_f32 v69, v41, v61\n"
"v_mfma_f32_16x16x32_bf16 v[44:47], a[180:183], v[108:111], v[44:47]\n"
"v_mul_f32 v32, v32, v36\n"
"s_waitcnt lgkmcnt(5)\n"
"v_mfma_f32_16x16x32_bf16 v[44:47], a[192:195], v[112:115], v[44:47]\n"
"ds_read_b128 v[92:95], v5 offset:27648\n"
"v_mul_f32 v33, v33, v37\n"
"v_mfma_f32_16x16x32_bf16 v[44:47], a[204:207], v[116:119], v[44:47]\n"
"buffer_load_dwordx4 v6, s[32:35], s63 offen offset:1024 sc1 lds\n"
"v_mul_f32 v34, v34, v38\n"
"v_mfma_f32_16x16x32_bf16 v[44:47], a[216:219], v[120:123], v[44:47]\n"
"v_exp_f32 v68, v68\n"
"v_mfma_f32_16x16x32_bf16 v[44:47], a[228:231], v[124:127], v[44:47]\n"
"ds_read_b128 v[96:99], v5 offset:28800\n"
"v_mul_f32 v35, v35, v39\n"
"v_mfma_f32_16x16x32_bf16 v[48:51], a[232:235], v[124:127], v[48:51]\n"
"v_exp_f32 v69, v69\n"
"v_mfma_f32_16x16x32_bf16 v[48:51], a[220:223], v[120:123], v[48:51]\n"
"buffer_load_dwordx2 v[12:13], v14, s[32:35], s66 offen sc1\n"
"v_mul_f32 v40, v40, v42\n"
"v_mfma_f32_16x16x32_bf16 v[48:51], a[208:211], v[116:119], v[48:51]\n"
"ds_read_b128 v[100:103], v5 offset:29952\n"
"v_mul_f32 v41, v41, v43\n"
"v_mfma_f32_16x16x32_bf16 v[48:51], a[196:199], v[112:115], v[48:51]\n"
"v_fma_f32 v64, v64, v62, v62\n"
"v_fma_f32 v65, v65, v62, v62\n"
"v_mfma_f32_16x16x32_bf16 v[52:55], a[200:203], v[112:115], v[52:55]\n"
"v_rcp_f32 v64, v64\n"
"v_mfma_f32_16x16x32_bf16 v[52:55], a[212:215], v[116:119], v[52:55]\n"
"buffer_load_dwordx4 v6, s[32:35], s63 offen offset:2048 sc1 lds\n"
"ds_read_b128 v[104:107], v5 offset:31104\n"
"v_mfma_f32_16x16x32_bf16 v[52:55], a[224:227], v[120:123], v[52:55]\n"
"v_rcp_f32 v65, v65\n"
"v_mfma_f32_16x16x32_bf16 v[52:55], a[236:239], v[124:127], v[52:55]\n"
"v_fma_f32 v66, v66, v62, v62\n"
"s_waitcnt lgkmcnt(5)\n"
"v_mfma_f32_16x16x32_bf16 v[52:55], a[248:251], v[76:79], v[52:55]\n"
"ds_read_b128 v[108:111], v5 offset:32256\n"
"v_fma_f32 v67, v67, v62, v62\n"
"v_mfma_f32_16x16x32_bf16 v[52:55], v[132:135], v[80:83], v[52:55]\n"
"v_rcp_f32 v66, v66\n"
"v_mfma_f32_16x16x32_bf16 v[52:55], v[144:147], v[84:87], v[52:55]\n"
"v_rcp_f32 v67, v67\n"
"v_mfma_f32_16x16x32_bf16 v[52:55], v[156:159], v[88:91], v[52:55]\n"
"ds_read_b128 v[112:115], v5 offset:33408\n"
"v_fma_f32 v68, v68, v62, v62\n"
"v_mfma_f32_16x16x32_bf16 v[48:51], v[152:155], v[88:91], v[48:51]\n"
"v_rcp_f32 v68, v68\n"
"v_mfma_f32_16x16x32_bf16 v[48:51], v[140:143], v[84:87], v[48:51]\n"
"buffer_load_dwordx4 v6, s[32:35], s63 offen offset:3072 sc1 lds\n"
"v_fma_f32 v69, v69, v62, v62\n"
"v_mfma_f32_16x16x32_bf16 v[48:51], v[128:131], v[80:83], v[48:51]\n"
"ds_read_b128 v[116:119], v5 offset:34560\n"
"v_mul_f32 v32, v32, v64\n"
"v_mfma_f32_16x16x32_bf16 v[48:51], a[244:247], v[76:79], v[48:51]\n"
"v_rcp_f32 v69, v69\n"
"v_mfma_f32_16x16x32_bf16 v[44:47], a[240:243], v[76:79], v[44:47]\n"
"v_mul_f32 v33, v33, v65\n"
"v_mul_f32 v34, v34, v66\n"
"v_mfma_f32_16x16x32_bf16 v[44:47], a[252:255], v[80:83], v[44:47]\n"
"ds_read_b128 v[120:123], v5 offset:35712\n"
"v_mul_f32 v35, v35, v67\n"
"v_mfma_f32_16x16x32_bf16 v[44:47], v[136:139], v[84:87], v[44:47]\n"
"s_waitcnt vmcnt(9)\n"
"s_waitcnt lgkmcnt(8)\n"
"s_barrier\n"
"v_mfma_f32_16x16x32_bf16 v[44:47], v[148:151], v[88:91], v[44:47]\n"
"v_xor_b32 v5, 73728, v5\n"
"s_waitcnt lgkmcnt(4)\n"
"v_mfma_f32_16x16x32_bf16 v[44:47], v[160:163], v[92:95], v[44:47]\n"
"ds_read_b128 v[76:79], v4 offset:0\n"
"s_add_u32 m0, s64, 45056\n"
"v_mfma_f32_16x16x32_bf16 v[44:47], v[172:175], v[96:99], v[44:47]\n"
"buffer_load_dwordx4 v7, s[32:35], s63 offen offset:0 sc1 lds\n"
"s_add_u32 s63, s63, 890880\n"
"v_mfma_f32_16x16x32_bf16 v[44:47], v[184:187], v[100:103], v[44:47]\n"
"s_xor_b32 s64, s64, s65\n"
"v_mul_f32 v40, v40, v68\n"
"v_mfma_f32_16x16x32_bf16 v[44:47], v[196:199], v[104:107], v[44:47]\n"
"ds_read_b128 v[80:83], v4 offset:1152\n"
"v_mul_f32 v41, v41, v69\n"
"v_mfma_f32_16x16x32_bf16 v[48:51], v[200:203], v[104:107], v[48:51]\n"
"v_cvt_pk_bf16_f32 v74, v40, v41\n"
"v_cvt_pk_bf16_f32 v72, v32, v33\n"
"v_mfma_f32_16x16x32_bf16 v[48:51], v[188:191], v[100:103], v[48:51]\n"
"v_permlane32_swap_b32 v74, v75\n"
"v_mfma_f32_16x16x32_bf16 v[48:51], v[176:179], v[96:99], v[48:51]\n"
"ds_read_b128 v[84:87], v4 offset:2304\n"
"v_cvt_pk_bf16_f32 v73, v34, v35\n"
"v_mfma_f32_16x16x32_bf16 v[48:51], v[164:167], v[92:95], v[48:51]\n"
"v_permlane16_swap_b32 v72, v74\n"
"v_mfma_f32_16x16x32_bf16 v[52:55], v[168:171], v[92:95], v[52:55]\n"
"v_permlane16_swap_b32 v73, v75\n"
"v_mfma_f32_16x16x32_bf16 v[52:55], v[180:183], v[96:99], v[52:55]\n"
"buffer_store_dwordx4 v[72:75], v15, s[32:35], s68 offen sc1\n"
"ds_read_b128 v[88:91], v4 offset:3456\n"
"v_mfma_f32_16x16x32_bf16 v[52:55], v[192:195], v[100:103], v[52:55]\n"
"v_mfma_f32_16x16x32_bf16 v[52:55], v[204:207], v[104:107], v[52:55]\n"
"s_waitcnt lgkmcnt(4)\n"
"v_mfma_f32_16x16x32_bf16 v[52:55], v[216:219], v[108:111], v[52:55]\n"
"ds_read_b128 v[92:95], v4 offset:4608\n"
"v_mfma_f32_16x16x32_bf16 v[52:55], v[228:231], v[112:115], v[52:55]\n"
"v_mfma_f32_16x16x32_bf16 v[52:55], v[240:243], v[116:119], v[52:55]\n"
"v_mfma_f32_16x16x32_bf16 v[52:55], v[252:255], v[120:123], v[52:55]\n"
"ds_read_b128 v[96:99], v4 offset:5760\n"
"v_mfma_f32_16x16x32_bf16 v[48:51], v[248:251], v[120:123], v[48:51]\n"
"v_mfma_f32_16x16x32_bf16 v[48:51], v[236:239], v[116:119], v[48:51]\n"
"v_mfma_f32_16x16x32_bf16 v[48:51], v[224:227], v[112:115], v[48:51]\n"
"ds_read_b128 v[100:103], v4 offset:6912\n"
"v_mfma_f32_16x16x32_bf16 v[48:51], v[212:215], v[108:111], v[48:51]\n"
"v_mfma_f32_16x16x32_bf16 v[44:47], v[208:211], v[108:111], v[44:47]\n"
"v_mfma_f32_16x16x32_bf16 v[44:47], v[220:223], v[112:115], v[44:47]\n"
"ds_read_b128 v[104:107], v4 offset:8064\n"
"v_mfma_f32_16x16x32_bf16 v[44:47], v[232:235], v[116:119], v[44:47]\n"
"s_sub_u32 s62, s62, 1\n"
"v_mfma_f32_16x16x32_bf16 v[44:47], v[244:247], v[120:123], v[44:47]\n"
"s_cbranch_scc0 .L_gl_1\n"
"s_nop 1\n"
"ds_read_b32 v62, v11 offset:128\n"
"s_add_u32 s68, s68, 890880\n"
"s_waitcnt lgkmcnt(0)\n"
"v_lshlrev_b32 v60, 16, v62\n"
"v_and_b32 v62, 0xffff0000, v62\n"
"v_rcp_f32 v61, v62\n"
"v_mul_f32 v62, v62, v62\n"
"v_mul_f32 v61, 0xbfb8aa3b, v61\n"
"v_fma_f32 v52, -v60, v28, v52\n"
"v_fma_f32 v53, -v60, v29, v53\n"
"v_fma_f32 v54, -v60, v30, v54\n"
"v_fma_f32 v55, -v60, v31, v55\n"
"v_fma_f32 v48, -v60, v24, v48\n"
"v_fma_f32 v49, -v60, v25, v49\n"
"v_fma_f32 v50, -v60, v26, v50\n"
"v_fma_f32 v51, -v60, v27, v51\n"
"v_fma_f32 v44, -v60, v20, v44\n"
"v_fma_f32 v45, -v60, v21, v45\n"
"v_fma_f32 v46, -v60, v22, v46\n"
"v_fma_f32 v47, -v60, v23, v47\n"
"v_permlane32_swap_b32 v52, v54\n"
"v_permlane32_swap_b32 v53, v55\n"
"v_mul_f32 v64, v44, v61\n"
"v_mul_f32 v65, v45, v61\n"
"v_mul_f32 v66, v46, v61\n"
"v_mul_f32 v67, v47, v61\n"
"v_mul_f32 v68, v52, v61\n"
"v_mul_f32 v69, v53, v61\n"
"v_exp_f32 v64, v64\n"
"v_exp_f32 v65, v65\n"
"v_mul_f32 v44, v44, v48\n"
"v_mul_f32 v45, v45, v49\n"
"v_exp_f32 v66, v66\n"
"v_exp_f32 v67, v67\n"
"v_mul_f32 v46, v46, v50\n"
"v_mul_f32 v47, v47, v51\n"
"v_exp_f32 v68, v68\n"
"v_exp_f32 v69, v69\n"
"v_mul_f32 v52, v52, v54\n"
"v_mul_f32 v53, v53, v55\n"
"v_fma_f32 v64, v64, v62, v62\n"
"v_fma_f32 v65, v65, v62, v62\n"
"v_rcp_f32 v64, v64\n"
"v_rcp_f32 v65, v65\n"
"v_fma_f32 v66, v66, v62, v62\n"
"v_fma_f32 v67, v67, v62, v62\n"
"v_rcp_f32 v66, v66\n"
"v_rcp_f32 v67, v67\n"
"v_fma_f32 v68, v68, v62, v62\n"
"v_fma_f32 v69, v69, v62, v62\n"
"v_rcp_f32 v68, v68\n"
"v_rcp_f32 v69, v69\n"
"v_mul_f32 v44, v44, v64\n"
"v_mul_f32 v45, v45, v65\n"
"v_mul_f32 v46, v46, v66\n"
"v_mul_f32 v47, v47, v67\n"
"v_mul_f32 v52, v52, v68\n"
"v_mul_f32 v53, v53, v69\n"
"v_cvt_pk_bf16_f32 v74, v52, v53\n"
"v_cvt_pk_bf16_f32 v72, v44, v45\n"
"v_cvt_pk_bf16_f32 v73, v46, v47\n"
"v_permlane32_swap_b32 v74, v75\n"
"s_nop 1\n"
"v_permlane16_swap_b32 v72, v74\n"
"v_permlane16_swap_b32 v73, v75\n"
"buffer_store_dwordx4 v[72:75], v15, s[32:35], s68 offen sc1\n"
"s_waitcnt vmcnt(1)\n"
"s_barrier\n"
"buffer_store_dwordx2 v[16:17], v2, s[92:95], s68 idxen sc1\n"
"s_add_u32 s68, s68, 890880\n"
"s_waitcnt vmcnt(1)\n"
"s_barrier\n"
"buffer_store_dwordx2 v[16:17], v2, s[92:95], s68 idxen sc1\n"
"s_branch .L_end\n"
".L_gt0_1:\n"
"s_nop 1\n"
"ds_read_b32 v62, v11 offset:0\n"
"s_add_u32 s68, s68, 890880\n"
"s_waitcnt lgkmcnt(0)\n"
"v_lshlrev_b32 v60, 16, v62\n"
"v_and_b32 v62, 0xffff0000, v62\n"
"v_rcp_f32 v61, v62\n"
"v_mul_f32 v62, v62, v62\n"
"v_mul_f32 v61, 0xbfb8aa3b, v61\n"
"v_fma_f32 v40, -v60, v28, v40\n"
"v_fma_f32 v41, -v60, v29, v41\n"
"v_fma_f32 v42, -v60, v30, v42\n"
"v_fma_f32 v43, -v60, v31, v43\n"
"v_fma_f32 v36, -v60, v24, v36\n"
"v_fma_f32 v37, -v60, v25, v37\n"
"v_fma_f32 v38, -v60, v26, v38\n"
"v_fma_f32 v39, -v60, v27, v39\n"
"v_fma_f32 v32, -v60, v20, v32\n"
"v_fma_f32 v33, -v60, v21, v33\n"
"v_fma_f32 v34, -v60, v22, v34\n"
"v_fma_f32 v35, -v60, v23, v35\n"
"v_permlane32_swap_b32 v40, v42\n"
"v_permlane32_swap_b32 v41, v43\n"
"v_mul_f32 v64, v32, v61\n"
"v_mul_f32 v65, v33, v61\n"
"v_mul_f32 v66, v34, v61\n"
"v_mul_f32 v67, v35, v61\n"
"v_mul_f32 v68, v40, v61\n"
"v_mul_f32 v69, v41, v61\n"
"v_exp_f32 v64, v64\n"
"v_exp_f32 v65, v65\n"
"v_mul_f32 v32, v32, v36\n"
"v_mul_f32 v33, v33, v37\n"
"v_exp_f32 v66, v66\n"
"v_exp_f32 v67, v67\n"
"v_mul_f32 v34, v34, v38\n"
"v_mul_f32 v35, v35, v39\n"
"v_exp_f32 v68, v68\n"
"v_exp_f32 v69, v69\n"
"v_mul_f32 v40, v40, v42\n"
"v_mul_f32 v41, v41, v43\n"
"v_fma_f32 v64, v64, v62, v62\n"
"v_fma_f32 v65, v65, v62, v62\n"
"v_rcp_f32 v64, v64\n"
"v_rcp_f32 v65, v65\n"
"v_fma_f32 v66, v66, v62, v62\n"
"v_fma_f32 v67, v67, v62, v62\n"
"v_rcp_f32 v66, v66\n"
"v_rcp_f32 v67, v67\n"
"v_fma_f32 v68, v68, v62, v62\n"
"v_fma_f32 v69, v69, v62, v62\n"
"v_rcp_f32 v68, v68\n"
"v_rcp_f32 v69, v69\n"
"v_mul_f32 v32, v32, v64\n"
"v_mul_f32 v33, v33, v65\n"
"v_mul_f32 v34, v34, v66\n"
"v_mul_f32 v35, v35, v67\n"
"v_mul_f32 v40, v40, v68\n"
"v_mul_f32 v41, v41, v69\n"
"v_cvt_pk_bf16_f32 v74, v40, v41\n"
"v_cvt_pk_bf16_f32 v72, v32, v33\n"
"v_cvt_pk_bf16_f32 v73, v34, v35\n"
"v_permlane32_swap_b32 v74, v75\n"
"s_nop 1\n"
"v_permlane16_swap_b32 v72, v74\n"
"v_permlane16_swap_b32 v73, v75\n"
"buffer_store_dwordx4 v[72:75], v15, s[32:35], s68 offen sc1\n"
"s_waitcnt vmcnt(1)\n"
"s_barrier\n"
"buffer_store_dwordx2 v[16:17], v2, s[92:95], s68 idxen sc1\n"
"s_add_u32 s68, s68, 890880\n"
"s_waitcnt vmcnt(1)\n"
"s_barrier\n"
"buffer_store_dwordx2 v[16:17], v2, s[92:95], s68 idxen sc1\n"
"s_branch .L_end\n"
".p2align 6\n"
".L_gv2:\n"
".L_gl_2:\n"
"s_waitcnt lgkmcnt(4)\n"
"v_mfma_f32_16x16x32_bf16 v[32:35], a[0:3], v[76:79], 0\n"
"ds_read_b128 v[108:111], v4 offset:9216\n"
"ds_read_b32 v62, v11 offset:128\n"
"v_mfma_f32_16x16x32_bf16 v[32:35], a[12:15], v[80:83], v[32:35]\n"
"s_add_u32 s68, s68, 890880\n"
"v_mfma_f32_16x16x32_bf16 v[32:35], a[24:27], v[84:87], v[32:35]\n"
"v_mfma_f32_16x16x32_bf16 v[32:35], a[36:39], v[88:91], v[32:35]\n"
"ds_read_b128 v[112:115], v4 offset:10368\n"
"v_mfma_f32_16x16x32_bf16 v[36:39], a[40:43], v[88:91], 0\n"
"ds_read2st64_b32 v[56:57], v8 offset0:0 offset1:36\n"
"v_mfma_f32_16x16x32_bf16 v[36:39], a[28:31], v[84:87], v[36:39]\n"
"s_waitcnt vmcnt(4)\n"
"v_cmp_ne_u64 vcc, v[16:17], v[12:13]\n"
"v_mfma_f32_16x16x32_bf16 v[36:39], a[16:19], v[80:83], v[36:39]\n"
"ds_read_b128 v[116:119], v4 offset:11520\n"
"ds_read2st64_b32 v[58:59], v8 offset0:72 offset1:108\n"
"v_mfma_f32_16x16x32_bf16 v[36:39], a[4:7], v[76:79], v[36:39]\n"
"s_cbranch_vccnz .L_rtg0_2\n"
".L_bkg0_2:\n"
"s_add_u32 s66, s66, 890880\n"
"v_mfma_f32_16x16x32_bf16 v[40:43], a[8:11], v[76:79], 0\n"
"s_add_u32 m0, s64, 0\n"
"s_waitcnt lgkmcnt(4)\n"
"v_mfma_f32_16x16x32_bf16 v[40:43], a[20:23], v[80:83], v[40:43]\n"
"buffer_load_dwordx4 v2, s[32:35], s63 offen offset:0 sc1 lds\n"
"ds_read_b128 v[120:123], v4 offset:12672\n"
"v_mfma_f32_16x16x32_bf16 v[40:43], a[32:35], v[84:87], v[40:43]\n"
"v_lshlrev_b32 v60, 16, v62\n"
"v_and_b32 v62, 0xffff0000, v62\n"
"v_mfma_f32_16x16x32_bf16 v[40:43], a[44:47], v[88:91], v[40:43]\n"
"v_fma_f32 v52, -v60, v28, v52\n"
"v_mfma_f32_16x16x32_bf16 v[40:43], a[56:59], v[92:95], v[40:43]\n"
"ds_read_b128 v[124:127], v4 offset:13824\n"
"v_fma_f32 v53, -v60, v29, v53\n"
"v_mfma_f32_16x16x32_bf16 v[40:43], a[68:71], v[96:99], v[40:43]\n"
"v_rcp_f32 v61, v62\n"
"v_mfma_f32_16x16x32_bf16 v[40:43], a[80:83], v[100:103], v[40:43]\n"
"v_mul_f32 v62, v62, v62\n"
"v_mul_f32 v61, 0xbfb8aa3b, v61\n"
"v_mfma_f32_16x16x32_bf16 v[40:43], a[92:95], v[104:107], v[40:43]\n"
"ds_read_b128 v[76:79], v4 offset:14976\n"
"v_fma_f32 v54, -v60, v30, v54\n"
"v_mfma_f32_16x16x32_bf16 v[36:39], a[88:91], v[104:107], v[36:39]\n"
"v_fma_f32 v55, -v60, v31, v55\n"
"v_fma_f32 v48, -v60, v24, v48\n"
"v_mfma_f32_16x16x32_bf16 v[36:39], a[76:79], v[100:103], v[36:39]\n"
"buffer_load_dwordx4 v2, s[32:35], s63 offen offset:1024 sc1 lds\n"
"v_fma_f32 v49, -v60, v25, v49\n"
"v_mfma_f32_16x16x32_bf16 v[36:39], a[64:67], v[96:99], v[36:39]\n"
"ds_read_b128 v[80:83], v4 offset:16128\n"
"v_fma_f32 v50, -v60, v26, v50\n"
"v_mfma_f32_16x16x32_bf16 v[36:39], a[52:55], v[92:95], v[36:39]\n"
"v_permlane32_swap_b32 v52, v54\n"
"v_mfma_f32_16x16x32_bf16 v[32:35], a[48:51], v[92:95], v[32:35]\n"
"v_permlane32_swap_b32 v53, v55\n"
"v_mfma_f32_16x16x32_bf16 v[32:35], a[60:63], v[96:99], v[32:35]\n"
"ds_read_b128 v[84:87], v4 offset:17280\n"
"v_fma_f32 v51, -v60, v27, v51\n"
"v_mfma_f32_16x16x32_bf16 v[32:35], a[72:75], v[100:103], v[32:35]\n"
"v_fma_f32 v44, -v60, v20, v44\n"
"v_fma_f32 v45, -v60, v21, v45\n"
"v_mfma_f32_16x16x32_bf16 v[32:35], a[84:87], v[104:107], v[32:35]\n"
"v_fma_f32 v46, -v60, v22, v46\n"
"s_waitcnt lgkmcnt(4)\n"
"v_mfma_f32_16x16x32_bf16 v[32:35], a[96:99], v[108:111], v[32:35]\n"
"ds_read_b128 v[88:91], v4 offset:18432\n"
"v_add_f32 v56, v56, v57\n"
"v_mfma_f32_16x16x32_bf16 v[32:35], a[108:111], v[112:115], v[32:35]\n"
"buffer_load_dwordx4 v2, s[32:35], s63 offen offset:2048 sc1 lds\n"
"v_add_f32 v58, v58, v59\n"
"v_mfma_f32_16x16x32_bf16 v[32:35], a[120:123], v[116:119], v[32:35]\n"
"v_add_f32 v56, v56, v58\n"
"v_fma_f32 v47, -v60, v23, v47\n"
"v_mfma_f32_16x16x32_bf16 v[32:35], a[132:135], v[120:123], v[32:35]\n"
"ds_read_b128 v[92:95], v4 offset:19584\n"
"v_add_f32_dpp v56, v56, v56 row_ror:8 row_mask:0xf bank_mask:0xf\n"
"v_mfma_f32_16x16x32_bf16 v[36:39], a[136:139], v[120:123], v[36:39]\n"
"v_mul_f32 v64, v44, v61\n"
"v_add_f32_dpp v56, v56, v56 row_ror:4 row_mask:0xf bank_mask:0xf\n"
"v_mfma_f32_16x16x32_bf16 v[36:39], a[124:127], v[116:119], v[36:39]\n"
"v_exp_f32 v64, v64\n"
"v_mfma_f32_16x16x32_bf16 v[36:39], a[112:115], v[112:115], v[36:39]\n"
"ds_read_b128 v[96:99], v4 offset:20736\n"
"v_add_f32_dpp v56, v56, v56 row_ror:2 row_mask:0xf bank_mask:0xf\n"
"v_mfma_f32_16x16x32_bf16 v[36:39], a[100:103], v[108:111], v[36:39]\n"
"v_mul_f32 v56, 0x3a800000, v56\n"
"v_mul_f32 v65, v45, v61\n"
"v_mfma_f32_16x16x32_bf16 v[40:43], a[104:107], v[108:111], v[40:43]\n"
"v_exp_f32 v65, v65\n"
"v_mfma_f32_16x16x32_bf16 v[40:43], a[116:119], v[112:115], v[40:43]\n"
"buffer_load_dwordx4 v2, s[32:35], s63 offen offset:3072 sc1 lds\n"
"ds_read_b128 v[100:103], v4 offset:21888\n"
"v_mfma_f32_16x16x32_bf16 v[40:43], a[128:131], v[116:119], v[40:43]\n"
"v_mul_f32 v57, v56, v56\n"
"v_mul_f32 v66, v46, v61\n"
"v_mfma_f32_16x16x32_bf16 v[40:43], a[140:143], v[120:123], v[40:43]\n"
"v_sub_f32_dpp v57, v56, v57 quad_perm:[1,1,3,3] row_mask:0xf bank_mask:0xf\n"
"s_waitcnt lgkmcnt(4)\n"
"v_mfma_f32_16x16x32_bf16 v[40:43], a[152:155], v[124:127], v[40:43]\n"
"ds_read_b128 v[104:107], v4 offset:23040\n"
"v_max_f32 v57, 0, v57\n"
"v_mfma_f32_16x16x32_bf16 v[40:43], a[164:167], v[76:79], v[40:43]\n"
"v_exp_f32 v66, v66\n"
"v_mfma_f32_16x16x32_bf16 v[40:43], a[176:179], v[80:83], v[40:43]\n"
"v_cvt_pk_bf16_f32 v57, 0, v57\n"
"v_mul_f32 v67, v47, v61\n"
"v_mfma_f32_16x16x32_bf16 v[40:43], a[188:191], v[84:87], v[40:43]\n"
"ds_read_b128 v[108:111], v4 offset:24192\n"
"s_add_u32 m0, s64, 4096\n"
"v_mfma_f32_16x16x32_bf16 v[36:39], a[184:187], v[84:87], v[36:39]\n"
"v_exp_f32 v67, v67\n"
"v_mfma_f32_16x16x32_bf16 v[36:39], a[172:175], v[80:83], v[36:39]\n"
"buffer_load_dwordx4 v6, s[32:35], s63 offen offset:0 sc1 lds\n"
"v_add_f32 v57, 0x3727c5ac, v57\n"
"v_mfma_f32_16x16x32_bf16 v[36:39], a[160:163], v[76:79], v[36:39]\n"
"ds_read_b128 v[112:115], v4 offset:25344\n"
"v_cvt_pk_bf16_f32 v57, 0, v57\n"
"v_mfma_f32_16x16x32_bf16 v[36:39], a[148:151], v[124:127], v[36:39]\n"
"v_sqrt_f32 v57, v57\n"
"v_mfma_f32_16x16x32_bf16 v[32:35], a[144:147], v[124:127], v[32:35]\n"
"v_cvt_pk_bf16_f32 v57, v56, v57\n"
"v_mul_f32 v68, v52, v61\n"
"v_mfma_f32_16x16x32_bf16 v[32:35], a[156:159], v[76:79], v[32:35]\n"
"ds_read_b128 v[116:119], v4 offset:26496\n"
"ds_write_b32 v10, v57 offset:0\n"
"v_mfma_f32_16x16x32_bf16 v[32:35], a[168:171], v[80:83], v[32:35]\n"
"v_exp_f32 v68, v68\n"
"v_mfma_f32_16x16x32_bf16 v[32:35], a[180:183], v[84:87], v[32:35]\n"
"v_mul_f32 v69, v53, v61\n"
"s_waitcnt lgkmcnt(5)\n"
"v_mfma_f32_16x16x32_bf16 v[32:35], a[192:195], v[88:91], v[32:35]\n"
"ds_read_b128 v[120:123], v4 offset:27648\n"
"v_mul_f32 v44, v44, v48\n"
"v_mfma_f32_16x16x32_bf16 v[32:35], a[204:207], v[92:95], v[32:35]\n"
"buffer_load_dwordx4 v6, s[32:35], s63 offen offset:1024 sc1 lds\n"
"v_mul_f32 v45, v45, v49\n"
"v_mfma_f32_16x16x32_bf16 v[32:35], a[216:219], v[96:99], v[32:35]\n"
"v_exp_f32 v69, v69\n"
"v_mfma_f32_16x16x32_bf16 v[32:35], a[228:231], v[100:103], v[32:35]\n"
"ds_read_b128 v[124:127], v4 offset:28800\n"
"v_mul_f32 v46, v46, v50\n"
"v_mfma_f32_16x16x32_bf16 v[36:39], a[232:235], v[100:103], v[36:39]\n"
"v_mul_f32 v47, v47, v51\n"
"v_mul_f32 v52, v52, v54\n"
"v_mfma_f32_16x16x32_bf16 v[36:39], a[220:223], v[96:99], v[36:39]\n"
"buffer_load_dwordx2 v[12:13], v14, s[32:35], s66 offen sc1\n"
"v_mul_f32 v53, v53, v55\n"
"v_mfma_f32_16x16x32_bf16 v[36:39], a[208:211], v[92:95], v[36:39]\n"
"ds_read_b128 v[76:79], v4 offset:29952\n"
"v_fma_f32 v64, v64, v62, v62\n"
"v_mfma_f32_16x16x32_bf16 v[36:39], a[196:199], v[88:91], v[36:39]\n"
"v_rcp_f32 v64, v64\n"
"v_mfma_f32_16x16x32_bf16 v[40:43], a[200:203], v[88:91], v[40:43]\n"
"v_fma_f32 v65, v65, v62, v62\n"
"v_fma_f32 v66, v66, v62, v62\n"
"v_mfma_f32_16x16x32_bf16 v[40:43], a[212:215], v[92:95], v[40:43]\n"
"buffer_load_dwordx4 v6, s[32:35], s63 offen offset:2048 sc1 lds\n"
"ds_read_b128 v[80:83], v4 offset:31104\n"
"v_mfma_f32_16x16x32_bf16 v[40:43], a[224:227], v[96:99], v[40:43]\n"
"v_rcp_f32 v65, v65\n"
"v_mfma_f32_16x16x32_bf16 v[40:43], a[236:239], v[100:103], v[40:43]\n"
"v_fma_f32 v67, v67, v62, v62\n"
"s_waitcnt lgkmcnt(5)\n"
"v_mfma_f32_16x16x32_bf16 v[40:43], a[248:251], v[104:107], v[40:43]\n"
"ds_read_b128 v[84:87], v4 offset:32256\n"
"v_fma_f32 v68, v68, v62, v62\n"
"v_mfma_f32_16x16x32_bf16 v[40:43], v[132:135], v[108:111], v[40:43]\n"
"v_rcp_f32 v66, v66\n"
"v_mfma_f32_16x16x32_bf16 v[40:43], v[144:147], v[112:115], v[40:43]\n"
"v_rcp_f32 v67, v67\n"
"v_mfma_f32_16x16x32_bf16 v[40:43], v[156:159], v[116:119], v[40:43]\n"
"ds_read_b128 v[88:91], v4 offset:33408\n"
"v_fma_f32 v69, v69, v62, v62\n"
"v_mfma_f32_16x16x32_bf16 v[36:39], v[152:155], v[116:119], v[36:39]\n"
"v_rcp_f32 v68, v68\n"
"v_mfma_f32_16x16x32_bf16 v[36:39], v[140:143], v[112:115], v[36:39]\n"
"buffer_load_dwordx4 v6, s[32:35], s63 offen offset:3072 sc1 lds\n"
"v_mul_f32 v44, v44, v64\n"
"v_mfma_f32_16x16x32_bf16 v[36:39], v[128:131], v[108:111], v[36:39]\n"
"ds_read_b128 v[92:95], v4 offset:34560\n"
"v_mul_f32 v45, v45, v65\n"
"v_mfma_f32_16x16x32_bf16 v[36:39], a[244:247], v[104:107], v[36:39]\n"
"v_rcp_f32 v69, v69\n"
"v_mfma_f32_16x16x32_bf16 v[32:35], a[240:243], v[104:107], v[32:35]\n"
"v_mul_f32 v46, v46, v66\n"
"v_mul_f32 v47, v47, v67\n"
"v_mfma_f32_16x16x32_bf16 v[32:35], a[252:255], v[108:111], v[32:35]\n"
"s_waitcnt vmcnt(9)\n"
"s_waitcnt lgkmcnt(7)\n"
"s_barrier\n"
"v_mfma_f32_16x16x32_bf16 v[32:35], v[136:139], v[112:115], v[32:35]\n"
"ds_read_b128 v[96:99], v4 offset:35712\n"
"v_xor_b32 v4, 73728, v4\n"
"v_mfma_f32_16x16x32_bf16 v[32:35], v[148:151], v[116:119], v[32:35]\n"
"s_add_u32 m0, s64, 8192\n"
"s_waitcnt lgkmcnt(4)\n"
"v_mfma_f32_16x16x32_bf16 v[32:35], v[160:163], v[120:123], v[32:35]\n"
"ds_read_b128 v[100:103], v5 offset:0\n"
"v_mul_f32 v52, v52, v68\n"
"v_mfma_f32_16x16x32_bf16 v[32:35], v[172:175], v[124:127], v[32:35]\n"
"buffer_load_dwordx4 v7, s[32:35], s63 offen offset:0 sc1 lds\n"
"s_add_u32 s63, s63, 890880\n"
"v_mfma_f32_16x16x32_bf16 v[32:35], v[184:187], v[76:79], v[32:35]\n"
"v_mul_f32 v53, v53, v69\n"
"v_cvt_pk_bf16_f32 v72, v44, v45\n"
"v_mfma_f32_16x16x32_bf16 v[32:35], v[196:199], v[80:83], v[32:35]\n"
"ds_read_b128 v[104:107], v5 offset:1152\n"
"v_cvt_pk_bf16_f32 v74, v52, v53\n"
"v_mfma_f32_16x16x32_bf16 v[36:39], v[200:203], v[80:83], v[36:39]\n"
"v_cvt_pk_bf16_f32 v73, v46, v47\n"
"v_mfma_f32_16x16x32_bf16 v[36:39], v[188:191], v[76:79], v[36:39]\n"
"v_permlane32_swap_b32 v74, v75\n"
"v_mfma_f32_16x16x32_bf16 v[36:39], v[176:179], v[124:127], v[36:39]\n"
"ds_read_b128 v[108:111], v5 offset:2304\n"
"v_mfma_f32_16x16x32_bf16 v[36:39], v[164:167], v[120:123], v[36:39]\n"
"v_permlane16_swap_b32 v72, v74\n"
"v_mfma_f32_16x16x32_bf16 v[40:43], v[168:171], v[120:123], v[40:43]\n"
"v_permlane16_swap_b32 v73, v75\n"
"v_mfma_f32_16x16x32_bf16 v[40:43], v[180:183], v[124:127], v[40:43]\n"
"buffer_store_dwordx4 v[72:75], v15, s[32:35], s68 offen sc1\n"
"ds_read_b128 v[112:115], v5 offset:3456\n"
"v_mfma_f32_16x16x32_bf16 v[40:43], v[192:195], v[76:79], v[40:43]\n"
"v_mfma_f32_16x16x32_bf16 v[40:43], v[204:207], v[80:83], v[40:43]\n"
"s_waitcnt lgkmcnt(4)\n"
"v_mfma_f32_16x16x32_bf16 v[40:43], v[216:219], v[84:87], v[40:43]\n"
"ds_read_b128 v[116:119], v5 offset:4608\n"
"v_mfma_f32_16x16x32_bf16 v[40:43], v[228:231], v[88:91], v[40:43]\n"
"v_mfma_f32_16x16x32_bf16 v[40:43], v[240:243], v[92:95], v[40:43]\n"
"v_mfma_f32_16x16x32_bf16 v[40:43], v[252:255], v[96:99], v[40:43]\n"
"ds_read_b128 v[120:123], v5 offset:5760\n"
"v_mfma_f32_16x16x32_bf16 v[36:39], v[248:251], v[96:99], v[36:39]\n"
"v_mfma_f32_16x16x32_bf16 v[36:39], v[236:239], v[92:95], v[36:39]\n"
"v_mfma_f32_16x16x32_bf16 v[36:39], v[224:227], v[88:91], v[36:39]\n"
"ds_read_b128 v[124:127], v5 offset:6912\n"
"v_mfma_f32_16x16x32_bf16 v[36:39], v[212:215], v[84:87], v[36:39]\n"
"v_mfma_f32_16x16x32_bf16 v[32:35], v[208:211], v[84:87], v[32:35]\n"
"v_mfma_f32_16x16x32_bf16 v[32:35], v[220:223], v[88:91], v[32:35]\n"
"ds_read_b128 v[76:79], v5 offset:8064\n"
"v_mfma_f32_16x16x32_bf16 v[32:35], v[232:235], v[92:95], v[32:35]\n"
"s_sub_u32 s62, s62, 1\n"
"v_mfma_f32_16x16x32_bf16 v[32:35], v[244:247], v[96:99], v[32:35]\n"
"s_cbranch_scc1 .L_gt0_2\n"
"s_waitcnt lgkmcnt(4)\n"
"v_mfma_f32_16x16x32_bf16 v[44:47], a[0:3], v[100:103], 0\n"
"ds_read_b128 v[80:83], v5 offset:9216\n"
"ds_read_b32 v62, v11 offset:0\n"
"v_mfma_f32_16x16x32_bf16 v[44:47], a[12:15], v[104:107], v[44:47]\n"
"s_add_u32 s68, s68, 890880\n"
"v_mfma_f32_16x16x32_bf16 v[44:47], a[24:27], v[108:111], v[44:47]\n"
"v_mfma_f32_16x16x32_bf16 v[44:47], a[36:39], v[112:115], v[44:47]\n"
"ds_read_b128 v[84:87], v5 offset:10368\n"
"v_mfma_f32_16x16x32_bf16 v[48:51], a[40:43], v[112:115], 0\n"
"ds_read2st64_b32 v[56:57], v8 offset0:144 offset1:180\n"
"v_mfma_f32_16x16x32_bf16 v[48:51], a[28:31], v[108:111], v[48:51]\n"
"s_waitcnt vmcnt(4)\n"
"v_cmp_ne_u64 vcc, v[16:17], v[12:13]\n"
"v_mfma_f32_16x16x32_bf16 v[48:51], a[16:19], v[104:107], v[48:51]\n"
"ds_read_b128 v[88:91], v5 offset:11520\n"
"ds_read2st64_b32 v[58:59], v8 offset0:216 offset1:252\n"
"v_mfma_f32_16x16x32_bf16 v[48:51], a[4:7], v[100:103], v[48:51]\n"
"s_cbranch_vccnz .L_rtg1_2\n"
".L_bkg1_2:\n"
"s_add_u32 s66, s66, 890880\n"
"v_mfma_f32_16x16x32_bf16 v[52:55], a[8:11], v[100:103], 0\n"
"s_add_u32 m0, s64, 36864\n"
"v_xor_b32 v8, v9, v8\n"
"v_mfma_f32_16x16x32_bf16 v[52:55], a[20:23], v[104:107], v[52:55]\n"
"buffer_load_dwordx4 v2, s[32:35], s63 offen offset:0 sc1 lds\n"
"ds_read_b128 v[92:95], v5 offset:12672\n"
"v_mfma_f32_16x16x32_bf16 v[52:55], a[32:35], v[108:111], v[52:55]\n"
"s_waitcnt lgkmcnt(5)\n"
"v_lshlrev_b32 v60, 16, v62\n"
"v_mfma_f32_16x16x32_bf16 v[52:55], a[44:47], v[112:115], v[52:55]\n"
"v_and_b32 v62, 0xffff0000, v62\n"
"v_mfma_f32_16x16x32_bf16 v[52:55], a[56:59], v[116:119], v[52:55]\n"
"ds_read_b128 v[96:99], v5 offset:13824\n"
"v_fma_f32 v40, -v60, v28, v40\n"
"v_mfma_f32_16x16x32_bf16 v[52:55], a[68:71], v[120:123], v[52:55]\n"
"v_rcp_f32 v61, v62\n"
"v_mfma_f32_16x16x32_bf16 v[52:55], a[80:83], v[124:127], v[52:55]\n"
"v_mul_f32 v62, v62, v62\n"
"v_mul_f32 v61, 0xbfb8aa3b, v61\n"
"v_mfma_f32_16x16x32_bf16 v[52:55], a[92:95], v[76:79], v[52:55]\n"
"ds_read_b128 v[100:103], v5 offset:14976\n"
"v_fma_f32 v41, -v60, v29, v41\n"
"v_mfma_f32_16x16x32_bf16 v[48:51], a[88:91], v[76:79], v[48:51]\n"
"v_fma_f32 v42, -v60, v30, v42\n"
"v_fma_f32 v43, -v60, v31, v43\n"
"v_mfma_f32_16x16x32_bf16 v[48:51], a[76:79], v[124:127], v[48:51]\n"
"buffer_load_dwordx4 v2, s[32:35], s63 offen offset:1024 sc1 lds\n"
"v_fma_f32 v36, -v60, v24, v36\n"
"v_mfma_f32_16x16x32_bf16 v[48:51], a[64:67], v[120:123], v[48:51]\n"
"ds_read_b128 v[104:107], v5 offset:16128\n"
"v_fma_f32 v37, -v60, v25, v37\n"
"v_mfma_f32_16x16x32_bf16 v[48:51], a[52:55], v[116:119], v[48:51]\n"
"v_permlane32_swap_b32 v40, v42\n"
"v_mfma_f32_16x16x32_bf16 v[44:47], a[48:51], v[116:119], v[44:47]\n"
"v_permlane32_swap_b32 v41, v43\n"
"v_mfma_f32_16x16x32_bf16 v[44:47], a[60:63], v[120:123], v[44:47]\n"
"ds_read_b128 v[108:111], v5 offset:17280\n"
"v_fma_f32 v38, -v60, v26, v38\n"
"v_mfma_f32_16x16x32_bf16 v[44:47], a[72:75], v[124:127], v[44:47]\n"
"v_fma_f32 v39, -v60, v27, v39\n"
"v_fma_f32 v32, -v60, v20, v32\n"
"v_mfma_f32_16x16x32_bf16 v[44:47], a[84:87], v[76:79], v[44:47]\n"
"v_fma_f32 v33, -v60, v21, v33\n"
"s_waitcnt lgkmcnt(4)\n"
"v_mfma_f32_16x16x32_bf16 v[44:47], a[96:99], v[80:83], v[44:47]\n"
"ds_read_b128 v[112:115], v5 offset:18432\n"
"v_add_f32 v56, v56, v57\n"
"v_mfma_f32_16x16x32_bf16 v[44:47], a[108:111], v[84:87], v[44:47]\n"
"buffer_load_dwordx4 v2, s[32:35], s63 offen offset:2048 sc1 lds\n"
"v_add_f32 v58, v58, v59\n"
"v_mfma_f32_16x16x32_bf16 v[44:47], a[120:123], v[88:91], v[44:47]\n"
"v_add_f32 v56, v56, v58\n"
"v_fma_f32 v34, -v60, v22, v34\n"
"v_mfma_f32_16x16x32_bf16 v[44:47], a[132:135], v[92:95], v[44:47]\n"
"ds_read_b128 v[116:119], v5 offset:19584\n"
"v_add_f32_dpp v56, v56, v56 row_ror:8 row_mask:0xf bank_mask:0xf\n"
"v_mfma_f32_16x16x32_bf16 v[48:51], a[136:139], v[92:95], v[48:51]\n"
"v_fma_f32 v35, -v60, v23, v35\n"
"v_add_f32_dpp v56, v56, v56 row_ror:4 row_mask:0xf bank_mask:0xf\n"
"v_mfma_f32_16x16x32_bf16 v[48:51], a[124:127], v[88:91], v[48:51]\n"
"v_mul_f32 v64, v32, v61\n"
"v_add_f32_dpp v56, v56, v56 row_ror:2 row_mask:0xf bank_mask:0xf\n"
"v_mfma_f32_16x16x32_bf16 v[48:51], a[112:115], v[84:87], v[48:51]\n"
"ds_read_b128 v[120:123], v5 offset:20736\n"
"v_mul_f32 v56, 0x3a800000, v56\n"
"v_mfma_f32_16x16x32_bf16 v[48:51], a[100:103], v[80:83], v[48:51]\n"
"v_exp_f32 v64, v64\n"
"v_mfma_f32_16x16x32_bf16 v[52:55], a[104:107], v[80:83], v[52:55]\n"
"v_mul_f32 v57, v56, v56\n"
"v_mul_f32 v65, v33, v61\n"
"v_mfma_f32_16x16x32_bf16 v[52:55], a[116:119], v[84:87], v[52:55]\n"
"buffer_load_dwordx4 v2, s[32:35], s63 offen offset:3072 sc1 lds\n"
"ds_read_b128 v[124:127], v5 offset:21888\n"
"v_mfma_f32_16x16x32_bf16 v[52:55], a[128:131], v[88:91], v[52:55]\n"
"v_exp_f32 v65, v65\n"
"v_mfma_f32_16x16x32_bf16 v[52:55], a[140:143], v[92:95], v[52:55]\n"
"v_sub_f32_dpp v57, v56, v57 quad_perm:[1,1,3,3] row_mask:0xf bank_mask:0xf\n"
"s_waitcnt lgkmcnt(4)\n"
"v_mfma_f32_16x16x32_bf16 v[52:55], a[152:155], v[96:99], v[52:55]\n"
"ds_read_b128 v[76:79], v5 offset:23040\n"
"v_max_f32 v57, 0, v57\n"
"v_mfma_f32_16x16x32_bf16 v[52:55], a[164:167], v[100:103], v[52:55]\n"
"v_cvt_pk_bf16_f32 v57, 0, v57\n"
"v_mul_f32 v66, v34, v61\n"
"v_mfma_f32_16x16x32_bf16 v[52:55], a[176:179], v[104:107], v[52:55]\n"
"v_exp_f32 v66, v66\n"
"v_mfma_f32_16x16x32_bf16 v[52:55], a[188:191], v[108:111], v[52:55]\n"
"ds_read_b128 v[80:83], v5 offset:24192\n"
"s_add_u32 m0, s64, 40960\n"
"v_mfma_f32_16x16x32_bf16 v[48:51], a[184:187], v[108:111], v[48:51]\n"
"v_add_f32 v57, 0x3727c5ac, v57\n"
"v_mul_f32 v67, v35, v61\n"
"v_mfma_f32_16x16x32_bf16 v[48:51], a[172:175], v[104:107], v[48:51]\n"
"buffer_load_dwordx4 v6, s[32:35], s63 offen offset:0 sc1 lds\n"
"v_cvt_pk_bf16_f32 v57, 0, v57\n"
"v_mfma_f32_16x16x32_bf16 v[48:51], a[160:163], v[100:103], v[48:51]\n"
"ds_read_b128 v[84:87], v5 offset:25344\n"
"v_mul_f32 v68, v40, v61\n"
"v_mfma_f32_16x16x32_bf16 v[48:51], a[148:151], v[96:99], v[48:51]\n"
"v_sqrt_f32 v57, v57\n"
"v_mfma_f32_16x16x32_bf16 v[44:47], a[144:147], v[96:99], v[44:47]\n"
"v_exp_f32 v67, v67\n"
"v_mfma_f32_16x16x32_bf16 v[44:47], a[156:159], v[100:103], v[44:47]\n"
"ds_read_b128 v[88:91], v5 offset:26496\n"
"v_cvt_pk_bf16_f32 v57, v56, v57\n"
"v_mfma_f32_16x16x32_bf16 v[44:47], a[168:171], v[104:107], v[44:47]\n"
"ds_write_b32 v10, v57 offset:128\n"
"v_mul_f32 v69, v41, v61\n"
"v_mfma_f32_16x16x32_bf16 v[44:47], a[180:183], v[108:111], v[44:47]\n"
"v_mul_f32 v32, v32, v36\n"
"s_waitcnt lgkmcnt(5)\n"
"v_mfma_f32_16x16x32_bf16 v[44:47], a[192:195], v[112:115], v[44:47]\n"
"ds_read_b128 v[92:95], v5 offset:27648\n"
"v_mul_f32 v33, v33, v37\n"
"v_mfma_f32_16x16x32_bf16 v[44:47], a[204:207], v[116:119], v[44:47]\n"
"buffer_load_dwordx4 v6, s[32:35], s63 offen offset:1024 sc1 lds\n"
"v_mul_f32 v34, v34, v38\n"
"v_mfma_f32_16x16x32_bf16 v[44:47], a[216:219], v[120:123], v[44:47]\n"
"v_exp_f32 v68, v68\n"
"v_mfma_f32_16x16x32_bf16 v[44:47], a[228:231], v[124:127], v[44:47]\n"
"ds_read_b128 v[96:99], v5 offset:28800\n"
"v_mul_f32 v35, v35, v39\n"
"v_mfma_f32_16x16x32_bf16 v[48:51], a[232:235], v[124:127], v[48:51]\n"
"v_exp_f32 v69, v69\n"
"v_mfma_f32_16x16x32_bf16 v[48:51], a[220:223], v[120:123], v[48:51]\n"
"buffer_load_dwordx2 v[12:13], v14, s[32:35], s66 offen sc1\n"
"v_mul_f32 v40, v40, v42\n"
"v_mfma_f32_16x16x32_bf16 v[48:51], a[208:211], v[116:119], v[48:51]\n"
"ds_read_b128 v[100:103], v5 offset:29952\n"
"v_mul_f32 v41, v41, v43\n"
"v_mfma_f32_16x16x32_bf16 v[48:51], a[196:199], v[112:115], v[48:51]\n"
"v_fma_f32 v64, v64, v62, v62\n"
"v_fma_f32 v65, v65, v62, v62\n"
"v_mfma_f32_16x16x32_bf16 v[52:55], a[200:203], v[112:115], v[52:55]\n"
"v_rcp_f32 v64, v64\n"
"v_mfma_f32_16x16x32_bf16 v[52:55], a[212:215], v[116:119], v[52:55]\n"
"buffer_load_dwordx4 v6, s[32:35], s63 offen offset:2048 sc1 lds\n"
"ds_read_b128 v[104:107], v5 offset:31104\n"
"v_mfma_f32_16x16x32_bf16 v[52:55], a[224:227], v[120:123], v[52:55]\n"
"v_rcp_f32 v65, v65\n"
"v_mfma_f32_16x16x32_bf16 v[52:55], a[236:239], v[124:127], v[52:55]\n"
"v_fma_f32 v66, v66, v62, v62\n"
"s_waitcnt lgkmcnt(5)\n"
"v_mfma_f32_16x16x32_bf16 v[52:55], a[248:251], v[76:79], v[52:55]\n"
"ds_read_b128 v[108:111], v5 offset:32256\n"
"v_fma_f32 v67, v67, v62, v62\n"
"v_mfma_f32_16x16x32_bf16 v[52:55], v[132:135], v[80:83], v[52:55]\n"
"v_rcp_f32 v66, v66\n"
"v_mfma_f32_16x16x32_bf16 v[52:55], v[144:147], v[84:87], v[52:55]\n"
"v_rcp_f32 v67, v67\n"
"v_mfma_f32_16x16x32_bf16 v[52:55], v[156:159], v[88:91], v[52:55]\n"
"ds_read_b128 v[112:115], v5 offset:33408\n"
"v_fma_f32 v68, v68, v62, v62\n"
"v_mfma_f32_16x16x32_bf16 v[48:51], v[152:155], v[88:91], v[48:51]\n"
"v_rcp_f32 v68, v68\n"
"v_mfma_f32_16x16x32_bf16 v[48:51], v[140:143], v[84:87], v[48:51]\n"
"buffer_load_dwordx4 v6, s[32:35], s63 offen offset:3072 sc1 lds\n"
"v_fma_f32 v69, v69, v62, v62\n"
"v_mfma_f32_16x16x32_bf16 v[48:51], v[128:131], v[80:83], v[48:51]\n"
"ds_read_b128 v[116:119], v5 offset:34560\n"
"v_mul_f32 v32, v32, v64\n"
"v_mfma_f32_16x16x32_bf16 v[48:51], a[244:247], v[76:79], v[48:51]\n"
"v_rcp_f32 v69, v69\n"
"v_mfma_f32_16x16x32_bf16 v[44:47], a[240:243], v[76:79], v[44:47]\n"
"v_mul_f32 v33, v33, v65\n"
"v_mul_f32 v34, v34, v66\n"
"v_mfma_f32_16x16x32_bf16 v[44:47], a[252:255], v[80:83], v[44:47]\n"
"s_waitcnt vmcnt(9)\n"
"s_waitcnt lgkmcnt(7)\n"
"s_barrier\n"
"v_mfma_f32_16x16x32_bf16 v[44:47], v[136:139], v[84:87], v[44:47]\n"
"ds_read_b128 v[120:123], v5 offset:35712\n"
"v_xor_b32 v5, 73728, v5\n"
"v_mfma_f32_16x16x32_bf16 v[44:47], v[148:151], v[88:91], v[44:47]\n"
"s_add_u32 m0, s64, 45056\n"
"s_waitcnt lgkmcnt(4)\n"
"v_mfma_f32_16x16x32_bf16 v[44:47], v[160:163], v[92:95], v[44:47]\n"
"ds_read_b128 v[76:79], v4 offset:0\n"
"v_mul_f32 v35, v35, v67\n"
"v_mfma_f32_16x16x32_bf16 v[44:47], v[172:175], v[96:99], v[44:47]\n"
"buffer_load_dwordx4 v7, s[32:35], s63 offen offset:0 sc1 lds\n"
"s_add_u32 s63, s63, 890880\n"
"v_mfma_f32_16x16x32_bf16 v[44:47], v[184:187], v[100:103], v[44:47]\n"
"s_xor_b32 s64, s64, s65\n"
"v_mul_f32 v40, v40, v68\n"
"v_mfma_f32_16x16x32_bf16 v[44:47], v[196:199], v[104:107], v[44:47]\n"
"ds_read_b128 v[80:83], v4 offset:1152\n"
"v_mul_f32 v41, v41, v69\n"
"v_mfma_f32_16x16x32_bf16 v[48:51], v[200:203], v[104:107], v[48:51]\n"
"v_cvt_pk_bf16_f32 v74, v40, v41\n"
"v_cvt_pk_bf16_f32 v72, v32, v33\n"
"v_mfma_f32_16x16x32_bf16 v[48:51], v[188:191], v[100:103], v[48:51]\n"
"v_permlane32_swap_b32 v74, v75\n"
"v_mfma_f32_16x16x32_bf16 v[48:51], v[176:179], v[96:99], v[48:51]\n"
"ds_read_b128 v[84:87], v4 offset:2304\n"
"v_cvt_pk_bf16_f32 v73, v34, v35\n"
"v_mfma_f32_16x16x32_bf16 v[48:51], v[164:167], v[92:95], v[48:51]\n"
"v_permlane16_swap_b32 v72, v74\n"
"v_mfma_f32_16x16x32_bf16 v[52:55], v[168:171], v[92:95], v[52:55]\n"
"v_permlane16_swap_b32 v73, v75\n"
"v_mfma_f32_16x16x32_bf16 v[52:55], v[180:183], v[96:99], v[52:55]\n"
"buffer_store_dwordx4 v[72:75], v15, s[32:35], s68 offen sc1\n"
"ds_read_b128 v[88:91], v4 offset:3456\n"
"v_mfma_f32_16x16x32_bf16 v[52:55], v[192:195], v[100:103], v[52:55]\n"
"v_mfma_f32_16x16x32_bf16 v[52:55], v[204:207], v[104:107], v[52:55]\n"
"s_waitcnt lgkmcnt(4)\n"
"v_mfma_f32_16x16x32_bf16 v[52:55], v[216:219], v[108:111], v[52:55]\n"
"ds_read_b128 v[92:95], v4 offset:4608\n"
"v_mfma_f32_16x16x32_bf16 v[52:55], v[228:231], v[112:115], v[52:55]\n"
"v_mfma_f32_16x16x32_bf16 v[52:55], v[240:243], v[116:119], v[52:55]\n"
"v_mfma_f32_16x16x32_bf16 v[52:55], v[252:255], v[120:123], v[52:55]\n"
"ds_read_b128 v[96:99], v4 offset:5760\n"
"v_mfma_f32_16x16x32_bf16 v[48:51], v[248:251], v[120:123], v[48:51]\n"
"v_mfma_f32_16x16x32_bf16 v[48:51], v[236:239], v[116:119], v[48:51]\n"
"v_mfma_f32_16x16x32_bf16 v[48:51], v[224:227], v[112:115], v[48:51]\n"
"ds_read_b128 v[100:103], v4 offset:6912\n"
"v_mfma_f32_16x16x32_bf16 v[48:51], v[212:215], v[108:111], v[48:51]\n"
"v_mfma_f32_16x16x32_bf16 v[44:47], v[208:211], v[108:111], v[44:47]\n"
"v_mfma_f32_16x16x32_bf16 v[44:47], v[220:223], v[112:115], v[44:47]\n"
"ds_read_b128 v[104:107], v4 offset:8064\n"
"v_mfma_f32_16x16x32_bf16 v[44:47], v[232:235], v[116:119], v[44:47]\n"
"s_sub_u32 s62, s62, 1\n"
"v_mfma_f32_16x16x32_bf16 v[44:47], v[244:247], v[120:123], v[44:47]\n"
"s_cbranch_scc0 .L_gl_2\n"
"s_nop 1\n"
"ds_read_b32 v62, v11 offset:128\n"
"s_add_u32 s68, s68, 890880\n"
"s_waitcnt lgkmcnt(0)\n"
"v_lshlrev_b32 v60, 16, v62\n"
"v_and_b32 v62, 0xffff0000, v62\n"
"v_rcp_f32 v61, v62\n"
"v_mul_f32 v62, v62, v62\n"
"v_mul_f32 v61, 0xbfb8aa3b, v61\n"
"v_fma_f32 v52, -v60, v28, v52\n"
"v_fma_f32 v53, -v60, v29, v53\n"
"v_fma_f32 v54, -v60, v30, v54\n"
"v_fma_f32 v55, -v60, v31, v55\n"
"v_fma_f32 v48, -v60, v24, v48\n"
"v_fma_f32 v49, -v60, v25, v49\n"
"v_fma_f32 v50, -v60, v26, v50\n"
"v_fma_f32 v51, -v60, v27, v51\n"
"v_fma_f32 v44, -v60, v20, v44\n"
"v_fma_f32 v45, -v60, v21, v45\n"
"v_fma_f32 v46, -v60, v22, v46\n"
"v_fma_f32 v47, -v60, v23, v47\n"
"v_permlane32_swap_b32 v52, v54\n"
"v_permlane32_swap_b32 v53, v55\n"
"v_mul_f32 v64, v44, v61\n"
"v_mul_f32 v65, v45, v61\n"
"v_mul_f32 v66, v46, v61\n"
"v_mul_f32 v67, v47, v61\n"
"v_mul_f32 v68, v52, v61\n"
"v_mul_f32 v69, v53, v61\n"
"v_exp_f32 v64, v64\n"
"v_exp_f32 v65, v65\n"
"v_mul_f32 v44, v44, v48\n"
"v_mul_f32 v45, v45, v49\n"
"v_exp_f32 v66, v66\n"
"v_exp_f32 v67, v67\n"
"v_mul_f32 v46, v46, v50\n"
"v_mul_f32 v47, v47, v51\n"
"v_exp_f32 v68, v68\n"
"v_exp_f32 v69, v69\n"
"v_mul_f32 v52, v52, v54\n"
"v_mul_f32 v53, v53, v55\n"
"v_fma_f32 v64, v64, v62, v62\n"
"v_fma_f32 v65, v65, v62, v62\n"
"v_rcp_f32 v64, v64\n"
"v_rcp_f32 v65, v65\n"
"v_fma_f32 v66, v66, v62, v62\n"
"v_fma_f32 v67, v67, v62, v62\n"
"v_rcp_f32 v66, v66\n"
"v_rcp_f32 v67, v67\n"
"v_fma_f32 v68, v68, v62, v62\n"
"v_fma_f32 v69, v69, v62, v62\n"
"v_rcp_f32 v68, v68\n"
"v_rcp_f32 v69, v69\n"
"v_mul_f32 v44, v44, v64\n"
"v_mul_f32 v45, v45, v65\n"
"v_mul_f32 v46, v46, v66\n"
"v_mul_f32 v47, v47, v67\n"
"v_mul_f32 v52, v52, v68\n"
"v_mul_f32 v53, v53, v69\n"
"v_cvt_pk_bf16_f32 v74, v52, v53\n"
"v_cvt_pk_bf16_f32 v72, v44, v45\n"
"v_cvt_pk_bf16_f32 v73, v46, v47\n"
"v_permlane32_swap_b32 v74, v75\n"
"s_nop 1\n"
"v_permlane16_swap_b32 v72, v74\n"
"v_permlane16_swap_b32 v73, v75\n"
"buffer_store_dwordx4 v[72:75], v15, s[32:35], s68 offen sc1\n"
"s_waitcnt vmcnt(1)\n"
"s_barrier\n"
"buffer_store_dwordx2 v[16:17], v2, s[92:95], s68 idxen sc1\n"
"s_add_u32 s68, s68, 890880\n"
"s_waitcnt vmcnt(1)\n"
"s_barrier\n"
"buffer_store_dwordx2 v[16:17], v2, s[92:95], s68 idxen sc1\n"
"s_branch .L_end\n"
".L_gt0_2:\n"
"s_nop 1\n"
"ds_read_b32 v62, v11 offset:0\n"
"s_add_u32 s68, s68, 890880\n"
"s_waitcnt lgkmcnt(0)\n"
"v_lshlrev_b32 v60, 16, v62\n"
"v_and_b32 v62, 0xffff0000, v62\n"
"v_rcp_f32 v61, v62\n"
"v_mul_f32 v62, v62, v62\n"
"v_mul_f32 v61, 0xbfb8aa3b, v61\n"
"v_fma_f32 v40, -v60, v28, v40\n"
"v_fma_f32 v41, -v60, v29, v41\n"
"v_fma_f32 v42, -v60, v30, v42\n"
"v_fma_f32 v43, -v60, v31, v43\n"
"v_fma_f32 v36, -v60, v24, v36\n"
"v_fma_f32 v37, -v60, v25, v37\n"
"v_fma_f32 v38, -v60, v26, v38\n"
"v_fma_f32 v39, -v60, v27, v39\n"
"v_fma_f32 v32, -v60, v20, v32\n"
"v_fma_f32 v33, -v60, v21, v33\n"
"v_fma_f32 v34, -v60, v22, v34\n"
"v_fma_f32 v35, -v60, v23, v35\n"
"v_permlane32_swap_b32 v40, v42\n"
"v_permlane32_swap_b32 v41, v43\n"
"v_mul_f32 v64, v32, v61\n"
"v_mul_f32 v65, v33, v61\n"
"v_mul_f32 v66, v34, v61\n"
"v_mul_f32 v67, v35, v61\n"
"v_mul_f32 v68, v40, v61\n"
"v_mul_f32 v69, v41, v61\n"
"v_exp_f32 v64, v64\n"
"v_exp_f32 v65, v65\n"
"v_mul_f32 v32, v32, v36\n"
"v_mul_f32 v33, v33, v37\n"
"v_exp_f32 v66, v66\n"
"v_exp_f32 v67, v67\n"
"v_mul_f32 v34, v34, v38\n"
"v_mul_f32 v35, v35, v39\n"
"v_exp_f32 v68, v68\n"
"v_exp_f32 v69, v69\n"
"v_mul_f32 v40, v40, v42\n"
"v_mul_f32 v41, v41, v43\n"
"v_fma_f32 v64, v64, v62, v62\n"
"v_fma_f32 v65, v65, v62, v62\n"
"v_rcp_f32 v64, v64\n"
"v_rcp_f32 v65, v65\n"
"v_fma_f32 v66, v66, v62, v62\n"
"v_fma_f32 v67, v67, v62, v62\n"
"v_rcp_f32 v66, v66\n"
"v_rcp_f32 v67, v67\n"
"v_fma_f32 v68, v68, v62, v62\n"
"v_fma_f32 v69, v69, v62, v62\n"
"v_rcp_f32 v68, v68\n"
"v_rcp_f32 v69, v69\n"
"v_mul_f32 v32, v32, v64\n"
"v_mul_f32 v33, v33, v65\n"
"v_mul_f32 v34, v34, v66\n"
"v_mul_f32 v35, v35, v67\n"
"v_mul_f32 v40, v40, v68\n"
"v_mul_f32 v41, v41, v69\n"
"v_cvt_pk_bf16_f32 v74, v40, v41\n"
"v_cvt_pk_bf16_f32 v72, v32, v33\n"
"v_cvt_pk_bf16_f32 v73, v34, v35\n"
"v_permlane32_swap_b32 v74, v75\n"
"s_nop 1\n"
"v_permlane16_swap_b32 v72, v74\n"
"v_permlane16_swap_b32 v73, v75\n"
"buffer_store_dwordx4 v[72:75], v15, s[32:35], s68 offen sc1\n"
"s_waitcnt vmcnt(1)\n"
"s_barrier\n"
"buffer_store_dwordx2 v[16:17], v2, s[92:95], s68 idxen sc1\n"
"s_add_u32 s68, s68, 890880\n"
"s_waitcnt vmcnt(1)\n"
"s_barrier\n"
"buffer_store_dwordx2 v[16:17], v2, s[92:95], s68 idxen sc1\n"
"s_branch .L_end\n"
".p2align 6\n"
".L_gv3:\n"
".L_gl_3:\n"
"s_waitcnt lgkmcnt(4)\n"
"v_mfma_f32_16x16x32_bf16 v[32:35], a[0:3], v[76:79], 0\n"
"ds_read_b128 v[108:111], v4 offset:9216\n"
"ds_read_b32 v62, v11 offset:128\n"
"v_mfma_f32_16x16x32_bf16 v[32:35], a[12:15], v[80:83], v[32:35]\n"
"s_add_u32 s68, s68, 890880\n"
"v_mfma_f32_16x16x32_bf16 v[32:35], a[24:27], v[84:87], v[32:35]\n"
"v_mfma_f32_16x16x32_bf16 v[32:35], a[36:39], v[88:91], v[32:35]\n"
"ds_read_b128 v[112:115], v4 offset:10368\n"
"v_mfma_f32_16x16x32_bf16 v[36:39], a[40:43], v[88:91], 0\n"
"ds_read2st64_b32 v[56:57], v8 offset0:0 offset1:36\n"
"v_mfma_f32_16x16x32_bf16 v[36:39], a[28:31], v[84:87], v[36:39]\n"
"s_waitcnt vmcnt(5)\n"
"v_cmp_ne_u64 vcc, v[16:17], v[12:13]\n"
"v_mfma_f32_16x16x32_bf16 v[36:39], a[16:19], v[80:83], v[36:39]\n"
"ds_read_b128 v[116:119], v4 offset:11520\n"
"ds_read2st64_b32 v[58:59], v8 offset0:72 offset1:108\n"
"v_mfma_f32_16x16x32_bf16 v[36:39], a[4:7], v[76:79], v[36:39]\n"
"s_cbranch_vccnz .L_rtg0_3\n"
".L_bkg0_3:\n"
"s_add_u32 s66, s66, 890880\n"
"v_mfma_f32_16x16x32_bf16 v[40:43], a[8:11], v[76:79], 0\n"
"s_add_u32 m0, s64, 0\n"
"s_waitcnt lgkmcnt(4)\n"
"v_mfma_f32_16x16x32_bf16 v[40:43], a[20:23], v[80:83], v[40:43]\n"
"buffer_load_dwordx4 v2, s[32:35], s63 offen offset:0 sc1 lds\n"
"ds_read_b128 v[120:123], v4 offset:12672\n"
"v_mfma_f32_16x16x32_bf16 v[40:43], a[32:35], v[84:87], v[40:43]\n"
"v_lshlrev_b32 v60, 16, v62\n"
"v_and_b32 v62, 0xffff0000, v62\n"
"v_mfma_f32_16x16x32_bf16 v[40:43], a[44:47], v[88:91], v[40:43]\n"
"v_fma_f32 v52, -v60, v28, v52\n"
"v_mfma_f32_16x16x32_bf16 v[40:43], a[56:59], v[92:95], v[40:43]\n"
"ds_read_b128 v[124:127], v4 offset:13824\n"
"v_fma_f32 v53, -v60, v29, v53\n"
"v_mfma_f32_16x16x32_bf16 v[40:43], a[68:71], v[96:99], v[40:43]\n"
"v_rcp_f32 v61, v62\n"
"v_mfma_f32_16x16x32_bf16 v[40:43], a[80:83], v[100:103], v[40:43]\n"
"v_mul_f32 v62, v62, v62\n"
"v_mul_f32 v61, 0xbfb8aa3b, v61\n"
"v_mfma_f32_16x16x32_bf16 v[40:43], a[92:95], v[104:107], v[40:43]\n"
"ds_read_b128 v[76:79], v4 offset:14976\n"
"v_fma_f32 v54, -v60, v30, v54\n"
"v_mfma_f32_16x16x32_bf16 v[36:39], a[88:91], v[104:107], v[36:39]\n"
"v_fma_f32 v55, -v60, v31, v55\n"
"v_fma_f32 v48, -v60, v24, v48\n"
"v_mfma_f32_16x16x32_bf16 v[36:39], a[76:79], v[100:103], v[36:39]\n"
"buffer_load_dwordx4 v2, s[32:35], s63 offen offset:1024 sc1 lds\n"
"v_fma_f32 v49, -v60, v25, v49\n"
"v_mfma_f32_16x16x32_bf16 v[36:39], a[64:67], v[96:99], v[36:39]\n"
"ds_read_b128 v[80:83], v4 offset:16128\n"
"v_fma_f32 v50, -v60, v26, v50\n"
"v_mfma_f32_16x16x32_bf16 v[36:39], a[52:55], v[92:95], v[36:39]\n"
"v_permlane32_swap_b32 v52, v54\n"
"v_mfma_f32_16x16x32_bf16 v[32:35], a[48:51], v[92:95], v[32:35]\n"
"v_permlane32_swap_b32 v53, v55\n"
"v_mfma_f32_16x16x32_bf16 v[32:35], a[60:63], v[96:99], v[32:35]\n"
"ds_read_b128 v[84:87], v4 offset:17280\n"
"v_fma_f32 v51, -v60, v27, v51\n"
"v_mfma_f32_16x16x32_bf16 v[32:35], a[72:75], v[100:103], v[32:35]\n"
"v_fma_f32 v44, -v60, v20, v44\n"
"v_fma_f32 v45, -v60, v21, v45\n"
"v_mfma_f32_16x16x32_bf16 v[32:35], a[84:87], v[104:107], v[32:35]\n"
"v_fma_f32 v46, -v60, v22, v46\n"
"s_waitcnt lgkmcnt(4)\n"
"v_mfma_f32_16x16x32_bf16 v[32:35], a[96:99], v[108:111], v[32:35]\n"
"ds_read_b128 v[88:91], v4 offset:18432\n"
"v_add_f32 v56, v56, v57\n"
"v_mfma_f32_16x16x32_bf16 v[32:35], a[108:111], v[112:115], v[32:35]\n"
"buffer_load_dwordx4 v2, s[32:35], s63 offen offset:2048 sc1 lds\n"
"v_add_f32 v58, v58, v59\n"
"v_mfma_f32_16x16x32_bf16 v[32:35], a[120:123], v[116:119], v[32:35]\n"
"v_add_f32 v56, v56, v58\n"
"v_fma_f32 v47, -v60, v23, v47\n"
"v_mfma_f32_16x16x32_bf16 v[32:35], a[132:135], v[120:123], v[32:35]\n"
"ds_read_b128 v[92:95], v4 offset:19584\n"
"v_add_f32_dpp v56, v56, v56 row_ror:8 row_mask:0xf bank_mask:0xf\n"
"v_mfma_f32_16x16x32_bf16 v[36:39], a[136:139], v[120:123], v[36:39]\n"
"v_mul_f32 v64, v44, v61\n"
"v_add_f32_dpp v56, v56, v56 row_ror:4 row_mask:0xf bank_mask:0xf\n"
"v_mfma_f32_16x16x32_bf16 v[36:39], a[124:127], v[116:119], v[36:39]\n"
"v_exp_f32 v64, v64\n"
"v_mfma_f32_16x16x32_bf16 v[36:39], a[112:115], v[112:115], v[36:39]\n"
"ds_read_b128 v[96:99], v4 offset:20736\n"
"v_add_f32_dpp v56, v56, v56 row_ror:2 row_mask:0xf bank_mask:0xf\n"
"v_mfma_f32_16x16x32_bf16 v[36:39], a[100:103], v[108:111], v[36:39]\n"
"v_mul_f32 v56, 0x3a800000, v56\n"
"v_mul_f32 v65, v45, v61\n"
"v_mfma_f32_16x16x32_bf16 v[40:43], a[104:107], v[108:111], v[40:43]\n"
"v_exp_f32 v65, v65\n"
"v_mfma_f32_16x16x32_bf16 v[40:43], a[116:119], v[112:115], v[40:43]\n"
"buffer_load_dwordx4 v2, s[32:35], s63 offen offset:3072 sc1 lds\n"
"ds_read_b128 v[100:103], v4 offset:21888\n"
"v_mfma_f32_16x16x32_bf16 v[40:43], a[128:131], v[116:119], v[40:43]\n"
"v_mul_f32 v57, v56, v56\n"
"v_mul_f32 v66, v46, v61\n"
"v_mfma_f32_16x16x32_bf16 v[40:43], a[140:143], v[120:123], v[40:43]\n"
"v_sub_f32_dpp v57, v56, v57 quad_perm:[1,1,3,3] row_mask:0xf bank_mask:0xf\n"
"s_waitcnt lgkmcnt(4)\n"
"v_mfma_f32_16x16x32_bf16 v[40:43], a[152:155], v[124:127], v[40:43]\n"
"ds_read_b128 v[104:107], v4 offset:23040\n"
"v_max_f32 v57, 0, v57\n"
"v_mfma_f32_16x16x32_bf16 v[40:43], a[164:167], v[76:79], v[40:43]\n"
"v_exp_f32 v66, v66\n"
"v_mfma_f32_16x16x32_bf16 v[40:43], a[176:179], v[80:83], v[40:43]\n"
"v_cvt_pk_bf16_f32 v57, 0, v57\n"
"v_mul_f32 v67, v47, v61\n"
"v_mfma_f32_16x16x32_bf16 v[40:43], a[188:191], v[84:87], v[40:43]\n"
"ds_read_b128 v[108:111], v4 offset:24192\n"
"s_add_u32 m0, s64, 4096\n"
"v_mfma_f32_16x16x32_bf16 v[36:39], a[184:187], v[84:87], v[36:39]\n"
"v_exp_f32 v67, v67\n"
"v_mfma_f32_16x16x32_bf16 v[36:39], a[172:175], v[80:83], v[36:39]\n"
"buffer_load_dwordx4 v6, s[32:35], s63 offen offset:0 sc1 lds\n"
"v_add_f32 v57, 0x3727c5ac, v57\n"
"v_mfma_f32_16x16x32_bf16 v[36:39], a[160:163], v[76:79], v[36:39]\n"
"ds_read_b128 v[112:115], v4 offset:25344\n"
"v_cvt_pk_bf16_f32 v57, 0, v57\n"
"v_mfma_f32_16x16x32_bf16 v[36:39], a[148:151], v[124:127], v[36:39]\n"
"v_sqrt_f32 v57, v57\n"
"v_mfma_f32_16x16x32_bf16 v[32:35], a[144:147], v[124:127], v[32:35]\n"
"v_cvt_pk_bf16_f32 v57, v56, v57\n"
"v_mul_f32 v68, v52, v61\n"
"v_mfma_f32_16x16x32_bf16 v[32:35], a[156:159], v[76:79], v[32:35]\n"
"ds_read_b128 v[116:119], v4 offset:26496\n"
"ds_write_b32 v10, v57 offset:0\n"
"v_mfma_f32_16x16x32_bf16 v[32:35], a[168:171], v[80:83], v[32:35]\n"
"v_exp_f32 v68, v68\n"
"v_mfma_f32_16x16x32_bf16 v[32:35], a[180:183], v[84:87], v[32:35]\n"
"v_mul_f32 v69, v53, v61\n"
"s_waitcnt lgkmcnt(5)\n"
"v_mfma_f32_16x16x32_bf16 v[32:35], a[192:195], v[88:91], v[32:35]\n"
"ds_read_b128 v[120:123], v4 offset:27648\n"
"v_mul_f32 v44, v44, v48\n"
"v_mfma_f32_16x16x32_bf16 v[32:35], a[204:207], v[92:95], v[32:35]\n"
"buffer_load_dwordx4 v6, s[32:35], s63 offen offset:1024 sc1 lds\n"
"v_mul_f32 v45, v45, v49\n"
"v_mfma_f32_16x16x32_bf16 v[32:35], a[216:219], v[96:99], v[32:35]\n"
"v_exp_f32 v69, v69\n"
"v_mfma_f32_16x16x32_bf16 v[32:35], a[228:231], v[100:103], v[32:35]\n"
"ds_read_b128 v[124:127], v4 offset:28800\n"
"v_mul_f32 v46, v46, v50\n"
"v_mfma_f32_16x16x32_bf16 v[36:39], a[232:235], v[100:103], v[36:39]\n"
"v_mul_f32 v47, v47, v51\n"
"v_mul_f32 v52, v52, v54\n"
"v_mfma_f32_16x16x32_bf16 v[36:39], a[220:223], v[96:99], v[36:39]\n"
"buffer_load_dwordx2 v[12:13], v14, s[32:35], s66 offen sc1\n"
"v_mul_f32 v53, v53, v55\n"
"v_mfma_f32_16x16x32_bf16 v[36:39], a[208:211], v[92:95], v[36:39]\n"
"ds_read_b128 v[76:79], v4 offset:29952\n"
"v_fma_f32 v64, v64, v62, v62\n"
"v_mfma_f32_16x16x32_bf16 v[36:39], a[196:199], v[88:91], v[36:39]\n"
"v_rcp_f32 v64, v64\n"
"v_mfma_f32_16x16x32_bf16 v[40:43], a[200:203], v[88:91], v[40:43]\n"
"v_fma_f32 v65, v65, v62, v62\n"
"v_fma_f32 v66, v66, v62, v62\n"
"v_mfma_f32_16x16x32_bf16 v[40:43], a[212:215], v[92:95], v[40:43]\n"
"buffer_load_dwordx4 v6, s[32:35], s63 offen offset:2048 sc1 lds\n"
"ds_read_b128 v[80:83], v4 offset:31104\n"
"v_mfma_f32_16x16x32_bf16 v[40:43], a[224:227], v[96:99], v[40:43]\n"
"v_rcp_f32 v65, v65\n"
"v_mfma_f32_16x16x32_bf16 v[40:43], a[236:239], v[100:103], v[40:43]\n"
"v_fma_f32 v67, v67, v62, v62\n"
"s_waitcnt lgkmcnt(5)\n"
"v_mfma_f32_16x16x32_bf16 v[40:43], a[248:251], v[104:107], v[40:43]\n"
"ds_read_b128 v[84:87], v4 offset:32256\n"
"v_fma_f32 v68, v68, v62, v62\n"
"v_mfma_f32_16x16x32_bf16 v[40:43], v[132:135], v[108:111], v[40:43]\n"
"v_rcp_f32 v66, v66\n"
"v_mfma_f32_16x16x32_bf16 v[40:43], v[144:147], v[112:115], v[40:43]\n"
"v_rcp_f32 v67, v67\n"
"v_mfma_f32_16x16x32_bf16 v[40:43], v[156:159], v[116:119], v[40:43]\n"
"ds_read_b128 v[88:91], v4 offset:33408\n"
"v_fma_f32 v69, v69, v62, v62\n"
"v_mfma_f32_16x16x32_bf16 v[36:39], v[152:155], v[116:119], v[36:39]\n"
"v_rcp_f32 v68, v68\n"
"v_mfma_f32_16x16x32_bf16 v[36:39], v[140:143], v[112:115], v[36:39]\n"
"buffer_load_dwordx4 v6, s[32:35], s63 offen offset:3072 sc1 lds\n"
"v_mul_f32 v44, v44, v64\n"
"v_mfma_f32_16x16x32_bf16 v[36:39], v[128:131], v[108:111], v[36:39]\n"
"ds_read_b128 v[92:95], v4 offset:34560\n"
"v_mul_f32 v45, v45, v65\n"
"v_mfma_f32_16x16x32_bf16 v[36:39], a[244:247], v[104:107], v[36:39]\n"
"v_rcp_f32 v69, v69\n"
"v_mfma_f32_16x16x32_bf16 v[32:35], a[240:243], v[104:107], v[32:35]\n"
"s_waitcnt vmcnt(9)\n"
"s_waitcnt lgkmcnt(7)\n"
"s_barrier\n"
"v_mfma_f32_16x16x32_bf16 v[32:35], a[252:255], v[108:111], v[32:35]\n"
"ds_read_b128 v[96:99], v4 offset:35712\n"
"v_mul_f32 v46, v46, v66\n"
"v_mfma_f32_16x16x32_bf16 v[32:35], v[136:139], v[112:115], v[32:35]\n"
"v_xor_b32 v4, 73728, v4\n"
"v_mul_f32 v47, v47, v67\n"
"v_mfma_f32_16x16x32_bf16 v[32:35], v[148:151], v[116:119], v[32:35]\n"
"s_add_u32 m0, s64, 8192\n"
"s_waitcnt lgkmcnt(4)\n"
"v_mfma_f32_16x16x32_bf16 v[32:35], v[160:163], v[120:123], v[32:35]\n"
"ds_read_b128 v[100:103], v5 offset:0\n"
"v_mul_f32 v52, v52, v68\n"
"v_mfma_f32_16x16x32_bf16 v[32:35], v[172:175], v[124:127], v[32:35]\n"
"buffer_store_dwordx2 v[16:17], v2, s[92:95], s68 idxen sc1\n"
"v_mul_f32 v53, v53, v69\n"
"v_mfma_f32_16x16x32_bf16 v[32:35], v[184:187], v[76:79], v[32:35]\n"
"v_cvt_pk_bf16_f32 v74, v52, v53\n"
"v_cvt_pk_bf16_f32 v72, v44, v45\n"
"v_mfma_f32_16x16x32_bf16 v[32:35], v[196:199], v[80:83], v[32:35]\n"
"ds_read_b128 v[104:107], v5 offset:1152\n"
"v_cvt_pk_bf16_f32 v73, v46, v47\n"
"v_mfma_f32_16x16x32_bf16 v[36:39], v[200:203], v[80:83], v[36:39]\n"
"v_permlane32_swap_b32 v74, v75\n"
"v_mfma_f32_16x16x32_bf16 v[36:39], v[188:191], v[76:79], v[36:39]\n"
"buffer_load_dwordx4 v7, s[32:35], s63 offen offset:0 sc1 lds\n"
"s_add_u32 s63, s63, 890880\n"
"v_mfma_f32_16x16x32_bf16 v[36:39], v[176:179], v[124:127], v[36:39]\n"
"ds_read_b128 v[108:111], v5 offset:2304\n"
"v_mfma_f32_16x16x32_bf16 v[36:39], v[164:167], v[120:123], v[36:39]\n"
"v_permlane16_swap_b32 v72, v74\n"
"v_mfma_f32_16x16x32_bf16 v[40:43], v[168:171], v[120:123], v[40:43]\n"
"v_permlane16_swap_b32 v73, v75\n"
"v_mfma_f32_16x16x32_bf16 v[40:43], v[180:183], v[124:127], v[40:43]\n"
"buffer_store_dwordx4 v[72:75], v15, s[32:35], s68 offen sc1\n"
"ds_read_b128 v[112:115], v5 offset:3456\n"
"v_mfma_f32_16x16x32_bf16 v[40:43], v[192:195], v[76:79], v[40:43]\n"
"v_mfma_f32_16x16x32_bf16 v[40:43], v[204:207], v[80:83], v[40:43]\n"
"s_waitcnt lgkmcnt(4)\n"
"v_mfma_f32_16x16x32_bf16 v[40:43], v[216:219], v[84:87], v[40:43]\n"
"ds_read_b128 v[116:119], v5 offset:4608\n"
"v_mfma_f32_16x16x32_bf16 v[40:43], v[228:231], v[88:91], v[40:43]\n"
"v_mfma_f32_16x16x32_bf16 v[40:43], v[240:243], v[92:95], v[40:43]\n"
"v_mfma_f32_16x16x32_bf16 v[40:43], v[252:255], v[96:99], v[40:43]\n"
"ds_read_b128 v[120:123], v5 offset:5760\n"
"v_mfma_f32_16x16x32_bf16 v[36:39], v[248:251], v[96:99], v[36:39]\n"
"v_mfma_f32_16x16x32_bf16 v[36:39], v[236:239], v[92:95], v[36:39]\n"
"v_mfma_f32_16x16x32_bf16 v[36:39], v[224:227], v[88:91], v[36:39]\n"
"ds_read_b128 v[124:127], v5 offset:6912\n"
"v_mfma_f32_16x16x32_bf16 v[36:39], v[212:215], v[84:87], v[36:39]\n"
"v_mfma_f32_16x16x32_bf16 v[32:35], v[208:211], v[84:87], v[32:35]\n"
"v_mfma_f32_16x16x32_bf16 v[32:35], v[220:223], v[88:91], v[32:35]\n"
"ds_read_b128 v[76:79], v5 offset:8064\n"
"v_mfma_f32_16x16x32_bf16 v[32:35], v[232:235], v[92:95], v[32:35]\n"
"s_sub_u32 s62, s62, 1\n"
"v_mfma_f32_16x16x32_bf16 v[32:35], v[244:247], v[96:99], v[32:35]\n"
"s_cbranch_scc1 .L_gt0_3\n"
"s_waitcnt lgkmcnt(4)\n"
"v_mfma_f32_16x16x32_bf16 v[44:47], a[0:3], v[100:103], 0\n"
"ds_read_b128 v[80:83], v5 offset:9216\n"
"ds_read_b32 v62, v11 offset:0\n"
"v_mfma_f32_16x16x32_bf16 v[44:47], a[12:15], v[104:107], v[44:47]\n"
"s_add_u32 s68, s68, 890880\n"
"v_mfma_f32_16x16x32_bf16 v[44:47], a[24:27], v[108:111], v[44:47]\n"
"v_mfma_f32_16x16x32_bf16 v[44:47], a[36:39], v[112:115], v[44:47]\n"
"ds_read_b128 v[84:87], v5 offset:10368\n"
"v_mfma_f32_16x16x32_bf16 v[48:51], a[40:43], v[112:115], 0\n"
"ds_read2st64_b32 v[56:57], v8 offset0:144 offset1:180\n"
"v_mfma_f32_16x16x32_bf16 v[48:51], a[28:31], v[108:111], v[48:51]\n"
"s_waitcnt vmcnt(5)\n"
"v_cmp_ne_u64 vcc, v[16:17], v[12:13]\n"
"v_mfma_f32_16x16x32_bf16 v[48:51], a[16:19], v[104:107], v[48:51]\n"
"ds_read_b128 v[88:91], v5 offset:11520\n"
"ds_read2st64_b32 v[58:59], v8 offset0:216 offset1:252\n"
"v_mfma_f32_16x16x32_bf16 v[48:51], a[4:7], v[100:103], v[48:51]\n"
"s_cbranch_vccnz .L_rtg1_3\n"
".L_bkg1_3:\n"
"s_add_u32 s66, s66, 890880\n"
"v_mfma_f32_16x16x32_bf16 v[52:55], a[8:11], v[100:103], 0\n"
"s_add_u32 m0, s64, 36864\n"
"v_xor_b32 v8, v9, v8\n"
"v_mfma_f32_16x16x32_bf16 v[52:55], a[20:23], v[104:107], v[52:55]\n"
"buffer_load_dwordx4 v2, s[32:35], s63 offen offset:0 sc1 lds\n"
"ds_read_b128 v[92:95], v5 offset:12672\n"
"v_mfma_f32_16x16x32_bf16 v[52:55], a[32:35], v[108:111], v[52:55]\n"
"s_waitcnt lgkmcnt(5)\n"
"v_lshlrev_b32 v60, 16, v62\n"
"v_mfma_f32_16x16x32_bf16 v[52:55], a[44:47], v[112:115], v[52:55]\n"
"v_and_b32 v62, 0xffff0000, v62\n"
"v_mfma_f32_16x16x32_bf16 v[52:55], a[56:59], v[116:119], v[52:55]\n"
"ds_read_b128 v[96:99], v5 offset:13824\n"
"v_fma_f32 v40, -v60, v28, v40\n"
"v_mfma_f32_16x16x32_bf16 v[52:55], a[68:71], v[120:123], v[52:55]\n"
"v_rcp_f32 v61, v62\n"
"v_mfma_f32_16x16x32_bf16 v[52:55], a[80:83], v[124:127], v[52:55]\n"
"v_mul_f32 v62, v62, v62\n"
"v_mul_f32 v61, 0xbfb8aa3b, v61\n"
"v_mfma_f32_16x16x32_bf16 v[52:55], a[92:95], v[76:79], v[52:55]\n"
"ds_read_b128 v[100:103], v5 offset:14976\n"
"v_fma_f32 v41, -v60, v29, v41\n"
"v_mfma_f32_16x16x32_bf16 v[48:51], a[88:91], v[76:79], v[48:51]\n"
"v_fma_f32 v42, -v60, v30, v42\n"
"v_fma_f32 v43, -v60, v31, v43\n"
"v_mfma_f32_16x16x32_bf16 v[48:51], a[76:79], v[124:127], v[48:51]\n"
"buffer_load_dwordx4 v2, s[32:35], s63 offen offset:1024 sc1 lds\n"
"v_fma_f32 v36, -v60, v24, v36\n"
"v_mfma_f32_16x16x32_bf16 v[48:51], a[64:67], v[120:123], v[48:51]\n"
"ds_read_b128 v[104:107], v5 offset:16128\n"
"v_fma_f32 v37, -v60, v25, v37\n"
"v_mfma_f32_16x16x32_bf16 v[48:51], a[52:55], v[116:119], v[48:51]\n"
"v_permlane32_swap_b32 v40, v42\n"
"v_mfma_f32_16x16x32_bf16 v[44:47], a[48:51], v[116:119], v[44:47]\n"
"v_permlane32_swap_b32 v41, v43\n"
"v_mfma_f32_16x16x32_bf16 v[44:47], a[60:63], v[120:123], v[44:47]\n"
"ds_read_b128 v[108:111], v5 offset:17280\n"
"v_fma_f32 v38, -v60, v26, v38\n"
"v_mfma_f32_16x16x32_bf16 v[44:47], a[72:75], v[124:127], v[44:47]\n"
"v_fma_f32 v39, -v60, v27, v39\n"
"v_fma_f32 v32, -v60, v20, v32\n"
"v_mfma_f32_16x16x32_bf16 v[44:47], a[84:87], v[76:79], v[44:47]\n"
"v_fma_f32 v33, -v60, v21, v33\n"
"s_waitcnt lgkmcnt(4)\n"
"v_mfma_f32_16x16x32_bf16 v[44:47], a[96:99], v[80:83], v[44:47]\n"
"ds_read_b128 v[112:115], v5 offset:18432\n"
"v_add_f32 v56, v56, v57\n"
"v_mfma_f32_16x16x32_bf16 v[44:47], a[108:111], v[84:87], v[44:47]\n"
"buffer_load_dwordx4 v2, s[32:35], s63 offen offset:2048 sc1 lds\n"
"v_add_f32 v58, v58, v59\n"
"v_mfma_f32_16x16x32_bf16 v[44:47], a[120:123], v[88:91], v[44:47]\n"
"v_add_f32 v56, v56, v58\n"
"v_fma_f32 v34, -v60, v22, v34\n"
"v_mfma_f32_16x16x32_bf16 v[44:47], a[132:135], v[92:95], v[44:47]\n"
"ds_read_b128 v[116:119], v5 offset:19584\n"
"v_add_f32_dpp v56, v56, v56 row_ror:8 row_mask:0xf bank_mask:0xf\n"
"v_mfma_f32_16x16x32_bf16 v[48:51], a[136:139], v[92:95], v[48:51]\n"
"v_fma_f32 v35, -v60, v23, v35\n"
"v_add_f32_dpp v56, v56, v56 row_ror:4 row_mask:0xf bank_mask:0xf\n"
"v_mfma_f32_16x16x32_bf16 v[48:51], a[124:127], v[88:91], v[48:51]\n"
"v_mul_f32 v64, v32, v61\n"
"v_add_f32_dpp v56, v56, v56 row_ror:2 row_mask:0xf bank_mask:0xf\n"
"v_mfma_f32_16x16x32_bf16 v[48:51], a[112:115], v[84:87], v[48:51]\n"
"ds_read_b128 v[120:123], v5 offset:20736\n"
"v_mul_f32 v56, 0x3a800000, v56\n"
"v_mfma_f32_16x16x32_bf16 v[48:51], a[100:103], v[80:83], v[48:51]\n"
"v_exp_f32 v64, v64\n"
"v_mfma_f32_16x16x32_bf16 v[52:55], a[104:107], v[80:83], v[52:55]\n"
"v_mul_f32 v57, v56, v56\n"
"v_mul_f32 v65, v33, v61\n"
"v_mfma_f32_16x16x32_bf16 v[52:55], a[116:119], v[84:87], v[52:55]\n"
"buffer_load_dwordx4 v2, s[32:35], s63 offen offset:3072 sc1 lds\n"
"ds_read_b128 v[124:127], v5 offset:21888\n"
"v_mfma_f32_16x16x32_bf16 v[52:55], a[128:131], v[88:91], v[52:55]\n"
"v_exp_f32 v65, v65\n"
"v_mfma_f32_16x16x32_bf16 v[52:55], a[140:143], v[92:95], v[52:55]\n"
"v_sub_f32_dpp v57, v56, v57 quad_perm:[1,1,3,3] row_mask:0xf bank_mask:0xf\n"
"s_waitcnt lgkmcnt(4)\n"
"v_mfma_f32_16x16x32_bf16 v[52:55], a[152:155], v[96:99], v[52:55]\n"
"ds_read_b128 v[76:79], v5 offset:23040\n"
"v_max_f32 v57, 0, v57\n"
"v_mfma_f32_16x16x32_bf16 v[52:55], a[164:167], v[100:103], v[52:55]\n"
"v_cvt_pk_bf16_f32 v57, 0, v57\n"
"v_mul_f32 v66, v34, v61\n"
"v_mfma_f32_16x16x32_bf16 v[52:55], a[176:179], v[104:107], v[52:55]\n"
"v_exp_f32 v66, v66\n"
"v_mfma_f32_16x16x32_bf16 v[52:55], a[188:191], v[108:111], v[52:55]\n"
"ds_read_b128 v[80:83], v5 offset:24192\n"
"s_add_u32 m0, s64, 40960\n"
"v_mfma_f32_16x16x32_bf16 v[48:51], a[184:187], v[108:111], v[48:51]\n"
"v_add_f32 v57, 0x3727c5ac, v57\n"
"v_mul_f32 v67, v35, v61\n"
"v_mfma_f32_16x16x32_bf16 v[48:51], a[172:175], v[104:107], v[48:51]\n"
"buffer_load_dwordx4 v6, s[32:35], s63 offen offset:0 sc1 lds\n"
"v_cvt_pk_bf16_f32 v57, 0, v57\n"
"v_mfma_f32_16x16x32_bf16 v[48:51], a[160:163], v[100:103], v[48:51]\n"
"ds_read_b128 v[84:87], v5 offset:25344\n"
"v_mul_f32 v68, v40, v61\n"
"v_mfma_f32_16x16x32_bf16 v[48:51], a[148:151], v[96:99], v[48:51]\n"
"v_sqrt_f32 v57, v57\n"
"v_mfma_f32_16x16x32_bf16 v[44:47], a[144:147], v[96:99], v[44:47]\n"
"v_exp_f32 v67, v67\n"
"v_mfma_f32_16x16x32_bf16 v[44:47], a[156:159], v[100:103], v[44:47]\n"
"ds_read_b128 v[88:91], v5 offset:26496\n"
"v_cvt_pk_bf16_f32 v57, v56, v57\n"
"v_mfma_f32_16x16x32_bf16 v[44:47], a[168:171], v[104:107], v[44:47]\n"
"ds_write_b32 v10, v57 offset:128\n"
"v_mul_f32 v69, v41, v61\n"
"v_mfma_f32_16x16x32_bf16 v[44:47], a[180:183], v[108:111], v[44:47]\n"
"v_mul_f32 v32, v32, v36\n"
"s_waitcnt lgkmcnt(5)\n"
"v_mfma_f32_16x16x32_bf16 v[44:47], a[192:195], v[112:115], v[44:47]\n"
"ds_read_b128 v[92:95], v5 offset:27648\n"
"v_mul_f32 v33, v33, v37\n"
"v_mfma_f32_16x16x32_bf16 v[44:47], a[204:207], v[116:119], v[44:47]\n"
"buffer_load_dwordx4 v6, s[32:35], s63 offen offset:1024 sc1 lds\n"
"v_mul_f32 v34, v34, v38\n"
"v_mfma_f32_16x16x32_bf16 v[44:47], a[216:219], v[120:123], v[44:47]\n"
"v_exp_f32 v68, v68\n"
"v_mfma_f32_16x16x32_bf16 v[44:47], a[228:231], v[124:127], v[44:47]\n"
"ds_read_b128 v[96:99], v5 offset:28800\n"
"v_mul_f32 v35, v35, v39\n"
"v_mfma_f32_16x16x32_bf16 v[48:51], a[232:235], v[124:127], v[48:51]\n"
"v_exp_f32 v69, v69\n"
"v_mfma_f32_16x16x32_bf16 v[48:51], a[220:223], v[120:123], v[48:51]\n"
"buffer_load_dwordx2 v[12:13], v14, s[32:35], s66 offen sc1\n"
"v_mul_f32 v40, v40, v42\n"
"v_mfma_f32_16x16x32_bf16 v[48:51], a[208:211], v[116:119], v[48:51]\n"
"ds_read_b128 v[100:103], v5 offset:29952\n"
"v_mul_f32 v41, v41, v43\n"
"v_mfma_f32_16x16x32_bf16 v[48:51], a[196:199], v[112:115], v[48:51]\n"
"v_fma_f32 v64, v64, v62, v62\n"
"v_fma_f32 v65, v65, v62, v62\n"
"v_mfma_f32_16x16x32_bf16 v[52:55], a[200:203], v[112:115], v[52:55]\n"
"v_rcp_f32 v64, v64\n"
"v_mfma_f32_16x16x32_bf16 v[52:55], a[212:215], v[116:119], v[52:55]\n"
"buffer_load_dwordx4 v6, s[32:35], s63 offen offset:2048 sc1 lds\n"
"ds_read_b128 v[104:107], v5 offset:31104\n"
"v_mfma_f32_16x16x32_bf16 v[52:55], a[224:227], v[120:123], v[52:55]\n"
"v_rcp_f32 v65, v65\n"
"v_mfma_f32_16x16x32_bf16 v[52:55], a[236:239], v[124:127], v[52:55]\n"
"v_fma_f32 v66, v66, v62, v62\n"
"s_waitcnt lgkmcnt(5)\n"
"v_mfma_f32_16x16x32_bf16 v[52:55], a[248:251], v[76:79], v[52:55]\n"
"ds_read_b128 v[108:111], v5 offset:32256\n"
"v_fma_f32 v67, v67, v62, v62\n"
"v_mfma_f32_16x16x32_bf16 v[52:55], v[132:135], v[80:83], v[52:55]\n"
"v_rcp_f32 v66, v66\n"
"v_mfma_f32_16x16x32_bf16 v[52:55], v[144:147], v[84:87], v[52:55]\n"
"v_rcp_f32 v67, v67\n"
"v_mfma_f32_16x16x32_bf16 v[52:55], v[156:159], v[88:91], v[52:55]\n"
"ds_read_b128 v[112:115], v5 offset:33408\n"
"v_fma_f32 v68, v68, v62, v62\n"
"v_mfma_f32_16x16x32_bf16 v[48:51], v[152:155], v[88:91], v[48:51]\n"
"v_rcp_f32 v68, v68\n"
"v_mfma_f32_16x16x32_bf16 v[48:51], v[140:143], v[84:87], v[48:51]\n"
"buffer_load_dwordx4 v6, s[32:35], s63 offen offset:3072 sc1 lds\n"
"v_fma_f32 v69, v69, v62, v62\n"
"v_mfma_f32_16x16x32_bf16 v[48:51], v[128:131], v[80:83], v[48:51]\n"
"ds_read_b128 v[116:119], v5 offset:34560\n"
"v_mul_f32 v32, v32, v64\n"
"v_mfma_f32_16x16x32_bf16 v[48:51], a[244:247], v[76:79], v[48:51]\n"
"v_rcp_f32 v69, v69\n"
"v_mfma_f32_16x16x32_bf16 v[44:47], a[240:243], v[76:79], v[44:47]\n"
"s_waitcnt vmcnt(9)\n"
"s_waitcnt lgkmcnt(7)\n"
"s_barrier\n"
"v_mfma_f32_16x16x32_bf16 v[44:47], a[252:255], v[80:83], v[44:47]\n"
"ds_read_b128 v[120:123], v5 offset:35712\n"
"v_mul_f32 v33, v33, v65\n"
"v_mfma_f32_16x16x32_bf16 v[44:47], v[136:139], v[84:87], v[44:47]\n"
"v_xor_b32 v5, 73728, v5\n"
"v_mul_f32 v34, v34, v66\n"
"v_mfma_f32_16x16x32_bf16 v[44:47], v[148:151], v[88:91], v[44:47]\n"
"s_add_u32 m0, s64, 45056\n"
"s_waitcnt lgkmcnt(4)\n"
"v_mfma_f32_16x16x32_bf16 v[44:47], v[160:163], v[92:95], v[44:47]\n"
"ds_read_b128 v[76:79], v4 offset:0\n"
"v_mul_f32 v35, v35, v67\n"
"v_mfma_f32_16x16x32_bf16 v[44:47], v[172:175], v[96:99], v[44:47]\n"
"buffer_store_dwordx2 v[16:17], v2, s[92:95], s68 idxen sc1\n"
"v_mul_f32 v40, v40, v68\n"
"v_mfma_f32_16x16x32_bf16 v[44:47], v[184:187], v[100:103], v[44:47]\n"
"v_mul_f32 v41, v41, v69\n"
"v_cvt_pk_bf16_f32 v72, v32, v33\n"
"v_mfma_f32_16x16x32_bf16 v[44:47], v[196:199], v[104:107], v[44:47]\n"
"ds_read_b128 v[80:83], v4 offset:1152\n"
"v_cvt_pk_bf16_f32 v74, v40, v41\n"
"v_mfma_f32_16x16x32_bf16 v[48:51], v[200:203], v[104:107], v[48:51]\n"
"v_cvt_pk_bf16_f32 v73, v34, v35\n"
"v_mfma_f32_16x16x32_bf16 v[48:51], v[188:191], v[100:103], v[48:51]\n"
"buffer_load_dwordx4 v7, s[32:35], s63 offen offset:0 sc1 lds\n"
"s_add_u32 s63, s63, 890880\n"
"v_mfma_f32_16x16x32_bf16 v[48:51], v[176:179], v[96:99], v[48:51]\n"
"ds_read_b128 v[84:87], v4 offset:2304\n"
"s_xor_b32 s64, s64, s65\n"
"v_mfma_f32_16x16x32_bf16 v[48:51], v[164:167], v[92:95], v[48:51]\n"
"v_permlane32_swap_b32 v74, v75\n"
"v_mfma_f32_16x16x32_bf16 v[52:55], v[168:171], v[92:95], v[52:55]\n"
"v_mfma_f32_16x16x32_bf16 v[52:55], v[180:183], v[96:99], v[52:55]\n"
"ds_read_b128 v[88:91], v4 offset:3456\n"
"v_mfma_f32_16x16x32_bf16 v[52:55], v[192:195], v[100:103], v[52:55]\n"
"v_permlane16_swap_b32 v72, v74\n"
"v_mfma_f32_16x16x32_bf16 v[52:55], v[204:207], v[104:107], v[52:55]\n"
"s_waitcnt lgkmcnt(4)\n"
"v_mfma_f32_16x16x32_bf16 v[52:55], v[216:219], v[108:111], v[52:55]\n"
"ds_read_b128 v[92:95], v4 offset:4608\n"
"v_mfma_f32_16x16x32_bf16 v[52:55], v[228:231], v[112:115], v[52:55]\n"
"v_permlane16_swap_b32 v73, v75\n"
"v_mfma_f32_16x16x32_bf16 v[52:55], v[240:243], v[116:119], v[52:55]\n"
"v_mfma_f32_16x16x32_bf16 v[52:55], v[252:255], v[120:123], v[52:55]\n"
"ds_read_b128 v[96:99], v4 offset:5760\n"
"v_mfma_f32_16x16x32_bf16 v[48:51], v[248:251], v[120:123], v[48:51]\n"
"v_mfma_f32_16x16x32_bf16 v[48:51], v[236:239], v[116:119], v[48:51]\n"
"buffer_store_dwordx4 v[72:75], v15, s[32:35], s68 offen sc1\n"
"v_mfma_f32_16x16x32_bf16 v[48:51], v[224:227], v[112:115], v[48:51]\n"
"ds_read_b128 v[100:103], v4 offset:6912\n"
"v_mfma_f32_16x16x32_bf16 v[48:51], v[212:215], v[108:111], v[48:51]\n"
"v_mfma_f32_16x16x32_bf16 v[44:47], v[208:211], v[108:111], v[44:47]\n"
"v_mfma_f32_16x16x32_bf16 v[44:47], v[220:223], v[112:115], v[44:47]\n"
"ds_read_b128 v[104:107], v4 offset:8064\n"
"v_mfma_f32_16x16x32_bf16 v[44:47], v[232:235], v[116:119], v[44:47]\n"
"s_sub_u32 s62, s62, 1\n"
"v_mfma_f32_16x16x32_bf16 v[44:47], v[244:247], v[120:123], v[44:47]\n"
"s_cbranch_scc0 .L_gl_3\n"
"s_nop 1\n"
"ds_read_b32 v62, v11 offset:128\n"
"s_add_u32 s68, s68, 890880\n"
"s_waitcnt lgkmcnt(0)\n"
"v_lshlrev_b32 v60, 16, v62\n"
"v_and_b32 v62, 0xffff0000, v62\n"
"v_rcp_f32 v61, v62\n"
"v_mul_f32 v62, v62, v62\n"
"v_mul_f32 v61, 0xbfb8aa3b, v61\n"
"v_fma_f32 v52, -v60, v28, v52\n"
"v_fma_f32 v53, -v60, v29, v53\n"
"v_fma_f32 v54, -v60, v30, v54\n"
"v_fma_f32 v55, -v60, v31, v55\n"
"v_fma_f32 v48, -v60, v24, v48\n"
"v_fma_f32 v49, -v60, v25, v49\n"
"v_fma_f32 v50, -v60, v26, v50\n"
"v_fma_f32 v51, -v60, v27, v51\n"
"v_fma_f32 v44, -v60, v20, v44\n"
"v_fma_f32 v45, -v60, v21, v45\n"
"v_fma_f32 v46, -v60, v22, v46\n"
"v_fma_f32 v47, -v60, v23, v47\n"
"v_permlane32_swap_b32 v52, v54\n"
"v_permlane32_swap_b32 v53, v55\n"
"v_mul_f32 v64, v44, v61\n"
"v_mul_f32 v65, v45, v61\n"
"v_mul_f32 v66, v46, v61\n"
"v_mul_f32 v67, v47, v61\n"
"v_mul_f32 v68, v52, v61\n"
"v_mul_f32 v69, v53, v61\n"
"v_exp_f32 v64, v64\n"
"v_exp_f32 v65, v65\n"
"v_mul_f32 v44, v44, v48\n"
"v_mul_f32 v45, v45, v49\n"
"v_exp_f32 v66, v66\n"
"v_exp_f32 v67, v67\n"
"v_mul_f32 v46, v46, v50\n"
"v_mul_f32 v47, v47, v51\n"
"v_exp_f32 v68, v68\n"
"v_exp_f32 v69, v69\n"
"v_mul_f32 v52, v52, v54\n"
"v_mul_f32 v53, v53, v55\n"
"v_fma_f32 v64, v64, v62, v62\n"
"v_fma_f32 v65, v65, v62, v62\n"
"v_rcp_f32 v64, v64\n"
"v_rcp_f32 v65, v65\n"
"v_fma_f32 v66, v66, v62, v62\n"
"v_fma_f32 v67, v67, v62, v62\n"
"v_rcp_f32 v66, v66\n"
"v_rcp_f32 v67, v67\n"
"v_fma_f32 v68, v68, v62, v62\n"
"v_fma_f32 v69, v69, v62, v62\n"
"v_rcp_f32 v68, v68\n"
"v_rcp_f32 v69, v69\n"
"v_mul_f32 v44, v44, v64\n"
"v_mul_f32 v45, v45, v65\n"
"v_mul_f32 v46, v46, v66\n"
"v_mul_f32 v47, v47, v67\n"
"v_mul_f32 v52, v52, v68\n"
"v_mul_f32 v53, v53, v69\n"
"v_cvt_pk_bf16_f32 v74, v52, v53\n"
"v_cvt_pk_bf16_f32 v72, v44, v45\n"
"v_cvt_pk_bf16_f32 v73, v46, v47\n"
"v_permlane32_swap_b32 v74, v75\n"
"s_nop 1\n"
"v_permlane16_swap_b32 v72, v74\n"
"v_permlane16_swap_b32 v73, v75\n"
"buffer_store_dwordx4 v[72:75], v15, s[32:35], s68 offen sc1\n"
"s_waitcnt vmcnt(1)\n"
"s_barrier\n"
"buffer_store_dwordx2 v[16:17], v2, s[92:95], s68 idxen sc1\n"
"s_add_u32 s68, s68, 890880\n"
"s_waitcnt vmcnt(1)\n"
"s_barrier\n"
"buffer_store_dwordx2 v[16:17], v2, s[92:95], s68 idxen sc1\n"
"s_branch .L_end\n"
".L_gt0_3:\n"
"s_nop 1\n"
"ds_read_b32 v62, v11 offset:0\n"
"s_add_u32 s68, s68, 890880\n"
"s_waitcnt lgkmcnt(0)\n"
"v_lshlrev_b32 v60, 16, v62\n"
"v_and_b32 v62, 0xffff0000, v62\n"
"v_rcp_f32 v61, v62\n"
"v_mul_f32 v62, v62, v62\n"
"v_mul_f32 v61, 0xbfb8aa3b, v61\n"
"v_fma_f32 v40, -v60, v28, v40\n"
"v_fma_f32 v41, -v60, v29, v41\n"
"v_fma_f32 v42, -v60, v30, v42\n"
"v_fma_f32 v43, -v60, v31, v43\n"
"v_fma_f32 v36, -v60, v24, v36\n"
"v_fma_f32 v37, -v60, v25, v37\n"
"v_fma_f32 v38, -v60, v26, v38\n"
"v_fma_f32 v39, -v60, v27, v39\n"
"v_fma_f32 v32, -v60, v20, v32\n"
"v_fma_f32 v33, -v60, v21, v33\n"
"v_fma_f32 v34, -v60, v22, v34\n"
"v_fma_f32 v35, -v60, v23, v35\n"
"v_permlane32_swap_b32 v40, v42\n"
"v_permlane32_swap_b32 v41, v43\n"
"v_mul_f32 v64, v32, v61\n"
"v_mul_f32 v65, v33, v61\n"
"v_mul_f32 v66, v34, v61\n"
"v_mul_f32 v67, v35, v61\n"
"v_mul_f32 v68, v40, v61\n"
"v_mul_f32 v69, v41, v61\n"
"v_exp_f32 v64, v64\n"
"v_exp_f32 v65, v65\n"
"v_mul_f32 v32, v32, v36\n"
"v_mul_f32 v33, v33, v37\n"
"v_exp_f32 v66, v66\n"
"v_exp_f32 v67, v67\n"
"v_mul_f32 v34, v34, v38\n"
"v_mul_f32 v35, v35, v39\n"
"v_exp_f32 v68, v68\n"
"v_exp_f32 v69, v69\n"
"v_mul_f32 v40, v40, v42\n"
"v_mul_f32 v41, v41, v43\n"
"v_fma_f32 v64, v64, v62, v62\n"
"v_fma_f32 v65, v65, v62, v62\n"
"v_rcp_f32 v64, v64\n"
"v_rcp_f32 v65, v65\n"
"v_fma_f32 v66, v66, v62, v62\n"
"v_fma_f32 v67, v67, v62, v62\n"
"v_rcp_f32 v66, v66\n"
"v_rcp_f32 v67, v67\n"
"v_fma_f32 v68, v68, v62, v62\n"
"v_fma_f32 v69, v69, v62, v62\n"
"v_rcp_f32 v68, v68\n"
"v_rcp_f32 v69, v69\n"
"v_mul_f32 v32, v32, v64\n"
"v_mul_f32 v33, v33, v65\n"
"v_mul_f32 v34, v34, v66\n"
"v_mul_f32 v35, v35, v67\n"
"v_mul_f32 v40, v40, v68\n"
"v_mul_f32 v41, v41, v69\n"
"v_cvt_pk_bf16_f32 v74, v40, v41\n"
"v_cvt_pk_bf16_f32 v72, v32, v33\n"
"v_cvt_pk_bf16_f32 v73, v34, v35\n"
"v_permlane32_swap_b32 v74, v75\n"
"s_nop 1\n"
"v_permlane16_swap_b32 v72, v74\n"
"v_permlane16_swap_b32 v73, v75\n"
"buffer_store_dwordx4 v[72:75], v15, s[32:35], s68 offen sc1\n"
"s_waitcnt vmcnt(1)\n"
"s_barrier\n"
"buffer_store_dwordx2 v[16:17], v2, s[92:95], s68 idxen sc1\n"
"s_add_u32 s68, s68, 890880\n"
"s_waitcnt vmcnt(1)\n"
"s_barrier\n"
"buffer_store_dwordx2 v[16:17], v2, s[92:95], s68 idxen sc1\n"
"s_branch .L_end\n"
".L_q:\n"
"v_mov_b32 v16, 0x3f803f80\n"
"v_mov_b32 v17, 0x3f803f80\n"
"v_mov_b32 v18, 0x3f803f80\n"
"v_mov_b32 v19, 0x3f803f80\n"
"s_add_u32 s36, s10, 0x0\n"
"s_addc_u32 s37, s11, 0x0\n"
"s_and_b32 s37, s37, 0xffff\n"
"s_mov_b32 s38, 0x600000\n"
"s_mov_b32 s39, 0x20000\n"
"s_add_u32 s40, s10, 0x400\n"
"s_addc_u32 s41, s11, 0x0\n"
"s_and_b32 s41, s41, 0xffff\n"
"s_mov_b32 s42, 0x5ffc00\n"
"s_mov_b32 s43, 0x20000\n"
"s_add_u32 s44, s10, 0x800\n"
"s_addc_u32 s45, s11, 0x0\n"
"s_and_b32 s45, s45, 0xffff\n"
"s_mov_b32 s46, 0x5ff800\n"
"s_mov_b32 s47, 0x20000\n"
"s_lshl_b32 s96, s27, 7\n"
"v_and_b32 v68, 3, v1\n"
"v_mul_u32_u24 v68, 8, v68\n"
"v_and_b32 v71, 1, v1\n"
"v_bfe_u32 v3, v1, 1, 1\n"
"v_lshl_add_u32 v71, v71, 1, v3\n"
"v_mul_u32_u24 v71, 8, v71\n"
"s_lshl_b32 s48, s26, 5\n"
"v_add_u32 v68, s48, v68\n"
"v_add_u32 v71, s48, v71\n"
"v_lshrrev_b32 v64, 3, v1\n"
"v_bfe_u32 v65, v1, 4, 1\n"
"s_and_b32 s48, s26, 1\n"
"s_lshl_b32 s48, s48, 1\n"
"v_or_b32 v65, s48, v65\n"
"v_lshlrev_b32 v65, 1, v65\n"
"v_and_b32 v66, 7, v1\n"
"v_xor_b32 v65, v65, v66\n"
"v_mul_u32_u24 v64, 6144, v64\n"
"v_lshl_add_u32 v67, v65, 4, v64\n"
"s_mul_i32 s48, s26, 49152\n"
"s_add_u32 s96, s96, s48\n"
"s_mul_i32 s97, s26, 3072\n"
"s_add_u32 m0, s97, 49152\n"
"s_nop 0\n"
"buffer_load_dwordx4 v67, s[36:39], s96 offen offset:0 lds\n"
"buffer_load_dwordx4 v67, s[40:43], s96 offen offset:1024 lds\n"
"buffer_load_dwordx4 v67, s[44:47], s96 offen offset:2048 lds\n"
"s_add_u32 s96, s96, 196608\n"
"s_add_u32 m0, s97, 61440\n"
"s_nop 0\n"
"buffer_load_dwordx4 v67, s[36:39], s96 offen offset:0 lds\n"
"buffer_load_dwordx4 v67, s[40:43], s96 offen offset:1024 lds\n"
"buffer_load_dwordx4 v67, s[44:47], s96 offen offset:2048 lds\n"
"s_add_u32 s96, s96, 196608\n"
"s_add_u32 m0, s97, 73728\n"
"s_nop 0\n"
"buffer_load_dwordx4 v67, s[36:39], s96 offen offset:0 lds\n"
"buffer_load_dwordx4 v67, s[40:43], s96 offen offset:1024 lds\n"
"buffer_load_dwordx4 v67, s[44:47], s96 offen offset:2048 lds\n"
"s_add_u32 s96, s96, 196608\n"
"s_add_u32 m0, s97, 86016\n"
"s_nop 0\n"
"buffer_load_dwordx4 v67, s[36:39], s96 offen offset:0 lds\n"
"buffer_load_dwordx4 v67, s[40:43], s96 offen offset:1024 lds\n"
"buffer_load_dwordx4 v67, s[44:47], s96 offen offset:2048 lds\n"
"s_add_u32 s96, s96, 196608\n"
"s_add_u32 m0, s97, 98304\n"
"s_nop 0\n"
"buffer_load_dwordx4 v67, s[36:39], s96 offen offset:0 lds\n"
"buffer_load_dwordx4 v67, s[40:43], s96 offen offset:1024 lds\n"
"buffer_load_dwordx4 v67, s[44:47], s96 offen offset:2048 lds\n"
"s_add_u32 s96, s96, 196608\n"
"s_add_u32 m0, s97, 110592\n"
"s_nop 0\n"
"buffer_load_dwordx4 v67, s[36:39], s96 offen offset:0 lds\n"
"buffer_load_dwordx4 v67, s[40:43], s96 offen offset:1024 lds\n"
"buffer_load_dwordx4 v67, s[44:47], s96 offen offset:2048 lds\n"
"s_add_u32 s96, s96, 196608\n"
"s_add_u32 m0, s97, 122880\n"
"s_nop 0\n"
"buffer_load_dwordx4 v67, s[36:39], s96 offen offset:0 lds\n"
"buffer_load_dwordx4 v67, s[40:43], s96 offen offset:1024 lds\n"
"buffer_load_dwordx4 v67, s[44:47], s96 offen offset:2048 lds\n"
"s_add_u32 s96, s96, 196608\n"
"s_add_u32 m0, s97, 135168\n"
"s_nop 0\n"
"buffer_load_dwordx4 v67, s[36:39], s96 offen offset:0 lds\n"
"buffer_load_dwordx4 v67, s[40:43], s96 offen offset:1024 lds\n"
"buffer_load_dwordx4 v67, s[44:47], s96 offen offset:2048 lds\n"
"s_add_u32 s96, s96, 196608\n"
"s_add_u32 m0, s97, 0\n"
"s_nop 0\n"
"buffer_load_dwordx4 v67, s[36:39], s96 offen offset:0 lds\n"
"buffer_load_dwordx4 v67, s[40:43], s96 offen offset:1024 lds\n"
"buffer_load_dwordx4 v67, s[44:47], s96 offen offset:2048 lds\n"
"s_add_u32 s96, s96, 196608\n"
"s_add_u32 m0, s97, 12288\n"
"s_nop 0\n"
"buffer_load_dwordx4 v67, s[36:39], s96 offen offset:0 lds\n"
"buffer_load_dwordx4 v67, s[40:43], s96 offen offset:1024 lds\n"
"buffer_load_dwordx4 v67, s[44:47], s96 offen offset:2048 lds\n"
"s_add_u32 s96, s96, 196608\n"
"v_lshrrev_b32 v65, 4, v1\n"
"v_bfe_u32 v64, v1, 3, 1\n"
"v_and_b32 v66, 1, v65\n"
"v_lshl_add_u32 v64, v66, 1, v64\n"
"v_lshlrev_b32 v64, 5, v64\n"
"v_mul_u32_u24 v65, 3072, v65\n"
"v_bfe_u32 v66, v1, 2, 2\n"
"v_lshl_add_u32 v65, v66, 7, v65\n"
"v_xor_b32 v68, v64, v68\n"
"v_add_u32 v68, v65, v68\n"
"v_add_u32 v69, 49152, v68\n"
"v_add_u32 v70, 98304, v68\n"
"v_xor_b32 v71, v64, v71\n"
"v_add_u32 v71, v65, v71\n"
"v_add_u32 v72, 49152, v71\n"
"v_add_u32 v73, 98304, v71\n"
"s_lshl2_add_u32 s48, s27, s26\n"
"s_lshl_b32 s48, s48, 9\n"
"s_add_u32 s68, s48, 4648960\n"
"s_lshl2_add_u32 s49, s27, 32\n"
"s_waitcnt lgkmcnt(0)\n"
"s_add_u32 s54, s52, 0x7f4a7c15\n"
"s_addc_u32 s55, s53, 0x9e3779b9\n"
"s_lshl_b32 s67, s49, 5\n"
"s_add_u32 s67, s67, 4898816\n"
"s_sub_u32 s51, s67, s68\n"
"s_add_u32 s92, s32, s51\n"
"s_addc_u32 s93, s33, 0x10000\n"
"s_cmp_eq_u32 s26, 0\n"
"s_cselect_b32 s94, 1, 0\n"
"s_mov_b32 s95, 0x20000\n"
"v_mov_b32 v30, s54\n"
"v_mov_b32 v31, s55\n"
"v_cmp_gt_u32 vcc, 12, v1\n"
"v_mul_u32_u24 v3, 296960, v1\n"
"v_mov_b32 v12, 0x7fff0000\n"
"v_cndmask_b32 v3, v12, v3, vcc\n"
"s_add_u32 s51, s67, 76021760\n"
"buffer_store_dwordx2 v[30:31], v3, s[32:35], s51 offen sc1\n"
"s_sub_u32 s67, s68, 296960\n"
"s_mul_i32 s49, s28, 296960\n"
"s_add_u32 s68, s68, s49\n"
"s_sub_u32 s68, s68, 3563520\n"
"s_mov_b32 s62, 32\n"
"s_add_u32 s48, s28, 3\n"
"s_and_b32 s48, s48, 3\n"
"s_mul_i32 s48, s48, 296960\n"
"s_add_u32 s66, s48, 41721856\n"
"s_sub_u32 s48, s48, s49\n"
"s_add_u32 s69, s48, 38010880\n"
"s_sub_u32 s70, 0xfdce2000, s48\n"
"s_mov_b32 s72, s70\n"
"s_mov_b32 s63, 0xfffba000\n"
"s_mul_i32 s64, s26, 9216\n"
"s_add_u32 s65, s64, 73728\n"
"s_xor_b32 s65, s65, s64\n"
"s_add_u32 s63, s63, s64\n"
"v_mov_b32 v4, v2\n"
"v_add_u32 v5, 36864, v2\n"
"s_mov_b32 s71, s63\n"
"v_bfe_u32 v3, v1, 1, 3\n"
"v_mul_u32_u24 v6, 1152, v3\n"
"v_lshrrev_b32 v3, 1, v3\n"
"v_add_u32 v3, s26, v3\n"
"v_lshrrev_b32 v7, 4, v1\n"
"v_lshl_add_u32 v3, v3, 2, v7\n"
"v_and_b32 v3, 15, v3\n"
"v_lshl_add_u32 v6, v3, 2, v6\n"
"v_and_b32 v3, 1, v1\n"
"v_lshl_add_u32 v6, v3, 6, v6\n"
"v_add_u32 v6, 1024, v6\n"
"s_lshl_b32 s48, s26, 2\n"
"v_add_u32 v8, s48, v7\n"
"v_and_b32 v9, 15, v1\n"
"v_add_u32 v3, 64, v1\n"
"v_cmp_eq_u32 vcc, 0, v9\n"
"v_add_u32 v7, 73728, v6\n"
"v_xor_b32 v7, v7, v6\n"
"v_cndmask_b32 v8, v3, v8, vcc\n"
"v_lshlrev_b32 v8, 2, v8\n"
"v_add_u32 v8, 147456, v8\n"
"v_lshlrev_b32 v9, 2, v9\n"
"v_add_u32 v9, 147456, v9\n"
"v_lshrrev_b32 v10, 4, v1\n"
"v_and_b32 v32, 15, v1\n"
"v_and_b32 v33, 1, v1\n"
"v_lshl_add_u32 v34, v10, 2, v33\n"
"v_lshlrev_b32 v33, 4, v33\n"
"v_mov_b32 v35, 0xffff\n"
"v_lshlrev_b32 v33, v33, v35\n"
"v_cmp_le_u32 vcc, v34, v32\n"
"v_add_u32 v34, 2, v34\n"
"s_nop 0\n"
"v_cndmask_b32 v124, 0, v33, vcc\n"
"v_xor_b32 v126, v124, v33\n"
"v_cmp_le_u32 vcc, v34, v32\n"
"s_nop 1\n"
"v_cndmask_b32 v125, 0, v33, vcc\n"
"v_xor_b32 v127, v125, v33\n"
"v_lshlrev_b32 v10, 4, v10\n"
"v_add_u32 v10, 147456, v10\n"
"s_lshl_b32 s48, s26, 10\n"
"s_add_u32 s48, s48, 148480\n"
"v_lshlrev_b32 v11, 3, v1\n"
"v_add_u32 v11, s48, v11\n"
"s_lshl_b32 s48, s26, 3\n"
"s_add_u32 s48, s48, 0\n"
"v_add_u32 v0, s48, v1\n"
"v_lshrrev_b32 v0, 2, v0\n"
"v_lshlrev_b32 v0, 7, v0\n"
"v_cmp_gt_u32 vcc, 8, v1\n"
"v_mov_b32 v3, 0x7fff0000\n"
"s_nop 0\n"
"v_cndmask_b32 v0, v3, v0, vcc\n"
"v_cndmask_b32 v30, 0, v30, vcc\n"
"v_cndmask_b32 v31, 0, v31, vcc\n"
"v_mov_b32 v116, 0xff61b1e6\n"
"v_mov_b32 v117, 0xff61b1e6\n"
"v_mov_b32 v118, 0xff61b1e6\n"
"v_mov_b32 v119, 0xff61b1e6\n"
"v_mov_b32 v120, 0xff61b1e6\n"
"v_mov_b32 v121, 0xff61b1e6\n"
"v_mov_b32 v122, 0xff61b1e6\n"
"v_mov_b32 v123, 0xff61b1e6\n"
"v_cmp_ne_u32 vcc, 0xffff, v124\n"
"s_nop 1\n"
"v_cndmask_b32 v116, 0, v116, vcc\n"
"v_cmp_ne_u32 vcc, 0xffff0000, v124\n"
"s_nop 1\n"
"v_cndmask_b32 v117, 0, v117, vcc\n"
"v_cmp_ne_u32 vcc, 0xffff, v125\n"
"s_nop 1\n"
"v_cndmask_b32 v118, 0, v118, vcc\n"
"v_cmp_ne_u32 vcc, 0xffff0000, v125\n"
"s_nop 1\n"
"v_cndmask_b32 v119, 0, v119, vcc\n"
"v_cmp_ne_u32 vcc, 0xffff, v126\n"
"s_nop 1\n"
"v_cndmask_b32 v120, 0, v120, vcc\n"
"v_cmp_ne_u32 vcc, 0xffff0000, v126\n"
"s_nop 1\n"
"v_cndmask_b32 v121, 0, v121, vcc\n"
"v_cmp_ne_u32 vcc, 0xffff, v127\n"
"s_nop 1\n"
"v_cndmask_b32 v122, 0, v122, vcc\n"
"v_cmp_ne_u32 vcc, 0xffff0000, v127\n"
"s_nop 1\n"
"v_cndmask_b32 v123, 0, v123, vcc\n"
"v_cmp_gt_u32 vcc, 32, v1\n"
"v_mov_b32 v1, 0x7fff0000\n"
"s_nop 0\n"
"v_cndmask_b32 v1, v1, v2, vcc\n"
"s_mov_b32 s58, 0x4003c000\n"
"s_mov_b32 s59, 0x0\n"
"s_mov_b32 s60, 0x80028000\n"
"s_mov_b32 s61, 0x0\n"
"v_subrev_u32 v3, 148480, v11\n"
"v_xor_b32 v3, 1024, v3\n"
"v_add_u32 v3, 148480, v3\n"
"s_add_u32 s74, s66, s72\n"
"s_waitcnt vmcnt(25)\n"
"s_barrier\n"
"s_add_u32 m0, s97, 24576\n"
"s_nop 0\n"
"buffer_load_dwordx4 v67, s[36:39], s96 offen offset:0 lds\n"
"buffer_load_dwordx4 v67, s[40:43], s96 offen offset:1024 lds\n"
"buffer_load_dwordx4 v67, s[44:47], s96 offen offset:2048 lds\n"
"s_add_u32 s96, s96, 196608\n"
"s_add_u32 m0, s97, 36864\n"
"s_nop 0\n"
"buffer_load_dwordx4 v67, s[36:39], s96 offen offset:0 lds\n"
"buffer_load_dwordx4 v67, s[40:43], s96 offen offset:1024 lds\n"
"buffer_load_dwordx4 v67, s[44:47], s96 offen offset:2048 lds\n"
"s_add_u32 s96, s96, 196608\n"
"ds_read_b64_tr_b16 a[0:1], v69 offset:0\n"
"ds_read_b64_tr_b16 a[2:3], v69 offset:512\n"
"ds_read_b64_tr_b16 a[4:5], v69 offset:1024\n"
"ds_read_b64_tr_b16 a[6:7], v69 offset:1536\n"
"ds_read_b64_tr_b16 a[8:9], v72 offset:2048\n"
"ds_read_b64_tr_b16 a[10:11], v72 offset:2560\n"
"ds_read_b64_tr_b16 a[12:13], v69 offset:12288\n"
"ds_read_b64_tr_b16 a[14:15], v69 offset:12800\n"
"ds_read_b64_tr_b16 a[16:17], v69 offset:13312\n"
"ds_read_b64_tr_b16 a[18:19], v69 offset:13824\n"
"ds_read_b64_tr_b16 a[20:21], v72 offset:14336\n"
"ds_read_b64_tr_b16 a[22:23], v72 offset:14848\n"
"s_waitcnt lgkmcnt(0)\n"
"v_mfma_f32_16x16x32_bf16 v[20:23], a[0:3], v[16:19], 0\n"
"v_mfma_f32_16x16x32_bf16 v[24:27], a[4:7], v[16:19], 0\n"
"v_mfma_f32_16x16x32_bf16 v[32:35], v[16:19], a[8:11], 0\n"
"v_mfma_f32_16x16x32_bf16 v[20:23], a[12:15], v[16:19], v[20:23]\n"
"v_mfma_f32_16x16x32_bf16 v[24:27], a[16:19], v[16:19], v[24:27]\n"
"v_mfma_f32_16x16x32_bf16 v[32:35], v[16:19], a[20:23], v[32:35]\n"
"s_waitcnt vmcnt(25)\n"
"s_barrier\n"
"s_add_u32 m0, s97, 49152\n"
"s_nop 0\n"
"buffer_load_dwordx4 v67, s[36:39], s96 offen offset:0 lds\n"
"buffer_load_dwordx4 v67, s[40:43], s96 offen offset:1024 lds\n"
"buffer_load_dwordx4 v67, s[44:47], s96 offen offset:2048 lds\n"
"s_add_u32 s96, s96, 196608\n"
"s_add_u32 m0, s97, 61440\n"
"s_nop 0\n"
"buffer_load_dwordx4 v67, s[36:39], s96 offen offset:0 lds\n"
"buffer_load_dwordx4 v67, s[40:43], s96 offen offset:1024 lds\n"
"buffer_load_dwordx4 v67, s[44:47], s96 offen offset:2048 lds\n"
"s_add_u32 s96, s96, 196608\n"
"ds_read_b64_tr_b16 a[24:25], v69 offset:24576\n"
"ds_read_b64_tr_b16 a[26:27], v69 offset:25088\n"
"ds_read_b64_tr_b16 a[28:29], v69 offset:25600\n"
"ds_read_b64_tr_b16 a[30:31], v69 offset:26112\n"
"ds_read_b64_tr_b16 a[32:33], v72 offset:26624\n"
"ds_read_b64_tr_b16 a[34:35], v72 offset:27136\n"
"ds_read_b64_tr_b16 a[36:37], v69 offset:36864\n"
"ds_read_b64_tr_b16 a[38:39], v69 offset:37376\n"
"ds_read_b64_tr_b16 a[40:41], v69 offset:37888\n"
"ds_read_b64_tr_b16 a[42:43], v69 offset:38400\n"
"ds_read_b64_tr_b16 a[44:45], v72 offset:38912\n"
"ds_read_b64_tr_b16 a[46:47], v72 offset:39424\n"
"s_waitcnt lgkmcnt(0)\n"
"v_mfma_f32_16x16x32_bf16 v[20:23], a[24:27], v[16:19], v[20:23]\n"
"v_mfma_f32_16x16x32_bf16 v[24:27], a[28:31], v[16:19], v[24:27]\n"
"v_mfma_f32_16x16x32_bf16 v[32:35], v[16:19], a[32:35], v[32:35]\n"
"v_mfma_f32_16x16x32_bf16 v[20:23], a[36:39], v[16:19], v[20:23]\n"
"v_mfma_f32_16x16x32_bf16 v[24:27], a[40:43], v[16:19], v[24:27]\n"
"v_mfma_f32_16x16x32_bf16 v[32:35], v[16:19], a[44:47], v[32:35]\n"
"s_waitcnt vmcnt(25)\n"
"s_barrier\n"
"s_add_u32 m0, s97, 73728\n"
"s_nop 0\n"
"buffer_load_dwordx4 v67, s[36:39], s96 offen offset:0 lds\n"
"buffer_load_dwordx4 v67, s[40:43], s96 offen offset:1024 lds\n"
"buffer_load_dwordx4 v67, s[44:47], s96 offen offset:2048 lds\n"
"s_add_u32 s96, s96, 196608\n"
"s_add_u32 m0, s97, 86016\n"
"s_nop 0\n"
"buffer_load_dwordx4 v67, s[36:39], s96 offen offset:0 lds\n"
"buffer_load_dwordx4 v67, s[40:43], s96 offen offset:1024 lds\n"
"buffer_load_dwordx4 v67, s[44:47], s96 offen offset:2048 lds\n"
"s_add_u32 s96, s96, 196608\n"
"ds_read_b64_tr_b16 a[48:49], v70 offset:0\n"
"ds_read_b64_tr_b16 a[50:51], v70 offset:512\n"
"ds_read_b64_tr_b16 a[52:53], v70 offset:1024\n"
"ds_read_b64_tr_b16 a[54:55], v70 offset:1536\n"
"ds_read_b64_tr_b16 a[56:57], v73 offset:2048\n"
"ds_read_b64_tr_b16 a[58:59], v73 offset:2560\n"
"ds_read_b64_tr_b16 a[60:61], v70 offset:12288\n"
"ds_read_b64_tr_b16 a[62:63], v70 offset:12800\n"
"ds_read_b64_tr_b16 a[64:65], v70 offset:13312\n"
"ds_read_b64_tr_b16 a[66:67], v70 offset:13824\n"
"ds_read_b64_tr_b16 a[68:69], v73 offset:14336\n"
"ds_read_b64_tr_b16 a[70:71], v73 offset:14848\n"
"s_waitcnt lgkmcnt(0)\n"
"v_mfma_f32_16x16x32_bf16 v[20:23], a[48:51], v[16:19], v[20:23]\n"
"v_mfma_f32_16x16x32_bf16 v[24:27], a[52:55], v[16:19], v[24:27]\n"
"v_mfma_f32_16x16x32_bf16 v[32:35], v[16:19], a[56:59], v[32:35]\n"
"v_mfma_f32_16x16x32_bf16 v[20:23], a[60:63], v[16:19], v[20:23]\n"
"v_mfma_f32_16x16x32_bf16 v[24:27], a[64:67], v[16:19], v[24:27]\n"
"v_mfma_f32_16x16x32_bf16 v[32:35], v[16:19], a[68:71], v[32:35]\n"
"s_waitcnt vmcnt(25)\n"
"s_barrier\n"
"s_add_u32 m0, s97, 98304\n"
"s_nop 0\n"
"buffer_load_dwordx4 v67, s[36:39], s96 offen offset:0 lds\n"
"buffer_load_dwordx4 v67, s[40:43], s96 offen offset:1024 lds\n"
"buffer_load_dwordx4 v67, s[44:47], s96 offen offset:2048 lds\n"
"s_add_u32 s96, s96, 196608\n"
"s_add_u32 m0, s97, 110592\n"
"s_nop 0\n"
"buffer_load_dwordx4 v67, s[36:39], s96 offen offset:0 lds\n"
"buffer_load_dwordx4 v67, s[40:43], s96 offen offset:1024 lds\n"
"buffer_load_dwordx4 v67, s[44:47], s96 offen offset:2048 lds\n"
"s_add_u32 s96, s96, 196608\n"
"ds_read_b64_tr_b16 a[72:73], v70 offset:24576\n"
"ds_read_b64_tr_b16 a[74:75], v70 offset:25088\n"
"ds_read_b64_tr_b16 a[76:77], v70 offset:25600\n"
"ds_read_b64_tr_b16 a[78:79], v70 offset:26112\n"
"ds_read_b64_tr_b16 a[80:81], v73 offset:26624\n"
"ds_read_b64_tr_b16 a[82:83], v73 offset:27136\n"
"ds_read_b64_tr_b16 a[84:85], v70 offset:36864\n"
"ds_read_b64_tr_b16 a[86:87], v70 offset:37376\n"
"ds_read_b64_tr_b16 a[88:89], v70 offset:37888\n"
"ds_read_b64_tr_b16 a[90:91], v70 offset:38400\n"
"ds_read_b64_tr_b16 a[92:93], v73 offset:38912\n"
"ds_read_b64_tr_b16 a[94:95], v73 offset:39424\n"
"s_waitcnt lgkmcnt(0)\n"
"v_mfma_f32_16x16x32_bf16 v[20:23], a[72:75], v[16:19], v[20:23]\n"
"v_mfma_f32_16x16x32_bf16 v[24:27], a[76:79], v[16:19], v[24:27]\n"
"v_mfma_f32_16x16x32_bf16 v[32:35], v[16:19], a[80:83], v[32:35]\n"
"v_mfma_f32_16x16x32_bf16 v[20:23], a[84:87], v[16:19], v[20:23]\n"
"v_mfma_f32_16x16x32_bf16 v[24:27], a[88:91], v[16:19], v[24:27]\n"
"v_mfma_f32_16x16x32_bf16 v[32:35], v[16:19], a[92:95], v[32:35]\n"
"s_waitcnt vmcnt(25)\n"
"s_barrier\n"
"s_add_u32 m0, s97, 122880\n"
"s_nop 0\n"
"buffer_load_dwordx4 v67, s[36:39], s96 offen offset:0 lds\n"
"buffer_load_dwordx4 v67, s[40:43], s96 offen offset:1024 lds\n"
"buffer_load_dwordx4 v67, s[44:47], s96 offen offset:2048 lds\n"
"s_add_u32 s96, s96, 196608\n"
"s_add_u32 m0, s97, 135168\n"
"s_nop 0\n"
"buffer_load_dwordx4 v67, s[36:39], s96 offen offset:0 lds\n"
"buffer_load_dwordx4 v67, s[40:43], s96 offen offset:1024 lds\n"
"buffer_load_dwordx4 v67, s[44:47], s96 offen offset:2048 lds\n"
"s_add_u32 s96, s96, 196608\n"
"ds_read_b64_tr_b16 a[96:97], v68 offset:0\n"
"ds_read_b64_tr_b16 a[98:99], v68 offset:512\n"
"ds_read_b64_tr_b16 a[100:101], v68 offset:1024\n"
"ds_read_b64_tr_b16 a[102:103], v68 offset:1536\n"
"ds_read_b64_tr_b16 a[104:105], v71 offset:2048\n"
"ds_read_b64_tr_b16 a[106:107], v71 offset:2560\n"
"ds_read_b64_tr_b16 a[108:109], v68 offset:12288\n"
"ds_read_b64_tr_b16 a[110:111], v68 offset:12800\n"
"ds_read_b64_tr_b16 a[112:113], v68 offset:13312\n"
"ds_read_b64_tr_b16 a[114:115], v68 offset:13824\n"
"ds_read_b64_tr_b16 a[116:117], v71 offset:14336\n"
"ds_read_b64_tr_b16 a[118:119], v71 offset:14848\n"
"s_waitcnt lgkmcnt(0)\n"
"v_mfma_f32_16x16x32_bf16 v[20:23], a[96:99], v[16:19], v[20:23]\n"
"v_mfma_f32_16x16x32_bf16 v[24:27], a[100:103], v[16:19], v[24:27]\n"
"v_mfma_f32_16x16x32_bf16 v[32:35], v[16:19], a[104:107], v[32:35]\n"
"v_mfma_f32_16x16x32_bf16 v[20:23], a[108:111], v[16:19], v[20:23]\n"
"v_mfma_f32_16x16x32_bf16 v[24:27], a[112:115], v[16:19], v[24:27]\n"
"v_mfma_f32_16x16x32_bf16 v[32:35], v[16:19], a[116:119], v[32:35]\n"
"s_waitcnt vmcnt(24)\n"
"s_barrier\n"
"s_add_u32 m0, s97, 0\n"
"s_nop 0\n"
"buffer_load_dwordx4 v67, s[36:39], s96 offen offset:0 lds\n"
"buffer_load_dwordx4 v67, s[40:43], s96 offen offset:1024 lds\n"
"buffer_load_dwordx4 v67, s[44:47], s96 offen offset:2048 lds\n"
"s_add_u32 s96, s96, 196608\n"
"s_add_u32 m0, s97, 12288\n"
"s_nop 0\n"
"buffer_load_dwordx4 v67, s[36:39], s96 offen offset:0 lds\n"
"buffer_load_dwordx4 v67, s[40:43], s96 offen offset:1024 lds\n"
"buffer_load_dwordx4 v67, s[44:47], s96 offen offset:2048 lds\n"
"s_add_u32 s96, s96, 196608\n"
"ds_read_b64_tr_b16 a[120:121], v68 offset:24576\n"
"ds_read_b64_tr_b16 a[122:123], v68 offset:25088\n"
"ds_read_b64_tr_b16 a[124:125], v68 offset:25600\n"
"ds_read_b64_tr_b16 a[126:127], v68 offset:26112\n"
"ds_read_b64_tr_b16 a[128:129], v71 offset:26624\n"
"ds_read_b64_tr_b16 a[130:131], v71 offset:27136\n"
"ds_read_b64_tr_b16 a[132:133], v68 offset:36864\n"
"ds_read_b64_tr_b16 a[134:135], v68 offset:37376\n"
"ds_read_b64_tr_b16 a[136:137], v68 offset:37888\n"
"ds_read_b64_tr_b16 a[138:139], v68 offset:38400\n"
"ds_read_b64_tr_b16 a[140:141], v71 offset:38912\n"
"ds_read_b64_tr_b16 a[142:143], v71 offset:39424\n"
"s_waitcnt lgkmcnt(0)\n"
"v_mfma_f32_16x16x32_bf16 v[20:23], a[120:123], v[16:19], v[20:23]\n"
"v_mfma_f32_16x16x32_bf16 v[24:27], a[124:127], v[16:19], v[24:27]\n"
"v_mfma_f32_16x16x32_bf16 v[32:35], v[16:19], a[128:131], v[32:35]\n"
"v_mfma_f32_16x16x32_bf16 v[20:23], a[132:135], v[16:19], v[20:23]\n"
"v_mfma_f32_16x16x32_bf16 v[24:27], a[136:139], v[16:19], v[24:27]\n"
"v_mfma_f32_16x16x32_bf16 v[32:35], v[16:19], a[140:143], v[32:35]\n"
"s_waitcnt vmcnt(24)\n"
"s_barrier\n"
"s_add_u32 m0, s97, 24576\n"
"s_nop 0\n"
"buffer_load_dwordx4 v67, s[36:39], s96 offen offset:0 lds\n"
"buffer_load_dwordx4 v67, s[40:43], s96 offen offset:1024 lds\n"
"buffer_load_dwordx4 v67, s[44:47], s96 offen offset:2048 lds\n"
"s_add_u32 s96, s96, 196608\n"
"s_add_u32 m0, s97, 36864\n"
"s_nop 0\n"
"buffer_load_dwordx4 v67, s[36:39], s96 offen offset:0 lds\n"
"buffer_load_dwordx4 v67, s[40:43], s96 offen offset:1024 lds\n"
"buffer_load_dwordx4 v67, s[44:47], s96 offen offset:2048 lds\n"
"s_add_u32 s96, s96, 196608\n"
"ds_read_b64_tr_b16 a[144:145], v69 offset:0\n"
"ds_read_b64_tr_b16 a[146:147], v69 offset:512\n"
"ds_read_b64_tr_b16 a[148:149], v69 offset:1024\n"
"ds_read_b64_tr_b16 a[150:151], v69 offset:1536\n"
"ds_read_b64_tr_b16 a[152:153], v72 offset:2048\n"
"ds_read_b64_tr_b16 a[154:155], v72 offset:2560\n"
"ds_read_b64_tr_b16 a[156:157], v69 offset:12288\n"
"ds_read_b64_tr_b16 a[158:159], v69 offset:12800\n"
"ds_read_b64_tr_b16 a[160:161], v69 offset:13312\n"
"ds_read_b64_tr_b16 a[162:163], v69 offset:13824\n"
"ds_read_b64_tr_b16 a[164:165], v72 offset:14336\n"
"ds_read_b64_tr_b16 a[166:167], v72 offset:14848\n"
"s_waitcnt lgkmcnt(0)\n"
"v_mfma_f32_16x16x32_bf16 v[20:23], a[144:147], v[16:19], v[20:23]\n"
"v_mfma_f32_16x16x32_bf16 v[24:27], a[148:151], v[16:19], v[24:27]\n"
"v_mfma_f32_16x16x32_bf16 v[32:35], v[16:19], a[152:155], v[32:35]\n"
"v_mfma_f32_16x16x32_bf16 v[20:23], a[156:159], v[16:19], v[20:23]\n"
"v_mfma_f32_16x16x32_bf16 v[24:27], a[160:163], v[16:19], v[24:27]\n"
"v_mfma_f32_16x16x32_bf16 v[32:35], v[16:19], a[164:167], v[32:35]\n"
"s_waitcnt vmcnt(24)\n"
"s_barrier\n"
"s_add_u32 m0, s97, 49152\n"
"s_nop 0\n"
"buffer_load_dwordx4 v67, s[36:39], s96 offen offset:0 lds\n"
"buffer_load_dwordx4 v67, s[40:43], s96 offen offset:1024 lds\n"
"buffer_load_dwordx4 v67, s[44:47], s96 offen offset:2048 lds\n"
"s_add_u32 s96, s96, 196608\n"
"s_add_u32 m0, s97, 61440\n"
"s_nop 0\n"
"buffer_load_dwordx4 v67, s[36:39], s96 offen offset:0 lds\n"
"buffer_load_dwordx4 v67, s[40:43], s96 offen offset:1024 lds\n"
"buffer_load_dwordx4 v67, s[44:47], s96 offen offset:2048 lds\n"
"s_add_u32 s96, s96, 196608\n"
"ds_read_b64_tr_b16 a[168:169], v69 offset:24576\n"
"ds_read_b64_tr_b16 a[170:171], v69 offset:25088\n"
"ds_read_b64_tr_b16 a[172:173], v69 offset:25600\n"
"ds_read_b64_tr_b16 a[174:175], v69 offset:26112\n"
"ds_read_b64_tr_b16 a[176:177], v72 offset:26624\n"
"ds_read_b64_tr_b16 a[178:179], v72 offset:27136\n"
"ds_read_b64_tr_b16 a[180:181], v69 offset:36864\n"
"ds_read_b64_tr_b16 a[182:183], v69 offset:37376\n"
"ds_read_b64_tr_b16 a[184:185], v69 offset:37888\n"
"ds_read_b64_tr_b16 a[186:187], v69 offset:38400\n"
"ds_read_b64_tr_b16 a[188:189], v72 offset:38912\n"
"ds_read_b64_tr_b16 a[190:191], v72 offset:39424\n"
"s_waitcnt lgkmcnt(0)\n"
"v_mfma_f32_16x16x32_bf16 v[20:23], a[168:171], v[16:19], v[20:23]\n"
"v_mfma_f32_16x16x32_bf16 v[24:27], a[172:175], v[16:19], v[24:27]\n"
"v_mfma_f32_16x16x32_bf16 v[32:35], v[16:19], a[176:179], v[32:35]\n"
"v_mfma_f32_16x16x32_bf16 v[20:23], a[180:183], v[16:19], v[20:23]\n"
"v_mfma_f32_16x16x32_bf16 v[24:27], a[184:187], v[16:19], v[24:27]\n"
"v_mfma_f32_16x16x32_bf16 v[32:35], v[16:19], a[188:191], v[32:35]\n"
"s_waitcnt vmcnt(24)\n"
"s_barrier\n"
"s_add_u32 m0, s97, 73728\n"
"s_nop 0\n"
"buffer_load_dwordx4 v67, s[36:39], s96 offen offset:0 lds\n"
"buffer_load_dwordx4 v67, s[40:43], s96 offen offset:1024 lds\n"
"buffer_load_dwordx4 v67, s[44:47], s96 offen offset:2048 lds\n"
"s_add_u32 s96, s96, 196608\n"
"s_add_u32 m0, s97, 86016\n"
"s_nop 0\n"
"buffer_load_dwordx4 v67, s[36:39], s96 offen offset:0 lds\n"
"buffer_load_dwordx4 v67, s[40:43], s96 offen offset:1024 lds\n"
"buffer_load_dwordx4 v67, s[44:47], s96 offen offset:2048 lds\n"
"s_add_u32 s96, s96, 196608\n"
"ds_read_b64_tr_b16 a[192:193], v70 offset:0\n"
"ds_read_b64_tr_b16 a[194:195], v70 offset:512\n"
"ds_read_b64_tr_b16 a[196:197], v70 offset:1024\n"
"ds_read_b64_tr_b16 a[198:199], v70 offset:1536\n"
"ds_read_b64_tr_b16 a[200:201], v73 offset:2048\n"
"ds_read_b64_tr_b16 a[202:203], v73 offset:2560\n"
"ds_read_b64_tr_b16 a[204:205], v70 offset:12288\n"
"ds_read_b64_tr_b16 a[206:207], v70 offset:12800\n"
"ds_read_b64_tr_b16 a[208:209], v70 offset:13312\n"
"ds_read_b64_tr_b16 a[210:211], v70 offset:13824\n"
"ds_read_b64_tr_b16 a[212:213], v73 offset:14336\n"
"ds_read_b64_tr_b16 a[214:215], v73 offset:14848\n"
"s_waitcnt lgkmcnt(0)\n"
"v_mfma_f32_16x16x32_bf16 v[20:23], a[192:195], v[16:19], v[20:23]\n"
"v_mfma_f32_16x16x32_bf16 v[24:27], a[196:199], v[16:19], v[24:27]\n"
"v_mfma_f32_16x16x32_bf16 v[32:35], v[16:19], a[200:203], v[32:35]\n"
"v_mfma_f32_16x16x32_bf16 v[20:23], a[204:207], v[16:19], v[20:23]\n"
"v_mfma_f32_16x16x32_bf16 v[24:27], a[208:211], v[16:19], v[24:27]\n"
"v_mfma_f32_16x16x32_bf16 v[32:35], v[16:19], a[212:215], v[32:35]\n"
"s_waitcnt vmcnt(24)\n"
"s_barrier\n"
"s_add_u32 m0, s97, 98304\n"
"s_nop 0\n"
"buffer_load_dwordx4 v67, s[36:39], s96 offen offset:0 lds\n"
"buffer_load_dwordx4 v67, s[40:43], s96 offen offset:1024 lds\n"
"buffer_load_dwordx4 v67, s[44:47], s96 offen offset:2048 lds\n"
"s_add_u32 s96, s96, 196608\n"
"s_add_u32 m0, s97, 110592\n"
"s_nop 0\n"
"buffer_load_dwordx4 v67, s[36:39], s96 offen offset:0 lds\n"
"buffer_load_dwordx4 v67, s[40:43], s96 offen offset:1024 lds\n"
"buffer_load_dwordx4 v67, s[44:47], s96 offen offset:2048 lds\n"
"s_add_u32 s96, s96, 196608\n"
"buffer_load_dwordx2 v[76:77], v0, s[32:35], s66 offen sc1\n"
"buffer_load_dwordx2 v[78:79], v0, s[32:35], s74 offen sc1\n"
"ds_read_b64_tr_b16 a[216:217], v70 offset:24576\n"
"ds_read_b64_tr_b16 a[218:219], v70 offset:25088\n"
"ds_read_b64_tr_b16 a[220:221], v70 offset:25600\n"
"ds_read_b64_tr_b16 a[222:223], v70 offset:26112\n"
"ds_read_b64_tr_b16 a[224:225], v73 offset:26624\n"
"ds_read_b64_tr_b16 a[226:227], v73 offset:27136\n"
"ds_read_b64_tr_b16 a[228:229], v70 offset:36864\n"
"ds_read_b64_tr_b16 a[230:231], v70 offset:37376\n"
"ds_read_b64_tr_b16 a[232:233], v70 offset:37888\n"
"ds_read_b64_tr_b16 a[234:235], v70 offset:38400\n"
"ds_read_b64_tr_b16 a[236:237], v73 offset:38912\n"
"ds_read_b64_tr_b16 a[238:239], v73 offset:39424\n"
"s_waitcnt lgkmcnt(0)\n"
"v_mfma_f32_16x16x32_bf16 v[20:23], a[216:219], v[16:19], v[20:23]\n"
"v_mfma_f32_16x16x32_bf16 v[24:27], a[220:223], v[16:19], v[24:27]\n"
"v_mfma_f32_16x16x32_bf16 v[32:35], v[16:19], a[224:227], v[32:35]\n"
"v_mfma_f32_16x16x32_bf16 v[20:23], a[228:231], v[16:19], v[20:23]\n"
"v_mfma_f32_16x16x32_bf16 v[24:27], a[232:235], v[16:19], v[24:27]\n"
"v_mfma_f32_16x16x32_bf16 v[32:35], v[16:19], a[236:239], v[32:35]\n"
"s_waitcnt vmcnt(26)\n"
"s_barrier\n"
"s_add_u32 m0, s97, 122880\n"
"s_nop 0\n"
"buffer_load_dwordx4 v67, s[36:39], s96 offen offset:0 lds\n"
"buffer_load_dwordx4 v67, s[40:43], s96 offen offset:1024 lds\n"
"buffer_load_dwordx4 v67, s[44:47], s96 offen offset:2048 lds\n"
"s_add_u32 s96, s96, 196608\n"
"s_add_u32 m0, s97, 135168\n"
"s_nop 0\n"
"buffer_load_dwordx4 v67, s[36:39], s96 offen offset:0 lds\n"
"buffer_load_dwordx4 v67, s[40:43], s96 offen offset:1024 lds\n"
"buffer_load_dwordx4 v67, s[44:47], s96 offen offset:2048 lds\n"
"s_add_u32 s96, s96, 196608\n"
"ds_read_b64_tr_b16 a[240:241], v68 offset:0\n"
"ds_read_b64_tr_b16 a[242:243], v68 offset:512\n"
"ds_read_b64_tr_b16 a[244:245], v68 offset:1024\n"
"ds_read_b64_tr_b16 a[246:247], v68 offset:1536\n"
"ds_read_b64_tr_b16 a[248:249], v71 offset:2048\n"
"ds_read_b64_tr_b16 a[250:251], v71 offset:2560\n"
"ds_read_b64_tr_b16 a[252:253], v68 offset:12288\n"
"ds_read_b64_tr_b16 a[254:255], v68 offset:12800\n"
"ds_read_b64_tr_b16 v[128:129], v68 offset:13312\n"
"ds_read_b64_tr_b16 v[130:131], v68 offset:13824\n"
"ds_read_b64_tr_b16 v[132:133], v71 offset:14336\n"
"ds_read_b64_tr_b16 v[134:135], v71 offset:14848\n"
"s_waitcnt lgkmcnt(0)\n"
"v_mfma_f32_16x16x32_bf16 v[20:23], a[240:243], v[16:19], v[20:23]\n"
"v_mfma_f32_16x16x32_bf16 v[24:27], a[244:247], v[16:19], v[24:27]\n"
"v_mfma_f32_16x16x32_bf16 v[32:35], v[16:19], a[248:251], v[32:35]\n"
"v_mfma_f32_16x16x32_bf16 v[20:23], a[252:255], v[16:19], v[20:23]\n"
"v_mfma_f32_16x16x32_bf16 v[24:27], v[128:131], v[16:19], v[24:27]\n"
"v_mfma_f32_16x16x32_bf16 v[32:35], v[16:19], v[132:135], v[32:35]\n"
"s_waitcnt vmcnt(26)\n"
"s_barrier\n"
"ds_read_b64_tr_b16 v[136:137], v68 offset:24576\n"
"ds_read_b64_tr_b16 v[138:139], v68 offset:25088\n"
"ds_read_b64_tr_b16 v[140:141], v68 offset:25600\n"
"ds_read_b64_tr_b16 v[142:143], v68 offset:26112\n"
"ds_read_b64_tr_b16 v[144:145], v71 offset:26624\n"
"ds_read_b64_tr_b16 v[146:147], v71 offset:27136\n"
"ds_read_b64_tr_b16 v[148:149], v68 offset:36864\n"
"ds_read_b64_tr_b16 v[150:151], v68 offset:37376\n"
"ds_read_b64_tr_b16 v[152:153], v68 offset:37888\n"
"ds_read_b64_tr_b16 v[154:155], v68 offset:38400\n"
"ds_read_b64_tr_b16 v[156:157], v71 offset:38912\n"
"ds_read_b64_tr_b16 v[158:159], v71 offset:39424\n"
"s_waitcnt lgkmcnt(0)\n"
"v_mfma_f32_16x16x32_bf16 v[20:23], v[136:139], v[16:19], v[20:23]\n"
"v_mfma_f32_16x16x32_bf16 v[24:27], v[140:143], v[16:19], v[24:27]\n"
"v_mfma_f32_16x16x32_bf16 v[32:35], v[16:19], v[144:147], v[32:35]\n"
"v_mfma_f32_16x16x32_bf16 v[20:23], v[148:151], v[16:19], v[20:23]\n"
"v_mfma_f32_16x16x32_bf16 v[24:27], v[152:155], v[16:19], v[24:27]\n"
"v_mfma_f32_16x16x32_bf16 v[32:35], v[16:19], v[156:159], v[32:35]\n"
"s_waitcnt vmcnt(20)\n"
"s_barrier\n"
"ds_read_b64_tr_b16 v[160:161], v69 offset:0\n"
"ds_read_b64_tr_b16 v[162:163], v69 offset:512\n"
"ds_read_b64_tr_b16 v[164:165], v69 offset:1024\n"
"ds_read_b64_tr_b16 v[166:167], v69 offset:1536\n"
"ds_read_b64_tr_b16 v[168:169], v72 offset:2048\n"
"ds_read_b64_tr_b16 v[170:171], v72 offset:2560\n"
"ds_read_b64_tr_b16 v[172:173], v69 offset:12288\n"
"ds_read_b64_tr_b16 v[174:175], v69 offset:12800\n"
"ds_read_b64_tr_b16 v[176:177], v69 offset:13312\n"
"ds_read_b64_tr_b16 v[178:179], v69 offset:13824\n"
"ds_read_b64_tr_b16 v[180:181], v72 offset:14336\n"
"ds_read_b64_tr_b16 v[182:183], v72 offset:14848\n"
"s_waitcnt lgkmcnt(0)\n"
"v_mfma_f32_16x16x32_bf16 v[20:23], v[160:163], v[16:19], v[20:23]\n"
"v_mfma_f32_16x16x32_bf16 v[24:27], v[164:167], v[16:19], v[24:27]\n"
"v_mfma_f32_16x16x32_bf16 v[32:35], v[16:19], v[168:171], v[32:35]\n"
"v_mfma_f32_16x16x32_bf16 v[20:23], v[172:175], v[16:19], v[20:23]\n"
"v_mfma_f32_16x16x32_bf16 v[24:27], v[176:179], v[16:19], v[24:27]\n"
"v_mfma_f32_16x16x32_bf16 v[32:35], v[16:19], v[180:183], v[32:35]\n"
"s_waitcnt vmcnt(14)\n"
"s_barrier\n"
"ds_read_b64_tr_b16 v[184:185], v69 offset:24576\n"
"ds_read_b64_tr_b16 v[186:187], v69 offset:25088\n"
"ds_read_b64_tr_b16 v[188:189], v69 offset:25600\n"
"ds_read_b64_tr_b16 v[190:191], v69 offset:26112\n"
"ds_read_b64_tr_b16 v[192:193], v72 offset:26624\n"
"ds_read_b64_tr_b16 v[194:195], v72 offset:27136\n"
"ds_read_b64_tr_b16 v[196:197], v69 offset:36864\n"
"ds_read_b64_tr_b16 v[198:199], v69 offset:37376\n"
"ds_read_b64_tr_b16 v[200:201], v69 offset:37888\n"
"ds_read_b64_tr_b16 v[202:203], v69 offset:38400\n"
"ds_read_b64_tr_b16 v[204:205], v72 offset:38912\n"
"ds_read_b64_tr_b16 v[206:207], v72 offset:39424\n"
"s_waitcnt lgkmcnt(0)\n"
"v_mfma_f32_16x16x32_bf16 v[20:23], v[184:187], v[16:19], v[20:23]\n"
"v_mfma_f32_16x16x32_bf16 v[24:27], v[188:191], v[16:19], v[24:27]\n"
"v_mfma_f32_16x16x32_bf16 v[32:35], v[16:19], v[192:195], v[32:35]\n"
"v_mfma_f32_16x16x32_bf16 v[20:23], v[196:199], v[16:19], v[20:23]\n"
"v_mfma_f32_16x16x32_bf16 v[24:27], v[200:203], v[16:19], v[24:27]\n"
"v_mfma_f32_16x16x32_bf16 v[32:35], v[16:19], v[204:207], v[32:35]\n"
"s_waitcnt vmcnt(8)\n"
"s_barrier\n"
"s_waitcnt vmcnt(7)\n"
"v_cmp_ne_u64 vcc, v[30:31], v[76:77]\n"
"s_cbranch_vccnz .L_rtqa\n"
".L_bkqa:\n"
"s_add_u32 s63, s66, s71\n"
"s_add_u32 s66, s66, s72\n"
"s_add_u32 m0, s64, 0\n"
"s_nop 0\n"
"buffer_load_dwordx4 v2, s[32:35], s63 offen offset:0 sc1 lds\n"
"buffer_load_dwordx4 v2, s[32:35], s63 offen offset:1024 sc1 lds\n"
"buffer_load_dwordx4 v2, s[32:35], s63 offen offset:2048 sc1 lds\n"
"buffer_load_dwordx4 v2, s[32:35], s63 offen offset:3072 sc1 lds\n"
"s_add_u32 s63, s63, 4096\n"
"s_add_u32 m0, s64, 4096\n"
"s_nop 0\n"
"buffer_load_dwordx4 v2, s[32:35], s63 offen offset:0 sc1 lds\n"
"buffer_load_dwordx4 v2, s[32:35], s63 offen offset:1024 sc1 lds\n"
"buffer_load_dwordx4 v2, s[32:35], s63 offen offset:2048 sc1 lds\n"
"buffer_load_dwordx4 v2, s[32:35], s63 offen offset:3072 sc1 lds\n"
"s_add_u32 s63, s63, 4096\n"
"s_add_u32 m0, s64, 8192\n"
"s_nop 0\n"
"buffer_load_dwordx4 v2, s[32:35], s63 offen offset:0 sc1 lds\n"
"s_mov_b32 s72, 1187840\n"
"s_waitcnt vmcnt(15)\n"
"v_cmp_ne_u64 vcc, v[30:31], v[78:79]\n"
"s_cbranch_vccnz .L_rtqb\n"
".L_bkqb:\n"
"s_add_u32 s63, s66, s71\n"
"s_add_u32 s66, s66, s72\n"
"s_add_u32 m0, s64, 36864\n"
"s_nop 0\n"
"buffer_load_dwordx4 v2, s[32:35], s63 offen offset:0 sc1 lds\n"
"buffer_load_dwordx4 v2, s[32:35], s63 offen offset:1024 sc1 lds\n"
"buffer_load_dwordx4 v2, s[32:35], s63 offen offset:2048 sc1 lds\n"
"buffer_load_dwordx4 v2, s[32:35], s63 offen offset:3072 sc1 lds\n"
"s_add_u32 s63, s63, 4096\n"
"s_add_u32 m0, s64, 40960\n"
"s_nop 0\n"
"buffer_load_dwordx4 v2, s[32:35], s63 offen offset:0 sc1 lds\n"
"buffer_load_dwordx4 v2, s[32:35], s63 offen offset:1024 sc1 lds\n"
"buffer_load_dwordx4 v2, s[32:35], s63 offen offset:2048 sc1 lds\n"
"buffer_load_dwordx4 v2, s[32:35], s63 offen offset:3072 sc1 lds\n"
"s_add_u32 s63, s63, 4096\n"
"s_add_u32 m0, s64, 45056\n"
"s_nop 0\n"
"buffer_load_dwordx4 v2, s[32:35], s63 offen offset:0 sc1 lds\n"
"s_xor_b32 s64, s64, s65\n"
"buffer_load_dwordx2 v[12:13], v0, s[32:35], s66 offen sc1\n"
"ds_read_b64_tr_b16 v[208:209], v70 offset:0\n"
"ds_read_b64_tr_b16 v[210:211], v70 offset:512\n"
"ds_read_b64_tr_b16 v[212:213], v70 offset:1024\n"
"ds_read_b64_tr_b16 v[214:215], v70 offset:1536\n"
"ds_read_b64_tr_b16 v[216:217], v73 offset:2048\n"
"ds_read_b64_tr_b16 v[218:219], v73 offset:2560\n"
"ds_read_b64_tr_b16 v[220:221], v70 offset:12288\n"
"ds_read_b64_tr_b16 v[222:223], v70 offset:12800\n"
"ds_read_b64_tr_b16 v[224:225], v70 offset:13312\n"
"ds_read_b64_tr_b16 v[226:227], v70 offset:13824\n"
"ds_read_b64_tr_b16 v[228:229], v73 offset:14336\n"
"ds_read_b64_tr_b16 v[230:231], v73 offset:14848\n"
"s_waitcnt lgkmcnt(0)\n"
"v_mfma_f32_16x16x32_bf16 v[20:23], v[208:211], v[16:19], v[20:23]\n"
"v_mfma_f32_16x16x32_bf16 v[24:27], v[212:215], v[16:19], v[24:27]\n"
"v_mfma_f32_16x16x32_bf16 v[32:35], v[16:19], v[216:219], v[32:35]\n"
"v_mfma_f32_16x16x32_bf16 v[20:23], v[220:223], v[16:19], v[20:23]\n"
"v_mfma_f32_16x16x32_bf16 v[24:27], v[224:227], v[16:19], v[24:27]\n"
"v_mfma_f32_16x16x32_bf16 v[32:35], v[16:19], v[228:231], v[32:35]\n"
"s_waitcnt vmcnt(19)\n"
"s_barrier\n"
"ds_read_b64_tr_b16 v[232:233], v70 offset:24576\n"
"ds_read_b64_tr_b16 v[234:235], v70 offset:25088\n"
"ds_read_b64_tr_b16 v[236:237], v70 offset:25600\n"
"ds_read_b64_tr_b16 v[238:239], v70 offset:26112\n"
"ds_read_b64_tr_b16 v[240:241], v73 offset:26624\n"
"ds_read_b64_tr_b16 v[242:243], v73 offset:27136\n"
"ds_read_b64_tr_b16 v[244:245], v70 offset:36864\n"
"ds_read_b64_tr_b16 v[246:247], v70 offset:37376\n"
"ds_read_b64_tr_b16 v[248:249], v70 offset:37888\n"
"ds_read_b64_tr_b16 v[250:251], v70 offset:38400\n"
"ds_read_b64_tr_b16 v[252:253], v73 offset:38912\n"
"ds_read_b64_tr_b16 v[254:255], v73 offset:39424\n"
"s_waitcnt lgkmcnt(0)\n"
"v_mfma_f32_16x16x32_bf16 v[20:23], v[232:235], v[16:19], v[20:23]\n"
"v_mfma_f32_16x16x32_bf16 v[24:27], v[236:239], v[16:19], v[24:27]\n"
"v_mfma_f32_16x16x32_bf16 v[32:35], v[16:19], v[240:243], v[32:35]\n"
"v_mfma_f32_16x16x32_bf16 v[20:23], v[244:247], v[16:19], v[20:23]\n"
"v_mfma_f32_16x16x32_bf16 v[24:27], v[248:251], v[16:19], v[24:27]\n"
"v_mfma_f32_16x16x32_bf16 v[32:35], v[16:19], v[252:255], v[32:35]\n"
"s_nop 7\n"
"v_mov_b32 v28, v32\n"
"s_waitcnt vmcnt(0)\n"
"s_barrier\n"
"ds_read_b128 v[56:59], v4 offset:0\n"
"ds_read_b128 v[60:63], v4 offset:1152\n"
"ds_read_b128 v[64:67], v4 offset:2304\n"
"ds_read_b128 v[68:71], v4 offset:3456\n"
"ds_read_b128 v[72:75], v4 offset:4608\n"
"ds_read_b128 v[76:79], v4 offset:5760\n"
".p2align 6\n"
".L_ql:\n"
"s_bitcmp1_b64 s[58:59], s62\n"
"s_cbranch_scc1 .L_qs0\n"
".L_qb0:\n"
"s_waitcnt lgkmcnt(2)\n"
"v_mfma_f32_16x16x32_bf16 v[32:35], a[0:3], v[56:59], 0\n"
"ds_read_b128 v[80:83], v4 offset:6912\n"
"ds_read2_b32 v[14:15], v9 offset0:32 offset1:48\n"
"v_mfma_f32_16x16x32_bf16 v[32:35], a[12:15], v[60:63], v[32:35]\n"
"ds_read_b128 v[92:95], v10 offset:128\n"
"ds_read_b128 v[96:99], v10 offset:192\n"
"v_mfma_f32_16x16x32_bf16 v[36:39], a[16:19], v[60:63], 0\n"
"s_add_u32 s68, s68, 1187840\n"
"v_mfma_f32_16x16x32_bf16 v[36:39], a[4:7], v[56:59], v[36:39]\n"
"ds_read_b128 v[84:87], v4 offset:8064\n"
"v_mfma_f32_16x16x32_bf16 v[40:43], v[56:59], a[8:11], 0\n"
"ds_read2st64_b32 v[88:89], v6 offset0:0 offset1:36\n"
"v_mfma_f32_16x16x32_bf16 v[40:43], v[60:63], a[20:23], v[40:43]\n"
"s_waitcnt vmcnt(5)\n"
"v_cmp_ne_u64 vcc, v[30:31], v[12:13]\n"
"v_mfma_f32_16x16x32_bf16 v[40:43], v[64:67], a[32:35], v[40:43]\n"
"ds_read_b128 v[56:59], v4 offset:9216\n"
"ds_read2st64_b32 v[90:91], v6 offset0:72 offset1:108\n"
"v_mfma_f32_16x16x32_bf16 v[40:43], v[68:71], a[44:47], v[40:43]\n"
"s_cbranch_vccnz .L_rtq0\n"
".L_bkq0:\n"
"s_add_u32 s63, s66, s71\n"
"v_mfma_f32_16x16x32_bf16 v[36:39], a[40:43], v[68:71], v[36:39]\n"
"s_add_u32 s66, s66, s72\n"
"s_add_u32 m0, s64, 0\n"
"v_mfma_f32_16x16x32_bf16 v[36:39], a[28:31], v[64:67], v[36:39]\n"
"ds_read_b128 v[60:63], v4 offset:10368\n"
"buffer_load_dwordx4 v2, s[32:35], s63 offen offset:0 sc1 lds\n"
"v_mfma_f32_16x16x32_bf16 v[32:35], a[24:27], v[64:67], v[32:35]\n"
"s_waitcnt lgkmcnt(5)\n"
"v_mul_f32 v29, 0x3e8293ee, v15\n"
"v_mfma_f32_16x16x32_bf16 v[32:35], a[36:39], v[68:71], v[32:35]\n"
"v_fma_f32 v48, -v14, v24, v48\n"
"s_waitcnt lgkmcnt(4)\n"
"v_mfma_f32_16x16x32_bf16 v[32:35], a[48:51], v[72:75], v[32:35]\n"
"ds_read_b128 v[64:67], v4 offset:11520\n"
"v_fma_f32 v49, -v14, v25, v49\n"
"v_mfma_f32_16x16x32_bf16 v[32:35], a[60:63], v[76:79], v[32:35]\n"
"v_fma_f32 v50, -v14, v26, v50\n"
"v_fma_f32 v51, -v14, v27, v51\n"
"v_mfma_f32_16x16x32_bf16 v[36:39], a[64:67], v[76:79], v[36:39]\n"
"v_fma_f32 v44, -v14, v20, v44\n"
"v_fma_f32 v45, -v14, v21, v45\n"
"v_mfma_f32_16x16x32_bf16 v[36:39], a[52:55], v[72:75], v[36:39]\n"
"ds_read_b128 v[68:71], v4 offset:12672\n"
"v_fma_f32 v46, -v14, v22, v46\n"
"v_mfma_f32_16x16x32_bf16 v[40:43], v[72:75], a[56:59], v[40:43]\n"
"buffer_load_dwordx4 v2, s[32:35], s63 offen offset:1024 sc1 lds\n"
"v_fma_f32 v47, -v14, v23, v47\n"
"v_mfma_f32_16x16x32_bf16 v[40:43], v[76:79], a[68:71], v[40:43]\n"
"v_mul_f32 v48, v48, v15\n"
"v_mul_f32 v49, v49, v15\n"
"v_mfma_f32_16x16x32_bf16 v[40:43], v[80:83], a[80:83], v[40:43]\n"
"ds_read_b128 v[72:75], v4 offset:13824\n"
"v_mul_f32 v50, v50, v15\n"
"v_mfma_f32_16x16x32_bf16 v[40:43], v[84:87], a[92:95], v[40:43]\n"
"v_mul_f32 v51, v51, v15\n"
"v_mul_f32 v44, v44, v29\n"
"v_mfma_f32_16x16x32_bf16 v[36:39], a[88:91], v[84:87], v[36:39]\n"
"v_mul_f32 v45, v45, v29\n"
"v_mul_f32 v46, v46, v29\n"
"v_mfma_f32_16x16x32_bf16 v[36:39], a[76:79], v[80:83], v[36:39]\n"
"ds_read_b128 v[76:79], v4 offset:14976\n"
"v_mul_f32 v47, v47, v29\n"
"v_mfma_f32_16x16x32_bf16 v[32:35], a[72:75], v[80:83], v[32:35]\n"
"v_cvt_pk_bf16_f32 v108, v48, v49\n"
"v_cvt_pk_bf16_f32 v109, v50, v51\n"
"v_mfma_f32_16x16x32_bf16 v[32:35], a[84:87], v[84:87], v[32:35]\n"
"v_cvt_pk_bf16_f32 v100, v44, v45\n"
"s_waitcnt lgkmcnt(2)\n"
"v_mfma_f32_16x16x32_bf16 v[32:35], a[96:99], v[56:59], v[32:35]\n"
"ds_read_b128 v[80:83], v4 offset:16128\n"
"buffer_load_dwordx4 v2, s[32:35], s63 offen offset:2048 sc1 lds\n"
"v_mfma_f32_16x16x32_bf16 v[32:35], a[108:111], v[60:63], v[32:35]\n"
"v_cvt_pk_bf16_f32 v101, v46, v47\n"
"v_mfma_f32_16x16x32_bf16 v[36:39], a[112:115], v[60:63], v[36:39]\n"
"ds_write2st64_b64 v11, v[100:101], v[108:109] offset0:0 offset1:1\n"
"v_fma_f32 v52, -v92, v28, v52\n"
"v_mfma_f32_16x16x32_bf16 v[36:39], a[100:103], v[56:59], v[36:39]\n"
"ds_read_b128 v[84:87], v4 offset:17280\n"
"v_fma_f32 v53, -v93, v28, v53\n"
"v_mfma_f32_16x16x32_bf16 v[40:43], v[56:59], a[104:107], v[40:43]\n"
"v_fma_f32 v54, -v94, v28, v54\n"
"v_fma_f32 v55, -v95, v28, v55\n"
"v_mfma_f32_16x16x32_bf16 v[40:43], v[60:63], a[116:119], v[40:43]\n"
"v_mul_f32 v52, v52, v96\n"
"v_mul_f32 v53, v53, v97\n"
"v_mfma_f32_16x16x32_bf16 v[40:43], v[64:67], a[128:131], v[40:43]\n"
"ds_read_b128 v[56:59], v4 offset:18432\n"
"v_mul_f32 v54, v54, v98\n"
"v_mfma_f32_16x16x32_bf16 v[40:43], v[68:71], a[140:143], v[40:43]\n"
"v_mul_f32 v55, v55, v99\n"
"s_waitcnt lgkmcnt(2)\n"
"v_mfma_f32_16x16x32_bf16 v[36:39], a[136:139], v[68:71], v[36:39]\n"
"buffer_load_dwordx4 v2, s[32:35], s63 offen offset:3072 sc1 lds\n"
"s_barrier\n"
"v_mfma_f32_16x16x32_bf16 v[36:39], a[124:127], v[64:67], v[36:39]\n"
"ds_read_b128 v[60:63], v4 offset:19584\n"
"ds_read_b64 v[102:103], v3\n"
"v_mfma_f32_16x16x32_bf16 v[32:35], a[120:123], v[64:67], v[32:35]\n"
"ds_read_b64 v[110:111], v3 offset:512\n"
"v_cvt_pk_bf16_f32 v114, v52, v53\n"
"v_mfma_f32_16x16x32_bf16 v[32:35], a[132:135], v[68:71], v[32:35]\n"
"v_cvt_pk_bf16_f32 v115, v54, v55\n"
"s_waitcnt lgkmcnt(4)\n"
"v_mfma_f32_16x16x32_bf16 v[32:35], a[144:147], v[72:75], v[32:35]\n"
"ds_read_b128 v[64:67], v4 offset:20736\n"
"v_add_f32 v88, v88, v89\n"
"v_mfma_f32_16x16x32_bf16 v[32:35], a[156:159], v[76:79], v[32:35]\n"
"v_add_f32 v90, v90, v91\n"
"v_mfma_f32_16x16x32_bf16 v[36:39], a[160:163], v[76:79], v[36:39]\n"
"v_add_f32 v88, v88, v90\n"
"v_mfma_f32_16x16x32_bf16 v[36:39], a[148:151], v[72:75], v[36:39]\n"
"ds_read_b128 v[68:71], v4 offset:21888\n"
"s_add_u32 s63, s63, 4096\n"
"v_mfma_f32_16x16x32_bf16 v[40:43], v[72:75], a[152:155], v[40:43]\n"
"s_add_u32 m0, s64, 4096\n"
"s_waitcnt lgkmcnt(2)\n"
"v_mfma_f32_16x16x32_bf16 v[48:51], v[104:107], v[100:103], v[120:123]\n"
"buffer_load_dwordx4 v2, s[32:35], s63 offen offset:0 sc1 lds\n"
"v_add_f32_dpp v88, v88, v88 row_ror:8 row_mask:0xf bank_mask:0xf\n"
"v_mfma_f32_16x16x32_bf16 v[44:47], v[108:111], v[100:103], v[116:119]\n"
"v_mfma_f32_16x16x32_bf16 v[40:43], v[76:79], a[164:167], v[40:43]\n"
"v_add_f32_dpp v88, v88, v88 row_ror:4 row_mask:0xf bank_mask:0xf\n"
"v_mfma_f32_16x16x32_bf16 v[40:43], v[80:83], a[176:179], v[40:43]\n"
"ds_read_b128 v[72:75], v4 offset:23040\n"
"v_add_f32_dpp v88, v88, v88 row_ror:2 row_mask:0xf bank_mask:0xf\n"
"v_mfma_f32_16x16x32_bf16 v[40:43], v[84:87], a[188:191], v[40:43]\n"
"v_mul_f32 v88, 0x3a800000, v88\n"
"v_mfma_f32_16x16x32_bf16 v[36:39], a[184:187], v[84:87], v[36:39]\n"
"v_max3_f32 v44, v44, v45, v48\n"
"v_max3_f32 v46, v46, v47, v50\n"
"v_mfma_f32_16x16x32_bf16 v[36:39], a[172:175], v[80:83], v[36:39]\n"
"ds_read_b128 v[76:79], v4 offset:24192\n"
"v_max_f32 v44, v44, v49\n"
"v_mfma_f32_16x16x32_bf16 v[32:35], a[168:171], v[80:83], v[32:35]\n"
"v_max_f32 v46, v46, v51\n"
"v_mul_f32 v89, v88, v88\n"
"v_mfma_f32_16x16x32_bf16 v[32:35], a[180:183], v[84:87], v[32:35]\n"
"v_max_f32 v96, v44, v46\n"
"s_waitcnt lgkmcnt(2)\n"
"v_mfma_f32_16x16x32_bf16 v[32:35], a[192:195], v[56:59], v[32:35]\n"
"ds_read_b128 v[80:83], v4 offset:25344\n"
"buffer_load_dwordx4 v2, s[32:35], s63 offen offset:1024 sc1 lds\n"
"v_mfma_f32_16x16x32_bf16 v[32:35], a[204:207], v[60:63], v[32:35]\n"
"v_max_f32 v97, v44, v46\n"
"v_sub_f32_dpp v89, v88, v89 quad_perm:[1,1,3,3] row_mask:0xf bank_mask:0xf\n"
"v_mfma_f32_16x16x32_bf16 v[36:39], a[208:211], v[60:63], v[36:39]\n"
"buffer_load_dwordx2 v[12:13], v0, s[32:35], s66 offen sc1\n"
"v_max_f32 v89, 0, v89\n"
"v_mfma_f32_16x16x32_bf16 v[36:39], a[196:199], v[56:59], v[36:39]\n"
"ds_read_b128 v[84:87], v4 offset:26496\n"
"v_cvt_pk_bf16_f32 v89, 0, v89\n"
"v_mfma_f32_16x16x32_bf16 v[40:43], v[56:59], a[200:203], v[40:43]\n"
"v_permlane16_swap_b32 v96, v97\n"
"v_mfma_f32_16x16x32_bf16 v[40:43], v[60:63], a[212:215], v[40:43]\n"
"v_max_f32 v98, v96, v97\n"
"v_max_f32 v99, v96, v97\n"
"v_mfma_f32_16x16x32_bf16 v[40:43], v[64:67], a[224:227], v[40:43]\n"
"ds_read_b128 v[56:59], v4 offset:27648\n"
"v_add_f32 v89, 0x3727c5ac, v89\n"
"v_mfma_f32_16x16x32_bf16 v[40:43], v[68:71], a[236:239], v[40:43]\n"
"v_permlane32_swap_b32 v98, v99\n"
"v_mfma_f32_16x16x32_bf16 v[36:39], a[232:235], v[68:71], v[36:39]\n"
"buffer_load_dwordx4 v2, s[32:35], s63 offen offset:2048 sc1 lds\n"
"v_max_f32 v96, v98, v99\n"
"v_mfma_f32_16x16x32_bf16 v[36:39], a[220:223], v[64:67], v[36:39]\n"
"ds_read_b128 v[60:63], v4 offset:28800\n"
"v_sub_f32 v44, v44, v96\n"
"v_mfma_f32_16x16x32_bf16 v[32:35], a[216:219], v[64:67], v[32:35]\n"
"v_exp_f32 v44, v44\n"
"v_mfma_f32_16x16x32_bf16 v[32:35], a[228:231], v[68:71], v[32:35]\n"
"v_sub_f32 v46, v46, v96\n"
"s_waitcnt lgkmcnt(2)\n"
"v_mfma_f32_16x16x32_bf16 v[32:35], a[240:243], v[72:75], v[32:35]\n"
"ds_read_b128 v[64:67], v4 offset:29952\n"
"v_cvt_pk_bf16_f32 v44, v44, v44\n"
"v_mfma_f32_16x16x32_bf16 v[32:35], a[252:255], v[76:79], v[32:35]\n"
"v_exp_f32 v46, v46\n"
"v_mfma_f32_16x16x32_bf16 v[36:39], v[128:131], v[76:79], v[36:39]\n"
"v_cvt_pk_bf16_f32 v46, v46, v46\n"
"v_and_b32 v94, v44, v124\n"
"v_mfma_f32_16x16x32_bf16 v[36:39], a[244:247], v[72:75], v[36:39]\n"
"ds_read_b128 v[68:71], v4 offset:31104\n"
"v_and_b32 v92, v44, v126\n"
"v_mfma_f32_16x16x32_bf16 v[40:43], v[72:75], a[248:251], v[40:43]\n"
"buffer_load_dwordx4 v2, s[32:35], s63 offen offset:3072 sc1 lds\n"
"v_and_b32 v95, v46, v125\n"
"v_mfma_f32_16x16x32_bf16 v[40:43], v[76:79], v[132:135], v[40:43]\n"
"v_and_b32 v93, v46, v127\n"
"v_cvt_pk_bf16_f32 v89, 0, v89\n"
"v_mfma_f32_16x16x32_bf16 v[40:43], v[80:83], v[144:147], v[40:43]\n"
"ds_read_b128 v[72:75], v4 offset:32256\n"
"v_cvt_pk_bf16_f32 v88, 0, v88\n"
"v_mfma_f32_16x16x32_bf16 v[48:51], v[16:19], v[92:95], 0\n"
"v_sqrt_f32 v89, v89\n"
"v_mfma_f32_16x16x32_bf16 v[52:55], v[112:115], v[92:95], 0\n"
"v_cvt_pk_bf16_f32 v89, 0, v89\n"
"v_mfma_f32_16x16x32_bf16 v[40:43], v[84:87], v[156:159], v[40:43]\n"
"v_rcp_f32 v89, v89\n"
"v_mfma_f32_16x16x32_bf16 v[36:39], v[152:155], v[84:87], v[36:39]\n"
"ds_write2_b32 v8, v88, v89 offset0:0 offset1:16\n"
"v_mfma_f32_16x16x32_bf16 v[36:39], v[140:143], v[80:83], v[36:39]\n"
"ds_read_b128 v[76:79], v4 offset:33408\n"
"v_mfma_f32_16x16x32_bf16 v[32:35], v[136:139], v[80:83], v[32:35]\n"
"v_rcp_f32 v48, v48\n"
"v_mfma_f32_16x16x32_bf16 v[32:35], v[148:151], v[84:87], v[32:35]\n"
"s_add_u32 s63, s63, 4096\n"
"s_waitcnt lgkmcnt(3)\n"
"v_mfma_f32_16x16x32_bf16 v[32:35], v[160:163], v[56:59], v[32:35]\n"
"ds_read_b128 v[80:83], v4 offset:34560\n"
"s_add_u32 m0, s64, 8192\n"
"v_mfma_f32_16x16x32_bf16 v[32:35], v[172:175], v[60:63], v[32:35]\n"
"buffer_load_dwordx4 v2, s[32:35], s63 offen offset:0 sc1 lds\n"
"v_mul_f32 v52, v52, v48\n"
"v_mfma_f32_16x16x32_bf16 v[36:39], v[176:179], v[60:63], v[36:39]\n"
"v_mul_f32 v53, v53, v48\n"
"v_mul_f32 v54, v54, v48\n"
"v_mfma_f32_16x16x32_bf16 v[36:39], v[164:167], v[56:59], v[36:39]\n"
"ds_read_b128 v[84:87], v4 offset:35712\n"
"v_mul_f32 v55, v55, v48\n"
"v_mfma_f32_16x16x32_bf16 v[40:43], v[56:59], v[168:171], v[40:43]\n"
"v_xor_b32 v4, 73728, v4\n"
"v_cvt_pk_bf16_f32 v100, v52, v53\n"
"v_mfma_f32_16x16x32_bf16 v[40:43], v[60:63], v[180:183], v[40:43]\n"
"s_waitcnt vmcnt(10)\n"
"s_waitcnt lgkmcnt(3)\n"
"s_barrier\n"
"v_mfma_f32_16x16x32_bf16 v[40:43], v[64:67], v[192:195], v[40:43]\n"
"ds_read_b128 v[56:59], v5 offset:0\n"
"v_cvt_pk_bf16_f32 v101, v54, v55\n"
"v_mfma_f32_16x16x32_bf16 v[40:43], v[68:71], v[204:207], v[40:43]\n"
"buffer_store_dwordx2 v[30:31], v2, s[92:95], s67 idxen sc1\n"
"v_mfma_f32_16x16x32_bf16 v[36:39], v[200:203], v[68:71], v[36:39]\n"
"v_permlane32_swap_b32 v100, v102\n"
"v_mfma_f32_16x16x32_bf16 v[36:39], v[188:191], v[64:67], v[36:39]\n"
"ds_read_b128 v[60:63], v5 offset:1152\n"
"v_mfma_f32_16x16x32_bf16 v[32:35], v[184:187], v[64:67], v[32:35]\n"
"v_permlane32_swap_b32 v101, v103\n"
"v_mfma_f32_16x16x32_bf16 v[32:35], v[196:199], v[68:71], v[32:35]\n"
"buffer_store_dwordx4 v[100:103], v1, s[32:35], s68 offen sc1\n"
"s_waitcnt lgkmcnt(2)\n"
"v_mfma_f32_16x16x32_bf16 v[32:35], v[208:211], v[72:75], v[32:35]\n"
"ds_read_b128 v[64:67], v5 offset:2304\n"
"s_mov_b32 s67, s68\n"
"v_mfma_f32_16x16x32_bf16 v[32:35], v[220:223], v[76:79], v[32:35]\n"
"v_mfma_f32_16x16x32_bf16 v[36:39], v[224:227], v[76:79], v[36:39]\n"
"v_mfma_f32_16x16x32_bf16 v[36:39], v[212:215], v[72:75], v[36:39]\n"
"ds_read_b128 v[68:71], v5 offset:3456\n"
"v_mfma_f32_16x16x32_bf16 v[40:43], v[72:75], v[216:219], v[40:43]\n"
"v_mfma_f32_16x16x32_bf16 v[40:43], v[76:79], v[228:231], v[40:43]\n"
"v_mfma_f32_16x16x32_bf16 v[40:43], v[80:83], v[240:243], v[40:43]\n"
"ds_read_b128 v[72:75], v5 offset:4608\n"
"v_mfma_f32_16x16x32_bf16 v[40:43], v[84:87], v[252:255], v[40:43]\n"
"v_mfma_f32_16x16x32_bf16 v[36:39], v[248:251], v[84:87], v[36:39]\n"
"v_mfma_f32_16x16x32_bf16 v[36:39], v[236:239], v[80:83], v[36:39]\n"
"ds_read_b128 v[76:79], v5 offset:5760\n"
"v_mfma_f32_16x16x32_bf16 v[32:35], v[232:235], v[80:83], v[32:35]\n"
"v_mfma_f32_16x16x32_bf16 v[32:35], v[244:247], v[84:87], v[32:35]\n"
"s_bitcmp1_b64 s[60:61], s62\n"
"s_cbranch_scc1 .L_qs1\n"
".L_qb1:\n"
"s_waitcnt lgkmcnt(2)\n"
"v_mfma_f32_16x16x32_bf16 v[44:47], a[0:3], v[56:59], 0\n"
"ds_read_b128 v[80:83], v5 offset:6912\n"
"ds_read2_b32 v[14:15], v9 offset0:0 offset1:16\n"
"v_mfma_f32_16x16x32_bf16 v[44:47], a[12:15], v[60:63], v[44:47]\n"
"ds_read_b128 v[92:95], v10 offset:0\n"
"ds_read_b128 v[96:99], v10 offset:64\n"
"v_mfma_f32_16x16x32_bf16 v[48:51], a[16:19], v[60:63], 0\n"
"s_add_u32 s68, s68, 1187840\n"
"v_mfma_f32_16x16x32_bf16 v[48:51], a[4:7], v[56:59], v[48:51]\n"
"ds_read_b128 v[84:87], v5 offset:8064\n"
"v_mfma_f32_16x16x32_bf16 v[52:55], v[56:59], a[8:11], 0\n"
"ds_read2st64_b32 v[88:89], v6 offset0:144 offset1:180\n"
"v_mfma_f32_16x16x32_bf16 v[52:55], v[60:63], a[20:23], v[52:55]\n"
"s_waitcnt vmcnt(5)\n"
"v_cmp_ne_u64 vcc, v[30:31], v[12:13]\n"
"v_mfma_f32_16x16x32_bf16 v[52:55], v[64:67], a[32:35], v[52:55]\n"
"ds_read_b128 v[56:59], v5 offset:9216\n"
"ds_read2st64_b32 v[90:91], v6 offset0:216 offset1:252\n"
"v_mfma_f32_16x16x32_bf16 v[52:55], v[68:71], a[44:47], v[52:55]\n"
"s_cbranch_vccnz .L_rtq1\n"
".L_bkq1:\n"
"s_add_u32 s63, s66, s71\n"
"v_mfma_f32_16x16x32_bf16 v[48:51], a[40:43], v[68:71], v[48:51]\n"
"s_add_u32 s66, s66, s72\n"
"s_add_u32 m0, s64, 36864\n"
"v_mfma_f32_16x16x32_bf16 v[48:51], a[28:31], v[64:67], v[48:51]\n"
"ds_read_b128 v[60:63], v5 offset:10368\n"
"buffer_load_dwordx4 v2, s[32:35], s63 offen offset:0 sc1 lds\n"
"v_mfma_f32_16x16x32_bf16 v[44:47], a[24:27], v[64:67], v[44:47]\n"
"v_xor_b32 v6, v7, v6\n"
"s_waitcnt lgkmcnt(5)\n"
"v_mfma_f32_16x16x32_bf16 v[44:47], a[36:39], v[68:71], v[44:47]\n"
"v_mul_f32 v29, 0x3e8293ee, v15\n"
"s_waitcnt lgkmcnt(4)\n"
"v_mfma_f32_16x16x32_bf16 v[44:47], a[48:51], v[72:75], v[44:47]\n"
"ds_read_b128 v[64:67], v5 offset:11520\n"
"v_fma_f32 v36, -v14, v24, v36\n"
"v_mfma_f32_16x16x32_bf16 v[44:47], a[60:63], v[76:79], v[44:47]\n"
"v_fma_f32 v37, -v14, v25, v37\n"
"v_fma_f32 v38, -v14, v26, v38\n"
"v_mfma_f32_16x16x32_bf16 v[48:51], a[64:67], v[76:79], v[48:51]\n"
"v_fma_f32 v39, -v14, v27, v39\n"
"v_fma_f32 v32, -v14, v20, v32\n"
"v_mfma_f32_16x16x32_bf16 v[48:51], a[52:55], v[72:75], v[48:51]\n"
"ds_read_b128 v[68:71], v5 offset:12672\n"
"v_fma_f32 v33, -v14, v21, v33\n"
"v_mfma_f32_16x16x32_bf16 v[52:55], v[72:75], a[56:59], v[52:55]\n"
"buffer_load_dwordx4 v2, s[32:35], s63 offen offset:1024 sc1 lds\n"
"v_fma_f32 v34, -v14, v22, v34\n"
"v_mfma_f32_16x16x32_bf16 v[52:55], v[76:79], a[68:71], v[52:55]\n"
"v_fma_f32 v35, -v14, v23, v35\n"
"v_mul_f32 v36, v36, v15\n"
"v_mfma_f32_16x16x32_bf16 v[52:55], v[80:83], a[80:83], v[52:55]\n"
"ds_read_b128 v[72:75], v5 offset:13824\n"
"v_mul_f32 v37, v37, v15\n"
"v_mfma_f32_16x16x32_bf16 v[52:55], v[84:87], a[92:95], v[52:55]\n"
"v_mul_f32 v38, v38, v15\n"
"v_mul_f32 v39, v39, v15\n"
"v_mfma_f32_16x16x32_bf16 v[48:51], a[88:91], v[84:87], v[48:51]\n"
"v_mul_f32 v32, v32, v29\n"
"v_mul_f32 v33, v33, v29\n"
"v_mfma_f32_16x16x32_bf16 v[48:51], a[76:79], v[80:83], v[48:51]\n"
"ds_read_b128 v[76:79], v5 offset:14976\n"
"v_mul_f32 v34, v34, v29\n"
"v_mfma_f32_16x16x32_bf16 v[44:47], a[72:75], v[80:83], v[44:47]\n"
"v_mul_f32 v35, v35, v29\n"
"v_cvt_pk_bf16_f32 v104, v36, v37\n"
"v_mfma_f32_16x16x32_bf16 v[44:47], a[84:87], v[84:87], v[44:47]\n"
"v_cvt_pk_bf16_f32 v105, v38, v39\n"
"s_waitcnt lgkmcnt(2)\n"
"v_mfma_f32_16x16x32_bf16 v[44:47], a[96:99], v[56:59], v[44:47]\n"
"ds_read_b128 v[80:83], v5 offset:16128\n"
"buffer_load_dwordx4 v2, s[32:35], s63 offen offset:2048 sc1 lds\n"
"v_mfma_f32_16x16x32_bf16 v[44:47], a[108:111], v[60:63], v[44:47]\n"
"v_cvt_pk_bf16_f32 v100, v32, v33\n"
"v_cvt_pk_bf16_f32 v101, v34, v35\n"
"v_mfma_f32_16x16x32_bf16 v[48:51], a[112:115], v[60:63], v[48:51]\n"
"ds_write2st64_b64 v11, v[100:101], v[104:105] offset0:0 offset1:1\n"
"v_fma_f32 v40, -v92, v28, v40\n"
"v_mfma_f32_16x16x32_bf16 v[48:51], a[100:103], v[56:59], v[48:51]\n"
"ds_read_b128 v[84:87], v5 offset:17280\n"
"v_fma_f32 v41, -v93, v28, v41\n"
"v_mfma_f32_16x16x32_bf16 v[52:55], v[56:59], a[104:107], v[52:55]\n"
"v_fma_f32 v42, -v94, v28, v42\n"
"v_fma_f32 v43, -v95, v28, v43\n"
"v_mfma_f32_16x16x32_bf16 v[52:55], v[60:63], a[116:119], v[52:55]\n"
"v_mul_f32 v40, v40, v96\n"
"v_mul_f32 v41, v41, v97\n"
"v_mfma_f32_16x16x32_bf16 v[52:55], v[64:67], a[128:131], v[52:55]\n"
"ds_read_b128 v[56:59], v5 offset:18432\n"
"v_mul_f32 v42, v42, v98\n"
"v_mfma_f32_16x16x32_bf16 v[52:55], v[68:71], a[140:143], v[52:55]\n"
"v_mul_f32 v43, v43, v99\n"
"s_waitcnt lgkmcnt(2)\n"
"v_mfma_f32_16x16x32_bf16 v[48:51], a[136:139], v[68:71], v[48:51]\n"
"buffer_load_dwordx4 v2, s[32:35], s63 offen offset:3072 sc1 lds\n"
"s_barrier\n"
"v_mfma_f32_16x16x32_bf16 v[48:51], a[124:127], v[64:67], v[48:51]\n"
"ds_read_b128 v[60:63], v5 offset:19584\n"
"ds_read_b64 v[102:103], v3\n"
"v_mfma_f32_16x16x32_bf16 v[44:47], a[120:123], v[64:67], v[44:47]\n"
"ds_read_b64 v[106:107], v3 offset:512\n"
"v_cvt_pk_bf16_f32 v112, v40, v41\n"
"v_mfma_f32_16x16x32_bf16 v[44:47], a[132:135], v[68:71], v[44:47]\n"
"v_cvt_pk_bf16_f32 v113, v42, v43\n"
"s_waitcnt lgkmcnt(4)\n"
"v_mfma_f32_16x16x32_bf16 v[44:47], a[144:147], v[72:75], v[44:47]\n"
"ds_read_b128 v[64:67], v5 offset:20736\n"
"v_add_f32 v88, v88, v89\n"
"v_mfma_f32_16x16x32_bf16 v[44:47], a[156:159], v[76:79], v[44:47]\n"
"v_add_f32 v90, v90, v91\n"
"v_mfma_f32_16x16x32_bf16 v[48:51], a[160:163], v[76:79], v[48:51]\n"
"v_add_f32 v88, v88, v90\n"
"v_mfma_f32_16x16x32_bf16 v[48:51], a[148:151], v[72:75], v[48:51]\n"
"ds_read_b128 v[68:71], v5 offset:21888\n"
"s_add_u32 s63, s63, 4096\n"
"v_mfma_f32_16x16x32_bf16 v[52:55], v[72:75], a[152:155], v[52:55]\n"
"s_add_u32 m0, s64, 40960\n"
"s_waitcnt lgkmcnt(2)\n"
"v_mfma_f32_16x16x32_bf16 v[36:39], v[108:111], v[100:103], v[120:123]\n"
"buffer_load_dwordx4 v2, s[32:35], s63 offen offset:0 sc1 lds\n"
"v_add_f32_dpp v88, v88, v88 row_ror:8 row_mask:0xf bank_mask:0xf\n"
"v_mfma_f32_16x16x32_bf16 v[32:35], v[104:107], v[100:103], v[116:119]\n"
"v_mfma_f32_16x16x32_bf16 v[52:55], v[76:79], a[164:167], v[52:55]\n"
"v_add_f32_dpp v88, v88, v88 row_ror:4 row_mask:0xf bank_mask:0xf\n"
"v_mfma_f32_16x16x32_bf16 v[52:55], v[80:83], a[176:179], v[52:55]\n"
"ds_read_b128 v[72:75], v5 offset:23040\n"
"v_add_f32_dpp v88, v88, v88 row_ror:2 row_mask:0xf bank_mask:0xf\n"
"v_mfma_f32_16x16x32_bf16 v[52:55], v[84:87], a[188:191], v[52:55]\n"
"v_mul_f32 v88, 0x3a800000, v88\n"
"v_mfma_f32_16x16x32_bf16 v[48:51], a[184:187], v[84:87], v[48:51]\n"
"v_max3_f32 v32, v32, v33, v36\n"
"v_max3_f32 v34, v34, v35, v38\n"
"v_mfma_f32_16x16x32_bf16 v[48:51], a[172:175], v[80:83], v[48:51]\n"
"ds_read_b128 v[76:79], v5 offset:24192\n"
"v_max_f32 v32, v32, v37\n"
"v_mfma_f32_16x16x32_bf16 v[44:47], a[168:171], v[80:83], v[44:47]\n"
"v_max_f32 v34, v34, v39\n"
"v_mul_f32 v89, v88, v88\n"
"v_mfma_f32_16x16x32_bf16 v[44:47], a[180:183], v[84:87], v[44:47]\n"
"v_max_f32 v96, v32, v34\n"
"s_waitcnt lgkmcnt(2)\n"
"v_mfma_f32_16x16x32_bf16 v[44:47], a[192:195], v[56:59], v[44:47]\n"
"ds_read_b128 v[80:83], v5 offset:25344\n"
"buffer_load_dwordx4 v2, s[32:35], s63 offen offset:1024 sc1 lds\n"
"v_mfma_f32_16x16x32_bf16 v[44:47], a[204:207], v[60:63], v[44:47]\n"
"v_max_f32 v97, v32, v34\n"
"v_sub_f32_dpp v89, v88, v89 quad_perm:[1,1,3,3] row_mask:0xf bank_mask:0xf\n"
"v_mfma_f32_16x16x32_bf16 v[48:51], a[208:211], v[60:63], v[48:51]\n"
"buffer_load_dwordx2 v[12:13], v0, s[32:35], s66 offen sc1\n"
"v_max_f32 v89, 0, v89\n"
"v_mfma_f32_16x16x32_bf16 v[48:51], a[196:199], v[56:59], v[48:51]\n"
"ds_read_b128 v[84:87], v5 offset:26496\n"
"v_cvt_pk_bf16_f32 v89, 0, v89\n"
"v_mfma_f32_16x16x32_bf16 v[52:55], v[56:59], a[200:203], v[52:55]\n"
"v_permlane16_swap_b32 v96, v97\n"
"v_mfma_f32_16x16x32_bf16 v[52:55], v[60:63], a[212:215], v[52:55]\n"
"v_max_f32 v98, v96, v97\n"
"v_max_f32 v99, v96, v97\n"
"v_mfma_f32_16x16x32_bf16 v[52:55], v[64:67], a[224:227], v[52:55]\n"
"ds_read_b128 v[56:59], v5 offset:27648\n"
"v_add_f32 v89, 0x3727c5ac, v89\n"
"v_mfma_f32_16x16x32_bf16 v[52:55], v[68:71], a[236:239], v[52:55]\n"
"v_permlane32_swap_b32 v98, v99\n"
"v_mfma_f32_16x16x32_bf16 v[48:51], a[232:235], v[68:71], v[48:51]\n"
"buffer_load_dwordx4 v2, s[32:35], s63 offen offset:2048 sc1 lds\n"
"v_max_f32 v96, v98, v99\n"
"v_mfma_f32_16x16x32_bf16 v[48:51], a[220:223], v[64:67], v[48:51]\n"
"ds_read_b128 v[60:63], v5 offset:28800\n"
"v_sub_f32 v32, v32, v96\n"
"v_mfma_f32_16x16x32_bf16 v[44:47], a[216:219], v[64:67], v[44:47]\n"
"v_exp_f32 v32, v32\n"
"v_mfma_f32_16x16x32_bf16 v[44:47], a[228:231], v[68:71], v[44:47]\n"
"v_sub_f32 v34, v34, v96\n"
"s_waitcnt lgkmcnt(2)\n"
"v_mfma_f32_16x16x32_bf16 v[44:47], a[240:243], v[72:75], v[44:47]\n"
"ds_read_b128 v[64:67], v5 offset:29952\n"
"v_cvt_pk_bf16_f32 v32, v32, v32\n"
"v_mfma_f32_16x16x32_bf16 v[44:47], a[252:255], v[76:79], v[44:47]\n"
"v_exp_f32 v34, v34\n"
"v_mfma_f32_16x16x32_bf16 v[48:51], v[128:131], v[76:79], v[48:51]\n"
"v_cvt_pk_bf16_f32 v34, v34, v34\n"
"v_and_b32 v92, v32, v124\n"
"v_mfma_f32_16x16x32_bf16 v[48:51], a[244:247], v[72:75], v[48:51]\n"
"ds_read_b128 v[68:71], v5 offset:31104\n"
"v_and_b32 v94, v32, v126\n"
"v_mfma_f32_16x16x32_bf16 v[52:55], v[72:75], a[248:251], v[52:55]\n"
"buffer_load_dwordx4 v2, s[32:35], s63 offen offset:3072 sc1 lds\n"
"v_and_b32 v93, v34, v125\n"
"v_mfma_f32_16x16x32_bf16 v[52:55], v[76:79], v[132:135], v[52:55]\n"
"v_and_b32 v95, v34, v127\n"
"v_cvt_pk_bf16_f32 v89, 0, v89\n"
"v_mfma_f32_16x16x32_bf16 v[52:55], v[80:83], v[144:147], v[52:55]\n"
"ds_read_b128 v[72:75], v5 offset:32256\n"
"v_cvt_pk_bf16_f32 v88, 0, v88\n"
"v_mfma_f32_16x16x32_bf16 v[36:39], v[16:19], v[92:95], 0\n"
"v_sqrt_f32 v89, v89\n"
"v_mfma_f32_16x16x32_bf16 v[40:43], v[112:115], v[92:95], 0\n"
"v_cvt_pk_bf16_f32 v89, 0, v89\n"
"v_mfma_f32_16x16x32_bf16 v[52:55], v[84:87], v[156:159], v[52:55]\n"
"v_rcp_f32 v89, v89\n"
"v_mfma_f32_16x16x32_bf16 v[48:51], v[152:155], v[84:87], v[48:51]\n"
"ds_write2_b32 v8, v88, v89 offset0:32 offset1:48\n"
"v_mfma_f32_16x16x32_bf16 v[48:51], v[140:143], v[80:83], v[48:51]\n"
"ds_read_b128 v[76:79], v5 offset:33408\n"
"v_mfma_f32_16x16x32_bf16 v[44:47], v[136:139], v[80:83], v[44:47]\n"
"v_rcp_f32 v36, v36\n"
"v_mfma_f32_16x16x32_bf16 v[44:47], v[148:151], v[84:87], v[44:47]\n"
"s_add_u32 s63, s63, 4096\n"
"s_waitcnt lgkmcnt(3)\n"
"v_mfma_f32_16x16x32_bf16 v[44:47], v[160:163], v[56:59], v[44:47]\n"
"ds_read_b128 v[80:83], v5 offset:34560\n"
"s_add_u32 m0, s64, 45056\n"
"v_mfma_f32_16x16x32_bf16 v[44:47], v[172:175], v[60:63], v[44:47]\n"
"buffer_load_dwordx4 v2, s[32:35], s63 offen offset:0 sc1 lds\n"
"s_xor_b32 s64, s64, s65\n"
"v_mfma_f32_16x16x32_bf16 v[48:51], v[176:179], v[60:63], v[48:51]\n"
"v_mul_f32 v40, v40, v36\n"
"v_mul_f32 v41, v41, v36\n"
"v_mfma_f32_16x16x32_bf16 v[48:51], v[164:167], v[56:59], v[48:51]\n"
"ds_read_b128 v[84:87], v5 offset:35712\n"
"v_mul_f32 v42, v42, v36\n"
"v_mfma_f32_16x16x32_bf16 v[52:55], v[56:59], v[168:171], v[52:55]\n"
"v_xor_b32 v5, 73728, v5\n"
"v_mul_f32 v43, v43, v36\n"
"v_mfma_f32_16x16x32_bf16 v[52:55], v[60:63], v[180:183], v[52:55]\n"
"s_waitcnt vmcnt(10)\n"
"s_waitcnt lgkmcnt(3)\n"
"s_barrier\n"
"v_mfma_f32_16x16x32_bf16 v[52:55], v[64:67], v[192:195], v[52:55]\n"
"ds_read_b128 v[56:59], v4 offset:0\n"
"v_cvt_pk_bf16_f32 v100, v40, v41\n"
"v_mfma_f32_16x16x32_bf16 v[52:55], v[68:71], v[204:207], v[52:55]\n"
"buffer_store_dwordx2 v[30:31], v2, s[92:95], s67 idxen sc1\n"
"v_cvt_pk_bf16_f32 v101, v42, v43\n"
"v_mfma_f32_16x16x32_bf16 v[48:51], v[200:203], v[68:71], v[48:51]\n"
"v_permlane32_swap_b32 v100, v102\n"
"v_mfma_f32_16x16x32_bf16 v[48:51], v[188:191], v[64:67], v[48:51]\n"
"ds_read_b128 v[60:63], v4 offset:1152\n"
"v_mfma_f32_16x16x32_bf16 v[44:47], v[184:187], v[64:67], v[44:47]\n"
"v_permlane32_swap_b32 v101, v103\n"
"v_mfma_f32_16x16x32_bf16 v[44:47], v[196:199], v[68:71], v[44:47]\n"
"buffer_store_dwordx4 v[100:103], v1, s[32:35], s68 offen sc1\n"
"s_waitcnt lgkmcnt(2)\n"
"v_mfma_f32_16x16x32_bf16 v[44:47], v[208:211], v[72:75], v[44:47]\n"
"ds_read_b128 v[64:67], v4 offset:2304\n"
"s_mov_b32 s67, s68\n"
"v_mfma_f32_16x16x32_bf16 v[44:47], v[220:223], v[76:79], v[44:47]\n"
"v_mfma_f32_16x16x32_bf16 v[48:51], v[224:227], v[76:79], v[48:51]\n"
"v_mfma_f32_16x16x32_bf16 v[48:51], v[212:215], v[72:75], v[48:51]\n"
"ds_read_b128 v[68:71], v4 offset:3456\n"
"v_mfma_f32_16x16x32_bf16 v[52:55], v[72:75], v[216:219], v[52:55]\n"
"v_mfma_f32_16x16x32_bf16 v[52:55], v[76:79], v[228:231], v[52:55]\n"
"v_mfma_f32_16x16x32_bf16 v[52:55], v[80:83], v[240:243], v[52:55]\n"
"ds_read_b128 v[72:75], v4 offset:4608\n"
"v_mfma_f32_16x16x32_bf16 v[52:55], v[84:87], v[252:255], v[52:55]\n"
"v_mfma_f32_16x16x32_bf16 v[48:51], v[248:251], v[84:87], v[48:51]\n"
"v_mfma_f32_16x16x32_bf16 v[48:51], v[236:239], v[80:83], v[48:51]\n"
"ds_read_b128 v[76:79], v4 offset:5760\n"
"v_mfma_f32_16x16x32_bf16 v[44:47], v[232:235], v[80:83], v[44:47]\n"
"s_sub_u32 s62, s62, 1\n"
"v_mfma_f32_16x16x32_bf16 v[44:47], v[244:247], v[84:87], v[44:47]\n"
"s_cbranch_scc0 .L_ql\n"
"s_nop 1\n"
"ds_read2_b32 v[14:15], v9 offset0:32 offset1:48\n"
"ds_read_b128 v[92:95], v10 offset:128\n"
"ds_read_b128 v[96:99], v10 offset:192\n"
"s_add_u32 s68, s68, 1187840\n"
"s_waitcnt lgkmcnt(0)\n"
"v_mul_f32 v29, 0x3e8293ee, v15\n"
"v_fma_f32 v48, -v14, v24, v48\n"
"v_fma_f32 v49, -v14, v25, v49\n"
"v_fma_f32 v50, -v14, v26, v50\n"
"v_fma_f32 v51, -v14, v27, v51\n"
"v_fma_f32 v44, -v14, v20, v44\n"
"v_fma_f32 v45, -v14, v21, v45\n"
"v_fma_f32 v46, -v14, v22, v46\n"
"v_fma_f32 v47, -v14, v23, v47\n"
"v_mul_f32 v48, v48, v15\n"
"v_mul_f32 v49, v49, v15\n"
"v_mul_f32 v50, v50, v15\n"
"v_mul_f32 v51, v51, v15\n"
"v_mul_f32 v44, v44, v29\n"
"v_mul_f32 v45, v45, v29\n"
"v_mul_f32 v46, v46, v29\n"
"v_mul_f32 v47, v47, v29\n"
"v_cvt_pk_bf16_f32 v108, v48, v49\n"
"v_cvt_pk_bf16_f32 v109, v50, v51\n"
"v_cvt_pk_bf16_f32 v100, v44, v45\n"
"v_cvt_pk_bf16_f32 v101, v46, v47\n"
"ds_write2st64_b64 v11, v[100:101], v[108:109] offset0:0 offset1:1\n"
"v_fma_f32 v52, -v92, v28, v52\n"
"v_fma_f32 v53, -v93, v28, v53\n"
"v_fma_f32 v54, -v94, v28, v54\n"
"v_fma_f32 v55, -v95, v28, v55\n"
"v_mul_f32 v52, v52, v96\n"
"v_mul_f32 v53, v53, v97\n"
"v_mul_f32 v54, v54, v98\n"
"v_mul_f32 v55, v55, v99\n"
"s_waitcnt lgkmcnt(0)\n"
"s_barrier\n"
"ds_read_b64 v[102:103], v3\n"
"ds_read_b64 v[110:111], v3 offset:512\n"
"v_cvt_pk_bf16_f32 v114, v52, v53\n"
"v_cvt_pk_bf16_f32 v115, v54, v55\n"
"s_waitcnt lgkmcnt(0)\n"
"v_mfma_f32_16x16x32_bf16 v[48:51], v[104:107], v[100:103], v[120:123]\n"
"v_mfma_f32_16x16x32_bf16 v[44:47], v[108:111], v[100:103], v[116:119]\n"
"s_nop 7\n"
"v_max3_f32 v44, v44, v45, v48\n"
"v_max3_f32 v46, v46, v47, v50\n"
"v_max_f32 v44, v44, v49\n"
"v_max_f32 v46, v46, v51\n"
"v_max_f32 v96, v44, v46\n"
"v_max_f32 v97, v44, v46\n"
"s_nop 1\n"
"v_permlane16_swap_b32 v96, v97\n"
"v_max_f32 v98, v96, v97\n"
"v_max_f32 v99, v96, v97\n"
"s_nop 1\n"
"v_permlane32_swap_b32 v98, v99\n"
"v_max_f32 v96, v98, v99\n"
"v_sub_f32 v44, v44, v96\n"
"v_sub_f32 v46, v46, v96\n"
"v_exp_f32 v44, v44\n"
"v_exp_f32 v46, v46\n"
"v_cvt_pk_bf16_f32 v44, v44, v44\n"
"v_cvt_pk_bf16_f32 v46, v46, v46\n"
"v_and_b32 v94, v44, v124\n"
"v_and_b32 v92, v44, v126\n"
"v_and_b32 v95, v46, v125\n"
"v_and_b32 v93, v46, v127\n"
"s_nop 1\n"
"v_mfma_f32_16x16x32_bf16 v[48:51], v[16:19], v[92:95], 0\n"
"v_mfma_f32_16x16x32_bf16 v[52:55], v[112:115], v[92:95], 0\n"
"s_nop 6\n"
"v_rcp_f32 v48, v48\n"
"s_nop 0\n"
"v_mul_f32 v52, v52, v48\n"
"v_mul_f32 v53, v53, v48\n"
"v_mul_f32 v54, v54, v48\n"
"v_mul_f32 v55, v55, v48\n"
"v_cvt_pk_bf16_f32 v100, v52, v53\n"
"v_cvt_pk_bf16_f32 v101, v54, v55\n"
"s_nop 0\n"
"v_permlane32_swap_b32 v100, v102\n"
"v_permlane32_swap_b32 v101, v103\n"
"buffer_store_dwordx4 v[100:103], v1, s[32:35], s68 offen sc1\n"
"s_waitcnt vmcnt(1)\n"
"s_barrier\n"
"buffer_store_dwordx2 v[30:31], v2, s[92:95], s67 idxen sc1\n"
"s_mov_b32 s67, s68\n"
"s_waitcnt vmcnt(1)\n"
"s_barrier\n"
"buffer_store_dwordx2 v[30:31], v2, s[92:95], s67 idxen sc1\n"
"s_branch .L_end\n"
".L_w:\n"
"s_mov_b32 s96, 0\n"
"s_add_u32 s36, s12, 0x0\n"
"s_addc_u32 s37, s13, 0x0\n"
"s_and_b32 s37, s37, 0xffff\n"
"s_mov_b32 s38, 0x200000\n"
"s_mov_b32 s39, 0x20000\n"
"s_add_u32 s40, s12, 0xfffffc80\n"
"s_addc_u32 s41, s13, 0xffffffff\n"
"s_and_b32 s41, s41, 0xffff\n"
"s_mov_b32 s42, 0x200380\n"
"s_mov_b32 s43, 0x20000\n"
"s_lshl_b32 s48, s27, 8\n"
"s_add_u32 s96, s96, s48\n"
"v_and_b32 v8, 3, v1\n"
"v_mul_u32_u24 v8, 16, v8\n"
"s_and_b32 s48, s26, 1\n"
"s_lshl_b32 s48, s48, 6\n"
"s_lshr_b32 s49, s26, 1\n"
"s_lshl_b32 s49, s49, 10\n"
"s_add_u32 s48, s48, s49\n"
"v_add_u32 v8, s48, v8\n"
"v_lshrrev_b32 v4, 3, v1\n"
"v_bfe_u32 v5, v1, 4, 1\n"
"s_and_b32 s48, s26, 1\n"
"s_lshl_b32 s48, s48, 1\n"
"v_or_b32 v5, s48, v5\n"
"v_lshlrev_b32 v5, 1, v5\n"
"v_and_b32 v6, 7, v1\n"
"v_xor_b32 v5, v5, v6\n"
"v_mul_u32_u24 v4, 2048, v4\n"
"v_lshl_add_u32 v7, v5, 4, v4\n"
"s_mul_i32 s48, s26, 16384\n"
"s_add_u32 s96, s96, s48\n"
"s_mul_i32 s97, s26, 2048\n"
"s_add_u32 m0, s97, 0\n"
"s_nop 0\n"
"buffer_load_dwordx4 v7, s[36:39], s96 offen offset:0 lds\n"
"buffer_load_dwordx4 v7, s[40:43], s96 offen offset:1024 lds\n"
"s_add_u32 s96, s96, 65536\n"
"s_add_u32 m0, s97, 8192\n"
"s_nop 0\n"
"buffer_load_dwordx4 v7, s[36:39], s96 offen offset:0 lds\n"
"buffer_load_dwordx4 v7, s[40:43], s96 offen offset:1024 lds\n"
"s_add_u32 s96, s96, 65536\n"
"s_add_u32 m0, s97, 16384\n"
"s_nop 0\n"
"buffer_load_dwordx4 v7, s[36:39], s96 offen offset:0 lds\n"
"buffer_load_dwordx4 v7, s[40:43], s96 offen offset:1024 lds\n"
"s_add_u32 s96, s96, 65536\n"
"s_add_u32 m0, s97, 24576\n"
"s_nop 0\n"
"buffer_load_dwordx4 v7, s[36:39], s96 offen offset:0 lds\n"
"buffer_load_dwordx4 v7, s[40:43], s96 offen offset:1024 lds\n"
"s_add_u32 s96, s96, 65536\n"
"s_add_u32 m0, s97, 32768\n"
"s_nop 0\n"
"buffer_load_dwordx4 v7, s[36:39], s96 offen offset:0 lds\n"
"buffer_load_dwordx4 v7, s[40:43], s96 offen offset:1024 lds\n"
"s_add_u32 s96, s96, 65536\n"
"s_add_u32 m0, s97, 40960\n"
"s_nop 0\n"
"buffer_load_dwordx4 v7, s[36:39], s96 offen offset:0 lds\n"
"buffer_load_dwordx4 v7, s[40:43], s96 offen offset:1024 lds\n"
"s_add_u32 s96, s96, 65536\n"
"s_add_u32 m0, s97, 49152\n"
"s_nop 0\n"
"buffer_load_dwordx4 v7, s[36:39], s96 offen offset:0 lds\n"
"buffer_load_dwordx4 v7, s[40:43], s96 offen offset:1024 lds\n"
"s_add_u32 s96, s96, 65536\n"
"s_add_u32 m0, s97, 57344\n"
"s_nop 0\n"
"buffer_load_dwordx4 v7, s[36:39], s96 offen offset:0 lds\n"
"buffer_load_dwordx4 v7, s[40:43], s96 offen offset:1024 lds\n"
"s_add_u32 s96, s96, 65536\n"
"s_add_u32 m0, s97, 65536\n"
"s_nop 0\n"
"buffer_load_dwordx4 v7, s[36:39], s96 offen offset:0 lds\n"
"buffer_load_dwordx4 v7, s[40:43], s96 offen offset:1024 lds\n"
"s_add_u32 s96, s96, 65536\n"
"s_add_u32 m0, s97, 73728\n"
"s_nop 0\n"
"buffer_load_dwordx4 v7, s[36:39], s96 offen offset:0 lds\n"
"buffer_load_dwordx4 v7, s[40:43], s96 offen offset:1024 lds\n"
"s_add_u32 s96, s96, 65536\n"
"s_add_u32 m0, s97, 81920\n"
"s_nop 0\n"
"buffer_load_dwordx4 v7, s[36:39], s96 offen offset:0 lds\n"
"buffer_load_dwordx4 v7, s[40:43], s96 offen offset:1024 lds\n"
"s_add_u32 s96, s96, 65536\n"
"s_add_u32 m0, s97, 90112\n"
"s_nop 0\n"
"buffer_load_dwordx4 v7, s[36:39], s96 offen offset:0 lds\n"
"buffer_load_dwordx4 v7, s[40:43], s96 offen offset:1024 lds\n"
"s_add_u32 s96, s96, 65536\n"
"s_add_u32 m0, s97, 98304\n"
"s_nop 0\n"
"buffer_load_dwordx4 v7, s[36:39], s96 offen offset:0 lds\n"
"buffer_load_dwordx4 v7, s[40:43], s96 offen offset:1024 lds\n"
"s_add_u32 s96, s96, 65536\n"
"s_add_u32 m0, s97, 106496\n"
"s_nop 0\n"
"buffer_load_dwordx4 v7, s[36:39], s96 offen offset:0 lds\n"
"buffer_load_dwordx4 v7, s[40:43], s96 offen offset:1024 lds\n"
"s_add_u32 s96, s96, 65536\n"
"v_lshrrev_b32 v5, 4, v1\n"
"v_bfe_u32 v4, v1, 3, 1\n"
"v_and_b32 v6, 1, v5\n"
"v_lshl_add_u32 v4, v6, 1, v4\n"
"v_lshlrev_b32 v4, 5, v4\n"
"v_mul_u32_u24 v5, 2048, v5\n"
"v_bfe_u32 v6, v1, 2, 2\n"
"v_lshl_add_u32 v5, v6, 7, v5\n"
"v_xor_b32 v8, v4, v8\n"
"v_add_u32 v8, v5, v8\n"
"v_add_u32 v9, 65536, v8\n"
"s_waitcnt vmcnt(24)\n"
"s_barrier\n"
"s_add_u32 m0, s97, 114688\n"
"s_nop 0\n"
"buffer_load_dwordx4 v7, s[36:39], s96 offen offset:0 lds\n"
"buffer_load_dwordx4 v7, s[40:43], s96 offen offset:1024 lds\n"
"s_add_u32 s96, s96, 65536\n"
"s_add_u32 m0, s97, 122880\n"
"s_nop 0\n"
"buffer_load_dwordx4 v7, s[36:39], s96 offen offset:0 lds\n"
"buffer_load_dwordx4 v7, s[40:43], s96 offen offset:1024 lds\n"
"s_add_u32 s96, s96, 65536\n"
"ds_read_b64_tr_b16 a[0:1], v8 offset:0\n"
"ds_read_b64_tr_b16 a[2:3], v8 offset:512\n"
"ds_read_b64_tr_b16 a[4:5], v8 offset:8\n"
"ds_read_b64_tr_b16 a[6:7], v8 offset:520\n"
"ds_read_b64_tr_b16 a[8:9], v8 offset:8192\n"
"ds_read_b64_tr_b16 a[10:11], v8 offset:8704\n"
"ds_read_b64_tr_b16 a[12:13], v8 offset:8200\n"
"ds_read_b64_tr_b16 a[14:15], v8 offset:8712\n"
"s_waitcnt lgkmcnt(0)\n"
"s_waitcnt vmcnt(24)\n"
"s_barrier\n"
"s_add_u32 m0, s97, 0\n"
"s_nop 0\n"
"buffer_load_dwordx4 v7, s[36:39], s96 offen offset:0 lds\n"
"buffer_load_dwordx4 v7, s[40:43], s96 offen offset:1024 lds\n"
"s_add_u32 s96, s96, 65536\n"
"s_add_u32 m0, s97, 8192\n"
"s_nop 0\n"
"buffer_load_dwordx4 v7, s[36:39], s96 offen offset:0 lds\n"
"buffer_load_dwordx4 v7, s[40:43], s96 offen offset:1024 lds\n"
"s_add_u32 s96, s96, 65536\n"
"ds_read_b64_tr_b16 a[16:17], v8 offset:16384\n"
"ds_read_b64_tr_b16 a[18:19], v8 offset:16896\n"
"ds_read_b64_tr_b16 a[20:21], v8 offset:16392\n"
"ds_read_b64_tr_b16 a[22:23], v8 offset:16904\n"
"ds_read_b64_tr_b16 a[24:25], v8 offset:24576\n"
"ds_read_b64_tr_b16 a[26:27], v8 offset:25088\n"
"ds_read_b64_tr_b16 a[28:29], v8 offset:24584\n"
"ds_read_b64_tr_b16 a[30:31], v8 offset:25096\n"
"s_waitcnt lgkmcnt(0)\n"
"s_waitcnt vmcnt(24)\n"
"s_barrier\n"
"s_add_u32 m0, s97, 16384\n"
"s_nop 0\n"
"buffer_load_dwordx4 v7, s[36:39], s96 offen offset:0 lds\n"
"buffer_load_dwordx4 v7, s[40:43], s96 offen offset:1024 lds\n"
"s_add_u32 s96, s96, 65536\n"
"s_add_u32 m0, s97, 24576\n"
"s_nop 0\n"
"buffer_load_dwordx4 v7, s[36:39], s96 offen offset:0 lds\n"
"buffer_load_dwordx4 v7, s[40:43], s96 offen offset:1024 lds\n"
"s_add_u32 s96, s96, 65536\n"
"ds_read_b64_tr_b16 a[32:33], v8 offset:32768\n"
"ds_read_b64_tr_b16 a[34:35], v8 offset:33280\n"
"ds_read_b64_tr_b16 a[36:37], v8 offset:32776\n"
"ds_read_b64_tr_b16 a[38:39], v8 offset:33288\n"
"ds_read_b64_tr_b16 a[40:41], v8 offset:40960\n"
"ds_read_b64_tr_b16 a[42:43], v8 offset:41472\n"
"ds_read_b64_tr_b16 a[44:45], v8 offset:40968\n"
"ds_read_b64_tr_b16 a[46:47], v8 offset:41480\n"
"s_waitcnt lgkmcnt(0)\n"
"s_waitcnt vmcnt(24)\n"
"s_barrier\n"
"s_add_u32 m0, s97, 32768\n"
"s_nop 0\n"
"buffer_load_dwordx4 v7, s[36:39], s96 offen offset:0 lds\n"
"buffer_load_dwordx4 v7, s[40:43], s96 offen offset:1024 lds\n"
"s_add_u32 s96, s96, 65536\n"
"s_add_u32 m0, s97, 40960\n"
"s_nop 0\n"
"buffer_load_dwordx4 v7, s[36:39], s96 offen offset:0 lds\n"
"buffer_load_dwordx4 v7, s[40:43], s96 offen offset:1024 lds\n"
"s_add_u32 s96, s96, 65536\n"
"ds_read_b64_tr_b16 a[48:49], v8 offset:49152\n"
"ds_read_b64_tr_b16 a[50:51], v8 offset:49664\n"
"ds_read_b64_tr_b16 a[52:53], v8 offset:49160\n"
"ds_read_b64_tr_b16 a[54:55], v8 offset:49672\n"
"ds_read_b64_tr_b16 a[56:57], v8 offset:57344\n"
"ds_read_b64_tr_b16 a[58:59], v8 offset:57856\n"
"ds_read_b64_tr_b16 a[60:61], v8 offset:57352\n"
"ds_read_b64_tr_b16 a[62:63], v8 offset:57864\n"
"s_waitcnt lgkmcnt(0)\n"
"s_waitcnt vmcnt(24)\n"
"s_barrier\n"
"s_add_u32 m0, s97, 49152\n"
"s_nop 0\n"
"buffer_load_dwordx4 v7, s[36:39], s96 offen offset:0 lds\n"
"buffer_load_dwordx4 v7, s[40:43], s96 offen offset:1024 lds\n"
"s_add_u32 s96, s96, 65536\n"
"s_add_u32 m0, s97, 57344\n"
"s_nop 0\n"
"buffer_load_dwordx4 v7, s[36:39], s96 offen offset:0 lds\n"
"buffer_load_dwordx4 v7, s[40:43], s96 offen offset:1024 lds\n"
"s_add_u32 s96, s96, 65536\n"
"ds_read_b64_tr_b16 a[64:65], v9 offset:0\n"
"ds_read_b64_tr_b16 a[66:67], v9 offset:512\n"
"ds_read_b64_tr_b16 a[68:69], v9 offset:8\n"
"ds_read_b64_tr_b16 a[70:71], v9 offset:520\n"
"ds_read_b64_tr_b16 a[72:73], v9 offset:8192\n"
"ds_read_b64_tr_b16 a[74:75], v9 offset:8704\n"
"ds_read_b64_tr_b16 a[76:77], v9 offset:8200\n"
"ds_read_b64_tr_b16 a[78:79], v9 offset:8712\n"
"s_waitcnt lgkmcnt(0)\n"
"s_waitcnt vmcnt(24)\n"
"s_barrier\n"
"s_add_u32 m0, s97, 65536\n"
"s_nop 0\n"
"buffer_load_dwordx4 v7, s[36:39], s96 offen offset:0 lds\n"
"buffer_load_dwordx4 v7, s[40:43], s96 offen offset:1024 lds\n"
"s_add_u32 s96, s96, 65536\n"
"s_add_u32 m0, s97, 73728\n"
"s_nop 0\n"
"buffer_load_dwordx4 v7, s[36:39], s96 offen offset:0 lds\n"
"buffer_load_dwordx4 v7, s[40:43], s96 offen offset:1024 lds\n"
"s_add_u32 s96, s96, 65536\n"
"ds_read_b64_tr_b16 a[80:81], v9 offset:16384\n"
"ds_read_b64_tr_b16 a[82:83], v9 offset:16896\n"
"ds_read_b64_tr_b16 a[84:85], v9 offset:16392\n"
"ds_read_b64_tr_b16 a[86:87], v9 offset:16904\n"
"ds_read_b64_tr_b16 a[88:89], v9 offset:24576\n"
"ds_read_b64_tr_b16 a[90:91], v9 offset:25088\n"
"ds_read_b64_tr_b16 a[92:93], v9 offset:24584\n"
"ds_read_b64_tr_b16 a[94:95], v9 offset:25096\n"
"s_waitcnt lgkmcnt(0)\n"
"s_waitcnt vmcnt(24)\n"
"s_barrier\n"
"s_add_u32 m0, s97, 81920\n"
"s_nop 0\n"
"buffer_load_dwordx4 v7, s[36:39], s96 offen offset:0 lds\n"
"buffer_load_dwordx4 v7, s[40:43], s96 offen offset:1024 lds\n"
"s_add_u32 s96, s96, 65536\n"
"s_add_u32 m0, s97, 90112\n"
"s_nop 0\n"
"buffer_load_dwordx4 v7, s[36:39], s96 offen offset:0 lds\n"
"buffer_load_dwordx4 v7, s[40:43], s96 offen offset:1024 lds\n"
"s_add_u32 s96, s96, 65536\n"
"ds_read_b64_tr_b16 a[96:97], v9 offset:32768\n"
"ds_read_b64_tr_b16 a[98:99], v9 offset:33280\n"
"ds_read_b64_tr_b16 a[100:101], v9 offset:32776\n"
"ds_read_b64_tr_b16 a[102:103], v9 offset:33288\n"
"ds_read_b64_tr_b16 a[104:105], v9 offset:40960\n"
"ds_read_b64_tr_b16 a[106:107], v9 offset:41472\n"
"ds_read_b64_tr_b16 a[108:109], v9 offset:40968\n"
"ds_read_b64_tr_b16 a[110:111], v9 offset:41480\n"
"s_waitcnt lgkmcnt(0)\n"
"s_waitcnt vmcnt(24)\n"
"s_barrier\n"
"s_add_u32 m0, s97, 98304\n"
"s_nop 0\n"
"buffer_load_dwordx4 v7, s[36:39], s96 offen offset:0 lds\n"
"buffer_load_dwordx4 v7, s[40:43], s96 offen offset:1024 lds\n"
"s_add_u32 s96, s96, 65536\n"
"s_add_u32 m0, s97, 106496\n"
"s_nop 0\n"
"buffer_load_dwordx4 v7, s[36:39], s96 offen offset:0 lds\n"
"buffer_load_dwordx4 v7, s[40:43], s96 offen offset:1024 lds\n"
"s_add_u32 s96, s96, 65536\n"
"ds_read_b64_tr_b16 a[112:113], v9 offset:49152\n"
"ds_read_b64_tr_b16 a[114:115], v9 offset:49664\n"
"ds_read_b64_tr_b16 a[116:117], v9 offset:49160\n"
"ds_read_b64_tr_b16 a[118:119], v9 offset:49672\n"
"ds_read_b64_tr_b16 a[120:121], v9 offset:57344\n"
"ds_read_b64_tr_b16 a[122:123], v9 offset:57856\n"
"ds_read_b64_tr_b16 a[124:125], v9 offset:57352\n"
"ds_read_b64_tr_b16 a[126:127], v9 offset:57864\n"
"s_waitcnt lgkmcnt(0)\n"
"s_waitcnt vmcnt(24)\n"
"s_barrier\n"
"s_add_u32 m0, s97, 114688\n"
"s_nop 0\n"
"buffer_load_dwordx4 v7, s[36:39], s96 offen offset:0 lds\n"
"buffer_load_dwordx4 v7, s[40:43], s96 offen offset:1024 lds\n"
"s_add_u32 s96, s96, 65536\n"
"s_add_u32 m0, s97, 122880\n"
"s_nop 0\n"
"buffer_load_dwordx4 v7, s[36:39], s96 offen offset:0 lds\n"
"buffer_load_dwordx4 v7, s[40:43], s96 offen offset:1024 lds\n"
"s_add_u32 s96, s96, 65536\n"
"ds_read_b64_tr_b16 a[128:129], v8 offset:0\n"
"ds_read_b64_tr_b16 a[130:131], v8 offset:512\n"
"ds_read_b64_tr_b16 a[132:133], v8 offset:8\n"
"ds_read_b64_tr_b16 a[134:135], v8 offset:520\n"
"ds_read_b64_tr_b16 a[136:137], v8 offset:8192\n"
"ds_read_b64_tr_b16 a[138:139], v8 offset:8704\n"
"ds_read_b64_tr_b16 a[140:141], v8 offset:8200\n"
"ds_read_b64_tr_b16 a[142:143], v8 offset:8712\n"
"s_waitcnt lgkmcnt(0)\n"
"s_waitcnt vmcnt(24)\n"
"s_barrier\n"
"ds_read_b64_tr_b16 a[144:145], v8 offset:16384\n"
"ds_read_b64_tr_b16 a[146:147], v8 offset:16896\n"
"ds_read_b64_tr_b16 a[148:149], v8 offset:16392\n"
"ds_read_b64_tr_b16 a[150:151], v8 offset:16904\n"
"ds_read_b64_tr_b16 a[152:153], v8 offset:24576\n"
"ds_read_b64_tr_b16 a[154:155], v8 offset:25088\n"
"ds_read_b64_tr_b16 a[156:157], v8 offset:24584\n"
"ds_read_b64_tr_b16 a[158:159], v8 offset:25096\n"
"s_waitcnt lgkmcnt(0)\n"
"s_waitcnt vmcnt(20)\n"
"s_barrier\n"
"ds_read_b64_tr_b16 a[160:161], v8 offset:32768\n"
"ds_read_b64_tr_b16 a[162:163], v8 offset:33280\n"
"ds_read_b64_tr_b16 a[164:165], v8 offset:32776\n"
"ds_read_b64_tr_b16 a[166:167], v8 offset:33288\n"
"ds_read_b64_tr_b16 a[168:169], v8 offset:40960\n"
"ds_read_b64_tr_b16 a[170:171], v8 offset:41472\n"
"ds_read_b64_tr_b16 a[172:173], v8 offset:40968\n"
"ds_read_b64_tr_b16 a[174:175], v8 offset:41480\n"
"s_waitcnt lgkmcnt(0)\n"
"s_waitcnt vmcnt(16)\n"
"s_barrier\n"
"ds_read_b64_tr_b16 a[176:177], v8 offset:49152\n"
"ds_read_b64_tr_b16 a[178:179], v8 offset:49664\n"
"ds_read_b64_tr_b16 a[180:181], v8 offset:49160\n"
"ds_read_b64_tr_b16 a[182:183], v8 offset:49672\n"
"ds_read_b64_tr_b16 a[184:185], v8 offset:57344\n"
"ds_read_b64_tr_b16 a[186:187], v8 offset:57856\n"
"ds_read_b64_tr_b16 a[188:189], v8 offset:57352\n"
"ds_read_b64_tr_b16 a[190:191], v8 offset:57864\n"
"s_waitcnt lgkmcnt(0)\n"
"s_waitcnt vmcnt(12)\n"
"s_barrier\n"
"ds_read_b64_tr_b16 a[192:193], v9 offset:0\n"
"ds_read_b64_tr_b16 a[194:195], v9 offset:512\n"
"ds_read_b64_tr_b16 a[196:197], v9 offset:8\n"
"ds_read_b64_tr_b16 a[198:199], v9 offset:520\n"
"ds_read_b64_tr_b16 a[200:201], v9 offset:8192\n"
"ds_read_b64_tr_b16 a[202:203], v9 offset:8704\n"
"ds_read_b64_tr_b16 a[204:205], v9 offset:8200\n"
"ds_read_b64_tr_b16 a[206:207], v9 offset:8712\n"
"s_waitcnt lgkmcnt(0)\n"
"s_waitcnt vmcnt(8)\n"
"s_barrier\n"
"ds_read_b64_tr_b16 a[208:209], v9 offset:16384\n"
"ds_read_b64_tr_b16 a[210:211], v9 offset:16896\n"
"ds_read_b64_tr_b16 a[212:213], v9 offset:16392\n"
"ds_read_b64_tr_b16 a[214:215], v9 offset:16904\n"
"ds_read_b64_tr_b16 a[216:217], v9 offset:24576\n"
"ds_read_b64_tr_b16 a[218:219], v9 offset:25088\n"
"ds_read_b64_tr_b16 a[220:221], v9 offset:24584\n"
"ds_read_b64_tr_b16 a[222:223], v9 offset:25096\n"
"s_waitcnt lgkmcnt(0)\n"
"s_waitcnt vmcnt(4)\n"
"s_barrier\n"
"ds_read_b64_tr_b16 a[224:225], v9 offset:32768\n"
"ds_read_b64_tr_b16 a[226:227], v9 offset:33280\n"
"ds_read_b64_tr_b16 a[228:229], v9 offset:32776\n"
"ds_read_b64_tr_b16 a[230:231], v9 offset:33288\n"
"ds_read_b64_tr_b16 a[232:233], v9 offset:40960\n"
"ds_read_b64_tr_b16 a[234:235], v9 offset:41472\n"
"ds_read_b64_tr_b16 a[236:237], v9 offset:40968\n"
"ds_read_b64_tr_b16 a[238:239], v9 offset:41480\n"
"s_waitcnt lgkmcnt(0)\n"
"s_waitcnt vmcnt(0)\n"
"s_barrier\n"
"ds_read_b64_tr_b16 a[240:241], v9 offset:49152\n"
"ds_read_b64_tr_b16 a[242:243], v9 offset:49664\n"
"ds_read_b64_tr_b16 a[244:245], v9 offset:49160\n"
"ds_read_b64_tr_b16 a[246:247], v9 offset:49672\n"
"ds_read_b64_tr_b16 a[248:249], v9 offset:57344\n"
"ds_read_b64_tr_b16 a[250:251], v9 offset:57856\n"
"ds_read_b64_tr_b16 a[252:253], v9 offset:57352\n"
"ds_read_b64_tr_b16 a[254:255], v9 offset:57864\n"
"s_waitcnt lgkmcnt(0)\n"
"s_barrier\n"
"s_lshl2_add_u32 s50, s27, s26\n"
"s_and_b32 s51, s50, 6\n"
"s_lshl_b32 s51, s51, 1\n"
"v_add_u32 v3, s51, v1\n"
"v_bfi_b32 v3, 15, v3, v1\n"
"v_lshlrev_b32 v3, 2, v3\n"
"v_cmp_gt_u32 vcc, 32, v1\n"
"v_mov_b32 v15, 0x7fff0000\n"
"s_nop 0\n"
"v_cndmask_b32 v15, v15, v3, vcc\n"
"s_mul_i32 s48, s50, 1152\n"
"s_add_u32 s68, s48, 4681728\n"
"s_lshl2_add_u32 s49, s27, 96\n"
"s_waitcnt lgkmcnt(0)\n"
"s_add_u32 s54, s52, 0x7f4a7c15\n"
"s_addc_u32 s55, s53, 0x9e3779b9\n"
"s_lshl_b32 s67, s49, 5\n"
"s_add_u32 s67, s67, 4898816\n"
"s_sub_u32 s51, s67, s68\n"
"s_add_u32 s92, s32, s51\n"
"s_addc_u32 s93, s33, 0x10000\n"
"s_sub_u32 s92, s92, 593920\n"
"s_subb_u32 s93, s93, 0\n"
"s_cmp_eq_u32 s26, 0\n"
"s_cselect_b32 s94, 1, 0\n"
"s_mov_b32 s95, 0x20000\n"
"v_mov_b32 v16, s54\n"
"v_mov_b32 v17, s55\n"
"v_cmp_gt_u32 vcc, 12, v1\n"
"v_mul_u32_u24 v3, 296960, v1\n"
"v_mov_b32 v12, 0x7fff0000\n"
"v_cndmask_b32 v3, v12, v3, vcc\n"
"s_add_u32 s51, s67, 76021760\n"
"buffer_store_dwordx2 v[16:17], v3, s[32:35], s51 offen sc1\n"
"s_sub_u32 s67, s68, 296960\n"
"s_mul_i32 s49, s28, 296960\n"
"s_add_u32 s48, s48, s49\n"
"s_add_u32 s71, s48, 4612096\n"
"s_add_u32 s68, s48, 3493888\n"
"s_mov_b32 s62, 63\n"
"s_add_u32 s63, s49, 4648960\n"
"s_add_u32 s66, s49, 4898816\n"
"s_mul_i32 s64, s26, 8192\n"
"s_add_u32 s65, s64, 65536\n"
"s_xor_b32 s65, s65, s64\n"
"s_add_u32 s63, s63, s64\n"
"v_mov_b32 v4, v2\n"
"v_add_u32 v6, 4096, v2\n"
"v_and_b32 v14, 15, v1\n"
"v_lshlrev_b32 v14, 2, v14\n"
"s_mov_b32 s48, 32\n"
"v_add_u32 v14, s48, v14\n"
"v_lshrrev_b32 v14, 2, v14\n"
"v_lshlrev_b32 v14, 7, v14\n"
"v_lshrrev_b32 v3, 4, v1\n"
"v_mul_u32_u24 v3, 593920, v3\n"
"v_add_u32 v14, v3, v14\n"
"v_cmp_gt_u32 vcc, 32, v1\n"
"v_mov_b32 v3, 0x7fff0000\n"
"s_nop 0\n"
"v_cndmask_b32 v14, v3, v14, vcc\n"
"v_cndmask_b32 v16, 0, v16, vcc\n"
"v_cndmask_b32 v17, 0, v17, vcc\n"
"s_mov_b32 s48, 20000\n"
"s_branch .L_rfwa\n"
".L_rlwa:\n"
"s_sleep 8\n"
".L_rfwa:\n"
"buffer_load_dwordx2 v[12:13], v14, s[32:35], s66 offen sc1\n"
"s_sub_u32 s48, s48, 1\n"
"s_cbranch_scc1 .L_end\n"
"s_waitcnt vmcnt(0)\n"
"v_cmp_ne_u64 vcc, v[16:17], v[12:13]\n"
"s_cbranch_vccnz .L_rlwa\n"
"s_add_u32 s66, s66, 1187840\n"
"s_add_u32 m0, s64, 0\n"
"s_nop 0\n"
"buffer_load_dwordx4 v2, s[32:35], s63 offen offset:0 sc1 lds\n"
"buffer_load_dwordx4 v2, s[32:35], s63 offen offset:1024 sc1 lds\n"
"buffer_load_dwordx4 v2, s[32:35], s63 offen offset:2048 sc1 lds\n"
"buffer_load_dwordx4 v2, s[32:35], s63 offen offset:3072 sc1 lds\n"
"s_add_u32 m0, s64, 4096\n"
"s_nop 0\n"
"buffer_load_dwordx4 v6, s[32:35], s63 offen offset:0 sc1 lds\n"
"buffer_load_dwordx4 v6, s[32:35], s63 offen offset:1024 sc1 lds\n"
"buffer_load_dwordx4 v6, s[32:35], s63 offen offset:2048 sc1 lds\n"
"buffer_load_dwordx4 v6, s[32:35], s63 offen offset:3072 sc1 lds\n"
"s_add_u32 s63, s63, 593920\n"
"buffer_load_dwordx4 v[48:51], v2, s[32:35], s71 offen sc1\n"
"s_add_u32 s71, s71, 593920\n"
"buffer_load_dwordx4 v[52:55], v2, s[32:35], s71 offen sc1\n"
"s_add_u32 s71, s71, 593920\n"
"s_add_u32 m0, s64, 32768\n"
"s_nop 0\n"
"buffer_load_dwordx4 v2, s[32:35], s63 offen offset:0 sc1 lds\n"
"buffer_load_dwordx4 v2, s[32:35], s63 offen offset:1024 sc1 lds\n"
"buffer_load_dwordx4 v2, s[32:35], s63 offen offset:2048 sc1 lds\n"
"buffer_load_dwordx4 v2, s[32:35], s63 offen offset:3072 sc1 lds\n"
"s_add_u32 m0, s64, 36864\n"
"s_nop 0\n"
"buffer_load_dwordx4 v6, s[32:35], s63 offen offset:0 sc1 lds\n"
"buffer_load_dwordx4 v6, s[32:35], s63 offen offset:1024 sc1 lds\n"
"buffer_load_dwordx4 v6, s[32:35], s63 offen offset:2048 sc1 lds\n"
"buffer_load_dwordx4 v6, s[32:35], s63 offen offset:3072 sc1 lds\n"
"s_add_u32 s63, s63, 593920\n"
"s_xor_b32 s64, s64, s65\n"
"s_mov_b32 s48, 20000\n"
"s_branch .L_rfwc\n"
".L_rlwc:\n"
"s_sleep 8\n"
".L_rfwc:\n"
"buffer_load_dwordx2 v[12:13], v14, s[32:35], s66 offen sc1\n"
"s_sub_u32 s48, s48, 1\n"
"s_cbranch_scc1 .L_end\n"
"s_waitcnt vmcnt(0)\n"
"v_cmp_ne_u64 vcc, v[16:17], v[12:13]\n"
"s_cbranch_vccnz .L_rlwc\n"
"s_add_u32 s66, s66, 1187840\n"
"s_add_u32 m0, s64, 0\n"
"s_nop 0\n"
"buffer_load_dwordx4 v2, s[32:35], s63 offen offset:0 sc1 lds\n"
"buffer_load_dwordx4 v2, s[32:35], s63 offen offset:1024 sc1 lds\n"
"buffer_load_dwordx4 v2, s[32:35], s63 offen offset:2048 sc1 lds\n"
"buffer_load_dwordx4 v2, s[32:35], s63 offen offset:3072 sc1 lds\n"
"s_add_u32 m0, s64, 4096\n"
"s_nop 0\n"
"buffer_load_dwordx4 v6, s[32:35], s63 offen offset:0 sc1 lds\n"
"s_barrier\n"
"ds_read_b128 v[128:131], v4 offset:0\n"
"ds_read_b128 v[132:135], v4 offset:1024\n"
"ds_read_b128 v[136:139], v4 offset:2048\n"
"ds_read_b128 v[140:143], v4 offset:3072\n"
"ds_read_b128 v[144:147], v4 offset:4096\n"
"ds_read_b128 v[148:151], v4 offset:5120\n"
"ds_read_b128 v[152:155], v4 offset:6144\n"
"ds_read_b128 v[156:159], v4 offset:7168\n"
"ds_read_b128 v[160:163], v4 offset:8192\n"
"ds_read_b128 v[164:167], v4 offset:9216\n"
"ds_read_b128 v[168:171], v4 offset:10240\n"
"ds_read_b128 v[172:175], v4 offset:11264\n"
"v_lshlrev_b32 v32, 16, v48\n"
"v_and_b32 v33, 0xffff0000, v48\n"
"v_lshlrev_b32 v34, 16, v49\n"
"v_and_b32 v35, 0xffff0000, v49\n"
"v_lshlrev_b32 v36, 16, v50\n"
"v_and_b32 v37, 0xffff0000, v50\n"
"v_lshlrev_b32 v38, 16, v51\n"
"v_and_b32 v39, 0xffff0000, v51\n"
"s_cmp_eq_u32 s26, 1\n"
"s_cbranch_scc1 .L_wv1\n"
"s_cmp_eq_u32 s26, 2\n"
"s_cbranch_scc1 .L_wv2\n"
"s_cmp_eq_u32 s26, 3\n"
"s_cbranch_scc1 .L_wv3\n"
".p2align 6\n"
".L_wv0:\n"
".L_wl_0:\n"
"s_waitcnt lgkmcnt(4)\n"
"v_mfma_f32_16x16x32_bf16 v[32:35], a[0:3], v[128:131], v[32:35]\n"
"ds_read_b128 v[176:179], v4 offset:12288\n"
"v_mfma_f32_16x16x32_bf16 v[32:35], a[8:11], v[132:135], v[32:35]\n"
"buffer_load_dwordx4 v6, s[32:35], s63 offen offset:1024 sc1 lds\n"
"v_mfma_f32_16x16x32_bf16 v[32:35], a[16:19], v[136:139], v[32:35]\n"
"ds_read_b128 v[180:183], v4 offset:13312\n"
"s_add_u32 s68, s68, 593920\n"
"v_mfma_f32_16x16x32_bf16 v[32:35], a[24:27], v[140:143], v[32:35]\n"
"v_cvt_pk_bf16_f32 v56, v40, v41\n"
"v_cvt_pk_bf16_f32 v57, v42, v43\n"
"v_mfma_f32_16x16x32_bf16 v[36:39], a[28:31], v[140:143], v[36:39]\n"
"ds_read_b128 v[184:187], v4 offset:14336\n"
"v_cvt_pk_bf16_f32 v58, v44, v45\n"
"v_mfma_f32_16x16x32_bf16 v[36:39], a[20:23], v[136:139], v[36:39]\n"
"v_cvt_pk_bf16_f32 v59, v46, v47\n"
"v_add_f32 v60, v40, v41\n"
"v_mfma_f32_16x16x32_bf16 v[36:39], a[12:15], v[132:135], v[36:39]\n"
"ds_read_b128 v[188:191], v4 offset:15360\n"
"v_mul_f32 v61, v40, v40\n"
"v_mfma_f32_16x16x32_bf16 v[36:39], a[4:7], v[128:131], v[36:39]\n"
"v_fmac_f32 v61, v41, v41\n"
"v_add_f32 v60, v60, v42\n"
"v_mfma_f32_16x16x32_bf16 v[36:39], a[36:39], v[144:147], v[36:39]\n"
"ds_read_b128 v[128:131], v4 offset:16384\n"
"v_fmac_f32 v61, v42, v42\n"
"v_mfma_f32_16x16x32_bf16 v[36:39], a[44:47], v[148:151], v[36:39]\n"
"buffer_store_dwordx4 v[56:59], v2, s[32:35], s68 offen sc1\n"
"v_mfma_f32_16x16x32_bf16 v[36:39], a[52:55], v[152:155], v[36:39]\n"
"ds_read_b128 v[132:135], v4 offset:17408\n"
"v_add_f32 v60, v60, v43\n"
"v_mfma_f32_16x16x32_bf16 v[36:39], a[60:63], v[156:159], v[36:39]\n"
"v_fmac_f32 v61, v43, v43\n"
"v_add_f32 v60, v60, v44\n"
"v_mfma_f32_16x16x32_bf16 v[32:35], a[56:59], v[156:159], v[32:35]\n"
"ds_read_b128 v[136:139], v4 offset:18432\n"
"v_fmac_f32 v61, v44, v44\n"
"v_mfma_f32_16x16x32_bf16 v[32:35], a[48:51], v[152:155], v[32:35]\n"
"v_add_f32 v60, v60, v45\n"
"v_fmac_f32 v61, v45, v45\n"
"v_mfma_f32_16x16x32_bf16 v[32:35], a[40:43], v[148:151], v[32:35]\n"
"ds_read_b128 v[140:143], v4 offset:19456\n"
"v_add_f32 v60, v60, v46\n"
"v_mfma_f32_16x16x32_bf16 v[32:35], a[32:35], v[144:147], v[32:35]\n"
"v_fmac_f32 v61, v46, v46\n"
"s_waitcnt lgkmcnt(4)\n"
"v_mfma_f32_16x16x32_bf16 v[32:35], a[64:67], v[160:163], v[32:35]\n"
"ds_read_b128 v[144:147], v4 offset:20480\n"
"v_add_f32 v60, v60, v47\n"
"v_mfma_f32_16x16x32_bf16 v[32:35], a[72:75], v[164:167], v[32:35]\n"
"v_fmac_f32 v61, v47, v47\n"
"v_mfma_f32_16x16x32_bf16 v[32:35], a[80:83], v[168:171], v[32:35]\n"
"ds_read_b128 v[148:151], v4 offset:21504\n"
"v_mfma_f32_16x16x32_bf16 v[32:35], a[88:91], v[172:175], v[32:35]\n"
"v_permlane16_swap_b32 v60, v61\n"
"v_mfma_f32_16x16x32_bf16 v[36:39], a[92:95], v[172:175], v[36:39]\n"
"ds_read_b128 v[152:155], v4 offset:22528\n"
"v_add_f32 v60, v60, v61\n"
"v_mfma_f32_16x16x32_bf16 v[36:39], a[84:87], v[168:171], v[36:39]\n"
"buffer_load_dwordx4 v[48:51], v2, s[32:35], s71 offen sc1\n"
"v_mfma_f32_16x16x32_bf16 v[36:39], a[76:79], v[164:167], v[36:39]\n"
"ds_read_b128 v[156:159], v4 offset:23552\n"
"s_add_u32 s71, s71, 593920\n"
"v_mfma_f32_16x16x32_bf16 v[36:39], a[68:71], v[160:163], v[36:39]\n"
"v_permlane32_swap_b32 v60, v61\n"
"v_mfma_f32_16x16x32_bf16 v[36:39], a[100:103], v[176:179], v[36:39]\n"
"ds_read_b128 v[160:163], v4 offset:24576\n"
"v_add_f32 v60, v60, v61\n"
"v_mfma_f32_16x16x32_bf16 v[36:39], a[108:111], v[180:183], v[36:39]\n"
"buffer_store_dword v60, v15, s[32:35], s68 offen offset:1024 sc1\n"
"v_mfma_f32_16x16x32_bf16 v[36:39], a[116:119], v[184:187], v[36:39]\n"
"ds_read_b128 v[164:167], v4 offset:25600\n"
"v_mfma_f32_16x16x32_bf16 v[36:39], a[124:127], v[188:191], v[36:39]\n"
"v_mfma_f32_16x16x32_bf16 v[32:35], a[120:123], v[188:191], v[32:35]\n"
"ds_read_b128 v[168:171], v4 offset:26624\n"
"v_mfma_f32_16x16x32_bf16 v[32:35], a[112:115], v[184:187], v[32:35]\n"
"buffer_load_dwordx4 v6, s[32:35], s63 offen offset:2048 sc1 lds\n"
"v_mfma_f32_16x16x32_bf16 v[32:35], a[104:107], v[180:183], v[32:35]\n"
"ds_read_b128 v[172:175], v4 offset:27648\n"
"v_mfma_f32_16x16x32_bf16 v[32:35], a[96:99], v[176:179], v[32:35]\n"
"s_waitcnt lgkmcnt(4)\n"
"v_mfma_f32_16x16x32_bf16 v[32:35], a[128:131], v[128:131], v[32:35]\n"
"ds_read_b128 v[176:179], v4 offset:28672\n"
"v_mfma_f32_16x16x32_bf16 v[32:35], a[136:139], v[132:135], v[32:35]\n"
"buffer_load_dwordx4 v6, s[32:35], s63 offen offset:3072 sc1 lds\n"
"v_mfma_f32_16x16x32_bf16 v[32:35], a[144:147], v[136:139], v[32:35]\n"
"ds_read_b128 v[180:183], v4 offset:29696\n"
"s_add_u32 s63, s63, 593920\n"
"v_mfma_f32_16x16x32_bf16 v[32:35], a[152:155], v[140:143], v[32:35]\n"
"v_mfma_f32_16x16x32_bf16 v[36:39], a[156:159], v[140:143], v[36:39]\n"
"ds_read_b128 v[184:187], v4 offset:30720\n"
"v_mfma_f32_16x16x32_bf16 v[36:39], a[148:151], v[136:139], v[36:39]\n"
"v_mfma_f32_16x16x32_bf16 v[36:39], a[140:143], v[132:135], v[36:39]\n"
"ds_read_b128 v[188:191], v4 offset:31744\n"
"v_mfma_f32_16x16x32_bf16 v[36:39], a[132:135], v[128:131], v[36:39]\n"
"s_waitcnt vmcnt(12)\n"
"s_barrier\n"
"v_mfma_f32_16x16x32_bf16 v[36:39], a[164:167], v[144:147], v[36:39]\n"
"ds_read_b128 v[128:131], v4 offset:32768\n"
"v_lshlrev_b32 v40, 16, v52\n"
"v_mfma_f32_16x16x32_bf16 v[36:39], a[172:175], v[148:151], v[36:39]\n"
"buffer_store_dwordx2 v[16:17], v2, s[92:95], s68 idxen sc1\n"
"v_mfma_f32_16x16x32_bf16 v[36:39], a[180:183], v[152:155], v[36:39]\n"
"ds_read_b128 v[132:135], v4 offset:33792\n"
"v_and_b32 v41, 0xffff0000, v52\n"
"v_mfma_f32_16x16x32_bf16 v[36:39], a[188:191], v[156:159], v[36:39]\n"
"v_lshlrev_b32 v42, 16, v53\n"
"v_and_b32 v43, 0xffff0000, v53\n"
"v_mfma_f32_16x16x32_bf16 v[32:35], a[184:187], v[156:159], v[32:35]\n"
"ds_read_b128 v[136:139], v4 offset:34816\n"
"s_add_u32 m0, s64, 32768\n"
"v_mfma_f32_16x16x32_bf16 v[32:35], a[176:179], v[152:155], v[32:35]\n"
"buffer_load_dwordx4 v2, s[32:35], s63 offen offset:0 sc1 lds\n"
"v_mfma_f32_16x16x32_bf16 v[32:35], a[168:171], v[148:151], v[32:35]\n"
"ds_read_b128 v[140:143], v4 offset:35840\n"
"v_lshlrev_b32 v44, 16, v54\n"
"v_mfma_f32_16x16x32_bf16 v[32:35], a[160:163], v[144:147], v[32:35]\n"
"v_and_b32 v45, 0xffff0000, v54\n"
"s_waitcnt lgkmcnt(4)\n"
"v_mfma_f32_16x16x32_bf16 v[32:35], a[192:195], v[160:163], v[32:35]\n"
"ds_read_b128 v[144:147], v4 offset:36864\n"
"v_lshlrev_b32 v46, 16, v55\n"
"v_mfma_f32_16x16x32_bf16 v[32:35], a[200:203], v[164:167], v[32:35]\n"
"buffer_load_dwordx4 v2, s[32:35], s63 offen offset:1024 sc1 lds\n"
"v_mfma_f32_16x16x32_bf16 v[32:35], a[208:211], v[168:171], v[32:35]\n"
"ds_read_b128 v[148:151], v4 offset:37888\n"
"v_and_b32 v47, 0xffff0000, v55\n"
"v_mfma_f32_16x16x32_bf16 v[32:35], a[216:219], v[172:175], v[32:35]\n"
"v_mfma_f32_16x16x32_bf16 v[36:39], a[220:223], v[172:175], v[36:39]\n"
"ds_read_b128 v[152:155], v4 offset:38912\n"
"v_mfma_f32_16x16x32_bf16 v[36:39], a[212:215], v[168:171], v[36:39]\n"
"buffer_load_dwordx4 v2, s[32:35], s63 offen offset:2048 sc1 lds\n"
"v_mfma_f32_16x16x32_bf16 v[36:39], a[204:207], v[164:167], v[36:39]\n"
"ds_read_b128 v[156:159], v4 offset:39936\n"
"v_mfma_f32_16x16x32_bf16 v[36:39], a[196:199], v[160:163], v[36:39]\n"
"v_mfma_f32_16x16x32_bf16 v[36:39], a[228:231], v[176:179], v[36:39]\n"
"ds_read_b128 v[160:163], v4 offset:40960\n"
"v_mfma_f32_16x16x32_bf16 v[36:39], a[236:239], v[180:183], v[36:39]\n"
"buffer_load_dwordx4 v2, s[32:35], s63 offen offset:3072 sc1 lds\n"
"v_mfma_f32_16x16x32_bf16 v[36:39], a[244:247], v[184:187], v[36:39]\n"
"ds_read_b128 v[164:167], v4 offset:41984\n"
"v_mfma_f32_16x16x32_bf16 v[36:39], a[252:255], v[188:191], v[36:39]\n"
"v_mfma_f32_16x16x32_bf16 v[32:35], a[248:251], v[188:191], v[32:35]\n"
"ds_read_b128 v[168:171], v4 offset:43008\n"
"s_add_u32 m0, s64, 36864\n"
"v_mfma_f32_16x16x32_bf16 v[32:35], a[240:243], v[184:187], v[32:35]\n"
"buffer_load_dwordx4 v6, s[32:35], s63 offen offset:0 sc1 lds\n"
"v_mfma_f32_16x16x32_bf16 v[32:35], a[232:235], v[180:183], v[32:35]\n"
"ds_read_b128 v[172:175], v4 offset:44032\n"
"v_mfma_f32_16x16x32_bf16 v[32:35], a[224:227], v[176:179], v[32:35]\n"
"s_waitcnt lgkmcnt(4)\n"
"v_mfma_f32_16x16x32_bf16 v[40:43], a[0:3], v[128:131], v[40:43]\n"
"ds_read_b128 v[176:179], v4 offset:45056\n"
"v_mfma_f32_16x16x32_bf16 v[40:43], a[8:11], v[132:135], v[40:43]\n"
"buffer_load_dwordx4 v6, s[32:35], s63 offen offset:1024 sc1 lds\n"
"v_mfma_f32_16x16x32_bf16 v[40:43], a[16:19], v[136:139], v[40:43]\n"
"ds_read_b128 v[180:183], v4 offset:46080\n"
"s_add_u32 s68, s68, 593920\n"
"v_mfma_f32_16x16x32_bf16 v[40:43], a[24:27], v[140:143], v[40:43]\n"
"v_cvt_pk_bf16_f32 v56, v32, v33\n"
"v_cvt_pk_bf16_f32 v57, v34, v35\n"
"v_mfma_f32_16x16x32_bf16 v[44:47], a[28:31], v[140:143], v[44:47]\n"
"ds_read_b128 v[184:187], v4 offset:47104\n"
"v_cvt_pk_bf16_f32 v58, v36, v37\n"
"v_mfma_f32_16x16x32_bf16 v[44:47], a[20:23], v[136:139], v[44:47]\n"
"v_cvt_pk_bf16_f32 v59, v38, v39\n"
"v_add_f32 v60, v32, v33\n"
"v_mfma_f32_16x16x32_bf16 v[44:47], a[12:15], v[132:135], v[44:47]\n"
"ds_read_b128 v[188:191], v4 offset:48128\n"
"v_mul_f32 v61, v32, v32\n"
"v_mfma_f32_16x16x32_bf16 v[44:47], a[4:7], v[128:131], v[44:47]\n"
"v_fmac_f32 v61, v33, v33\n"
"v_add_f32 v60, v60, v34\n"
"v_mfma_f32_16x16x32_bf16 v[44:47], a[36:39], v[144:147], v[44:47]\n"
"ds_read_b128 v[128:131], v4 offset:49152\n"
"v_fmac_f32 v61, v34, v34\n"
"v_mfma_f32_16x16x32_bf16 v[44:47], a[44:47], v[148:151], v[44:47]\n"
"buffer_store_dwordx4 v[56:59], v2, s[32:35], s68 offen sc1\n"
"v_mfma_f32_16x16x32_bf16 v[44:47], a[52:55], v[152:155], v[44:47]\n"
"ds_read_b128 v[132:135], v4 offset:50176\n"
"v_add_f32 v60, v60, v35\n"
"v_mfma_f32_16x16x32_bf16 v[44:47], a[60:63], v[156:159], v[44:47]\n"
"v_fmac_f32 v61, v35, v35\n"
"v_add_f32 v60, v60, v36\n"
"v_mfma_f32_16x16x32_bf16 v[40:43], a[56:59], v[156:159], v[40:43]\n"
"ds_read_b128 v[136:139], v4 offset:51200\n"
"v_fmac_f32 v61, v36, v36\n"
"v_mfma_f32_16x16x32_bf16 v[40:43], a[48:51], v[152:155], v[40:43]\n"
"v_add_f32 v60, v60, v37\n"
"v_fmac_f32 v61, v37, v37\n"
"v_mfma_f32_16x16x32_bf16 v[40:43], a[40:43], v[148:151], v[40:43]\n"
"ds_read_b128 v[140:143], v4 offset:52224\n"
"v_add_f32 v60, v60, v38\n"
"v_mfma_f32_16x16x32_bf16 v[40:43], a[32:35], v[144:147], v[40:43]\n"
"v_fmac_f32 v61, v38, v38\n"
"s_waitcnt lgkmcnt(4)\n"
"v_mfma_f32_16x16x32_bf16 v[40:43], a[64:67], v[160:163], v[40:43]\n"
"ds_read_b128 v[144:147], v4 offset:53248\n"
"v_add_f32 v60, v60, v39\n"
"v_mfma_f32_16x16x32_bf16 v[40:43], a[72:75], v[164:167], v[40:43]\n"
"v_fmac_f32 v61, v39, v39\n"
"v_mfma_f32_16x16x32_bf16 v[40:43], a[80:83], v[168:171], v[40:43]\n"
"ds_read_b128 v[148:151], v4 offset:54272\n"
"v_mfma_f32_16x16x32_bf16 v[40:43], a[88:91], v[172:175], v[40:43]\n"
"v_permlane16_swap_b32 v60, v61\n"
"v_mfma_f32_16x16x32_bf16 v[44:47], a[92:95], v[172:175], v[44:47]\n"
"ds_read_b128 v[152:155], v4 offset:55296\n"
"v_add_f32 v60, v60, v61\n"
"v_mfma_f32_16x16x32_bf16 v[44:47], a[84:87], v[168:171], v[44:47]\n"
"buffer_load_dwordx4 v[52:55], v2, s[32:35], s71 offen sc1\n"
"v_mfma_f32_16x16x32_bf16 v[44:47], a[76:79], v[164:167], v[44:47]\n"
"ds_read_b128 v[156:159], v4 offset:56320\n"
"s_add_u32 s71, s71, 593920\n"
"v_mfma_f32_16x16x32_bf16 v[44:47], a[68:71], v[160:163], v[44:47]\n"
"v_permlane32_swap_b32 v60, v61\n"
"v_mfma_f32_16x16x32_bf16 v[44:47], a[100:103], v[176:179], v[44:47]\n"
"ds_read_b128 v[160:163], v4 offset:57344\n"
"v_add_f32 v60, v60, v61\n"
"v_mfma_f32_16x16x32_bf16 v[44:47], a[108:111], v[180:183], v[44:47]\n"
"buffer_store_dword v60, v15, s[32:35], s68 offen offset:1024 sc1\n"
"v_mfma_f32_16x16x32_bf16 v[44:47], a[116:119], v[184:187], v[44:47]\n"
"ds_read_b128 v[164:167], v4 offset:58368\n"
"v_mfma_f32_16x16x32_bf16 v[44:47], a[124:127], v[188:191], v[44:47]\n"
"v_mfma_f32_16x16x32_bf16 v[40:43], a[120:123], v[188:191], v[40:43]\n"
"ds_read_b128 v[168:171], v4 offset:59392\n"
"v_mfma_f32_16x16x32_bf16 v[40:43], a[112:115], v[184:187], v[40:43]\n"
"buffer_load_dwordx4 v6, s[32:35], s63 offen offset:2048 sc1 lds\n"
"v_mfma_f32_16x16x32_bf16 v[40:43], a[104:107], v[180:183], v[40:43]\n"
"ds_read_b128 v[172:175], v4 offset:60416\n"
"v_mfma_f32_16x16x32_bf16 v[40:43], a[96:99], v[176:179], v[40:43]\n"
"s_waitcnt lgkmcnt(4)\n"
"v_mfma_f32_16x16x32_bf16 v[40:43], a[128:131], v[128:131], v[40:43]\n"
"ds_read_b128 v[176:179], v4 offset:61440\n"
"v_mfma_f32_16x16x32_bf16 v[40:43], a[136:139], v[132:135], v[40:43]\n"
"buffer_load_dwordx4 v6, s[32:35], s63 offen offset:3072 sc1 lds\n"
"v_mfma_f32_16x16x32_bf16 v[40:43], a[144:147], v[136:139], v[40:43]\n"
"ds_read_b128 v[180:183], v4 offset:62464\n"
"s_add_u32 s63, s63, 593920\n"
"v_mfma_f32_16x16x32_bf16 v[40:43], a[152:155], v[140:143], v[40:43]\n"
"s_xor_b32 s64, s64, s65\n"
"v_mfma_f32_16x16x32_bf16 v[44:47], a[156:159], v[140:143], v[44:47]\n"
"ds_read_b128 v[184:187], v4 offset:63488\n"
"v_mfma_f32_16x16x32_bf16 v[44:47], a[148:151], v[136:139], v[44:47]\n"
"v_mfma_f32_16x16x32_bf16 v[44:47], a[140:143], v[132:135], v[44:47]\n"
"ds_read_b128 v[188:191], v4 offset:64512\n"
"v_xor_b32 v4, 65536, v4\n"
"v_mfma_f32_16x16x32_bf16 v[44:47], a[132:135], v[128:131], v[44:47]\n"
"s_waitcnt vmcnt(12)\n"
"s_barrier\n"
"v_mfma_f32_16x16x32_bf16 v[44:47], a[164:167], v[144:147], v[44:47]\n"
"ds_read_b128 v[128:131], v4 offset:0\n"
"v_lshlrev_b32 v32, 16, v48\n"
"v_mfma_f32_16x16x32_bf16 v[44:47], a[172:175], v[148:151], v[44:47]\n"
"buffer_store_dwordx2 v[16:17], v2, s[92:95], s68 idxen sc1\n"
"v_mfma_f32_16x16x32_bf16 v[44:47], a[180:183], v[152:155], v[44:47]\n"
"ds_read_b128 v[132:135], v4 offset:1024\n"
"v_and_b32 v33, 0xffff0000, v48\n"
"v_mfma_f32_16x16x32_bf16 v[44:47], a[188:191], v[156:159], v[44:47]\n"
"v_lshlrev_b32 v34, 16, v49\n"
"v_and_b32 v35, 0xffff0000, v49\n"
"v_mfma_f32_16x16x32_bf16 v[40:43], a[184:187], v[156:159], v[40:43]\n"
"ds_read_b128 v[136:139], v4 offset:2048\n"
"s_add_u32 m0, s64, 0\n"
"v_mfma_f32_16x16x32_bf16 v[40:43], a[176:179], v[152:155], v[40:43]\n"
"buffer_load_dwordx4 v2, s[32:35], s63 offen offset:0 sc1 lds\n"
"v_mfma_f32_16x16x32_bf16 v[40:43], a[168:171], v[148:151], v[40:43]\n"
"ds_read_b128 v[140:143], v4 offset:3072\n"
"v_lshlrev_b32 v36, 16, v50\n"
"v_mfma_f32_16x16x32_bf16 v[40:43], a[160:163], v[144:147], v[40:43]\n"
"v_and_b32 v37, 0xffff0000, v50\n"
"s_waitcnt lgkmcnt(4)\n"
"v_mfma_f32_16x16x32_bf16 v[40:43], a[192:195], v[160:163], v[40:43]\n"
"ds_read_b128 v[144:147], v4 offset:4096\n"
"v_lshlrev_b32 v38, 16, v51\n"
"v_mfma_f32_16x16x32_bf16 v[40:43], a[200:203], v[164:167], v[40:43]\n"
"buffer_load_dwordx4 v2, s[32:35], s63 offen offset:1024 sc1 lds\n"
"v_mfma_f32_16x16x32_bf16 v[40:43], a[208:211], v[168:171], v[40:43]\n"
"ds_read_b128 v[148:151], v4 offset:5120\n"
"v_and_b32 v39, 0xffff0000, v51\n"
"v_mfma_f32_16x16x32_bf16 v[40:43], a[216:219], v[172:175], v[40:43]\n"
"v_mfma_f32_16x16x32_bf16 v[44:47], a[220:223], v[172:175], v[44:47]\n"
"ds_read_b128 v[152:155], v4 offset:6144\n"
"v_mfma_f32_16x16x32_bf16 v[44:47], a[212:215], v[168:171], v[44:47]\n"
"buffer_load_dwordx4 v2, s[32:35], s63 offen offset:2048 sc1 lds\n"
"v_mfma_f32_16x16x32_bf16 v[44:47], a[204:207], v[164:167], v[44:47]\n"
"ds_read_b128 v[156:159], v4 offset:7168\n"
"v_mfma_f32_16x16x32_bf16 v[44:47], a[196:199], v[160:163], v[44:47]\n"
"v_mfma_f32_16x16x32_bf16 v[44:47], a[228:231], v[176:179], v[44:47]\n"
"ds_read_b128 v[160:163], v4 offset:8192\n"
"v_mfma_f32_16x16x32_bf16 v[44:47], a[236:239], v[180:183], v[44:47]\n"
"buffer_load_dwordx4 v2, s[32:35], s63 offen offset:3072 sc1 lds\n"
"v_mfma_f32_16x16x32_bf16 v[44:47], a[244:247], v[184:187], v[44:47]\n"
"ds_read_b128 v[164:167], v4 offset:9216\n"
"v_mfma_f32_16x16x32_bf16 v[44:47], a[252:255], v[188:191], v[44:47]\n"
"v_mfma_f32_16x16x32_bf16 v[40:43], a[248:251], v[188:191], v[40:43]\n"
"ds_read_b128 v[168:171], v4 offset:10240\n"
"s_add_u32 m0, s64, 4096\n"
"v_mfma_f32_16x16x32_bf16 v[40:43], a[240:243], v[184:187], v[40:43]\n"
"buffer_load_dwordx4 v6, s[32:35], s63 offen offset:0 sc1 lds\n"
"v_mfma_f32_16x16x32_bf16 v[40:43], a[232:235], v[180:183], v[40:43]\n"
"ds_read_b128 v[172:175], v4 offset:11264\n"
"s_sub_u32 s62, s62, 1\n"
"v_mfma_f32_16x16x32_bf16 v[40:43], a[224:227], v[176:179], v[40:43]\n"
"s_cbranch_scc0 .L_wl_0\n"
"s_nop 1\n"
"s_add_u32 s68, s68, 593920\n"
"s_nop 3\n"
"v_cvt_pk_bf16_f32 v56, v40, v41\n"
"v_cvt_pk_bf16_f32 v57, v42, v43\n"
"v_cvt_pk_bf16_f32 v58, v44, v45\n"
"v_cvt_pk_bf16_f32 v59, v46, v47\n"
"buffer_store_dwordx4 v[56:59], v2, s[32:35], s68 offen sc1\n"
"v_add_f32 v60, v40, v41\n"
"v_mul_f32 v61, v40, v40\n"
"v_fmac_f32 v61, v41, v41\n"
"v_add_f32 v60, v60, v42\n"
"v_fmac_f32 v61, v42, v42\n"
"v_add_f32 v60, v60, v43\n"
"v_fmac_f32 v61, v43, v43\n"
"v_add_f32 v60, v60, v44\n"
"v_fmac_f32 v61, v44, v44\n"
"v_add_f32 v60, v60, v45\n"
"v_fmac_f32 v61, v45, v45\n"
"v_add_f32 v60, v60, v46\n"
"v_fmac_f32 v61, v46, v46\n"
"v_add_f32 v60, v60, v47\n"
"v_fmac_f32 v61, v47, v47\n"
"s_nop 1\n"
"v_permlane16_swap_b32 v60, v61\n"
"v_add_f32 v60, v60, v61\n"
"s_nop 1\n"
"v_permlane32_swap_b32 v60, v61\n"
"v_add_f32 v60, v60, v61\n"
"buffer_store_dword v60, v15, s[32:35], s68 offen offset:1024 sc1\n"
"s_waitcnt vmcnt(10)\n"
"s_barrier\n"
"buffer_store_dwordx2 v[16:17], v2, s[92:95], s68 idxen sc1\n"
"s_add_u32 s68, s68, 593920\n"
"s_waitcnt vmcnt(1)\n"
"s_barrier\n"
"buffer_store_dwordx2 v[16:17], v2, s[92:95], s68 idxen sc1\n"
"s_branch .L_end\n"
".p2align 6\n"
".L_wv1:\n"
".L_wl_1:\n"
"s_waitcnt lgkmcnt(4)\n"
"v_mfma_f32_16x16x32_bf16 v[32:35], a[0:3], v[128:131], v[32:35]\n"
"ds_read_b128 v[176:179], v4 offset:12288\n"
"v_mfma_f32_16x16x32_bf16 v[32:35], a[8:11], v[132:135], v[32:35]\n"
"buffer_load_dwordx4 v6, s[32:35], s63 offen offset:1024 sc1 lds\n"
"v_mfma_f32_16x16x32_bf16 v[32:35], a[16:19], v[136:139], v[32:35]\n"
"ds_read_b128 v[180:183], v4 offset:13312\n"
"s_add_u32 s68, s68, 593920\n"
"v_mfma_f32_16x16x32_bf16 v[32:35], a[24:27], v[140:143], v[32:35]\n"
"v_cvt_pk_bf16_f32 v56, v40, v41\n"
"v_cvt_pk_bf16_f32 v57, v42, v43\n"
"v_mfma_f32_16x16x32_bf16 v[36:39], a[28:31], v[140:143], v[36:39]\n"
"ds_read_b128 v[184:187], v4 offset:14336\n"
"v_cvt_pk_bf16_f32 v58, v44, v45\n"
"v_mfma_f32_16x16x32_bf16 v[36:39], a[20:23], v[136:139], v[36:39]\n"
"v_cvt_pk_bf16_f32 v59, v46, v47\n"
"v_add_f32 v60, v40, v41\n"
"v_mfma_f32_16x16x32_bf16 v[36:39], a[12:15], v[132:135], v[36:39]\n"
"ds_read_b128 v[188:191], v4 offset:15360\n"
"v_mul_f32 v61, v40, v40\n"
"v_mfma_f32_16x16x32_bf16 v[36:39], a[4:7], v[128:131], v[36:39]\n"
"v_fmac_f32 v61, v41, v41\n"
"v_add_f32 v60, v60, v42\n"
"v_mfma_f32_16x16x32_bf16 v[36:39], a[36:39], v[144:147], v[36:39]\n"
"ds_read_b128 v[128:131], v4 offset:16384\n"
"v_fmac_f32 v61, v42, v42\n"
"v_mfma_f32_16x16x32_bf16 v[36:39], a[44:47], v[148:151], v[36:39]\n"
"buffer_store_dwordx4 v[56:59], v2, s[32:35], s68 offen sc1\n"
"v_mfma_f32_16x16x32_bf16 v[36:39], a[52:55], v[152:155], v[36:39]\n"
"ds_read_b128 v[132:135], v4 offset:17408\n"
"v_add_f32 v60, v60, v43\n"
"v_mfma_f32_16x16x32_bf16 v[36:39], a[60:63], v[156:159], v[36:39]\n"
"v_fmac_f32 v61, v43, v43\n"
"v_add_f32 v60, v60, v44\n"
"v_mfma_f32_16x16x32_bf16 v[32:35], a[56:59], v[156:159], v[32:35]\n"
"ds_read_b128 v[136:139], v4 offset:18432\n"
"v_fmac_f32 v61, v44, v44\n"
"v_mfma_f32_16x16x32_bf16 v[32:35], a[48:51], v[152:155], v[32:35]\n"
"v_add_f32 v60, v60, v45\n"
"v_fmac_f32 v61, v45, v45\n"
"v_mfma_f32_16x16x32_bf16 v[32:35], a[40:43], v[148:151], v[32:35]\n"
"ds_read_b128 v[140:143], v4 offset:19456\n"
"v_add_f32 v60, v60, v46\n"
"v_mfma_f32_16x16x32_bf16 v[32:35], a[32:35], v[144:147], v[32:35]\n"
"v_fmac_f32 v61, v46, v46\n"
"s_waitcnt lgkmcnt(4)\n"
"v_mfma_f32_16x16x32_bf16 v[32:35], a[64:67], v[160:163], v[32:35]\n"
"ds_read_b128 v[144:147], v4 offset:20480\n"
"v_add_f32 v60, v60, v47\n"
"v_mfma_f32_16x16x32_bf16 v[32:35], a[72:75], v[164:167], v[32:35]\n"
"v_fmac_f32 v61, v47, v47\n"
"v_mfma_f32_16x16x32_bf16 v[32:35], a[80:83], v[168:171], v[32:35]\n"
"ds_read_b128 v[148:151], v4 offset:21504\n"
"v_mfma_f32_16x16x32_bf16 v[32:35], a[88:91], v[172:175], v[32:35]\n"
"v_permlane16_swap_b32 v60, v61\n"
"v_mfma_f32_16x16x32_bf16 v[36:39], a[92:95], v[172:175], v[36:39]\n"
"ds_read_b128 v[152:155], v4 offset:22528\n"
"v_add_f32 v60, v60, v61\n"
"v_mfma_f32_16x16x32_bf16 v[36:39], a[84:87], v[168:171], v[36:39]\n"
"buffer_load_dwordx4 v[48:51], v2, s[32:35], s71 offen sc1\n"
"v_mfma_f32_16x16x32_bf16 v[36:39], a[76:79], v[164:167], v[36:39]\n"
"ds_read_b128 v[156:159], v4 offset:23552\n"
"s_add_u32 s71, s71, 593920\n"
"v_mfma_f32_16x16x32_bf16 v[36:39], a[68:71], v[160:163], v[36:39]\n"
"v_permlane32_swap_b32 v60, v61\n"
"v_mfma_f32_16x16x32_bf16 v[36:39], a[100:103], v[176:179], v[36:39]\n"
"ds_read_b128 v[160:163], v4 offset:24576\n"
"v_add_f32 v60, v60, v61\n"
"v_mfma_f32_16x16x32_bf16 v[36:39], a[108:111], v[180:183], v[36:39]\n"
"buffer_store_dword v60, v15, s[32:35], s68 offen offset:1024 sc1\n"
"v_mfma_f32_16x16x32_bf16 v[36:39], a[116:119], v[184:187], v[36:39]\n"
"ds_read_b128 v[164:167], v4 offset:25600\n"
"v_mfma_f32_16x16x32_bf16 v[36:39], a[124:127], v[188:191], v[36:39]\n"
"v_mfma_f32_16x16x32_bf16 v[32:35], a[120:123], v[188:191], v[32:35]\n"
"ds_read_b128 v[168:171], v4 offset:26624\n"
"v_mfma_f32_16x16x32_bf16 v[32:35], a[112:115], v[184:187], v[32:35]\n"
"buffer_load_dwordx4 v6, s[32:35], s63 offen offset:2048 sc1 lds\n"
"v_mfma_f32_16x16x32_bf16 v[32:35], a[104:107], v[180:183], v[32:35]\n"
"ds_read_b128 v[172:175], v4 offset:27648\n"
"v_mfma_f32_16x16x32_bf16 v[32:35], a[96:99], v[176:179], v[32:35]\n"
"s_waitcnt lgkmcnt(4)\n"
"v_mfma_f32_16x16x32_bf16 v[32:35], a[128:131], v[128:131], v[32:35]\n"
"ds_read_b128 v[176:179], v4 offset:28672\n"
"v_mfma_f32_16x16x32_bf16 v[32:35], a[136:139], v[132:135], v[32:35]\n"
"buffer_load_dwordx4 v6, s[32:35], s63 offen offset:3072 sc1 lds\n"
"v_mfma_f32_16x16x32_bf16 v[32:35], a[144:147], v[136:139], v[32:35]\n"
"ds_read_b128 v[180:183], v4 offset:29696\n"
"s_add_u32 s63, s63, 593920\n"
"v_mfma_f32_16x16x32_bf16 v[32:35], a[152:155], v[140:143], v[32:35]\n"
"v_mfma_f32_16x16x32_bf16 v[36:39], a[156:159], v[140:143], v[36:39]\n"
"ds_read_b128 v[184:187], v4 offset:30720\n"
"v_mfma_f32_16x16x32_bf16 v[36:39], a[148:151], v[136:139], v[36:39]\n"
"buffer_load_dwordx2 v[12:13], v14, s[32:35], s66 offen sc1\n"
"v_mfma_f32_16x16x32_bf16 v[36:39], a[140:143], v[132:135], v[36:39]\n"
"s_waitcnt vmcnt(12)\n"
"s_barrier\n"
"v_mfma_f32_16x16x32_bf16 v[36:39], a[132:135], v[128:131], v[36:39]\n"
"ds_read_b128 v[188:191], v4 offset:31744\n"
"v_mfma_f32_16x16x32_bf16 v[36:39], a[164:167], v[144:147], v[36:39]\n"
"ds_read_b128 v[128:131], v4 offset:32768\n"
"v_lshlrev_b32 v40, 16, v52\n"
"v_mfma_f32_16x16x32_bf16 v[36:39], a[172:175], v[148:151], v[36:39]\n"
"v_and_b32 v41, 0xffff0000, v52\n"
"v_lshlrev_b32 v42, 16, v53\n"
"v_mfma_f32_16x16x32_bf16 v[36:39], a[180:183], v[152:155], v[36:39]\n"
"ds_read_b128 v[132:135], v4 offset:33792\n"
"v_and_b32 v43, 0xffff0000, v53\n"
"v_mfma_f32_16x16x32_bf16 v[36:39], a[188:191], v[156:159], v[36:39]\n"
"v_lshlrev_b32 v44, 16, v54\n"
"v_and_b32 v45, 0xffff0000, v54\n"
"v_mfma_f32_16x16x32_bf16 v[32:35], a[184:187], v[156:159], v[32:35]\n"
"ds_read_b128 v[136:139], v4 offset:34816\n"
"s_add_u32 m0, s64, 32768\n"
"v_mfma_f32_16x16x32_bf16 v[32:35], a[176:179], v[152:155], v[32:35]\n"
"buffer_load_dwordx4 v2, s[32:35], s63 offen offset:0 sc1 lds\n"
"v_mfma_f32_16x16x32_bf16 v[32:35], a[168:171], v[148:151], v[32:35]\n"
"ds_read_b128 v[140:143], v4 offset:35840\n"
"v_lshlrev_b32 v46, 16, v55\n"
"v_mfma_f32_16x16x32_bf16 v[32:35], a[160:163], v[144:147], v[32:35]\n"
"v_and_b32 v47, 0xffff0000, v55\n"
"s_waitcnt lgkmcnt(4)\n"
"v_mfma_f32_16x16x32_bf16 v[32:35], a[192:195], v[160:163], v[32:35]\n"
"ds_read_b128 v[144:147], v4 offset:36864\n"
"v_mfma_f32_16x16x32_bf16 v[32:35], a[200:203], v[164:167], v[32:35]\n"
"buffer_load_dwordx4 v2, s[32:35], s63 offen offset:1024 sc1 lds\n"
"v_mfma_f32_16x16x32_bf16 v[32:35], a[208:211], v[168:171], v[32:35]\n"
"ds_read_b128 v[148:151], v4 offset:37888\n"
"v_mfma_f32_16x16x32_bf16 v[32:35], a[216:219], v[172:175], v[32:35]\n"
"v_mfma_f32_16x16x32_bf16 v[36:39], a[220:223], v[172:175], v[36:39]\n"
"ds_read_b128 v[152:155], v4 offset:38912\n"
"v_mfma_f32_16x16x32_bf16 v[36:39], a[212:215], v[168:171], v[36:39]\n"
"buffer_load_dwordx4 v2, s[32:35], s63 offen offset:2048 sc1 lds\n"
"v_mfma_f32_16x16x32_bf16 v[36:39], a[204:207], v[164:167], v[36:39]\n"
"ds_read_b128 v[156:159], v4 offset:39936\n"
"v_mfma_f32_16x16x32_bf16 v[36:39], a[196:199], v[160:163], v[36:39]\n"
"v_mfma_f32_16x16x32_bf16 v[36:39], a[228:231], v[176:179], v[36:39]\n"
"ds_read_b128 v[160:163], v4 offset:40960\n"
"v_mfma_f32_16x16x32_bf16 v[36:39], a[236:239], v[180:183], v[36:39]\n"
"buffer_load_dwordx4 v2, s[32:35], s63 offen offset:3072 sc1 lds\n"
"v_mfma_f32_16x16x32_bf16 v[36:39], a[244:247], v[184:187], v[36:39]\n"
"ds_read_b128 v[164:167], v4 offset:41984\n"
"v_mfma_f32_16x16x32_bf16 v[36:39], a[252:255], v[188:191], v[36:39]\n"
"v_mfma_f32_16x16x32_bf16 v[32:35], a[248:251], v[188:191], v[32:35]\n"
"ds_read_b128 v[168:171], v4 offset:43008\n"
"s_add_u32 m0, s64, 36864\n"
"v_mfma_f32_16x16x32_bf16 v[32:35], a[240:243], v[184:187], v[32:35]\n"
"buffer_load_dwordx4 v6, s[32:35], s63 offen offset:0 sc1 lds\n"
"v_mfma_f32_16x16x32_bf16 v[32:35], a[232:235], v[180:183], v[32:35]\n"
"ds_read_b128 v[172:175], v4 offset:44032\n"
"v_mfma_f32_16x16x32_bf16 v[32:35], a[224:227], v[176:179], v[32:35]\n"
"s_waitcnt lgkmcnt(4)\n"
"v_mfma_f32_16x16x32_bf16 v[40:43], a[0:3], v[128:131], v[40:43]\n"
"ds_read_b128 v[176:179], v4 offset:45056\n"
"v_mfma_f32_16x16x32_bf16 v[40:43], a[8:11], v[132:135], v[40:43]\n"
"buffer_load_dwordx4 v6, s[32:35], s63 offen offset:1024 sc1 lds\n"
"v_mfma_f32_16x16x32_bf16 v[40:43], a[16:19], v[136:139], v[40:43]\n"
"ds_read_b128 v[180:183], v4 offset:46080\n"
"s_add_u32 s68, s68, 593920\n"
"v_mfma_f32_16x16x32_bf16 v[40:43], a[24:27], v[140:143], v[40:43]\n"
"v_cvt_pk_bf16_f32 v56, v32, v33\n"
"v_cvt_pk_bf16_f32 v57, v34, v35\n"
"v_mfma_f32_16x16x32_bf16 v[44:47], a[28:31], v[140:143], v[44:47]\n"
"ds_read_b128 v[184:187], v4 offset:47104\n"
"v_cvt_pk_bf16_f32 v58, v36, v37\n"
"v_mfma_f32_16x16x32_bf16 v[44:47], a[20:23], v[136:139], v[44:47]\n"
"v_cvt_pk_bf16_f32 v59, v38, v39\n"
"v_add_f32 v60, v32, v33\n"
"v_mfma_f32_16x16x32_bf16 v[44:47], a[12:15], v[132:135], v[44:47]\n"
"ds_read_b128 v[188:191], v4 offset:48128\n"
"v_mul_f32 v61, v32, v32\n"
"v_mfma_f32_16x16x32_bf16 v[44:47], a[4:7], v[128:131], v[44:47]\n"
"v_fmac_f32 v61, v33, v33\n"
"v_add_f32 v60, v60, v34\n"
"v_mfma_f32_16x16x32_bf16 v[44:47], a[36:39], v[144:147], v[44:47]\n"
"ds_read_b128 v[128:131], v4 offset:49152\n"
"v_fmac_f32 v61, v34, v34\n"
"v_mfma_f32_16x16x32_bf16 v[44:47], a[44:47], v[148:151], v[44:47]\n"
"buffer_store_dwordx4 v[56:59], v2, s[32:35], s68 offen sc1\n"
"v_mfma_f32_16x16x32_bf16 v[44:47], a[52:55], v[152:155], v[44:47]\n"
"ds_read_b128 v[132:135], v4 offset:50176\n"
"v_add_f32 v60, v60, v35\n"
"v_mfma_f32_16x16x32_bf16 v[44:47], a[60:63], v[156:159], v[44:47]\n"
"v_fmac_f32 v61, v35, v35\n"
"v_add_f32 v60, v60, v36\n"
"v_mfma_f32_16x16x32_bf16 v[40:43], a[56:59], v[156:159], v[40:43]\n"
"ds_read_b128 v[136:139], v4 offset:51200\n"
"v_fmac_f32 v61, v36, v36\n"
"v_mfma_f32_16x16x32_bf16 v[40:43], a[48:51], v[152:155], v[40:43]\n"
"v_add_f32 v60, v60, v37\n"
"v_fmac_f32 v61, v37, v37\n"
"v_mfma_f32_16x16x32_bf16 v[40:43], a[40:43], v[148:151], v[40:43]\n"
"ds_read_b128 v[140:143], v4 offset:52224\n"
"v_add_f32 v60, v60, v38\n"
"v_mfma_f32_16x16x32_bf16 v[40:43], a[32:35], v[144:147], v[40:43]\n"
"v_fmac_f32 v61, v38, v38\n"
"s_waitcnt lgkmcnt(4)\n"
"v_mfma_f32_16x16x32_bf16 v[40:43], a[64:67], v[160:163], v[40:43]\n"
"ds_read_b128 v[144:147], v4 offset:53248\n"
"v_add_f32 v60, v60, v39\n"
"v_mfma_f32_16x16x32_bf16 v[40:43], a[72:75], v[164:167], v[40:43]\n"
"v_fmac_f32 v61, v39, v39\n"
"v_mfma_f32_16x16x32_bf16 v[40:43], a[80:83], v[168:171], v[40:43]\n"
"ds_read_b128 v[148:151], v4 offset:54272\n"
"v_mfma_f32_16x16x32_bf16 v[40:43], a[88:91], v[172:175], v[40:43]\n"
"v_permlane16_swap_b32 v60, v61\n"
"v_mfma_f32_16x16x32_bf16 v[44:47], a[92:95], v[172:175], v[44:47]\n"
"ds_read_b128 v[152:155], v4 offset:55296\n"
"v_add_f32 v60, v60, v61\n"
"v_mfma_f32_16x16x32_bf16 v[44:47], a[84:87], v[168:171], v[44:47]\n"
"buffer_load_dwordx4 v[52:55], v2, s[32:35], s71 offen sc1\n"
"v_mfma_f32_16x16x32_bf16 v[44:47], a[76:79], v[164:167], v[44:47]\n"
"ds_read_b128 v[156:159], v4 offset:56320\n"
"s_add_u32 s71, s71, 593920\n"
"v_mfma_f32_16x16x32_bf16 v[44:47], a[68:71], v[160:163], v[44:47]\n"
"v_permlane32_swap_b32 v60, v61\n"
"v_mfma_f32_16x16x32_bf16 v[44:47], a[100:103], v[176:179], v[44:47]\n"
"ds_read_b128 v[160:163], v4 offset:57344\n"
"v_add_f32 v60, v60, v61\n"
"v_mfma_f32_16x16x32_bf16 v[44:47], a[108:111], v[180:183], v[44:47]\n"
"buffer_store_dword v60, v15, s[32:35], s68 offen offset:1024 sc1\n"
"v_mfma_f32_16x16x32_bf16 v[44:47], a[116:119], v[184:187], v[44:47]\n"
"ds_read_b128 v[164:167], v4 offset:58368\n"
"v_mfma_f32_16x16x32_bf16 v[44:47], a[124:127], v[188:191], v[44:47]\n"
"v_mfma_f32_16x16x32_bf16 v[40:43], a[120:123], v[188:191], v[40:43]\n"
"ds_read_b128 v[168:171], v4 offset:59392\n"
"v_mfma_f32_16x16x32_bf16 v[40:43], a[112:115], v[184:187], v[40:43]\n"
"buffer_load_dwordx4 v6, s[32:35], s63 offen offset:2048 sc1 lds\n"
"v_mfma_f32_16x16x32_bf16 v[40:43], a[104:107], v[180:183], v[40:43]\n"
"ds_read_b128 v[172:175], v4 offset:60416\n"
"v_mfma_f32_16x16x32_bf16 v[40:43], a[96:99], v[176:179], v[40:43]\n"
"s_waitcnt lgkmcnt(4)\n"
"v_mfma_f32_16x16x32_bf16 v[40:43], a[128:131], v[128:131], v[40:43]\n"
"ds_read_b128 v[176:179], v4 offset:61440\n"
"v_mfma_f32_16x16x32_bf16 v[40:43], a[136:139], v[132:135], v[40:43]\n"
"buffer_load_dwordx4 v6, s[32:35], s63 offen offset:3072 sc1 lds\n"
"v_mfma_f32_16x16x32_bf16 v[40:43], a[144:147], v[136:139], v[40:43]\n"
"ds_read_b128 v[180:183], v4 offset:62464\n"
"s_waitcnt vmcnt(11)\n"
"v_mfma_f32_16x16x32_bf16 v[40:43], a[152:155], v[140:143], v[40:43]\n"
"v_cmp_ne_u64 vcc, v[16:17], v[12:13]\n"
"s_add_u32 s63, s63, 593920\n"
"v_mfma_f32_16x16x32_bf16 v[44:47], a[156:159], v[140:143], v[44:47]\n"
"ds_read_b128 v[184:187], v4 offset:63488\n"
"s_cbranch_vccnz .L_rtw1_1\n"
".L_bkw1_1:\n"
"v_mfma_f32_16x16x32_bf16 v[44:47], a[148:151], v[136:139], v[44:47]\n"
"s_add_u32 s66, s66, 1187840\n"
"s_xor_b32 s64, s64, s65\n"
"v_mfma_f32_16x16x32_bf16 v[44:47], a[140:143], v[132:135], v[44:47]\n"
"ds_read_b128 v[188:191], v4 offset:64512\n"
"s_barrier\n"
"v_mfma_f32_16x16x32_bf16 v[44:47], a[132:135], v[128:131], v[44:47]\n"
"v_xor_b32 v4, 65536, v4\n"
"v_mfma_f32_16x16x32_bf16 v[44:47], a[164:167], v[144:147], v[44:47]\n"
"ds_read_b128 v[128:131], v4 offset:0\n"
"v_lshlrev_b32 v32, 16, v48\n"
"v_mfma_f32_16x16x32_bf16 v[44:47], a[172:175], v[148:151], v[44:47]\n"
"v_and_b32 v33, 0xffff0000, v48\n"
"v_lshlrev_b32 v34, 16, v49\n"
"v_mfma_f32_16x16x32_bf16 v[44:47], a[180:183], v[152:155], v[44:47]\n"
"ds_read_b128 v[132:135], v4 offset:1024\n"
"v_and_b32 v35, 0xffff0000, v49\n"
"v_mfma_f32_16x16x32_bf16 v[44:47], a[188:191], v[156:159], v[44:47]\n"
"v_lshlrev_b32 v36, 16, v50\n"
"v_and_b32 v37, 0xffff0000, v50\n"
"v_mfma_f32_16x16x32_bf16 v[40:43], a[184:187], v[156:159], v[40:43]\n"
"ds_read_b128 v[136:139], v4 offset:2048\n"
"s_add_u32 m0, s64, 0\n"
"v_mfma_f32_16x16x32_bf16 v[40:43], a[176:179], v[152:155], v[40:43]\n"
"buffer_load_dwordx4 v2, s[32:35], s63 offen offset:0 sc1 lds\n"
"v_mfma_f32_16x16x32_bf16 v[40:43], a[168:171], v[148:151], v[40:43]\n"
"ds_read_b128 v[140:143], v4 offset:3072\n"
"v_lshlrev_b32 v38, 16, v51\n"
"v_mfma_f32_16x16x32_bf16 v[40:43], a[160:163], v[144:147], v[40:43]\n"
"v_and_b32 v39, 0xffff0000, v51\n"
"s_waitcnt lgkmcnt(4)\n"
"v_mfma_f32_16x16x32_bf16 v[40:43], a[192:195], v[160:163], v[40:43]\n"
"ds_read_b128 v[144:147], v4 offset:4096\n"
"v_mfma_f32_16x16x32_bf16 v[40:43], a[200:203], v[164:167], v[40:43]\n"
"buffer_load_dwordx4 v2, s[32:35], s63 offen offset:1024 sc1 lds\n"
"v_mfma_f32_16x16x32_bf16 v[40:43], a[208:211], v[168:171], v[40:43]\n"
"ds_read_b128 v[148:151], v4 offset:5120\n"
"v_mfma_f32_16x16x32_bf16 v[40:43], a[216:219], v[172:175], v[40:43]\n"
"v_mfma_f32_16x16x32_bf16 v[44:47], a[220:223], v[172:175], v[44:47]\n"
"ds_read_b128 v[152:155], v4 offset:6144\n"
"v_mfma_f32_16x16x32_bf16 v[44:47], a[212:215], v[168:171], v[44:47]\n"
"buffer_load_dwordx4 v2, s[32:35], s63 offen offset:2048 sc1 lds\n"
"v_mfma_f32_16x16x32_bf16 v[44:47], a[204:207], v[164:167], v[44:47]\n"
"ds_read_b128 v[156:159], v4 offset:7168\n"
"v_mfma_f32_16x16x32_bf16 v[44:47], a[196:199], v[160:163], v[44:47]\n"
"v_mfma_f32_16x16x32_bf16 v[44:47], a[228:231], v[176:179], v[44:47]\n"
"ds_read_b128 v[160:163], v4 offset:8192\n"
"v_mfma_f32_16x16x32_bf16 v[44:47], a[236:239], v[180:183], v[44:47]\n"
"buffer_load_dwordx4 v2, s[32:35], s63 offen offset:3072 sc1 lds\n"
"v_mfma_f32_16x16x32_bf16 v[44:47], a[244:247], v[184:187], v[44:47]\n"
"ds_read_b128 v[164:167], v4 offset:9216\n"
"v_mfma_f32_16x16x32_bf16 v[44:47], a[252:255], v[188:191], v[44:47]\n"
"v_mfma_f32_16x16x32_bf16 v[40:43], a[248:251], v[188:191], v[40:43]\n"
"ds_read_b128 v[168:171], v4 offset:10240\n"
"s_add_u32 m0, s64, 4096\n"
"v_mfma_f32_16x16x32_bf16 v[40:43], a[240:243], v[184:187], v[40:43]\n"
"buffer_load_dwordx4 v6, s[32:35], s63 offen offset:0 sc1 lds\n"
"v_mfma_f32_16x16x32_bf16 v[40:43], a[232:235], v[180:183], v[40:43]\n"
"ds_read_b128 v[172:175], v4 offset:11264\n"
"s_sub_u32 s62, s62, 1\n"
"v_mfma_f32_16x16x32_bf16 v[40:43], a[224:227], v[176:179], v[40:43]\n"
"s_cbranch_scc0 .L_wl_1\n"
"s_nop 1\n"
"s_add_u32 s68, s68, 593920\n"
"s_nop 3\n"
"v_cvt_pk_bf16_f32 v56, v40, v41\n"
"v_cvt_pk_bf16_f32 v57, v42, v43\n"
"v_cvt_pk_bf16_f32 v58, v44, v45\n"
"v_cvt_pk_bf16_f32 v59, v46, v47\n"
"buffer_store_dwordx4 v[56:59], v2, s[32:35], s68 offen sc1\n"
"v_add_f32 v60, v40, v41\n"
"v_mul_f32 v61, v40, v40\n"
"v_fmac_f32 v61, v41, v41\n"
"v_add_f32 v60, v60, v42\n"
"v_fmac_f32 v61, v42, v42\n"
"v_add_f32 v60, v60, v43\n"
"v_fmac_f32 v61, v43, v43\n"
"v_add_f32 v60, v60, v44\n"
"v_fmac_f32 v61, v44, v44\n"
"v_add_f32 v60, v60, v45\n"
"v_fmac_f32 v61, v45, v45\n"
"v_add_f32 v60, v60, v46\n"
"v_fmac_f32 v61, v46, v46\n"
"v_add_f32 v60, v60, v47\n"
"v_fmac_f32 v61, v47, v47\n"
"s_nop 1\n"
"v_permlane16_swap_b32 v60, v61\n"
"v_add_f32 v60, v60, v61\n"
"s_nop 1\n"
"v_permlane32_swap_b32 v60, v61\n"
"v_add_f32 v60, v60, v61\n"
"buffer_store_dword v60, v15, s[32:35], s68 offen offset:1024 sc1\n"
"s_waitcnt vmcnt(9)\n"
"s_barrier\n"
"buffer_store_dwordx2 v[16:17], v2, s[92:95], s68 idxen sc1\n"
"s_add_u32 s68, s68, 593920\n"
"s_waitcnt vmcnt(1)\n"
"s_barrier\n"
"buffer_store_dwordx2 v[16:17], v2, s[92:95], s68 idxen sc1\n"
"s_branch .L_end\n"
".p2align 6\n"
".L_wv2:\n"
".L_wl_2:\n"
"s_waitcnt lgkmcnt(4)\n"
"v_mfma_f32_16x16x32_bf16 v[32:35], a[0:3], v[128:131], v[32:35]\n"
"ds_read_b128 v[176:179], v4 offset:12288\n"
"v_mfma_f32_16x16x32_bf16 v[32:35], a[8:11], v[132:135], v[32:35]\n"
"buffer_load_dwordx4 v6, s[32:35], s63 offen offset:1024 sc1 lds\n"
"v_mfma_f32_16x16x32_bf16 v[32:35], a[16:19], v[136:139], v[32:35]\n"
"ds_read_b128 v[180:183], v4 offset:13312\n"
"s_add_u32 s68, s68, 593920\n"
"v_mfma_f32_16x16x32_bf16 v[32:35], a[24:27], v[140:143], v[32:35]\n"
"v_cvt_pk_bf16_f32 v56, v40, v41\n"
"v_cvt_pk_bf16_f32 v57, v42, v43\n"
"v_mfma_f32_16x16x32_bf16 v[36:39], a[28:31], v[140:143], v[36:39]\n"
"ds_read_b128 v[184:187], v4 offset:14336\n"
"v_cvt_pk_bf16_f32 v58, v44, v45\n"
"v_mfma_f32_16x16x32_bf16 v[36:39], a[20:23], v[136:139], v[36:39]\n"
"v_cvt_pk_bf16_f32 v59, v46, v47\n"
"v_add_f32 v60, v40, v41\n"
"v_mfma_f32_16x16x32_bf16 v[36:39], a[12:15], v[132:135], v[36:39]\n"
"ds_read_b128 v[188:191], v4 offset:15360\n"
"v_mul_f32 v61, v40, v40\n"
"v_mfma_f32_16x16x32_bf16 v[36:39], a[4:7], v[128:131], v[36:39]\n"
"v_fmac_f32 v61, v41, v41\n"
"v_add_f32 v60, v60, v42\n"
"v_mfma_f32_16x16x32_bf16 v[36:39], a[36:39], v[144:147], v[36:39]\n"
"ds_read_b128 v[128:131], v4 offset:16384\n"
"v_fmac_f32 v61, v42, v42\n"
"v_mfma_f32_16x16x32_bf16 v[36:39], a[44:47], v[148:151], v[36:39]\n"
"buffer_store_dwordx4 v[56:59], v2, s[32:35], s68 offen sc1\n"
"v_mfma_f32_16x16x32_bf16 v[36:39], a[52:55], v[152:155], v[36:39]\n"
"ds_read_b128 v[132:135], v4 offset:17408\n"
"v_add_f32 v60, v60, v43\n"
"v_mfma_f32_16x16x32_bf16 v[36:39], a[60:63], v[156:159], v[36:39]\n"
"v_fmac_f32 v61, v43, v43\n"
"v_add_f32 v60, v60, v44\n"
"v_mfma_f32_16x16x32_bf16 v[32:35], a[56:59], v[156:159], v[32:35]\n"
"ds_read_b128 v[136:139], v4 offset:18432\n"
"v_fmac_f32 v61, v44, v44\n"
"v_mfma_f32_16x16x32_bf16 v[32:35], a[48:51], v[152:155], v[32:35]\n"
"v_add_f32 v60, v60, v45\n"
"v_fmac_f32 v61, v45, v45\n"
"v_mfma_f32_16x16x32_bf16 v[32:35], a[40:43], v[148:151], v[32:35]\n"
"ds_read_b128 v[140:143], v4 offset:19456\n"
"v_add_f32 v60, v60, v46\n"
"v_mfma_f32_16x16x32_bf16 v[32:35], a[32:35], v[144:147], v[32:35]\n"
"v_fmac_f32 v61, v46, v46\n"
"s_waitcnt lgkmcnt(4)\n"
"v_mfma_f32_16x16x32_bf16 v[32:35], a[64:67], v[160:163], v[32:35]\n"
"ds_read_b128 v[144:147], v4 offset:20480\n"
"v_add_f32 v60, v60, v47\n"
"v_mfma_f32_16x16x32_bf16 v[32:35], a[72:75], v[164:167], v[32:35]\n"
"v_fmac_f32 v61, v47, v47\n"
"v_mfma_f32_16x16x32_bf16 v[32:35], a[80:83], v[168:171], v[32:35]\n"
"ds_read_b128 v[148:151], v4 offset:21504\n"
"v_mfma_f32_16x16x32_bf16 v[32:35], a[88:91], v[172:175], v[32:35]\n"
"v_permlane16_swap_b32 v60, v61\n"
"v_mfma_f32_16x16x32_bf16 v[36:39], a[92:95], v[172:175], v[36:39]\n"
"ds_read_b128 v[152:155], v4 offset:22528\n"
"v_add_f32 v60, v60, v61\n"
"v_mfma_f32_16x16x32_bf16 v[36:39], a[84:87], v[168:171], v[36:39]\n"
"buffer_load_dwordx4 v[48:51], v2, s[32:35], s71 offen sc1\n"
"v_mfma_f32_16x16x32_bf16 v[36:39], a[76:79], v[164:167], v[36:39]\n"
"ds_read_b128 v[156:159], v4 offset:23552\n"
"s_add_u32 s71, s71, 593920\n"
"v_mfma_f32_16x16x32_bf16 v[36:39], a[68:71], v[160:163], v[36:39]\n"
"v_permlane32_swap_b32 v60, v61\n"
"v_mfma_f32_16x16x32_bf16 v[36:39], a[100:103], v[176:179], v[36:39]\n"
"ds_read_b128 v[160:163], v4 offset:24576\n"
"v_add_f32 v60, v60, v61\n"
"v_mfma_f32_16x16x32_bf16 v[36:39], a[108:111], v[180:183], v[36:39]\n"
"buffer_store_dword v60, v15, s[32:35], s68 offen offset:1024 sc1\n"
"v_mfma_f32_16x16x32_bf16 v[36:39], a[116:119], v[184:187], v[36:39]\n"
"ds_read_b128 v[164:167], v4 offset:25600\n"
"v_mfma_f32_16x16x32_bf16 v[36:39], a[124:127], v[188:191], v[36:39]\n"
"v_mfma_f32_16x16x32_bf16 v[32:35], a[120:123], v[188:191], v[32:35]\n"
"ds_read_b128 v[168:171], v4 offset:26624\n"
"v_mfma_f32_16x16x32_bf16 v[32:35], a[112:115], v[184:187], v[32:35]\n"
"buffer_load_dwordx4 v6, s[32:35], s63 offen offset:2048 sc1 lds\n"
"v_mfma_f32_16x16x32_bf16 v[32:35], a[104:107], v[180:183], v[32:35]\n"
"ds_read_b128 v[172:175], v4 offset:27648\n"
"v_mfma_f32_16x16x32_bf16 v[32:35], a[96:99], v[176:179], v[32:35]\n"
"s_waitcnt lgkmcnt(4)\n"
"v_mfma_f32_16x16x32_bf16 v[32:35], a[128:131], v[128:131], v[32:35]\n"
"ds_read_b128 v[176:179], v4 offset:28672\n"
"v_mfma_f32_16x16x32_bf16 v[32:35], a[136:139], v[132:135], v[32:35]\n"
"buffer_load_dwordx4 v6, s[32:35], s63 offen offset:3072 sc1 lds\n"
"v_mfma_f32_16x16x32_bf16 v[32:35], a[144:147], v[136:139], v[32:35]\n"
"ds_read_b128 v[180:183], v4 offset:29696\n"
"s_add_u32 s63, s63, 593920\n"
"v_mfma_f32_16x16x32_bf16 v[32:35], a[152:155], v[140:143], v[32:35]\n"
"v_mfma_f32_16x16x32_bf16 v[36:39], a[156:159], v[140:143], v[36:39]\n"
"ds_read_b128 v[184:187], v4 offset:30720\n"
"v_mfma_f32_16x16x32_bf16 v[36:39], a[148:151], v[136:139], v[36:39]\n"
"s_waitcnt vmcnt(11)\n"
"s_barrier\n"
"v_mfma_f32_16x16x32_bf16 v[36:39], a[140:143], v[132:135], v[36:39]\n"
"ds_read_b128 v[188:191], v4 offset:31744\n"
"v_mfma_f32_16x16x32_bf16 v[36:39], a[132:135], v[128:131], v[36:39]\n"
"v_mfma_f32_16x16x32_bf16 v[36:39], a[164:167], v[144:147], v[36:39]\n"
"ds_read_b128 v[128:131], v4 offset:32768\n"
"v_lshlrev_b32 v40, 16, v52\n"
"v_mfma_f32_16x16x32_bf16 v[36:39], a[172:175], v[148:151], v[36:39]\n"
"v_and_b32 v41, 0xffff0000, v52\n"
"v_lshlrev_b32 v42, 16, v53\n"
"v_mfma_f32_16x16x32_bf16 v[36:39], a[180:183], v[152:155], v[36:39]\n"
"ds_read_b128 v[132:135], v4 offset:33792\n"
"v_and_b32 v43, 0xffff0000, v53\n"
"v_mfma_f32_16x16x32_bf16 v[36:39], a[188:191], v[156:159], v[36:39]\n"
"v_lshlrev_b32 v44, 16, v54\n"
"v_and_b32 v45, 0xffff0000, v54\n"
"v_mfma_f32_16x16x32_bf16 v[32:35], a[184:187], v[156:159], v[32:35]\n"
"ds_read_b128 v[136:139], v4 offset:34816\n"
"s_add_u32 m0, s64, 32768\n"
"v_mfma_f32_16x16x32_bf16 v[32:35], a[176:179], v[152:155], v[32:35]\n"
"buffer_load_dwordx4 v2, s[32:35], s63 offen offset:0 sc1 lds\n"
"v_mfma_f32_16x16x32_bf16 v[32:35], a[168:171], v[148:151], v[32:35]\n"
"ds_read_b128 v[140:143], v4 offset:35840\n"
"v_lshlrev_b32 v46, 16, v55\n"
"v_mfma_f32_16x16x32_bf16 v[32:35], a[160:163], v[144:147], v[32:35]\n"
"v_and_b32 v47, 0xffff0000, v55\n"
"s_waitcnt lgkmcnt(4)\n"
"v_mfma_f32_16x16x32_bf16 v[32:35], a[192:195], v[160:163], v[32:35]\n"
"ds_read_b128 v[144:147], v4 offset:36864\n"
"v_mfma_f32_16x16x32_bf16 v[32:35], a[200:203], v[164:167], v[32:35]\n"
"buffer_load_dwordx4 v2, s[32:35], s63 offen offset:1024 sc1 lds\n"
"v_mfma_f32_16x16x32_bf16 v[32:35], a[208:211], v[168:171], v[32:35]\n"
"ds_read_b128 v[148:151], v4 offset:37888\n"
"v_mfma_f32_16x16x32_bf16 v[32:35], a[216:219], v[172:175], v[32:35]\n"
"v_mfma_f32_16x16x32_bf16 v[36:39], a[220:223], v[172:175], v[36:39]\n"
"ds_read_b128 v[152:155], v4 offset:38912\n"
"v_mfma_f32_16x16x32_bf16 v[36:39], a[212:215], v[168:171], v[36:39]\n"
"buffer_load_dwordx4 v2, s[32:35], s63 offen offset:2048 sc1 lds\n"
"v_mfma_f32_16x16x32_bf16 v[36:39], a[204:207], v[164:167], v[36:39]\n"
"ds_read_b128 v[156:159], v4 offset:39936\n"
"v_mfma_f32_16x16x32_bf16 v[36:39], a[196:199], v[160:163], v[36:39]\n"
"v_mfma_f32_16x16x32_bf16 v[36:39], a[228:231], v[176:179], v[36:39]\n"
"ds_read_b128 v[160:163], v4 offset:40960\n"
"v_mfma_f32_16x16x32_bf16 v[36:39], a[236:239], v[180:183], v[36:39]\n"
"buffer_load_dwordx4 v2, s[32:35], s63 offen offset:3072 sc1 lds\n"
"v_mfma_f32_16x16x32_bf16 v[36:39], a[244:247], v[184:187], v[36:39]\n"
"ds_read_b128 v[164:167], v4 offset:41984\n"
"v_mfma_f32_16x16x32_bf16 v[36:39], a[252:255], v[188:191], v[36:39]\n"
"v_mfma_f32_16x16x32_bf16 v[32:35], a[248:251], v[188:191], v[32:35]\n"
"ds_read_b128 v[168:171], v4 offset:43008\n"
"s_add_u32 m0, s64, 36864\n"
"v_mfma_f32_16x16x32_bf16 v[32:35], a[240:243], v[184:187], v[32:35]\n"
"buffer_load_dwordx4 v6, s[32:35], s63 offen offset:0 sc1 lds\n"
"v_mfma_f32_16x16x32_bf16 v[32:35], a[232:235], v[180:183], v[32:35]\n"
"ds_read_b128 v[172:175], v4 offset:44032\n"
"v_mfma_f32_16x16x32_bf16 v[32:35], a[224:227], v[176:179], v[32:35]\n"
"s_waitcnt lgkmcnt(4)\n"
"v_mfma_f32_16x16x32_bf16 v[40:43], a[0:3], v[128:131], v[40:43]\n"
"ds_read_b128 v[176:179], v4 offset:45056\n"
"v_mfma_f32_16x16x32_bf16 v[40:43], a[8:11], v[132:135], v[40:43]\n"
"buffer_load_dwordx4 v6, s[32:35], s63 offen offset:1024 sc1 lds\n"
"v_mfma_f32_16x16x32_bf16 v[40:43], a[16:19], v[136:139], v[40:43]\n"
"ds_read_b128 v[180:183], v4 offset:46080\n"
"s_add_u32 s68, s68, 593920\n"
"v_mfma_f32_16x16x32_bf16 v[40:43], a[24:27], v[140:143], v[40:43]\n"
"v_cvt_pk_bf16_f32 v56, v32, v33\n"
"v_cvt_pk_bf16_f32 v57, v34, v35\n"
"v_mfma_f32_16x16x32_bf16 v[44:47], a[28:31], v[140:143], v[44:47]\n"
"ds_read_b128 v[184:187], v4 offset:47104\n"
"v_cvt_pk_bf16_f32 v58, v36, v37\n"
"v_mfma_f32_16x16x32_bf16 v[44:47], a[20:23], v[136:139], v[44:47]\n"
"v_cvt_pk_bf16_f32 v59, v38, v39\n"
"v_add_f32 v60, v32, v33\n"
"v_mfma_f32_16x16x32_bf16 v[44:47], a[12:15], v[132:135], v[44:47]\n"
"ds_read_b128 v[188:191], v4 offset:48128\n"
"v_mul_f32 v61, v32, v32\n"
"v_mfma_f32_16x16x32_bf16 v[44:47], a[4:7], v[128:131], v[44:47]\n"
"v_fmac_f32 v61, v33, v33\n"
"v_add_f32 v60, v60, v34\n"
"v_mfma_f32_16x16x32_bf16 v[44:47], a[36:39], v[144:147], v[44:47]\n"
"ds_read_b128 v[128:131], v4 offset:49152\n"
"v_fmac_f32 v61, v34, v34\n"
"v_mfma_f32_16x16x32_bf16 v[44:47], a[44:47], v[148:151], v[44:47]\n"
"buffer_store_dwordx4 v[56:59], v2, s[32:35], s68 offen sc1\n"
"v_mfma_f32_16x16x32_bf16 v[44:47], a[52:55], v[152:155], v[44:47]\n"
"ds_read_b128 v[132:135], v4 offset:50176\n"
"v_add_f32 v60, v60, v35\n"
"v_mfma_f32_16x16x32_bf16 v[44:47], a[60:63], v[156:159], v[44:47]\n"
"v_fmac_f32 v61, v35, v35\n"
"v_add_f32 v60, v60, v36\n"
"v_mfma_f32_16x16x32_bf16 v[40:43], a[56:59], v[156:159], v[40:43]\n"
"ds_read_b128 v[136:139], v4 offset:51200\n"
"v_fmac_f32 v61, v36, v36\n"
"v_mfma_f32_16x16x32_bf16 v[40:43], a[48:51], v[152:155], v[40:43]\n"
"v_add_f32 v60, v60, v37\n"
"v_fmac_f32 v61, v37, v37\n"
"v_mfma_f32_16x16x32_bf16 v[40:43], a[40:43], v[148:151], v[40:43]\n"
"ds_read_b128 v[140:143], v4 offset:52224\n"
"v_add_f32 v60, v60, v38\n"
"v_mfma_f32_16x16x32_bf16 v[40:43], a[32:35], v[144:147], v[40:43]\n"
"v_fmac_f32 v61, v38, v38\n"
"s_waitcnt lgkmcnt(4)\n"
"v_mfma_f32_16x16x32_bf16 v[40:43], a[64:67], v[160:163], v[40:43]\n"
"ds_read_b128 v[144:147], v4 offset:53248\n"
"v_add_f32 v60, v60, v39\n"
"v_mfma_f32_16x16x32_bf16 v[40:43], a[72:75], v[164:167], v[40:43]\n"
"v_fmac_f32 v61, v39, v39\n"
"v_mfma_f32_16x16x32_bf16 v[40:43], a[80:83], v[168:171], v[40:43]\n"
"ds_read_b128 v[148:151], v4 offset:54272\n"
"v_mfma_f32_16x16x32_bf16 v[40:43], a[88:91], v[172:175], v[40:43]\n"
"v_permlane16_swap_b32 v60, v61\n"
"v_mfma_f32_16x16x32_bf16 v[44:47], a[92:95], v[172:175], v[44:47]\n"
"ds_read_b128 v[152:155], v4 offset:55296\n"
"v_add_f32 v60, v60, v61\n"
"v_mfma_f32_16x16x32_bf16 v[44:47], a[84:87], v[168:171], v[44:47]\n"
"buffer_load_dwordx4 v[52:55], v2, s[32:35], s71 offen sc1\n"
"v_mfma_f32_16x16x32_bf16 v[44:47], a[76:79], v[164:167], v[44:47]\n"
"ds_read_b128 v[156:159], v4 offset:56320\n"
"s_add_u32 s71, s71, 593920\n"
"v_mfma_f32_16x16x32_bf16 v[44:47], a[68:71], v[160:163], v[44:47]\n"
"v_permlane32_swap_b32 v60, v61\n"
"v_mfma_f32_16x16x32_bf16 v[44:47], a[100:103], v[176:179], v[44:47]\n"
"ds_read_b128 v[160:163], v4 offset:57344\n"
"v_add_f32 v60, v60, v61\n"
"v_mfma_f32_16x16x32_bf16 v[44:47], a[108:111], v[180:183], v[44:47]\n"
"buffer_store_dword v60, v15, s[32:35], s68 offen offset:1024 sc1\n"
"v_mfma_f32_16x16x32_bf16 v[44:47], a[116:119], v[184:187], v[44:47]\n"
"ds_read_b128 v[164:167], v4 offset:58368\n"
"v_mfma_f32_16x16x32_bf16 v[44:47], a[124:127], v[188:191], v[44:47]\n"
"v_mfma_f32_16x16x32_bf16 v[40:43], a[120:123], v[188:191], v[40:43]\n"
"ds_read_b128 v[168:171], v4 offset:59392\n"
"v_mfma_f32_16x16x32_bf16 v[40:43], a[112:115], v[184:187], v[40:43]\n"
"buffer_load_dwordx4 v6, s[32:35], s63 offen offset:2048 sc1 lds\n"
"v_mfma_f32_16x16x32_bf16 v[40:43], a[104:107], v[180:183], v[40:43]\n"
"ds_read_b128 v[172:175], v4 offset:60416\n"
"v_mfma_f32_16x16x32_bf16 v[40:43], a[96:99], v[176:179], v[40:43]\n"
"s_waitcnt lgkmcnt(4)\n"
"v_mfma_f32_16x16x32_bf16 v[40:43], a[128:131], v[128:131], v[40:43]\n"
"ds_read_b128 v[176:179], v4 offset:61440\n"
"v_mfma_f32_16x16x32_bf16 v[40:43], a[136:139], v[132:135], v[40:43]\n"
"buffer_load_dwordx4 v6, s[32:35], s63 offen offset:3072 sc1 lds\n"
"v_mfma_f32_16x16x32_bf16 v[40:43], a[144:147], v[136:139], v[40:43]\n"
"ds_read_b128 v[180:183], v4 offset:62464\n"
"s_add_u32 s63, s63, 593920\n"
"v_mfma_f32_16x16x32_bf16 v[40:43], a[152:155], v[140:143], v[40:43]\n"
"s_xor_b32 s64, s64, s65\n"
"v_mfma_f32_16x16x32_bf16 v[44:47], a[156:159], v[140:143], v[44:47]\n"
"ds_read_b128 v[184:187], v4 offset:63488\n"
"v_mfma_f32_16x16x32_bf16 v[44:47], a[148:151], v[136:139], v[44:47]\n"
"s_waitcnt vmcnt(11)\n"
"s_barrier\n"
"v_mfma_f32_16x16x32_bf16 v[44:47], a[140:143], v[132:135], v[44:47]\n"
"ds_read_b128 v[188:191], v4 offset:64512\n"
"v_xor_b32 v4, 65536, v4\n"
"v_mfma_f32_16x16x32_bf16 v[44:47], a[132:135], v[128:131], v[44:47]\n"
"v_mfma_f32_16x16x32_bf16 v[44:47], a[164:167], v[144:147], v[44:47]\n"
"ds_read_b128 v[128:131], v4 offset:0\n"
"v_lshlrev_b32 v32, 16, v48\n"
"v_mfma_f32_16x16x32_bf16 v[44:47], a[172:175], v[148:151], v[44:47]\n"
"v_and_b32 v33, 0xffff0000, v48\n"
"v_lshlrev_b32 v34, 16, v49\n"
"v_mfma_f32_16x16x32_bf16 v[44:47], a[180:183], v[152:155], v[44:47]\n"
"ds_read_b128 v[132:135], v4 offset:1024\n"
"v_and_b32 v35, 0xffff0000, v49\n"
"v_mfma_f32_16x16x32_bf16 v[44:47], a[188:191], v[156:159], v[44:47]\n"
"v_lshlrev_b32 v36, 16, v50\n"
"v_and_b32 v37, 0xffff0000, v50\n"
"v_mfma_f32_16x16x32_bf16 v[40:43], a[184:187], v[156:159], v[40:43]\n"
"ds_read_b128 v[136:139], v4 offset:2048\n"
"s_add_u32 m0, s64, 0\n"
"v_mfma_f32_16x16x32_bf16 v[40:43], a[176:179], v[152:155], v[40:43]\n"
"buffer_load_dwordx4 v2, s[32:35], s63 offen offset:0 sc1 lds\n"
"v_mfma_f32_16x16x32_bf16 v[40:43], a[168:171], v[148:151], v[40:43]\n"
"ds_read_b128 v[140:143], v4 offset:3072\n"
"v_lshlrev_b32 v38, 16, v51\n"
"v_mfma_f32_16x16x32_bf16 v[40:43], a[160:163], v[144:147], v[40:43]\n"
"v_and_b32 v39, 0xffff0000, v51\n"
"s_waitcnt lgkmcnt(4)\n"
"v_mfma_f32_16x16x32_bf16 v[40:43], a[192:195], v[160:163], v[40:43]\n"
"ds_read_b128 v[144:147], v4 offset:4096\n"
"v_mfma_f32_16x16x32_bf16 v[40:43], a[200:203], v[164:167], v[40:43]\n"
"buffer_load_dwordx4 v2, s[32:35], s63 offen offset:1024 sc1 lds\n"
"v_mfma_f32_16x16x32_bf16 v[40:43], a[208:211], v[168:171], v[40:43]\n"
"ds_read_b128 v[148:151], v4 offset:5120\n"
"v_mfma_f32_16x16x32_bf16 v[40:43], a[216:219], v[172:175], v[40:43]\n"
"v_mfma_f32_16x16x32_bf16 v[44:47], a[220:223], v[172:175], v[44:47]\n"
"ds_read_b128 v[152:155], v4 offset:6144\n"
"v_mfma_f32_16x16x32_bf16 v[44:47], a[212:215], v[168:171], v[44:47]\n"
"buffer_load_dwordx4 v2, s[32:35], s63 offen offset:2048 sc1 lds\n"
"v_mfma_f32_16x16x32_bf16 v[44:47], a[204:207], v[164:167], v[44:47]\n"
"ds_read_b128 v[156:159], v4 offset:7168\n"
"v_mfma_f32_16x16x32_bf16 v[44:47], a[196:199], v[160:163], v[44:47]\n"
"v_mfma_f32_16x16x32_bf16 v[44:47], a[228:231], v[176:179], v[44:47]\n"
"ds_read_b128 v[160:163], v4 offset:8192\n"
"v_mfma_f32_16x16x32_bf16 v[44:47], a[236:239], v[180:183], v[44:47]\n"
"buffer_load_dwordx4 v2, s[32:35], s63 offen offset:3072 sc1 lds\n"
"v_mfma_f32_16x16x32_bf16 v[44:47], a[244:247], v[184:187], v[44:47]\n"
"ds_read_b128 v[164:167], v4 offset:9216\n"
"v_mfma_f32_16x16x32_bf16 v[44:47], a[252:255], v[188:191], v[44:47]\n"
"v_mfma_f32_16x16x32_bf16 v[40:43], a[248:251], v[188:191], v[40:43]\n"
"ds_read_b128 v[168:171], v4 offset:10240\n"
"s_add_u32 m0, s64, 4096\n"
"v_mfma_f32_16x16x32_bf16 v[40:43], a[240:243], v[184:187], v[40:43]\n"
"buffer_load_dwordx4 v6, s[32:35], s63 offen offset:0 sc1 lds\n"
"v_mfma_f32_16x16x32_bf16 v[40:43], a[232:235], v[180:183], v[40:43]\n"
"ds_read_b128 v[172:175], v4 offset:11264\n"
"s_sub_u32 s62, s62, 1\n"
"v_mfma_f32_16x16x32_bf16 v[40:43], a[224:227], v[176:179], v[40:43]\n"
"s_cbranch_scc0 .L_wl_2\n"
"s_nop 1\n"
"s_add_u32 s68, s68, 593920\n"
"s_nop 3\n"
"v_cvt_pk_bf16_f32 v56, v40, v41\n"
"v_cvt_pk_bf16_f32 v57, v42, v43\n"
"v_cvt_pk_bf16_f32 v58, v44, v45\n"
"v_cvt_pk_bf16_f32 v59, v46, v47\n"
"buffer_store_dwordx4 v[56:59], v2, s[32:35], s68 offen sc1\n"
"v_add_f32 v60, v40, v41\n"
"v_mul_f32 v61, v40, v40\n"
"v_fmac_f32 v61, v41, v41\n"
"v_add_f32 v60, v60, v42\n"
"v_fmac_f32 v61, v42, v42\n"
"v_add_f32 v60, v60, v43\n"
"v_fmac_f32 v61, v43, v43\n"
"v_add_f32 v60, v60, v44\n"
"v_fmac_f32 v61, v44, v44\n"
"v_add_f32 v60, v60, v45\n"
"v_fmac_f32 v61, v45, v45\n"
"v_add_f32 v60, v60, v46\n"
"v_fmac_f32 v61, v46, v46\n"
"v_add_f32 v60, v60, v47\n"
"v_fmac_f32 v61, v47, v47\n"
"s_nop 1\n"
"v_permlane16_swap_b32 v60, v61\n"
"v_add_f32 v60, v60, v61\n"
"s_nop 1\n"
"v_permlane32_swap_b32 v60, v61\n"
"v_add_f32 v60, v60, v61\n"
"buffer_store_dword v60, v15, s[32:35], s68 offen offset:1024 sc1\n"
"s_waitcnt vmcnt(9)\n"
"s_barrier\n"
"buffer_store_dwordx2 v[16:17], v2, s[92:95], s68 idxen sc1\n"
"s_add_u32 s68, s68, 593920\n"
"s_waitcnt vmcnt(1)\n"
"s_barrier\n"
"buffer_store_dwordx2 v[16:17], v2, s[92:95], s68 idxen sc1\n"
"s_branch .L_end\n"
".p2align 6\n"
".L_wv3:\n"
".L_wl_3:\n"
"s_waitcnt lgkmcnt(4)\n"
"v_mfma_f32_16x16x32_bf16 v[32:35], a[0:3], v[128:131], v[32:35]\n"
"ds_read_b128 v[176:179], v4 offset:12288\n"
"v_mfma_f32_16x16x32_bf16 v[32:35], a[8:11], v[132:135], v[32:35]\n"
"buffer_load_dwordx4 v6, s[32:35], s63 offen offset:1024 sc1 lds\n"
"v_mfma_f32_16x16x32_bf16 v[32:35], a[16:19], v[136:139], v[32:35]\n"
"ds_read_b128 v[180:183], v4 offset:13312\n"
"s_add_u32 s68, s68, 593920\n"
"v_mfma_f32_16x16x32_bf16 v[32:35], a[24:27], v[140:143], v[32:35]\n"
"v_cvt_pk_bf16_f32 v56, v40, v41\n"
"v_cvt_pk_bf16_f32 v57, v42, v43\n"
"v_mfma_f32_16x16x32_bf16 v[36:39], a[28:31], v[140:143], v[36:39]\n"
"ds_read_b128 v[184:187], v4 offset:14336\n"
"v_cvt_pk_bf16_f32 v58, v44, v45\n"
"v_mfma_f32_16x16x32_bf16 v[36:39], a[20:23], v[136:139], v[36:39]\n"
"v_cvt_pk_bf16_f32 v59, v46, v47\n"
"v_add_f32 v60, v40, v41\n"
"v_mfma_f32_16x16x32_bf16 v[36:39], a[12:15], v[132:135], v[36:39]\n"
"ds_read_b128 v[188:191], v4 offset:15360\n"
"v_mul_f32 v61, v40, v40\n"
"v_mfma_f32_16x16x32_bf16 v[36:39], a[4:7], v[128:131], v[36:39]\n"
"v_fmac_f32 v61, v41, v41\n"
"v_add_f32 v60, v60, v42\n"
"v_mfma_f32_16x16x32_bf16 v[36:39], a[36:39], v[144:147], v[36:39]\n"
"ds_read_b128 v[128:131], v4 offset:16384\n"
"v_fmac_f32 v61, v42, v42\n"
"v_mfma_f32_16x16x32_bf16 v[36:39], a[44:47], v[148:151], v[36:39]\n"
"buffer_store_dwordx4 v[56:59], v2, s[32:35], s68 offen sc1\n"
"v_mfma_f32_16x16x32_bf16 v[36:39], a[52:55], v[152:155], v[36:39]\n"
"ds_read_b128 v[132:135], v4 offset:17408\n"
"v_add_f32 v60, v60, v43\n"
"v_mfma_f32_16x16x32_bf16 v[36:39], a[60:63], v[156:159], v[36:39]\n"
"v_fmac_f32 v61, v43, v43\n"
"v_add_f32 v60, v60, v44\n"
"v_mfma_f32_16x16x32_bf16 v[32:35], a[56:59], v[156:159], v[32:35]\n"
"ds_read_b128 v[136:139], v4 offset:18432\n"
"v_fmac_f32 v61, v44, v44\n"
"v_mfma_f32_16x16x32_bf16 v[32:35], a[48:51], v[152:155], v[32:35]\n"
"v_add_f32 v60, v60, v45\n"
"v_fmac_f32 v61, v45, v45\n"
"v_mfma_f32_16x16x32_bf16 v[32:35], a[40:43], v[148:151], v[32:35]\n"
"ds_read_b128 v[140:143], v4 offset:19456\n"
"v_add_f32 v60, v60, v46\n"
"v_mfma_f32_16x16x32_bf16 v[32:35], a[32:35], v[144:147], v[32:35]\n"
"v_fmac_f32 v61, v46, v46\n"
"s_waitcnt lgkmcnt(4)\n"
"v_mfma_f32_16x16x32_bf16 v[32:35], a[64:67], v[160:163], v[32:35]\n"
"ds_read_b128 v[144:147], v4 offset:20480\n"
"v_add_f32 v60, v60, v47\n"
"v_mfma_f32_16x16x32_bf16 v[32:35], a[72:75], v[164:167], v[32:35]\n"
"v_fmac_f32 v61, v47, v47\n"
"v_mfma_f32_16x16x32_bf16 v[32:35], a[80:83], v[168:171], v[32:35]\n"
"ds_read_b128 v[148:151], v4 offset:21504\n"
"v_mfma_f32_16x16x32_bf16 v[32:35], a[88:91], v[172:175], v[32:35]\n"
"v_permlane16_swap_b32 v60, v61\n"
"v_mfma_f32_16x16x32_bf16 v[36:39], a[92:95], v[172:175], v[36:39]\n"
"ds_read_b128 v[152:155], v4 offset:22528\n"
"v_add_f32 v60, v60, v61\n"
"v_mfma_f32_16x16x32_bf16 v[36:39], a[84:87], v[168:171], v[36:39]\n"
"buffer_load_dwordx4 v[48:51], v2, s[32:35], s71 offen sc1\n"
"v_mfma_f32_16x16x32_bf16 v[36:39], a[76:79], v[164:167], v[36:39]\n"
"ds_read_b128 v[156:159], v4 offset:23552\n"
"s_add_u32 s71, s71, 593920\n"
"v_mfma_f32_16x16x32_bf16 v[36:39], a[68:71], v[160:163], v[36:39]\n"
"v_permlane32_swap_b32 v60, v61\n"
"v_mfma_f32_16x16x32_bf16 v[36:39], a[100:103], v[176:179], v[36:39]\n"
"ds_read_b128 v[160:163], v4 offset:24576\n"
"v_add_f32 v60, v60, v61\n"
"v_mfma_f32_16x16x32_bf16 v[36:39], a[108:111], v[180:183], v[36:39]\n"
"buffer_store_dword v60, v15, s[32:35], s68 offen offset:1024 sc1\n"
"v_mfma_f32_16x16x32_bf16 v[36:39], a[116:119], v[184:187], v[36:39]\n"
"ds_read_b128 v[164:167], v4 offset:25600\n"
"v_mfma_f32_16x16x32_bf16 v[36:39], a[124:127], v[188:191], v[36:39]\n"
"v_mfma_f32_16x16x32_bf16 v[32:35], a[120:123], v[188:191], v[32:35]\n"
"ds_read_b128 v[168:171], v4 offset:26624\n"
"v_mfma_f32_16x16x32_bf16 v[32:35], a[112:115], v[184:187], v[32:35]\n"
"buffer_load_dwordx4 v6, s[32:35], s63 offen offset:2048 sc1 lds\n"
"v_mfma_f32_16x16x32_bf16 v[32:35], a[104:107], v[180:183], v[32:35]\n"
"ds_read_b128 v[172:175], v4 offset:27648\n"
"v_mfma_f32_16x16x32_bf16 v[32:35], a[96:99], v[176:179], v[32:35]\n"
"s_waitcnt lgkmcnt(4)\n"
"v_mfma_f32_16x16x32_bf16 v[32:35], a[128:131], v[128:131], v[32:35]\n"
"ds_read_b128 v[176:179], v4 offset:28672\n"
"v_mfma_f32_16x16x32_bf16 v[32:35], a[136:139], v[132:135], v[32:35]\n"
"buffer_load_dwordx4 v6, s[32:35], s63 offen offset:3072 sc1 lds\n"
"v_mfma_f32_16x16x32_bf16 v[32:35], a[144:147], v[136:139], v[32:35]\n"
"ds_read_b128 v[180:183], v4 offset:29696\n"
"s_add_u32 s63, s63, 593920\n"
"v_mfma_f32_16x16x32_bf16 v[32:35], a[152:155], v[140:143], v[32:35]\n"
"v_mfma_f32_16x16x32_bf16 v[36:39], a[156:159], v[140:143], v[36:39]\n"
"s_waitcnt vmcnt(11)\n"
"s_barrier\n"
"v_mfma_f32_16x16x32_bf16 v[36:39], a[148:151], v[136:139], v[36:39]\n"
"ds_read_b128 v[184:187], v4 offset:30720\n"
"v_mfma_f32_16x16x32_bf16 v[36:39], a[140:143], v[132:135], v[36:39]\n"
"ds_read_b128 v[188:191], v4 offset:31744\n"
"v_mfma_f32_16x16x32_bf16 v[36:39], a[132:135], v[128:131], v[36:39]\n"
"v_mfma_f32_16x16x32_bf16 v[36:39], a[164:167], v[144:147], v[36:39]\n"
"ds_read_b128 v[128:131], v4 offset:32768\n"
"v_lshlrev_b32 v40, 16, v52\n"
"v_mfma_f32_16x16x32_bf16 v[36:39], a[172:175], v[148:151], v[36:39]\n"
"v_and_b32 v41, 0xffff0000, v52\n"
"v_lshlrev_b32 v42, 16, v53\n"
"v_mfma_f32_16x16x32_bf16 v[36:39], a[180:183], v[152:155], v[36:39]\n"
"ds_read_b128 v[132:135], v4 offset:33792\n"
"v_and_b32 v43, 0xffff0000, v53\n"
"v_mfma_f32_16x16x32_bf16 v[36:39], a[188:191], v[156:159], v[36:39]\n"
"v_lshlrev_b32 v44, 16, v54\n"
"v_and_b32 v45, 0xffff0000, v54\n"
"v_mfma_f32_16x16x32_bf16 v[32:35], a[184:187], v[156:159], v[32:35]\n"
"ds_read_b128 v[136:139], v4 offset:34816\n"
"s_add_u32 m0, s64, 32768\n"
"v_mfma_f32_16x16x32_bf16 v[32:35], a[176:179], v[152:155], v[32:35]\n"
"buffer_load_dwordx4 v2, s[32:35], s63 offen offset:0 sc1 lds\n"
"v_mfma_f32_16x16x32_bf16 v[32:35], a[168:171], v[148:151], v[32:35]\n"
"ds_read_b128 v[140:143], v4 offset:35840\n"
"v_lshlrev_b32 v46, 16, v55\n"
"v_mfma_f32_16x16x32_bf16 v[32:35], a[160:163], v[144:147], v[32:35]\n"
"v_and_b32 v47, 0xffff0000, v55\n"
"s_waitcnt lgkmcnt(4)\n"
"v_mfma_f32_16x16x32_bf16 v[32:35], a[192:195], v[160:163], v[32:35]\n"
"ds_read_b128 v[144:147], v4 offset:36864\n"
"v_mfma_f32_16x16x32_bf16 v[32:35], a[200:203], v[164:167], v[32:35]\n"
"buffer_load_dwordx4 v2, s[32:35], s63 offen offset:1024 sc1 lds\n"
"v_mfma_f32_16x16x32_bf16 v[32:35], a[208:211], v[168:171], v[32:35]\n"
"ds_read_b128 v[148:151], v4 offset:37888\n"
"v_mfma_f32_16x16x32_bf16 v[32:35], a[216:219], v[172:175], v[32:35]\n"
"v_mfma_f32_16x16x32_bf16 v[36:39], a[220:223], v[172:175], v[36:39]\n"
"ds_read_b128 v[152:155], v4 offset:38912\n"
"v_mfma_f32_16x16x32_bf16 v[36:39], a[212:215], v[168:171], v[36:39]\n"
"buffer_load_dwordx4 v2, s[32:35], s63 offen offset:2048 sc1 lds\n"
"v_mfma_f32_16x16x32_bf16 v[36:39], a[204:207], v[164:167], v[36:39]\n"
"ds_read_b128 v[156:159], v4 offset:39936\n"
"v_mfma_f32_16x16x32_bf16 v[36:39], a[196:199], v[160:163], v[36:39]\n"
"v_mfma_f32_16x16x32_bf16 v[36:39], a[228:231], v[176:179], v[36:39]\n"
"ds_read_b128 v[160:163], v4 offset:40960\n"
"v_mfma_f32_16x16x32_bf16 v[36:39], a[236:239], v[180:183], v[36:39]\n"
"buffer_load_dwordx4 v2, s[32:35], s63 offen offset:3072 sc1 lds\n"
"v_mfma_f32_16x16x32_bf16 v[36:39], a[244:247], v[184:187], v[36:39]\n"
"ds_read_b128 v[164:167], v4 offset:41984\n"
"v_mfma_f32_16x16x32_bf16 v[36:39], a[252:255], v[188:191], v[36:39]\n"
"v_mfma_f32_16x16x32_bf16 v[32:35], a[248:251], v[188:191], v[32:35]\n"
"ds_read_b128 v[168:171], v4 offset:43008\n"
"s_add_u32 m0, s64, 36864\n"
"v_mfma_f32_16x16x32_bf16 v[32:35], a[240:243], v[184:187], v[32:35]\n"
"buffer_load_dwordx4 v6, s[32:35], s63 offen offset:0 sc1 lds\n"
"v_mfma_f32_16x16x32_bf16 v[32:35], a[232:235], v[180:183], v[32:35]\n"
"ds_read_b128 v[172:175], v4 offset:44032\n"
"v_mfma_f32_16x16x32_bf16 v[32:35], a[224:227], v[176:179], v[32:35]\n"
"s_waitcnt lgkmcnt(4)\n"
"v_mfma_f32_16x16x32_bf16 v[40:43], a[0:3], v[128:131], v[40:43]\n"
"ds_read_b128 v[176:179], v4 offset:45056\n"
"v_mfma_f32_16x16x32_bf16 v[40:43], a[8:11], v[132:135], v[40:43]\n"
"buffer_load_dwordx4 v6, s[32:35], s63 offen offset:1024 sc1 lds\n"
"v_mfma_f32_16x16x32_bf16 v[40:43], a[16:19], v[136:139], v[40:43]\n"
"ds_read_b128 v[180:183], v4 offset:46080\n"
"s_add_u32 s68, s68, 593920\n"
"v_mfma_f32_16x16x32_bf16 v[40:43], a[24:27], v[140:143], v[40:43]\n"
"v_cvt_pk_bf16_f32 v56, v32, v33\n"
"v_cvt_pk_bf16_f32 v57, v34, v35\n"
"v_mfma_f32_16x16x32_bf16 v[44:47], a[28:31], v[140:143], v[44:47]\n"
"ds_read_b128 v[184:187], v4 offset:47104\n"
"v_cvt_pk_bf16_f32 v58, v36, v37\n"
"v_mfma_f32_16x16x32_bf16 v[44:47], a[20:23], v[136:139], v[44:47]\n"
"v_cvt_pk_bf16_f32 v59, v38, v39\n"
"v_add_f32 v60, v32, v33\n"
"v_mfma_f32_16x16x32_bf16 v[44:47], a[12:15], v[132:135], v[44:47]\n"
"ds_read_b128 v[188:191], v4 offset:48128\n"
"v_mul_f32 v61, v32, v32\n"
"v_mfma_f32_16x16x32_bf16 v[44:47], a[4:7], v[128:131], v[44:47]\n"
"v_fmac_f32 v61, v33, v33\n"
"v_add_f32 v60, v60, v34\n"
"v_mfma_f32_16x16x32_bf16 v[44:47], a[36:39], v[144:147], v[44:47]\n"
"ds_read_b128 v[128:131], v4 offset:49152\n"
"v_fmac_f32 v61, v34, v34\n"
"v_mfma_f32_16x16x32_bf16 v[44:47], a[44:47], v[148:151], v[44:47]\n"
"buffer_store_dwordx4 v[56:59], v2, s[32:35], s68 offen sc1\n"
"v_mfma_f32_16x16x32_bf16 v[44:47], a[52:55], v[152:155], v[44:47]\n"
"ds_read_b128 v[132:135], v4 offset:50176\n"
"v_add_f32 v60, v60, v35\n"
"v_mfma_f32_16x16x32_bf16 v[44:47], a[60:63], v[156:159], v[44:47]\n"
"v_fmac_f32 v61, v35, v35\n"
"v_add_f32 v60, v60, v36\n"
"v_mfma_f32_16x16x32_bf16 v[40:43], a[56:59], v[156:159], v[40:43]\n"
"ds_read_b128 v[136:139], v4 offset:51200\n"
"v_fmac_f32 v61, v36, v36\n"
"v_mfma_f32_16x16x32_bf16 v[40:43], a[48:51], v[152:155], v[40:43]\n"
"v_add_f32 v60, v60, v37\n"
"v_fmac_f32 v61, v37, v37\n"
"v_mfma_f32_16x16x32_bf16 v[40:43], a[40:43], v[148:151], v[40:43]\n"
"ds_read_b128 v[140:143], v4 offset:52224\n"
"v_add_f32 v60, v60, v38\n"
"v_mfma_f32_16x16x32_bf16 v[40:43], a[32:35], v[144:147], v[40:43]\n"
"v_fmac_f32 v61, v38, v38\n"
"s_waitcnt lgkmcnt(4)\n"
"v_mfma_f32_16x16x32_bf16 v[40:43], a[64:67], v[160:163], v[40:43]\n"
"ds_read_b128 v[144:147], v4 offset:53248\n"
"v_add_f32 v60, v60, v39\n"
"v_mfma_f32_16x16x32_bf16 v[40:43], a[72:75], v[164:167], v[40:43]\n"
"v_fmac_f32 v61, v39, v39\n"
"v_mfma_f32_16x16x32_bf16 v[40:43], a[80:83], v[168:171], v[40:43]\n"
"ds_read_b128 v[148:151], v4 offset:54272\n"
"v_mfma_f32_16x16x32_bf16 v[40:43], a[88:91], v[172:175], v[40:43]\n"
"v_permlane16_swap_b32 v60, v61\n"
"v_mfma_f32_16x16x32_bf16 v[44:47], a[92:95], v[172:175], v[44:47]\n"
"ds_read_b128 v[152:155], v4 offset:55296\n"
"v_add_f32 v60, v60, v61\n"
"v_mfma_f32_16x16x32_bf16 v[44:47], a[84:87], v[168:171], v[44:47]\n"
"buffer_load_dwordx4 v[52:55], v2, s[32:35], s71 offen sc1\n"
"v_mfma_f32_16x16x32_bf16 v[44:47], a[76:79], v[164:167], v[44:47]\n"
"ds_read_b128 v[156:159], v4 offset:56320\n"
"s_add_u32 s71, s71, 593920\n"
"v_mfma_f32_16x16x32_bf16 v[44:47], a[68:71], v[160:163], v[44:47]\n"
"v_permlane32_swap_b32 v60, v61\n"
"v_mfma_f32_16x16x32_bf16 v[44:47], a[100:103], v[176:179], v[44:47]\n"
"ds_read_b128 v[160:163], v4 offset:57344\n"
"v_add_f32 v60, v60, v61\n"
"v_mfma_f32_16x16x32_bf16 v[44:47], a[108:111], v[180:183], v[44:47]\n"
"buffer_store_dword v60, v15, s[32:35], s68 offen offset:1024 sc1\n"
"v_mfma_f32_16x16x32_bf16 v[44:47], a[116:119], v[184:187], v[44:47]\n"
"ds_read_b128 v[164:167], v4 offset:58368\n"
"v_mfma_f32_16x16x32_bf16 v[44:47], a[124:127], v[188:191], v[44:47]\n"
"v_mfma_f32_16x16x32_bf16 v[40:43], a[120:123], v[188:191], v[40:43]\n"
"ds_read_b128 v[168:171], v4 offset:59392\n"
"v_mfma_f32_16x16x32_bf16 v[40:43], a[112:115], v[184:187], v[40:43]\n"
"buffer_load_dwordx4 v6, s[32:35], s63 offen offset:2048 sc1 lds\n"
"v_mfma_f32_16x16x32_bf16 v[40:43], a[104:107], v[180:183], v[40:43]\n"
"ds_read_b128 v[172:175], v4 offset:60416\n"
"v_mfma_f32_16x16x32_bf16 v[40:43], a[96:99], v[176:179], v[40:43]\n"
"s_waitcnt lgkmcnt(4)\n"
"v_mfma_f32_16x16x32_bf16 v[40:43], a[128:131], v[128:131], v[40:43]\n"
"ds_read_b128 v[176:179], v4 offset:61440\n"
"v_mfma_f32_16x16x32_bf16 v[40:43], a[136:139], v[132:135], v[40:43]\n"
"buffer_load_dwordx4 v6, s[32:35], s63 offen offset:3072 sc1 lds\n"
"v_mfma_f32_16x16x32_bf16 v[40:43], a[144:147], v[136:139], v[40:43]\n"
"ds_read_b128 v[180:183], v4 offset:62464\n"
"s_add_u32 s63, s63, 593920\n"
"v_mfma_f32_16x16x32_bf16 v[40:43], a[152:155], v[140:143], v[40:43]\n"
"s_xor_b32 s64, s64, s65\n"
"v_mfma_f32_16x16x32_bf16 v[44:47], a[156:159], v[140:143], v[44:47]\n"
"s_waitcnt vmcnt(11)\n"
"s_barrier\n"
"v_mfma_f32_16x16x32_bf16 v[44:47], a[148:151], v[136:139], v[44:47]\n"
"ds_read_b128 v[184:187], v4 offset:63488\n"
"v_mfma_f32_16x16x32_bf16 v[44:47], a[140:143], v[132:135], v[44:47]\n"
"ds_read_b128 v[188:191], v4 offset:64512\n"
"v_xor_b32 v4, 65536, v4\n"
"v_mfma_f32_16x16x32_bf16 v[44:47], a[132:135], v[128:131], v[44:47]\n"
"v_mfma_f32_16x16x32_bf16 v[44:47], a[164:167], v[144:147], v[44:47]\n"
"ds_read_b128 v[128:131], v4 offset:0\n"
"v_lshlrev_b32 v32, 16, v48\n"
"v_mfma_f32_16x16x32_bf16 v[44:47], a[172:175], v[148:151], v[44:47]\n"
"v_and_b32 v33, 0xffff0000, v48\n"
"v_lshlrev_b32 v34, 16, v49\n"
"v_mfma_f32_16x16x32_bf16 v[44:47], a[180:183], v[152:155], v[44:47]\n"
"ds_read_b128 v[132:135], v4 offset:1024\n"
"v_and_b32 v35, 0xffff0000, v49\n"
"v_mfma_f32_16x16x32_bf16 v[44:47], a[188:191], v[156:159], v[44:47]\n"
"v_lshlrev_b32 v36, 16, v50\n"
"v_and_b32 v37, 0xffff0000, v50\n"
"v_mfma_f32_16x16x32_bf16 v[40:43], a[184:187], v[156:159], v[40:43]\n"
"ds_read_b128 v[136:139], v4 offset:2048\n"
"s_add_u32 m0, s64, 0\n"
"v_mfma_f32_16x16x32_bf16 v[40:43], a[176:179], v[152:155], v[40:43]\n"
"buffer_load_dwordx4 v2, s[32:35], s63 offen offset:0 sc1 lds\n"
"v_mfma_f32_16x16x32_bf16 v[40:43], a[168:171], v[148:151], v[40:43]\n"
"ds_read_b128 v[140:143], v4 offset:3072\n"
"v_lshlrev_b32 v38, 16, v51\n"
"v_mfma_f32_16x16x32_bf16 v[40:43], a[160:163], v[144:147], v[40:43]\n"
"v_and_b32 v39, 0xffff0000, v51\n"
"s_waitcnt lgkmcnt(4)\n"
"v_mfma_f32_16x16x32_bf16 v[40:43], a[192:195], v[160:163], v[40:43]\n"
"ds_read_b128 v[144:147], v4 offset:4096\n"
"v_mfma_f32_16x16x32_bf16 v[40:43], a[200:203], v[164:167], v[40:43]\n"
"buffer_load_dwordx4 v2, s[32:35], s63 offen offset:1024 sc1 lds\n"
"v_mfma_f32_16x16x32_bf16 v[40:43], a[208:211], v[168:171], v[40:43]\n"
"ds_read_b128 v[148:151], v4 offset:5120\n"
"v_mfma_f32_16x16x32_bf16 v[40:43], a[216:219], v[172:175], v[40:43]\n"
"v_mfma_f32_16x16x32_bf16 v[44:47], a[220:223], v[172:175], v[44:47]\n"
"ds_read_b128 v[152:155], v4 offset:6144\n"
"v_mfma_f32_16x16x32_bf16 v[44:47], a[212:215], v[168:171], v[44:47]\n"
"buffer_load_dwordx4 v2, s[32:35], s63 offen offset:2048 sc1 lds\n"
"v_mfma_f32_16x16x32_bf16 v[44:47], a[204:207], v[164:167], v[44:47]\n"
"ds_read_b128 v[156:159], v4 offset:7168\n"
"v_mfma_f32_16x16x32_bf16 v[44:47], a[196:199], v[160:163], v[44:47]\n"
"v_mfma_f32_16x16x32_bf16 v[44:47], a[228:231], v[176:179], v[44:47]\n"
"ds_read_b128 v[160:163], v4 offset:8192\n"
"v_mfma_f32_16x16x32_bf16 v[44:47], a[236:239], v[180:183], v[44:47]\n"
"buffer_load_dwordx4 v2, s[32:35], s63 offen offset:3072 sc1 lds\n"
"v_mfma_f32_16x16x32_bf16 v[44:47], a[244:247], v[184:187], v[44:47]\n"
"ds_read_b128 v[164:167], v4 offset:9216\n"
"v_mfma_f32_16x16x32_bf16 v[44:47], a[252:255], v[188:191], v[44:47]\n"
"v_mfma_f32_16x16x32_bf16 v[40:43], a[248:251], v[188:191], v[40:43]\n"
"ds_read_b128 v[168:171], v4 offset:10240\n"
"s_add_u32 m0, s64, 4096\n"
"v_mfma_f32_16x16x32_bf16 v[40:43], a[240:243], v[184:187], v[40:43]\n"
"buffer_load_dwordx4 v6, s[32:35], s63 offen offset:0 sc1 lds\n"
"v_mfma_f32_16x16x32_bf16 v[40:43], a[232:235], v[180:183], v[40:43]\n"
"ds_read_b128 v[172:175], v4 offset:11264\n"
"s_sub_u32 s62, s62, 1\n"
"v_mfma_f32_16x16x32_bf16 v[40:43], a[224:227], v[176:179], v[40:43]\n"
"s_cbranch_scc0 .L_wl_3\n"
"s_nop 1\n"
"s_add_u32 s68, s68, 593920\n"
"s_nop 3\n"
"v_cvt_pk_bf16_f32 v56, v40, v41\n"
"v_cvt_pk_bf16_f32 v57, v42, v43\n"
"v_cvt_pk_bf16_f32 v58, v44, v45\n"
"v_cvt_pk_bf16_f32 v59, v46, v47\n"
"buffer_store_dwordx4 v[56:59], v2, s[32:35], s68 offen sc1\n"
"v_add_f32 v60, v40, v41\n"
"v_mul_f32 v61, v40, v40\n"
"v_fmac_f32 v61, v41, v41\n"
"v_add_f32 v60, v60, v42\n"
"v_fmac_f32 v61, v42, v42\n"
"v_add_f32 v60, v60, v43\n"
"v_fmac_f32 v61, v43, v43\n"
"v_add_f32 v60, v60, v44\n"
"v_fmac_f32 v61, v44, v44\n"
"v_add_f32 v60, v60, v45\n"
"v_fmac_f32 v61, v45, v45\n"
"v_add_f32 v60, v60, v46\n"
"v_fmac_f32 v61, v46, v46\n"
"v_add_f32 v60, v60, v47\n"
"v_fmac_f32 v61, v47, v47\n"
"s_nop 1\n"
"v_permlane16_swap_b32 v60, v61\n"
"v_add_f32 v60, v60, v61\n"
"s_nop 1\n"
"v_permlane32_swap_b32 v60, v61\n"
"v_add_f32 v60, v60, v61\n"
"buffer_store_dword v60, v15, s[32:35], s68 offen offset:1024 sc1\n"
"s_waitcnt vmcnt(9)\n"
"s_barrier\n"
"buffer_store_dwordx2 v[16:17], v2, s[92:95], s68 idxen sc1\n"
"s_add_u32 s68, s68, 593920\n"
"s_waitcnt vmcnt(1)\n"
"s_barrier\n"
"buffer_store_dwordx2 v[16:17], v2, s[92:95], s68 idxen sc1\n"
"s_branch .L_end\n"
".L_h:\n"
"v_mov_b32 v16, 0x3f803f80\n"
"v_mov_b32 v17, 0x3f803f80\n"
"v_mov_b32 v18, 0x3f803f80\n"
"v_mov_b32 v19, 0x3f803f80\n"
"s_add_u32 s36, s14, 0x0\n"
"s_addc_u32 s37, s15, 0x0\n"
"s_and_b32 s37, s37, 0xffff\n"
"s_mov_b32 s38, 0x700000\n"
"s_mov_b32 s39, 0x20000\n"
"s_add_u32 s40, s16, 0xfffffc00\n"
"s_addc_u32 s41, s17, 0xffffffff\n"
"s_and_b32 s41, s41, 0xffff\n"
"s_mov_b32 s42, 0x700400\n"
"s_mov_b32 s43, 0x20000\n"
"s_lshl_b32 s96, s27, 7\n"
"s_add_u32 s96, s96, 6912\n"
"v_and_b32 v8, 1, v1\n"
"v_bfe_u32 v3, v1, 1, 1\n"
"v_lshl_add_u32 v8, v8, 1, v3\n"
"v_mul_u32_u24 v8, 8, v8\n"
"s_lshl_b32 s48, s26, 5\n"
"v_add_u32 v8, s48, v8\n"
"v_bfe_u32 v3, v1, 4, 1\n"
"v_lshl_add_u32 v8, v3, 10, v8\n"
"v_lshrrev_b32 v4, 3, v1\n"
"v_bfe_u32 v5, v1, 4, 1\n"
"s_and_b32 s48, s26, 1\n"
"s_lshl_b32 s48, s48, 1\n"
"v_or_b32 v5, s48, v5\n"
"v_lshlrev_b32 v5, 1, v5\n"
"v_and_b32 v6, 7, v1\n"
"v_xor_b32 v5, v5, v6\n"
"v_mul_u32_u24 v4, 7168, v4\n"
"v_lshl_add_u32 v7, v5, 4, v4\n"
"s_mul_i32 s48, s26, 57344\n"
"s_add_u32 s96, s96, s48\n"
"s_mul_i32 s97, s26, 2048\n"
"s_add_u32 m0, s97, 0\n"
"s_nop 0\n"
"buffer_load_dwordx4 v7, s[36:39], s96 offen offset:0 lds\n"
"buffer_load_dwordx4 v7, s[40:43], s96 offen offset:1024 lds\n"
"s_add_u32 s96, s96, 229376\n"
"s_add_u32 m0, s97, 8192\n"
"s_nop 0\n"
"buffer_load_dwordx4 v7, s[36:39], s96 offen offset:0 lds\n"
"buffer_load_dwordx4 v7, s[40:43], s96 offen offset:1024 lds\n"
"s_add_u32 s96, s96, 229376\n"
"s_add_u32 m0, s97, 16384\n"
"s_nop 0\n"
"buffer_load_dwordx4 v7, s[36:39], s96 offen offset:0 lds\n"
"buffer_load_dwordx4 v7, s[40:43], s96 offen offset:1024 lds\n"
"s_add_u32 s96, s96, 229376\n"
"s_add_u32 m0, s97, 24576\n"
"s_nop 0\n"
"buffer_load_dwordx4 v7, s[36:39], s96 offen offset:0 lds\n"
"buffer_load_dwordx4 v7, s[40:43], s96 offen offset:1024 lds\n"
"s_add_u32 s96, s96, 229376\n"
"s_add_u32 m0, s97, 32768\n"
"s_nop 0\n"
"buffer_load_dwordx4 v7, s[36:39], s96 offen offset:0 lds\n"
"buffer_load_dwordx4 v7, s[40:43], s96 offen offset:1024 lds\n"
"s_add_u32 s96, s96, 229376\n"
"s_add_u32 m0, s97, 40960\n"
"s_nop 0\n"
"buffer_load_dwordx4 v7, s[36:39], s96 offen offset:0 lds\n"
"buffer_load_dwordx4 v7, s[40:43], s96 offen offset:1024 lds\n"
"s_add_u32 s96, s96, 229376\n"
"s_add_u32 m0, s97, 49152\n"
"s_nop 0\n"
"buffer_load_dwordx4 v7, s[36:39], s96 offen offset:0 lds\n"
"buffer_load_dwordx4 v7, s[40:43], s96 offen offset:1024 lds\n"
"s_add_u32 s96, s96, 229376\n"
"s_add_u32 m0, s97, 57344\n"
"s_nop 0\n"
"buffer_load_dwordx4 v7, s[36:39], s96 offen offset:0 lds\n"
"buffer_load_dwordx4 v7, s[40:43], s96 offen offset:1024 lds\n"
"s_add_u32 s96, s96, 229376\n"
"s_add_u32 m0, s97, 65536\n"
"s_nop 0\n"
"buffer_load_dwordx4 v7, s[36:39], s96 offen offset:0 lds\n"
"buffer_load_dwordx4 v7, s[40:43], s96 offen offset:1024 lds\n"
"s_add_u32 s96, s96, 229376\n"
"s_add_u32 m0, s97, 73728\n"
"s_nop 0\n"
"buffer_load_dwordx4 v7, s[36:39], s96 offen offset:0 lds\n"
"buffer_load_dwordx4 v7, s[40:43], s96 offen offset:1024 lds\n"
"s_add_u32 s96, s96, 229376\n"
"s_add_u32 m0, s97, 81920\n"
"s_nop 0\n"
"buffer_load_dwordx4 v7, s[36:39], s96 offen offset:0 lds\n"
"buffer_load_dwordx4 v7, s[40:43], s96 offen offset:1024 lds\n"
"s_add_u32 s96, s96, 229376\n"
"s_add_u32 m0, s97, 90112\n"
"s_nop 0\n"
"buffer_load_dwordx4 v7, s[36:39], s96 offen offset:0 lds\n"
"buffer_load_dwordx4 v7, s[40:43], s96 offen offset:1024 lds\n"
"s_add_u32 s96, s96, 229376\n"
"s_add_u32 m0, s97, 98304\n"
"s_nop 0\n"
"buffer_load_dwordx4 v7, s[36:39], s96 offen offset:0 lds\n"
"buffer_load_dwordx4 v7, s[40:43], s96 offen offset:1024 lds\n"
"s_add_u32 s96, s96, 229376\n"
"s_add_u32 m0, s97, 106496\n"
"s_nop 0\n"
"buffer_load_dwordx4 v7, s[36:39], s96 offen offset:0 lds\n"
"buffer_load_dwordx4 v7, s[40:43], s96 offen offset:1024 lds\n"
"s_add_u32 s96, s96, 229376\n"
"v_lshrrev_b32 v5, 5, v1\n"
"v_bfe_u32 v4, v1, 3, 1\n"
"v_and_b32 v6, 1, v5\n"
"v_lshl_add_u32 v4, v6, 1, v4\n"
"v_lshlrev_b32 v4, 5, v4\n"
"v_mul_u32_u24 v5, 2048, v5\n"
"v_bfe_u32 v6, v1, 2, 2\n"
"v_lshl_add_u32 v5, v6, 7, v5\n"
"v_xor_b32 v8, v4, v8\n"
"v_add_u32 v8, v5, v8\n"
"v_add_u32 v9, 65536, v8\n"
"s_waitcnt vmcnt(24)\n"
"s_barrier\n"
"s_add_u32 m0, s97, 114688\n"
"s_nop 0\n"
"buffer_load_dwordx4 v7, s[36:39], s96 offen offset:0 lds\n"
"buffer_load_dwordx4 v7, s[40:43], s96 offen offset:1024 lds\n"
"s_add_u32 s96, s96, 229376\n"
"s_add_u32 m0, s97, 122880\n"
"s_nop 0\n"
"buffer_load_dwordx4 v7, s[36:39], s96 offen offset:0 lds\n"
"buffer_load_dwordx4 v7, s[40:43], s96 offen offset:1024 lds\n"
"s_add_u32 s96, s96, 229376\n"
"ds_read_b64_tr_b16 a[0:1], v8 offset:0\n"
"ds_read_b64_tr_b16 a[2:3], v8 offset:512\n"
"ds_read_b64_tr_b16 a[4:5], v8 offset:4096\n"
"ds_read_b64_tr_b16 a[6:7], v8 offset:4608\n"
"ds_read_b64_tr_b16 a[32:33], v8 offset:8192\n"
"ds_read_b64_tr_b16 a[34:35], v8 offset:8704\n"
"ds_read_b64_tr_b16 a[36:37], v8 offset:12288\n"
"ds_read_b64_tr_b16 a[38:39], v8 offset:12800\n"
"s_waitcnt lgkmcnt(0)\n"
"v_mfma_f32_32x32x16_bf16 v[112:127], a[0:3], v[16:19], 0\n"
"v_mfma_f32_32x32x16_bf16 v[112:127], a[4:7], v[16:19], v[112:127]\n"
"v_mfma_f32_32x32x16_bf16 v[112:127], a[32:35], v[16:19], v[112:127]\n"
"v_mfma_f32_32x32x16_bf16 v[112:127], a[36:39], v[16:19], v[112:127]\n"
"s_waitcnt vmcnt(24)\n"
"s_barrier\n"
"s_add_u32 m0, s97, 0\n"
"s_nop 0\n"
"buffer_load_dwordx4 v7, s[36:39], s96 offen offset:0 lds\n"
"buffer_load_dwordx4 v7, s[40:43], s96 offen offset:1024 lds\n"
"s_add_u32 s96, s96, 229376\n"
"s_add_u32 m0, s97, 8192\n"
"s_nop 0\n"
"buffer_load_dwordx4 v7, s[36:39], s96 offen offset:0 lds\n"
"buffer_load_dwordx4 v7, s[40:43], s96 offen offset:1024 lds\n"
"s_add_u32 s96, s96, 229376\n"
"ds_read_b64_tr_b16 a[64:65], v8 offset:16384\n"
"ds_read_b64_tr_b16 a[66:67], v8 offset:16896\n"
"ds_read_b64_tr_b16 a[68:69], v8 offset:20480\n"
"ds_read_b64_tr_b16 a[70:71], v8 offset:20992\n"
"ds_read_b64_tr_b16 a[96:97], v8 offset:24576\n"
"ds_read_b64_tr_b16 a[98:99], v8 offset:25088\n"
"ds_read_b64_tr_b16 a[100:101], v8 offset:28672\n"
"ds_read_b64_tr_b16 a[102:103], v8 offset:29184\n"
"s_waitcnt lgkmcnt(0)\n"
"v_mfma_f32_32x32x16_bf16 v[112:127], a[64:67], v[16:19], v[112:127]\n"
"v_mfma_f32_32x32x16_bf16 v[112:127], a[68:71], v[16:19], v[112:127]\n"
"v_mfma_f32_32x32x16_bf16 v[112:127], a[96:99], v[16:19], v[112:127]\n"
"v_mfma_f32_32x32x16_bf16 v[112:127], a[100:103], v[16:19], v[112:127]\n"
"s_waitcnt vmcnt(24)\n"
"s_barrier\n"
"s_add_u32 m0, s97, 16384\n"
"s_nop 0\n"
"buffer_load_dwordx4 v7, s[36:39], s96 offen offset:0 lds\n"
"buffer_load_dwordx4 v7, s[40:43], s96 offen offset:1024 lds\n"
"s_add_u32 s96, s96, 229376\n"
"s_add_u32 m0, s97, 24576\n"
"s_nop 0\n"
"buffer_load_dwordx4 v7, s[36:39], s96 offen offset:0 lds\n"
"buffer_load_dwordx4 v7, s[40:43], s96 offen offset:1024 lds\n"
"s_add_u32 s96, s96, 229376\n"
"ds_read_b64_tr_b16 a[128:129], v8 offset:32768\n"
"ds_read_b64_tr_b16 a[130:131], v8 offset:33280\n"
"ds_read_b64_tr_b16 a[132:133], v8 offset:36864\n"
"ds_read_b64_tr_b16 a[134:135], v8 offset:37376\n"
"ds_read_b64_tr_b16 a[160:161], v8 offset:40960\n"
"ds_read_b64_tr_b16 a[162:163], v8 offset:41472\n"
"ds_read_b64_tr_b16 a[164:165], v8 offset:45056\n"
"ds_read_b64_tr_b16 a[166:167], v8 offset:45568\n"
"s_waitcnt lgkmcnt(0)\n"
"v_mfma_f32_32x32x16_bf16 v[112:127], a[128:131], v[16:19], v[112:127]\n"
"v_mfma_f32_32x32x16_bf16 v[112:127], a[132:135], v[16:19], v[112:127]\n"
"v_mfma_f32_32x32x16_bf16 v[112:127], a[160:163], v[16:19], v[112:127]\n"
"v_mfma_f32_32x32x16_bf16 v[112:127], a[164:167], v[16:19], v[112:127]\n"
"s_waitcnt vmcnt(24)\n"
"s_barrier\n"
"s_add_u32 m0, s97, 32768\n"
"s_nop 0\n"
"buffer_load_dwordx4 v7, s[36:39], s96 offen offset:0 lds\n"
"buffer_load_dwordx4 v7, s[40:43], s96 offen offset:1024 lds\n"
"s_add_u32 s96, s96, 229376\n"
"s_add_u32 m0, s97, 40960\n"
"s_nop 0\n"
"buffer_load_dwordx4 v7, s[36:39], s96 offen offset:0 lds\n"
"buffer_load_dwordx4 v7, s[40:43], s96 offen offset:1024 lds\n"
"s_add_u32 s96, s96, 229376\n"
"ds_read_b64_tr_b16 a[192:193], v8 offset:49152\n"
"ds_read_b64_tr_b16 a[194:195], v8 offset:49664\n"
"ds_read_b64_tr_b16 a[196:197], v8 offset:53248\n"
"ds_read_b64_tr_b16 a[198:199], v8 offset:53760\n"
"ds_read_b64_tr_b16 a[224:225], v8 offset:57344\n"
"ds_read_b64_tr_b16 a[226:227], v8 offset:57856\n"
"ds_read_b64_tr_b16 a[228:229], v8 offset:61440\n"
"ds_read_b64_tr_b16 a[230:231], v8 offset:61952\n"
"s_waitcnt lgkmcnt(0)\n"
"v_mfma_f32_32x32x16_bf16 v[112:127], a[192:195], v[16:19], v[112:127]\n"
"v_mfma_f32_32x32x16_bf16 v[112:127], a[196:199], v[16:19], v[112:127]\n"
"v_mfma_f32_32x32x16_bf16 v[112:127], a[224:227], v[16:19], v[112:127]\n"
"v_mfma_f32_32x32x16_bf16 v[112:127], a[228:231], v[16:19], v[112:127]\n"
"s_waitcnt vmcnt(24)\n"
"s_barrier\n"
"s_add_u32 m0, s97, 49152\n"
"s_nop 0\n"
"buffer_load_dwordx4 v7, s[36:39], s96 offen offset:0 lds\n"
"buffer_load_dwordx4 v7, s[40:43], s96 offen offset:1024 lds\n"
"s_add_u32 s96, s96, 229376\n"
"s_add_u32 m0, s97, 57344\n"
"s_nop 0\n"
"buffer_load_dwordx4 v7, s[36:39], s96 offen offset:0 lds\n"
"buffer_load_dwordx4 v7, s[40:43], s96 offen offset:1024 lds\n"
"s_add_u32 s96, s96, 229376\n"
"ds_read_b64_tr_b16 a[8:9], v9 offset:0\n"
"ds_read_b64_tr_b16 a[10:11], v9 offset:512\n"
"ds_read_b64_tr_b16 a[12:13], v9 offset:4096\n"
"ds_read_b64_tr_b16 a[14:15], v9 offset:4608\n"
"ds_read_b64_tr_b16 a[40:41], v9 offset:8192\n"
"ds_read_b64_tr_b16 a[42:43], v9 offset:8704\n"
"ds_read_b64_tr_b16 a[44:45], v9 offset:12288\n"
"ds_read_b64_tr_b16 a[46:47], v9 offset:12800\n"
"s_waitcnt lgkmcnt(0)\n"
"v_mfma_f32_32x32x16_bf16 v[112:127], a[8:11], v[16:19], v[112:127]\n"
"v_mfma_f32_32x32x16_bf16 v[112:127], a[12:15], v[16:19], v[112:127]\n"
"v_mfma_f32_32x32x16_bf16 v[112:127], a[40:43], v[16:19], v[112:127]\n"
"v_mfma_f32_32x32x16_bf16 v[112:127], a[44:47], v[16:19], v[112:127]\n"
"s_waitcnt vmcnt(24)\n"
"s_barrier\n"
"s_add_u32 m0, s97, 65536\n"
"s_nop 0\n"
"buffer_load_dwordx4 v7, s[36:39], s96 offen offset:0 lds\n"
"buffer_load_dwordx4 v7, s[40:43], s96 offen offset:1024 lds\n"
"s_add_u32 s96, s96, 229376\n"
"s_add_u32 m0, s97, 73728\n"
"s_nop 0\n"
"buffer_load_dwordx4 v7, s[36:39], s96 offen offset:0 lds\n"
"buffer_load_dwordx4 v7, s[40:43], s96 offen offset:1024 lds\n"
"s_add_u32 s96, s96, 229376\n"
"ds_read_b64_tr_b16 a[72:73], v9 offset:16384\n"
"ds_read_b64_tr_b16 a[74:75], v9 offset:16896\n"
"ds_read_b64_tr_b16 a[76:77], v9 offset:20480\n"
"ds_read_b64_tr_b16 a[78:79], v9 offset:20992\n"
"ds_read_b64_tr_b16 a[104:105], v9 offset:24576\n"
"ds_read_b64_tr_b16 a[106:107], v9 offset:25088\n"
"ds_read_b64_tr_b16 a[108:109], v9 offset:28672\n"
"ds_read_b64_tr_b16 a[110:111], v9 offset:29184\n"
"s_waitcnt lgkmcnt(0)\n"
"v_mfma_f32_32x32x16_bf16 v[112:127], a[72:75], v[16:19], v[112:127]\n"
"v_mfma_f32_32x32x16_bf16 v[112:127], a[76:79], v[16:19], v[112:127]\n"
"v_mfma_f32_32x32x16_bf16 v[112:127], a[104:107], v[16:19], v[112:127]\n"
"v_mfma_f32_32x32x16_bf16 v[112:127], a[108:111], v[16:19], v[112:127]\n"
"s_waitcnt vmcnt(24)\n"
"s_barrier\n"
"s_add_u32 m0, s97, 81920\n"
"s_nop 0\n"
"buffer_load_dwordx4 v7, s[36:39], s96 offen offset:0 lds\n"
"buffer_load_dwordx4 v7, s[40:43], s96 offen offset:1024 lds\n"
"s_add_u32 s96, s96, 229376\n"
"s_add_u32 m0, s97, 90112\n"
"s_nop 0\n"
"buffer_load_dwordx4 v7, s[36:39], s96 offen offset:0 lds\n"
"buffer_load_dwordx4 v7, s[40:43], s96 offen offset:1024 lds\n"
"s_add_u32 s96, s96, 229376\n"
"ds_read_b64_tr_b16 a[136:137], v9 offset:32768\n"
"ds_read_b64_tr_b16 a[138:139], v9 offset:33280\n"
"ds_read_b64_tr_b16 a[140:141], v9 offset:36864\n"
"ds_read_b64_tr_b16 a[142:143], v9 offset:37376\n"
"ds_read_b64_tr_b16 a[168:169], v9 offset:40960\n"
"ds_read_b64_tr_b16 a[170:171], v9 offset:41472\n"
"ds_read_b64_tr_b16 a[172:173], v9 offset:45056\n"
"ds_read_b64_tr_b16 a[174:175], v9 offset:45568\n"
"s_waitcnt lgkmcnt(0)\n"
"v_mfma_f32_32x32x16_bf16 v[112:127], a[136:139], v[16:19], v[112:127]\n"
"v_mfma_f32_32x32x16_bf16 v[112:127], a[140:143], v[16:19], v[112:127]\n"
"v_mfma_f32_32x32x16_bf16 v[112:127], a[168:171], v[16:19], v[112:127]\n"
"v_mfma_f32_32x32x16_bf16 v[112:127], a[172:175], v[16:19], v[112:127]\n"
"s_waitcnt vmcnt(24)\n"
"s_barrier\n"
"s_add_u32 m0, s97, 98304\n"
"s_nop 0\n"
"buffer_load_dwordx4 v7, s[36:39], s96 offen offset:0 lds\n"
"buffer_load_dwordx4 v7, s[40:43], s96 offen offset:1024 lds\n"
"s_add_u32 s96, s96, 229376\n"
"s_add_u32 m0, s97, 106496\n"
"s_nop 0\n"
"buffer_load_dwordx4 v7, s[36:39], s96 offen offset:0 lds\n"
"buffer_load_dwordx4 v7, s[40:43], s96 offen offset:1024 lds\n"
"s_add_u32 s96, s96, 229376\n"
"ds_read_b64_tr_b16 a[200:201], v9 offset:49152\n"
"ds_read_b64_tr_b16 a[202:203], v9 offset:49664\n"
"ds_read_b64_tr_b16 a[204:205], v9 offset:53248\n"
"ds_read_b64_tr_b16 a[206:207], v9 offset:53760\n"
"ds_read_b64_tr_b16 a[232:233], v9 offset:57344\n"
"ds_read_b64_tr_b16 a[234:235], v9 offset:57856\n"
"ds_read_b64_tr_b16 a[236:237], v9 offset:61440\n"
"ds_read_b64_tr_b16 a[238:239], v9 offset:61952\n"
"s_waitcnt lgkmcnt(0)\n"
"v_mfma_f32_32x32x16_bf16 v[112:127], a[200:203], v[16:19], v[112:127]\n"
"v_mfma_f32_32x32x16_bf16 v[112:127], a[204:207], v[16:19], v[112:127]\n"
"v_mfma_f32_32x32x16_bf16 v[112:127], a[232:235], v[16:19], v[112:127]\n"
"v_mfma_f32_32x32x16_bf16 v[112:127], a[236:239], v[16:19], v[112:127]\n"
"s_waitcnt vmcnt(24)\n"
"s_barrier\n"
"s_add_u32 m0, s97, 114688\n"
"s_nop 0\n"
"buffer_load_dwordx4 v7, s[36:39], s96 offen offset:0 lds\n"
"buffer_load_dwordx4 v7, s[40:43], s96 offen offset:1024 lds\n"
"s_add_u32 s96, s96, 229376\n"
"s_add_u32 m0, s97, 122880\n"
"s_nop 0\n"
"buffer_load_dwordx4 v7, s[36:39], s96 offen offset:0 lds\n"
"buffer_load_dwordx4 v7, s[40:43], s96 offen offset:1024 lds\n"
"s_add_u32 s96, s96, 229376\n"
"ds_read_b64_tr_b16 a[16:17], v8 offset:0\n"
"ds_read_b64_tr_b16 a[18:19], v8 offset:512\n"
"ds_read_b64_tr_b16 a[20:21], v8 offset:4096\n"
"ds_read_b64_tr_b16 a[22:23], v8 offset:4608\n"
"ds_read_b64_tr_b16 a[48:49], v8 offset:8192\n"
"ds_read_b64_tr_b16 a[50:51], v8 offset:8704\n"
"ds_read_b64_tr_b16 a[52:53], v8 offset:12288\n"
"ds_read_b64_tr_b16 a[54:55], v8 offset:12800\n"
"s_waitcnt lgkmcnt(0)\n"
"v_mfma_f32_32x32x16_bf16 v[112:127], a[16:19], v[16:19], v[112:127]\n"
"v_mfma_f32_32x32x16_bf16 v[112:127], a[20:23], v[16:19], v[112:127]\n"
"v_mfma_f32_32x32x16_bf16 v[112:127], a[48:51], v[16:19], v[112:127]\n"
"v_mfma_f32_32x32x16_bf16 v[112:127], a[52:55], v[16:19], v[112:127]\n"
"s_waitcnt vmcnt(24)\n"
"s_barrier\n"
"ds_read_b64_tr_b16 a[80:81], v8 offset:16384\n"
"ds_read_b64_tr_b16 a[82:83], v8 offset:16896\n"
"ds_read_b64_tr_b16 a[84:85], v8 offset:20480\n"
"ds_read_b64_tr_b16 a[86:87], v8 offset:20992\n"
"ds_read_b64_tr_b16 a[112:113], v8 offset:24576\n"
"ds_read_b64_tr_b16 a[114:115], v8 offset:25088\n"
"ds_read_b64_tr_b16 a[116:117], v8 offset:28672\n"
"ds_read_b64_tr_b16 a[118:119], v8 offset:29184\n"
"s_waitcnt lgkmcnt(0)\n"
"v_mfma_f32_32x32x16_bf16 v[112:127], a[80:83], v[16:19], v[112:127]\n"
"v_mfma_f32_32x32x16_bf16 v[112:127], a[84:87], v[16:19], v[112:127]\n"
"v_mfma_f32_32x32x16_bf16 v[112:127], a[112:115], v[16:19], v[112:127]\n"
"v_mfma_f32_32x32x16_bf16 v[112:127], a[116:119], v[16:19], v[112:127]\n"
"s_waitcnt vmcnt(20)\n"
"s_barrier\n"
"ds_read_b64_tr_b16 a[144:145], v8 offset:32768\n"
"ds_read_b64_tr_b16 a[146:147], v8 offset:33280\n"
"ds_read_b64_tr_b16 a[148:149], v8 offset:36864\n"
"ds_read_b64_tr_b16 a[150:151], v8 offset:37376\n"
"ds_read_b64_tr_b16 a[176:177], v8 offset:40960\n"
"ds_read_b64_tr_b16 a[178:179], v8 offset:41472\n"
"ds_read_b64_tr_b16 a[180:181], v8 offset:45056\n"
"ds_read_b64_tr_b16 a[182:183], v8 offset:45568\n"
"s_waitcnt lgkmcnt(0)\n"
"v_mfma_f32_32x32x16_bf16 v[112:127], a[144:147], v[16:19], v[112:127]\n"
"v_mfma_f32_32x32x16_bf16 v[112:127], a[148:151], v[16:19], v[112:127]\n"
"v_mfma_f32_32x32x16_bf16 v[112:127], a[176:179], v[16:19], v[112:127]\n"
"v_mfma_f32_32x32x16_bf16 v[112:127], a[180:183], v[16:19], v[112:127]\n"
"s_waitcnt vmcnt(16)\n"
"s_barrier\n"
"ds_read_b64_tr_b16 a[208:209], v8 offset:49152\n"
"ds_read_b64_tr_b16 a[210:211], v8 offset:49664\n"
"ds_read_b64_tr_b16 a[212:213], v8 offset:53248\n"
"ds_read_b64_tr_b16 a[214:215], v8 offset:53760\n"
"ds_read_b64_tr_b16 a[240:241], v8 offset:57344\n"
"ds_read_b64_tr_b16 a[242:243], v8 offset:57856\n"
"ds_read_b64_tr_b16 a[244:245], v8 offset:61440\n"
"ds_read_b64_tr_b16 a[246:247], v8 offset:61952\n"
"s_waitcnt lgkmcnt(0)\n"
"v_mfma_f32_32x32x16_bf16 v[112:127], a[208:211], v[16:19], v[112:127]\n"
"v_mfma_f32_32x32x16_bf16 v[112:127], a[212:215], v[16:19], v[112:127]\n"
"v_mfma_f32_32x32x16_bf16 v[112:127], a[240:243], v[16:19], v[112:127]\n"
"v_mfma_f32_32x32x16_bf16 v[112:127], a[244:247], v[16:19], v[112:127]\n"
"s_waitcnt vmcnt(12)\n"
"s_barrier\n"
"ds_read_b64_tr_b16 a[24:25], v9 offset:0\n"
"ds_read_b64_tr_b16 a[26:27], v9 offset:512\n"
"ds_read_b64_tr_b16 a[28:29], v9 offset:4096\n"
"ds_read_b64_tr_b16 a[30:31], v9 offset:4608\n"
"ds_read_b64_tr_b16 a[56:57], v9 offset:8192\n"
"ds_read_b64_tr_b16 a[58:59], v9 offset:8704\n"
"ds_read_b64_tr_b16 a[60:61], v9 offset:12288\n"
"ds_read_b64_tr_b16 a[62:63], v9 offset:12800\n"
"s_waitcnt lgkmcnt(0)\n"
"v_mfma_f32_32x32x16_bf16 v[112:127], a[24:27], v[16:19], v[112:127]\n"
"v_mfma_f32_32x32x16_bf16 v[112:127], a[28:31], v[16:19], v[112:127]\n"
"v_mfma_f32_32x32x16_bf16 v[112:127], a[56:59], v[16:19], v[112:127]\n"
"v_mfma_f32_32x32x16_bf16 v[112:127], a[60:63], v[16:19], v[112:127]\n"
"s_waitcnt vmcnt(8)\n"
"s_barrier\n"
"ds_read_b64_tr_b16 a[88:89], v9 offset:16384\n"
"ds_read_b64_tr_b16 a[90:91], v9 offset:16896\n"
"ds_read_b64_tr_b16 a[92:93], v9 offset:20480\n"
"ds_read_b64_tr_b16 a[94:95], v9 offset:20992\n"
"ds_read_b64_tr_b16 a[120:121], v9 offset:24576\n"
"ds_read_b64_tr_b16 a[122:123], v9 offset:25088\n"
"ds_read_b64_tr_b16 a[124:125], v9 offset:28672\n"
"ds_read_b64_tr_b16 a[126:127], v9 offset:29184\n"
"s_waitcnt lgkmcnt(0)\n"
"v_mfma_f32_32x32x16_bf16 v[112:127], a[88:91], v[16:19], v[112:127]\n"
"v_mfma_f32_32x32x16_bf16 v[112:127], a[92:95], v[16:19], v[112:127]\n"
"v_mfma_f32_32x32x16_bf16 v[112:127], a[120:123], v[16:19], v[112:127]\n"
"v_mfma_f32_32x32x16_bf16 v[112:127], a[124:127], v[16:19], v[112:127]\n"
"s_waitcnt vmcnt(4)\n"
"s_barrier\n"
"ds_read_b64_tr_b16 a[152:153], v9 offset:32768\n"
"ds_read_b64_tr_b16 a[154:155], v9 offset:33280\n"
"ds_read_b64_tr_b16 a[156:157], v9 offset:36864\n"
"ds_read_b64_tr_b16 a[158:159], v9 offset:37376\n"
"ds_read_b64_tr_b16 a[184:185], v9 offset:40960\n"
"ds_read_b64_tr_b16 a[186:187], v9 offset:41472\n"
"ds_read_b64_tr_b16 a[188:189], v9 offset:45056\n"
"ds_read_b64_tr_b16 a[190:191], v9 offset:45568\n"
"s_waitcnt lgkmcnt(0)\n"
"v_mfma_f32_32x32x16_bf16 v[112:127], a[152:155], v[16:19], v[112:127]\n"
"v_mfma_f32_32x32x16_bf16 v[112:127], a[156:159], v[16:19], v[112:127]\n"
"v_mfma_f32_32x32x16_bf16 v[112:127], a[184:187], v[16:19], v[112:127]\n"
"v_mfma_f32_32x32x16_bf16 v[112:127], a[188:191], v[16:19], v[112:127]\n"
"s_waitcnt vmcnt(0)\n"
"s_barrier\n"
"ds_read_b64_tr_b16 a[216:217], v9 offset:49152\n"
"ds_read_b64_tr_b16 a[218:219], v9 offset:49664\n"
"ds_read_b64_tr_b16 a[220:221], v9 offset:53248\n"
"ds_read_b64_tr_b16 a[222:223], v9 offset:53760\n"
"ds_read_b64_tr_b16 a[248:249], v9 offset:57344\n"
"ds_read_b64_tr_b16 a[250:251], v9 offset:57856\n"
"ds_read_b64_tr_b16 a[252:253], v9 offset:61440\n"
"ds_read_b64_tr_b16 a[254:255], v9 offset:61952\n"
"s_waitcnt lgkmcnt(0)\n"
"v_mfma_f32_32x32x16_bf16 v[112:127], a[216:219], v[16:19], v[112:127]\n"
"v_mfma_f32_32x32x16_bf16 v[112:127], a[220:223], v[16:19], v[112:127]\n"
"v_mfma_f32_32x32x16_bf16 v[112:127], a[248:251], v[16:19], v[112:127]\n"
"v_mfma_f32_32x32x16_bf16 v[112:127], a[252:255], v[16:19], v[112:127]\n"
"s_barrier\n"
"s_lshl2_add_u32 s49, s27, 272\n"
"s_lshl2_add_u32 s68, s27, s26\n"
"s_lshl_b32 s68, s68, 9\n"
"s_add_u32 s68, s68, 4829184\n"
"s_mov_b32 s63, 4681728\n"
"s_waitcnt lgkmcnt(0)\n"
"s_add_u32 s54, s52, 0x7f4a7c15\n"
"s_addc_u32 s55, s53, 0x9e3779b9\n"
"s_lshl_b32 s67, s49, 5\n"
"s_add_u32 s67, s67, 4898816\n"
"s_sub_u32 s51, s67, s68\n"
"s_add_u32 s92, s32, s51\n"
"s_addc_u32 s93, s33, 0x10000\n"
"s_cmp_eq_u32 s26, 0\n"
"s_cselect_b32 s94, 1, 0\n"
"s_mov_b32 s95, 0x20000\n"
"v_mov_b32 v16, s54\n"
"v_mov_b32 v17, s55\n"
"v_cmp_gt_u32 vcc, 12, v1\n"
"v_mul_u32_u24 v3, 296960, v1\n"
"v_mov_b32 v12, 0x7fff0000\n"
"v_cndmask_b32 v3, v12, v3, vcc\n"
"s_add_u32 s51, s67, 76021760\n"
"buffer_store_dwordx2 v[16:17], v3, s[32:35], s51 offen sc1\n"
"s_sub_u32 s67, s68, 296960\n"
"s_sub_u32 s51, s51, 76021760\n"
"s_sub_u32 s51, s51, s68\n"
"v_mul_u32_u24 v3, 296960, v1\n"
"v_add_u32 v3, s51, v3\n"
"v_cmp_gt_u32 vcc, 2, v0\n"
"v_mov_b32 v18, 0x7fff0000\n"
"s_nop 0\n"
"v_cndmask_b32 v18, v18, v3, vcc\n"
"s_mul_i32 s48, s28, 593920\n"
"s_add_u32 s68, s68, s48\n"
"s_sub_u32 s68, s68, 3563520\n"
"s_add_u32 s63, s63, s48\n"
"s_add_u32 s66, s48, 4898816\n"
"s_mul_i32 s64, s26, 9216\n"
"s_add_u32 s63, s63, s64\n"
"s_mov_b32 s62, 31\n"
"v_and_b32 v4, 0xf0, v2\n"
"v_lshrrev_b32 v3, 5, v1\n"
"v_lshl_add_u32 v4, v3, 8, v4\n"
"v_bfe_u32 v3, v1, 4, 1\n"
"v_mul_u32_u24 v15, 296960, v3\n"
"v_add_u32 v15, v15, v4\n"
"v_add_u32 v15, 1187840, v15\n"
"v_mul_u32_u24 v3, 36864, v3\n"
"v_add_u32 v4, v3, v4\n"
"v_add_u32 v5, 73728, v4\n"
"v_add_u32 v20, 0, v2\n"
"v_add_u32 v21, 296960, v2\n"
"v_add_u32 v22, 4096, v2\n"
"v_add_u32 v23, 301056, v2\n"
"v_add_u32 v24, 5120, v2\n"
"v_add_u32 v25, 302080, v2\n"
"v_and_b32 v14, 7, v1\n"
"s_mul_i32 s48, s26, 8\n"
"s_add_u32 s48, s48, 96\n"
"v_add_u32 v14, s48, v14\n"
"v_lshrrev_b32 v14, 2, v14\n"
"v_lshlrev_b32 v14, 7, v14\n"
"v_lshrrev_b32 v3, 4, v1\n"
"v_mul_u32_u24 v3, 296960, v3\n"
"v_add_u32 v14, v3, v14\n"
"v_cmp_gt_u32 vcc, 32, v1\n"
"v_mov_b32 v3, 0x7fff0000\n"
"s_nop 0\n"
"v_cndmask_b32 v14, v3, v14, vcc\n"
"v_cndmask_b32 v16, 0, v16, vcc\n"
"v_cndmask_b32 v17, 0, v17, vcc\n"
"v_bfe_u32 v3, v1, 1, 3\n"
"v_mul_u32_u24 v8, 1152, v3\n"
"v_lshrrev_b32 v3, 1, v3\n"
"v_add_u32 v3, s26, v3\n"
"v_lshrrev_b32 v9, 4, v1\n"
"v_lshl_add_u32 v3, v3, 2, v9\n"
"v_and_b32 v3, 15, v3\n"
"v_lshl_add_u32 v8, v3, 2, v8\n"
"v_and_b32 v3, 1, v1\n"
"v_lshl_add_u32 v8, v3, 6, v8\n"
"v_add_u32 v8, 1024, v8\n"
"s_lshl_b32 s48, s26, 2\n"
"v_add_u32 v10, s48, v9\n"
"v_and_b32 v11, 15, v1\n"
"v_add_u32 v3, 64, v1\n"
"v_cmp_eq_u32 vcc, 0, v11\n"
"v_add_u32 v9, 73728, v8\n"
"v_xor_b32 v9, v9, v8\n"
"v_cndmask_b32 v10, v3, v10, vcc\n"
"v_lshlrev_b32 v10, 2, v10\n"
"v_add_u32 v10, 147456, v10\n"
"v_lshlrev_b32 v11, 2, v11\n"
"v_add_u32 v11, 147456, v11\n"
"v_and_b32 v3, 15, v1\n"
"v_cmp_eq_u32 vcc, 8, v3\n"
"v_lshrrev_b32 v3, 4, v1\n"
"s_lshl2_add_u32 s48, s26, 36880\n"
"v_add_lshl_u32 v3, v3, s48, 2\n"
"v_cndmask_b32 v10, v10, v3, vcc\n"
"v_and_b32 v3, 16, v1\n"
"v_lshl_add_u32 v11, v3, 2, v11\n"
"s_mov_b32 s48, 20000\n"
"s_branch .L_rfha\n"
".L_rlha:\n"
"s_sleep 8\n"
".L_rfha:\n"
"buffer_load_dwordx2 v[12:13], v14, s[32:35], s66 offen sc1\n"
"s_sub_u32 s48, s48, 1\n"
"s_cbranch_scc1 .L_end\n"
"s_waitcnt vmcnt(0)\n"
"v_cmp_ne_u64 vcc, v[16:17], v[12:13]\n"
"s_cbranch_vccnz .L_rlha\n"
"s_add_u32 s66, s66, 1187840\n"
"buffer_load_dwordx2 v[12:13], v14, s[32:35], s66 offen sc1\n"
"s_add_u32 m0, s64, 0\n"
"s_nop 0\n"
"buffer_load_dwordx4 v20, s[32:35], s63 offen offset:0 sc1 lds\n"
"buffer_load_dwordx4 v20, s[32:35], s63 offen offset:1024 sc1 lds\n"
"buffer_load_dwordx4 v20, s[32:35], s63 offen offset:2048 sc1 lds\n"
"buffer_load_dwordx4 v20, s[32:35], s63 offen offset:3072 sc1 lds\n"
"s_add_u32 m0, s64, 36864\n"
"s_nop 0\n"
"buffer_load_dwordx4 v21, s[32:35], s63 offen offset:0 sc1 lds\n"
"buffer_load_dwordx4 v21, s[32:35], s63 offen offset:1024 sc1 lds\n"
"buffer_load_dwordx4 v21, s[32:35], s63 offen offset:2048 sc1 lds\n"
"buffer_load_dwordx4 v21, s[32:35], s63 offen offset:3072 sc1 lds\n"
"s_add_u32 m0, s64, 4096\n"
"s_nop 0\n"
"buffer_load_dwordx4 v22, s[32:35], s63 offen offset:0 sc1 lds\n"
"s_add_u32 m0, s64, 40960\n"
"s_nop 0\n"
"buffer_load_dwordx4 v23, s[32:35], s63 offen offset:0 sc1 lds\n"
"s_add_u32 m0, s64, 5120\n"
"s_nop 0\n"
"buffer_load_dwordx4 v24, s[32:35], s63 offen offset:0 sc1 lds\n"
"buffer_load_dwordx4 v24, s[32:35], s63 offen offset:1024 sc1 lds\n"
"buffer_load_dwordx4 v24, s[32:35], s63 offen offset:2048 sc1 lds\n"
"buffer_load_dwordx4 v24, s[32:35], s63 offen offset:3072 sc1 lds\n"
"s_add_u32 m0, s64, 41984\n"
"s_nop 0\n"
"buffer_load_dwordx4 v25, s[32:35], s63 offen offset:0 sc1 lds\n"
"buffer_load_dwordx4 v25, s[32:35], s63 offen offset:1024 sc1 lds\n"
"buffer_load_dwordx4 v25, s[32:35], s63 offen offset:2048 sc1 lds\n"
"buffer_load_dwordx4 v25, s[32:35], s63 offen offset:3072 sc1 lds\n"
"s_add_u32 s63, s63, 1187840\n"
"s_waitcnt vmcnt(8)\n"
"s_barrier\n"
"ds_read_b128 v[128:131], v4 offset:0\n"
"ds_read_b128 v[132:135], v4 offset:512\n"
"ds_read_b128 v[136:139], v4 offset:9216\n"
"ds_read_b128 v[140:143], v4 offset:9728\n"
"ds_read_b128 v[144:147], v4 offset:18432\n"
"ds_read_b128 v[148:151], v4 offset:18944\n"
"ds_read_b128 v[152:155], v4 offset:27648\n"
"ds_read_b128 v[156:159], v4 offset:28160\n"
".p2align 6\n"
".L_hl:\n"
"s_waitcnt lgkmcnt(4)\n"
"v_mfma_f32_32x32x16_bf16 v[32:47], a[0:3], v[128:131], 0\n"
"ds_read_b128 v[160:163], v4 offset:1152\n"
"s_waitcnt vmcnt(10)\n"
"v_cmp_ne_u64 vcc, v[16:17], v[12:13]\n"
"ds_read_b32 v74, v11 offset:128\n"
"s_add_u32 s68, s68, 1187840\n"
"v_mfma_f32_32x32x16_bf16 v[32:47], a[4:7], v[132:135], v[32:47]\n"
"ds_read_b128 v[164:167], v4 offset:1664\n"
"s_cbranch_vccnz .L_rth0\n"
".L_bkh0:\n"
"s_add_u32 s66, s66, 1187840\n"
"v_mfma_f32_32x32x16_bf16 v[32:47], a[8:11], v[136:139], v[32:47]\n"
"ds_read_b128 v[168:171], v4 offset:10368\n"
"s_add_u32 m0, s64, 73728\n"
"v_mfma_f32_32x32x16_bf16 v[32:47], a[12:15], v[140:143], v[32:47]\n"
"ds_read_b128 v[172:175], v4 offset:10880\n"
"buffer_load_dwordx4 v20, s[32:35], s63 offen offset:0 sc1 lds\n"
"s_waitcnt lgkmcnt(3)\n"
"v_mfma_f32_32x32x16_bf16 v[32:47], a[16:19], v[144:147], v[32:47]\n"
"ds_read_b128 v[176:179], v4 offset:19584\n"
"buffer_load_dwordx4 v20, s[32:35], s63 offen offset:1024 sc1 lds\n"
"v_lshlrev_b32 v72, 16, v74\n"
"v_and_b32 v74, 0xffff0000, v74\n"
"v_mfma_f32_32x32x16_bf16 v[32:47], a[20:23], v[148:151], v[32:47]\n"
"ds_read_b128 v[180:183], v4 offset:20096\n"
"v_rcp_f32 v73, v74\n"
"v_mul_f32 v74, v74, v74\n"
"v_fma_f32 v48, -v72, v112, v48\n"
"v_fma_f32 v49, -v72, v113, v49\n"
"v_mfma_f32_32x32x16_bf16 v[32:47], a[24:27], v[152:155], v[32:47]\n"
"ds_read_b128 v[184:187], v4 offset:28800\n"
"buffer_load_dwordx4 v20, s[32:35], s63 offen offset:2048 sc1 lds\n"
"v_mul_f32 v73, 0xbfb8aa3b, v73\n"
"v_fma_f32 v50, -v72, v114, v50\n"
"v_mfma_f32_32x32x16_bf16 v[32:47], a[28:31], v[156:159], v[32:47]\n"
"ds_read_b128 v[188:191], v4 offset:29312\n"
"v_fma_f32 v51, -v72, v115, v51\n"
"v_fma_f32 v52, -v72, v116, v52\n"
"v_fma_f32 v53, -v72, v117, v53\n"
"v_fma_f32 v54, -v72, v118, v54\n"
"s_waitcnt lgkmcnt(4)\n"
"v_mfma_f32_32x32x16_bf16 v[32:47], a[32:35], v[160:163], v[32:47]\n"
"ds_read_b128 v[128:131], v4 offset:2304\n"
"buffer_load_dwordx4 v20, s[32:35], s63 offen offset:3072 sc1 lds\n"
"v_fma_f32 v55, -v72, v119, v55\n"
"v_fma_f32 v56, -v72, v120, v56\n"
"v_mfma_f32_32x32x16_bf16 v[32:47], a[36:39], v[164:167], v[32:47]\n"
"ds_read_b128 v[132:135], v4 offset:2816\n"
"v_fma_f32 v57, -v72, v121, v57\n"
"v_fma_f32 v58, -v72, v122, v58\n"
"v_fma_f32 v59, -v72, v123, v59\n"
"v_fma_f32 v60, -v72, v124, v60\n"
"v_fma_f32 v61, -v72, v125, v61\n"
"v_mfma_f32_32x32x16_bf16 v[32:47], a[40:43], v[168:171], v[32:47]\n"
"ds_read_b128 v[136:139], v4 offset:11520\n"
"s_add_u32 m0, s64, 110592\n"
"v_fma_f32 v62, -v72, v126, v62\n"
"buffer_load_dwordx4 v21, s[32:35], s63 offen offset:0 sc1 lds\n"
"v_mfma_f32_32x32x16_bf16 v[32:47], a[44:47], v[172:175], v[32:47]\n"
"ds_read_b128 v[140:143], v4 offset:12032\n"
"v_fma_f32 v63, -v72, v127, v63\n"
"v_mul_f32 v76, v48, v73\n"
"v_mul_f32 v77, v49, v73\n"
"v_mul_f32 v78, v50, v73\n"
"s_waitcnt lgkmcnt(4)\n"
"v_mfma_f32_32x32x16_bf16 v[32:47], a[48:51], v[176:179], v[32:47]\n"
"ds_read_b128 v[144:147], v4 offset:20736\n"
"buffer_load_dwordx4 v21, s[32:35], s63 offen offset:1024 sc1 lds\n"
"v_exp_f32 v76, v76\n"
"v_mfma_f32_32x32x16_bf16 v[32:47], a[52:55], v[180:183], v[32:47]\n"
"ds_read_b128 v[148:151], v4 offset:21248\n"
"v_exp_f32 v77, v77\n"
"v_exp_f32 v78, v78\n"
"v_mul_f32 v79, v51, v73\n"
"v_mfma_f32_32x32x16_bf16 v[32:47], a[56:59], v[184:187], v[32:47]\n"
"ds_read_b128 v[152:155], v4 offset:29952\n"
"buffer_load_dwordx4 v21, s[32:35], s63 offen offset:2048 sc1 lds\n"
"v_exp_f32 v79, v79\n"
"v_mfma_f32_32x32x16_bf16 v[32:47], a[60:63], v[188:191], v[32:47]\n"
"ds_read_b128 v[156:159], v4 offset:30464\n"
"v_mul_f32 v80, v52, v73\n"
"v_mul_f32 v81, v53, v73\n"
"v_exp_f32 v80, v80\n"
"s_waitcnt lgkmcnt(4)\n"
"v_mfma_f32_32x32x16_bf16 v[32:47], a[64:67], v[128:131], v[32:47]\n"
"ds_read_b128 v[160:163], v4 offset:3456\n"
"buffer_load_dwordx4 v21, s[32:35], s63 offen offset:3072 sc1 lds\n"
"v_exp_f32 v81, v81\n"
"v_mfma_f32_32x32x16_bf16 v[32:47], a[68:71], v[132:135], v[32:47]\n"
"ds_read_b128 v[164:167], v4 offset:3968\n"
"v_mul_f32 v82, v54, v73\n"
"v_mul_f32 v83, v55, v73\n"
"v_exp_f32 v82, v82\n"
"v_mul_f32 v48, v48, v56\n"
"v_mfma_f32_32x32x16_bf16 v[32:47], a[72:75], v[136:139], v[32:47]\n"
"ds_read_b128 v[168:171], v4 offset:12672\n"
"s_add_u32 m0, s64, 77824\n"
"v_exp_f32 v83, v83\n"
"v_mul_f32 v49, v49, v57\n"
"v_mul_f32 v50, v50, v58\n"
"v_mfma_f32_32x32x16_bf16 v[32:47], a[76:79], v[140:143], v[32:47]\n"
"ds_read_b128 v[172:175], v4 offset:13184\n"
"buffer_load_dwordx4 v22, s[32:35], s63 offen offset:0 sc1 lds\n"
"v_mul_f32 v51, v51, v59\n"
"s_waitcnt lgkmcnt(4)\n"
"v_mfma_f32_32x32x16_bf16 v[32:47], a[80:83], v[144:147], v[32:47]\n"
"ds_read_b128 v[176:179], v4 offset:21888\n"
"s_add_u32 m0, s64, 114688\n"
"v_mul_f32 v52, v52, v60\n"
"buffer_load_dwordx4 v23, s[32:35], s63 offen offset:0 sc1 lds\n"
"v_mfma_f32_32x32x16_bf16 v[32:47], a[84:87], v[148:151], v[32:47]\n"
"ds_read_b128 v[180:183], v4 offset:22400\n"
"v_mul_f32 v53, v53, v61\n"
"v_mul_f32 v54, v54, v62\n"
"v_mul_f32 v55, v55, v63\n"
"v_fma_f32 v76, v76, v74, v74\n"
"v_fma_f32 v77, v77, v74, v74\n"
"v_mfma_f32_32x32x16_bf16 v[32:47], a[88:91], v[152:155], v[32:47]\n"
"ds_read_b128 v[184:187], v4 offset:31104\n"
"buffer_load_dwordx2 v[12:13], v14, s[32:35], s66 offen sc1\n"
"v_rcp_f32 v76, v76\n"
"v_mfma_f32_32x32x16_bf16 v[32:47], a[92:95], v[156:159], v[32:47]\n"
"ds_read_b128 v[188:191], v4 offset:31616\n"
"v_rcp_f32 v77, v77\n"
"v_fma_f32 v78, v78, v74, v74\n"
"v_fma_f32 v79, v79, v74, v74\n"
"s_waitcnt lgkmcnt(4)\n"
"v_mfma_f32_32x32x16_bf16 v[32:47], a[96:99], v[160:163], v[32:47]\n"
"s_waitcnt vmcnt(11)\n"
"s_barrier\n"
"ds_read_b128 v[128:131], v4 offset:4608\n"
"v_rcp_f32 v78, v78\n"
"v_fma_f32 v80, v80, v74, v74\n"
"v_mfma_f32_32x32x16_bf16 v[32:47], a[100:103], v[164:167], v[32:47]\n"
"ds_read_b128 v[132:135], v4 offset:5120\n"
"buffer_store_dwordx2 v[16:17], v18, s[32:35], s68 offen sc1\n"
"ds_read2st64_b32 v[64:65], v8 offset0:0 offset1:36\n"
"ds_read2st64_b32 v[66:67], v8 offset0:72 offset1:108\n"
"v_mfma_f32_32x32x16_bf16 v[32:47], a[104:107], v[168:171], v[32:47]\n"
"ds_read_b128 v[136:139], v4 offset:13824\n"
"s_add_u32 m0, s64, 78848\n"
"ds_read2st64_b32 v[68:69], v8 offset0:144 offset1:180\n"
"buffer_load_dwordx4 v24, s[32:35], s63 offen offset:0 sc1 lds\n"
"v_mfma_f32_32x32x16_bf16 v[32:47], a[108:111], v[172:175], v[32:47]\n"
"ds_read_b128 v[140:143], v4 offset:14336\n"
"ds_read2st64_b32 v[70:71], v8 offset0:216 offset1:252\n"
"v_xor_b32 v8, v9, v8\n"
"v_rcp_f32 v79, v79\n"
"s_waitcnt lgkmcnt(8)\n"
"v_mfma_f32_32x32x16_bf16 v[32:47], a[112:115], v[176:179], v[32:47]\n"
"ds_read_b128 v[144:147], v4 offset:23040\n"
"v_rcp_f32 v80, v80\n"
"v_fma_f32 v81, v81, v74, v74\n"
"v_fma_f32 v82, v82, v74, v74\n"
"v_fma_f32 v83, v83, v74, v74\n"
"v_mfma_f32_32x32x16_bf16 v[32:47], a[116:119], v[180:183], v[32:47]\n"
"ds_read_b128 v[148:151], v4 offset:23552\n"
"buffer_load_dwordx4 v24, s[32:35], s63 offen offset:1024 sc1 lds\n"
"v_rcp_f32 v81, v81\n"
"v_mfma_f32_32x32x16_bf16 v[32:47], a[120:123], v[184:187], v[32:47]\n"
"ds_read_b128 v[152:155], v4 offset:32256\n"
"v_rcp_f32 v82, v82\n"
"v_rcp_f32 v83, v83\n"
"v_mul_f32 v48, v48, v76\n"
"v_mfma_f32_32x32x16_bf16 v[32:47], a[124:127], v[188:191], v[32:47]\n"
"ds_read_b128 v[156:159], v4 offset:32768\n"
"s_waitcnt lgkmcnt(4)\n"
"v_add_f32 v64, v64, v65\n"
"v_add_f32 v66, v66, v67\n"
"v_add_f32 v68, v68, v69\n"
"v_mfma_f32_32x32x16_bf16 v[32:47], a[128:131], v[128:131], v[32:47]\n"
"ds_read_b128 v[160:163], v4 offset:5760\n"
"buffer_load_dwordx4 v24, s[32:35], s63 offen offset:2048 sc1 lds\n"
"v_add_f32 v70, v70, v71\n"
"v_add_f32 v66, v64, v66\n"
"v_mfma_f32_32x32x16_bf16 v[32:47], a[132:135], v[132:135], v[32:47]\n"
"ds_read_b128 v[164:167], v4 offset:6272\n"
"v_add_f32 v68, v68, v70\n"
"v_add_f32_dpp v64, v66, v66 row_ror:8 row_mask:0xf bank_mask:0x3\n"
"v_mul_f32 v49, v49, v77\n"
"v_mul_f32 v50, v50, v78\n"
"v_add_f32_dpp v64, v68, v68 row_ror:8 row_mask:0xf bank_mask:0xc\n"
"v_mfma_f32_32x32x16_bf16 v[32:47], a[136:139], v[136:139], v[32:47]\n"
"ds_read_b128 v[168:171], v4 offset:14976\n"
"v_add_f32_dpp v64, v64, v64 row_ror:12 row_mask:0xf bank_mask:0xf\n"
"v_mul_f32 v51, v51, v79\n"
"v_mul_f32 v52, v52, v80\n"
"v_add_f32_dpp v64, v64, v64 quad_perm:[2,3,0,1] row_mask:0xf bank_mask:0xf\n"
"v_mul_f32 v53, v53, v81\n"
"v_mfma_f32_32x32x16_bf16 v[32:47], a[140:143], v[140:143], v[32:47]\n"
"ds_read_b128 v[172:175], v4 offset:15488\n"
"v_mul_f32 v64, 0x3a800000, v64\n"
"v_mul_f32 v54, v54, v82\n"
"v_mul_f32 v65, v64, v64\n"
"v_mul_f32 v55, v55, v83\n"
"s_waitcnt lgkmcnt(4)\n"
"v_mfma_f32_32x32x16_bf16 v[32:47], a[144:147], v[144:147], v[32:47]\n"
"ds_read_b128 v[176:179], v4 offset:24192\n"
"buffer_load_dwordx4 v24, s[32:35], s63 offen offset:3072 sc1 lds\n"
"v_sub_f32_dpp v65, v64, v65 quad_perm:[1,1,3,3] row_mask:0xf bank_mask:0xf\n"
"v_cvt_pk_bf16_f32 v84, v48, v49\n"
"v_mfma_f32_32x32x16_bf16 v[32:47], a[148:151], v[148:151], v[32:47]\n"
"ds_read_b128 v[180:183], v4 offset:24704\n"
"v_max_f32 v65, 0, v65\n"
"v_cvt_pk_bf16_f32 v85, v50, v51\n"
"v_cvt_pk_bf16_f32 v65, 0, v65\n"
"v_cvt_pk_bf16_f32 v86, v52, v53\n"
"v_add_f32 v65, 0x3727c5ac, v65\n"
"v_mfma_f32_32x32x16_bf16 v[32:47], a[152:155], v[152:155], v[32:47]\n"
"ds_read_b128 v[184:187], v4 offset:33408\n"
"v_cvt_pk_bf16_f32 v65, 0, v65\n"
"v_cvt_pk_bf16_f32 v87, v54, v55\n"
"v_sqrt_f32 v65, v65\n"
"v_mfma_f32_32x32x16_bf16 v[32:47], a[156:159], v[156:159], v[32:47]\n"
"ds_read_b128 v[188:191], v4 offset:33920\n"
"s_add_u32 m0, s64, 115712\n"
"v_cvt_pk_bf16_f32 v65, v64, v65\n"
"s_waitcnt lgkmcnt(4)\n"
"v_mfma_f32_32x32x16_bf16 v[32:47], a[160:163], v[160:163], v[32:47]\n"
"ds_read_b128 v[128:131], v4 offset:6912\n"
"buffer_load_dwordx4 v25, s[32:35], s63 offen offset:0 sc1 lds\n"
"ds_write_b32 v10, v65 offset:0\n"
"v_mfma_f32_32x32x16_bf16 v[32:47], a[164:167], v[164:167], v[32:47]\n"
"ds_read_b128 v[132:135], v4 offset:7424\n"
"buffer_store_dwordx4 v[84:87], v15, s[32:35], s68 offen sc1\n"
"v_mfma_f32_32x32x16_bf16 v[32:47], a[168:171], v[168:171], v[32:47]\n"
"ds_read_b128 v[136:139], v4 offset:16128\n"
"v_mfma_f32_32x32x16_bf16 v[32:47], a[172:175], v[172:175], v[32:47]\n"
"ds_read_b128 v[140:143], v4 offset:16640\n"
"buffer_load_dwordx4 v25, s[32:35], s63 offen offset:1024 sc1 lds\n"
"s_waitcnt lgkmcnt(5)\n"
"v_mfma_f32_32x32x16_bf16 v[32:47], a[176:179], v[176:179], v[32:47]\n"
"ds_read_b128 v[144:147], v4 offset:25344\n"
"v_mfma_f32_32x32x16_bf16 v[32:47], a[180:183], v[180:183], v[32:47]\n"
"ds_read_b128 v[148:151], v4 offset:25856\n"
"v_mfma_f32_32x32x16_bf16 v[32:47], a[184:187], v[184:187], v[32:47]\n"
"ds_read_b128 v[152:155], v4 offset:34560\n"
"buffer_load_dwordx4 v25, s[32:35], s63 offen offset:2048 sc1 lds\n"
"v_mfma_f32_32x32x16_bf16 v[32:47], a[188:191], v[188:191], v[32:47]\n"
"ds_read_b128 v[156:159], v4 offset:35072\n"
"s_waitcnt lgkmcnt(4)\n"
"v_mfma_f32_32x32x16_bf16 v[32:47], a[192:195], v[128:131], v[32:47]\n"
"ds_read_b128 v[160:163], v4 offset:8064\n"
"v_mfma_f32_32x32x16_bf16 v[32:47], a[196:199], v[132:135], v[32:47]\n"
"ds_read_b128 v[164:167], v4 offset:8576\n"
"v_mfma_f32_32x32x16_bf16 v[32:47], a[200:203], v[136:139], v[32:47]\n"
"ds_read_b128 v[168:171], v4 offset:17280\n"
"buffer_load_dwordx4 v25, s[32:35], s63 offen offset:3072 sc1 lds\n"
"s_add_u32 s63, s63, 1187840\n"
"v_mfma_f32_32x32x16_bf16 v[32:47], a[204:207], v[140:143], v[32:47]\n"
"ds_read_b128 v[172:175], v4 offset:17792\n"
"s_waitcnt lgkmcnt(4)\n"
"v_mfma_f32_32x32x16_bf16 v[32:47], a[208:211], v[144:147], v[32:47]\n"
"ds_read_b128 v[176:179], v4 offset:26496\n"
"v_mfma_f32_32x32x16_bf16 v[32:47], a[212:215], v[148:151], v[32:47]\n"
"ds_read_b128 v[180:183], v4 offset:27008\n"
"v_mfma_f32_32x32x16_bf16 v[32:47], a[216:219], v[152:155], v[32:47]\n"
"ds_read_b128 v[184:187], v4 offset:35712\n"
"v_mfma_f32_32x32x16_bf16 v[32:47], a[220:223], v[156:159], v[32:47]\n"
"ds_read_b128 v[188:191], v4 offset:36224\n"
"s_waitcnt lgkmcnt(4)\n"
"v_mfma_f32_32x32x16_bf16 v[32:47], a[224:227], v[160:163], v[32:47]\n"
"s_waitcnt vmcnt(11)\n"
"s_barrier\n"
"ds_read_b128 v[128:131], v5 offset:0\n"
"v_mfma_f32_32x32x16_bf16 v[32:47], a[228:231], v[164:167], v[32:47]\n"
"ds_read_b128 v[132:135], v5 offset:512\n"
"v_mfma_f32_32x32x16_bf16 v[32:47], a[232:235], v[168:171], v[32:47]\n"
"ds_read_b128 v[136:139], v5 offset:9216\n"
"v_mfma_f32_32x32x16_bf16 v[32:47], a[236:239], v[172:175], v[32:47]\n"
"ds_read_b128 v[140:143], v5 offset:9728\n"
"s_waitcnt lgkmcnt(4)\n"
"v_mfma_f32_32x32x16_bf16 v[32:47], a[240:243], v[176:179], v[32:47]\n"
"ds_read_b128 v[144:147], v5 offset:18432\n"
"v_mfma_f32_32x32x16_bf16 v[32:47], a[244:247], v[180:183], v[32:47]\n"
"ds_read_b128 v[148:151], v5 offset:18944\n"
"v_mfma_f32_32x32x16_bf16 v[32:47], a[248:251], v[184:187], v[32:47]\n"
"ds_read_b128 v[152:155], v5 offset:27648\n"
"v_mfma_f32_32x32x16_bf16 v[32:47], a[252:255], v[188:191], v[32:47]\n"
"ds_read_b128 v[156:159], v5 offset:28160\n"
"s_waitcnt lgkmcnt(4)\n"
"v_mfma_f32_32x32x16_bf16 v[48:63], a[0:3], v[128:131], 0\n"
"ds_read_b128 v[160:163], v5 offset:1152\n"
"s_waitcnt vmcnt(10)\n"
"v_cmp_ne_u64 vcc, v[16:17], v[12:13]\n"
"ds_read_b32 v74, v11 offset:0\n"
"s_add_u32 s68, s68, 1187840\n"
"v_mfma_f32_32x32x16_bf16 v[48:63], a[4:7], v[132:135], v[48:63]\n"
"ds_read_b128 v[164:167], v5 offset:1664\n"
"s_cbranch_vccnz .L_rth1\n"
".L_bkh1:\n"
"s_add_u32 s66, s66, 1187840\n"
"v_mfma_f32_32x32x16_bf16 v[48:63], a[8:11], v[136:139], v[48:63]\n"
"ds_read_b128 v[168:171], v5 offset:10368\n"
"s_add_u32 m0, s64, 0\n"
"v_mfma_f32_32x32x16_bf16 v[48:63], a[12:15], v[140:143], v[48:63]\n"
"ds_read_b128 v[172:175], v5 offset:10880\n"
"buffer_load_dwordx4 v20, s[32:35], s63 offen offset:0 sc1 lds\n"
"s_waitcnt lgkmcnt(3)\n"
"v_mfma_f32_32x32x16_bf16 v[48:63], a[16:19], v[144:147], v[48:63]\n"
"ds_read_b128 v[176:179], v5 offset:19584\n"
"buffer_load_dwordx4 v20, s[32:35], s63 offen offset:1024 sc1 lds\n"
"v_lshlrev_b32 v72, 16, v74\n"
"v_and_b32 v74, 0xffff0000, v74\n"
"v_mfma_f32_32x32x16_bf16 v[48:63], a[20:23], v[148:151], v[48:63]\n"
"ds_read_b128 v[180:183], v5 offset:20096\n"
"v_rcp_f32 v73, v74\n"
"v_mul_f32 v74, v74, v74\n"
"v_fma_f32 v32, -v72, v112, v32\n"
"v_fma_f32 v33, -v72, v113, v33\n"
"v_mfma_f32_32x32x16_bf16 v[48:63], a[24:27], v[152:155], v[48:63]\n"
"ds_read_b128 v[184:187], v5 offset:28800\n"
"buffer_load_dwordx4 v20, s[32:35], s63 offen offset:2048 sc1 lds\n"
"v_mul_f32 v73, 0xbfb8aa3b, v73\n"
"v_fma_f32 v34, -v72, v114, v34\n"
"v_mfma_f32_32x32x16_bf16 v[48:63], a[28:31], v[156:159], v[48:63]\n"
"ds_read_b128 v[188:191], v5 offset:29312\n"
"v_fma_f32 v35, -v72, v115, v35\n"
"v_fma_f32 v36, -v72, v116, v36\n"
"v_fma_f32 v37, -v72, v117, v37\n"
"v_fma_f32 v38, -v72, v118, v38\n"
"s_waitcnt lgkmcnt(4)\n"
"v_mfma_f32_32x32x16_bf16 v[48:63], a[32:35], v[160:163], v[48:63]\n"
"ds_read_b128 v[128:131], v5 offset:2304\n"
"buffer_load_dwordx4 v20, s[32:35], s63 offen offset:3072 sc1 lds\n"
"v_fma_f32 v39, -v72, v119, v39\n"
"v_fma_f32 v40, -v72, v120, v40\n"
"v_mfma_f32_32x32x16_bf16 v[48:63], a[36:39], v[164:167], v[48:63]\n"
"ds_read_b128 v[132:135], v5 offset:2816\n"
"v_fma_f32 v41, -v72, v121, v41\n"
"v_fma_f32 v42, -v72, v122, v42\n"
"v_fma_f32 v43, -v72, v123, v43\n"
"v_fma_f32 v44, -v72, v124, v44\n"
"v_fma_f32 v45, -v72, v125, v45\n"
"v_mfma_f32_32x32x16_bf16 v[48:63], a[40:43], v[168:171], v[48:63]\n"
"ds_read_b128 v[136:139], v5 offset:11520\n"
"s_add_u32 m0, s64, 36864\n"
"v_fma_f32 v46, -v72, v126, v46\n"
"buffer_load_dwordx4 v21, s[32:35], s63 offen offset:0 sc1 lds\n"
"v_mfma_f32_32x32x16_bf16 v[48:63], a[44:47], v[172:175], v[48:63]\n"
"ds_read_b128 v[140:143], v5 offset:12032\n"
"v_fma_f32 v47, -v72, v127, v47\n"
"v_mul_f32 v76, v32, v73\n"
"v_mul_f32 v77, v33, v73\n"
"v_mul_f32 v78, v34, v73\n"
"s_waitcnt lgkmcnt(4)\n"
"v_mfma_f32_32x32x16_bf16 v[48:63], a[48:51], v[176:179], v[48:63]\n"
"ds_read_b128 v[144:147], v5 offset:20736\n"
"buffer_load_dwordx4 v21, s[32:35], s63 offen offset:1024 sc1 lds\n"
"v_exp_f32 v76, v76\n"
"v_mfma_f32_32x32x16_bf16 v[48:63], a[52:55], v[180:183], v[48:63]\n"
"ds_read_b128 v[148:151], v5 offset:21248\n"
"v_exp_f32 v77, v77\n"
"v_exp_f32 v78, v78\n"
"v_mul_f32 v79, v35, v73\n"
"v_mfma_f32_32x32x16_bf16 v[48:63], a[56:59], v[184:187], v[48:63]\n"
"ds_read_b128 v[152:155], v5 offset:29952\n"
"buffer_load_dwordx4 v21, s[32:35], s63 offen offset:2048 sc1 lds\n"
"v_exp_f32 v79, v79\n"
"v_mfma_f32_32x32x16_bf16 v[48:63], a[60:63], v[188:191], v[48:63]\n"
"ds_read_b128 v[156:159], v5 offset:30464\n"
"v_mul_f32 v80, v36, v73\n"
"v_mul_f32 v81, v37, v73\n"
"v_exp_f32 v80, v80\n"
"s_waitcnt lgkmcnt(4)\n"
"v_mfma_f32_32x32x16_bf16 v[48:63], a[64:67], v[128:131], v[48:63]\n"
"ds_read_b128 v[160:163], v5 offset:3456\n"
"buffer_load_dwordx4 v21, s[32:35], s63 offen offset:3072 sc1 lds\n"
"v_exp_f32 v81, v81\n"
"v_mfma_f32_32x32x16_bf16 v[48:63], a[68:71], v[132:135], v[48:63]\n"
"ds_read_b128 v[164:167], v5 offset:3968\n"
"v_mul_f32 v82, v38, v73\n"
"v_mul_f32 v83, v39, v73\n"
"v_exp_f32 v82, v82\n"
"v_mul_f32 v32, v32, v40\n"
"v_mfma_f32_32x32x16_bf16 v[48:63], a[72:75], v[136:139], v[48:63]\n"
"ds_read_b128 v[168:171], v5 offset:12672\n"
"s_add_u32 m0, s64, 4096\n"
"v_exp_f32 v83, v83\n"
"v_mul_f32 v33, v33, v41\n"
"v_mul_f32 v34, v34, v42\n"
"v_mfma_f32_32x32x16_bf16 v[48:63], a[76:79], v[140:143], v[48:63]\n"
"ds_read_b128 v[172:175], v5 offset:13184\n"
"buffer_load_dwordx4 v22, s[32:35], s63 offen offset:0 sc1 lds\n"
"v_mul_f32 v35, v35, v43\n"
"s_waitcnt lgkmcnt(4)\n"
"v_mfma_f32_32x32x16_bf16 v[48:63], a[80:83], v[144:147], v[48:63]\n"
"ds_read_b128 v[176:179], v5 offset:21888\n"
"s_add_u32 m0, s64, 40960\n"
"v_mul_f32 v36, v36, v44\n"
"buffer_load_dwordx4 v23, s[32:35], s63 offen offset:0 sc1 lds\n"
"v_mfma_f32_32x32x16_bf16 v[48:63], a[84:87], v[148:151], v[48:63]\n"
"ds_read_b128 v[180:183], v5 offset:22400\n"
"v_mul_f32 v37, v37, v45\n"
"v_mul_f32 v38, v38, v46\n"
"v_mul_f32 v39, v39, v47\n"
"v_fma_f32 v76, v76, v74, v74\n"
"v_fma_f32 v77, v77, v74, v74\n"
"v_mfma_f32_32x32x16_bf16 v[48:63], a[88:91], v[152:155], v[48:63]\n"
"ds_read_b128 v[184:187], v5 offset:31104\n"
"buffer_load_dwordx2 v[12:13], v14, s[32:35], s66 offen sc1\n"
"v_rcp_f32 v76, v76\n"
"v_mfma_f32_32x32x16_bf16 v[48:63], a[92:95], v[156:159], v[48:63]\n"
"ds_read_b128 v[188:191], v5 offset:31616\n"
"v_rcp_f32 v77, v77\n"
"v_fma_f32 v78, v78, v74, v74\n"
"v_fma_f32 v79, v79, v74, v74\n"
"s_waitcnt lgkmcnt(4)\n"
"v_mfma_f32_32x32x16_bf16 v[48:63], a[96:99], v[160:163], v[48:63]\n"
"s_waitcnt vmcnt(11)\n"
"s_barrier\n"
"ds_read_b128 v[128:131], v5 offset:4608\n"
"v_rcp_f32 v78, v78\n"
"v_fma_f32 v80, v80, v74, v74\n"
"v_mfma_f32_32x32x16_bf16 v[48:63], a[100:103], v[164:167], v[48:63]\n"
"ds_read_b128 v[132:135], v5 offset:5120\n"
"buffer_store_dwordx2 v[16:17], v18, s[32:35], s68 offen sc1\n"
"ds_read2st64_b32 v[64:65], v8 offset0:0 offset1:36\n"
"ds_read2st64_b32 v[66:67], v8 offset0:72 offset1:108\n"
"v_mfma_f32_32x32x16_bf16 v[48:63], a[104:107], v[168:171], v[48:63]\n"
"ds_read_b128 v[136:139], v5 offset:13824\n"
"s_add_u32 m0, s64, 5120\n"
"ds_read2st64_b32 v[68:69], v8 offset0:144 offset1:180\n"
"buffer_load_dwordx4 v24, s[32:35], s63 offen offset:0 sc1 lds\n"
"v_mfma_f32_32x32x16_bf16 v[48:63], a[108:111], v[172:175], v[48:63]\n"
"ds_read_b128 v[140:143], v5 offset:14336\n"
"ds_read2st64_b32 v[70:71], v8 offset0:216 offset1:252\n"
"v_xor_b32 v8, v9, v8\n"
"v_rcp_f32 v79, v79\n"
"s_waitcnt lgkmcnt(8)\n"
"v_mfma_f32_32x32x16_bf16 v[48:63], a[112:115], v[176:179], v[48:63]\n"
"ds_read_b128 v[144:147], v5 offset:23040\n"
"v_rcp_f32 v80, v80\n"
"v_fma_f32 v81, v81, v74, v74\n"
"v_fma_f32 v82, v82, v74, v74\n"
"v_fma_f32 v83, v83, v74, v74\n"
"v_mfma_f32_32x32x16_bf16 v[48:63], a[116:119], v[180:183], v[48:63]\n"
"ds_read_b128 v[148:151], v5 offset:23552\n"
"buffer_load_dwordx4 v24, s[32:35], s63 offen offset:1024 sc1 lds\n"
"v_rcp_f32 v81, v81\n"
"v_mfma_f32_32x32x16_bf16 v[48:63], a[120:123], v[184:187], v[48:63]\n"
"ds_read_b128 v[152:155], v5 offset:32256\n"
"v_rcp_f32 v82, v82\n"
"v_rcp_f32 v83, v83\n"
"v_mul_f32 v32, v32, v76\n"
"v_mfma_f32_32x32x16_bf16 v[48:63], a[124:127], v[188:191], v[48:63]\n"
"ds_read_b128 v[156:159], v5 offset:32768\n"
"s_waitcnt lgkmcnt(4)\n"
"v_add_f32 v64, v64, v65\n"
"v_add_f32 v66, v66, v67\n"
"v_add_f32 v68, v68, v69\n"
"v_mfma_f32_32x32x16_bf16 v[48:63], a[128:131], v[128:131], v[48:63]\n"
"ds_read_b128 v[160:163], v5 offset:5760\n"
"buffer_load_dwordx4 v24, s[32:35], s63 offen offset:2048 sc1 lds\n"
"v_add_f32 v70, v70, v71\n"
"v_add_f32 v66, v64, v66\n"
"v_mfma_f32_32x32x16_bf16 v[48:63], a[132:135], v[132:135], v[48:63]\n"
"ds_read_b128 v[164:167], v5 offset:6272\n"
"v_add_f32 v68, v68, v70\n"
"v_add_f32_dpp v64, v66, v66 row_ror:8 row_mask:0xf bank_mask:0x3\n"
"v_mul_f32 v33, v33, v77\n"
"v_mul_f32 v34, v34, v78\n"
"v_add_f32_dpp v64, v68, v68 row_ror:8 row_mask:0xf bank_mask:0xc\n"
"v_mfma_f32_32x32x16_bf16 v[48:63], a[136:139], v[136:139], v[48:63]\n"
"ds_read_b128 v[168:171], v5 offset:14976\n"
"v_add_f32_dpp v64, v64, v64 row_ror:12 row_mask:0xf bank_mask:0xf\n"
"v_mul_f32 v35, v35, v79\n"
"v_mul_f32 v36, v36, v80\n"
"v_add_f32_dpp v64, v64, v64 quad_perm:[2,3,0,1] row_mask:0xf bank_mask:0xf\n"
"v_mul_f32 v37, v37, v81\n"
"v_mfma_f32_32x32x16_bf16 v[48:63], a[140:143], v[140:143], v[48:63]\n"
"ds_read_b128 v[172:175], v5 offset:15488\n"
"v_mul_f32 v64, 0x3a800000, v64\n"
"v_mul_f32 v38, v38, v82\n"
"v_mul_f32 v65, v64, v64\n"
"v_mul_f32 v39, v39, v83\n"
"s_waitcnt lgkmcnt(4)\n"
"v_mfma_f32_32x32x16_bf16 v[48:63], a[144:147], v[144:147], v[48:63]\n"
"ds_read_b128 v[176:179], v5 offset:24192\n"
"buffer_load_dwordx4 v24, s[32:35], s63 offen offset:3072 sc1 lds\n"
"v_sub_f32_dpp v65, v64, v65 quad_perm:[1,1,3,3] row_mask:0xf bank_mask:0xf\n"
"v_cvt_pk_bf16_f32 v84, v32, v33\n"
"v_mfma_f32_32x32x16_bf16 v[48:63], a[148:151], v[148:151], v[48:63]\n"
"ds_read_b128 v[180:183], v5 offset:24704\n"
"v_max_f32 v65, 0, v65\n"
"v_cvt_pk_bf16_f32 v85, v34, v35\n"
"v_cvt_pk_bf16_f32 v65, 0, v65\n"
"v_cvt_pk_bf16_f32 v86, v36, v37\n"
"v_add_f32 v65, 0x3727c5ac, v65\n"
"v_mfma_f32_32x32x16_bf16 v[48:63], a[152:155], v[152:155], v[48:63]\n"
"ds_read_b128 v[184:187], v5 offset:33408\n"
"v_cvt_pk_bf16_f32 v65, 0, v65\n"
"v_cvt_pk_bf16_f32 v87, v38, v39\n"
"v_sqrt_f32 v65, v65\n"
"v_mfma_f32_32x32x16_bf16 v[48:63], a[156:159], v[156:159], v[48:63]\n"
"ds_read_b128 v[188:191], v5 offset:33920\n"
"s_add_u32 m0, s64, 41984\n"
"v_cvt_pk_bf16_f32 v65, v64, v65\n"
"s_waitcnt lgkmcnt(4)\n"
"v_mfma_f32_32x32x16_bf16 v[48:63], a[160:163], v[160:163], v[48:63]\n"
"ds_read_b128 v[128:131], v5 offset:6912\n"
"buffer_load_dwordx4 v25, s[32:35], s63 offen offset:0 sc1 lds\n"
"ds_write_b32 v10, v65 offset:128\n"
"v_mfma_f32_32x32x16_bf16 v[48:63], a[164:167], v[164:167], v[48:63]\n"
"ds_read_b128 v[132:135], v5 offset:7424\n"
"buffer_store_dwordx4 v[84:87], v15, s[32:35], s68 offen sc1\n"
"v_mfma_f32_32x32x16_bf16 v[48:63], a[168:171], v[168:171], v[48:63]\n"
"ds_read_b128 v[136:139], v5 offset:16128\n"
"v_mfma_f32_32x32x16_bf16 v[48:63], a[172:175], v[172:175], v[48:63]\n"
"ds_read_b128 v[140:143], v5 offset:16640\n"
"buffer_load_dwordx4 v25, s[32:35], s63 offen offset:1024 sc1 lds\n"
"s_waitcnt lgkmcnt(5)\n"
"v_mfma_f32_32x32x16_bf16 v[48:63], a[176:179], v[176:179], v[48:63]\n"
"ds_read_b128 v[144:147], v5 offset:25344\n"
"v_mfma_f32_32x32x16_bf16 v[48:63], a[180:183], v[180:183], v[48:63]\n"
"ds_read_b128 v[148:151], v5 offset:25856\n"
"v_mfma_f32_32x32x16_bf16 v[48:63], a[184:187], v[184:187], v[48:63]\n"
"ds_read_b128 v[152:155], v5 offset:34560\n"
"buffer_load_dwordx4 v25, s[32:35], s63 offen offset:2048 sc1 lds\n"
"v_mfma_f32_32x32x16_bf16 v[48:63], a[188:191], v[188:191], v[48:63]\n"
"ds_read_b128 v[156:159], v5 offset:35072\n"
"s_waitcnt lgkmcnt(4)\n"
"v_mfma_f32_32x32x16_bf16 v[48:63], a[192:195], v[128:131], v[48:63]\n"
"ds_read_b128 v[160:163], v5 offset:8064\n"
"v_mfma_f32_32x32x16_bf16 v[48:63], a[196:199], v[132:135], v[48:63]\n"
"ds_read_b128 v[164:167], v5 offset:8576\n"
"v_mfma_f32_32x32x16_bf16 v[48:63], a[200:203], v[136:139], v[48:63]\n"
"ds_read_b128 v[168:171], v5 offset:17280\n"
"buffer_load_dwordx4 v25, s[32:35], s63 offen offset:3072 sc1 lds\n"
"s_add_u32 s63, s63, 1187840\n"
"v_mfma_f32_32x32x16_bf16 v[48:63], a[204:207], v[140:143], v[48:63]\n"
"ds_read_b128 v[172:175], v5 offset:17792\n"
"s_waitcnt lgkmcnt(4)\n"
"v_mfma_f32_32x32x16_bf16 v[48:63], a[208:211], v[144:147], v[48:63]\n"
"ds_read_b128 v[176:179], v5 offset:26496\n"
"v_mfma_f32_32x32x16_bf16 v[48:63], a[212:215], v[148:151], v[48:63]\n"
"ds_read_b128 v[180:183], v5 offset:27008\n"
"v_mfma_f32_32x32x16_bf16 v[48:63], a[216:219], v[152:155], v[48:63]\n"
"ds_read_b128 v[184:187], v5 offset:35712\n"
"v_mfma_f32_32x32x16_bf16 v[48:63], a[220:223], v[156:159], v[48:63]\n"
"ds_read_b128 v[188:191], v5 offset:36224\n"
"s_waitcnt lgkmcnt(4)\n"
"v_mfma_f32_32x32x16_bf16 v[48:63], a[224:227], v[160:163], v[48:63]\n"
"s_waitcnt vmcnt(11)\n"
"s_barrier\n"
"ds_read_b128 v[128:131], v4 offset:0\n"
"v_mfma_f32_32x32x16_bf16 v[48:63], a[228:231], v[164:167], v[48:63]\n"
"ds_read_b128 v[132:135], v4 offset:512\n"
"v_mfma_f32_32x32x16_bf16 v[48:63], a[232:235], v[168:171], v[48:63]\n"
"ds_read_b128 v[136:139], v4 offset:9216\n"
"v_mfma_f32_32x32x16_bf16 v[48:63], a[236:239], v[172:175], v[48:63]\n"
"ds_read_b128 v[140:143], v4 offset:9728\n"
"s_waitcnt lgkmcnt(4)\n"
"v_mfma_f32_32x32x16_bf16 v[48:63], a[240:243], v[176:179], v[48:63]\n"
"ds_read_b128 v[144:147], v4 offset:18432\n"
"v_mfma_f32_32x32x16_bf16 v[48:63], a[244:247], v[180:183], v[48:63]\n"
"ds_read_b128 v[148:151], v4 offset:18944\n"
"v_mfma_f32_32x32x16_bf16 v[48:63], a[248:251], v[184:187], v[48:63]\n"
"ds_read_b128 v[152:155], v4 offset:27648\n"
"s_sub_u32 s62, s62, 1\n"
"v_mfma_f32_32x32x16_bf16 v[48:63], a[252:255], v[188:191], v[48:63]\n"
"ds_read_b128 v[156:159], v4 offset:28160\n"
"s_cbranch_scc0 .L_hl\n"
"s_nop 1\n"
"ds_read_b32 v74, v11 offset:128\n"
"s_add_u32 s68, s68, 1187840\n"
"s_waitcnt lgkmcnt(0)\n"
"v_lshlrev_b32 v72, 16, v74\n"
"v_and_b32 v74, 0xffff0000, v74\n"
"v_rcp_f32 v73, v74\n"
"v_mul_f32 v74, v74, v74\n"
"v_mul_f32 v73, 0xbfb8aa3b, v73\n"
"v_fma_f32 v48, -v72, v112, v48\n"
"v_fma_f32 v49, -v72, v113, v49\n"
"v_fma_f32 v50, -v72, v114, v50\n"
"v_fma_f32 v51, -v72, v115, v51\n"
"v_fma_f32 v52, -v72, v116, v52\n"
"v_fma_f32 v53, -v72, v117, v53\n"
"v_fma_f32 v54, -v72, v118, v54\n"
"v_fma_f32 v55, -v72, v119, v55\n"
"v_fma_f32 v56, -v72, v120, v56\n"
"v_fma_f32 v57, -v72, v121, v57\n"
"v_fma_f32 v58, -v72, v122, v58\n"
"v_fma_f32 v59, -v72, v123, v59\n"
"v_fma_f32 v60, -v72, v124, v60\n"
"v_fma_f32 v61, -v72, v125, v61\n"
"v_fma_f32 v62, -v72, v126, v62\n"
"v_fma_f32 v63, -v72, v127, v63\n"
"v_mul_f32 v76, v48, v73\n"
"v_mul_f32 v77, v49, v73\n"
"v_mul_f32 v78, v50, v73\n"
"v_mul_f32 v79, v51, v73\n"
"v_mul_f32 v80, v52, v73\n"
"v_mul_f32 v81, v53, v73\n"
"v_mul_f32 v82, v54, v73\n"
"v_mul_f32 v83, v55, v73\n"
"v_exp_f32 v76, v76\n"
"v_exp_f32 v77, v77\n"
"v_mul_f32 v48, v48, v56\n"
"v_mul_f32 v49, v49, v57\n"
"v_exp_f32 v78, v78\n"
"v_exp_f32 v79, v79\n"
"v_mul_f32 v50, v50, v58\n"
"v_mul_f32 v51, v51, v59\n"
"v_exp_f32 v80, v80\n"
"v_exp_f32 v81, v81\n"
"v_mul_f32 v52, v52, v60\n"
"v_mul_f32 v53, v53, v61\n"
"v_exp_f32 v82, v82\n"
"v_exp_f32 v83, v83\n"
"v_mul_f32 v54, v54, v62\n"
"v_mul_f32 v55, v55, v63\n"
"v_fma_f32 v76, v76, v74, v74\n"
"v_fma_f32 v77, v77, v74, v74\n"
"v_rcp_f32 v76, v76\n"
"v_rcp_f32 v77, v77\n"
"v_fma_f32 v78, v78, v74, v74\n"
"v_fma_f32 v79, v79, v74, v74\n"
"v_rcp_f32 v78, v78\n"
"v_rcp_f32 v79, v79\n"
"v_fma_f32 v80, v80, v74, v74\n"
"v_fma_f32 v81, v81, v74, v74\n"
"v_rcp_f32 v80, v80\n"
"v_rcp_f32 v81, v81\n"
"v_fma_f32 v82, v82, v74, v74\n"
"v_fma_f32 v83, v83, v74, v74\n"
"v_rcp_f32 v82, v82\n"
"v_rcp_f32 v83, v83\n"
"v_mul_f32 v48, v48, v76\n"
"v_mul_f32 v49, v49, v77\n"
"v_mul_f32 v50, v50, v78\n"
"v_mul_f32 v51, v51, v79\n"
"v_mul_f32 v52, v52, v80\n"
"v_mul_f32 v53, v53, v81\n"
"v_mul_f32 v54, v54, v82\n"
"v_mul_f32 v55, v55, v83\n"
"v_cvt_pk_bf16_f32 v84, v48, v49\n"
"v_cvt_pk_bf16_f32 v85, v50, v51\n"
"v_cvt_pk_bf16_f32 v86, v52, v53\n"
"v_cvt_pk_bf16_f32 v87, v54, v55\n"
"buffer_store_dwordx4 v[84:87], v15, s[32:35], s68 offen sc1\n"
"s_waitcnt vmcnt(4)\n"
"s_barrier\n"
"buffer_store_dwordx2 v[16:17], v18, s[32:35], s68 offen sc1\n"
"s_add_u32 s68, s68, 1187840\n"
"s_waitcnt vmcnt(1)\n"
"s_barrier\n"
"buffer_store_dwordx2 v[16:17], v18, s[32:35], s68 offen sc1\n"
"s_branch .L_end\n"
".L_d:\n"
"s_lshl_b32 s65, s50, 11\n"
"s_mul_i32 s48, s50, 1152\n"
"s_add_u32 s48, s48, 4612096\n"
"s_add_u32 s65, s65, 4833280\n"
"s_cmp_eq_u32 s29, 0\n"
"s_cselect_b32 s48, s65, s48\n"
"s_cselect_b32 s49, 280, 0\n"
"s_lshl2_add_u32 s49, s27, s49\n"
"s_waitcnt lgkmcnt(0)\n"
"s_add_u32 s54, s52, 0x7f4a7c15\n"
"s_addc_u32 s55, s53, 0x9e3779b9\n"
"s_lshl_b32 s67, s49, 5\n"
"s_add_u32 s67, s67, 4898816\n"
"s_sub_u32 s51, s67, s48\n"
"s_add_u32 s92, s32, s51\n"
"s_addc_u32 s93, s33, 0x10000\n"
"s_cmp_eq_u32 s26, 0\n"
"s_cselect_b32 s94, 1, 0\n"
"s_mov_b32 s95, 0x20000\n"
"v_mov_b32 v8, s54\n"
"v_mov_b32 v9, s55\n"
"v_cmp_gt_u32 vcc, 12, v1\n"
"v_mul_u32_u24 v3, 296960, v1\n"
"v_mov_b32 v6, 0x7fff0000\n"
"v_cndmask_b32 v3, v6, v3, vcc\n"
"s_add_u32 s51, s67, 76021760\n"
"buffer_store_dwordx2 v[8:9], v3, s[32:35], s51 offen sc1\n"
"s_sub_u32 s67, s48, 296960\n"
"s_mul_i32 s48, s28, 296960\n"
"s_add_u32 s65, s65, s48\n"
"s_mul_i32 s96, s29, 3670016\n"
"s_add_u32 s36, s18, 0x0\n"
"s_addc_u32 s37, s19, 0x0\n"
"s_and_b32 s37, s37, 0xffff\n"
"s_mov_b32 s38, 0x700000\n"
"s_mov_b32 s39, 0x20000\n"
"s_add_u32 s40, s18, 0xfffffc80\n"
"s_addc_u32 s41, s19, 0xffffffff\n"
"s_and_b32 s41, s41, 0xffff\n"
"s_mov_b32 s42, 0x700380\n"
"s_mov_b32 s43, 0x20000\n"
"s_lshl_b32 s48, s27, 8\n"
"s_add_u32 s96, s96, s48\n"
"v_and_b32 v8, 3, v1\n"
"v_mul_u32_u24 v8, 16, v8\n"
"s_and_b32 s48, s26, 1\n"
"s_lshl_b32 s48, s48, 6\n"
"s_lshr_b32 s49, s26, 1\n"
"s_lshl_b32 s49, s49, 10\n"
"s_add_u32 s48, s48, s49\n"
"v_add_u32 v8, s48, v8\n"
"v_lshrrev_b32 v4, 3, v1\n"
"v_bfe_u32 v5, v1, 4, 1\n"
"s_and_b32 s48, s26, 1\n"
"s_lshl_b32 s48, s48, 1\n"
"v_or_b32 v5, s48, v5\n"
"v_lshlrev_b32 v5, 1, v5\n"
"v_and_b32 v6, 7, v1\n"
"v_xor_b32 v5, v5, v6\n"
"v_mul_u32_u24 v4, 2048, v4\n"
"v_lshl_add_u32 v7, v5, 4, v4\n"
"s_mul_i32 s48, s26, 16384\n"
"s_add_u32 s96, s96, s48\n"
"s_mul_i32 s97, s26, 2048\n"
"s_add_u32 m0, s97, 0\n"
"s_nop 0\n"
"buffer_load_dwordx4 v7, s[36:39], s96 offen offset:0 lds\n"
"buffer_load_dwordx4 v7, s[40:43], s96 offen offset:1024 lds\n"
"s_add_u32 s96, s96, 65536\n"
"s_add_u32 m0, s97, 8192\n"
"s_nop 0\n"
"buffer_load_dwordx4 v7, s[36:39], s96 offen offset:0 lds\n"
"buffer_load_dwordx4 v7, s[40:43], s96 offen offset:1024 lds\n"
"s_add_u32 s96, s96, 65536\n"
"s_add_u32 m0, s97, 16384\n"
"s_nop 0\n"
"buffer_load_dwordx4 v7, s[36:39], s96 offen offset:0 lds\n"
"buffer_load_dwordx4 v7, s[40:43], s96 offen offset:1024 lds\n"
"s_add_u32 s96, s96, 65536\n"
"s_add_u32 m0, s97, 24576\n"
"s_nop 0\n"
"buffer_load_dwordx4 v7, s[36:39], s96 offen offset:0 lds\n"
"buffer_load_dwordx4 v7, s[40:43], s96 offen offset:1024 lds\n"
"s_add_u32 s96, s96, 65536\n"
"s_add_u32 m0, s97, 32768\n"
"s_nop 0\n"
"buffer_load_dwordx4 v7, s[36:39], s96 offen offset:0 lds\n"
"buffer_load_dwordx4 v7, s[40:43], s96 offen offset:1024 lds\n"
"s_add_u32 s96, s96, 65536\n"
"s_add_u32 m0, s97, 40960\n"
"s_nop 0\n"
"buffer_load_dwordx4 v7, s[36:39], s96 offen offset:0 lds\n"
"buffer_load_dwordx4 v7, s[40:43], s96 offen offset:1024 lds\n"
"s_add_u32 s96, s96, 65536\n"
"s_add_u32 m0, s97, 49152\n"
"s_nop 0\n"
"buffer_load_dwordx4 v7, s[36:39], s96 offen offset:0 lds\n"
"buffer_load_dwordx4 v7, s[40:43], s96 offen offset:1024 lds\n"
"s_add_u32 s96, s96, 65536\n"
"s_add_u32 m0, s97, 57344\n"
"s_nop 0\n"
"buffer_load_dwordx4 v7, s[36:39], s96 offen offset:0 lds\n"
"buffer_load_dwordx4 v7, s[40:43], s96 offen offset:1024 lds\n"
"s_add_u32 s96, s96, 65536\n"
"s_add_u32 m0, s97, 65536\n"
"s_nop 0\n"
"buffer_load_dwordx4 v7, s[36:39], s96 offen offset:0 lds\n"
"buffer_load_dwordx4 v7, s[40:43], s96 offen offset:1024 lds\n"
"s_add_u32 s96, s96, 65536\n"
"s_add_u32 m0, s97, 73728\n"
"s_nop 0\n"
"buffer_load_dwordx4 v7, s[36:39], s96 offen offset:0 lds\n"
"buffer_load_dwordx4 v7, s[40:43], s96 offen offset:1024 lds\n"
"s_add_u32 s96, s96, 65536\n"
"s_add_u32 m0, s97, 81920\n"
"s_nop 0\n"
"buffer_load_dwordx4 v7, s[36:39], s96 offen offset:0 lds\n"
"buffer_load_dwordx4 v7, s[40:43], s96 offen offset:1024 lds\n"
"s_add_u32 s96, s96, 65536\n"
"s_add_u32 m0, s97, 90112\n"
"s_nop 0\n"
"buffer_load_dwordx4 v7, s[36:39], s96 offen offset:0 lds\n"
"buffer_load_dwordx4 v7, s[40:43], s96 offen offset:1024 lds\n"
"s_add_u32 s96, s96, 65536\n"
"s_add_u32 m0, s97, 98304\n"
"s_nop 0\n"
"buffer_load_dwordx4 v7, s[36:39], s96 offen offset:0 lds\n"
"buffer_load_dwordx4 v7, s[40:43], s96 offen offset:1024 lds\n"
"s_add_u32 s96, s96, 65536\n"
"s_add_u32 m0, s97, 106496\n"
"s_nop 0\n"
"buffer_load_dwordx4 v7, s[36:39], s96 offen offset:0 lds\n"
"buffer_load_dwordx4 v7, s[40:43], s96 offen offset:1024 lds\n"
"s_add_u32 s96, s96, 65536\n"
"v_lshrrev_b32 v5, 4, v1\n"
"v_bfe_u32 v4, v1, 3, 1\n"
"v_and_b32 v6, 1, v5\n"
"v_lshl_add_u32 v4, v6, 1, v4\n"
"v_lshlrev_b32 v4, 5, v4\n"
"v_mul_u32_u24 v5, 2048, v5\n"
"v_bfe_u32 v6, v1, 2, 2\n"
"v_lshl_add_u32 v5, v6, 7, v5\n"
"v_xor_b32 v8, v4, v8\n"
"v_add_u32 v8, v5, v8\n"
"v_add_u32 v9, 65536, v8\n"
"s_waitcnt vmcnt(24)\n"
"s_barrier\n"
"s_add_u32 m0, s97, 114688\n"
"s_nop 0\n"
"buffer_load_dwordx4 v7, s[36:39], s96 offen offset:0 lds\n"
"buffer_load_dwordx4 v7, s[40:43], s96 offen offset:1024 lds\n"
"s_add_u32 s96, s96, 65536\n"
"s_add_u32 m0, s97, 122880\n"
"s_nop 0\n"
"buffer_load_dwordx4 v7, s[36:39], s96 offen offset:0 lds\n"
"buffer_load_dwordx4 v7, s[40:43], s96 offen offset:1024 lds\n"
"s_add_u32 s96, s96, 65536\n"
"ds_read_b64_tr_b16 a[0:1], v8 offset:0\n"
"ds_read_b64_tr_b16 a[2:3], v8 offset:512\n"
"ds_read_b64_tr_b16 a[4:5], v8 offset:8\n"
"ds_read_b64_tr_b16 a[6:7], v8 offset:520\n"
"ds_read_b64_tr_b16 a[8:9], v8 offset:8192\n"
"ds_read_b64_tr_b16 a[10:11], v8 offset:8704\n"
"ds_read_b64_tr_b16 a[12:13], v8 offset:8200\n"
"ds_read_b64_tr_b16 a[14:15], v8 offset:8712\n"
"s_waitcnt lgkmcnt(0)\n"
"s_waitcnt vmcnt(24)\n"
"s_barrier\n"
"s_add_u32 m0, s97, 0\n"
"s_nop 0\n"
"buffer_load_dwordx4 v7, s[36:39], s96 offen offset:0 lds\n"
"buffer_load_dwordx4 v7, s[40:43], s96 offen offset:1024 lds\n"
"s_add_u32 s96, s96, 65536\n"
"s_add_u32 m0, s97, 8192\n"
"s_nop 0\n"
"buffer_load_dwordx4 v7, s[36:39], s96 offen offset:0 lds\n"
"buffer_load_dwordx4 v7, s[40:43], s96 offen offset:1024 lds\n"
"s_add_u32 s96, s96, 65536\n"
"ds_read_b64_tr_b16 a[16:17], v8 offset:16384\n"
"ds_read_b64_tr_b16 a[18:19], v8 offset:16896\n"
"ds_read_b64_tr_b16 a[20:21], v8 offset:16392\n"
"ds_read_b64_tr_b16 a[22:23], v8 offset:16904\n"
"ds_read_b64_tr_b16 a[24:25], v8 offset:24576\n"
"ds_read_b64_tr_b16 a[26:27], v8 offset:25088\n"
"ds_read_b64_tr_b16 a[28:29], v8 offset:24584\n"
"ds_read_b64_tr_b16 a[30:31], v8 offset:25096\n"
"s_waitcnt lgkmcnt(0)\n"
"s_waitcnt vmcnt(24)\n"
"s_barrier\n"
"s_add_u32 m0, s97, 16384\n"
"s_nop 0\n"
"buffer_load_dwordx4 v7, s[36:39], s96 offen offset:0 lds\n"
"buffer_load_dwordx4 v7, s[40:43], s96 offen offset:1024 lds\n"
"s_add_u32 s96, s96, 65536\n"
"s_add_u32 m0, s97, 24576\n"
"s_nop 0\n"
"buffer_load_dwordx4 v7, s[36:39], s96 offen offset:0 lds\n"
"buffer_load_dwordx4 v7, s[40:43], s96 offen offset:1024 lds\n"
"s_add_u32 s96, s96, 65536\n"
"ds_read_b64_tr_b16 a[32:33], v8 offset:32768\n"
"ds_read_b64_tr_b16 a[34:35], v8 offset:33280\n"
"ds_read_b64_tr_b16 a[36:37], v8 offset:32776\n"
"ds_read_b64_tr_b16 a[38:39], v8 offset:33288\n"
"ds_read_b64_tr_b16 a[40:41], v8 offset:40960\n"
"ds_read_b64_tr_b16 a[42:43], v8 offset:41472\n"
"ds_read_b64_tr_b16 a[44:45], v8 offset:40968\n"
"ds_read_b64_tr_b16 a[46:47], v8 offset:41480\n"
"s_waitcnt lgkmcnt(0)\n"
"s_waitcnt vmcnt(24)\n"
"s_barrier\n"
"s_add_u32 m0, s97, 32768\n"
"s_nop 0\n"
"buffer_load_dwordx4 v7, s[36:39], s96 offen offset:0 lds\n"
"buffer_load_dwordx4 v7, s[40:43], s96 offen offset:1024 lds\n"
"s_add_u32 s96, s96, 65536\n"
"s_add_u32 m0, s97, 40960\n"
"s_nop 0\n"
"buffer_load_dwordx4 v7, s[36:39], s96 offen offset:0 lds\n"
"buffer_load_dwordx4 v7, s[40:43], s96 offen offset:1024 lds\n"
"s_add_u32 s96, s96, 65536\n"
"ds_read_b64_tr_b16 a[48:49], v8 offset:49152\n"
"ds_read_b64_tr_b16 a[50:51], v8 offset:49664\n"
"ds_read_b64_tr_b16 a[52:53], v8 offset:49160\n"
"ds_read_b64_tr_b16 a[54:55], v8 offset:49672\n"
"ds_read_b64_tr_b16 a[56:57], v8 offset:57344\n"
"ds_read_b64_tr_b16 a[58:59], v8 offset:57856\n"
"ds_read_b64_tr_b16 a[60:61], v8 offset:57352\n"
"ds_read_b64_tr_b16 a[62:63], v8 offset:57864\n"
"s_waitcnt lgkmcnt(0)\n"
"s_waitcnt vmcnt(24)\n"
"s_barrier\n"
"s_add_u32 m0, s97, 49152\n"
"s_nop 0\n"
"buffer_load_dwordx4 v7, s[36:39], s96 offen offset:0 lds\n"
"buffer_load_dwordx4 v7, s[40:43], s96 offen offset:1024 lds\n"
"s_add_u32 s96, s96, 65536\n"
"s_add_u32 m0, s97, 57344\n"
"s_nop 0\n"
"buffer_load_dwordx4 v7, s[36:39], s96 offen offset:0 lds\n"
"buffer_load_dwordx4 v7, s[40:43], s96 offen offset:1024 lds\n"
"s_add_u32 s96, s96, 65536\n"
"ds_read_b64_tr_b16 a[64:65], v9 offset:0\n"
"ds_read_b64_tr_b16 a[66:67], v9 offset:512\n"
"ds_read_b64_tr_b16 a[68:69], v9 offset:8\n"
"ds_read_b64_tr_b16 a[70:71], v9 offset:520\n"
"ds_read_b64_tr_b16 a[72:73], v9 offset:8192\n"
"ds_read_b64_tr_b16 a[74:75], v9 offset:8704\n"
"ds_read_b64_tr_b16 a[76:77], v9 offset:8200\n"
"ds_read_b64_tr_b16 a[78:79], v9 offset:8712\n"
"s_waitcnt lgkmcnt(0)\n"
"s_waitcnt vmcnt(24)\n"
"s_barrier\n"
"s_add_u32 m0, s97, 65536\n"
"s_nop 0\n"
"buffer_load_dwordx4 v7, s[36:39], s96 offen offset:0 lds\n"
"buffer_load_dwordx4 v7, s[40:43], s96 offen offset:1024 lds\n"
"s_add_u32 s96, s96, 65536\n"
"s_add_u32 m0, s97, 73728\n"
"s_nop 0\n"
"buffer_load_dwordx4 v7, s[36:39], s96 offen offset:0 lds\n"
"buffer_load_dwordx4 v7, s[40:43], s96 offen offset:1024 lds\n"
"s_add_u32 s96, s96, 65536\n"
"ds_read_b64_tr_b16 a[80:81], v9 offset:16384\n"
"ds_read_b64_tr_b16 a[82:83], v9 offset:16896\n"
"ds_read_b64_tr_b16 a[84:85], v9 offset:16392\n"
"ds_read_b64_tr_b16 a[86:87], v9 offset:16904\n"
"ds_read_b64_tr_b16 a[88:89], v9 offset:24576\n"
"ds_read_b64_tr_b16 a[90:91], v9 offset:25088\n"
"ds_read_b64_tr_b16 a[92:93], v9 offset:24584\n"
"ds_read_b64_tr_b16 a[94:95], v9 offset:25096\n"
"s_waitcnt lgkmcnt(0)\n"
"s_waitcnt vmcnt(24)\n"
"s_barrier\n"
"s_add_u32 m0, s97, 81920\n"
"s_nop 0\n"
"buffer_load_dwordx4 v7, s[36:39], s96 offen offset:0 lds\n"
"buffer_load_dwordx4 v7, s[40:43], s96 offen offset:1024 lds\n"
"s_add_u32 s96, s96, 65536\n"
"s_add_u32 m0, s97, 90112\n"
"s_nop 0\n"
"buffer_load_dwordx4 v7, s[36:39], s96 offen offset:0 lds\n"
"buffer_load_dwordx4 v7, s[40:43], s96 offen offset:1024 lds\n"
"s_add_u32 s96, s96, 65536\n"
"ds_read_b64_tr_b16 a[96:97], v9 offset:32768\n"
"ds_read_b64_tr_b16 a[98:99], v9 offset:33280\n"
"ds_read_b64_tr_b16 a[100:101], v9 offset:32776\n"
"ds_read_b64_tr_b16 a[102:103], v9 offset:33288\n"
"ds_read_b64_tr_b16 a[104:105], v9 offset:40960\n"
"ds_read_b64_tr_b16 a[106:107], v9 offset:41472\n"
"ds_read_b64_tr_b16 a[108:109], v9 offset:40968\n"
"ds_read_b64_tr_b16 a[110:111], v9 offset:41480\n"
"s_waitcnt lgkmcnt(0)\n"
"s_waitcnt vmcnt(24)\n"
"s_barrier\n"
"s_add_u32 m0, s97, 98304\n"
"s_nop 0\n"
"buffer_load_dwordx4 v7, s[36:39], s96 offen offset:0 lds\n"
"buffer_load_dwordx4 v7, s[40:43], s96 offen offset:1024 lds\n"
"s_add_u32 s96, s96, 65536\n"
"s_add_u32 m0, s97, 106496\n"
"s_nop 0\n"
"buffer_load_dwordx4 v7, s[36:39], s96 offen offset:0 lds\n"
"buffer_load_dwordx4 v7, s[40:43], s96 offen offset:1024 lds\n"
"s_add_u32 s96, s96, 65536\n"
"ds_read_b64_tr_b16 a[112:113], v9 offset:49152\n"
"ds_read_b64_tr_b16 a[114:115], v9 offset:49664\n"
"ds_read_b64_tr_b16 a[116:117], v9 offset:49160\n"
"ds_read_b64_tr_b16 a[118:119], v9 offset:49672\n"
"ds_read_b64_tr_b16 a[120:121], v9 offset:57344\n"
"ds_read_b64_tr_b16 a[122:123], v9 offset:57856\n"
"ds_read_b64_tr_b16 a[124:125], v9 offset:57352\n"
"ds_read_b64_tr_b16 a[126:127], v9 offset:57864\n"
"s_waitcnt lgkmcnt(0)\n"
"s_waitcnt vmcnt(24)\n"
"s_barrier\n"
"s_add_u32 m0, s97, 114688\n"
"s_nop 0\n"
"buffer_load_dwordx4 v7, s[36:39], s96 offen offset:0 lds\n"
"buffer_load_dwordx4 v7, s[40:43], s96 offen offset:1024 lds\n"
"s_add_u32 s96, s96, 65536\n"
"s_add_u32 m0, s97, 122880\n"
"s_nop 0\n"
"buffer_load_dwordx4 v7, s[36:39], s96 offen offset:0 lds\n"
"buffer_load_dwordx4 v7, s[40:43], s96 offen offset:1024 lds\n"
"s_add_u32 s96, s96, 65536\n"
"ds_read_b64_tr_b16 a[128:129], v8 offset:0\n"
"ds_read_b64_tr_b16 a[130:131], v8 offset:512\n"
"ds_read_b64_tr_b16 a[132:133], v8 offset:8\n"
"ds_read_b64_tr_b16 a[134:135], v8 offset:520\n"
"ds_read_b64_tr_b16 a[136:137], v8 offset:8192\n"
"ds_read_b64_tr_b16 a[138:139], v8 offset:8704\n"
"ds_read_b64_tr_b16 a[140:141], v8 offset:8200\n"
"ds_read_b64_tr_b16 a[142:143], v8 offset:8712\n"
"s_waitcnt lgkmcnt(0)\n"
"s_waitcnt vmcnt(24)\n"
"s_barrier\n"
"s_add_u32 m0, s97, 0\n"
"s_nop 0\n"
"buffer_load_dwordx4 v7, s[36:39], s96 offen offset:0 lds\n"
"buffer_load_dwordx4 v7, s[40:43], s96 offen offset:1024 lds\n"
"s_add_u32 s96, s96, 65536\n"
"s_add_u32 m0, s97, 8192\n"
"s_nop 0\n"
"buffer_load_dwordx4 v7, s[36:39], s96 offen offset:0 lds\n"
"buffer_load_dwordx4 v7, s[40:43], s96 offen offset:1024 lds\n"
"s_add_u32 s96, s96, 65536\n"
"ds_read_b64_tr_b16 a[144:145], v8 offset:16384\n"
"ds_read_b64_tr_b16 a[146:147], v8 offset:16896\n"
"ds_read_b64_tr_b16 a[148:149], v8 offset:16392\n"
"ds_read_b64_tr_b16 a[150:151], v8 offset:16904\n"
"ds_read_b64_tr_b16 a[152:153], v8 offset:24576\n"
"ds_read_b64_tr_b16 a[154:155], v8 offset:25088\n"
"ds_read_b64_tr_b16 a[156:157], v8 offset:24584\n"
"ds_read_b64_tr_b16 a[158:159], v8 offset:25096\n"
"s_waitcnt lgkmcnt(0)\n"
"s_waitcnt vmcnt(24)\n"
"s_barrier\n"
"s_add_u32 m0, s97, 16384\n"
"s_nop 0\n"
"buffer_load_dwordx4 v7, s[36:39], s96 offen offset:0 lds\n"
"buffer_load_dwordx4 v7, s[40:43], s96 offen offset:1024 lds\n"
"s_add_u32 s96, s96, 65536\n"
"s_add_u32 m0, s97, 24576\n"
"s_nop 0\n"
"buffer_load_dwordx4 v7, s[36:39], s96 offen offset:0 lds\n"
"buffer_load_dwordx4 v7, s[40:43], s96 offen offset:1024 lds\n"
"s_add_u32 s96, s96, 65536\n"
"ds_read_b64_tr_b16 a[160:161], v8 offset:32768\n"
"ds_read_b64_tr_b16 a[162:163], v8 offset:33280\n"
"ds_read_b64_tr_b16 a[164:165], v8 offset:32776\n"
"ds_read_b64_tr_b16 a[166:167], v8 offset:33288\n"
"ds_read_b64_tr_b16 a[168:169], v8 offset:40960\n"
"ds_read_b64_tr_b16 a[170:171], v8 offset:41472\n"
"ds_read_b64_tr_b16 a[172:173], v8 offset:40968\n"
"ds_read_b64_tr_b16 a[174:175], v8 offset:41480\n"
"s_waitcnt lgkmcnt(0)\n"
"s_waitcnt vmcnt(24)\n"
"s_barrier\n"
"s_add_u32 m0, s97, 32768\n"
"s_nop 0\n"
"buffer_load_dwordx4 v7, s[36:39], s96 offen offset:0 lds\n"
"buffer_load_dwordx4 v7, s[40:43], s96 offen offset:1024 lds\n"
"s_add_u32 s96, s96, 65536\n"
"s_add_u32 m0, s97, 40960\n"
"s_nop 0\n"
"buffer_load_dwordx4 v7, s[36:39], s96 offen offset:0 lds\n"
"buffer_load_dwordx4 v7, s[40:43], s96 offen offset:1024 lds\n"
"s_add_u32 s96, s96, 65536\n"
"ds_read_b64_tr_b16 a[176:177], v8 offset:49152\n"
"ds_read_b64_tr_b16 a[178:179], v8 offset:49664\n"
"ds_read_b64_tr_b16 a[180:181], v8 offset:49160\n"
"ds_read_b64_tr_b16 a[182:183], v8 offset:49672\n"
"ds_read_b64_tr_b16 a[184:185], v8 offset:57344\n"
"ds_read_b64_tr_b16 a[186:187], v8 offset:57856\n"
"ds_read_b64_tr_b16 a[188:189], v8 offset:57352\n"
"ds_read_b64_tr_b16 a[190:191], v8 offset:57864\n"
"s_waitcnt lgkmcnt(0)\n"
"s_waitcnt vmcnt(24)\n"
"s_barrier\n"
"s_add_u32 m0, s97, 49152\n"
"s_nop 0\n"
"buffer_load_dwordx4 v7, s[36:39], s96 offen offset:0 lds\n"
"buffer_load_dwordx4 v7, s[40:43], s96 offen offset:1024 lds\n"
"s_add_u32 s96, s96, 65536\n"
"s_add_u32 m0, s97, 57344\n"
"s_nop 0\n"
"buffer_load_dwordx4 v7, s[36:39], s96 offen offset:0 lds\n"
"buffer_load_dwordx4 v7, s[40:43], s96 offen offset:1024 lds\n"
"s_add_u32 s96, s96, 65536\n"
"ds_read_b64_tr_b16 a[192:193], v9 offset:0\n"
"ds_read_b64_tr_b16 a[194:195], v9 offset:512\n"
"ds_read_b64_tr_b16 a[196:197], v9 offset:8\n"
"ds_read_b64_tr_b16 a[198:199], v9 offset:520\n"
"ds_read_b64_tr_b16 a[200:201], v9 offset:8192\n"
"ds_read_b64_tr_b16 a[202:203], v9 offset:8704\n"
"ds_read_b64_tr_b16 a[204:205], v9 offset:8200\n"
"ds_read_b64_tr_b16 a[206:207], v9 offset:8712\n"
"s_waitcnt lgkmcnt(0)\n"
"s_waitcnt vmcnt(24)\n"
"s_barrier\n"
"s_add_u32 m0, s97, 65536\n"
"s_nop 0\n"
"buffer_load_dwordx4 v7, s[36:39], s96 offen offset:0 lds\n"
"buffer_load_dwordx4 v7, s[40:43], s96 offen offset:1024 lds\n"
"s_add_u32 s96, s96, 65536\n"
"s_add_u32 m0, s97, 73728\n"
"s_nop 0\n"
"buffer_load_dwordx4 v7, s[36:39], s96 offen offset:0 lds\n"
"buffer_load_dwordx4 v7, s[40:43], s96 offen offset:1024 lds\n"
"s_add_u32 s96, s96, 65536\n"
"ds_read_b64_tr_b16 a[208:209], v9 offset:16384\n"
"ds_read_b64_tr_b16 a[210:211], v9 offset:16896\n"
"ds_read_b64_tr_b16 a[212:213], v9 offset:16392\n"
"ds_read_b64_tr_b16 a[214:215], v9 offset:16904\n"
"ds_read_b64_tr_b16 a[216:217], v9 offset:24576\n"
"ds_read_b64_tr_b16 a[218:219], v9 offset:25088\n"
"ds_read_b64_tr_b16 a[220:221], v9 offset:24584\n"
"ds_read_b64_tr_b16 a[222:223], v9 offset:25096\n"
"s_waitcnt lgkmcnt(0)\n"
"s_waitcnt vmcnt(24)\n"
"s_barrier\n"
"s_add_u32 m0, s97, 81920\n"
"s_nop 0\n"
"buffer_load_dwordx4 v7, s[36:39], s96 offen offset:0 lds\n"
"buffer_load_dwordx4 v7, s[40:43], s96 offen offset:1024 lds\n"
"s_add_u32 s96, s96, 65536\n"
"s_add_u32 m0, s97, 90112\n"
"s_nop 0\n"
"buffer_load_dwordx4 v7, s[36:39], s96 offen offset:0 lds\n"
"buffer_load_dwordx4 v7, s[40:43], s96 offen offset:1024 lds\n"
"s_add_u32 s96, s96, 65536\n"
"ds_read_b64_tr_b16 a[224:225], v9 offset:32768\n"
"ds_read_b64_tr_b16 a[226:227], v9 offset:33280\n"
"ds_read_b64_tr_b16 a[228:229], v9 offset:32776\n"
"ds_read_b64_tr_b16 a[230:231], v9 offset:33288\n"
"ds_read_b64_tr_b16 a[232:233], v9 offset:40960\n"
"ds_read_b64_tr_b16 a[234:235], v9 offset:41472\n"
"ds_read_b64_tr_b16 a[236:237], v9 offset:40968\n"
"ds_read_b64_tr_b16 a[238:239], v9 offset:41480\n"
"s_waitcnt lgkmcnt(0)\n"
"s_waitcnt vmcnt(24)\n"
"s_barrier\n"
"s_add_u32 m0, s97, 98304\n"
"s_nop 0\n"
"buffer_load_dwordx4 v7, s[36:39], s96 offen offset:0 lds\n"
"buffer_load_dwordx4 v7, s[40:43], s96 offen offset:1024 lds\n"
"s_add_u32 s96, s96, 65536\n"
"s_add_u32 m0, s97, 106496\n"
"s_nop 0\n"
"buffer_load_dwordx4 v7, s[36:39], s96 offen offset:0 lds\n"
"buffer_load_dwordx4 v7, s[40:43], s96 offen offset:1024 lds\n"
"s_add_u32 s96, s96, 65536\n"
"ds_read_b64_tr_b16 a[240:241], v9 offset:49152\n"
"ds_read_b64_tr_b16 a[242:243], v9 offset:49664\n"
"ds_read_b64_tr_b16 a[244:245], v9 offset:49160\n"
"ds_read_b64_tr_b16 a[246:247], v9 offset:49672\n"
"ds_read_b64_tr_b16 a[248:249], v9 offset:57344\n"
"ds_read_b64_tr_b16 a[250:251], v9 offset:57856\n"
"ds_read_b64_tr_b16 a[252:253], v9 offset:57352\n"
"ds_read_b64_tr_b16 a[254:255], v9 offset:57864\n"
"s_waitcnt lgkmcnt(0)\n"
"s_waitcnt vmcnt(24)\n"
"s_barrier\n"
"s_add_u32 m0, s97, 114688\n"
"s_nop 0\n"
"buffer_load_dwordx4 v7, s[36:39], s96 offen offset:0 lds\n"
"buffer_load_dwordx4 v7, s[40:43], s96 offen offset:1024 lds\n"
"s_add_u32 s96, s96, 65536\n"
"s_add_u32 m0, s97, 122880\n"
"s_nop 0\n"
"buffer_load_dwordx4 v7, s[36:39], s96 offen offset:0 lds\n"
"buffer_load_dwordx4 v7, s[40:43], s96 offen offset:1024 lds\n"
"s_add_u32 s96, s96, 65536\n"
"ds_read_b64_tr_b16 v[64:65], v8 offset:0\n"
"ds_read_b64_tr_b16 v[66:67], v8 offset:512\n"
"ds_read_b64_tr_b16 v[68:69], v8 offset:8\n"
"ds_read_b64_tr_b16 v[70:71], v8 offset:520\n"
"ds_read_b64_tr_b16 v[72:73], v8 offset:8192\n"
"ds_read_b64_tr_b16 v[74:75], v8 offset:8704\n"
"ds_read_b64_tr_b16 v[76:77], v8 offset:8200\n"
"ds_read_b64_tr_b16 v[78:79], v8 offset:8712\n"
"s_waitcnt lgkmcnt(0)\n"
"s_waitcnt vmcnt(24)\n"
"s_barrier\n"
"s_add_u32 m0, s97, 0\n"
"s_nop 0\n"
"buffer_load_dwordx4 v7, s[36:39], s96 offen offset:0 lds\n"
"buffer_load_dwordx4 v7, s[40:43], s96 offen offset:1024 lds\n"
"s_add_u32 s96, s96, 65536\n"
"s_add_u32 m0, s97, 8192\n"
"s_nop 0\n"
"buffer_load_dwordx4 v7, s[36:39], s96 offen offset:0 lds\n"
"buffer_load_dwordx4 v7, s[40:43], s96 offen offset:1024 lds\n"
"s_add_u32 s96, s96, 65536\n"
"ds_read_b64_tr_b16 v[80:81], v8 offset:16384\n"
"ds_read_b64_tr_b16 v[82:83], v8 offset:16896\n"
"ds_read_b64_tr_b16 v[84:85], v8 offset:16392\n"
"ds_read_b64_tr_b16 v[86:87], v8 offset:16904\n"
"ds_read_b64_tr_b16 v[88:89], v8 offset:24576\n"
"ds_read_b64_tr_b16 v[90:91], v8 offset:25088\n"
"ds_read_b64_tr_b16 v[92:93], v8 offset:24584\n"
"ds_read_b64_tr_b16 v[94:95], v8 offset:25096\n"
"s_waitcnt lgkmcnt(0)\n"
"s_waitcnt vmcnt(24)\n"
"s_barrier\n"
"s_add_u32 m0, s97, 16384\n"
"s_nop 0\n"
"buffer_load_dwordx4 v7, s[36:39], s96 offen offset:0 lds\n"
"buffer_load_dwordx4 v7, s[40:43], s96 offen offset:1024 lds\n"
"s_add_u32 s96, s96, 65536\n"
"s_add_u32 m0, s97, 24576\n"
"s_nop 0\n"
"buffer_load_dwordx4 v7, s[36:39], s96 offen offset:0 lds\n"
"buffer_load_dwordx4 v7, s[40:43], s96 offen offset:1024 lds\n"
"s_add_u32 s96, s96, 65536\n"
"ds_read_b64_tr_b16 v[96:97], v8 offset:32768\n"
"ds_read_b64_tr_b16 v[98:99], v8 offset:33280\n"
"ds_read_b64_tr_b16 v[100:101], v8 offset:32776\n"
"ds_read_b64_tr_b16 v[102:103], v8 offset:33288\n"
"ds_read_b64_tr_b16 v[104:105], v8 offset:40960\n"
"ds_read_b64_tr_b16 v[106:107], v8 offset:41472\n"
"ds_read_b64_tr_b16 v[108:109], v8 offset:40968\n"
"ds_read_b64_tr_b16 v[110:111], v8 offset:41480\n"
"s_waitcnt lgkmcnt(0)\n"
"s_waitcnt vmcnt(24)\n"
"s_barrier\n"
"s_add_u32 m0, s97, 32768\n"
"s_nop 0\n"
"buffer_load_dwordx4 v7, s[36:39], s96 offen offset:0 lds\n"
"buffer_load_dwordx4 v7, s[40:43], s96 offen offset:1024 lds\n"
"s_add_u32 s96, s96, 65536\n"
"s_add_u32 m0, s97, 40960\n"
"s_nop 0\n"
"buffer_load_dwordx4 v7, s[36:39], s96 offen offset:0 lds\n"
"buffer_load_dwordx4 v7, s[40:43], s96 offen offset:1024 lds\n"
"s_add_u32 s96, s96, 65536\n"
"ds_read_b64_tr_b16 v[112:113], v8 offset:49152\n"
"ds_read_b64_tr_b16 v[114:115], v8 offset:49664\n"
"ds_read_b64_tr_b16 v[116:117], v8 offset:49160\n"
"ds_read_b64_tr_b16 v[118:119], v8 offset:49672\n"
"ds_read_b64_tr_b16 v[120:121], v8 offset:57344\n"
"ds_read_b64_tr_b16 v[122:123], v8 offset:57856\n"
"ds_read_b64_tr_b16 v[124:125], v8 offset:57352\n"
"ds_read_b64_tr_b16 v[126:127], v8 offset:57864\n"
"s_waitcnt lgkmcnt(0)\n"
"s_waitcnt vmcnt(24)\n"
"s_barrier\n"
"s_add_u32 m0, s97, 49152\n"
"s_nop 0\n"
"buffer_load_dwordx4 v7, s[36:39], s96 offen offset:0 lds\n"
"buffer_load_dwordx4 v7, s[40:43], s96 offen offset:1024 lds\n"
"s_add_u32 s96, s96, 65536\n"
"s_add_u32 m0, s97, 57344\n"
"s_nop 0\n"
"buffer_load_dwordx4 v7, s[36:39], s96 offen offset:0 lds\n"
"buffer_load_dwordx4 v7, s[40:43], s96 offen offset:1024 lds\n"
"s_add_u32 s96, s96, 65536\n"
"ds_read_b64_tr_b16 v[128:129], v9 offset:0\n"
"ds_read_b64_tr_b16 v[130:131], v9 offset:512\n"
"ds_read_b64_tr_b16 v[132:133], v9 offset:8\n"
"ds_read_b64_tr_b16 v[134:135], v9 offset:520\n"
"ds_read_b64_tr_b16 v[136:137], v9 offset:8192\n"
"ds_read_b64_tr_b16 v[138:139], v9 offset:8704\n"
"ds_read_b64_tr_b16 v[140:141], v9 offset:8200\n"
"ds_read_b64_tr_b16 v[142:143], v9 offset:8712\n"
"s_waitcnt lgkmcnt(0)\n"
"s_waitcnt vmcnt(24)\n"
"s_barrier\n"
"ds_read_b64_tr_b16 v[144:145], v9 offset:16384\n"
"ds_read_b64_tr_b16 v[146:147], v9 offset:16896\n"
"ds_read_b64_tr_b16 v[148:149], v9 offset:16392\n"
"ds_read_b64_tr_b16 v[150:151], v9 offset:16904\n"
"ds_read_b64_tr_b16 v[152:153], v9 offset:24576\n"
"ds_read_b64_tr_b16 v[154:155], v9 offset:25088\n"
"ds_read_b64_tr_b16 v[156:157], v9 offset:24584\n"
"ds_read_b64_tr_b16 v[158:159], v9 offset:25096\n"
"s_waitcnt lgkmcnt(0)\n"
"s_waitcnt vmcnt(20)\n"
"s_barrier\n"
"ds_read_b64_tr_b16 v[160:161], v9 offset:32768\n"
"ds_read_b64_tr_b16 v[162:163], v9 offset:33280\n"
"ds_read_b64_tr_b16 v[164:165], v9 offset:32776\n"
"ds_read_b64_tr_b16 v[166:167], v9 offset:33288\n"
"ds_read_b64_tr_b16 v[168:169], v9 offset:40960\n"
"ds_read_b64_tr_b16 v[170:171], v9 offset:41472\n"
"ds_read_b64_tr_b16 v[172:173], v9 offset:40968\n"
"ds_read_b64_tr_b16 v[174:175], v9 offset:41480\n"
"s_waitcnt lgkmcnt(0)\n"
"s_waitcnt vmcnt(16)\n"
"s_barrier\n"
"ds_read_b64_tr_b16 v[176:177], v9 offset:49152\n"
"ds_read_b64_tr_b16 v[178:179], v9 offset:49664\n"
"ds_read_b64_tr_b16 v[180:181], v9 offset:49160\n"
"ds_read_b64_tr_b16 v[182:183], v9 offset:49672\n"
"ds_read_b64_tr_b16 v[184:185], v9 offset:57344\n"
"ds_read_b64_tr_b16 v[186:187], v9 offset:57856\n"
"ds_read_b64_tr_b16 v[188:189], v9 offset:57352\n"
"ds_read_b64_tr_b16 v[190:191], v9 offset:57864\n"
"s_waitcnt lgkmcnt(0)\n"
"s_waitcnt vmcnt(12)\n"
"s_barrier\n"
"ds_read_b64_tr_b16 v[192:193], v8 offset:0\n"
"ds_read_b64_tr_b16 v[194:195], v8 offset:512\n"
"ds_read_b64_tr_b16 v[196:197], v8 offset:8\n"
"ds_read_b64_tr_b16 v[198:199], v8 offset:520\n"
"ds_read_b64_tr_b16 v[200:201], v8 offset:8192\n"
"ds_read_b64_tr_b16 v[202:203], v8 offset:8704\n"
"ds_read_b64_tr_b16 v[204:205], v8 offset:8200\n"
"ds_read_b64_tr_b16 v[206:207], v8 offset:8712\n"
"s_waitcnt lgkmcnt(0)\n"
"s_waitcnt vmcnt(8)\n"
"s_barrier\n"
"ds_read_b64_tr_b16 v[208:209], v8 offset:16384\n"
"ds_read_b64_tr_b16 v[210:211], v8 offset:16896\n"
"ds_read_b64_tr_b16 v[212:213], v8 offset:16392\n"
"ds_read_b64_tr_b16 v[214:215], v8 offset:16904\n"
"ds_read_b64_tr_b16 v[216:217], v8 offset:24576\n"
"ds_read_b64_tr_b16 v[218:219], v8 offset:25088\n"
"ds_read_b64_tr_b16 v[220:221], v8 offset:24584\n"
"ds_read_b64_tr_b16 v[222:223], v8 offset:25096\n"
"s_waitcnt lgkmcnt(0)\n"
"s_waitcnt vmcnt(4)\n"
"s_barrier\n"
"ds_read_b64_tr_b16 v[224:225], v8 offset:32768\n"
"ds_read_b64_tr_b16 v[226:227], v8 offset:33280\n"
"ds_read_b64_tr_b16 v[228:229], v8 offset:32776\n"
"ds_read_b64_tr_b16 v[230:231], v8 offset:33288\n"
"ds_read_b64_tr_b16 v[232:233], v8 offset:40960\n"
"ds_read_b64_tr_b16 v[234:235], v8 offset:41472\n"
"ds_read_b64_tr_b16 v[236:237], v8 offset:40968\n"
"ds_read_b64_tr_b16 v[238:239], v8 offset:41480\n"
"s_waitcnt lgkmcnt(0)\n"
"s_waitcnt vmcnt(0)\n"
"s_barrier\n"
"ds_read_b64_tr_b16 v[240:241], v8 offset:49152\n"
"ds_read_b64_tr_b16 v[242:243], v8 offset:49664\n"
"ds_read_b64_tr_b16 v[244:245], v8 offset:49160\n"
"ds_read_b64_tr_b16 v[246:247], v8 offset:49672\n"
"ds_read_b64_tr_b16 v[248:249], v8 offset:57344\n"
"ds_read_b64_tr_b16 v[250:251], v8 offset:57856\n"
"ds_read_b64_tr_b16 v[252:253], v8 offset:57352\n"
"ds_read_b64_tr_b16 v[254:255], v8 offset:57864\n"
"s_waitcnt lgkmcnt(0)\n"
"s_barrier\n"
"v_mov_b32 v8, s54\n"
"v_mov_b32 v9, s55\n"
"s_lshl2_add_u32 s48, s29, s26\n"
"s_mul_i32 s63, s48, 14336\n"
"s_add_u32 s48, s29, 18\n"
"s_mul_i32 s49, s29, 72\n"
"s_add_u32 s49, s49, 128\n"
"v_lshl_add_u32 v0, v1, 2, s49\n"
"s_add_u32 s49, s50, 280\n"
"v_writelane_b32 v0, s49, 63\n"
"v_lshrrev_b32 v0, 2, v0\n"
"v_lshlrev_b32 v0, 7, v0\n"
"v_mov_b32 v3, 1187840\n"
"v_writelane_b32 v3, 0, 63\n"
"v_add_u32 v0, v3, v0\n"
"v_cmp_ge_u32 vcc, s48, v1\n"
"s_lshl_b32 s49, s29, 31\n"
"s_nop 4\n"
"s_or_b32 vcc_hi, vcc_hi, s49\n"
"s_nop 4\n"
"v_mov_b32 v3, 0x7fff0000\n"
"v_cndmask_b32 v0, v3, v0, vcc\n"
"v_cndmask_b32 v8, 0, v8, vcc\n"
"v_cndmask_b32 v9, 0, v9, vcc\n"
"v_mov_b32 v3, v21\n"
"s_mul_i32 s48, s28, 296960\n"
"s_add_u32 s63, s63, s48\n"
"s_add_u32 s63, s63, 4718592\n"
"s_add_u32 s66, s48, 3710976\n"
"s_mul_i32 s64, s26, 14336\n"
"s_lshl_b32 s74, s26, 11\n"
"s_add_u32 s74, s74, 114688\n"
"v_add_u32 v5, s74, v2\n"
"v_add_u32 v4, 57344, v2\n"
"s_add_u32 s44, s20, 0x0\n"
"s_addc_u32 s45, s21, 0x0\n"
"s_and_b32 s45, s45, 0xffff\n"
"s_mov_b32 s46, 0x400000\n"
"s_mov_b32 s47, 0x20000\n"
"s_mov_b32 s62, 16\n"
"s_cmp_lg_u32 s29, 0\n"
"s_cbranch_scc1 .L_dh\n"
"v_lshrrev_b32 v10, 4, v1\n"
"v_and_b32 v1, 15, v1\n"
"s_lshl_b32 s48, s50, 6\n"
"v_lshl_add_u32 v1, v1, 2, s48\n"
"v_lshl_add_u32 v1, v10, 12, v1\n"
"s_add_u32 s71, s70, 4681728\n"
"s_sub_u32 s68, s65, 2375680\n"
"s_lshl_b32 s65, s28, 20\n"
"s_mov_b32 s48, 20000\n"
"s_branch .L_rfdl\n"
".L_rldl:\n"
"s_sleep 8\n"
".L_rfdl:\n"
"buffer_load_dwordx2 v[6:7], v0, s[32:35], s66 offen sc1\n"
"s_sub_u32 s48, s48, 1\n"
"s_cbranch_scc1 .L_end\n"
"s_waitcnt vmcnt(0)\n"
"v_cmp_ne_u64 vcc, v[8:9], v[6:7]\n"
"s_cbranch_vccnz .L_rldl\n"
"s_add_u32 s66, s66, 1187840\n"
"s_add_u32 m0, s64, 0\n"
"s_nop 0\n"
"buffer_load_dwordx4 v2, s[32:35], s63 offen offset:0 sc1 lds\n"
"buffer_load_dwordx4 v2, s[32:35], s63 offen offset:1024 sc1 lds\n"
"buffer_load_dwordx4 v2, s[32:35], s63 offen offset:2048 sc1 lds\n"
"buffer_load_dwordx4 v2, s[32:35], s63 offen offset:3072 sc1 lds\n"
"s_add_u32 s63, s63, 4096\n"
"s_add_u32 m0, s64, 4096\n"
"s_nop 0\n"
"buffer_load_dwordx4 v2, s[32:35], s63 offen offset:0 sc1 lds\n"
"buffer_load_dwordx4 v2, s[32:35], s63 offen offset:1024 sc1 lds\n"
"buffer_load_dwordx4 v2, s[32:35], s63 offen offset:2048 sc1 lds\n"
"buffer_load_dwordx4 v2, s[32:35], s63 offen offset:3072 sc1 lds\n"
"s_add_u32 s63, s63, 4096\n"
"s_add_u32 m0, s64, 8192\n"
"s_nop 0\n"
"buffer_load_dwordx4 v2, s[32:35], s63 offen offset:0 sc1 lds\n"
"buffer_load_dwordx4 v2, s[32:35], s63 offen offset:1024 sc1 lds\n"
"buffer_load_dwordx4 v2, s[32:35], s63 offen offset:2048 sc1 lds\n"
"buffer_load_dwordx4 v2, s[32:35], s63 offen offset:3072 sc1 lds\n"
"s_add_u32 s63, s63, 4096\n"
"s_add_u32 m0, s64, 12288\n"
"s_nop 0\n"
"buffer_load_dwordx4 v2, s[32:35], s63 offen offset:0 sc1 lds\n"
"buffer_load_dwordx4 v2, s[32:35], s63 offen offset:1024 sc1 lds\n"
"s_add_u32 s63, s63, 1175552\n"
"buffer_load_dwordx4 v[12:15], v2, s[32:35], s71 offen sc1\n"
"s_add_u32 s71, s71, 1187840\n"
"buffer_load_dwordx2 v[6:7], v0, s[32:35], s66 offen sc1\n"
"s_waitcnt vmcnt(0)\n"
"s_barrier\n"
"v_lshlrev_b32 v16, 16, v12\n"
"v_and_b32 v17, 0xffff0000, v12\n"
"v_lshlrev_b32 v18, 16, v13\n"
"v_and_b32 v19, 0xffff0000, v13\n"
"v_lshlrev_b32 v20, 16, v14\n"
"v_and_b32 v21, 0xffff0000, v14\n"
"v_lshlrev_b32 v22, 16, v15\n"
"v_and_b32 v23, 0xffff0000, v15\n"
"ds_read_b128 v[32:35], v2 offset:0\n"
"ds_read_b128 v[36:39], v2 offset:14336\n"
"ds_read_b128 v[40:43], v2 offset:28672\n"
"ds_read_b128 v[44:47], v2 offset:43008\n"
"ds_read_b128 v[48:51], v2 offset:1024\n"
"ds_read_b128 v[52:55], v2 offset:15360\n"
"s_cmp_eq_u32 s26, 1\n"
"s_cbranch_scc1 .L_dlv1\n"
"s_cmp_eq_u32 s26, 2\n"
"s_cbranch_scc1 .L_dlv2\n"
"s_cmp_eq_u32 s26, 3\n"
"s_cbranch_scc1 .L_dlv3\n"
".p2align 6\n"
".L_dlv0:\n"
".L_dll_0:\n"
"s_waitcnt lgkmcnt(4)\n"
"v_mfma_f32_16x16x32_bf16 v[16:19], a[0:3], v[32:35], v[16:19]\n"
"ds_read_b128 v[56:59], v2 offset:29696\n"
"v_mfma_f32_16x16x32_bf16 v[16:19], a[112:115], v[36:39], v[16:19]\n"
"v_mfma_f32_16x16x32_bf16 v[20:23], a[116:119], v[36:39], v[20:23]\n"
"ds_read_b128 v[60:63], v2 offset:44032\n"
"v_mfma_f32_16x16x32_bf16 v[20:23], a[4:7], v[32:35], v[20:23]\n"
"s_add_u32 s68, s68, 1187840\n"
"s_waitcnt lgkmcnt(4)\n"
"v_mfma_f32_16x16x32_bf16 v[20:23], a[228:231], v[40:43], v[20:23]\n"
"ds_read_b128 v[32:35], v2 offset:2048\n"
"v_mfma_f32_16x16x32_bf16 v[20:23], v[148:151], v[44:47], v[20:23]\n"
"buffer_store_dwordx4 v[28:31], v2, s[32:35], s68 offen offset:1024 sc1\n"
"v_mfma_f32_16x16x32_bf16 v[16:19], v[144:147], v[44:47], v[16:19]\n"
"ds_read_b128 v[36:39], v2 offset:16384\n"
"v_mfma_f32_16x16x32_bf16 v[16:19], a[224:227], v[40:43], v[16:19]\n"
"s_waitcnt lgkmcnt(4)\n"
"v_mfma_f32_16x16x32_bf16 v[16:19], a[8:11], v[48:51], v[16:19]\n"
"ds_read_b128 v[40:43], v2 offset:30720\n"
"v_mfma_f32_16x16x32_bf16 v[16:19], a[120:123], v[52:55], v[16:19]\n"
"buffer_store_dwordx4 v[24:27], v2, s[32:35], s68 offen sc1\n"
"s_mov_b32 s67, s68\n"
"v_mfma_f32_16x16x32_bf16 v[20:23], a[124:127], v[52:55], v[20:23]\n"
"ds_read_b128 v[44:47], v2 offset:45056\n"
"v_mfma_f32_16x16x32_bf16 v[20:23], a[12:15], v[48:51], v[20:23]\n"
"s_waitcnt lgkmcnt(4)\n"
"v_mfma_f32_16x16x32_bf16 v[20:23], a[236:239], v[56:59], v[20:23]\n"
"ds_read_b128 v[48:51], v2 offset:3072\n"
"v_mfma_f32_16x16x32_bf16 v[20:23], v[156:159], v[60:63], v[20:23]\n"
"v_mfma_f32_16x16x32_bf16 v[16:19], v[152:155], v[60:63], v[16:19]\n"
"ds_read_b128 v[52:55], v2 offset:17408\n"
"v_mfma_f32_16x16x32_bf16 v[16:19], a[232:235], v[56:59], v[16:19]\n"
"s_waitcnt lgkmcnt(4)\n"
"v_mfma_f32_16x16x32_bf16 v[16:19], a[16:19], v[32:35], v[16:19]\n"
"ds_read_b128 v[56:59], v2 offset:31744\n"
"v_mfma_f32_16x16x32_bf16 v[16:19], a[128:131], v[36:39], v[16:19]\n"
"v_mfma_f32_16x16x32_bf16 v[20:23], a[132:135], v[36:39], v[20:23]\n"
"ds_read_b128 v[60:63], v2 offset:46080\n"
"v_mfma_f32_16x16x32_bf16 v[20:23], a[20:23], v[32:35], v[20:23]\n"
"s_waitcnt lgkmcnt(4)\n"
"s_waitcnt vmcnt(3)\n"
"s_barrier\n"
"v_mfma_f32_16x16x32_bf16 v[20:23], a[244:247], v[40:43], v[20:23]\n"
"ds_read_b128 v[32:35], v2 offset:4096\n"
"s_add_u32 m0, s64, 57344\n"
"v_mfma_f32_16x16x32_bf16 v[20:23], v[164:167], v[44:47], v[20:23]\n"
"buffer_load_dwordx4 v2, s[32:35], s63 offen offset:0 sc1 lds\n"
"v_mfma_f32_16x16x32_bf16 v[16:19], v[160:163], v[44:47], v[16:19]\n"
"ds_read_b128 v[36:39], v2 offset:18432\n"
"v_mfma_f32_16x16x32_bf16 v[16:19], a[240:243], v[40:43], v[16:19]\n"
"s_waitcnt lgkmcnt(4)\n"
"v_mfma_f32_16x16x32_bf16 v[16:19], a[24:27], v[48:51], v[16:19]\n"
"ds_read_b128 v[40:43], v2 offset:32768\n"
"v_mfma_f32_16x16x32_bf16 v[16:19], a[136:139], v[52:55], v[16:19]\n"
"v_mfma_f32_16x16x32_bf16 v[20:23], a[140:143], v[52:55], v[20:23]\n"
"ds_read_b128 v[44:47], v2 offset:47104\n"
"v_mfma_f32_16x16x32_bf16 v[20:23], a[28:31], v[48:51], v[20:23]\n"
"s_waitcnt lgkmcnt(4)\n"
"v_mfma_f32_16x16x32_bf16 v[20:23], a[252:255], v[56:59], v[20:23]\n"
"ds_read_b128 v[48:51], v2 offset:5120\n"
"v_mfma_f32_16x16x32_bf16 v[20:23], v[172:175], v[60:63], v[20:23]\n"
"buffer_load_dwordx4 v2, s[32:35], s63 offen offset:1024 sc1 lds\n"
"v_mfma_f32_16x16x32_bf16 v[16:19], v[168:171], v[60:63], v[16:19]\n"
"ds_read_b128 v[52:55], v2 offset:19456\n"
"v_mfma_f32_16x16x32_bf16 v[16:19], a[248:251], v[56:59], v[16:19]\n"
"s_waitcnt lgkmcnt(4)\n"
"v_mfma_f32_16x16x32_bf16 v[16:19], a[32:35], v[32:35], v[16:19]\n"
"ds_read_b128 v[56:59], v2 offset:33792\n"
"v_mfma_f32_16x16x32_bf16 v[16:19], a[144:147], v[36:39], v[16:19]\n"
"buffer_load_dwordx4 v2, s[32:35], s63 offen offset:2048 sc1 lds\n"
"v_mfma_f32_16x16x32_bf16 v[20:23], a[148:151], v[36:39], v[20:23]\n"
"ds_read_b128 v[60:63], v2 offset:48128\n"
"v_mfma_f32_16x16x32_bf16 v[20:23], a[36:39], v[32:35], v[20:23]\n"
"s_waitcnt lgkmcnt(4)\n"
"v_mfma_f32_16x16x32_bf16 v[20:23], v[68:71], v[40:43], v[20:23]\n"
"ds_read_b128 v[32:35], v2 offset:6144\n"
"v_mfma_f32_16x16x32_bf16 v[20:23], v[180:183], v[44:47], v[20:23]\n"
"buffer_load_dwordx4 v2, s[32:35], s63 offen offset:3072 sc1 lds\n"
"v_mfma_f32_16x16x32_bf16 v[16:19], v[176:179], v[44:47], v[16:19]\n"
"ds_read_b128 v[36:39], v2 offset:20480\n"
"v_mfma_f32_16x16x32_bf16 v[16:19], v[64:67], v[40:43], v[16:19]\n"
"s_waitcnt lgkmcnt(4)\n"
"v_mfma_f32_16x16x32_bf16 v[16:19], a[40:43], v[48:51], v[16:19]\n"
"ds_read_b128 v[40:43], v2 offset:34816\n"
"v_mfma_f32_16x16x32_bf16 v[16:19], a[152:155], v[52:55], v[16:19]\n"
"buffer_load_dwordx4 v[12:15], v2, s[32:35], s71 offen sc1\n"
"s_add_u32 s71, s71, 1187840\n"
"v_mfma_f32_16x16x32_bf16 v[20:23], a[156:159], v[52:55], v[20:23]\n"
"ds_read_b128 v[44:47], v2 offset:49152\n"
"s_add_u32 s63, s63, 4096\n"
"v_mfma_f32_16x16x32_bf16 v[20:23], a[44:47], v[48:51], v[20:23]\n"
"s_add_u32 m0, s64, 61440\n"
"s_waitcnt lgkmcnt(4)\n"
"v_mfma_f32_16x16x32_bf16 v[20:23], v[76:79], v[56:59], v[20:23]\n"
"ds_read_b128 v[48:51], v2 offset:7168\n"
"v_mfma_f32_16x16x32_bf16 v[20:23], v[188:191], v[60:63], v[20:23]\n"
"buffer_load_dwordx4 v2, s[32:35], s63 offen offset:0 sc1 lds\n"
"v_mfma_f32_16x16x32_bf16 v[16:19], v[184:187], v[60:63], v[16:19]\n"
"ds_read_b128 v[52:55], v2 offset:21504\n"
"v_mfma_f32_16x16x32_bf16 v[16:19], v[72:75], v[56:59], v[16:19]\n"
"s_waitcnt lgkmcnt(4)\n"
"v_mfma_f32_16x16x32_bf16 v[16:19], a[48:51], v[32:35], v[16:19]\n"
"ds_read_b128 v[56:59], v2 offset:35840\n"
"v_mfma_f32_16x16x32_bf16 v[16:19], a[160:163], v[36:39], v[16:19]\n"
"buffer_load_dwordx4 v2, s[32:35], s63 offen offset:1024 sc1 lds\n"
"v_mfma_f32_16x16x32_bf16 v[20:23], a[164:167], v[36:39], v[20:23]\n"
"ds_read_b128 v[60:63], v2 offset:50176\n"
"v_mfma_f32_16x16x32_bf16 v[20:23], a[52:55], v[32:35], v[20:23]\n"
"s_waitcnt lgkmcnt(4)\n"
"v_mfma_f32_16x16x32_bf16 v[20:23], v[84:87], v[40:43], v[20:23]\n"
"ds_read_b128 v[32:35], v2 offset:8192\n"
"v_mfma_f32_16x16x32_bf16 v[20:23], v[196:199], v[44:47], v[20:23]\n"
"buffer_load_dwordx4 v2, s[32:35], s63 offen offset:2048 sc1 lds\n"
"v_mfma_f32_16x16x32_bf16 v[16:19], v[192:195], v[44:47], v[16:19]\n"
"ds_read_b128 v[36:39], v2 offset:22528\n"
"v_mfma_f32_16x16x32_bf16 v[16:19], v[80:83], v[40:43], v[16:19]\n"
"s_waitcnt lgkmcnt(4)\n"
"v_mfma_f32_16x16x32_bf16 v[16:19], a[56:59], v[48:51], v[16:19]\n"
"ds_read_b128 v[40:43], v2 offset:36864\n"
"v_mfma_f32_16x16x32_bf16 v[16:19], a[168:171], v[52:55], v[16:19]\n"
"buffer_load_dwordx4 v2, s[32:35], s63 offen offset:3072 sc1 lds\n"
"v_mfma_f32_16x16x32_bf16 v[20:23], a[172:175], v[52:55], v[20:23]\n"
"ds_read_b128 v[44:47], v2 offset:51200\n"
"v_mfma_f32_16x16x32_bf16 v[20:23], a[60:63], v[48:51], v[20:23]\n"
"s_waitcnt lgkmcnt(4)\n"
"v_mfma_f32_16x16x32_bf16 v[20:23], v[92:95], v[56:59], v[20:23]\n"
"ds_read_b128 v[48:51], v2 offset:9216\n"
"v_mfma_f32_16x16x32_bf16 v[20:23], v[204:207], v[60:63], v[20:23]\n"
"s_add_u32 s63, s63, 4096\n"
"s_add_u32 m0, s64, 65536\n"
"v_mfma_f32_16x16x32_bf16 v[16:19], v[200:203], v[60:63], v[16:19]\n"
"ds_read_b128 v[52:55], v2 offset:23552\n"
"v_mfma_f32_16x16x32_bf16 v[16:19], v[88:91], v[56:59], v[16:19]\n"
"s_waitcnt lgkmcnt(4)\n"
"v_mfma_f32_16x16x32_bf16 v[16:19], a[64:67], v[32:35], v[16:19]\n"
"ds_read_b128 v[56:59], v2 offset:37888\n"
"v_mfma_f32_16x16x32_bf16 v[16:19], a[176:179], v[36:39], v[16:19]\n"
"buffer_load_dwordx4 v2, s[32:35], s63 offen offset:0 sc1 lds\n"
"v_mfma_f32_16x16x32_bf16 v[20:23], a[180:183], v[36:39], v[20:23]\n"
"ds_read_b128 v[60:63], v2 offset:52224\n"
"v_mfma_f32_16x16x32_bf16 v[20:23], a[68:71], v[32:35], v[20:23]\n"
"s_waitcnt lgkmcnt(4)\n"
"v_mfma_f32_16x16x32_bf16 v[20:23], v[100:103], v[40:43], v[20:23]\n"
"ds_read_b128 v[32:35], v2 offset:10240\n"
"v_mfma_f32_16x16x32_bf16 v[20:23], v[212:215], v[44:47], v[20:23]\n"
"buffer_load_dwordx4 v2, s[32:35], s63 offen offset:1024 sc1 lds\n"
"v_mfma_f32_16x16x32_bf16 v[16:19], v[208:211], v[44:47], v[16:19]\n"
"ds_read_b128 v[36:39], v2 offset:24576\n"
"v_mfma_f32_16x16x32_bf16 v[16:19], v[96:99], v[40:43], v[16:19]\n"
"s_waitcnt lgkmcnt(4)\n"
"v_mfma_f32_16x16x32_bf16 v[16:19], a[72:75], v[48:51], v[16:19]\n"
"ds_read_b128 v[40:43], v2 offset:38912\n"
"v_mfma_f32_16x16x32_bf16 v[16:19], a[184:187], v[52:55], v[16:19]\n"
"buffer_load_dwordx4 v2, s[32:35], s63 offen offset:2048 sc1 lds\n"
"v_mfma_f32_16x16x32_bf16 v[20:23], a[188:191], v[52:55], v[20:23]\n"
"ds_read_b128 v[44:47], v2 offset:53248\n"
"v_mfma_f32_16x16x32_bf16 v[20:23], a[76:79], v[48:51], v[20:23]\n"
"s_waitcnt lgkmcnt(4)\n"
"v_mfma_f32_16x16x32_bf16 v[20:23], v[108:111], v[56:59], v[20:23]\n"
"ds_read_b128 v[48:51], v2 offset:11264\n"
"v_mfma_f32_16x16x32_bf16 v[20:23], v[220:223], v[60:63], v[20:23]\n"
"v_mfma_f32_16x16x32_bf16 v[16:19], v[216:219], v[60:63], v[16:19]\n"
"ds_read_b128 v[52:55], v2 offset:25600\n"
"v_mfma_f32_16x16x32_bf16 v[16:19], v[104:107], v[56:59], v[16:19]\n"
"s_waitcnt lgkmcnt(4)\n"
"v_mfma_f32_16x16x32_bf16 v[16:19], a[80:83], v[32:35], v[16:19]\n"
"ds_read_b128 v[56:59], v2 offset:39936\n"
"v_mfma_f32_16x16x32_bf16 v[16:19], a[192:195], v[36:39], v[16:19]\n"
"buffer_load_dwordx4 v2, s[32:35], s63 offen offset:3072 sc1 lds\n"
"s_add_u32 s63, s63, 4096\n"
"v_mfma_f32_16x16x32_bf16 v[20:23], a[196:199], v[36:39], v[20:23]\n"
"ds_read_b128 v[60:63], v2 offset:54272\n"
"s_add_u32 m0, s64, 69632\n"
"v_mfma_f32_16x16x32_bf16 v[20:23], a[84:87], v[32:35], v[20:23]\n"
"s_waitcnt lgkmcnt(4)\n"
"v_mfma_f32_16x16x32_bf16 v[20:23], v[116:119], v[40:43], v[20:23]\n"
"ds_read_b128 v[32:35], v2 offset:12288\n"
"v_mfma_f32_16x16x32_bf16 v[20:23], v[228:231], v[44:47], v[20:23]\n"
"buffer_load_dwordx4 v2, s[32:35], s63 offen offset:0 sc1 lds\n"
"v_mfma_f32_16x16x32_bf16 v[16:19], v[224:227], v[44:47], v[16:19]\n"
"ds_read_b128 v[36:39], v2 offset:26624\n"
"v_mfma_f32_16x16x32_bf16 v[16:19], v[112:115], v[40:43], v[16:19]\n"
"s_waitcnt lgkmcnt(4)\n"
"v_mfma_f32_16x16x32_bf16 v[16:19], a[88:91], v[48:51], v[16:19]\n"
"ds_read_b128 v[40:43], v2 offset:40960\n"
"v_mfma_f32_16x16x32_bf16 v[16:19], a[200:203], v[52:55], v[16:19]\n"
"buffer_load_dwordx4 v2, s[32:35], s63 offen offset:1024 sc1 lds\n"
"s_add_u32 s63, s63, 1175552\n"
"v_mfma_f32_16x16x32_bf16 v[20:23], a[204:207], v[52:55], v[20:23]\n"
"ds_read_b128 v[44:47], v2 offset:55296\n"
"v_mfma_f32_16x16x32_bf16 v[20:23], a[92:95], v[48:51], v[20:23]\n"
"s_waitcnt lgkmcnt(4)\n"
"v_mfma_f32_16x16x32_bf16 v[20:23], v[124:127], v[56:59], v[20:23]\n"
"ds_read_b128 v[48:51], v2 offset:13312\n"
"s_waitcnt vmcnt(10)\n"
"v_mfma_f32_16x16x32_bf16 v[20:23], v[236:239], v[60:63], v[20:23]\n"
"v_lshlrev_b32 v24, 16, v12\n"
"v_and_b32 v25, 0xffff0000, v12\n"
"v_mfma_f32_16x16x32_bf16 v[16:19], v[232:235], v[60:63], v[16:19]\n"
"ds_read_b128 v[52:55], v2 offset:27648\n"
"v_lshlrev_b32 v26, 16, v13\n"
"v_mfma_f32_16x16x32_bf16 v[16:19], v[120:123], v[56:59], v[16:19]\n"
"v_and_b32 v27, 0xffff0000, v13\n"
"s_waitcnt lgkmcnt(4)\n"
"v_mfma_f32_16x16x32_bf16 v[16:19], a[96:99], v[32:35], v[16:19]\n"
"ds_read_b128 v[56:59], v2 offset:41984\n"
"v_lshlrev_b32 v28, 16, v14\n"
"v_mfma_f32_16x16x32_bf16 v[16:19], a[208:211], v[36:39], v[16:19]\n"
"v_and_b32 v29, 0xffff0000, v14\n"
"v_lshlrev_b32 v30, 16, v15\n"
"v_mfma_f32_16x16x32_bf16 v[20:23], a[212:215], v[36:39], v[20:23]\n"
"ds_read_b128 v[60:63], v2 offset:56320\n"
"v_and_b32 v31, 0xffff0000, v15\n"
"v_mfma_f32_16x16x32_bf16 v[20:23], a[100:103], v[32:35], v[20:23]\n"
"s_waitcnt lgkmcnt(4)\n"
"s_barrier\n"
"v_mfma_f32_16x16x32_bf16 v[20:23], v[132:135], v[40:43], v[20:23]\n"
"ds_read_b128 v[32:35], v4 offset:0\n"
"v_mfma_f32_16x16x32_bf16 v[20:23], v[244:247], v[44:47], v[20:23]\n"
"v_mfma_f32_16x16x32_bf16 v[16:19], v[240:243], v[44:47], v[16:19]\n"
"ds_read_b128 v[36:39], v4 offset:14336\n"
"v_mfma_f32_16x16x32_bf16 v[16:19], v[128:131], v[40:43], v[16:19]\n"
"s_waitcnt lgkmcnt(4)\n"
"v_mfma_f32_16x16x32_bf16 v[16:19], a[104:107], v[48:51], v[16:19]\n"
"ds_read_b128 v[40:43], v4 offset:28672\n"
"v_mfma_f32_16x16x32_bf16 v[16:19], a[216:219], v[52:55], v[16:19]\n"
"buffer_store_dwordx2 v[8:9], v2, s[92:95], s67 idxen sc1\n"
"v_mfma_f32_16x16x32_bf16 v[20:23], a[220:223], v[52:55], v[20:23]\n"
"ds_read_b128 v[44:47], v4 offset:43008\n"
"v_mfma_f32_16x16x32_bf16 v[20:23], a[108:111], v[48:51], v[20:23]\n"
"s_waitcnt lgkmcnt(4)\n"
"v_mfma_f32_16x16x32_bf16 v[20:23], v[140:143], v[56:59], v[20:23]\n"
"ds_read_b128 v[48:51], v4 offset:1024\n"
"v_mfma_f32_16x16x32_bf16 v[20:23], v[252:255], v[60:63], v[20:23]\n"
"v_mfma_f32_16x16x32_bf16 v[16:19], v[248:251], v[60:63], v[16:19]\n"
"ds_read_b128 v[52:55], v4 offset:15360\n"
"s_sub_u32 s62, s62, 1\n"
"v_mfma_f32_16x16x32_bf16 v[16:19], v[136:139], v[56:59], v[16:19]\n"
"s_cbranch_scc1 .L_dlb_0\n"
"s_waitcnt lgkmcnt(4)\n"
"v_mfma_f32_16x16x32_bf16 v[24:27], a[0:3], v[32:35], v[24:27]\n"
"ds_read_b128 v[56:59], v4 offset:29696\n"
"v_mfma_f32_16x16x32_bf16 v[24:27], a[112:115], v[36:39], v[24:27]\n"
"v_mfma_f32_16x16x32_bf16 v[28:31], a[116:119], v[36:39], v[28:31]\n"
"ds_read_b128 v[60:63], v4 offset:44032\n"
"v_mfma_f32_16x16x32_bf16 v[28:31], a[4:7], v[32:35], v[28:31]\n"
"s_add_u32 s68, s68, 1187840\n"
"s_waitcnt lgkmcnt(4)\n"
"v_mfma_f32_16x16x32_bf16 v[28:31], a[228:231], v[40:43], v[28:31]\n"
"ds_read_b128 v[32:35], v4 offset:2048\n"
"v_mfma_f32_16x16x32_bf16 v[28:31], v[148:151], v[44:47], v[28:31]\n"
"buffer_store_dwordx4 v[20:23], v2, s[32:35], s68 offen offset:1024 sc1\n"
"v_mfma_f32_16x16x32_bf16 v[24:27], v[144:147], v[44:47], v[24:27]\n"
"ds_read_b128 v[36:39], v4 offset:16384\n"
"v_mfma_f32_16x16x32_bf16 v[24:27], a[224:227], v[40:43], v[24:27]\n"
"s_waitcnt lgkmcnt(4)\n"
"v_mfma_f32_16x16x32_bf16 v[24:27], a[8:11], v[48:51], v[24:27]\n"
"ds_read_b128 v[40:43], v4 offset:30720\n"
"v_mfma_f32_16x16x32_bf16 v[24:27], a[120:123], v[52:55], v[24:27]\n"
"buffer_store_dwordx4 v[16:19], v2, s[32:35], s68 offen sc1\n"
"s_mov_b32 s67, s68\n"
"v_mfma_f32_16x16x32_bf16 v[28:31], a[124:127], v[52:55], v[28:31]\n"
"ds_read_b128 v[44:47], v4 offset:45056\n"
"v_mfma_f32_16x16x32_bf16 v[28:31], a[12:15], v[48:51], v[28:31]\n"
"s_waitcnt lgkmcnt(4)\n"
"v_mfma_f32_16x16x32_bf16 v[28:31], a[236:239], v[56:59], v[28:31]\n"
"ds_read_b128 v[48:51], v4 offset:3072\n"
"v_mfma_f32_16x16x32_bf16 v[28:31], v[156:159], v[60:63], v[28:31]\n"
"v_mfma_f32_16x16x32_bf16 v[24:27], v[152:155], v[60:63], v[24:27]\n"
"ds_read_b128 v[52:55], v4 offset:17408\n"
"v_mfma_f32_16x16x32_bf16 v[24:27], a[232:235], v[56:59], v[24:27]\n"
"s_waitcnt lgkmcnt(4)\n"
"v_mfma_f32_16x16x32_bf16 v[24:27], a[16:19], v[32:35], v[24:27]\n"
"ds_read_b128 v[56:59], v4 offset:31744\n"
"v_mfma_f32_16x16x32_bf16 v[24:27], a[128:131], v[36:39], v[24:27]\n"
"v_mfma_f32_16x16x32_bf16 v[28:31], a[132:135], v[36:39], v[28:31]\n"
"ds_read_b128 v[60:63], v4 offset:46080\n"
"v_mfma_f32_16x16x32_bf16 v[28:31], a[20:23], v[32:35], v[28:31]\n"
"s_waitcnt lgkmcnt(4)\n"
"s_waitcnt vmcnt(3)\n"
"s_barrier\n"
"v_mfma_f32_16x16x32_bf16 v[28:31], a[244:247], v[40:43], v[28:31]\n"
"ds_read_b128 v[32:35], v4 offset:4096\n"
"s_add_u32 m0, s64, 0\n"
"v_mfma_f32_16x16x32_bf16 v[28:31], v[164:167], v[44:47], v[28:31]\n"
"buffer_load_dwordx4 v2, s[32:35], s63 offen offset:0 sc1 lds\n"
"v_mfma_f32_16x16x32_bf16 v[24:27], v[160:163], v[44:47], v[24:27]\n"
"ds_read_b128 v[36:39], v4 offset:18432\n"
"v_mfma_f32_16x16x32_bf16 v[24:27], a[240:243], v[40:43], v[24:27]\n"
"s_waitcnt lgkmcnt(4)\n"
"v_mfma_f32_16x16x32_bf16 v[24:27], a[24:27], v[48:51], v[24:27]\n"
"ds_read_b128 v[40:43], v4 offset:32768\n"
"v_mfma_f32_16x16x32_bf16 v[24:27], a[136:139], v[52:55], v[24:27]\n"
"v_mfma_f32_16x16x32_bf16 v[28:31], a[140:143], v[52:55], v[28:31]\n"
"ds_read_b128 v[44:47], v4 offset:47104\n"
"v_mfma_f32_16x16x32_bf16 v[28:31], a[28:31], v[48:51], v[28:31]\n"
"s_waitcnt lgkmcnt(4)\n"
"v_mfma_f32_16x16x32_bf16 v[28:31], a[252:255], v[56:59], v[28:31]\n"
"ds_read_b128 v[48:51], v4 offset:5120\n"
"v_mfma_f32_16x16x32_bf16 v[28:31], v[172:175], v[60:63], v[28:31]\n"
"buffer_load_dwordx4 v2, s[32:35], s63 offen offset:1024 sc1 lds\n"
"v_mfma_f32_16x16x32_bf16 v[24:27], v[168:171], v[60:63], v[24:27]\n"
"ds_read_b128 v[52:55], v4 offset:19456\n"
"v_mfma_f32_16x16x32_bf16 v[24:27], a[248:251], v[56:59], v[24:27]\n"
"s_waitcnt lgkmcnt(4)\n"
"v_mfma_f32_16x16x32_bf16 v[24:27], a[32:35], v[32:35], v[24:27]\n"
"ds_read_b128 v[56:59], v4 offset:33792\n"
"v_mfma_f32_16x16x32_bf16 v[24:27], a[144:147], v[36:39], v[24:27]\n"
"buffer_load_dwordx4 v2, s[32:35], s63 offen offset:2048 sc1 lds\n"
"v_mfma_f32_16x16x32_bf16 v[28:31], a[148:151], v[36:39], v[28:31]\n"
"ds_read_b128 v[60:63], v4 offset:48128\n"
"v_mfma_f32_16x16x32_bf16 v[28:31], a[36:39], v[32:35], v[28:31]\n"
"s_waitcnt lgkmcnt(4)\n"
"v_mfma_f32_16x16x32_bf16 v[28:31], v[68:71], v[40:43], v[28:31]\n"
"ds_read_b128 v[32:35], v4 offset:6144\n"
"v_mfma_f32_16x16x32_bf16 v[28:31], v[180:183], v[44:47], v[28:31]\n"
"buffer_load_dwordx4 v2, s[32:35], s63 offen offset:3072 sc1 lds\n"
"v_mfma_f32_16x16x32_bf16 v[24:27], v[176:179], v[44:47], v[24:27]\n"
"ds_read_b128 v[36:39], v4 offset:20480\n"
"v_mfma_f32_16x16x32_bf16 v[24:27], v[64:67], v[40:43], v[24:27]\n"
"s_waitcnt lgkmcnt(4)\n"
"v_mfma_f32_16x16x32_bf16 v[24:27], a[40:43], v[48:51], v[24:27]\n"
"ds_read_b128 v[40:43], v4 offset:34816\n"
"v_mfma_f32_16x16x32_bf16 v[24:27], a[152:155], v[52:55], v[24:27]\n"
"buffer_load_dwordx4 v[12:15], v2, s[32:35], s71 offen sc1\n"
"s_add_u32 s71, s71, 1187840\n"
"v_mfma_f32_16x16x32_bf16 v[28:31], a[156:159], v[52:55], v[28:31]\n"
"ds_read_b128 v[44:47], v4 offset:49152\n"
"s_add_u32 s63, s63, 4096\n"
"v_mfma_f32_16x16x32_bf16 v[28:31], a[44:47], v[48:51], v[28:31]\n"
"s_add_u32 m0, s64, 4096\n"
"s_waitcnt lgkmcnt(4)\n"
"v_mfma_f32_16x16x32_bf16 v[28:31], v[76:79], v[56:59], v[28:31]\n"
"ds_read_b128 v[48:51], v4 offset:7168\n"
"v_mfma_f32_16x16x32_bf16 v[28:31], v[188:191], v[60:63], v[28:31]\n"
"buffer_load_dwordx4 v2, s[32:35], s63 offen offset:0 sc1 lds\n"
"v_mfma_f32_16x16x32_bf16 v[24:27], v[184:187], v[60:63], v[24:27]\n"
"ds_read_b128 v[52:55], v4 offset:21504\n"
"v_mfma_f32_16x16x32_bf16 v[24:27], v[72:75], v[56:59], v[24:27]\n"
"s_waitcnt lgkmcnt(4)\n"
"v_mfma_f32_16x16x32_bf16 v[24:27], a[48:51], v[32:35], v[24:27]\n"
"ds_read_b128 v[56:59], v4 offset:35840\n"
"v_mfma_f32_16x16x32_bf16 v[24:27], a[160:163], v[36:39], v[24:27]\n"
"buffer_load_dwordx4 v2, s[32:35], s63 offen offset:1024 sc1 lds\n"
"v_mfma_f32_16x16x32_bf16 v[28:31], a[164:167], v[36:39], v[28:31]\n"
"ds_read_b128 v[60:63], v4 offset:50176\n"
"v_mfma_f32_16x16x32_bf16 v[28:31], a[52:55], v[32:35], v[28:31]\n"
"s_waitcnt lgkmcnt(4)\n"
"v_mfma_f32_16x16x32_bf16 v[28:31], v[84:87], v[40:43], v[28:31]\n"
"ds_read_b128 v[32:35], v4 offset:8192\n"
"v_mfma_f32_16x16x32_bf16 v[28:31], v[196:199], v[44:47], v[28:31]\n"
"buffer_load_dwordx4 v2, s[32:35], s63 offen offset:2048 sc1 lds\n"
"v_mfma_f32_16x16x32_bf16 v[24:27], v[192:195], v[44:47], v[24:27]\n"
"ds_read_b128 v[36:39], v4 offset:22528\n"
"v_mfma_f32_16x16x32_bf16 v[24:27], v[80:83], v[40:43], v[24:27]\n"
"s_waitcnt lgkmcnt(4)\n"
"v_mfma_f32_16x16x32_bf16 v[24:27], a[56:59], v[48:51], v[24:27]\n"
"ds_read_b128 v[40:43], v4 offset:36864\n"
"v_mfma_f32_16x16x32_bf16 v[24:27], a[168:171], v[52:55], v[24:27]\n"
"buffer_load_dwordx4 v2, s[32:35], s63 offen offset:3072 sc1 lds\n"
"v_mfma_f32_16x16x32_bf16 v[28:31], a[172:175], v[52:55], v[28:31]\n"
"ds_read_b128 v[44:47], v4 offset:51200\n"
"v_mfma_f32_16x16x32_bf16 v[28:31], a[60:63], v[48:51], v[28:31]\n"
"s_waitcnt lgkmcnt(4)\n"
"v_mfma_f32_16x16x32_bf16 v[28:31], v[92:95], v[56:59], v[28:31]\n"
"ds_read_b128 v[48:51], v4 offset:9216\n"
"v_mfma_f32_16x16x32_bf16 v[28:31], v[204:207], v[60:63], v[28:31]\n"
"s_add_u32 s63, s63, 4096\n"
"s_add_u32 m0, s64, 8192\n"
"v_mfma_f32_16x16x32_bf16 v[24:27], v[200:203], v[60:63], v[24:27]\n"
"ds_read_b128 v[52:55], v4 offset:23552\n"
"v_mfma_f32_16x16x32_bf16 v[24:27], v[88:91], v[56:59], v[24:27]\n"
"s_waitcnt lgkmcnt(4)\n"
"v_mfma_f32_16x16x32_bf16 v[24:27], a[64:67], v[32:35], v[24:27]\n"
"ds_read_b128 v[56:59], v4 offset:37888\n"
"v_mfma_f32_16x16x32_bf16 v[24:27], a[176:179], v[36:39], v[24:27]\n"
"buffer_load_dwordx4 v2, s[32:35], s63 offen offset:0 sc1 lds\n"
"v_mfma_f32_16x16x32_bf16 v[28:31], a[180:183], v[36:39], v[28:31]\n"
"ds_read_b128 v[60:63], v4 offset:52224\n"
"v_mfma_f32_16x16x32_bf16 v[28:31], a[68:71], v[32:35], v[28:31]\n"
"s_waitcnt lgkmcnt(4)\n"
"v_mfma_f32_16x16x32_bf16 v[28:31], v[100:103], v[40:43], v[28:31]\n"
"ds_read_b128 v[32:35], v4 offset:10240\n"
"v_mfma_f32_16x16x32_bf16 v[28:31], v[212:215], v[44:47], v[28:31]\n"
"buffer_load_dwordx4 v2, s[32:35], s63 offen offset:1024 sc1 lds\n"
"v_mfma_f32_16x16x32_bf16 v[24:27], v[208:211], v[44:47], v[24:27]\n"
"ds_read_b128 v[36:39], v4 offset:24576\n"
"v_mfma_f32_16x16x32_bf16 v[24:27], v[96:99], v[40:43], v[24:27]\n"
"s_waitcnt lgkmcnt(4)\n"
"v_mfma_f32_16x16x32_bf16 v[24:27], a[72:75], v[48:51], v[24:27]\n"
"ds_read_b128 v[40:43], v4 offset:38912\n"
"v_mfma_f32_16x16x32_bf16 v[24:27], a[184:187], v[52:55], v[24:27]\n"
"buffer_load_dwordx4 v2, s[32:35], s63 offen offset:2048 sc1 lds\n"
"v_mfma_f32_16x16x32_bf16 v[28:31], a[188:191], v[52:55], v[28:31]\n"
"ds_read_b128 v[44:47], v4 offset:53248\n"
"v_mfma_f32_16x16x32_bf16 v[28:31], a[76:79], v[48:51], v[28:31]\n"
"s_waitcnt lgkmcnt(4)\n"
"v_mfma_f32_16x16x32_bf16 v[28:31], v[108:111], v[56:59], v[28:31]\n"
"ds_read_b128 v[48:51], v4 offset:11264\n"
"v_mfma_f32_16x16x32_bf16 v[28:31], v[220:223], v[60:63], v[28:31]\n"
"v_mfma_f32_16x16x32_bf16 v[24:27], v[216:219], v[60:63], v[24:27]\n"
"ds_read_b128 v[52:55], v4 offset:25600\n"
"v_mfma_f32_16x16x32_bf16 v[24:27], v[104:107], v[56:59], v[24:27]\n"
"s_waitcnt lgkmcnt(4)\n"
"v_mfma_f32_16x16x32_bf16 v[24:27], a[80:83], v[32:35], v[24:27]\n"
"ds_read_b128 v[56:59], v4 offset:39936\n"
"v_mfma_f32_16x16x32_bf16 v[24:27], a[192:195], v[36:39], v[24:27]\n"
"buffer_load_dwordx4 v2, s[32:35], s63 offen offset:3072 sc1 lds\n"
"s_add_u32 s63, s63, 4096\n"
"v_mfma_f32_16x16x32_bf16 v[28:31], a[196:199], v[36:39], v[28:31]\n"
"ds_read_b128 v[60:63], v4 offset:54272\n"
"s_add_u32 m0, s64, 12288\n"
"v_mfma_f32_16x16x32_bf16 v[28:31], a[84:87], v[32:35], v[28:31]\n"
"s_waitcnt lgkmcnt(4)\n"
"v_mfma_f32_16x16x32_bf16 v[28:31], v[116:119], v[40:43], v[28:31]\n"
"ds_read_b128 v[32:35], v4 offset:12288\n"
"v_mfma_f32_16x16x32_bf16 v[28:31], v[228:231], v[44:47], v[28:31]\n"
"buffer_load_dwordx4 v2, s[32:35], s63 offen offset:0 sc1 lds\n"
"v_mfma_f32_16x16x32_bf16 v[24:27], v[224:227], v[44:47], v[24:27]\n"
"ds_read_b128 v[36:39], v4 offset:26624\n"
"v_mfma_f32_16x16x32_bf16 v[24:27], v[112:115], v[40:43], v[24:27]\n"
"s_waitcnt lgkmcnt(4)\n"
"v_mfma_f32_16x16x32_bf16 v[24:27], a[88:91], v[48:51], v[24:27]\n"
"ds_read_b128 v[40:43], v4 offset:40960\n"
"v_mfma_f32_16x16x32_bf16 v[24:27], a[200:203], v[52:55], v[24:27]\n"
"buffer_load_dwordx4 v2, s[32:35], s63 offen offset:1024 sc1 lds\n"
"s_add_u32 s63, s63, 1175552\n"
"v_mfma_f32_16x16x32_bf16 v[28:31], a[204:207], v[52:55], v[28:31]\n"
"ds_read_b128 v[44:47], v4 offset:55296\n"
"v_mfma_f32_16x16x32_bf16 v[28:31], a[92:95], v[48:51], v[28:31]\n"
"s_waitcnt lgkmcnt(4)\n"
"v_mfma_f32_16x16x32_bf16 v[28:31], v[124:127], v[56:59], v[28:31]\n"
"ds_read_b128 v[48:51], v4 offset:13312\n"
"s_waitcnt vmcnt(10)\n"
"v_mfma_f32_16x16x32_bf16 v[28:31], v[236:239], v[60:63], v[28:31]\n"
"v_lshlrev_b32 v16, 16, v12\n"
"v_and_b32 v17, 0xffff0000, v12\n"
"v_mfma_f32_16x16x32_bf16 v[24:27], v[232:235], v[60:63], v[24:27]\n"
"ds_read_b128 v[52:55], v4 offset:27648\n"
"v_lshlrev_b32 v18, 16, v13\n"
"v_mfma_f32_16x16x32_bf16 v[24:27], v[120:123], v[56:59], v[24:27]\n"
"v_and_b32 v19, 0xffff0000, v13\n"
"s_waitcnt lgkmcnt(4)\n"
"v_mfma_f32_16x16x32_bf16 v[24:27], a[96:99], v[32:35], v[24:27]\n"
"ds_read_b128 v[56:59], v4 offset:41984\n"
"v_lshlrev_b32 v20, 16, v14\n"
"v_mfma_f32_16x16x32_bf16 v[24:27], a[208:211], v[36:39], v[24:27]\n"
"v_and_b32 v21, 0xffff0000, v14\n"
"v_lshlrev_b32 v22, 16, v15\n"
"v_mfma_f32_16x16x32_bf16 v[28:31], a[212:215], v[36:39], v[28:31]\n"
"ds_read_b128 v[60:63], v4 offset:56320\n"
"v_and_b32 v23, 0xffff0000, v15\n"
"v_mfma_f32_16x16x32_bf16 v[28:31], a[100:103], v[32:35], v[28:31]\n"
"s_waitcnt lgkmcnt(4)\n"
"s_barrier\n"
"v_mfma_f32_16x16x32_bf16 v[28:31], v[132:135], v[40:43], v[28:31]\n"
"ds_read_b128 v[32:35], v2 offset:0\n"
"v_mfma_f32_16x16x32_bf16 v[28:31], v[244:247], v[44:47], v[28:31]\n"
"v_mfma_f32_16x16x32_bf16 v[24:27], v[240:243], v[44:47], v[24:27]\n"
"ds_read_b128 v[36:39], v2 offset:14336\n"
"v_mfma_f32_16x16x32_bf16 v[24:27], v[128:131], v[40:43], v[24:27]\n"
"s_waitcnt lgkmcnt(4)\n"
"v_mfma_f32_16x16x32_bf16 v[24:27], a[104:107], v[48:51], v[24:27]\n"
"ds_read_b128 v[40:43], v2 offset:28672\n"
"v_mfma_f32_16x16x32_bf16 v[24:27], a[216:219], v[52:55], v[24:27]\n"
"buffer_store_dwordx2 v[8:9], v2, s[92:95], s67 idxen sc1\n"
"v_mfma_f32_16x16x32_bf16 v[28:31], a[220:223], v[52:55], v[28:31]\n"
"ds_read_b128 v[44:47], v2 offset:43008\n"
"v_mfma_f32_16x16x32_bf16 v[28:31], a[108:111], v[48:51], v[28:31]\n"
"s_waitcnt lgkmcnt(4)\n"
"v_mfma_f32_16x16x32_bf16 v[28:31], v[140:143], v[56:59], v[28:31]\n"
"ds_read_b128 v[48:51], v2 offset:1024\n"
"v_mfma_f32_16x16x32_bf16 v[28:31], v[252:255], v[60:63], v[28:31]\n"
"v_mfma_f32_16x16x32_bf16 v[24:27], v[248:251], v[60:63], v[24:27]\n"
"ds_read_b128 v[52:55], v2 offset:15360\n"
"v_mfma_f32_16x16x32_bf16 v[24:27], v[136:139], v[56:59], v[24:27]\n"
"s_branch .L_dll_0\n"
".L_dlb_0:\n"
"s_mov_b32 s62, 15\n"
"s_add_u32 s74, s74, 8192\n"
"v_lshrrev_b32 v5, 8, v2\n"
"v_lshl_add_u32 v5, v5, 4, s74\n"
"v_and_b32 v3, 128, v2\n"
"v_lshl_add_u32 v5, v3, 2, v5\n"
"v_and_b32 v3, 16, v2\n"
"v_lshl_add_u32 v5, v3, 4, v5\n"
"v_and_b32 v3, 96, v2\n"
"v_lshl_add_u32 v5, v3, 1, v5\n"
"v_lshrrev_b32 v3, 2, v2\n"
"v_add_u32 v3, s74, v3\n"
".p2align 6\n"
".L_dml_0:\n"
"s_waitcnt lgkmcnt(4)\n"
"v_mfma_f32_16x16x32_bf16 v[24:27], a[0:3], v[32:35], v[24:27]\n"
"ds_read_b128 v[56:59], v4 offset:29696\n"
"v_mfma_f32_16x16x32_bf16 v[24:27], a[112:115], v[36:39], v[24:27]\n"
"v_mfma_f32_16x16x32_bf16 v[28:31], a[116:119], v[36:39], v[28:31]\n"
"ds_read_b128 v[60:63], v4 offset:44032\n"
"v_mfma_f32_16x16x32_bf16 v[28:31], a[4:7], v[32:35], v[28:31]\n"
"v_cvt_pk_bf16_f32 v15, v22, v23\n"
"s_waitcnt lgkmcnt(4)\n"
"v_mfma_f32_16x16x32_bf16 v[28:31], a[228:231], v[40:43], v[28:31]\n"
"ds_read_b128 v[32:35], v4 offset:2048\n"
"v_cvt_pk_bf16_f32 v14, v20, v21\n"
"v_mfma_f32_16x16x32_bf16 v[28:31], v[148:151], v[44:47], v[28:31]\n"
"v_cvt_pk_bf16_f32 v13, v18, v19\n"
"v_cvt_pk_bf16_f32 v12, v16, v17\n"
"v_mfma_f32_16x16x32_bf16 v[24:27], v[144:147], v[44:47], v[24:27]\n"
"ds_read_b128 v[36:39], v4 offset:16384\n"
"ds_write_b128 v5, v[12:15]\n"
"v_mfma_f32_16x16x32_bf16 v[24:27], a[224:227], v[40:43], v[24:27]\n"
"ds_read2st64_b32 v[12:13], v3 offset0:0 offset1:1\n"
"s_waitcnt lgkmcnt(6)\n"
"v_mfma_f32_16x16x32_bf16 v[24:27], a[8:11], v[48:51], v[24:27]\n"
"ds_read_b128 v[40:43], v4 offset:30720\n"
"ds_read2st64_b32 v[14:15], v3 offset0:2 offset1:3\n"
"v_mfma_f32_16x16x32_bf16 v[24:27], a[120:123], v[52:55], v[24:27]\n"
"v_mfma_f32_16x16x32_bf16 v[28:31], a[124:127], v[52:55], v[28:31]\n"
"ds_read_b128 v[44:47], v4 offset:45056\n"
"v_mfma_f32_16x16x32_bf16 v[28:31], a[12:15], v[48:51], v[28:31]\n"
"s_waitcnt lgkmcnt(7)\n"
"v_mfma_f32_16x16x32_bf16 v[28:31], a[236:239], v[56:59], v[28:31]\n"
"ds_read_b128 v[48:51], v4 offset:3072\n"
"v_mfma_f32_16x16x32_bf16 v[28:31], v[156:159], v[60:63], v[28:31]\n"
"s_waitcnt lgkmcnt(2)\n"
"buffer_atomic_pk_add_bf16 v12, v1, s[44:47], s65 offen offset:0 sc1\n"
"v_mfma_f32_16x16x32_bf16 v[24:27], v[152:155], v[60:63], v[24:27]\n"
"ds_read_b128 v[52:55], v4 offset:17408\n"
"v_mfma_f32_16x16x32_bf16 v[24:27], a[232:235], v[56:59], v[24:27]\n"
"buffer_atomic_pk_add_bf16 v13, v1, s[44:47], s65 offen offset:2048 sc1\n"
"v_mfma_f32_16x16x32_bf16 v[24:27], a[16:19], v[32:35], v[24:27]\n"
"ds_read_b128 v[56:59], v4 offset:31744\n"
"s_add_u32 s65, s65, 16384\n"
"v_mfma_f32_16x16x32_bf16 v[24:27], a[128:131], v[36:39], v[24:27]\n"
"buffer_atomic_pk_add_bf16 v14, v1, s[44:47], s65 offen offset:0 sc1\n"
"v_mfma_f32_16x16x32_bf16 v[28:31], a[132:135], v[36:39], v[28:31]\n"
"ds_read_b128 v[60:63], v4 offset:46080\n"
"v_mfma_f32_16x16x32_bf16 v[28:31], a[20:23], v[32:35], v[28:31]\n"
"s_waitcnt lgkmcnt(4)\n"
"s_waitcnt vmcnt(3)\n"
"s_barrier\n"
"v_mfma_f32_16x16x32_bf16 v[28:31], a[244:247], v[40:43], v[28:31]\n"
"ds_read_b128 v[32:35], v4 offset:4096\n"
"s_add_u32 m0, s64, 0\n"
"v_mfma_f32_16x16x32_bf16 v[28:31], v[164:167], v[44:47], v[28:31]\n"
"buffer_atomic_pk_add_bf16 v15, v1, s[44:47], s65 offen offset:2048 sc1\n"
"s_add_u32 s65, s65, 16384\n"
"v_mfma_f32_16x16x32_bf16 v[24:27], v[160:163], v[44:47], v[24:27]\n"
"ds_read_b128 v[36:39], v4 offset:18432\n"
"v_mfma_f32_16x16x32_bf16 v[24:27], a[240:243], v[40:43], v[24:27]\n"
"s_waitcnt lgkmcnt(4)\n"
"v_mfma_f32_16x16x32_bf16 v[24:27], a[24:27], v[48:51], v[24:27]\n"
"ds_read_b128 v[40:43], v4 offset:32768\n"
"v_mfma_f32_16x16x32_bf16 v[24:27], a[136:139], v[52:55], v[24:27]\n"
"buffer_load_dwordx4 v2, s[32:35], s63 offen offset:0 sc1 lds\n"
"v_mfma_f32_16x16x32_bf16 v[28:31], a[140:143], v[52:55], v[28:31]\n"
"ds_read_b128 v[44:47], v4 offset:47104\n"
"v_mfma_f32_16x16x32_bf16 v[28:31], a[28:31], v[48:51], v[28:31]\n"
"s_waitcnt lgkmcnt(4)\n"
"v_mfma_f32_16x16x32_bf16 v[28:31], a[252:255], v[56:59], v[28:31]\n"
"ds_read_b128 v[48:51], v4 offset:5120\n"
"v_mfma_f32_16x16x32_bf16 v[28:31], v[172:175], v[60:63], v[28:31]\n"
"buffer_load_dwordx4 v2, s[32:35], s63 offen offset:1024 sc1 lds\n"
"v_mfma_f32_16x16x32_bf16 v[24:27], v[168:171], v[60:63], v[24:27]\n"
"ds_read_b128 v[52:55], v4 offset:19456\n"
"v_mfma_f32_16x16x32_bf16 v[24:27], a[248:251], v[56:59], v[24:27]\n"
"s_waitcnt lgkmcnt(4)\n"
"v_mfma_f32_16x16x32_bf16 v[24:27], a[32:35], v[32:35], v[24:27]\n"
"ds_read_b128 v[56:59], v4 offset:33792\n"
"v_mfma_f32_16x16x32_bf16 v[24:27], a[144:147], v[36:39], v[24:27]\n"
"buffer_load_dwordx4 v2, s[32:35], s63 offen offset:2048 sc1 lds\n"
"v_mfma_f32_16x16x32_bf16 v[28:31], a[148:151], v[36:39], v[28:31]\n"
"ds_read_b128 v[60:63], v4 offset:48128\n"
"v_mfma_f32_16x16x32_bf16 v[28:31], a[36:39], v[32:35], v[28:31]\n"
"s_waitcnt lgkmcnt(4)\n"
"v_mfma_f32_16x16x32_bf16 v[28:31], v[68:71], v[40:43], v[28:31]\n"
"ds_read_b128 v[32:35], v4 offset:6144\n"
"v_mfma_f32_16x16x32_bf16 v[28:31], v[180:183], v[44:47], v[28:31]\n"
"buffer_load_dwordx4 v2, s[32:35], s63 offen offset:3072 sc1 lds\n"
"v_mfma_f32_16x16x32_bf16 v[24:27], v[176:179], v[44:47], v[24:27]\n"
"ds_read_b128 v[36:39], v4 offset:20480\n"
"v_mfma_f32_16x16x32_bf16 v[24:27], v[64:67], v[40:43], v[24:27]\n"
"s_waitcnt lgkmcnt(4)\n"
"v_mfma_f32_16x16x32_bf16 v[24:27], a[40:43], v[48:51], v[24:27]\n"
"ds_read_b128 v[40:43], v4 offset:34816\n"
"v_mfma_f32_16x16x32_bf16 v[24:27], a[152:155], v[52:55], v[24:27]\n"
"buffer_load_dwordx4 v[12:15], v2, s[32:35], s71 offen sc1\n"
"s_add_u32 s71, s71, 1187840\n"
"v_mfma_f32_16x16x32_bf16 v[28:31], a[156:159], v[52:55], v[28:31]\n"
"ds_read_b128 v[44:47], v4 offset:49152\n"
"s_add_u32 s63, s63, 4096\n"
"v_mfma_f32_16x16x32_bf16 v[28:31], a[44:47], v[48:51], v[28:31]\n"
"s_add_u32 m0, s64, 4096\n"
"s_waitcnt lgkmcnt(4)\n"
"v_mfma_f32_16x16x32_bf16 v[28:31], v[76:79], v[56:59], v[28:31]\n"
"ds_read_b128 v[48:51], v4 offset:7168\n"
"v_mfma_f32_16x16x32_bf16 v[28:31], v[188:191], v[60:63], v[28:31]\n"
"buffer_load_dwordx4 v2, s[32:35], s63 offen offset:0 sc1 lds\n"
"v_mfma_f32_16x16x32_bf16 v[24:27], v[184:187], v[60:63], v[24:27]\n"
"ds_read_b128 v[52:55], v4 offset:21504\n"
"v_mfma_f32_16x16x32_bf16 v[24:27], v[72:75], v[56:59], v[24:27]\n"
"s_waitcnt lgkmcnt(4)\n"
"v_mfma_f32_16x16x32_bf16 v[24:27], a[48:51], v[32:35], v[24:27]\n"
"ds_read_b128 v[56:59], v4 offset:35840\n"
"v_mfma_f32_16x16x32_bf16 v[24:27], a[160:163], v[36:39], v[24:27]\n"
"buffer_load_dwordx4 v2, s[32:35], s63 offen offset:1024 sc1 lds\n"
"v_mfma_f32_16x16x32_bf16 v[28:31], a[164:167], v[36:39], v[28:31]\n"
"ds_read_b128 v[60:63], v4 offset:50176\n"
"v_mfma_f32_16x16x32_bf16 v[28:31], a[52:55], v[32:35], v[28:31]\n"
"s_waitcnt lgkmcnt(4)\n"
"v_mfma_f32_16x16x32_bf16 v[28:31], v[84:87], v[40:43], v[28:31]\n"
"ds_read_b128 v[32:35], v4 offset:8192\n"
"v_mfma_f32_16x16x32_bf16 v[28:31], v[196:199], v[44:47], v[28:31]\n"
"buffer_load_dwordx4 v2, s[32:35], s63 offen offset:2048 sc1 lds\n"
"v_mfma_f32_16x16x32_bf16 v[24:27], v[192:195], v[44:47], v[24:27]\n"
"ds_read_b128 v[36:39], v4 offset:22528\n"
"v_mfma_f32_16x16x32_bf16 v[24:27], v[80:83], v[40:43], v[24:27]\n"
"s_waitcnt lgkmcnt(4)\n"
"v_mfma_f32_16x16x32_bf16 v[24:27], a[56:59], v[48:51], v[24:27]\n"
"ds_read_b128 v[40:43], v4 offset:36864\n"
"v_mfma_f32_16x16x32_bf16 v[24:27], a[168:171], v[52:55], v[24:27]\n"
"buffer_load_dwordx4 v2, s[32:35], s63 offen offset:3072 sc1 lds\n"
"v_mfma_f32_16x16x32_bf16 v[28:31], a[172:175], v[52:55], v[28:31]\n"
"ds_read_b128 v[44:47], v4 offset:51200\n"
"v_mfma_f32_16x16x32_bf16 v[28:31], a[60:63], v[48:51], v[28:31]\n"
"s_waitcnt lgkmcnt(4)\n"
"v_mfma_f32_16x16x32_bf16 v[28:31], v[92:95], v[56:59], v[28:31]\n"
"ds_read_b128 v[48:51], v4 offset:9216\n"
"v_mfma_f32_16x16x32_bf16 v[28:31], v[204:207], v[60:63], v[28:31]\n"
"s_add_u32 s63, s63, 4096\n"
"s_add_u32 m0, s64, 8192\n"
"v_mfma_f32_16x16x32_bf16 v[24:27], v[200:203], v[60:63], v[24:27]\n"
"ds_read_b128 v[52:55], v4 offset:23552\n"
"v_mfma_f32_16x16x32_bf16 v[24:27], v[88:91], v[56:59], v[24:27]\n"
"s_waitcnt lgkmcnt(4)\n"
"v_mfma_f32_16x16x32_bf16 v[24:27], a[64:67], v[32:35], v[24:27]\n"
"ds_read_b128 v[56:59], v4 offset:37888\n"
"v_mfma_f32_16x16x32_bf16 v[24:27], a[176:179], v[36:39], v[24:27]\n"
"buffer_load_dwordx4 v2, s[32:35], s63 offen offset:0 sc1 lds\n"
"v_mfma_f32_16x16x32_bf16 v[28:31], a[180:183], v[36:39], v[28:31]\n"
"ds_read_b128 v[60:63], v4 offset:52224\n"
"v_mfma_f32_16x16x32_bf16 v[28:31], a[68:71], v[32:35], v[28:31]\n"
"s_waitcnt lgkmcnt(4)\n"
"v_mfma_f32_16x16x32_bf16 v[28:31], v[100:103], v[40:43], v[28:31]\n"
"ds_read_b128 v[32:35], v4 offset:10240\n"
"v_mfma_f32_16x16x32_bf16 v[28:31], v[212:215], v[44:47], v[28:31]\n"
"buffer_load_dwordx4 v2, s[32:35], s63 offen offset:1024 sc1 lds\n"
"v_mfma_f32_16x16x32_bf16 v[24:27], v[208:211], v[44:47], v[24:27]\n"
"ds_read_b128 v[36:39], v4 offset:24576\n"
"v_mfma_f32_16x16x32_bf16 v[24:27], v[96:99], v[40:43], v[24:27]\n"
"s_waitcnt lgkmcnt(4)\n"
"v_mfma_f32_16x16x32_bf16 v[24:27], a[72:75], v[48:51], v[24:27]\n"
"ds_read_b128 v[40:43], v4 offset:38912\n"
"v_mfma_f32_16x16x32_bf16 v[24:27], a[184:187], v[52:55], v[24:27]\n"
"buffer_load_dwordx4 v2, s[32:35], s63 offen offset:2048 sc1 lds\n"
"v_mfma_f32_16x16x32_bf16 v[28:31], a[188:191], v[52:55], v[28:31]\n"
"ds_read_b128 v[44:47], v4 offset:53248\n"
"v_mfma_f32_16x16x32_bf16 v[28:31], a[76:79], v[48:51], v[28:31]\n"
"s_waitcnt lgkmcnt(4)\n"
"v_mfma_f32_16x16x32_bf16 v[28:31], v[108:111], v[56:59], v[28:31]\n"
"ds_read_b128 v[48:51], v4 offset:11264\n"
"v_mfma_f32_16x16x32_bf16 v[28:31], v[220:223], v[60:63], v[28:31]\n"
"v_mfma_f32_16x16x32_bf16 v[24:27], v[216:219], v[60:63], v[24:27]\n"
"ds_read_b128 v[52:55], v4 offset:25600\n"
"v_mfma_f32_16x16x32_bf16 v[24:27], v[104:107], v[56:59], v[24:27]\n"
"s_waitcnt lgkmcnt(4)\n"
"v_mfma_f32_16x16x32_bf16 v[24:27], a[80:83], v[32:35], v[24:27]\n"
"ds_read_b128 v[56:59], v4 offset:39936\n"
"v_mfma_f32_16x16x32_bf16 v[24:27], a[192:195], v[36:39], v[24:27]\n"
"buffer_load_dwordx4 v2, s[32:35], s63 offen offset:3072 sc1 lds\n"
"s_add_u32 s63, s63, 4096\n"
"v_mfma_f32_16x16x32_bf16 v[28:31], a[196:199], v[36:39], v[28:31]\n"
"ds_read_b128 v[60:63], v4 offset:54272\n"
"s_add_u32 m0, s64, 12288\n"
"v_mfma_f32_16x16x32_bf16 v[28:31], a[84:87], v[32:35], v[28:31]\n"
"s_waitcnt lgkmcnt(4)\n"
"v_mfma_f32_16x16x32_bf16 v[28:31], v[116:119], v[40:43], v[28:31]\n"
"ds_read_b128 v[32:35], v4 offset:12288\n"
"v_mfma_f32_16x16x32_bf16 v[28:31], v[228:231], v[44:47], v[28:31]\n"
"buffer_load_dwordx4 v2, s[32:35], s63 offen offset:0 sc1 lds\n"
"v_mfma_f32_16x16x32_bf16 v[24:27], v[224:227], v[44:47], v[24:27]\n"
"ds_read_b128 v[36:39], v4 offset:26624\n"
"v_mfma_f32_16x16x32_bf16 v[24:27], v[112:115], v[40:43], v[24:27]\n"
"s_waitcnt lgkmcnt(4)\n"
"v_mfma_f32_16x16x32_bf16 v[24:27], a[88:91], v[48:51], v[24:27]\n"
"ds_read_b128 v[40:43], v4 offset:40960\n"
"v_mfma_f32_16x16x32_bf16 v[24:27], a[200:203], v[52:55], v[24:27]\n"
"buffer_load_dwordx4 v2, s[32:35], s63 offen offset:1024 sc1 lds\n"
"s_add_u32 s63, s63, 1175552\n"
"v_mfma_f32_16x16x32_bf16 v[28:31], a[204:207], v[52:55], v[28:31]\n"
"ds_read_b128 v[44:47], v4 offset:55296\n"
"v_mfma_f32_16x16x32_bf16 v[28:31], a[92:95], v[48:51], v[28:31]\n"
"s_waitcnt lgkmcnt(4)\n"
"v_mfma_f32_16x16x32_bf16 v[28:31], v[124:127], v[56:59], v[28:31]\n"
"ds_read_b128 v[48:51], v4 offset:13312\n"
"s_waitcnt vmcnt(10)\n"
"v_mfma_f32_16x16x32_bf16 v[28:31], v[236:239], v[60:63], v[28:31]\n"
"v_lshlrev_b32 v16, 16, v12\n"
"v_and_b32 v17, 0xffff0000, v12\n"
"v_mfma_f32_16x16x32_bf16 v[24:27], v[232:235], v[60:63], v[24:27]\n"
"ds_read_b128 v[52:55], v4 offset:27648\n"
"v_lshlrev_b32 v18, 16, v13\n"
"v_mfma_f32_16x16x32_bf16 v[24:27], v[120:123], v[56:59], v[24:27]\n"
"v_and_b32 v19, 0xffff0000, v13\n"
"s_waitcnt lgkmcnt(4)\n"
"v_mfma_f32_16x16x32_bf16 v[24:27], a[96:99], v[32:35], v[24:27]\n"
"ds_read_b128 v[56:59], v4 offset:41984\n"
"v_lshlrev_b32 v20, 16, v14\n"
"v_mfma_f32_16x16x32_bf16 v[24:27], a[208:211], v[36:39], v[24:27]\n"
"v_and_b32 v21, 0xffff0000, v14\n"
"v_lshlrev_b32 v22, 16, v15\n"
"v_mfma_f32_16x16x32_bf16 v[28:31], a[212:215], v[36:39], v[28:31]\n"
"ds_read_b128 v[60:63], v4 offset:56320\n"
"v_and_b32 v23, 0xffff0000, v15\n"
"v_mfma_f32_16x16x32_bf16 v[28:31], a[100:103], v[32:35], v[28:31]\n"
"s_waitcnt lgkmcnt(4)\n"
"s_barrier\n"
"v_mfma_f32_16x16x32_bf16 v[28:31], v[132:135], v[40:43], v[28:31]\n"
"ds_read_b128 v[32:35], v2 offset:0\n"
"v_mfma_f32_16x16x32_bf16 v[28:31], v[244:247], v[44:47], v[28:31]\n"
"v_mfma_f32_16x16x32_bf16 v[24:27], v[240:243], v[44:47], v[24:27]\n"
"ds_read_b128 v[36:39], v2 offset:14336\n"
"v_mfma_f32_16x16x32_bf16 v[24:27], v[128:131], v[40:43], v[24:27]\n"
"s_waitcnt lgkmcnt(4)\n"
"v_mfma_f32_16x16x32_bf16 v[24:27], a[104:107], v[48:51], v[24:27]\n"
"ds_read_b128 v[40:43], v2 offset:28672\n"
"v_mfma_f32_16x16x32_bf16 v[24:27], a[216:219], v[52:55], v[24:27]\n"
"v_mfma_f32_16x16x32_bf16 v[28:31], a[220:223], v[52:55], v[28:31]\n"
"ds_read_b128 v[44:47], v2 offset:43008\n"
"v_mfma_f32_16x16x32_bf16 v[28:31], a[108:111], v[48:51], v[28:31]\n"
"s_waitcnt lgkmcnt(4)\n"
"v_mfma_f32_16x16x32_bf16 v[28:31], v[140:143], v[56:59], v[28:31]\n"
"ds_read_b128 v[48:51], v2 offset:1024\n"
"v_mfma_f32_16x16x32_bf16 v[28:31], v[252:255], v[60:63], v[28:31]\n"
"v_mfma_f32_16x16x32_bf16 v[24:27], v[248:251], v[60:63], v[24:27]\n"
"ds_read_b128 v[52:55], v2 offset:15360\n"
"s_sub_u32 s62, s62, 1\n"
"v_mfma_f32_16x16x32_bf16 v[24:27], v[136:139], v[56:59], v[24:27]\n"
"s_cbranch_scc1 .L_dmt_0\n"
"s_waitcnt lgkmcnt(4)\n"
"v_mfma_f32_16x16x32_bf16 v[16:19], a[0:3], v[32:35], v[16:19]\n"
"ds_read_b128 v[56:59], v2 offset:29696\n"
"v_mfma_f32_16x16x32_bf16 v[16:19], a[112:115], v[36:39], v[16:19]\n"
"v_mfma_f32_16x16x32_bf16 v[20:23], a[116:119], v[36:39], v[20:23]\n"
"ds_read_b128 v[60:63], v2 offset:44032\n"
"v_mfma_f32_16x16x32_bf16 v[20:23], a[4:7], v[32:35], v[20:23]\n"
"v_cvt_pk_bf16_f32 v15, v30, v31\n"
"s_waitcnt lgkmcnt(4)\n"
"v_mfma_f32_16x16x32_bf16 v[20:23], a[228:231], v[40:43], v[20:23]\n"
"ds_read_b128 v[32:35], v2 offset:2048\n"
"v_cvt_pk_bf16_f32 v14, v28, v29\n"
"v_mfma_f32_16x16x32_bf16 v[20:23], v[148:151], v[44:47], v[20:23]\n"
"v_cvt_pk_bf16_f32 v13, v26, v27\n"
"v_cvt_pk_bf16_f32 v12, v24, v25\n"
"v_mfma_f32_16x16x32_bf16 v[16:19], v[144:147], v[44:47], v[16:19]\n"
"ds_read_b128 v[36:39], v2 offset:16384\n"
"ds_write_b128 v5, v[12:15]\n"
"v_mfma_f32_16x16x32_bf16 v[16:19], a[224:227], v[40:43], v[16:19]\n"
"ds_read2st64_b32 v[12:13], v3 offset0:0 offset1:1\n"
"s_waitcnt lgkmcnt(6)\n"
"v_mfma_f32_16x16x32_bf16 v[16:19], a[8:11], v[48:51], v[16:19]\n"
"ds_read_b128 v[40:43], v2 offset:30720\n"
"ds_read2st64_b32 v[14:15], v3 offset0:2 offset1:3\n"
"v_mfma_f32_16x16x32_bf16 v[16:19], a[120:123], v[52:55], v[16:19]\n"
"v_mfma_f32_16x16x32_bf16 v[20:23], a[124:127], v[52:55], v[20:23]\n"
"ds_read_b128 v[44:47], v2 offset:45056\n"
"v_mfma_f32_16x16x32_bf16 v[20:23], a[12:15], v[48:51], v[20:23]\n"
"s_waitcnt lgkmcnt(7)\n"
"v_mfma_f32_16x16x32_bf16 v[20:23], a[236:239], v[56:59], v[20:23]\n"
"ds_read_b128 v[48:51], v2 offset:3072\n"
"v_mfma_f32_16x16x32_bf16 v[20:23], v[156:159], v[60:63], v[20:23]\n"
"s_waitcnt lgkmcnt(2)\n"
"buffer_atomic_pk_add_bf16 v12, v1, s[44:47], s65 offen offset:0 sc1\n"
"v_mfma_f32_16x16x32_bf16 v[16:19], v[152:155], v[60:63], v[16:19]\n"
"ds_read_b128 v[52:55], v2 offset:17408\n"
"v_mfma_f32_16x16x32_bf16 v[16:19], a[232:235], v[56:59], v[16:19]\n"
"buffer_atomic_pk_add_bf16 v13, v1, s[44:47], s65 offen offset:2048 sc1\n"
"v_mfma_f32_16x16x32_bf16 v[16:19], a[16:19], v[32:35], v[16:19]\n"
"ds_read_b128 v[56:59], v2 offset:31744\n"
"s_add_u32 s65, s65, 16384\n"
"v_mfma_f32_16x16x32_bf16 v[16:19], a[128:131], v[36:39], v[16:19]\n"
"buffer_atomic_pk_add_bf16 v14, v1, s[44:47], s65 offen offset:0 sc1\n"
"v_mfma_f32_16x16x32_bf16 v[20:23], a[132:135], v[36:39], v[20:23]\n"
"ds_read_b128 v[60:63], v2 offset:46080\n"
"v_mfma_f32_16x16x32_bf16 v[20:23], a[20:23], v[32:35], v[20:23]\n"
"s_waitcnt lgkmcnt(4)\n"
"s_waitcnt vmcnt(3)\n"
"s_barrier\n"
"v_mfma_f32_16x16x32_bf16 v[20:23], a[244:247], v[40:43], v[20:23]\n"
"ds_read_b128 v[32:35], v2 offset:4096\n"
"s_add_u32 m0, s64, 57344\n"
"v_mfma_f32_16x16x32_bf16 v[20:23], v[164:167], v[44:47], v[20:23]\n"
"buffer_atomic_pk_add_bf16 v15, v1, s[44:47], s65 offen offset:2048 sc1\n"
"s_add_u32 s65, s65, 16384\n"
"v_mfma_f32_16x16x32_bf16 v[16:19], v[160:163], v[44:47], v[16:19]\n"
"ds_read_b128 v[36:39], v2 offset:18432\n"
"v_mfma_f32_16x16x32_bf16 v[16:19], a[240:243], v[40:43], v[16:19]\n"
"s_waitcnt lgkmcnt(4)\n"
"v_mfma_f32_16x16x32_bf16 v[16:19], a[24:27], v[48:51], v[16:19]\n"
"ds_read_b128 v[40:43], v2 offset:32768\n"
"v_mfma_f32_16x16x32_bf16 v[16:19], a[136:139], v[52:55], v[16:19]\n"
"buffer_load_dwordx4 v2, s[32:35], s63 offen offset:0 sc1 lds\n"
"v_mfma_f32_16x16x32_bf16 v[20:23], a[140:143], v[52:55], v[20:23]\n"
"ds_read_b128 v[44:47], v2 offset:47104\n"
"v_mfma_f32_16x16x32_bf16 v[20:23], a[28:31], v[48:51], v[20:23]\n"
"s_waitcnt lgkmcnt(4)\n"
"v_mfma_f32_16x16x32_bf16 v[20:23], a[252:255], v[56:59], v[20:23]\n"
"ds_read_b128 v[48:51], v2 offset:5120\n"
"v_mfma_f32_16x16x32_bf16 v[20:23], v[172:175], v[60:63], v[20:23]\n"
"buffer_load_dwordx4 v2, s[32:35], s63 offen offset:1024 sc1 lds\n"
"v_mfma_f32_16x16x32_bf16 v[16:19], v[168:171], v[60:63], v[16:19]\n"
"ds_read_b128 v[52:55], v2 offset:19456\n"
"v_mfma_f32_16x16x32_bf16 v[16:19], a[248:251], v[56:59], v[16:19]\n"
"s_waitcnt lgkmcnt(4)\n"
"v_mfma_f32_16x16x32_bf16 v[16:19], a[32:35], v[32:35], v[16:19]\n"
"ds_read_b128 v[56:59], v2 offset:33792\n"
"v_mfma_f32_16x16x32_bf16 v[16:19], a[144:147], v[36:39], v[16:19]\n"
"buffer_load_dwordx4 v2, s[32:35], s63 offen offset:2048 sc1 lds\n"
"v_mfma_f32_16x16x32_bf16 v[20:23], a[148:151], v[36:39], v[20:23]\n"
"ds_read_b128 v[60:63], v2 offset:48128\n"
"v_mfma_f32_16x16x32_bf16 v[20:23], a[36:39], v[32:35], v[20:23]\n"
"s_waitcnt lgkmcnt(4)\n"
"v_mfma_f32_16x16x32_bf16 v[20:23], v[68:71], v[40:43], v[20:23]\n"
"ds_read_b128 v[32:35], v2 offset:6144\n"
"v_mfma_f32_16x16x32_bf16 v[20:23], v[180:183], v[44:47], v[20:23]\n"
"buffer_load_dwordx4 v2, s[32:35], s63 offen offset:3072 sc1 lds\n"
"v_mfma_f32_16x16x32_bf16 v[16:19], v[176:179], v[44:47], v[16:19]\n"
"ds_read_b128 v[36:39], v2 offset:20480\n"
"v_mfma_f32_16x16x32_bf16 v[16:19], v[64:67], v[40:43], v[16:19]\n"
"s_waitcnt lgkmcnt(4)\n"
"v_mfma_f32_16x16x32_bf16 v[16:19], a[40:43], v[48:51], v[16:19]\n"
"ds_read_b128 v[40:43], v2 offset:34816\n"
"v_mfma_f32_16x16x32_bf16 v[16:19], a[152:155], v[52:55], v[16:19]\n"
"buffer_load_dwordx4 v[12:15], v2, s[32:35], s71 offen sc1\n"
"s_add_u32 s71, s71, 1187840\n"
"v_mfma_f32_16x16x32_bf16 v[20:23], a[156:159], v[52:55], v[20:23]\n"
"ds_read_b128 v[44:47], v2 offset:49152\n"
"s_add_u32 s63, s63, 4096\n"
"v_mfma_f32_16x16x32_bf16 v[20:23], a[44:47], v[48:51], v[20:23]\n"
"s_add_u32 m0, s64, 61440\n"
"s_waitcnt lgkmcnt(4)\n"
"v_mfma_f32_16x16x32_bf16 v[20:23], v[76:79], v[56:59], v[20:23]\n"
"ds_read_b128 v[48:51], v2 offset:7168\n"
"v_mfma_f32_16x16x32_bf16 v[20:23], v[188:191], v[60:63], v[20:23]\n"
"buffer_load_dwordx4 v2, s[32:35], s63 offen offset:0 sc1 lds\n"
"v_mfma_f32_16x16x32_bf16 v[16:19], v[184:187], v[60:63], v[16:19]\n"
"ds_read_b128 v[52:55], v2 offset:21504\n"
"v_mfma_f32_16x16x32_bf16 v[16:19], v[72:75], v[56:59], v[16:19]\n"
"s_waitcnt lgkmcnt(4)\n"
"v_mfma_f32_16x16x32_bf16 v[16:19], a[48:51], v[32:35], v[16:19]\n"
"ds_read_b128 v[56:59], v2 offset:35840\n"
"v_mfma_f32_16x16x32_bf16 v[16:19], a[160:163], v[36:39], v[16:19]\n"
"buffer_load_dwordx4 v2, s[32:35], s63 offen offset:1024 sc1 lds\n"
"v_mfma_f32_16x16x32_bf16 v[20:23], a[164:167], v[36:39], v[20:23]\n"
"ds_read_b128 v[60:63], v2 offset:50176\n"
"v_mfma_f32_16x16x32_bf16 v[20:23], a[52:55], v[32:35], v[20:23]\n"
"s_waitcnt lgkmcnt(4)\n"
"v_mfma_f32_16x16x32_bf16 v[20:23], v[84:87], v[40:43], v[20:23]\n"
"ds_read_b128 v[32:35], v2 offset:8192\n"
"v_mfma_f32_16x16x32_bf16 v[20:23], v[196:199], v[44:47], v[20:23]\n"
"buffer_load_dwordx4 v2, s[32:35], s63 offen offset:2048 sc1 lds\n"
"v_mfma_f32_16x16x32_bf16 v[16:19], v[192:195], v[44:47], v[16:19]\n"
"ds_read_b128 v[36:39], v2 offset:22528\n"
"v_mfma_f32_16x16x32_bf16 v[16:19], v[80:83], v[40:43], v[16:19]\n"
"s_waitcnt lgkmcnt(4)\n"
"v_mfma_f32_16x16x32_bf16 v[16:19], a[56:59], v[48:51], v[16:19]\n"
"ds_read_b128 v[40:43], v2 offset:36864\n"
"v_mfma_f32_16x16x32_bf16 v[16:19], a[168:171], v[52:55], v[16:19]\n"
"buffer_load_dwordx4 v2, s[32:35], s63 offen offset:3072 sc1 lds\n"
"v_mfma_f32_16x16x32_bf16 v[20:23], a[172:175], v[52:55], v[20:23]\n"
"ds_read_b128 v[44:47], v2 offset:51200\n"
"v_mfma_f32_16x16x32_bf16 v[20:23], a[60:63], v[48:51], v[20:23]\n"
"s_waitcnt lgkmcnt(4)\n"
"v_mfma_f32_16x16x32_bf16 v[20:23], v[92:95], v[56:59], v[20:23]\n"
"ds_read_b128 v[48:51], v2 offset:9216\n"
"v_mfma_f32_16x16x32_bf16 v[20:23], v[204:207], v[60:63], v[20:23]\n"
"s_add_u32 s63, s63, 4096\n"
"s_add_u32 m0, s64, 65536\n"
"v_mfma_f32_16x16x32_bf16 v[16:19], v[200:203], v[60:63], v[16:19]\n"
"ds_read_b128 v[52:55], v2 offset:23552\n"
"v_mfma_f32_16x16x32_bf16 v[16:19], v[88:91], v[56:59], v[16:19]\n"
"s_waitcnt lgkmcnt(4)\n"
"v_mfma_f32_16x16x32_bf16 v[16:19], a[64:67], v[32:35], v[16:19]\n"
"ds_read_b128 v[56:59], v2 offset:37888\n"
"v_mfma_f32_16x16x32_bf16 v[16:19], a[176:179], v[36:39], v[16:19]\n"
"buffer_load_dwordx4 v2, s[32:35], s63 offen offset:0 sc1 lds\n"
"v_mfma_f32_16x16x32_bf16 v[20:23], a[180:183], v[36:39], v[20:23]\n"
"ds_read_b128 v[60:63], v2 offset:52224\n"
"v_mfma_f32_16x16x32_bf16 v[20:23], a[68:71], v[32:35], v[20:23]\n"
"s_waitcnt lgkmcnt(4)\n"
"v_mfma_f32_16x16x32_bf16 v[20:23], v[100:103], v[40:43], v[20:23]\n"
"ds_read_b128 v[32:35], v2 offset:10240\n"
"v_mfma_f32_16x16x32_bf16 v[20:23], v[212:215], v[44:47], v[20:23]\n"
"buffer_load_dwordx4 v2, s[32:35], s63 offen offset:1024 sc1 lds\n"
"v_mfma_f32_16x16x32_bf16 v[16:19], v[208:211], v[44:47], v[16:19]\n"
"ds_read_b128 v[36:39], v2 offset:24576\n"
"v_mfma_f32_16x16x32_bf16 v[16:19], v[96:99], v[40:43], v[16:19]\n"
"s_waitcnt lgkmcnt(4)\n"
"v_mfma_f32_16x16x32_bf16 v[16:19], a[72:75], v[48:51], v[16:19]\n"
"ds_read_b128 v[40:43], v2 offset:38912\n"
"v_mfma_f32_16x16x32_bf16 v[16:19], a[184:187], v[52:55], v[16:19]\n"
"buffer_load_dwordx4 v2, s[32:35], s63 offen offset:2048 sc1 lds\n"
"v_mfma_f32_16x16x32_bf16 v[20:23], a[188:191], v[52:55], v[20:23]\n"
"ds_read_b128 v[44:47], v2 offset:53248\n"
"v_mfma_f32_16x16x32_bf16 v[20:23], a[76:79], v[48:51], v[20:23]\n"
"s_waitcnt lgkmcnt(4)\n"
"v_mfma_f32_16x16x32_bf16 v[20:23], v[108:111], v[56:59], v[20:23]\n"
"ds_read_b128 v[48:51], v2 offset:11264\n"
"v_mfma_f32_16x16x32_bf16 v[20:23], v[220:223], v[60:63], v[20:23]\n"
"v_mfma_f32_16x16x32_bf16 v[16:19], v[216:219], v[60:63], v[16:19]\n"
"ds_read_b128 v[52:55], v2 offset:25600\n"
"v_mfma_f32_16x16x32_bf16 v[16:19], v[104:107], v[56:59], v[16:19]\n"
"s_waitcnt lgkmcnt(4)\n"
"v_mfma_f32_16x16x32_bf16 v[16:19], a[80:83], v[32:35], v[16:19]\n"
"ds_read_b128 v[56:59], v2 offset:39936\n"
"v_mfma_f32_16x16x32_bf16 v[16:19], a[192:195], v[36:39], v[16:19]\n"
"buffer_load_dwordx4 v2, s[32:35], s63 offen offset:3072 sc1 lds\n"
"s_add_u32 s63, s63, 4096\n"
"v_mfma_f32_16x16x32_bf16 v[20:23], a[196:199], v[36:39], v[20:23]\n"
"ds_read_b128 v[60:63], v2 offset:54272\n"
"s_add_u32 m0, s64, 69632\n"
"v_mfma_f32_16x16x32_bf16 v[20:23], a[84:87], v[32:35], v[20:23]\n"
"s_waitcnt lgkmcnt(4)\n"
"v_mfma_f32_16x16x32_bf16 v[20:23], v[116:119], v[40:43], v[20:23]\n"
"ds_read_b128 v[32:35], v2 offset:12288\n"
"v_mfma_f32_16x16x32_bf16 v[20:23], v[228:231], v[44:47], v[20:23]\n"
"buffer_load_dwordx4 v2, s[32:35], s63 offen offset:0 sc1 lds\n"
"v_mfma_f32_16x16x32_bf16 v[16:19], v[224:227], v[44:47], v[16:19]\n"
"ds_read_b128 v[36:39], v2 offset:26624\n"
"v_mfma_f32_16x16x32_bf16 v[16:19], v[112:115], v[40:43], v[16:19]\n"
"s_waitcnt lgkmcnt(4)\n"
"v_mfma_f32_16x16x32_bf16 v[16:19], a[88:91], v[48:51], v[16:19]\n"
"ds_read_b128 v[40:43], v2 offset:40960\n"
"v_mfma_f32_16x16x32_bf16 v[16:19], a[200:203], v[52:55], v[16:19]\n"
"buffer_load_dwordx4 v2, s[32:35], s63 offen offset:1024 sc1 lds\n"
"s_add_u32 s63, s63, 1175552\n"
"v_mfma_f32_16x16x32_bf16 v[20:23], a[204:207], v[52:55], v[20:23]\n"
"ds_read_b128 v[44:47], v2 offset:55296\n"
"v_mfma_f32_16x16x32_bf16 v[20:23], a[92:95], v[48:51], v[20:23]\n"
"s_waitcnt lgkmcnt(4)\n"
"v_mfma_f32_16x16x32_bf16 v[20:23], v[124:127], v[56:59], v[20:23]\n"
"ds_read_b128 v[48:51], v2 offset:13312\n"
"s_waitcnt vmcnt(10)\n"
"v_mfma_f32_16x16x32_bf16 v[20:23], v[236:239], v[60:63], v[20:23]\n"
"v_lshlrev_b32 v24, 16, v12\n"
"v_and_b32 v25, 0xffff0000, v12\n"
"v_mfma_f32_16x16x32_bf16 v[16:19], v[232:235], v[60:63], v[16:19]\n"
"ds_read_b128 v[52:55], v2 offset:27648\n"
"v_lshlrev_b32 v26, 16, v13\n"
"v_mfma_f32_16x16x32_bf16 v[16:19], v[120:123], v[56:59], v[16:19]\n"
"v_and_b32 v27, 0xffff0000, v13\n"
"s_waitcnt lgkmcnt(4)\n"
"v_mfma_f32_16x16x32_bf16 v[16:19], a[96:99], v[32:35], v[16:19]\n"
"ds_read_b128 v[56:59], v2 offset:41984\n"
"v_lshlrev_b32 v28, 16, v14\n"
"v_mfma_f32_16x16x32_bf16 v[16:19], a[208:211], v[36:39], v[16:19]\n"
"v_and_b32 v29, 0xffff0000, v14\n"
"v_lshlrev_b32 v30, 16, v15\n"
"v_mfma_f32_16x16x32_bf16 v[20:23], a[212:215], v[36:39], v[20:23]\n"
"ds_read_b128 v[60:63], v2 offset:56320\n"
"v_and_b32 v31, 0xffff0000, v15\n"
"v_mfma_f32_16x16x32_bf16 v[20:23], a[100:103], v[32:35], v[20:23]\n"
"s_waitcnt lgkmcnt(4)\n"
"s_barrier\n"
"v_mfma_f32_16x16x32_bf16 v[20:23], v[132:135], v[40:43], v[20:23]\n"
"ds_read_b128 v[32:35], v4 offset:0\n"
"v_mfma_f32_16x16x32_bf16 v[20:23], v[244:247], v[44:47], v[20:23]\n"
"v_mfma_f32_16x16x32_bf16 v[16:19], v[240:243], v[44:47], v[16:19]\n"
"ds_read_b128 v[36:39], v4 offset:14336\n"
"v_mfma_f32_16x16x32_bf16 v[16:19], v[128:131], v[40:43], v[16:19]\n"
"s_waitcnt lgkmcnt(4)\n"
"v_mfma_f32_16x16x32_bf16 v[16:19], a[104:107], v[48:51], v[16:19]\n"
"ds_read_b128 v[40:43], v4 offset:28672\n"
"v_mfma_f32_16x16x32_bf16 v[16:19], a[216:219], v[52:55], v[16:19]\n"
"v_mfma_f32_16x16x32_bf16 v[20:23], a[220:223], v[52:55], v[20:23]\n"
"ds_read_b128 v[44:47], v4 offset:43008\n"
"v_mfma_f32_16x16x32_bf16 v[20:23], a[108:111], v[48:51], v[20:23]\n"
"s_waitcnt lgkmcnt(4)\n"
"v_mfma_f32_16x16x32_bf16 v[20:23], v[140:143], v[56:59], v[20:23]\n"
"ds_read_b128 v[48:51], v4 offset:1024\n"
"v_mfma_f32_16x16x32_bf16 v[20:23], v[252:255], v[60:63], v[20:23]\n"
"v_mfma_f32_16x16x32_bf16 v[16:19], v[248:251], v[60:63], v[16:19]\n"
"ds_read_b128 v[52:55], v4 offset:15360\n"
"v_mfma_f32_16x16x32_bf16 v[16:19], v[136:139], v[56:59], v[16:19]\n"
"s_branch .L_dml_0\n"
".L_dmt_0:\n"
"s_nop 1\n"
"s_nop 0\n"
"v_cvt_pk_bf16_f32 v15, v30, v31\n"
"v_cvt_pk_bf16_f32 v14, v28, v29\n"
"s_nop 1\n"
"v_cvt_pk_bf16_f32 v13, v26, v27\n"
"v_cvt_pk_bf16_f32 v12, v24, v25\n"
"ds_write_b128 v5, v[12:15]\n"
"ds_read2st64_b32 v[12:13], v3 offset0:0 offset1:1\n"
"ds_read2st64_b32 v[14:15], v3 offset0:2 offset1:3\n"
"s_waitcnt lgkmcnt(0)\n"
"buffer_atomic_pk_add_bf16 v12, v1, s[44:47], s65 offen offset:0 sc1\n"
"buffer_atomic_pk_add_bf16 v13, v1, s[44:47], s65 offen offset:2048 sc1\n"
"s_add_u32 s65, s65, 16384\n"
"buffer_atomic_pk_add_bf16 v14, v1, s[44:47], s65 offen offset:0 sc1\n"
"buffer_atomic_pk_add_bf16 v15, v1, s[44:47], s65 offen offset:2048 sc1\n"
"s_add_u32 s65, s65, 16384\n"
"s_branch .L_end\n"
".p2align 6\n"
".L_dlv1:\n"
".L_dll_1:\n"
"s_waitcnt lgkmcnt(4)\n"
"v_mfma_f32_16x16x32_bf16 v[16:19], a[0:3], v[32:35], v[16:19]\n"
"ds_read_b128 v[56:59], v2 offset:29696\n"
"v_mfma_f32_16x16x32_bf16 v[16:19], a[112:115], v[36:39], v[16:19]\n"
"v_mfma_f32_16x16x32_bf16 v[20:23], a[116:119], v[36:39], v[20:23]\n"
"ds_read_b128 v[60:63], v2 offset:44032\n"
"v_mfma_f32_16x16x32_bf16 v[20:23], a[4:7], v[32:35], v[20:23]\n"
"s_add_u32 s68, s68, 1187840\n"
"s_waitcnt lgkmcnt(4)\n"
"v_mfma_f32_16x16x32_bf16 v[20:23], a[228:231], v[40:43], v[20:23]\n"
"ds_read_b128 v[32:35], v2 offset:2048\n"
"v_mfma_f32_16x16x32_bf16 v[20:23], v[148:151], v[44:47], v[20:23]\n"
"buffer_store_dwordx4 v[28:31], v2, s[32:35], s68 offen offset:1024 sc1\n"
"v_mfma_f32_16x16x32_bf16 v[16:19], v[144:147], v[44:47], v[16:19]\n"
"ds_read_b128 v[36:39], v2 offset:16384\n"
"v_mfma_f32_16x16x32_bf16 v[16:19], a[224:227], v[40:43], v[16:19]\n"
"s_waitcnt lgkmcnt(4)\n"
"v_mfma_f32_16x16x32_bf16 v[16:19], a[8:11], v[48:51], v[16:19]\n"
"ds_read_b128 v[40:43], v2 offset:30720\n"
"v_mfma_f32_16x16x32_bf16 v[16:19], a[120:123], v[52:55], v[16:19]\n"
"buffer_store_dwordx4 v[24:27], v2, s[32:35], s68 offen sc1\n"
"s_mov_b32 s67, s68\n"
"v_mfma_f32_16x16x32_bf16 v[20:23], a[124:127], v[52:55], v[20:23]\n"
"ds_read_b128 v[44:47], v2 offset:45056\n"
"v_mfma_f32_16x16x32_bf16 v[20:23], a[12:15], v[48:51], v[20:23]\n"
"s_waitcnt lgkmcnt(4)\n"
"v_mfma_f32_16x16x32_bf16 v[20:23], a[236:239], v[56:59], v[20:23]\n"
"ds_read_b128 v[48:51], v2 offset:3072\n"
"v_mfma_f32_16x16x32_bf16 v[20:23], v[156:159], v[60:63], v[20:23]\n"
"v_mfma_f32_16x16x32_bf16 v[16:19], v[152:155], v[60:63], v[16:19]\n"
"ds_read_b128 v[52:55], v2 offset:17408\n"
"s_waitcnt vmcnt(5)\n"
"v_mfma_f32_16x16x32_bf16 v[16:19], a[232:235], v[56:59], v[16:19]\n"
"v_cmp_ne_u64 vcc, v[8:9], v[6:7]\n"
"s_waitcnt lgkmcnt(4)\n"
"v_mfma_f32_16x16x32_bf16 v[16:19], a[16:19], v[32:35], v[16:19]\n"
"ds_read_b128 v[56:59], v2 offset:31744\n"
"s_cbranch_vccnz .L_rtl0_1\n"
".L_bkl0_1:\n"
"v_mfma_f32_16x16x32_bf16 v[16:19], a[128:131], v[36:39], v[16:19]\n"
"s_add_u32 s66, s66, 1187840\n"
"v_mfma_f32_16x16x32_bf16 v[20:23], a[132:135], v[36:39], v[20:23]\n"
"s_waitcnt vmcnt(2)\n"
"s_barrier\n"
"v_mfma_f32_16x16x32_bf16 v[20:23], a[20:23], v[32:35], v[20:23]\n"
"ds_read_b128 v[60:63], v2 offset:46080\n"
"s_waitcnt lgkmcnt(4)\n"
"v_mfma_f32_16x16x32_bf16 v[20:23], a[244:247], v[40:43], v[20:23]\n"
"ds_read_b128 v[32:35], v2 offset:4096\n"
"s_add_u32 m0, s64, 57344\n"
"v_mfma_f32_16x16x32_bf16 v[20:23], v[164:167], v[44:47], v[20:23]\n"
"buffer_load_dwordx4 v2, s[32:35], s63 offen offset:0 sc1 lds\n"
"v_mfma_f32_16x16x32_bf16 v[16:19], v[160:163], v[44:47], v[16:19]\n"
"ds_read_b128 v[36:39], v2 offset:18432\n"
"v_mfma_f32_16x16x32_bf16 v[16:19], a[240:243], v[40:43], v[16:19]\n"
"s_waitcnt lgkmcnt(4)\n"
"v_mfma_f32_16x16x32_bf16 v[16:19], a[24:27], v[48:51], v[16:19]\n"
"ds_read_b128 v[40:43], v2 offset:32768\n"
"v_mfma_f32_16x16x32_bf16 v[16:19], a[136:139], v[52:55], v[16:19]\n"
"v_mfma_f32_16x16x32_bf16 v[20:23], a[140:143], v[52:55], v[20:23]\n"
"ds_read_b128 v[44:47], v2 offset:47104\n"
"v_mfma_f32_16x16x32_bf16 v[20:23], a[28:31], v[48:51], v[20:23]\n"
"s_waitcnt lgkmcnt(4)\n"
"v_mfma_f32_16x16x32_bf16 v[20:23], a[252:255], v[56:59], v[20:23]\n"
"ds_read_b128 v[48:51], v2 offset:5120\n"
"v_mfma_f32_16x16x32_bf16 v[20:23], v[172:175], v[60:63], v[20:23]\n"
"buffer_load_dwordx4 v2, s[32:35], s63 offen offset:1024 sc1 lds\n"
"v_mfma_f32_16x16x32_bf16 v[16:19], v[168:171], v[60:63], v[16:19]\n"
"ds_read_b128 v[52:55], v2 offset:19456\n"
"v_mfma_f32_16x16x32_bf16 v[16:19], a[248:251], v[56:59], v[16:19]\n"
"s_waitcnt lgkmcnt(4)\n"
"v_mfma_f32_16x16x32_bf16 v[16:19], a[32:35], v[32:35], v[16:19]\n"
"ds_read_b128 v[56:59], v2 offset:33792\n"
"v_mfma_f32_16x16x32_bf16 v[16:19], a[144:147], v[36:39], v[16:19]\n"
"buffer_load_dwordx4 v2, s[32:35], s63 offen offset:2048 sc1 lds\n"
"v_mfma_f32_16x16x32_bf16 v[20:23], a[148:151], v[36:39], v[20:23]\n"
"ds_read_b128 v[60:63], v2 offset:48128\n"
"v_mfma_f32_16x16x32_bf16 v[20:23], a[36:39], v[32:35], v[20:23]\n"
"s_waitcnt lgkmcnt(4)\n"
"v_mfma_f32_16x16x32_bf16 v[20:23], v[68:71], v[40:43], v[20:23]\n"
"ds_read_b128 v[32:35], v2 offset:6144\n"
"v_mfma_f32_16x16x32_bf16 v[20:23], v[180:183], v[44:47], v[20:23]\n"
"buffer_load_dwordx4 v2, s[32:35], s63 offen offset:3072 sc1 lds\n"
"v_mfma_f32_16x16x32_bf16 v[16:19], v[176:179], v[44:47], v[16:19]\n"
"ds_read_b128 v[36:39], v2 offset:20480\n"
"v_mfma_f32_16x16x32_bf16 v[16:19], v[64:67], v[40:43], v[16:19]\n"
"s_waitcnt lgkmcnt(4)\n"
"v_mfma_f32_16x16x32_bf16 v[16:19], a[40:43], v[48:51], v[16:19]\n"
"ds_read_b128 v[40:43], v2 offset:34816\n"
"v_mfma_f32_16x16x32_bf16 v[16:19], a[152:155], v[52:55], v[16:19]\n"
"buffer_load_dwordx4 v[12:15], v2, s[32:35], s71 offen sc1\n"
"s_add_u32 s71, s71, 1187840\n"
"v_mfma_f32_16x16x32_bf16 v[20:23], a[156:159], v[52:55], v[20:23]\n"
"ds_read_b128 v[44:47], v2 offset:49152\n"
"s_add_u32 s63, s63, 4096\n"
"v_mfma_f32_16x16x32_bf16 v[20:23], a[44:47], v[48:51], v[20:23]\n"
"s_add_u32 m0, s64, 61440\n"
"s_waitcnt lgkmcnt(4)\n"
"v_mfma_f32_16x16x32_bf16 v[20:23], v[76:79], v[56:59], v[20:23]\n"
"ds_read_b128 v[48:51], v2 offset:7168\n"
"v_mfma_f32_16x16x32_bf16 v[20:23], v[188:191], v[60:63], v[20:23]\n"
"buffer_load_dwordx4 v2, s[32:35], s63 offen offset:0 sc1 lds\n"
"v_mfma_f32_16x16x32_bf16 v[16:19], v[184:187], v[60:63], v[16:19]\n"
"ds_read_b128 v[52:55], v2 offset:21504\n"
"v_mfma_f32_16x16x32_bf16 v[16:19], v[72:75], v[56:59], v[16:19]\n"
"s_waitcnt lgkmcnt(4)\n"
"v_mfma_f32_16x16x32_bf16 v[16:19], a[48:51], v[32:35], v[16:19]\n"
"ds_read_b128 v[56:59], v2 offset:35840\n"
"v_mfma_f32_16x16x32_bf16 v[16:19], a[160:163], v[36:39], v[16:19]\n"
"buffer_load_dwordx4 v2, s[32:35], s63 offen offset:1024 sc1 lds\n"
"v_mfma_f32_16x16x32_bf16 v[20:23], a[164:167], v[36:39], v[20:23]\n"
"ds_read_b128 v[60:63], v2 offset:50176\n"
"v_mfma_f32_16x16x32_bf16 v[20:23], a[52:55], v[32:35], v[20:23]\n"
"s_waitcnt lgkmcnt(4)\n"
"v_mfma_f32_16x16x32_bf16 v[20:23], v[84:87], v[40:43], v[20:23]\n"
"ds_read_b128 v[32:35], v2 offset:8192\n"
"v_mfma_f32_16x16x32_bf16 v[20:23], v[196:199], v[44:47], v[20:23]\n"
"buffer_load_dwordx4 v2, s[32:35], s63 offen offset:2048 sc1 lds\n"
"v_mfma_f32_16x16x32_bf16 v[16:19], v[192:195], v[44:47], v[16:19]\n"
"ds_read_b128 v[36:39], v2 offset:22528\n"
"v_mfma_f32_16x16x32_bf16 v[16:19], v[80:83], v[40:43], v[16:19]\n"
"s_waitcnt lgkmcnt(4)\n"
"v_mfma_f32_16x16x32_bf16 v[16:19], a[56:59], v[48:51], v[16:19]\n"
"ds_read_b128 v[40:43], v2 offset:36864\n"
"v_mfma_f32_16x16x32_bf16 v[16:19], a[168:171], v[52:55], v[16:19]\n"
"buffer_load_dwordx4 v2, s[32:35], s63 offen offset:3072 sc1 lds\n"
"v_mfma_f32_16x16x32_bf16 v[20:23], a[172:175], v[52:55], v[20:23]\n"
"ds_read_b128 v[44:47], v2 offset:51200\n"
"v_mfma_f32_16x16x32_bf16 v[20:23], a[60:63], v[48:51], v[20:23]\n"
"s_waitcnt lgkmcnt(4)\n"
"v_mfma_f32_16x16x32_bf16 v[20:23], v[92:95], v[56:59], v[20:23]\n"
"ds_read_b128 v[48:51], v2 offset:9216\n"
"v_mfma_f32_16x16x32_bf16 v[20:23], v[204:207], v[60:63], v[20:23]\n"
"s_add_u32 s63, s63, 4096\n"
"s_add_u32 m0, s64, 65536\n"
"v_mfma_f32_16x16x32_bf16 v[16:19], v[200:203], v[60:63], v[16:19]\n"
"ds_read_b128 v[52:55], v2 offset:23552\n"
"v_mfma_f32_16x16x32_bf16 v[16:19], v[88:91], v[56:59], v[16:19]\n"
"s_waitcnt lgkmcnt(4)\n"
"v_mfma_f32_16x16x32_bf16 v[16:19], a[64:67], v[32:35], v[16:19]\n"
"ds_read_b128 v[56:59], v2 offset:37888\n"
"v_mfma_f32_16x16x32_bf16 v[16:19], a[176:179], v[36:39], v[16:19]\n"
"buffer_load_dwordx4 v2, s[32:35], s63 offen offset:0 sc1 lds\n"
"v_mfma_f32_16x16x32_bf16 v[20:23], a[180:183], v[36:39], v[20:23]\n"
"ds_read_b128 v[60:63], v2 offset:52224\n"
"v_mfma_f32_16x16x32_bf16 v[20:23], a[68:71], v[32:35], v[20:23]\n"
"s_waitcnt lgkmcnt(4)\n"
"v_mfma_f32_16x16x32_bf16 v[20:23], v[100:103], v[40:43], v[20:23]\n"
"ds_read_b128 v[32:35], v2 offset:10240\n"
"v_mfma_f32_16x16x32_bf16 v[20:23], v[212:215], v[44:47], v[20:23]\n"
"buffer_load_dwordx4 v2, s[32:35], s63 offen offset:1024 sc1 lds\n"
"v_mfma_f32_16x16x32_bf16 v[16:19], v[208:211], v[44:47], v[16:19]\n"
"ds_read_b128 v[36:39], v2 offset:24576\n"
"v_mfma_f32_16x16x32_bf16 v[16:19], v[96:99], v[40:43], v[16:19]\n"
"s_waitcnt lgkmcnt(4)\n"
"v_mfma_f32_16x16x32_bf16 v[16:19], a[72:75], v[48:51], v[16:19]\n"
"ds_read_b128 v[40:43], v2 offset:38912\n"
"v_mfma_f32_16x16x32_bf16 v[16:19], a[184:187], v[52:55], v[16:19]\n"
"buffer_load_dwordx4 v2, s[32:35], s63 offen offset:2048 sc1 lds\n"
"v_mfma_f32_16x16x32_bf16 v[20:23], a[188:191], v[52:55], v[20:23]\n"
"ds_read_b128 v[44:47], v2 offset:53248\n"
"v_mfma_f32_16x16x32_bf16 v[20:23], a[76:79], v[48:51], v[20:23]\n"
"s_waitcnt lgkmcnt(4)\n"
"v_mfma_f32_16x16x32_bf16 v[20:23], v[108:111], v[56:59], v[20:23]\n"
"ds_read_b128 v[48:51], v2 offset:11264\n"
"v_mfma_f32_16x16x32_bf16 v[20:23], v[220:223], v[60:63], v[20:23]\n"
"buffer_load_dwordx2 v[6:7], v0, s[32:35], s66 offen sc1\n"
"v_mfma_f32_16x16x32_bf16 v[16:19], v[216:219], v[60:63], v[16:19]\n"
"ds_read_b128 v[52:55], v2 offset:25600\n"
"v_mfma_f32_16x16x32_bf16 v[16:19], v[104:107], v[56:59], v[16:19]\n"
"s_waitcnt lgkmcnt(4)\n"
"v_mfma_f32_16x16x32_bf16 v[16:19], a[80:83], v[32:35], v[16:19]\n"
"ds_read_b128 v[56:59], v2 offset:39936\n"
"v_mfma_f32_16x16x32_bf16 v[16:19], a[192:195], v[36:39], v[16:19]\n"
"buffer_load_dwordx4 v2, s[32:35], s63 offen offset:3072 sc1 lds\n"
"s_add_u32 s63, s63, 4096\n"
"v_mfma_f32_16x16x32_bf16 v[20:23], a[196:199], v[36:39], v[20:23]\n"
"ds_read_b128 v[60:63], v2 offset:54272\n"
"s_add_u32 m0, s64, 69632\n"
"v_mfma_f32_16x16x32_bf16 v[20:23], a[84:87], v[32:35], v[20:23]\n"
"s_waitcnt lgkmcnt(4)\n"
"v_mfma_f32_16x16x32_bf16 v[20:23], v[116:119], v[40:43], v[20:23]\n"
"ds_read_b128 v[32:35], v2 offset:12288\n"
"v_mfma_f32_16x16x32_bf16 v[20:23], v[228:231], v[44:47], v[20:23]\n"
"buffer_load_dwordx4 v2, s[32:35], s63 offen offset:0 sc1 lds\n"
"v_mfma_f32_16x16x32_bf16 v[16:19], v[224:227], v[44:47], v[16:19]\n"
"ds_read_b128 v[36:39], v2 offset:26624\n"
"v_mfma_f32_16x16x32_bf16 v[16:19], v[112:115], v[40:43], v[16:19]\n"
"s_waitcnt lgkmcnt(4)\n"
"v_mfma_f32_16x16x32_bf16 v[16:19], a[88:91], v[48:51], v[16:19]\n"
"ds_read_b128 v[40:43], v2 offset:40960\n"
"v_mfma_f32_16x16x32_bf16 v[16:19], a[200:203], v[52:55], v[16:19]\n"
"buffer_load_dwordx4 v2, s[32:35], s63 offen offset:1024 sc1 lds\n"
"s_add_u32 s63, s63, 1175552\n"
"v_mfma_f32_16x16x32_bf16 v[20:23], a[204:207], v[52:55], v[20:23]\n"
"ds_read_b128 v[44:47], v2 offset:55296\n"
"v_mfma_f32_16x16x32_bf16 v[20:23], a[92:95], v[48:51], v[20:23]\n"
"s_waitcnt lgkmcnt(4)\n"
"v_mfma_f32_16x16x32_bf16 v[20:23], v[124:127], v[56:59], v[20:23]\n"
"ds_read_b128 v[48:51], v2 offset:13312\n"
"s_waitcnt vmcnt(11)\n"
"v_mfma_f32_16x16x32_bf16 v[20:23], v[236:239], v[60:63], v[20:23]\n"
"v_lshlrev_b32 v24, 16, v12\n"
"v_and_b32 v25, 0xffff0000, v12\n"
"v_mfma_f32_16x16x32_bf16 v[16:19], v[232:235], v[60:63], v[16:19]\n"
"ds_read_b128 v[52:55], v2 offset:27648\n"
"v_lshlrev_b32 v26, 16, v13\n"
"v_mfma_f32_16x16x32_bf16 v[16:19], v[120:123], v[56:59], v[16:19]\n"
"v_and_b32 v27, 0xffff0000, v13\n"
"s_waitcnt lgkmcnt(4)\n"
"v_mfma_f32_16x16x32_bf16 v[16:19], a[96:99], v[32:35], v[16:19]\n"
"ds_read_b128 v[56:59], v2 offset:41984\n"
"v_lshlrev_b32 v28, 16, v14\n"
"v_mfma_f32_16x16x32_bf16 v[16:19], a[208:211], v[36:39], v[16:19]\n"
"v_and_b32 v29, 0xffff0000, v14\n"
"v_lshlrev_b32 v30, 16, v15\n"
"v_mfma_f32_16x16x32_bf16 v[20:23], a[212:215], v[36:39], v[20:23]\n"
"s_barrier\n"
"v_mfma_f32_16x16x32_bf16 v[20:23], a[100:103], v[32:35], v[20:23]\n"
"ds_read_b128 v[60:63], v2 offset:56320\n"
"s_waitcnt lgkmcnt(4)\n"
"v_mfma_f32_16x16x32_bf16 v[20:23], v[132:135], v[40:43], v[20:23]\n"
"ds_read_b128 v[32:35], v4 offset:0\n"
"v_and_b32 v31, 0xffff0000, v15\n"
"v_mfma_f32_16x16x32_bf16 v[20:23], v[244:247], v[44:47], v[20:23]\n"
"v_mfma_f32_16x16x32_bf16 v[16:19], v[240:243], v[44:47], v[16:19]\n"
"ds_read_b128 v[36:39], v4 offset:14336\n"
"v_mfma_f32_16x16x32_bf16 v[16:19], v[128:131], v[40:43], v[16:19]\n"
"s_waitcnt lgkmcnt(4)\n"
"v_mfma_f32_16x16x32_bf16 v[16:19], a[104:107], v[48:51], v[16:19]\n"
"ds_read_b128 v[40:43], v4 offset:28672\n"
"v_mfma_f32_16x16x32_bf16 v[16:19], a[216:219], v[52:55], v[16:19]\n"
"v_mfma_f32_16x16x32_bf16 v[20:23], a[220:223], v[52:55], v[20:23]\n"
"ds_read_b128 v[44:47], v4 offset:43008\n"
"v_mfma_f32_16x16x32_bf16 v[20:23], a[108:111], v[48:51], v[20:23]\n"
"s_waitcnt lgkmcnt(4)\n"
"v_mfma_f32_16x16x32_bf16 v[20:23], v[140:143], v[56:59], v[20:23]\n"
"ds_read_b128 v[48:51], v4 offset:1024\n"
"v_mfma_f32_16x16x32_bf16 v[20:23], v[252:255], v[60:63], v[20:23]\n"
"v_mfma_f32_16x16x32_bf16 v[16:19], v[248:251], v[60:63], v[16:19]\n"
"ds_read_b128 v[52:55], v4 offset:15360\n"
"s_sub_u32 s62, s62, 1\n"
"v_mfma_f32_16x16x32_bf16 v[16:19], v[136:139], v[56:59], v[16:19]\n"
"s_cbranch_scc1 .L_dlb_1\n"
"s_waitcnt lgkmcnt(4)\n"
"v_mfma_f32_16x16x32_bf16 v[24:27], a[0:3], v[32:35], v[24:27]\n"
"ds_read_b128 v[56:59], v4 offset:29696\n"
"v_mfma_f32_16x16x32_bf16 v[24:27], a[112:115], v[36:39], v[24:27]\n"
"v_mfma_f32_16x16x32_bf16 v[28:31], a[116:119], v[36:39], v[28:31]\n"
"ds_read_b128 v[60:63], v4 offset:44032\n"
"v_mfma_f32_16x16x32_bf16 v[28:31], a[4:7], v[32:35], v[28:31]\n"
"s_add_u32 s68, s68, 1187840\n"
"s_waitcnt lgkmcnt(4)\n"
"v_mfma_f32_16x16x32_bf16 v[28:31], a[228:231], v[40:43], v[28:31]\n"
"ds_read_b128 v[32:35], v4 offset:2048\n"
"v_mfma_f32_16x16x32_bf16 v[28:31], v[148:151], v[44:47], v[28:31]\n"
"buffer_store_dwordx4 v[20:23], v2, s[32:35], s68 offen offset:1024 sc1\n"
"v_mfma_f32_16x16x32_bf16 v[24:27], v[144:147], v[44:47], v[24:27]\n"
"ds_read_b128 v[36:39], v4 offset:16384\n"
"v_mfma_f32_16x16x32_bf16 v[24:27], a[224:227], v[40:43], v[24:27]\n"
"s_waitcnt lgkmcnt(4)\n"
"v_mfma_f32_16x16x32_bf16 v[24:27], a[8:11], v[48:51], v[24:27]\n"
"ds_read_b128 v[40:43], v4 offset:30720\n"
"v_mfma_f32_16x16x32_bf16 v[24:27], a[120:123], v[52:55], v[24:27]\n"
"buffer_store_dwordx4 v[16:19], v2, s[32:35], s68 offen sc1\n"
"s_mov_b32 s67, s68\n"
"v_mfma_f32_16x16x32_bf16 v[28:31], a[124:127], v[52:55], v[28:31]\n"
"ds_read_b128 v[44:47], v4 offset:45056\n"
"v_mfma_f32_16x16x32_bf16 v[28:31], a[12:15], v[48:51], v[28:31]\n"
"s_waitcnt lgkmcnt(4)\n"
"v_mfma_f32_16x16x32_bf16 v[28:31], a[236:239], v[56:59], v[28:31]\n"
"ds_read_b128 v[48:51], v4 offset:3072\n"
"v_mfma_f32_16x16x32_bf16 v[28:31], v[156:159], v[60:63], v[28:31]\n"
"v_mfma_f32_16x16x32_bf16 v[24:27], v[152:155], v[60:63], v[24:27]\n"
"ds_read_b128 v[52:55], v4 offset:17408\n"
"s_waitcnt vmcnt(5)\n"
"v_mfma_f32_16x16x32_bf16 v[24:27], a[232:235], v[56:59], v[24:27]\n"
"v_cmp_ne_u64 vcc, v[8:9], v[6:7]\n"
"s_waitcnt lgkmcnt(4)\n"
"v_mfma_f32_16x16x32_bf16 v[24:27], a[16:19], v[32:35], v[24:27]\n"
"ds_read_b128 v[56:59], v4 offset:31744\n"
"s_cbranch_vccnz .L_rtl1_1\n"
".L_bkl1_1:\n"
"v_mfma_f32_16x16x32_bf16 v[24:27], a[128:131], v[36:39], v[24:27]\n"
"s_add_u32 s66, s66, 1187840\n"
"v_mfma_f32_16x16x32_bf16 v[28:31], a[132:135], v[36:39], v[28:31]\n"
"s_waitcnt vmcnt(2)\n"
"s_barrier\n"
"v_mfma_f32_16x16x32_bf16 v[28:31], a[20:23], v[32:35], v[28:31]\n"
"ds_read_b128 v[60:63], v4 offset:46080\n"
"s_waitcnt lgkmcnt(4)\n"
"v_mfma_f32_16x16x32_bf16 v[28:31], a[244:247], v[40:43], v[28:31]\n"
"ds_read_b128 v[32:35], v4 offset:4096\n"
"s_add_u32 m0, s64, 0\n"
"v_mfma_f32_16x16x32_bf16 v[28:31], v[164:167], v[44:47], v[28:31]\n"
"buffer_load_dwordx4 v2, s[32:35], s63 offen offset:0 sc1 lds\n"
"v_mfma_f32_16x16x32_bf16 v[24:27], v[160:163], v[44:47], v[24:27]\n"
"ds_read_b128 v[36:39], v4 offset:18432\n"
"v_mfma_f32_16x16x32_bf16 v[24:27], a[240:243], v[40:43], v[24:27]\n"
"s_waitcnt lgkmcnt(4)\n"
"v_mfma_f32_16x16x32_bf16 v[24:27], a[24:27], v[48:51], v[24:27]\n"
"ds_read_b128 v[40:43], v4 offset:32768\n"
"v_mfma_f32_16x16x32_bf16 v[24:27], a[136:139], v[52:55], v[24:27]\n"
"v_mfma_f32_16x16x32_bf16 v[28:31], a[140:143], v[52:55], v[28:31]\n"
"ds_read_b128 v[44:47], v4 offset:47104\n"
"v_mfma_f32_16x16x32_bf16 v[28:31], a[28:31], v[48:51], v[28:31]\n"
"s_waitcnt lgkmcnt(4)\n"
"v_mfma_f32_16x16x32_bf16 v[28:31], a[252:255], v[56:59], v[28:31]\n"
"ds_read_b128 v[48:51], v4 offset:5120\n"
"v_mfma_f32_16x16x32_bf16 v[28:31], v[172:175], v[60:63], v[28:31]\n"
"buffer_load_dwordx4 v2, s[32:35], s63 offen offset:1024 sc1 lds\n"
"v_mfma_f32_16x16x32_bf16 v[24:27], v[168:171], v[60:63], v[24:27]\n"
"ds_read_b128 v[52:55], v4 offset:19456\n"
"v_mfma_f32_16x16x32_bf16 v[24:27], a[248:251], v[56:59], v[24:27]\n"
"s_waitcnt lgkmcnt(4)\n"
"v_mfma_f32_16x16x32_bf16 v[24:27], a[32:35], v[32:35], v[24:27]\n"
"ds_read_b128 v[56:59], v4 offset:33792\n"
"v_mfma_f32_16x16x32_bf16 v[24:27], a[144:147], v[36:39], v[24:27]\n"
"buffer_load_dwordx4 v2, s[32:35], s63 offen offset:2048 sc1 lds\n"
"v_mfma_f32_16x16x32_bf16 v[28:31], a[148:151], v[36:39], v[28:31]\n"
"ds_read_b128 v[60:63], v4 offset:48128\n"
"v_mfma_f32_16x16x32_bf16 v[28:31], a[36:39], v[32:35], v[28:31]\n"
"s_waitcnt lgkmcnt(4)\n"
"v_mfma_f32_16x16x32_bf16 v[28:31], v[68:71], v[40:43], v[28:31]\n"
"ds_read_b128 v[32:35], v4 offset:6144\n"
"v_mfma_f32_16x16x32_bf16 v[28:31], v[180:183], v[44:47], v[28:31]\n"
"buffer_load_dwordx4 v2, s[32:35], s63 offen offset:3072 sc1 lds\n"
"v_mfma_f32_16x16x32_bf16 v[24:27], v[176:179], v[44:47], v[24:27]\n"
"ds_read_b128 v[36:39], v4 offset:20480\n"
"v_mfma_f32_16x16x32_bf16 v[24:27], v[64:67], v[40:43], v[24:27]\n"
"s_waitcnt lgkmcnt(4)\n"
"v_mfma_f32_16x16x32_bf16 v[24:27], a[40:43], v[48:51], v[24:27]\n"
"ds_read_b128 v[40:43], v4 offset:34816\n"
"v_mfma_f32_16x16x32_bf16 v[24:27], a[152:155], v[52:55], v[24:27]\n"
"buffer_load_dwordx4 v[12:15], v2, s[32:35], s71 offen sc1\n"
"s_add_u32 s71, s71, 1187840\n"
"v_mfma_f32_16x16x32_bf16 v[28:31], a[156:159], v[52:55], v[28:31]\n"
"ds_read_b128 v[44:47], v4 offset:49152\n"
"s_add_u32 s63, s63, 4096\n"
"v_mfma_f32_16x16x32_bf16 v[28:31], a[44:47], v[48:51], v[28:31]\n"
"s_add_u32 m0, s64, 4096\n"
"s_waitcnt lgkmcnt(4)\n"
"v_mfma_f32_16x16x32_bf16 v[28:31], v[76:79], v[56:59], v[28:31]\n"
"ds_read_b128 v[48:51], v4 offset:7168\n"
"v_mfma_f32_16x16x32_bf16 v[28:31], v[188:191], v[60:63], v[28:31]\n"
"buffer_load_dwordx4 v2, s[32:35], s63 offen offset:0 sc1 lds\n"
"v_mfma_f32_16x16x32_bf16 v[24:27], v[184:187], v[60:63], v[24:27]\n"
"ds_read_b128 v[52:55], v4 offset:21504\n"
"v_mfma_f32_16x16x32_bf16 v[24:27], v[72:75], v[56:59], v[24:27]\n"
"s_waitcnt lgkmcnt(4)\n"
"v_mfma_f32_16x16x32_bf16 v[24:27], a[48:51], v[32:35], v[24:27]\n"
"ds_read_b128 v[56:59], v4 offset:35840\n"
"v_mfma_f32_16x16x32_bf16 v[24:27], a[160:163], v[36:39], v[24:27]\n"
"buffer_load_dwordx4 v2, s[32:35], s63 offen offset:1024 sc1 lds\n"
"v_mfma_f32_16x16x32_bf16 v[28:31], a[164:167], v[36:39], v[28:31]\n"
"ds_read_b128 v[60:63], v4 offset:50176\n"
"v_mfma_f32_16x16x32_bf16 v[28:31], a[52:55], v[32:35], v[28:31]\n"
"s_waitcnt lgkmcnt(4)\n"
"v_mfma_f32_16x16x32_bf16 v[28:31], v[84:87], v[40:43], v[28:31]\n"
"ds_read_b128 v[32:35], v4 offset:8192\n"
"v_mfma_f32_16x16x32_bf16 v[28:31], v[196:199], v[44:47], v[28:31]\n"
"buffer_load_dwordx4 v2, s[32:35], s63 offen offset:2048 sc1 lds\n"
"v_mfma_f32_16x16x32_bf16 v[24:27], v[192:195], v[44:47], v[24:27]\n"
"ds_read_b128 v[36:39], v4 offset:22528\n"
"v_mfma_f32_16x16x32_bf16 v[24:27], v[80:83], v[40:43], v[24:27]\n"
"s_waitcnt lgkmcnt(4)\n"
"v_mfma_f32_16x16x32_bf16 v[24:27], a[56:59], v[48:51], v[24:27]\n"
"ds_read_b128 v[40:43], v4 offset:36864\n"
"v_mfma_f32_16x16x32_bf16 v[24:27], a[168:171], v[52:55], v[24:27]\n"
"buffer_load_dwordx4 v2, s[32:35], s63 offen offset:3072 sc1 lds\n"
"v_mfma_f32_16x16x32_bf16 v[28:31], a[172:175], v[52:55], v[28:31]\n"
"ds_read_b128 v[44:47], v4 offset:51200\n"
"v_mfma_f32_16x16x32_bf16 v[28:31], a[60:63], v[48:51], v[28:31]\n"
"s_waitcnt lgkmcnt(4)\n"
"v_mfma_f32_16x16x32_bf16 v[28:31], v[92:95], v[56:59], v[28:31]\n"
"ds_read_b128 v[48:51], v4 offset:9216\n"
"v_mfma_f32_16x16x32_bf16 v[28:31], v[204:207], v[60:63], v[28:31]\n"
"s_add_u32 s63, s63, 4096\n"
"s_add_u32 m0, s64, 8192\n"
"v_mfma_f32_16x16x32_bf16 v[24:27], v[200:203], v[60:63], v[24:27]\n"
"ds_read_b128 v[52:55], v4 offset:23552\n"
"v_mfma_f32_16x16x32_bf16 v[24:27], v[88:91], v[56:59], v[24:27]\n"
"s_waitcnt lgkmcnt(4)\n"
"v_mfma_f32_16x16x32_bf16 v[24:27], a[64:67], v[32:35], v[24:27]\n"
"ds_read_b128 v[56:59], v4 offset:37888\n"
"v_mfma_f32_16x16x32_bf16 v[24:27], a[176:179], v[36:39], v[24:27]\n"
"buffer_load_dwordx4 v2, s[32:35], s63 offen offset:0 sc1 lds\n"
"v_mfma_f32_16x16x32_bf16 v[28:31], a[180:183], v[36:39], v[28:31]\n"
"ds_read_b128 v[60:63], v4 offset:52224\n"
"v_mfma_f32_16x16x32_bf16 v[28:31], a[68:71], v[32:35], v[28:31]\n"
"s_waitcnt lgkmcnt(4)\n"
"v_mfma_f32_16x16x32_bf16 v[28:31], v[100:103], v[40:43], v[28:31]\n"
"ds_read_b128 v[32:35], v4 offset:10240\n"
"v_mfma_f32_16x16x32_bf16 v[28:31], v[212:215], v[44:47], v[28:31]\n"
"buffer_load_dwordx4 v2, s[32:35], s63 offen offset:1024 sc1 lds\n"
"v_mfma_f32_16x16x32_bf16 v[24:27], v[208:211], v[44:47], v[24:27]\n"
"ds_read_b128 v[36:39], v4 offset:24576\n"
"v_mfma_f32_16x16x32_bf16 v[24:27], v[96:99], v[40:43], v[24:27]\n"
"s_waitcnt lgkmcnt(4)\n"
"v_mfma_f32_16x16x32_bf16 v[24:27], a[72:75], v[48:51], v[24:27]\n"
"ds_read_b128 v[40:43], v4 offset:38912\n"
"v_mfma_f32_16x16x32_bf16 v[24:27], a[184:187], v[52:55], v[24:27]\n"
"buffer_load_dwordx4 v2, s[32:35], s63 offen offset:2048 sc1 lds\n"
"v_mfma_f32_16x16x32_bf16 v[28:31], a[188:191], v[52:55], v[28:31]\n"
"ds_read_b128 v[44:47], v4 offset:53248\n"
"v_mfma_f32_16x16x32_bf16 v[28:31], a[76:79], v[48:51], v[28:31]\n"
"s_waitcnt lgkmcnt(4)\n"
"v_mfma_f32_16x16x32_bf16 v[28:31], v[108:111], v[56:59], v[28:31]\n"
"ds_read_b128 v[48:51], v4 offset:11264\n"
"v_mfma_f32_16x16x32_bf16 v[28:31], v[220:223], v[60:63], v[28:31]\n"
"buffer_load_dwordx2 v[6:7], v0, s[32:35], s66 offen sc1\n"
"v_mfma_f32_16x16x32_bf16 v[24:27], v[216:219], v[60:63], v[24:27]\n"
"ds_read_b128 v[52:55], v4 offset:25600\n"
"v_mfma_f32_16x16x32_bf16 v[24:27], v[104:107], v[56:59], v[24:27]\n"
"s_waitcnt lgkmcnt(4)\n"
"v_mfma_f32_16x16x32_bf16 v[24:27], a[80:83], v[32:35], v[24:27]\n"
"ds_read_b128 v[56:59], v4 offset:39936\n"
"v_mfma_f32_16x16x32_bf16 v[24:27], a[192:195], v[36:39], v[24:27]\n"
"buffer_load_dwordx4 v2, s[32:35], s63 offen offset:3072 sc1 lds\n"
"s_add_u32 s63, s63, 4096\n"
"v_mfma_f32_16x16x32_bf16 v[28:31], a[196:199], v[36:39], v[28:31]\n"
"ds_read_b128 v[60:63], v4 offset:54272\n"
"s_add_u32 m0, s64, 12288\n"
"v_mfma_f32_16x16x32_bf16 v[28:31], a[84:87], v[32:35], v[28:31]\n"
"s_waitcnt lgkmcnt(4)\n"
"v_mfma_f32_16x16x32_bf16 v[28:31], v[116:119], v[40:43], v[28:31]\n"
"ds_read_b128 v[32:35], v4 offset:12288\n"
"v_mfma_f32_16x16x32_bf16 v[28:31], v[228:231], v[44:47], v[28:31]\n"
"buffer_load_dwordx4 v2, s[32:35], s63 offen offset:0 sc1 lds\n"
"v_mfma_f32_16x16x32_bf16 v[24:27], v[224:227], v[44:47], v[24:27]\n"
"ds_read_b128 v[36:39], v4 offset:26624\n"
"v_mfma_f32_16x16x32_bf16 v[24:27], v[112:115], v[40:43], v[24:27]\n"
"s_waitcnt lgkmcnt(4)\n"
"v_mfma_f32_16x16x32_bf16 v[24:27], a[88:91], v[48:51], v[24:27]\n"
"ds_read_b128 v[40:43], v4 offset:40960\n"
"v_mfma_f32_16x16x32_bf16 v[24:27], a[200:203], v[52:55], v[24:27]\n"
"buffer_load_dwordx4 v2, s[32:35], s63 offen offset:1024 sc1 lds\n"
"s_add_u32 s63, s63, 1175552\n"
"v_mfma_f32_16x16x32_bf16 v[28:31], a[204:207], v[52:55], v[28:31]\n"
"ds_read_b128 v[44:47], v4 offset:55296\n"
"v_mfma_f32_16x16x32_bf16 v[28:31], a[92:95], v[48:51], v[28:31]\n"
"s_waitcnt lgkmcnt(4)\n"
"v_mfma_f32_16x16x32_bf16 v[28:31], v[124:127], v[56:59], v[28:31]\n"
"ds_read_b128 v[48:51], v4 offset:13312\n"
"s_waitcnt vmcnt(11)\n"
"v_mfma_f32_16x16x32_bf16 v[28:31], v[236:239], v[60:63], v[28:31]\n"
"v_lshlrev_b32 v16, 16, v12\n"
"v_and_b32 v17, 0xffff0000, v12\n"
"v_mfma_f32_16x16x32_bf16 v[24:27], v[232:235], v[60:63], v[24:27]\n"
"ds_read_b128 v[52:55], v4 offset:27648\n"
"v_lshlrev_b32 v18, 16, v13\n"
"v_mfma_f32_16x16x32_bf16 v[24:27], v[120:123], v[56:59], v[24:27]\n"
"v_and_b32 v19, 0xffff0000, v13\n"
"s_waitcnt lgkmcnt(4)\n"
"v_mfma_f32_16x16x32_bf16 v[24:27], a[96:99], v[32:35], v[24:27]\n"
"ds_read_b128 v[56:59], v4 offset:41984\n"
"v_lshlrev_b32 v20, 16, v14\n"
"v_mfma_f32_16x16x32_bf16 v[24:27], a[208:211], v[36:39], v[24:27]\n"
"v_and_b32 v21, 0xffff0000, v14\n"
"v_lshlrev_b32 v22, 16, v15\n"
"v_mfma_f32_16x16x32_bf16 v[28:31], a[212:215], v[36:39], v[28:31]\n"
"s_barrier\n"
"v_mfma_f32_16x16x32_bf16 v[28:31], a[100:103], v[32:35], v[28:31]\n"
"ds_read_b128 v[60:63], v4 offset:56320\n"
"s_waitcnt lgkmcnt(4)\n"
"v_mfma_f32_16x16x32_bf16 v[28:31], v[132:135], v[40:43], v[28:31]\n"
"ds_read_b128 v[32:35], v2 offset:0\n"
"v_and_b32 v23, 0xffff0000, v15\n"
"v_mfma_f32_16x16x32_bf16 v[28:31], v[244:247], v[44:47], v[28:31]\n"
"v_mfma_f32_16x16x32_bf16 v[24:27], v[240:243], v[44:47], v[24:27]\n"
"ds_read_b128 v[36:39], v2 offset:14336\n"
"v_mfma_f32_16x16x32_bf16 v[24:27], v[128:131], v[40:43], v[24:27]\n"
"s_waitcnt lgkmcnt(4)\n"
"v_mfma_f32_16x16x32_bf16 v[24:27], a[104:107], v[48:51], v[24:27]\n"
"ds_read_b128 v[40:43], v2 offset:28672\n"
"v_mfma_f32_16x16x32_bf16 v[24:27], a[216:219], v[52:55], v[24:27]\n"
"v_mfma_f32_16x16x32_bf16 v[28:31], a[220:223], v[52:55], v[28:31]\n"
"ds_read_b128 v[44:47], v2 offset:43008\n"
"v_mfma_f32_16x16x32_bf16 v[28:31], a[108:111], v[48:51], v[28:31]\n"
"s_waitcnt lgkmcnt(4)\n"
"v_mfma_f32_16x16x32_bf16 v[28:31], v[140:143], v[56:59], v[28:31]\n"
"ds_read_b128 v[48:51], v2 offset:1024\n"
"v_mfma_f32_16x16x32_bf16 v[28:31], v[252:255], v[60:63], v[28:31]\n"
"v_mfma_f32_16x16x32_bf16 v[24:27], v[248:251], v[60:63], v[24:27]\n"
"ds_read_b128 v[52:55], v2 offset:15360\n"
"v_mfma_f32_16x16x32_bf16 v[24:27], v[136:139], v[56:59], v[24:27]\n"
"s_branch .L_dll_1\n"
".L_dlb_1:\n"
"s_mov_b32 s62, 15\n"
"s_add_u32 s74, s74, 8192\n"
"v_lshrrev_b32 v5, 8, v2\n"
"v_lshl_add_u32 v5, v5, 4, s74\n"
"v_and_b32 v3, 128, v2\n"
"v_lshl_add_u32 v5, v3, 2, v5\n"
"v_and_b32 v3, 16, v2\n"
"v_lshl_add_u32 v5, v3, 4, v5\n"
"v_and_b32 v3, 96, v2\n"
"v_lshl_add_u32 v5, v3, 1, v5\n"
"v_lshrrev_b32 v3, 2, v2\n"
"v_add_u32 v3, s74, v3\n"
".p2align 6\n"
".L_dml_1:\n"
"s_waitcnt lgkmcnt(4)\n"
"v_mfma_f32_16x16x32_bf16 v[24:27], a[0:3], v[32:35], v[24:27]\n"
"ds_read_b128 v[56:59], v4 offset:29696\n"
"v_mfma_f32_16x16x32_bf16 v[24:27], a[112:115], v[36:39], v[24:27]\n"
"v_mfma_f32_16x16x32_bf16 v[28:31], a[116:119], v[36:39], v[28:31]\n"
"ds_read_b128 v[60:63], v4 offset:44032\n"
"v_mfma_f32_16x16x32_bf16 v[28:31], a[4:7], v[32:35], v[28:31]\n"
"v_cvt_pk_bf16_f32 v15, v22, v23\n"
"s_waitcnt lgkmcnt(4)\n"
"v_mfma_f32_16x16x32_bf16 v[28:31], a[228:231], v[40:43], v[28:31]\n"
"ds_read_b128 v[32:35], v4 offset:2048\n"
"v_cvt_pk_bf16_f32 v14, v20, v21\n"
"v_mfma_f32_16x16x32_bf16 v[28:31], v[148:151], v[44:47], v[28:31]\n"
"v_cvt_pk_bf16_f32 v13, v18, v19\n"
"v_cvt_pk_bf16_f32 v12, v16, v17\n"
"v_mfma_f32_16x16x32_bf16 v[24:27], v[144:147], v[44:47], v[24:27]\n"
"ds_read_b128 v[36:39], v4 offset:16384\n"
"ds_write_b128 v5, v[12:15]\n"
"v_mfma_f32_16x16x32_bf16 v[24:27], a[224:227], v[40:43], v[24:27]\n"
"ds_read2st64_b32 v[12:13], v3 offset0:0 offset1:1\n"
"s_waitcnt lgkmcnt(6)\n"
"v_mfma_f32_16x16x32_bf16 v[24:27], a[8:11], v[48:51], v[24:27]\n"
"ds_read_b128 v[40:43], v4 offset:30720\n"
"ds_read2st64_b32 v[14:15], v3 offset0:2 offset1:3\n"
"v_mfma_f32_16x16x32_bf16 v[24:27], a[120:123], v[52:55], v[24:27]\n"
"v_mfma_f32_16x16x32_bf16 v[28:31], a[124:127], v[52:55], v[28:31]\n"
"ds_read_b128 v[44:47], v4 offset:45056\n"
"v_mfma_f32_16x16x32_bf16 v[28:31], a[12:15], v[48:51], v[28:31]\n"
"s_waitcnt lgkmcnt(7)\n"
"v_mfma_f32_16x16x32_bf16 v[28:31], a[236:239], v[56:59], v[28:31]\n"
"ds_read_b128 v[48:51], v4 offset:3072\n"
"v_mfma_f32_16x16x32_bf16 v[28:31], v[156:159], v[60:63], v[28:31]\n"
"s_waitcnt lgkmcnt(2)\n"
"buffer_atomic_pk_add_bf16 v12, v1, s[44:47], s65 offen offset:0 sc1\n"
"v_mfma_f32_16x16x32_bf16 v[24:27], v[152:155], v[60:63], v[24:27]\n"
"ds_read_b128 v[52:55], v4 offset:17408\n"
"s_waitcnt vmcnt(4)\n"
"v_mfma_f32_16x16x32_bf16 v[24:27], a[232:235], v[56:59], v[24:27]\n"
"buffer_atomic_pk_add_bf16 v13, v1, s[44:47], s65 offen offset:2048 sc1\n"
"v_mfma_f32_16x16x32_bf16 v[24:27], a[16:19], v[32:35], v[24:27]\n"
"ds_read_b128 v[56:59], v4 offset:31744\n"
"v_cmp_ne_u64 vcc, v[8:9], v[6:7]\n"
"v_mfma_f32_16x16x32_bf16 v[24:27], a[128:131], v[36:39], v[24:27]\n"
"s_cbranch_vccnz .L_rtm1_1\n"
".L_bkm1_1:\n"
"s_add_u32 s66, s66, 1187840\n"
"v_mfma_f32_16x16x32_bf16 v[28:31], a[132:135], v[36:39], v[28:31]\n"
"s_waitcnt vmcnt(2)\n"
"s_barrier\n"
"v_mfma_f32_16x16x32_bf16 v[28:31], a[20:23], v[32:35], v[28:31]\n"
"ds_read_b128 v[60:63], v4 offset:46080\n"
"s_waitcnt lgkmcnt(4)\n"
"v_mfma_f32_16x16x32_bf16 v[28:31], a[244:247], v[40:43], v[28:31]\n"
"ds_read_b128 v[32:35], v4 offset:4096\n"
"s_add_u32 m0, s64, 0\n"
"v_mfma_f32_16x16x32_bf16 v[28:31], v[164:167], v[44:47], v[28:31]\n"
"buffer_load_dwordx4 v2, s[32:35], s63 offen offset:0 sc1 lds\n"
"s_add_u32 s65, s65, 16384\n"
"v_mfma_f32_16x16x32_bf16 v[24:27], v[160:163], v[44:47], v[24:27]\n"
"ds_read_b128 v[36:39], v4 offset:18432\n"
"v_mfma_f32_16x16x32_bf16 v[24:27], a[240:243], v[40:43], v[24:27]\n"
"buffer_atomic_pk_add_bf16 v14, v1, s[44:47], s65 offen offset:0 sc1\n"
"s_waitcnt lgkmcnt(4)\n"
"v_mfma_f32_16x16x32_bf16 v[24:27], a[24:27], v[48:51], v[24:27]\n"
"ds_read_b128 v[40:43], v4 offset:32768\n"
"v_mfma_f32_16x16x32_bf16 v[24:27], a[136:139], v[52:55], v[24:27]\n"
"buffer_atomic_pk_add_bf16 v15, v1, s[44:47], s65 offen offset:2048 sc1\n"
"s_add_u32 s65, s65, 16384\n"
"v_mfma_f32_16x16x32_bf16 v[28:31], a[140:143], v[52:55], v[28:31]\n"
"ds_read_b128 v[44:47], v4 offset:47104\n"
"v_mfma_f32_16x16x32_bf16 v[28:31], a[28:31], v[48:51], v[28:31]\n"
"s_waitcnt lgkmcnt(4)\n"
"v_mfma_f32_16x16x32_bf16 v[28:31], a[252:255], v[56:59], v[28:31]\n"
"ds_read_b128 v[48:51], v4 offset:5120\n"
"v_mfma_f32_16x16x32_bf16 v[28:31], v[172:175], v[60:63], v[28:31]\n"
"buffer_load_dwordx4 v2, s[32:35], s63 offen offset:1024 sc1 lds\n"
"v_mfma_f32_16x16x32_bf16 v[24:27], v[168:171], v[60:63], v[24:27]\n"
"ds_read_b128 v[52:55], v4 offset:19456\n"
"v_mfma_f32_16x16x32_bf16 v[24:27], a[248:251], v[56:59], v[24:27]\n"
"s_waitcnt lgkmcnt(4)\n"
"v_mfma_f32_16x16x32_bf16 v[24:27], a[32:35], v[32:35], v[24:27]\n"
"ds_read_b128 v[56:59], v4 offset:33792\n"
"v_mfma_f32_16x16x32_bf16 v[24:27], a[144:147], v[36:39], v[24:27]\n"
"buffer_load_dwordx4 v2, s[32:35], s63 offen offset:2048 sc1 lds\n"
"v_mfma_f32_16x16x32_bf16 v[28:31], a[148:151], v[36:39], v[28:31]\n"
"ds_read_b128 v[60:63], v4 offset:48128\n"
"v_mfma_f32_16x16x32_bf16 v[28:31], a[36:39], v[32:35], v[28:31]\n"
"s_waitcnt lgkmcnt(4)\n"
"v_mfma_f32_16x16x32_bf16 v[28:31], v[68:71], v[40:43], v[28:31]\n"
"ds_read_b128 v[32:35], v4 offset:6144\n"
"v_mfma_f32_16x16x32_bf16 v[28:31], v[180:183], v[44:47], v[28:31]\n"
"buffer_load_dwordx4 v2, s[32:35], s63 offen offset:3072 sc1 lds\n"
"v_mfma_f32_16x16x32_bf16 v[24:27], v[176:179], v[44:47], v[24:27]\n"
"ds_read_b128 v[36:39], v4 offset:20480\n"
"v_mfma_f32_16x16x32_bf16 v[24:27], v[64:67], v[40:43], v[24:27]\n"
"s_waitcnt lgkmcnt(4)\n"
"v_mfma_f32_16x16x32_bf16 v[24:27], a[40:43], v[48:51], v[24:27]\n"
"ds_read_b128 v[40:43], v4 offset:34816\n"
"v_mfma_f32_16x16x32_bf16 v[24:27], a[152:155], v[52:55], v[24:27]\n"
"buffer_load_dwordx4 v[12:15], v2, s[32:35], s71 offen sc1\n"
"s_add_u32 s71, s71, 1187840\n"
"v_mfma_f32_16x16x32_bf16 v[28:31], a[156:159], v[52:55], v[28:31]\n"
"ds_read_b128 v[44:47], v4 offset:49152\n"
"s_add_u32 s63, s63, 4096\n"
"v_mfma_f32_16x16x32_bf16 v[28:31], a[44:47], v[48:51], v[28:31]\n"
"s_add_u32 m0, s64, 4096\n"
"s_waitcnt lgkmcnt(4)\n"
"v_mfma_f32_16x16x32_bf16 v[28:31], v[76:79], v[56:59], v[28:31]\n"
"ds_read_b128 v[48:51], v4 offset:7168\n"
"v_mfma_f32_16x16x32_bf16 v[28:31], v[188:191], v[60:63], v[28:31]\n"
"buffer_load_dwordx4 v2, s[32:35], s63 offen offset:0 sc1 lds\n"
"v_mfma_f32_16x16x32_bf16 v[24:27], v[184:187], v[60:63], v[24:27]\n"
"ds_read_b128 v[52:55], v4 offset:21504\n"
"v_mfma_f32_16x16x32_bf16 v[24:27], v[72:75], v[56:59], v[24:27]\n"
"s_waitcnt lgkmcnt(4)\n"
"v_mfma_f32_16x16x32_bf16 v[24:27], a[48:51], v[32:35], v[24:27]\n"
"ds_read_b128 v[56:59], v4 offset:35840\n"
"v_mfma_f32_16x16x32_bf16 v[24:27], a[160:163], v[36:39], v[24:27]\n"
"buffer_load_dwordx4 v2, s[32:35], s63 offen offset:1024 sc1 lds\n"
"v_mfma_f32_16x16x32_bf16 v[28:31], a[164:167], v[36:39], v[28:31]\n"
"ds_read_b128 v[60:63], v4 offset:50176\n"
"v_mfma_f32_16x16x32_bf16 v[28:31], a[52:55], v[32:35], v[28:31]\n"
"s_waitcnt lgkmcnt(4)\n"
"v_mfma_f32_16x16x32_bf16 v[28:31], v[84:87], v[40:43], v[28:31]\n"
"ds_read_b128 v[32:35], v4 offset:8192\n"
"v_mfma_f32_16x16x32_bf16 v[28:31], v[196:199], v[44:47], v[28:31]\n"
"buffer_load_dwordx4 v2, s[32:35], s63 offen offset:2048 sc1 lds\n"
"v_mfma_f32_16x16x32_bf16 v[24:27], v[192:195], v[44:47], v[24:27]\n"
"ds_read_b128 v[36:39], v4 offset:22528\n"
"v_mfma_f32_16x16x32_bf16 v[24:27], v[80:83], v[40:43], v[24:27]\n"
"s_waitcnt lgkmcnt(4)\n"
"v_mfma_f32_16x16x32_bf16 v[24:27], a[56:59], v[48:51], v[24:27]\n"
"ds_read_b128 v[40:43], v4 offset:36864\n"
"v_mfma_f32_16x16x32_bf16 v[24:27], a[168:171], v[52:55], v[24:27]\n"
"buffer_load_dwordx4 v2, s[32:35], s63 offen offset:3072 sc1 lds\n"
"v_mfma_f32_16x16x32_bf16 v[28:31], a[172:175], v[52:55], v[28:31]\n"
"ds_read_b128 v[44:47], v4 offset:51200\n"
"v_mfma_f32_16x16x32_bf16 v[28:31], a[60:63], v[48:51], v[28:31]\n"
"s_waitcnt lgkmcnt(4)\n"
"v_mfma_f32_16x16x32_bf16 v[28:31], v[92:95], v[56:59], v[28:31]\n"
"ds_read_b128 v[48:51], v4 offset:9216\n"
"v_mfma_f32_16x16x32_bf16 v[28:31], v[204:207], v[60:63], v[28:31]\n"
"s_add_u32 s63, s63, 4096\n"
"s_add_u32 m0, s64, 8192\n"
"v_mfma_f32_16x16x32_bf16 v[24:27], v[200:203], v[60:63], v[24:27]\n"
"ds_read_b128 v[52:55], v4 offset:23552\n"
"v_mfma_f32_16x16x32_bf16 v[24:27], v[88:91], v[56:59], v[24:27]\n"
"s_waitcnt lgkmcnt(4)\n"
"v_mfma_f32_16x16x32_bf16 v[24:27], a[64:67], v[32:35], v[24:27]\n"
"ds_read_b128 v[56:59], v4 offset:37888\n"
"v_mfma_f32_16x16x32_bf16 v[24:27], a[176:179], v[36:39], v[24:27]\n"
"buffer_load_dwordx4 v2, s[32:35], s63 offen offset:0 sc1 lds\n"
"v_mfma_f32_16x16x32_bf16 v[28:31], a[180:183], v[36:39], v[28:31]\n"
"ds_read_b128 v[60:63], v4 offset:52224\n"
"v_mfma_f32_16x16x32_bf16 v[28:31], a[68:71], v[32:35], v[28:31]\n"
"s_waitcnt lgkmcnt(4)\n"
"v_mfma_f32_16x16x32_bf16 v[28:31], v[100:103], v[40:43], v[28:31]\n"
"ds_read_b128 v[32:35], v4 offset:10240\n"
"v_mfma_f32_16x16x32_bf16 v[28:31], v[212:215], v[44:47], v[28:31]\n"
"buffer_load_dwordx4 v2, s[32:35], s63 offen offset:1024 sc1 lds\n"
"v_mfma_f32_16x16x32_bf16 v[24:27], v[208:211], v[44:47], v[24:27]\n"
"ds_read_b128 v[36:39], v4 offset:24576\n"
"v_mfma_f32_16x16x32_bf16 v[24:27], v[96:99], v[40:43], v[24:27]\n"
"s_waitcnt lgkmcnt(4)\n"
"v_mfma_f32_16x16x32_bf16 v[24:27], a[72:75], v[48:51], v[24:27]\n"
"ds_read_b128 v[40:43], v4 offset:38912\n"
"v_mfma_f32_16x16x32_bf16 v[24:27], a[184:187], v[52:55], v[24:27]\n"
"buffer_load_dwordx4 v2, s[32:35], s63 offen offset:2048 sc1 lds\n"
"v_mfma_f32_16x16x32_bf16 v[28:31], a[188:191], v[52:55], v[28:31]\n"
"ds_read_b128 v[44:47], v4 offset:53248\n"
"v_mfma_f32_16x16x32_bf16 v[28:31], a[76:79], v[48:51], v[28:31]\n"
"s_waitcnt lgkmcnt(4)\n"
"v_mfma_f32_16x16x32_bf16 v[28:31], v[108:111], v[56:59], v[28:31]\n"
"ds_read_b128 v[48:51], v4 offset:11264\n"
"v_mfma_f32_16x16x32_bf16 v[28:31], v[220:223], v[60:63], v[28:31]\n"
"buffer_load_dwordx2 v[6:7], v0, s[32:35], s66 offen sc1\n"
"v_mfma_f32_16x16x32_bf16 v[24:27], v[216:219], v[60:63], v[24:27]\n"
"ds_read_b128 v[52:55], v4 offset:25600\n"
"v_mfma_f32_16x16x32_bf16 v[24:27], v[104:107], v[56:59], v[24:27]\n"
"s_waitcnt lgkmcnt(4)\n"
"v_mfma_f32_16x16x32_bf16 v[24:27], a[80:83], v[32:35], v[24:27]\n"
"ds_read_b128 v[56:59], v4 offset:39936\n"
"v_mfma_f32_16x16x32_bf16 v[24:27], a[192:195], v[36:39], v[24:27]\n"
"buffer_load_dwordx4 v2, s[32:35], s63 offen offset:3072 sc1 lds\n"
"s_add_u32 s63, s63, 4096\n"
"v_mfma_f32_16x16x32_bf16 v[28:31], a[196:199], v[36:39], v[28:31]\n"
"ds_read_b128 v[60:63], v4 offset:54272\n"
"s_add_u32 m0, s64, 12288\n"
"v_mfma_f32_16x16x32_bf16 v[28:31], a[84:87], v[32:35], v[28:31]\n"
"s_waitcnt lgkmcnt(4)\n"
"v_mfma_f32_16x16x32_bf16 v[28:31], v[116:119], v[40:43], v[28:31]\n"
"ds_read_b128 v[32:35], v4 offset:12288\n"
"v_mfma_f32_16x16x32_bf16 v[28:31], v[228:231], v[44:47], v[28:31]\n"
"buffer_load_dwordx4 v2, s[32:35], s63 offen offset:0 sc1 lds\n"
"v_mfma_f32_16x16x32_bf16 v[24:27], v[224:227], v[44:47], v[24:27]\n"
"ds_read_b128 v[36:39], v4 offset:26624\n"
"v_mfma_f32_16x16x32_bf16 v[24:27], v[112:115], v[40:43], v[24:27]\n"
"s_waitcnt lgkmcnt(4)\n"
"v_mfma_f32_16x16x32_bf16 v[24:27], a[88:91], v[48:51], v[24:27]\n"
"ds_read_b128 v[40:43], v4 offset:40960\n"
"v_mfma_f32_16x16x32_bf16 v[24:27], a[200:203], v[52:55], v[24:27]\n"
"buffer_load_dwordx4 v2, s[32:35], s63 offen offset:1024 sc1 lds\n"
"s_add_u32 s63, s63, 1175552\n"
"v_mfma_f32_16x16x32_bf16 v[28:31], a[204:207], v[52:55], v[28:31]\n"
"ds_read_b128 v[44:47], v4 offset:55296\n"
"v_mfma_f32_16x16x32_bf16 v[28:31], a[92:95], v[48:51], v[28:31]\n"
"s_waitcnt lgkmcnt(4)\n"
"v_mfma_f32_16x16x32_bf16 v[28:31], v[124:127], v[56:59], v[28:31]\n"
"ds_read_b128 v[48:51], v4 offset:13312\n"
"s_waitcnt vmcnt(11)\n"
"v_mfma_f32_16x16x32_bf16 v[28:31], v[236:239], v[60:63], v[28:31]\n"
"v_lshlrev_b32 v16, 16, v12\n"
"v_and_b32 v17, 0xffff0000, v12\n"
"v_mfma_f32_16x16x32_bf16 v[24:27], v[232:235], v[60:63], v[24:27]\n"
"ds_read_b128 v[52:55], v4 offset:27648\n"
"v_lshlrev_b32 v18, 16, v13\n"
"v_mfma_f32_16x16x32_bf16 v[24:27], v[120:123], v[56:59], v[24:27]\n"
"v_and_b32 v19, 0xffff0000, v13\n"
"s_waitcnt lgkmcnt(4)\n"
"v_mfma_f32_16x16x32_bf16 v[24:27], a[96:99], v[32:35], v[24:27]\n"
"ds_read_b128 v[56:59], v4 offset:41984\n"
"v_lshlrev_b32 v20, 16, v14\n"
"v_mfma_f32_16x16x32_bf16 v[24:27], a[208:211], v[36:39], v[24:27]\n"
"v_and_b32 v21, 0xffff0000, v14\n"
"v_lshlrev_b32 v22, 16, v15\n"
"v_mfma_f32_16x16x32_bf16 v[28:31], a[212:215], v[36:39], v[28:31]\n"
"s_barrier\n"
"v_mfma_f32_16x16x32_bf16 v[28:31], a[100:103], v[32:35], v[28:31]\n"
"ds_read_b128 v[60:63], v4 offset:56320\n"
"s_waitcnt lgkmcnt(4)\n"
"v_mfma_f32_16x16x32_bf16 v[28:31], v[132:135], v[40:43], v[28:31]\n"
"ds_read_b128 v[32:35], v2 offset:0\n"
"v_and_b32 v23, 0xffff0000, v15\n"
"v_mfma_f32_16x16x32_bf16 v[28:31], v[244:247], v[44:47], v[28:31]\n"
"v_mfma_f32_16x16x32_bf16 v[24:27], v[240:243], v[44:47], v[24:27]\n"
"ds_read_b128 v[36:39], v2 offset:14336\n"
"v_mfma_f32_16x16x32_bf16 v[24:27], v[128:131], v[40:43], v[24:27]\n"
"s_waitcnt lgkmcnt(4)\n"
"v_mfma_f32_16x16x32_bf16 v[24:27], a[104:107], v[48:51], v[24:27]\n"
"ds_read_b128 v[40:43], v2 offset:28672\n"
"v_mfma_f32_16x16x32_bf16 v[24:27], a[216:219], v[52:55], v[24:27]\n"
"v_mfma_f32_16x16x32_bf16 v[28:31], a[220:223], v[52:55], v[28:31]\n"
"ds_read_b128 v[44:47], v2 offset:43008\n"
"v_mfma_f32_16x16x32_bf16 v[28:31], a[108:111], v[48:51], v[28:31]\n"
"s_waitcnt lgkmcnt(4)\n"
"v_mfma_f32_16x16x32_bf16 v[28:31], v[140:143], v[56:59], v[28:31]\n"
"ds_read_b128 v[48:51], v2 offset:1024\n"
"v_mfma_f32_16x16x32_bf16 v[28:31], v[252:255], v[60:63], v[28:31]\n"
"v_mfma_f32_16x16x32_bf16 v[24:27], v[248:251], v[60:63], v[24:27]\n"
"ds_read_b128 v[52:55], v2 offset:15360\n"
"s_sub_u32 s62, s62, 1\n"
"v_mfma_f32_16x16x32_bf16 v[24:27], v[136:139], v[56:59], v[24:27]\n"
"s_cbranch_scc1 .L_dmt_1\n"
"s_waitcnt lgkmcnt(4)\n"
"v_mfma_f32_16x16x32_bf16 v[16:19], a[0:3], v[32:35], v[16:19]\n"
"ds_read_b128 v[56:59], v2 offset:29696\n"
"v_mfma_f32_16x16x32_bf16 v[16:19], a[112:115], v[36:39], v[16:19]\n"
"v_mfma_f32_16x16x32_bf16 v[20:23], a[116:119], v[36:39], v[20:23]\n"
"ds_read_b128 v[60:63], v2 offset:44032\n"
"v_mfma_f32_16x16x32_bf16 v[20:23], a[4:7], v[32:35], v[20:23]\n"
"v_cvt_pk_bf16_f32 v15, v30, v31\n"
"s_waitcnt lgkmcnt(4)\n"
"v_mfma_f32_16x16x32_bf16 v[20:23], a[228:231], v[40:43], v[20:23]\n"
"ds_read_b128 v[32:35], v2 offset:2048\n"
"v_cvt_pk_bf16_f32 v14, v28, v29\n"
"v_mfma_f32_16x16x32_bf16 v[20:23], v[148:151], v[44:47], v[20:23]\n"
"v_cvt_pk_bf16_f32 v13, v26, v27\n"
"v_cvt_pk_bf16_f32 v12, v24, v25\n"
"v_mfma_f32_16x16x32_bf16 v[16:19], v[144:147], v[44:47], v[16:19]\n"
"ds_read_b128 v[36:39], v2 offset:16384\n"
"ds_write_b128 v5, v[12:15]\n"
"v_mfma_f32_16x16x32_bf16 v[16:19], a[224:227], v[40:43], v[16:19]\n"
"ds_read2st64_b32 v[12:13], v3 offset0:0 offset1:1\n"
"s_waitcnt lgkmcnt(6)\n"
"v_mfma_f32_16x16x32_bf16 v[16:19], a[8:11], v[48:51], v[16:19]\n"
"ds_read_b128 v[40:43], v2 offset:30720\n"
"ds_read2st64_b32 v[14:15], v3 offset0:2 offset1:3\n"
"v_mfma_f32_16x16x32_bf16 v[16:19], a[120:123], v[52:55], v[16:19]\n"
"v_mfma_f32_16x16x32_bf16 v[20:23], a[124:127], v[52:55], v[20:23]\n"
"ds_read_b128 v[44:47], v2 offset:45056\n"
"v_mfma_f32_16x16x32_bf16 v[20:23], a[12:15], v[48:51], v[20:23]\n"
"s_waitcnt lgkmcnt(7)\n"
"v_mfma_f32_16x16x32_bf16 v[20:23], a[236:239], v[56:59], v[20:23]\n"
"ds_read_b128 v[48:51], v2 offset:3072\n"
"v_mfma_f32_16x16x32_bf16 v[20:23], v[156:159], v[60:63], v[20:23]\n"
"s_waitcnt lgkmcnt(2)\n"
"buffer_atomic_pk_add_bf16 v12, v1, s[44:47], s65 offen offset:0 sc1\n"
"v_mfma_f32_16x16x32_bf16 v[16:19], v[152:155], v[60:63], v[16:19]\n"
"ds_read_b128 v[52:55], v2 offset:17408\n"
"s_waitcnt vmcnt(4)\n"
"v_mfma_f32_16x16x32_bf16 v[16:19], a[232:235], v[56:59], v[16:19]\n"
"buffer_atomic_pk_add_bf16 v13, v1, s[44:47], s65 offen offset:2048 sc1\n"
"v_mfma_f32_16x16x32_bf16 v[16:19], a[16:19], v[32:35], v[16:19]\n"
"ds_read_b128 v[56:59], v2 offset:31744\n"
"v_cmp_ne_u64 vcc, v[8:9], v[6:7]\n"
"v_mfma_f32_16x16x32_bf16 v[16:19], a[128:131], v[36:39], v[16:19]\n"
"s_cbranch_vccnz .L_rtm0_1\n"
".L_bkm0_1:\n"
"s_add_u32 s66, s66, 1187840\n"
"v_mfma_f32_16x16x32_bf16 v[20:23], a[132:135], v[36:39], v[20:23]\n"
"s_waitcnt vmcnt(2)\n"
"s_barrier\n"
"v_mfma_f32_16x16x32_bf16 v[20:23], a[20:23], v[32:35], v[20:23]\n"
"ds_read_b128 v[60:63], v2 offset:46080\n"
"s_waitcnt lgkmcnt(4)\n"
"v_mfma_f32_16x16x32_bf16 v[20:23], a[244:247], v[40:43], v[20:23]\n"
"ds_read_b128 v[32:35], v2 offset:4096\n"
"s_add_u32 m0, s64, 57344\n"
"v_mfma_f32_16x16x32_bf16 v[20:23], v[164:167], v[44:47], v[20:23]\n"
"buffer_load_dwordx4 v2, s[32:35], s63 offen offset:0 sc1 lds\n"
"s_add_u32 s65, s65, 16384\n"
"v_mfma_f32_16x16x32_bf16 v[16:19], v[160:163], v[44:47], v[16:19]\n"
"ds_read_b128 v[36:39], v2 offset:18432\n"
"v_mfma_f32_16x16x32_bf16 v[16:19], a[240:243], v[40:43], v[16:19]\n"
"buffer_atomic_pk_add_bf16 v14, v1, s[44:47], s65 offen offset:0 sc1\n"
"s_waitcnt lgkmcnt(4)\n"
"v_mfma_f32_16x16x32_bf16 v[16:19], a[24:27], v[48:51], v[16:19]\n"
"ds_read_b128 v[40:43], v2 offset:32768\n"
"v_mfma_f32_16x16x32_bf16 v[16:19], a[136:139], v[52:55], v[16:19]\n"
"buffer_atomic_pk_add_bf16 v15, v1, s[44:47], s65 offen offset:2048 sc1\n"
"s_add_u32 s65, s65, 16384\n"
"v_mfma_f32_16x16x32_bf16 v[20:23], a[140:143], v[52:55], v[20:23]\n"
"ds_read_b128 v[44:47], v2 offset:47104\n"
"v_mfma_f32_16x16x32_bf16 v[20:23], a[28:31], v[48:51], v[20:23]\n"
"s_waitcnt lgkmcnt(4)\n"
"v_mfma_f32_16x16x32_bf16 v[20:23], a[252:255], v[56:59], v[20:23]\n"
"ds_read_b128 v[48:51], v2 offset:5120\n"
"v_mfma_f32_16x16x32_bf16 v[20:23], v[172:175], v[60:63], v[20:23]\n"
"buffer_load_dwordx4 v2, s[32:35], s63 offen offset:1024 sc1 lds\n"
"v_mfma_f32_16x16x32_bf16 v[16:19], v[168:171], v[60:63], v[16:19]\n"
"ds_read_b128 v[52:55], v2 offset:19456\n"
"v_mfma_f32_16x16x32_bf16 v[16:19], a[248:251], v[56:59], v[16:19]\n"
"s_waitcnt lgkmcnt(4)\n"
"v_mfma_f32_16x16x32_bf16 v[16:19], a[32:35], v[32:35], v[16:19]\n"
"ds_read_b128 v[56:59], v2 offset:33792\n"
"v_mfma_f32_16x16x32_bf16 v[16:19], a[144:147], v[36:39], v[16:19]\n"
"buffer_load_dwordx4 v2, s[32:35], s63 offen offset:2048 sc1 lds\n"
"v_mfma_f32_16x16x32_bf16 v[20:23], a[148:151], v[36:39], v[20:23]\n"
"ds_read_b128 v[60:63], v2 offset:48128\n"
"v_mfma_f32_16x16x32_bf16 v[20:23], a[36:39], v[32:35], v[20:23]\n"
"s_waitcnt lgkmcnt(4)\n"
"v_mfma_f32_16x16x32_bf16 v[20:23], v[68:71], v[40:43], v[20:23]\n"
"ds_read_b128 v[32:35], v2 offset:6144\n"
"v_mfma_f32_16x16x32_bf16 v[20:23], v[180:183], v[44:47], v[20:23]\n"
"buffer_load_dwordx4 v2, s[32:35], s63 offen offset:3072 sc1 lds\n"
"v_mfma_f32_16x16x32_bf16 v[16:19], v[176:179], v[44:47], v[16:19]\n"
"ds_read_b128 v[36:39], v2 offset:20480\n"
"v_mfma_f32_16x16x32_bf16 v[16:19], v[64:67], v[40:43], v[16:19]\n"
"s_waitcnt lgkmcnt(4)\n"
"v_mfma_f32_16x16x32_bf16 v[16:19], a[40:43], v[48:51], v[16:19]\n"
"ds_read_b128 v[40:43], v2 offset:34816\n"
"v_mfma_f32_16x16x32_bf16 v[16:19], a[152:155], v[52:55], v[16:19]\n"
"buffer_load_dwordx4 v[12:15], v2, s[32:35], s71 offen sc1\n"
"s_add_u32 s71, s71, 1187840\n"
"v_mfma_f32_16x16x32_bf16 v[20:23], a[156:159], v[52:55], v[20:23]\n"
"ds_read_b128 v[44:47], v2 offset:49152\n"
"s_add_u32 s63, s63, 4096\n"
"v_mfma_f32_16x16x32_bf16 v[20:23], a[44:47], v[48:51], v[20:23]\n"
"s_add_u32 m0, s64, 61440\n"
"s_waitcnt lgkmcnt(4)\n"
"v_mfma_f32_16x16x32_bf16 v[20:23], v[76:79], v[56:59], v[20:23]\n"
"ds_read_b128 v[48:51], v2 offset:7168\n"
"v_mfma_f32_16x16x32_bf16 v[20:23], v[188:191], v[60:63], v[20:23]\n"
"buffer_load_dwordx4 v2, s[32:35], s63 offen offset:0 sc1 lds\n"
"v_mfma_f32_16x16x32_bf16 v[16:19], v[184:187], v[60:63], v[16:19]\n"
"ds_read_b128 v[52:55], v2 offset:21504\n"
"v_mfma_f32_16x16x32_bf16 v[16:19], v[72:75], v[56:59], v[16:19]\n"
"s_waitcnt lgkmcnt(4)\n"
"v_mfma_f32_16x16x32_bf16 v[16:19], a[48:51], v[32:35], v[16:19]\n"
"ds_read_b128 v[56:59], v2 offset:35840\n"
"v_mfma_f32_16x16x32_bf16 v[16:19], a[160:163], v[36:39], v[16:19]\n"
"buffer_load_dwordx4 v2, s[32:35], s63 offen offset:1024 sc1 lds\n"
"v_mfma_f32_16x16x32_bf16 v[20:23], a[164:167], v[36:39], v[20:23]\n"
"ds_read_b128 v[60:63], v2 offset:50176\n"
"v_mfma_f32_16x16x32_bf16 v[20:23], a[52:55], v[32:35], v[20:23]\n"
"s_waitcnt lgkmcnt(4)\n"
"v_mfma_f32_16x16x32_bf16 v[20:23], v[84:87], v[40:43], v[20:23]\n"
"ds_read_b128 v[32:35], v2 offset:8192\n"
"v_mfma_f32_16x16x32_bf16 v[20:23], v[196:199], v[44:47], v[20:23]\n"
"buffer_load_dwordx4 v2, s[32:35], s63 offen offset:2048 sc1 lds\n"
"v_mfma_f32_16x16x32_bf16 v[16:19], v[192:195], v[44:47], v[16:19]\n"
"ds_read_b128 v[36:39], v2 offset:22528\n"
"v_mfma_f32_16x16x32_bf16 v[16:19], v[80:83], v[40:43], v[16:19]\n"
"s_waitcnt lgkmcnt(4)\n"
"v_mfma_f32_16x16x32_bf16 v[16:19], a[56:59], v[48:51], v[16:19]\n"
"ds_read_b128 v[40:43], v2 offset:36864\n"
"v_mfma_f32_16x16x32_bf16 v[16:19], a[168:171], v[52:55], v[16:19]\n"
"buffer_load_dwordx4 v2, s[32:35], s63 offen offset:3072 sc1 lds\n"
"v_mfma_f32_16x16x32_bf16 v[20:23], a[172:175], v[52:55], v[20:23]\n"
"ds_read_b128 v[44:47], v2 offset:51200\n"
"v_mfma_f32_16x16x32_bf16 v[20:23], a[60:63], v[48:51], v[20:23]\n"
"s_waitcnt lgkmcnt(4)\n"
"v_mfma_f32_16x16x32_bf16 v[20:23], v[92:95], v[56:59], v[20:23]\n"
"ds_read_b128 v[48:51], v2 offset:9216\n"
"v_mfma_f32_16x16x32_bf16 v[20:23], v[204:207], v[60:63], v[20:23]\n"
"s_add_u32 s63, s63, 4096\n"
"s_add_u32 m0, s64, 65536\n"
"v_mfma_f32_16x16x32_bf16 v[16:19], v[200:203], v[60:63], v[16:19]\n"
"ds_read_b128 v[52:55], v2 offset:23552\n"
"v_mfma_f32_16x16x32_bf16 v[16:19], v[88:91], v[56:59], v[16:19]\n"
"s_waitcnt lgkmcnt(4)\n"
"v_mfma_f32_16x16x32_bf16 v[16:19], a[64:67], v[32:35], v[16:19]\n"
"ds_read_b128 v[56:59], v2 offset:37888\n"
"v_mfma_f32_16x16x32_bf16 v[16:19], a[176:179], v[36:39], v[16:19]\n"
"buffer_load_dwordx4 v2, s[32:35], s63 offen offset:0 sc1 lds\n"
"v_mfma_f32_16x16x32_bf16 v[20:23], a[180:183], v[36:39], v[20:23]\n"
"ds_read_b128 v[60:63], v2 offset:52224\n"
"v_mfma_f32_16x16x32_bf16 v[20:23], a[68:71], v[32:35], v[20:23]\n"
"s_waitcnt lgkmcnt(4)\n"
"v_mfma_f32_16x16x32_bf16 v[20:23], v[100:103], v[40:43], v[20:23]\n"
"ds_read_b128 v[32:35], v2 offset:10240\n"
"v_mfma_f32_16x16x32_bf16 v[20:23], v[212:215], v[44:47], v[20:23]\n"
"buffer_load_dwordx4 v2, s[32:35], s63 offen offset:1024 sc1 lds\n"
"v_mfma_f32_16x16x32_bf16 v[16:19], v[208:211], v[44:47], v[16:19]\n"
"ds_read_b128 v[36:39], v2 offset:24576\n"
"v_mfma_f32_16x16x32_bf16 v[16:19], v[96:99], v[40:43], v[16:19]\n"
"s_waitcnt lgkmcnt(4)\n"
"v_mfma_f32_16x16x32_bf16 v[16:19], a[72:75], v[48:51], v[16:19]\n"
"ds_read_b128 v[40:43], v2 offset:38912\n"
"v_mfma_f32_16x16x32_bf16 v[16:19], a[184:187], v[52:55], v[16:19]\n"
"buffer_load_dwordx4 v2, s[32:35], s63 offen offset:2048 sc1 lds\n"
"v_mfma_f32_16x16x32_bf16 v[20:23], a[188:191], v[52:55], v[20:23]\n"
"ds_read_b128 v[44:47], v2 offset:53248\n"
"v_mfma_f32_16x16x32_bf16 v[20:23], a[76:79], v[48:51], v[20:23]\n"
"s_waitcnt lgkmcnt(4)\n"
"v_mfma_f32_16x16x32_bf16 v[20:23], v[108:111], v[56:59], v[20:23]\n"
"ds_read_b128 v[48:51], v2 offset:11264\n"
"v_mfma_f32_16x16x32_bf16 v[20:23], v[220:223], v[60:63], v[20:23]\n"
"buffer_load_dwordx2 v[6:7], v0, s[32:35], s66 offen sc1\n"
"v_mfma_f32_16x16x32_bf16 v[16:19], v[216:219], v[60:63], v[16:19]\n"
"ds_read_b128 v[52:55], v2 offset:25600\n"
"v_mfma_f32_16x16x32_bf16 v[16:19], v[104:107], v[56:59], v[16:19]\n"
"s_waitcnt lgkmcnt(4)\n"
"v_mfma_f32_16x16x32_bf16 v[16:19], a[80:83], v[32:35], v[16:19]\n"
"ds_read_b128 v[56:59], v2 offset:39936\n"
"v_mfma_f32_16x16x32_bf16 v[16:19], a[192:195], v[36:39], v[16:19]\n"
"buffer_load_dwordx4 v2, s[32:35], s63 offen offset:3072 sc1 lds\n"
"s_add_u32 s63, s63, 4096\n"
"v_mfma_f32_16x16x32_bf16 v[20:23], a[196:199], v[36:39], v[20:23]\n"
"ds_read_b128 v[60:63], v2 offset:54272\n"
"s_add_u32 m0, s64, 69632\n"
"v_mfma_f32_16x16x32_bf16 v[20:23], a[84:87], v[32:35], v[20:23]\n"
"s_waitcnt lgkmcnt(4)\n"
"v_mfma_f32_16x16x32_bf16 v[20:23], v[116:119], v[40:43], v[20:23]\n"
"ds_read_b128 v[32:35], v2 offset:12288\n"
"v_mfma_f32_16x16x32_bf16 v[20:23], v[228:231], v[44:47], v[20:23]\n"
"buffer_load_dwordx4 v2, s[32:35], s63 offen offset:0 sc1 lds\n"
"v_mfma_f32_16x16x32_bf16 v[16:19], v[224:227], v[44:47], v[16:19]\n"
"ds_read_b128 v[36:39], v2 offset:26624\n"
"v_mfma_f32_16x16x32_bf16 v[16:19], v[112:115], v[40:43], v[16:19]\n"
"s_waitcnt lgkmcnt(4)\n"
"v_mfma_f32_16x16x32_bf16 v[16:19], a[88:91], v[48:51], v[16:19]\n"
"ds_read_b128 v[40:43], v2 offset:40960\n"
"v_mfma_f32_16x16x32_bf16 v[16:19], a[200:203], v[52:55], v[16:19]\n"
"buffer_load_dwordx4 v2, s[32:35], s63 offen offset:1024 sc1 lds\n"
"s_add_u32 s63, s63, 1175552\n"
"v_mfma_f32_16x16x32_bf16 v[20:23], a[204:207], v[52:55], v[20:23]\n"
"ds_read_b128 v[44:47], v2 offset:55296\n"
"v_mfma_f32_16x16x32_bf16 v[20:23], a[92:95], v[48:51], v[20:23]\n"
"s_waitcnt lgkmcnt(4)\n"
"v_mfma_f32_16x16x32_bf16 v[20:23], v[124:127], v[56:59], v[20:23]\n"
"ds_read_b128 v[48:51], v2 offset:13312\n"
"s_waitcnt vmcnt(11)\n"
"v_mfma_f32_16x16x32_bf16 v[20:23], v[236:239], v[60:63], v[20:23]\n"
"v_lshlrev_b32 v24, 16, v12\n"
"v_and_b32 v25, 0xffff0000, v12\n"
"v_mfma_f32_16x16x32_bf16 v[16:19], v[232:235], v[60:63], v[16:19]\n"
"ds_read_b128 v[52:55], v2 offset:27648\n"
"v_lshlrev_b32 v26, 16, v13\n"
"v_mfma_f32_16x16x32_bf16 v[16:19], v[120:123], v[56:59], v[16:19]\n"
"v_and_b32 v27, 0xffff0000, v13\n"
"s_waitcnt lgkmcnt(4)\n"
"v_mfma_f32_16x16x32_bf16 v[16:19], a[96:99], v[32:35], v[16:19]\n"
"ds_read_b128 v[56:59], v2 offset:41984\n"
"v_lshlrev_b32 v28, 16, v14\n"
"v_mfma_f32_16x16x32_bf16 v[16:19], a[208:211], v[36:39], v[16:19]\n"
"v_and_b32 v29, 0xffff0000, v14\n"
"v_lshlrev_b32 v30, 16, v15\n"
"v_mfma_f32_16x16x32_bf16 v[20:23], a[212:215], v[36:39], v[20:23]\n"
"s_barrier\n"
"v_mfma_f32_16x16x32_bf16 v[20:23], a[100:103], v[32:35], v[20:23]\n"
"ds_read_b128 v[60:63], v2 offset:56320\n"
"s_waitcnt lgkmcnt(4)\n"
"v_mfma_f32_16x16x32_bf16 v[20:23], v[132:135], v[40:43], v[20:23]\n"
"ds_read_b128 v[32:35], v4 offset:0\n"
"v_and_b32 v31, 0xffff0000, v15\n"
"v_mfma_f32_16x16x32_bf16 v[20:23], v[244:247], v[44:47], v[20:23]\n"
"v_mfma_f32_16x16x32_bf16 v[16:19], v[240:243], v[44:47], v[16:19]\n"
"ds_read_b128 v[36:39], v4 offset:14336\n"
"v_mfma_f32_16x16x32_bf16 v[16:19], v[128:131], v[40:43], v[16:19]\n"
"s_waitcnt lgkmcnt(4)\n"
"v_mfma_f32_16x16x32_bf16 v[16:19], a[104:107], v[48:51], v[16:19]\n"
"ds_read_b128 v[40:43], v4 offset:28672\n"
"v_mfma_f32_16x16x32_bf16 v[16:19], a[216:219], v[52:55], v[16:19]\n"
"v_mfma_f32_16x16x32_bf16 v[20:23], a[220:223], v[52:55], v[20:23]\n"
"ds_read_b128 v[44:47], v4 offset:43008\n"
"v_mfma_f32_16x16x32_bf16 v[20:23], a[108:111], v[48:51], v[20:23]\n"
"s_waitcnt lgkmcnt(4)\n"
"v_mfma_f32_16x16x32_bf16 v[20:23], v[140:143], v[56:59], v[20:23]\n"
"ds_read_b128 v[48:51], v4 offset:1024\n"
"v_mfma_f32_16x16x32_bf16 v[20:23], v[252:255], v[60:63], v[20:23]\n"
"v_mfma_f32_16x16x32_bf16 v[16:19], v[248:251], v[60:63], v[16:19]\n"
"ds_read_b128 v[52:55], v4 offset:15360\n"
"v_mfma_f32_16x16x32_bf16 v[16:19], v[136:139], v[56:59], v[16:19]\n"
"s_branch .L_dml_1\n"
".L_dmt_1:\n"
"s_nop 1\n"
"s_nop 0\n"
"v_cvt_pk_bf16_f32 v15, v30, v31\n"
"v_cvt_pk_bf16_f32 v14, v28, v29\n"
"s_nop 1\n"
"v_cvt_pk_bf16_f32 v13, v26, v27\n"
"v_cvt_pk_bf16_f32 v12, v24, v25\n"
"ds_write_b128 v5, v[12:15]\n"
"ds_read2st64_b32 v[12:13], v3 offset0:0 offset1:1\n"
"ds_read2st64_b32 v[14:15], v3 offset0:2 offset1:3\n"
"s_waitcnt lgkmcnt(0)\n"
"buffer_atomic_pk_add_bf16 v12, v1, s[44:47], s65 offen offset:0 sc1\n"
"buffer_atomic_pk_add_bf16 v13, v1, s[44:47], s65 offen offset:2048 sc1\n"
"s_add_u32 s65, s65, 16384\n"
"buffer_atomic_pk_add_bf16 v14, v1, s[44:47], s65 offen offset:0 sc1\n"
"buffer_atomic_pk_add_bf16 v15, v1, s[44:47], s65 offen offset:2048 sc1\n"
"s_add_u32 s65, s65, 16384\n"
"s_branch .L_end\n"
".p2align 6\n"
".L_dlv2:\n"
".L_dll_2:\n"
"s_waitcnt lgkmcnt(4)\n"
"v_mfma_f32_16x16x32_bf16 v[16:19], a[0:3], v[32:35], v[16:19]\n"
"ds_read_b128 v[56:59], v2 offset:29696\n"
"v_mfma_f32_16x16x32_bf16 v[16:19], a[112:115], v[36:39], v[16:19]\n"
"v_mfma_f32_16x16x32_bf16 v[20:23], a[116:119], v[36:39], v[20:23]\n"
"ds_read_b128 v[60:63], v2 offset:44032\n"
"v_mfma_f32_16x16x32_bf16 v[20:23], a[4:7], v[32:35], v[20:23]\n"
"s_add_u32 s68, s68, 1187840\n"
"s_waitcnt lgkmcnt(4)\n"
"v_mfma_f32_16x16x32_bf16 v[20:23], a[228:231], v[40:43], v[20:23]\n"
"ds_read_b128 v[32:35], v2 offset:2048\n"
"v_mfma_f32_16x16x32_bf16 v[20:23], v[148:151], v[44:47], v[20:23]\n"
"buffer_store_dwordx4 v[28:31], v2, s[32:35], s68 offen offset:1024 sc1\n"
"v_mfma_f32_16x16x32_bf16 v[16:19], v[144:147], v[44:47], v[16:19]\n"
"ds_read_b128 v[36:39], v2 offset:16384\n"
"v_mfma_f32_16x16x32_bf16 v[16:19], a[224:227], v[40:43], v[16:19]\n"
"s_waitcnt lgkmcnt(4)\n"
"v_mfma_f32_16x16x32_bf16 v[16:19], a[8:11], v[48:51], v[16:19]\n"
"ds_read_b128 v[40:43], v2 offset:30720\n"
"v_mfma_f32_16x16x32_bf16 v[16:19], a[120:123], v[52:55], v[16:19]\n"
"buffer_store_dwordx4 v[24:27], v2, s[32:35], s68 offen sc1\n"
"s_mov_b32 s67, s68\n"
"v_mfma_f32_16x16x32_bf16 v[20:23], a[124:127], v[52:55], v[20:23]\n"
"ds_read_b128 v[44:47], v2 offset:45056\n"
"v_mfma_f32_16x16x32_bf16 v[20:23], a[12:15], v[48:51], v[20:23]\n"
"s_waitcnt lgkmcnt(4)\n"
"v_mfma_f32_16x16x32_bf16 v[20:23], a[236:239], v[56:59], v[20:23]\n"
"ds_read_b128 v[48:51], v2 offset:3072\n"
"v_mfma_f32_16x16x32_bf16 v[20:23], v[156:159], v[60:63], v[20:23]\n"
"v_mfma_f32_16x16x32_bf16 v[16:19], v[152:155], v[60:63], v[16:19]\n"
"ds_read_b128 v[52:55], v2 offset:17408\n"
"v_mfma_f32_16x16x32_bf16 v[16:19], a[232:235], v[56:59], v[16:19]\n"
"s_waitcnt lgkmcnt(4)\n"
"v_mfma_f32_16x16x32_bf16 v[16:19], a[16:19], v[32:35], v[16:19]\n"
"ds_read_b128 v[56:59], v2 offset:31744\n"
"v_mfma_f32_16x16x32_bf16 v[16:19], a[128:131], v[36:39], v[16:19]\n"
"s_waitcnt vmcnt(2)\n"
"s_barrier\n"
"v_mfma_f32_16x16x32_bf16 v[20:23], a[132:135], v[36:39], v[20:23]\n"
"ds_read_b128 v[60:63], v2 offset:46080\n"
"v_mfma_f32_16x16x32_bf16 v[20:23], a[20:23], v[32:35], v[20:23]\n"
"s_waitcnt lgkmcnt(4)\n"
"v_mfma_f32_16x16x32_bf16 v[20:23], a[244:247], v[40:43], v[20:23]\n"
"ds_read_b128 v[32:35], v2 offset:4096\n"
"s_add_u32 m0, s64, 57344\n"
"v_mfma_f32_16x16x32_bf16 v[20:23], v[164:167], v[44:47], v[20:23]\n"
"buffer_load_dwordx4 v2, s[32:35], s63 offen offset:0 sc1 lds\n"
"v_mfma_f32_16x16x32_bf16 v[16:19], v[160:163], v[44:47], v[16:19]\n"
"ds_read_b128 v[36:39], v2 offset:18432\n"
"v_mfma_f32_16x16x32_bf16 v[16:19], a[240:243], v[40:43], v[16:19]\n"
"s_waitcnt lgkmcnt(4)\n"
"v_mfma_f32_16x16x32_bf16 v[16:19], a[24:27], v[48:51], v[16:19]\n"
"ds_read_b128 v[40:43], v2 offset:32768\n"
"v_mfma_f32_16x16x32_bf16 v[16:19], a[136:139], v[52:55], v[16:19]\n"
"v_mfma_f32_16x16x32_bf16 v[20:23], a[140:143], v[52:55], v[20:23]\n"
"ds_read_b128 v[44:47], v2 offset:47104\n"
"v_mfma_f32_16x16x32_bf16 v[20:23], a[28:31], v[48:51], v[20:23]\n"
"s_waitcnt lgkmcnt(4)\n"
"v_mfma_f32_16x16x32_bf16 v[20:23], a[252:255], v[56:59], v[20:23]\n"
"ds_read_b128 v[48:51], v2 offset:5120\n"
"v_mfma_f32_16x16x32_bf16 v[20:23], v[172:175], v[60:63], v[20:23]\n"
"buffer_load_dwordx4 v2, s[32:35], s63 offen offset:1024 sc1 lds\n"
"v_mfma_f32_16x16x32_bf16 v[16:19], v[168:171], v[60:63], v[16:19]\n"
"ds_read_b128 v[52:55], v2 offset:19456\n"
"v_mfma_f32_16x16x32_bf16 v[16:19], a[248:251], v[56:59], v[16:19]\n"
"s_waitcnt lgkmcnt(4)\n"
"v_mfma_f32_16x16x32_bf16 v[16:19], a[32:35], v[32:35], v[16:19]\n"
"ds_read_b128 v[56:59], v2 offset:33792\n"
"v_mfma_f32_16x16x32_bf16 v[16:19], a[144:147], v[36:39], v[16:19]\n"
"buffer_load_dwordx4 v2, s[32:35], s63 offen offset:2048 sc1 lds\n"
"v_mfma_f32_16x16x32_bf16 v[20:23], a[148:151], v[36:39], v[20:23]\n"
"ds_read_b128 v[60:63], v2 offset:48128\n"
"v_mfma_f32_16x16x32_bf16 v[20:23], a[36:39], v[32:35], v[20:23]\n"
"s_waitcnt lgkmcnt(4)\n"
"v_mfma_f32_16x16x32_bf16 v[20:23], v[68:71], v[40:43], v[20:23]\n"
"ds_read_b128 v[32:35], v2 offset:6144\n"
"v_mfma_f32_16x16x32_bf16 v[20:23], v[180:183], v[44:47], v[20:23]\n"
"buffer_load_dwordx4 v2, s[32:35], s63 offen offset:3072 sc1 lds\n"
"v_mfma_f32_16x16x32_bf16 v[16:19], v[176:179], v[44:47], v[16:19]\n"
"ds_read_b128 v[36:39], v2 offset:20480\n"
"v_mfma_f32_16x16x32_bf16 v[16:19], v[64:67], v[40:43], v[16:19]\n"
"s_waitcnt lgkmcnt(4)\n"
"v_mfma_f32_16x16x32_bf16 v[16:19], a[40:43], v[48:51], v[16:19]\n"
"ds_read_b128 v[40:43], v2 offset:34816\n"
"v_mfma_f32_16x16x32_bf16 v[16:19], a[152:155], v[52:55], v[16:19]\n"
"buffer_load_dwordx4 v[12:15], v2, s[32:35], s71 offen sc1\n"
"s_add_u32 s71, s71, 1187840\n"
"v_mfma_f32_16x16x32_bf16 v[20:23], a[156:159], v[52:55], v[20:23]\n"
"ds_read_b128 v[44:47], v2 offset:49152\n"
"s_add_u32 s63, s63, 4096\n"
"v_mfma_f32_16x16x32_bf16 v[20:23], a[44:47], v[48:51], v[20:23]\n"
"s_add_u32 m0, s64, 61440\n"
"s_waitcnt lgkmcnt(4)\n"
"v_mfma_f32_16x16x32_bf16 v[20:23], v[76:79], v[56:59], v[20:23]\n"
"ds_read_b128 v[48:51], v2 offset:7168\n"
"v_mfma_f32_16x16x32_bf16 v[20:23], v[188:191], v[60:63], v[20:23]\n"
"buffer_load_dwordx4 v2, s[32:35], s63 offen offset:0 sc1 lds\n"
"v_mfma_f32_16x16x32_bf16 v[16:19], v[184:187], v[60:63], v[16:19]\n"
"ds_read_b128 v[52:55], v2 offset:21504\n"
"v_mfma_f32_16x16x32_bf16 v[16:19], v[72:75], v[56:59], v[16:19]\n"
"s_waitcnt lgkmcnt(4)\n"
"v_mfma_f32_16x16x32_bf16 v[16:19], a[48:51], v[32:35], v[16:19]\n"
"ds_read_b128 v[56:59], v2 offset:35840\n"
"v_mfma_f32_16x16x32_bf16 v[16:19], a[160:163], v[36:39], v[16:19]\n"
"buffer_load_dwordx4 v2, s[32:35], s63 offen offset:1024 sc1 lds\n"
"v_mfma_f32_16x16x32_bf16 v[20:23], a[164:167], v[36:39], v[20:23]\n"
"ds_read_b128 v[60:63], v2 offset:50176\n"
"v_mfma_f32_16x16x32_bf16 v[20:23], a[52:55], v[32:35], v[20:23]\n"
"s_waitcnt lgkmcnt(4)\n"
"v_mfma_f32_16x16x32_bf16 v[20:23], v[84:87], v[40:43], v[20:23]\n"
"ds_read_b128 v[32:35], v2 offset:8192\n"
"v_mfma_f32_16x16x32_bf16 v[20:23], v[196:199], v[44:47], v[20:23]\n"
"buffer_load_dwordx4 v2, s[32:35], s63 offen offset:2048 sc1 lds\n"
"v_mfma_f32_16x16x32_bf16 v[16:19], v[192:195], v[44:47], v[16:19]\n"
"ds_read_b128 v[36:39], v2 offset:22528\n"
"v_mfma_f32_16x16x32_bf16 v[16:19], v[80:83], v[40:43], v[16:19]\n"
"s_waitcnt lgkmcnt(4)\n"
"v_mfma_f32_16x16x32_bf16 v[16:19], a[56:59], v[48:51], v[16:19]\n"
"ds_read_b128 v[40:43], v2 offset:36864\n"
"v_mfma_f32_16x16x32_bf16 v[16:19], a[168:171], v[52:55], v[16:19]\n"
"buffer_load_dwordx4 v2, s[32:35], s63 offen offset:3072 sc1 lds\n"
"v_mfma_f32_16x16x32_bf16 v[20:23], a[172:175], v[52:55], v[20:23]\n"
"ds_read_b128 v[44:47], v2 offset:51200\n"
"v_mfma_f32_16x16x32_bf16 v[20:23], a[60:63], v[48:51], v[20:23]\n"
"s_waitcnt lgkmcnt(4)\n"
"v_mfma_f32_16x16x32_bf16 v[20:23], v[92:95], v[56:59], v[20:23]\n"
"ds_read_b128 v[48:51], v2 offset:9216\n"
"v_mfma_f32_16x16x32_bf16 v[20:23], v[204:207], v[60:63], v[20:23]\n"
"s_add_u32 s63, s63, 4096\n"
"s_add_u32 m0, s64, 65536\n"
"v_mfma_f32_16x16x32_bf16 v[16:19], v[200:203], v[60:63], v[16:19]\n"
"ds_read_b128 v[52:55], v2 offset:23552\n"
"v_mfma_f32_16x16x32_bf16 v[16:19], v[88:91], v[56:59], v[16:19]\n"
"s_waitcnt lgkmcnt(4)\n"
"v_mfma_f32_16x16x32_bf16 v[16:19], a[64:67], v[32:35], v[16:19]\n"
"ds_read_b128 v[56:59], v2 offset:37888\n"
"v_mfma_f32_16x16x32_bf16 v[16:19], a[176:179], v[36:39], v[16:19]\n"
"buffer_load_dwordx4 v2, s[32:35], s63 offen offset:0 sc1 lds\n"
"v_mfma_f32_16x16x32_bf16 v[20:23], a[180:183], v[36:39], v[20:23]\n"
"ds_read_b128 v[60:63], v2 offset:52224\n"
"v_mfma_f32_16x16x32_bf16 v[20:23], a[68:71], v[32:35], v[20:23]\n"
"s_waitcnt lgkmcnt(4)\n"
"v_mfma_f32_16x16x32_bf16 v[20:23], v[100:103], v[40:43], v[20:23]\n"
"ds_read_b128 v[32:35], v2 offset:10240\n"
"v_mfma_f32_16x16x32_bf16 v[20:23], v[212:215], v[44:47], v[20:23]\n"
"buffer_load_dwordx4 v2, s[32:35], s63 offen offset:1024 sc1 lds\n"
"v_mfma_f32_16x16x32_bf16 v[16:19], v[208:211], v[44:47], v[16:19]\n"
"ds_read_b128 v[36:39], v2 offset:24576\n"
"v_mfma_f32_16x16x32_bf16 v[16:19], v[96:99], v[40:43], v[16:19]\n"
"s_waitcnt lgkmcnt(4)\n"
"v_mfma_f32_16x16x32_bf16 v[16:19], a[72:75], v[48:51], v[16:19]\n"
"ds_read_b128 v[40:43], v2 offset:38912\n"
"v_mfma_f32_16x16x32_bf16 v[16:19], a[184:187], v[52:55], v[16:19]\n"
"buffer_load_dwordx4 v2, s[32:35], s63 offen offset:2048 sc1 lds\n"
"v_mfma_f32_16x16x32_bf16 v[20:23], a[188:191], v[52:55], v[20:23]\n"
"ds_read_b128 v[44:47], v2 offset:53248\n"
"v_mfma_f32_16x16x32_bf16 v[20:23], a[76:79], v[48:51], v[20:23]\n"
"s_waitcnt lgkmcnt(4)\n"
"v_mfma_f32_16x16x32_bf16 v[20:23], v[108:111], v[56:59], v[20:23]\n"
"ds_read_b128 v[48:51], v2 offset:11264\n"
"v_mfma_f32_16x16x32_bf16 v[20:23], v[220:223], v[60:63], v[20:23]\n"
"v_mfma_f32_16x16x32_bf16 v[16:19], v[216:219], v[60:63], v[16:19]\n"
"ds_read_b128 v[52:55], v2 offset:25600\n"
"v_mfma_f32_16x16x32_bf16 v[16:19], v[104:107], v[56:59], v[16:19]\n"
"s_waitcnt lgkmcnt(4)\n"
"v_mfma_f32_16x16x32_bf16 v[16:19], a[80:83], v[32:35], v[16:19]\n"
"ds_read_b128 v[56:59], v2 offset:39936\n"
"v_mfma_f32_16x16x32_bf16 v[16:19], a[192:195], v[36:39], v[16:19]\n"
"buffer_load_dwordx4 v2, s[32:35], s63 offen offset:3072 sc1 lds\n"
"s_add_u32 s63, s63, 4096\n"
"v_mfma_f32_16x16x32_bf16 v[20:23], a[196:199], v[36:39], v[20:23]\n"
"ds_read_b128 v[60:63], v2 offset:54272\n"
"s_add_u32 m0, s64, 69632\n"
"v_mfma_f32_16x16x32_bf16 v[20:23], a[84:87], v[32:35], v[20:23]\n"
"s_waitcnt lgkmcnt(4)\n"
"v_mfma_f32_16x16x32_bf16 v[20:23], v[116:119], v[40:43], v[20:23]\n"
"ds_read_b128 v[32:35], v2 offset:12288\n"
"v_mfma_f32_16x16x32_bf16 v[20:23], v[228:231], v[44:47], v[20:23]\n"
"buffer_load_dwordx4 v2, s[32:35], s63 offen offset:0 sc1 lds\n"
"v_mfma_f32_16x16x32_bf16 v[16:19], v[224:227], v[44:47], v[16:19]\n"
"ds_read_b128 v[36:39], v2 offset:26624\n"
"v_mfma_f32_16x16x32_bf16 v[16:19], v[112:115], v[40:43], v[16:19]\n"
"s_waitcnt lgkmcnt(4)\n"
"v_mfma_f32_16x16x32_bf16 v[16:19], a[88:91], v[48:51], v[16:19]\n"
"ds_read_b128 v[40:43], v2 offset:40960\n"
"v_mfma_f32_16x16x32_bf16 v[16:19], a[200:203], v[52:55], v[16:19]\n"
"buffer_load_dwordx4 v2, s[32:35], s63 offen offset:1024 sc1 lds\n"
"s_add_u32 s63, s63, 1175552\n"
"v_mfma_f32_16x16x32_bf16 v[20:23], a[204:207], v[52:55], v[20:23]\n"
"ds_read_b128 v[44:47], v2 offset:55296\n"
"v_mfma_f32_16x16x32_bf16 v[20:23], a[92:95], v[48:51], v[20:23]\n"
"s_waitcnt lgkmcnt(4)\n"
"v_mfma_f32_16x16x32_bf16 v[20:23], v[124:127], v[56:59], v[20:23]\n"
"ds_read_b128 v[48:51], v2 offset:13312\n"
"s_waitcnt vmcnt(10)\n"
"v_mfma_f32_16x16x32_bf16 v[20:23], v[236:239], v[60:63], v[20:23]\n"
"v_lshlrev_b32 v24, 16, v12\n"
"v_and_b32 v25, 0xffff0000, v12\n"
"v_mfma_f32_16x16x32_bf16 v[16:19], v[232:235], v[60:63], v[16:19]\n"
"ds_read_b128 v[52:55], v2 offset:27648\n"
"v_lshlrev_b32 v26, 16, v13\n"
"v_mfma_f32_16x16x32_bf16 v[16:19], v[120:123], v[56:59], v[16:19]\n"
"v_and_b32 v27, 0xffff0000, v13\n"
"s_waitcnt lgkmcnt(4)\n"
"v_mfma_f32_16x16x32_bf16 v[16:19], a[96:99], v[32:35], v[16:19]\n"
"ds_read_b128 v[56:59], v2 offset:41984\n"
"v_lshlrev_b32 v28, 16, v14\n"
"v_mfma_f32_16x16x32_bf16 v[16:19], a[208:211], v[36:39], v[16:19]\n"
"s_barrier\n"
"v_mfma_f32_16x16x32_bf16 v[20:23], a[212:215], v[36:39], v[20:23]\n"
"ds_read_b128 v[60:63], v2 offset:56320\n"
"v_and_b32 v29, 0xffff0000, v14\n"
"v_mfma_f32_16x16x32_bf16 v[20:23], a[100:103], v[32:35], v[20:23]\n"
"v_lshlrev_b32 v30, 16, v15\n"
"s_waitcnt lgkmcnt(4)\n"
"v_mfma_f32_16x16x32_bf16 v[20:23], v[132:135], v[40:43], v[20:23]\n"
"ds_read_b128 v[32:35], v4 offset:0\n"
"v_and_b32 v31, 0xffff0000, v15\n"
"v_mfma_f32_16x16x32_bf16 v[20:23], v[244:247], v[44:47], v[20:23]\n"
"v_mfma_f32_16x16x32_bf16 v[16:19], v[240:243], v[44:47], v[16:19]\n"
"ds_read_b128 v[36:39], v4 offset:14336\n"
"v_mfma_f32_16x16x32_bf16 v[16:19], v[128:131], v[40:43], v[16:19]\n"
"s_waitcnt lgkmcnt(4)\n"
"v_mfma_f32_16x16x32_bf16 v[16:19], a[104:107], v[48:51], v[16:19]\n"
"ds_read_b128 v[40:43], v4 offset:28672\n"
"v_mfma_f32_16x16x32_bf16 v[16:19], a[216:219], v[52:55], v[16:19]\n"
"v_mfma_f32_16x16x32_bf16 v[20:23], a[220:223], v[52:55], v[20:23]\n"
"ds_read_b128 v[44:47], v4 offset:43008\n"
"v_mfma_f32_16x16x32_bf16 v[20:23], a[108:111], v[48:51], v[20:23]\n"
"s_waitcnt lgkmcnt(4)\n"
"v_mfma_f32_16x16x32_bf16 v[20:23], v[140:143], v[56:59], v[20:23]\n"
"ds_read_b128 v[48:51], v4 offset:1024\n"
"v_mfma_f32_16x16x32_bf16 v[20:23], v[252:255], v[60:63], v[20:23]\n"
"v_mfma_f32_16x16x32_bf16 v[16:19], v[248:251], v[60:63], v[16:19]\n"
"ds_read_b128 v[52:55], v4 offset:15360\n"
"s_sub_u32 s62, s62, 1\n"
"v_mfma_f32_16x16x32_bf16 v[16:19], v[136:139], v[56:59], v[16:19]\n"
"s_cbranch_scc1 .L_dlb_2\n"
"s_waitcnt lgkmcnt(4)\n"
"v_mfma_f32_16x16x32_bf16 v[24:27], a[0:3], v[32:35], v[24:27]\n"
"ds_read_b128 v[56:59], v4 offset:29696\n"
"v_mfma_f32_16x16x32_bf16 v[24:27], a[112:115], v[36:39], v[24:27]\n"
"v_mfma_f32_16x16x32_bf16 v[28:31], a[116:119], v[36:39], v[28:31]\n"
"ds_read_b128 v[60:63], v4 offset:44032\n"
"v_mfma_f32_16x16x32_bf16 v[28:31], a[4:7], v[32:35], v[28:31]\n"
"s_add_u32 s68, s68, 1187840\n"
"s_waitcnt lgkmcnt(4)\n"
"v_mfma_f32_16x16x32_bf16 v[28:31], a[228:231], v[40:43], v[28:31]\n"
"ds_read_b128 v[32:35], v4 offset:2048\n"
"v_mfma_f32_16x16x32_bf16 v[28:31], v[148:151], v[44:47], v[28:31]\n"
"buffer_store_dwordx4 v[20:23], v2, s[32:35], s68 offen offset:1024 sc1\n"
"v_mfma_f32_16x16x32_bf16 v[24:27], v[144:147], v[44:47], v[24:27]\n"
"ds_read_b128 v[36:39], v4 offset:16384\n"
"v_mfma_f32_16x16x32_bf16 v[24:27], a[224:227], v[40:43], v[24:27]\n"
"s_waitcnt lgkmcnt(4)\n"
"v_mfma_f32_16x16x32_bf16 v[24:27], a[8:11], v[48:51], v[24:27]\n"
"ds_read_b128 v[40:43], v4 offset:30720\n"
"v_mfma_f32_16x16x32_bf16 v[24:27], a[120:123], v[52:55], v[24:27]\n"
"buffer_store_dwordx4 v[16:19], v2, s[32:35], s68 offen sc1\n"
"s_mov_b32 s67, s68\n"
"v_mfma_f32_16x16x32_bf16 v[28:31], a[124:127], v[52:55], v[28:31]\n"
"ds_read_b128 v[44:47], v4 offset:45056\n"
"v_mfma_f32_16x16x32_bf16 v[28:31], a[12:15], v[48:51], v[28:31]\n"
"s_waitcnt lgkmcnt(4)\n"
"v_mfma_f32_16x16x32_bf16 v[28:31], a[236:239], v[56:59], v[28:31]\n"
"ds_read_b128 v[48:51], v4 offset:3072\n"
"v_mfma_f32_16x16x32_bf16 v[28:31], v[156:159], v[60:63], v[28:31]\n"
"v_mfma_f32_16x16x32_bf16 v[24:27], v[152:155], v[60:63], v[24:27]\n"
"ds_read_b128 v[52:55], v4 offset:17408\n"
"v_mfma_f32_16x16x32_bf16 v[24:27], a[232:235], v[56:59], v[24:27]\n"
"s_waitcnt lgkmcnt(4)\n"
"v_mfma_f32_16x16x32_bf16 v[24:27], a[16:19], v[32:35], v[24:27]\n"
"ds_read_b128 v[56:59], v4 offset:31744\n"
"v_mfma_f32_16x16x32_bf16 v[24:27], a[128:131], v[36:39], v[24:27]\n"
"s_waitcnt vmcnt(2)\n"
"s_barrier\n"
"v_mfma_f32_16x16x32_bf16 v[28:31], a[132:135], v[36:39], v[28:31]\n"
"ds_read_b128 v[60:63], v4 offset:46080\n"
"v_mfma_f32_16x16x32_bf16 v[28:31], a[20:23], v[32:35], v[28:31]\n"
"s_waitcnt lgkmcnt(4)\n"
"v_mfma_f32_16x16x32_bf16 v[28:31], a[244:247], v[40:43], v[28:31]\n"
"ds_read_b128 v[32:35], v4 offset:4096\n"
"s_add_u32 m0, s64, 0\n"
"v_mfma_f32_16x16x32_bf16 v[28:31], v[164:167], v[44:47], v[28:31]\n"
"buffer_load_dwordx4 v2, s[32:35], s63 offen offset:0 sc1 lds\n"
"v_mfma_f32_16x16x32_bf16 v[24:27], v[160:163], v[44:47], v[24:27]\n"
"ds_read_b128 v[36:39], v4 offset:18432\n"
"v_mfma_f32_16x16x32_bf16 v[24:27], a[240:243], v[40:43], v[24:27]\n"
"s_waitcnt lgkmcnt(4)\n"
"v_mfma_f32_16x16x32_bf16 v[24:27], a[24:27], v[48:51], v[24:27]\n"
"ds_read_b128 v[40:43], v4 offset:32768\n"
"v_mfma_f32_16x16x32_bf16 v[24:27], a[136:139], v[52:55], v[24:27]\n"
"v_mfma_f32_16x16x32_bf16 v[28:31], a[140:143], v[52:55], v[28:31]\n"
"ds_read_b128 v[44:47], v4 offset:47104\n"
"v_mfma_f32_16x16x32_bf16 v[28:31], a[28:31], v[48:51], v[28:31]\n"
"s_waitcnt lgkmcnt(4)\n"
"v_mfma_f32_16x16x32_bf16 v[28:31], a[252:255], v[56:59], v[28:31]\n"
"ds_read_b128 v[48:51], v4 offset:5120\n"
"v_mfma_f32_16x16x32_bf16 v[28:31], v[172:175], v[60:63], v[28:31]\n"
"buffer_load_dwordx4 v2, s[32:35], s63 offen offset:1024 sc1 lds\n"
"v_mfma_f32_16x16x32_bf16 v[24:27], v[168:171], v[60:63], v[24:27]\n"
"ds_read_b128 v[52:55], v4 offset:19456\n"
"v_mfma_f32_16x16x32_bf16 v[24:27], a[248:251], v[56:59], v[24:27]\n"
"s_waitcnt lgkmcnt(4)\n"
"v_mfma_f32_16x16x32_bf16 v[24:27], a[32:35], v[32:35], v[24:27]\n"
"ds_read_b128 v[56:59], v4 offset:33792\n"
"v_mfma_f32_16x16x32_bf16 v[24:27], a[144:147], v[36:39], v[24:27]\n"
"buffer_load_dwordx4 v2, s[32:35], s63 offen offset:2048 sc1 lds\n"
"v_mfma_f32_16x16x32_bf16 v[28:31], a[148:151], v[36:39], v[28:31]\n"
"ds_read_b128 v[60:63], v4 offset:48128\n"
"v_mfma_f32_16x16x32_bf16 v[28:31], a[36:39], v[32:35], v[28:31]\n"
"s_waitcnt lgkmcnt(4)\n"
"v_mfma_f32_16x16x32_bf16 v[28:31], v[68:71], v[40:43], v[28:31]\n"
"ds_read_b128 v[32:35], v4 offset:6144\n"
"v_mfma_f32_16x16x32_bf16 v[28:31], v[180:183], v[44:47], v[28:31]\n"
"buffer_load_dwordx4 v2, s[32:35], s63 offen offset:3072 sc1 lds\n"
"v_mfma_f32_16x16x32_bf16 v[24:27], v[176:179], v[44:47], v[24:27]\n"
"ds_read_b128 v[36:39], v4 offset:20480\n"
"v_mfma_f32_16x16x32_bf16 v[24:27], v[64:67], v[40:43], v[24:27]\n"
"s_waitcnt lgkmcnt(4)\n"
"v_mfma_f32_16x16x32_bf16 v[24:27], a[40:43], v[48:51], v[24:27]\n"
"ds_read_b128 v[40:43], v4 offset:34816\n"
"v_mfma_f32_16x16x32_bf16 v[24:27], a[152:155], v[52:55], v[24:27]\n"
"buffer_load_dwordx4 v[12:15], v2, s[32:35], s71 offen sc1\n"
"s_add_u32 s71, s71, 1187840\n"
"v_mfma_f32_16x16x32_bf16 v[28:31], a[156:159], v[52:55], v[28:31]\n"
"ds_read_b128 v[44:47], v4 offset:49152\n"
"s_add_u32 s63, s63, 4096\n"
"v_mfma_f32_16x16x32_bf16 v[28:31], a[44:47], v[48:51], v[28:31]\n"
"s_add_u32 m0, s64, 4096\n"
"s_waitcnt lgkmcnt(4)\n"
"v_mfma_f32_16x16x32_bf16 v[28:31], v[76:79], v[56:59], v[28:31]\n"
"ds_read_b128 v[48:51], v4 offset:7168\n"
"v_mfma_f32_16x16x32_bf16 v[28:31], v[188:191], v[60:63], v[28:31]\n"
"buffer_load_dwordx4 v2, s[32:35], s63 offen offset:0 sc1 lds\n"
"v_mfma_f32_16x16x32_bf16 v[24:27], v[184:187], v[60:63], v[24:27]\n"
"ds_read_b128 v[52:55], v4 offset:21504\n"
"v_mfma_f32_16x16x32_bf16 v[24:27], v[72:75], v[56:59], v[24:27]\n"
"s_waitcnt lgkmcnt(4)\n"
"v_mfma_f32_16x16x32_bf16 v[24:27], a[48:51], v[32:35], v[24:27]\n"
"ds_read_b128 v[56:59], v4 offset:35840\n"
"v_mfma_f32_16x16x32_bf16 v[24:27], a[160:163], v[36:39], v[24:27]\n"
"buffer_load_dwordx4 v2, s[32:35], s63 offen offset:1024 sc1 lds\n"
"v_mfma_f32_16x16x32_bf16 v[28:31], a[164:167], v[36:39], v[28:31]\n"
"ds_read_b128 v[60:63], v4 offset:50176\n"
"v_mfma_f32_16x16x32_bf16 v[28:31], a[52:55], v[32:35], v[28:31]\n"
"s_waitcnt lgkmcnt(4)\n"
"v_mfma_f32_16x16x32_bf16 v[28:31], v[84:87], v[40:43], v[28:31]\n"
"ds_read_b128 v[32:35], v4 offset:8192\n"
"v_mfma_f32_16x16x32_bf16 v[28:31], v[196:199], v[44:47], v[28:31]\n"
"buffer_load_dwordx4 v2, s[32:35], s63 offen offset:2048 sc1 lds\n"
"v_mfma_f32_16x16x32_bf16 v[24:27], v[192:195], v[44:47], v[24:27]\n"
"ds_read_b128 v[36:39], v4 offset:22528\n"
"v_mfma_f32_16x16x32_bf16 v[24:27], v[80:83], v[40:43], v[24:27]\n"
"s_waitcnt lgkmcnt(4)\n"
"v_mfma_f32_16x16x32_bf16 v[24:27], a[56:59], v[48:51], v[24:27]\n"
"ds_read_b128 v[40:43], v4 offset:36864\n"
"v_mfma_f32_16x16x32_bf16 v[24:27], a[168:171], v[52:55], v[24:27]\n"
"buffer_load_dwordx4 v2, s[32:35], s63 offen offset:3072 sc1 lds\n"
"v_mfma_f32_16x16x32_bf16 v[28:31], a[172:175], v[52:55], v[28:31]\n"
"ds_read_b128 v[44:47], v4 offset:51200\n"
"v_mfma_f32_16x16x32_bf16 v[28:31], a[60:63], v[48:51], v[28:31]\n"
"s_waitcnt lgkmcnt(4)\n"
"v_mfma_f32_16x16x32_bf16 v[28:31], v[92:95], v[56:59], v[28:31]\n"
"ds_read_b128 v[48:51], v4 offset:9216\n"
"v_mfma_f32_16x16x32_bf16 v[28:31], v[204:207], v[60:63], v[28:31]\n"
"s_add_u32 s63, s63, 4096\n"
"s_add_u32 m0, s64, 8192\n"
"v_mfma_f32_16x16x32_bf16 v[24:27], v[200:203], v[60:63], v[24:27]\n"
"ds_read_b128 v[52:55], v4 offset:23552\n"
"v_mfma_f32_16x16x32_bf16 v[24:27], v[88:91], v[56:59], v[24:27]\n"
"s_waitcnt lgkmcnt(4)\n"
"v_mfma_f32_16x16x32_bf16 v[24:27], a[64:67], v[32:35], v[24:27]\n"
"ds_read_b128 v[56:59], v4 offset:37888\n"
"v_mfma_f32_16x16x32_bf16 v[24:27], a[176:179], v[36:39], v[24:27]\n"
"buffer_load_dwordx4 v2, s[32:35], s63 offen offset:0 sc1 lds\n"
"v_mfma_f32_16x16x32_bf16 v[28:31], a[180:183], v[36:39], v[28:31]\n"
"ds_read_b128 v[60:63], v4 offset:52224\n"
"v_mfma_f32_16x16x32_bf16 v[28:31], a[68:71], v[32:35], v[28:31]\n"
"s_waitcnt lgkmcnt(4)\n"
"v_mfma_f32_16x16x32_bf16 v[28:31], v[100:103], v[40:43], v[28:31]\n"
"ds_read_b128 v[32:35], v4 offset:10240\n"
"v_mfma_f32_16x16x32_bf16 v[28:31], v[212:215], v[44:47], v[28:31]\n"
"buffer_load_dwordx4 v2, s[32:35], s63 offen offset:1024 sc1 lds\n"
"v_mfma_f32_16x16x32_bf16 v[24:27], v[208:211], v[44:47], v[24:27]\n"
"ds_read_b128 v[36:39], v4 offset:24576\n"
"v_mfma_f32_16x16x32_bf16 v[24:27], v[96:99], v[40:43], v[24:27]\n"
"s_waitcnt lgkmcnt(4)\n"
"v_mfma_f32_16x16x32_bf16 v[24:27], a[72:75], v[48:51], v[24:27]\n"
"ds_read_b128 v[40:43], v4 offset:38912\n"
"v_mfma_f32_16x16x32_bf16 v[24:27], a[184:187], v[52:55], v[24:27]\n"
"buffer_load_dwordx4 v2, s[32:35], s63 offen offset:2048 sc1 lds\n"
"v_mfma_f32_16x16x32_bf16 v[28:31], a[188:191], v[52:55], v[28:31]\n"
"ds_read_b128 v[44:47], v4 offset:53248\n"
"v_mfma_f32_16x16x32_bf16 v[28:31], a[76:79], v[48:51], v[28:31]\n"
"s_waitcnt lgkmcnt(4)\n"
"v_mfma_f32_16x16x32_bf16 v[28:31], v[108:111], v[56:59], v[28:31]\n"
"ds_read_b128 v[48:51], v4 offset:11264\n"
"v_mfma_f32_16x16x32_bf16 v[28:31], v[220:223], v[60:63], v[28:31]\n"
"v_mfma_f32_16x16x32_bf16 v[24:27], v[216:219], v[60:63], v[24:27]\n"
"ds_read_b128 v[52:55], v4 offset:25600\n"
"v_mfma_f32_16x16x32_bf16 v[24:27], v[104:107], v[56:59], v[24:27]\n"
"s_waitcnt lgkmcnt(4)\n"
"v_mfma_f32_16x16x32_bf16 v[24:27], a[80:83], v[32:35], v[24:27]\n"
"ds_read_b128 v[56:59], v4 offset:39936\n"
"v_mfma_f32_16x16x32_bf16 v[24:27], a[192:195], v[36:39], v[24:27]\n"
"buffer_load_dwordx4 v2, s[32:35], s63 offen offset:3072 sc1 lds\n"
"s_add_u32 s63, s63, 4096\n"
"v_mfma_f32_16x16x32_bf16 v[28:31], a[196:199], v[36:39], v[28:31]\n"
"ds_read_b128 v[60:63], v4 offset:54272\n"
"s_add_u32 m0, s64, 12288\n"
"v_mfma_f32_16x16x32_bf16 v[28:31], a[84:87], v[32:35], v[28:31]\n"
"s_waitcnt lgkmcnt(4)\n"
"v_mfma_f32_16x16x32_bf16 v[28:31], v[116:119], v[40:43], v[28:31]\n"
"ds_read_b128 v[32:35], v4 offset:12288\n"
"v_mfma_f32_16x16x32_bf16 v[28:31], v[228:231], v[44:47], v[28:31]\n"
"buffer_load_dwordx4 v2, s[32:35], s63 offen offset:0 sc1 lds\n"
"v_mfma_f32_16x16x32_bf16 v[24:27], v[224:227], v[44:47], v[24:27]\n"
"ds_read_b128 v[36:39], v4 offset:26624\n"
"v_mfma_f32_16x16x32_bf16 v[24:27], v[112:115], v[40:43], v[24:27]\n"
"s_waitcnt lgkmcnt(4)\n"
"v_mfma_f32_16x16x32_bf16 v[24:27], a[88:91], v[48:51], v[24:27]\n"
"ds_read_b128 v[40:43], v4 offset:40960\n"
"v_mfma_f32_16x16x32_bf16 v[24:27], a[200:203], v[52:55], v[24:27]\n"
"buffer_load_dwordx4 v2, s[32:35], s63 offen offset:1024 sc1 lds\n"
"s_add_u32 s63, s63, 1175552\n"
"v_mfma_f32_16x16x32_bf16 v[28:31], a[204:207], v[52:55], v[28:31]\n"
"ds_read_b128 v[44:47], v4 offset:55296\n"
"v_mfma_f32_16x16x32_bf16 v[28:31], a[92:95], v[48:51], v[28:31]\n"
"s_waitcnt lgkmcnt(4)\n"
"v_mfma_f32_16x16x32_bf16 v[28:31], v[124:127], v[56:59], v[28:31]\n"
"ds_read_b128 v[48:51], v4 offset:13312\n"
"s_waitcnt vmcnt(10)\n"
"v_mfma_f32_16x16x32_bf16 v[28:31], v[236:239], v[60:63], v[28:31]\n"
"v_lshlrev_b32 v16, 16, v12\n"
"v_and_b32 v17, 0xffff0000, v12\n"
"v_mfma_f32_16x16x32_bf16 v[24:27], v[232:235], v[60:63], v[24:27]\n"
"ds_read_b128 v[52:55], v4 offset:27648\n"
"v_lshlrev_b32 v18, 16, v13\n"
"v_mfma_f32_16x16x32_bf16 v[24:27], v[120:123], v[56:59], v[24:27]\n"
"v_and_b32 v19, 0xffff0000, v13\n"
"s_waitcnt lgkmcnt(4)\n"
"v_mfma_f32_16x16x32_bf16 v[24:27], a[96:99], v[32:35], v[24:27]\n"
"ds_read_b128 v[56:59], v4 offset:41984\n"
"v_lshlrev_b32 v20, 16, v14\n"
"v_mfma_f32_16x16x32_bf16 v[24:27], a[208:211], v[36:39], v[24:27]\n"
"s_barrier\n"
"v_mfma_f32_16x16x32_bf16 v[28:31], a[212:215], v[36:39], v[28:31]\n"
"ds_read_b128 v[60:63], v4 offset:56320\n"
"v_and_b32 v21, 0xffff0000, v14\n"
"v_mfma_f32_16x16x32_bf16 v[28:31], a[100:103], v[32:35], v[28:31]\n"
"v_lshlrev_b32 v22, 16, v15\n"
"s_waitcnt lgkmcnt(4)\n"
"v_mfma_f32_16x16x32_bf16 v[28:31], v[132:135], v[40:43], v[28:31]\n"
"ds_read_b128 v[32:35], v2 offset:0\n"
"v_and_b32 v23, 0xffff0000, v15\n"
"v_mfma_f32_16x16x32_bf16 v[28:31], v[244:247], v[44:47], v[28:31]\n"
"v_mfma_f32_16x16x32_bf16 v[24:27], v[240:243], v[44:47], v[24:27]\n"
"ds_read_b128 v[36:39], v2 offset:14336\n"
"v_mfma_f32_16x16x32_bf16 v[24:27], v[128:131], v[40:43], v[24:27]\n"
"s_waitcnt lgkmcnt(4)\n"
"v_mfma_f32_16x16x32_bf16 v[24:27], a[104:107], v[48:51], v[24:27]\n"
"ds_read_b128 v[40:43], v2 offset:28672\n"
"v_mfma_f32_16x16x32_bf16 v[24:27], a[216:219], v[52:55], v[24:27]\n"
"v_mfma_f32_16x16x32_bf16 v[28:31], a[220:223], v[52:55], v[28:31]\n"
"ds_read_b128 v[44:47], v2 offset:43008\n"
"v_mfma_f32_16x16x32_bf16 v[28:31], a[108:111], v[48:51], v[28:31]\n"
"s_waitcnt lgkmcnt(4)\n"
"v_mfma_f32_16x16x32_bf16 v[28:31], v[140:143], v[56:59], v[28:31]\n"
"ds_read_b128 v[48:51], v2 offset:1024\n"
"v_mfma_f32_16x16x32_bf16 v[28:31], v[252:255], v[60:63], v[28:31]\n"
"v_mfma_f32_16x16x32_bf16 v[24:27], v[248:251], v[60:63], v[24:27]\n"
"ds_read_b128 v[52:55], v2 offset:15360\n"
"v_mfma_f32_16x16x32_bf16 v[24:27], v[136:139], v[56:59], v[24:27]\n"
"s_branch .L_dll_2\n"
".L_dlb_2:\n"
"s_mov_b32 s62, 15\n"
"s_add_u32 s74, s74, 8192\n"
"v_lshrrev_b32 v5, 8, v2\n"
"v_lshl_add_u32 v5, v5, 4, s74\n"
"v_and_b32 v3, 128, v2\n"
"v_lshl_add_u32 v5, v3, 2, v5\n"
"v_and_b32 v3, 16, v2\n"
"v_lshl_add_u32 v5, v3, 4, v5\n"
"v_and_b32 v3, 96, v2\n"
"v_lshl_add_u32 v5, v3, 1, v5\n"
"v_lshrrev_b32 v3, 2, v2\n"
"v_add_u32 v3, s74, v3\n"
".p2align 6\n"
".L_dml_2:\n"
"s_waitcnt lgkmcnt(4)\n"
"v_mfma_f32_16x16x32_bf16 v[24:27], a[0:3], v[32:35], v[24:27]\n"
"ds_read_b128 v[56:59], v4 offset:29696\n"
"v_mfma_f32_16x16x32_bf16 v[24:27], a[112:115], v[36:39], v[24:27]\n"
"v_mfma_f32_16x16x32_bf16 v[28:31], a[116:119], v[36:39], v[28:31]\n"
"ds_read_b128 v[60:63], v4 offset:44032\n"
"v_mfma_f32_16x16x32_bf16 v[28:31], a[4:7], v[32:35], v[28:31]\n"
"v_cvt_pk_bf16_f32 v15, v22, v23\n"
"s_waitcnt lgkmcnt(4)\n"
"v_mfma_f32_16x16x32_bf16 v[28:31], a[228:231], v[40:43], v[28:31]\n"
"ds_read_b128 v[32:35], v4 offset:2048\n"
"v_cvt_pk_bf16_f32 v14, v20, v21\n"
"v_mfma_f32_16x16x32_bf16 v[28:31], v[148:151], v[44:47], v[28:31]\n"
"v_cvt_pk_bf16_f32 v13, v18, v19\n"
"v_cvt_pk_bf16_f32 v12, v16, v17\n"
"v_mfma_f32_16x16x32_bf16 v[24:27], v[144:147], v[44:47], v[24:27]\n"
"ds_read_b128 v[36:39], v4 offset:16384\n"
"ds_write_b128 v5, v[12:15]\n"
"v_mfma_f32_16x16x32_bf16 v[24:27], a[224:227], v[40:43], v[24:27]\n"
"ds_read2st64_b32 v[12:13], v3 offset0:0 offset1:1\n"
"s_waitcnt lgkmcnt(6)\n"
"v_mfma_f32_16x16x32_bf16 v[24:27], a[8:11], v[48:51], v[24:27]\n"
"ds_read_b128 v[40:43], v4 offset:30720\n"
"ds_read2st64_b32 v[14:15], v3 offset0:2 offset1:3\n"
"v_mfma_f32_16x16x32_bf16 v[24:27], a[120:123], v[52:55], v[24:27]\n"
"v_mfma_f32_16x16x32_bf16 v[28:31], a[124:127], v[52:55], v[28:31]\n"
"ds_read_b128 v[44:47], v4 offset:45056\n"
"v_mfma_f32_16x16x32_bf16 v[28:31], a[12:15], v[48:51], v[28:31]\n"
"s_waitcnt lgkmcnt(7)\n"
"v_mfma_f32_16x16x32_bf16 v[28:31], a[236:239], v[56:59], v[28:31]\n"
"ds_read_b128 v[48:51], v4 offset:3072\n"
"v_mfma_f32_16x16x32_bf16 v[28:31], v[156:159], v[60:63], v[28:31]\n"
"s_waitcnt lgkmcnt(2)\n"
"buffer_atomic_pk_add_bf16 v12, v1, s[44:47], s65 offen offset:0 sc1\n"
"v_mfma_f32_16x16x32_bf16 v[24:27], v[152:155], v[60:63], v[24:27]\n"
"ds_read_b128 v[52:55], v4 offset:17408\n"
"v_mfma_f32_16x16x32_bf16 v[24:27], a[232:235], v[56:59], v[24:27]\n"
"buffer_atomic_pk_add_bf16 v13, v1, s[44:47], s65 offen offset:2048 sc1\n"
"v_mfma_f32_16x16x32_bf16 v[24:27], a[16:19], v[32:35], v[24:27]\n"
"ds_read_b128 v[56:59], v4 offset:31744\n"
"s_add_u32 s65, s65, 16384\n"
"v_mfma_f32_16x16x32_bf16 v[24:27], a[128:131], v[36:39], v[24:27]\n"
"s_waitcnt vmcnt(2)\n"
"s_barrier\n"
"v_mfma_f32_16x16x32_bf16 v[28:31], a[132:135], v[36:39], v[28:31]\n"
"ds_read_b128 v[60:63], v4 offset:46080\n"
"v_mfma_f32_16x16x32_bf16 v[28:31], a[20:23], v[32:35], v[28:31]\n"
"buffer_atomic_pk_add_bf16 v14, v1, s[44:47], s65 offen offset:0 sc1\n"
"s_waitcnt lgkmcnt(4)\n"
"v_mfma_f32_16x16x32_bf16 v[28:31], a[244:247], v[40:43], v[28:31]\n"
"ds_read_b128 v[32:35], v4 offset:4096\n"
"s_add_u32 m0, s64, 0\n"
"v_mfma_f32_16x16x32_bf16 v[28:31], v[164:167], v[44:47], v[28:31]\n"
"buffer_atomic_pk_add_bf16 v15, v1, s[44:47], s65 offen offset:2048 sc1\n"
"s_add_u32 s65, s65, 16384\n"
"v_mfma_f32_16x16x32_bf16 v[24:27], v[160:163], v[44:47], v[24:27]\n"
"ds_read_b128 v[36:39], v4 offset:18432\n"
"v_mfma_f32_16x16x32_bf16 v[24:27], a[240:243], v[40:43], v[24:27]\n"
"s_waitcnt lgkmcnt(4)\n"
"v_mfma_f32_16x16x32_bf16 v[24:27], a[24:27], v[48:51], v[24:27]\n"
"ds_read_b128 v[40:43], v4 offset:32768\n"
"v_mfma_f32_16x16x32_bf16 v[24:27], a[136:139], v[52:55], v[24:27]\n"
"buffer_load_dwordx4 v2, s[32:35], s63 offen offset:0 sc1 lds\n"
"v_mfma_f32_16x16x32_bf16 v[28:31], a[140:143], v[52:55], v[28:31]\n"
"ds_read_b128 v[44:47], v4 offset:47104\n"
"v_mfma_f32_16x16x32_bf16 v[28:31], a[28:31], v[48:51], v[28:31]\n"
"s_waitcnt lgkmcnt(4)\n"
"v_mfma_f32_16x16x32_bf16 v[28:31], a[252:255], v[56:59], v[28:31]\n"
"ds_read_b128 v[48:51], v4 offset:5120\n"
"v_mfma_f32_16x16x32_bf16 v[28:31], v[172:175], v[60:63], v[28:31]\n"
"buffer_load_dwordx4 v2, s[32:35], s63 offen offset:1024 sc1 lds\n"
"v_mfma_f32_16x16x32_bf16 v[24:27], v[168:171], v[60:63], v[24:27]\n"
"ds_read_b128 v[52:55], v4 offset:19456\n"
"v_mfma_f32_16x16x32_bf16 v[24:27], a[248:251], v[56:59], v[24:27]\n"
"s_waitcnt lgkmcnt(4)\n"
"v_mfma_f32_16x16x32_bf16 v[24:27], a[32:35], v[32:35], v[24:27]\n"
"ds_read_b128 v[56:59], v4 offset:33792\n"
"v_mfma_f32_16x16x32_bf16 v[24:27], a[144:147], v[36:39], v[24:27]\n"
"buffer_load_dwordx4 v2, s[32:35], s63 offen offset:2048 sc1 lds\n"
"v_mfma_f32_16x16x32_bf16 v[28:31], a[148:151], v[36:39], v[28:31]\n"
"ds_read_b128 v[60:63], v4 offset:48128\n"
"v_mfma_f32_16x16x32_bf16 v[28:31], a[36:39], v[32:35], v[28:31]\n"
"s_waitcnt lgkmcnt(4)\n"
"v_mfma_f32_16x16x32_bf16 v[28:31], v[68:71], v[40:43], v[28:31]\n"
"ds_read_b128 v[32:35], v4 offset:6144\n"
"v_mfma_f32_16x16x32_bf16 v[28:31], v[180:183], v[44:47], v[28:31]\n"
"buffer_load_dwordx4 v2, s[32:35], s63 offen offset:3072 sc1 lds\n"
"v_mfma_f32_16x16x32_bf16 v[24:27], v[176:179], v[44:47], v[24:27]\n"
"ds_read_b128 v[36:39], v4 offset:20480\n"
"v_mfma_f32_16x16x32_bf16 v[24:27], v[64:67], v[40:43], v[24:27]\n"
"s_waitcnt lgkmcnt(4)\n"
"v_mfma_f32_16x16x32_bf16 v[24:27], a[40:43], v[48:51], v[24:27]\n"
"ds_read_b128 v[40:43], v4 offset:34816\n"
"v_mfma_f32_16x16x32_bf16 v[24:27], a[152:155], v[52:55], v[24:27]\n"
"buffer_load_dwordx4 v[12:15], v2, s[32:35], s71 offen sc1\n"
"s_add_u32 s71, s71, 1187840\n"
"v_mfma_f32_16x16x32_bf16 v[28:31], a[156:159], v[52:55], v[28:31]\n"
"ds_read_b128 v[44:47], v4 offset:49152\n"
"s_add_u32 s63, s63, 4096\n"
"v_mfma_f32_16x16x32_bf16 v[28:31], a[44:47], v[48:51], v[28:31]\n"
"s_add_u32 m0, s64, 4096\n"
"s_waitcnt lgkmcnt(4)\n"
"v_mfma_f32_16x16x32_bf16 v[28:31], v[76:79], v[56:59], v[28:31]\n"
"ds_read_b128 v[48:51], v4 offset:7168\n"
"v_mfma_f32_16x16x32_bf16 v[28:31], v[188:191], v[60:63], v[28:31]\n"
"buffer_load_dwordx4 v2, s[32:35], s63 offen offset:0 sc1 lds\n"
"v_mfma_f32_16x16x32_bf16 v[24:27], v[184:187], v[60:63], v[24:27]\n"
"ds_read_b128 v[52:55], v4 offset:21504\n"
"v_mfma_f32_16x16x32_bf16 v[24:27], v[72:75], v[56:59], v[24:27]\n"
"s_waitcnt lgkmcnt(4)\n"
"v_mfma_f32_16x16x32_bf16 v[24:27], a[48:51], v[32:35], v[24:27]\n"
"ds_read_b128 v[56:59], v4 offset:35840\n"
"v_mfma_f32_16x16x32_bf16 v[24:27], a[160:163], v[36:39], v[24:27]\n"
"buffer_load_dwordx4 v2, s[32:35], s63 offen offset:1024 sc1 lds\n"
"v_mfma_f32_16x16x32_bf16 v[28:31], a[164:167], v[36:39], v[28:31]\n"
"ds_read_b128 v[60:63], v4 offset:50176\n"
"v_mfma_f32_16x16x32_bf16 v[28:31], a[52:55], v[32:35], v[28:31]\n"
"s_waitcnt lgkmcnt(4)\n"
"v_mfma_f32_16x16x32_bf16 v[28:31], v[84:87], v[40:43], v[28:31]\n"
"ds_read_b128 v[32:35], v4 offset:8192\n"
"v_mfma_f32_16x16x32_bf16 v[28:31], v[196:199], v[44:47], v[28:31]\n"
"buffer_load_dwordx4 v2, s[32:35], s63 offen offset:2048 sc1 lds\n"
"v_mfma_f32_16x16x32_bf16 v[24:27], v[192:195], v[44:47], v[24:27]\n"
"ds_read_b128 v[36:39], v4 offset:22528\n"
"v_mfma_f32_16x16x32_bf16 v[24:27], v[80:83], v[40:43], v[24:27]\n"
"s_waitcnt lgkmcnt(4)\n"
"v_mfma_f32_16x16x32_bf16 v[24:27], a[56:59], v[48:51], v[24:27]\n"
"ds_read_b128 v[40:43], v4 offset:36864\n"
"v_mfma_f32_16x16x32_bf16 v[24:27], a[168:171], v[52:55], v[24:27]\n"
"buffer_load_dwordx4 v2, s[32:35], s63 offen offset:3072 sc1 lds\n"
"v_mfma_f32_16x16x32_bf16 v[28:31], a[172:175], v[52:55], v[28:31]\n"
"ds_read_b128 v[44:47], v4 offset:51200\n"
"v_mfma_f32_16x16x32_bf16 v[28:31], a[60:63], v[48:51], v[28:31]\n"
"s_waitcnt lgkmcnt(4)\n"
"v_mfma_f32_16x16x32_bf16 v[28:31], v[92:95], v[56:59], v[28:31]\n"
"ds_read_b128 v[48:51], v4 offset:9216\n"
"v_mfma_f32_16x16x32_bf16 v[28:31], v[204:207], v[60:63], v[28:31]\n"
"s_add_u32 s63, s63, 4096\n"
"s_add_u32 m0, s64, 8192\n"
"v_mfma_f32_16x16x32_bf16 v[24:27], v[200:203], v[60:63], v[24:27]\n"
"ds_read_b128 v[52:55], v4 offset:23552\n"
"v_mfma_f32_16x16x32_bf16 v[24:27], v[88:91], v[56:59], v[24:27]\n"
"s_waitcnt lgkmcnt(4)\n"
"v_mfma_f32_16x16x32_bf16 v[24:27], a[64:67], v[32:35], v[24:27]\n"
"ds_read_b128 v[56:59], v4 offset:37888\n"
"v_mfma_f32_16x16x32_bf16 v[24:27], a[176:179], v[36:39], v[24:27]\n"
"buffer_load_dwordx4 v2, s[32:35], s63 offen offset:0 sc1 lds\n"
"v_mfma_f32_16x16x32_bf16 v[28:31], a[180:183], v[36:39], v[28:31]\n"
"ds_read_b128 v[60:63], v4 offset:52224\n"
"v_mfma_f32_16x16x32_bf16 v[28:31], a[68:71], v[32:35], v[28:31]\n"
"s_waitcnt lgkmcnt(4)\n"
"v_mfma_f32_16x16x32_bf16 v[28:31], v[100:103], v[40:43], v[28:31]\n"
"ds_read_b128 v[32:35], v4 offset:10240\n"
"v_mfma_f32_16x16x32_bf16 v[28:31], v[212:215], v[44:47], v[28:31]\n"
"buffer_load_dwordx4 v2, s[32:35], s63 offen offset:1024 sc1 lds\n"
"v_mfma_f32_16x16x32_bf16 v[24:27], v[208:211], v[44:47], v[24:27]\n"
"ds_read_b128 v[36:39], v4 offset:24576\n"
"v_mfma_f32_16x16x32_bf16 v[24:27], v[96:99], v[40:43], v[24:27]\n"
"s_waitcnt lgkmcnt(4)\n"
"v_mfma_f32_16x16x32_bf16 v[24:27], a[72:75], v[48:51], v[24:27]\n"
"ds_read_b128 v[40:43], v4 offset:38912\n"
"v_mfma_f32_16x16x32_bf16 v[24:27], a[184:187], v[52:55], v[24:27]\n"
"buffer_load_dwordx4 v2, s[32:35], s63 offen offset:2048 sc1 lds\n"
"v_mfma_f32_16x16x32_bf16 v[28:31], a[188:191], v[52:55], v[28:31]\n"
"ds_read_b128 v[44:47], v4 offset:53248\n"
"v_mfma_f32_16x16x32_bf16 v[28:31], a[76:79], v[48:51], v[28:31]\n"
"s_waitcnt lgkmcnt(4)\n"
"v_mfma_f32_16x16x32_bf16 v[28:31], v[108:111], v[56:59], v[28:31]\n"
"ds_read_b128 v[48:51], v4 offset:11264\n"
"v_mfma_f32_16x16x32_bf16 v[28:31], v[220:223], v[60:63], v[28:31]\n"
"v_mfma_f32_16x16x32_bf16 v[24:27], v[216:219], v[60:63], v[24:27]\n"
"ds_read_b128 v[52:55], v4 offset:25600\n"
"v_mfma_f32_16x16x32_bf16 v[24:27], v[104:107], v[56:59], v[24:27]\n"
"s_waitcnt lgkmcnt(4)\n"
"v_mfma_f32_16x16x32_bf16 v[24:27], a[80:83], v[32:35], v[24:27]\n"
"ds_read_b128 v[56:59], v4 offset:39936\n"
"v_mfma_f32_16x16x32_bf16 v[24:27], a[192:195], v[36:39], v[24:27]\n"
"buffer_load_dwordx4 v2, s[32:35], s63 offen offset:3072 sc1 lds\n"
"s_add_u32 s63, s63, 4096\n"
"v_mfma_f32_16x16x32_bf16 v[28:31], a[196:199], v[36:39], v[28:31]\n"
"ds_read_b128 v[60:63], v4 offset:54272\n"
"s_add_u32 m0, s64, 12288\n"
"v_mfma_f32_16x16x32_bf16 v[28:31], a[84:87], v[32:35], v[28:31]\n"
"s_waitcnt lgkmcnt(4)\n"
"v_mfma_f32_16x16x32_bf16 v[28:31], v[116:119], v[40:43], v[28:31]\n"
"ds_read_b128 v[32:35], v4 offset:12288\n"
"v_mfma_f32_16x16x32_bf16 v[28:31], v[228:231], v[44:47], v[28:31]\n"
"buffer_load_dwordx4 v2, s[32:35], s63 offen offset:0 sc1 lds\n"
"v_mfma_f32_16x16x32_bf16 v[24:27], v[224:227], v[44:47], v[24:27]\n"
"ds_read_b128 v[36:39], v4 offset:26624\n"
"v_mfma_f32_16x16x32_bf16 v[24:27], v[112:115], v[40:43], v[24:27]\n"
"s_waitcnt lgkmcnt(4)\n"
"v_mfma_f32_16x16x32_bf16 v[24:27], a[88:91], v[48:51], v[24:27]\n"
"ds_read_b128 v[40:43], v4 offset:40960\n"
"v_mfma_f32_16x16x32_bf16 v[24:27], a[200:203], v[52:55], v[24:27]\n"
"buffer_load_dwordx4 v2, s[32:35], s63 offen offset:1024 sc1 lds\n"
"s_add_u32 s63, s63, 1175552\n"
"v_mfma_f32_16x16x32_bf16 v[28:31], a[204:207], v[52:55], v[28:31]\n"
"ds_read_b128 v[44:47], v4 offset:55296\n"
"v_mfma_f32_16x16x32_bf16 v[28:31], a[92:95], v[48:51], v[28:31]\n"
"s_waitcnt lgkmcnt(4)\n"
"v_mfma_f32_16x16x32_bf16 v[28:31], v[124:127], v[56:59], v[28:31]\n"
"ds_read_b128 v[48:51], v4 offset:13312\n"
"s_waitcnt vmcnt(10)\n"
"v_mfma_f32_16x16x32_bf16 v[28:31], v[236:239], v[60:63], v[28:31]\n"
"v_lshlrev_b32 v16, 16, v12\n"
"v_and_b32 v17, 0xffff0000, v12\n"
"v_mfma_f32_16x16x32_bf16 v[24:27], v[232:235], v[60:63], v[24:27]\n"
"ds_read_b128 v[52:55], v4 offset:27648\n"
"v_lshlrev_b32 v18, 16, v13\n"
"v_mfma_f32_16x16x32_bf16 v[24:27], v[120:123], v[56:59], v[24:27]\n"
"v_and_b32 v19, 0xffff0000, v13\n"
"s_waitcnt lgkmcnt(4)\n"
"v_mfma_f32_16x16x32_bf16 v[24:27], a[96:99], v[32:35], v[24:27]\n"
"ds_read_b128 v[56:59], v4 offset:41984\n"
"v_lshlrev_b32 v20, 16, v14\n"
"v_mfma_f32_16x16x32_bf16 v[24:27], a[208:211], v[36:39], v[24:27]\n"
"s_barrier\n"
"v_mfma_f32_16x16x32_bf16 v[28:31], a[212:215], v[36:39], v[28:31]\n"
"ds_read_b128 v[60:63], v4 offset:56320\n"
"v_and_b32 v21, 0xffff0000, v14\n"
"v_mfma_f32_16x16x32_bf16 v[28:31], a[100:103], v[32:35], v[28:31]\n"
"v_lshlrev_b32 v22, 16, v15\n"
"s_waitcnt lgkmcnt(4)\n"
"v_mfma_f32_16x16x32_bf16 v[28:31], v[132:135], v[40:43], v[28:31]\n"
"ds_read_b128 v[32:35], v2 offset:0\n"
"v_and_b32 v23, 0xffff0000, v15\n"
"v_mfma_f32_16x16x32_bf16 v[28:31], v[244:247], v[44:47], v[28:31]\n"
"v_mfma_f32_16x16x32_bf16 v[24:27], v[240:243], v[44:47], v[24:27]\n"
"ds_read_b128 v[36:39], v2 offset:14336\n"
"v_mfma_f32_16x16x32_bf16 v[24:27], v[128:131], v[40:43], v[24:27]\n"
"s_waitcnt lgkmcnt(4)\n"
"v_mfma_f32_16x16x32_bf16 v[24:27], a[104:107], v[48:51], v[24:27]\n"
"ds_read_b128 v[40:43], v2 offset:28672\n"
"v_mfma_f32_16x16x32_bf16 v[24:27], a[216:219], v[52:55], v[24:27]\n"
"v_mfma_f32_16x16x32_bf16 v[28:31], a[220:223], v[52:55], v[28:31]\n"
"ds_read_b128 v[44:47], v2 offset:43008\n"
"v_mfma_f32_16x16x32_bf16 v[28:31], a[108:111], v[48:51], v[28:31]\n"
"s_waitcnt lgkmcnt(4)\n"
"v_mfma_f32_16x16x32_bf16 v[28:31], v[140:143], v[56:59], v[28:31]\n"
"ds_read_b128 v[48:51], v2 offset:1024\n"
"v_mfma_f32_16x16x32_bf16 v[28:31], v[252:255], v[60:63], v[28:31]\n"
"v_mfma_f32_16x16x32_bf16 v[24:27], v[248:251], v[60:63], v[24:27]\n"
"ds_read_b128 v[52:55], v2 offset:15360\n"
"s_sub_u32 s62, s62, 1\n"
"v_mfma_f32_16x16x32_bf16 v[24:27], v[136:139], v[56:59], v[24:27]\n"
"s_cbranch_scc1 .L_dmt_2\n"
"s_waitcnt lgkmcnt(4)\n"
"v_mfma_f32_16x16x32_bf16 v[16:19], a[0:3], v[32:35], v[16:19]\n"
"ds_read_b128 v[56:59], v2 offset:29696\n"
"v_mfma_f32_16x16x32_bf16 v[16:19], a[112:115], v[36:39], v[16:19]\n"
"v_mfma_f32_16x16x32_bf16 v[20:23], a[116:119], v[36:39], v[20:23]\n"
"ds_read_b128 v[60:63], v2 offset:44032\n"
"v_mfma_f32_16x16x32_bf16 v[20:23], a[4:7], v[32:35], v[20:23]\n"
"v_cvt_pk_bf16_f32 v15, v30, v31\n"
"s_waitcnt lgkmcnt(4)\n"
"v_mfma_f32_16x16x32_bf16 v[20:23], a[228:231], v[40:43], v[20:23]\n"
"ds_read_b128 v[32:35], v2 offset:2048\n"
"v_cvt_pk_bf16_f32 v14, v28, v29\n"
"v_mfma_f32_16x16x32_bf16 v[20:23], v[148:151], v[44:47], v[20:23]\n"
"v_cvt_pk_bf16_f32 v13, v26, v27\n"
"v_cvt_pk_bf16_f32 v12, v24, v25\n"
"v_mfma_f32_16x16x32_bf16 v[16:19], v[144:147], v[44:47], v[16:19]\n"
"ds_read_b128 v[36:39], v2 offset:16384\n"
"ds_write_b128 v5, v[12:15]\n"
"v_mfma_f32_16x16x32_bf16 v[16:19], a[224:227], v[40:43], v[16:19]\n"
"ds_read2st64_b32 v[12:13], v3 offset0:0 offset1:1\n"
"s_waitcnt lgkmcnt(6)\n"
"v_mfma_f32_16x16x32_bf16 v[16:19], a[8:11], v[48:51], v[16:19]\n"
"ds_read_b128 v[40:43], v2 offset:30720\n"
"ds_read2st64_b32 v[14:15], v3 offset0:2 offset1:3\n"
"v_mfma_f32_16x16x32_bf16 v[16:19], a[120:123], v[52:55], v[16:19]\n"
"v_mfma_f32_16x16x32_bf16 v[20:23], a[124:127], v[52:55], v[20:23]\n"
"ds_read_b128 v[44:47], v2 offset:45056\n"
"v_mfma_f32_16x16x32_bf16 v[20:23], a[12:15], v[48:51], v[20:23]\n"
"s_waitcnt lgkmcnt(7)\n"
"v_mfma_f32_16x16x32_bf16 v[20:23], a[236:239], v[56:59], v[20:23]\n"
"ds_read_b128 v[48:51], v2 offset:3072\n"
"v_mfma_f32_16x16x32_bf16 v[20:23], v[156:159], v[60:63], v[20:23]\n"
"s_waitcnt lgkmcnt(2)\n"
"buffer_atomic_pk_add_bf16 v12, v1, s[44:47], s65 offen offset:0 sc1\n"
"v_mfma_f32_16x16x32_bf16 v[16:19], v[152:155], v[60:63], v[16:19]\n"
"ds_read_b128 v[52:55], v2 offset:17408\n"
"v_mfma_f32_16x16x32_bf16 v[16:19], a[232:235], v[56:59], v[16:19]\n"
"buffer_atomic_pk_add_bf16 v13, v1, s[44:47], s65 offen offset:2048 sc1\n"
"v_mfma_f32_16x16x32_bf16 v[16:19], a[16:19], v[32:35], v[16:19]\n"
"ds_read_b128 v[56:59], v2 offset:31744\n"
"s_add_u32 s65, s65, 16384\n"
"v_mfma_f32_16x16x32_bf16 v[16:19], a[128:131], v[36:39], v[16:19]\n"
"s_waitcnt vmcnt(2)\n"
"s_barrier\n"
"v_mfma_f32_16x16x32_bf16 v[20:23], a[132:135], v[36:39], v[20:23]\n"
"ds_read_b128 v[60:63], v2 offset:46080\n"
"v_mfma_f32_16x16x32_bf16 v[20:23], a[20:23], v[32:35], v[20:23]\n"
"buffer_atomic_pk_add_bf16 v14, v1, s[44:47], s65 offen offset:0 sc1\n"
"s_waitcnt lgkmcnt(4)\n"
"v_mfma_f32_16x16x32_bf16 v[20:23], a[244:247], v[40:43], v[20:23]\n"
"ds_read_b128 v[32:35], v2 offset:4096\n"
"s_add_u32 m0, s64, 57344\n"
"v_mfma_f32_16x16x32_bf16 v[20:23], v[164:167], v[44:47], v[20:23]\n"
"buffer_atomic_pk_add_bf16 v15, v1, s[44:47], s65 offen offset:2048 sc1\n"
"s_add_u32 s65, s65, 16384\n"
"v_mfma_f32_16x16x32_bf16 v[16:19], v[160:163], v[44:47], v[16:19]\n"
"ds_read_b128 v[36:39], v2 offset:18432\n"
"v_mfma_f32_16x16x32_bf16 v[16:19], a[240:243], v[40:43], v[16:19]\n"
"s_waitcnt lgkmcnt(4)\n"
"v_mfma_f32_16x16x32_bf16 v[16:19], a[24:27], v[48:51], v[16:19]\n"
"ds_read_b128 v[40:43], v2 offset:32768\n"
"v_mfma_f32_16x16x32_bf16 v[16:19], a[136:139], v[52:55], v[16:19]\n"
"buffer_load_dwordx4 v2, s[32:35], s63 offen offset:0 sc1 lds\n"
"v_mfma_f32_16x16x32_bf16 v[20:23], a[140:143], v[52:55], v[20:23]\n"
"ds_read_b128 v[44:47], v2 offset:47104\n"
"v_mfma_f32_16x16x32_bf16 v[20:23], a[28:31], v[48:51], v[20:23]\n"
"s_waitcnt lgkmcnt(4)\n"
"v_mfma_f32_16x16x32_bf16 v[20:23], a[252:255], v[56:59], v[20:23]\n"
"ds_read_b128 v[48:51], v2 offset:5120\n"
"v_mfma_f32_16x16x32_bf16 v[20:23], v[172:175], v[60:63], v[20:23]\n"
"buffer_load_dwordx4 v2, s[32:35], s63 offen offset:1024 sc1 lds\n"
"v_mfma_f32_16x16x32_bf16 v[16:19], v[168:171], v[60:63], v[16:19]\n"
"ds_read_b128 v[52:55], v2 offset:19456\n"
"v_mfma_f32_16x16x32_bf16 v[16:19], a[248:251], v[56:59], v[16:19]\n"
"s_waitcnt lgkmcnt(4)\n"
"v_mfma_f32_16x16x32_bf16 v[16:19], a[32:35], v[32:35], v[16:19]\n"
"ds_read_b128 v[56:59], v2 offset:33792\n"
"v_mfma_f32_16x16x32_bf16 v[16:19], a[144:147], v[36:39], v[16:19]\n"
"buffer_load_dwordx4 v2, s[32:35], s63 offen offset:2048 sc1 lds\n"
"v_mfma_f32_16x16x32_bf16 v[20:23], a[148:151], v[36:39], v[20:23]\n"
"ds_read_b128 v[60:63], v2 offset:48128\n"
"v_mfma_f32_16x16x32_bf16 v[20:23], a[36:39], v[32:35], v[20:23]\n"
"s_waitcnt lgkmcnt(4)\n"
"v_mfma_f32_16x16x32_bf16 v[20:23], v[68:71], v[40:43], v[20:23]\n"
"ds_read_b128 v[32:35], v2 offset:6144\n"
"v_mfma_f32_16x16x32_bf16 v[20:23], v[180:183], v[44:47], v[20:23]\n"
"buffer_load_dwordx4 v2, s[32:35], s63 offen offset:3072 sc1 lds\n"
"v_mfma_f32_16x16x32_bf16 v[16:19], v[176:179], v[44:47], v[16:19]\n"
"ds_read_b128 v[36:39], v2 offset:20480\n"
"v_mfma_f32_16x16x32_bf16 v[16:19], v[64:67], v[40:43], v[16:19]\n"
"s_waitcnt lgkmcnt(4)\n"
"v_mfma_f32_16x16x32_bf16 v[16:19], a[40:43], v[48:51], v[16:19]\n"
"ds_read_b128 v[40:43], v2 offset:34816\n"
"v_mfma_f32_16x16x32_bf16 v[16:19], a[152:155], v[52:55], v[16:19]\n"
"buffer_load_dwordx4 v[12:15], v2, s[32:35], s71 offen sc1\n"
"s_add_u32 s71, s71, 1187840\n"
"v_mfma_f32_16x16x32_bf16 v[20:23], a[156:159], v[52:55], v[20:23]\n"
"ds_read_b128 v[44:47], v2 offset:49152\n"
"s_add_u32 s63, s63, 4096\n"
"v_mfma_f32_16x16x32_bf16 v[20:23], a[44:47], v[48:51], v[20:23]\n"
"s_add_u32 m0, s64, 61440\n"
"s_waitcnt lgkmcnt(4)\n"
"v_mfma_f32_16x16x32_bf16 v[20:23], v[76:79], v[56:59], v[20:23]\n"
"ds_read_b128 v[48:51], v2 offset:7168\n"
"v_mfma_f32_16x16x32_bf16 v[20:23], v[188:191], v[60:63], v[20:23]\n"
"buffer_load_dwordx4 v2, s[32:35], s63 offen offset:0 sc1 lds\n"
"v_mfma_f32_16x16x32_bf16 v[16:19], v[184:187], v[60:63], v[16:19]\n"
"ds_read_b128 v[52:55], v2 offset:21504\n"
"v_mfma_f32_16x16x32_bf16 v[16:19], v[72:75], v[56:59], v[16:19]\n"
"s_waitcnt lgkmcnt(4)\n"
"v_mfma_f32_16x16x32_bf16 v[16:19], a[48:51], v[32:35], v[16:19]\n"
"ds_read_b128 v[56:59], v2 offset:35840\n"
"v_mfma_f32_16x16x32_bf16 v[16:19], a[160:163], v[36:39], v[16:19]\n"
"buffer_load_dwordx4 v2, s[32:35], s63 offen offset:1024 sc1 lds\n"
"v_mfma_f32_16x16x32_bf16 v[20:23], a[164:167], v[36:39], v[20:23]\n"
"ds_read_b128 v[60:63], v2 offset:50176\n"
"v_mfma_f32_16x16x32_bf16 v[20:23], a[52:55], v[32:35], v[20:23]\n"
"s_waitcnt lgkmcnt(4)\n"
"v_mfma_f32_16x16x32_bf16 v[20:23], v[84:87], v[40:43], v[20:23]\n"
"ds_read_b128 v[32:35], v2 offset:8192\n"
"v_mfma_f32_16x16x32_bf16 v[20:23], v[196:199], v[44:47], v[20:23]\n"
"buffer_load_dwordx4 v2, s[32:35], s63 offen offset:2048 sc1 lds\n"
"v_mfma_f32_16x16x32_bf16 v[16:19], v[192:195], v[44:47], v[16:19]\n"
"ds_read_b128 v[36:39], v2 offset:22528\n"
"v_mfma_f32_16x16x32_bf16 v[16:19], v[80:83], v[40:43], v[16:19]\n"
"s_waitcnt lgkmcnt(4)\n"
"v_mfma_f32_16x16x32_bf16 v[16:19], a[56:59], v[48:51], v[16:19]\n"
"ds_read_b128 v[40:43], v2 offset:36864\n"
"v_mfma_f32_16x16x32_bf16 v[16:19], a[168:171], v[52:55], v[16:19]\n"
"buffer_load_dwordx4 v2, s[32:35], s63 offen offset:3072 sc1 lds\n"
"v_mfma_f32_16x16x32_bf16 v[20:23], a[172:175], v[52:55], v[20:23]\n"
"ds_read_b128 v[44:47], v2 offset:51200\n"
"v_mfma_f32_16x16x32_bf16 v[20:23], a[60:63], v[48:51], v[20:23]\n"
"s_waitcnt lgkmcnt(4)\n"
"v_mfma_f32_16x16x32_bf16 v[20:23], v[92:95], v[56:59], v[20:23]\n"
"ds_read_b128 v[48:51], v2 offset:9216\n"
"v_mfma_f32_16x16x32_bf16 v[20:23], v[204:207], v[60:63], v[20:23]\n"
"s_add_u32 s63, s63, 4096\n"
"s_add_u32 m0, s64, 65536\n"
"v_mfma_f32_16x16x32_bf16 v[16:19], v[200:203], v[60:63], v[16:19]\n"
"ds_read_b128 v[52:55], v2 offset:23552\n"
"v_mfma_f32_16x16x32_bf16 v[16:19], v[88:91], v[56:59], v[16:19]\n"
"s_waitcnt lgkmcnt(4)\n"
"v_mfma_f32_16x16x32_bf16 v[16:19], a[64:67], v[32:35], v[16:19]\n"
"ds_read_b128 v[56:59], v2 offset:37888\n"
"v_mfma_f32_16x16x32_bf16 v[16:19], a[176:179], v[36:39], v[16:19]\n"
"buffer_load_dwordx4 v2, s[32:35], s63 offen offset:0 sc1 lds\n"
"v_mfma_f32_16x16x32_bf16 v[20:23], a[180:183], v[36:39], v[20:23]\n"
"ds_read_b128 v[60:63], v2 offset:52224\n"
"v_mfma_f32_16x16x32_bf16 v[20:23], a[68:71], v[32:35], v[20:23]\n"
"s_waitcnt lgkmcnt(4)\n"
"v_mfma_f32_16x16x32_bf16 v[20:23], v[100:103], v[40:43], v[20:23]\n"
"ds_read_b128 v[32:35], v2 offset:10240\n"
"v_mfma_f32_16x16x32_bf16 v[20:23], v[212:215], v[44:47], v[20:23]\n"
"buffer_load_dwordx4 v2, s[32:35], s63 offen offset:1024 sc1 lds\n"
"v_mfma_f32_16x16x32_bf16 v[16:19], v[208:211], v[44:47], v[16:19]\n"
"ds_read_b128 v[36:39], v2 offset:24576\n"
"v_mfma_f32_16x16x32_bf16 v[16:19], v[96:99], v[40:43], v[16:19]\n"
"s_waitcnt lgkmcnt(4)\n"
"v_mfma_f32_16x16x32_bf16 v[16:19], a[72:75], v[48:51], v[16:19]\n"
"ds_read_b128 v[40:43], v2 offset:38912\n"
"v_mfma_f32_16x16x32_bf16 v[16:19], a[184:187], v[52:55], v[16:19]\n"
"buffer_load_dwordx4 v2, s[32:35], s63 offen offset:2048 sc1 lds\n"
"v_mfma_f32_16x16x32_bf16 v[20:23], a[188:191], v[52:55], v[20:23]\n"
"ds_read_b128 v[44:47], v2 offset:53248\n"
"v_mfma_f32_16x16x32_bf16 v[20:23], a[76:79], v[48:51], v[20:23]\n"
"s_waitcnt lgkmcnt(4)\n"
"v_mfma_f32_16x16x32_bf16 v[20:23], v[108:111], v[56:59], v[20:23]\n"
"ds_read_b128 v[48:51], v2 offset:11264\n"
"v_mfma_f32_16x16x32_bf16 v[20:23], v[220:223], v[60:63], v[20:23]\n"
"v_mfma_f32_16x16x32_bf16 v[16:19], v[216:219], v[60:63], v[16:19]\n"
"ds_read_b128 v[52:55], v2 offset:25600\n"
"v_mfma_f32_16x16x32_bf16 v[16:19], v[104:107], v[56:59], v[16:19]\n"
"s_waitcnt lgkmcnt(4)\n"
"v_mfma_f32_16x16x32_bf16 v[16:19], a[80:83], v[32:35], v[16:19]\n"
"ds_read_b128 v[56:59], v2 offset:39936\n"
"v_mfma_f32_16x16x32_bf16 v[16:19], a[192:195], v[36:39], v[16:19]\n"
"buffer_load_dwordx4 v2, s[32:35], s63 offen offset:3072 sc1 lds\n"
"s_add_u32 s63, s63, 4096\n"
"v_mfma_f32_16x16x32_bf16 v[20:23], a[196:199], v[36:39], v[20:23]\n"
"ds_read_b128 v[60:63], v2 offset:54272\n"
"s_add_u32 m0, s64, 69632\n"
"v_mfma_f32_16x16x32_bf16 v[20:23], a[84:87], v[32:35], v[20:23]\n"
"s_waitcnt lgkmcnt(4)\n"
"v_mfma_f32_16x16x32_bf16 v[20:23], v[116:119], v[40:43], v[20:23]\n"
"ds_read_b128 v[32:35], v2 offset:12288\n"
"v_mfma_f32_16x16x32_bf16 v[20:23], v[228:231], v[44:47], v[20:23]\n"
"buffer_load_dwordx4 v2, s[32:35], s63 offen offset:0 sc1 lds\n"
"v_mfma_f32_16x16x32_bf16 v[16:19], v[224:227], v[44:47], v[16:19]\n"
"ds_read_b128 v[36:39], v2 offset:26624\n"
"v_mfma_f32_16x16x32_bf16 v[16:19], v[112:115], v[40:43], v[16:19]\n"
"s_waitcnt lgkmcnt(4)\n"
"v_mfma_f32_16x16x32_bf16 v[16:19], a[88:91], v[48:51], v[16:19]\n"
"ds_read_b128 v[40:43], v2 offset:40960\n"
"v_mfma_f32_16x16x32_bf16 v[16:19], a[200:203], v[52:55], v[16:19]\n"
"buffer_load_dwordx4 v2, s[32:35], s63 offen offset:1024 sc1 lds\n"
"s_add_u32 s63, s63, 1175552\n"
"v_mfma_f32_16x16x32_bf16 v[20:23], a[204:207], v[52:55], v[20:23]\n"
"ds_read_b128 v[44:47], v2 offset:55296\n"
"v_mfma_f32_16x16x32_bf16 v[20:23], a[92:95], v[48:51], v[20:23]\n"
"s_waitcnt lgkmcnt(4)\n"
"v_mfma_f32_16x16x32_bf16 v[20:23], v[124:127], v[56:59], v[20:23]\n"
"ds_read_b128 v[48:51], v2 offset:13312\n"
"s_waitcnt vmcnt(10)\n"
"v_mfma_f32_16x16x32_bf16 v[20:23], v[236:239], v[60:63], v[20:23]\n"
"v_lshlrev_b32 v24, 16, v12\n"
"v_and_b32 v25, 0xffff0000, v12\n"
"v_mfma_f32_16x16x32_bf16 v[16:19], v[232:235], v[60:63], v[16:19]\n"
"ds_read_b128 v[52:55], v2 offset:27648\n"
"v_lshlrev_b32 v26, 16, v13\n"
"v_mfma_f32_16x16x32_bf16 v[16:19], v[120:123], v[56:59], v[16:19]\n"
"v_and_b32 v27, 0xffff0000, v13\n"
"s_waitcnt lgkmcnt(4)\n"
"v_mfma_f32_16x16x32_bf16 v[16:19], a[96:99], v[32:35], v[16:19]\n"
"ds_read_b128 v[56:59], v2 offset:41984\n"
"v_lshlrev_b32 v28, 16, v14\n"
"v_mfma_f32_16x16x32_bf16 v[16:19], a[208:211], v[36:39], v[16:19]\n"
"s_barrier\n"
"v_mfma_f32_16x16x32_bf16 v[20:23], a[212:215], v[36:39], v[20:23]\n"
"ds_read_b128 v[60:63], v2 offset:56320\n"
"v_and_b32 v29, 0xffff0000, v14\n"
"v_mfma_f32_16x16x32_bf16 v[20:23], a[100:103], v[32:35], v[20:23]\n"
"v_lshlrev_b32 v30, 16, v15\n"
"s_waitcnt lgkmcnt(4)\n"
"v_mfma_f32_16x16x32_bf16 v[20:23], v[132:135], v[40:43], v[20:23]\n"
"ds_read_b128 v[32:35], v4 offset:0\n"
"v_and_b32 v31, 0xffff0000, v15\n"
"v_mfma_f32_16x16x32_bf16 v[20:23], v[244:247], v[44:47], v[20:23]\n"
"v_mfma_f32_16x16x32_bf16 v[16:19], v[240:243], v[44:47], v[16:19]\n"
"ds_read_b128 v[36:39], v4 offset:14336\n"
"v_mfma_f32_16x16x32_bf16 v[16:19], v[128:131], v[40:43], v[16:19]\n"
"s_waitcnt lgkmcnt(4)\n"
"v_mfma_f32_16x16x32_bf16 v[16:19], a[104:107], v[48:51], v[16:19]\n"
"ds_read_b128 v[40:43], v4 offset:28672\n"
"v_mfma_f32_16x16x32_bf16 v[16:19], a[216:219], v[52:55], v[16:19]\n"
"v_mfma_f32_16x16x32_bf16 v[20:23], a[220:223], v[52:55], v[20:23]\n"
"ds_read_b128 v[44:47], v4 offset:43008\n"
"v_mfma_f32_16x16x32_bf16 v[20:23], a[108:111], v[48:51], v[20:23]\n"
"s_waitcnt lgkmcnt(4)\n"
"v_mfma_f32_16x16x32_bf16 v[20:23], v[140:143], v[56:59], v[20:23]\n"
"ds_read_b128 v[48:51], v4 offset:1024\n"
"v_mfma_f32_16x16x32_bf16 v[20:23], v[252:255], v[60:63], v[20:23]\n"
"v_mfma_f32_16x16x32_bf16 v[16:19], v[248:251], v[60:63], v[16:19]\n"
"ds_read_b128 v[52:55], v4 offset:15360\n"
"v_mfma_f32_16x16x32_bf16 v[16:19], v[136:139], v[56:59], v[16:19]\n"
"s_branch .L_dml_2\n"
".L_dmt_2:\n"
"s_nop 1\n"
"s_nop 0\n"
"v_cvt_pk_bf16_f32 v15, v30, v31\n"
"v_cvt_pk_bf16_f32 v14, v28, v29\n"
"s_nop 1\n"
"v_cvt_pk_bf16_f32 v13, v26, v27\n"
"v_cvt_pk_bf16_f32 v12, v24, v25\n"
"ds_write_b128 v5, v[12:15]\n"
"ds_read2st64_b32 v[12:13], v3 offset0:0 offset1:1\n"
"ds_read2st64_b32 v[14:15], v3 offset0:2 offset1:3\n"
"s_waitcnt lgkmcnt(0)\n"
"buffer_atomic_pk_add_bf16 v12, v1, s[44:47], s65 offen offset:0 sc1\n"
"buffer_atomic_pk_add_bf16 v13, v1, s[44:47], s65 offen offset:2048 sc1\n"
"s_add_u32 s65, s65, 16384\n"
"buffer_atomic_pk_add_bf16 v14, v1, s[44:47], s65 offen offset:0 sc1\n"
"buffer_atomic_pk_add_bf16 v15, v1, s[44:47], s65 offen offset:2048 sc1\n"
"s_add_u32 s65, s65, 16384\n"
"s_branch .L_end\n"
".p2align 6\n"
".L_dlv3:\n"
".L_dll_3:\n"
"s_waitcnt lgkmcnt(4)\n"
"v_mfma_f32_16x16x32_bf16 v[16:19], a[0:3], v[32:35], v[16:19]\n"
"ds_read_b128 v[56:59], v2 offset:29696\n"
"v_mfma_f32_16x16x32_bf16 v[16:19], a[112:115], v[36:39], v[16:19]\n"
"v_mfma_f32_16x16x32_bf16 v[20:23], a[116:119], v[36:39], v[20:23]\n"
"ds_read_b128 v[60:63], v2 offset:44032\n"
"v_mfma_f32_16x16x32_bf16 v[20:23], a[4:7], v[32:35], v[20:23]\n"
"s_add_u32 s68, s68, 1187840\n"
"s_waitcnt lgkmcnt(4)\n"
"v_mfma_f32_16x16x32_bf16 v[20:23], a[228:231], v[40:43], v[20:23]\n"
"ds_read_b128 v[32:35], v2 offset:2048\n"
"v_mfma_f32_16x16x32_bf16 v[20:23], v[148:151], v[44:47], v[20:23]\n"
"buffer_store_dwordx4 v[28:31], v2, s[32:35], s68 offen offset:1024 sc1\n"
"v_mfma_f32_16x16x32_bf16 v[16:19], v[144:147], v[44:47], v[16:19]\n"
"ds_read_b128 v[36:39], v2 offset:16384\n"
"v_mfma_f32_16x16x32_bf16 v[16:19], a[224:227], v[40:43], v[16:19]\n"
"s_waitcnt lgkmcnt(4)\n"
"v_mfma_f32_16x16x32_bf16 v[16:19], a[8:11], v[48:51], v[16:19]\n"
"ds_read_b128 v[40:43], v2 offset:30720\n"
"v_mfma_f32_16x16x32_bf16 v[16:19], a[120:123], v[52:55], v[16:19]\n"
"buffer_store_dwordx4 v[24:27], v2, s[32:35], s68 offen sc1\n"
"s_mov_b32 s67, s68\n"
"v_mfma_f32_16x16x32_bf16 v[20:23], a[124:127], v[52:55], v[20:23]\n"
"ds_read_b128 v[44:47], v2 offset:45056\n"
"v_mfma_f32_16x16x32_bf16 v[20:23], a[12:15], v[48:51], v[20:23]\n"
"s_waitcnt lgkmcnt(4)\n"
"v_mfma_f32_16x16x32_bf16 v[20:23], a[236:239], v[56:59], v[20:23]\n"
"ds_read_b128 v[48:51], v2 offset:3072\n"
"v_mfma_f32_16x16x32_bf16 v[20:23], v[156:159], v[60:63], v[20:23]\n"
"v_mfma_f32_16x16x32_bf16 v[16:19], v[152:155], v[60:63], v[16:19]\n"
"ds_read_b128 v[52:55], v2 offset:17408\n"
"v_mfma_f32_16x16x32_bf16 v[16:19], a[232:235], v[56:59], v[16:19]\n"
"s_waitcnt lgkmcnt(4)\n"
"v_mfma_f32_16x16x32_bf16 v[16:19], a[16:19], v[32:35], v[16:19]\n"
"s_waitcnt vmcnt(2)\n"
"s_barrier\n"
"v_mfma_f32_16x16x32_bf16 v[16:19], a[128:131], v[36:39], v[16:19]\n"
"ds_read_b128 v[56:59], v2 offset:31744\n"
"v_mfma_f32_16x16x32_bf16 v[20:23], a[132:135], v[36:39], v[20:23]\n"
"ds_read_b128 v[60:63], v2 offset:46080\n"
"v_mfma_f32_16x16x32_bf16 v[20:23], a[20:23], v[32:35], v[20:23]\n"
"s_waitcnt lgkmcnt(4)\n"
"v_mfma_f32_16x16x32_bf16 v[20:23], a[244:247], v[40:43], v[20:23]\n"
"ds_read_b128 v[32:35], v2 offset:4096\n"
"s_add_u32 m0, s64, 57344\n"
"v_mfma_f32_16x16x32_bf16 v[20:23], v[164:167], v[44:47], v[20:23]\n"
"buffer_load_dwordx4 v2, s[32:35], s63 offen offset:0 sc1 lds\n"
"v_mfma_f32_16x16x32_bf16 v[16:19], v[160:163], v[44:47], v[16:19]\n"
"ds_read_b128 v[36:39], v2 offset:18432\n"
"v_mfma_f32_16x16x32_bf16 v[16:19], a[240:243], v[40:43], v[16:19]\n"
"s_waitcnt lgkmcnt(4)\n"
"v_mfma_f32_16x16x32_bf16 v[16:19], a[24:27], v[48:51], v[16:19]\n"
"ds_read_b128 v[40:43], v2 offset:32768\n"
"v_mfma_f32_16x16x32_bf16 v[16:19], a[136:139], v[52:55], v[16:19]\n"
"v_mfma_f32_16x16x32_bf16 v[20:23], a[140:143], v[52:55], v[20:23]\n"
"ds_read_b128 v[44:47], v2 offset:47104\n"
"v_mfma_f32_16x16x32_bf16 v[20:23], a[28:31], v[48:51], v[20:23]\n"
"s_waitcnt lgkmcnt(4)\n"
"v_mfma_f32_16x16x32_bf16 v[20:23], a[252:255], v[56:59], v[20:23]\n"
"ds_read_b128 v[48:51], v2 offset:5120\n"
"v_mfma_f32_16x16x32_bf16 v[20:23], v[172:175], v[60:63], v[20:23]\n"
"buffer_load_dwordx4 v2, s[32:35], s63 offen offset:1024 sc1 lds\n"
"v_mfma_f32_16x16x32_bf16 v[16:19], v[168:171], v[60:63], v[16:19]\n"
"ds_read_b128 v[52:55], v2 offset:19456\n"
"v_mfma_f32_16x16x32_bf16 v[16:19], a[248:251], v[56:59], v[16:19]\n"
"s_waitcnt lgkmcnt(4)\n"
"v_mfma_f32_16x16x32_bf16 v[16:19], a[32:35], v[32:35], v[16:19]\n"
"ds_read_b128 v[56:59], v2 offset:33792\n"
"v_mfma_f32_16x16x32_bf16 v[16:19], a[144:147], v[36:39], v[16:19]\n"
"buffer_load_dwordx4 v2, s[32:35], s63 offen offset:2048 sc1 lds\n"
"v_mfma_f32_16x16x32_bf16 v[20:23], a[148:151], v[36:39], v[20:23]\n"
"ds_read_b128 v[60:63], v2 offset:48128\n"
"v_mfma_f32_16x16x32_bf16 v[20:23], a[36:39], v[32:35], v[20:23]\n"
"s_waitcnt lgkmcnt(4)\n"
"v_mfma_f32_16x16x32_bf16 v[20:23], v[68:71], v[40:43], v[20:23]\n"
"ds_read_b128 v[32:35], v2 offset:6144\n"
"v_mfma_f32_16x16x32_bf16 v[20:23], v[180:183], v[44:47], v[20:23]\n"
"buffer_load_dwordx4 v2, s[32:35], s63 offen offset:3072 sc1 lds\n"
"v_mfma_f32_16x16x32_bf16 v[16:19], v[176:179], v[44:47], v[16:19]\n"
"ds_read_b128 v[36:39], v2 offset:20480\n"
"v_mfma_f32_16x16x32_bf16 v[16:19], v[64:67], v[40:43], v[16:19]\n"
"s_waitcnt lgkmcnt(4)\n"
"v_mfma_f32_16x16x32_bf16 v[16:19], a[40:43], v[48:51], v[16:19]\n"
"ds_read_b128 v[40:43], v2 offset:34816\n"
"v_mfma_f32_16x16x32_bf16 v[16:19], a[152:155], v[52:55], v[16:19]\n"
"buffer_load_dwordx4 v[12:15], v2, s[32:35], s71 offen sc1\n"
"s_add_u32 s71, s71, 1187840\n"
"v_mfma_f32_16x16x32_bf16 v[20:23], a[156:159], v[52:55], v[20:23]\n"
"ds_read_b128 v[44:47], v2 offset:49152\n"
"s_add_u32 s63, s63, 4096\n"
"v_mfma_f32_16x16x32_bf16 v[20:23], a[44:47], v[48:51], v[20:23]\n"
"s_add_u32 m0, s64, 61440\n"
"s_waitcnt lgkmcnt(4)\n"
"v_mfma_f32_16x16x32_bf16 v[20:23], v[76:79], v[56:59], v[20:23]\n"
"ds_read_b128 v[48:51], v2 offset:7168\n"
"v_mfma_f32_16x16x32_bf16 v[20:23], v[188:191], v[60:63], v[20:23]\n"
"buffer_load_dwordx4 v2, s[32:35], s63 offen offset:0 sc1 lds\n"
"v_mfma_f32_16x16x32_bf16 v[16:19], v[184:187], v[60:63], v[16:19]\n"
"ds_read_b128 v[52:55], v2 offset:21504\n"
"v_mfma_f32_16x16x32_bf16 v[16:19], v[72:75], v[56:59], v[16:19]\n"
"s_waitcnt lgkmcnt(4)\n"
"v_mfma_f32_16x16x32_bf16 v[16:19], a[48:51], v[32:35], v[16:19]\n"
"ds_read_b128 v[56:59], v2 offset:35840\n"
"v_mfma_f32_16x16x32_bf16 v[16:19], a[160:163], v[36:39], v[16:19]\n"
"buffer_load_dwordx4 v2, s[32:35], s63 offen offset:1024 sc1 lds\n"
"v_mfma_f32_16x16x32_bf16 v[20:23], a[164:167], v[36:39], v[20:23]\n"
"ds_read_b128 v[60:63], v2 offset:50176\n"
"v_mfma_f32_16x16x32_bf16 v[20:23], a[52:55], v[32:35], v[20:23]\n"
"s_waitcnt lgkmcnt(4)\n"
"v_mfma_f32_16x16x32_bf16 v[20:23], v[84:87], v[40:43], v[20:23]\n"
"ds_read_b128 v[32:35], v2 offset:8192\n"
"v_mfma_f32_16x16x32_bf16 v[20:23], v[196:199], v[44:47], v[20:23]\n"
"buffer_load_dwordx4 v2, s[32:35], s63 offen offset:2048 sc1 lds\n"
"v_mfma_f32_16x16x32_bf16 v[16:19], v[192:195], v[44:47], v[16:19]\n"
"ds_read_b128 v[36:39], v2 offset:22528\n"
"v_mfma_f32_16x16x32_bf16 v[16:19], v[80:83], v[40:43], v[16:19]\n"
"s_waitcnt lgkmcnt(4)\n"
"v_mfma_f32_16x16x32_bf16 v[16:19], a[56:59], v[48:51], v[16:19]\n"
"ds_read_b128 v[40:43], v2 offset:36864\n"
"v_mfma_f32_16x16x32_bf16 v[16:19], a[168:171], v[52:55], v[16:19]\n"
"buffer_load_dwordx4 v2, s[32:35], s63 offen offset:3072 sc1 lds\n"
"v_mfma_f32_16x16x32_bf16 v[20:23], a[172:175], v[52:55], v[20:23]\n"
"ds_read_b128 v[44:47], v2 offset:51200\n"
"v_mfma_f32_16x16x32_bf16 v[20:23], a[60:63], v[48:51], v[20:23]\n"
"s_waitcnt lgkmcnt(4)\n"
"v_mfma_f32_16x16x32_bf16 v[20:23], v[92:95], v[56:59], v[20:23]\n"
"ds_read_b128 v[48:51], v2 offset:9216\n"
"v_mfma_f32_16x16x32_bf16 v[20:23], v[204:207], v[60:63], v[20:23]\n"
"s_add_u32 s63, s63, 4096\n"
"s_add_u32 m0, s64, 65536\n"
"v_mfma_f32_16x16x32_bf16 v[16:19], v[200:203], v[60:63], v[16:19]\n"
"ds_read_b128 v[52:55], v2 offset:23552\n"
"v_mfma_f32_16x16x32_bf16 v[16:19], v[88:91], v[56:59], v[16:19]\n"
"s_waitcnt lgkmcnt(4)\n"
"v_mfma_f32_16x16x32_bf16 v[16:19], a[64:67], v[32:35], v[16:19]\n"
"ds_read_b128 v[56:59], v2 offset:37888\n"
"v_mfma_f32_16x16x32_bf16 v[16:19], a[176:179], v[36:39], v[16:19]\n"
"buffer_load_dwordx4 v2, s[32:35], s63 offen offset:0 sc1 lds\n"
"v_mfma_f32_16x16x32_bf16 v[20:23], a[180:183], v[36:39], v[20:23]\n"
"ds_read_b128 v[60:63], v2 offset:52224\n"
"v_mfma_f32_16x16x32_bf16 v[20:23], a[68:71], v[32:35], v[20:23]\n"
"s_waitcnt lgkmcnt(4)\n"
"v_mfma_f32_16x16x32_bf16 v[20:23], v[100:103], v[40:43], v[20:23]\n"
"ds_read_b128 v[32:35], v2 offset:10240\n"
"v_mfma_f32_16x16x32_bf16 v[20:23], v[212:215], v[44:47], v[20:23]\n"
"buffer_load_dwordx4 v2, s[32:35], s63 offen offset:1024 sc1 lds\n"
"v_mfma_f32_16x16x32_bf16 v[16:19], v[208:211], v[44:47], v[16:19]\n"
"ds_read_b128 v[36:39], v2 offset:24576\n"
"v_mfma_f32_16x16x32_bf16 v[16:19], v[96:99], v[40:43], v[16:19]\n"
"s_waitcnt lgkmcnt(4)\n"
"v_mfma_f32_16x16x32_bf16 v[16:19], a[72:75], v[48:51], v[16:19]\n"
"ds_read_b128 v[40:43], v2 offset:38912\n"
"v_mfma_f32_16x16x32_bf16 v[16:19], a[184:187], v[52:55], v[16:19]\n"
"buffer_load_dwordx4 v2, s[32:35], s63 offen offset:2048 sc1 lds\n"
"v_mfma_f32_16x16x32_bf16 v[20:23], a[188:191], v[52:55], v[20:23]\n"
"ds_read_b128 v[44:47], v2 offset:53248\n"
"v_mfma_f32_16x16x32_bf16 v[20:23], a[76:79], v[48:51], v[20:23]\n"
"s_waitcnt lgkmcnt(4)\n"
"v_mfma_f32_16x16x32_bf16 v[20:23], v[108:111], v[56:59], v[20:23]\n"
"ds_read_b128 v[48:51], v2 offset:11264\n"
"v_mfma_f32_16x16x32_bf16 v[20:23], v[220:223], v[60:63], v[20:23]\n"
"v_mfma_f32_16x16x32_bf16 v[16:19], v[216:219], v[60:63], v[16:19]\n"
"ds_read_b128 v[52:55], v2 offset:25600\n"
"v_mfma_f32_16x16x32_bf16 v[16:19], v[104:107], v[56:59], v[16:19]\n"
"s_waitcnt lgkmcnt(4)\n"
"v_mfma_f32_16x16x32_bf16 v[16:19], a[80:83], v[32:35], v[16:19]\n"
"ds_read_b128 v[56:59], v2 offset:39936\n"
"v_mfma_f32_16x16x32_bf16 v[16:19], a[192:195], v[36:39], v[16:19]\n"
"buffer_load_dwordx4 v2, s[32:35], s63 offen offset:3072 sc1 lds\n"
"s_add_u32 s63, s63, 4096\n"
"v_mfma_f32_16x16x32_bf16 v[20:23], a[196:199], v[36:39], v[20:23]\n"
"ds_read_b128 v[60:63], v2 offset:54272\n"
"s_add_u32 m0, s64, 69632\n"
"v_mfma_f32_16x16x32_bf16 v[20:23], a[84:87], v[32:35], v[20:23]\n"
"s_waitcnt lgkmcnt(4)\n"
"v_mfma_f32_16x16x32_bf16 v[20:23], v[116:119], v[40:43], v[20:23]\n"
"ds_read_b128 v[32:35], v2 offset:12288\n"
"v_mfma_f32_16x16x32_bf16 v[20:23], v[228:231], v[44:47], v[20:23]\n"
"buffer_load_dwordx4 v2, s[32:35], s63 offen offset:0 sc1 lds\n"
"v_mfma_f32_16x16x32_bf16 v[16:19], v[224:227], v[44:47], v[16:19]\n"
"ds_read_b128 v[36:39], v2 offset:26624\n"
"v_mfma_f32_16x16x32_bf16 v[16:19], v[112:115], v[40:43], v[16:19]\n"
"s_waitcnt lgkmcnt(4)\n"
"v_mfma_f32_16x16x32_bf16 v[16:19], a[88:91], v[48:51], v[16:19]\n"
"ds_read_b128 v[40:43], v2 offset:40960\n"
"v_mfma_f32_16x16x32_bf16 v[16:19], a[200:203], v[52:55], v[16:19]\n"
"buffer_load_dwordx4 v2, s[32:35], s63 offen offset:1024 sc1 lds\n"
"s_add_u32 s63, s63, 1175552\n"
"v_mfma_f32_16x16x32_bf16 v[20:23], a[204:207], v[52:55], v[20:23]\n"
"ds_read_b128 v[44:47], v2 offset:55296\n"
"v_mfma_f32_16x16x32_bf16 v[20:23], a[92:95], v[48:51], v[20:23]\n"
"s_waitcnt lgkmcnt(4)\n"
"v_mfma_f32_16x16x32_bf16 v[20:23], v[124:127], v[56:59], v[20:23]\n"
"ds_read_b128 v[48:51], v2 offset:13312\n"
"s_waitcnt vmcnt(10)\n"
"v_mfma_f32_16x16x32_bf16 v[20:23], v[236:239], v[60:63], v[20:23]\n"
"v_lshlrev_b32 v24, 16, v12\n"
"v_and_b32 v25, 0xffff0000, v12\n"
"v_mfma_f32_16x16x32_bf16 v[16:19], v[232:235], v[60:63], v[16:19]\n"
"ds_read_b128 v[52:55], v2 offset:27648\n"
"v_lshlrev_b32 v26, 16, v13\n"
"v_mfma_f32_16x16x32_bf16 v[16:19], v[120:123], v[56:59], v[16:19]\n"
"v_and_b32 v27, 0xffff0000, v13\n"
"s_waitcnt lgkmcnt(4)\n"
"v_mfma_f32_16x16x32_bf16 v[16:19], a[96:99], v[32:35], v[16:19]\n"
"s_barrier\n"
"v_mfma_f32_16x16x32_bf16 v[16:19], a[208:211], v[36:39], v[16:19]\n"
"ds_read_b128 v[56:59], v2 offset:41984\n"
"v_lshlrev_b32 v28, 16, v14\n"
"v_mfma_f32_16x16x32_bf16 v[20:23], a[212:215], v[36:39], v[20:23]\n"
"ds_read_b128 v[60:63], v2 offset:56320\n"
"v_and_b32 v29, 0xffff0000, v14\n"
"v_mfma_f32_16x16x32_bf16 v[20:23], a[100:103], v[32:35], v[20:23]\n"
"v_lshlrev_b32 v30, 16, v15\n"
"s_waitcnt lgkmcnt(4)\n"
"v_mfma_f32_16x16x32_bf16 v[20:23], v[132:135], v[40:43], v[20:23]\n"
"ds_read_b128 v[32:35], v4 offset:0\n"
"v_and_b32 v31, 0xffff0000, v15\n"
"v_mfma_f32_16x16x32_bf16 v[20:23], v[244:247], v[44:47], v[20:23]\n"
"v_mfma_f32_16x16x32_bf16 v[16:19], v[240:243], v[44:47], v[16:19]\n"
"ds_read_b128 v[36:39], v4 offset:14336\n"
"v_mfma_f32_16x16x32_bf16 v[16:19], v[128:131], v[40:43], v[16:19]\n"
"s_waitcnt lgkmcnt(4)\n"
"v_mfma_f32_16x16x32_bf16 v[16:19], a[104:107], v[48:51], v[16:19]\n"
"ds_read_b128 v[40:43], v4 offset:28672\n"
"v_mfma_f32_16x16x32_bf16 v[16:19], a[216:219], v[52:55], v[16:19]\n"
"v_mfma_f32_16x16x32_bf16 v[20:23], a[220:223], v[52:55], v[20:23]\n"
"ds_read_b128 v[44:47], v4 offset:43008\n"
"v_mfma_f32_16x16x32_bf16 v[20:23], a[108:111], v[48:51], v[20:23]\n"
"s_waitcnt lgkmcnt(4)\n"
"v_mfma_f32_16x16x32_bf16 v[20:23], v[140:143], v[56:59], v[20:23]\n"
"ds_read_b128 v[48:51], v4 offset:1024\n"
"v_mfma_f32_16x16x32_bf16 v[20:23], v[252:255], v[60:63], v[20:23]\n"
"v_mfma_f32_16x16x32_bf16 v[16:19], v[248:251], v[60:63], v[16:19]\n"
"ds_read_b128 v[52:55], v4 offset:15360\n"
"s_sub_u32 s62, s62, 1\n"
"v_mfma_f32_16x16x32_bf16 v[16:19], v[136:139], v[56:59], v[16:19]\n"
"s_cbranch_scc1 .L_dlb_3\n"
"s_waitcnt lgkmcnt(4)\n"
"v_mfma_f32_16x16x32_bf16 v[24:27], a[0:3], v[32:35], v[24:27]\n"
"ds_read_b128 v[56:59], v4 offset:29696\n"
"v_mfma_f32_16x16x32_bf16 v[24:27], a[112:115], v[36:39], v[24:27]\n"
"v_mfma_f32_16x16x32_bf16 v[28:31], a[116:119], v[36:39], v[28:31]\n"
"ds_read_b128 v[60:63], v4 offset:44032\n"
"v_mfma_f32_16x16x32_bf16 v[28:31], a[4:7], v[32:35], v[28:31]\n"
"s_add_u32 s68, s68, 1187840\n"
"s_waitcnt lgkmcnt(4)\n"
"v_mfma_f32_16x16x32_bf16 v[28:31], a[228:231], v[40:43], v[28:31]\n"
"ds_read_b128 v[32:35], v4 offset:2048\n"
"v_mfma_f32_16x16x32_bf16 v[28:31], v[148:151], v[44:47], v[28:31]\n"
"buffer_store_dwordx4 v[20:23], v2, s[32:35], s68 offen offset:1024 sc1\n"
"v_mfma_f32_16x16x32_bf16 v[24:27], v[144:147], v[44:47], v[24:27]\n"
"ds_read_b128 v[36:39], v4 offset:16384\n"
"v_mfma_f32_16x16x32_bf16 v[24:27], a[224:227], v[40:43], v[24:27]\n"
"s_waitcnt lgkmcnt(4)\n"
"v_mfma_f32_16x16x32_bf16 v[24:27], a[8:11], v[48:51], v[24:27]\n"
"ds_read_b128 v[40:43], v4 offset:30720\n"
"v_mfma_f32_16x16x32_bf16 v[24:27], a[120:123], v[52:55], v[24:27]\n"
"buffer_store_dwordx4 v[16:19], v2, s[32:35], s68 offen sc1\n"
"s_mov_b32 s67, s68\n"
"v_mfma_f32_16x16x32_bf16 v[28:31], a[124:127], v[52:55], v[28:31]\n"
"ds_read_b128 v[44:47], v4 offset:45056\n"
"v_mfma_f32_16x16x32_bf16 v[28:31], a[12:15], v[48:51], v[28:31]\n"
"s_waitcnt lgkmcnt(4)\n"
"v_mfma_f32_16x16x32_bf16 v[28:31], a[236:239], v[56:59], v[28:31]\n"
"ds_read_b128 v[48:51], v4 offset:3072\n"
"v_mfma_f32_16x16x32_bf16 v[28:31], v[156:159], v[60:63], v[28:31]\n"
"v_mfma_f32_16x16x32_bf16 v[24:27], v[152:155], v[60:63], v[24:27]\n"
"ds_read_b128 v[52:55], v4 offset:17408\n"
"v_mfma_f32_16x16x32_bf16 v[24:27], a[232:235], v[56:59], v[24:27]\n"
"s_waitcnt lgkmcnt(4)\n"
"v_mfma_f32_16x16x32_bf16 v[24:27], a[16:19], v[32:35], v[24:27]\n"
"s_waitcnt vmcnt(2)\n"
"s_barrier\n"
"v_mfma_f32_16x16x32_bf16 v[24:27], a[128:131], v[36:39], v[24:27]\n"
"ds_read_b128 v[56:59], v4 offset:31744\n"
"v_mfma_f32_16x16x32_bf16 v[28:31], a[132:135], v[36:39], v[28:31]\n"
"ds_read_b128 v[60:63], v4 offset:46080\n"
"v_mfma_f32_16x16x32_bf16 v[28:31], a[20:23], v[32:35], v[28:31]\n"
"s_waitcnt lgkmcnt(4)\n"
"v_mfma_f32_16x16x32_bf16 v[28:31], a[244:247], v[40:43], v[28:31]\n"
"ds_read_b128 v[32:35], v4 offset:4096\n"
"s_add_u32 m0, s64, 0\n"
"v_mfma_f32_16x16x32_bf16 v[28:31], v[164:167], v[44:47], v[28:31]\n"
"buffer_load_dwordx4 v2, s[32:35], s63 offen offset:0 sc1 lds\n"
"v_mfma_f32_16x16x32_bf16 v[24:27], v[160:163], v[44:47], v[24:27]\n"
"ds_read_b128 v[36:39], v4 offset:18432\n"
"v_mfma_f32_16x16x32_bf16 v[24:27], a[240:243], v[40:43], v[24:27]\n"
"s_waitcnt lgkmcnt(4)\n"
"v_mfma_f32_16x16x32_bf16 v[24:27], a[24:27], v[48:51], v[24:27]\n"
"ds_read_b128 v[40:43], v4 offset:32768\n"
"v_mfma_f32_16x16x32_bf16 v[24:27], a[136:139], v[52:55], v[24:27]\n"
"v_mfma_f32_16x16x32_bf16 v[28:31], a[140:143], v[52:55], v[28:31]\n"
"ds_read_b128 v[44:47], v4 offset:47104\n"
"v_mfma_f32_16x16x32_bf16 v[28:31], a[28:31], v[48:51], v[28:31]\n"
"s_waitcnt lgkmcnt(4)\n"
"v_mfma_f32_16x16x32_bf16 v[28:31], a[252:255], v[56:59], v[28:31]\n"
"ds_read_b128 v[48:51], v4 offset:5120\n"
"v_mfma_f32_16x16x32_bf16 v[28:31], v[172:175], v[60:63], v[28:31]\n"
"buffer_load_dwordx4 v2, s[32:35], s63 offen offset:1024 sc1 lds\n"
"v_mfma_f32_16x16x32_bf16 v[24:27], v[168:171], v[60:63], v[24:27]\n"
"ds_read_b128 v[52:55], v4 offset:19456\n"
"v_mfma_f32_16x16x32_bf16 v[24:27], a[248:251], v[56:59], v[24:27]\n"
"s_waitcnt lgkmcnt(4)\n"
"v_mfma_f32_16x16x32_bf16 v[24:27], a[32:35], v[32:35], v[24:27]\n"
"ds_read_b128 v[56:59], v4 offset:33792\n"
"v_mfma_f32_16x16x32_bf16 v[24:27], a[144:147], v[36:39], v[24:27]\n"
"buffer_load_dwordx4 v2, s[32:35], s63 offen offset:2048 sc1 lds\n"
"v_mfma_f32_16x16x32_bf16 v[28:31], a[148:151], v[36:39], v[28:31]\n"
"ds_read_b128 v[60:63], v4 offset:48128\n"
"v_mfma_f32_16x16x32_bf16 v[28:31], a[36:39], v[32:35], v[28:31]\n"
"s_waitcnt lgkmcnt(4)\n"
"v_mfma_f32_16x16x32_bf16 v[28:31], v[68:71], v[40:43], v[28:31]\n"
"ds_read_b128 v[32:35], v4 offset:6144\n"
"v_mfma_f32_16x16x32_bf16 v[28:31], v[180:183], v[44:47], v[28:31]\n"
"buffer_load_dwordx4 v2, s[32:35], s63 offen offset:3072 sc1 lds\n"
"v_mfma_f32_16x16x32_bf16 v[24:27], v[176:179], v[44:47], v[24:27]\n"
"ds_read_b128 v[36:39], v4 offset:20480\n"
"v_mfma_f32_16x16x32_bf16 v[24:27], v[64:67], v[40:43], v[24:27]\n"
"s_waitcnt lgkmcnt(4)\n"
"v_mfma_f32_16x16x32_bf16 v[24:27], a[40:43], v[48:51], v[24:27]\n"
"ds_read_b128 v[40:43], v4 offset:34816\n"
"v_mfma_f32_16x16x32_bf16 v[24:27], a[152:155], v[52:55], v[24:27]\n"
"buffer_load_dwordx4 v[12:15], v2, s[32:35], s71 offen sc1\n"
"s_add_u32 s71, s71, 1187840\n"
"v_mfma_f32_16x16x32_bf16 v[28:31], a[156:159], v[52:55], v[28:31]\n"
"ds_read_b128 v[44:47], v4 offset:49152\n"
"s_add_u32 s63, s63, 4096\n"
"v_mfma_f32_16x16x32_bf16 v[28:31], a[44:47], v[48:51], v[28:31]\n"
"s_add_u32 m0, s64, 4096\n"
"s_waitcnt lgkmcnt(4)\n"
"v_mfma_f32_16x16x32_bf16 v[28:31], v[76:79], v[56:59], v[28:31]\n"
"ds_read_b128 v[48:51], v4 offset:7168\n"
"v_mfma_f32_16x16x32_bf16 v[28:31], v[188:191], v[60:63], v[28:31]\n"
"buffer_load_dwordx4 v2, s[32:35], s63 offen offset:0 sc1 lds\n"
"v_mfma_f32_16x16x32_bf16 v[24:27], v[184:187], v[60:63], v[24:27]\n"
"ds_read_b128 v[52:55], v4 offset:21504\n"
"v_mfma_f32_16x16x32_bf16 v[24:27], v[72:75], v[56:59], v[24:27]\n"
"s_waitcnt lgkmcnt(4)\n"
"v_mfma_f32_16x16x32_bf16 v[24:27], a[48:51], v[32:35], v[24:27]\n"
"ds_read_b128 v[56:59], v4 offset:35840\n"
"v_mfma_f32_16x16x32_bf16 v[24:27], a[160:163], v[36:39], v[24:27]\n"
"buffer_load_dwordx4 v2, s[32:35], s63 offen offset:1024 sc1 lds\n"
"v_mfma_f32_16x16x32_bf16 v[28:31], a[164:167], v[36:39], v[28:31]\n"
"ds_read_b128 v[60:63], v4 offset:50176\n"
"v_mfma_f32_16x16x32_bf16 v[28:31], a[52:55], v[32:35], v[28:31]\n"
"s_waitcnt lgkmcnt(4)\n"
"v_mfma_f32_16x16x32_bf16 v[28:31], v[84:87], v[40:43], v[28:31]\n"
"ds_read_b128 v[32:35], v4 offset:8192\n"
"v_mfma_f32_16x16x32_bf16 v[28:31], v[196:199], v[44:47], v[28:31]\n"
"buffer_load_dwordx4 v2, s[32:35], s63 offen offset:2048 sc1 lds\n"
"v_mfma_f32_16x16x32_bf16 v[24:27], v[192:195], v[44:47], v[24:27]\n"
"ds_read_b128 v[36:39], v4 offset:22528\n"
"v_mfma_f32_16x16x32_bf16 v[24:27], v[80:83], v[40:43], v[24:27]\n"
"s_waitcnt lgkmcnt(4)\n"
"v_mfma_f32_16x16x32_bf16 v[24:27], a[56:59], v[48:51], v[24:27]\n"
"ds_read_b128 v[40:43], v4 offset:36864\n"
"v_mfma_f32_16x16x32_bf16 v[24:27], a[168:171], v[52:55], v[24:27]\n"
"buffer_load_dwordx4 v2, s[32:35], s63 offen offset:3072 sc1 lds\n"
"v_mfma_f32_16x16x32_bf16 v[28:31], a[172:175], v[52:55], v[28:31]\n"
"ds_read_b128 v[44:47], v4 offset:51200\n"
"v_mfma_f32_16x16x32_bf16 v[28:31], a[60:63], v[48:51], v[28:31]\n"
"s_waitcnt lgkmcnt(4)\n"
"v_mfma_f32_16x16x32_bf16 v[28:31], v[92:95], v[56:59], v[28:31]\n"
"ds_read_b128 v[48:51], v4 offset:9216\n"
"v_mfma_f32_16x16x32_bf16 v[28:31], v[204:207], v[60:63], v[28:31]\n"
"s_add_u32 s63, s63, 4096\n"
"s_add_u32 m0, s64, 8192\n"
"v_mfma_f32_16x16x32_bf16 v[24:27], v[200:203], v[60:63], v[24:27]\n"
"ds_read_b128 v[52:55], v4 offset:23552\n"
"v_mfma_f32_16x16x32_bf16 v[24:27], v[88:91], v[56:59], v[24:27]\n"
"s_waitcnt lgkmcnt(4)\n"
"v_mfma_f32_16x16x32_bf16 v[24:27], a[64:67], v[32:35], v[24:27]\n"
"ds_read_b128 v[56:59], v4 offset:37888\n"
"v_mfma_f32_16x16x32_bf16 v[24:27], a[176:179], v[36:39], v[24:27]\n"
"buffer_load_dwordx4 v2, s[32:35], s63 offen offset:0 sc1 lds\n"
"v_mfma_f32_16x16x32_bf16 v[28:31], a[180:183], v[36:39], v[28:31]\n"
"ds_read_b128 v[60:63], v4 offset:52224\n"
"v_mfma_f32_16x16x32_bf16 v[28:31], a[68:71], v[32:35], v[28:31]\n"
"s_waitcnt lgkmcnt(4)\n"
"v_mfma_f32_16x16x32_bf16 v[28:31], v[100:103], v[40:43], v[28:31]\n"
"ds_read_b128 v[32:35], v4 offset:10240\n"
"v_mfma_f32_16x16x32_bf16 v[28:31], v[212:215], v[44:47], v[28:31]\n"
"buffer_load_dwordx4 v2, s[32:35], s63 offen offset:1024 sc1 lds\n"
"v_mfma_f32_16x16x32_bf16 v[24:27], v[208:211], v[44:47], v[24:27]\n"
"ds_read_b128 v[36:39], v4 offset:24576\n"
"v_mfma_f32_16x16x32_bf16 v[24:27], v[96:99], v[40:43], v[24:27]\n"
"s_waitcnt lgkmcnt(4)\n"
"v_mfma_f32_16x16x32_bf16 v[24:27], a[72:75], v[48:51], v[24:27]\n"
"ds_read_b128 v[40:43], v4 offset:38912\n"
"v_mfma_f32_16x16x32_bf16 v[24:27], a[184:187], v[52:55], v[24:27]\n"
"buffer_load_dwordx4 v2, s[32:35], s63 offen offset:2048 sc1 lds\n"
"v_mfma_f32_16x16x32_bf16 v[28:31], a[188:191], v[52:55], v[28:31]\n"
"ds_read_b128 v[44:47], v4 offset:53248\n"
"v_mfma_f32_16x16x32_bf16 v[28:31], a[76:79], v[48:51], v[28:31]\n"
"s_waitcnt lgkmcnt(4)\n"
"v_mfma_f32_16x16x32_bf16 v[28:31], v[108:111], v[56:59], v[28:31]\n"
"ds_read_b128 v[48:51], v4 offset:11264\n"
"v_mfma_f32_16x16x32_bf16 v[28:31], v[220:223], v[60:63], v[28:31]\n"
"v_mfma_f32_16x16x32_bf16 v[24:27], v[216:219], v[60:63], v[24:27]\n"
"ds_read_b128 v[52:55], v4 offset:25600\n"
"v_mfma_f32_16x16x32_bf16 v[24:27], v[104:107], v[56:59], v[24:27]\n"
"s_waitcnt lgkmcnt(4)\n"
"v_mfma_f32_16x16x32_bf16 v[24:27], a[80:83], v[32:35], v[24:27]\n"
"ds_read_b128 v[56:59], v4 offset:39936\n"
"v_mfma_f32_16x16x32_bf16 v[24:27], a[192:195], v[36:39], v[24:27]\n"
"buffer_load_dwordx4 v2, s[32:35], s63 offen offset:3072 sc1 lds\n"
"s_add_u32 s63, s63, 4096\n"
"v_mfma_f32_16x16x32_bf16 v[28:31], a[196:199], v[36:39], v[28:31]\n"
"ds_read_b128 v[60:63], v4 offset:54272\n"
"s_add_u32 m0, s64, 12288\n"
"v_mfma_f32_16x16x32_bf16 v[28:31], a[84:87], v[32:35], v[28:31]\n"
"s_waitcnt lgkmcnt(4)\n"
"v_mfma_f32_16x16x32_bf16 v[28:31], v[116:119], v[40:43], v[28:31]\n"
"ds_read_b128 v[32:35], v4 offset:12288\n"
"v_mfma_f32_16x16x32_bf16 v[28:31], v[228:231], v[44:47], v[28:31]\n"
"buffer_load_dwordx4 v2, s[32:35], s63 offen offset:0 sc1 lds\n"
"v_mfma_f32_16x16x32_bf16 v[24:27], v[224:227], v[44:47], v[24:27]\n"
"ds_read_b128 v[36:39], v4 offset:26624\n"
"v_mfma_f32_16x16x32_bf16 v[24:27], v[112:115], v[40:43], v[24:27]\n"
"s_waitcnt lgkmcnt(4)\n"
"v_mfma_f32_16x16x32_bf16 v[24:27], a[88:91], v[48:51], v[24:27]\n"
"ds_read_b128 v[40:43], v4 offset:40960\n"
"v_mfma_f32_16x16x32_bf16 v[24:27], a[200:203], v[52:55], v[24:27]\n"
"buffer_load_dwordx4 v2, s[32:35], s63 offen offset:1024 sc1 lds\n"
"s_add_u32 s63, s63, 1175552\n"
"v_mfma_f32_16x16x32_bf16 v[28:31], a[204:207], v[52:55], v[28:31]\n"
"ds_read_b128 v[44:47], v4 offset:55296\n"
"v_mfma_f32_16x16x32_bf16 v[28:31], a[92:95], v[48:51], v[28:31]\n"
"s_waitcnt lgkmcnt(4)\n"
"v_mfma_f32_16x16x32_bf16 v[28:31], v[124:127], v[56:59], v[28:31]\n"
"ds_read_b128 v[48:51], v4 offset:13312\n"
"s_waitcnt vmcnt(10)\n"
"v_mfma_f32_16x16x32_bf16 v[28:31], v[236:239], v[60:63], v[28:31]\n"
"v_lshlrev_b32 v16, 16, v12\n"
"v_and_b32 v17, 0xffff0000, v12\n"
"v_mfma_f32_16x16x32_bf16 v[24:27], v[232:235], v[60:63], v[24:27]\n"
"ds_read_b128 v[52:55], v4 offset:27648\n"
"v_lshlrev_b32 v18, 16, v13\n"
"v_mfma_f32_16x16x32_bf16 v[24:27], v[120:123], v[56:59], v[24:27]\n"
"v_and_b32 v19, 0xffff0000, v13\n"
"s_waitcnt lgkmcnt(4)\n"
"v_mfma_f32_16x16x32_bf16 v[24:27], a[96:99], v[32:35], v[24:27]\n"
"s_barrier\n"
"v_mfma_f32_16x16x32_bf16 v[24:27], a[208:211], v[36:39], v[24:27]\n"
"ds_read_b128 v[56:59], v4 offset:41984\n"
"v_lshlrev_b32 v20, 16, v14\n"
"v_mfma_f32_16x16x32_bf16 v[28:31], a[212:215], v[36:39], v[28:31]\n"
"ds_read_b128 v[60:63], v4 offset:56320\n"
"v_and_b32 v21, 0xffff0000, v14\n"
"v_mfma_f32_16x16x32_bf16 v[28:31], a[100:103], v[32:35], v[28:31]\n"
"v_lshlrev_b32 v22, 16, v15\n"
"s_waitcnt lgkmcnt(4)\n"
"v_mfma_f32_16x16x32_bf16 v[28:31], v[132:135], v[40:43], v[28:31]\n"
"ds_read_b128 v[32:35], v2 offset:0\n"
"v_and_b32 v23, 0xffff0000, v15\n"
"v_mfma_f32_16x16x32_bf16 v[28:31], v[244:247], v[44:47], v[28:31]\n"
"v_mfma_f32_16x16x32_bf16 v[24:27], v[240:243], v[44:47], v[24:27]\n"
"ds_read_b128 v[36:39], v2 offset:14336\n"
"v_mfma_f32_16x16x32_bf16 v[24:27], v[128:131], v[40:43], v[24:27]\n"
"s_waitcnt lgkmcnt(4)\n"
"v_mfma_f32_16x16x32_bf16 v[24:27], a[104:107], v[48:51], v[24:27]\n"
"ds_read_b128 v[40:43], v2 offset:28672\n"
"v_mfma_f32_16x16x32_bf16 v[24:27], a[216:219], v[52:55], v[24:27]\n"
"v_mfma_f32_16x16x32_bf16 v[28:31], a[220:223], v[52:55], v[28:31]\n"
"ds_read_b128 v[44:47], v2 offset:43008\n"
"v_mfma_f32_16x16x32_bf16 v[28:31], a[108:111], v[48:51], v[28:31]\n"
"s_waitcnt lgkmcnt(4)\n"
"v_mfma_f32_16x16x32_bf16 v[28:31], v[140:143], v[56:59], v[28:31]\n"
"ds_read_b128 v[48:51], v2 offset:1024\n"
"v_mfma_f32_16x16x32_bf16 v[28:31], v[252:255], v[60:63], v[28:31]\n"
"v_mfma_f32_16x16x32_bf16 v[24:27], v[248:251], v[60:63], v[24:27]\n"
"ds_read_b128 v[52:55], v2 offset:15360\n"
"v_mfma_f32_16x16x32_bf16 v[24:27], v[136:139], v[56:59], v[24:27]\n"
"s_branch .L_dll_3\n"
".L_dlb_3:\n"
"s_mov_b32 s62, 15\n"
"s_add_u32 s74, s74, 8192\n"
"v_lshrrev_b32 v5, 8, v2\n"
"v_lshl_add_u32 v5, v5, 4, s74\n"
"v_and_b32 v3, 128, v2\n"
"v_lshl_add_u32 v5, v3, 2, v5\n"
"v_and_b32 v3, 16, v2\n"
"v_lshl_add_u32 v5, v3, 4, v5\n"
"v_and_b32 v3, 96, v2\n"
"v_lshl_add_u32 v5, v3, 1, v5\n"
"v_lshrrev_b32 v3, 2, v2\n"
"v_add_u32 v3, s74, v3\n"
".p2align 6\n"
".L_dml_3:\n"
"s_waitcnt lgkmcnt(4)\n"
"v_mfma_f32_16x16x32_bf16 v[24:27], a[0:3], v[32:35], v[24:27]\n"
"ds_read_b128 v[56:59], v4 offset:29696\n"
"v_mfma_f32_16x16x32_bf16 v[24:27], a[112:115], v[36:39], v[24:27]\n"
"v_mfma_f32_16x16x32_bf16 v[28:31], a[116:119], v[36:39], v[28:31]\n"
"ds_read_b128 v[60:63], v4 offset:44032\n"
"v_mfma_f32_16x16x32_bf16 v[28:31], a[4:7], v[32:35], v[28:31]\n"
"v_cvt_pk_bf16_f32 v15, v22, v23\n"
"s_waitcnt lgkmcnt(4)\n"
"v_mfma_f32_16x16x32_bf16 v[28:31], a[228:231], v[40:43], v[28:31]\n"
"ds_read_b128 v[32:35], v4 offset:2048\n"
"v_cvt_pk_bf16_f32 v14, v20, v21\n"
"v_mfma_f32_16x16x32_bf16 v[28:31], v[148:151], v[44:47], v[28:31]\n"
"v_cvt_pk_bf16_f32 v13, v18, v19\n"
"v_cvt_pk_bf16_f32 v12, v16, v17\n"
"v_mfma_f32_16x16x32_bf16 v[24:27], v[144:147], v[44:47], v[24:27]\n"
"ds_read_b128 v[36:39], v4 offset:16384\n"
"ds_write_b128 v5, v[12:15]\n"
"v_mfma_f32_16x16x32_bf16 v[24:27], a[224:227], v[40:43], v[24:27]\n"
"ds_read2st64_b32 v[12:13], v3 offset0:0 offset1:1\n"
"s_waitcnt lgkmcnt(6)\n"
"v_mfma_f32_16x16x32_bf16 v[24:27], a[8:11], v[48:51], v[24:27]\n"
"ds_read_b128 v[40:43], v4 offset:30720\n"
"ds_read2st64_b32 v[14:15], v3 offset0:2 offset1:3\n"
"v_mfma_f32_16x16x32_bf16 v[24:27], a[120:123], v[52:55], v[24:27]\n"
"v_mfma_f32_16x16x32_bf16 v[28:31], a[124:127], v[52:55], v[28:31]\n"
"ds_read_b128 v[44:47], v4 offset:45056\n"
"v_mfma_f32_16x16x32_bf16 v[28:31], a[12:15], v[48:51], v[28:31]\n"
"s_waitcnt lgkmcnt(7)\n"
"v_mfma_f32_16x16x32_bf16 v[28:31], a[236:239], v[56:59], v[28:31]\n"
"ds_read_b128 v[48:51], v4 offset:3072\n"
"v_mfma_f32_16x16x32_bf16 v[28:31], v[156:159], v[60:63], v[28:31]\n"
"s_waitcnt lgkmcnt(2)\n"
"buffer_atomic_pk_add_bf16 v12, v1, s[44:47], s65 offen offset:0 sc1\n"
"v_mfma_f32_16x16x32_bf16 v[24:27], v[152:155], v[60:63], v[24:27]\n"
"ds_read_b128 v[52:55], v4 offset:17408\n"
"v_mfma_f32_16x16x32_bf16 v[24:27], a[232:235], v[56:59], v[24:27]\n"
"buffer_atomic_pk_add_bf16 v13, v1, s[44:47], s65 offen offset:2048 sc1\n"
"v_mfma_f32_16x16x32_bf16 v[24:27], a[16:19], v[32:35], v[24:27]\n"
"s_waitcnt vmcnt(2)\n"
"s_barrier\n"
"v_mfma_f32_16x16x32_bf16 v[24:27], a[128:131], v[36:39], v[24:27]\n"
"ds_read_b128 v[56:59], v4 offset:31744\n"
"s_add_u32 s65, s65, 16384\n"
"v_mfma_f32_16x16x32_bf16 v[28:31], a[132:135], v[36:39], v[28:31]\n"
"ds_read_b128 v[60:63], v4 offset:46080\n"
"v_mfma_f32_16x16x32_bf16 v[28:31], a[20:23], v[32:35], v[28:31]\n"
"buffer_atomic_pk_add_bf16 v14, v1, s[44:47], s65 offen offset:0 sc1\n"
"s_waitcnt lgkmcnt(4)\n"
"v_mfma_f32_16x16x32_bf16 v[28:31], a[244:247], v[40:43], v[28:31]\n"
"ds_read_b128 v[32:35], v4 offset:4096\n"
"s_add_u32 m0, s64, 0\n"
"v_mfma_f32_16x16x32_bf16 v[28:31], v[164:167], v[44:47], v[28:31]\n"
"buffer_atomic_pk_add_bf16 v15, v1, s[44:47], s65 offen offset:2048 sc1\n"
"s_add_u32 s65, s65, 16384\n"
"v_mfma_f32_16x16x32_bf16 v[24:27], v[160:163], v[44:47], v[24:27]\n"
"ds_read_b128 v[36:39], v4 offset:18432\n"
"v_mfma_f32_16x16x32_bf16 v[24:27], a[240:243], v[40:43], v[24:27]\n"
"s_waitcnt lgkmcnt(4)\n"
"v_mfma_f32_16x16x32_bf16 v[24:27], a[24:27], v[48:51], v[24:27]\n"
"ds_read_b128 v[40:43], v4 offset:32768\n"
"v_mfma_f32_16x16x32_bf16 v[24:27], a[136:139], v[52:55], v[24:27]\n"
"buffer_load_dwordx4 v2, s[32:35], s63 offen offset:0 sc1 lds\n"
"v_mfma_f32_16x16x32_bf16 v[28:31], a[140:143], v[52:55], v[28:31]\n"
"ds_read_b128 v[44:47], v4 offset:47104\n"
"v_mfma_f32_16x16x32_bf16 v[28:31], a[28:31], v[48:51], v[28:31]\n"
"s_waitcnt lgkmcnt(4)\n"
"v_mfma_f32_16x16x32_bf16 v[28:31], a[252:255], v[56:59], v[28:31]\n"
"ds_read_b128 v[48:51], v4 offset:5120\n"
"v_mfma_f32_16x16x32_bf16 v[28:31], v[172:175], v[60:63], v[28:31]\n"
"buffer_load_dwordx4 v2, s[32:35], s63 offen offset:1024 sc1 lds\n"
"v_mfma_f32_16x16x32_bf16 v[24:27], v[168:171], v[60:63], v[24:27]\n"
"ds_read_b128 v[52:55], v4 offset:19456\n"
"v_mfma_f32_16x16x32_bf16 v[24:27], a[248:251], v[56:59], v[24:27]\n"
"s_waitcnt lgkmcnt(4)\n"
"v_mfma_f32_16x16x32_bf16 v[24:27], a[32:35], v[32:35], v[24:27]\n"
"ds_read_b128 v[56:59], v4 offset:33792\n"
"v_mfma_f32_16x16x32_bf16 v[24:27], a[144:147], v[36:39], v[24:27]\n"
"buffer_load_dwordx4 v2, s[32:35], s63 offen offset:2048 sc1 lds\n"
"v_mfma_f32_16x16x32_bf16 v[28:31], a[148:151], v[36:39], v[28:31]\n"
"ds_read_b128 v[60:63], v4 offset:48128\n"
"v_mfma_f32_16x16x32_bf16 v[28:31], a[36:39], v[32:35], v[28:31]\n"
"s_waitcnt lgkmcnt(4)\n"
"v_mfma_f32_16x16x32_bf16 v[28:31], v[68:71], v[40:43], v[28:31]\n"
"ds_read_b128 v[32:35], v4 offset:6144\n"
"v_mfma_f32_16x16x32_bf16 v[28:31], v[180:183], v[44:47], v[28:31]\n"
"buffer_load_dwordx4 v2, s[32:35], s63 offen offset:3072 sc1 lds\n"
"v_mfma_f32_16x16x32_bf16 v[24:27], v[176:179], v[44:47], v[24:27]\n"
"ds_read_b128 v[36:39], v4 offset:20480\n"
"v_mfma_f32_16x16x32_bf16 v[24:27], v[64:67], v[40:43], v[24:27]\n"
"s_waitcnt lgkmcnt(4)\n"
"v_mfma_f32_16x16x32_bf16 v[24:27], a[40:43], v[48:51], v[24:27]\n"
"ds_read_b128 v[40:43], v4 offset:34816\n"
"v_mfma_f32_16x16x32_bf16 v[24:27], a[152:155], v[52:55], v[24:27]\n"
"buffer_load_dwordx4 v[12:15], v2, s[32:35], s71 offen sc1\n"
"s_add_u32 s71, s71, 1187840\n"
"v_mfma_f32_16x16x32_bf16 v[28:31], a[156:159], v[52:55], v[28:31]\n"
"ds_read_b128 v[44:47], v4 offset:49152\n"
"s_add_u32 s63, s63, 4096\n"
"v_mfma_f32_16x16x32_bf16 v[28:31], a[44:47], v[48:51], v[28:31]\n"
"s_add_u32 m0, s64, 4096\n"
"s_waitcnt lgkmcnt(4)\n"
"v_mfma_f32_16x16x32_bf16 v[28:31], v[76:79], v[56:59], v[28:31]\n"
"ds_read_b128 v[48:51], v4 offset:7168\n"
"v_mfma_f32_16x16x32_bf16 v[28:31], v[188:191], v[60:63], v[28:31]\n"
"buffer_load_dwordx4 v2, s[32:35], s63 offen offset:0 sc1 lds\n"
"v_mfma_f32_16x16x32_bf16 v[24:27], v[184:187], v[60:63], v[24:27]\n"
"ds_read_b128 v[52:55], v4 offset:21504\n"
"v_mfma_f32_16x16x32_bf16 v[24:27], v[72:75], v[56:59], v[24:27]\n"
"s_waitcnt lgkmcnt(4)\n"
"v_mfma_f32_16x16x32_bf16 v[24:27], a[48:51], v[32:35], v[24:27]\n"
"ds_read_b128 v[56:59], v4 offset:35840\n"
"v_mfma_f32_16x16x32_bf16 v[24:27], a[160:163], v[36:39], v[24:27]\n"
"buffer_load_dwordx4 v2, s[32:35], s63 offen offset:1024 sc1 lds\n"
"v_mfma_f32_16x16x32_bf16 v[28:31], a[164:167], v[36:39], v[28:31]\n"
"ds_read_b128 v[60:63], v4 offset:50176\n"
"v_mfma_f32_16x16x32_bf16 v[28:31], a[52:55], v[32:35], v[28:31]\n"
"s_waitcnt lgkmcnt(4)\n"
"v_mfma_f32_16x16x32_bf16 v[28:31], v[84:87], v[40:43], v[28:31]\n"
"ds_read_b128 v[32:35], v4 offset:8192\n"
"v_mfma_f32_16x16x32_bf16 v[28:31], v[196:199], v[44:47], v[28:31]\n"
"buffer_load_dwordx4 v2, s[32:35], s63 offen offset:2048 sc1 lds\n"
"v_mfma_f32_16x16x32_bf16 v[24:27], v[192:195], v[44:47], v[24:27]\n"
"ds_read_b128 v[36:39], v4 offset:22528\n"
"v_mfma_f32_16x16x32_bf16 v[24:27], v[80:83], v[40:43], v[24:27]\n"
"s_waitcnt lgkmcnt(4)\n"
"v_mfma_f32_16x16x32_bf16 v[24:27], a[56:59], v[48:51], v[24:27]\n"
"ds_read_b128 v[40:43], v4 offset:36864\n"
"v_mfma_f32_16x16x32_bf16 v[24:27], a[168:171], v[52:55], v[24:27]\n"
"buffer_load_dwordx4 v2, s[32:35], s63 offen offset:3072 sc1 lds\n"
"v_mfma_f32_16x16x32_bf16 v[28:31], a[172:175], v[52:55], v[28:31]\n"
"ds_read_b128 v[44:47], v4 offset:51200\n"
"v_mfma_f32_16x16x32_bf16 v[28:31], a[60:63], v[48:51], v[28:31]\n"
"s_waitcnt lgkmcnt(4)\n"
"v_mfma_f32_16x16x32_bf16 v[28:31], v[92:95], v[56:59], v[28:31]\n"
"ds_read_b128 v[48:51], v4 offset:9216\n"
"v_mfma_f32_16x16x32_bf16 v[28:31], v[204:207], v[60:63], v[28:31]\n"
"s_add_u32 s63, s63, 4096\n"
"s_add_u32 m0, s64, 8192\n"
"v_mfma_f32_16x16x32_bf16 v[24:27], v[200:203], v[60:63], v[24:27]\n"
"ds_read_b128 v[52:55], v4 offset:23552\n"
"v_mfma_f32_16x16x32_bf16 v[24:27], v[88:91], v[56:59], v[24:27]\n"
"s_waitcnt lgkmcnt(4)\n"
"v_mfma_f32_16x16x32_bf16 v[24:27], a[64:67], v[32:35], v[24:27]\n"
"ds_read_b128 v[56:59], v4 offset:37888\n"
"v_mfma_f32_16x16x32_bf16 v[24:27], a[176:179], v[36:39], v[24:27]\n"
"buffer_load_dwordx4 v2, s[32:35], s63 offen offset:0 sc1 lds\n"
"v_mfma_f32_16x16x32_bf16 v[28:31], a[180:183], v[36:39], v[28:31]\n"
"ds_read_b128 v[60:63], v4 offset:52224\n"
"v_mfma_f32_16x16x32_bf16 v[28:31], a[68:71], v[32:35], v[28:31]\n"
"s_waitcnt lgkmcnt(4)\n"
"v_mfma_f32_16x16x32_bf16 v[28:31], v[100:103], v[40:43], v[28:31]\n"
"ds_read_b128 v[32:35], v4 offset:10240\n"
"v_mfma_f32_16x16x32_bf16 v[28:31], v[212:215], v[44:47], v[28:31]\n"
"buffer_load_dwordx4 v2, s[32:35], s63 offen offset:1024 sc1 lds\n"
"v_mfma_f32_16x16x32_bf16 v[24:27], v[208:211], v[44:47], v[24:27]\n"
"ds_read_b128 v[36:39], v4 offset:24576\n"
"v_mfma_f32_16x16x32_bf16 v[24:27], v[96:99], v[40:43], v[24:27]\n"
"s_waitcnt lgkmcnt(4)\n"
"v_mfma_f32_16x16x32_bf16 v[24:27], a[72:75], v[48:51], v[24:27]\n"
"ds_read_b128 v[40:43], v4 offset:38912\n"
"v_mfma_f32_16x16x32_bf16 v[24:27], a[184:187], v[52:55], v[24:27]\n"
"buffer_load_dwordx4 v2, s[32:35], s63 offen offset:2048 sc1 lds\n"
"v_mfma_f32_16x16x32_bf16 v[28:31], a[188:191], v[52:55], v[28:31]\n"
"ds_read_b128 v[44:47], v4 offset:53248\n"
"v_mfma_f32_16x16x32_bf16 v[28:31], a[76:79], v[48:51], v[28:31]\n"
"s_waitcnt lgkmcnt(4)\n"
"v_mfma_f32_16x16x32_bf16 v[28:31], v[108:111], v[56:59], v[28:31]\n"
"ds_read_b128 v[48:51], v4 offset:11264\n"
"v_mfma_f32_16x16x32_bf16 v[28:31], v[220:223], v[60:63], v[28:31]\n"
"v_mfma_f32_16x16x32_bf16 v[24:27], v[216:219], v[60:63], v[24:27]\n"
"ds_read_b128 v[52:55], v4 offset:25600\n"
"v_mfma_f32_16x16x32_bf16 v[24:27], v[104:107], v[56:59], v[24:27]\n"
"s_waitcnt lgkmcnt(4)\n"
"v_mfma_f32_16x16x32_bf16 v[24:27], a[80:83], v[32:35], v[24:27]\n"
"ds_read_b128 v[56:59], v4 offset:39936\n"
"v_mfma_f32_16x16x32_bf16 v[24:27], a[192:195], v[36:39], v[24:27]\n"
"buffer_load_dwordx4 v2, s[32:35], s63 offen offset:3072 sc1 lds\n"
"s_add_u32 s63, s63, 4096\n"
"v_mfma_f32_16x16x32_bf16 v[28:31], a[196:199], v[36:39], v[28:31]\n"
"ds_read_b128 v[60:63], v4 offset:54272\n"
"s_add_u32 m0, s64, 12288\n"
"v_mfma_f32_16x16x32_bf16 v[28:31], a[84:87], v[32:35], v[28:31]\n"
"s_waitcnt lgkmcnt(4)\n"
"v_mfma_f32_16x16x32_bf16 v[28:31], v[116:119], v[40:43], v[28:31]\n"
"ds_read_b128 v[32:35], v4 offset:12288\n"
"v_mfma_f32_16x16x32_bf16 v[28:31], v[228:231], v[44:47], v[28:31]\n"
"buffer_load_dwordx4 v2, s[32:35], s63 offen offset:0 sc1 lds\n"
"v_mfma_f32_16x16x32_bf16 v[24:27], v[224:227], v[44:47], v[24:27]\n"
"ds_read_b128 v[36:39], v4 offset:26624\n"
"v_mfma_f32_16x16x32_bf16 v[24:27], v[112:115], v[40:43], v[24:27]\n"
"s_waitcnt lgkmcnt(4)\n"
"v_mfma_f32_16x16x32_bf16 v[24:27], a[88:91], v[48:51], v[24:27]\n"
"ds_read_b128 v[40:43], v4 offset:40960\n"
"v_mfma_f32_16x16x32_bf16 v[24:27], a[200:203], v[52:55], v[24:27]\n"
"buffer_load_dwordx4 v2, s[32:35], s63 offen offset:1024 sc1 lds\n"
"s_add_u32 s63, s63, 1175552\n"
"v_mfma_f32_16x16x32_bf16 v[28:31], a[204:207], v[52:55], v[28:31]\n"
"ds_read_b128 v[44:47], v4 offset:55296\n"
"v_mfma_f32_16x16x32_bf16 v[28:31], a[92:95], v[48:51], v[28:31]\n"
"s_waitcnt lgkmcnt(4)\n"
"v_mfma_f32_16x16x32_bf16 v[28:31], v[124:127], v[56:59], v[28:31]\n"
"ds_read_b128 v[48:51], v4 offset:13312\n"
"s_waitcnt vmcnt(10)\n"
"v_mfma_f32_16x16x32_bf16 v[28:31], v[236:239], v[60:63], v[28:31]\n"
"v_lshlrev_b32 v16, 16, v12\n"
"v_and_b32 v17, 0xffff0000, v12\n"
"v_mfma_f32_16x16x32_bf16 v[24:27], v[232:235], v[60:63], v[24:27]\n"
"ds_read_b128 v[52:55], v4 offset:27648\n"
"v_lshlrev_b32 v18, 16, v13\n"
"v_mfma_f32_16x16x32_bf16 v[24:27], v[120:123], v[56:59], v[24:27]\n"
"v_and_b32 v19, 0xffff0000, v13\n"
"s_waitcnt lgkmcnt(4)\n"
"v_mfma_f32_16x16x32_bf16 v[24:27], a[96:99], v[32:35], v[24:27]\n"
"s_barrier\n"
"v_mfma_f32_16x16x32_bf16 v[24:27], a[208:211], v[36:39], v[24:27]\n"
"ds_read_b128 v[56:59], v4 offset:41984\n"
"v_lshlrev_b32 v20, 16, v14\n"
"v_mfma_f32_16x16x32_bf16 v[28:31], a[212:215], v[36:39], v[28:31]\n"
"ds_read_b128 v[60:63], v4 offset:56320\n"
"v_and_b32 v21, 0xffff0000, v14\n"
"v_mfma_f32_16x16x32_bf16 v[28:31], a[100:103], v[32:35], v[28:31]\n"
"v_lshlrev_b32 v22, 16, v15\n"
"s_waitcnt lgkmcnt(4)\n"
"v_mfma_f32_16x16x32_bf16 v[28:31], v[132:135], v[40:43], v[28:31]\n"
"ds_read_b128 v[32:35], v2 offset:0\n"
"v_and_b32 v23, 0xffff0000, v15\n"
"v_mfma_f32_16x16x32_bf16 v[28:31], v[244:247], v[44:47], v[28:31]\n"
"v_mfma_f32_16x16x32_bf16 v[24:27], v[240:243], v[44:47], v[24:27]\n"
"ds_read_b128 v[36:39], v2 offset:14336\n"
"v_mfma_f32_16x16x32_bf16 v[24:27], v[128:131], v[40:43], v[24:27]\n"
"s_waitcnt lgkmcnt(4)\n"
"v_mfma_f32_16x16x32_bf16 v[24:27], a[104:107], v[48:51], v[24:27]\n"
"ds_read_b128 v[40:43], v2 offset:28672\n"
"v_mfma_f32_16x16x32_bf16 v[24:27], a[216:219], v[52:55], v[24:27]\n"
"v_mfma_f32_16x16x32_bf16 v[28:31], a[220:223], v[52:55], v[28:31]\n"
"ds_read_b128 v[44:47], v2 offset:43008\n"
"v_mfma_f32_16x16x32_bf16 v[28:31], a[108:111], v[48:51], v[28:31]\n"
"s_waitcnt lgkmcnt(4)\n"
"v_mfma_f32_16x16x32_bf16 v[28:31], v[140:143], v[56:59], v[28:31]\n"
"ds_read_b128 v[48:51], v2 offset:1024\n"
"v_mfma_f32_16x16x32_bf16 v[28:31], v[252:255], v[60:63], v[28:31]\n"
"v_mfma_f32_16x16x32_bf16 v[24:27], v[248:251], v[60:63], v[24:27]\n"
"ds_read_b128 v[52:55], v2 offset:15360\n"
"s_sub_u32 s62, s62, 1\n"
"v_mfma_f32_16x16x32_bf16 v[24:27], v[136:139], v[56:59], v[24:27]\n"
"s_cbranch_scc1 .L_dmt_3\n"
"s_waitcnt lgkmcnt(4)\n"
"v_mfma_f32_16x16x32_bf16 v[16:19], a[0:3], v[32:35], v[16:19]\n"
"ds_read_b128 v[56:59], v2 offset:29696\n"
"v_mfma_f32_16x16x32_bf16 v[16:19], a[112:115], v[36:39], v[16:19]\n"
"v_mfma_f32_16x16x32_bf16 v[20:23], a[116:119], v[36:39], v[20:23]\n"
"ds_read_b128 v[60:63], v2 offset:44032\n"
"v_mfma_f32_16x16x32_bf16 v[20:23], a[4:7], v[32:35], v[20:23]\n"
"v_cvt_pk_bf16_f32 v15, v30, v31\n"
"s_waitcnt lgkmcnt(4)\n"
"v_mfma_f32_16x16x32_bf16 v[20:23], a[228:231], v[40:43], v[20:23]\n"
"ds_read_b128 v[32:35], v2 offset:2048\n"
"v_cvt_pk_bf16_f32 v14, v28, v29\n"
"v_mfma_f32_16x16x32_bf16 v[20:23], v[148:151], v[44:47], v[20:23]\n"
"v_cvt_pk_bf16_f32 v13, v26, v27\n"
"v_cvt_pk_bf16_f32 v12, v24, v25\n"
"v_mfma_f32_16x16x32_bf16 v[16:19], v[144:147], v[44:47], v[16:19]\n"
"ds_read_b128 v[36:39], v2 offset:16384\n"
"ds_write_b128 v5, v[12:15]\n"
"v_mfma_f32_16x16x32_bf16 v[16:19], a[224:227], v[40:43], v[16:19]\n"
"ds_read2st64_b32 v[12:13], v3 offset0:0 offset1:1\n"
"s_waitcnt lgkmcnt(6)\n"
"v_mfma_f32_16x16x32_bf16 v[16:19], a[8:11], v[48:51], v[16:19]\n"
"ds_read_b128 v[40:43], v2 offset:30720\n"
"ds_read2st64_b32 v[14:15], v3 offset0:2 offset1:3\n"
"v_mfma_f32_16x16x32_bf16 v[16:19], a[120:123], v[52:55], v[16:19]\n"
"v_mfma_f32_16x16x32_bf16 v[20:23], a[124:127], v[52:55], v[20:23]\n"
"ds_read_b128 v[44:47], v2 offset:45056\n"
"v_mfma_f32_16x16x32_bf16 v[20:23], a[12:15], v[48:51], v[20:23]\n"
"s_waitcnt lgkmcnt(7)\n"
"v_mfma_f32_16x16x32_bf16 v[20:23], a[236:239], v[56:59], v[20:23]\n"
"ds_read_b128 v[48:51], v2 offset:3072\n"
"v_mfma_f32_16x16x32_bf16 v[20:23], v[156:159], v[60:63], v[20:23]\n"
"s_waitcnt lgkmcnt(2)\n"
"buffer_atomic_pk_add_bf16 v12, v1, s[44:47], s65 offen offset:0 sc1\n"
"v_mfma_f32_16x16x32_bf16 v[16:19], v[152:155], v[60:63], v[16:19]\n"
"ds_read_b128 v[52:55], v2 offset:17408\n"
"v_mfma_f32_16x16x32_bf16 v[16:19], a[232:235], v[56:59], v[16:19]\n"
"buffer_atomic_pk_add_bf16 v13, v1, s[44:47], s65 offen offset:2048 sc1\n"
"v_mfma_f32_16x16x32_bf16 v[16:19], a[16:19], v[32:35], v[16:19]\n"
"s_waitcnt vmcnt(2)\n"
"s_barrier\n"
"v_mfma_f32_16x16x32_bf16 v[16:19], a[128:131], v[36:39], v[16:19]\n"
"ds_read_b128 v[56:59], v2 offset:31744\n"
"s_add_u32 s65, s65, 16384\n"
"v_mfma_f32_16x16x32_bf16 v[20:23], a[132:135], v[36:39], v[20:23]\n"
"ds_read_b128 v[60:63], v2 offset:46080\n"
"v_mfma_f32_16x16x32_bf16 v[20:23], a[20:23], v[32:35], v[20:23]\n"
"buffer_atomic_pk_add_bf16 v14, v1, s[44:47], s65 offen offset:0 sc1\n"
"s_waitcnt lgkmcnt(4)\n"
"v_mfma_f32_16x16x32_bf16 v[20:23], a[244:247], v[40:43], v[20:23]\n"
"ds_read_b128 v[32:35], v2 offset:4096\n"
"s_add_u32 m0, s64, 57344\n"
"v_mfma_f32_16x16x32_bf16 v[20:23], v[164:167], v[44:47], v[20:23]\n"
"buffer_atomic_pk_add_bf16 v15, v1, s[44:47], s65 offen offset:2048 sc1\n"
"s_add_u32 s65, s65, 16384\n"
"v_mfma_f32_16x16x32_bf16 v[16:19], v[160:163], v[44:47], v[16:19]\n"
"ds_read_b128 v[36:39], v2 offset:18432\n"
"v_mfma_f32_16x16x32_bf16 v[16:19], a[240:243], v[40:43], v[16:19]\n"
"s_waitcnt lgkmcnt(4)\n"
"v_mfma_f32_16x16x32_bf16 v[16:19], a[24:27], v[48:51], v[16:19]\n"
"ds_read_b128 v[40:43], v2 offset:32768\n"
"v_mfma_f32_16x16x32_bf16 v[16:19], a[136:139], v[52:55], v[16:19]\n"
"buffer_load_dwordx4 v2, s[32:35], s63 offen offset:0 sc1 lds\n"
"v_mfma_f32_16x16x32_bf16 v[20:23], a[140:143], v[52:55], v[20:23]\n"
"ds_read_b128 v[44:47], v2 offset:47104\n"
"v_mfma_f32_16x16x32_bf16 v[20:23], a[28:31], v[48:51], v[20:23]\n"
"s_waitcnt lgkmcnt(4)\n"
"v_mfma_f32_16x16x32_bf16 v[20:23], a[252:255], v[56:59], v[20:23]\n"
"ds_read_b128 v[48:51], v2 offset:5120\n"
"v_mfma_f32_16x16x32_bf16 v[20:23], v[172:175], v[60:63], v[20:23]\n"
"buffer_load_dwordx4 v2, s[32:35], s63 offen offset:1024 sc1 lds\n"
"v_mfma_f32_16x16x32_bf16 v[16:19], v[168:171], v[60:63], v[16:19]\n"
"ds_read_b128 v[52:55], v2 offset:19456\n"
"v_mfma_f32_16x16x32_bf16 v[16:19], a[248:251], v[56:59], v[16:19]\n"
"s_waitcnt lgkmcnt(4)\n"
"v_mfma_f32_16x16x32_bf16 v[16:19], a[32:35], v[32:35], v[16:19]\n"
"ds_read_b128 v[56:59], v2 offset:33792\n"
"v_mfma_f32_16x16x32_bf16 v[16:19], a[144:147], v[36:39], v[16:19]\n"
"buffer_load_dwordx4 v2, s[32:35], s63 offen offset:2048 sc1 lds\n"
"v_mfma_f32_16x16x32_bf16 v[20:23], a[148:151], v[36:39], v[20:23]\n"
"ds_read_b128 v[60:63], v2 offset:48128\n"
"v_mfma_f32_16x16x32_bf16 v[20:23], a[36:39], v[32:35], v[20:23]\n"
"s_waitcnt lgkmcnt(4)\n"
"v_mfma_f32_16x16x32_bf16 v[20:23], v[68:71], v[40:43], v[20:23]\n"
"ds_read_b128 v[32:35], v2 offset:6144\n"
"v_mfma_f32_16x16x32_bf16 v[20:23], v[180:183], v[44:47], v[20:23]\n"
"buffer_load_dwordx4 v2, s[32:35], s63 offen offset:3072 sc1 lds\n"
"v_mfma_f32_16x16x32_bf16 v[16:19], v[176:179], v[44:47], v[16:19]\n"
"ds_read_b128 v[36:39], v2 offset:20480\n"
"v_mfma_f32_16x16x32_bf16 v[16:19], v[64:67], v[40:43], v[16:19]\n"
"s_waitcnt lgkmcnt(4)\n"
"v_mfma_f32_16x16x32_bf16 v[16:19], a[40:43], v[48:51], v[16:19]\n"
"ds_read_b128 v[40:43], v2 offset:34816\n"
"v_mfma_f32_16x16x32_bf16 v[16:19], a[152:155], v[52:55], v[16:19]\n"
"buffer_load_dwordx4 v[12:15], v2, s[32:35], s71 offen sc1\n"
"s_add_u32 s71, s71, 1187840\n"
"v_mfma_f32_16x16x32_bf16 v[20:23], a[156:159], v[52:55], v[20:23]\n"
"ds_read_b128 v[44:47], v2 offset:49152\n"
"s_add_u32 s63, s63, 4096\n"
"v_mfma_f32_16x16x32_bf16 v[20:23], a[44:47], v[48:51], v[20:23]\n"
"s_add_u32 m0, s64, 61440\n"
"s_waitcnt lgkmcnt(4)\n"
"v_mfma_f32_16x16x32_bf16 v[20:23], v[76:79], v[56:59], v[20:23]\n"
"ds_read_b128 v[48:51], v2 offset:7168\n"
"v_mfma_f32_16x16x32_bf16 v[20:23], v[188:191], v[60:63], v[20:23]\n"
"buffer_load_dwordx4 v2, s[32:35], s63 offen offset:0 sc1 lds\n"
"v_mfma_f32_16x16x32_bf16 v[16:19], v[184:187], v[60:63], v[16:19]\n"
"ds_read_b128 v[52:55], v2 offset:21504\n"
"v_mfma_f32_16x16x32_bf16 v[16:19], v[72:75], v[56:59], v[16:19]\n"
"s_waitcnt lgkmcnt(4)\n"
"v_mfma_f32_16x16x32_bf16 v[16:19], a[48:51], v[32:35], v[16:19]\n"
"ds_read_b128 v[56:59], v2 offset:35840\n"
"v_mfma_f32_16x16x32_bf16 v[16:19], a[160:163], v[36:39], v[16:19]\n"
"buffer_load_dwordx4 v2, s[32:35], s63 offen offset:1024 sc1 lds\n"
"v_mfma_f32_16x16x32_bf16 v[20:23], a[164:167], v[36:39], v[20:23]\n"
"ds_read_b128 v[60:63], v2 offset:50176\n"
"v_mfma_f32_16x16x32_bf16 v[20:23], a[52:55], v[32:35], v[20:23]\n"
"s_waitcnt lgkmcnt(4)\n"
"v_mfma_f32_16x16x32_bf16 v[20:23], v[84:87], v[40:43], v[20:23]\n"
"ds_read_b128 v[32:35], v2 offset:8192\n"
"v_mfma_f32_16x16x32_bf16 v[20:23], v[196:199], v[44:47], v[20:23]\n"
"buffer_load_dwordx4 v2, s[32:35], s63 offen offset:2048 sc1 lds\n"
"v_mfma_f32_16x16x32_bf16 v[16:19], v[192:195], v[44:47], v[16:19]\n"
"ds_read_b128 v[36:39], v2 offset:22528\n"
"v_mfma_f32_16x16x32_bf16 v[16:19], v[80:83], v[40:43], v[16:19]\n"
"s_waitcnt lgkmcnt(4)\n"
"v_mfma_f32_16x16x32_bf16 v[16:19], a[56:59], v[48:51], v[16:19]\n"
"ds_read_b128 v[40:43], v2 offset:36864\n"
"v_mfma_f32_16x16x32_bf16 v[16:19], a[168:171], v[52:55], v[16:19]\n"
"buffer_load_dwordx4 v2, s[32:35], s63 offen offset:3072 sc1 lds\n"
"v_mfma_f32_16x16x32_bf16 v[20:23], a[172:175], v[52:55], v[20:23]\n"
"ds_read_b128 v[44:47], v2 offset:51200\n"
"v_mfma_f32_16x16x32_bf16 v[20:23], a[60:63], v[48:51], v[20:23]\n"
"s_waitcnt lgkmcnt(4)\n"
"v_mfma_f32_16x16x32_bf16 v[20:23], v[92:95], v[56:59], v[20:23]\n"
"ds_read_b128 v[48:51], v2 offset:9216\n"
"v_mfma_f32_16x16x32_bf16 v[20:23], v[204:207], v[60:63], v[20:23]\n"
"s_add_u32 s63, s63, 4096\n"
"s_add_u32 m0, s64, 65536\n"
"v_mfma_f32_16x16x32_bf16 v[16:19], v[200:203], v[60:63], v[16:19]\n"
"ds_read_b128 v[52:55], v2 offset:23552\n"
"v_mfma_f32_16x16x32_bf16 v[16:19], v[88:91], v[56:59], v[16:19]\n"
"s_waitcnt lgkmcnt(4)\n"
"v_mfma_f32_16x16x32_bf16 v[16:19], a[64:67], v[32:35], v[16:19]\n"
"ds_read_b128 v[56:59], v2 offset:37888\n"
"v_mfma_f32_16x16x32_bf16 v[16:19], a[176:179], v[36:39], v[16:19]\n"
"buffer_load_dwordx4 v2, s[32:35], s63 offen offset:0 sc1 lds\n"
"v_mfma_f32_16x16x32_bf16 v[20:23], a[180:183], v[36:39], v[20:23]\n"
"ds_read_b128 v[60:63], v2 offset:52224\n"
"v_mfma_f32_16x16x32_bf16 v[20:23], a[68:71], v[32:35], v[20:23]\n"
"s_waitcnt lgkmcnt(4)\n"
"v_mfma_f32_16x16x32_bf16 v[20:23], v[100:103], v[40:43], v[20:23]\n"
"ds_read_b128 v[32:35], v2 offset:10240\n"
"v_mfma_f32_16x16x32_bf16 v[20:23], v[212:215], v[44:47], v[20:23]\n"
"buffer_load_dwordx4 v2, s[32:35], s63 offen offset:1024 sc1 lds\n"
"v_mfma_f32_16x16x32_bf16 v[16:19], v[208:211], v[44:47], v[16:19]\n"
"ds_read_b128 v[36:39], v2 offset:24576\n"
"v_mfma_f32_16x16x32_bf16 v[16:19], v[96:99], v[40:43], v[16:19]\n"
"s_waitcnt lgkmcnt(4)\n"
"v_mfma_f32_16x16x32_bf16 v[16:19], a[72:75], v[48:51], v[16:19]\n"
"ds_read_b128 v[40:43], v2 offset:38912\n"
"v_mfma_f32_16x16x32_bf16 v[16:19], a[184:187], v[52:55], v[16:19]\n"
"buffer_load_dwordx4 v2, s[32:35], s63 offen offset:2048 sc1 lds\n"
"v_mfma_f32_16x16x32_bf16 v[20:23], a[188:191], v[52:55], v[20:23]\n"
"ds_read_b128 v[44:47], v2 offset:53248\n"
"v_mfma_f32_16x16x32_bf16 v[20:23], a[76:79], v[48:51], v[20:23]\n"
"s_waitcnt lgkmcnt(4)\n"
"v_mfma_f32_16x16x32_bf16 v[20:23], v[108:111], v[56:59], v[20:23]\n"
"ds_read_b128 v[48:51], v2 offset:11264\n"
"v_mfma_f32_16x16x32_bf16 v[20:23], v[220:223], v[60:63], v[20:23]\n"
"v_mfma_f32_16x16x32_bf16 v[16:19], v[216:219], v[60:63], v[16:19]\n"
"ds_read_b128 v[52:55], v2 offset:25600\n"
"v_mfma_f32_16x16x32_bf16 v[16:19], v[104:107], v[56:59], v[16:19]\n"
"s_waitcnt lgkmcnt(4)\n"
"v_mfma_f32_16x16x32_bf16 v[16:19], a[80:83], v[32:35], v[16:19]\n"
"ds_read_b128 v[56:59], v2 offset:39936\n"
"v_mfma_f32_16x16x32_bf16 v[16:19], a[192:195], v[36:39], v[16:19]\n"
"buffer_load_dwordx4 v2, s[32:35], s63 offen offset:3072 sc1 lds\n"
"s_add_u32 s63, s63, 4096\n"
"v_mfma_f32_16x16x32_bf16 v[20:23], a[196:199], v[36:39], v[20:23]\n"
"ds_read_b128 v[60:63], v2 offset:54272\n"
"s_add_u32 m0, s64, 69632\n"
"v_mfma_f32_16x16x32_bf16 v[20:23], a[84:87], v[32:35], v[20:23]\n"
"s_waitcnt lgkmcnt(4)\n"
"v_mfma_f32_16x16x32_bf16 v[20:23], v[116:119], v[40:43], v[20:23]\n"
"ds_read_b128 v[32:35], v2 offset:12288\n"
"v_mfma_f32_16x16x32_bf16 v[20:23], v[228:231], v[44:47], v[20:23]\n"
"buffer_load_dwordx4 v2, s[32:35], s63 offen offset:0 sc1 lds\n"
"v_mfma_f32_16x16x32_bf16 v[16:19], v[224:227], v[44:47], v[16:19]\n"
"ds_read_b128 v[36:39], v2 offset:26624\n"
"v_mfma_f32_16x16x32_bf16 v[16:19], v[112:115], v[40:43], v[16:19]\n"
"s_waitcnt lgkmcnt(4)\n"
"v_mfma_f32_16x16x32_bf16 v[16:19], a[88:91], v[48:51], v[16:19]\n"
"ds_read_b128 v[40:43], v2 offset:40960\n"
"v_mfma_f32_16x16x32_bf16 v[16:19], a[200:203], v[52:55], v[16:19]\n"
"buffer_load_dwordx4 v2, s[32:35], s63 offen offset:1024 sc1 lds\n"
"s_add_u32 s63, s63, 1175552\n"
"v_mfma_f32_16x16x32_bf16 v[20:23], a[204:207], v[52:55], v[20:23]\n"
"ds_read_b128 v[44:47], v2 offset:55296\n"
"v_mfma_f32_16x16x32_bf16 v[20:23], a[92:95], v[48:51], v[20:23]\n"
"s_waitcnt lgkmcnt(4)\n"
"v_mfma_f32_16x16x32_bf16 v[20:23], v[124:127], v[56:59], v[20:23]\n"
"ds_read_b128 v[48:51], v2 offset:13312\n"
"s_waitcnt vmcnt(10)\n"
"v_mfma_f32_16x16x32_bf16 v[20:23], v[236:239], v[60:63], v[20:23]\n"
"v_lshlrev_b32 v24, 16, v12\n"
"v_and_b32 v25, 0xffff0000, v12\n"
"v_mfma_f32_16x16x32_bf16 v[16:19], v[232:235], v[60:63], v[16:19]\n"
"ds_read_b128 v[52:55], v2 offset:27648\n"
"v_lshlrev_b32 v26, 16, v13\n"
"v_mfma_f32_16x16x32_bf16 v[16:19], v[120:123], v[56:59], v[16:19]\n"
"v_and_b32 v27, 0xffff0000, v13\n"
"s_waitcnt lgkmcnt(4)\n"
"v_mfma_f32_16x16x32_bf16 v[16:19], a[96:99], v[32:35], v[16:19]\n"
"s_barrier\n"
"v_mfma_f32_16x16x32_bf16 v[16:19], a[208:211], v[36:39], v[16:19]\n"
"ds_read_b128 v[56:59], v2 offset:41984\n"
"v_lshlrev_b32 v28, 16, v14\n"
"v_mfma_f32_16x16x32_bf16 v[20:23], a[212:215], v[36:39], v[20:23]\n"
"ds_read_b128 v[60:63], v2 offset:56320\n"
"v_and_b32 v29, 0xffff0000, v14\n"
"v_mfma_f32_16x16x32_bf16 v[20:23], a[100:103], v[32:35], v[20:23]\n"
"v_lshlrev_b32 v30, 16, v15\n"
"s_waitcnt lgkmcnt(4)\n"
"v_mfma_f32_16x16x32_bf16 v[20:23], v[132:135], v[40:43], v[20:23]\n"
"ds_read_b128 v[32:35], v4 offset:0\n"
"v_and_b32 v31, 0xffff0000, v15\n"
"v_mfma_f32_16x16x32_bf16 v[20:23], v[244:247], v[44:47], v[20:23]\n"
"v_mfma_f32_16x16x32_bf16 v[16:19], v[240:243], v[44:47], v[16:19]\n"
"ds_read_b128 v[36:39], v4 offset:14336\n"
"v_mfma_f32_16x16x32_bf16 v[16:19], v[128:131], v[40:43], v[16:19]\n"
"s_waitcnt lgkmcnt(4)\n"
"v_mfma_f32_16x16x32_bf16 v[16:19], a[104:107], v[48:51], v[16:19]\n"
"ds_read_b128 v[40:43], v4 offset:28672\n"
"v_mfma_f32_16x16x32_bf16 v[16:19], a[216:219], v[52:55], v[16:19]\n"
"v_mfma_f32_16x16x32_bf16 v[20:23], a[220:223], v[52:55], v[20:23]\n"
"ds_read_b128 v[44:47], v4 offset:43008\n"
"v_mfma_f32_16x16x32_bf16 v[20:23], a[108:111], v[48:51], v[20:23]\n"
"s_waitcnt lgkmcnt(4)\n"
"v_mfma_f32_16x16x32_bf16 v[20:23], v[140:143], v[56:59], v[20:23]\n"
"ds_read_b128 v[48:51], v4 offset:1024\n"
"v_mfma_f32_16x16x32_bf16 v[20:23], v[252:255], v[60:63], v[20:23]\n"
"v_mfma_f32_16x16x32_bf16 v[16:19], v[248:251], v[60:63], v[16:19]\n"
"ds_read_b128 v[52:55], v4 offset:15360\n"
"v_mfma_f32_16x16x32_bf16 v[16:19], v[136:139], v[56:59], v[16:19]\n"
"s_branch .L_dml_3\n"
".L_dmt_3:\n"
"s_nop 1\n"
"s_nop 0\n"
"v_cvt_pk_bf16_f32 v15, v30, v31\n"
"v_cvt_pk_bf16_f32 v14, v28, v29\n"
"s_nop 1\n"
"v_cvt_pk_bf16_f32 v13, v26, v27\n"
"v_cvt_pk_bf16_f32 v12, v24, v25\n"
"ds_write_b128 v5, v[12:15]\n"
"ds_read2st64_b32 v[12:13], v3 offset0:0 offset1:1\n"
"ds_read2st64_b32 v[14:15], v3 offset0:2 offset1:3\n"
"s_waitcnt lgkmcnt(0)\n"
"buffer_atomic_pk_add_bf16 v12, v1, s[44:47], s65 offen offset:0 sc1\n"
"buffer_atomic_pk_add_bf16 v13, v1, s[44:47], s65 offen offset:2048 sc1\n"
"s_add_u32 s65, s65, 16384\n"
"buffer_atomic_pk_add_bf16 v14, v1, s[44:47], s65 offen offset:0 sc1\n"
"buffer_atomic_pk_add_bf16 v15, v1, s[44:47], s65 offen offset:2048 sc1\n"
"s_add_u32 s65, s65, 16384\n"
"s_branch .L_end\n"
".L_dh:\n"
"v_mov_b32 v12, 0\n"
"v_mov_b32 v13, 0\n"
"v_mov_b32 v14, 0\n"
"v_mov_b32 v15, 0\n"
"v_lshrrev_b32 v10, 4, v1\n"
"v_and_b32 v11, 0xf0, v2\n"
"v_lshl_add_u32 v10, v10, 11, v11\n"
"s_lshl2_add_u32 s48, s28, s26\n"
"s_lshl_b32 s48, s48, 10\n"
"s_add_u32 s48, s48, s27\n"
"s_lshl_b32 s48, s48, 8\n"
"s_mov_b32 s49, 31\n"
".L_dz:\n"
"buffer_store_dwordx4 v[12:15], v10, s[44:47], s48 offen sc1\n"
"s_add_u32 s48, s48, 8192\n"
"s_sub_u32 s49, s49, 1\n"
"s_cbranch_scc0 .L_dz\n"
"v_lshrrev_b32 v10, 4, v1\n"
"v_and_b32 v1, 15, v1\n"
"s_lshl_b32 s48, s50, 6\n"
"v_lshl_add_u32 v1, v1, 2, s48\n"
"v_lshl_add_u32 v1, v10, 12, v1\n"
"s_mov_b32 s71, s65\n"
"s_add_u32 s69, s69, 38010880\n"
"s_add_u32 s68, s70, 2236416\n"
"s_add_u32 s70, s70, 42622976\n"
"s_lshl_b32 s65, s28, 20\n"
"s_mov_b32 s48, 20000\n"
"s_branch .L_rfda\n"
".L_rlda:\n"
"s_sleep 8\n"
".L_rfda:\n"
"buffer_load_dwordx2 v[6:7], v0, s[32:35], s66 offen sc1\n"
"s_sub_u32 s48, s48, 1\n"
"s_cbranch_scc1 .L_end\n"
"s_waitcnt vmcnt(0)\n"
"v_cmp_ne_u64 vcc, v[8:9], v[6:7]\n"
"s_cbranch_vccnz .L_rlda\n"
"s_add_u32 s66, s66, 1187840\n"
"s_add_u32 m0, s64, 0\n"
"s_nop 0\n"
"buffer_load_dwordx4 v2, s[32:35], s63 offen offset:0 sc1 lds\n"
"buffer_load_dwordx4 v2, s[32:35], s63 offen offset:1024 sc1 lds\n"
"buffer_load_dwordx4 v2, s[32:35], s63 offen offset:2048 sc1 lds\n"
"buffer_load_dwordx4 v2, s[32:35], s63 offen offset:3072 sc1 lds\n"
"s_add_u32 s63, s63, 4096\n"
"s_add_u32 m0, s64, 4096\n"
"s_nop 0\n"
"buffer_load_dwordx4 v2, s[32:35], s63 offen offset:0 sc1 lds\n"
"buffer_load_dwordx4 v2, s[32:35], s63 offen offset:1024 sc1 lds\n"
"buffer_load_dwordx4 v2, s[32:35], s63 offen offset:2048 sc1 lds\n"
"buffer_load_dwordx4 v2, s[32:35], s63 offen offset:3072 sc1 lds\n"
"s_add_u32 s63, s63, 4096\n"
"s_add_u32 m0, s64, 8192\n"
"s_nop 0\n"
"buffer_load_dwordx4 v2, s[32:35], s63 offen offset:0 sc1 lds\n"
"buffer_load_dwordx4 v2, s[32:35], s63 offen offset:1024 sc1 lds\n"
"buffer_load_dwordx4 v2, s[32:35], s63 offen offset:2048 sc1 lds\n"
"buffer_load_dwordx4 v2, s[32:35], s63 offen offset:3072 sc1 lds\n"
"s_add_u32 s63, s63, 4096\n"
"s_add_u32 m0, s64, 12288\n"
"s_nop 0\n"
"buffer_load_dwordx4 v2, s[32:35], s63 offen offset:0 sc1 lds\n"
"buffer_load_dwordx4 v2, s[32:35], s63 offen offset:1024 sc1 lds\n"
"s_add_u32 s63, s63, 1175552\n"
"buffer_load_dwordx2 v[6:7], v0, s[32:35], s66 offen sc1\n"
"s_waitcnt vmcnt(0)\n"
"s_barrier\n"
"ds_read_b128 v[32:35], v2 offset:0\n"
"ds_read_b128 v[36:39], v2 offset:14336\n"
"ds_read_b128 v[40:43], v2 offset:28672\n"
"ds_read_b128 v[44:47], v2 offset:43008\n"
"ds_read_b128 v[48:51], v2 offset:1024\n"
"ds_read_b128 v[52:55], v2 offset:15360\n"
"s_cmp_eq_u32 s26, 1\n"
"s_cbranch_scc1 .L_dhv1\n"
"s_cmp_eq_u32 s26, 2\n"
"s_cbranch_scc1 .L_dhv2\n"
"s_cmp_eq_u32 s26, 3\n"
"s_cbranch_scc1 .L_dhv3\n"
".p2align 6\n"
".L_dhv0:\n"
".L_dal_0:\n"
"s_waitcnt lgkmcnt(4)\n"
"v_mfma_f32_16x16x32_bf16 v[16:19], a[0:3], v[32:35], 0\n"
"ds_read_b128 v[56:59], v2 offset:29696\n"
"ds_read_b128 v[12:15], v5 offset:8192\n"
"v_mfma_f32_16x16x32_bf16 v[16:19], a[112:115], v[36:39], v[16:19]\n"
"v_mfma_f32_16x16x32_bf16 v[20:23], a[116:119], v[36:39], 0\n"
"ds_read_b128 v[60:63], v2 offset:44032\n"
"v_mfma_f32_16x16x32_bf16 v[20:23], a[4:7], v[32:35], v[20:23]\n"
"s_waitcnt lgkmcnt(5)\n"
"v_mfma_f32_16x16x32_bf16 v[20:23], a[228:231], v[40:43], v[20:23]\n"
"ds_read_b128 v[32:35], v2 offset:2048\n"
"v_mfma_f32_16x16x32_bf16 v[20:23], v[148:151], v[44:47], v[20:23]\n"
"s_waitcnt lgkmcnt(2)\n"
"v_add_f32 v24, v24, v12\n"
"v_mfma_f32_16x16x32_bf16 v[16:19], v[144:147], v[44:47], v[16:19]\n"
"ds_read_b128 v[36:39], v2 offset:16384\n"
"v_add_f32 v25, v25, v13\n"
"v_mfma_f32_16x16x32_bf16 v[16:19], a[224:227], v[40:43], v[16:19]\n"
"v_add_f32 v26, v26, v14\n"
"v_mfma_f32_16x16x32_bf16 v[16:19], a[8:11], v[48:51], v[16:19]\n"
"ds_read_b128 v[40:43], v2 offset:30720\n"
"v_add_f32 v27, v27, v15\n"
"v_mfma_f32_16x16x32_bf16 v[16:19], a[120:123], v[52:55], v[16:19]\n"
"ds_read_b128 v[12:15], v5 offset:9216\n"
"v_mfma_f32_16x16x32_bf16 v[20:23], a[124:127], v[52:55], v[20:23]\n"
"ds_read_b128 v[44:47], v2 offset:45056\n"
"v_mfma_f32_16x16x32_bf16 v[20:23], a[12:15], v[48:51], v[20:23]\n"
"s_waitcnt lgkmcnt(5)\n"
"v_mfma_f32_16x16x32_bf16 v[20:23], a[236:239], v[56:59], v[20:23]\n"
"ds_read_b128 v[48:51], v2 offset:3072\n"
"v_mfma_f32_16x16x32_bf16 v[20:23], v[156:159], v[60:63], v[20:23]\n"
"v_mfma_f32_16x16x32_bf16 v[16:19], v[152:155], v[60:63], v[16:19]\n"
"ds_read_b128 v[52:55], v2 offset:17408\n"
"s_waitcnt lgkmcnt(3)\n"
"v_mfma_f32_16x16x32_bf16 v[16:19], a[232:235], v[56:59], v[16:19]\n"
"v_add_f32 v28, v28, v12\n"
"v_mfma_f32_16x16x32_bf16 v[16:19], a[16:19], v[32:35], v[16:19]\n"
"ds_read_b128 v[56:59], v2 offset:31744\n"
"v_add_f32 v29, v29, v13\n"
"v_mfma_f32_16x16x32_bf16 v[16:19], a[128:131], v[36:39], v[16:19]\n"
"v_add_f32 v30, v30, v14\n"
"v_add_f32 v31, v31, v15\n"
"v_mfma_f32_16x16x32_bf16 v[20:23], a[132:135], v[36:39], v[20:23]\n"
"ds_read_b128 v[60:63], v2 offset:46080\n"
"v_cvt_pk_bf16_f32 v12, v24, v25\n"
"v_mfma_f32_16x16x32_bf16 v[20:23], a[20:23], v[32:35], v[20:23]\n"
"s_waitcnt lgkmcnt(4)\n"
"s_waitcnt vmcnt(1)\n"
"s_barrier\n"
"v_mfma_f32_16x16x32_bf16 v[20:23], a[244:247], v[40:43], v[20:23]\n"
"ds_read_b128 v[32:35], v2 offset:4096\n"
"s_add_u32 m0, s64, 57344\n"
"v_mfma_f32_16x16x32_bf16 v[20:23], v[164:167], v[44:47], v[20:23]\n"
"buffer_load_dwordx4 v2, s[32:35], s63 offen offset:0 sc1 lds\n"
"v_cvt_pk_bf16_f32 v13, v26, v27\n"
"v_mfma_f32_16x16x32_bf16 v[16:19], v[160:163], v[44:47], v[16:19]\n"
"ds_read_b128 v[36:39], v2 offset:18432\n"
"v_cvt_pk_bf16_f32 v14, v28, v29\n"
"v_mfma_f32_16x16x32_bf16 v[16:19], a[240:243], v[40:43], v[16:19]\n"
"v_cvt_pk_bf16_f32 v15, v30, v31\n"
"s_waitcnt lgkmcnt(4)\n"
"v_mfma_f32_16x16x32_bf16 v[16:19], a[24:27], v[48:51], v[16:19]\n"
"ds_read_b128 v[40:43], v2 offset:32768\n"
"v_mfma_f32_16x16x32_bf16 v[16:19], a[136:139], v[52:55], v[16:19]\n"
"buffer_store_dwordx4 v[12:15], v2, s[32:35], s68 offen sc1\n"
"v_add_f32 v10, v24, v25\n"
"v_mfma_f32_16x16x32_bf16 v[20:23], a[140:143], v[52:55], v[20:23]\n"
"ds_read_b128 v[44:47], v2 offset:47104\n"
"v_mul_f32 v11, v24, v24\n"
"v_mfma_f32_16x16x32_bf16 v[20:23], a[28:31], v[48:51], v[20:23]\n"
"v_fmac_f32 v11, v25, v25\n"
"s_waitcnt lgkmcnt(4)\n"
"v_mfma_f32_16x16x32_bf16 v[20:23], a[252:255], v[56:59], v[20:23]\n"
"ds_read_b128 v[48:51], v2 offset:5120\n"
"v_add_f32 v10, v10, v26\n"
"v_mfma_f32_16x16x32_bf16 v[20:23], v[172:175], v[60:63], v[20:23]\n"
"buffer_load_dwordx4 v2, s[32:35], s63 offen offset:1024 sc1 lds\n"
"v_fmac_f32 v11, v26, v26\n"
"v_mfma_f32_16x16x32_bf16 v[16:19], v[168:171], v[60:63], v[16:19]\n"
"ds_read_b128 v[52:55], v2 offset:19456\n"
"v_add_f32 v10, v10, v27\n"
"v_mfma_f32_16x16x32_bf16 v[16:19], a[248:251], v[56:59], v[16:19]\n"
"v_fmac_f32 v11, v27, v27\n"
"s_waitcnt lgkmcnt(4)\n"
"v_mfma_f32_16x16x32_bf16 v[16:19], a[32:35], v[32:35], v[16:19]\n"
"ds_read_b128 v[56:59], v2 offset:33792\n"
"v_add_f32 v10, v10, v28\n"
"v_mfma_f32_16x16x32_bf16 v[16:19], a[144:147], v[36:39], v[16:19]\n"
"buffer_load_dwordx4 v2, s[32:35], s63 offen offset:2048 sc1 lds\n"
"v_fmac_f32 v11, v28, v28\n"
"v_mfma_f32_16x16x32_bf16 v[20:23], a[148:151], v[36:39], v[20:23]\n"
"ds_read_b128 v[60:63], v2 offset:48128\n"
"v_add_f32 v10, v10, v29\n"
"v_mfma_f32_16x16x32_bf16 v[20:23], a[36:39], v[32:35], v[20:23]\n"
"v_fmac_f32 v11, v29, v29\n"
"s_waitcnt lgkmcnt(4)\n"
"v_mfma_f32_16x16x32_bf16 v[20:23], v[68:71], v[40:43], v[20:23]\n"
"ds_read_b128 v[32:35], v2 offset:6144\n"
"v_add_f32 v10, v10, v30\n"
"v_mfma_f32_16x16x32_bf16 v[20:23], v[180:183], v[44:47], v[20:23]\n"
"buffer_load_dwordx4 v2, s[32:35], s63 offen offset:3072 sc1 lds\n"
"s_add_u32 m0, s74, 0\n"
"v_mfma_f32_16x16x32_bf16 v[16:19], v[176:179], v[44:47], v[16:19]\n"
"ds_read_b128 v[36:39], v2 offset:20480\n"
"v_fmac_f32 v11, v30, v30\n"
"v_mfma_f32_16x16x32_bf16 v[16:19], v[64:67], v[40:43], v[16:19]\n"
"v_add_f32 v10, v10, v31\n"
"s_waitcnt lgkmcnt(4)\n"
"v_mfma_f32_16x16x32_bf16 v[16:19], a[40:43], v[48:51], v[16:19]\n"
"ds_read_b128 v[40:43], v2 offset:34816\n"
"v_fmac_f32 v11, v31, v31\n"
"v_mfma_f32_16x16x32_bf16 v[16:19], a[152:155], v[52:55], v[16:19]\n"
"buffer_load_dwordx4 v2, s[32:35], s71 offen offset:0 sc1 lds\n"
"v_permlane16_swap_b32 v10, v11\n"
"v_mfma_f32_16x16x32_bf16 v[20:23], a[156:159], v[52:55], v[20:23]\n"
"ds_read_b128 v[44:47], v2 offset:49152\n"
"v_add_f32 v10, v10, v11\n"
"v_mfma_f32_16x16x32_bf16 v[20:23], a[44:47], v[48:51], v[20:23]\n"
"s_waitcnt lgkmcnt(4)\n"
"v_mfma_f32_16x16x32_bf16 v[20:23], v[76:79], v[56:59], v[20:23]\n"
"ds_read_b128 v[48:51], v2 offset:7168\n"
"v_permlane32_swap_b32 v10, v11\n"
"v_mfma_f32_16x16x32_bf16 v[20:23], v[188:191], v[60:63], v[20:23]\n"
"buffer_load_dwordx4 v2, s[32:35], s71 offen offset:1024 sc1 lds\n"
"s_add_u32 s71, s71, 1187840\n"
"v_mfma_f32_16x16x32_bf16 v[16:19], v[184:187], v[60:63], v[16:19]\n"
"ds_read_b128 v[52:55], v2 offset:21504\n"
"s_add_u32 s63, s63, 4096\n"
"v_mfma_f32_16x16x32_bf16 v[16:19], v[72:75], v[56:59], v[16:19]\n"
"s_add_u32 m0, s64, 61440\n"
"s_waitcnt lgkmcnt(4)\n"
"v_mfma_f32_16x16x32_bf16 v[16:19], a[48:51], v[32:35], v[16:19]\n"
"ds_read_b128 v[56:59], v2 offset:35840\n"
"v_add_f32 v10, v10, v11\n"
"v_mfma_f32_16x16x32_bf16 v[16:19], a[160:163], v[36:39], v[16:19]\n"
"buffer_store_dword v10, v3, s[32:35], s68 offen offset:1024 sc1\n"
"s_mov_b32 s67, s68\n"
"v_mfma_f32_16x16x32_bf16 v[20:23], a[164:167], v[36:39], v[20:23]\n"
"ds_read_b128 v[60:63], v2 offset:50176\n"
"s_mov_b32 s68, s70\n"
"v_mfma_f32_16x16x32_bf16 v[20:23], a[52:55], v[32:35], v[20:23]\n"
"s_add_u32 s70, s70, 1187840\n"
"s_waitcnt lgkmcnt(4)\n"
"v_mfma_f32_16x16x32_bf16 v[20:23], v[84:87], v[40:43], v[20:23]\n"
"ds_read_b128 v[32:35], v2 offset:8192\n"
"v_mfma_f32_16x16x32_bf16 v[20:23], v[196:199], v[44:47], v[20:23]\n"
"buffer_load_dwordx4 v2, s[32:35], s63 offen offset:0 sc1 lds\n"
"v_mfma_f32_16x16x32_bf16 v[16:19], v[192:195], v[44:47], v[16:19]\n"
"ds_read_b128 v[36:39], v2 offset:22528\n"
"v_mfma_f32_16x16x32_bf16 v[16:19], v[80:83], v[40:43], v[16:19]\n"
"s_waitcnt lgkmcnt(4)\n"
"v_mfma_f32_16x16x32_bf16 v[16:19], a[56:59], v[48:51], v[16:19]\n"
"ds_read_b128 v[40:43], v2 offset:36864\n"
"v_mfma_f32_16x16x32_bf16 v[16:19], a[168:171], v[52:55], v[16:19]\n"
"buffer_load_dwordx4 v2, s[32:35], s63 offen offset:1024 sc1 lds\n"
"v_mfma_f32_16x16x32_bf16 v[20:23], a[172:175], v[52:55], v[20:23]\n"
"ds_read_b128 v[44:47], v2 offset:51200\n"
"v_mfma_f32_16x16x32_bf16 v[20:23], a[60:63], v[48:51], v[20:23]\n"
"s_waitcnt lgkmcnt(4)\n"
"v_mfma_f32_16x16x32_bf16 v[20:23], v[92:95], v[56:59], v[20:23]\n"
"ds_read_b128 v[48:51], v2 offset:9216\n"
"v_mfma_f32_16x16x32_bf16 v[20:23], v[204:207], v[60:63], v[20:23]\n"
"buffer_load_dwordx4 v2, s[32:35], s63 offen offset:2048 sc1 lds\n"
"v_mfma_f32_16x16x32_bf16 v[16:19], v[200:203], v[60:63], v[16:19]\n"
"ds_read_b128 v[52:55], v2 offset:23552\n"
"v_mfma_f32_16x16x32_bf16 v[16:19], v[88:91], v[56:59], v[16:19]\n"
"s_waitcnt lgkmcnt(4)\n"
"v_mfma_f32_16x16x32_bf16 v[16:19], a[64:67], v[32:35], v[16:19]\n"
"ds_read_b128 v[56:59], v2 offset:37888\n"
"v_mfma_f32_16x16x32_bf16 v[16:19], a[176:179], v[36:39], v[16:19]\n"
"buffer_load_dwordx4 v2, s[32:35], s63 offen offset:3072 sc1 lds\n"
"s_add_u32 s63, s63, 4096\n"
"v_mfma_f32_16x16x32_bf16 v[20:23], a[180:183], v[36:39], v[20:23]\n"
"ds_read_b128 v[60:63], v2 offset:52224\n"
"s_add_u32 m0, s64, 65536\n"
"v_mfma_f32_16x16x32_bf16 v[20:23], a[68:71], v[32:35], v[20:23]\n"
"s_waitcnt lgkmcnt(4)\n"
"v_mfma_f32_16x16x32_bf16 v[20:23], v[100:103], v[40:43], v[20:23]\n"
"ds_read_b128 v[32:35], v2 offset:10240\n"
"v_mfma_f32_16x16x32_bf16 v[20:23], v[212:215], v[44:47], v[20:23]\n"
"buffer_load_dwordx4 v2, s[32:35], s63 offen offset:0 sc1 lds\n"
"v_mfma_f32_16x16x32_bf16 v[16:19], v[208:211], v[44:47], v[16:19]\n"
"ds_read_b128 v[36:39], v2 offset:24576\n"
"v_mfma_f32_16x16x32_bf16 v[16:19], v[96:99], v[40:43], v[16:19]\n"
"s_waitcnt lgkmcnt(4)\n"
"v_mfma_f32_16x16x32_bf16 v[16:19], a[72:75], v[48:51], v[16:19]\n"
"ds_read_b128 v[40:43], v2 offset:38912\n"
"v_mfma_f32_16x16x32_bf16 v[16:19], a[184:187], v[52:55], v[16:19]\n"
"buffer_load_dwordx4 v2, s[32:35], s63 offen offset:1024 sc1 lds\n"
"v_mfma_f32_16x16x32_bf16 v[20:23], a[188:191], v[52:55], v[20:23]\n"
"ds_read_b128 v[44:47], v2 offset:53248\n"
"v_mfma_f32_16x16x32_bf16 v[20:23], a[76:79], v[48:51], v[20:23]\n"
"s_waitcnt lgkmcnt(4)\n"
"v_mfma_f32_16x16x32_bf16 v[20:23], v[108:111], v[56:59], v[20:23]\n"
"ds_read_b128 v[48:51], v2 offset:11264\n"
"v_mfma_f32_16x16x32_bf16 v[20:23], v[220:223], v[60:63], v[20:23]\n"
"buffer_load_dwordx4 v2, s[32:35], s63 offen offset:2048 sc1 lds\n"
"v_mfma_f32_16x16x32_bf16 v[16:19], v[216:219], v[60:63], v[16:19]\n"
"ds_read_b128 v[52:55], v2 offset:25600\n"
"v_mfma_f32_16x16x32_bf16 v[16:19], v[104:107], v[56:59], v[16:19]\n"
"s_waitcnt lgkmcnt(4)\n"
"v_mfma_f32_16x16x32_bf16 v[16:19], a[80:83], v[32:35], v[16:19]\n"
"ds_read_b128 v[56:59], v2 offset:39936\n"
"v_mfma_f32_16x16x32_bf16 v[16:19], a[192:195], v[36:39], v[16:19]\n"
"buffer_load_dwordx4 v2, s[32:35], s63 offen offset:3072 sc1 lds\n"
"s_add_u32 s63, s63, 4096\n"
"v_mfma_f32_16x16x32_bf16 v[20:23], a[196:199], v[36:39], v[20:23]\n"
"ds_read_b128 v[60:63], v2 offset:54272\n"
"s_add_u32 m0, s64, 69632\n"
"v_mfma_f32_16x16x32_bf16 v[20:23], a[84:87], v[32:35], v[20:23]\n"
"s_waitcnt lgkmcnt(4)\n"
"v_mfma_f32_16x16x32_bf16 v[20:23], v[116:119], v[40:43], v[20:23]\n"
"ds_read_b128 v[32:35], v2 offset:12288\n"
"v_mfma_f32_16x16x32_bf16 v[20:23], v[228:231], v[44:47], v[20:23]\n"
"buffer_load_dwordx4 v2, s[32:35], s63 offen offset:0 sc1 lds\n"
"v_mfma_f32_16x16x32_bf16 v[16:19], v[224:227], v[44:47], v[16:19]\n"
"ds_read_b128 v[36:39], v2 offset:26624\n"
"v_mfma_f32_16x16x32_bf16 v[16:19], v[112:115], v[40:43], v[16:19]\n"
"s_waitcnt lgkmcnt(4)\n"
"v_mfma_f32_16x16x32_bf16 v[16:19], a[88:91], v[48:51], v[16:19]\n"
"ds_read_b128 v[40:43], v2 offset:40960\n"
"v_mfma_f32_16x16x32_bf16 v[16:19], a[200:203], v[52:55], v[16:19]\n"
"buffer_load_dwordx4 v2, s[32:35], s63 offen offset:1024 sc1 lds\n"
"s_add_u32 s63, s63, 1175552\n"
"v_mfma_f32_16x16x32_bf16 v[20:23], a[204:207], v[52:55], v[20:23]\n"
"ds_read_b128 v[44:47], v2 offset:55296\n"
"v_mfma_f32_16x16x32_bf16 v[20:23], a[92:95], v[48:51], v[20:23]\n"
"s_waitcnt lgkmcnt(4)\n"
"v_mfma_f32_16x16x32_bf16 v[20:23], v[124:127], v[56:59], v[20:23]\n"
"ds_read_b128 v[48:51], v2 offset:13312\n"
"v_mfma_f32_16x16x32_bf16 v[20:23], v[236:239], v[60:63], v[20:23]\n"
"v_mfma_f32_16x16x32_bf16 v[16:19], v[232:235], v[60:63], v[16:19]\n"
"ds_read_b128 v[52:55], v2 offset:27648\n"
"v_mfma_f32_16x16x32_bf16 v[16:19], v[120:123], v[56:59], v[16:19]\n"
"s_waitcnt lgkmcnt(4)\n"
"v_mfma_f32_16x16x32_bf16 v[16:19], a[96:99], v[32:35], v[16:19]\n"
"ds_read_b128 v[56:59], v2 offset:41984\n"
"v_mfma_f32_16x16x32_bf16 v[16:19], a[208:211], v[36:39], v[16:19]\n"
"v_mfma_f32_16x16x32_bf16 v[20:23], a[212:215], v[36:39], v[20:23]\n"
"ds_read_b128 v[60:63], v2 offset:56320\n"
"v_mfma_f32_16x16x32_bf16 v[20:23], a[100:103], v[32:35], v[20:23]\n"
"s_waitcnt lgkmcnt(4)\n"
"s_waitcnt vmcnt(13)\n"
"s_waitcnt vmcnt(10)\n"
"s_barrier\n"
"v_mfma_f32_16x16x32_bf16 v[20:23], v[132:135], v[40:43], v[20:23]\n"
"ds_read_b128 v[32:35], v4 offset:0\n"
"v_mfma_f32_16x16x32_bf16 v[20:23], v[244:247], v[44:47], v[20:23]\n"
"v_mfma_f32_16x16x32_bf16 v[16:19], v[240:243], v[44:47], v[16:19]\n"
"ds_read_b128 v[36:39], v4 offset:14336\n"
"v_mfma_f32_16x16x32_bf16 v[16:19], v[128:131], v[40:43], v[16:19]\n"
"s_waitcnt lgkmcnt(4)\n"
"v_mfma_f32_16x16x32_bf16 v[16:19], a[104:107], v[48:51], v[16:19]\n"
"ds_read_b128 v[40:43], v4 offset:28672\n"
"v_mfma_f32_16x16x32_bf16 v[16:19], a[216:219], v[52:55], v[16:19]\n"
"buffer_store_dwordx2 v[8:9], v2, s[92:95], s67 idxen sc1\n"
"v_mfma_f32_16x16x32_bf16 v[20:23], a[220:223], v[52:55], v[20:23]\n"
"ds_read_b128 v[44:47], v4 offset:43008\n"
"v_mfma_f32_16x16x32_bf16 v[20:23], a[108:111], v[48:51], v[20:23]\n"
"s_waitcnt lgkmcnt(4)\n"
"v_mfma_f32_16x16x32_bf16 v[20:23], v[140:143], v[56:59], v[20:23]\n"
"ds_read_b128 v[48:51], v4 offset:1024\n"
"v_mfma_f32_16x16x32_bf16 v[20:23], v[252:255], v[60:63], v[20:23]\n"
"v_mfma_f32_16x16x32_bf16 v[16:19], v[248:251], v[60:63], v[16:19]\n"
"ds_read_b128 v[52:55], v4 offset:15360\n"
"s_sub_u32 s62, s62, 1\n"
"v_mfma_f32_16x16x32_bf16 v[16:19], v[136:139], v[56:59], v[16:19]\n"
"s_cbranch_scc1 .L_dab_0\n"
"s_waitcnt lgkmcnt(4)\n"
"v_mfma_f32_16x16x32_bf16 v[24:27], a[0:3], v[32:35], 0\n"
"ds_read_b128 v[56:59], v4 offset:29696\n"
"ds_read_b128 v[12:15], v5 offset:0\n"
"v_mfma_f32_16x16x32_bf16 v[24:27], a[112:115], v[36:39], v[24:27]\n"
"v_mfma_f32_16x16x32_bf16 v[28:31], a[116:119], v[36:39], 0\n"
"ds_read_b128 v[60:63], v4 offset:44032\n"
"v_mfma_f32_16x16x32_bf16 v[28:31], a[4:7], v[32:35], v[28:31]\n"
"s_waitcnt lgkmcnt(5)\n"
"v_mfma_f32_16x16x32_bf16 v[28:31], a[228:231], v[40:43], v[28:31]\n"
"ds_read_b128 v[32:35], v4 offset:2048\n"
"v_mfma_f32_16x16x32_bf16 v[28:31], v[148:151], v[44:47], v[28:31]\n"
"s_waitcnt lgkmcnt(2)\n"
"v_add_f32 v16, v16, v12\n"
"v_mfma_f32_16x16x32_bf16 v[24:27], v[144:147], v[44:47], v[24:27]\n"
"ds_read_b128 v[36:39], v4 offset:16384\n"
"v_add_f32 v17, v17, v13\n"
"v_mfma_f32_16x16x32_bf16 v[24:27], a[224:227], v[40:43], v[24:27]\n"
"v_add_f32 v18, v18, v14\n"
"v_mfma_f32_16x16x32_bf16 v[24:27], a[8:11], v[48:51], v[24:27]\n"
"ds_read_b128 v[40:43], v4 offset:30720\n"
"v_add_f32 v19, v19, v15\n"
"v_mfma_f32_16x16x32_bf16 v[24:27], a[120:123], v[52:55], v[24:27]\n"
"ds_read_b128 v[12:15], v5 offset:1024\n"
"v_mfma_f32_16x16x32_bf16 v[28:31], a[124:127], v[52:55], v[28:31]\n"
"ds_read_b128 v[44:47], v4 offset:45056\n"
"v_mfma_f32_16x16x32_bf16 v[28:31], a[12:15], v[48:51], v[28:31]\n"
"s_waitcnt lgkmcnt(5)\n"
"v_mfma_f32_16x16x32_bf16 v[28:31], a[236:239], v[56:59], v[28:31]\n"
"ds_read_b128 v[48:51], v4 offset:3072\n"
"v_mfma_f32_16x16x32_bf16 v[28:31], v[156:159], v[60:63], v[28:31]\n"
"v_mfma_f32_16x16x32_bf16 v[24:27], v[152:155], v[60:63], v[24:27]\n"
"ds_read_b128 v[52:55], v4 offset:17408\n"
"s_waitcnt lgkmcnt(3)\n"
"v_mfma_f32_16x16x32_bf16 v[24:27], a[232:235], v[56:59], v[24:27]\n"
"v_add_f32 v20, v20, v12\n"
"v_mfma_f32_16x16x32_bf16 v[24:27], a[16:19], v[32:35], v[24:27]\n"
"ds_read_b128 v[56:59], v4 offset:31744\n"
"v_add_f32 v21, v21, v13\n"
"v_mfma_f32_16x16x32_bf16 v[24:27], a[128:131], v[36:39], v[24:27]\n"
"v_add_f32 v22, v22, v14\n"
"v_add_f32 v23, v23, v15\n"
"v_mfma_f32_16x16x32_bf16 v[28:31], a[132:135], v[36:39], v[28:31]\n"
"ds_read_b128 v[60:63], v4 offset:46080\n"
"v_cvt_pk_bf16_f32 v12, v16, v17\n"
"v_mfma_f32_16x16x32_bf16 v[28:31], a[20:23], v[32:35], v[28:31]\n"
"s_waitcnt lgkmcnt(4)\n"
"s_waitcnt vmcnt(1)\n"
"s_barrier\n"
"v_mfma_f32_16x16x32_bf16 v[28:31], a[244:247], v[40:43], v[28:31]\n"
"ds_read_b128 v[32:35], v4 offset:4096\n"
"s_add_u32 m0, s64, 0\n"
"v_mfma_f32_16x16x32_bf16 v[28:31], v[164:167], v[44:47], v[28:31]\n"
"buffer_load_dwordx4 v2, s[32:35], s63 offen offset:0 sc1 lds\n"
"v_cvt_pk_bf16_f32 v13, v18, v19\n"
"v_mfma_f32_16x16x32_bf16 v[24:27], v[160:163], v[44:47], v[24:27]\n"
"ds_read_b128 v[36:39], v4 offset:18432\n"
"v_cvt_pk_bf16_f32 v14, v20, v21\n"
"v_mfma_f32_16x16x32_bf16 v[24:27], a[240:243], v[40:43], v[24:27]\n"
"v_cvt_pk_bf16_f32 v15, v22, v23\n"
"s_waitcnt lgkmcnt(4)\n"
"v_mfma_f32_16x16x32_bf16 v[24:27], a[24:27], v[48:51], v[24:27]\n"
"ds_read_b128 v[40:43], v4 offset:32768\n"
"v_mfma_f32_16x16x32_bf16 v[24:27], a[136:139], v[52:55], v[24:27]\n"
"buffer_store_dwordx4 v[12:15], v2, s[32:35], s69 offen sc1\n"
"v_add_f32 v10, v16, v17\n"
"v_mfma_f32_16x16x32_bf16 v[28:31], a[140:143], v[52:55], v[28:31]\n"
"ds_read_b128 v[44:47], v4 offset:47104\n"
"v_mul_f32 v11, v16, v16\n"
"v_mfma_f32_16x16x32_bf16 v[28:31], a[28:31], v[48:51], v[28:31]\n"
"v_fmac_f32 v11, v17, v17\n"
"s_waitcnt lgkmcnt(4)\n"
"v_mfma_f32_16x16x32_bf16 v[28:31], a[252:255], v[56:59], v[28:31]\n"
"ds_read_b128 v[48:51], v4 offset:5120\n"
"v_add_f32 v10, v10, v18\n"
"v_mfma_f32_16x16x32_bf16 v[28:31], v[172:175], v[60:63], v[28:31]\n"
"buffer_load_dwordx4 v2, s[32:35], s63 offen offset:1024 sc1 lds\n"
"v_fmac_f32 v11, v18, v18\n"
"v_mfma_f32_16x16x32_bf16 v[24:27], v[168:171], v[60:63], v[24:27]\n"
"ds_read_b128 v[52:55], v4 offset:19456\n"
"v_add_f32 v10, v10, v19\n"
"v_mfma_f32_16x16x32_bf16 v[24:27], a[248:251], v[56:59], v[24:27]\n"
"v_fmac_f32 v11, v19, v19\n"
"s_waitcnt lgkmcnt(4)\n"
"v_mfma_f32_16x16x32_bf16 v[24:27], a[32:35], v[32:35], v[24:27]\n"
"ds_read_b128 v[56:59], v4 offset:33792\n"
"v_add_f32 v10, v10, v20\n"
"v_mfma_f32_16x16x32_bf16 v[24:27], a[144:147], v[36:39], v[24:27]\n"
"buffer_load_dwordx4 v2, s[32:35], s63 offen offset:2048 sc1 lds\n"
"v_fmac_f32 v11, v20, v20\n"
"v_mfma_f32_16x16x32_bf16 v[28:31], a[148:151], v[36:39], v[28:31]\n"
"ds_read_b128 v[60:63], v4 offset:48128\n"
"v_add_f32 v10, v10, v21\n"
"v_mfma_f32_16x16x32_bf16 v[28:31], a[36:39], v[32:35], v[28:31]\n"
"v_fmac_f32 v11, v21, v21\n"
"s_waitcnt lgkmcnt(4)\n"
"v_mfma_f32_16x16x32_bf16 v[28:31], v[68:71], v[40:43], v[28:31]\n"
"ds_read_b128 v[32:35], v4 offset:6144\n"
"v_add_f32 v10, v10, v22\n"
"v_mfma_f32_16x16x32_bf16 v[28:31], v[180:183], v[44:47], v[28:31]\n"
"buffer_load_dwordx4 v2, s[32:35], s63 offen offset:3072 sc1 lds\n"
"s_add_u32 m0, s74, 8192\n"
"v_mfma_f32_16x16x32_bf16 v[24:27], v[176:179], v[44:47], v[24:27]\n"
"ds_read_b128 v[36:39], v4 offset:20480\n"
"v_fmac_f32 v11, v22, v22\n"
"v_mfma_f32_16x16x32_bf16 v[24:27], v[64:67], v[40:43], v[24:27]\n"
"v_add_f32 v10, v10, v23\n"
"s_waitcnt lgkmcnt(4)\n"
"v_mfma_f32_16x16x32_bf16 v[24:27], a[40:43], v[48:51], v[24:27]\n"
"ds_read_b128 v[40:43], v4 offset:34816\n"
"v_fmac_f32 v11, v23, v23\n"
"v_mfma_f32_16x16x32_bf16 v[24:27], a[152:155], v[52:55], v[24:27]\n"
"buffer_load_dwordx4 v2, s[32:35], s71 offen offset:0 sc1 lds\n"
"v_permlane16_swap_b32 v10, v11\n"
"v_mfma_f32_16x16x32_bf16 v[28:31], a[156:159], v[52:55], v[28:31]\n"
"ds_read_b128 v[44:47], v4 offset:49152\n"
"v_add_f32 v10, v10, v11\n"
"v_mfma_f32_16x16x32_bf16 v[28:31], a[44:47], v[48:51], v[28:31]\n"
"s_waitcnt lgkmcnt(4)\n"
"v_mfma_f32_16x16x32_bf16 v[28:31], v[76:79], v[56:59], v[28:31]\n"
"ds_read_b128 v[48:51], v4 offset:7168\n"
"v_permlane32_swap_b32 v10, v11\n"
"v_mfma_f32_16x16x32_bf16 v[28:31], v[188:191], v[60:63], v[28:31]\n"
"buffer_load_dwordx4 v2, s[32:35], s71 offen offset:1024 sc1 lds\n"
"s_add_u32 s71, s71, 1187840\n"
"v_mfma_f32_16x16x32_bf16 v[24:27], v[184:187], v[60:63], v[24:27]\n"
"ds_read_b128 v[52:55], v4 offset:21504\n"
"s_add_u32 s63, s63, 4096\n"
"v_mfma_f32_16x16x32_bf16 v[24:27], v[72:75], v[56:59], v[24:27]\n"
"s_add_u32 m0, s64, 4096\n"
"s_waitcnt lgkmcnt(4)\n"
"v_mfma_f32_16x16x32_bf16 v[24:27], a[48:51], v[32:35], v[24:27]\n"
"ds_read_b128 v[56:59], v4 offset:35840\n"
"v_add_f32 v10, v10, v11\n"
"v_mfma_f32_16x16x32_bf16 v[24:27], a[160:163], v[36:39], v[24:27]\n"
"buffer_store_dword v10, v3, s[32:35], s69 offen offset:1024 sc1\n"
"s_mov_b32 s67, s69\n"
"v_mfma_f32_16x16x32_bf16 v[28:31], a[164:167], v[36:39], v[28:31]\n"
"ds_read_b128 v[60:63], v4 offset:50176\n"
"s_mov_b32 s69, s70\n"
"v_mfma_f32_16x16x32_bf16 v[28:31], a[52:55], v[32:35], v[28:31]\n"
"s_add_u32 s70, s70, 1187840\n"
"s_waitcnt lgkmcnt(4)\n"
"v_mfma_f32_16x16x32_bf16 v[28:31], v[84:87], v[40:43], v[28:31]\n"
"ds_read_b128 v[32:35], v4 offset:8192\n"
"v_mfma_f32_16x16x32_bf16 v[28:31], v[196:199], v[44:47], v[28:31]\n"
"buffer_load_dwordx4 v2, s[32:35], s63 offen offset:0 sc1 lds\n"
"v_mfma_f32_16x16x32_bf16 v[24:27], v[192:195], v[44:47], v[24:27]\n"
"ds_read_b128 v[36:39], v4 offset:22528\n"
"v_mfma_f32_16x16x32_bf16 v[24:27], v[80:83], v[40:43], v[24:27]\n"
"s_waitcnt lgkmcnt(4)\n"
"v_mfma_f32_16x16x32_bf16 v[24:27], a[56:59], v[48:51], v[24:27]\n"
"ds_read_b128 v[40:43], v4 offset:36864\n"
"v_mfma_f32_16x16x32_bf16 v[24:27], a[168:171], v[52:55], v[24:27]\n"
"buffer_load_dwordx4 v2, s[32:35], s63 offen offset:1024 sc1 lds\n"
"v_mfma_f32_16x16x32_bf16 v[28:31], a[172:175], v[52:55], v[28:31]\n"
"ds_read_b128 v[44:47], v4 offset:51200\n"
"v_mfma_f32_16x16x32_bf16 v[28:31], a[60:63], v[48:51], v[28:31]\n"
"s_waitcnt lgkmcnt(4)\n"
"v_mfma_f32_16x16x32_bf16 v[28:31], v[92:95], v[56:59], v[28:31]\n"
"ds_read_b128 v[48:51], v4 offset:9216\n"
"v_mfma_f32_16x16x32_bf16 v[28:31], v[204:207], v[60:63], v[28:31]\n"
"buffer_load_dwordx4 v2, s[32:35], s63 offen offset:2048 sc1 lds\n"
"v_mfma_f32_16x16x32_bf16 v[24:27], v[200:203], v[60:63], v[24:27]\n"
"ds_read_b128 v[52:55], v4 offset:23552\n"
"v_mfma_f32_16x16x32_bf16 v[24:27], v[88:91], v[56:59], v[24:27]\n"
"s_waitcnt lgkmcnt(4)\n"
"v_mfma_f32_16x16x32_bf16 v[24:27], a[64:67], v[32:35], v[24:27]\n"
"ds_read_b128 v[56:59], v4 offset:37888\n"
"v_mfma_f32_16x16x32_bf16 v[24:27], a[176:179], v[36:39], v[24:27]\n"
"buffer_load_dwordx4 v2, s[32:35], s63 offen offset:3072 sc1 lds\n"
"s_add_u32 s63, s63, 4096\n"
"v_mfma_f32_16x16x32_bf16 v[28:31], a[180:183], v[36:39], v[28:31]\n"
"ds_read_b128 v[60:63], v4 offset:52224\n"
"s_add_u32 m0, s64, 8192\n"
"v_mfma_f32_16x16x32_bf16 v[28:31], a[68:71], v[32:35], v[28:31]\n"
"s_waitcnt lgkmcnt(4)\n"
"v_mfma_f32_16x16x32_bf16 v[28:31], v[100:103], v[40:43], v[28:31]\n"
"ds_read_b128 v[32:35], v4 offset:10240\n"
"v_mfma_f32_16x16x32_bf16 v[28:31], v[212:215], v[44:47], v[28:31]\n"
"buffer_load_dwordx4 v2, s[32:35], s63 offen offset:0 sc1 lds\n"
"v_mfma_f32_16x16x32_bf16 v[24:27], v[208:211], v[44:47], v[24:27]\n"
"ds_read_b128 v[36:39], v4 offset:24576\n"
"v_mfma_f32_16x16x32_bf16 v[24:27], v[96:99], v[40:43], v[24:27]\n"
"s_waitcnt lgkmcnt(4)\n"
"v_mfma_f32_16x16x32_bf16 v[24:27], a[72:75], v[48:51], v[24:27]\n"
"ds_read_b128 v[40:43], v4 offset:38912\n"
"v_mfma_f32_16x16x32_bf16 v[24:27], a[184:187], v[52:55], v[24:27]\n"
"buffer_load_dwordx4 v2, s[32:35], s63 offen offset:1024 sc1 lds\n"
"v_mfma_f32_16x16x32_bf16 v[28:31], a[188:191], v[52:55], v[28:31]\n"
"ds_read_b128 v[44:47], v4 offset:53248\n"
"v_mfma_f32_16x16x32_bf16 v[28:31], a[76:79], v[48:51], v[28:31]\n"
"s_waitcnt lgkmcnt(4)\n"
"v_mfma_f32_16x16x32_bf16 v[28:31], v[108:111], v[56:59], v[28:31]\n"
"ds_read_b128 v[48:51], v4 offset:11264\n"
"v_mfma_f32_16x16x32_bf16 v[28:31], v[220:223], v[60:63], v[28:31]\n"
"buffer_load_dwordx4 v2, s[32:35], s63 offen offset:2048 sc1 lds\n"
"v_mfma_f32_16x16x32_bf16 v[24:27], v[216:219], v[60:63], v[24:27]\n"
"ds_read_b128 v[52:55], v4 offset:25600\n"
"v_mfma_f32_16x16x32_bf16 v[24:27], v[104:107], v[56:59], v[24:27]\n"
"s_waitcnt lgkmcnt(4)\n"
"v_mfma_f32_16x16x32_bf16 v[24:27], a[80:83], v[32:35], v[24:27]\n"
"ds_read_b128 v[56:59], v4 offset:39936\n"
"v_mfma_f32_16x16x32_bf16 v[24:27], a[192:195], v[36:39], v[24:27]\n"
"buffer_load_dwordx4 v2, s[32:35], s63 offen offset:3072 sc1 lds\n"
"s_add_u32 s63, s63, 4096\n"
"v_mfma_f32_16x16x32_bf16 v[28:31], a[196:199], v[36:39], v[28:31]\n"
"ds_read_b128 v[60:63], v4 offset:54272\n"
"s_add_u32 m0, s64, 12288\n"
"v_mfma_f32_16x16x32_bf16 v[28:31], a[84:87], v[32:35], v[28:31]\n"
"s_waitcnt lgkmcnt(4)\n"
"v_mfma_f32_16x16x32_bf16 v[28:31], v[116:119], v[40:43], v[28:31]\n"
"ds_read_b128 v[32:35], v4 offset:12288\n"
"v_mfma_f32_16x16x32_bf16 v[28:31], v[228:231], v[44:47], v[28:31]\n"
"buffer_load_dwordx4 v2, s[32:35], s63 offen offset:0 sc1 lds\n"
"v_mfma_f32_16x16x32_bf16 v[24:27], v[224:227], v[44:47], v[24:27]\n"
"ds_read_b128 v[36:39], v4 offset:26624\n"
"v_mfma_f32_16x16x32_bf16 v[24:27], v[112:115], v[40:43], v[24:27]\n"
"s_waitcnt lgkmcnt(4)\n"
"v_mfma_f32_16x16x32_bf16 v[24:27], a[88:91], v[48:51], v[24:27]\n"
"ds_read_b128 v[40:43], v4 offset:40960\n"
"v_mfma_f32_16x16x32_bf16 v[24:27], a[200:203], v[52:55], v[24:27]\n"
"buffer_load_dwordx4 v2, s[32:35], s63 offen offset:1024 sc1 lds\n"
"s_add_u32 s63, s63, 1175552\n"
"v_mfma_f32_16x16x32_bf16 v[28:31], a[204:207], v[52:55], v[28:31]\n"
"ds_read_b128 v[44:47], v4 offset:55296\n"
"v_mfma_f32_16x16x32_bf16 v[28:31], a[92:95], v[48:51], v[28:31]\n"
"s_waitcnt lgkmcnt(4)\n"
"v_mfma_f32_16x16x32_bf16 v[28:31], v[124:127], v[56:59], v[28:31]\n"
"ds_read_b128 v[48:51], v4 offset:13312\n"
"v_mfma_f32_16x16x32_bf16 v[28:31], v[236:239], v[60:63], v[28:31]\n"
"v_mfma_f32_16x16x32_bf16 v[24:27], v[232:235], v[60:63], v[24:27]\n"
"ds_read_b128 v[52:55], v4 offset:27648\n"
"v_mfma_f32_16x16x32_bf16 v[24:27], v[120:123], v[56:59], v[24:27]\n"
"s_waitcnt lgkmcnt(4)\n"
"v_mfma_f32_16x16x32_bf16 v[24:27], a[96:99], v[32:35], v[24:27]\n"
"ds_read_b128 v[56:59], v4 offset:41984\n"
"v_mfma_f32_16x16x32_bf16 v[24:27], a[208:211], v[36:39], v[24:27]\n"
"v_mfma_f32_16x16x32_bf16 v[28:31], a[212:215], v[36:39], v[28:31]\n"
"ds_read_b128 v[60:63], v4 offset:56320\n"
"v_mfma_f32_16x16x32_bf16 v[28:31], a[100:103], v[32:35], v[28:31]\n"
"s_waitcnt lgkmcnt(4)\n"
"s_waitcnt vmcnt(13)\n"
"s_waitcnt vmcnt(10)\n"
"s_barrier\n"
"v_mfma_f32_16x16x32_bf16 v[28:31], v[132:135], v[40:43], v[28:31]\n"
"ds_read_b128 v[32:35], v2 offset:0\n"
"v_mfma_f32_16x16x32_bf16 v[28:31], v[244:247], v[44:47], v[28:31]\n"
"v_mfma_f32_16x16x32_bf16 v[24:27], v[240:243], v[44:47], v[24:27]\n"
"ds_read_b128 v[36:39], v2 offset:14336\n"
"v_mfma_f32_16x16x32_bf16 v[24:27], v[128:131], v[40:43], v[24:27]\n"
"s_waitcnt lgkmcnt(4)\n"
"v_mfma_f32_16x16x32_bf16 v[24:27], a[104:107], v[48:51], v[24:27]\n"
"ds_read_b128 v[40:43], v2 offset:28672\n"
"v_mfma_f32_16x16x32_bf16 v[24:27], a[216:219], v[52:55], v[24:27]\n"
"buffer_store_dwordx2 v[8:9], v2, s[92:95], s67 idxen sc1\n"
"v_mfma_f32_16x16x32_bf16 v[28:31], a[220:223], v[52:55], v[28:31]\n"
"ds_read_b128 v[44:47], v2 offset:43008\n"
"v_mfma_f32_16x16x32_bf16 v[28:31], a[108:111], v[48:51], v[28:31]\n"
"s_waitcnt lgkmcnt(4)\n"
"v_mfma_f32_16x16x32_bf16 v[28:31], v[140:143], v[56:59], v[28:31]\n"
"ds_read_b128 v[48:51], v2 offset:1024\n"
"v_mfma_f32_16x16x32_bf16 v[28:31], v[252:255], v[60:63], v[28:31]\n"
"v_mfma_f32_16x16x32_bf16 v[24:27], v[248:251], v[60:63], v[24:27]\n"
"ds_read_b128 v[52:55], v2 offset:15360\n"
"s_cmp_eq_u32 s62, 0\n"
"v_mfma_f32_16x16x32_bf16 v[24:27], v[136:139], v[56:59], v[24:27]\n"
"s_cbranch_scc0 .L_dal_0\n"
"s_mov_b32 s48, 0x7fff0000\n"
"v_writelane_b32 v8, 0, 63\n"
"v_writelane_b32 v9, 0, 63\n"
"v_writelane_b32 v6, 0, 63\n"
"v_writelane_b32 v7, 0, 63\n"
"v_writelane_b32 v0, s48, 63\n"
"s_branch .L_dal_0\n"
".L_dab_0:\n"
"s_mov_b32 s62, 15\n"
"s_add_u32 s74, s74, 8192\n"
"v_lshrrev_b32 v5, 8, v2\n"
"v_lshl_add_u32 v5, v5, 4, s74\n"
"v_and_b32 v3, 128, v2\n"
"v_lshl_add_u32 v5, v3, 2, v5\n"
"v_and_b32 v3, 16, v2\n"
"v_lshl_add_u32 v5, v3, 4, v5\n"
"v_and_b32 v3, 96, v2\n"
"v_lshl_add_u32 v5, v3, 1, v5\n"
"v_lshrrev_b32 v3, 2, v2\n"
"v_add_u32 v3, s74, v3\n"
".p2align 6\n"
".L_dbl_0:\n"
"s_waitcnt lgkmcnt(4)\n"
"v_mfma_f32_16x16x32_bf16 v[24:27], a[0:3], v[32:35], 0\n"
"ds_read_b128 v[56:59], v4 offset:29696\n"
"v_mfma_f32_16x16x32_bf16 v[24:27], a[112:115], v[36:39], v[24:27]\n"
"v_mfma_f32_16x16x32_bf16 v[28:31], a[116:119], v[36:39], 0\n"
"ds_read_b128 v[60:63], v4 offset:44032\n"
"v_mfma_f32_16x16x32_bf16 v[28:31], a[4:7], v[32:35], v[28:31]\n"
"v_cvt_pk_bf16_f32 v15, v22, v23\n"
"s_waitcnt lgkmcnt(4)\n"
"v_mfma_f32_16x16x32_bf16 v[28:31], a[228:231], v[40:43], v[28:31]\n"
"ds_read_b128 v[32:35], v4 offset:2048\n"
"v_cvt_pk_bf16_f32 v14, v20, v21\n"
"v_mfma_f32_16x16x32_bf16 v[28:31], v[148:151], v[44:47], v[28:31]\n"
"v_cvt_pk_bf16_f32 v13, v18, v19\n"
"v_cvt_pk_bf16_f32 v12, v16, v17\n"
"v_mfma_f32_16x16x32_bf16 v[24:27], v[144:147], v[44:47], v[24:27]\n"
"ds_read_b128 v[36:39], v4 offset:16384\n"
"ds_write_b128 v5, v[12:15]\n"
"v_mfma_f32_16x16x32_bf16 v[24:27], a[224:227], v[40:43], v[24:27]\n"
"ds_read2st64_b32 v[12:13], v3 offset0:0 offset1:1\n"
"s_waitcnt lgkmcnt(6)\n"
"v_mfma_f32_16x16x32_bf16 v[24:27], a[8:11], v[48:51], v[24:27]\n"
"ds_read_b128 v[40:43], v4 offset:30720\n"
"ds_read2st64_b32 v[14:15], v3 offset0:2 offset1:3\n"
"v_mfma_f32_16x16x32_bf16 v[24:27], a[120:123], v[52:55], v[24:27]\n"
"v_mfma_f32_16x16x32_bf16 v[28:31], a[124:127], v[52:55], v[28:31]\n"
"ds_read_b128 v[44:47], v4 offset:45056\n"
"v_mfma_f32_16x16x32_bf16 v[28:31], a[12:15], v[48:51], v[28:31]\n"
"s_waitcnt lgkmcnt(7)\n"
"v_mfma_f32_16x16x32_bf16 v[28:31], a[236:239], v[56:59], v[28:31]\n"
"ds_read_b128 v[48:51], v4 offset:3072\n"
"v_mfma_f32_16x16x32_bf16 v[28:31], v[156:159], v[60:63], v[28:31]\n"
"s_waitcnt lgkmcnt(2)\n"
"buffer_atomic_pk_add_bf16 v12, v1, s[44:47], s65 offen offset:0 sc1\n"
"v_mfma_f32_16x16x32_bf16 v[24:27], v[152:155], v[60:63], v[24:27]\n"
"ds_read_b128 v[52:55], v4 offset:17408\n"
"v_mfma_f32_16x16x32_bf16 v[24:27], a[232:235], v[56:59], v[24:27]\n"
"buffer_atomic_pk_add_bf16 v13, v1, s[44:47], s65 offen offset:2048 sc1\n"
"v_mfma_f32_16x16x32_bf16 v[24:27], a[16:19], v[32:35], v[24:27]\n"
"ds_read_b128 v[56:59], v4 offset:31744\n"
"s_add_u32 s65, s65, 16384\n"
"v_mfma_f32_16x16x32_bf16 v[24:27], a[128:131], v[36:39], v[24:27]\n"
"buffer_atomic_pk_add_bf16 v14, v1, s[44:47], s65 offen offset:0 sc1\n"
"v_mfma_f32_16x16x32_bf16 v[28:31], a[132:135], v[36:39], v[28:31]\n"
"ds_read_b128 v[60:63], v4 offset:46080\n"
"v_mfma_f32_16x16x32_bf16 v[28:31], a[20:23], v[32:35], v[28:31]\n"
"s_waitcnt lgkmcnt(4)\n"
"s_waitcnt vmcnt(3)\n"
"s_barrier\n"
"v_mfma_f32_16x16x32_bf16 v[28:31], a[244:247], v[40:43], v[28:31]\n"
"ds_read_b128 v[32:35], v4 offset:4096\n"
"s_add_u32 m0, s64, 0\n"
"v_mfma_f32_16x16x32_bf16 v[28:31], v[164:167], v[44:47], v[28:31]\n"
"buffer_atomic_pk_add_bf16 v15, v1, s[44:47], s65 offen offset:2048 sc1\n"
"s_add_u32 s65, s65, 16384\n"
"v_mfma_f32_16x16x32_bf16 v[24:27], v[160:163], v[44:47], v[24:27]\n"
"ds_read_b128 v[36:39], v4 offset:18432\n"
"v_mfma_f32_16x16x32_bf16 v[24:27], a[240:243], v[40:43], v[24:27]\n"
"s_waitcnt lgkmcnt(4)\n"
"v_mfma_f32_16x16x32_bf16 v[24:27], a[24:27], v[48:51], v[24:27]\n"
"ds_read_b128 v[40:43], v4 offset:32768\n"
"v_mfma_f32_16x16x32_bf16 v[24:27], a[136:139], v[52:55], v[24:27]\n"
"buffer_load_dwordx4 v2, s[32:35], s63 offen offset:0 sc1 lds\n"
"v_mfma_f32_16x16x32_bf16 v[28:31], a[140:143], v[52:55], v[28:31]\n"
"ds_read_b128 v[44:47], v4 offset:47104\n"
"v_mfma_f32_16x16x32_bf16 v[28:31], a[28:31], v[48:51], v[28:31]\n"
"s_waitcnt lgkmcnt(4)\n"
"v_mfma_f32_16x16x32_bf16 v[28:31], a[252:255], v[56:59], v[28:31]\n"
"ds_read_b128 v[48:51], v4 offset:5120\n"
"v_mfma_f32_16x16x32_bf16 v[28:31], v[172:175], v[60:63], v[28:31]\n"
"buffer_load_dwordx4 v2, s[32:35], s63 offen offset:1024 sc1 lds\n"
"v_mfma_f32_16x16x32_bf16 v[24:27], v[168:171], v[60:63], v[24:27]\n"
"ds_read_b128 v[52:55], v4 offset:19456\n"
"v_mfma_f32_16x16x32_bf16 v[24:27], a[248:251], v[56:59], v[24:27]\n"
"s_waitcnt lgkmcnt(4)\n"
"v_mfma_f32_16x16x32_bf16 v[24:27], a[32:35], v[32:35], v[24:27]\n"
"ds_read_b128 v[56:59], v4 offset:33792\n"
"v_mfma_f32_16x16x32_bf16 v[24:27], a[144:147], v[36:39], v[24:27]\n"
"buffer_load_dwordx4 v2, s[32:35], s63 offen offset:2048 sc1 lds\n"
"v_mfma_f32_16x16x32_bf16 v[28:31], a[148:151], v[36:39], v[28:31]\n"
"ds_read_b128 v[60:63], v4 offset:48128\n"
"v_mfma_f32_16x16x32_bf16 v[28:31], a[36:39], v[32:35], v[28:31]\n"
"s_waitcnt lgkmcnt(4)\n"
"v_mfma_f32_16x16x32_bf16 v[28:31], v[68:71], v[40:43], v[28:31]\n"
"ds_read_b128 v[32:35], v4 offset:6144\n"
"v_mfma_f32_16x16x32_bf16 v[28:31], v[180:183], v[44:47], v[28:31]\n"
"buffer_load_dwordx4 v2, s[32:35], s63 offen offset:3072 sc1 lds\n"
"v_mfma_f32_16x16x32_bf16 v[24:27], v[176:179], v[44:47], v[24:27]\n"
"ds_read_b128 v[36:39], v4 offset:20480\n"
"v_mfma_f32_16x16x32_bf16 v[24:27], v[64:67], v[40:43], v[24:27]\n"
"s_waitcnt lgkmcnt(4)\n"
"v_mfma_f32_16x16x32_bf16 v[24:27], a[40:43], v[48:51], v[24:27]\n"
"ds_read_b128 v[40:43], v4 offset:34816\n"
"s_add_u32 s63, s63, 4096\n"
"v_mfma_f32_16x16x32_bf16 v[24:27], a[152:155], v[52:55], v[24:27]\n"
"s_add_u32 m0, s64, 4096\n"
"v_mfma_f32_16x16x32_bf16 v[28:31], a[156:159], v[52:55], v[28:31]\n"
"ds_read_b128 v[44:47], v4 offset:49152\n"
"v_mfma_f32_16x16x32_bf16 v[28:31], a[44:47], v[48:51], v[28:31]\n"
"s_waitcnt lgkmcnt(4)\n"
"v_mfma_f32_16x16x32_bf16 v[28:31], v[76:79], v[56:59], v[28:31]\n"
"ds_read_b128 v[48:51], v4 offset:7168\n"
"v_mfma_f32_16x16x32_bf16 v[28:31], v[188:191], v[60:63], v[28:31]\n"
"buffer_load_dwordx4 v2, s[32:35], s63 offen offset:0 sc1 lds\n"
"v_mfma_f32_16x16x32_bf16 v[24:27], v[184:187], v[60:63], v[24:27]\n"
"ds_read_b128 v[52:55], v4 offset:21504\n"
"v_mfma_f32_16x16x32_bf16 v[24:27], v[72:75], v[56:59], v[24:27]\n"
"s_waitcnt lgkmcnt(4)\n"
"v_mfma_f32_16x16x32_bf16 v[24:27], a[48:51], v[32:35], v[24:27]\n"
"ds_read_b128 v[56:59], v4 offset:35840\n"
"v_mfma_f32_16x16x32_bf16 v[24:27], a[160:163], v[36:39], v[24:27]\n"
"buffer_load_dwordx4 v2, s[32:35], s63 offen offset:1024 sc1 lds\n"
"v_mfma_f32_16x16x32_bf16 v[28:31], a[164:167], v[36:39], v[28:31]\n"
"ds_read_b128 v[60:63], v4 offset:50176\n"
"v_mfma_f32_16x16x32_bf16 v[28:31], a[52:55], v[32:35], v[28:31]\n"
"s_waitcnt lgkmcnt(4)\n"
"v_mfma_f32_16x16x32_bf16 v[28:31], v[84:87], v[40:43], v[28:31]\n"
"ds_read_b128 v[32:35], v4 offset:8192\n"
"v_mfma_f32_16x16x32_bf16 v[28:31], v[196:199], v[44:47], v[28:31]\n"
"buffer_load_dwordx4 v2, s[32:35], s63 offen offset:2048 sc1 lds\n"
"v_mfma_f32_16x16x32_bf16 v[24:27], v[192:195], v[44:47], v[24:27]\n"
"ds_read_b128 v[36:39], v4 offset:22528\n"
"v_mfma_f32_16x16x32_bf16 v[24:27], v[80:83], v[40:43], v[24:27]\n"
"s_waitcnt lgkmcnt(4)\n"
"v_mfma_f32_16x16x32_bf16 v[24:27], a[56:59], v[48:51], v[24:27]\n"
"ds_read_b128 v[40:43], v4 offset:36864\n"
"v_mfma_f32_16x16x32_bf16 v[24:27], a[168:171], v[52:55], v[24:27]\n"
"buffer_load_dwordx4 v2, s[32:35], s63 offen offset:3072 sc1 lds\n"
"v_mfma_f32_16x16x32_bf16 v[28:31], a[172:175], v[52:55], v[28:31]\n"
"ds_read_b128 v[44:47], v4 offset:51200\n"
"v_mfma_f32_16x16x32_bf16 v[28:31], a[60:63], v[48:51], v[28:31]\n"
"s_waitcnt lgkmcnt(4)\n"
"v_mfma_f32_16x16x32_bf16 v[28:31], v[92:95], v[56:59], v[28:31]\n"
"ds_read_b128 v[48:51], v4 offset:9216\n"
"v_mfma_f32_16x16x32_bf16 v[28:31], v[204:207], v[60:63], v[28:31]\n"
"s_add_u32 s63, s63, 4096\n"
"s_add_u32 m0, s64, 8192\n"
"v_mfma_f32_16x16x32_bf16 v[24:27], v[200:203], v[60:63], v[24:27]\n"
"ds_read_b128 v[52:55], v4 offset:23552\n"
"v_mfma_f32_16x16x32_bf16 v[24:27], v[88:91], v[56:59], v[24:27]\n"
"s_waitcnt lgkmcnt(4)\n"
"v_mfma_f32_16x16x32_bf16 v[24:27], a[64:67], v[32:35], v[24:27]\n"
"ds_read_b128 v[56:59], v4 offset:37888\n"
"v_mfma_f32_16x16x32_bf16 v[24:27], a[176:179], v[36:39], v[24:27]\n"
"buffer_load_dwordx4 v2, s[32:35], s63 offen offset:0 sc1 lds\n"
"v_mfma_f32_16x16x32_bf16 v[28:31], a[180:183], v[36:39], v[28:31]\n"
"ds_read_b128 v[60:63], v4 offset:52224\n"
"v_mfma_f32_16x16x32_bf16 v[28:31], a[68:71], v[32:35], v[28:31]\n"
"s_waitcnt lgkmcnt(4)\n"
"v_mfma_f32_16x16x32_bf16 v[28:31], v[100:103], v[40:43], v[28:31]\n"
"ds_read_b128 v[32:35], v4 offset:10240\n"
"v_mfma_f32_16x16x32_bf16 v[28:31], v[212:215], v[44:47], v[28:31]\n"
"buffer_load_dwordx4 v2, s[32:35], s63 offen offset:1024 sc1 lds\n"
"v_mfma_f32_16x16x32_bf16 v[24:27], v[208:211], v[44:47], v[24:27]\n"
"ds_read_b128 v[36:39], v4 offset:24576\n"
"v_mfma_f32_16x16x32_bf16 v[24:27], v[96:99], v[40:43], v[24:27]\n"
"s_waitcnt lgkmcnt(4)\n"
"v_mfma_f32_16x16x32_bf16 v[24:27], a[72:75], v[48:51], v[24:27]\n"
"ds_read_b128 v[40:43], v4 offset:38912\n"
"v_mfma_f32_16x16x32_bf16 v[24:27], a[184:187], v[52:55], v[24:27]\n"
"buffer_load_dwordx4 v2, s[32:35], s63 offen offset:2048 sc1 lds\n"
"v_mfma_f32_16x16x32_bf16 v[28:31], a[188:191], v[52:55], v[28:31]\n"
"ds_read_b128 v[44:47], v4 offset:53248\n"
"v_mfma_f32_16x16x32_bf16 v[28:31], a[76:79], v[48:51], v[28:31]\n"
"s_waitcnt lgkmcnt(4)\n"
"v_mfma_f32_16x16x32_bf16 v[28:31], v[108:111], v[56:59], v[28:31]\n"
"ds_read_b128 v[48:51], v4 offset:11264\n"
"v_mfma_f32_16x16x32_bf16 v[28:31], v[220:223], v[60:63], v[28:31]\n"
"v_mfma_f32_16x16x32_bf16 v[24:27], v[216:219], v[60:63], v[24:27]\n"
"ds_read_b128 v[52:55], v4 offset:25600\n"
"v_mfma_f32_16x16x32_bf16 v[24:27], v[104:107], v[56:59], v[24:27]\n"
"s_waitcnt lgkmcnt(4)\n"
"v_mfma_f32_16x16x32_bf16 v[24:27], a[80:83], v[32:35], v[24:27]\n"
"ds_read_b128 v[56:59], v4 offset:39936\n"
"v_mfma_f32_16x16x32_bf16 v[24:27], a[192:195], v[36:39], v[24:27]\n"
"buffer_load_dwordx4 v2, s[32:35], s63 offen offset:3072 sc1 lds\n"
"s_add_u32 s63, s63, 4096\n"
"v_mfma_f32_16x16x32_bf16 v[28:31], a[196:199], v[36:39], v[28:31]\n"
"ds_read_b128 v[60:63], v4 offset:54272\n"
"s_add_u32 m0, s64, 12288\n"
"v_mfma_f32_16x16x32_bf16 v[28:31], a[84:87], v[32:35], v[28:31]\n"
"s_waitcnt lgkmcnt(4)\n"
"v_mfma_f32_16x16x32_bf16 v[28:31], v[116:119], v[40:43], v[28:31]\n"
"ds_read_b128 v[32:35], v4 offset:12288\n"
"v_mfma_f32_16x16x32_bf16 v[28:31], v[228:231], v[44:47], v[28:31]\n"
"buffer_load_dwordx4 v2, s[32:35], s63 offen offset:0 sc1 lds\n"
"v_mfma_f32_16x16x32_bf16 v[24:27], v[224:227], v[44:47], v[24:27]\n"
"ds_read_b128 v[36:39], v4 offset:26624\n"
"v_mfma_f32_16x16x32_bf16 v[24:27], v[112:115], v[40:43], v[24:27]\n"
"s_waitcnt lgkmcnt(4)\n"
"v_mfma_f32_16x16x32_bf16 v[24:27], a[88:91], v[48:51], v[24:27]\n"
"ds_read_b128 v[40:43], v4 offset:40960\n"
"v_mfma_f32_16x16x32_bf16 v[24:27], a[200:203], v[52:55], v[24:27]\n"
"buffer_load_dwordx4 v2, s[32:35], s63 offen offset:1024 sc1 lds\n"
"s_add_u32 s63, s63, 1175552\n"
"v_mfma_f32_16x16x32_bf16 v[28:31], a[204:207], v[52:55], v[28:31]\n"
"ds_read_b128 v[44:47], v4 offset:55296\n"
"v_mfma_f32_16x16x32_bf16 v[28:31], a[92:95], v[48:51], v[28:31]\n"
"s_waitcnt lgkmcnt(4)\n"
"v_mfma_f32_16x16x32_bf16 v[28:31], v[124:127], v[56:59], v[28:31]\n"
"ds_read_b128 v[48:51], v4 offset:13312\n"
"v_mfma_f32_16x16x32_bf16 v[28:31], v[236:239], v[60:63], v[28:31]\n"
"v_mfma_f32_16x16x32_bf16 v[24:27], v[232:235], v[60:63], v[24:27]\n"
"ds_read_b128 v[52:55], v4 offset:27648\n"
"v_mfma_f32_16x16x32_bf16 v[24:27], v[120:123], v[56:59], v[24:27]\n"
"s_waitcnt lgkmcnt(4)\n"
"v_mfma_f32_16x16x32_bf16 v[24:27], a[96:99], v[32:35], v[24:27]\n"
"ds_read_b128 v[56:59], v4 offset:41984\n"
"v_mfma_f32_16x16x32_bf16 v[24:27], a[208:211], v[36:39], v[24:27]\n"
"v_mfma_f32_16x16x32_bf16 v[28:31], a[212:215], v[36:39], v[28:31]\n"
"ds_read_b128 v[60:63], v4 offset:56320\n"
"v_mfma_f32_16x16x32_bf16 v[28:31], a[100:103], v[32:35], v[28:31]\n"
"s_waitcnt lgkmcnt(4)\n"
"s_waitcnt vmcnt(10)\n"
"s_barrier\n"
"v_mfma_f32_16x16x32_bf16 v[28:31], v[132:135], v[40:43], v[28:31]\n"
"ds_read_b128 v[32:35], v2 offset:0\n"
"v_mfma_f32_16x16x32_bf16 v[28:31], v[244:247], v[44:47], v[28:31]\n"
"v_mfma_f32_16x16x32_bf16 v[24:27], v[240:243], v[44:47], v[24:27]\n"
"ds_read_b128 v[36:39], v2 offset:14336\n"
"v_mfma_f32_16x16x32_bf16 v[24:27], v[128:131], v[40:43], v[24:27]\n"
"s_waitcnt lgkmcnt(4)\n"
"v_mfma_f32_16x16x32_bf16 v[24:27], a[104:107], v[48:51], v[24:27]\n"
"ds_read_b128 v[40:43], v2 offset:28672\n"
"v_mfma_f32_16x16x32_bf16 v[24:27], a[216:219], v[52:55], v[24:27]\n"
"v_mfma_f32_16x16x32_bf16 v[28:31], a[220:223], v[52:55], v[28:31]\n"
"ds_read_b128 v[44:47], v2 offset:43008\n"
"v_mfma_f32_16x16x32_bf16 v[28:31], a[108:111], v[48:51], v[28:31]\n"
"s_waitcnt lgkmcnt(4)\n"
"v_mfma_f32_16x16x32_bf16 v[28:31], v[140:143], v[56:59], v[28:31]\n"
"ds_read_b128 v[48:51], v2 offset:1024\n"
"v_mfma_f32_16x16x32_bf16 v[28:31], v[252:255], v[60:63], v[28:31]\n"
"v_mfma_f32_16x16x32_bf16 v[24:27], v[248:251], v[60:63], v[24:27]\n"
"ds_read_b128 v[52:55], v2 offset:15360\n"
"s_sub_u32 s62, s62, 1\n"
"v_mfma_f32_16x16x32_bf16 v[24:27], v[136:139], v[56:59], v[24:27]\n"
"s_cbranch_scc1 .L_dbt_0\n"
"s_waitcnt lgkmcnt(4)\n"
"v_mfma_f32_16x16x32_bf16 v[16:19], a[0:3], v[32:35], 0\n"
"ds_read_b128 v[56:59], v2 offset:29696\n"
"v_mfma_f32_16x16x32_bf16 v[16:19], a[112:115], v[36:39], v[16:19]\n"
"v_mfma_f32_16x16x32_bf16 v[20:23], a[116:119], v[36:39], 0\n"
"ds_read_b128 v[60:63], v2 offset:44032\n"
"v_mfma_f32_16x16x32_bf16 v[20:23], a[4:7], v[32:35], v[20:23]\n"
"v_cvt_pk_bf16_f32 v15, v30, v31\n"
"s_waitcnt lgkmcnt(4)\n"
"v_mfma_f32_16x16x32_bf16 v[20:23], a[228:231], v[40:43], v[20:23]\n"
"ds_read_b128 v[32:35], v2 offset:2048\n"
"v_cvt_pk_bf16_f32 v14, v28, v29\n"
"v_mfma_f32_16x16x32_bf16 v[20:23], v[148:151], v[44:47], v[20:23]\n"
"v_cvt_pk_bf16_f32 v13, v26, v27\n"
"v_cvt_pk_bf16_f32 v12, v24, v25\n"
"v_mfma_f32_16x16x32_bf16 v[16:19], v[144:147], v[44:47], v[16:19]\n"
"ds_read_b128 v[36:39], v2 offset:16384\n"
"ds_write_b128 v5, v[12:15]\n"
"v_mfma_f32_16x16x32_bf16 v[16:19], a[224:227], v[40:43], v[16:19]\n"
"ds_read2st64_b32 v[12:13], v3 offset0:0 offset1:1\n"
"s_waitcnt lgkmcnt(6)\n"
"v_mfma_f32_16x16x32_bf16 v[16:19], a[8:11], v[48:51], v[16:19]\n"
"ds_read_b128 v[40:43], v2 offset:30720\n"
"ds_read2st64_b32 v[14:15], v3 offset0:2 offset1:3\n"
"v_mfma_f32_16x16x32_bf16 v[16:19], a[120:123], v[52:55], v[16:19]\n"
"v_mfma_f32_16x16x32_bf16 v[20:23], a[124:127], v[52:55], v[20:23]\n"
"ds_read_b128 v[44:47], v2 offset:45056\n"
"v_mfma_f32_16x16x32_bf16 v[20:23], a[12:15], v[48:51], v[20:23]\n"
"s_waitcnt lgkmcnt(7)\n"
"v_mfma_f32_16x16x32_bf16 v[20:23], a[236:239], v[56:59], v[20:23]\n"
"ds_read_b128 v[48:51], v2 offset:3072\n"
"v_mfma_f32_16x16x32_bf16 v[20:23], v[156:159], v[60:63], v[20:23]\n"
"s_waitcnt lgkmcnt(2)\n"
"buffer_atomic_pk_add_bf16 v12, v1, s[44:47], s65 offen offset:0 sc1\n"
"v_mfma_f32_16x16x32_bf16 v[16:19], v[152:155], v[60:63], v[16:19]\n"
"ds_read_b128 v[52:55], v2 offset:17408\n"
"v_mfma_f32_16x16x32_bf16 v[16:19], a[232:235], v[56:59], v[16:19]\n"
"buffer_atomic_pk_add_bf16 v13, v1, s[44:47], s65 offen offset:2048 sc1\n"
"v_mfma_f32_16x16x32_bf16 v[16:19], a[16:19], v[32:35], v[16:19]\n"
"ds_read_b128 v[56:59], v2 offset:31744\n"
"s_add_u32 s65, s65, 16384\n"
"v_mfma_f32_16x16x32_bf16 v[16:19], a[128:131], v[36:39], v[16:19]\n"
"buffer_atomic_pk_add_bf16 v14, v1, s[44:47], s65 offen offset:0 sc1\n"
"v_mfma_f32_16x16x32_bf16 v[20:23], a[132:135], v[36:39], v[20:23]\n"
"ds_read_b128 v[60:63], v2 offset:46080\n"
"v_mfma_f32_16x16x32_bf16 v[20:23], a[20:23], v[32:35], v[20:23]\n"
"s_waitcnt lgkmcnt(4)\n"
"s_waitcnt vmcnt(3)\n"
"s_barrier\n"
"v_mfma_f32_16x16x32_bf16 v[20:23], a[244:247], v[40:43], v[20:23]\n"
"ds_read_b128 v[32:35], v2 offset:4096\n"
"s_add_u32 m0, s64, 57344\n"
"v_mfma_f32_16x16x32_bf16 v[20:23], v[164:167], v[44:47], v[20:23]\n"
"buffer_atomic_pk_add_bf16 v15, v1, s[44:47], s65 offen offset:2048 sc1\n"
"s_add_u32 s65, s65, 16384\n"
"v_mfma_f32_16x16x32_bf16 v[16:19], v[160:163], v[44:47], v[16:19]\n"
"ds_read_b128 v[36:39], v2 offset:18432\n"
"v_mfma_f32_16x16x32_bf16 v[16:19], a[240:243], v[40:43], v[16:19]\n"
"s_waitcnt lgkmcnt(4)\n"
"v_mfma_f32_16x16x32_bf16 v[16:19], a[24:27], v[48:51], v[16:19]\n"
"ds_read_b128 v[40:43], v2 offset:32768\n"
"v_mfma_f32_16x16x32_bf16 v[16:19], a[136:139], v[52:55], v[16:19]\n"
"buffer_load_dwordx4 v2, s[32:35], s63 offen offset:0 sc1 lds\n"
"v_mfma_f32_16x16x32_bf16 v[20:23], a[140:143], v[52:55], v[20:23]\n"
"ds_read_b128 v[44:47], v2 offset:47104\n"
"v_mfma_f32_16x16x32_bf16 v[20:23], a[28:31], v[48:51], v[20:23]\n"
"s_waitcnt lgkmcnt(4)\n"
"v_mfma_f32_16x16x32_bf16 v[20:23], a[252:255], v[56:59], v[20:23]\n"
"ds_read_b128 v[48:51], v2 offset:5120\n"
"v_mfma_f32_16x16x32_bf16 v[20:23], v[172:175], v[60:63], v[20:23]\n"
"buffer_load_dwordx4 v2, s[32:35], s63 offen offset:1024 sc1 lds\n"
"v_mfma_f32_16x16x32_bf16 v[16:19], v[168:171], v[60:63], v[16:19]\n"
"ds_read_b128 v[52:55], v2 offset:19456\n"
"v_mfma_f32_16x16x32_bf16 v[16:19], a[248:251], v[56:59], v[16:19]\n"
"s_waitcnt lgkmcnt(4)\n"
"v_mfma_f32_16x16x32_bf16 v[16:19], a[32:35], v[32:35], v[16:19]\n"
"ds_read_b128 v[56:59], v2 offset:33792\n"
"v_mfma_f32_16x16x32_bf16 v[16:19], a[144:147], v[36:39], v[16:19]\n"
"buffer_load_dwordx4 v2, s[32:35], s63 offen offset:2048 sc1 lds\n"
"v_mfma_f32_16x16x32_bf16 v[20:23], a[148:151], v[36:39], v[20:23]\n"
"ds_read_b128 v[60:63], v2 offset:48128\n"
"v_mfma_f32_16x16x32_bf16 v[20:23], a[36:39], v[32:35], v[20:23]\n"
"s_waitcnt lgkmcnt(4)\n"
"v_mfma_f32_16x16x32_bf16 v[20:23], v[68:71], v[40:43], v[20:23]\n"
"ds_read_b128 v[32:35], v2 offset:6144\n"
"v_mfma_f32_16x16x32_bf16 v[20:23], v[180:183], v[44:47], v[20:23]\n"
"buffer_load_dwordx4 v2, s[32:35], s63 offen offset:3072 sc1 lds\n"
"v_mfma_f32_16x16x32_bf16 v[16:19], v[176:179], v[44:47], v[16:19]\n"
"ds_read_b128 v[36:39], v2 offset:20480\n"
"v_mfma_f32_16x16x32_bf16 v[16:19], v[64:67], v[40:43], v[16:19]\n"
"s_waitcnt lgkmcnt(4)\n"
"v_mfma_f32_16x16x32_bf16 v[16:19], a[40:43], v[48:51], v[16:19]\n"
"ds_read_b128 v[40:43], v2 offset:34816\n"
"s_add_u32 s63, s63, 4096\n"
"v_mfma_f32_16x16x32_bf16 v[16:19], a[152:155], v[52:55], v[16:19]\n"
"s_add_u32 m0, s64, 61440\n"
"v_mfma_f32_16x16x32_bf16 v[20:23], a[156:159], v[52:55], v[20:23]\n"
"ds_read_b128 v[44:47], v2 offset:49152\n"
"v_mfma_f32_16x16x32_bf16 v[20:23], a[44:47], v[48:51], v[20:23]\n"
"s_waitcnt lgkmcnt(4)\n"
"v_mfma_f32_16x16x32_bf16 v[20:23], v[76:79], v[56:59], v[20:23]\n"
"ds_read_b128 v[48:51], v2 offset:7168\n"
"v_mfma_f32_16x16x32_bf16 v[20:23], v[188:191], v[60:63], v[20:23]\n"
"buffer_load_dwordx4 v2, s[32:35], s63 offen offset:0 sc1 lds\n"
"v_mfma_f32_16x16x32_bf16 v[16:19], v[184:187], v[60:63], v[16:19]\n"
"ds_read_b128 v[52:55], v2 offset:21504\n"
"v_mfma_f32_16x16x32_bf16 v[16:19], v[72:75], v[56:59], v[16:19]\n"
"s_waitcnt lgkmcnt(4)\n"
"v_mfma_f32_16x16x32_bf16 v[16:19], a[48:51], v[32:35], v[16:19]\n"
"ds_read_b128 v[56:59], v2 offset:35840\n"
"v_mfma_f32_16x16x32_bf16 v[16:19], a[160:163], v[36:39], v[16:19]\n"
"buffer_load_dwordx4 v2, s[32:35], s63 offen offset:1024 sc1 lds\n"
"v_mfma_f32_16x16x32_bf16 v[20:23], a[164:167], v[36:39], v[20:23]\n"
"ds_read_b128 v[60:63], v2 offset:50176\n"
"v_mfma_f32_16x16x32_bf16 v[20:23], a[52:55], v[32:35], v[20:23]\n"
"s_waitcnt lgkmcnt(4)\n"
"v_mfma_f32_16x16x32_bf16 v[20:23], v[84:87], v[40:43], v[20:23]\n"
"ds_read_b128 v[32:35], v2 offset:8192\n"
"v_mfma_f32_16x16x32_bf16 v[20:23], v[196:199], v[44:47], v[20:23]\n"
"buffer_load_dwordx4 v2, s[32:35], s63 offen offset:2048 sc1 lds\n"
"v_mfma_f32_16x16x32_bf16 v[16:19], v[192:195], v[44:47], v[16:19]\n"
"ds_read_b128 v[36:39], v2 offset:22528\n"
"v_mfma_f32_16x16x32_bf16 v[16:19], v[80:83], v[40:43], v[16:19]\n"
"s_waitcnt lgkmcnt(4)\n"
"v_mfma_f32_16x16x32_bf16 v[16:19], a[56:59], v[48:51], v[16:19]\n"
"ds_read_b128 v[40:43], v2 offset:36864\n"
"v_mfma_f32_16x16x32_bf16 v[16:19], a[168:171], v[52:55], v[16:19]\n"
"buffer_load_dwordx4 v2, s[32:35], s63 offen offset:3072 sc1 lds\n"
"v_mfma_f32_16x16x32_bf16 v[20:23], a[172:175], v[52:55], v[20:23]\n"
"ds_read_b128 v[44:47], v2 offset:51200\n"
"v_mfma_f32_16x16x32_bf16 v[20:23], a[60:63], v[48:51], v[20:23]\n"
"s_waitcnt lgkmcnt(4)\n"
"v_mfma_f32_16x16x32_bf16 v[20:23], v[92:95], v[56:59], v[20:23]\n"
"ds_read_b128 v[48:51], v2 offset:9216\n"
"v_mfma_f32_16x16x32_bf16 v[20:23], v[204:207], v[60:63], v[20:23]\n"
"s_add_u32 s63, s63, 4096\n"
"s_add_u32 m0, s64, 65536\n"
"v_mfma_f32_16x16x32_bf16 v[16:19], v[200:203], v[60:63], v[16:19]\n"
"ds_read_b128 v[52:55], v2 offset:23552\n"
"v_mfma_f32_16x16x32_bf16 v[16:19], v[88:91], v[56:59], v[16:19]\n"
"s_waitcnt lgkmcnt(4)\n"
"v_mfma_f32_16x16x32_bf16 v[16:19], a[64:67], v[32:35], v[16:19]\n"
"ds_read_b128 v[56:59], v2 offset:37888\n"
"v_mfma_f32_16x16x32_bf16 v[16:19], a[176:179], v[36:39], v[16:19]\n"
"buffer_load_dwordx4 v2, s[32:35], s63 offen offset:0 sc1 lds\n"
"v_mfma_f32_16x16x32_bf16 v[20:23], a[180:183], v[36:39], v[20:23]\n"
"ds_read_b128 v[60:63], v2 offset:52224\n"
"v_mfma_f32_16x16x32_bf16 v[20:23], a[68:71], v[32:35], v[20:23]\n"
"s_waitcnt lgkmcnt(4)\n"
"v_mfma_f32_16x16x32_bf16 v[20:23], v[100:103], v[40:43], v[20:23]\n"
"ds_read_b128 v[32:35], v2 offset:10240\n"
"v_mfma_f32_16x16x32_bf16 v[20:23], v[212:215], v[44:47], v[20:23]\n"
"buffer_load_dwordx4 v2, s[32:35], s63 offen offset:1024 sc1 lds\n"
"v_mfma_f32_16x16x32_bf16 v[16:19], v[208:211], v[44:47], v[16:19]\n"
"ds_read_b128 v[36:39], v2 offset:24576\n"
"v_mfma_f32_16x16x32_bf16 v[16:19], v[96:99], v[40:43], v[16:19]\n"
"s_waitcnt lgkmcnt(4)\n"
"v_mfma_f32_16x16x32_bf16 v[16:19], a[72:75], v[48:51], v[16:19]\n"
"ds_read_b128 v[40:43], v2 offset:38912\n"
"v_mfma_f32_16x16x32_bf16 v[16:19], a[184:187], v[52:55], v[16:19]\n"
"buffer_load_dwordx4 v2, s[32:35], s63 offen offset:2048 sc1 lds\n"
"v_mfma_f32_16x16x32_bf16 v[20:23], a[188:191], v[52:55], v[20:23]\n"
"ds_read_b128 v[44:47], v2 offset:53248\n"
"v_mfma_f32_16x16x32_bf16 v[20:23], a[76:79], v[48:51], v[20:23]\n"
"s_waitcnt lgkmcnt(4)\n"
"v_mfma_f32_16x16x32_bf16 v[20:23], v[108:111], v[56:59], v[20:23]\n"
"ds_read_b128 v[48:51], v2 offset:11264\n"
"v_mfma_f32_16x16x32_bf16 v[20:23], v[220:223], v[60:63], v[20:23]\n"
"v_mfma_f32_16x16x32_bf16 v[16:19], v[216:219], v[60:63], v[16:19]\n"
"ds_read_b128 v[52:55], v2 offset:25600\n"
"v_mfma_f32_16x16x32_bf16 v[16:19], v[104:107], v[56:59], v[16:19]\n"
"s_waitcnt lgkmcnt(4)\n"
"v_mfma_f32_16x16x32_bf16 v[16:19], a[80:83], v[32:35], v[16:19]\n"
"ds_read_b128 v[56:59], v2 offset:39936\n"
"v_mfma_f32_16x16x32_bf16 v[16:19], a[192:195], v[36:39], v[16:19]\n"
"buffer_load_dwordx4 v2, s[32:35], s63 offen offset:3072 sc1 lds\n"
"s_add_u32 s63, s63, 4096\n"
"v_mfma_f32_16x16x32_bf16 v[20:23], a[196:199], v[36:39], v[20:23]\n"
"ds_read_b128 v[60:63], v2 offset:54272\n"
"s_add_u32 m0, s64, 69632\n"
"v_mfma_f32_16x16x32_bf16 v[20:23], a[84:87], v[32:35], v[20:23]\n"
"s_waitcnt lgkmcnt(4)\n"
"v_mfma_f32_16x16x32_bf16 v[20:23], v[116:119], v[40:43], v[20:23]\n"
"ds_read_b128 v[32:35], v2 offset:12288\n"
"v_mfma_f32_16x16x32_bf16 v[20:23], v[228:231], v[44:47], v[20:23]\n"
"buffer_load_dwordx4 v2, s[32:35], s63 offen offset:0 sc1 lds\n"
"v_mfma_f32_16x16x32_bf16 v[16:19], v[224:227], v[44:47], v[16:19]\n"
"ds_read_b128 v[36:39], v2 offset:26624\n"
"v_mfma_f32_16x16x32_bf16 v[16:19], v[112:115], v[40:43], v[16:19]\n"
"s_waitcnt lgkmcnt(4)\n"
"v_mfma_f32_16x16x32_bf16 v[16:19], a[88:91], v[48:51], v[16:19]\n"
"ds_read_b128 v[40:43], v2 offset:40960\n"
"v_mfma_f32_16x16x32_bf16 v[16:19], a[200:203], v[52:55], v[16:19]\n"
"buffer_load_dwordx4 v2, s[32:35], s63 offen offset:1024 sc1 lds\n"
"s_add_u32 s63, s63, 1175552\n"
"v_mfma_f32_16x16x32_bf16 v[20:23], a[204:207], v[52:55], v[20:23]\n"
"ds_read_b128 v[44:47], v2 offset:55296\n"
"v_mfma_f32_16x16x32_bf16 v[20:23], a[92:95], v[48:51], v[20:23]\n"
"s_waitcnt lgkmcnt(4)\n"
"v_mfma_f32_16x16x32_bf16 v[20:23], v[124:127], v[56:59], v[20:23]\n"
"ds_read_b128 v[48:51], v2 offset:13312\n"
"v_mfma_f32_16x16x32_bf16 v[20:23], v[236:239], v[60:63], v[20:23]\n"
"v_mfma_f32_16x16x32_bf16 v[16:19], v[232:235], v[60:63], v[16:19]\n"
"ds_read_b128 v[52:55], v2 offset:27648\n"
"v_mfma_f32_16x16x32_bf16 v[16:19], v[120:123], v[56:59], v[16:19]\n"
"s_waitcnt lgkmcnt(4)\n"
"v_mfma_f32_16x16x32_bf16 v[16:19], a[96:99], v[32:35], v[16:19]\n"
"ds_read_b128 v[56:59], v2 offset:41984\n"
"v_mfma_f32_16x16x32_bf16 v[16:19], a[208:211], v[36:39], v[16:19]\n"
"v_mfma_f32_16x16x32_bf16 v[20:23], a[212:215], v[36:39], v[20:23]\n"
"ds_read_b128 v[60:63], v2 offset:56320\n"
"v_mfma_f32_16x16x32_bf16 v[20:23], a[100:103], v[32:35], v[20:23]\n"
"s_waitcnt lgkmcnt(4)\n"
"s_waitcnt vmcnt(10)\n"
"s_barrier\n"
"v_mfma_f32_16x16x32_bf16 v[20:23], v[132:135], v[40:43], v[20:23]\n"
"ds_read_b128 v[32:35], v4 offset:0\n"
"v_mfma_f32_16x16x32_bf16 v[20:23], v[244:247], v[44:47], v[20:23]\n"
"v_mfma_f32_16x16x32_bf16 v[16:19], v[240:243], v[44:47], v[16:19]\n"
"ds_read_b128 v[36:39], v4 offset:14336\n"
"v_mfma_f32_16x16x32_bf16 v[16:19], v[128:131], v[40:43], v[16:19]\n"
"s_waitcnt lgkmcnt(4)\n"
"v_mfma_f32_16x16x32_bf16 v[16:19], a[104:107], v[48:51], v[16:19]\n"
"ds_read_b128 v[40:43], v4 offset:28672\n"
"v_mfma_f32_16x16x32_bf16 v[16:19], a[216:219], v[52:55], v[16:19]\n"
"v_mfma_f32_16x16x32_bf16 v[20:23], a[220:223], v[52:55], v[20:23]\n"
"ds_read_b128 v[44:47], v4 offset:43008\n"
"v_mfma_f32_16x16x32_bf16 v[20:23], a[108:111], v[48:51], v[20:23]\n"
"s_waitcnt lgkmcnt(4)\n"
"v_mfma_f32_16x16x32_bf16 v[20:23], v[140:143], v[56:59], v[20:23]\n"
"ds_read_b128 v[48:51], v4 offset:1024\n"
"v_mfma_f32_16x16x32_bf16 v[20:23], v[252:255], v[60:63], v[20:23]\n"
"v_mfma_f32_16x16x32_bf16 v[16:19], v[248:251], v[60:63], v[16:19]\n"
"ds_read_b128 v[52:55], v4 offset:15360\n"
"v_mfma_f32_16x16x32_bf16 v[16:19], v[136:139], v[56:59], v[16:19]\n"
"s_branch .L_dbl_0\n"
".L_dbt_0:\n"
"s_nop 1\n"
"s_nop 0\n"
"v_cvt_pk_bf16_f32 v15, v30, v31\n"
"v_cvt_pk_bf16_f32 v14, v28, v29\n"
"s_nop 1\n"
"v_cvt_pk_bf16_f32 v13, v26, v27\n"
"v_cvt_pk_bf16_f32 v12, v24, v25\n"
"ds_write_b128 v5, v[12:15]\n"
"ds_read2st64_b32 v[12:13], v3 offset0:0 offset1:1\n"
"ds_read2st64_b32 v[14:15], v3 offset0:2 offset1:3\n"
"s_waitcnt lgkmcnt(0)\n"
"buffer_atomic_pk_add_bf16 v12, v1, s[44:47], s65 offen offset:0 sc1\n"
"buffer_atomic_pk_add_bf16 v13, v1, s[44:47], s65 offen offset:2048 sc1\n"
"s_add_u32 s65, s65, 16384\n"
"buffer_atomic_pk_add_bf16 v14, v1, s[44:47], s65 offen offset:0 sc1\n"
"buffer_atomic_pk_add_bf16 v15, v1, s[44:47], s65 offen offset:2048 sc1\n"
"s_add_u32 s65, s65, 16384\n"
"s_or_b32 s48, s28, s27\n"
"s_or_b32 s48, s48, s26\n"
"s_cbranch_scc1 .L_end\n"
"v_mov_b32 v8, s54\n"
"v_mov_b32 v9, s55\n"
"buffer_store_dwordx2 v[8:9], off, s[32:35], 0 sc1\n"
"s_branch .L_end\n"
".p2align 6\n"
".L_dhv1:\n"
".L_dal_1:\n"
"s_waitcnt lgkmcnt(4)\n"
"v_mfma_f32_16x16x32_bf16 v[16:19], a[0:3], v[32:35], 0\n"
"ds_read_b128 v[56:59], v2 offset:29696\n"
"ds_read_b128 v[12:15], v5 offset:8192\n"
"v_mfma_f32_16x16x32_bf16 v[16:19], a[112:115], v[36:39], v[16:19]\n"
"v_mfma_f32_16x16x32_bf16 v[20:23], a[116:119], v[36:39], 0\n"
"ds_read_b128 v[60:63], v2 offset:44032\n"
"v_mfma_f32_16x16x32_bf16 v[20:23], a[4:7], v[32:35], v[20:23]\n"
"s_waitcnt lgkmcnt(5)\n"
"v_mfma_f32_16x16x32_bf16 v[20:23], a[228:231], v[40:43], v[20:23]\n"
"ds_read_b128 v[32:35], v2 offset:2048\n"
"v_mfma_f32_16x16x32_bf16 v[20:23], v[148:151], v[44:47], v[20:23]\n"
"s_waitcnt lgkmcnt(2)\n"
"v_add_f32 v24, v24, v12\n"
"v_mfma_f32_16x16x32_bf16 v[16:19], v[144:147], v[44:47], v[16:19]\n"
"ds_read_b128 v[36:39], v2 offset:16384\n"
"v_add_f32 v25, v25, v13\n"
"v_mfma_f32_16x16x32_bf16 v[16:19], a[224:227], v[40:43], v[16:19]\n"
"v_add_f32 v26, v26, v14\n"
"v_mfma_f32_16x16x32_bf16 v[16:19], a[8:11], v[48:51], v[16:19]\n"
"ds_read_b128 v[40:43], v2 offset:30720\n"
"v_add_f32 v27, v27, v15\n"
"v_mfma_f32_16x16x32_bf16 v[16:19], a[120:123], v[52:55], v[16:19]\n"
"ds_read_b128 v[12:15], v5 offset:9216\n"
"v_mfma_f32_16x16x32_bf16 v[20:23], a[124:127], v[52:55], v[20:23]\n"
"ds_read_b128 v[44:47], v2 offset:45056\n"
"v_mfma_f32_16x16x32_bf16 v[20:23], a[12:15], v[48:51], v[20:23]\n"
"s_waitcnt lgkmcnt(5)\n"
"v_mfma_f32_16x16x32_bf16 v[20:23], a[236:239], v[56:59], v[20:23]\n"
"ds_read_b128 v[48:51], v2 offset:3072\n"
"v_mfma_f32_16x16x32_bf16 v[20:23], v[156:159], v[60:63], v[20:23]\n"
"v_mfma_f32_16x16x32_bf16 v[16:19], v[152:155], v[60:63], v[16:19]\n"
"ds_read_b128 v[52:55], v2 offset:17408\n"
"s_waitcnt vmcnt(5)\n"
"v_mfma_f32_16x16x32_bf16 v[16:19], a[232:235], v[56:59], v[16:19]\n"
"v_cmp_ne_u64 vcc, v[8:9], v[6:7]\n"
"s_waitcnt lgkmcnt(5)\n"
"v_mfma_f32_16x16x32_bf16 v[16:19], a[16:19], v[32:35], v[16:19]\n"
"ds_read_b128 v[56:59], v2 offset:31744\n"
"s_cbranch_vccnz .L_rta0_1\n"
".L_bka0_1:\n"
"v_mfma_f32_16x16x32_bf16 v[16:19], a[128:131], v[36:39], v[16:19]\n"
"s_add_u32 s66, s66, 1187840\n"
"s_waitcnt lgkmcnt(4)\n"
"v_mfma_f32_16x16x32_bf16 v[20:23], a[132:135], v[36:39], v[20:23]\n"
"s_waitcnt vmcnt(0)\n"
"s_barrier\n"
"v_mfma_f32_16x16x32_bf16 v[20:23], a[20:23], v[32:35], v[20:23]\n"
"ds_read_b128 v[60:63], v2 offset:46080\n"
"s_waitcnt lgkmcnt(4)\n"
"v_mfma_f32_16x16x32_bf16 v[20:23], a[244:247], v[40:43], v[20:23]\n"
"ds_read_b128 v[32:35], v2 offset:4096\n"
"s_add_u32 m0, s64, 57344\n"
"v_mfma_f32_16x16x32_bf16 v[20:23], v[164:167], v[44:47], v[20:23]\n"
"buffer_load_dwordx4 v2, s[32:35], s63 offen offset:0 sc1 lds\n"
"v_add_f32 v28, v28, v12\n"
"v_mfma_f32_16x16x32_bf16 v[16:19], v[160:163], v[44:47], v[16:19]\n"
"ds_read_b128 v[36:39], v2 offset:18432\n"
"v_add_f32 v29, v29, v13\n"
"v_mfma_f32_16x16x32_bf16 v[16:19], a[240:243], v[40:43], v[16:19]\n"
"v_add_f32 v30, v30, v14\n"
"s_waitcnt lgkmcnt(4)\n"
"v_mfma_f32_16x16x32_bf16 v[16:19], a[24:27], v[48:51], v[16:19]\n"
"ds_read_b128 v[40:43], v2 offset:32768\n"
"v_add_f32 v31, v31, v15\n"
"v_mfma_f32_16x16x32_bf16 v[16:19], a[136:139], v[52:55], v[16:19]\n"
"v_cvt_pk_bf16_f32 v12, v24, v25\n"
"v_cvt_pk_bf16_f32 v13, v26, v27\n"
"v_mfma_f32_16x16x32_bf16 v[20:23], a[140:143], v[52:55], v[20:23]\n"
"ds_read_b128 v[44:47], v2 offset:47104\n"
"v_cvt_pk_bf16_f32 v14, v28, v29\n"
"v_mfma_f32_16x16x32_bf16 v[20:23], a[28:31], v[48:51], v[20:23]\n"
"v_cvt_pk_bf16_f32 v15, v30, v31\n"
"s_waitcnt lgkmcnt(4)\n"
"v_mfma_f32_16x16x32_bf16 v[20:23], a[252:255], v[56:59], v[20:23]\n"
"ds_read_b128 v[48:51], v2 offset:5120\n"
"v_mfma_f32_16x16x32_bf16 v[20:23], v[172:175], v[60:63], v[20:23]\n"
"buffer_store_dwordx4 v[12:15], v2, s[32:35], s68 offen sc1\n"
"v_add_f32 v10, v24, v25\n"
"v_mfma_f32_16x16x32_bf16 v[16:19], v[168:171], v[60:63], v[16:19]\n"
"ds_read_b128 v[52:55], v2 offset:19456\n"
"v_mul_f32 v11, v24, v24\n"
"v_mfma_f32_16x16x32_bf16 v[16:19], a[248:251], v[56:59], v[16:19]\n"
"v_fmac_f32 v11, v25, v25\n"
"s_waitcnt lgkmcnt(4)\n"
"v_mfma_f32_16x16x32_bf16 v[16:19], a[32:35], v[32:35], v[16:19]\n"
"ds_read_b128 v[56:59], v2 offset:33792\n"
"v_add_f32 v10, v10, v26\n"
"v_mfma_f32_16x16x32_bf16 v[16:19], a[144:147], v[36:39], v[16:19]\n"
"buffer_load_dwordx4 v2, s[32:35], s63 offen offset:1024 sc1 lds\n"
"v_fmac_f32 v11, v26, v26\n"
"v_mfma_f32_16x16x32_bf16 v[20:23], a[148:151], v[36:39], v[20:23]\n"
"ds_read_b128 v[60:63], v2 offset:48128\n"
"v_add_f32 v10, v10, v27\n"
"v_mfma_f32_16x16x32_bf16 v[20:23], a[36:39], v[32:35], v[20:23]\n"
"v_fmac_f32 v11, v27, v27\n"
"s_waitcnt lgkmcnt(4)\n"
"v_mfma_f32_16x16x32_bf16 v[20:23], v[68:71], v[40:43], v[20:23]\n"
"ds_read_b128 v[32:35], v2 offset:6144\n"
"v_add_f32 v10, v10, v28\n"
"v_mfma_f32_16x16x32_bf16 v[20:23], v[180:183], v[44:47], v[20:23]\n"
"buffer_load_dwordx4 v2, s[32:35], s63 offen offset:2048 sc1 lds\n"
"v_fmac_f32 v11, v28, v28\n"
"v_mfma_f32_16x16x32_bf16 v[16:19], v[176:179], v[44:47], v[16:19]\n"
"ds_read_b128 v[36:39], v2 offset:20480\n"
"v_add_f32 v10, v10, v29\n"
"v_mfma_f32_16x16x32_bf16 v[16:19], v[64:67], v[40:43], v[16:19]\n"
"v_fmac_f32 v11, v29, v29\n"
"s_waitcnt lgkmcnt(4)\n"
"v_mfma_f32_16x16x32_bf16 v[16:19], a[40:43], v[48:51], v[16:19]\n"
"ds_read_b128 v[40:43], v2 offset:34816\n"
"v_add_f32 v10, v10, v30\n"
"v_mfma_f32_16x16x32_bf16 v[16:19], a[152:155], v[52:55], v[16:19]\n"
"buffer_load_dwordx4 v2, s[32:35], s63 offen offset:3072 sc1 lds\n"
"s_add_u32 m0, s74, 0\n"
"v_mfma_f32_16x16x32_bf16 v[20:23], a[156:159], v[52:55], v[20:23]\n"
"ds_read_b128 v[44:47], v2 offset:49152\n"
"v_fmac_f32 v11, v30, v30\n"
"v_mfma_f32_16x16x32_bf16 v[20:23], a[44:47], v[48:51], v[20:23]\n"
"v_add_f32 v10, v10, v31\n"
"s_waitcnt lgkmcnt(4)\n"
"v_mfma_f32_16x16x32_bf16 v[20:23], v[76:79], v[56:59], v[20:23]\n"
"ds_read_b128 v[48:51], v2 offset:7168\n"
"v_fmac_f32 v11, v31, v31\n"
"v_mfma_f32_16x16x32_bf16 v[20:23], v[188:191], v[60:63], v[20:23]\n"
"buffer_load_dwordx4 v2, s[32:35], s71 offen offset:0 sc1 lds\n"
"v_permlane16_swap_b32 v10, v11\n"
"v_mfma_f32_16x16x32_bf16 v[16:19], v[184:187], v[60:63], v[16:19]\n"
"ds_read_b128 v[52:55], v2 offset:21504\n"
"v_add_f32 v10, v10, v11\n"
"v_mfma_f32_16x16x32_bf16 v[16:19], v[72:75], v[56:59], v[16:19]\n"
"s_waitcnt lgkmcnt(4)\n"
"v_mfma_f32_16x16x32_bf16 v[16:19], a[48:51], v[32:35], v[16:19]\n"
"ds_read_b128 v[56:59], v2 offset:35840\n"
"v_permlane32_swap_b32 v10, v11\n"
"v_mfma_f32_16x16x32_bf16 v[16:19], a[160:163], v[36:39], v[16:19]\n"
"buffer_load_dwordx4 v2, s[32:35], s71 offen offset:1024 sc1 lds\n"
"s_add_u32 s71, s71, 1187840\n"
"v_mfma_f32_16x16x32_bf16 v[20:23], a[164:167], v[36:39], v[20:23]\n"
"ds_read_b128 v[60:63], v2 offset:50176\n"
"s_add_u32 s63, s63, 4096\n"
"v_mfma_f32_16x16x32_bf16 v[20:23], a[52:55], v[32:35], v[20:23]\n"
"s_add_u32 m0, s64, 61440\n"
"s_waitcnt lgkmcnt(4)\n"
"v_mfma_f32_16x16x32_bf16 v[20:23], v[84:87], v[40:43], v[20:23]\n"
"ds_read_b128 v[32:35], v2 offset:8192\n"
"v_add_f32 v10, v10, v11\n"
"v_mfma_f32_16x16x32_bf16 v[20:23], v[196:199], v[44:47], v[20:23]\n"
"buffer_store_dword v10, v3, s[32:35], s68 offen offset:1024 sc1\n"
"s_mov_b32 s67, s68\n"
"v_mfma_f32_16x16x32_bf16 v[16:19], v[192:195], v[44:47], v[16:19]\n"
"ds_read_b128 v[36:39], v2 offset:22528\n"
"s_mov_b32 s68, s70\n"
"v_mfma_f32_16x16x32_bf16 v[16:19], v[80:83], v[40:43], v[16:19]\n"
"s_add_u32 s70, s70, 1187840\n"
"s_waitcnt lgkmcnt(4)\n"
"v_mfma_f32_16x16x32_bf16 v[16:19], a[56:59], v[48:51], v[16:19]\n"
"ds_read_b128 v[40:43], v2 offset:36864\n"
"v_mfma_f32_16x16x32_bf16 v[16:19], a[168:171], v[52:55], v[16:19]\n"
"buffer_load_dwordx4 v2, s[32:35], s63 offen offset:0 sc1 lds\n"
"v_mfma_f32_16x16x32_bf16 v[20:23], a[172:175], v[52:55], v[20:23]\n"
"ds_read_b128 v[44:47], v2 offset:51200\n"
"v_mfma_f32_16x16x32_bf16 v[20:23], a[60:63], v[48:51], v[20:23]\n"
"s_waitcnt lgkmcnt(4)\n"
"v_mfma_f32_16x16x32_bf16 v[20:23], v[92:95], v[56:59], v[20:23]\n"
"ds_read_b128 v[48:51], v2 offset:9216\n"
"v_mfma_f32_16x16x32_bf16 v[20:23], v[204:207], v[60:63], v[20:23]\n"
"buffer_load_dwordx4 v2, s[32:35], s63 offen offset:1024 sc1 lds\n"
"v_mfma_f32_16x16x32_bf16 v[16:19], v[200:203], v[60:63], v[16:19]\n"
"ds_read_b128 v[52:55], v2 offset:23552\n"
"v_mfma_f32_16x16x32_bf16 v[16:19], v[88:91], v[56:59], v[16:19]\n"
"s_waitcnt lgkmcnt(4)\n"
"v_mfma_f32_16x16x32_bf16 v[16:19], a[64:67], v[32:35], v[16:19]\n"
"ds_read_b128 v[56:59], v2 offset:37888\n"
"v_mfma_f32_16x16x32_bf16 v[16:19], a[176:179], v[36:39], v[16:19]\n"
"buffer_load_dwordx4 v2, s[32:35], s63 offen offset:2048 sc1 lds\n"
"v_mfma_f32_16x16x32_bf16 v[20:23], a[180:183], v[36:39], v[20:23]\n"
"ds_read_b128 v[60:63], v2 offset:52224\n"
"v_mfma_f32_16x16x32_bf16 v[20:23], a[68:71], v[32:35], v[20:23]\n"
"s_waitcnt lgkmcnt(4)\n"
"v_mfma_f32_16x16x32_bf16 v[20:23], v[100:103], v[40:43], v[20:23]\n"
"ds_read_b128 v[32:35], v2 offset:10240\n"
"v_mfma_f32_16x16x32_bf16 v[20:23], v[212:215], v[44:47], v[20:23]\n"
"buffer_load_dwordx4 v2, s[32:35], s63 offen offset:3072 sc1 lds\n"
"s_add_u32 s63, s63, 4096\n"
"v_mfma_f32_16x16x32_bf16 v[16:19], v[208:211], v[44:47], v[16:19]\n"
"ds_read_b128 v[36:39], v2 offset:24576\n"
"s_add_u32 m0, s64, 65536\n"
"v_mfma_f32_16x16x32_bf16 v[16:19], v[96:99], v[40:43], v[16:19]\n"
"s_waitcnt lgkmcnt(4)\n"
"v_mfma_f32_16x16x32_bf16 v[16:19], a[72:75], v[48:51], v[16:19]\n"
"ds_read_b128 v[40:43], v2 offset:38912\n"
"v_mfma_f32_16x16x32_bf16 v[16:19], a[184:187], v[52:55], v[16:19]\n"
"buffer_load_dwordx4 v2, s[32:35], s63 offen offset:0 sc1 lds\n"
"v_mfma_f32_16x16x32_bf16 v[20:23], a[188:191], v[52:55], v[20:23]\n"
"ds_read_b128 v[44:47], v2 offset:53248\n"
"v_mfma_f32_16x16x32_bf16 v[20:23], a[76:79], v[48:51], v[20:23]\n"
"s_waitcnt lgkmcnt(4)\n"
"v_mfma_f32_16x16x32_bf16 v[20:23], v[108:111], v[56:59], v[20:23]\n"
"ds_read_b128 v[48:51], v2 offset:11264\n"
"v_mfma_f32_16x16x32_bf16 v[20:23], v[220:223], v[60:63], v[20:23]\n"
"buffer_load_dwordx2 v[6:7], v0, s[32:35], s66 offen sc1\n"
"v_mfma_f32_16x16x32_bf16 v[16:19], v[216:219], v[60:63], v[16:19]\n"
"ds_read_b128 v[52:55], v2 offset:25600\n"
"v_mfma_f32_16x16x32_bf16 v[16:19], v[104:107], v[56:59], v[16:19]\n"
"s_waitcnt lgkmcnt(4)\n"
"v_mfma_f32_16x16x32_bf16 v[16:19], a[80:83], v[32:35], v[16:19]\n"
"ds_read_b128 v[56:59], v2 offset:39936\n"
"v_mfma_f32_16x16x32_bf16 v[16:19], a[192:195], v[36:39], v[16:19]\n"
"buffer_load_dwordx4 v2, s[32:35], s63 offen offset:1024 sc1 lds\n"
"v_mfma_f32_16x16x32_bf16 v[20:23], a[196:199], v[36:39], v[20:23]\n"
"ds_read_b128 v[60:63], v2 offset:54272\n"
"v_mfma_f32_16x16x32_bf16 v[20:23], a[84:87], v[32:35], v[20:23]\n"
"s_waitcnt lgkmcnt(4)\n"
"v_mfma_f32_16x16x32_bf16 v[20:23], v[116:119], v[40:43], v[20:23]\n"
"ds_read_b128 v[32:35], v2 offset:12288\n"
"v_mfma_f32_16x16x32_bf16 v[20:23], v[228:231], v[44:47], v[20:23]\n"
"buffer_load_dwordx4 v2, s[32:35], s63 offen offset:2048 sc1 lds\n"
"v_mfma_f32_16x16x32_bf16 v[16:19], v[224:227], v[44:47], v[16:19]\n"
"ds_read_b128 v[36:39], v2 offset:26624\n"
"v_mfma_f32_16x16x32_bf16 v[16:19], v[112:115], v[40:43], v[16:19]\n"
"s_waitcnt lgkmcnt(4)\n"
"v_mfma_f32_16x16x32_bf16 v[16:19], a[88:91], v[48:51], v[16:19]\n"
"ds_read_b128 v[40:43], v2 offset:40960\n"
"v_mfma_f32_16x16x32_bf16 v[16:19], a[200:203], v[52:55], v[16:19]\n"
"buffer_load_dwordx4 v2, s[32:35], s63 offen offset:3072 sc1 lds\n"
"s_add_u32 s63, s63, 4096\n"
"v_mfma_f32_16x16x32_bf16 v[20:23], a[204:207], v[52:55], v[20:23]\n"
"ds_read_b128 v[44:47], v2 offset:55296\n"
"s_add_u32 m0, s64, 69632\n"
"v_mfma_f32_16x16x32_bf16 v[20:23], a[92:95], v[48:51], v[20:23]\n"
"s_waitcnt lgkmcnt(4)\n"
"v_mfma_f32_16x16x32_bf16 v[20:23], v[124:127], v[56:59], v[20:23]\n"
"ds_read_b128 v[48:51], v2 offset:13312\n"
"v_mfma_f32_16x16x32_bf16 v[20:23], v[236:239], v[60:63], v[20:23]\n"
"buffer_load_dwordx4 v2, s[32:35], s63 offen offset:0 sc1 lds\n"
"v_mfma_f32_16x16x32_bf16 v[16:19], v[232:235], v[60:63], v[16:19]\n"
"ds_read_b128 v[52:55], v2 offset:27648\n"
"v_mfma_f32_16x16x32_bf16 v[16:19], v[120:123], v[56:59], v[16:19]\n"
"s_waitcnt lgkmcnt(4)\n"
"v_mfma_f32_16x16x32_bf16 v[16:19], a[96:99], v[32:35], v[16:19]\n"
"ds_read_b128 v[56:59], v2 offset:41984\n"
"v_mfma_f32_16x16x32_bf16 v[16:19], a[208:211], v[36:39], v[16:19]\n"
"buffer_load_dwordx4 v2, s[32:35], s63 offen offset:1024 sc1 lds\n"
"s_add_u32 s63, s63, 1175552\n"
"v_mfma_f32_16x16x32_bf16 v[20:23], a[212:215], v[36:39], v[20:23]\n"
"s_waitcnt vmcnt(14)\n"
"s_waitcnt vmcnt(11)\n"
"s_barrier\n"
"v_mfma_f32_16x16x32_bf16 v[20:23], a[100:103], v[32:35], v[20:23]\n"
"ds_read_b128 v[60:63], v2 offset:56320\n"
"s_waitcnt lgkmcnt(4)\n"
"v_mfma_f32_16x16x32_bf16 v[20:23], v[132:135], v[40:43], v[20:23]\n"
"ds_read_b128 v[32:35], v4 offset:0\n"
"v_mfma_f32_16x16x32_bf16 v[20:23], v[244:247], v[44:47], v[20:23]\n"
"v_mfma_f32_16x16x32_bf16 v[16:19], v[240:243], v[44:47], v[16:19]\n"
"ds_read_b128 v[36:39], v4 offset:14336\n"
"v_mfma_f32_16x16x32_bf16 v[16:19], v[128:131], v[40:43], v[16:19]\n"
"s_waitcnt lgkmcnt(4)\n"
"v_mfma_f32_16x16x32_bf16 v[16:19], a[104:107], v[48:51], v[16:19]\n"
"ds_read_b128 v[40:43], v4 offset:28672\n"
"v_mfma_f32_16x16x32_bf16 v[16:19], a[216:219], v[52:55], v[16:19]\n"
"v_mfma_f32_16x16x32_bf16 v[20:23], a[220:223], v[52:55], v[20:23]\n"
"ds_read_b128 v[44:47], v4 offset:43008\n"
"v_mfma_f32_16x16x32_bf16 v[20:23], a[108:111], v[48:51], v[20:23]\n"
"s_waitcnt lgkmcnt(4)\n"
"v_mfma_f32_16x16x32_bf16 v[20:23], v[140:143], v[56:59], v[20:23]\n"
"ds_read_b128 v[48:51], v4 offset:1024\n"
"v_mfma_f32_16x16x32_bf16 v[20:23], v[252:255], v[60:63], v[20:23]\n"
"v_mfma_f32_16x16x32_bf16 v[16:19], v[248:251], v[60:63], v[16:19]\n"
"ds_read_b128 v[52:55], v4 offset:15360\n"
"s_sub_u32 s62, s62, 1\n"
"v_mfma_f32_16x16x32_bf16 v[16:19], v[136:139], v[56:59], v[16:19]\n"
"s_cbranch_scc1 .L_dab_1\n"
"s_waitcnt lgkmcnt(4)\n"
"v_mfma_f32_16x16x32_bf16 v[24:27], a[0:3], v[32:35], 0\n"
"ds_read_b128 v[56:59], v4 offset:29696\n"
"ds_read_b128 v[12:15], v5 offset:0\n"
"v_mfma_f32_16x16x32_bf16 v[24:27], a[112:115], v[36:39], v[24:27]\n"
"v_mfma_f32_16x16x32_bf16 v[28:31], a[116:119], v[36:39], 0\n"
"ds_read_b128 v[60:63], v4 offset:44032\n"
"v_mfma_f32_16x16x32_bf16 v[28:31], a[4:7], v[32:35], v[28:31]\n"
"s_waitcnt lgkmcnt(5)\n"
"v_mfma_f32_16x16x32_bf16 v[28:31], a[228:231], v[40:43], v[28:31]\n"
"ds_read_b128 v[32:35], v4 offset:2048\n"
"v_mfma_f32_16x16x32_bf16 v[28:31], v[148:151], v[44:47], v[28:31]\n"
"s_waitcnt lgkmcnt(2)\n"
"v_add_f32 v16, v16, v12\n"
"v_mfma_f32_16x16x32_bf16 v[24:27], v[144:147], v[44:47], v[24:27]\n"
"ds_read_b128 v[36:39], v4 offset:16384\n"
"v_add_f32 v17, v17, v13\n"
"v_mfma_f32_16x16x32_bf16 v[24:27], a[224:227], v[40:43], v[24:27]\n"
"v_add_f32 v18, v18, v14\n"
"v_mfma_f32_16x16x32_bf16 v[24:27], a[8:11], v[48:51], v[24:27]\n"
"ds_read_b128 v[40:43], v4 offset:30720\n"
"v_add_f32 v19, v19, v15\n"
"v_mfma_f32_16x16x32_bf16 v[24:27], a[120:123], v[52:55], v[24:27]\n"
"ds_read_b128 v[12:15], v5 offset:1024\n"
"v_mfma_f32_16x16x32_bf16 v[28:31], a[124:127], v[52:55], v[28:31]\n"
"ds_read_b128 v[44:47], v4 offset:45056\n"
"v_mfma_f32_16x16x32_bf16 v[28:31], a[12:15], v[48:51], v[28:31]\n"
"s_waitcnt lgkmcnt(5)\n"
"v_mfma_f32_16x16x32_bf16 v[28:31], a[236:239], v[56:59], v[28:31]\n"
"ds_read_b128 v[48:51], v4 offset:3072\n"
"v_mfma_f32_16x16x32_bf16 v[28:31], v[156:159], v[60:63], v[28:31]\n"
"v_mfma_f32_16x16x32_bf16 v[24:27], v[152:155], v[60:63], v[24:27]\n"
"ds_read_b128 v[52:55], v4 offset:17408\n"
"s_waitcnt vmcnt(5)\n"
"v_mfma_f32_16x16x32_bf16 v[24:27], a[232:235], v[56:59], v[24:27]\n"
"v_cmp_ne_u64 vcc, v[8:9], v[6:7]\n"
"s_waitcnt lgkmcnt(5)\n"
"v_mfma_f32_16x16x32_bf16 v[24:27], a[16:19], v[32:35], v[24:27]\n"
"ds_read_b128 v[56:59], v4 offset:31744\n"
"s_cbranch_vccnz .L_rta1_1\n"
".L_bka1_1:\n"
"v_mfma_f32_16x16x32_bf16 v[24:27], a[128:131], v[36:39], v[24:27]\n"
"s_add_u32 s66, s66, 1187840\n"
"s_waitcnt lgkmcnt(4)\n"
"v_mfma_f32_16x16x32_bf16 v[28:31], a[132:135], v[36:39], v[28:31]\n"
"s_waitcnt vmcnt(0)\n"
"s_barrier\n"
"v_mfma_f32_16x16x32_bf16 v[28:31], a[20:23], v[32:35], v[28:31]\n"
"ds_read_b128 v[60:63], v4 offset:46080\n"
"s_waitcnt lgkmcnt(4)\n"
"v_mfma_f32_16x16x32_bf16 v[28:31], a[244:247], v[40:43], v[28:31]\n"
"ds_read_b128 v[32:35], v4 offset:4096\n"
"s_add_u32 m0, s64, 0\n"
"v_mfma_f32_16x16x32_bf16 v[28:31], v[164:167], v[44:47], v[28:31]\n"
"buffer_load_dwordx4 v2, s[32:35], s63 offen offset:0 sc1 lds\n"
"v_add_f32 v20, v20, v12\n"
"v_mfma_f32_16x16x32_bf16 v[24:27], v[160:163], v[44:47], v[24:27]\n"
"ds_read_b128 v[36:39], v4 offset:18432\n"
"v_add_f32 v21, v21, v13\n"
"v_mfma_f32_16x16x32_bf16 v[24:27], a[240:243], v[40:43], v[24:27]\n"
"v_add_f32 v22, v22, v14\n"
"s_waitcnt lgkmcnt(4)\n"
"v_mfma_f32_16x16x32_bf16 v[24:27], a[24:27], v[48:51], v[24:27]\n"
"ds_read_b128 v[40:43], v4 offset:32768\n"
"v_add_f32 v23, v23, v15\n"
"v_mfma_f32_16x16x32_bf16 v[24:27], a[136:139], v[52:55], v[24:27]\n"
"v_cvt_pk_bf16_f32 v12, v16, v17\n"
"v_cvt_pk_bf16_f32 v13, v18, v19\n"
"v_mfma_f32_16x16x32_bf16 v[28:31], a[140:143], v[52:55], v[28:31]\n"
"ds_read_b128 v[44:47], v4 offset:47104\n"
"v_cvt_pk_bf16_f32 v14, v20, v21\n"
"v_mfma_f32_16x16x32_bf16 v[28:31], a[28:31], v[48:51], v[28:31]\n"
"v_cvt_pk_bf16_f32 v15, v22, v23\n"
"s_waitcnt lgkmcnt(4)\n"
"v_mfma_f32_16x16x32_bf16 v[28:31], a[252:255], v[56:59], v[28:31]\n"
"ds_read_b128 v[48:51], v4 offset:5120\n"
"v_mfma_f32_16x16x32_bf16 v[28:31], v[172:175], v[60:63], v[28:31]\n"
"buffer_store_dwordx4 v[12:15], v2, s[32:35], s69 offen sc1\n"
"v_add_f32 v10, v16, v17\n"
"v_mfma_f32_16x16x32_bf16 v[24:27], v[168:171], v[60:63], v[24:27]\n"
"ds_read_b128 v[52:55], v4 offset:19456\n"
"v_mul_f32 v11, v16, v16\n"
"v_mfma_f32_16x16x32_bf16 v[24:27], a[248:251], v[56:59], v[24:27]\n"
"v_fmac_f32 v11, v17, v17\n"
"s_waitcnt lgkmcnt(4)\n"
"v_mfma_f32_16x16x32_bf16 v[24:27], a[32:35], v[32:35], v[24:27]\n"
"ds_read_b128 v[56:59], v4 offset:33792\n"
"v_add_f32 v10, v10, v18\n"
"v_mfma_f32_16x16x32_bf16 v[24:27], a[144:147], v[36:39], v[24:27]\n"
"buffer_load_dwordx4 v2, s[32:35], s63 offen offset:1024 sc1 lds\n"
"v_fmac_f32 v11, v18, v18\n"
"v_mfma_f32_16x16x32_bf16 v[28:31], a[148:151], v[36:39], v[28:31]\n"
"ds_read_b128 v[60:63], v4 offset:48128\n"
"v_add_f32 v10, v10, v19\n"
"v_mfma_f32_16x16x32_bf16 v[28:31], a[36:39], v[32:35], v[28:31]\n"
"v_fmac_f32 v11, v19, v19\n"
"s_waitcnt lgkmcnt(4)\n"
"v_mfma_f32_16x16x32_bf16 v[28:31], v[68:71], v[40:43], v[28:31]\n"
"ds_read_b128 v[32:35], v4 offset:6144\n"
"v_add_f32 v10, v10, v20\n"
"v_mfma_f32_16x16x32_bf16 v[28:31], v[180:183], v[44:47], v[28:31]\n"
"buffer_load_dwordx4 v2, s[32:35], s63 offen offset:2048 sc1 lds\n"
"v_fmac_f32 v11, v20, v20\n"
"v_mfma_f32_16x16x32_bf16 v[24:27], v[176:179], v[44:47], v[24:27]\n"
"ds_read_b128 v[36:39], v4 offset:20480\n"
"v_add_f32 v10, v10, v21\n"
"v_mfma_f32_16x16x32_bf16 v[24:27], v[64:67], v[40:43], v[24:27]\n"
"v_fmac_f32 v11, v21, v21\n"
"s_waitcnt lgkmcnt(4)\n"
"v_mfma_f32_16x16x32_bf16 v[24:27], a[40:43], v[48:51], v[24:27]\n"
"ds_read_b128 v[40:43], v4 offset:34816\n"
"v_add_f32 v10, v10, v22\n"
"v_mfma_f32_16x16x32_bf16 v[24:27], a[152:155], v[52:55], v[24:27]\n"
"buffer_load_dwordx4 v2, s[32:35], s63 offen offset:3072 sc1 lds\n"
"s_add_u32 m0, s74, 8192\n"
"v_mfma_f32_16x16x32_bf16 v[28:31], a[156:159], v[52:55], v[28:31]\n"
"ds_read_b128 v[44:47], v4 offset:49152\n"
"v_fmac_f32 v11, v22, v22\n"
"v_mfma_f32_16x16x32_bf16 v[28:31], a[44:47], v[48:51], v[28:31]\n"
"v_add_f32 v10, v10, v23\n"
"s_waitcnt lgkmcnt(4)\n"
"v_mfma_f32_16x16x32_bf16 v[28:31], v[76:79], v[56:59], v[28:31]\n"
"ds_read_b128 v[48:51], v4 offset:7168\n"
"v_fmac_f32 v11, v23, v23\n"
"v_mfma_f32_16x16x32_bf16 v[28:31], v[188:191], v[60:63], v[28:31]\n"
"buffer_load_dwordx4 v2, s[32:35], s71 offen offset:0 sc1 lds\n"
"v_permlane16_swap_b32 v10, v11\n"
"v_mfma_f32_16x16x32_bf16 v[24:27], v[184:187], v[60:63], v[24:27]\n"
"ds_read_b128 v[52:55], v4 offset:21504\n"
"v_add_f32 v10, v10, v11\n"
"v_mfma_f32_16x16x32_bf16 v[24:27], v[72:75], v[56:59], v[24:27]\n"
"s_waitcnt lgkmcnt(4)\n"
"v_mfma_f32_16x16x32_bf16 v[24:27], a[48:51], v[32:35], v[24:27]\n"
"ds_read_b128 v[56:59], v4 offset:35840\n"
"v_permlane32_swap_b32 v10, v11\n"
"v_mfma_f32_16x16x32_bf16 v[24:27], a[160:163], v[36:39], v[24:27]\n"
"buffer_load_dwordx4 v2, s[32:35], s71 offen offset:1024 sc1 lds\n"
"s_add_u32 s71, s71, 1187840\n"
"v_mfma_f32_16x16x32_bf16 v[28:31], a[164:167], v[36:39], v[28:31]\n"
"ds_read_b128 v[60:63], v4 offset:50176\n"
"s_add_u32 s63, s63, 4096\n"
"v_mfma_f32_16x16x32_bf16 v[28:31], a[52:55], v[32:35], v[28:31]\n"
"s_add_u32 m0, s64, 4096\n"
"s_waitcnt lgkmcnt(4)\n"
"v_mfma_f32_16x16x32_bf16 v[28:31], v[84:87], v[40:43], v[28:31]\n"
"ds_read_b128 v[32:35], v4 offset:8192\n"
"v_add_f32 v10, v10, v11\n"
"v_mfma_f32_16x16x32_bf16 v[28:31], v[196:199], v[44:47], v[28:31]\n"
"buffer_store_dword v10, v3, s[32:35], s69 offen offset:1024 sc1\n"
"s_mov_b32 s67, s69\n"
"v_mfma_f32_16x16x32_bf16 v[24:27], v[192:195], v[44:47], v[24:27]\n"
"ds_read_b128 v[36:39], v4 offset:22528\n"
"s_mov_b32 s69, s70\n"
"v_mfma_f32_16x16x32_bf16 v[24:27], v[80:83], v[40:43], v[24:27]\n"
"s_add_u32 s70, s70, 1187840\n"
"s_waitcnt lgkmcnt(4)\n"
"v_mfma_f32_16x16x32_bf16 v[24:27], a[56:59], v[48:51], v[24:27]\n"
"ds_read_b128 v[40:43], v4 offset:36864\n"
"v_mfma_f32_16x16x32_bf16 v[24:27], a[168:171], v[52:55], v[24:27]\n"
"buffer_load_dwordx4 v2, s[32:35], s63 offen offset:0 sc1 lds\n"
"v_mfma_f32_16x16x32_bf16 v[28:31], a[172:175], v[52:55], v[28:31]\n"
"ds_read_b128 v[44:47], v4 offset:51200\n"
"v_mfma_f32_16x16x32_bf16 v[28:31], a[60:63], v[48:51], v[28:31]\n"
"s_waitcnt lgkmcnt(4)\n"
"v_mfma_f32_16x16x32_bf16 v[28:31], v[92:95], v[56:59], v[28:31]\n"
"ds_read_b128 v[48:51], v4 offset:9216\n"
"v_mfma_f32_16x16x32_bf16 v[28:31], v[204:207], v[60:63], v[28:31]\n"
"buffer_load_dwordx4 v2, s[32:35], s63 offen offset:1024 sc1 lds\n"
"v_mfma_f32_16x16x32_bf16 v[24:27], v[200:203], v[60:63], v[24:27]\n"
"ds_read_b128 v[52:55], v4 offset:23552\n"
"v_mfma_f32_16x16x32_bf16 v[24:27], v[88:91], v[56:59], v[24:27]\n"
"s_waitcnt lgkmcnt(4)\n"
"v_mfma_f32_16x16x32_bf16 v[24:27], a[64:67], v[32:35], v[24:27]\n"
"ds_read_b128 v[56:59], v4 offset:37888\n"
"v_mfma_f32_16x16x32_bf16 v[24:27], a[176:179], v[36:39], v[24:27]\n"
"buffer_load_dwordx4 v2, s[32:35], s63 offen offset:2048 sc1 lds\n"
"v_mfma_f32_16x16x32_bf16 v[28:31], a[180:183], v[36:39], v[28:31]\n"
"ds_read_b128 v[60:63], v4 offset:52224\n"
"v_mfma_f32_16x16x32_bf16 v[28:31], a[68:71], v[32:35], v[28:31]\n"
"s_waitcnt lgkmcnt(4)\n"
"v_mfma_f32_16x16x32_bf16 v[28:31], v[100:103], v[40:43], v[28:31]\n"
"ds_read_b128 v[32:35], v4 offset:10240\n"
"v_mfma_f32_16x16x32_bf16 v[28:31], v[212:215], v[44:47], v[28:31]\n"
"buffer_load_dwordx4 v2, s[32:35], s63 offen offset:3072 sc1 lds\n"
"s_add_u32 s63, s63, 4096\n"
"v_mfma_f32_16x16x32_bf16 v[24:27], v[208:211], v[44:47], v[24:27]\n"
"ds_read_b128 v[36:39], v4 offset:24576\n"
"s_add_u32 m0, s64, 8192\n"
"v_mfma_f32_16x16x32_bf16 v[24:27], v[96:99], v[40:43], v[24:27]\n"
"s_waitcnt lgkmcnt(4)\n"
"v_mfma_f32_16x16x32_bf16 v[24:27], a[72:75], v[48:51], v[24:27]\n"
"ds_read_b128 v[40:43], v4 offset:38912\n"
"v_mfma_f32_16x16x32_bf16 v[24:27], a[184:187], v[52:55], v[24:27]\n"
"buffer_load_dwordx4 v2, s[32:35], s63 offen offset:0 sc1 lds\n"
"v_mfma_f32_16x16x32_bf16 v[28:31], a[188:191], v[52:55], v[28:31]\n"
"ds_read_b128 v[44:47], v4 offset:53248\n"
"v_mfma_f32_16x16x32_bf16 v[28:31], a[76:79], v[48:51], v[28:31]\n"
"s_waitcnt lgkmcnt(4)\n"
"v_mfma_f32_16x16x32_bf16 v[28:31], v[108:111], v[56:59], v[28:31]\n"
"ds_read_b128 v[48:51], v4 offset:11264\n"
"v_mfma_f32_16x16x32_bf16 v[28:31], v[220:223], v[60:63], v[28:31]\n"
"buffer_load_dwordx2 v[6:7], v0, s[32:35], s66 offen sc1\n"
"v_mfma_f32_16x16x32_bf16 v[24:27], v[216:219], v[60:63], v[24:27]\n"
"ds_read_b128 v[52:55], v4 offset:25600\n"
"v_mfma_f32_16x16x32_bf16 v[24:27], v[104:107], v[56:59], v[24:27]\n"
"s_waitcnt lgkmcnt(4)\n"
"v_mfma_f32_16x16x32_bf16 v[24:27], a[80:83], v[32:35], v[24:27]\n"
"ds_read_b128 v[56:59], v4 offset:39936\n"
"v_mfma_f32_16x16x32_bf16 v[24:27], a[192:195], v[36:39], v[24:27]\n"
"buffer_load_dwordx4 v2, s[32:35], s63 offen offset:1024 sc1 lds\n"
"v_mfma_f32_16x16x32_bf16 v[28:31], a[196:199], v[36:39], v[28:31]\n"
"ds_read_b128 v[60:63], v4 offset:54272\n"
"v_mfma_f32_16x16x32_bf16 v[28:31], a[84:87], v[32:35], v[28:31]\n"
"s_waitcnt lgkmcnt(4)\n"
"v_mfma_f32_16x16x32_bf16 v[28:31], v[116:119], v[40:43], v[28:31]\n"
"ds_read_b128 v[32:35], v4 offset:12288\n"
"v_mfma_f32_16x16x32_bf16 v[28:31], v[228:231], v[44:47], v[28:31]\n"
"buffer_load_dwordx4 v2, s[32:35], s63 offen offset:2048 sc1 lds\n"
"v_mfma_f32_16x16x32_bf16 v[24:27], v[224:227], v[44:47], v[24:27]\n"
"ds_read_b128 v[36:39], v4 offset:26624\n"
"v_mfma_f32_16x16x32_bf16 v[24:27], v[112:115], v[40:43], v[24:27]\n"
"s_waitcnt lgkmcnt(4)\n"
"v_mfma_f32_16x16x32_bf16 v[24:27], a[88:91], v[48:51], v[24:27]\n"
"ds_read_b128 v[40:43], v4 offset:40960\n"
"v_mfma_f32_16x16x32_bf16 v[24:27], a[200:203], v[52:55], v[24:27]\n"
"buffer_load_dwordx4 v2, s[32:35], s63 offen offset:3072 sc1 lds\n"
"s_add_u32 s63, s63, 4096\n"
"v_mfma_f32_16x16x32_bf16 v[28:31], a[204:207], v[52:55], v[28:31]\n"
"ds_read_b128 v[44:47], v4 offset:55296\n"
"s_add_u32 m0, s64, 12288\n"
"v_mfma_f32_16x16x32_bf16 v[28:31], a[92:95], v[48:51], v[28:31]\n"
"s_waitcnt lgkmcnt(4)\n"
"v_mfma_f32_16x16x32_bf16 v[28:31], v[124:127], v[56:59], v[28:31]\n"
"ds_read_b128 v[48:51], v4 offset:13312\n"
"v_mfma_f32_16x16x32_bf16 v[28:31], v[236:239], v[60:63], v[28:31]\n"
"buffer_load_dwordx4 v2, s[32:35], s63 offen offset:0 sc1 lds\n"
"v_mfma_f32_16x16x32_bf16 v[24:27], v[232:235], v[60:63], v[24:27]\n"
"ds_read_b128 v[52:55], v4 offset:27648\n"
"v_mfma_f32_16x16x32_bf16 v[24:27], v[120:123], v[56:59], v[24:27]\n"
"s_waitcnt lgkmcnt(4)\n"
"v_mfma_f32_16x16x32_bf16 v[24:27], a[96:99], v[32:35], v[24:27]\n"
"ds_read_b128 v[56:59], v4 offset:41984\n"
"v_mfma_f32_16x16x32_bf16 v[24:27], a[208:211], v[36:39], v[24:27]\n"
"buffer_load_dwordx4 v2, s[32:35], s63 offen offset:1024 sc1 lds\n"
"s_add_u32 s63, s63, 1175552\n"
"v_mfma_f32_16x16x32_bf16 v[28:31], a[212:215], v[36:39], v[28:31]\n"
"s_waitcnt vmcnt(14)\n"
"s_waitcnt vmcnt(11)\n"
"s_barrier\n"
"v_mfma_f32_16x16x32_bf16 v[28:31], a[100:103], v[32:35], v[28:31]\n"
"ds_read_b128 v[60:63], v4 offset:56320\n"
"s_waitcnt lgkmcnt(4)\n"
"v_mfma_f32_16x16x32_bf16 v[28:31], v[132:135], v[40:43], v[28:31]\n"
"ds_read_b128 v[32:35], v2 offset:0\n"
"v_mfma_f32_16x16x32_bf16 v[28:31], v[244:247], v[44:47], v[28:31]\n"
"v_mfma_f32_16x16x32_bf16 v[24:27], v[240:243], v[44:47], v[24:27]\n"
"ds_read_b128 v[36:39], v2 offset:14336\n"
"v_mfma_f32_16x16x32_bf16 v[24:27], v[128:131], v[40:43], v[24:27]\n"
"s_waitcnt lgkmcnt(4)\n"
"v_mfma_f32_16x16x32_bf16 v[24:27], a[104:107], v[48:51], v[24:27]\n"
"ds_read_b128 v[40:43], v2 offset:28672\n"
"v_mfma_f32_16x16x32_bf16 v[24:27], a[216:219], v[52:55], v[24:27]\n"
"v_mfma_f32_16x16x32_bf16 v[28:31], a[220:223], v[52:55], v[28:31]\n"
"ds_read_b128 v[44:47], v2 offset:43008\n"
"v_mfma_f32_16x16x32_bf16 v[28:31], a[108:111], v[48:51], v[28:31]\n"
"s_waitcnt lgkmcnt(4)\n"
"v_mfma_f32_16x16x32_bf16 v[28:31], v[140:143], v[56:59], v[28:31]\n"
"ds_read_b128 v[48:51], v2 offset:1024\n"
"v_mfma_f32_16x16x32_bf16 v[28:31], v[252:255], v[60:63], v[28:31]\n"
"v_mfma_f32_16x16x32_bf16 v[24:27], v[248:251], v[60:63], v[24:27]\n"
"ds_read_b128 v[52:55], v2 offset:15360\n"
"s_cmp_eq_u32 s62, 0\n"
"v_mfma_f32_16x16x32_bf16 v[24:27], v[136:139], v[56:59], v[24:27]\n"
"s_cbranch_scc0 .L_dal_1\n"
"s_mov_b32 s48, 0x7fff0000\n"
"s_waitcnt vmcnt(5)\n"
"v_writelane_b32 v8, 0, 63\n"
"v_writelane_b32 v9, 0, 63\n"
"v_writelane_b32 v6, 0, 63\n"
"v_writelane_b32 v7, 0, 63\n"
"v_writelane_b32 v0, s48, 63\n"
"s_branch .L_dal_1\n"
".L_dab_1:\n"
"s_mov_b32 s62, 15\n"
"s_add_u32 s74, s74, 8192\n"
"v_lshrrev_b32 v5, 8, v2\n"
"v_lshl_add_u32 v5, v5, 4, s74\n"
"v_and_b32 v3, 128, v2\n"
"v_lshl_add_u32 v5, v3, 2, v5\n"
"v_and_b32 v3, 16, v2\n"
"v_lshl_add_u32 v5, v3, 4, v5\n"
"v_and_b32 v3, 96, v2\n"
"v_lshl_add_u32 v5, v3, 1, v5\n"
"v_lshrrev_b32 v3, 2, v2\n"
"v_add_u32 v3, s74, v3\n"
".p2align 6\n"
".L_dbl_1:\n"
"s_waitcnt lgkmcnt(4)\n"
"v_mfma_f32_16x16x32_bf16 v[24:27], a[0:3], v[32:35], 0\n"
"ds_read_b128 v[56:59], v4 offset:29696\n"
"v_mfma_f32_16x16x32_bf16 v[24:27], a[112:115], v[36:39], v[24:27]\n"
"v_mfma_f32_16x16x32_bf16 v[28:31], a[116:119], v[36:39], 0\n"
"ds_read_b128 v[60:63], v4 offset:44032\n"
"v_mfma_f32_16x16x32_bf16 v[28:31], a[4:7], v[32:35], v[28:31]\n"
"v_cvt_pk_bf16_f32 v15, v22, v23\n"
"s_waitcnt lgkmcnt(4)\n"
"v_mfma_f32_16x16x32_bf16 v[28:31], a[228:231], v[40:43], v[28:31]\n"
"ds_read_b128 v[32:35], v4 offset:2048\n"
"v_cvt_pk_bf16_f32 v14, v20, v21\n"
"v_mfma_f32_16x16x32_bf16 v[28:31], v[148:151], v[44:47], v[28:31]\n"
"v_cvt_pk_bf16_f32 v13, v18, v19\n"
"v_cvt_pk_bf16_f32 v12, v16, v17\n"
"v_mfma_f32_16x16x32_bf16 v[24:27], v[144:147], v[44:47], v[24:27]\n"
"ds_read_b128 v[36:39], v4 offset:16384\n"
"ds_write_b128 v5, v[12:15]\n"
"v_mfma_f32_16x16x32_bf16 v[24:27], a[224:227], v[40:43], v[24:27]\n"
"ds_read2st64_b32 v[12:13], v3 offset0:0 offset1:1\n"
"s_waitcnt lgkmcnt(6)\n"
"v_mfma_f32_16x16x32_bf16 v[24:27], a[8:11], v[48:51], v[24:27]\n"
"ds_read_b128 v[40:43], v4 offset:30720\n"
"ds_read2st64_b32 v[14:15], v3 offset0:2 offset1:3\n"
"v_mfma_f32_16x16x32_bf16 v[24:27], a[120:123], v[52:55], v[24:27]\n"
"v_mfma_f32_16x16x32_bf16 v[28:31], a[124:127], v[52:55], v[28:31]\n"
"ds_read_b128 v[44:47], v4 offset:45056\n"
"v_mfma_f32_16x16x32_bf16 v[28:31], a[12:15], v[48:51], v[28:31]\n"
"s_waitcnt lgkmcnt(7)\n"
"v_mfma_f32_16x16x32_bf16 v[28:31], a[236:239], v[56:59], v[28:31]\n"
"ds_read_b128 v[48:51], v4 offset:3072\n"
"v_mfma_f32_16x16x32_bf16 v[28:31], v[156:159], v[60:63], v[28:31]\n"
"s_waitcnt lgkmcnt(2)\n"
"buffer_atomic_pk_add_bf16 v12, v1, s[44:47], s65 offen offset:0 sc1\n"
"v_mfma_f32_16x16x32_bf16 v[24:27], v[152:155], v[60:63], v[24:27]\n"
"ds_read_b128 v[52:55], v4 offset:17408\n"
"s_waitcnt vmcnt(4)\n"
"v_mfma_f32_16x16x32_bf16 v[24:27], a[232:235], v[56:59], v[24:27]\n"
"buffer_atomic_pk_add_bf16 v13, v1, s[44:47], s65 offen offset:2048 sc1\n"
"v_mfma_f32_16x16x32_bf16 v[24:27], a[16:19], v[32:35], v[24:27]\n"
"ds_read_b128 v[56:59], v4 offset:31744\n"
"v_cmp_ne_u64 vcc, v[8:9], v[6:7]\n"
"v_mfma_f32_16x16x32_bf16 v[24:27], a[128:131], v[36:39], v[24:27]\n"
"s_cbranch_vccnz .L_rtb1_1\n"
".L_bkb1_1:\n"
"s_add_u32 s66, s66, 1187840\n"
"v_mfma_f32_16x16x32_bf16 v[28:31], a[132:135], v[36:39], v[28:31]\n"
"s_waitcnt vmcnt(2)\n"
"s_barrier\n"
"v_mfma_f32_16x16x32_bf16 v[28:31], a[20:23], v[32:35], v[28:31]\n"
"ds_read_b128 v[60:63], v4 offset:46080\n"
"s_waitcnt lgkmcnt(4)\n"
"v_mfma_f32_16x16x32_bf16 v[28:31], a[244:247], v[40:43], v[28:31]\n"
"ds_read_b128 v[32:35], v4 offset:4096\n"
"s_add_u32 m0, s64, 0\n"
"v_mfma_f32_16x16x32_bf16 v[28:31], v[164:167], v[44:47], v[28:31]\n"
"buffer_load_dwordx4 v2, s[32:35], s63 offen offset:0 sc1 lds\n"
"s_add_u32 s65, s65, 16384\n"
"v_mfma_f32_16x16x32_bf16 v[24:27], v[160:163], v[44:47], v[24:27]\n"
"ds_read_b128 v[36:39], v4 offset:18432\n"
"v_mfma_f32_16x16x32_bf16 v[24:27], a[240:243], v[40:43], v[24:27]\n"
"buffer_atomic_pk_add_bf16 v14, v1, s[44:47], s65 offen offset:0 sc1\n"
"s_waitcnt lgkmcnt(4)\n"
"v_mfma_f32_16x16x32_bf16 v[24:27], a[24:27], v[48:51], v[24:27]\n"
"ds_read_b128 v[40:43], v4 offset:32768\n"
"v_mfma_f32_16x16x32_bf16 v[24:27], a[136:139], v[52:55], v[24:27]\n"
"buffer_atomic_pk_add_bf16 v15, v1, s[44:47], s65 offen offset:2048 sc1\n"
"s_add_u32 s65, s65, 16384\n"
"v_mfma_f32_16x16x32_bf16 v[28:31], a[140:143], v[52:55], v[28:31]\n"
"ds_read_b128 v[44:47], v4 offset:47104\n"
"v_mfma_f32_16x16x32_bf16 v[28:31], a[28:31], v[48:51], v[28:31]\n"
"s_waitcnt lgkmcnt(4)\n"
"v_mfma_f32_16x16x32_bf16 v[28:31], a[252:255], v[56:59], v[28:31]\n"
"ds_read_b128 v[48:51], v4 offset:5120\n"
"v_mfma_f32_16x16x32_bf16 v[28:31], v[172:175], v[60:63], v[28:31]\n"
"buffer_load_dwordx4 v2, s[32:35], s63 offen offset:1024 sc1 lds\n"
"v_mfma_f32_16x16x32_bf16 v[24:27], v[168:171], v[60:63], v[24:27]\n"
"ds_read_b128 v[52:55], v4 offset:19456\n"
"v_mfma_f32_16x16x32_bf16 v[24:27], a[248:251], v[56:59], v[24:27]\n"
"s_waitcnt lgkmcnt(4)\n"
"v_mfma_f32_16x16x32_bf16 v[24:27], a[32:35], v[32:35], v[24:27]\n"
"ds_read_b128 v[56:59], v4 offset:33792\n"
"v_mfma_f32_16x16x32_bf16 v[24:27], a[144:147], v[36:39], v[24:27]\n"
"buffer_load_dwordx4 v2, s[32:35], s63 offen offset:2048 sc1 lds\n"
"v_mfma_f32_16x16x32_bf16 v[28:31], a[148:151], v[36:39], v[28:31]\n"
"ds_read_b128 v[60:63], v4 offset:48128\n"
"v_mfma_f32_16x16x32_bf16 v[28:31], a[36:39], v[32:35], v[28:31]\n"
"s_waitcnt lgkmcnt(4)\n"
"v_mfma_f32_16x16x32_bf16 v[28:31], v[68:71], v[40:43], v[28:31]\n"
"ds_read_b128 v[32:35], v4 offset:6144\n"
"v_mfma_f32_16x16x32_bf16 v[28:31], v[180:183], v[44:47], v[28:31]\n"
"buffer_load_dwordx4 v2, s[32:35], s63 offen offset:3072 sc1 lds\n"
"v_mfma_f32_16x16x32_bf16 v[24:27], v[176:179], v[44:47], v[24:27]\n"
"ds_read_b128 v[36:39], v4 offset:20480\n"
"v_mfma_f32_16x16x32_bf16 v[24:27], v[64:67], v[40:43], v[24:27]\n"
"s_waitcnt lgkmcnt(4)\n"
"v_mfma_f32_16x16x32_bf16 v[24:27], a[40:43], v[48:51], v[24:27]\n"
"ds_read_b128 v[40:43], v4 offset:34816\n"
"s_add_u32 s63, s63, 4096\n"
"v_mfma_f32_16x16x32_bf16 v[24:27], a[152:155], v[52:55], v[24:27]\n"
"s_add_u32 m0, s64, 4096\n"
"v_mfma_f32_16x16x32_bf16 v[28:31], a[156:159], v[52:55], v[28:31]\n"
"ds_read_b128 v[44:47], v4 offset:49152\n"
"v_mfma_f32_16x16x32_bf16 v[28:31], a[44:47], v[48:51], v[28:31]\n"
"s_waitcnt lgkmcnt(4)\n"
"v_mfma_f32_16x16x32_bf16 v[28:31], v[76:79], v[56:59], v[28:31]\n"
"ds_read_b128 v[48:51], v4 offset:7168\n"
"v_mfma_f32_16x16x32_bf16 v[28:31], v[188:191], v[60:63], v[28:31]\n"
"buffer_load_dwordx4 v2, s[32:35], s63 offen offset:0 sc1 lds\n"
"v_mfma_f32_16x16x32_bf16 v[24:27], v[184:187], v[60:63], v[24:27]\n"
"ds_read_b128 v[52:55], v4 offset:21504\n"
"v_mfma_f32_16x16x32_bf16 v[24:27], v[72:75], v[56:59], v[24:27]\n"
"s_waitcnt lgkmcnt(4)\n"
"v_mfma_f32_16x16x32_bf16 v[24:27], a[48:51], v[32:35], v[24:27]\n"
"ds_read_b128 v[56:59], v4 offset:35840\n"
"v_mfma_f32_16x16x32_bf16 v[24:27], a[160:163], v[36:39], v[24:27]\n"
"buffer_load_dwordx4 v2, s[32:35], s63 offen offset:1024 sc1 lds\n"
"v_mfma_f32_16x16x32_bf16 v[28:31], a[164:167], v[36:39], v[28:31]\n"
"ds_read_b128 v[60:63], v4 offset:50176\n"
"v_mfma_f32_16x16x32_bf16 v[28:31], a[52:55], v[32:35], v[28:31]\n"
"s_waitcnt lgkmcnt(4)\n"
"v_mfma_f32_16x16x32_bf16 v[28:31], v[84:87], v[40:43], v[28:31]\n"
"ds_read_b128 v[32:35], v4 offset:8192\n"
"v_mfma_f32_16x16x32_bf16 v[28:31], v[196:199], v[44:47], v[28:31]\n"
"buffer_load_dwordx4 v2, s[32:35], s63 offen offset:2048 sc1 lds\n"
"v_mfma_f32_16x16x32_bf16 v[24:27], v[192:195], v[44:47], v[24:27]\n"
"ds_read_b128 v[36:39], v4 offset:22528\n"
"v_mfma_f32_16x16x32_bf16 v[24:27], v[80:83], v[40:43], v[24:27]\n"
"s_waitcnt lgkmcnt(4)\n"
"v_mfma_f32_16x16x32_bf16 v[24:27], a[56:59], v[48:51], v[24:27]\n"
"ds_read_b128 v[40:43], v4 offset:36864\n"
"v_mfma_f32_16x16x32_bf16 v[24:27], a[168:171], v[52:55], v[24:27]\n"
"buffer_load_dwordx4 v2, s[32:35], s63 offen offset:3072 sc1 lds\n"
"v_mfma_f32_16x16x32_bf16 v[28:31], a[172:175], v[52:55], v[28:31]\n"
"ds_read_b128 v[44:47], v4 offset:51200\n"
"v_mfma_f32_16x16x32_bf16 v[28:31], a[60:63], v[48:51], v[28:31]\n"
"s_waitcnt lgkmcnt(4)\n"
"v_mfma_f32_16x16x32_bf16 v[28:31], v[92:95], v[56:59], v[28:31]\n"
"ds_read_b128 v[48:51], v4 offset:9216\n"
"v_mfma_f32_16x16x32_bf16 v[28:31], v[204:207], v[60:63], v[28:31]\n"
"s_add_u32 s63, s63, 4096\n"
"s_add_u32 m0, s64, 8192\n"
"v_mfma_f32_16x16x32_bf16 v[24:27], v[200:203], v[60:63], v[24:27]\n"
"ds_read_b128 v[52:55], v4 offset:23552\n"
"v_mfma_f32_16x16x32_bf16 v[24:27], v[88:91], v[56:59], v[24:27]\n"
"s_waitcnt lgkmcnt(4)\n"
"v_mfma_f32_16x16x32_bf16 v[24:27], a[64:67], v[32:35], v[24:27]\n"
"ds_read_b128 v[56:59], v4 offset:37888\n"
"v_mfma_f32_16x16x32_bf16 v[24:27], a[176:179], v[36:39], v[24:27]\n"
"buffer_load_dwordx4 v2, s[32:35], s63 offen offset:0 sc1 lds\n"
"v_mfma_f32_16x16x32_bf16 v[28:31], a[180:183], v[36:39], v[28:31]\n"
"ds_read_b128 v[60:63], v4 offset:52224\n"
"v_mfma_f32_16x16x32_bf16 v[28:31], a[68:71], v[32:35], v[28:31]\n"
"s_waitcnt lgkmcnt(4)\n"
"v_mfma_f32_16x16x32_bf16 v[28:31], v[100:103], v[40:43], v[28:31]\n"
"ds_read_b128 v[32:35], v4 offset:10240\n"
"v_mfma_f32_16x16x32_bf16 v[28:31], v[212:215], v[44:47], v[28:31]\n"
"buffer_load_dwordx4 v2, s[32:35], s63 offen offset:1024 sc1 lds\n"
"v_mfma_f32_16x16x32_bf16 v[24:27], v[208:211], v[44:47], v[24:27]\n"
"ds_read_b128 v[36:39], v4 offset:24576\n"
"v_mfma_f32_16x16x32_bf16 v[24:27], v[96:99], v[40:43], v[24:27]\n"
"s_waitcnt lgkmcnt(4)\n"
"v_mfma_f32_16x16x32_bf16 v[24:27], a[72:75], v[48:51], v[24:27]\n"
"ds_read_b128 v[40:43], v4 offset:38912\n"
"v_mfma_f32_16x16x32_bf16 v[24:27], a[184:187], v[52:55], v[24:27]\n"
"buffer_load_dwordx4 v2, s[32:35], s63 offen offset:2048 sc1 lds\n"
"v_mfma_f32_16x16x32_bf16 v[28:31], a[188:191], v[52:55], v[28:31]\n"
"ds_read_b128 v[44:47], v4 offset:53248\n"
"v_mfma_f32_16x16x32_bf16 v[28:31], a[76:79], v[48:51], v[28:31]\n"
"s_waitcnt lgkmcnt(4)\n"
"v_mfma_f32_16x16x32_bf16 v[28:31], v[108:111], v[56:59], v[28:31]\n"
"ds_read_b128 v[48:51], v4 offset:11264\n"
"v_mfma_f32_16x16x32_bf16 v[28:31], v[220:223], v[60:63], v[28:31]\n"
"buffer_load_dwordx2 v[6:7], v0, s[32:35], s66 offen sc1\n"
"v_mfma_f32_16x16x32_bf16 v[24:27], v[216:219], v[60:63], v[24:27]\n"
"ds_read_b128 v[52:55], v4 offset:25600\n"
"v_mfma_f32_16x16x32_bf16 v[24:27], v[104:107], v[56:59], v[24:27]\n"
"s_waitcnt lgkmcnt(4)\n"
"v_mfma_f32_16x16x32_bf16 v[24:27], a[80:83], v[32:35], v[24:27]\n"
"ds_read_b128 v[56:59], v4 offset:39936\n"
"v_mfma_f32_16x16x32_bf16 v[24:27], a[192:195], v[36:39], v[24:27]\n"
"buffer_load_dwordx4 v2, s[32:35], s63 offen offset:3072 sc1 lds\n"
"s_add_u32 s63, s63, 4096\n"
"v_mfma_f32_16x16x32_bf16 v[28:31], a[196:199], v[36:39], v[28:31]\n"
"ds_read_b128 v[60:63], v4 offset:54272\n"
"s_add_u32 m0, s64, 12288\n"
"v_mfma_f32_16x16x32_bf16 v[28:31], a[84:87], v[32:35], v[28:31]\n"
"s_waitcnt lgkmcnt(4)\n"
"v_mfma_f32_16x16x32_bf16 v[28:31], v[116:119], v[40:43], v[28:31]\n"
"ds_read_b128 v[32:35], v4 offset:12288\n"
"v_mfma_f32_16x16x32_bf16 v[28:31], v[228:231], v[44:47], v[28:31]\n"
"buffer_load_dwordx4 v2, s[32:35], s63 offen offset:0 sc1 lds\n"
"v_mfma_f32_16x16x32_bf16 v[24:27], v[224:227], v[44:47], v[24:27]\n"
"ds_read_b128 v[36:39], v4 offset:26624\n"
"v_mfma_f32_16x16x32_bf16 v[24:27], v[112:115], v[40:43], v[24:27]\n"
"s_waitcnt lgkmcnt(4)\n"
"v_mfma_f32_16x16x32_bf16 v[24:27], a[88:91], v[48:51], v[24:27]\n"
"ds_read_b128 v[40:43], v4 offset:40960\n"
"v_mfma_f32_16x16x32_bf16 v[24:27], a[200:203], v[52:55], v[24:27]\n"
"buffer_load_dwordx4 v2, s[32:35], s63 offen offset:1024 sc1 lds\n"
"s_add_u32 s63, s63, 1175552\n"
"v_mfma_f32_16x16x32_bf16 v[28:31], a[204:207], v[52:55], v[28:31]\n"
"ds_read_b128 v[44:47], v4 offset:55296\n"
"v_mfma_f32_16x16x32_bf16 v[28:31], a[92:95], v[48:51], v[28:31]\n"
"s_waitcnt lgkmcnt(4)\n"
"v_mfma_f32_16x16x32_bf16 v[28:31], v[124:127], v[56:59], v[28:31]\n"
"ds_read_b128 v[48:51], v4 offset:13312\n"
"v_mfma_f32_16x16x32_bf16 v[28:31], v[236:239], v[60:63], v[28:31]\n"
"v_mfma_f32_16x16x32_bf16 v[24:27], v[232:235], v[60:63], v[24:27]\n"
"ds_read_b128 v[52:55], v4 offset:27648\n"
"v_mfma_f32_16x16x32_bf16 v[24:27], v[120:123], v[56:59], v[24:27]\n"
"s_waitcnt lgkmcnt(4)\n"
"v_mfma_f32_16x16x32_bf16 v[24:27], a[96:99], v[32:35], v[24:27]\n"
"ds_read_b128 v[56:59], v4 offset:41984\n"
"v_mfma_f32_16x16x32_bf16 v[24:27], a[208:211], v[36:39], v[24:27]\n"
"v_mfma_f32_16x16x32_bf16 v[28:31], a[212:215], v[36:39], v[28:31]\n"
"s_waitcnt vmcnt(11)\n"
"s_barrier\n"
"v_mfma_f32_16x16x32_bf16 v[28:31], a[100:103], v[32:35], v[28:31]\n"
"ds_read_b128 v[60:63], v4 offset:56320\n"
"s_waitcnt lgkmcnt(4)\n"
"v_mfma_f32_16x16x32_bf16 v[28:31], v[132:135], v[40:43], v[28:31]\n"
"ds_read_b128 v[32:35], v2 offset:0\n"
"v_mfma_f32_16x16x32_bf16 v[28:31], v[244:247], v[44:47], v[28:31]\n"
"v_mfma_f32_16x16x32_bf16 v[24:27], v[240:243], v[44:47], v[24:27]\n"
"ds_read_b128 v[36:39], v2 offset:14336\n"
"v_mfma_f32_16x16x32_bf16 v[24:27], v[128:131], v[40:43], v[24:27]\n"
"s_waitcnt lgkmcnt(4)\n"
"v_mfma_f32_16x16x32_bf16 v[24:27], a[104:107], v[48:51], v[24:27]\n"
"ds_read_b128 v[40:43], v2 offset:28672\n"
"v_mfma_f32_16x16x32_bf16 v[24:27], a[216:219], v[52:55], v[24:27]\n"
"v_mfma_f32_16x16x32_bf16 v[28:31], a[220:223], v[52:55], v[28:31]\n"
"ds_read_b128 v[44:47], v2 offset:43008\n"
"v_mfma_f32_16x16x32_bf16 v[28:31], a[108:111], v[48:51], v[28:31]\n"
"s_waitcnt lgkmcnt(4)\n"
"v_mfma_f32_16x16x32_bf16 v[28:31], v[140:143], v[56:59], v[28:31]\n"
"ds_read_b128 v[48:51], v2 offset:1024\n"
"v_mfma_f32_16x16x32_bf16 v[28:31], v[252:255], v[60:63], v[28:31]\n"
"v_mfma_f32_16x16x32_bf16 v[24:27], v[248:251], v[60:63], v[24:27]\n"
"ds_read_b128 v[52:55], v2 offset:15360\n"
"s_sub_u32 s62, s62, 1\n"
"v_mfma_f32_16x16x32_bf16 v[24:27], v[136:139], v[56:59], v[24:27]\n"
"s_cbranch_scc1 .L_dbt_1\n"
"s_waitcnt lgkmcnt(4)\n"
"v_mfma_f32_16x16x32_bf16 v[16:19], a[0:3], v[32:35], 0\n"
"ds_read_b128 v[56:59], v2 offset:29696\n"
"v_mfma_f32_16x16x32_bf16 v[16:19], a[112:115], v[36:39], v[16:19]\n"
"v_mfma_f32_16x16x32_bf16 v[20:23], a[116:119], v[36:39], 0\n"
"ds_read_b128 v[60:63], v2 offset:44032\n"
"v_mfma_f32_16x16x32_bf16 v[20:23], a[4:7], v[32:35], v[20:23]\n"
"v_cvt_pk_bf16_f32 v15, v30, v31\n"
"s_waitcnt lgkmcnt(4)\n"
"v_mfma_f32_16x16x32_bf16 v[20:23], a[228:231], v[40:43], v[20:23]\n"
"ds_read_b128 v[32:35], v2 offset:2048\n"
"v_cvt_pk_bf16_f32 v14, v28, v29\n"
"v_mfma_f32_16x16x32_bf16 v[20:23], v[148:151], v[44:47], v[20:23]\n"
"v_cvt_pk_bf16_f32 v13, v26, v27\n"
"v_cvt_pk_bf16_f32 v12, v24, v25\n"
"v_mfma_f32_16x16x32_bf16 v[16:19], v[144:147], v[44:47], v[16:19]\n"
"ds_read_b128 v[36:39], v2 offset:16384\n"
"ds_write_b128 v5, v[12:15]\n"
"v_mfma_f32_16x16x32_bf16 v[16:19], a[224:227], v[40:43], v[16:19]\n"
"ds_read2st64_b32 v[12:13], v3 offset0:0 offset1:1\n"
"s_waitcnt lgkmcnt(6)\n"
"v_mfma_f32_16x16x32_bf16 v[16:19], a[8:11], v[48:51], v[16:19]\n"
"ds_read_b128 v[40:43], v2 offset:30720\n"
"ds_read2st64_b32 v[14:15], v3 offset0:2 offset1:3\n"
"v_mfma_f32_16x16x32_bf16 v[16:19], a[120:123], v[52:55], v[16:19]\n"
"v_mfma_f32_16x16x32_bf16 v[20:23], a[124:127], v[52:55], v[20:23]\n"
"ds_read_b128 v[44:47], v2 offset:45056\n"
"v_mfma_f32_16x16x32_bf16 v[20:23], a[12:15], v[48:51], v[20:23]\n"
"s_waitcnt lgkmcnt(7)\n"
"v_mfma_f32_16x16x32_bf16 v[20:23], a[236:239], v[56:59], v[20:23]\n"
"ds_read_b128 v[48:51], v2 offset:3072\n"
"v_mfma_f32_16x16x32_bf16 v[20:23], v[156:159], v[60:63], v[20:23]\n"
"s_waitcnt lgkmcnt(2)\n"
"buffer_atomic_pk_add_bf16 v12, v1, s[44:47], s65 offen offset:0 sc1\n"
"v_mfma_f32_16x16x32_bf16 v[16:19], v[152:155], v[60:63], v[16:19]\n"
"ds_read_b128 v[52:55], v2 offset:17408\n"
"s_waitcnt vmcnt(4)\n"
"v_mfma_f32_16x16x32_bf16 v[16:19], a[232:235], v[56:59], v[16:19]\n"
"buffer_atomic_pk_add_bf16 v13, v1, s[44:47], s65 offen offset:2048 sc1\n"
"v_mfma_f32_16x16x32_bf16 v[16:19], a[16:19], v[32:35], v[16:19]\n"
"ds_read_b128 v[56:59], v2 offset:31744\n"
"v_cmp_ne_u64 vcc, v[8:9], v[6:7]\n"
"v_mfma_f32_16x16x32_bf16 v[16:19], a[128:131], v[36:39], v[16:19]\n"
"s_cbranch_vccnz .L_rtb0_1\n"
".L_bkb0_1:\n"
"s_add_u32 s66, s66, 1187840\n"
"v_mfma_f32_16x16x32_bf16 v[20:23], a[132:135], v[36:39], v[20:23]\n"
"s_waitcnt vmcnt(2)\n"
"s_barrier\n"
"v_mfma_f32_16x16x32_bf16 v[20:23], a[20:23], v[32:35], v[20:23]\n"
"ds_read_b128 v[60:63], v2 offset:46080\n"
"s_waitcnt lgkmcnt(4)\n"
"v_mfma_f32_16x16x32_bf16 v[20:23], a[244:247], v[40:43], v[20:23]\n"
"ds_read_b128 v[32:35], v2 offset:4096\n"
"s_add_u32 m0, s64, 57344\n"
"v_mfma_f32_16x16x32_bf16 v[20:23], v[164:167], v[44:47], v[20:23]\n"
"buffer_load_dwordx4 v2, s[32:35], s63 offen offset:0 sc1 lds\n"
"s_add_u32 s65, s65, 16384\n"
"v_mfma_f32_16x16x32_bf16 v[16:19], v[160:163], v[44:47], v[16:19]\n"
"ds_read_b128 v[36:39], v2 offset:18432\n"
"v_mfma_f32_16x16x32_bf16 v[16:19], a[240:243], v[40:43], v[16:19]\n"
"buffer_atomic_pk_add_bf16 v14, v1, s[44:47], s65 offen offset:0 sc1\n"
"s_waitcnt lgkmcnt(4)\n"
"v_mfma_f32_16x16x32_bf16 v[16:19], a[24:27], v[48:51], v[16:19]\n"
"ds_read_b128 v[40:43], v2 offset:32768\n"
"v_mfma_f32_16x16x32_bf16 v[16:19], a[136:139], v[52:55], v[16:19]\n"
"buffer_atomic_pk_add_bf16 v15, v1, s[44:47], s65 offen offset:2048 sc1\n"
"s_add_u32 s65, s65, 16384\n"
"v_mfma_f32_16x16x32_bf16 v[20:23], a[140:143], v[52:55], v[20:23]\n"
"ds_read_b128 v[44:47], v2 offset:47104\n"
"v_mfma_f32_16x16x32_bf16 v[20:23], a[28:31], v[48:51], v[20:23]\n"
"s_waitcnt lgkmcnt(4)\n"
"v_mfma_f32_16x16x32_bf16 v[20:23], a[252:255], v[56:59], v[20:23]\n"
"ds_read_b128 v[48:51], v2 offset:5120\n"
"v_mfma_f32_16x16x32_bf16 v[20:23], v[172:175], v[60:63], v[20:23]\n"
"buffer_load_dwordx4 v2, s[32:35], s63 offen offset:1024 sc1 lds\n"
"v_mfma_f32_16x16x32_bf16 v[16:19], v[168:171], v[60:63], v[16:19]\n"
"ds_read_b128 v[52:55], v2 offset:19456\n"
"v_mfma_f32_16x16x32_bf16 v[16:19], a[248:251], v[56:59], v[16:19]\n"
"s_waitcnt lgkmcnt(4)\n"
"v_mfma_f32_16x16x32_bf16 v[16:19], a[32:35], v[32:35], v[16:19]\n"
"ds_read_b128 v[56:59], v2 offset:33792\n"
"v_mfma_f32_16x16x32_bf16 v[16:19], a[144:147], v[36:39], v[16:19]\n"
"buffer_load_dwordx4 v2, s[32:35], s63 offen offset:2048 sc1 lds\n"
"v_mfma_f32_16x16x32_bf16 v[20:23], a[148:151], v[36:39], v[20:23]\n"
"ds_read_b128 v[60:63], v2 offset:48128\n"
"v_mfma_f32_16x16x32_bf16 v[20:23], a[36:39], v[32:35], v[20:23]\n"
"s_waitcnt lgkmcnt(4)\n"
"v_mfma_f32_16x16x32_bf16 v[20:23], v[68:71], v[40:43], v[20:23]\n"
"ds_read_b128 v[32:35], v2 offset:6144\n"
"v_mfma_f32_16x16x32_bf16 v[20:23], v[180:183], v[44:47], v[20:23]\n"
"buffer_load_dwordx4 v2, s[32:35], s63 offen offset:3072 sc1 lds\n"
"v_mfma_f32_16x16x32_bf16 v[16:19], v[176:179], v[44:47], v[16:19]\n"
"ds_read_b128 v[36:39], v2 offset:20480\n"
"v_mfma_f32_16x16x32_bf16 v[16:19], v[64:67], v[40:43], v[16:19]\n"
"s_waitcnt lgkmcnt(4)\n"
"v_mfma_f32_16x16x32_bf16 v[16:19], a[40:43], v[48:51], v[16:19]\n"
"ds_read_b128 v[40:43], v2 offset:34816\n"
"s_add_u32 s63, s63, 4096\n"
"v_mfma_f32_16x16x32_bf16 v[16:19], a[152:155], v[52:55], v[16:19]\n"
"s_add_u32 m0, s64, 61440\n"
"v_mfma_f32_16x16x32_bf16 v[20:23], a[156:159], v[52:55], v[20:23]\n"
"ds_read_b128 v[44:47], v2 offset:49152\n"
"v_mfma_f32_16x16x32_bf16 v[20:23], a[44:47], v[48:51], v[20:23]\n"
"s_waitcnt lgkmcnt(4)\n"
"v_mfma_f32_16x16x32_bf16 v[20:23], v[76:79], v[56:59], v[20:23]\n"
"ds_read_b128 v[48:51], v2 offset:7168\n"
"v_mfma_f32_16x16x32_bf16 v[20:23], v[188:191], v[60:63], v[20:23]\n"
"buffer_load_dwordx4 v2, s[32:35], s63 offen offset:0 sc1 lds\n"
"v_mfma_f32_16x16x32_bf16 v[16:19], v[184:187], v[60:63], v[16:19]\n"
"ds_read_b128 v[52:55], v2 offset:21504\n"
"v_mfma_f32_16x16x32_bf16 v[16:19], v[72:75], v[56:59], v[16:19]\n"
"s_waitcnt lgkmcnt(4)\n"
"v_mfma_f32_16x16x32_bf16 v[16:19], a[48:51], v[32:35], v[16:19]\n"
"ds_read_b128 v[56:59], v2 offset:35840\n"
"v_mfma_f32_16x16x32_bf16 v[16:19], a[160:163], v[36:39], v[16:19]\n"
"buffer_load_dwordx4 v2, s[32:35], s63 offen offset:1024 sc1 lds\n"
"v_mfma_f32_16x16x32_bf16 v[20:23], a[164:167], v[36:39], v[20:23]\n"
"ds_read_b128 v[60:63], v2 offset:50176\n"
"v_mfma_f32_16x16x32_bf16 v[20:23], a[52:55], v[32:35], v[20:23]\n"
"s_waitcnt lgkmcnt(4)\n"
"v_mfma_f32_16x16x32_bf16 v[20:23], v[84:87], v[40:43], v[20:23]\n"
"ds_read_b128 v[32:35], v2 offset:8192\n"
"v_mfma_f32_16x16x32_bf16 v[20:23], v[196:199], v[44:47], v[20:23]\n"
"buffer_load_dwordx4 v2, s[32:35], s63 offen offset:2048 sc1 lds\n"
"v_mfma_f32_16x16x32_bf16 v[16:19], v[192:195], v[44:47], v[16:19]\n"
"ds_read_b128 v[36:39], v2 offset:22528\n"
"v_mfma_f32_16x16x32_bf16 v[16:19], v[80:83], v[40:43], v[16:19]\n"
"s_waitcnt lgkmcnt(4)\n"
"v_mfma_f32_16x16x32_bf16 v[16:19], a[56:59], v[48:51], v[16:19]\n"
"ds_read_b128 v[40:43], v2 offset:36864\n"
"v_mfma_f32_16x16x32_bf16 v[16:19], a[168:171], v[52:55], v[16:19]\n"
"buffer_load_dwordx4 v2, s[32:35], s63 offen offset:3072 sc1 lds\n"
"v_mfma_f32_16x16x32_bf16 v[20:23], a[172:175], v[52:55], v[20:23]\n"
"ds_read_b128 v[44:47], v2 offset:51200\n"
"v_mfma_f32_16x16x32_bf16 v[20:23], a[60:63], v[48:51], v[20:23]\n"
"s_waitcnt lgkmcnt(4)\n"
"v_mfma_f32_16x16x32_bf16 v[20:23], v[92:95], v[56:59], v[20:23]\n"
"ds_read_b128 v[48:51], v2 offset:9216\n"
"v_mfma_f32_16x16x32_bf16 v[20:23], v[204:207], v[60:63], v[20:23]\n"
"s_add_u32 s63, s63, 4096\n"
"s_add_u32 m0, s64, 65536\n"
"v_mfma_f32_16x16x32_bf16 v[16:19], v[200:203], v[60:63], v[16:19]\n"
"ds_read_b128 v[52:55], v2 offset:23552\n"
"v_mfma_f32_16x16x32_bf16 v[16:19], v[88:91], v[56:59], v[16:19]\n"
"s_waitcnt lgkmcnt(4)\n"
"v_mfma_f32_16x16x32_bf16 v[16:19], a[64:67], v[32:35], v[16:19]\n"
"ds_read_b128 v[56:59], v2 offset:37888\n"
"v_mfma_f32_16x16x32_bf16 v[16:19], a[176:179], v[36:39], v[16:19]\n"
"buffer_load_dwordx4 v2, s[32:35], s63 offen offset:0 sc1 lds\n"
"v_mfma_f32_16x16x32_bf16 v[20:23], a[180:183], v[36:39], v[20:23]\n"
"ds_read_b128 v[60:63], v2 offset:52224\n"
"v_mfma_f32_16x16x32_bf16 v[20:23], a[68:71], v[32:35], v[20:23]\n"
"s_waitcnt lgkmcnt(4)\n"
"v_mfma_f32_16x16x32_bf16 v[20:23], v[100:103], v[40:43], v[20:23]\n"
"ds_read_b128 v[32:35], v2 offset:10240\n"
"v_mfma_f32_16x16x32_bf16 v[20:23], v[212:215], v[44:47], v[20:23]\n"
"buffer_load_dwordx4 v2, s[32:35], s63 offen offset:1024 sc1 lds\n"
"v_mfma_f32_16x16x32_bf16 v[16:19], v[208:211], v[44:47], v[16:19]\n"
"ds_read_b128 v[36:39], v2 offset:24576\n"
"v_mfma_f32_16x16x32_bf16 v[16:19], v[96:99], v[40:43], v[16:19]\n"
"s_waitcnt lgkmcnt(4)\n"
"v_mfma_f32_16x16x32_bf16 v[16:19], a[72:75], v[48:51], v[16:19]\n"
"ds_read_b128 v[40:43], v2 offset:38912\n"
"v_mfma_f32_16x16x32_bf16 v[16:19], a[184:187], v[52:55], v[16:19]\n"
"buffer_load_dwordx4 v2, s[32:35], s63 offen offset:2048 sc1 lds\n"
"v_mfma_f32_16x16x32_bf16 v[20:23], a[188:191], v[52:55], v[20:23]\n"
"ds_read_b128 v[44:47], v2 offset:53248\n"
"v_mfma_f32_16x16x32_bf16 v[20:23], a[76:79], v[48:51], v[20:23]\n"
"s_waitcnt lgkmcnt(4)\n"
"v_mfma_f32_16x16x32_bf16 v[20:23], v[108:111], v[56:59], v[20:23]\n"
"ds_read_b128 v[48:51], v2 offset:11264\n"
"v_mfma_f32_16x16x32_bf16 v[20:23], v[220:223], v[60:63], v[20:23]\n"
"buffer_load_dwordx2 v[6:7], v0, s[32:35], s66 offen sc1\n"
"v_mfma_f32_16x16x32_bf16 v[16:19], v[216:219], v[60:63], v[16:19]\n"
"ds_read_b128 v[52:55], v2 offset:25600\n"
"v_mfma_f32_16x16x32_bf16 v[16:19], v[104:107], v[56:59], v[16:19]\n"
"s_waitcnt lgkmcnt(4)\n"
"v_mfma_f32_16x16x32_bf16 v[16:19], a[80:83], v[32:35], v[16:19]\n"
"ds_read_b128 v[56:59], v2 offset:39936\n"
"v_mfma_f32_16x16x32_bf16 v[16:19], a[192:195], v[36:39], v[16:19]\n"
"buffer_load_dwordx4 v2, s[32:35], s63 offen offset:3072 sc1 lds\n"
"s_add_u32 s63, s63, 4096\n"
"v_mfma_f32_16x16x32_bf16 v[20:23], a[196:199], v[36:39], v[20:23]\n"
"ds_read_b128 v[60:63], v2 offset:54272\n"
"s_add_u32 m0, s64, 69632\n"
"v_mfma_f32_16x16x32_bf16 v[20:23], a[84:87], v[32:35], v[20:23]\n"
"s_waitcnt lgkmcnt(4)\n"
"v_mfma_f32_16x16x32_bf16 v[20:23], v[116:119], v[40:43], v[20:23]\n"
"ds_read_b128 v[32:35], v2 offset:12288\n"
"v_mfma_f32_16x16x32_bf16 v[20:23], v[228:231], v[44:47], v[20:23]\n"
"buffer_load_dwordx4 v2, s[32:35], s63 offen offset:0 sc1 lds\n"
"v_mfma_f32_16x16x32_bf16 v[16:19], v[224:227], v[44:47], v[16:19]\n"
"ds_read_b128 v[36:39], v2 offset:26624\n"
"v_mfma_f32_16x16x32_bf16 v[16:19], v[112:115], v[40:43], v[16:19]\n"
"s_waitcnt lgkmcnt(4)\n"
"v_mfma_f32_16x16x32_bf16 v[16:19], a[88:91], v[48:51], v[16:19]\n"
"ds_read_b128 v[40:43], v2 offset:40960\n"
"v_mfma_f32_16x16x32_bf16 v[16:19], a[200:203], v[52:55], v[16:19]\n"
"buffer_load_dwordx4 v2, s[32:35], s63 offen offset:1024 sc1 lds\n"
"s_add_u32 s63, s63, 1175552\n"
"v_mfma_f32_16x16x32_bf16 v[20:23], a[204:207], v[52:55], v[20:23]\n"
"ds_read_b128 v[44:47], v2 offset:55296\n"
"v_mfma_f32_16x16x32_bf16 v[20:23], a[92:95], v[48:51], v[20:23]\n"
"s_waitcnt lgkmcnt(4)\n"
"v_mfma_f32_16x16x32_bf16 v[20:23], v[124:127], v[56:59], v[20:23]\n"
"ds_read_b128 v[48:51], v2 offset:13312\n"
"v_mfma_f32_16x16x32_bf16 v[20:23], v[236:239], v[60:63], v[20:23]\n"
"v_mfma_f32_16x16x32_bf16 v[16:19], v[232:235], v[60:63], v[16:19]\n"
"ds_read_b128 v[52:55], v2 offset:27648\n"
"v_mfma_f32_16x16x32_bf16 v[16:19], v[120:123], v[56:59], v[16:19]\n"
"s_waitcnt lgkmcnt(4)\n"
"v_mfma_f32_16x16x32_bf16 v[16:19], a[96:99], v[32:35], v[16:19]\n"
"ds_read_b128 v[56:59], v2 offset:41984\n"
"v_mfma_f32_16x16x32_bf16 v[16:19], a[208:211], v[36:39], v[16:19]\n"
"v_mfma_f32_16x16x32_bf16 v[20:23], a[212:215], v[36:39], v[20:23]\n"
"s_waitcnt vmcnt(11)\n"
"s_barrier\n"
"v_mfma_f32_16x16x32_bf16 v[20:23], a[100:103], v[32:35], v[20:23]\n"
"ds_read_b128 v[60:63], v2 offset:56320\n"
"s_waitcnt lgkmcnt(4)\n"
"v_mfma_f32_16x16x32_bf16 v[20:23], v[132:135], v[40:43], v[20:23]\n"
"ds_read_b128 v[32:35], v4 offset:0\n"
"v_mfma_f32_16x16x32_bf16 v[20:23], v[244:247], v[44:47], v[20:23]\n"
"v_mfma_f32_16x16x32_bf16 v[16:19], v[240:243], v[44:47], v[16:19]\n"
"ds_read_b128 v[36:39], v4 offset:14336\n"
"v_mfma_f32_16x16x32_bf16 v[16:19], v[128:131], v[40:43], v[16:19]\n"
"s_waitcnt lgkmcnt(4)\n"
"v_mfma_f32_16x16x32_bf16 v[16:19], a[104:107], v[48:51], v[16:19]\n"
"ds_read_b128 v[40:43], v4 offset:28672\n"
"v_mfma_f32_16x16x32_bf16 v[16:19], a[216:219], v[52:55], v[16:19]\n"
"v_mfma_f32_16x16x32_bf16 v[20:23], a[220:223], v[52:55], v[20:23]\n"
"ds_read_b128 v[44:47], v4 offset:43008\n"
"v_mfma_f32_16x16x32_bf16 v[20:23], a[108:111], v[48:51], v[20:23]\n"
"s_waitcnt lgkmcnt(4)\n"
"v_mfma_f32_16x16x32_bf16 v[20:23], v[140:143], v[56:59], v[20:23]\n"
"ds_read_b128 v[48:51], v4 offset:1024\n"
"v_mfma_f32_16x16x32_bf16 v[20:23], v[252:255], v[60:63], v[20:23]\n"
"v_mfma_f32_16x16x32_bf16 v[16:19], v[248:251], v[60:63], v[16:19]\n"
"ds_read_b128 v[52:55], v4 offset:15360\n"
"v_mfma_f32_16x16x32_bf16 v[16:19], v[136:139], v[56:59], v[16:19]\n"
"s_branch .L_dbl_1\n"
".L_dbt_1:\n"
"s_nop 1\n"
"s_nop 0\n"
"v_cvt_pk_bf16_f32 v15, v30, v31\n"
"v_cvt_pk_bf16_f32 v14, v28, v29\n"
"s_nop 1\n"
"v_cvt_pk_bf16_f32 v13, v26, v27\n"
"v_cvt_pk_bf16_f32 v12, v24, v25\n"
"ds_write_b128 v5, v[12:15]\n"
"ds_read2st64_b32 v[12:13], v3 offset0:0 offset1:1\n"
"ds_read2st64_b32 v[14:15], v3 offset0:2 offset1:3\n"
"s_waitcnt lgkmcnt(0)\n"
"buffer_atomic_pk_add_bf16 v12, v1, s[44:47], s65 offen offset:0 sc1\n"
"buffer_atomic_pk_add_bf16 v13, v1, s[44:47], s65 offen offset:2048 sc1\n"
"s_add_u32 s65, s65, 16384\n"
"buffer_atomic_pk_add_bf16 v14, v1, s[44:47], s65 offen offset:0 sc1\n"
"buffer_atomic_pk_add_bf16 v15, v1, s[44:47], s65 offen offset:2048 sc1\n"
"s_add_u32 s65, s65, 16384\n"
"s_or_b32 s48, s28, s27\n"
"s_or_b32 s48, s48, s26\n"
"s_cbranch_scc1 .L_end\n"
"v_mov_b32 v8, s54\n"
"v_mov_b32 v9, s55\n"
"buffer_store_dwordx2 v[8:9], off, s[32:35], 0 sc1\n"
"s_branch .L_end\n"
".p2align 6\n"
".L_dhv2:\n"
".L_dal_2:\n"
"s_waitcnt lgkmcnt(4)\n"
"v_mfma_f32_16x16x32_bf16 v[16:19], a[0:3], v[32:35], 0\n"
"ds_read_b128 v[56:59], v2 offset:29696\n"
"ds_read_b128 v[12:15], v5 offset:8192\n"
"v_mfma_f32_16x16x32_bf16 v[16:19], a[112:115], v[36:39], v[16:19]\n"
"v_mfma_f32_16x16x32_bf16 v[20:23], a[116:119], v[36:39], 0\n"
"ds_read_b128 v[60:63], v2 offset:44032\n"
"v_mfma_f32_16x16x32_bf16 v[20:23], a[4:7], v[32:35], v[20:23]\n"
"s_waitcnt lgkmcnt(5)\n"
"v_mfma_f32_16x16x32_bf16 v[20:23], a[228:231], v[40:43], v[20:23]\n"
"ds_read_b128 v[32:35], v2 offset:2048\n"
"v_mfma_f32_16x16x32_bf16 v[20:23], v[148:151], v[44:47], v[20:23]\n"
"s_waitcnt lgkmcnt(2)\n"
"v_add_f32 v24, v24, v12\n"
"v_mfma_f32_16x16x32_bf16 v[16:19], v[144:147], v[44:47], v[16:19]\n"
"ds_read_b128 v[36:39], v2 offset:16384\n"
"v_add_f32 v25, v25, v13\n"
"v_mfma_f32_16x16x32_bf16 v[16:19], a[224:227], v[40:43], v[16:19]\n"
"v_add_f32 v26, v26, v14\n"
"v_mfma_f32_16x16x32_bf16 v[16:19], a[8:11], v[48:51], v[16:19]\n"
"ds_read_b128 v[40:43], v2 offset:30720\n"
"v_add_f32 v27, v27, v15\n"
"v_mfma_f32_16x16x32_bf16 v[16:19], a[120:123], v[52:55], v[16:19]\n"
"ds_read_b128 v[12:15], v5 offset:9216\n"
"v_mfma_f32_16x16x32_bf16 v[20:23], a[124:127], v[52:55], v[20:23]\n"
"ds_read_b128 v[44:47], v2 offset:45056\n"
"v_mfma_f32_16x16x32_bf16 v[20:23], a[12:15], v[48:51], v[20:23]\n"
"s_waitcnt lgkmcnt(5)\n"
"v_mfma_f32_16x16x32_bf16 v[20:23], a[236:239], v[56:59], v[20:23]\n"
"ds_read_b128 v[48:51], v2 offset:3072\n"
"v_mfma_f32_16x16x32_bf16 v[20:23], v[156:159], v[60:63], v[20:23]\n"
"v_mfma_f32_16x16x32_bf16 v[16:19], v[152:155], v[60:63], v[16:19]\n"
"ds_read_b128 v[52:55], v2 offset:17408\n"
"s_waitcnt lgkmcnt(3)\n"
"v_mfma_f32_16x16x32_bf16 v[16:19], a[232:235], v[56:59], v[16:19]\n"
"v_add_f32 v28, v28, v12\n"
"v_mfma_f32_16x16x32_bf16 v[16:19], a[16:19], v[32:35], v[16:19]\n"
"ds_read_b128 v[56:59], v2 offset:31744\n"
"v_add_f32 v29, v29, v13\n"
"v_mfma_f32_16x16x32_bf16 v[16:19], a[128:131], v[36:39], v[16:19]\n"
"s_waitcnt vmcnt(0)\n"
"s_barrier\n"
"v_mfma_f32_16x16x32_bf16 v[20:23], a[132:135], v[36:39], v[20:23]\n"
"ds_read_b128 v[60:63], v2 offset:46080\n"
"v_add_f32 v30, v30, v14\n"
"v_mfma_f32_16x16x32_bf16 v[20:23], a[20:23], v[32:35], v[20:23]\n"
"v_add_f32 v31, v31, v15\n"
"s_waitcnt lgkmcnt(4)\n"
"v_mfma_f32_16x16x32_bf16 v[20:23], a[244:247], v[40:43], v[20:23]\n"
"ds_read_b128 v[32:35], v2 offset:4096\n"
"s_add_u32 m0, s64, 57344\n"
"v_mfma_f32_16x16x32_bf16 v[20:23], v[164:167], v[44:47], v[20:23]\n"
"buffer_load_dwordx4 v2, s[32:35], s63 offen offset:0 sc1 lds\n"
"v_cvt_pk_bf16_f32 v12, v24, v25\n"
"v_mfma_f32_16x16x32_bf16 v[16:19], v[160:163], v[44:47], v[16:19]\n"
"ds_read_b128 v[36:39], v2 offset:18432\n"
"v_cvt_pk_bf16_f32 v13, v26, v27\n"
"v_mfma_f32_16x16x32_bf16 v[16:19], a[240:243], v[40:43], v[16:19]\n"
"v_cvt_pk_bf16_f32 v14, v28, v29\n"
"s_waitcnt lgkmcnt(4)\n"
"v_mfma_f32_16x16x32_bf16 v[16:19], a[24:27], v[48:51], v[16:19]\n"
"ds_read_b128 v[40:43], v2 offset:32768\n"
"v_cvt_pk_bf16_f32 v15, v30, v31\n"
"v_mfma_f32_16x16x32_bf16 v[16:19], a[136:139], v[52:55], v[16:19]\n"
"buffer_store_dwordx4 v[12:15], v2, s[32:35], s68 offen sc1\n"
"v_add_f32 v10, v24, v25\n"
"v_mfma_f32_16x16x32_bf16 v[20:23], a[140:143], v[52:55], v[20:23]\n"
"ds_read_b128 v[44:47], v2 offset:47104\n"
"v_mul_f32 v11, v24, v24\n"
"v_mfma_f32_16x16x32_bf16 v[20:23], a[28:31], v[48:51], v[20:23]\n"
"v_fmac_f32 v11, v25, v25\n"
"s_waitcnt lgkmcnt(4)\n"
"v_mfma_f32_16x16x32_bf16 v[20:23], a[252:255], v[56:59], v[20:23]\n"
"ds_read_b128 v[48:51], v2 offset:5120\n"
"v_add_f32 v10, v10, v26\n"
"v_mfma_f32_16x16x32_bf16 v[20:23], v[172:175], v[60:63], v[20:23]\n"
"buffer_load_dwordx4 v2, s[32:35], s63 offen offset:1024 sc1 lds\n"
"v_fmac_f32 v11, v26, v26\n"
"v_mfma_f32_16x16x32_bf16 v[16:19], v[168:171], v[60:63], v[16:19]\n"
"ds_read_b128 v[52:55], v2 offset:19456\n"
"v_add_f32 v10, v10, v27\n"
"v_mfma_f32_16x16x32_bf16 v[16:19], a[248:251], v[56:59], v[16:19]\n"
"v_fmac_f32 v11, v27, v27\n"
"s_waitcnt lgkmcnt(4)\n"
"v_mfma_f32_16x16x32_bf16 v[16:19], a[32:35], v[32:35], v[16:19]\n"
"ds_read_b128 v[56:59], v2 offset:33792\n"
"v_add_f32 v10, v10, v28\n"
"v_mfma_f32_16x16x32_bf16 v[16:19], a[144:147], v[36:39], v[16:19]\n"
"buffer_load_dwordx4 v2, s[32:35], s63 offen offset:2048 sc1 lds\n"
"v_fmac_f32 v11, v28, v28\n"
"v_mfma_f32_16x16x32_bf16 v[20:23], a[148:151], v[36:39], v[20:23]\n"
"ds_read_b128 v[60:63], v2 offset:48128\n"
"v_add_f32 v10, v10, v29\n"
"v_mfma_f32_16x16x32_bf16 v[20:23], a[36:39], v[32:35], v[20:23]\n"
"v_fmac_f32 v11, v29, v29\n"
"s_waitcnt lgkmcnt(4)\n"
"v_mfma_f32_16x16x32_bf16 v[20:23], v[68:71], v[40:43], v[20:23]\n"
"ds_read_b128 v[32:35], v2 offset:6144\n"
"v_add_f32 v10, v10, v30\n"
"v_mfma_f32_16x16x32_bf16 v[20:23], v[180:183], v[44:47], v[20:23]\n"
"buffer_load_dwordx4 v2, s[32:35], s63 offen offset:3072 sc1 lds\n"
"s_add_u32 m0, s74, 0\n"
"v_mfma_f32_16x16x32_bf16 v[16:19], v[176:179], v[44:47], v[16:19]\n"
"ds_read_b128 v[36:39], v2 offset:20480\n"
"v_fmac_f32 v11, v30, v30\n"
"v_mfma_f32_16x16x32_bf16 v[16:19], v[64:67], v[40:43], v[16:19]\n"
"v_add_f32 v10, v10, v31\n"
"s_waitcnt lgkmcnt(4)\n"
"v_mfma_f32_16x16x32_bf16 v[16:19], a[40:43], v[48:51], v[16:19]\n"
"ds_read_b128 v[40:43], v2 offset:34816\n"
"v_fmac_f32 v11, v31, v31\n"
"v_mfma_f32_16x16x32_bf16 v[16:19], a[152:155], v[52:55], v[16:19]\n"
"buffer_load_dwordx4 v2, s[32:35], s71 offen offset:0 sc1 lds\n"
"v_permlane16_swap_b32 v10, v11\n"
"v_mfma_f32_16x16x32_bf16 v[20:23], a[156:159], v[52:55], v[20:23]\n"
"ds_read_b128 v[44:47], v2 offset:49152\n"
"v_add_f32 v10, v10, v11\n"
"v_mfma_f32_16x16x32_bf16 v[20:23], a[44:47], v[48:51], v[20:23]\n"
"s_waitcnt lgkmcnt(4)\n"
"v_mfma_f32_16x16x32_bf16 v[20:23], v[76:79], v[56:59], v[20:23]\n"
"ds_read_b128 v[48:51], v2 offset:7168\n"
"v_permlane32_swap_b32 v10, v11\n"
"v_mfma_f32_16x16x32_bf16 v[20:23], v[188:191], v[60:63], v[20:23]\n"
"buffer_load_dwordx4 v2, s[32:35], s71 offen offset:1024 sc1 lds\n"
"s_add_u32 s71, s71, 1187840\n"
"v_mfma_f32_16x16x32_bf16 v[16:19], v[184:187], v[60:63], v[16:19]\n"
"ds_read_b128 v[52:55], v2 offset:21504\n"
"s_add_u32 s63, s63, 4096\n"
"v_mfma_f32_16x16x32_bf16 v[16:19], v[72:75], v[56:59], v[16:19]\n"
"s_add_u32 m0, s64, 61440\n"
"s_waitcnt lgkmcnt(4)\n"
"v_mfma_f32_16x16x32_bf16 v[16:19], a[48:51], v[32:35], v[16:19]\n"
"ds_read_b128 v[56:59], v2 offset:35840\n"
"v_add_f32 v10, v10, v11\n"
"v_mfma_f32_16x16x32_bf16 v[16:19], a[160:163], v[36:39], v[16:19]\n"
"buffer_store_dword v10, v3, s[32:35], s68 offen offset:1024 sc1\n"
"s_mov_b32 s67, s68\n"
"v_mfma_f32_16x16x32_bf16 v[20:23], a[164:167], v[36:39], v[20:23]\n"
"ds_read_b128 v[60:63], v2 offset:50176\n"
"s_mov_b32 s68, s70\n"
"v_mfma_f32_16x16x32_bf16 v[20:23], a[52:55], v[32:35], v[20:23]\n"
"s_add_u32 s70, s70, 1187840\n"
"s_waitcnt lgkmcnt(4)\n"
"v_mfma_f32_16x16x32_bf16 v[20:23], v[84:87], v[40:43], v[20:23]\n"
"ds_read_b128 v[32:35], v2 offset:8192\n"
"v_mfma_f32_16x16x32_bf16 v[20:23], v[196:199], v[44:47], v[20:23]\n"
"buffer_load_dwordx4 v2, s[32:35], s63 offen offset:0 sc1 lds\n"
"v_mfma_f32_16x16x32_bf16 v[16:19], v[192:195], v[44:47], v[16:19]\n"
"ds_read_b128 v[36:39], v2 offset:22528\n"
"v_mfma_f32_16x16x32_bf16 v[16:19], v[80:83], v[40:43], v[16:19]\n"
"s_waitcnt lgkmcnt(4)\n"
"v_mfma_f32_16x16x32_bf16 v[16:19], a[56:59], v[48:51], v[16:19]\n"
"ds_read_b128 v[40:43], v2 offset:36864\n"
"v_mfma_f32_16x16x32_bf16 v[16:19], a[168:171], v[52:55], v[16:19]\n"
"buffer_load_dwordx4 v2, s[32:35], s63 offen offset:1024 sc1 lds\n"
"v_mfma_f32_16x16x32_bf16 v[20:23], a[172:175], v[52:55], v[20:23]\n"
"ds_read_b128 v[44:47], v2 offset:51200\n"
"v_mfma_f32_16x16x32_bf16 v[20:23], a[60:63], v[48:51], v[20:23]\n"
"s_waitcnt lgkmcnt(4)\n"
"v_mfma_f32_16x16x32_bf16 v[20:23], v[92:95], v[56:59], v[20:23]\n"
"ds_read_b128 v[48:51], v2 offset:9216\n"
"v_mfma_f32_16x16x32_bf16 v[20:23], v[204:207], v[60:63], v[20:23]\n"
"buffer_load_dwordx4 v2, s[32:35], s63 offen offset:2048 sc1 lds\n"
"v_mfma_f32_16x16x32_bf16 v[16:19], v[200:203], v[60:63], v[16:19]\n"
"ds_read_b128 v[52:55], v2 offset:23552\n"
"v_mfma_f32_16x16x32_bf16 v[16:19], v[88:91], v[56:59], v[16:19]\n"
"s_waitcnt lgkmcnt(4)\n"
"v_mfma_f32_16x16x32_bf16 v[16:19], a[64:67], v[32:35], v[16:19]\n"
"ds_read_b128 v[56:59], v2 offset:37888\n"
"v_mfma_f32_16x16x32_bf16 v[16:19], a[176:179], v[36:39], v[16:19]\n"
"buffer_load_dwordx4 v2, s[32:35], s63 offen offset:3072 sc1 lds\n"
"s_add_u32 s63, s63, 4096\n"
"v_mfma_f32_16x16x32_bf16 v[20:23], a[180:183], v[36:39], v[20:23]\n"
"ds_read_b128 v[60:63], v2 offset:52224\n"
"s_add_u32 m0, s64, 65536\n"
"v_mfma_f32_16x16x32_bf16 v[20:23], a[68:71], v[32:35], v[20:23]\n"
"s_waitcnt lgkmcnt(4)\n"
"v_mfma_f32_16x16x32_bf16 v[20:23], v[100:103], v[40:43], v[20:23]\n"
"ds_read_b128 v[32:35], v2 offset:10240\n"
"v_mfma_f32_16x16x32_bf16 v[20:23], v[212:215], v[44:47], v[20:23]\n"
"buffer_load_dwordx4 v2, s[32:35], s63 offen offset:0 sc1 lds\n"
"v_mfma_f32_16x16x32_bf16 v[16:19], v[208:211], v[44:47], v[16:19]\n"
"ds_read_b128 v[36:39], v2 offset:24576\n"
"v_mfma_f32_16x16x32_bf16 v[16:19], v[96:99], v[40:43], v[16:19]\n"
"s_waitcnt lgkmcnt(4)\n"
"v_mfma_f32_16x16x32_bf16 v[16:19], a[72:75], v[48:51], v[16:19]\n"
"ds_read_b128 v[40:43], v2 offset:38912\n"
"v_mfma_f32_16x16x32_bf16 v[16:19], a[184:187], v[52:55], v[16:19]\n"
"buffer_load_dwordx4 v2, s[32:35], s63 offen offset:1024 sc1 lds\n"
"v_mfma_f32_16x16x32_bf16 v[20:23], a[188:191], v[52:55], v[20:23]\n"
"ds_read_b128 v[44:47], v2 offset:53248\n"
"v_mfma_f32_16x16x32_bf16 v[20:23], a[76:79], v[48:51], v[20:23]\n"
"s_waitcnt lgkmcnt(4)\n"
"v_mfma_f32_16x16x32_bf16 v[20:23], v[108:111], v[56:59], v[20:23]\n"
"ds_read_b128 v[48:51], v2 offset:11264\n"
"v_mfma_f32_16x16x32_bf16 v[20:23], v[220:223], v[60:63], v[20:23]\n"
"buffer_load_dwordx4 v2, s[32:35], s63 offen offset:2048 sc1 lds\n"
"v_mfma_f32_16x16x32_bf16 v[16:19], v[216:219], v[60:63], v[16:19]\n"
"ds_read_b128 v[52:55], v2 offset:25600\n"
"v_mfma_f32_16x16x32_bf16 v[16:19], v[104:107], v[56:59], v[16:19]\n"
"s_waitcnt lgkmcnt(4)\n"
"v_mfma_f32_16x16x32_bf16 v[16:19], a[80:83], v[32:35], v[16:19]\n"
"ds_read_b128 v[56:59], v2 offset:39936\n"
"v_mfma_f32_16x16x32_bf16 v[16:19], a[192:195], v[36:39], v[16:19]\n"
"buffer_load_dwordx4 v2, s[32:35], s63 offen offset:3072 sc1 lds\n"
"s_add_u32 s63, s63, 4096\n"
"v_mfma_f32_16x16x32_bf16 v[20:23], a[196:199], v[36:39], v[20:23]\n"
"ds_read_b128 v[60:63], v2 offset:54272\n"
"s_add_u32 m0, s64, 69632\n"
"v_mfma_f32_16x16x32_bf16 v[20:23], a[84:87], v[32:35], v[20:23]\n"
"s_waitcnt lgkmcnt(4)\n"
"v_mfma_f32_16x16x32_bf16 v[20:23], v[116:119], v[40:43], v[20:23]\n"
"ds_read_b128 v[32:35], v2 offset:12288\n"
"v_mfma_f32_16x16x32_bf16 v[20:23], v[228:231], v[44:47], v[20:23]\n"
"buffer_load_dwordx4 v2, s[32:35], s63 offen offset:0 sc1 lds\n"
"v_mfma_f32_16x16x32_bf16 v[16:19], v[224:227], v[44:47], v[16:19]\n"
"ds_read_b128 v[36:39], v2 offset:26624\n"
"v_mfma_f32_16x16x32_bf16 v[16:19], v[112:115], v[40:43], v[16:19]\n"
"s_waitcnt lgkmcnt(4)\n"
"v_mfma_f32_16x16x32_bf16 v[16:19], a[88:91], v[48:51], v[16:19]\n"
"ds_read_b128 v[40:43], v2 offset:40960\n"
"v_mfma_f32_16x16x32_bf16 v[16:19], a[200:203], v[52:55], v[16:19]\n"
"buffer_load_dwordx4 v2, s[32:35], s63 offen offset:1024 sc1 lds\n"
"s_add_u32 s63, s63, 1175552\n"
"v_mfma_f32_16x16x32_bf16 v[20:23], a[204:207], v[52:55], v[20:23]\n"
"ds_read_b128 v[44:47], v2 offset:55296\n"
"v_mfma_f32_16x16x32_bf16 v[20:23], a[92:95], v[48:51], v[20:23]\n"
"s_waitcnt lgkmcnt(4)\n"
"v_mfma_f32_16x16x32_bf16 v[20:23], v[124:127], v[56:59], v[20:23]\n"
"ds_read_b128 v[48:51], v2 offset:13312\n"
"v_mfma_f32_16x16x32_bf16 v[20:23], v[236:239], v[60:63], v[20:23]\n"
"v_mfma_f32_16x16x32_bf16 v[16:19], v[232:235], v[60:63], v[16:19]\n"
"ds_read_b128 v[52:55], v2 offset:27648\n"
"v_mfma_f32_16x16x32_bf16 v[16:19], v[120:123], v[56:59], v[16:19]\n"
"s_waitcnt lgkmcnt(4)\n"
"v_mfma_f32_16x16x32_bf16 v[16:19], a[96:99], v[32:35], v[16:19]\n"
"ds_read_b128 v[56:59], v2 offset:41984\n"
"v_mfma_f32_16x16x32_bf16 v[16:19], a[208:211], v[36:39], v[16:19]\n"
"s_waitcnt vmcnt(13)\n"
"s_waitcnt vmcnt(10)\n"
"s_barrier\n"
"v_mfma_f32_16x16x32_bf16 v[20:23], a[212:215], v[36:39], v[20:23]\n"
"ds_read_b128 v[60:63], v2 offset:56320\n"
"v_mfma_f32_16x16x32_bf16 v[20:23], a[100:103], v[32:35], v[20:23]\n"
"s_waitcnt lgkmcnt(4)\n"
"v_mfma_f32_16x16x32_bf16 v[20:23], v[132:135], v[40:43], v[20:23]\n"
"ds_read_b128 v[32:35], v4 offset:0\n"
"v_mfma_f32_16x16x32_bf16 v[20:23], v[244:247], v[44:47], v[20:23]\n"
"v_mfma_f32_16x16x32_bf16 v[16:19], v[240:243], v[44:47], v[16:19]\n"
"ds_read_b128 v[36:39], v4 offset:14336\n"
"v_mfma_f32_16x16x32_bf16 v[16:19], v[128:131], v[40:43], v[16:19]\n"
"s_waitcnt lgkmcnt(4)\n"
"v_mfma_f32_16x16x32_bf16 v[16:19], a[104:107], v[48:51], v[16:19]\n"
"ds_read_b128 v[40:43], v4 offset:28672\n"
"v_mfma_f32_16x16x32_bf16 v[16:19], a[216:219], v[52:55], v[16:19]\n"
"v_mfma_f32_16x16x32_bf16 v[20:23], a[220:223], v[52:55], v[20:23]\n"
"ds_read_b128 v[44:47], v4 offset:43008\n"
"v_mfma_f32_16x16x32_bf16 v[20:23], a[108:111], v[48:51], v[20:23]\n"
"s_waitcnt lgkmcnt(4)\n"
"v_mfma_f32_16x16x32_bf16 v[20:23], v[140:143], v[56:59], v[20:23]\n"
"ds_read_b128 v[48:51], v4 offset:1024\n"
"v_mfma_f32_16x16x32_bf16 v[20:23], v[252:255], v[60:63], v[20:23]\n"
"v_mfma_f32_16x16x32_bf16 v[16:19], v[248:251], v[60:63], v[16:19]\n"
"ds_read_b128 v[52:55], v4 offset:15360\n"
"s_sub_u32 s62, s62, 1\n"
"v_mfma_f32_16x16x32_bf16 v[16:19], v[136:139], v[56:59], v[16:19]\n"
"s_cbranch_scc1 .L_dab_2\n"
"s_waitcnt lgkmcnt(4)\n"
"v_mfma_f32_16x16x32_bf16 v[24:27], a[0:3], v[32:35], 0\n"
"ds_read_b128 v[56:59], v4 offset:29696\n"
"ds_read_b128 v[12:15], v5 offset:0\n"
"v_mfma_f32_16x16x32_bf16 v[24:27], a[112:115], v[36:39], v[24:27]\n"
"v_mfma_f32_16x16x32_bf16 v[28:31], a[116:119], v[36:39], 0\n"
"ds_read_b128 v[60:63], v4 offset:44032\n"
"v_mfma_f32_16x16x32_bf16 v[28:31], a[4:7], v[32:35], v[28:31]\n"
"s_waitcnt lgkmcnt(5)\n"
"v_mfma_f32_16x16x32_bf16 v[28:31], a[228:231], v[40:43], v[28:31]\n"
"ds_read_b128 v[32:35], v4 offset:2048\n"
"v_mfma_f32_16x16x32_bf16 v[28:31], v[148:151], v[44:47], v[28:31]\n"
"s_waitcnt lgkmcnt(2)\n"
"v_add_f32 v16, v16, v12\n"
"v_mfma_f32_16x16x32_bf16 v[24:27], v[144:147], v[44:47], v[24:27]\n"
"ds_read_b128 v[36:39], v4 offset:16384\n"
"v_add_f32 v17, v17, v13\n"
"v_mfma_f32_16x16x32_bf16 v[24:27], a[224:227], v[40:43], v[24:27]\n"
"v_add_f32 v18, v18, v14\n"
"v_mfma_f32_16x16x32_bf16 v[24:27], a[8:11], v[48:51], v[24:27]\n"
"ds_read_b128 v[40:43], v4 offset:30720\n"
"v_add_f32 v19, v19, v15\n"
"v_mfma_f32_16x16x32_bf16 v[24:27], a[120:123], v[52:55], v[24:27]\n"
"ds_read_b128 v[12:15], v5 offset:1024\n"
"v_mfma_f32_16x16x32_bf16 v[28:31], a[124:127], v[52:55], v[28:31]\n"
"ds_read_b128 v[44:47], v4 offset:45056\n"
"v_mfma_f32_16x16x32_bf16 v[28:31], a[12:15], v[48:51], v[28:31]\n"
"s_waitcnt lgkmcnt(5)\n"
"v_mfma_f32_16x16x32_bf16 v[28:31], a[236:239], v[56:59], v[28:31]\n"
"ds_read_b128 v[48:51], v4 offset:3072\n"
"v_mfma_f32_16x16x32_bf16 v[28:31], v[156:159], v[60:63], v[28:31]\n"
"v_mfma_f32_16x16x32_bf16 v[24:27], v[152:155], v[60:63], v[24:27]\n"
"ds_read_b128 v[52:55], v4 offset:17408\n"
"s_waitcnt lgkmcnt(3)\n"
"v_mfma_f32_16x16x32_bf16 v[24:27], a[232:235], v[56:59], v[24:27]\n"
"v_add_f32 v20, v20, v12\n"
"v_mfma_f32_16x16x32_bf16 v[24:27], a[16:19], v[32:35], v[24:27]\n"
"ds_read_b128 v[56:59], v4 offset:31744\n"
"v_add_f32 v21, v21, v13\n"
"v_mfma_f32_16x16x32_bf16 v[24:27], a[128:131], v[36:39], v[24:27]\n"
"s_waitcnt vmcnt(0)\n"
"s_barrier\n"
"v_mfma_f32_16x16x32_bf16 v[28:31], a[132:135], v[36:39], v[28:31]\n"
"ds_read_b128 v[60:63], v4 offset:46080\n"
"v_add_f32 v22, v22, v14\n"
"v_mfma_f32_16x16x32_bf16 v[28:31], a[20:23], v[32:35], v[28:31]\n"
"v_add_f32 v23, v23, v15\n"
"s_waitcnt lgkmcnt(4)\n"
"v_mfma_f32_16x16x32_bf16 v[28:31], a[244:247], v[40:43], v[28:31]\n"
"ds_read_b128 v[32:35], v4 offset:4096\n"
"s_add_u32 m0, s64, 0\n"
"v_mfma_f32_16x16x32_bf16 v[28:31], v[164:167], v[44:47], v[28:31]\n"
"buffer_load_dwordx4 v2, s[32:35], s63 offen offset:0 sc1 lds\n"
"v_cvt_pk_bf16_f32 v12, v16, v17\n"
"v_mfma_f32_16x16x32_bf16 v[24:27], v[160:163], v[44:47], v[24:27]\n"
"ds_read_b128 v[36:39], v4 offset:18432\n"
"v_cvt_pk_bf16_f32 v13, v18, v19\n"
"v_mfma_f32_16x16x32_bf16 v[24:27], a[240:243], v[40:43], v[24:27]\n"
"v_cvt_pk_bf16_f32 v14, v20, v21\n"
"s_waitcnt lgkmcnt(4)\n"
"v_mfma_f32_16x16x32_bf16 v[24:27], a[24:27], v[48:51], v[24:27]\n"
"ds_read_b128 v[40:43], v4 offset:32768\n"
"v_cvt_pk_bf16_f32 v15, v22, v23\n"
"v_mfma_f32_16x16x32_bf16 v[24:27], a[136:139], v[52:55], v[24:27]\n"
"buffer_store_dwordx4 v[12:15], v2, s[32:35], s69 offen sc1\n"
"v_add_f32 v10, v16, v17\n"
"v_mfma_f32_16x16x32_bf16 v[28:31], a[140:143], v[52:55], v[28:31]\n"
"ds_read_b128 v[44:47], v4 offset:47104\n"
"v_mul_f32 v11, v16, v16\n"
"v_mfma_f32_16x16x32_bf16 v[28:31], a[28:31], v[48:51], v[28:31]\n"
"v_fmac_f32 v11, v17, v17\n"
"s_waitcnt lgkmcnt(4)\n"
"v_mfma_f32_16x16x32_bf16 v[28:31], a[252:255], v[56:59], v[28:31]\n"
"ds_read_b128 v[48:51], v4 offset:5120\n"
"v_add_f32 v10, v10, v18\n"
"v_mfma_f32_16x16x32_bf16 v[28:31], v[172:175], v[60:63], v[28:31]\n"
"buffer_load_dwordx4 v2, s[32:35], s63 offen offset:1024 sc1 lds\n"
"v_fmac_f32 v11, v18, v18\n"
"v_mfma_f32_16x16x32_bf16 v[24:27], v[168:171], v[60:63], v[24:27]\n"
"ds_read_b128 v[52:55], v4 offset:19456\n"
"v_add_f32 v10, v10, v19\n"
"v_mfma_f32_16x16x32_bf16 v[24:27], a[248:251], v[56:59], v[24:27]\n"
"v_fmac_f32 v11, v19, v19\n"
"s_waitcnt lgkmcnt(4)\n"
"v_mfma_f32_16x16x32_bf16 v[24:27], a[32:35], v[32:35], v[24:27]\n"
"ds_read_b128 v[56:59], v4 offset:33792\n"
"v_add_f32 v10, v10, v20\n"
"v_mfma_f32_16x16x32_bf16 v[24:27], a[144:147], v[36:39], v[24:27]\n"
"buffer_load_dwordx4 v2, s[32:35], s63 offen offset:2048 sc1 lds\n"
"v_fmac_f32 v11, v20, v20\n"
"v_mfma_f32_16x16x32_bf16 v[28:31], a[148:151], v[36:39], v[28:31]\n"
"ds_read_b128 v[60:63], v4 offset:48128\n"
"v_add_f32 v10, v10, v21\n"
"v_mfma_f32_16x16x32_bf16 v[28:31], a[36:39], v[32:35], v[28:31]\n"
"v_fmac_f32 v11, v21, v21\n"
"s_waitcnt lgkmcnt(4)\n"
"v_mfma_f32_16x16x32_bf16 v[28:31], v[68:71], v[40:43], v[28:31]\n"
"ds_read_b128 v[32:35], v4 offset:6144\n"
"v_add_f32 v10, v10, v22\n"
"v_mfma_f32_16x16x32_bf16 v[28:31], v[180:183], v[44:47], v[28:31]\n"
"buffer_load_dwordx4 v2, s[32:35], s63 offen offset:3072 sc1 lds\n"
"s_add_u32 m0, s74, 8192\n"
"v_mfma_f32_16x16x32_bf16 v[24:27], v[176:179], v[44:47], v[24:27]\n"
"ds_read_b128 v[36:39], v4 offset:20480\n"
"v_fmac_f32 v11, v22, v22\n"
"v_mfma_f32_16x16x32_bf16 v[24:27], v[64:67], v[40:43], v[24:27]\n"
"v_add_f32 v10, v10, v23\n"
"s_waitcnt lgkmcnt(4)\n"
"v_mfma_f32_16x16x32_bf16 v[24:27], a[40:43], v[48:51], v[24:27]\n"
"ds_read_b128 v[40:43], v4 offset:34816\n"
"v_fmac_f32 v11, v23, v23\n"
"v_mfma_f32_16x16x32_bf16 v[24:27], a[152:155], v[52:55], v[24:27]\n"
"buffer_load_dwordx4 v2, s[32:35], s71 offen offset:0 sc1 lds\n"
"v_permlane16_swap_b32 v10, v11\n"
"v_mfma_f32_16x16x32_bf16 v[28:31], a[156:159], v[52:55], v[28:31]\n"
"ds_read_b128 v[44:47], v4 offset:49152\n"
"v_add_f32 v10, v10, v11\n"
"v_mfma_f32_16x16x32_bf16 v[28:31], a[44:47], v[48:51], v[28:31]\n"
"s_waitcnt lgkmcnt(4)\n"
"v_mfma_f32_16x16x32_bf16 v[28:31], v[76:79], v[56:59], v[28:31]\n"
"ds_read_b128 v[48:51], v4 offset:7168\n"
"v_permlane32_swap_b32 v10, v11\n"
"v_mfma_f32_16x16x32_bf16 v[28:31], v[188:191], v[60:63], v[28:31]\n"
"buffer_load_dwordx4 v2, s[32:35], s71 offen offset:1024 sc1 lds\n"
"s_add_u32 s71, s71, 1187840\n"
"v_mfma_f32_16x16x32_bf16 v[24:27], v[184:187], v[60:63], v[24:27]\n"
"ds_read_b128 v[52:55], v4 offset:21504\n"
"s_add_u32 s63, s63, 4096\n"
"v_mfma_f32_16x16x32_bf16 v[24:27], v[72:75], v[56:59], v[24:27]\n"
"s_add_u32 m0, s64, 4096\n"
"s_waitcnt lgkmcnt(4)\n"
"v_mfma_f32_16x16x32_bf16 v[24:27], a[48:51], v[32:35], v[24:27]\n"
"ds_read_b128 v[56:59], v4 offset:35840\n"
"v_add_f32 v10, v10, v11\n"
"v_mfma_f32_16x16x32_bf16 v[24:27], a[160:163], v[36:39], v[24:27]\n"
"buffer_store_dword v10, v3, s[32:35], s69 offen offset:1024 sc1\n"
"s_mov_b32 s67, s69\n"
"v_mfma_f32_16x16x32_bf16 v[28:31], a[164:167], v[36:39], v[28:31]\n"
"ds_read_b128 v[60:63], v4 offset:50176\n"
"s_mov_b32 s69, s70\n"
"v_mfma_f32_16x16x32_bf16 v[28:31], a[52:55], v[32:35], v[28:31]\n"
"s_add_u32 s70, s70, 1187840\n"
"s_waitcnt lgkmcnt(4)\n"
"v_mfma_f32_16x16x32_bf16 v[28:31], v[84:87], v[40:43], v[28:31]\n"
"ds_read_b128 v[32:35], v4 offset:8192\n"
"v_mfma_f32_16x16x32_bf16 v[28:31], v[196:199], v[44:47], v[28:31]\n"
"buffer_load_dwordx4 v2, s[32:35], s63 offen offset:0 sc1 lds\n"
"v_mfma_f32_16x16x32_bf16 v[24:27], v[192:195], v[44:47], v[24:27]\n"
"ds_read_b128 v[36:39], v4 offset:22528\n"
"v_mfma_f32_16x16x32_bf16 v[24:27], v[80:83], v[40:43], v[24:27]\n"
"s_waitcnt lgkmcnt(4)\n"
"v_mfma_f32_16x16x32_bf16 v[24:27], a[56:59], v[48:51], v[24:27]\n"
"ds_read_b128 v[40:43], v4 offset:36864\n"
"v_mfma_f32_16x16x32_bf16 v[24:27], a[168:171], v[52:55], v[24:27]\n"
"buffer_load_dwordx4 v2, s[32:35], s63 offen offset:1024 sc1 lds\n"
"v_mfma_f32_16x16x32_bf16 v[28:31], a[172:175], v[52:55], v[28:31]\n"
"ds_read_b128 v[44:47], v4 offset:51200\n"
"v_mfma_f32_16x16x32_bf16 v[28:31], a[60:63], v[48:51], v[28:31]\n"
"s_waitcnt lgkmcnt(4)\n"
"v_mfma_f32_16x16x32_bf16 v[28:31], v[92:95], v[56:59], v[28:31]\n"
"ds_read_b128 v[48:51], v4 offset:9216\n"
"v_mfma_f32_16x16x32_bf16 v[28:31], v[204:207], v[60:63], v[28:31]\n"
"buffer_load_dwordx4 v2, s[32:35], s63 offen offset:2048 sc1 lds\n"
"v_mfma_f32_16x16x32_bf16 v[24:27], v[200:203], v[60:63], v[24:27]\n"
"ds_read_b128 v[52:55], v4 offset:23552\n"
"v_mfma_f32_16x16x32_bf16 v[24:27], v[88:91], v[56:59], v[24:27]\n"
"s_waitcnt lgkmcnt(4)\n"
"v_mfma_f32_16x16x32_bf16 v[24:27], a[64:67], v[32:35], v[24:27]\n"
"ds_read_b128 v[56:59], v4 offset:37888\n"
"v_mfma_f32_16x16x32_bf16 v[24:27], a[176:179], v[36:39], v[24:27]\n"
"buffer_load_dwordx4 v2, s[32:35], s63 offen offset:3072 sc1 lds\n"
"s_add_u32 s63, s63, 4096\n"
"v_mfma_f32_16x16x32_bf16 v[28:31], a[180:183], v[36:39], v[28:31]\n"
"ds_read_b128 v[60:63], v4 offset:52224\n"
"s_add_u32 m0, s64, 8192\n"
"v_mfma_f32_16x16x32_bf16 v[28:31], a[68:71], v[32:35], v[28:31]\n"
"s_waitcnt lgkmcnt(4)\n"
"v_mfma_f32_16x16x32_bf16 v[28:31], v[100:103], v[40:43], v[28:31]\n"
"ds_read_b128 v[32:35], v4 offset:10240\n"
"v_mfma_f32_16x16x32_bf16 v[28:31], v[212:215], v[44:47], v[28:31]\n"
"buffer_load_dwordx4 v2, s[32:35], s63 offen offset:0 sc1 lds\n"
"v_mfma_f32_16x16x32_bf16 v[24:27], v[208:211], v[44:47], v[24:27]\n"
"ds_read_b128 v[36:39], v4 offset:24576\n"
"v_mfma_f32_16x16x32_bf16 v[24:27], v[96:99], v[40:43], v[24:27]\n"
"s_waitcnt lgkmcnt(4)\n"
"v_mfma_f32_16x16x32_bf16 v[24:27], a[72:75], v[48:51], v[24:27]\n"
"ds_read_b128 v[40:43], v4 offset:38912\n"
"v_mfma_f32_16x16x32_bf16 v[24:27], a[184:187], v[52:55], v[24:27]\n"
"buffer_load_dwordx4 v2, s[32:35], s63 offen offset:1024 sc1 lds\n"
"v_mfma_f32_16x16x32_bf16 v[28:31], a[188:191], v[52:55], v[28:31]\n"
"ds_read_b128 v[44:47], v4 offset:53248\n"
"v_mfma_f32_16x16x32_bf16 v[28:31], a[76:79], v[48:51], v[28:31]\n"
"s_waitcnt lgkmcnt(4)\n"
"v_mfma_f32_16x16x32_bf16 v[28:31], v[108:111], v[56:59], v[28:31]\n"
"ds_read_b128 v[48:51], v4 offset:11264\n"
"v_mfma_f32_16x16x32_bf16 v[28:31], v[220:223], v[60:63], v[28:31]\n"
"buffer_load_dwordx4 v2, s[32:35], s63 offen offset:2048 sc1 lds\n"
"v_mfma_f32_16x16x32_bf16 v[24:27], v[216:219], v[60:63], v[24:27]\n"
"ds_read_b128 v[52:55], v4 offset:25600\n"
"v_mfma_f32_16x16x32_bf16 v[24:27], v[104:107], v[56:59], v[24:27]\n"
"s_waitcnt lgkmcnt(4)\n"
"v_mfma_f32_16x16x32_bf16 v[24:27], a[80:83], v[32:35], v[24:27]\n"
"ds_read_b128 v[56:59], v4 offset:39936\n"
"v_mfma_f32_16x16x32_bf16 v[24:27], a[192:195], v[36:39], v[24:27]\n"
"buffer_load_dwordx4 v2, s[32:35], s63 offen offset:3072 sc1 lds\n"
"s_add_u32 s63, s63, 4096\n"
"v_mfma_f32_16x16x32_bf16 v[28:31], a[196:199], v[36:39], v[28:31]\n"
"ds_read_b128 v[60:63], v4 offset:54272\n"
"s_add_u32 m0, s64, 12288\n"
"v_mfma_f32_16x16x32_bf16 v[28:31], a[84:87], v[32:35], v[28:31]\n"
"s_waitcnt lgkmcnt(4)\n"
"v_mfma_f32_16x16x32_bf16 v[28:31], v[116:119], v[40:43], v[28:31]\n"
"ds_read_b128 v[32:35], v4 offset:12288\n"
"v_mfma_f32_16x16x32_bf16 v[28:31], v[228:231], v[44:47], v[28:31]\n"
"buffer_load_dwordx4 v2, s[32:35], s63 offen offset:0 sc1 lds\n"
"v_mfma_f32_16x16x32_bf16 v[24:27], v[224:227], v[44:47], v[24:27]\n"
"ds_read_b128 v[36:39], v4 offset:26624\n"
"v_mfma_f32_16x16x32_bf16 v[24:27], v[112:115], v[40:43], v[24:27]\n"
"s_waitcnt lgkmcnt(4)\n"
"v_mfma_f32_16x16x32_bf16 v[24:27], a[88:91], v[48:51], v[24:27]\n"
"ds_read_b128 v[40:43], v4 offset:40960\n"
"v_mfma_f32_16x16x32_bf16 v[24:27], a[200:203], v[52:55], v[24:27]\n"
"buffer_load_dwordx4 v2, s[32:35], s63 offen offset:1024 sc1 lds\n"
"s_add_u32 s63, s63, 1175552\n"
"v_mfma_f32_16x16x32_bf16 v[28:31], a[204:207], v[52:55], v[28:31]\n"
"ds_read_b128 v[44:47], v4 offset:55296\n"
"v_mfma_f32_16x16x32_bf16 v[28:31], a[92:95], v[48:51], v[28:31]\n"
"s_waitcnt lgkmcnt(4)\n"
"v_mfma_f32_16x16x32_bf16 v[28:31], v[124:127], v[56:59], v[28:31]\n"
"ds_read_b128 v[48:51], v4 offset:13312\n"
"v_mfma_f32_16x16x32_bf16 v[28:31], v[236:239], v[60:63], v[28:31]\n"
"v_mfma_f32_16x16x32_bf16 v[24:27], v[232:235], v[60:63], v[24:27]\n"
"ds_read_b128 v[52:55], v4 offset:27648\n"
"v_mfma_f32_16x16x32_bf16 v[24:27], v[120:123], v[56:59], v[24:27]\n"
"s_waitcnt lgkmcnt(4)\n"
"v_mfma_f32_16x16x32_bf16 v[24:27], a[96:99], v[32:35], v[24:27]\n"
"ds_read_b128 v[56:59], v4 offset:41984\n"
"v_mfma_f32_16x16x32_bf16 v[24:27], a[208:211], v[36:39], v[24:27]\n"
"s_waitcnt vmcnt(13)\n"
"s_waitcnt vmcnt(10)\n"
"s_barrier\n"
"v_mfma_f32_16x16x32_bf16 v[28:31], a[212:215], v[36:39], v[28:31]\n"
"ds_read_b128 v[60:63], v4 offset:56320\n"
"v_mfma_f32_16x16x32_bf16 v[28:31], a[100:103], v[32:35], v[28:31]\n"
"s_waitcnt lgkmcnt(4)\n"
"v_mfma_f32_16x16x32_bf16 v[28:31], v[132:135], v[40:43], v[28:31]\n"
"ds_read_b128 v[32:35], v2 offset:0\n"
"v_mfma_f32_16x16x32_bf16 v[28:31], v[244:247], v[44:47], v[28:31]\n"
"v_mfma_f32_16x16x32_bf16 v[24:27], v[240:243], v[44:47], v[24:27]\n"
"ds_read_b128 v[36:39], v2 offset:14336\n"
"v_mfma_f32_16x16x32_bf16 v[24:27], v[128:131], v[40:43], v[24:27]\n"
"s_waitcnt lgkmcnt(4)\n"
"v_mfma_f32_16x16x32_bf16 v[24:27], a[104:107], v[48:51], v[24:27]\n"
"ds_read_b128 v[40:43], v2 offset:28672\n"
"v_mfma_f32_16x16x32_bf16 v[24:27], a[216:219], v[52:55], v[24:27]\n"
"v_mfma_f32_16x16x32_bf16 v[28:31], a[220:223], v[52:55], v[28:31]\n"
"ds_read_b128 v[44:47], v2 offset:43008\n"
"v_mfma_f32_16x16x32_bf16 v[28:31], a[108:111], v[48:51], v[28:31]\n"
"s_waitcnt lgkmcnt(4)\n"
"v_mfma_f32_16x16x32_bf16 v[28:31], v[140:143], v[56:59], v[28:31]\n"
"ds_read_b128 v[48:51], v2 offset:1024\n"
"v_mfma_f32_16x16x32_bf16 v[28:31], v[252:255], v[60:63], v[28:31]\n"
"v_mfma_f32_16x16x32_bf16 v[24:27], v[248:251], v[60:63], v[24:27]\n"
"ds_read_b128 v[52:55], v2 offset:15360\n"
"s_cmp_eq_u32 s62, 0\n"
"v_mfma_f32_16x16x32_bf16 v[24:27], v[136:139], v[56:59], v[24:27]\n"
"s_cbranch_scc0 .L_dal_2\n"
"s_mov_b32 s48, 0x7fff0000\n"
"v_writelane_b32 v8, 0, 63\n"
"v_writelane_b32 v9, 0, 63\n"
"v_writelane_b32 v6, 0, 63\n"
"v_writelane_b32 v7, 0, 63\n"
"v_writelane_b32 v0, s48, 63\n"
"s_branch .L_dal_2\n"
".L_dab_2:\n"
"s_mov_b32 s62, 15\n"
"s_add_u32 s74, s74, 8192\n"
"v_lshrrev_b32 v5, 8, v2\n"
"v_lshl_add_u32 v5, v5, 4, s74\n"
"v_and_b32 v3, 128, v2\n"
"v_lshl_add_u32 v5, v3, 2, v5\n"
"v_and_b32 v3, 16, v2\n"
"v_lshl_add_u32 v5, v3, 4, v5\n"
"v_and_b32 v3, 96, v2\n"
"v_lshl_add_u32 v5, v3, 1, v5\n"
"v_lshrrev_b32 v3, 2, v2\n"
"v_add_u32 v3, s74, v3\n"
".p2align 6\n"
".L_dbl_2:\n"
"s_waitcnt lgkmcnt(4)\n"
"v_mfma_f32_16x16x32_bf16 v[24:27], a[0:3], v[32:35], 0\n"
"ds_read_b128 v[56:59], v4 offset:29696\n"
"v_mfma_f32_16x16x32_bf16 v[24:27], a[112:115], v[36:39], v[24:27]\n"
"v_mfma_f32_16x16x32_bf16 v[28:31], a[116:119], v[36:39], 0\n"
"ds_read_b128 v[60:63], v4 offset:44032\n"
"v_mfma_f32_16x16x32_bf16 v[28:31], a[4:7], v[32:35], v[28:31]\n"
"v_cvt_pk_bf16_f32 v15, v22, v23\n"
"s_waitcnt lgkmcnt(4)\n"
"v_mfma_f32_16x16x32_bf16 v[28:31], a[228:231], v[40:43], v[28:31]\n"
"ds_read_b128 v[32:35], v4 offset:2048\n"
"v_cvt_pk_bf16_f32 v14, v20, v21\n"
"v_mfma_f32_16x16x32_bf16 v[28:31], v[148:151], v[44:47], v[28:31]\n"
"v_cvt_pk_bf16_f32 v13, v18, v19\n"
"v_cvt_pk_bf16_f32 v12, v16, v17\n"
"v_mfma_f32_16x16x32_bf16 v[24:27], v[144:147], v[44:47], v[24:27]\n"
"ds_read_b128 v[36:39], v4 offset:16384\n"
"ds_write_b128 v5, v[12:15]\n"
"v_mfma_f32_16x16x32_bf16 v[24:27], a[224:227], v[40:43], v[24:27]\n"
"ds_read2st64_b32 v[12:13], v3 offset0:0 offset1:1\n"
"s_waitcnt lgkmcnt(6)\n"
"v_mfma_f32_16x16x32_bf16 v[24:27], a[8:11], v[48:51], v[24:27]\n"
"ds_read_b128 v[40:43], v4 offset:30720\n"
"ds_read2st64_b32 v[14:15], v3 offset0:2 offset1:3\n"
"v_mfma_f32_16x16x32_bf16 v[24:27], a[120:123], v[52:55], v[24:27]\n"
"v_mfma_f32_16x16x32_bf16 v[28:31], a[124:127], v[52:55], v[28:31]\n"
"ds_read_b128 v[44:47], v4 offset:45056\n"
"v_mfma_f32_16x16x32_bf16 v[28:31], a[12:15], v[48:51], v[28:31]\n"
"s_waitcnt lgkmcnt(7)\n"
"v_mfma_f32_16x16x32_bf16 v[28:31], a[236:239], v[56:59], v[28:31]\n"
"ds_read_b128 v[48:51], v4 offset:3072\n"
"v_mfma_f32_16x16x32_bf16 v[28:31], v[156:159], v[60:63], v[28:31]\n"
"s_waitcnt lgkmcnt(2)\n"
"buffer_atomic_pk_add_bf16 v12, v1, s[44:47], s65 offen offset:0 sc1\n"
"v_mfma_f32_16x16x32_bf16 v[24:27], v[152:155], v[60:63], v[24:27]\n"
"ds_read_b128 v[52:55], v4 offset:17408\n"
"v_mfma_f32_16x16x32_bf16 v[24:27], a[232:235], v[56:59], v[24:27]\n"
"buffer_atomic_pk_add_bf16 v13, v1, s[44:47], s65 offen offset:2048 sc1\n"
"v_mfma_f32_16x16x32_bf16 v[24:27], a[16:19], v[32:35], v[24:27]\n"
"ds_read_b128 v[56:59], v4 offset:31744\n"
"s_add_u32 s65, s65, 16384\n"
"v_mfma_f32_16x16x32_bf16 v[24:27], a[128:131], v[36:39], v[24:27]\n"
"s_waitcnt vmcnt(2)\n"
"s_barrier\n"
"v_mfma_f32_16x16x32_bf16 v[28:31], a[132:135], v[36:39], v[28:31]\n"
"ds_read_b128 v[60:63], v4 offset:46080\n"
"v_mfma_f32_16x16x32_bf16 v[28:31], a[20:23], v[32:35], v[28:31]\n"
"buffer_atomic_pk_add_bf16 v14, v1, s[44:47], s65 offen offset:0 sc1\n"
"s_waitcnt lgkmcnt(4)\n"
"v_mfma_f32_16x16x32_bf16 v[28:31], a[244:247], v[40:43], v[28:31]\n"
"ds_read_b128 v[32:35], v4 offset:4096\n"
"s_add_u32 m0, s64, 0\n"
"v_mfma_f32_16x16x32_bf16 v[28:31], v[164:167], v[44:47], v[28:31]\n"
"buffer_atomic_pk_add_bf16 v15, v1, s[44:47], s65 offen offset:2048 sc1\n"
"s_add_u32 s65, s65, 16384\n"
"v_mfma_f32_16x16x32_bf16 v[24:27], v[160:163], v[44:47], v[24:27]\n"
"ds_read_b128 v[36:39], v4 offset:18432\n"
"v_mfma_f32_16x16x32_bf16 v[24:27], a[240:243], v[40:43], v[24:27]\n"
"s_waitcnt lgkmcnt(4)\n"
"v_mfma_f32_16x16x32_bf16 v[24:27], a[24:27], v[48:51], v[24:27]\n"
"ds_read_b128 v[40:43], v4 offset:32768\n"
"v_mfma_f32_16x16x32_bf16 v[24:27], a[136:139], v[52:55], v[24:27]\n"
"buffer_load_dwordx4 v2, s[32:35], s63 offen offset:0 sc1 lds\n"
"v_mfma_f32_16x16x32_bf16 v[28:31], a[140:143], v[52:55], v[28:31]\n"
"ds_read_b128 v[44:47], v4 offset:47104\n"
"v_mfma_f32_16x16x32_bf16 v[28:31], a[28:31], v[48:51], v[28:31]\n"
"s_waitcnt lgkmcnt(4)\n"
"v_mfma_f32_16x16x32_bf16 v[28:31], a[252:255], v[56:59], v[28:31]\n"
"ds_read_b128 v[48:51], v4 offset:5120\n"
"v_mfma_f32_16x16x32_bf16 v[28:31], v[172:175], v[60:63], v[28:31]\n"
"buffer_load_dwordx4 v2, s[32:35], s63 offen offset:1024 sc1 lds\n"
"v_mfma_f32_16x16x32_bf16 v[24:27], v[168:171], v[60:63], v[24:27]\n"
"ds_read_b128 v[52:55], v4 offset:19456\n"
"v_mfma_f32_16x16x32_bf16 v[24:27], a[248:251], v[56:59], v[24:27]\n"
"s_waitcnt lgkmcnt(4)\n"
"v_mfma_f32_16x16x32_bf16 v[24:27], a[32:35], v[32:35], v[24:27]\n"
"ds_read_b128 v[56:59], v4 offset:33792\n"
"v_mfma_f32_16x16x32_bf16 v[24:27], a[144:147], v[36:39], v[24:27]\n"
"buffer_load_dwordx4 v2, s[32:35], s63 offen offset:2048 sc1 lds\n"
"v_mfma_f32_16x16x32_bf16 v[28:31], a[148:151], v[36:39], v[28:31]\n"
"ds_read_b128 v[60:63], v4 offset:48128\n"
"v_mfma_f32_16x16x32_bf16 v[28:31], a[36:39], v[32:35], v[28:31]\n"
"s_waitcnt lgkmcnt(4)\n"
"v_mfma_f32_16x16x32_bf16 v[28:31], v[68:71], v[40:43], v[28:31]\n"
"ds_read_b128 v[32:35], v4 offset:6144\n"
"v_mfma_f32_16x16x32_bf16 v[28:31], v[180:183], v[44:47], v[28:31]\n"
"buffer_load_dwordx4 v2, s[32:35], s63 offen offset:3072 sc1 lds\n"
"v_mfma_f32_16x16x32_bf16 v[24:27], v[176:179], v[44:47], v[24:27]\n"
"ds_read_b128 v[36:39], v4 offset:20480\n"
"v_mfma_f32_16x16x32_bf16 v[24:27], v[64:67], v[40:43], v[24:27]\n"
"s_waitcnt lgkmcnt(4)\n"
"v_mfma_f32_16x16x32_bf16 v[24:27], a[40:43], v[48:51], v[24:27]\n"
"ds_read_b128 v[40:43], v4 offset:34816\n"
"s_add_u32 s63, s63, 4096\n"
"v_mfma_f32_16x16x32_bf16 v[24:27], a[152:155], v[52:55], v[24:27]\n"
"s_add_u32 m0, s64, 4096\n"
"v_mfma_f32_16x16x32_bf16 v[28:31], a[156:159], v[52:55], v[28:31]\n"
"ds_read_b128 v[44:47], v4 offset:49152\n"
"v_mfma_f32_16x16x32_bf16 v[28:31], a[44:47], v[48:51], v[28:31]\n"
"s_waitcnt lgkmcnt(4)\n"
"v_mfma_f32_16x16x32_bf16 v[28:31], v[76:79], v[56:59], v[28:31]\n"
"ds_read_b128 v[48:51], v4 offset:7168\n"
"v_mfma_f32_16x16x32_bf16 v[28:31], v[188:191], v[60:63], v[28:31]\n"
"buffer_load_dwordx4 v2, s[32:35], s63 offen offset:0 sc1 lds\n"
"v_mfma_f32_16x16x32_bf16 v[24:27], v[184:187], v[60:63], v[24:27]\n"
"ds_read_b128 v[52:55], v4 offset:21504\n"
"v_mfma_f32_16x16x32_bf16 v[24:27], v[72:75], v[56:59], v[24:27]\n"
"s_waitcnt lgkmcnt(4)\n"
"v_mfma_f32_16x16x32_bf16 v[24:27], a[48:51], v[32:35], v[24:27]\n"
"ds_read_b128 v[56:59], v4 offset:35840\n"
"v_mfma_f32_16x16x32_bf16 v[24:27], a[160:163], v[36:39], v[24:27]\n"
"buffer_load_dwordx4 v2, s[32:35], s63 offen offset:1024 sc1 lds\n"
"v_mfma_f32_16x16x32_bf16 v[28:31], a[164:167], v[36:39], v[28:31]\n"
"ds_read_b128 v[60:63], v4 offset:50176\n"
"v_mfma_f32_16x16x32_bf16 v[28:31], a[52:55], v[32:35], v[28:31]\n"
"s_waitcnt lgkmcnt(4)\n"
"v_mfma_f32_16x16x32_bf16 v[28:31], v[84:87], v[40:43], v[28:31]\n"
"ds_read_b128 v[32:35], v4 offset:8192\n"
"v_mfma_f32_16x16x32_bf16 v[28:31], v[196:199], v[44:47], v[28:31]\n"
"buffer_load_dwordx4 v2, s[32:35], s63 offen offset:2048 sc1 lds\n"
"v_mfma_f32_16x16x32_bf16 v[24:27], v[192:195], v[44:47], v[24:27]\n"
"ds_read_b128 v[36:39], v4 offset:22528\n"
"v_mfma_f32_16x16x32_bf16 v[24:27], v[80:83], v[40:43], v[24:27]\n"
"s_waitcnt lgkmcnt(4)\n"
"v_mfma_f32_16x16x32_bf16 v[24:27], a[56:59], v[48:51], v[24:27]\n"
"ds_read_b128 v[40:43], v4 offset:36864\n"
"v_mfma_f32_16x16x32_bf16 v[24:27], a[168:171], v[52:55], v[24:27]\n"
"buffer_load_dwordx4 v2, s[32:35], s63 offen offset:3072 sc1 lds\n"
"v_mfma_f32_16x16x32_bf16 v[28:31], a[172:175], v[52:55], v[28:31]\n"
"ds_read_b128 v[44:47], v4 offset:51200\n"
"v_mfma_f32_16x16x32_bf16 v[28:31], a[60:63], v[48:51], v[28:31]\n"
"s_waitcnt lgkmcnt(4)\n"
"v_mfma_f32_16x16x32_bf16 v[28:31], v[92:95], v[56:59], v[28:31]\n"
"ds_read_b128 v[48:51], v4 offset:9216\n"
"v_mfma_f32_16x16x32_bf16 v[28:31], v[204:207], v[60:63], v[28:31]\n"
"s_add_u32 s63, s63, 4096\n"
"s_add_u32 m0, s64, 8192\n"
"v_mfma_f32_16x16x32_bf16 v[24:27], v[200:203], v[60:63], v[24:27]\n"
"ds_read_b128 v[52:55], v4 offset:23552\n"
"v_mfma_f32_16x16x32_bf16 v[24:27], v[88:91], v[56:59], v[24:27]\n"
"s_waitcnt lgkmcnt(4)\n"
"v_mfma_f32_16x16x32_bf16 v[24:27], a[64:67], v[32:35], v[24:27]\n"
"ds_read_b128 v[56:59], v4 offset:37888\n"
"v_mfma_f32_16x16x32_bf16 v[24:27], a[176:179], v[36:39], v[24:27]\n"
"buffer_load_dwordx4 v2, s[32:35], s63 offen offset:0 sc1 lds\n"
"v_mfma_f32_16x16x32_bf16 v[28:31], a[180:183], v[36:39], v[28:31]\n"
"ds_read_b128 v[60:63], v4 offset:52224\n"
"v_mfma_f32_16x16x32_bf16 v[28:31], a[68:71], v[32:35], v[28:31]\n"
"s_waitcnt lgkmcnt(4)\n"
"v_mfma_f32_16x16x32_bf16 v[28:31], v[100:103], v[40:43], v[28:31]\n"
"ds_read_b128 v[32:35], v4 offset:10240\n"
"v_mfma_f32_16x16x32_bf16 v[28:31], v[212:215], v[44:47], v[28:31]\n"
"buffer_load_dwordx4 v2, s[32:35], s63 offen offset:1024 sc1 lds\n"
"v_mfma_f32_16x16x32_bf16 v[24:27], v[208:211], v[44:47], v[24:27]\n"
"ds_read_b128 v[36:39], v4 offset:24576\n"
"v_mfma_f32_16x16x32_bf16 v[24:27], v[96:99], v[40:43], v[24:27]\n"
"s_waitcnt lgkmcnt(4)\n"
"v_mfma_f32_16x16x32_bf16 v[24:27], a[72:75], v[48:51], v[24:27]\n"
"ds_read_b128 v[40:43], v4 offset:38912\n"
"v_mfma_f32_16x16x32_bf16 v[24:27], a[184:187], v[52:55], v[24:27]\n"
"buffer_load_dwordx4 v2, s[32:35], s63 offen offset:2048 sc1 lds\n"
"v_mfma_f32_16x16x32_bf16 v[28:31], a[188:191], v[52:55], v[28:31]\n"
"ds_read_b128 v[44:47], v4 offset:53248\n"
"v_mfma_f32_16x16x32_bf16 v[28:31], a[76:79], v[48:51], v[28:31]\n"
"s_waitcnt lgkmcnt(4)\n"
"v_mfma_f32_16x16x32_bf16 v[28:31], v[108:111], v[56:59], v[28:31]\n"
"ds_read_b128 v[48:51], v4 offset:11264\n"
"v_mfma_f32_16x16x32_bf16 v[28:31], v[220:223], v[60:63], v[28:31]\n"
"v_mfma_f32_16x16x32_bf16 v[24:27], v[216:219], v[60:63], v[24:27]\n"
"ds_read_b128 v[52:55], v4 offset:25600\n"
"v_mfma_f32_16x16x32_bf16 v[24:27], v[104:107], v[56:59], v[24:27]\n"
"s_waitcnt lgkmcnt(4)\n"
"v_mfma_f32_16x16x32_bf16 v[24:27], a[80:83], v[32:35], v[24:27]\n"
"ds_read_b128 v[56:59], v4 offset:39936\n"
"v_mfma_f32_16x16x32_bf16 v[24:27], a[192:195], v[36:39], v[24:27]\n"
"buffer_load_dwordx4 v2, s[32:35], s63 offen offset:3072 sc1 lds\n"
"s_add_u32 s63, s63, 4096\n"
"v_mfma_f32_16x16x32_bf16 v[28:31], a[196:199], v[36:39], v[28:31]\n"
"ds_read_b128 v[60:63], v4 offset:54272\n"
"s_add_u32 m0, s64, 12288\n"
"v_mfma_f32_16x16x32_bf16 v[28:31], a[84:87], v[32:35], v[28:31]\n"
"s_waitcnt lgkmcnt(4)\n"
"v_mfma_f32_16x16x32_bf16 v[28:31], v[116:119], v[40:43], v[28:31]\n"
"ds_read_b128 v[32:35], v4 offset:12288\n"
"v_mfma_f32_16x16x32_bf16 v[28:31], v[228:231], v[44:47], v[28:31]\n"
"buffer_load_dwordx4 v2, s[32:35], s63 offen offset:0 sc1 lds\n"
"v_mfma_f32_16x16x32_bf16 v[24:27], v[224:227], v[44:47], v[24:27]\n"
"ds_read_b128 v[36:39], v4 offset:26624\n"
"v_mfma_f32_16x16x32_bf16 v[24:27], v[112:115], v[40:43], v[24:27]\n"
"s_waitcnt lgkmcnt(4)\n"
"v_mfma_f32_16x16x32_bf16 v[24:27], a[88:91], v[48:51], v[24:27]\n"
"ds_read_b128 v[40:43], v4 offset:40960\n"
"v_mfma_f32_16x16x32_bf16 v[24:27], a[200:203], v[52:55], v[24:27]\n"
"buffer_load_dwordx4 v2, s[32:35], s63 offen offset:1024 sc1 lds\n"
"s_add_u32 s63, s63, 1175552\n"
"v_mfma_f32_16x16x32_bf16 v[28:31], a[204:207], v[52:55], v[28:31]\n"
"ds_read_b128 v[44:47], v4 offset:55296\n"
"v_mfma_f32_16x16x32_bf16 v[28:31], a[92:95], v[48:51], v[28:31]\n"
"s_waitcnt lgkmcnt(4)\n"
"v_mfma_f32_16x16x32_bf16 v[28:31], v[124:127], v[56:59], v[28:31]\n"
"ds_read_b128 v[48:51], v4 offset:13312\n"
"v_mfma_f32_16x16x32_bf16 v[28:31], v[236:239], v[60:63], v[28:31]\n"
"v_mfma_f32_16x16x32_bf16 v[24:27], v[232:235], v[60:63], v[24:27]\n"
"ds_read_b128 v[52:55], v4 offset:27648\n"
"v_mfma_f32_16x16x32_bf16 v[24:27], v[120:123], v[56:59], v[24:27]\n"
"s_waitcnt lgkmcnt(4)\n"
"v_mfma_f32_16x16x32_bf16 v[24:27], a[96:99], v[32:35], v[24:27]\n"
"ds_read_b128 v[56:59], v4 offset:41984\n"
"v_mfma_f32_16x16x32_bf16 v[24:27], a[208:211], v[36:39], v[24:27]\n"
"s_waitcnt vmcnt(10)\n"
"s_barrier\n"
"v_mfma_f32_16x16x32_bf16 v[28:31], a[212:215], v[36:39], v[28:31]\n"
"ds_read_b128 v[60:63], v4 offset:56320\n"
"v_mfma_f32_16x16x32_bf16 v[28:31], a[100:103], v[32:35], v[28:31]\n"
"s_waitcnt lgkmcnt(4)\n"
"v_mfma_f32_16x16x32_bf16 v[28:31], v[132:135], v[40:43], v[28:31]\n"
"ds_read_b128 v[32:35], v2 offset:0\n"
"v_mfma_f32_16x16x32_bf16 v[28:31], v[244:247], v[44:47], v[28:31]\n"
"v_mfma_f32_16x16x32_bf16 v[24:27], v[240:243], v[44:47], v[24:27]\n"
"ds_read_b128 v[36:39], v2 offset:14336\n"
"v_mfma_f32_16x16x32_bf16 v[24:27], v[128:131], v[40:43], v[24:27]\n"
"s_waitcnt lgkmcnt(4)\n"
"v_mfma_f32_16x16x32_bf16 v[24:27], a[104:107], v[48:51], v[24:27]\n"
"ds_read_b128 v[40:43], v2 offset:28672\n"
"v_mfma_f32_16x16x32_bf16 v[24:27], a[216:219], v[52:55], v[24:27]\n"
"v_mfma_f32_16x16x32_bf16 v[28:31], a[220:223], v[52:55], v[28:31]\n"
"ds_read_b128 v[44:47], v2 offset:43008\n"
"v_mfma_f32_16x16x32_bf16 v[28:31], a[108:111], v[48:51], v[28:31]\n"
"s_waitcnt lgkmcnt(4)\n"
"v_mfma_f32_16x16x32_bf16 v[28:31], v[140:143], v[56:59], v[28:31]\n"
"ds_read_b128 v[48:51], v2 offset:1024\n"
"v_mfma_f32_16x16x32_bf16 v[28:31], v[252:255], v[60:63], v[28:31]\n"
"v_mfma_f32_16x16x32_bf16 v[24:27], v[248:251], v[60:63], v[24:27]\n"
"ds_read_b128 v[52:55], v2 offset:15360\n"
"s_sub_u32 s62, s62, 1\n"
"v_mfma_f32_16x16x32_bf16 v[24:27], v[136:139], v[56:59], v[24:27]\n"
"s_cbranch_scc1 .L_dbt_2\n"
"s_waitcnt lgkmcnt(4)\n"
"v_mfma_f32_16x16x32_bf16 v[16:19], a[0:3], v[32:35], 0\n"
"ds_read_b128 v[56:59], v2 offset:29696\n"
"v_mfma_f32_16x16x32_bf16 v[16:19], a[112:115], v[36:39], v[16:19]\n"
"v_mfma_f32_16x16x32_bf16 v[20:23], a[116:119], v[36:39], 0\n"
"ds_read_b128 v[60:63], v2 offset:44032\n"
"v_mfma_f32_16x16x32_bf16 v[20:23], a[4:7], v[32:35], v[20:23]\n"
"v_cvt_pk_bf16_f32 v15, v30, v31\n"
"s_waitcnt lgkmcnt(4)\n"
"v_mfma_f32_16x16x32_bf16 v[20:23], a[228:231], v[40:43], v[20:23]\n"
"ds_read_b128 v[32:35], v2 offset:2048\n"
"v_cvt_pk_bf16_f32 v14, v28, v29\n"
"v_mfma_f32_16x16x32_bf16 v[20:23], v[148:151], v[44:47], v[20:23]\n"
"v_cvt_pk_bf16_f32 v13, v26, v27\n"
"v_cvt_pk_bf16_f32 v12, v24, v25\n"
"v_mfma_f32_16x16x32_bf16 v[16:19], v[144:147], v[44:47], v[16:19]\n"
"ds_read_b128 v[36:39], v2 offset:16384\n"
"ds_write_b128 v5, v[12:15]\n"
"v_mfma_f32_16x16x32_bf16 v[16:19], a[224:227], v[40:43], v[16:19]\n"
"ds_read2st64_b32 v[12:13], v3 offset0:0 offset1:1\n"
"s_waitcnt lgkmcnt(6)\n"
"v_mfma_f32_16x16x32_bf16 v[16:19], a[8:11], v[48:51], v[16:19]\n"
"ds_read_b128 v[40:43], v2 offset:30720\n"
"ds_read2st64_b32 v[14:15], v3 offset0:2 offset1:3\n"
"v_mfma_f32_16x16x32_bf16 v[16:19], a[120:123], v[52:55], v[16:19]\n"
"v_mfma_f32_16x16x32_bf16 v[20:23], a[124:127], v[52:55], v[20:23]\n"
"ds_read_b128 v[44:47], v2 offset:45056\n"
"v_mfma_f32_16x16x32_bf16 v[20:23], a[12:15], v[48:51], v[20:23]\n"
"s_waitcnt lgkmcnt(7)\n"
"v_mfma_f32_16x16x32_bf16 v[20:23], a[236:239], v[56:59], v[20:23]\n"
"ds_read_b128 v[48:51], v2 offset:3072\n"
"v_mfma_f32_16x16x32_bf16 v[20:23], v[156:159], v[60:63], v[20:23]\n"
"s_waitcnt lgkmcnt(2)\n"
"buffer_atomic_pk_add_bf16 v12, v1, s[44:47], s65 offen offset:0 sc1\n"
"v_mfma_f32_16x16x32_bf16 v[16:19], v[152:155], v[60:63], v[16:19]\n"
"ds_read_b128 v[52:55], v2 offset:17408\n"
"v_mfma_f32_16x16x32_bf16 v[16:19], a[232:235], v[56:59], v[16:19]\n"
"buffer_atomic_pk_add_bf16 v13, v1, s[44:47], s65 offen offset:2048 sc1\n"
"v_mfma_f32_16x16x32_bf16 v[16:19], a[16:19], v[32:35], v[16:19]\n"
"ds_read_b128 v[56:59], v2 offset:31744\n"
"s_add_u32 s65, s65, 16384\n"
"v_mfma_f32_16x16x32_bf16 v[16:19], a[128:131], v[36:39], v[16:19]\n"
"s_waitcnt vmcnt(2)\n"
"s_barrier\n"
"v_mfma_f32_16x16x32_bf16 v[20:23], a[132:135], v[36:39], v[20:23]\n"
"ds_read_b128 v[60:63], v2 offset:46080\n"
"v_mfma_f32_16x16x32_bf16 v[20:23], a[20:23], v[32:35], v[20:23]\n"
"buffer_atomic_pk_add_bf16 v14, v1, s[44:47], s65 offen offset:0 sc1\n"
"s_waitcnt lgkmcnt(4)\n"
"v_mfma_f32_16x16x32_bf16 v[20:23], a[244:247], v[40:43], v[20:23]\n"
"ds_read_b128 v[32:35], v2 offset:4096\n"
"s_add_u32 m0, s64, 57344\n"
"v_mfma_f32_16x16x32_bf16 v[20:23], v[164:167], v[44:47], v[20:23]\n"
"buffer_atomic_pk_add_bf16 v15, v1, s[44:47], s65 offen offset:2048 sc1\n"
"s_add_u32 s65, s65, 16384\n"
"v_mfma_f32_16x16x32_bf16 v[16:19], v[160:163], v[44:47], v[16:19]\n"
"ds_read_b128 v[36:39], v2 offset:18432\n"
"v_mfma_f32_16x16x32_bf16 v[16:19], a[240:243], v[40:43], v[16:19]\n"
"s_waitcnt lgkmcnt(4)\n"
"v_mfma_f32_16x16x32_bf16 v[16:19], a[24:27], v[48:51], v[16:19]\n"
"ds_read_b128 v[40:43], v2 offset:32768\n"
"v_mfma_f32_16x16x32_bf16 v[16:19], a[136:139], v[52:55], v[16:19]\n"
"buffer_load_dwordx4 v2, s[32:35], s63 offen offset:0 sc1 lds\n"
"v_mfma_f32_16x16x32_bf16 v[20:23], a[140:143], v[52:55], v[20:23]\n"
"ds_read_b128 v[44:47], v2 offset:47104\n"
"v_mfma_f32_16x16x32_bf16 v[20:23], a[28:31], v[48:51], v[20:23]\n"
"s_waitcnt lgkmcnt(4)\n"
"v_mfma_f32_16x16x32_bf16 v[20:23], a[252:255], v[56:59], v[20:23]\n"
"ds_read_b128 v[48:51], v2 offset:5120\n"
"v_mfma_f32_16x16x32_bf16 v[20:23], v[172:175], v[60:63], v[20:23]\n"
"buffer_load_dwordx4 v2, s[32:35], s63 offen offset:1024 sc1 lds\n"
"v_mfma_f32_16x16x32_bf16 v[16:19], v[168:171], v[60:63], v[16:19]\n"
"ds_read_b128 v[52:55], v2 offset:19456\n"
"v_mfma_f32_16x16x32_bf16 v[16:19], a[248:251], v[56:59], v[16:19]\n"
"s_waitcnt lgkmcnt(4)\n"
"v_mfma_f32_16x16x32_bf16 v[16:19], a[32:35], v[32:35], v[16:19]\n"
"ds_read_b128 v[56:59], v2 offset:33792\n"
"v_mfma_f32_16x16x32_bf16 v[16:19], a[144:147], v[36:39], v[16:19]\n"
"buffer_load_dwordx4 v2, s[32:35], s63 offen offset:2048 sc1 lds\n"
"v_mfma_f32_16x16x32_bf16 v[20:23], a[148:151], v[36:39], v[20:23]\n"
"ds_read_b128 v[60:63], v2 offset:48128\n"
"v_mfma_f32_16x16x32_bf16 v[20:23], a[36:39], v[32:35], v[20:23]\n"
"s_waitcnt lgkmcnt(4)\n"
"v_mfma_f32_16x16x32_bf16 v[20:23], v[68:71], v[40:43], v[20:23]\n"
"ds_read_b128 v[32:35], v2 offset:6144\n"
"v_mfma_f32_16x16x32_bf16 v[20:23], v[180:183], v[44:47], v[20:23]\n"
"buffer_load_dwordx4 v2, s[32:35], s63 offen offset:3072 sc1 lds\n"
"v_mfma_f32_16x16x32_bf16 v[16:19], v[176:179], v[44:47], v[16:19]\n"
"ds_read_b128 v[36:39], v2 offset:20480\n"
"v_mfma_f32_16x16x32_bf16 v[16:19], v[64:67], v[40:43], v[16:19]\n"
"s_waitcnt lgkmcnt(4)\n"
"v_mfma_f32_16x16x32_bf16 v[16:19], a[40:43], v[48:51], v[16:19]\n"
"ds_read_b128 v[40:43], v2 offset:34816\n"
"s_add_u32 s63, s63, 4096\n"
"v_mfma_f32_16x16x32_bf16 v[16:19], a[152:155], v[52:55], v[16:19]\n"
"s_add_u32 m0, s64, 61440\n"
"v_mfma_f32_16x16x32_bf16 v[20:23], a[156:159], v[52:55], v[20:23]\n"
"ds_read_b128 v[44:47], v2 offset:49152\n"
"v_mfma_f32_16x16x32_bf16 v[20:23], a[44:47], v[48:51], v[20:23]\n"
"s_waitcnt lgkmcnt(4)\n"
"v_mfma_f32_16x16x32_bf16 v[20:23], v[76:79], v[56:59], v[20:23]\n"
"ds_read_b128 v[48:51], v2 offset:7168\n"
"v_mfma_f32_16x16x32_bf16 v[20:23], v[188:191], v[60:63], v[20:23]\n"
"buffer_load_dwordx4 v2, s[32:35], s63 offen offset:0 sc1 lds\n"
"v_mfma_f32_16x16x32_bf16 v[16:19], v[184:187], v[60:63], v[16:19]\n"
"ds_read_b128 v[52:55], v2 offset:21504\n"
"v_mfma_f32_16x16x32_bf16 v[16:19], v[72:75], v[56:59], v[16:19]\n"
"s_waitcnt lgkmcnt(4)\n"
"v_mfma_f32_16x16x32_bf16 v[16:19], a[48:51], v[32:35], v[16:19]\n"
"ds_read_b128 v[56:59], v2 offset:35840\n"
"v_mfma_f32_16x16x32_bf16 v[16:19], a[160:163], v[36:39], v[16:19]\n"
"buffer_load_dwordx4 v2, s[32:35], s63 offen offset:1024 sc1 lds\n"
"v_mfma_f32_16x16x32_bf16 v[20:23], a[164:167], v[36:39], v[20:23]\n"
"ds_read_b128 v[60:63], v2 offset:50176\n"
"v_mfma_f32_16x16x32_bf16 v[20:23], a[52:55], v[32:35], v[20:23]\n"
"s_waitcnt lgkmcnt(4)\n"
"v_mfma_f32_16x16x32_bf16 v[20:23], v[84:87], v[40:43], v[20:23]\n"
"ds_read_b128 v[32:35], v2 offset:8192\n"
"v_mfma_f32_16x16x32_bf16 v[20:23], v[196:199], v[44:47], v[20:23]\n"
"buffer_load_dwordx4 v2, s[32:35], s63 offen offset:2048 sc1 lds\n"
"v_mfma_f32_16x16x32_bf16 v[16:19], v[192:195], v[44:47], v[16:19]\n"
"ds_read_b128 v[36:39], v2 offset:22528\n"
"v_mfma_f32_16x16x32_bf16 v[16:19], v[80:83], v[40:43], v[16:19]\n"
"s_waitcnt lgkmcnt(4)\n"
"v_mfma_f32_16x16x32_bf16 v[16:19], a[56:59], v[48:51], v[16:19]\n"
"ds_read_b128 v[40:43], v2 offset:36864\n"
"v_mfma_f32_16x16x32_bf16 v[16:19], a[168:171], v[52:55], v[16:19]\n"
"buffer_load_dwordx4 v2, s[32:35], s63 offen offset:3072 sc1 lds\n"
"v_mfma_f32_16x16x32_bf16 v[20:23], a[172:175], v[52:55], v[20:23]\n"
"ds_read_b128 v[44:47], v2 offset:51200\n"
"v_mfma_f32_16x16x32_bf16 v[20:23], a[60:63], v[48:51], v[20:23]\n"
"s_waitcnt lgkmcnt(4)\n"
"v_mfma_f32_16x16x32_bf16 v[20:23], v[92:95], v[56:59], v[20:23]\n"
"ds_read_b128 v[48:51], v2 offset:9216\n"
"v_mfma_f32_16x16x32_bf16 v[20:23], v[204:207], v[60:63], v[20:23]\n"
"s_add_u32 s63, s63, 4096\n"
"s_add_u32 m0, s64, 65536\n"
"v_mfma_f32_16x16x32_bf16 v[16:19], v[200:203], v[60:63], v[16:19]\n"
"ds_read_b128 v[52:55], v2 offset:23552\n"
"v_mfma_f32_16x16x32_bf16 v[16:19], v[88:91], v[56:59], v[16:19]\n"
"s_waitcnt lgkmcnt(4)\n"
"v_mfma_f32_16x16x32_bf16 v[16:19], a[64:67], v[32:35], v[16:19]\n"
"ds_read_b128 v[56:59], v2 offset:37888\n"
"v_mfma_f32_16x16x32_bf16 v[16:19], a[176:179], v[36:39], v[16:19]\n"
"buffer_load_dwordx4 v2, s[32:35], s63 offen offset:0 sc1 lds\n"
"v_mfma_f32_16x16x32_bf16 v[20:23], a[180:183], v[36:39], v[20:23]\n"
"ds_read_b128 v[60:63], v2 offset:52224\n"
"v_mfma_f32_16x16x32_bf16 v[20:23], a[68:71], v[32:35], v[20:23]\n"
"s_waitcnt lgkmcnt(4)\n"
"v_mfma_f32_16x16x32_bf16 v[20:23], v[100:103], v[40:43], v[20:23]\n"
"ds_read_b128 v[32:35], v2 offset:10240\n"
"v_mfma_f32_16x16x32_bf16 v[20:23], v[212:215], v[44:47], v[20:23]\n"
"buffer_load_dwordx4 v2, s[32:35], s63 offen offset:1024 sc1 lds\n"
"v_mfma_f32_16x16x32_bf16 v[16:19], v[208:211], v[44:47], v[16:19]\n"
"ds_read_b128 v[36:39], v2 offset:24576\n"
"v_mfma_f32_16x16x32_bf16 v[16:19], v[96:99], v[40:43], v[16:19]\n"
"s_waitcnt lgkmcnt(4)\n"
"v_mfma_f32_16x16x32_bf16 v[16:19], a[72:75], v[48:51], v[16:19]\n"
"ds_read_b128 v[40:43], v2 offset:38912\n"
"v_mfma_f32_16x16x32_bf16 v[16:19], a[184:187], v[52:55], v[16:19]\n"
"buffer_load_dwordx4 v2, s[32:35], s63 offen offset:2048 sc1 lds\n"
"v_mfma_f32_16x16x32_bf16 v[20:23], a[188:191], v[52:55], v[20:23]\n"
"ds_read_b128 v[44:47], v2 offset:53248\n"
"v_mfma_f32_16x16x32_bf16 v[20:23], a[76:79], v[48:51], v[20:23]\n"
"s_waitcnt lgkmcnt(4)\n"
"v_mfma_f32_16x16x32_bf16 v[20:23], v[108:111], v[56:59], v[20:23]\n"
"ds_read_b128 v[48:51], v2 offset:11264\n"
"v_mfma_f32_16x16x32_bf16 v[20:23], v[220:223], v[60:63], v[20:23]\n"
"v_mfma_f32_16x16x32_bf16 v[16:19], v[216:219], v[60:63], v[16:19]\n"
"ds_read_b128 v[52:55], v2 offset:25600\n"
"v_mfma_f32_16x16x32_bf16 v[16:19], v[104:107], v[56:59], v[16:19]\n"
"s_waitcnt lgkmcnt(4)\n"
"v_mfma_f32_16x16x32_bf16 v[16:19], a[80:83], v[32:35], v[16:19]\n"
"ds_read_b128 v[56:59], v2 offset:39936\n"
"v_mfma_f32_16x16x32_bf16 v[16:19], a[192:195], v[36:39], v[16:19]\n"
"buffer_load_dwordx4 v2, s[32:35], s63 offen offset:3072 sc1 lds\n"
"s_add_u32 s63, s63, 4096\n"
"v_mfma_f32_16x16x32_bf16 v[20:23], a[196:199], v[36:39], v[20:23]\n"
"ds_read_b128 v[60:63], v2 offset:54272\n"
"s_add_u32 m0, s64, 69632\n"
"v_mfma_f32_16x16x32_bf16 v[20:23], a[84:87], v[32:35], v[20:23]\n"
"s_waitcnt lgkmcnt(4)\n"
"v_mfma_f32_16x16x32_bf16 v[20:23], v[116:119], v[40:43], v[20:23]\n"
"ds_read_b128 v[32:35], v2 offset:12288\n"
"v_mfma_f32_16x16x32_bf16 v[20:23], v[228:231], v[44:47], v[20:23]\n"
"buffer_load_dwordx4 v2, s[32:35], s63 offen offset:0 sc1 lds\n"
"v_mfma_f32_16x16x32_bf16 v[16:19], v[224:227], v[44:47], v[16:19]\n"
"ds_read_b128 v[36:39], v2 offset:26624\n"
"v_mfma_f32_16x16x32_bf16 v[16:19], v[112:115], v[40:43], v[16:19]\n"
"s_waitcnt lgkmcnt(4)\n"
"v_mfma_f32_16x16x32_bf16 v[16:19], a[88:91], v[48:51], v[16:19]\n"
"ds_read_b128 v[40:43], v2 offset:40960\n"
"v_mfma_f32_16x16x32_bf16 v[16:19], a[200:203], v[52:55], v[16:19]\n"
"buffer_load_dwordx4 v2, s[32:35], s63 offen offset:1024 sc1 lds\n"
"s_add_u32 s63, s63, 1175552\n"
"v_mfma_f32_16x16x32_bf16 v[20:23], a[204:207], v[52:55], v[20:23]\n"
"ds_read_b128 v[44:47], v2 offset:55296\n"
"v_mfma_f32_16x16x32_bf16 v[20:23], a[92:95], v[48:51], v[20:23]\n"
"s_waitcnt lgkmcnt(4)\n"
"v_mfma_f32_16x16x32_bf16 v[20:23], v[124:127], v[56:59], v[20:23]\n"
"ds_read_b128 v[48:51], v2 offset:13312\n"
"v_mfma_f32_16x16x32_bf16 v[20:23], v[236:239], v[60:63], v[20:23]\n"
"v_mfma_f32_16x16x32_bf16 v[16:19], v[232:235], v[60:63], v[16:19]\n"
"ds_read_b128 v[52:55], v2 offset:27648\n"
"v_mfma_f32_16x16x32_bf16 v[16:19], v[120:123], v[56:59], v[16:19]\n"
"s_waitcnt lgkmcnt(4)\n"
"v_mfma_f32_16x16x32_bf16 v[16:19], a[96:99], v[32:35], v[16:19]\n"
"ds_read_b128 v[56:59], v2 offset:41984\n"
"v_mfma_f32_16x16x32_bf16 v[16:19], a[208:211], v[36:39], v[16:19]\n"
"s_waitcnt vmcnt(10)\n"
"s_barrier\n"
"v_mfma_f32_16x16x32_bf16 v[20:23], a[212:215], v[36:39], v[20:23]\n"
"ds_read_b128 v[60:63], v2 offset:56320\n"
"v_mfma_f32_16x16x32_bf16 v[20:23], a[100:103], v[32:35], v[20:23]\n"
"s_waitcnt lgkmcnt(4)\n"
"v_mfma_f32_16x16x32_bf16 v[20:23], v[132:135], v[40:43], v[20:23]\n"
"ds_read_b128 v[32:35], v4 offset:0\n"
"v_mfma_f32_16x16x32_bf16 v[20:23], v[244:247], v[44:47], v[20:23]\n"
"v_mfma_f32_16x16x32_bf16 v[16:19], v[240:243], v[44:47], v[16:19]\n"
"ds_read_b128 v[36:39], v4 offset:14336\n"
"v_mfma_f32_16x16x32_bf16 v[16:19], v[128:131], v[40:43], v[16:19]\n"
"s_waitcnt lgkmcnt(4)\n"
"v_mfma_f32_16x16x32_bf16 v[16:19], a[104:107], v[48:51], v[16:19]\n"
"ds_read_b128 v[40:43], v4 offset:28672\n"
"v_mfma_f32_16x16x32_bf16 v[16:19], a[216:219], v[52:55], v[16:19]\n"
"v_mfma_f32_16x16x32_bf16 v[20:23], a[220:223], v[52:55], v[20:23]\n"
"ds_read_b128 v[44:47], v4 offset:43008\n"
"v_mfma_f32_16x16x32_bf16 v[20:23], a[108:111], v[48:51], v[20:23]\n"
"s_waitcnt lgkmcnt(4)\n"
"v_mfma_f32_16x16x32_bf16 v[20:23], v[140:143], v[56:59], v[20:23]\n"
"ds_read_b128 v[48:51], v4 offset:1024\n"
"v_mfma_f32_16x16x32_bf16 v[20:23], v[252:255], v[60:63], v[20:23]\n"
"v_mfma_f32_16x16x32_bf16 v[16:19], v[248:251], v[60:63], v[16:19]\n"
"ds_read_b128 v[52:55], v4 offset:15360\n"
"v_mfma_f32_16x16x32_bf16 v[16:19], v[136:139], v[56:59], v[16:19]\n"
"s_branch .L_dbl_2\n"
".L_dbt_2:\n"
"s_nop 1\n"
"s_nop 0\n"
"v_cvt_pk_bf16_f32 v15, v30, v31\n"
"v_cvt_pk_bf16_f32 v14, v28, v29\n"
"s_nop 1\n"
"v_cvt_pk_bf16_f32 v13, v26, v27\n"
"v_cvt_pk_bf16_f32 v12, v24, v25\n"
"ds_write_b128 v5, v[12:15]\n"
"ds_read2st64_b32 v[12:13], v3 offset0:0 offset1:1\n"
"ds_read2st64_b32 v[14:15], v3 offset0:2 offset1:3\n"
"s_waitcnt lgkmcnt(0)\n"
"buffer_atomic_pk_add_bf16 v12, v1, s[44:47], s65 offen offset:0 sc1\n"
"buffer_atomic_pk_add_bf16 v13, v1, s[44:47], s65 offen offset:2048 sc1\n"
"s_add_u32 s65, s65, 16384\n"
"buffer_atomic_pk_add_bf16 v14, v1, s[44:47], s65 offen offset:0 sc1\n"
"buffer_atomic_pk_add_bf16 v15, v1, s[44:47], s65 offen offset:2048 sc1\n"
"s_add_u32 s65, s65, 16384\n"
"s_or_b32 s48, s28, s27\n"
"s_or_b32 s48, s48, s26\n"
"s_cbranch_scc1 .L_end\n"
"v_mov_b32 v8, s54\n"
"v_mov_b32 v9, s55\n"
"buffer_store_dwordx2 v[8:9], off, s[32:35], 0 sc1\n"
"s_branch .L_end\n"
".p2align 6\n"
".L_dhv3:\n"
".L_dal_3:\n"
"s_waitcnt lgkmcnt(4)\n"
"v_mfma_f32_16x16x32_bf16 v[16:19], a[0:3], v[32:35], 0\n"
"ds_read_b128 v[56:59], v2 offset:29696\n"
"ds_read_b128 v[12:15], v5 offset:8192\n"
"v_mfma_f32_16x16x32_bf16 v[16:19], a[112:115], v[36:39], v[16:19]\n"
"v_mfma_f32_16x16x32_bf16 v[20:23], a[116:119], v[36:39], 0\n"
"ds_read_b128 v[60:63], v2 offset:44032\n"
"v_mfma_f32_16x16x32_bf16 v[20:23], a[4:7], v[32:35], v[20:23]\n"
"s_waitcnt lgkmcnt(5)\n"
"v_mfma_f32_16x16x32_bf16 v[20:23], a[228:231], v[40:43], v[20:23]\n"
"ds_read_b128 v[32:35], v2 offset:2048\n"
"v_mfma_f32_16x16x32_bf16 v[20:23], v[148:151], v[44:47], v[20:23]\n"
"s_waitcnt lgkmcnt(2)\n"
"v_add_f32 v24, v24, v12\n"
"v_mfma_f32_16x16x32_bf16 v[16:19], v[144:147], v[44:47], v[16:19]\n"
"ds_read_b128 v[36:39], v2 offset:16384\n"
"v_add_f32 v25, v25, v13\n"
"v_mfma_f32_16x16x32_bf16 v[16:19], a[224:227], v[40:43], v[16:19]\n"
"v_add_f32 v26, v26, v14\n"
"v_mfma_f32_16x16x32_bf16 v[16:19], a[8:11], v[48:51], v[16:19]\n"
"ds_read_b128 v[40:43], v2 offset:30720\n"
"v_add_f32 v27, v27, v15\n"
"v_mfma_f32_16x16x32_bf16 v[16:19], a[120:123], v[52:55], v[16:19]\n"
"ds_read_b128 v[12:15], v5 offset:9216\n"
"v_mfma_f32_16x16x32_bf16 v[20:23], a[124:127], v[52:55], v[20:23]\n"
"ds_read_b128 v[44:47], v2 offset:45056\n"
"v_mfma_f32_16x16x32_bf16 v[20:23], a[12:15], v[48:51], v[20:23]\n"
"s_waitcnt lgkmcnt(5)\n"
"v_mfma_f32_16x16x32_bf16 v[20:23], a[236:239], v[56:59], v[20:23]\n"
"ds_read_b128 v[48:51], v2 offset:3072\n"
"v_mfma_f32_16x16x32_bf16 v[20:23], v[156:159], v[60:63], v[20:23]\n"
"v_mfma_f32_16x16x32_bf16 v[16:19], v[152:155], v[60:63], v[16:19]\n"
"ds_read_b128 v[52:55], v2 offset:17408\n"
"s_waitcnt lgkmcnt(3)\n"
"v_mfma_f32_16x16x32_bf16 v[16:19], a[232:235], v[56:59], v[16:19]\n"
"v_add_f32 v28, v28, v12\n"
"v_mfma_f32_16x16x32_bf16 v[16:19], a[16:19], v[32:35], v[16:19]\n"
"s_waitcnt vmcnt(0)\n"
"s_barrier\n"
"v_mfma_f32_16x16x32_bf16 v[16:19], a[128:131], v[36:39], v[16:19]\n"
"ds_read_b128 v[56:59], v2 offset:31744\n"
"v_add_f32 v29, v29, v13\n"
"v_mfma_f32_16x16x32_bf16 v[20:23], a[132:135], v[36:39], v[20:23]\n"
"ds_read_b128 v[60:63], v2 offset:46080\n"
"v_add_f32 v30, v30, v14\n"
"v_mfma_f32_16x16x32_bf16 v[20:23], a[20:23], v[32:35], v[20:23]\n"
"v_add_f32 v31, v31, v15\n"
"s_waitcnt lgkmcnt(4)\n"
"v_mfma_f32_16x16x32_bf16 v[20:23], a[244:247], v[40:43], v[20:23]\n"
"ds_read_b128 v[32:35], v2 offset:4096\n"
"s_add_u32 m0, s64, 57344\n"
"v_mfma_f32_16x16x32_bf16 v[20:23], v[164:167], v[44:47], v[20:23]\n"
"buffer_load_dwordx4 v2, s[32:35], s63 offen offset:0 sc1 lds\n"
"v_cvt_pk_bf16_f32 v12, v24, v25\n"
"v_mfma_f32_16x16x32_bf16 v[16:19], v[160:163], v[44:47], v[16:19]\n"
"ds_read_b128 v[36:39], v2 offset:18432\n"
"v_cvt_pk_bf16_f32 v13, v26, v27\n"
"v_mfma_f32_16x16x32_bf16 v[16:19], a[240:243], v[40:43], v[16:19]\n"
"v_cvt_pk_bf16_f32 v14, v28, v29\n"
"s_waitcnt lgkmcnt(4)\n"
"v_mfma_f32_16x16x32_bf16 v[16:19], a[24:27], v[48:51], v[16:19]\n"
"ds_read_b128 v[40:43], v2 offset:32768\n"
"v_cvt_pk_bf16_f32 v15, v30, v31\n"
"v_mfma_f32_16x16x32_bf16 v[16:19], a[136:139], v[52:55], v[16:19]\n"
"buffer_store_dwordx4 v[12:15], v2, s[32:35], s68 offen sc1\n"
"v_add_f32 v10, v24, v25\n"
"v_mfma_f32_16x16x32_bf16 v[20:23], a[140:143], v[52:55], v[20:23]\n"
"ds_read_b128 v[44:47], v2 offset:47104\n"
"v_mul_f32 v11, v24, v24\n"
"v_mfma_f32_16x16x32_bf16 v[20:23], a[28:31], v[48:51], v[20:23]\n"
"v_fmac_f32 v11, v25, v25\n"
"s_waitcnt lgkmcnt(4)\n"
"v_mfma_f32_16x16x32_bf16 v[20:23], a[252:255], v[56:59], v[20:23]\n"
"ds_read_b128 v[48:51], v2 offset:5120\n"
"v_add_f32 v10, v10, v26\n"
"v_mfma_f32_16x16x32_bf16 v[20:23], v[172:175], v[60:63], v[20:23]\n"
"buffer_load_dwordx4 v2, s[32:35], s63 offen offset:1024 sc1 lds\n"
"v_fmac_f32 v11, v26, v26\n"
"v_mfma_f32_16x16x32_bf16 v[16:19], v[168:171], v[60:63], v[16:19]\n"
"ds_read_b128 v[52:55], v2 offset:19456\n"
"v_add_f32 v10, v10, v27\n"
"v_mfma_f32_16x16x32_bf16 v[16:19], a[248:251], v[56:59], v[16:19]\n"
"v_fmac_f32 v11, v27, v27\n"
"s_waitcnt lgkmcnt(4)\n"
"v_mfma_f32_16x16x32_bf16 v[16:19], a[32:35], v[32:35], v[16:19]\n"
"ds_read_b128 v[56:59], v2 offset:33792\n"
"v_add_f32 v10, v10, v28\n"
"v_mfma_f32_16x16x32_bf16 v[16:19], a[144:147], v[36:39], v[16:19]\n"
"buffer_load_dwordx4 v2, s[32:35], s63 offen offset:2048 sc1 lds\n"
"v_fmac_f32 v11, v28, v28\n"
"v_mfma_f32_16x16x32_bf16 v[20:23], a[148:151], v[36:39], v[20:23]\n"
"ds_read_b128 v[60:63], v2 offset:48128\n"
"v_add_f32 v10, v10, v29\n"
"v_mfma_f32_16x16x32_bf16 v[20:23], a[36:39], v[32:35], v[20:23]\n"
"v_fmac_f32 v11, v29, v29\n"
"s_waitcnt lgkmcnt(4)\n"
"v_mfma_f32_16x16x32_bf16 v[20:23], v[68:71], v[40:43], v[20:23]\n"
"ds_read_b128 v[32:35], v2 offset:6144\n"
"v_add_f32 v10, v10, v30\n"
"v_mfma_f32_16x16x32_bf16 v[20:23], v[180:183], v[44:47], v[20:23]\n"
"buffer_load_dwordx4 v2, s[32:35], s63 offen offset:3072 sc1 lds\n"
"s_add_u32 m0, s74, 0\n"
"v_mfma_f32_16x16x32_bf16 v[16:19], v[176:179], v[44:47], v[16:19]\n"
"ds_read_b128 v[36:39], v2 offset:20480\n"
"v_fmac_f32 v11, v30, v30\n"
"v_mfma_f32_16x16x32_bf16 v[16:19], v[64:67], v[40:43], v[16:19]\n"
"v_add_f32 v10, v10, v31\n"
"s_waitcnt lgkmcnt(4)\n"
"v_mfma_f32_16x16x32_bf16 v[16:19], a[40:43], v[48:51], v[16:19]\n"
"ds_read_b128 v[40:43], v2 offset:34816\n"
"v_fmac_f32 v11, v31, v31\n"
"v_mfma_f32_16x16x32_bf16 v[16:19], a[152:155], v[52:55], v[16:19]\n"
"buffer_load_dwordx4 v2, s[32:35], s71 offen offset:0 sc1 lds\n"
"v_permlane16_swap_b32 v10, v11\n"
"v_mfma_f32_16x16x32_bf16 v[20:23], a[156:159], v[52:55], v[20:23]\n"
"ds_read_b128 v[44:47], v2 offset:49152\n"
"v_add_f32 v10, v10, v11\n"
"v_mfma_f32_16x16x32_bf16 v[20:23], a[44:47], v[48:51], v[20:23]\n"
"s_waitcnt lgkmcnt(4)\n"
"v_mfma_f32_16x16x32_bf16 v[20:23], v[76:79], v[56:59], v[20:23]\n"
"ds_read_b128 v[48:51], v2 offset:7168\n"
"v_permlane32_swap_b32 v10, v11\n"
"v_mfma_f32_16x16x32_bf16 v[20:23], v[188:191], v[60:63], v[20:23]\n"
"buffer_load_dwordx4 v2, s[32:35], s71 offen offset:1024 sc1 lds\n"
"s_add_u32 s71, s71, 1187840\n"
"v_mfma_f32_16x16x32_bf16 v[16:19], v[184:187], v[60:63], v[16:19]\n"
"ds_read_b128 v[52:55], v2 offset:21504\n"
"s_add_u32 s63, s63, 4096\n"
"v_mfma_f32_16x16x32_bf16 v[16:19], v[72:75], v[56:59], v[16:19]\n"
"s_add_u32 m0, s64, 61440\n"
"s_waitcnt lgkmcnt(4)\n"
"v_mfma_f32_16x16x32_bf16 v[16:19], a[48:51], v[32:35], v[16:19]\n"
"ds_read_b128 v[56:59], v2 offset:35840\n"
"v_add_f32 v10, v10, v11\n"
"v_mfma_f32_16x16x32_bf16 v[16:19], a[160:163], v[36:39], v[16:19]\n"
"buffer_store_dword v10, v3, s[32:35], s68 offen offset:1024 sc1\n"
"s_mov_b32 s67, s68\n"
"v_mfma_f32_16x16x32_bf16 v[20:23], a[164:167], v[36:39], v[20:23]\n"
"ds_read_b128 v[60:63], v2 offset:50176\n"
"s_mov_b32 s68, s70\n"
"v_mfma_f32_16x16x32_bf16 v[20:23], a[52:55], v[32:35], v[20:23]\n"
"s_add_u32 s70, s70, 1187840\n"
"s_waitcnt lgkmcnt(4)\n"
"v_mfma_f32_16x16x32_bf16 v[20:23], v[84:87], v[40:43], v[20:23]\n"
"ds_read_b128 v[32:35], v2 offset:8192\n"
"v_mfma_f32_16x16x32_bf16 v[20:23], v[196:199], v[44:47], v[20:23]\n"
"buffer_load_dwordx4 v2, s[32:35], s63 offen offset:0 sc1 lds\n"
"v_mfma_f32_16x16x32_bf16 v[16:19], v[192:195], v[44:47], v[16:19]\n"
"ds_read_b128 v[36:39], v2 offset:22528\n"
"v_mfma_f32_16x16x32_bf16 v[16:19], v[80:83], v[40:43], v[16:19]\n"
"s_waitcnt lgkmcnt(4)\n"
"v_mfma_f32_16x16x32_bf16 v[16:19], a[56:59], v[48:51], v[16:19]\n"
"ds_read_b128 v[40:43], v2 offset:36864\n"
"v_mfma_f32_16x16x32_bf16 v[16:19], a[168:171], v[52:55], v[16:19]\n"
"buffer_load_dwordx4 v2, s[32:35], s63 offen offset:1024 sc1 lds\n"
"v_mfma_f32_16x16x32_bf16 v[20:23], a[172:175], v[52:55], v[20:23]\n"
"ds_read_b128 v[44:47], v2 offset:51200\n"
"v_mfma_f32_16x16x32_bf16 v[20:23], a[60:63], v[48:51], v[20:23]\n"
"s_waitcnt lgkmcnt(4)\n"
"v_mfma_f32_16x16x32_bf16 v[20:23], v[92:95], v[56:59], v[20:23]\n"
"ds_read_b128 v[48:51], v2 offset:9216\n"
"v_mfma_f32_16x16x32_bf16 v[20:23], v[204:207], v[60:63], v[20:23]\n"
"buffer_load_dwordx4 v2, s[32:35], s63 offen offset:2048 sc1 lds\n"
"v_mfma_f32_16x16x32_bf16 v[16:19], v[200:203], v[60:63], v[16:19]\n"
"ds_read_b128 v[52:55], v2 offset:23552\n"
"v_mfma_f32_16x16x32_bf16 v[16:19], v[88:91], v[56:59], v[16:19]\n"
"s_waitcnt lgkmcnt(4)\n"
"v_mfma_f32_16x16x32_bf16 v[16:19], a[64:67], v[32:35], v[16:19]\n"
"ds_read_b128 v[56:59], v2 offset:37888\n"
"v_mfma_f32_16x16x32_bf16 v[16:19], a[176:179], v[36:39], v[16:19]\n"
"buffer_load_dwordx4 v2, s[32:35], s63 offen offset:3072 sc1 lds\n"
"s_add_u32 s63, s63, 4096\n"
"v_mfma_f32_16x16x32_bf16 v[20:23], a[180:183], v[36:39], v[20:23]\n"
"ds_read_b128 v[60:63], v2 offset:52224\n"
"s_add_u32 m0, s64, 65536\n"
"v_mfma_f32_16x16x32_bf16 v[20:23], a[68:71], v[32:35], v[20:23]\n"
"s_waitcnt lgkmcnt(4)\n"
"v_mfma_f32_16x16x32_bf16 v[20:23], v[100:103], v[40:43], v[20:23]\n"
"ds_read_b128 v[32:35], v2 offset:10240\n"
"v_mfma_f32_16x16x32_bf16 v[20:23], v[212:215], v[44:47], v[20:23]\n"
"buffer_load_dwordx4 v2, s[32:35], s63 offen offset:0 sc1 lds\n"
"v_mfma_f32_16x16x32_bf16 v[16:19], v[208:211], v[44:47], v[16:19]\n"
"ds_read_b128 v[36:39], v2 offset:24576\n"
"v_mfma_f32_16x16x32_bf16 v[16:19], v[96:99], v[40:43], v[16:19]\n"
"s_waitcnt lgkmcnt(4)\n"
"v_mfma_f32_16x16x32_bf16 v[16:19], a[72:75], v[48:51], v[16:19]\n"
"ds_read_b128 v[40:43], v2 offset:38912\n"
"v_mfma_f32_16x16x32_bf16 v[16:19], a[184:187], v[52:55], v[16:19]\n"
"buffer_load_dwordx4 v2, s[32:35], s63 offen offset:1024 sc1 lds\n"
"v_mfma_f32_16x16x32_bf16 v[20:23], a[188:191], v[52:55], v[20:23]\n"
"ds_read_b128 v[44:47], v2 offset:53248\n"
"v_mfma_f32_16x16x32_bf16 v[20:23], a[76:79], v[48:51], v[20:23]\n"
"s_waitcnt lgkmcnt(4)\n"
"v_mfma_f32_16x16x32_bf16 v[20:23], v[108:111], v[56:59], v[20:23]\n"
"ds_read_b128 v[48:51], v2 offset:11264\n"
"v_mfma_f32_16x16x32_bf16 v[20:23], v[220:223], v[60:63], v[20:23]\n"
"buffer_load_dwordx4 v2, s[32:35], s63 offen offset:2048 sc1 lds\n"
"v_mfma_f32_16x16x32_bf16 v[16:19], v[216:219], v[60:63], v[16:19]\n"
"ds_read_b128 v[52:55], v2 offset:25600\n"
"v_mfma_f32_16x16x32_bf16 v[16:19], v[104:107], v[56:59], v[16:19]\n"
"s_waitcnt lgkmcnt(4)\n"
"v_mfma_f32_16x16x32_bf16 v[16:19], a[80:83], v[32:35], v[16:19]\n"
"ds_read_b128 v[56:59], v2 offset:39936\n"
"v_mfma_f32_16x16x32_bf16 v[16:19], a[192:195], v[36:39], v[16:19]\n"
"buffer_load_dwordx4 v2, s[32:35], s63 offen offset:3072 sc1 lds\n"
"s_add_u32 s63, s63, 4096\n"
"v_mfma_f32_16x16x32_bf16 v[20:23], a[196:199], v[36:39], v[20:23]\n"
"ds_read_b128 v[60:63], v2 offset:54272\n"
"s_add_u32 m0, s64, 69632\n"
"v_mfma_f32_16x16x32_bf16 v[20:23], a[84:87], v[32:35], v[20:23]\n"
"s_waitcnt lgkmcnt(4)\n"
"v_mfma_f32_16x16x32_bf16 v[20:23], v[116:119], v[40:43], v[20:23]\n"
"ds_read_b128 v[32:35], v2 offset:12288\n"
"v_mfma_f32_16x16x32_bf16 v[20:23], v[228:231], v[44:47], v[20:23]\n"
"buffer_load_dwordx4 v2, s[32:35], s63 offen offset:0 sc1 lds\n"
"v_mfma_f32_16x16x32_bf16 v[16:19], v[224:227], v[44:47], v[16:19]\n"
"ds_read_b128 v[36:39], v2 offset:26624\n"
"v_mfma_f32_16x16x32_bf16 v[16:19], v[112:115], v[40:43], v[16:19]\n"
"s_waitcnt lgkmcnt(4)\n"
"v_mfma_f32_16x16x32_bf16 v[16:19], a[88:91], v[48:51], v[16:19]\n"
"ds_read_b128 v[40:43], v2 offset:40960\n"
"v_mfma_f32_16x16x32_bf16 v[16:19], a[200:203], v[52:55], v[16:19]\n"
"buffer_load_dwordx4 v2, s[32:35], s63 offen offset:1024 sc1 lds\n"
"s_add_u32 s63, s63, 1175552\n"
"v_mfma_f32_16x16x32_bf16 v[20:23], a[204:207], v[52:55], v[20:23]\n"
"ds_read_b128 v[44:47], v2 offset:55296\n"
"v_mfma_f32_16x16x32_bf16 v[20:23], a[92:95], v[48:51], v[20:23]\n"
"s_waitcnt lgkmcnt(4)\n"
"v_mfma_f32_16x16x32_bf16 v[20:23], v[124:127], v[56:59], v[20:23]\n"
"ds_read_b128 v[48:51], v2 offset:13312\n"
"v_mfma_f32_16x16x32_bf16 v[20:23], v[236:239], v[60:63], v[20:23]\n"
"v_mfma_f32_16x16x32_bf16 v[16:19], v[232:235], v[60:63], v[16:19]\n"
"ds_read_b128 v[52:55], v2 offset:27648\n"
"v_mfma_f32_16x16x32_bf16 v[16:19], v[120:123], v[56:59], v[16:19]\n"
"s_waitcnt lgkmcnt(4)\n"
"v_mfma_f32_16x16x32_bf16 v[16:19], a[96:99], v[32:35], v[16:19]\n"
"s_waitcnt vmcnt(13)\n"
"s_waitcnt vmcnt(10)\n"
"s_barrier\n"
"v_mfma_f32_16x16x32_bf16 v[16:19], a[208:211], v[36:39], v[16:19]\n"
"ds_read_b128 v[56:59], v2 offset:41984\n"
"v_mfma_f32_16x16x32_bf16 v[20:23], a[212:215], v[36:39], v[20:23]\n"
"ds_read_b128 v[60:63], v2 offset:56320\n"
"v_mfma_f32_16x16x32_bf16 v[20:23], a[100:103], v[32:35], v[20:23]\n"
"s_waitcnt lgkmcnt(4)\n"
"v_mfma_f32_16x16x32_bf16 v[20:23], v[132:135], v[40:43], v[20:23]\n"
"ds_read_b128 v[32:35], v4 offset:0\n"
"v_mfma_f32_16x16x32_bf16 v[20:23], v[244:247], v[44:47], v[20:23]\n"
"v_mfma_f32_16x16x32_bf16 v[16:19], v[240:243], v[44:47], v[16:19]\n"
"ds_read_b128 v[36:39], v4 offset:14336\n"
"v_mfma_f32_16x16x32_bf16 v[16:19], v[128:131], v[40:43], v[16:19]\n"
"s_waitcnt lgkmcnt(4)\n"
"v_mfma_f32_16x16x32_bf16 v[16:19], a[104:107], v[48:51], v[16:19]\n"
"ds_read_b128 v[40:43], v4 offset:28672\n"
"v_mfma_f32_16x16x32_bf16 v[16:19], a[216:219], v[52:55], v[16:19]\n"
"v_mfma_f32_16x16x32_bf16 v[20:23], a[220:223], v[52:55], v[20:23]\n"
"ds_read_b128 v[44:47], v4 offset:43008\n"
"v_mfma_f32_16x16x32_bf16 v[20:23], a[108:111], v[48:51], v[20:23]\n"
"s_waitcnt lgkmcnt(4)\n"
"v_mfma_f32_16x16x32_bf16 v[20:23], v[140:143], v[56:59], v[20:23]\n"
"ds_read_b128 v[48:51], v4 offset:1024\n"
"v_mfma_f32_16x16x32_bf16 v[20:23], v[252:255], v[60:63], v[20:23]\n"
"v_mfma_f32_16x16x32_bf16 v[16:19], v[248:251], v[60:63], v[16:19]\n"
"ds_read_b128 v[52:55], v4 offset:15360\n"
"s_sub_u32 s62, s62, 1\n"
"v_mfma_f32_16x16x32_bf16 v[16:19], v[136:139], v[56:59], v[16:19]\n"
"s_cbranch_scc1 .L_dab_3\n"
"s_waitcnt lgkmcnt(4)\n"
"v_mfma_f32_16x16x32_bf16 v[24:27], a[0:3], v[32:35], 0\n"
"ds_read_b128 v[56:59], v4 offset:29696\n"
"ds_read_b128 v[12:15], v5 offset:0\n"
"v_mfma_f32_16x16x32_bf16 v[24:27], a[112:115], v[36:39], v[24:27]\n"
"v_mfma_f32_16x16x32_bf16 v[28:31], a[116:119], v[36:39], 0\n"
"ds_read_b128 v[60:63], v4 offset:44032\n"
"v_mfma_f32_16x16x32_bf16 v[28:31], a[4:7], v[32:35], v[28:31]\n"
"s_waitcnt lgkmcnt(5)\n"
"v_mfma_f32_16x16x32_bf16 v[28:31], a[228:231], v[40:43], v[28:31]\n"
"ds_read_b128 v[32:35], v4 offset:2048\n"
"v_mfma_f32_16x16x32_bf16 v[28:31], v[148:151], v[44:47], v[28:31]\n"
"s_waitcnt lgkmcnt(2)\n"
"v_add_f32 v16, v16, v12\n"
"v_mfma_f32_16x16x32_bf16 v[24:27], v[144:147], v[44:47], v[24:27]\n"
"ds_read_b128 v[36:39], v4 offset:16384\n"
"v_add_f32 v17, v17, v13\n"
"v_mfma_f32_16x16x32_bf16 v[24:27], a[224:227], v[40:43], v[24:27]\n"
"v_add_f32 v18, v18, v14\n"
"v_mfma_f32_16x16x32_bf16 v[24:27], a[8:11], v[48:51], v[24:27]\n"
"ds_read_b128 v[40:43], v4 offset:30720\n"
"v_add_f32 v19, v19, v15\n"
"v_mfma_f32_16x16x32_bf16 v[24:27], a[120:123], v[52:55], v[24:27]\n"
"ds_read_b128 v[12:15], v5 offset:1024\n"
"v_mfma_f32_16x16x32_bf16 v[28:31], a[124:127], v[52:55], v[28:31]\n"
"ds_read_b128 v[44:47], v4 offset:45056\n"
"v_mfma_f32_16x16x32_bf16 v[28:31], a[12:15], v[48:51], v[28:31]\n"
"s_waitcnt lgkmcnt(5)\n"
"v_mfma_f32_16x16x32_bf16 v[28:31], a[236:239], v[56:59], v[28:31]\n"
"ds_read_b128 v[48:51], v4 offset:3072\n"
"v_mfma_f32_16x16x32_bf16 v[28:31], v[156:159], v[60:63], v[28:31]\n"
"v_mfma_f32_16x16x32_bf16 v[24:27], v[152:155], v[60:63], v[24:27]\n"
"ds_read_b128 v[52:55], v4 offset:17408\n"
"s_waitcnt lgkmcnt(3)\n"
"v_mfma_f32_16x16x32_bf16 v[24:27], a[232:235], v[56:59], v[24:27]\n"
"v_add_f32 v20, v20, v12\n"
"v_mfma_f32_16x16x32_bf16 v[24:27], a[16:19], v[32:35], v[24:27]\n"
"s_waitcnt vmcnt(0)\n"
"s_barrier\n"
"v_mfma_f32_16x16x32_bf16 v[24:27], a[128:131], v[36:39], v[24:27]\n"
"ds_read_b128 v[56:59], v4 offset:31744\n"
"v_add_f32 v21, v21, v13\n"
"v_mfma_f32_16x16x32_bf16 v[28:31], a[132:135], v[36:39], v[28:31]\n"
"ds_read_b128 v[60:63], v4 offset:46080\n"
"v_add_f32 v22, v22, v14\n"
"v_mfma_f32_16x16x32_bf16 v[28:31], a[20:23], v[32:35], v[28:31]\n"
"v_add_f32 v23, v23, v15\n"
"s_waitcnt lgkmcnt(4)\n"
"v_mfma_f32_16x16x32_bf16 v[28:31], a[244:247], v[40:43], v[28:31]\n"
"ds_read_b128 v[32:35], v4 offset:4096\n"
"s_add_u32 m0, s64, 0\n"
"v_mfma_f32_16x16x32_bf16 v[28:31], v[164:167], v[44:47], v[28:31]\n"
"buffer_load_dwordx4 v2, s[32:35], s63 offen offset:0 sc1 lds\n"
"v_cvt_pk_bf16_f32 v12, v16, v17\n"
"v_mfma_f32_16x16x32_bf16 v[24:27], v[160:163], v[44:47], v[24:27]\n"
"ds_read_b128 v[36:39], v4 offset:18432\n"
"v_cvt_pk_bf16_f32 v13, v18, v19\n"
"v_mfma_f32_16x16x32_bf16 v[24:27], a[240:243], v[40:43], v[24:27]\n"
"v_cvt_pk_bf16_f32 v14, v20, v21\n"
"s_waitcnt lgkmcnt(4)\n"
"v_mfma_f32_16x16x32_bf16 v[24:27], a[24:27], v[48:51], v[24:27]\n"
"ds_read_b128 v[40:43], v4 offset:32768\n"
"v_cvt_pk_bf16_f32 v15, v22, v23\n"
"v_mfma_f32_16x16x32_bf16 v[24:27], a[136:139], v[52:55], v[24:27]\n"
"buffer_store_dwordx4 v[12:15], v2, s[32:35], s69 offen sc1\n"
"v_add_f32 v10, v16, v17\n"
"v_mfma_f32_16x16x32_bf16 v[28:31], a[140:143], v[52:55], v[28:31]\n"
"ds_read_b128 v[44:47], v4 offset:47104\n"
"v_mul_f32 v11, v16, v16\n"
"v_mfma_f32_16x16x32_bf16 v[28:31], a[28:31], v[48:51], v[28:31]\n"
"v_fmac_f32 v11, v17, v17\n"
"s_waitcnt lgkmcnt(4)\n"
"v_mfma_f32_16x16x32_bf16 v[28:31], a[252:255], v[56:59], v[28:31]\n"
"ds_read_b128 v[48:51], v4 offset:5120\n"
"v_add_f32 v10, v10, v18\n"
"v_mfma_f32_16x16x32_bf16 v[28:31], v[172:175], v[60:63], v[28:31]\n"
"buffer_load_dwordx4 v2, s[32:35], s63 offen offset:1024 sc1 lds\n"
"v_fmac_f32 v11, v18, v18\n"
"v_mfma_f32_16x16x32_bf16 v[24:27], v[168:171], v[60:63], v[24:27]\n"
"ds_read_b128 v[52:55], v4 offset:19456\n"
"v_add_f32 v10, v10, v19\n"
"v_mfma_f32_16x16x32_bf16 v[24:27], a[248:251], v[56:59], v[24:27]\n"
"v_fmac_f32 v11, v19, v19\n"
"s_waitcnt lgkmcnt(4)\n"
"v_mfma_f32_16x16x32_bf16 v[24:27], a[32:35], v[32:35], v[24:27]\n"
"ds_read_b128 v[56:59], v4 offset:33792\n"
"v_add_f32 v10, v10, v20\n"
"v_mfma_f32_16x16x32_bf16 v[24:27], a[144:147], v[36:39], v[24:27]\n"
"buffer_load_dwordx4 v2, s[32:35], s63 offen offset:2048 sc1 lds\n"
"v_fmac_f32 v11, v20, v20\n"
"v_mfma_f32_16x16x32_bf16 v[28:31], a[148:151], v[36:39], v[28:31]\n"
"ds_read_b128 v[60:63], v4 offset:48128\n"
"v_add_f32 v10, v10, v21\n"
"v_mfma_f32_16x16x32_bf16 v[28:31], a[36:39], v[32:35], v[28:31]\n"
"v_fmac_f32 v11, v21, v21\n"
"s_waitcnt lgkmcnt(4)\n"
"v_mfma_f32_16x16x32_bf16 v[28:31], v[68:71], v[40:43], v[28:31]\n"
"ds_read_b128 v[32:35], v4 offset:6144\n"
"v_add_f32 v10, v10, v22\n"
"v_mfma_f32_16x16x32_bf16 v[28:31], v[180:183], v[44:47], v[28:31]\n"
"buffer_load_dwordx4 v2, s[32:35], s63 offen offset:3072 sc1 lds\n"
"s_add_u32 m0, s74, 8192\n"
"v_mfma_f32_16x16x32_bf16 v[24:27], v[176:179], v[44:47], v[24:27]\n"
"ds_read_b128 v[36:39], v4 offset:20480\n"
"v_fmac_f32 v11, v22, v22\n"
"v_mfma_f32_16x16x32_bf16 v[24:27], v[64:67], v[40:43], v[24:27]\n"
"v_add_f32 v10, v10, v23\n"
"s_waitcnt lgkmcnt(4)\n"
"v_mfma_f32_16x16x32_bf16 v[24:27], a[40:43], v[48:51], v[24:27]\n"
"ds_read_b128 v[40:43], v4 offset:34816\n"
"v_fmac_f32 v11, v23, v23\n"
"v_mfma_f32_16x16x32_bf16 v[24:27], a[152:155], v[52:55], v[24:27]\n"
"buffer_load_dwordx4 v2, s[32:35], s71 offen offset:0 sc1 lds\n"
"v_permlane16_swap_b32 v10, v11\n"
"v_mfma_f32_16x16x32_bf16 v[28:31], a[156:159], v[52:55], v[28:31]\n"
"ds_read_b128 v[44:47], v4 offset:49152\n"
"v_add_f32 v10, v10, v11\n"
"v_mfma_f32_16x16x32_bf16 v[28:31], a[44:47], v[48:51], v[28:31]\n"
"s_waitcnt lgkmcnt(4)\n"
"v_mfma_f32_16x16x32_bf16 v[28:31], v[76:79], v[56:59], v[28:31]\n"
"ds_read_b128 v[48:51], v4 offset:7168\n"
"v_permlane32_swap_b32 v10, v11\n"
"v_mfma_f32_16x16x32_bf16 v[28:31], v[188:191], v[60:63], v[28:31]\n"
"buffer_load_dwordx4 v2, s[32:35], s71 offen offset:1024 sc1 lds\n"
"s_add_u32 s71, s71, 1187840\n"
"v_mfma_f32_16x16x32_bf16 v[24:27], v[184:187], v[60:63], v[24:27]\n"
"ds_read_b128 v[52:55], v4 offset:21504\n"
"s_add_u32 s63, s63, 4096\n"
"v_mfma_f32_16x16x32_bf16 v[24:27], v[72:75], v[56:59], v[24:27]\n"
"s_add_u32 m0, s64, 4096\n"
"s_waitcnt lgkmcnt(4)\n"
"v_mfma_f32_16x16x32_bf16 v[24:27], a[48:51], v[32:35], v[24:27]\n"
"ds_read_b128 v[56:59], v4 offset:35840\n"
"v_add_f32 v10, v10, v11\n"
"v_mfma_f32_16x16x32_bf16 v[24:27], a[160:163], v[36:39], v[24:27]\n"
"buffer_store_dword v10, v3, s[32:35], s69 offen offset:1024 sc1\n"
"s_mov_b32 s67, s69\n"
"v_mfma_f32_16x16x32_bf16 v[28:31], a[164:167], v[36:39], v[28:31]\n"
"ds_read_b128 v[60:63], v4 offset:50176\n"
"s_mov_b32 s69, s70\n"
"v_mfma_f32_16x16x32_bf16 v[28:31], a[52:55], v[32:35], v[28:31]\n"
"s_add_u32 s70, s70, 1187840\n"
"s_waitcnt lgkmcnt(4)\n"
"v_mfma_f32_16x16x32_bf16 v[28:31], v[84:87], v[40:43], v[28:31]\n"
"ds_read_b128 v[32:35], v4 offset:8192\n"
"v_mfma_f32_16x16x32_bf16 v[28:31], v[196:199], v[44:47], v[28:31]\n"
"buffer_load_dwordx4 v2, s[32:35], s63 offen offset:0 sc1 lds\n"
"v_mfma_f32_16x16x32_bf16 v[24:27], v[192:195], v[44:47], v[24:27]\n"
"ds_read_b128 v[36:39], v4 offset:22528\n"
"v_mfma_f32_16x16x32_bf16 v[24:27], v[80:83], v[40:43], v[24:27]\n"
"s_waitcnt lgkmcnt(4)\n"
"v_mfma_f32_16x16x32_bf16 v[24:27], a[56:59], v[48:51], v[24:27]\n"
"ds_read_b128 v[40:43], v4 offset:36864\n"
"v_mfma_f32_16x16x32_bf16 v[24:27], a[168:171], v[52:55], v[24:27]\n"
"buffer_load_dwordx4 v2, s[32:35], s63 offen offset:1024 sc1 lds\n"
"v_mfma_f32_16x16x32_bf16 v[28:31], a[172:175], v[52:55], v[28:31]\n"
"ds_read_b128 v[44:47], v4 offset:51200\n"
"v_mfma_f32_16x16x32_bf16 v[28:31], a[60:63], v[48:51], v[28:31]\n"
"s_waitcnt lgkmcnt(4)\n"
"v_mfma_f32_16x16x32_bf16 v[28:31], v[92:95], v[56:59], v[28:31]\n"
"ds_read_b128 v[48:51], v4 offset:9216\n"
"v_mfma_f32_16x16x32_bf16 v[28:31], v[204:207], v[60:63], v[28:31]\n"
"buffer_load_dwordx4 v2, s[32:35], s63 offen offset:2048 sc1 lds\n"
"v_mfma_f32_16x16x32_bf16 v[24:27], v[200:203], v[60:63], v[24:27]\n"
"ds_read_b128 v[52:55], v4 offset:23552\n"
"v_mfma_f32_16x16x32_bf16 v[24:27], v[88:91], v[56:59], v[24:27]\n"
"s_waitcnt lgkmcnt(4)\n"
"v_mfma_f32_16x16x32_bf16 v[24:27], a[64:67], v[32:35], v[24:27]\n"
"ds_read_b128 v[56:59], v4 offset:37888\n"
"v_mfma_f32_16x16x32_bf16 v[24:27], a[176:179], v[36:39], v[24:27]\n"
"buffer_load_dwordx4 v2, s[32:35], s63 offen offset:3072 sc1 lds\n"
"s_add_u32 s63, s63, 4096\n"
"v_mfma_f32_16x16x32_bf16 v[28:31], a[180:183], v[36:39], v[28:31]\n"
"ds_read_b128 v[60:63], v4 offset:52224\n"
"s_add_u32 m0, s64, 8192\n"
"v_mfma_f32_16x16x32_bf16 v[28:31], a[68:71], v[32:35], v[28:31]\n"
"s_waitcnt lgkmcnt(4)\n"
"v_mfma_f32_16x16x32_bf16 v[28:31], v[100:103], v[40:43], v[28:31]\n"
"ds_read_b128 v[32:35], v4 offset:10240\n"
"v_mfma_f32_16x16x32_bf16 v[28:31], v[212:215], v[44:47], v[28:31]\n"
"buffer_load_dwordx4 v2, s[32:35], s63 offen offset:0 sc1 lds\n"
"v_mfma_f32_16x16x32_bf16 v[24:27], v[208:211], v[44:47], v[24:27]\n"
"ds_read_b128 v[36:39], v4 offset:24576\n"
"v_mfma_f32_16x16x32_bf16 v[24:27], v[96:99], v[40:43], v[24:27]\n"
"s_waitcnt lgkmcnt(4)\n"
"v_mfma_f32_16x16x32_bf16 v[24:27], a[72:75], v[48:51], v[24:27]\n"
"ds_read_b128 v[40:43], v4 offset:38912\n"
"v_mfma_f32_16x16x32_bf16 v[24:27], a[184:187], v[52:55], v[24:27]\n"
"buffer_load_dwordx4 v2, s[32:35], s63 offen offset:1024 sc1 lds\n"
"v_mfma_f32_16x16x32_bf16 v[28:31], a[188:191], v[52:55], v[28:31]\n"
"ds_read_b128 v[44:47], v4 offset:53248\n"
"v_mfma_f32_16x16x32_bf16 v[28:31], a[76:79], v[48:51], v[28:31]\n"
"s_waitcnt lgkmcnt(4)\n"
"v_mfma_f32_16x16x32_bf16 v[28:31], v[108:111], v[56:59], v[28:31]\n"
"ds_read_b128 v[48:51], v4 offset:11264\n"
"v_mfma_f32_16x16x32_bf16 v[28:31], v[220:223], v[60:63], v[28:31]\n"
"buffer_load_dwordx4 v2, s[32:35], s63 offen offset:2048 sc1 lds\n"
"v_mfma_f32_16x16x32_bf16 v[24:27], v[216:219], v[60:63], v[24:27]\n"
"ds_read_b128 v[52:55], v4 offset:25600\n"
"v_mfma_f32_16x16x32_bf16 v[24:27], v[104:107], v[56:59], v[24:27]\n"
"s_waitcnt lgkmcnt(4)\n"
"v_mfma_f32_16x16x32_bf16 v[24:27], a[80:83], v[32:35], v[24:27]\n"
"ds_read_b128 v[56:59], v4 offset:39936\n"
"v_mfma_f32_16x16x32_bf16 v[24:27], a[192:195], v[36:39], v[24:27]\n"
"buffer_load_dwordx4 v2, s[32:35], s63 offen offset:3072 sc1 lds\n"
"s_add_u32 s63, s63, 4096\n"
"v_mfma_f32_16x16x32_bf16 v[28:31], a[196:199], v[36:39], v[28:31]\n"
"ds_read_b128 v[60:63], v4 offset:54272\n"
"s_add_u32 m0, s64, 12288\n"
"v_mfma_f32_16x16x32_bf16 v[28:31], a[84:87], v[32:35], v[28:31]\n"
"s_waitcnt lgkmcnt(4)\n"
"v_mfma_f32_16x16x32_bf16 v[28:31], v[116:119], v[40:43], v[28:31]\n"
"ds_read_b128 v[32:35], v4 offset:12288\n"
"v_mfma_f32_16x16x32_bf16 v[28:31], v[228:231], v[44:47], v[28:31]\n"
"buffer_load_dwordx4 v2, s[32:35], s63 offen offset:0 sc1 lds\n"
"v_mfma_f32_16x16x32_bf16 v[24:27], v[224:227], v[44:47], v[24:27]\n"
"ds_read_b128 v[36:39], v4 offset:26624\n"
"v_mfma_f32_16x16x32_bf16 v[24:27], v[112:115], v[40:43], v[24:27]\n"
"s_waitcnt lgkmcnt(4)\n"
"v_mfma_f32_16x16x32_bf16 v[24:27], a[88:91], v[48:51], v[24:27]\n"
"ds_read_b128 v[40:43], v4 offset:40960\n"
"v_mfma_f32_16x16x32_bf16 v[24:27], a[200:203], v[52:55], v[24:27]\n"
"buffer_load_dwordx4 v2, s[32:35], s63 offen offset:1024 sc1 lds\n"
"s_add_u32 s63, s63, 1175552\n"
"v_mfma_f32_16x16x32_bf16 v[28:31], a[204:207], v[52:55], v[28:31]\n"
"ds_read_b128 v[44:47], v4 offset:55296\n"
"v_mfma_f32_16x16x32_bf16 v[28:31], a[92:95], v[48:51], v[28:31]\n"
"s_waitcnt lgkmcnt(4)\n"
"v_mfma_f32_16x16x32_bf16 v[28:31], v[124:127], v[56:59], v[28:31]\n"
"ds_read_b128 v[48:51], v4 offset:13312\n"
"v_mfma_f32_16x16x32_bf16 v[28:31], v[236:239], v[60:63], v[28:31]\n"
"v_mfma_f32_16x16x32_bf16 v[24:27], v[232:235], v[60:63], v[24:27]\n"
"ds_read_b128 v[52:55], v4 offset:27648\n"
"v_mfma_f32_16x16x32_bf16 v[24:27], v[120:123], v[56:59], v[24:27]\n"
"s_waitcnt lgkmcnt(4)\n"
"v_mfma_f32_16x16x32_bf16 v[24:27], a[96:99], v[32:35], v[24:27]\n"
"s_waitcnt vmcnt(13)\n"
"s_waitcnt vmcnt(10)\n"
"s_barrier\n"
"v_mfma_f32_16x16x32_bf16 v[24:27], a[208:211], v[36:39], v[24:27]\n"
"ds_read_b128 v[56:59], v4 offset:41984\n"
"v_mfma_f32_16x16x32_bf16 v[28:31], a[212:215], v[36:39], v[28:31]\n"
"ds_read_b128 v[60:63], v4 offset:56320\n"
"v_mfma_f32_16x16x32_bf16 v[28:31], a[100:103], v[32:35], v[28:31]\n"
"s_waitcnt lgkmcnt(4)\n"
"v_mfma_f32_16x16x32_bf16 v[28:31], v[132:135], v[40:43], v[28:31]\n"
"ds_read_b128 v[32:35], v2 offset:0\n"
"v_mfma_f32_16x16x32_bf16 v[28:31], v[244:247], v[44:47], v[28:31]\n"
"v_mfma_f32_16x16x32_bf16 v[24:27], v[240:243], v[44:47], v[24:27]\n"
"ds_read_b128 v[36:39], v2 offset:14336\n"
"v_mfma_f32_16x16x32_bf16 v[24:27], v[128:131], v[40:43], v[24:27]\n"
"s_waitcnt lgkmcnt(4)\n"
"v_mfma_f32_16x16x32_bf16 v[24:27], a[104:107], v[48:51], v[24:27]\n"
"ds_read_b128 v[40:43], v2 offset:28672\n"
"v_mfma_f32_16x16x32_bf16 v[24:27], a[216:219], v[52:55], v[24:27]\n"
"v_mfma_f32_16x16x32_bf16 v[28:31], a[220:223], v[52:55], v[28:31]\n"
"ds_read_b128 v[44:47], v2 offset:43008\n"
"v_mfma_f32_16x16x32_bf16 v[28:31], a[108:111], v[48:51], v[28:31]\n"
"s_waitcnt lgkmcnt(4)\n"
"v_mfma_f32_16x16x32_bf16 v[28:31], v[140:143], v[56:59], v[28:31]\n"
"ds_read_b128 v[48:51], v2 offset:1024\n"
"v_mfma_f32_16x16x32_bf16 v[28:31], v[252:255], v[60:63], v[28:31]\n"
"v_mfma_f32_16x16x32_bf16 v[24:27], v[248:251], v[60:63], v[24:27]\n"
"ds_read_b128 v[52:55], v2 offset:15360\n"
"s_cmp_eq_u32 s62, 0\n"
"v_mfma_f32_16x16x32_bf16 v[24:27], v[136:139], v[56:59], v[24:27]\n"
"s_cbranch_scc0 .L_dal_3\n"
"s_mov_b32 s48, 0x7fff0000\n"
"v_writelane_b32 v8, 0, 63\n"
"v_writelane_b32 v9, 0, 63\n"
"v_writelane_b32 v6, 0, 63\n"
"v_writelane_b32 v7, 0, 63\n"
"v_writelane_b32 v0, s48, 63\n"
"s_branch .L_dal_3\n"
".L_dab_3:\n"
"s_mov_b32 s62, 15\n"
"s_add_u32 s74, s74, 8192\n"
"v_lshrrev_b32 v5, 8, v2\n"
"v_lshl_add_u32 v5, v5, 4, s74\n"
"v_and_b32 v3, 128, v2\n"
"v_lshl_add_u32 v5, v3, 2, v5\n"
"v_and_b32 v3, 16, v2\n"
"v_lshl_add_u32 v5, v3, 4, v5\n"
"v_and_b32 v3, 96, v2\n"
"v_lshl_add_u32 v5, v3, 1, v5\n"
"v_lshrrev_b32 v3, 2, v2\n"
"v_add_u32 v3, s74, v3\n"
".p2align 6\n"
".L_dbl_3:\n"
"s_waitcnt lgkmcnt(4)\n"
"v_mfma_f32_16x16x32_bf16 v[24:27], a[0:3], v[32:35], 0\n"
"ds_read_b128 v[56:59], v4 offset:29696\n"
"v_mfma_f32_16x16x32_bf16 v[24:27], a[112:115], v[36:39], v[24:27]\n"
"v_mfma_f32_16x16x32_bf16 v[28:31], a[116:119], v[36:39], 0\n"
"ds_read_b128 v[60:63], v4 offset:44032\n"
"v_mfma_f32_16x16x32_bf16 v[28:31], a[4:7], v[32:35], v[28:31]\n"
"v_cvt_pk_bf16_f32 v15, v22, v23\n"
"s_waitcnt lgkmcnt(4)\n"
"v_mfma_f32_16x16x32_bf16 v[28:31], a[228:231], v[40:43], v[28:31]\n"
"ds_read_b128 v[32:35], v4 offset:2048\n"
"v_cvt_pk_bf16_f32 v14, v20, v21\n"
"v_mfma_f32_16x16x32_bf16 v[28:31], v[148:151], v[44:47], v[28:31]\n"
"v_cvt_pk_bf16_f32 v13, v18, v19\n"
"v_cvt_pk_bf16_f32 v12, v16, v17\n"
"v_mfma_f32_16x16x32_bf16 v[24:27], v[144:147], v[44:47], v[24:27]\n"
"ds_read_b128 v[36:39], v4 offset:16384\n"
"ds_write_b128 v5, v[12:15]\n"
"v_mfma_f32_16x16x32_bf16 v[24:27], a[224:227], v[40:43], v[24:27]\n"
"ds_read2st64_b32 v[12:13], v3 offset0:0 offset1:1\n"
"s_waitcnt lgkmcnt(6)\n"
"v_mfma_f32_16x16x32_bf16 v[24:27], a[8:11], v[48:51], v[24:27]\n"
"ds_read_b128 v[40:43], v4 offset:30720\n"
"ds_read2st64_b32 v[14:15], v3 offset0:2 offset1:3\n"
"v_mfma_f32_16x16x32_bf16 v[24:27], a[120:123], v[52:55], v[24:27]\n"
"v_mfma_f32_16x16x32_bf16 v[28:31], a[124:127], v[52:55], v[28:31]\n"
"ds_read_b128 v[44:47], v4 offset:45056\n"
"v_mfma_f32_16x16x32_bf16 v[28:31], a[12:15], v[48:51], v[28:31]\n"
"s_waitcnt lgkmcnt(7)\n"
"v_mfma_f32_16x16x32_bf16 v[28:31], a[236:239], v[56:59], v[28:31]\n"
"ds_read_b128 v[48:51], v4 offset:3072\n"
"v_mfma_f32_16x16x32_bf16 v[28:31], v[156:159], v[60:63], v[28:31]\n"
"s_waitcnt lgkmcnt(2)\n"
"buffer_atomic_pk_add_bf16 v12, v1, s[44:47], s65 offen offset:0 sc1\n"
"v_mfma_f32_16x16x32_bf16 v[24:27], v[152:155], v[60:63], v[24:27]\n"
"ds_read_b128 v[52:55], v4 offset:17408\n"
"v_mfma_f32_16x16x32_bf16 v[24:27], a[232:235], v[56:59], v[24:27]\n"
"buffer_atomic_pk_add_bf16 v13, v1, s[44:47], s65 offen offset:2048 sc1\n"
"v_mfma_f32_16x16x32_bf16 v[24:27], a[16:19], v[32:35], v[24:27]\n"
"s_waitcnt vmcnt(2)\n"
"s_barrier\n"
"v_mfma_f32_16x16x32_bf16 v[24:27], a[128:131], v[36:39], v[24:27]\n"
"ds_read_b128 v[56:59], v4 offset:31744\n"
"s_add_u32 s65, s65, 16384\n"
"v_mfma_f32_16x16x32_bf16 v[28:31], a[132:135], v[36:39], v[28:31]\n"
"ds_read_b128 v[60:63], v4 offset:46080\n"
"v_mfma_f32_16x16x32_bf16 v[28:31], a[20:23], v[32:35], v[28:31]\n"
"buffer_atomic_pk_add_bf16 v14, v1, s[44:47], s65 offen offset:0 sc1\n"
"s_waitcnt lgkmcnt(4)\n"
"v_mfma_f32_16x16x32_bf16 v[28:31], a[244:247], v[40:43], v[28:31]\n"
"ds_read_b128 v[32:35], v4 offset:4096\n"
"s_add_u32 m0, s64, 0\n"
"v_mfma_f32_16x16x32_bf16 v[28:31], v[164:167], v[44:47], v[28:31]\n"
"buffer_atomic_pk_add_bf16 v15, v1, s[44:47], s65 offen offset:2048 sc1\n"
"s_add_u32 s65, s65, 16384\n"
"v_mfma_f32_16x16x32_bf16 v[24:27], v[160:163], v[44:47], v[24:27]\n"
"ds_read_b128 v[36:39], v4 offset:18432\n"
"v_mfma_f32_16x16x32_bf16 v[24:27], a[240:243], v[40:43], v[24:27]\n"
"s_waitcnt lgkmcnt(4)\n"
"v_mfma_f32_16x16x32_bf16 v[24:27], a[24:27], v[48:51], v[24:27]\n"
"ds_read_b128 v[40:43], v4 offset:32768\n"
"v_mfma_f32_16x16x32_bf16 v[24:27], a[136:139], v[52:55], v[24:27]\n"
"buffer_load_dwordx4 v2, s[32:35], s63 offen offset:0 sc1 lds\n"
"v_mfma_f32_16x16x32_bf16 v[28:31], a[140:143], v[52:55], v[28:31]\n"
"ds_read_b128 v[44:47], v4 offset:47104\n"
"v_mfma_f32_16x16x32_bf16 v[28:31], a[28:31], v[48:51], v[28:31]\n"
"s_waitcnt lgkmcnt(4)\n"
"v_mfma_f32_16x16x32_bf16 v[28:31], a[252:255], v[56:59], v[28:31]\n"
"ds_read_b128 v[48:51], v4 offset:5120\n"
"v_mfma_f32_16x16x32_bf16 v[28:31], v[172:175], v[60:63], v[28:31]\n"
"buffer_load_dwordx4 v2, s[32:35], s63 offen offset:1024 sc1 lds\n"
"v_mfma_f32_16x16x32_bf16 v[24:27], v[168:171], v[60:63], v[24:27]\n"
"ds_read_b128 v[52:55], v4 offset:19456\n"
"v_mfma_f32_16x16x32_bf16 v[24:27], a[248:251], v[56:59], v[24:27]\n"
"s_waitcnt lgkmcnt(4)\n"
"v_mfma_f32_16x16x32_bf16 v[24:27], a[32:35], v[32:35], v[24:27]\n"
"ds_read_b128 v[56:59], v4 offset:33792\n"
"v_mfma_f32_16x16x32_bf16 v[24:27], a[144:147], v[36:39], v[24:27]\n"
"buffer_load_dwordx4 v2, s[32:35], s63 offen offset:2048 sc1 lds\n"
"v_mfma_f32_16x16x32_bf16 v[28:31], a[148:151], v[36:39], v[28:31]\n"
"ds_read_b128 v[60:63], v4 offset:48128\n"
"v_mfma_f32_16x16x32_bf16 v[28:31], a[36:39], v[32:35], v[28:31]\n"
"s_waitcnt lgkmcnt(4)\n"
"v_mfma_f32_16x16x32_bf16 v[28:31], v[68:71], v[40:43], v[28:31]\n"
"ds_read_b128 v[32:35], v4 offset:6144\n"
"v_mfma_f32_16x16x32_bf16 v[28:31], v[180:183], v[44:47], v[28:31]\n"
"buffer_load_dwordx4 v2, s[32:35], s63 offen offset:3072 sc1 lds\n"
"v_mfma_f32_16x16x32_bf16 v[24:27], v[176:179], v[44:47], v[24:27]\n"
"ds_read_b128 v[36:39], v4 offset:20480\n"
"v_mfma_f32_16x16x32_bf16 v[24:27], v[64:67], v[40:43], v[24:27]\n"
"s_waitcnt lgkmcnt(4)\n"
"v_mfma_f32_16x16x32_bf16 v[24:27], a[40:43], v[48:51], v[24:27]\n"
"ds_read_b128 v[40:43], v4 offset:34816\n"
"s_add_u32 s63, s63, 4096\n"
"v_mfma_f32_16x16x32_bf16 v[24:27], a[152:155], v[52:55], v[24:27]\n"
"s_add_u32 m0, s64, 4096\n"
"v_mfma_f32_16x16x32_bf16 v[28:31], a[156:159], v[52:55], v[28:31]\n"
"ds_read_b128 v[44:47], v4 offset:49152\n"
"v_mfma_f32_16x16x32_bf16 v[28:31], a[44:47], v[48:51], v[28:31]\n"
"s_waitcnt lgkmcnt(4)\n"
"v_mfma_f32_16x16x32_bf16 v[28:31], v[76:79], v[56:59], v[28:31]\n"
"ds_read_b128 v[48:51], v4 offset:7168\n"
"v_mfma_f32_16x16x32_bf16 v[28:31], v[188:191], v[60:63], v[28:31]\n"
"buffer_load_dwordx4 v2, s[32:35], s63 offen offset:0 sc1 lds\n"
"v_mfma_f32_16x16x32_bf16 v[24:27], v[184:187], v[60:63], v[24:27]\n"
"ds_read_b128 v[52:55], v4 offset:21504\n"
"v_mfma_f32_16x16x32_bf16 v[24:27], v[72:75], v[56:59], v[24:27]\n"
"s_waitcnt lgkmcnt(4)\n"
"v_mfma_f32_16x16x32_bf16 v[24:27], a[48:51], v[32:35], v[24:27]\n"
"ds_read_b128 v[56:59], v4 offset:35840\n"
"v_mfma_f32_16x16x32_bf16 v[24:27], a[160:163], v[36:39], v[24:27]\n"
"buffer_load_dwordx4 v2, s[32:35], s63 offen offset:1024 sc1 lds\n"
"v_mfma_f32_16x16x32_bf16 v[28:31], a[164:167], v[36:39], v[28:31]\n"
"ds_read_b128 v[60:63], v4 offset:50176\n"
"v_mfma_f32_16x16x32_bf16 v[28:31], a[52:55], v[32:35], v[28:31]\n"
"s_waitcnt lgkmcnt(4)\n"
"v_mfma_f32_16x16x32_bf16 v[28:31], v[84:87], v[40:43], v[28:31]\n"
"ds_read_b128 v[32:35], v4 offset:8192\n"
"v_mfma_f32_16x16x32_bf16 v[28:31], v[196:199], v[44:47], v[28:31]\n"
"buffer_load_dwordx4 v2, s[32:35], s63 offen offset:2048 sc1 lds\n"
"v_mfma_f32_16x16x32_bf16 v[24:27], v[192:195], v[44:47], v[24:27]\n"
"ds_read_b128 v[36:39], v4 offset:22528\n"
"v_mfma_f32_16x16x32_bf16 v[24:27], v[80:83], v[40:43], v[24:27]\n"
"s_waitcnt lgkmcnt(4)\n"
"v_mfma_f32_16x16x32_bf16 v[24:27], a[56:59], v[48:51], v[24:27]\n"
"ds_read_b128 v[40:43], v4 offset:36864\n"
"v_mfma_f32_16x16x32_bf16 v[24:27], a[168:171], v[52:55], v[24:27]\n"
"buffer_load_dwordx4 v2, s[32:35], s63 offen offset:3072 sc1 lds\n"
"v_mfma_f32_16x16x32_bf16 v[28:31], a[172:175], v[52:55], v[28:31]\n"
"ds_read_b128 v[44:47], v4 offset:51200\n"
"v_mfma_f32_16x16x32_bf16 v[28:31], a[60:63], v[48:51], v[28:31]\n"
"s_waitcnt lgkmcnt(4)\n"
"v_mfma_f32_16x16x32_bf16 v[28:31], v[92:95], v[56:59], v[28:31]\n"
"ds_read_b128 v[48:51], v4 offset:9216\n"
"v_mfma_f32_16x16x32_bf16 v[28:31], v[204:207], v[60:63], v[28:31]\n"
"s_add_u32 s63, s63, 4096\n"
"s_add_u32 m0, s64, 8192\n"
"v_mfma_f32_16x16x32_bf16 v[24:27], v[200:203], v[60:63], v[24:27]\n"
"ds_read_b128 v[52:55], v4 offset:23552\n"
"v_mfma_f32_16x16x32_bf16 v[24:27], v[88:91], v[56:59], v[24:27]\n"
"s_waitcnt lgkmcnt(4)\n"
"v_mfma_f32_16x16x32_bf16 v[24:27], a[64:67], v[32:35], v[24:27]\n"
"ds_read_b128 v[56:59], v4 offset:37888\n"
"v_mfma_f32_16x16x32_bf16 v[24:27], a[176:179], v[36:39], v[24:27]\n"
"buffer_load_dwordx4 v2, s[32:35], s63 offen offset:0 sc1 lds\n"
"v_mfma_f32_16x16x32_bf16 v[28:31], a[180:183], v[36:39], v[28:31]\n"
"ds_read_b128 v[60:63], v4 offset:52224\n"
"v_mfma_f32_16x16x32_bf16 v[28:31], a[68:71], v[32:35], v[28:31]\n"
"s_waitcnt lgkmcnt(4)\n"
"v_mfma_f32_16x16x32_bf16 v[28:31], v[100:103], v[40:43], v[28:31]\n"
"ds_read_b128 v[32:35], v4 offset:10240\n"
"v_mfma_f32_16x16x32_bf16 v[28:31], v[212:215], v[44:47], v[28:31]\n"
"buffer_load_dwordx4 v2, s[32:35], s63 offen offset:1024 sc1 lds\n"
"v_mfma_f32_16x16x32_bf16 v[24:27], v[208:211], v[44:47], v[24:27]\n"
"ds_read_b128 v[36:39], v4 offset:24576\n"
"v_mfma_f32_16x16x32_bf16 v[24:27], v[96:99], v[40:43], v[24:27]\n"
"s_waitcnt lgkmcnt(4)\n"
"v_mfma_f32_16x16x32_bf16 v[24:27], a[72:75], v[48:51], v[24:27]\n"
"ds_read_b128 v[40:43], v4 offset:38912\n"
"v_mfma_f32_16x16x32_bf16 v[24:27], a[184:187], v[52:55], v[24:27]\n"
"buffer_load_dwordx4 v2, s[32:35], s63 offen offset:2048 sc1 lds\n"
"v_mfma_f32_16x16x32_bf16 v[28:31], a[188:191], v[52:55], v[28:31]\n"
"ds_read_b128 v[44:47], v4 offset:53248\n"
"v_mfma_f32_16x16x32_bf16 v[28:31], a[76:79], v[48:51], v[28:31]\n"
"s_waitcnt lgkmcnt(4)\n"
"v_mfma_f32_16x16x32_bf16 v[28:31], v[108:111], v[56:59], v[28:31]\n"
"ds_read_b128 v[48:51], v4 offset:11264\n"
"v_mfma_f32_16x16x32_bf16 v[28:31], v[220:223], v[60:63], v[28:31]\n"
"v_mfma_f32_16x16x32_bf16 v[24:27], v[216:219], v[60:63], v[24:27]\n"
"ds_read_b128 v[52:55], v4 offset:25600\n"
"v_mfma_f32_16x16x32_bf16 v[24:27], v[104:107], v[56:59], v[24:27]\n"
"s_waitcnt lgkmcnt(4)\n"
"v_mfma_f32_16x16x32_bf16 v[24:27], a[80:83], v[32:35], v[24:27]\n"
"ds_read_b128 v[56:59], v4 offset:39936\n"
"v_mfma_f32_16x16x32_bf16 v[24:27], a[192:195], v[36:39], v[24:27]\n"
"buffer_load_dwordx4 v2, s[32:35], s63 offen offset:3072 sc1 lds\n"
"s_add_u32 s63, s63, 4096\n"
"v_mfma_f32_16x16x32_bf16 v[28:31], a[196:199], v[36:39], v[28:31]\n"
"ds_read_b128 v[60:63], v4 offset:54272\n"
"s_add_u32 m0, s64, 12288\n"
"v_mfma_f32_16x16x32_bf16 v[28:31], a[84:87], v[32:35], v[28:31]\n"
"s_waitcnt lgkmcnt(4)\n"
"v_mfma_f32_16x16x32_bf16 v[28:31], v[116:119], v[40:43], v[28:31]\n"
"ds_read_b128 v[32:35], v4 offset:12288\n"
"v_mfma_f32_16x16x32_bf16 v[28:31], v[228:231], v[44:47], v[28:31]\n"
"buffer_load_dwordx4 v2, s[32:35], s63 offen offset:0 sc1 lds\n"
"v_mfma_f32_16x16x32_bf16 v[24:27], v[224:227], v[44:47], v[24:27]\n"
"ds_read_b128 v[36:39], v4 offset:26624\n"
"v_mfma_f32_16x16x32_bf16 v[24:27], v[112:115], v[40:43], v[24:27]\n"
"s_waitcnt lgkmcnt(4)\n"
"v_mfma_f32_16x16x32_bf16 v[24:27], a[88:91], v[48:51], v[24:27]\n"
"ds_read_b128 v[40:43], v4 offset:40960\n"
"v_mfma_f32_16x16x32_bf16 v[24:27], a[200:203], v[52:55], v[24:27]\n"
"buffer_load_dwordx4 v2, s[32:35], s63 offen offset:1024 sc1 lds\n"
"s_add_u32 s63, s63, 1175552\n"
"v_mfma_f32_16x16x32_bf16 v[28:31], a[204:207], v[52:55], v[28:31]\n"
"ds_read_b128 v[44:47], v4 offset:55296\n"
"v_mfma_f32_16x16x32_bf16 v[28:31], a[92:95], v[48:51], v[28:31]\n"
"s_waitcnt lgkmcnt(4)\n"
"v_mfma_f32_16x16x32_bf16 v[28:31], v[124:127], v[56:59], v[28:31]\n"
"ds_read_b128 v[48:51], v4 offset:13312\n"
"v_mfma_f32_16x16x32_bf16 v[28:31], v[236:239], v[60:63], v[28:31]\n"
"v_mfma_f32_16x16x32_bf16 v[24:27], v[232:235], v[60:63], v[24:27]\n"
"ds_read_b128 v[52:55], v4 offset:27648\n"
"v_mfma_f32_16x16x32_bf16 v[24:27], v[120:123], v[56:59], v[24:27]\n"
"s_waitcnt lgkmcnt(4)\n"
"v_mfma_f32_16x16x32_bf16 v[24:27], a[96:99], v[32:35], v[24:27]\n"
"s_waitcnt vmcnt(10)\n"
"s_barrier\n"
"v_mfma_f32_16x16x32_bf16 v[24:27], a[208:211], v[36:39], v[24:27]\n"
"ds_read_b128 v[56:59], v4 offset:41984\n"
"v_mfma_f32_16x16x32_bf16 v[28:31], a[212:215], v[36:39], v[28:31]\n"
"ds_read_b128 v[60:63], v4 offset:56320\n"
"v_mfma_f32_16x16x32_bf16 v[28:31], a[100:103], v[32:35], v[28:31]\n"
"s_waitcnt lgkmcnt(4)\n"
"v_mfma_f32_16x16x32_bf16 v[28:31], v[132:135], v[40:43], v[28:31]\n"
"ds_read_b128 v[32:35], v2 offset:0\n"
"v_mfma_f32_16x16x32_bf16 v[28:31], v[244:247], v[44:47], v[28:31]\n"
"v_mfma_f32_16x16x32_bf16 v[24:27], v[240:243], v[44:47], v[24:27]\n"
"ds_read_b128 v[36:39], v2 offset:14336\n"
"v_mfma_f32_16x16x32_bf16 v[24:27], v[128:131], v[40:43], v[24:27]\n"
"s_waitcnt lgkmcnt(4)\n"
"v_mfma_f32_16x16x32_bf16 v[24:27], a[104:107], v[48:51], v[24:27]\n"
"ds_read_b128 v[40:43], v2 offset:28672\n"
"v_mfma_f32_16x16x32_bf16 v[24:27], a[216:219], v[52:55], v[24:27]\n"
"v_mfma_f32_16x16x32_bf16 v[28:31], a[220:223], v[52:55], v[28:31]\n"
"ds_read_b128 v[44:47], v2 offset:43008\n"
"v_mfma_f32_16x16x32_bf16 v[28:31], a[108:111], v[48:51], v[28:31]\n"
"s_waitcnt lgkmcnt(4)\n"
"v_mfma_f32_16x16x32_bf16 v[28:31], v[140:143], v[56:59], v[28:31]\n"
"ds_read_b128 v[48:51], v2 offset:1024\n"
"v_mfma_f32_16x16x32_bf16 v[28:31], v[252:255], v[60:63], v[28:31]\n"
"v_mfma_f32_16x16x32_bf16 v[24:27], v[248:251], v[60:63], v[24:27]\n"
"ds_read_b128 v[52:55], v2 offset:15360\n"
"s_sub_u32 s62, s62, 1\n"
"v_mfma_f32_16x16x32_bf16 v[24:27], v[136:139], v[56:59], v[24:27]\n"
"s_cbranch_scc1 .L_dbt_3\n"
"s_waitcnt lgkmcnt(4)\n"
"v_mfma_f32_16x16x32_bf16 v[16:19], a[0:3], v[32:35], 0\n"
"ds_read_b128 v[56:59], v2 offset:29696\n"
"v_mfma_f32_16x16x32_bf16 v[16:19], a[112:115], v[36:39], v[16:19]\n"
"v_mfma_f32_16x16x32_bf16 v[20:23], a[116:119], v[36:39], 0\n"
"ds_read_b128 v[60:63], v2 offset:44032\n"
"v_mfma_f32_16x16x32_bf16 v[20:23], a[4:7], v[32:35], v[20:23]\n"
"v_cvt_pk_bf16_f32 v15, v30, v31\n"
"s_waitcnt lgkmcnt(4)\n"
"v_mfma_f32_16x16x32_bf16 v[20:23], a[228:231], v[40:43], v[20:23]\n"
"ds_read_b128 v[32:35], v2 offset:2048\n"
"v_cvt_pk_bf16_f32 v14, v28, v29\n"
"v_mfma_f32_16x16x32_bf16 v[20:23], v[148:151], v[44:47], v[20:23]\n"
"v_cvt_pk_bf16_f32 v13, v26, v27\n"
"v_cvt_pk_bf16_f32 v12, v24, v25\n"
"v_mfma_f32_16x16x32_bf16 v[16:19], v[144:147], v[44:47], v[16:19]\n"
"ds_read_b128 v[36:39], v2 offset:16384\n"
"ds_write_b128 v5, v[12:15]\n"
"v_mfma_f32_16x16x32_bf16 v[16:19], a[224:227], v[40:43], v[16:19]\n"
"ds_read2st64_b32 v[12:13], v3 offset0:0 offset1:1\n"
"s_waitcnt lgkmcnt(6)\n"
"v_mfma_f32_16x16x32_bf16 v[16:19], a[8:11], v[48:51], v[16:19]\n"
"ds_read_b128 v[40:43], v2 offset:30720\n"
"ds_read2st64_b32 v[14:15], v3 offset0:2 offset1:3\n"
"v_mfma_f32_16x16x32_bf16 v[16:19], a[120:123], v[52:55], v[16:19]\n"
"v_mfma_f32_16x16x32_bf16 v[20:23], a[124:127], v[52:55], v[20:23]\n"
"ds_read_b128 v[44:47], v2 offset:45056\n"
"v_mfma_f32_16x16x32_bf16 v[20:23], a[12:15], v[48:51], v[20:23]\n"
"s_waitcnt lgkmcnt(7)\n"
"v_mfma_f32_16x16x32_bf16 v[20:23], a[236:239], v[56:59], v[20:23]\n"
"ds_read_b128 v[48:51], v2 offset:3072\n"
"v_mfma_f32_16x16x32_bf16 v[20:23], v[156:159], v[60:63], v[20:23]\n"
"s_waitcnt lgkmcnt(2)\n"
"buffer_atomic_pk_add_bf16 v12, v1, s[44:47], s65 offen offset:0 sc1\n"
"v_mfma_f32_16x16x32_bf16 v[16:19], v[152:155], v[60:63], v[16:19]\n"
"ds_read_b128 v[52:55], v2 offset:17408\n"
"v_mfma_f32_16x16x32_bf16 v[16:19], a[232:235], v[56:59], v[16:19]\n"
"buffer_atomic_pk_add_bf16 v13, v1, s[44:47], s65 offen offset:2048 sc1\n"
"v_mfma_f32_16x16x32_bf16 v[16:19], a[16:19], v[32:35], v[16:19]\n"
"s_waitcnt vmcnt(2)\n"
"s_barrier\n"
"v_mfma_f32_16x16x32_bf16 v[16:19], a[128:131], v[36:39], v[16:19]\n"
"ds_read_b128 v[56:59], v2 offset:31744\n"
"s_add_u32 s65, s65, 16384\n"
"v_mfma_f32_16x16x32_bf16 v[20:23], a[132:135], v[36:39], v[20:23]\n"
"ds_read_b128 v[60:63], v2 offset:46080\n"
"v_mfma_f32_16x16x32_bf16 v[20:23], a[20:23], v[32:35], v[20:23]\n"
"buffer_atomic_pk_add_bf16 v14, v1, s[44:47], s65 offen offset:0 sc1\n"
"s_waitcnt lgkmcnt(4)\n"
"v_mfma_f32_16x16x32_bf16 v[20:23], a[244:247], v[40:43], v[20:23]\n"
"ds_read_b128 v[32:35], v2 offset:4096\n"
"s_add_u32 m0, s64, 57344\n"
"v_mfma_f32_16x16x32_bf16 v[20:23], v[164:167], v[44:47], v[20:23]\n"
"buffer_atomic_pk_add_bf16 v15, v1, s[44:47], s65 offen offset:2048 sc1\n"
"s_add_u32 s65, s65, 16384\n"
"v_mfma_f32_16x16x32_bf16 v[16:19], v[160:163], v[44:47], v[16:19]\n"
"ds_read_b128 v[36:39], v2 offset:18432\n"
"v_mfma_f32_16x16x32_bf16 v[16:19], a[240:243], v[40:43], v[16:19]\n"
"s_waitcnt lgkmcnt(4)\n"
"v_mfma_f32_16x16x32_bf16 v[16:19], a[24:27], v[48:51], v[16:19]\n"
"ds_read_b128 v[40:43], v2 offset:32768\n"
"v_mfma_f32_16x16x32_bf16 v[16:19], a[136:139], v[52:55], v[16:19]\n"
"buffer_load_dwordx4 v2, s[32:35], s63 offen offset:0 sc1 lds\n"
"v_mfma_f32_16x16x32_bf16 v[20:23], a[140:143], v[52:55], v[20:23]\n"
"ds_read_b128 v[44:47], v2 offset:47104\n"
"v_mfma_f32_16x16x32_bf16 v[20:23], a[28:31], v[48:51], v[20:23]\n"
"s_waitcnt lgkmcnt(4)\n"
"v_mfma_f32_16x16x32_bf16 v[20:23], a[252:255], v[56:59], v[20:23]\n"
"ds_read_b128 v[48:51], v2 offset:5120\n"
"v_mfma_f32_16x16x32_bf16 v[20:23], v[172:175], v[60:63], v[20:23]\n"
"buffer_load_dwordx4 v2, s[32:35], s63 offen offset:1024 sc1 lds\n"
"v_mfma_f32_16x16x32_bf16 v[16:19], v[168:171], v[60:63], v[16:19]\n"
"ds_read_b128 v[52:55], v2 offset:19456\n"
"v_mfma_f32_16x16x32_bf16 v[16:19], a[248:251], v[56:59], v[16:19]\n"
"s_waitcnt lgkmcnt(4)\n"
"v_mfma_f32_16x16x32_bf16 v[16:19], a[32:35], v[32:35], v[16:19]\n"
"ds_read_b128 v[56:59], v2 offset:33792\n"
"v_mfma_f32_16x16x32_bf16 v[16:19], a[144:147], v[36:39], v[16:19]\n"
"buffer_load_dwordx4 v2, s[32:35], s63 offen offset:2048 sc1 lds\n"
"v_mfma_f32_16x16x32_bf16 v[20:23], a[148:151], v[36:39], v[20:23]\n"
"ds_read_b128 v[60:63], v2 offset:48128\n"
"v_mfma_f32_16x16x32_bf16 v[20:23], a[36:39], v[32:35], v[20:23]\n"
"s_waitcnt lgkmcnt(4)\n"
"v_mfma_f32_16x16x32_bf16 v[20:23], v[68:71], v[40:43], v[20:23]\n"
"ds_read_b128 v[32:35], v2 offset:6144\n"
"v_mfma_f32_16x16x32_bf16 v[20:23], v[180:183], v[44:47], v[20:23]\n"
"buffer_load_dwordx4 v2, s[32:35], s63 offen offset:3072 sc1 lds\n"
"v_mfma_f32_16x16x32_bf16 v[16:19], v[176:179], v[44:47], v[16:19]\n"
"ds_read_b128 v[36:39], v2 offset:20480\n"
"v_mfma_f32_16x16x32_bf16 v[16:19], v[64:67], v[40:43], v[16:19]\n"
"s_waitcnt lgkmcnt(4)\n"
"v_mfma_f32_16x16x32_bf16 v[16:19], a[40:43], v[48:51], v[16:19]\n"
"ds_read_b128 v[40:43], v2 offset:34816\n"
"s_add_u32 s63, s63, 4096\n"
"v_mfma_f32_16x16x32_bf16 v[16:19], a[152:155], v[52:55], v[16:19]\n"
"s_add_u32 m0, s64, 61440\n"
"v_mfma_f32_16x16x32_bf16 v[20:23], a[156:159], v[52:55], v[20:23]\n"
"ds_read_b128 v[44:47], v2 offset:49152\n"
"v_mfma_f32_16x16x32_bf16 v[20:23], a[44:47], v[48:51], v[20:23]\n"
"s_waitcnt lgkmcnt(4)\n"
"v_mfma_f32_16x16x32_bf16 v[20:23], v[76:79], v[56:59], v[20:23]\n"
"ds_read_b128 v[48:51], v2 offset:7168\n"
"v_mfma_f32_16x16x32_bf16 v[20:23], v[188:191], v[60:63], v[20:23]\n"
"buffer_load_dwordx4 v2, s[32:35], s63 offen offset:0 sc1 lds\n"
"v_mfma_f32_16x16x32_bf16 v[16:19], v[184:187], v[60:63], v[16:19]\n"
"ds_read_b128 v[52:55], v2 offset:21504\n"
"v_mfma_f32_16x16x32_bf16 v[16:19], v[72:75], v[56:59], v[16:19]\n"
"s_waitcnt lgkmcnt(4)\n"
"v_mfma_f32_16x16x32_bf16 v[16:19], a[48:51], v[32:35], v[16:19]\n"
"ds_read_b128 v[56:59], v2 offset:35840\n"
"v_mfma_f32_16x16x32_bf16 v[16:19], a[160:163], v[36:39], v[16:19]\n"
"buffer_load_dwordx4 v2, s[32:35], s63 offen offset:1024 sc1 lds\n"
"v_mfma_f32_16x16x32_bf16 v[20:23], a[164:167], v[36:39], v[20:23]\n"
"ds_read_b128 v[60:63], v2 offset:50176\n"
"v_mfma_f32_16x16x32_bf16 v[20:23], a[52:55], v[32:35], v[20:23]\n"
"s_waitcnt lgkmcnt(4)\n"
"v_mfma_f32_16x16x32_bf16 v[20:23], v[84:87], v[40:43], v[20:23]\n"
"ds_read_b128 v[32:35], v2 offset:8192\n"
"v_mfma_f32_16x16x32_bf16 v[20:23], v[196:199], v[44:47], v[20:23]\n"
"buffer_load_dwordx4 v2, s[32:35], s63 offen offset:2048 sc1 lds\n"
"v_mfma_f32_16x16x32_bf16 v[16:19], v[192:195], v[44:47], v[16:19]\n"
"ds_read_b128 v[36:39], v2 offset:22528\n"
"v_mfma_f32_16x16x32_bf16 v[16:19], v[80:83], v[40:43], v[16:19]\n"
"s_waitcnt lgkmcnt(4)\n"
"v_mfma_f32_16x16x32_bf16 v[16:19], a[56:59], v[48:51], v[16:19]\n"
"ds_read_b128 v[40:43], v2 offset:36864\n"
"v_mfma_f32_16x16x32_bf16 v[16:19], a[168:171], v[52:55], v[16:19]\n"
"buffer_load_dwordx4 v2, s[32:35], s63 offen offset:3072 sc1 lds\n"
"v_mfma_f32_16x16x32_bf16 v[20:23], a[172:175], v[52:55], v[20:23]\n"
"ds_read_b128 v[44:47], v2 offset:51200\n"
"v_mfma_f32_16x16x32_bf16 v[20:23], a[60:63], v[48:51], v[20:23]\n"
"s_waitcnt lgkmcnt(4)\n"
"v_mfma_f32_16x16x32_bf16 v[20:23], v[92:95], v[56:59], v[20:23]\n"
"ds_read_b128 v[48:51], v2 offset:9216\n"
"v_mfma_f32_16x16x32_bf16 v[20:23], v[204:207], v[60:63], v[20:23]\n"
"s_add_u32 s63, s63, 4096\n"
"s_add_u32 m0, s64, 65536\n"
"v_mfma_f32_16x16x32_bf16 v[16:19], v[200:203], v[60:63], v[16:19]\n"
"ds_read_b128 v[52:55], v2 offset:23552\n"
"v_mfma_f32_16x16x32_bf16 v[16:19], v[88:91], v[56:59], v[16:19]\n"
"s_waitcnt lgkmcnt(4)\n"
"v_mfma_f32_16x16x32_bf16 v[16:19], a[64:67], v[32:35], v[16:19]\n"
"ds_read_b128 v[56:59], v2 offset:37888\n"
"v_mfma_f32_16x16x32_bf16 v[16:19], a[176:179], v[36:39], v[16:19]\n"
"buffer_load_dwordx4 v2, s[32:35], s63 offen offset:0 sc1 lds\n"
"v_mfma_f32_16x16x32_bf16 v[20:23], a[180:183], v[36:39], v[20:23]\n"
"ds_read_b128 v[60:63], v2 offset:52224\n"
"v_mfma_f32_16x16x32_bf16 v[20:23], a[68:71], v[32:35], v[20:23]\n"
"s_waitcnt lgkmcnt(4)\n"
"v_mfma_f32_16x16x32_bf16 v[20:23], v[100:103], v[40:43], v[20:23]\n"
"ds_read_b128 v[32:35], v2 offset:10240\n"
"v_mfma_f32_16x16x32_bf16 v[20:23], v[212:215], v[44:47], v[20:23]\n"
"buffer_load_dwordx4 v2, s[32:35], s63 offen offset:1024 sc1 lds\n"
"v_mfma_f32_16x16x32_bf16 v[16:19], v[208:211], v[44:47], v[16:19]\n"
"ds_read_b128 v[36:39], v2 offset:24576\n"
"v_mfma_f32_16x16x32_bf16 v[16:19], v[96:99], v[40:43], v[16:19]\n"
"s_waitcnt lgkmcnt(4)\n"
"v_mfma_f32_16x16x32_bf16 v[16:19], a[72:75], v[48:51], v[16:19]\n"
"ds_read_b128 v[40:43], v2 offset:38912\n"
"v_mfma_f32_16x16x32_bf16 v[16:19], a[184:187], v[52:55], v[16:19]\n"
"buffer_load_dwordx4 v2, s[32:35], s63 offen offset:2048 sc1 lds\n"
"v_mfma_f32_16x16x32_bf16 v[20:23], a[188:191], v[52:55], v[20:23]\n"
"ds_read_b128 v[44:47], v2 offset:53248\n"
"v_mfma_f32_16x16x32_bf16 v[20:23], a[76:79], v[48:51], v[20:23]\n"
"s_waitcnt lgkmcnt(4)\n"
"v_mfma_f32_16x16x32_bf16 v[20:23], v[108:111], v[56:59], v[20:23]\n"
"ds_read_b128 v[48:51], v2 offset:11264\n"
"v_mfma_f32_16x16x32_bf16 v[20:23], v[220:223], v[60:63], v[20:23]\n"
"v_mfma_f32_16x16x32_bf16 v[16:19], v[216:219], v[60:63], v[16:19]\n"
"ds_read_b128 v[52:55], v2 offset:25600\n"
"v_mfma_f32_16x16x32_bf16 v[16:19], v[104:107], v[56:59], v[16:19]\n"
"s_waitcnt lgkmcnt(4)\n"
"v_mfma_f32_16x16x32_bf16 v[16:19], a[80:83], v[32:35], v[16:19]\n"
"ds_read_b128 v[56:59], v2 offset:39936\n"
"v_mfma_f32_16x16x32_bf16 v[16:19], a[192:195], v[36:39], v[16:19]\n"
"buffer_load_dwordx4 v2, s[32:35], s63 offen offset:3072 sc1 lds\n"
"s_add_u32 s63, s63, 4096\n"
"v_mfma_f32_16x16x32_bf16 v[20:23], a[196:199], v[36:39], v[20:23]\n"
"ds_read_b128 v[60:63], v2 offset:54272\n"
"s_add_u32 m0, s64, 69632\n"
"v_mfma_f32_16x16x32_bf16 v[20:23], a[84:87], v[32:35], v[20:23]\n"
"s_waitcnt lgkmcnt(4)\n"
"v_mfma_f32_16x16x32_bf16 v[20:23], v[116:119], v[40:43], v[20:23]\n"
"ds_read_b128 v[32:35], v2 offset:12288\n"
"v_mfma_f32_16x16x32_bf16 v[20:23], v[228:231], v[44:47], v[20:23]\n"
"buffer_load_dwordx4 v2, s[32:35], s63 offen offset:0 sc1 lds\n"
"v_mfma_f32_16x16x32_bf16 v[16:19], v[224:227], v[44:47], v[16:19]\n"
"ds_read_b128 v[36:39], v2 offset:26624\n"
"v_mfma_f32_16x16x32_bf16 v[16:19], v[112:115], v[40:43], v[16:19]\n"
"s_waitcnt lgkmcnt(4)\n"
"v_mfma_f32_16x16x32_bf16 v[16:19], a[88:91], v[48:51], v[16:19]\n"
"ds_read_b128 v[40:43], v2 offset:40960\n"
"v_mfma_f32_16x16x32_bf16 v[16:19], a[200:203], v[52:55], v[16:19]\n"
"buffer_load_dwordx4 v2, s[32:35], s63 offen offset:1024 sc1 lds\n"
"s_add_u32 s63, s63, 1175552\n"
"v_mfma_f32_16x16x32_bf16 v[20:23], a[204:207], v[52:55], v[20:23]\n"
"ds_read_b128 v[44:47], v2 offset:55296\n"
"v_mfma_f32_16x16x32_bf16 v[20:23], a[92:95], v[48:51], v[20:23]\n"
"s_waitcnt lgkmcnt(4)\n"
"v_mfma_f32_16x16x32_bf16 v[20:23], v[124:127], v[56:59], v[20:23]\n"
"ds_read_b128 v[48:51], v2 offset:13312\n"
"v_mfma_f32_16x16x32_bf16 v[20:23], v[236:239], v[60:63], v[20:23]\n"
"v_mfma_f32_16x16x32_bf16 v[16:19], v[232:235], v[60:63], v[16:19]\n"
"ds_read_b128 v[52:55], v2 offset:27648\n"
"v_mfma_f32_16x16x32_bf16 v[16:19], v[120:123], v[56:59], v[16:19]\n"
"s_waitcnt lgkmcnt(4)\n"
"v_mfma_f32_16x16x32_bf16 v[16:19], a[96:99], v[32:35], v[16:19]\n"
"s_waitcnt vmcnt(10)\n"
"s_barrier\n"
"v_mfma_f32_16x16x32_bf16 v[16:19], a[208:211], v[36:39], v[16:19]\n"
"ds_read_b128 v[56:59], v2 offset:41984\n"
"v_mfma_f32_16x16x32_bf16 v[20:23], a[212:215], v[36:39], v[20:23]\n"
"ds_read_b128 v[60:63], v2 offset:56320\n"
"v_mfma_f32_16x16x32_bf16 v[20:23], a[100:103], v[32:35], v[20:23]\n"
"s_waitcnt lgkmcnt(4)\n"
"v_mfma_f32_16x16x32_bf16 v[20:23], v[132:135], v[40:43], v[20:23]\n"
"ds_read_b128 v[32:35], v4 offset:0\n"
"v_mfma_f32_16x16x32_bf16 v[20:23], v[244:247], v[44:47], v[20:23]\n"
"v_mfma_f32_16x16x32_bf16 v[16:19], v[240:243], v[44:47], v[16:19]\n"
"ds_read_b128 v[36:39], v4 offset:14336\n"
"v_mfma_f32_16x16x32_bf16 v[16:19], v[128:131], v[40:43], v[16:19]\n"
"s_waitcnt lgkmcnt(4)\n"
"v_mfma_f32_16x16x32_bf16 v[16:19], a[104:107], v[48:51], v[16:19]\n"
"ds_read_b128 v[40:43], v4 offset:28672\n"
"v_mfma_f32_16x16x32_bf16 v[16:19], a[216:219], v[52:55], v[16:19]\n"
"v_mfma_f32_16x16x32_bf16 v[20:23], a[220:223], v[52:55], v[20:23]\n"
"ds_read_b128 v[44:47], v4 offset:43008\n"
"v_mfma_f32_16x16x32_bf16 v[20:23], a[108:111], v[48:51], v[20:23]\n"
"s_waitcnt lgkmcnt(4)\n"
"v_mfma_f32_16x16x32_bf16 v[20:23], v[140:143], v[56:59], v[20:23]\n"
"ds_read_b128 v[48:51], v4 offset:1024\n"
"v_mfma_f32_16x16x32_bf16 v[20:23], v[252:255], v[60:63], v[20:23]\n"
"v_mfma_f32_16x16x32_bf16 v[16:19], v[248:251], v[60:63], v[16:19]\n"
"ds_read_b128 v[52:55], v4 offset:15360\n"
"v_mfma_f32_16x16x32_bf16 v[16:19], v[136:139], v[56:59], v[16:19]\n"
"s_branch .L_dbl_3\n"
".L_dbt_3:\n"
"s_nop 1\n"
"s_nop 0\n"
"v_cvt_pk_bf16_f32 v15, v30, v31\n"
"v_cvt_pk_bf16_f32 v14, v28, v29\n"
"s_nop 1\n"
"v_cvt_pk_bf16_f32 v13, v26, v27\n"
"v_cvt_pk_bf16_f32 v12, v24, v25\n"
"ds_write_b128 v5, v[12:15]\n"
"ds_read2st64_b32 v[12:13], v3 offset0:0 offset1:1\n"
"ds_read2st64_b32 v[14:15], v3 offset0:2 offset1:3\n"
"s_waitcnt lgkmcnt(0)\n"
"buffer_atomic_pk_add_bf16 v12, v1, s[44:47], s65 offen offset:0 sc1\n"
"buffer_atomic_pk_add_bf16 v13, v1, s[44:47], s65 offen offset:2048 sc1\n"
"s_add_u32 s65, s65, 16384\n"
"buffer_atomic_pk_add_bf16 v14, v1, s[44:47], s65 offen offset:0 sc1\n"
"buffer_atomic_pk_add_bf16 v15, v1, s[44:47], s65 offen offset:2048 sc1\n"
"s_add_u32 s65, s65, 16384\n"
"s_or_b32 s48, s28, s27\n"
"s_or_b32 s48, s48, s26\n"
"s_cbranch_scc1 .L_end\n"
"v_mov_b32 v8, s54\n"
"v_mov_b32 v9, s55\n"
"buffer_store_dwordx2 v[8:9], off, s[32:35], 0 sc1\n"
"s_branch .L_end\n"
".L_rtg0_0:\n"
"s_mov_b32 s48, 20000\n"
".L_rlg0_0:\n"
"s_sleep 8\n"
".L_rfg0_0:\n"
"buffer_load_dwordx2 v[12:13], v14, s[32:35], s66 offen sc1\n"
"s_sub_u32 s48, s48, 1\n"
"s_cbranch_scc1 .L_end\n"
"s_waitcnt vmcnt(0)\n"
"v_cmp_ne_u64 vcc, v[16:17], v[12:13]\n"
"s_cbranch_vccnz .L_rlg0_0\n"
"s_branch .L_bkg0_0\n"
".L_rtg1_0:\n"
"s_mov_b32 s48, 20000\n"
".L_rlg1_0:\n"
"s_sleep 8\n"
".L_rfg1_0:\n"
"buffer_load_dwordx2 v[12:13], v14, s[32:35], s66 offen sc1\n"
"s_sub_u32 s48, s48, 1\n"
"s_cbranch_scc1 .L_end\n"
"s_waitcnt vmcnt(0)\n"
"v_cmp_ne_u64 vcc, v[16:17], v[12:13]\n"
"s_cbranch_vccnz .L_rlg1_0\n"
"s_branch .L_bkg1_0\n"
".L_rtg0_1:\n"
"s_mov_b32 s48, 20000\n"
".L_rlg0_1:\n"
"s_sleep 8\n"
".L_rfg0_1:\n"
"buffer_load_dwordx2 v[12:13], v14, s[32:35], s66 offen sc1\n"
"s_sub_u32 s48, s48, 1\n"
"s_cbranch_scc1 .L_end\n"
"s_waitcnt vmcnt(0)\n"
"v_cmp_ne_u64 vcc, v[16:17], v[12:13]\n"
"s_cbranch_vccnz .L_rlg0_1\n"
"s_branch .L_bkg0_1\n"
".L_rtg1_1:\n"
"s_mov_b32 s48, 20000\n"
".L_rlg1_1:\n"
"s_sleep 8\n"
".L_rfg1_1:\n"
"buffer_load_dwordx2 v[12:13], v14, s[32:35], s66 offen sc1\n"
"s_sub_u32 s48, s48, 1\n"
"s_cbranch_scc1 .L_end\n"
"s_waitcnt vmcnt(0)\n"
"v_cmp_ne_u64 vcc, v[16:17], v[12:13]\n"
"s_cbranch_vccnz .L_rlg1_1\n"
"s_branch .L_bkg1_1\n"
".L_rtg0_2:\n"
"s_mov_b32 s48, 20000\n"
".L_rlg0_2:\n"
"s_sleep 8\n"
".L_rfg0_2:\n"
"buffer_load_dwordx2 v[12:13], v14, s[32:35], s66 offen sc1\n"
"s_sub_u32 s48, s48, 1\n"
"s_cbranch_scc1 .L_end\n"
"s_waitcnt vmcnt(0)\n"
"v_cmp_ne_u64 vcc, v[16:17], v[12:13]\n"
"s_cbranch_vccnz .L_rlg0_2\n"
"s_branch .L_bkg0_2\n"
".L_rtg1_2:\n"
"s_mov_b32 s48, 20000\n"
".L_rlg1_2:\n"
"s_sleep 8\n"
".L_rfg1_2:\n"
"buffer_load_dwordx2 v[12:13], v14, s[32:35], s66 offen sc1\n"
"s_sub_u32 s48, s48, 1\n"
"s_cbranch_scc1 .L_end\n"
"s_waitcnt vmcnt(0)\n"
"v_cmp_ne_u64 vcc, v[16:17], v[12:13]\n"
"s_cbranch_vccnz .L_rlg1_2\n"
"s_branch .L_bkg1_2\n"
".L_rtg0_3:\n"
"s_mov_b32 s48, 20000\n"
".L_rlg0_3:\n"
"s_sleep 8\n"
".L_rfg0_3:\n"
"buffer_load_dwordx2 v[12:13], v14, s[32:35], s66 offen sc1\n"
"s_sub_u32 s48, s48, 1\n"
"s_cbranch_scc1 .L_end\n"
"s_waitcnt vmcnt(0)\n"
"v_cmp_ne_u64 vcc, v[16:17], v[12:13]\n"
"s_cbranch_vccnz .L_rlg0_3\n"
"s_branch .L_bkg0_3\n"
".L_rtg1_3:\n"
"s_mov_b32 s48, 20000\n"
".L_rlg1_3:\n"
"s_sleep 8\n"
".L_rfg1_3:\n"
"buffer_load_dwordx2 v[12:13], v14, s[32:35], s66 offen sc1\n"
"s_sub_u32 s48, s48, 1\n"
"s_cbranch_scc1 .L_end\n"
"s_waitcnt vmcnt(0)\n"
"v_cmp_ne_u64 vcc, v[16:17], v[12:13]\n"
"s_cbranch_vccnz .L_rlg1_3\n"
"s_branch .L_bkg1_3\n"
".L_rtqa:\n"
"s_mov_b32 s48, 20000\n"
"s_branch .L_rfqa\n"
".L_rlqa:\n"
"s_sleep 8\n"
".L_rfqa:\n"
"buffer_load_dwordx2 v[12:13], v0, s[32:35], s66 offen sc1\n"
"s_sub_u32 s48, s48, 1\n"
"s_cbranch_scc1 .L_end\n"
"s_waitcnt vmcnt(0)\n"
"v_cmp_ne_u64 vcc, v[30:31], v[12:13]\n"
"s_cbranch_vccnz .L_rlqa\n"
"s_branch .L_bkqa\n"
".L_rtqb:\n"
"s_mov_b32 s48, 20000\n"
"s_branch .L_rfqb\n"
".L_rlqb:\n"
"s_sleep 8\n"
".L_rfqb:\n"
"buffer_load_dwordx2 v[12:13], v0, s[32:35], s66 offen sc1\n"
"s_sub_u32 s48, s48, 1\n"
"s_cbranch_scc1 .L_end\n"
"s_waitcnt vmcnt(0)\n"
"v_cmp_ne_u64 vcc, v[30:31], v[12:13]\n"
"s_cbranch_vccnz .L_rlqb\n"
"s_branch .L_bkqb\n"
".L_rtq0:\n"
"s_mov_b32 s48, 20000\n"
".L_rlq0:\n"
"s_sleep 8\n"
".L_rfq0:\n"
"buffer_load_dwordx2 v[12:13], v0, s[32:35], s66 offen sc1\n"
"s_sub_u32 s48, s48, 1\n"
"s_cbranch_scc1 .L_end\n"
"s_waitcnt vmcnt(0)\n"
"v_cmp_ne_u64 vcc, v[30:31], v[12:13]\n"
"s_cbranch_vccnz .L_rlq0\n"
"s_branch .L_bkq0\n"
".L_rtq1:\n"
"s_mov_b32 s48, 20000\n"
".L_rlq1:\n"
"s_sleep 8\n"
".L_rfq1:\n"
"buffer_load_dwordx2 v[12:13], v0, s[32:35], s66 offen sc1\n"
"s_sub_u32 s48, s48, 1\n"
"s_cbranch_scc1 .L_end\n"
"s_waitcnt vmcnt(0)\n"
"v_cmp_ne_u64 vcc, v[30:31], v[12:13]\n"
"s_cbranch_vccnz .L_rlq1\n"
"s_branch .L_bkq1\n"
".L_qs0:\n"
"s_cmp_lg_u32 s62, 30\n"
"s_cbranch_scc1 .L_qn4\n"
"v_mov_b32 v120, 0xff61b1e6\n"
"v_mov_b32 v121, 0xff61b1e6\n"
"v_mov_b32 v122, 0xff61b1e6\n"
"v_mov_b32 v123, 0xff61b1e6\n"
"v_cmp_ne_u32 vcc, 0xffff, v126\n"
"s_nop 1\n"
"v_cndmask_b32 v120, 0, v120, vcc\n"
"v_cmp_ne_u32 vcc, 0xffff0000, v126\n"
"s_nop 1\n"
"v_cndmask_b32 v121, 0, v121, vcc\n"
"v_cmp_ne_u32 vcc, 0xffff, v127\n"
"s_nop 1\n"
"v_cndmask_b32 v122, 0, v122, vcc\n"
"v_cmp_ne_u32 vcc, 0xffff0000, v127\n"
"s_nop 1\n"
"v_cndmask_b32 v123, 0, v123, vcc\n"
"s_nop 1\n"
"s_branch .L_qb0\n"
".L_qn4:\n"
"s_cmp_lg_u32 s62, 17\n"
"s_cbranch_scc1 .L_qn30\n"
"s_mov_b32 s72, s69\n"
"s_nop 1\n"
"s_branch .L_qb0\n"
".L_qn30:\n"
"s_cmp_lg_u32 s62, 16\n"
"s_cbranch_scc1 .L_qn32\n"
"s_mov_b32 s72, 1187840\n"
"s_nop 1\n"
"s_branch .L_qb0\n"
".L_qn32:\n"
"s_cmp_lg_u32 s62, 15\n"
"s_cbranch_scc1 .L_qn34\n"
"s_sub_u32 s68, s68, 39198720\n"
"s_nop 1\n"
"s_branch .L_qb0\n"
".L_qn34:\n"
"s_cmp_lg_u32 s62, 14\n"
"s_cbranch_scc1 .L_qn36\n"
"v_mov_b32 v120, 0xff61b1e6\n"
"v_mov_b32 v121, 0xff61b1e6\n"
"v_mov_b32 v122, 0xff61b1e6\n"
"v_mov_b32 v123, 0xff61b1e6\n"
"v_cmp_ne_u32 vcc, 0xffff, v126\n"
"s_nop 1\n"
"v_cndmask_b32 v120, 0, v120, vcc\n"
"v_cmp_ne_u32 vcc, 0xffff0000, v126\n"
"s_nop 1\n"
"v_cndmask_b32 v121, 0, v121, vcc\n"
"v_cmp_ne_u32 vcc, 0xffff, v127\n"
"s_nop 1\n"
"v_cndmask_b32 v122, 0, v122, vcc\n"
"v_cmp_ne_u32 vcc, 0xffff0000, v127\n"
"s_nop 1\n"
"v_cndmask_b32 v123, 0, v123, vcc\n"
"s_nop 1\n"
"s_branch .L_qb0\n"
".L_qn36:\n"
"s_branch .L_qb0\n"
".L_qs1:\n"
"s_cmp_lg_u32 s62, 31\n"
"s_cbranch_scc1 .L_qn3\n"
"s_cmp_lg_u32 s28, 0\n"
"s_cbranch_scc1 .L_qb1\n"
"v_mov_b32 v120, 0xff61b1e6\n"
"v_mov_b32 v121, 0xff61b1e6\n"
"v_mov_b32 v122, 0xff61b1e6\n"
"v_mov_b32 v123, 0xff61b1e6\n"
"s_nop 1\n"
"s_branch .L_qb1\n"
".L_qn3:\n"
"s_cmp_lg_u32 s62, 17\n"
"s_cbranch_scc1 .L_qn31\n"
"s_mov_b32 s72, s70\n"
"s_nop 1\n"
"s_branch .L_qb1\n"
".L_qn31:\n"
"s_cmp_lg_u32 s62, 15\n"
"s_cbranch_scc1 .L_qn35\n"
"s_add_u32 s68, s68, 38010880\n"
"s_cmp_lg_u32 s28, 0\n"
"s_cbranch_scc1 .L_qb1\n"
"v_mov_b32 v120, 0xff61b1e6\n"
"v_mov_b32 v121, 0xff61b1e6\n"
"v_mov_b32 v122, 0xff61b1e6\n"
"v_mov_b32 v123, 0xff61b1e6\n"
"s_nop 1\n"
"s_branch .L_qb1\n"
".L_qn35:\n"
"s_branch .L_qb1\n"
".L_rtw1_1:\n"
"s_mov_b32 s48, 20000\n"
".L_rlw1_1:\n"
"s_sleep 8\n"
".L_rfw1_1:\n"
"buffer_load_dwordx2 v[12:13], v14, s[32:35], s66 offen sc1\n"
"s_sub_u32 s48, s48, 1\n"
"s_cbranch_scc1 .L_end\n"
"s_waitcnt vmcnt(0)\n"
"v_cmp_ne_u64 vcc, v[16:17], v[12:13]\n"
"s_cbranch_vccnz .L_rlw1_1\n"
"s_branch .L_bkw1_1\n"
".L_rth0:\n"
"s_mov_b32 s48, 20000\n"
".L_rlh0:\n"
"s_sleep 8\n"
".L_rfh0:\n"
"buffer_load_dwordx2 v[12:13], v14, s[32:35], s66 offen sc1\n"
"s_sub_u32 s48, s48, 1\n"
"s_cbranch_scc1 .L_end\n"
"s_waitcnt vmcnt(0)\n"
"v_cmp_ne_u64 vcc, v[16:17], v[12:13]\n"
"s_cbranch_vccnz .L_rlh0\n"
"s_branch .L_bkh0\n"
".L_rth1:\n"
"s_mov_b32 s48, 20000\n"
".L_rlh1:\n"
"s_sleep 8\n"
".L_rfh1:\n"
"buffer_load_dwordx2 v[12:13], v14, s[32:35], s66 offen sc1\n"
"s_sub_u32 s48, s48, 1\n"
"s_cbranch_scc1 .L_end\n"
"s_waitcnt vmcnt(0)\n"
"v_cmp_ne_u64 vcc, v[16:17], v[12:13]\n"
"s_cbranch_vccnz .L_rlh1\n"
"s_branch .L_bkh1\n"
".L_rtl0_1:\n"
"s_mov_b32 s48, 20000\n"
".L_rll0_1:\n"
"s_sleep 8\n"
".L_rfl0_1:\n"
"buffer_load_dwordx2 v[6:7], v0, s[32:35], s66 offen sc1\n"
"s_sub_u32 s48, s48, 1\n"
"s_cbranch_scc1 .L_end\n"
"s_waitcnt vmcnt(0)\n"
"v_cmp_ne_u64 vcc, v[8:9], v[6:7]\n"
"s_cbranch_vccnz .L_rll0_1\n"
"s_branch .L_bkl0_1\n"
".L_rtl1_1:\n"
"s_mov_b32 s48, 20000\n"
".L_rll1_1:\n"
"s_sleep 8\n"
".L_rfl1_1:\n"
"buffer_load_dwordx2 v[6:7], v0, s[32:35], s66 offen sc1\n"
"s_sub_u32 s48, s48, 1\n"
"s_cbranch_scc1 .L_end\n"
"s_waitcnt vmcnt(0)\n"
"v_cmp_ne_u64 vcc, v[8:9], v[6:7]\n"
"s_cbranch_vccnz .L_rll1_1\n"
"s_branch .L_bkl1_1\n"
".L_rtm1_1:\n"
"s_mov_b32 s48, 20000\n"
".L_rlm1_1:\n"
"s_sleep 8\n"
".L_rfm1_1:\n"
"buffer_load_dwordx2 v[6:7], v0, s[32:35], s66 offen sc1\n"
"s_sub_u32 s48, s48, 1\n"
"s_cbranch_scc1 .L_end\n"
"s_waitcnt vmcnt(0)\n"
"v_cmp_ne_u64 vcc, v[8:9], v[6:7]\n"
"s_cbranch_vccnz .L_rlm1_1\n"
"s_branch .L_bkm1_1\n"
".L_rtm0_1:\n"
"s_mov_b32 s48, 20000\n"
".L_rlm0_1:\n"
"s_sleep 8\n"
".L_rfm0_1:\n"
"buffer_load_dwordx2 v[6:7], v0, s[32:35], s66 offen sc1\n"
"s_sub_u32 s48, s48, 1\n"
"s_cbranch_scc1 .L_end\n"
"s_waitcnt vmcnt(0)\n"
"v_cmp_ne_u64 vcc, v[8:9], v[6:7]\n"
"s_cbranch_vccnz .L_rlm0_1\n"
"s_branch .L_bkm0_1\n"
".L_rta0_1:\n"
"s_mov_b32 s48, 20000\n"
".L_rla0_1:\n"
"s_sleep 8\n"
".L_rfa0_1:\n"
"buffer_load_dwordx2 v[6:7], v0, s[32:35], s66 offen sc1\n"
"s_sub_u32 s48, s48, 1\n"
"s_cbranch_scc1 .L_end\n"
"s_waitcnt vmcnt(0)\n"
"v_cmp_ne_u64 vcc, v[8:9], v[6:7]\n"
"s_cbranch_vccnz .L_rla0_1\n"
"s_branch .L_bka0_1\n"
".L_rta1_1:\n"
"s_mov_b32 s48, 20000\n"
".L_rla1_1:\n"
"s_sleep 8\n"
".L_rfa1_1:\n"
"buffer_load_dwordx2 v[6:7], v0, s[32:35], s66 offen sc1\n"
"s_sub_u32 s48, s48, 1\n"
"s_cbranch_scc1 .L_end\n"
"s_waitcnt vmcnt(0)\n"
"v_cmp_ne_u64 vcc, v[8:9], v[6:7]\n"
"s_cbranch_vccnz .L_rla1_1\n"
"s_branch .L_bka1_1\n"
".L_rtb1_1:\n"
"s_mov_b32 s48, 20000\n"
".L_rlb1_1:\n"
"s_sleep 8\n"
".L_rfb1_1:\n"
"buffer_load_dwordx2 v[6:7], v0, s[32:35], s66 offen sc1\n"
"s_sub_u32 s48, s48, 1\n"
"s_cbranch_scc1 .L_end\n"
"s_waitcnt vmcnt(0)\n"
"v_cmp_ne_u64 vcc, v[8:9], v[6:7]\n"
"s_cbranch_vccnz .L_rlb1_1\n"
"s_branch .L_bkb1_1\n"
".L_rtb0_1:\n"
"s_mov_b32 s48, 20000\n"
".L_rlb0_1:\n"
"s_sleep 8\n"
".L_rfb0_1:\n"
"buffer_load_dwordx2 v[6:7], v0, s[32:35], s66 offen sc1\n"
"s_sub_u32 s48, s48, 1\n"
"s_cbranch_scc1 .L_end\n"
"s_waitcnt vmcnt(0)\n"
"v_cmp_ne_u64 vcc, v[8:9], v[6:7]\n"
"s_cbranch_vccnz .L_rlb0_1\n"
"s_branch .L_bkb0_1\n"
".L_end:\n"
"s_waitcnt vmcnt(0) lgkmcnt(0)\n"
".L_last:\n"
    : "+{v0}"(tid)
    : "{s[8:9]}"(x), "{s[10:11]}"(wq), "{s[12:13]}"(wo), "{s[14:15]}"(wg), "{s[16:17]}"(wu), "{s[18:19]}"(wd),
      "{s[20:21]}"(out), "{s[22:23]}"(ws), "{s24}"(blockIdx.x)
    : "v1", "v2", "v3", "v4", "v5", "v6", "v7", "v8", "v9", "v10", "v11", "v12", "v13", "v14", "v15", "v16", "v17", "v18", "v19", "v20", "v21", "v22", "v23", "v24", "v25", "v26", "v27", "v28", "v29", "v30", "v31", "v32", "v33", "v34", "v35", "v36", "v37", "v38", "v39", "v40", "v41", "v42", "v43", "v44", "v45", "v46", "v47", "v48", "v49", "v50", "v51", "v52", "v53", "v54", "v55", "v56", "v57", "v58", "v59", "v60", "v61", "v62", "v63", "v64", "v65", "v66", "v67", "v68", "v69", "v70", "v71", "v72", "v73", "v74", "v75", "v76", "v77", "v78", "v79", "v80", "v81", "v82", "v83", "v84", "v85", "v86", "v87", "v88", "v89", "v90", "v91", "v92", "v93", "v94", "v95", "v96", "v97", "v98", "v99", "v100", "v101", "v102", "v103", "v104", "v105", "v106", "v107", "v108", "v109", "v110", "v111", "v112", "v113", "v114", "v115", "v116", "v117", "v118", "v119", "v120", "v121", "v122", "v123", "v124", "v125", "v126", "v127", "v128", "v129", "v130", "v131", "v132", "v133", "v134", "v135", "v136", "v137", "v138", "v139", "v140", "v141", "v142", "v143", "v144", "v145", "v146", "v147", "v148", "v149", "v150", "v151", "v152", "v153", "v154", "v155", "v156", "v157", "v158", "v159", "v160", "v161", "v162", "v163", "v164", "v165", "v166", "v167", "v168", "v169", "v170", "v171", "v172", "v173", "v174", "v175", "v176", "v177", "v178", "v179", "v180", "v181", "v182", "v183", "v184", "v185", "v186", "v187", "v188", "v189", "v190", "v191", "v192", "v193", "v194", "v195", "v196", "v197", "v198", "v199", "v200", "v201", "v202", "v203", "v204", "v205", "v206", "v207", "v208", "v209", "v210", "v211", "v212", "v213", "v214", "v215", "v216", "v217", "v218", "v219", "v220", "v221", "v222", "v223", "v224", "v225", "v226", "v227", "v228", "v229", "v230", "v231", "v232", "v233", "v234", "v235", "v236", "v237", "v238", "v239", "v240", "v241", "v242", "v243", "v244", "v245", "v246", "v247", "v248", "v249", "v250", "v251", "v252", "v253", "v254", "v255", "a0", "a1", "a2", "a3", "a4", "a5", "a6", "a7", "a8", "a9", "a10", "a11", "a12", "a13", "a14", "a15", "a16", "a17", "a18", "a19", "a20", "a21", "a22", "a23", "a24", "a25", "a26", "a27", "a28", "a29", "a30", "a31", "a32", "a33", "a34", "a35", "a36", "a37", "a38", "a39", "a40", "a41", "a42", "a43", "a44", "a45", "a46", "a47", "a48", "a49", "a50", "a51", "a52", "a53", "a54", "a55", "a56", "a57", "a58", "a59", "a60", "a61", "a62", "a63", "a64", "a65", "a66", "a67", "a68", "a69", "a70", "a71", "a72", "a73", "a74", "a75", "a76", "a77", "a78", "a79", "a80", "a81", "a82", "a83", "a84", "a85", "a86", "a87", "a88", "a89", "a90", "a91", "a92", "a93", "a94", "a95", "a96", "a97", "a98", "a99", "a100", "a101", "a102", "a103", "a104", "a105", "a106", "a107", "a108", "a109", "a110", "a111", "a112", "a113", "a114", "a115", "a116", "a117", "a118", "a119", "a120", "a121", "a122", "a123", "a124", "a125", "a126", "a127", "a128", "a129", "a130", "a131", "a132", "a133", "a134", "a135", "a136", "a137", "a138", "a139", "a140", "a141", "a142", "a143", "a144", "a145", "a146", "a147", "a148", "a149", "a150", "a151", "a152", "a153", "a154", "a155", "a156", "a157", "a158", "a159", "a160", "a161", "a162", "a163", "a164", "a165", "a166", "a167", "a168", "a169", "a170", "a171", "a172", "a173", "a174", "a175", "a176", "a177", "a178", "a179", "a180", "a181", "a182", "a183", "a184", "a185", "a186", "a187", "a188", "a189", "a190", "a191", "a192", "a193", "a194", "a195", "a196", "a197", "a198", "a199", "a200", "a201", "a202", "a203", "a204", "a205", "a206", "a207", "a208", "a209", "a210", "a211", "a212", "a213", "a214", "a215", "a216", "a217", "a218", "a219", "a220", "a221", "a222", "a223", "a224", "a225", "a226", "a227", "a228", "a229", "a230", "a231", "a232", "a233", "a234", "a235", "a236", "a237", "a238", "a239", "a240", "a241", "a242", "a243", "a244", "a245", "a246", "a247", "a248", "a249", "a250", "a251", "a252", "a253", "a254", "a255", "s25", "s26", "s27", "s28", "s29", "s30", "s31", "s32", "s33", "s34", "s35", "s36", "s37", "s38", "s39", "s40", "s41", "s42", "s43", "s44", "s45", "s46", "s47", "s48", "s49", "s50", "s51", "s52", "s53", "s54", "s55", "s56", "s57", "s58", "s59", "s60", "s61", "s62", "s63", "s64", "s65", "s66", "s67", "s68", "s69", "s70", "s71", "s72", "s73", "s74", "s75", "s76", "s77", "s78", "s79", "s80", "s81", "s82", "s83", "s84", "s85", "s86", "s87", "s88", "s89", "s90", "s91", "s92", "s93", "s94", "s95", "s96", "s97", "s98", "s99", "vcc", "scc", "m0", "memory");
}

extern "C" void kernel_launch(void* const* d_in, const int* in_sizes, int n_in, void* d_out, int out_size,
                              void* d_ws, size_t ws_size, hipStream_t stream) {
    constexpr int smem = 163840;
    (void)hipFuncSetAttribute((const void*)body_k, hipFuncAttributeMaxDynamicSharedMemorySize, smem);
    body_k<<<256, 256, smem, stream>>>(d_in[0], d_in[1], d_in[2], d_in[4], d_in[5], d_in[6], d_out, d_ws);
}
```

```cpp
#include <hip/hip_runtime.h>
#pragma clang diagnostic ignored "-Winline-asm"
#define W_XI 4612096
#define W_O 4648960
#define W_XA 4681728
#define W_H 4718592
#define W_P 4833280
#define W_EB 296960
#define W_MAP 1
extern "C" __global__ __launch_bounds__(256, 1) void body_k(const void* x, const void* wq, const void* wo, const void* wg, const void* wu,
                                                            const void* wd, void* out, void* ws) {
    unsigned tid = threadIdx.x;
    asm volatile(
"s_load_dwordx2 s[52:53], s[22:23], 0x0\n"
"v_lshrrev_b32 v3, 6, v0\n"
"v_and_b32 v1, 63, v0\n"
"v_readfirstlane_b32 s26, v3\n"
"v_lshlrev_b32 v2, 4, v1\n"
"s_add_u32 s32, s22, 0x0\n"
"s_addc_u32 s33, s23, 0x0\n"
"s_and_b32 s33, s33, 0xffff\n"
"s_mov_b32 s34, 0x504c000\n"
"s_mov_b32 s35, 0x20000\n"
"s_mov_b32 s73, 0\n"
"s_lshr_b32 s49, s24, 3\n"
"s_cmp_lt_u32 s49, 16\n"
"s_cbranch_scc1 .L_nc\n"
"s_bfe_u32 s28, s24, 0x20001\n"
"s_and_b32 s27, s49, 7\n"
"s_bfe_u32 s29, s49, 0x10003\n"
"s_mov_b32 s62, 0\n"
"s_mov_b32 s74, 1\n"
"s_bitcmp1_b32 s24, 0\n"
"s_cbranch_scc0 .L_nt\n"
"s_getpc_b64 s[40:41]\n"
".L_ct16:\n"
"s_and_b32 s41, s41, 0xffff\n"
"s_mov_b32 s42, .L_last-.L_ct16\n"
"s_mov_b32 s43, 0x20000\n"
"s_sub_u32 s48, s49, 16\n"
"s_lshl_b32 s48, s48, 8\n"
"v_add_lshl_u32 v5, v0, s48, 7\n"
"buffer_load_dword v10, v5, s[40:43], 0 offen\n"
"s_add_u32 s36, s10, 0x0\n"
"s_addc_u32 s37, s11, 0x0\n"
"s_and_b32 s37, s37, 0xffff\n"
"s_mov_b32 s38, 0x600000\n"
"s_mov_b32 s39, 0x20000\n"
"s_lshl_b32 s48, s49, 8\n"
"s_sub_u32 s48, s48, 4096\n"
"v_add_u32 v4, s48, v0\n"
"s_lshl_b32 s48, s28, 9\n"
"s_add_u32 s44, s8, 0x0\n"
"s_addc_u32 s45, s9, 0x0\n"
"s_and_b32 s45, s45, 0xffff\n"
"s_mov_b32 s46, 0x400000\n"
"s_mov_b32 s47, 0x20000\n"
"v_lshrrev_b32 v5, 10, v4\n"
"v_bfe_u32 v6, v4, 1, 9\n"
"v_lshl_add_u32 v5, v5, 13, v6\n"
"v_subrev_u32 v5, 512, v5\n"
"v_and_b32 v5, 0x7fff, v5\n"
"v_lshlrev_b32 v5, 7, v5\n"
"buffer_load_dword v10, v5, s[44:47], 0 offen\n"
"v_mul_u32_u24 v5, 43691, v4\n"
"v_lshrrev_b32 v5, 19, v5\n"
"v_mul_u32_u24 v6, 12, v5\n"
"v_sub_u32 v6, v4, v6\n"
"v_mul_u32_u24 v5, 48, v5\n"
"v_lshrrev_b32 v7, 2, v6\n"
"v_mul_u32_u24 v7, 12, v7\n"
"v_add3_u32 v5, v5, v6, v7\n"
"v_lshlrev_b32 v5, 7, v5\n"
"v_add_u32 v4, 4096, v4\n"
"buffer_load_dword v10, v5, s[36:39], s48 offen\n"
"v_mul_u32_u24 v5, 43691, v4\n"
"v_lshrrev_b32 v5, 19, v5\n"
"v_mul_u32_u24 v6, 12, v5\n"
"v_sub_u32 v6, v4, v6\n"
"v_mul_u32_u24 v5, 48, v5\n"
"v_lshrrev_b32 v7, 2, v6\n"
"v_mul_u32_u24 v7, 12, v7\n"
"v_add3_u32 v5, v5, v6, v7\n"
"v_lshlrev_b32 v5, 7, v5\n"
"v_add_u32 v4, 4096, v4\n"
"buffer_load_dword v10, v5, s[36:39], s48 offen\n"
"v_mul_u32_u24 v5, 43691, v4\n"
"v_lshrrev_b32 v5, 19, v5\n"
"v_mul_u32_u24 v6, 12, v5\n"
"v_sub_u32 v6, v4, v6\n"
"v_mul_u32_u24 v5, 48, v5\n"
"v_lshrrev_b32 v7, 2, v6\n"
"v_mul_u32_u24 v7, 12, v7\n"
"v_add3_u32 v5, v5, v6, v7\n"
"v_lshlrev_b32 v5, 7, v5\n"
"v_add_u32 v4, 4096, v4\n"
"buffer_load_dword v10, v5, s[36:39], s48 offen\n"
"s_mov_b32 s62, 1\n"
"s_mov_b32 s74, 4\n"
".L_nt:\n"
"s_lshl2_add_u32 s50, s27, s26\n"
"s_mul_i32 s70, s50, 1152\n"
"v_and_b32 v20, 15, v1\n"
"v_lshlrev_b32 v20, 11, v20\n"
"v_and_b32 v21, 48, v1\n"
"s_lshl_b32 s48, s50, 6\n"
"v_add3_u32 v20, v20, v21, s48\n"
"s_add_u32 s36, s8, 0x0\n"
"s_addc_u32 s37, s9, 0x0\n"
"s_and_b32 s37, s37, 0xffff\n"
"s_mov_b32 s38, 0x400000\n"
"s_mov_b32 s39, 0x20000\n"
"s_lshl2_add_u32 s48, s28, s62\n"
"s_lshl_b32 s48, s48, 3\n"
"s_sub_u32 s48, s48, s29\n"
"s_add_u32 s48, s48, 127\n"
"s_lshl_b32 s66, s27, 7\n"
"s_add_u32 s66, s66, 286720\n"
"s_sub_u32 s66, s66, s70\n"
"s_add_u32 s49, s28, 3\n"
"s_and_b32 s49, s49, 3\n"
"s_mul_i32 s49, s49, 296960\n"
"s_add_u32 s69, s70, s49\n"
"s_add_u32 s69, s69, 41435136\n"
"s_mul_i32 s49, s28, 296960\n"
"s_add_u32 s70, s70, s49\n"
"s_and_b32 s51, s50, 6\n"
"s_lshl_b32 s51, s51, 1\n"
"v_add_u32 v3, s51, v1\n"
"v_bfi_b32 v3, 15, v3, v1\n"
"v_lshlrev_b32 v3, 2, v3\n"
"v_cmp_gt_u32 vcc, 32, v1\n"
"v_mov_b32 v21, 0x7fff0000\n"
"s_nop 0\n"
"v_cndmask_b32 v21, v21, v3, vcc\n"
"s_mul_i32 s49, s29, 1187840\n"
"s_sub_u32 s71, s70, s49\n"
"s_add_u32 s71, s71, 4612096\n"
"v_mul_u32_u24 v24, 2375680, v1\n"
"v_add_u32 v24, s71, v24\n"
"s_cmp_eq_u32 s29, 0\n"
"s_cselect_b32 s68, s71, s69\n"
"v_writelane_b32 v24, s68, 0\n"
"s_mul_i32 s49, s62, 9502720\n"
"s_add_u32 s71, s71, s49\n"
"s_cmp_eq_u32 s62, 0\n"
"s_cselect_b32 s68, s68, s71\n"
"s_add_u32 s71, s71, 2375680\n"
"v_lshrrev_b32 v22, 2, v0\n"
"v_mov_b32 v23, 0x3f803f80\n"
".L_dc:\n"
"s_and_b32 s49, s48, 127\n"
"s_lshl_b32 s49, s49, 15\n"
"s_add_u32 s48, s48, 2\n"
"buffer_load_dwordx4 v[32:35], v20, s[36:39], s49 offen\n"
"s_and_b32 s49, s48, 127\n"
"s_lshl_b32 s49, s49, 15\n"
"s_add_u32 s48, s48, 2\n"
"buffer_load_dwordx4 v[36:39], v20, s[36:39], s49 offen\n"
"s_and_b32 s49, s48, 127\n"
"s_lshl_b32 s49, s49, 15\n"
"s_add_u32 s48, s48, 2\n"
"buffer_load_dwordx4 v[40:43], v20, s[36:39], s49 offen\n"
"s_and_b32 s49, s48, 127\n"
"s_lshl_b32 s49, s49, 15\n"
"s_add_u32 s48, s48, 2\n"
"buffer_load_dwordx4 v[44:47], v20, s[36:39], s49 offen\n"
"v_cmp_eq_u32 vcc, s62, v22\n"
"v_mov_b32 v25, 0x7fff0000\n"
"s_add_u32 s62, s62, 1\n"
"v_cndmask_b32 v25, v25, v24, vcc\n"
"s_waitcnt lgkmcnt(0)\n"
"s_add_u32 s54, s52, 0x7f4a7c15\n"
"s_addc_u32 s55, s53, 0x9e3779b9\n"
"v_mov_b32 v8, s54\n"
"v_mov_b32 v9, s55\n"
"s_waitcnt vmcnt(0)\n"
"v_dot2_f32_bf16 v48, v32, v23, 0\n"
"v_dot2_f32_bf16 v49, v32, v32, 0\n"
"v_dot2_f32_bf16 v50, v36, v23, 0\n"
"v_dot2_f32_bf16 v51, v36, v36, 0\n"
"v_dot2_f32_bf16 v52, v40, v23, 0\n"
"v_dot2_f32_bf16 v53, v40, v40, 0\n"
"v_dot2_f32_bf16 v54, v44, v23, 0\n"
"v_dot2_f32_bf16 v55, v44, v44, 0\n"
"v_dot2_f32_bf16 v48, v33, v23, v48\n"
"v_dot2_f32_bf16 v49, v33, v33, v49\n"
"v_dot2_f32_bf16 v50, v37, v23, v50\n"
"v_dot2_f32_bf16 v51, v37, v37, v51\n"
"v_dot2_f32_bf16 v52, v41, v23, v52\n"
"v_dot2_f32_bf16 v53, v41, v41, v53\n"
"v_dot2_f32_bf16 v54, v45, v23, v54\n"
"v_dot2_f32_bf16 v55, v45, v45, v55\n"
"v_dot2_f32_bf16 v48, v34, v23, v48\n"
"v_dot2_f32_bf16 v49, v34, v34, v49\n"
"v_dot2_f32_bf16 v50, v38, v23, v50\n"
"v_dot2_f32_bf16 v51, v38, v38, v51\n"
"v_dot2_f32_bf16 v52, v42, v23, v52\n"
"v_dot2_f32_bf16 v53, v42, v42, v53\n"
"v_dot2_f32_bf16 v54, v46, v23, v54\n"
"v_dot2_f32_bf16 v55, v46, v46, v55\n"
"v_dot2_f32_bf16 v48, v35, v23, v48\n"
"v_dot2_f32_bf16 v49, v35, v35, v49\n"
"v_dot2_f32_bf16 v50, v39, v23, v50\n"
"v_dot2_f32_bf16 v51, v39, v39, v51\n"
"v_dot2_f32_bf16 v52, v43, v23, v52\n"
"v_dot2_f32_bf16 v53, v43, v43, v53\n"
"v_dot2_f32_bf16 v54, v47, v23, v54\n"
"v_dot2_f32_bf16 v55, v47, v47, v55\n"
"v_permlane16_swap_b32 v48, v49\n"
"v_permlane16_swap_b32 v50, v51\n"
"v_permlane16_swap_b32 v52, v53\n"
"v_permlane16_swap_b32 v54, v55\n"
"v_add_f32 v48, v48, v49\n"
"v_add_f32 v50, v50, v51\n"
"v_add_f32 v52, v52, v53\n"
"v_add_f32 v54, v54, v55\n"
"v_permlane32_swap_b32 v48, v49\n"
"v_permlane32_swap_b32 v50, v51\n"
"v_permlane32_swap_b32 v52, v53\n"
"v_permlane32_swap_b32 v54, v55\n"
"v_add_f32 v48, v48, v49\n"
"v_add_f32 v50, v50, v51\n"
"v_add_f32 v52, v52, v53\n"
"v_add_f32 v54, v54, v55\n"
"buffer_store_dwordx4 v[32:35], v2, s[32:35], s68 offen sc1\n"
"buffer_store_dword v48, v21, s[32:35], s68 offen offset:1024 sc1\n"
"s_mov_b32 s68, s71\n"
"s_add_u32 s71, s71, 2375680\n"
"buffer_store_dwordx4 v[36:39], v2, s[32:35], s68 offen sc1\n"
"buffer_store_dword v50, v21, s[32:35], s68 offen offset:1024 sc1\n"
"s_mov_b32 s68, s71\n"
"s_add_u32 s71, s71, 2375680\n"
"buffer_store_dwordx4 v[40:43], v2, s[32:35], s68 offen sc1\n"
"buffer_store_dword v52, v21, s[32:35], s68 offen offset:1024 sc1\n"
"s_mov_b32 s68, s71\n"
"s_add_u32 s71, s71, 2375680\n"
"buffer_store_dwordx4 v[44:47], v2, s[32:35], s68 offen sc1\n"
"buffer_store_dword v54, v21, s[32:35], s68 offen offset:1024 sc1\n"
"s_mov_b32 s68, s71\n"
"s_add_u32 s71, s71, 2375680\n"
"s_waitcnt vmcnt(0)\n"
"s_barrier\n"
"buffer_store_dwordx2 v[8:9], v25, s[32:35], s66 offen sc1\n"
"s_cmp_lt_u32 s62, s74\n"
"s_cbranch_scc1 .L_dc\n"
"s_bitcmp1_b32 s24, 0\n"
"s_cbranch_scc1 .L_d\n"
"s_lshr_b32 s49, s24, 3\n"
".L_nc:\n"
"s_bfe_u32 s50, s24, 0x20001\n"
"s_and_b32 s27, s49, 3\n"
"s_lshl2_add_u32 s27, s50, s27\n"
"s_lshr_b32 s28, s49, 2\n"
"s_bitcmp1_b32 s24, 0\n"
"s_cbranch_scc1 .L_q\n"
"s_and_b32 s27, s49, 1\n"
"s_lshl1_add_u32 s27, s50, s27\n"
"s_lshr_b32 s28, s49, 1\n"
"s_cmp_lt_u32 s49, 4\n"
"s_cbranch_scc1 .L_w\n"
"s_cmp_ge_u32 s49, 16\n"
"s_cbranch_scc1 .L_ns\n"
"s_getpc_b64 s[40:41]\n"
".L_ct5:\n"
"s_and_b32 s41, s41, 0xffff\n"
"s_mov_b32 s42, .L_last-.L_ct5\n"
"s_mov_b32 s43, 0x20000\n"
"s_sub_u32 s48, s49, 5\n"
"s_lshl_b32 s48, s48, 8\n"
"v_add_lshl_u32 v5, v0, s48, 7\n"
"buffer_load_dword v120, v5, s[40:43], 0 offen\n"
"s_lshl2_add_u32 s48, s49, s50\n"
"s_lshl_b32 s48, s48, 8\n"
"s_sub_u32 s48, s48, 4096\n"
"v_add_u32 v4, s48, v0\n"
"v_mul_u32_u24 v5, 52429, v4\n"
"v_lshrrev_b32 v5, 22, v5\n"
"v_mul_u32_u24 v5, 2240, v5\n"
"v_add_u32 v5, v5, v4\n"
"v_lshlrev_b32 v5, 7, v5\n"
"v_add_u32 v5, 4898816, v5\n"
"v_add_u32 v4, 12288, v4\n"
"buffer_load_dword v120, v5, s[32:35], 0 offen sc1\n"
"v_mul_u32_u24 v5, 52429, v4\n"
"v_lshrrev_b32 v5, 22, v5\n"
"v_mul_u32_u24 v5, 2240, v5\n"
"v_add_u32 v5, v5, v4\n"
"v_lshlrev_b32 v5, 7, v5\n"
"v_add_u32 v5, 4898816, v5\n"
"v_add_u32 v4, 12288, v4\n"
"buffer_load_dword v120, v5, s[32:35], 0 offen sc1\n"
"s_sleep 30\n"
".L_ns:\n"
"s_lshr_b32 s27, s50, 1\n"
"s_and_b32 s28, s50, 1\n"
"s_cmp_eq_u32 s49, 4\n"
"s_cbranch_scc1 .L_h\n"
"s_sub_u32 s27, s49, 14\n"
"s_lshr_b32 s27, s27, 1\n"
"s_and_b32 s28, s49, 1\n"
"s_add_u32 s28, s28, 1\n"
"s_sub_u32 s48, s49, 5\n"
"s_cmp_lt_u32 s49, 14\n"
"s_cselect_b32 s27, s48, s27\n"
"s_cselect_b32 s28, 0, s28\n"
"s_mul_i32 s50, s50, 9\n"
"s_add_u32 s27, s27, s50\n"
".L_g:\n"
"v_mov_b32 v16, 0x3f803f80\n"
"v_mov_b32 v17, 0x3f803f80\n"
"v_mov_b32 v18, 0x3f803f80\n"
"v_mov_b32 v19, 0x3f803f80\n"
"s_add_u32 s36, s14, 0x0\n"
"s_addc_u32 s37, s15, 0x0\n"
"s_and_b32 s37, s37, 0xffff\n"
"s_mov_b32 s38, 0x700000\n"
"s_mov_b32 s39, 0x20000\n"
"s_add_u32 s40, s16, 0x0\n"
"s_addc_u32 s41, s17, 0x0\n"
"s_and_b32 s41, s41, 0xffff\n"
"s_mov_b32 s42, 0x700000\n"
"s_mov_b32 s43, 0x20000\n"
"v_add_u32 v8, 0, v1\n"
"v_mul_u32_u24 v9, 5462, v8\n"
"v_lshrrev_b32 v9, 16, v9\n"
"v_mul_u32_u24 v10, 12, v9\n"
"v_sub_u32 v10, v8, v10\n"
"v_mul_u32_u24 v9, 7168, v9\n"
"v_lshl_add_u32 v9, v10, 4, v9\n"
"v_subrev_u32 v4, 0, v9\n"
"v_add_u32 v8, 64, v1\n"
"v_mul_u32_u24 v9, 5462, v8\n"
"v_lshrrev_b32 v9, 16, v9\n"
"v_mul_u32_u24 v10, 12, v9\n"
"v_sub_u32 v10, v8, v10\n"
"v_mul_u32_u24 v9, 7168, v9\n"
"v_lshl_add_u32 v9, v10, 4, v9\n"
"v_subrev_u32 v5, 1024, v9\n"
"v_add_u32 v8, 128, v1\n"
"v_mul_u32_u24 v9, 5462, v8\n"
"v_lshrrev_b32 v9, 16, v9\n"
"v_mul_u32_u24 v10, 12, v9\n"
"v_sub_u32 v10, v8, v10\n"
"v_mul_u32_u24 v9, 7168, v9\n"
"v_lshl_add_u32 v9, v10, 4, v9\n"
"v_subrev_u32 v6, 2048, v9\n"
"s_mul_i32 s48, s26, 114688\n"
"s_mul_i32 s49, s27, 192\n"
"s_add_u32 s63, s48, s49\n"
"s_mul_i32 s64, s26, 3072\n"
"s_add_u32 m0, s64, 0\n"
"s_nop 0\n"
"buffer_load_dwordx4 v4, s[36:39], s63 offen offset:0 lds\n"
"buffer_load_dwordx4 v5, s[36:39], s63 offen offset:1024 lds\n"
"buffer_load_dwordx4 v6, s[36:39], s63 offen offset:2048 lds\n"
"s_add_u32 m0, s64, 12288\n"
"s_nop 0\n"
"buffer_load_dwordx4 v4, s[40:43], s63 offen offset:0 lds\n"
"buffer_load_dwordx4 v5, s[40:43], s63 offen offset:1024 lds\n"
"buffer_load_dwordx4 v6, s[40:43], s63 offen offset:2048 lds\n"
"s_add_u32 s63, s63, 458752\n"
"s_add_u32 m0, s64, 24576\n"
"s_nop 0\n"
"buffer_load_dwordx4 v4, s[36:39], s63 offen offset:0 lds\n"
"buffer_load_dwordx4 v5, s[36:39], s63 offen offset:1024 lds\n"
"buffer_load_dwordx4 v6, s[36:39], s63 offen offset:2048 lds\n"
"s_add_u32 m0, s64, 36864\n"
"s_nop 0\n"
"buffer_load_dwordx4 v4, s[40:43], s63 offen offset:0 lds\n"
"buffer_load_dwordx4 v5, s[40:43], s63 offen offset:1024 lds\n"
"buffer_load_dwordx4 v6, s[40:43], s63 offen offset:2048 lds\n"
"s_add_u32 s63, s63, 458752\n"
"s_add_u32 m0, s64, 49152\n"
"s_nop 0\n"
"buffer_load_dwordx4 v4, s[36:39], s63 offen offset:0 lds\n"
"buffer_load_dwordx4 v5, s[36:39], s63 offen offset:1024 lds\n"
"buffer_load_dwordx4 v6, s[36:39], s63 offen offset:2048 lds\n"
"s_add_u32 m0, s64, 61440\n"
"s_nop 0\n"
"buffer_load_dwordx4 v4, s[40:43], s63 offen offset:0 lds\n"
"buffer_load_dwordx4 v5, s[40:43], s63 offen offset:1024 lds\n"
"buffer_load_dwordx4 v6, s[40:43], s63 offen offset:2048 lds\n"
"s_add_u32 s63, s63, 458752\n"
"s_add_u32 m0, s64, 73728\n"
"s_nop 0\n"
"buffer_load_dwordx4 v4, s[36:39], s63 offen offset:0 lds\n"
"buffer_load_dwordx4 v5, s[36:39], s63 offen offset:1024 lds\n"
"buffer_load_dwordx4 v6, s[36:39], s63 offen offset:2048 lds\n"
"s_add_u32 m0, s64, 86016\n"
"s_nop 0\n"
"buffer_load_dwordx4 v4, s[40:43], s63 offen offset:0 lds\n"
"buffer_load_dwordx4 v5, s[40:43], s63 offen offset:1024 lds\n"
"buffer_load_dwordx4 v6, s[40:43], s63 offen offset:2048 lds\n"
"s_add_u32 s63, s63, 458752\n"
"s_add_u32 m0, s64, 98304\n"
"s_nop 0\n"
"buffer_load_dwordx4 v4, s[36:39], s63 offen offset:0 lds\n"
"buffer_load_dwordx4 v5, s[36:39], s63 offen offset:1024 lds\n"
"buffer_load_dwordx4 v6, s[36:39], s63 offen offset:2048 lds\n"
"s_add_u32 m0, s64, 110592\n"
"s_nop 0\n"
"buffer_load_dwordx4 v4, s[40:43], s63 offen offset:0 lds\n"
"buffer_load_dwordx4 v5, s[40:43], s63 offen offset:1024 lds\n"
"buffer_load_dwordx4 v6, s[40:43], s63 offen offset:2048 lds\n"
"s_add_u32 s63, s63, 458752\n"
"v_and_b32 v12, 15, v1\n"
"v_lshrrev_b32 v13, 4, v1\n"
"v_lshrrev_b32 v14, 2, v12\n"
"v_and_b32 v15, 3, v12\n"
"v_lshl_add_u32 v13, v13, 3, v14\n"
"v_mul_u32_u24 v13, 192, v13\n"
"s_mul_i32 s48, s26, 48\n"
"v_add_u32 v13, s48, v13\n"
"v_and_b32 v14, 1, v15\n"
"v_lshrrev_b32 v15, 1, v15\n"
"v_lshl_add_u32 v13, v14, 3, v13\n"
"v_lshl_add_u32 v8, v15, 5, v13\n"
"v_mul_u32_u24 v15, 12288, v15\n"
"v_add3_u32 v9, v13, v15, 16\n"
"v_add_u32 v10, 49152, v8\n"
"v_add_u32 v11, 49152, v9\n"
"v_add_u32 v12, 98304, v8\n"
"v_add_u32 v13, 98304, v9\n"
"s_waitcnt vmcnt(24)\n"
"s_barrier\n"
"s_add_u32 m0, s64, 122880\n"
"s_nop 0\n"
"buffer_load_dwordx4 v4, s[36:39], s63 offen offset:0 lds\n"
"buffer_load_dwordx4 v5, s[36:39], s63 offen offset:1024 lds\n"
"buffer_load_dwordx4 v6, s[36:39], s63 offen offset:2048 lds\n"
"s_add_u32 m0, s64, 135168\n"
"s_nop 0\n"
"buffer_load_dwordx4 v4, s[40:43], s63 offen offset:0 lds\n"
"buffer_load_dwordx4 v5, s[40:43], s63 offen offset:1024 lds\n"
"buffer_load_dwordx4 v6, s[40:43], s63 offen offset:2048 lds\n"
"s_add_u32 s63, s63, 458752\n"
"ds_read_b64_tr_b16 a[0:1], v8 offset:0\n"
"ds_read_b64_tr_b16 a[2:3], v8 offset:768\n"
"ds_read_b64_tr_b16 a[4:5], v8 offset:12288\n"
"ds_read_b64_tr_b16 a[6:7], v8 offset:13056\n"
"ds_read_b64_tr_b16 a[8:9], v9 offset:0\n"
"ds_read_b64_tr_b16 a[10:11], v9 offset:768\n"
"ds_read_b64_tr_b16 a[12:13], v8 offset:6144\n"
"ds_read_b64_tr_b16 a[14:15], v8 offset:6912\n"
"ds_read_b64_tr_b16 a[16:17], v8 offset:18432\n"
"ds_read_b64_tr_b16 a[18:19], v8 offset:19200\n"
"ds_read_b64_tr_b16 a[20:21], v9 offset:6144\n"
"ds_read_b64_tr_b16 a[22:23], v9 offset:6912\n"
"s_waitcnt lgkmcnt(0)\n"
"v_mfma_f32_16x16x32_bf16 v[20:23], a[0:3], v[16:19], 0\n"
"v_mfma_f32_16x16x32_bf16 v[24:27], a[4:7], v[16:19], 0\n"
"v_mfma_f32_16x16x32_bf16 v[28:31], a[8:11], v[16:19], 0\n"
"v_mfma_f32_16x16x32_bf16 v[20:23], a[12:15], v[16:19], v[20:23]\n"
"v_mfma_f32_16x16x32_bf16 v[24:27], a[16:19], v[16:19], v[24:27]\n"
"v_mfma_f32_16x16x32_bf16 v[28:31], a[20:23], v[16:19], v[28:31]\n"
"s_waitcnt vmcnt(24)\n"
"s_barrier\n"
"s_add_u32 m0, s64, 0\n"
"s_nop 0\n"
"buffer_load_dwordx4 v4, s[36:39], s63 offen offset:0 lds\n"
"buffer_load_dwordx4 v5, s[36:39], s63 offen offset:1024 lds\n"
"buffer_load_dwordx4 v6, s[36:39], s63 offen offset:2048 lds\n"
"s_add_u32 m0, s64, 12288\n"
"s_nop 0\n"
"buffer_load_dwordx4 v4, s[40:43], s63 offen offset:0 lds\n"
"buffer_load_dwordx4 v5, s[40:43], s63 offen offset:1024 lds\n"
"buffer_load_dwordx4 v6, s[40:43], s63 offen offset:2048 lds\n"
"s_add_u32 s63, s63, 458752\n"
"ds_read_b64_tr_b16 a[24:25], v8 offset:24576\n"
"ds_read_b64_tr_b16 a[26:27], v8 offset:25344\n"
"ds_read_b64_tr_b16 a[28:29], v8 offset:36864\n"
"ds_read_b64_tr_b16 a[30:31], v8 offset:37632\n"
"ds_read_b64_tr_b16 a[32:33], v9 offset:24576\n"
"ds_read_b64_tr_b16 a[34:35], v9 offset:25344\n"
"ds_read_b64_tr_b16 a[36:37], v8 offset:30720\n"
"ds_read_b64_tr_b16 a[38:39], v8 offset:31488\n"
"ds_read_b64_tr_b16 a[40:41], v8 offset:43008\n"
"ds_read_b64_tr_b16 a[42:43], v8 offset:43776\n"
"ds_read_b64_tr_b16 a[44:45], v9 offset:30720\n"
"ds_read_b64_tr_b16 a[46:47], v9 offset:31488\n"
"s_waitcnt lgkmcnt(0)\n"
"v_mfma_f32_16x16x32_bf16 v[20:23], a[24:27], v[16:19], v[20:23]\n"
"v_mfma_f32_16x16x32_bf16 v[24:27], a[28:31], v[16:19], v[24:27]\n"
"v_mfma_f32_16x16x32_bf16 v[28:31], a[32:35], v[16:19], v[28:31]\n"
"v_mfma_f32_16x16x32_bf16 v[20:23], a[36:39], v[16:19], v[20:23]\n"
"v_mfma_f32_16x16x32_bf16 v[24:27], a[40:43], v[16:19], v[24:27]\n"
"v_mfma_f32_16x16x32_bf16 v[28:31], a[44:47], v[16:19], v[28:31]\n"
"s_waitcnt vmcnt(24)\n"
"s_barrier\n"
"s_add_u32 m0, s64, 24576\n"
"s_nop 0\n"
"buffer_load_dwordx4 v4, s[36:39], s63 offen offset:0 lds\n"
"buffer_load_dwordx4 v5, s[36:39], s63 offen offset:1024 lds\n"
"buffer_load_dwordx4 v6, s[36:39], s63 offen offset:2048 lds\n"
"s_add_u32 m0, s64, 36864\n"
"s_nop 0\n"
"buffer_load_dwordx4 v4, s[40:43], s63 offen offset:0 lds\n"
"buffer_load_dwordx4 v5, s[40:43], s63 offen offset:1024 lds\n"
"buffer_load_dwordx4 v6, s[40:43], s63 offen offset:2048 lds\n"
"s_add_u32 s63, s63, 458752\n"
"ds_read_b64_tr_b16 a[48:49], v10 offset:0\n"
"ds_read_b64_tr_b16 a[50:51], v10 offset:768\n"
"ds_read_b64_tr_b16 a[52:53], v10 offset:12288\n"
"ds_read_b64_tr_b16 a[54:55], v10 offset:13056\n"
"ds_read_b64_tr_b16 a[56:57], v11 offset:0\n"
"ds_read_b64_tr_b16 a[58:59], v11 offset:768\n"
"ds_read_b64_tr_b16 a[60:61], v10 offset:6144\n"
"ds_read_b64_tr_b16 a[62:63], v10 offset:6912\n"
"ds_read_b64_tr_b16 a[64:65], v10 offset:18432\n"
"ds_read_b64_tr_b16 a[66:67], v10 offset:19200\n"
"ds_read_b64_tr_b16 a[68:69], v11 offset:6144\n"
"ds_read_b64_tr_b16 a[70:71], v11 offset:6912\n"
"s_waitcnt lgkmcnt(0)\n"
"v_mfma_f32_16x16x32_bf16 v[20:23], a[48:51], v[16:19], v[20:23]\n"
"v_mfma_f32_16x16x32_bf16 v[24:27], a[52:55], v[16:19], v[24:27]\n"
"v_mfma_f32_16x16x32_bf16 v[28:31], a[56:59], v[16:19], v[28:31]\n"
"v_mfma_f32_16x16x32_bf16 v[20:23], a[60:63], v[16:19], v[20:23]\n"
"v_mfma_f32_16x16x32_bf16 v[24:27], a[64:67], v[16:19], v[24:27]\n"
"v_mfma_f32_16x16x32_bf16 v[28:31], a[68:71], v[16:19], v[28:31]\n"
"s_waitcnt vmcnt(24)\n"
"s_barrier\n"
"s_add_u32 m0, s64, 49152\n"
"s_nop 0\n"
"buffer_load_dwordx4 v4, s[36:39], s63 offen offset:0 lds\n"
"buffer_load_dwordx4 v5, s[36:39], s63 offen offset:1024 lds\n"
"buffer_load_dwordx4 v6, s[36:39], s63 offen offset:2048 lds\n"
"s_add_u32 m0, s64, 61440\n"
"s_nop 0\n"
"buffer_load_dwordx4 v4, s[40:43], s63 offen offset:0 lds\n"
"buffer_load_dwordx4 v5, s[40:43], s63 offen offset:1024 lds\n"
"buffer_load_dwordx4 v6, s[40:43], s63 offen offset:2048 lds\n"
"s_add_u32 s63, s63, 458752\n"
"ds_read_b64_tr_b16 a[72:73], v10 offset:24576\n"
"ds_read_b64_tr_b16 a[74:75], v10 offset:25344\n"
"ds_read_b64_tr_b16 a[76:77], v10 offset:36864\n"
"ds_read_b64_tr_b16 a[78:79], v10 offset:37632\n"
"ds_read_b64_tr_b16 a[80:81], v11 offset:24576\n"
"ds_read_b64_tr_b16 a[82:83], v11 offset:25344\n"
"ds_read_b64_tr_b16 a[84:85], v10 offset:30720\n"
"ds_read_b64_tr_b16 a[86:87], v10 offset:31488\n"
"ds_read_b64_tr_b16 a[88:89], v10 offset:43008\n"
"ds_read_b64_tr_b16 a[90:91], v10 offset:43776\n"
"ds_read_b64_tr_b16 a[92:93], v11 offset:30720\n"
"ds_read_b64_tr_b16 a[94:95], v11 offset:31488\n"
"s_waitcnt lgkmcnt(0)\n"
"v_mfma_f32_16x16x32_bf16 v[20:23], a[72:75], v[16:19], v[20:23]\n"
"v_mfma_f32_16x16x32_bf16 v[24:27], a[76:79], v[16:19], v[24:27]\n"
"v_mfma_f32_16x16x32_bf16 v[28:31], a[80:83], v[16:19], v[28:31]\n"
"v_mfma_f32_16x16x32_bf16 v[20:23], a[84:87], v[16:19], v[20:23]\n"
"v_mfma_f32_16x16x32_bf16 v[24:27], a[88:91], v[16:19], v[24:27]\n"
"v_mfma_f32_16x16x32_bf16 v[28:31], a[92:95], v[16:19], v[28:31]\n"
"s_waitcnt vmcnt(24)\n"
"s_barrier\n"
"s_add_u32 m0, s64, 73728\n"
"s_nop 0\n"
"buffer_load_dwordx4 v4, s[36:39], s63 offen offset:0 lds\n"
"buffer_load_dwordx4 v5, s[36:39], s63 offen offset:1024 lds\n"
"buffer_load_dwordx4 v6, s[36:39], s63 offen offset:2048 lds\n"
"s_add_u32 m0, s64, 86016\n"
"s_nop 0\n"
"buffer_load_dwordx4 v4, s[40:43], s63 offen offset:0 lds\n"
"buffer_load_dwordx4 v5, s[40:43], s63 offen offset:1024 lds\n"
"buffer_load_dwordx4 v6, s[40:43], s63 offen offset:2048 lds\n"
"s_add_u32 s63, s63, 458752\n"
"ds_read_b64_tr_b16 a[96:97], v12 offset:0\n"
"ds_read_b64_tr_b16 a[98:99], v12 offset:768\n"
"ds_read_b64_tr_b16 a[100:101], v12 offset:12288\n"
"ds_read_b64_tr_b16 a[102:103], v12 offset:13056\n"
"ds_read_b64_tr_b16 a[104:105], v13 offset:0\n"
"ds_read_b64_tr_b16 a[106:107], v13 offset:768\n"
"ds_read_b64_tr_b16 a[108:109], v12 offset:6144\n"
"ds_read_b64_tr_b16 a[110:111], v12 offset:6912\n"
"ds_read_b64_tr_b16 a[112:113], v12 offset:18432\n"
"ds_read_b64_tr_b16 a[114:115], v12 offset:19200\n"
"ds_read_b64_tr_b16 a[116:117], v13 offset:6144\n"
"ds_read_b64_tr_b16 a[118:119], v13 offset:6912\n"
"s_waitcnt lgkmcnt(0)\n"
"v_mfma_f32_16x16x32_bf16 v[20:23], a[96:99], v[16:19], v[20:23]\n"
"v_mfma_f32_16x16x32_bf16 v[24:27], a[100:103], v[16:19], v[24:27]\n"
"v_mfma_f32_16x16x32_bf16 v[28:31], a[104:107], v[16:19], v[28:31]\n"
"v_mfma_f32_16x16x32_bf16 v[20:23], a[108:111], v[16:19], v[20:23]\n"
"v_mfma_f32_16x16x32_bf16 v[24:27], a[112:115], v[16:19], v[24:27]\n"
"v_mfma_f32_16x16x32_bf16 v[28:31], a[116:119], v[16:19], v[28:31]\n"
"s_waitcnt vmcnt(24)\n"
"s_barrier\n"
"s_add_u32 m0, s64, 98304\n"
"s_nop 0\n"
"buffer_load_dwordx4 v4, s[36:39], s63 offen offset:0 lds\n"
"buffer_load_dwordx4 v5, s[36:39], s63 offen offset:1024 lds\n"
"buffer_load_dwordx4 v6, s[36:39], s63 offen offset:2048 lds\n"
"s_add_u32 m0, s64, 110592\n"
"s_nop 0\n"
"buffer_load_dwordx4 v4, s[40:43], s63 offen offset:0 lds\n"
"buffer_load_dwordx4 v5, s[40:43], s63 offen offset:1024 lds\n"
"buffer_load_dwordx4 v6, s[40:43], s63 offen offset:2048 lds\n"
"s_add_u32 s63, s63, 458752\n"
"ds_read_b64_tr_b16 a[120:121], v12 offset:24576\n"
"ds_read_b64_tr_b16 a[122:123], v12 offset:25344\n"
"ds_read_b64_tr_b16 a[124:125], v12 offset:36864\n"
"ds_read_b64_tr_b16 a[126:127], v12 offset:37632\n"
"ds_read_b64_tr_b16 a[128:129], v13 offset:24576\n"
"ds_read_b64_tr_b16 a[130:131], v13 offset:25344\n"
"ds_read_b64_tr_b16 a[132:133], v12 offset:30720\n"
"ds_read_b64_tr_b16 a[134:135], v12 offset:31488\n"
"ds_read_b64_tr_b16 a[136:137], v12 offset:43008\n"
"ds_read_b64_tr_b16 a[138:139], v12 offset:43776\n"
"ds_read_b64_tr_b16 a[140:141], v13 offset:30720\n"
"ds_read_b64_tr_b16 a[142:143], v13 offset:31488\n"
"s_waitcnt lgkmcnt(0)\n"
"v_mfma_f32_16x16x32_bf16 v[20:23], a[120:123], v[16:19], v[20:23]\n"
"v_mfma_f32_16x16x32_bf16 v[24:27], a[124:127], v[16:19], v[24:27]\n"
"v_mfma_f32_16x16x32_bf16 v[28:31], a[128:131], v[16:19], v[28:31]\n"
"v_mfma_f32_16x16x32_bf16 v[20:23], a[132:135], v[16:19], v[20:23]\n"
"v_mfma_f32_16x16x32_bf16 v[24:27], a[136:139], v[16:19], v[24:27]\n"
"v_mfma_f32_16x16x32_bf16 v[28:31], a[140:143], v[16:19], v[28:31]\n"
"s_waitcnt vmcnt(24)\n"
"s_barrier\n"
"s_add_u32 m0, s64, 122880\n"
"s_nop 0\n"
"buffer_load_dwordx4 v4, s[36:39], s63 offen offset:0 lds\n"
"buffer_load_dwordx4 v5, s[36:39], s63 offen offset:1024 lds\n"
"buffer_load_dwordx4 v6, s[36:39], s63 offen offset:2048 lds\n"
"s_add_u32 m0, s64, 135168\n"
"s_nop 0\n"
"buffer_load_dwordx4 v4, s[40:43], s63 offen offset:0 lds\n"
"buffer_load_dwordx4 v5, s[40:43], s63 offen offset:1024 lds\n"
"buffer_load_dwordx4 v6, s[40:43], s63 offen offset:2048 lds\n"
"s_add_u32 s63, s63, 458752\n"
"ds_read_b64_tr_b16 a[144:145], v8 offset:0\n"
"ds_read_b64_tr_b16 a[146:147], v8 offset:768\n"
"ds_read_b64_tr_b16 a[148:149], v8 offset:12288\n"
"ds_read_b64_tr_b16 a[150:151], v8 offset:13056\n"
"ds_read_b64_tr_b16 a[152:153], v9 offset:0\n"
"ds_read_b64_tr_b16 a[154:155], v9 offset:768\n"
"ds_read_b64_tr_b16 a[156:157], v8 offset:6144\n"
"ds_read_b64_tr_b16 a[158:159], v8 offset:6912\n"
"ds_read_b64_tr_b16 a[160:161], v8 offset:18432\n"
"ds_read_b64_tr_b16 a[162:163], v8 offset:19200\n"
"ds_read_b64_tr_b16 a[164:165], v9 offset:6144\n"
"ds_read_b64_tr_b16 a[166:167], v9 offset:6912\n"
"s_waitcnt lgkmcnt(0)\n"
"v_mfma_f32_16x16x32_bf16 v[20:23], a[144:147], v[16:19], v[20:23]\n"
"v_mfma_f32_16x16x32_bf16 v[24:27], a[148:151], v[16:19], v[24:27]\n"
"v_mfma_f32_16x16x32_bf16 v[28:31], a[152:155], v[16:19], v[28:31]\n"
"v_mfma_f32_16x16x32_bf16 v[20:23], a[156:159], v[16:19], v[20:23]\n"
"v_mfma_f32_16x16x32_bf16 v[24:27], a[160:163], v[16:19], v[24:27]\n"
"v_mfma_f32_16x16x32_bf16 v[28:31], a[164:167], v[16:19], v[28:31]\n"
"s_waitcnt vmcnt(24)\n"
"s_barrier\n"
"s_add_u32 m0, s64, 0\n"
"s_nop 0\n"
"buffer_load_dwordx4 v4, s[36:39], s63 offen offset:0 lds\n"
"buffer_load_dwordx4 v5, s[36:39], s63 offen offset:1024 lds\n"
"buffer_load_dwordx4 v6, s[36:39], s63 offen offset:2048 lds\n"
"s_add_u32 m0, s64, 12288\n"
"s_nop 0\n"
"buffer_load_dwordx4 v4, s[40:43], s63 offen offset:0 lds\n"
"buffer_load_dwordx4 v5, s[40:43], s63 offen offset:1024 lds\n"
"buffer_load_dwordx4 v6, s[40:43], s63 offen offset:2048 lds\n"
"s_add_u32 s63, s63, 458752\n"
"ds_read_b64_tr_b16 a[168:169], v8 offset:24576\n"
"ds_read_b64_tr_b16 a[170:171], v8 offset:25344\n"
"ds_read_b64_tr_b16 a[172:173], v8 offset:36864\n"
"ds_read_b64_tr_b16 a[174:175], v8 offset:37632\n"
"ds_read_b64_tr_b16 a[176:177], v9 offset:24576\n"
"ds_read_b64_tr_b16 a[178:179], v9 offset:25344\n"
"ds_read_b64_tr_b16 a[180:181], v8 offset:30720\n"
"ds_read_b64_tr_b16 a[182:183], v8 offset:31488\n"
"ds_read_b64_tr_b16 a[184:185], v8 offset:43008\n"
"ds_read_b64_tr_b16 a[186:187], v8 offset:43776\n"
"ds_read_b64_tr_b16 a[188:189], v9 offset:30720\n"
"ds_read_b64_tr_b16 a[190:191], v9 offset:31488\n"
"s_waitcnt lgkmcnt(0)\n"
"v_mfma_f32_16x16x32_bf16 v[20:23], a[168:171], v[16:19], v[20:23]\n"
"v_mfma_f32_16x16x32_bf16 v[24:27], a[172:175], v[16:19], v[24:27]\n"
"v_mfma_f32_16x16x32_bf16 v[28:31], a[176:179], v[16:19], v[28:31]\n"
"v_mfma_f32_16x16x32_bf16 v[20:23], a[180:183], v[16:19], v[20:23]\n"
"v_mfma_f32_16x16x32_bf16 v[24:27], a[184:187], v[16:19], v[24:27]\n"
"v_mfma_f32_16x16x32_bf16 v[28:31], a[188:191], v[16:19], v[28:31]\n"
"s_waitcnt vmcnt(24)\n"
"s_barrier\n"
"s_add_u32 m0, s64, 24576\n"
"s_nop 0\n"
"buffer_load_dwordx4 v4, s[36:39], s63 offen offset:0 lds\n"
"buffer_load_dwordx4 v5, s[36:39], s63 offen offset:1024 lds\n"
"buffer_load_dwordx4 v6, s[36:39], s63 offen offset:2048 lds\n"
"s_add_u32 m0, s64, 36864\n"
"s_nop 0\n"
"buffer_load_dwordx4 v4, s[40:43], s63 offen offset:0 lds\n"
"buffer_load_dwordx4 v5, s[40:43], s63 offen offset:1024 lds\n"
"buffer_load_dwordx4 v6, s[40:43], s63 offen offset:2048 lds\n"
"s_add_u32 s63, s63, 458752\n"
"ds_read_b64_tr_b16 a[192:193], v10 offset:0\n"
"ds_read_b64_tr_b16 a[194:195], v10 offset:768\n"
"ds_read_b64_tr_b16 a[196:197], v10 offset:12288\n"
"ds_read_b64_tr_b16 a[198:199], v10 offset:13056\n"
"ds_read_b64_tr_b16 a[200:201], v11 offset:0\n"
"ds_read_b64_tr_b16 a[202:203], v11 offset:768\n"
"ds_read_b64_tr_b16 a[204:205], v10 offset:6144\n"
"ds_read_b64_tr_b16 a[206:207], v10 offset:6912\n"
"ds_read_b64_tr_b16 a[208:209], v10 offset:18432\n"
"ds_read_b64_tr_b16 a[210:211], v10 offset:19200\n"
"ds_read_b64_tr_b16 a[212:213], v11 offset:6144\n"
"ds_read_b64_tr_b16 a[214:215], v11 offset:6912\n"
"s_waitcnt lgkmcnt(0)\n"
"v_mfma_f32_16x16x32_bf16 v[20:23], a[192:195], v[16:19], v[20:23]\n"
"v_mfma_f32_16x16x32_bf16 v[24:27], a[196:199], v[16:19], v[24:27]\n"
"v_mfma_f32_16x16x32_bf16 v[28:31], a[200:203], v[16:19], v[28:31]\n"
"v_mfma_f32_16x16x32_bf16 v[20:23], a[204:207], v[16:19], v[20:23]\n"
"v_mfma_f32_16x16x32_bf16 v[24:27], a[208:211], v[16:19], v[24:27]\n"
"v_mfma_f32_16x16x32_bf16 v[28:31], a[212:215], v[16:19], v[28:31]\n"
"s_waitcnt vmcnt(24)\n"
"s_barrier\n"
"s_add_u32 m0, s64, 49152\n"
"s_nop 0\n"
"buffer_load_dwordx4 v4, s[36:39], s63 offen offset:0 lds\n"
"buffer_load_dwordx4 v5, s[36:39], s63 offen offset:1024 lds\n"
"buffer_load_dwordx4 v6, s[36:39], s63 offen offset:2048 lds\n"
"s_add_u32 m0, s64, 61440\n"
"s_nop 0\n"
"buffer_load_dwordx4 v4, s[40:43], s63 offen offset:0 lds\n"
"buffer_load_dwordx4 v5, s[40:43], s63 offen offset:1024 lds\n"
"buffer_load_dwordx4 v6, s[40:43], s63 offen offset:2048 lds\n"
"s_add_u32 s63, s63, 458752\n"
"ds_read_b64_tr_b16 a[216:217], v10 offset:24576\n"
"ds_read_b64_tr_b16 a[218:219], v10 offset:25344\n"
"ds_read_b64_tr_b16 a[220:221], v10 offset:36864\n"
"ds_read_b64_tr_b16 a[222:223], v10 offset:37632\n"
"ds_read_b64_tr_b16 a[224:225], v11 offset:24576\n"
"ds_read_b64_tr_b16 a[226:227], v11 offset:25344\n"
"ds_read_b64_tr_b16 a[228:229], v10 offset:30720\n"
"ds_read_b64_tr_b16 a[230:231], v10 offset:31488\n"
"ds_read_b64_tr_b16 a[232:233], v10 offset:43008\n"
"ds_read_b64_tr_b16 a[234:235], v10 offset:43776\n"
"ds_read_b64_tr_b16 a[236:237], v11 offset:30720\n"
"ds_read_b64_tr_b16 a[238:239], v11 offset:31488\n"
"s_waitcnt lgkmcnt(0)\n"
"v_mfma_f32_16x16x32_bf16 v[20:23], a[216:219], v[16:19], v[20:23]\n"
"v_mfma_f32_16x16x32_bf16 v[24:27], a[220:223], v[16:19], v[24:27]\n"
"v_mfma_f32_16x16x32_bf16 v[28:31], a[224:227], v[16:19], v[28:31]\n"
"v_mfma_f32_16x16x32_bf16 v[20:23], a[228:231], v[16:19], v[20:23]\n"
"v_mfma_f32_16x16x32_bf16 v[24:27], a[232:235], v[16:19], v[24:27]\n"
"v_mfma_f32_16x16x32_bf16 v[28:31], a[236:239], v[16:19], v[28:31]\n"
"s_waitcnt vmcnt(24)\n"
"s_barrier\n"
"s_add_u32 m0, s64, 73728\n"
"s_nop 0\n"
"buffer_load_dwordx4 v4, s[36:39], s63 offen offset:0 lds\n"
"buffer_load_dwordx4 v5, s[36:39], s63 offen offset:1024 lds\n"
"buffer_load_dwordx4 v6, s[36:39], s63 offen offset:2048 lds\n"
"s_add_u32 m0, s64, 86016\n"
"s_nop 0\n"
"buffer_load_dwordx4 v4, s[40:43], s63 offen offset:0 lds\n"
"buffer_load_dwordx4 v5, s[40:43], s63 offen offset:1024 lds\n"
"buffer_load_dwordx4 v6, s[40:43], s63 offen offset:2048 lds\n"
"s_add_u32 s63, s63, 458752\n"
"ds_read_b64_tr_b16 a[240:241], v12 offset:0\n"
"ds_read_b64_tr_b16 a[242:243], v12 offset:768\n"
"ds_read_b64_tr_b16 a[244:245], v12 offset:12288\n"
"ds_read_b64_tr_b16 a[246:247], v12 offset:13056\n"
"ds_read_b64_tr_b16 a[248:249], v13 offset:0\n"
"ds_read_b64_tr_b16 a[250:251], v13 offset:768\n"
"ds_read_b64_tr_b16 a[252:253], v12 offset:6144\n"
"ds_read_b64_tr_b16 a[254:255], v12 offset:6912\n"
"ds_read_b64_tr_b16 v[128:129], v12 offset:18432\n"
"ds_read_b64_tr_b16 v[130:131], v12 offset:19200\n"
"ds_read_b64_tr_b16 v[132:133], v13 offset:6144\n"
"ds_read_b64_tr_b16 v[134:135], v13 offset:6912\n"
"s_waitcnt lgkmcnt(0)\n"
"v_mfma_f32_16x16x32_bf16 v[20:23], a[240:243], v[16:19], v[20:23]\n"
"v_mfma_f32_16x16x32_bf16 v[24:27], a[244:247], v[16:19], v[24:27]\n"
"v_mfma_f32_16x16x32_bf16 v[28:31], a[248:251], v[16:19], v[28:31]\n"
"v_mfma_f32_16x16x32_bf16 v[20:23], a[252:255], v[16:19], v[20:23]\n"
"v_mfma_f32_16x16x32_bf16 v[24:27], v[128:131], v[16:19], v[24:27]\n"
"v_mfma_f32_16x16x32_bf16 v[28:31], v[132:135], v[16:19], v[28:31]\n"
"s_waitcnt vmcnt(24)\n"
"s_barrier\n"
"ds_read_b64_tr_b16 v[136:137], v12 offset:24576\n"
"ds_read_b64_tr_b16 v[138:139], v12 offset:25344\n"
"ds_read_b64_tr_b16 v[140:141], v12 offset:36864\n"
"ds_read_b64_tr_b16 v[142:143], v12 offset:37632\n"
"ds_read_b64_tr_b16 v[144:145], v13 offset:24576\n"
"ds_read_b64_tr_b16 v[146:147], v13 offset:25344\n"
"ds_read_b64_tr_b16 v[148:149], v12 offset:30720\n"
"ds_read_b64_tr_b16 v[150:151], v12 offset:31488\n"
"ds_read_b64_tr_b16 v[152:153], v12 offset:43008\n"
"ds_read_b64_tr_b16 v[154:155], v12 offset:43776\n"
"ds_read_b64_tr_b16 v[156:157], v13 offset:30720\n"
"ds_read_b64_tr_b16 v[158:159], v13 offset:31488\n"
"s_waitcnt lgkmcnt(0)\n"
"v_mfma_f32_16x16x32_bf16 v[20:23], v[136:139], v[16:19], v[20:23]\n"
"v_mfma_f32_16x16x32_bf16 v[24:27], v[140:143], v[16:19], v[24:27]\n"
"v_mfma_f32_16x16x32_bf16 v[28:31], v[144:147], v[16:19], v[28:31]\n"
"v_mfma_f32_16x16x32_bf16 v[20:23], v[148:151], v[16:19], v[20:23]\n"
"v_mfma_f32_16x16x32_bf16 v[24:27], v[152:155], v[16:19], v[24:27]\n"
"v_mfma_f32_16x16x32_bf16 v[28:31], v[156:159], v[16:19], v[28:31]\n"
"s_waitcnt vmcnt(18)\n"
"s_barrier\n"
"ds_read_b64_tr_b16 v[160:161], v8 offset:0\n"
"ds_read_b64_tr_b16 v[162:163], v8 offset:768\n"
"ds_read_b64_tr_b16 v[164:165], v8 offset:12288\n"
"ds_read_b64_tr_b16 v[166:167], v8 offset:13056\n"
"ds_read_b64_tr_b16 v[168:169], v9 offset:0\n"
"ds_read_b64_tr_b16 v[170:171], v9 offset:768\n"
"ds_read_b64_tr_b16 v[172:173], v8 offset:6144\n"
"ds_read_b64_tr_b16 v[174:175], v8 offset:6912\n"
"ds_read_b64_tr_b16 v[176:177], v8 offset:18432\n"
"ds_read_b64_tr_b16 v[178:179], v8 offset:19200\n"
"ds_read_b64_tr_b16 v[180:181], v9 offset:6144\n"
"ds_read_b64_tr_b16 v[182:183], v9 offset:6912\n"
"s_waitcnt lgkmcnt(0)\n"
"v_mfma_f32_16x16x32_bf16 v[20:23], v[160:163], v[16:19], v[20:23]\n"
"v_mfma_f32_16x16x32_bf16 v[24:27], v[164:167], v[16:19], v[24:27]\n"
"v_mfma_f32_16x16x32_bf16 v[28:31], v[168:171], v[16:19], v[28:31]\n"
"v_mfma_f32_16x16x32_bf16 v[20:23], v[172:175], v[16:19], v[20:23]\n"
"v_mfma_f32_16x16x32_bf16 v[24:27], v[176:179], v[16:19], v[24:27]\n"
"v_mfma_f32_16x16x32_bf16 v[28:31], v[180:183], v[16:19], v[28:31]\n"
"s_waitcnt vmcnt(12)\n"
"s_barrier\n"
"ds_read_b64_tr_b16 v[184:185], v8 offset:24576\n"
"ds_read_b64_tr_b16 v[186:187], v8 offset:25344\n"
"ds_read_b64_tr_b16 v[188:189], v8 offset:36864\n"
"ds_read_b64_tr_b16 v[190:191], v8 offset:37632\n"
"ds_read_b64_tr_b16 v[192:193], v9 offset:24576\n"
"ds_read_b64_tr_b16 v[194:195], v9 offset:25344\n"
"ds_read_b64_tr_b16 v[196:197], v8 offset:30720\n"
"ds_read_b64_tr_b16 v[198:199], v8 offset:31488\n"
"ds_read_b64_tr_b16 v[200:201], v8 offset:43008\n"
"ds_read_b64_tr_b16 v[202:203], v8 offset:43776\n"
"ds_read_b64_tr_b16 v[204:205], v9 offset:30720\n"
"ds_read_b64_tr_b16 v[206:207], v9 offset:31488\n"
"s_waitcnt lgkmcnt(0)\n"
"v_mfma_f32_16x16x32_bf16 v[20:23], v[184:187], v[16:19], v[20:23]\n"
"v_mfma_f32_16x16x32_bf16 v[24:27], v[188:191], v[16:19], v[24:27]\n"
"v_mfma_f32_16x16x32_bf16 v[28:31], v[192:195], v[16:19], v[28:31]\n"
"v_mfma_f32_16x16x32_bf16 v[20:23], v[196:199], v[16:19], v[20:23]\n"
"v_mfma_f32_16x16x32_bf16 v[24:27], v[200:203], v[16:19], v[24:27]\n"
"v_mfma_f32_16x16x32_bf16 v[28:31], v[204:207], v[16:19], v[28:31]\n"
"s_waitcnt vmcnt(6)\n"
"s_barrier\n"
"ds_read_b64_tr_b16 v[208:209], v10 offset:0\n"
"ds_read_b64_tr_b16 v[210:211], v10 offset:768\n"
"ds_read_b64_tr_b16 v[212:213], v10 offset:12288\n"
"ds_read_b64_tr_b16 v[214:215], v10 offset:13056\n"
"ds_read_b64_tr_b16 v[216:217], v11 offset:0\n"
"ds_read_b64_tr_b16 v[218:219], v11 offset:768\n"
"ds_read_b64_tr_b16 v[220:221], v10 offset:6144\n"
"ds_read_b64_tr_b16 v[222:223], v10 offset:6912\n"
"ds_read_b64_tr_b16 v[224:225], v10 offset:18432\n"
"ds_read_b64_tr_b16 v[226:227], v10 offset:19200\n"
"ds_read_b64_tr_b16 v[228:229], v11 offset:6144\n"
"ds_read_b64_tr_b16 v[230:231], v11 offset:6912\n"
"s_waitcnt lgkmcnt(0)\n"
"v_mfma_f32_16x16x32_bf16 v[20:23], v[208:211], v[16:19], v[20:23]\n"
"v_mfma_f32_16x16x32_bf16 v[24:27], v[212:215], v[16:19], v[24:27]\n"
"v_mfma_f32_16x16x32_bf16 v[28:31], v[216:219], v[16:19], v[28:31]\n"
"v_mfma_f32_16x16x32_bf16 v[20:23], v[220:223], v[16:19], v[20:23]\n"
"v_mfma_f32_16x16x32_bf16 v[24:27], v[224:227], v[16:19], v[24:27]\n"
"v_mfma_f32_16x16x32_bf16 v[28:31], v[228:231], v[16:19], v[28:31]\n"
"s_waitcnt vmcnt(0)\n"
"s_barrier\n"
"ds_read_b64_tr_b16 v[232:233], v10 offset:24576\n"
"ds_read_b64_tr_b16 v[234:235], v10 offset:25344\n"
"ds_read_b64_tr_b16 v[236:237], v10 offset:36864\n"
"ds_read_b64_tr_b16 v[238:239], v10 offset:37632\n"
"ds_read_b64_tr_b16 v[240:241], v11 offset:24576\n"
"ds_read_b64_tr_b16 v[242:243], v11 offset:25344\n"
"ds_read_b64_tr_b16 v[244:245], v10 offset:30720\n"
"ds_read_b64_tr_b16 v[246:247], v10 offset:31488\n"
"ds_read_b64_tr_b16 v[248:249], v10 offset:43008\n"
"ds_read_b64_tr_b16 v[250:251], v10 offset:43776\n"
"ds_read_b64_tr_b16 v[252:253], v11 offset:30720\n"
"ds_read_b64_tr_b16 v[254:255], v11 offset:31488\n"
"s_waitcnt lgkmcnt(0)\n"
"v_mfma_f32_16x16x32_bf16 v[20:23], v[232:235], v[16:19], v[20:23]\n"
"v_mfma_f32_16x16x32_bf16 v[24:27], v[236:239], v[16:19], v[24:27]\n"
"v_mfma_f32_16x16x32_bf16 v[28:31], v[240:243], v[16:19], v[28:31]\n"
"v_mfma_f32_16x16x32_bf16 v[20:23], v[244:247], v[16:19], v[20:23]\n"
"v_mfma_f32_16x16x32_bf16 v[24:27], v[248:251], v[16:19], v[24:27]\n"
"v_mfma_f32_16x16x32_bf16 v[28:31], v[252:255], v[16:19], v[28:31]\n"
"s_barrier\n"
"s_lshl2_add_u32 s49, s27, 128\n"
"s_mul_i32 s68, s27, 3072\n"
"s_mul_i32 s48, s26, 768\n"
"s_cmp_eq_u32 s28, 0\n"
"s_addc_u32 s62, 84, 0\n"
"v_cmp_gt_u32 vcc, 48, v1\n"
"v_mov_b32 v15, 0x7fff0000\n"
"s_nop 0\n"
"v_cndmask_b32 v15, v15, v2, vcc\n"
"s_add_u32 s68, s68, s48\n"
"s_add_u32 s68, s68, 4718592\n"
"s_waitcnt lgkmcnt(0)\n"
"s_add_u32 s54, s52, 0x7f4a7c15\n"
"s_addc_u32 s55, s53, 0x9e3779b9\n"
"s_lshl_b32 s67, s49, 5\n"
"s_add_u32 s67, s67, 4898816\n"
"s_sub_u32 s51, s67, s68\n"
"s_add_u32 s92, s32, s51\n"
"s_addc_u32 s93, s33, 0x10000\n"
"s_sub_u32 s92, s92, 890880\n"
"s_subb_u32 s93, s93, 0\n"
"s_cmp_eq_u32 s26, 3\n"
"s_cselect_b32 s94, 1, 0\n"
"s_mov_b32 s95, 0x20000\n"
"v_mov_b32 v16, s54\n"
"v_mov_b32 v17, s55\n"
"v_cmp_gt_u32 vcc, 12, v1\n"
"v_mul_u32_u24 v3, 296960, v1\n"
"v_mov_b32 v12, 0x7fff0000\n"
"v_cndmask_b32 v3, v12, v3, vcc\n"
"s_add_u32 s51, s67, 76021760\n"
"buffer_store_dwordx2 v[16:17], v3, s[32:35], s51 offen sc1\n"
"s_sub_u32 s67, s68, 296960\n"
"s_mul_i32 s48, s28, 296960\n"
"s_add_u32 s68, s68, s48\n"
"s_sub_u32 s68, s68, 1781760\n"
"s_add_u32 s63, s48, 4681728\n"
"s_add_u32 s66, s48, 4898816\n"
"s_mul_i32 s64, s26, 9216\n"
"s_add_u32 s65, s64, 73728\n"
"s_xor_b32 s65, s65, s64\n"
"s_add_u32 s63, s63, s64\n"
"v_mov_b32 v4, v2\n"
"v_add_u32 v5, 36864, v2\n"
"v_add_u32 v6, 4096, v2\n"
"v_add_u32 v7, 8192, v2\n"
"v_bfe_u32 v3, v1, 1, 3\n"
"v_mul_u32_u24 v8, 1152, v3\n"
"v_lshrrev_b32 v3, 1, v3\n"
"v_add_u32 v3, s26, v3\n"
"v_lshrrev_b32 v9, 4, v1\n"
"v_lshl_add_u32 v3, v3, 2, v9\n"
"v_and_b32 v3, 15, v3\n"
"v_lshl_add_u32 v8, v3, 2, v8\n"
"v_and_b32 v3, 1, v1\n"
"v_lshl_add_u32 v8, v3, 6, v8\n"
"v_add_u32 v8, 1024, v8\n"
"s_lshl_b32 s48, s26, 2\n"
"v_add_u32 v10, s48, v9\n"
"v_and_b32 v11, 15, v1\n"
"v_add_u32 v3, 64, v1\n"
"v_cmp_eq_u32 vcc, 0, v11\n"
"v_add_u32 v9, 73728, v8\n"
"v_xor_b32 v9, v9, v8\n"
"v_cndmask_b32 v10, v3, v10, vcc\n"
"v_lshlrev_b32 v10, 2, v10\n"
"v_add_u32 v10, 147456, v10\n"
"v_lshlrev_b32 v11, 2, v11\n"
"v_add_u32 v11, 147456, v11\n"
"s_lshl_b32 s48, s26, 3\n"
"s_add_u32 s48, s48, 96\n"
"v_add_u32 v14, s48, v1\n"
"v_lshrrev_b32 v14, 2, v14\n"
"v_lshlrev_b32 v14, 7, v14\n"
"v_cmp_gt_u32 vcc, 8, v1\n"
"v_mov_b32 v3, 0x7fff0000\n"
"s_nop 0\n"
"v_cndmask_b32 v14, v3, v14, vcc\n"
"v_cndmask_b32 v16, 0, v16, vcc\n"
"v_cndmask_b32 v17, 0, v17, vcc\n"
"s_mov_b32 s48, 20000\n"
"s_branch .L_rfga\n"
".L_rlga:\n"
"s_sleep 8\n"
".L_rfga:\n"
"buffer_load_dwordx2 v[12:13], v14, s[32:35], s66 offen sc1\n"
"s_sub_u32 s48, s48, 1\n"
"s_cbranch_scc1 .L_end\n"
"s_waitcnt vmcnt(0)\n"
"v_cmp_ne_u64 vcc, v[16:17], v[12:13]\n"
"s_cbranch_vccnz .L_rlga\n"
"s_add_u32 s66, s66, 890880\n"
"s_add_u32 m0, s64, 0\n"
"s_nop 0\n"
"buffer_load_dwordx4 v2, s[32:35], s63 offen offset:0 sc1 lds\n"
"buffer_load_dwordx4 v2, s[32:35], s63 offen offset:1024 sc1 lds\n"
"buffer_load_dwordx4 v2, s[32:35], s63 offen offset:2048 sc1 lds\n"
"buffer_load_dwordx4 v2, s[32:35], s63 offen offset:3072 sc1 lds\n"
"s_add_u32 m0, s64, 4096\n"
"s_nop 0\n"
"buffer_load_dwordx4 v6, s[32:35], s63 offen offset:0 sc1 lds\n"
"buffer_load_dwordx4 v6, s[32:35], s63 offen offset:1024 sc1 lds\n"
"buffer_load_dwordx4 v6, s[32:35], s63 offen offset:2048 sc1 lds\n"
"buffer_load_dwordx4 v6, s[32:35], s63 offen offset:3072 sc1 lds\n"
"s_add_u32 m0, s64, 8192\n"
"s_nop 0\n"
"buffer_load_dwordx4 v7, s[32:35], s63 offen offset:0 sc1 lds\n"
"s_add_u32 s63, s63, 890880\n"
"s_mov_b32 s48, 20000\n"
"s_branch .L_rfgb\n"
".L_rlgb:\n"
"s_sleep 8\n"
".L_rfgb:\n"
"buffer_load_dwordx2 v[12:13], v14, s[32:35], s66 offen sc1\n"
"s_sub_u32 s48, s48, 1\n"
"s_cbranch_scc1 .L_end\n"
"s_waitcnt vmcnt(0)\n"
"v_cmp_ne_u64 vcc, v[16:17], v[12:13]\n"
"s_cbranch_vccnz .L_rlgb\n"
"s_add_u32 s66, s66, 890880\n"
"s_barrier\n"
"s_add_u32 m0, s64, 36864\n"
"s_nop 0\n"
"buffer_load_dwordx4 v2, s[32:35], s63 offen offset:0 sc1 lds\n"
"buffer_load_dwordx4 v2, s[32:35], s63 offen offset:1024 sc1 lds\n"
"buffer_load_dwordx4 v2, s[32:35], s63 offen offset:2048 sc1 lds\n"
"buffer_load_dwordx4 v2, s[32:35], s63 offen offset:3072 sc1 lds\n"
"s_add_u32 m0, s64, 40960\n"
"s_nop 0\n"
"buffer_load_dwordx4 v6, s[32:35], s63 offen offset:0 sc1 lds\n"
"buffer_load_dwordx4 v6, s[32:35], s63 offen offset:1024 sc1 lds\n"
"buffer_load_dwordx4 v6, s[32:35], s63 offen offset:2048 sc1 lds\n"
"buffer_load_dwordx4 v6, s[32:35], s63 offen offset:3072 sc1 lds\n"
"s_add_u32 m0, s64, 45056\n"
"s_nop 0\n"
"buffer_load_dwordx4 v7, s[32:35], s63 offen offset:0 sc1 lds\n"
"s_add_u32 s63, s63, 890880\n"
"s_xor_b32 s64, s64, s65\n"
"ds_read2st64_b32 v[56:57], v8 offset0:0 offset1:36\n"
"ds_read2st64_b32 v[58:59], v8 offset0:72 offset1:108\n"
"s_waitcnt lgkmcnt(0)\n"
"v_add_f32 v56, v56, v57\n"
"v_add_f32 v58, v58, v59\n"
"v_add_f32 v56, v56, v58\n"
"s_nop 1\n"
"v_add_f32_dpp v56, v56, v56 row_ror:8 row_mask:0xf bank_mask:0xf\n"
"s_nop 1\n"
"v_add_f32_dpp v56, v56, v56 row_ror:4 row_mask:0xf bank_mask:0xf\n"
"s_nop 1\n"
"v_add_f32_dpp v56, v56, v56 row_ror:2 row_mask:0xf bank_mask:0xf\n"
"v_mul_f32 v56, 0x3a800000, v56\n"
"v_mul_f32 v57, v56, v56\n"
"s_nop 1\n"
"v_sub_f32_dpp v57, v56, v57 quad_perm:[1,1,3,3] row_mask:0xf bank_mask:0xf\n"
"v_max_f32 v57, 0, v57\n"
"v_cvt_pk_bf16_f32 v57, 0, v57\n"
"v_add_f32 v57, 0x3727c5ac, v57\n"
"v_cvt_pk_bf16_f32 v57, 0, v57\n"
"v_sqrt_f32 v57, v57\n"
"s_nop 0\n"
"v_cvt_pk_bf16_f32 v57, v56, v57\n"
"ds_write_b32 v10, v57 offset:0\n"
"ds_read_b128 v[76:79], v4 offset:0\n"
"ds_read_b128 v[80:83], v4 offset:1152\n"
"ds_read_b128 v[84:87], v4 offset:2304\n"
"ds_read_b128 v[88:91], v4 offset:3456\n"
"ds_read_b128 v[92:95], v4 offset:4608\n"
"ds_read_b128 v[96:99], v4 offset:5760\n"
"ds_read_b128 v[100:103], v4 offset:6912\n"
"ds_read_b128 v[104:107], v4 offset:8064\n"
"s_mov_b32 s48, 20000\n"
"s_branch .L_rfgc\n"
".L_rlgc:\n"
"s_sleep 8\n"
".L_rfgc:\n"
"buffer_load_dwordx2 v[12:13], v14, s[32:35], s66 offen sc1\n"
"s_sub_u32 s48, s48, 1\n"
"s_cbranch_scc1 .L_end\n"
"s_waitcnt vmcnt(0)\n"
"v_cmp_ne_u64 vcc, v[16:17], v[12:13]\n"
"s_cbranch_vccnz .L_rlgc\n"
"s_waitcnt lgkmcnt(0)\n"
"s_cmp_eq_u32 s26, 1\n"
"s_cbranch_scc1 .L_gv1\n"
"s_cmp_eq_u32 s26, 2\n"
"s_cbranch_scc1 .L_gv2\n"
"s_cmp_eq_u32 s26, 3\n"
"s_cbranch_scc1 .L_gv3\n"
".p2align 6\n"
".L_gv0:\n"
".L_gl_0:\n"
"s_waitcnt lgkmcnt(5)\n"
"v_mfma_f32_16x16x32_bf16 v[32:35], a[0:3], v[76:79], 0\n"
"ds_read_b128 v[108:111], v4 offset:9216\n"
"s_add_u32 s68, s68, 890880\n"
"v_mfma_f32_16x16x32_bf16 v[32:35], a[12:15], v[80:83], v[32:35]\n"
"v_fma_f32 v52, -v60, v28, v52\n"
"v_fma_f32 v53, -v60, v29, v53\n"
"v_mfma_f32_16x16x32_bf16 v[32:35], a[24:27], v[84:87], v[32:35]\n"
"v_fma_f32 v54, -v60, v30, v54\n"
"v_fma_f32 v55, -v60, v31, v55\n"
"v_mfma_f32_16x16x32_bf16 v[32:35], a[36:39], v[88:91], v[32:35]\n"
"ds_read_b128 v[112:115], v4 offset:10368\n"
"v_fma_f32 v48, -v60, v24, v48\n"
"v_mfma_f32_16x16x32_bf16 v[36:39], a[40:43], v[88:91], 0\n"
"v_permlane32_swap_b32 v52, v54\n"
"v_mfma_f32_16x16x32_bf16 v[36:39], a[28:31], v[84:87], v[36:39]\n"
"s_waitcnt vmcnt(4)\n"
"v_cmp_ne_u64 vcc, v[16:17], v[12:13]\n"
"v_mfma_f32_16x16x32_bf16 v[36:39], a[16:19], v[80:83], v[36:39]\n"
"ds_read_b128 v[116:119], v4 offset:11520\n"
"v_fma_f32 v49, -v60, v25, v49\n"
"v_mfma_f32_16x16x32_bf16 v[36:39], a[4:7], v[76:79], v[36:39]\n"
"s_cbranch_vccnz .L_rtg0_0\n"
".L_bkg0_0:\n"
"s_add_u32 s66, s66, 890880\n"
"v_mfma_f32_16x16x32_bf16 v[40:43], a[8:11], v[76:79], 0\n"
"s_add_u32 m0, s64, 0\n"
"v_fma_f32 v50, -v60, v26, v50\n"
"v_mfma_f32_16x16x32_bf16 v[40:43], a[20:23], v[80:83], v[40:43]\n"
"buffer_load_dwordx4 v2, s[32:35], s63 offen offset:0 sc1 lds\n"
"ds_read_b128 v[120:123], v4 offset:12672\n"
"v_mfma_f32_16x16x32_bf16 v[40:43], a[32:35], v[84:87], v[40:43]\n"
"v_permlane32_swap_b32 v53, v55\n"
"v_mfma_f32_16x16x32_bf16 v[40:43], a[44:47], v[88:91], v[40:43]\n"
"v_fma_f32 v51, -v60, v27, v51\n"
"s_waitcnt lgkmcnt(4)\n"
"v_mfma_f32_16x16x32_bf16 v[40:43], a[56:59], v[92:95], v[40:43]\n"
"ds_read_b128 v[124:127], v4 offset:13824\n"
"v_fma_f32 v44, -v60, v20, v44\n"
"v_mfma_f32_16x16x32_bf16 v[40:43], a[68:71], v[96:99], v[40:43]\n"
"v_fma_f32 v45, -v60, v21, v45\n"
"v_fma_f32 v46, -v60, v22, v46\n"
"v_mfma_f32_16x16x32_bf16 v[40:43], a[80:83], v[100:103], v[40:43]\n"
"v_fma_f32 v47, -v60, v23, v47\n"
"v_mul_f32 v64, v44, v61\n"
"v_mfma_f32_16x16x32_bf16 v[40:43], a[92:95], v[104:107], v[40:43]\n"
"ds_read_b128 v[76:79], v4 offset:14976\n"
"v_mul_f32 v65, v45, v61\n"
"v_mfma_f32_16x16x32_bf16 v[36:39], a[88:91], v[104:107], v[36:39]\n"
"v_exp_f32 v64, v64\n"
"v_mfma_f32_16x16x32_bf16 v[36:39], a[76:79], v[100:103], v[36:39]\n"
"buffer_load_dwordx4 v2, s[32:35], s63 offen offset:1024 sc1 lds\n"
"v_mul_f32 v66, v46, v61\n"
"v_mfma_f32_16x16x32_bf16 v[36:39], a[64:67], v[96:99], v[36:39]\n"
"ds_read_b128 v[80:83], v4 offset:16128\n"
"v_mul_f32 v67, v47, v61\n"
"v_mfma_f32_16x16x32_bf16 v[36:39], a[52:55], v[92:95], v[36:39]\n"
"s_waitcnt vmcnt(3)\n"
"s_waitcnt vmcnt(2)\n"
"s_barrier\n"
"v_mfma_f32_16x16x32_bf16 v[32:35], a[48:51], v[92:95], v[32:35]\n"
"v_exp_f32 v65, v65\n"
"v_mfma_f32_16x16x32_bf16 v[32:35], a[60:63], v[96:99], v[32:35]\n"
"buffer_load_dwordx4 v2, s[32:35], s63 offen offset:2048 sc1 lds\n"
"ds_read_b128 v[84:87], v4 offset:17280\n"
"v_mfma_f32_16x16x32_bf16 v[32:35], a[72:75], v[100:103], v[32:35]\n"
"v_exp_f32 v66, v66\n"
"v_mfma_f32_16x16x32_bf16 v[32:35], a[84:87], v[104:107], v[32:35]\n"
"v_mul_f32 v68, v52, v61\n"
"s_waitcnt lgkmcnt(4)\n"
"v_mfma_f32_16x16x32_bf16 v[32:35], a[96:99], v[108:111], v[32:35]\n"
"ds_read_b128 v[88:91], v4 offset:18432\n"
"v_mul_f32 v69, v53, v61\n"
"v_mfma_f32_16x16x32_bf16 v[32:35], a[108:111], v[112:115], v[32:35]\n"
"v_exp_f32 v67, v67\n"
"v_mfma_f32_16x16x32_bf16 v[32:35], a[120:123], v[116:119], v[32:35]\n"
"v_exp_f32 v68, v68\n"
"v_mfma_f32_16x16x32_bf16 v[32:35], a[132:135], v[120:123], v[32:35]\n"
"ds_read_b128 v[92:95], v4 offset:19584\n"
"v_mul_f32 v44, v44, v48\n"
"v_mfma_f32_16x16x32_bf16 v[36:39], a[136:139], v[120:123], v[36:39]\n"
"v_exp_f32 v69, v69\n"
"v_mfma_f32_16x16x32_bf16 v[36:39], a[124:127], v[116:119], v[36:39]\n"
"buffer_load_dwordx4 v2, s[32:35], s63 offen offset:3072 sc1 lds\n"
"v_mul_f32 v45, v45, v49\n"
"v_mfma_f32_16x16x32_bf16 v[36:39], a[112:115], v[112:115], v[36:39]\n"
"ds_read_b128 v[96:99], v4 offset:20736\n"
"v_mul_f32 v46, v46, v50\n"
"v_mfma_f32_16x16x32_bf16 v[36:39], a[100:103], v[108:111], v[36:39]\n"
"v_mul_f32 v47, v47, v51\n"
"v_mul_f32 v52, v52, v54\n"
"v_mfma_f32_16x16x32_bf16 v[40:43], a[104:107], v[108:111], v[40:43]\n"
"v_mul_f32 v53, v53, v55\n"
"v_fma_f32 v64, v64, v62, v62\n"
"v_mfma_f32_16x16x32_bf16 v[40:43], a[116:119], v[112:115], v[40:43]\n"
"ds_read_b128 v[100:103], v4 offset:21888\n"
"s_add_u32 m0, s64, 4096\n"
"v_mfma_f32_16x16x32_bf16 v[40:43], a[128:131], v[116:119], v[40:43]\n"
"v_rcp_f32 v64, v64\n"
"v_mfma_f32_16x16x32_bf16 v[40:43], a[140:143], v[120:123], v[40:43]\n"
"v_fma_f32 v65, v65, v62, v62\n"
"s_waitcnt lgkmcnt(4)\n"
"v_mfma_f32_16x16x32_bf16 v[40:43], a[152:155], v[124:127], v[40:43]\n"
"ds_read_b128 v[104:107], v4 offset:23040\n"
"v_fma_f32 v66, v66, v62, v62\n"
"v_mfma_f32_16x16x32_bf16 v[40:43], a[164:167], v[76:79], v[40:43]\n"
"buffer_load_dwordx4 v6, s[32:35], s63 offen offset:0 sc1 lds\n"
"v_fma_f32 v67, v67, v62, v62\n"
"v_mfma_f32_16x16x32_bf16 v[40:43], a[176:179], v[80:83], v[40:43]\n"
"v_rcp_f32 v65, v65\n"
"v_mfma_f32_16x16x32_bf16 v[40:43], a[188:191], v[84:87], v[40:43]\n"
"ds_read_b128 v[108:111], v4 offset:24192\n"
"v_fma_f32 v68, v68, v62, v62\n"
"v_mfma_f32_16x16x32_bf16 v[36:39], a[184:187], v[84:87], v[36:39]\n"
"v_rcp_f32 v66, v66\n"
"v_mfma_f32_16x16x32_bf16 v[36:39], a[172:175], v[80:83], v[36:39]\n"
"buffer_load_dwordx4 v6, s[32:35], s63 offen offset:1024 sc1 lds\n"
"v_fma_f32 v69, v69, v62, v62\n"
"v_mfma_f32_16x16x32_bf16 v[36:39], a[160:163], v[76:79], v[36:39]\n"
"ds_read_b128 v[112:115], v4 offset:25344\n"
"v_mul_f32 v44, v44, v64\n"
"v_mfma_f32_16x16x32_bf16 v[36:39], a[148:151], v[124:127], v[36:39]\n"
"v_rcp_f32 v67, v67\n"
"v_mfma_f32_16x16x32_bf16 v[32:35], a[144:147], v[124:127], v[32:35]\n"
"v_rcp_f32 v68, v68\n"
"v_mfma_f32_16x16x32_bf16 v[32:35], a[156:159], v[76:79], v[32:35]\n"
"buffer_load_dwordx2 v[12:13], v14, s[32:35], s66 offen sc1\n"
"ds_read_b128 v[116:119], v4 offset:26496\n"
"v_mfma_f32_16x16x32_bf16 v[32:35], a[168:171], v[80:83], v[32:35]\n"
"v_rcp_f32 v69, v69\n"
"v_mfma_f32_16x16x32_bf16 v[32:35], a[180:183], v[84:87], v[32:35]\n"
"v_mul_f32 v45, v45, v65\n"
"s_waitcnt lgkmcnt(4)\n"
"v_mfma_f32_16x16x32_bf16 v[32:35], a[192:195], v[88:91], v[32:35]\n"
"ds_read_b128 v[120:123], v4 offset:27648\n"
"v_mul_f32 v46, v46, v66\n"
"v_mfma_f32_16x16x32_bf16 v[32:35], a[204:207], v[92:95], v[32:35]\n"
"buffer_load_dwordx4 v6, s[32:35], s63 offen offset:2048 sc1 lds\n"
"v_mul_f32 v47, v47, v67\n"
"v_mfma_f32_16x16x32_bf16 v[32:35], a[216:219], v[96:99], v[32:35]\n"
"v_mul_f32 v52, v52, v68\n"
"v_mul_f32 v53, v53, v69\n"
"v_mfma_f32_16x16x32_bf16 v[32:35], a[228:231], v[100:103], v[32:35]\n"
"ds_read_b128 v[124:127], v4 offset:28800\n"
"v_cvt_pk_bf16_f32 v74, v52, v53\n"
"v_mfma_f32_16x16x32_bf16 v[36:39], a[232:235], v[100:103], v[36:39]\n"
"v_cvt_pk_bf16_f32 v72, v44, v45\n"
"v_cvt_pk_bf16_f32 v73, v46, v47\n"
"v_mfma_f32_16x16x32_bf16 v[36:39], a[220:223], v[96:99], v[36:39]\n"
"buffer_load_dwordx4 v6, s[32:35], s63 offen offset:3072 sc1 lds\n"
"v_mfma_f32_16x16x32_bf16 v[36:39], a[208:211], v[92:95], v[36:39]\n"
"ds_read_b128 v[76:79], v4 offset:29952\n"
"ds_read2st64_b32 v[56:57], v8 offset0:144 offset1:180\n"
"v_mfma_f32_16x16x32_bf16 v[36:39], a[196:199], v[88:91], v[36:39]\n"
"ds_read2st64_b32 v[58:59], v8 offset0:216 offset1:252\n"
"v_xor_b32 v8, v9, v8\n"
"v_mfma_f32_16x16x32_bf16 v[40:43], a[200:203], v[88:91], v[40:43]\n"
"v_permlane32_swap_b32 v74, v75\n"
"v_mfma_f32_16x16x32_bf16 v[40:43], a[212:215], v[92:95], v[40:43]\n"
"ds_read_b128 v[80:83], v4 offset:31104\n"
"v_mfma_f32_16x16x32_bf16 v[40:43], a[224:227], v[96:99], v[40:43]\n"
"v_permlane16_swap_b32 v72, v74\n"
"v_mfma_f32_16x16x32_bf16 v[40:43], a[236:239], v[100:103], v[40:43]\n"
"s_add_u32 m0, s64, 8192\n"
"s_waitcnt lgkmcnt(6)\n"
"v_mfma_f32_16x16x32_bf16 v[40:43], a[248:251], v[104:107], v[40:43]\n"
"ds_read_b128 v[84:87], v4 offset:32256\n"
"v_mfma_f32_16x16x32_bf16 v[40:43], v[132:135], v[108:111], v[40:43]\n"
"buffer_load_dwordx4 v7, s[32:35], s63 offen offset:0 sc1 lds\n"
"s_add_u32 s63, s63, 890880\n"
"v_mfma_f32_16x16x32_bf16 v[40:43], v[144:147], v[112:115], v[40:43]\n"
"v_permlane16_swap_b32 v73, v75\n"
"v_mfma_f32_16x16x32_bf16 v[40:43], v[156:159], v[116:119], v[40:43]\n"
"ds_read_b128 v[88:91], v4 offset:33408\n"
"v_mfma_f32_16x16x32_bf16 v[36:39], v[152:155], v[116:119], v[36:39]\n"
"v_mfma_f32_16x16x32_bf16 v[36:39], v[140:143], v[112:115], v[36:39]\n"
"buffer_store_dwordx4 v[72:75], v15, s[32:35], s68 offen sc1\n"
"ds_read_b32 v62, v11 offset:0\n"
"v_mfma_f32_16x16x32_bf16 v[36:39], v[128:131], v[108:111], v[36:39]\n"
"ds_read_b128 v[92:95], v4 offset:34560\n"
"s_waitcnt lgkmcnt(5)\n"
"v_add_f32 v56, v56, v57\n"
"v_mfma_f32_16x16x32_bf16 v[36:39], a[244:247], v[104:107], v[36:39]\n"
"v_add_f32 v58, v58, v59\n"
"v_mfma_f32_16x16x32_bf16 v[32:35], a[240:243], v[104:107], v[32:35]\n"
"v_add_f32 v56, v56, v58\n"
"v_mfma_f32_16x16x32_bf16 v[32:35], a[252:255], v[108:111], v[32:35]\n"
"ds_read_b128 v[96:99], v4 offset:35712\n"
"v_add_f32_dpp v56, v56, v56 row_ror:8 row_mask:0xf bank_mask:0xf\n"
"v_mfma_f32_16x16x32_bf16 v[32:35], v[136:139], v[112:115], v[32:35]\n"
"v_xor_b32 v4, 73728, v4\n"
"v_add_f32_dpp v56, v56, v56 row_ror:4 row_mask:0xf bank_mask:0xf\n"
"v_mfma_f32_16x16x32_bf16 v[32:35], v[148:151], v[116:119], v[32:35]\n"
"s_waitcnt lgkmcnt(2)\n"
"v_mfma_f32_16x16x32_bf16 v[32:35], v[160:163], v[120:123], v[32:35]\n"
"ds_read_b128 v[100:103], v5 offset:0\n"
"v_lshlrev_b32 v60, 16, v62\n"
"v_mfma_f32_16x16x32_bf16 v[32:35], v[172:175], v[124:127], v[32:35]\n"
"v_and_b32 v62, 0xffff0000, v62\n"
"v_add_f32_dpp v56, v56, v56 row_ror:2 row_mask:0xf bank_mask:0xf\n"
"v_mfma_f32_16x16x32_bf16 v[32:35], v[184:187], v[76:79], v[32:35]\n"
"v_rcp_f32 v61, v62\n"
"v_mfma_f32_16x16x32_bf16 v[32:35], v[196:199], v[80:83], v[32:35]\n"
"ds_read_b128 v[104:107], v5 offset:1152\n"
"v_mul_f32 v62, v62, v62\n"
"v_mfma_f32_16x16x32_bf16 v[36:39], v[200:203], v[80:83], v[36:39]\n"
"v_mul_f32 v61, 0xbfb8aa3b, v61\n"
"v_mul_f32 v56, 0x3a800000, v56\n"
"v_mfma_f32_16x16x32_bf16 v[36:39], v[188:191], v[76:79], v[36:39]\n"
"v_mul_f32 v57, v56, v56\n"
"v_mfma_f32_16x16x32_bf16 v[36:39], v[176:179], v[124:127], v[36:39]\n"
"ds_read_b128 v[108:111], v5 offset:2304\n"
"v_sub_f32_dpp v57, v56, v57 quad_perm:[1,1,3,3] row_mask:0xf bank_mask:0xf\n"
"v_mfma_f32_16x16x32_bf16 v[36:39], v[164:167], v[120:123], v[36:39]\n"
"v_max_f32 v57, 0, v57\n"
"v_mfma_f32_16x16x32_bf16 v[40:43], v[168:171], v[120:123], v[40:43]\n"
"v_cvt_pk_bf16_f32 v57, 0, v57\n"
"v_mfma_f32_16x16x32_bf16 v[40:43], v[180:183], v[124:127], v[40:43]\n"
"ds_read_b128 v[112:115], v5 offset:3456\n"
"v_add_f32 v57, 0x3727c5ac, v57\n"
"v_mfma_f32_16x16x32_bf16 v[40:43], v[192:195], v[76:79], v[40:43]\n"
"v_cvt_pk_bf16_f32 v57, 0, v57\n"
"v_mfma_f32_16x16x32_bf16 v[40:43], v[204:207], v[80:83], v[40:43]\n"
"s_waitcnt lgkmcnt(4)\n"
"v_mfma_f32_16x16x32_bf16 v[40:43], v[216:219], v[84:87], v[40:43]\n"
"ds_read_b128 v[116:119], v5 offset:4608\n"
"v_mfma_f32_16x16x32_bf16 v[40:43], v[228:231], v[88:91], v[40:43]\n"
"v_sqrt_f32 v57, v57\n"
"v_mfma_f32_16x16x32_bf16 v[40:43], v[240:243], v[92:95], v[40:43]\n"
"v_cvt_pk_bf16_f32 v57, v56, v57\n"
"v_mfma_f32_16x16x32_bf16 v[40:43], v[252:255], v[96:99], v[40:43]\n"
"ds_read_b128 v[120:123], v5 offset:5760\n"
"ds_write_b32 v10, v57 offset:128\n"
"v_mfma_f32_16x16x32_bf16 v[36:39], v[248:251], v[96:99], v[36:39]\n"
"v_mfma_f32_16x16x32_bf16 v[36:39], v[236:239], v[92:95], v[36:39]\n"
"v_mfma_f32_16x16x32_bf16 v[36:39], v[224:227], v[88:91], v[36:39]\n"
"ds_read_b128 v[124:127], v5 offset:6912\n"
"v_mfma_f32_16x16x32_bf16 v[36:39], v[212:215], v[84:87], v[36:39]\n"
"v_mfma_f32_16x16x32_bf16 v[32:35], v[208:211], v[84:87], v[32:35]\n"
"v_mfma_f32_16x16x32_bf16 v[32:35], v[220:223], v[88:91], v[32:35]\n"
"ds_read_b128 v[76:79], v5 offset:8064\n"
"v_mfma_f32_16x16x32_bf16 v[32:35], v[232:235], v[92:95], v[32:35]\n"
"s_sub_u32 s62, s62, 1\n"
"v_mfma_f32_16x16x32_bf16 v[32:35], v[244:247], v[96:99], v[32:35]\n"
"s_cbranch_scc1 .L_gt0_0\n"
"s_waitcnt lgkmcnt(5)\n"
"v_mfma_f32_16x16x32_bf16 v[44:47], a[0:3], v[100:103], 0\n"
"ds_read_b128 v[80:83], v5 offset:9216\n"
"s_add_u32 s68, s68, 890880\n"
"v_mfma_f32_16x16x32_bf16 v[44:47], a[12:15], v[104:107], v[44:47]\n"
"v_fma_f32 v40, -v60, v28, v40\n"
"v_fma_f32 v41, -v60, v29, v41\n"
"v_mfma_f32_16x16x32_bf16 v[44:47], a[24:27], v[108:111], v[44:47]\n"
"v_fma_f32 v42, -v60, v30, v42\n"
"v_fma_f32 v43, -v60, v31, v43\n"
"v_mfma_f32_16x16x32_bf16 v[44:47], a[36:39], v[112:115], v[44:47]\n"
"ds_read_b128 v[84:87], v5 offset:10368\n"
"v_fma_f32 v36, -v60, v24, v36\n"
"v_mfma_f32_16x16x32_bf16 v[48:51], a[40:43], v[112:115], 0\n"
"v_permlane32_swap_b32 v40, v42\n"
"v_mfma_f32_16x16x32_bf16 v[48:51], a[28:31], v[108:111], v[48:51]\n"
"s_waitcnt vmcnt(4)\n"
"v_cmp_ne_u64 vcc, v[16:17], v[12:13]\n"
"v_mfma_f32_16x16x32_bf16 v[48:51], a[16:19], v[104:107], v[48:51]\n"
"ds_read_b128 v[88:91], v5 offset:11520\n"
"v_fma_f32 v37, -v60, v25, v37\n"
"v_mfma_f32_16x16x32_bf16 v[48:51], a[4:7], v[100:103], v[48:51]\n"
"s_cbranch_vccnz .L_rtg1_0\n"
".L_bkg1_0:\n"
"s_add_u32 s66, s66, 890880\n"
"v_mfma_f32_16x16x32_bf16 v[52:55], a[8:11], v[100:103], 0\n"
"s_add_u32 m0, s64, 36864\n"
"v_fma_f32 v38, -v60, v26, v38\n"
"v_mfma_f32_16x16x32_bf16 v[52:55], a[20:23], v[104:107], v[52:55]\n"
"buffer_load_dwordx4 v2, s[32:35], s63 offen offset:0 sc1 lds\n"
"ds_read_b128 v[92:95], v5 offset:12672\n"
"v_mfma_f32_16x16x32_bf16 v[52:55], a[32:35], v[108:111], v[52:55]\n"
"v_permlane32_swap_b32 v41, v43\n"
"v_mfma_f32_16x16x32_bf16 v[52:55], a[44:47], v[112:115], v[52:55]\n"
"v_fma_f32 v39, -v60, v27, v39\n"
"s_waitcnt lgkmcnt(4)\n"
"v_mfma_f32_16x16x32_bf16 v[52:55], a[56:59], v[116:119], v[52:55]\n"
"ds_read_b128 v[96:99], v5 offset:13824\n"
"v_fma_f32 v32, -v60, v20, v32\n"
"v_mfma_f32_16x16x32_bf16 v[52:55], a[68:71], v[120:123], v[52:55]\n"
"v_fma_f32 v33, -v60, v21, v33\n"
"v_fma_f32 v34, -v60, v22, v34\n"
"v_mfma_f32_16x16x32_bf16 v[52:55], a[80:83], v[124:127], v[52:55]\n"
"v_fma_f32 v35, -v60, v23, v35\n"
"v_mul_f32 v64, v32, v61\n"
"v_mfma_f32_16x16x32_bf16 v[52:55], a[92:95], v[76:79], v[52:55]\n"
"ds_read_b128 v[100:103], v5 offset:14976\n"
"v_mul_f32 v65, v33, v61\n"
"v_mfma_f32_16x16x32_bf16 v[48:51], a[88:91], v[76:79], v[48:51]\n"
"v_exp_f32 v64, v64\n"
"v_mfma_f32_16x16x32_bf16 v[48:51], a[76:79], v[124:127], v[48:51]\n"
"buffer_load_dwordx4 v2, s[32:35], s63 offen offset:1024 sc1 lds\n"
"v_mul_f32 v66, v34, v61\n"
"v_mfma_f32_16x16x32_bf16 v[48:51], a[64:67], v[120:123], v[48:51]\n"
"ds_read_b128 v[104:107], v5 offset:16128\n"
"v_mul_f32 v67, v35, v61\n"
"v_mfma_f32_16x16x32_bf16 v[48:51], a[52:55], v[116:119], v[48:51]\n"
"s_waitcnt vmcnt(3)\n"
"s_waitcnt vmcnt(2)\n"
"s_barrier\n"
"v_mfma_f32_16x16x32_bf16 v[44:47], a[48:51], v[116:119], v[44:47]\n"
"v_exp_f32 v65, v65\n"
"v_mfma_f32_16x16x32_bf16 v[44:47], a[60:63], v[120:123], v[44:47]\n"
"buffer_load_dwordx4 v2, s[32:35], s63 offen offset:2048 sc1 lds\n"
"ds_read_b128 v[108:111], v5 offset:17280\n"
"v_mfma_f32_16x16x32_bf16 v[44:47], a[72:75], v[124:127], v[44:47]\n"
"v_exp_f32 v66, v66\n"
"v_mfma_f32_16x16x32_bf16 v[44:47], a[84:87], v[76:79], v[44:47]\n"
"v_mul_f32 v68, v40, v61\n"
"s_waitcnt lgkmcnt(4)\n"
"v_mfma_f32_16x16x32_bf16 v[44:47], a[96:99], v[80:83], v[44:47]\n"
"ds_read_b128 v[112:115], v5 offset:18432\n"
"v_mul_f32 v69, v41, v61\n"
"v_mfma_f32_16x16x32_bf16 v[44:47], a[108:111], v[84:87], v[44:47]\n"
"v_exp_f32 v67, v67\n"
"v_mfma_f32_16x16x32_bf16 v[44:47], a[120:123], v[88:91], v[44:47]\n"
"v_exp_f32 v68, v68\n"
"v_mfma_f32_16x16x32_bf16 v[44:47], a[132:135], v[92:95], v[44:47]\n"
"ds_read_b128 v[116:119], v5 offset:19584\n"
"v_mul_f32 v32, v32, v36\n"
"v_mfma_f32_16x16x32_bf16 v[48:51], a[136:139], v[92:95], v[48:51]\n"
"v_exp_f32 v69, v69\n"
"v_mfma_f32_16x16x32_bf16 v[48:51], a[124:127], v[88:91], v[48:51]\n"
"buffer_load_dwordx4 v2, s[32:35], s63 offen offset:3072 sc1 lds\n"
"v_mul_f32 v33, v33, v37\n"
"v_mfma_f32_16x16x32_bf16 v[48:51], a[112:115], v[84:87], v[48:51]\n"
"ds_read_b128 v[120:123], v5 offset:20736\n"
"v_mul_f32 v34, v34, v38\n"
"v_mfma_f32_16x16x32_bf16 v[48:51], a[100:103], v[80:83], v[48:51]\n"
"v_mul_f32 v35, v35, v39\n"
"v_mul_f32 v40, v40, v42\n"
"v_mfma_f32_16x16x32_bf16 v[52:55], a[104:107], v[80:83], v[52:55]\n"
"v_mul_f32 v41, v41, v43\n"
"v_fma_f32 v64, v64, v62, v62\n"
"v_mfma_f32_16x16x32_bf16 v[52:55], a[116:119], v[84:87], v[52:55]\n"
"ds_read_b128 v[124:127], v5 offset:21888\n"
"s_add_u32 m0, s64, 40960\n"
"v_mfma_f32_16x16x32_bf16 v[52:55], a[128:131], v[88:91], v[52:55]\n"
"v_rcp_f32 v64, v64\n"
"v_mfma_f32_16x16x32_bf16 v[52:55], a[140:143], v[92:95], v[52:55]\n"
"v_fma_f32 v65, v65, v62, v62\n"
"s_waitcnt lgkmcnt(4)\n"
"v_mfma_f32_16x16x32_bf16 v[52:55], a[152:155], v[96:99], v[52:55]\n"
"ds_read_b128 v[76:79], v5 offset:23040\n"
"v_fma_f32 v66, v66, v62, v62\n"
"v_mfma_f32_16x16x32_bf16 v[52:55], a[164:167], v[100:103], v[52:55]\n"
"buffer_load_dwordx4 v6, s[32:35], s63 offen offset:0 sc1 lds\n"
"v_fma_f32 v67, v67, v62, v62\n"
"v_mfma_f32_16x16x32_bf16 v[52:55], a[176:179], v[104:107], v[52:55]\n"
"v_rcp_f32 v65, v65\n"
"v_mfma_f32_16x16x32_bf16 v[52:55], a[188:191], v[108:111], v[52:55]\n"
"ds_read_b128 v[80:83], v5 offset:24192\n"
"v_fma_f32 v68, v68, v62, v62\n"
"v_mfma_f32_16x16x32_bf16 v[48:51], a[184:187], v[108:111], v[48:51]\n"
"v_rcp_f32 v66, v66\n"
"v_mfma_f32_16x16x32_bf16 v[48:51], a[172:175], v[104:107], v[48:51]\n"
"buffer_load_dwordx4 v6, s[32:35], s63 offen offset:1024 sc1 lds\n"
"v_fma_f32 v69, v69, v62, v62\n"
"v_mfma_f32_16x16x32_bf16 v[48:51], a[160:163], v[100:103], v[48:51]\n"
"ds_read_b128 v[84:87], v5 offset:25344\n"
"v_mul_f32 v32, v32, v64\n"
"v_mfma_f32_16x16x32_bf16 v[48:51], a[148:151], v[96:99], v[48:51]\n"
"v_rcp_f32 v67, v67\n"
"v_mfma_f32_16x16x32_bf16 v[44:47], a[144:147], v[96:99], v[44:47]\n"
"v_rcp_f32 v68, v68\n"
"v_mfma_f32_16x16x32_bf16 v[44:47], a[156:159], v[100:103], v[44:47]\n"
"buffer_load_dwordx2 v[12:13], v14, s[32:35], s66 offen sc1\n"
"ds_read_b128 v[88:91], v5 offset:26496\n"
"v_mfma_f32_16x16x32_bf16 v[44:47], a[168:171], v[104:107], v[44:47]\n"
"v_rcp_f32 v69, v69\n"
"v_mfma_f32_16x16x32_bf16 v[44:47], a[180:183], v[108:111], v[44:47]\n"
"v_mul_f32 v33, v33, v65\n"
"s_waitcnt lgkmcnt(4)\n"
"v_mfma_f32_16x16x32_bf16 v[44:47], a[192:195], v[112:115], v[44:47]\n"
"ds_read_b128 v[92:95], v5 offset:27648\n"
"v_mul_f32 v34, v34, v66\n"
"v_mfma_f32_16x16x32_bf16 v[44:47], a[204:207], v[116:119], v[44:47]\n"
"buffer_load_dwordx4 v6, s[32:35], s63 offen offset:2048 sc1 lds\n"
"v_mul_f32 v35, v35, v67\n"
"v_mfma_f32_16x16x32_bf16 v[44:47], a[216:219], v[120:123], v[44:47]\n"
"v_mul_f32 v40, v40, v68\n"
"v_mul_f32 v41, v41, v69\n"
"v_mfma_f32_16x16x32_bf16 v[44:47], a[228:231], v[124:127], v[44:47]\n"
"ds_read_b128 v[96:99], v5 offset:28800\n"
"v_cvt_pk_bf16_f32 v74, v40, v41\n"
"v_mfma_f32_16x16x32_bf16 v[48:51], a[232:235], v[124:127], v[48:51]\n"
"v_cvt_pk_bf16_f32 v72, v32, v33\n"
"v_cvt_pk_bf16_f32 v73, v34, v35\n"
"v_mfma_f32_16x16x32_bf16 v[48:51], a[220:223], v[120:123], v[48:51]\n"
"buffer_load_dwordx4 v6, s[32:35], s63 offen offset:3072 sc1 lds\n"
"v_mfma_f32_16x16x32_bf16 v[48:51], a[208:211], v[116:119], v[48:51]\n"
"ds_read_b128 v[100:103], v5 offset:29952\n"
"ds_read2st64_b32 v[56:57], v8 offset0:0 offset1:36\n"
"v_mfma_f32_16x16x32_bf16 v[48:51], a[196:199], v[112:115], v[48:51]\n"
"ds_read2st64_b32 v[58:59], v8 offset0:72 offset1:108\n"
"v_mfma_f32_16x16x32_bf16 v[52:55], a[200:203], v[112:115], v[52:55]\n"
"v_permlane32_swap_b32 v74, v75\n"
"v_mfma_f32_16x16x32_bf16 v[52:55], a[212:215], v[116:119], v[52:55]\n"
"ds_read_b128 v[104:107], v5 offset:31104\n"
"v_mfma_f32_16x16x32_bf16 v[52:55], a[224:227], v[120:123], v[52:55]\n"
"v_permlane16_swap_b32 v72, v74\n"
"v_mfma_f32_16x16x32_bf16 v[52:55], a[236:239], v[124:127], v[52:55]\n"
"s_add_u32 m0, s64, 45056\n"
"s_waitcnt lgkmcnt(6)\n"
"v_mfma_f32_16x16x32_bf16 v[52:55], a[248:251], v[76:79], v[52:55]\n"
"ds_read_b128 v[108:111], v5 offset:32256\n"
"v_mfma_f32_16x16x32_bf16 v[52:55], v[132:135], v[80:83], v[52:55]\n"
"buffer_load_dwordx4 v7, s[32:35], s63 offen offset:0 sc1 lds\n"
"s_add_u32 s63, s63, 890880\n"
"v_mfma_f32_16x16x32_bf16 v[52:55], v[144:147], v[84:87], v[52:55]\n"
"s_xor_b32 s64, s64, s65\n"
"v_mfma_f32_16x16x32_bf16 v[52:55], v[156:159], v[88:91], v[52:55]\n"
"ds_read_b128 v[112:115], v5 offset:33408\n"
"v_mfma_f32_16x16x32_bf16 v[48:51], v[152:155], v[88:91], v[48:51]\n"
"v_permlane16_swap_b32 v73, v75\n"
"v_mfma_f32_16x16x32_bf16 v[48:51], v[140:143], v[84:87], v[48:51]\n"
"buffer_store_dwordx4 v[72:75], v15, s[32:35], s68 offen sc1\n"
"ds_read_b32 v62, v11 offset:128\n"
"v_mfma_f32_16x16x32_bf16 v[48:51], v[128:131], v[80:83], v[48:51]\n"
"ds_read_b128 v[116:119], v5 offset:34560\n"
"s_waitcnt lgkmcnt(5)\n"
"v_add_f32 v56, v56, v57\n"
"v_mfma_f32_16x16x32_bf16 v[48:51], a[244:247], v[76:79], v[48:51]\n"
"v_add_f32 v58, v58, v59\n"
"v_mfma_f32_16x16x32_bf16 v[44:47], a[240:243], v[76:79], v[44:47]\n"
"v_add_f32 v56, v56, v58\n"
"v_mfma_f32_16x16x32_bf16 v[44:47], a[252:255], v[80:83], v[44:47]\n"
"ds_read_b128 v[120:123], v5 offset:35712\n"
"v_add_f32_dpp v56, v56, v56 row_ror:8 row_mask:0xf bank_mask:0xf\n"
"v_mfma_f32_16x16x32_bf16 v[44:47], v[136:139], v[84:87], v[44:47]\n"
"v_xor_b32 v5, 73728, v5\n"
"v_add_f32_dpp v56, v56, v56 row_ror:4 row_mask:0xf bank_mask:0xf\n"
"v_mfma_f32_16x16x32_bf16 v[44:47], v[148:151], v[88:91], v[44:47]\n"
"s_waitcnt lgkmcnt(2)\n"
"v_mfma_f32_16x16x32_bf16 v[44:47], v[160:163], v[92:95], v[44:47]\n"
"ds_read_b128 v[76:79], v4 offset:0\n"
"v_lshlrev_b32 v60, 16, v62\n"
"v_mfma_f32_16x16x32_bf16 v[44:47], v[172:175], v[96:99], v[44:47]\n"
"v_and_b32 v62, 0xffff0000, v62\n"
"v_add_f32_dpp v56, v56, v56 row_ror:2 row_mask:0xf bank_mask:0xf\n"
"v_mfma_f32_16x16x32_bf16 v[44:47], v[184:187], v[100:103], v[44:47]\n"
"v_rcp_f32 v61, v62\n"
"v_mfma_f32_16x16x32_bf16 v[44:47], v[196:199], v[104:107], v[44:47]\n"
"ds_read_b128 v[80:83], v4 offset:1152\n"
"v_mul_f32 v62, v62, v62\n"
"v_mfma_f32_16x16x32_bf16 v[48:51], v[200:203], v[104:107], v[48:51]\n"
"v_mul_f32 v61, 0xbfb8aa3b, v61\n"
"v_mul_f32 v56, 0x3a800000, v56\n"
"v_mfma_f32_16x16x32_bf16 v[48:51], v[188:191], v[100:103], v[48:51]\n"
"v_mul_f32 v57, v56, v56\n"
"v_mfma_f32_16x16x32_bf16 v[48:51], v[176:179], v[96:99], v[48:51]\n"
"ds_read_b128 v[84:87], v4 offset:2304\n"
"v_sub_f32_dpp v57, v56, v57 quad_perm:[1,1,3,3] row_mask:0xf bank_mask:0xf\n"
"v_mfma_f32_16x16x32_bf16 v[48:51], v[164:167], v[92:95], v[48:51]\n"
"v_max_f32 v57, 0, v57\n"
"v_mfma_f32_16x16x32_bf16 v[52:55], v[168:171], v[92:95], v[52:55]\n"
"v_cvt_pk_bf16_f32 v57, 0, v57\n"
"v_mfma_f32_16x16x32_bf16 v[52:55], v[180:183], v[96:99], v[52:55]\n"
"ds_read_b128 v[88:91], v4 offset:3456\n"
"v_add_f32 v57, 0x3727c5ac, v57\n"
"v_mfma_f32_16x16x32_bf16 v[52:55], v[192:195], v[100:103], v[52:55]\n"
"v_cvt_pk_bf16_f32 v57, 0, v57\n"
"v_mfma_f32_16x16x32_bf16 v[52:55], v[204:207], v[104:107], v[52:55]\n"
"s_waitcnt lgkmcnt(4)\n"
"v_mfma_f32_16x16x32_bf16 v[52:55], v[216:219], v[108:111], v[52:55]\n"
"ds_read_b128 v[92:95], v4 offset:4608\n"
"v_mfma_f32_16x16x32_bf16 v[52:55], v[228:231], v[112:115], v[52:55]\n"
"v_sqrt_f32 v57, v57\n"
"v_mfma_f32_16x16x32_bf16 v[52:55], v[240:243], v[116:119], v[52:55]\n"
"v_cvt_pk_bf16_f32 v57, v56, v57\n"
"v_mfma_f32_16x16x32_bf16 v[52:55], v[252:255], v[120:123], v[52:55]\n"
"ds_read_b128 v[96:99], v4 offset:5760\n"
"ds_write_b32 v10, v57 offset:0\n"
"v_mfma_f32_16x16x32_bf16 v[48:51], v[248:251], v[120:123], v[48:51]\n"
"v_mfma_f32_16x16x32_bf16 v[48:51], v[236:239], v[116:119], v[48:51]\n"
"v_mfma_f32_16x16x32_bf16 v[48:51], v[224:227], v[112:115], v[48:51]\n"
"ds_read_b128 v[100:103], v4 offset:6912\n"
"v_mfma_f32_16x16x32_bf16 v[48:51], v[212:215], v[108:111], v[48:51]\n"
"v_mfma_f32_16x16x32_bf16 v[44:47], v[208:211], v[108:111], v[44:47]\n"
"v_mfma_f32_16x16x32_bf16 v[44:47], v[220:223], v[112:115], v[44:47]\n"
"ds_read_b128 v[104:107], v4 offset:8064\n"
"v_mfma_f32_16x16x32_bf16 v[44:47], v[232:235], v[116:119], v[44:47]\n"
"s_sub_u32 s62, s62, 1\n"
"v_mfma_f32_16x16x32_bf16 v[44:47], v[244:247], v[120:123], v[44:47]\n"
"s_cbranch_scc0 .L_gl_0\n"
"s_nop 1\n"
"s_add_u32 s68, s68, 890880\n"
"s_waitcnt vmcnt(0)\n"
"s_barrier\n"
"buffer_store_dwordx2 v[16:17], v2, s[92:95], s68 idxen sc1\n"
"v_fma_f32 v52, -v60, v28, v52\n"
"v_fma_f32 v53, -v60, v29, v53\n"
"v_fma_f32 v54, -v60, v30, v54\n"
"v_fma_f32 v55, -v60, v31, v55\n"
"v_fma_f32 v48, -v60, v24, v48\n"
"v_fma_f32 v49, -v60, v25, v49\n"
"v_fma_f32 v50, -v60, v26, v50\n"
"v_fma_f32 v51, -v60, v27, v51\n"
"v_fma_f32 v44, -v60, v20, v44\n"
"v_fma_f32 v45, -v60, v21, v45\n"
"v_fma_f32 v46, -v60, v22, v46\n"
"v_fma_f32 v47, -v60, v23, v47\n"
"v_permlane32_swap_b32 v52, v54\n"
"v_permlane32_swap_b32 v53, v55\n"
"v_mul_f32 v64, v44, v61\n"
"v_mul_f32 v65, v45, v61\n"
"v_mul_f32 v66, v46, v61\n"
"v_mul_f32 v67, v47, v61\n"
"v_mul_f32 v68, v52, v61\n"
"v_mul_f32 v69, v53, v61\n"
"v_exp_f32 v64, v64\n"
"v_exp_f32 v65, v65\n"
"v_mul_f32 v44, v44, v48\n"
"v_mul_f32 v45, v45, v49\n"
"v_exp_f32 v66, v66\n"
"v_exp_f32 v67, v67\n"
"v_mul_f32 v46, v46, v50\n"
"v_mul_f32 v47, v47, v51\n"
"v_exp_f32 v68, v68\n"
"v_exp_f32 v69, v69\n"
"v_mul_f32 v52, v52, v54\n"
"v_mul_f32 v53, v53, v55\n"
"v_fma_f32 v64, v64, v62, v62\n"
"v_fma_f32 v65, v65, v62, v62\n"
"v_rcp_f32 v64, v64\n"
"v_rcp_f32 v65, v65\n"
"v_fma_f32 v66, v66, v62, v62\n"
"v_fma_f32 v67, v67, v62, v62\n"
"v_rcp_f32 v66, v66\n"
"v_rcp_f32 v67, v67\n"
"v_fma_f32 v68, v68, v62, v62\n"
"v_fma_f32 v69, v69, v62, v62\n"
"v_rcp_f32 v68, v68\n"
"v_rcp_f32 v69, v69\n"
"v_mul_f32 v44, v44, v64\n"
"v_mul_f32 v45, v45, v65\n"
"v_mul_f32 v46, v46, v66\n"
"v_mul_f32 v47, v47, v67\n"
"v_mul_f32 v52, v52, v68\n"
"v_mul_f32 v53, v53, v69\n"
"v_cvt_pk_bf16_f32 v74, v52, v53\n"
"v_cvt_pk_bf16_f32 v72, v44, v45\n"
"v_cvt_pk_bf16_f32 v73, v46, v47\n"
"v_permlane32_swap_b32 v74, v75\n"
"s_nop 1\n"
"v_permlane16_swap_b32 v72, v74\n"
"v_permlane16_swap_b32 v73, v75\n"
"buffer_store_dwordx4 v[72:75], v15, s[32:35], s68 offen sc1\n"
"s_add_u32 s68, s68, 890880\n"
"s_waitcnt vmcnt(0)\n"
"s_barrier\n"
"buffer_store_dwordx2 v[16:17], v2, s[92:95], s68 idxen sc1\n"
"s_branch .L_end\n"
".L_gt0_0:\n"
"s_nop 1\n"
"s_add_u32 s68, s68, 890880\n"
"s_waitcnt vmcnt(0)\n"
"s_barrier\n"
"buffer_store_dwordx2 v[16:17], v2, s[92:95], s68 idxen sc1\n"
"v_fma_f32 v40, -v60, v28, v40\n"
"v_fma_f32 v41, -v60, v29, v41\n"
"v_fma_f32 v42, -v60, v30, v42\n"
"v_fma_f32 v43, -v60, v31, v43\n"
"v_fma_f32 v36, -v60, v24, v36\n"
"v_fma_f32 v37, -v60, v25, v37\n"
"v_fma_f32 v38, -v60, v26, v38\n"
"v_fma_f32 v39, -v60, v27, v39\n"
"v_fma_f32 v32, -v60, v20, v32\n"
"v_fma_f32 v33, -v60, v21, v33\n"
"v_fma_f32 v34, -v60, v22, v34\n"
"v_fma_f32 v35, -v60, v23, v35\n"
"v_permlane32_swap_b32 v40, v42\n"
"v_permlane32_swap_b32 v41, v43\n"
"v_mul_f32 v64, v32, v61\n"
"v_mul_f32 v65, v33, v61\n"
"v_mul_f32 v66, v34, v61\n"
"v_mul_f32 v67, v35, v61\n"
"v_mul_f32 v68, v40, v61\n"
"v_mul_f32 v69, v41, v61\n"
"v_exp_f32 v64, v64\n"
"v_exp_f32 v65, v65\n"
"v_mul_f32 v32, v32, v36\n"
"v_mul_f32 v33, v33, v37\n"
"v_exp_f32 v66, v66\n"
"v_exp_f32 v67, v67\n"
"v_mul_f32 v34, v34, v38\n"
"v_mul_f32 v35, v35, v39\n"
"v_exp_f32 v68, v68\n"
"v_exp_f32 v69, v69\n"
"v_mul_f32 v40, v40, v42\n"
"v_mul_f32 v41, v41, v43\n"
"v_fma_f32 v64, v64, v62, v62\n"
"v_fma_f32 v65, v65, v62, v62\n"
"v_rcp_f32 v64, v64\n"
"v_rcp_f32 v65, v65\n"
"v_fma_f32 v66, v66, v62, v62\n"
"v_fma_f32 v67, v67, v62, v62\n"
"v_rcp_f32 v66, v66\n"
"v_rcp_f32 v67, v67\n"
"v_fma_f32 v68, v68, v62, v62\n"
"v_fma_f32 v69, v69, v62, v62\n"
"v_rcp_f32 v68, v68\n"
"v_rcp_f32 v69, v69\n"
"v_mul_f32 v32, v32, v64\n"
"v_mul_f32 v33, v33, v65\n"
"v_mul_f32 v34, v34, v66\n"
"v_mul_f32 v35, v35, v67\n"
"v_mul_f32 v40, v40, v68\n"
"v_mul_f32 v41, v41, v69\n"
"v_cvt_pk_bf16_f32 v74, v40, v41\n"
"v_cvt_pk_bf16_f32 v72, v32, v33\n"
"v_cvt_pk_bf16_f32 v73, v34, v35\n"
"v_permlane32_swap_b32 v74, v75\n"
"s_nop 1\n"
"v_permlane16_swap_b32 v72, v74\n"
"v_permlane16_swap_b32 v73, v75\n"
"buffer_store_dwordx4 v[72:75], v15, s[32:35], s68 offen sc1\n"
"s_add_u32 s68, s68, 890880\n"
"s_waitcnt vmcnt(0)\n"
"s_barrier\n"
"buffer_store_dwordx2 v[16:17], v2, s[92:95], s68 idxen sc1\n"
"s_branch .L_end\n"
".p2align 6\n"
".L_gv1:\n"
".L_gl_1:\n"
"s_waitcnt lgkmcnt(5)\n"
"v_mfma_f32_16x16x32_bf16 v[32:35], a[0:3], v[76:79], 0\n"
"ds_read_b128 v[108:111], v4 offset:9216\n"
"s_add_u32 s68, s68, 890880\n"
"v_mfma_f32_16x16x32_bf16 v[32:35], a[12:15], v[80:83], v[32:35]\n"
"v_fma_f32 v52, -v60, v28, v52\n"
"v_fma_f32 v53, -v60, v29, v53\n"
"v_mfma_f32_16x16x32_bf16 v[32:35], a[24:27], v[84:87], v[32:35]\n"
"v_fma_f32 v54, -v60, v30, v54\n"
"v_fma_f32 v55, -v60, v31, v55\n"
"v_mfma_f32_16x16x32_bf16 v[32:35], a[36:39], v[88:91], v[32:35]\n"
"ds_read_b128 v[112:115], v4 offset:10368\n"
"v_fma_f32 v48, -v60, v24, v48\n"
"v_mfma_f32_16x16x32_bf16 v[36:39], a[40:43], v[88:91], 0\n"
"v_permlane32_swap_b32 v52, v54\n"
"v_mfma_f32_16x16x32_bf16 v[36:39], a[28:31], v[84:87], v[36:39]\n"
"s_waitcnt vmcnt(4)\n"
"v_cmp_ne_u64 vcc, v[16:17], v[12:13]\n"
"v_mfma_f32_16x16x32_bf16 v[36:39], a[16:19], v[80:83], v[36:39]\n"
"ds_read_b128 v[116:119], v4 offset:11520\n"
"v_fma_f32 v49, -v60, v25, v49\n"
"v_mfma_f32_16x16x32_bf16 v[36:39], a[4:7], v[76:79], v[36:39]\n"
"s_cbranch_vccnz .L_rtg0_1\n"
".L_bkg0_1:\n"
"s_add_u32 s66, s66, 890880\n"
"v_mfma_f32_16x16x32_bf16 v[40:43], a[8:11], v[76:79], 0\n"
"s_add_u32 m0, s64, 0\n"
"v_fma_f32 v50, -v60, v26, v50\n"
"v_mfma_f32_16x16x32_bf16 v[40:43], a[20:23], v[80:83], v[40:43]\n"
"buffer_load_dwordx4 v2, s[32:35], s63 offen offset:0 sc1 lds\n"
"ds_read_b128 v[120:123], v4 offset:12672\n"
"v_mfma_f32_16x16x32_bf16 v[40:43], a[32:35], v[84:87], v[40:43]\n"
"v_permlane32_swap_b32 v53, v55\n"
"v_mfma_f32_16x16x32_bf16 v[40:43], a[44:47], v[88:91], v[40:43]\n"
"v_fma_f32 v51, -v60, v27, v51\n"
"s_waitcnt lgkmcnt(4)\n"
"v_mfma_f32_16x16x32_bf16 v[40:43], a[56:59], v[92:95], v[40:43]\n"
"ds_read_b128 v[124:127], v4 offset:13824\n"
"v_fma_f32 v44, -v60, v20, v44\n"
"v_mfma_f32_16x16x32_bf16 v[40:43], a[68:71], v[96:99], v[40:43]\n"
"v_fma_f32 v45, -v60, v21, v45\n"
"v_fma_f32 v46, -v60, v22, v46\n"
"v_mfma_f32_16x16x32_bf16 v[40:43], a[80:83], v[100:103], v[40:43]\n"
"v_fma_f32 v47, -v60, v23, v47\n"
"v_mul_f32 v64, v44, v61\n"
"v_mfma_f32_16x16x32_bf16 v[40:43], a[92:95], v[104:107], v[40:43]\n"
"ds_read_b128 v[76:79], v4 offset:14976\n"
"v_mul_f32 v65, v45, v61\n"
"v_mfma_f32_16x16x32_bf16 v[36:39], a[88:91], v[104:107], v[36:39]\n"
"v_exp_f32 v64, v64\n"
"v_mfma_f32_16x16x32_bf16 v[36:39], a[76:79], v[100:103], v[36:39]\n"
"buffer_load_dwordx4 v2, s[32:35], s63 offen offset:1024 sc1 lds\n"
"v_mul_f32 v66, v46, v61\n"
"v_mfma_f32_16x16x32_bf16 v[36:39], a[64:67], v[96:99], v[36:39]\n"
"s_waitcnt vmcnt(3)\n"
"s_waitcnt vmcnt(2)\n"
"s_barrier\n"
"v_mfma_f32_16x16x32_bf16 v[36:39], a[52:55], v[92:95], v[36:39]\n"
"ds_read_b128 v[80:83], v4 offset:16128\n"
"v_mul_f32 v67, v47, v61\n"
"v_mfma_f32_16x16x32_bf16 v[32:35], a[48:51], v[92:95], v[32:35]\n"
"v_exp_f32 v65, v65\n"
"v_mfma_f32_16x16x32_bf16 v[32:35], a[60:63], v[96:99], v[32:35]\n"
"buffer_load_dwordx4 v2, s[32:35], s63 offen offset:2048 sc1 lds\n"
"ds_read_b128 v[84:87], v4 offset:17280\n"
"v_mfma_f32_16x16x32_bf16 v[32:35], a[72:75], v[100:103], v[32:35]\n"
"v_exp_f32 v66, v66\n"
"v_mfma_f32_16x16x32_bf16 v[32:35], a[84:87], v[104:107], v[32:35]\n"
"v_mul_f32 v68, v52, v61\n"
"s_waitcnt lgkmcnt(4)\n"
"v_mfma_f32_16x16x32_bf16 v[32:35], a[96:99], v[108:111], v[32:35]\n"
"ds_read_b128 v[88:91], v4 offset:18432\n"
"v_mul_f32 v69, v53, v61\n"
"v_mfma_f32_16x16x32_bf16 v[32:35], a[108:111], v[112:115], v[32:35]\n"
"v_exp_f32 v67, v67\n"
"v_mfma_f32_16x16x32_bf16 v[32:35], a[120:123], v[116:119], v[32:35]\n"
"v_exp_f32 v68, v68\n"
"v_mfma_f32_16x16x32_bf16 v[32:35], a[132:135], v[120:123], v[32:35]\n"
"ds_read_b128 v[92:95], v4 offset:19584\n"
"v_mul_f32 v44, v44, v48\n"
"v_mfma_f32_16x16x32_bf16 v[36:39], a[136:139], v[120:123], v[36:39]\n"
"v_exp_f32 v69, v69\n"
"v_mfma_f32_16x16x32_bf16 v[36:39], a[124:127], v[116:119], v[36:39]\n"
"buffer_load_dwordx4 v2, s[32:35], s63 offen offset:3072 sc1 lds\n"
"v_mul_f32 v45, v45, v49\n"
"v_mfma_f32_16x16x32_bf16 v[36:39], a[112:115], v[112:115], v[36:39]\n"
"ds_read_b128 v[96:99], v4 offset:20736\n"
"v_mul_f32 v46, v46, v50\n"
"v_mfma_f32_16x16x32_bf16 v[36:39], a[100:103], v[108:111], v[36:39]\n"
"v_mul_f32 v47, v47, v51\n"
"v_mul_f32 v52, v52, v54\n"
"v_mfma_f32_16x16x32_bf16 v[40:43], a[104:107], v[108:111], v[40:43]\n"
"v_mul_f32 v53, v53, v55\n"
"v_fma_f32 v64, v64, v62, v62\n"
"v_mfma_f32_16x16x32_bf16 v[40:43], a[116:119], v[112:115], v[40:43]\n"
"ds_read_b128 v[100:103], v4 offset:21888\n"
"s_add_u32 m0, s64, 4096\n"
"v_mfma_f32_16x16x32_bf16 v[40:43], a[128:131], v[116:119], v[40:43]\n"
"v_rcp_f32 v64, v64\n"
"v_mfma_f32_16x16x32_bf16 v[40:43], a[140:143], v[120:123], v[40:43]\n"
"v_fma_f32 v65, v65, v62, v62\n"
"s_waitcnt lgkmcnt(4)\n"
"v_mfma_f32_16x16x32_bf16 v[40:43], a[152:155], v[124:127], v[40:43]\n"
"ds_read_b128 v[104:107], v4 offset:23040\n"
"v_fma_f32 v66, v66, v62, v62\n"
"v_mfma_f32_16x16x32_bf16 v[40:43], a[164:167], v[76:79], v[40:43]\n"
"buffer_load_dwordx4 v6, s[32:35], s63 offen offset:0 sc1 lds\n"
"v_fma_f32 v67, v67, v62, v62\n"
"v_mfma_f32_16x16x32_bf16 v[40:43], a[176:179], v[80:83], v[40:43]\n"
"v_rcp_f32 v65, v65\n"
"v_mfma_f32_16x16x32_bf16 v[40:43], a[188:191], v[84:87], v[40:43]\n"
"ds_read_b128 v[108:111], v4 offset:24192\n"
"v_fma_f32 v68, v68, v62, v62\n"
"v_mfma_f32_16x16x32_bf16 v[36:39], a[184:187], v[84:87], v[36:39]\n"
"v_rcp_f32 v66, v66\n"
"v_mfma_f32_16x16x32_bf16 v[36:39], a[172:175], v[80:83], v[36:39]\n"
"buffer_load_dwordx4 v6, s[32:35], s63 offen offset:1024 sc1 lds\n"
"v_fma_f32 v69, v69, v62, v62\n"
"v_mfma_f32_16x16x32_bf16 v[36:39], a[160:163], v[76:79], v[36:39]\n"
"ds_read_b128 v[112:115], v4 offset:25344\n"
"v_mul_f32 v44, v44, v64\n"
"v_mfma_f32_16x16x32_bf16 v[36:39], a[148:151], v[124:127], v[36:39]\n"
"v_rcp_f32 v67, v67\n"
"v_mfma_f32_16x16x32_bf16 v[32:35], a[144:147], v[124:127], v[32:35]\n"
"v_rcp_f32 v68, v68\n"
"v_mfma_f32_16x16x32_bf16 v[32:35], a[156:159], v[76:79], v[32:35]\n"
"buffer_load_dwordx2 v[12:13], v14, s[32:35], s66 offen sc1\n"
"ds_read_b128 v[116:119], v4 offset:26496\n"
"v_mfma_f32_16x16x32_bf16 v[32:35], a[168:171], v[80:83], v[32:35]\n"
"v_rcp_f32 v69, v69\n"
"v_mfma_f32_16x16x32_bf16 v[32:35], a[180:183], v[84:87], v[32:35]\n"
"v_mul_f32 v45, v45, v65\n"
"s_waitcnt lgkmcnt(4)\n"
"v_mfma_f32_16x16x32_bf16 v[32:35], a[192:195], v[88:91], v[32:35]\n"
"ds_read_b128 v[120:123], v4 offset:27648\n"
"v_mul_f32 v46, v46, v66\n"
"v_mfma_f32_16x16x32_bf16 v[32:35], a[204:207], v[92:95], v[32:35]\n"
"buffer_load_dwordx4 v6, s[32:35], s63 offen offset:2048 sc1 lds\n"
"v_mul_f32 v47, v47, v67\n"
"v_mfma_f32_16x16x32_bf16 v[32:35], a[216:219], v[96:99], v[32:35]\n"
"v_mul_f32 v52, v52, v68\n"
"v_mul_f32 v53, v53, v69\n"
"v_mfma_f32_16x16x32_bf16 v[32:35], a[228:231], v[100:103], v[32:35]\n"
"ds_read_b128 v[124:127], v4 offset:28800\n"
"v_cvt_pk_bf16_f32 v74, v52, v53\n"
"v_mfma_f32_16x16x32_bf16 v[36:39], a[232:235], v[100:103], v[36:39]\n"
"v_cvt_pk_bf16_f32 v72, v44, v45\n"
"v_cvt_pk_bf16_f32 v73, v46, v47\n"
"v_mfma_f32_16x16x32_bf16 v[36:39], a[220:223], v[96:99], v[36:39]\n"
"buffer_load_dwordx4 v6, s[32:35], s63 offen offset:3072 sc1 lds\n"
"v_mfma_f32_16x16x32_bf16 v[36:39], a[208:211], v[92:95], v[36:39]\n"
"ds_read_b128 v[76:79], v4 offset:29952\n"
"ds_read2st64_b32 v[56:57], v8 offset0:144 offset1:180\n"
"v_mfma_f32_16x16x32_bf16 v[36:39], a[196:199], v[88:91], v[36:39]\n"
"ds_read2st64_b32 v[58:59], v8 offset0:216 offset1:252\n"
"v_xor_b32 v8, v9, v8\n"
"v_mfma_f32_16x16x32_bf16 v[40:43], a[200:203], v[88:91], v[40:43]\n"
"v_permlane32_swap_b32 v74, v75\n"
"v_mfma_f32_16x16x32_bf16 v[40:43], a[212:215], v[92:95], v[40:43]\n"
"ds_read_b128 v[80:83], v4 offset:31104\n"
"v_mfma_f32_16x16x32_bf16 v[40:43], a[224:227], v[96:99], v[40:43]\n"
"v_permlane16_swap_b32 v72, v74\n"
"v_mfma_f32_16x16x32_bf16 v[40:43], a[236:239], v[100:103], v[40:43]\n"
"s_add_u32 m0, s64, 8192\n"
"s_waitcnt lgkmcnt(6)\n"
"v_mfma_f32_16x16x32_bf16 v[40:43], a[248:251], v[104:107], v[40:43]\n"
"ds_read_b128 v[84:87], v4 offset:32256\n"
"v_mfma_f32_16x16x32_bf16 v[40:43], v[132:135], v[108:111], v[40:43]\n"
"buffer_load_dwordx4 v7, s[32:35], s63 offen offset:0 sc1 lds\n"
"s_add_u32 s63, s63, 890880\n"
"v_mfma_f32_16x16x32_bf16 v[40:43], v[144:147], v[112:115], v[40:43]\n"
"v_permlane16_swap_b32 v73, v75\n"
"v_mfma_f32_16x16x32_bf16 v[40:43], v[156:159], v[116:119], v[40:43]\n"
"ds_read_b128 v[88:91], v4 offset:33408\n"
"v_mfma_f32_16x16x32_bf16 v[36:39], v[152:155], v[116:119], v[36:39]\n"
"v_mfma_f32_16x16x32_bf16 v[36:39], v[140:143], v[112:115], v[36:39]\n"
"buffer_store_dwordx4 v[72:75], v15, s[32:35], s68 offen sc1\n"
"ds_read_b32 v62, v11 offset:0\n"
"v_mfma_f32_16x16x32_bf16 v[36:39], v[128:131], v[108:111], v[36:39]\n"
"ds_read_b128 v[92:95], v4 offset:34560\n"
"s_waitcnt lgkmcnt(5)\n"
"v_add_f32 v56, v56, v57\n"
"v_mfma_f32_16x16x32_bf16 v[36:39], a[244:247], v[104:107], v[36:39]\n"
"v_add_f32 v58, v58, v59\n"
"v_mfma_f32_16x16x32_bf16 v[32:35], a[240:243], v[104:107], v[32:35]\n"
"v_add_f32 v56, v56, v58\n"
"v_mfma_f32_16x16x32_bf16 v[32:35], a[252:255], v[108:111], v[32:35]\n"
"ds_read_b128 v[96:99], v4 offset:35712\n"
"v_add_f32_dpp v56, v56, v56 row_ror:8 row_mask:0xf bank_mask:0xf\n"
"v_mfma_f32_16x16x32_bf16 v[32:35], v[136:139], v[112:115], v[32:35]\n"
"v_xor_b32 v4, 73728, v4\n"
"v_add_f32_dpp v56, v56, v56 row_ror:4 row_mask:0xf bank_mask:0xf\n"
"v_mfma_f32_16x16x32_bf16 v[32:35], v[148:151], v[116:119], v[32:35]\n"
"s_waitcnt lgkmcnt(2)\n"
"v_mfma_f32_16x16x32_bf16 v[32:35], v[160:163], v[120:123], v[32:35]\n"
"ds_read_b128 v[100:103], v5 offset:0\n"
"v_lshlrev_b32 v60, 16, v62\n"
"v_mfma_f32_16x16x32_bf16 v[32:35], v[172:175], v[124:127], v[32:35]\n"
"v_and_b32 v62, 0xffff0000, v62\n"
"v_add_f32_dpp v56, v56, v56 row_ror:2 row_mask:0xf bank_mask:0xf\n"
"v_mfma_f32_16x16x32_bf16 v[32:35], v[184:187], v[76:79], v[32:35]\n"
"v_rcp_f32 v61, v62\n"
"v_mfma_f32_16x16x32_bf16 v[32:35], v[196:199], v[80:83], v[32:35]\n"
"ds_read_b128 v[104:107], v5 offset:1152\n"
"v_mul_f32 v62, v62, v62\n"
"v_mfma_f32_16x16x32_bf16 v[36:39], v[200:203], v[80:83], v[36:39]\n"
"v_mul_f32 v61, 0xbfb8aa3b, v61\n"
"v_mul_f32 v56, 0x3a800000, v56\n"
"v_mfma_f32_16x16x32_bf16 v[36:39], v[188:191], v[76:79], v[36:39]\n"
"v_mul_f32 v57, v56, v56\n"
"v_mfma_f32_16x16x32_bf16 v[36:39], v[176:179], v[124:127], v[36:39]\n"
"ds_read_b128 v[108:111], v5 offset:2304\n"
"v_sub_f32_dpp v57, v56, v57 quad_perm:[1,1,3,3] row_mask:0xf bank_mask:0xf\n"
"v_mfma_f32_16x16x32_bf16 v[36:39], v[164:167], v[120:123], v[36:39]\n"
"v_max_f32 v57, 0, v57\n"
"v_mfma_f32_16x16x32_bf16 v[40:43], v[168:171], v[120:123], v[40:43]\n"
"v_cvt_pk_bf16_f32 v57, 0, v57\n"
"v_mfma_f32_16x16x32_bf16 v[40:43], v[180:183], v[124:127], v[40:43]\n"
"ds_read_b128 v[112:115], v5 offset:3456\n"
"v_add_f32 v57, 0x3727c5ac, v57\n"
"v_mfma_f32_16x16x32_bf16 v[40:43], v[192:195], v[76:79], v[40:43]\n"
"v_cvt_pk_bf16_f32 v57, 0, v57\n"
"v_mfma_f32_16x16x32_bf16 v[40:43], v[204:207], v[80:83], v[40:43]\n"
"s_waitcnt lgkmcnt(4)\n"
"v_mfma_f32_16x16x32_bf16 v[40:43], v[216:219], v[84:87], v[40:43]\n"
"ds_read_b128 v[116:119], v5 offset:4608\n"
"v_mfma_f32_16x16x32_bf16 v[40:43], v[228:231], v[88:91], v[40:43]\n"
"v_sqrt_f32 v57, v57\n"
"v_mfma_f32_16x16x32_bf16 v[40:43], v[240:243], v[92:95], v[40:43]\n"
"v_cvt_pk_bf16_f32 v57, v56, v57\n"
"v_mfma_f32_16x16x32_bf16 v[40:43], v[252:255], v[96:99], v[40:43]\n"
"ds_read_b128 v[120:123], v5 offset:5760\n"
"ds_write_b32 v10, v57 offset:128\n"
"v_mfma_f32_16x16x32_bf16 v[36:39], v[248:251], v[96:99], v[36:39]\n"
"v_mfma_f32_16x16x32_bf16 v[36:39], v[236:239], v[92:95], v[36:39]\n"
"v_mfma_f32_16x16x32_bf16 v[36:39], v[224:227], v[88:91], v[36:39]\n"
"ds_read_b128 v[124:127], v5 offset:6912\n"
"v_mfma_f32_16x16x32_bf16 v[36:39], v[212:215], v[84:87], v[36:39]\n"
"v_mfma_f32_16x16x32_bf16 v[32:35], v[208:211], v[84:87], v[32:35]\n"
"v_mfma_f32_16x16x32_bf16 v[32:35], v[220:223], v[88:91], v[32:35]\n"
"ds_read_b128 v[76:79], v5 offset:8064\n"
"v_mfma_f32_16x16x32_bf16 v[32:35], v[232:235], v[92:95], v[32:35]\n"
"s_sub_u32 s62, s62, 1\n"
"v_mfma_f32_16x16x32_bf16 v[32:35], v[244:247], v[96:99], v[32:35]\n"
"s_cbranch_scc1 .L_gt0_1\n"
"s_waitcnt lgkmcnt(5)\n"
"v_mfma_f32_16x16x32_bf16 v[44:47], a[0:3], v[100:103], 0\n"
"ds_read_b128 v[80:83], v5 offset:9216\n"
"s_add_u32 s68, s68, 890880\n"
"v_mfma_f32_16x16x32_bf16 v[44:47], a[12:15], v[104:107], v[44:47]\n"
"v_fma_f32 v40, -v60, v28, v40\n"
"v_fma_f32 v41, -v60, v29, v41\n"
"v_mfma_f32_16x16x32_bf16 v[44:47], a[24:27], v[108:111], v[44:47]\n"
"v_fma_f32 v42, -v60, v30, v42\n"
"v_fma_f32 v43, -v60, v31, v43\n"
"v_mfma_f32_16x16x32_bf16 v[44:47], a[36:39], v[112:115], v[44:47]\n"
"ds_read_b128 v[84:87], v5 offset:10368\n"
"v_fma_f32 v36, -v60, v24, v36\n"
"v_mfma_f32_16x16x32_bf16 v[48:51], a[40:43], v[112:115], 0\n"
"v_permlane32_swap_b32 v40, v42\n"
"v_mfma_f32_16x16x32_bf16 v[48:51], a[28:31], v[108:111], v[48:51]\n"
"s_waitcnt vmcnt(4)\n"
"v_cmp_ne_u64 vcc, v[16:17], v[12:13]\n"
"v_mfma_f32_16x16x32_bf16 v[48:51], a[16:19], v[104:107], v[48:51]\n"
"ds_read_b128 v[88:91], v5 offset:11520\n"
"v_fma_f32 v37, -v60, v25, v37\n"
"v_mfma_f32_16x16x32_bf16 v[48:51], a[4:7], v[100:103], v[48:51]\n"
"s_cbranch_vccnz .L_rtg1_1\n"
".L_bkg1_1:\n"
"s_add_u32 s66, s66, 890880\n"
"v_mfma_f32_16x16x32_bf16 v[52:55], a[8:11], v[100:103], 0\n"
"s_add_u32 m0, s64, 36864\n"
"v_fma_f32 v38, -v60, v26, v38\n"
"v_mfma_f32_16x16x32_bf16 v[52:55], a[20:23], v[104:107], v[52:55]\n"
"buffer_load_dwordx4 v2, s[32:35], s63 offen offset:0 sc1 lds\n"
"ds_read_b128 v[92:95], v5 offset:12672\n"
"v_mfma_f32_16x16x32_bf16 v[52:55], a[32:35], v[108:111], v[52:55]\n"
"v_permlane32_swap_b32 v41, v43\n"
"v_mfma_f32_16x16x32_bf16 v[52:55], a[44:47], v[112:115], v[52:55]\n"
"v_fma_f32 v39, -v60, v27, v39\n"
"s_waitcnt lgkmcnt(4)\n"
"v_mfma_f32_16x16x32_bf16 v[52:55], a[56:59], v[116:119], v[52:55]\n"
"ds_read_b128 v[96:99], v5 offset:13824\n"
"v_fma_f32 v32, -v60, v20, v32\n"
"v_mfma_f32_16x16x32_bf16 v[52:55], a[68:71], v[120:123], v[52:55]\n"
"v_fma_f32 v33, -v60, v21, v33\n"
"v_fma_f32 v34, -v60, v22, v34\n"
"v_mfma_f32_16x16x32_bf16 v[52:55], a[80:83], v[124:127], v[52:55]\n"
"v_fma_f32 v35, -v60, v23, v35\n"
"v_mul_f32 v64, v32, v61\n"
"v_mfma_f32_16x16x32_bf16 v[52:55], a[92:95], v[76:79], v[52:55]\n"
"ds_read_b128 v[100:103], v5 offset:14976\n"
"v_mul_f32 v65, v33, v61\n"
"v_mfma_f32_16x16x32_bf16 v[48:51], a[88:91], v[76:79], v[48:51]\n"
"v_exp_f32 v64, v64\n"
"v_mfma_f32_16x16x32_bf16 v[48:51], a[76:79], v[124:127], v[48:51]\n"
"buffer_load_dwordx4 v2, s[32:35], s63 offen offset:1024 sc1 lds\n"
"v_mul_f32 v66, v34, v61\n"
"v_mfma_f32_16x16x32_bf16 v[48:51], a[64:67], v[120:123], v[48:51]\n"
"s_waitcnt vmcnt(3)\n"
"s_waitcnt vmcnt(2)\n"
"s_barrier\n"
"v_mfma_f32_16x16x32_bf16 v[48:51], a[52:55], v[116:119], v[48:51]\n"
"ds_read_b128 v[104:107], v5 offset:16128\n"
"v_mul_f32 v67, v35, v61\n"
"v_mfma_f32_16x16x32_bf16 v[44:47], a[48:51], v[116:119], v[44:47]\n"
"v_exp_f32 v65, v65\n"
"v_mfma_f32_16x16x32_bf16 v[44:47], a[60:63], v[120:123], v[44:47]\n"
"buffer_load_dwordx4 v2, s[32:35], s63 offen offset:2048 sc1 lds\n"
"ds_read_b128 v[108:111], v5 offset:17280\n"
"v_mfma_f32_16x16x32_bf16 v[44:47], a[72:75], v[124:127], v[44:47]\n"
"v_exp_f32 v66, v66\n"
"v_mfma_f32_16x16x32_bf16 v[44:47], a[84:87], v[76:79], v[44:47]\n"
"v_mul_f32 v68, v40, v61\n"
"s_waitcnt lgkmcnt(4)\n"
"v_mfma_f32_16x16x32_bf16 v[44:47], a[96:99], v[80:83], v[44:47]\n"
"ds_read_b128 v[112:115], v5 offset:18432\n"
"v_mul_f32 v69, v41, v61\n"
"v_mfma_f32_16x16x32_bf16 v[44:47], a[108:111], v[84:87], v[44:47]\n"
"v_exp_f32 v67, v67\n"
"v_mfma_f32_16x16x32_bf16 v[44:47], a[120:123], v[88:91], v[44:47]\n"
"v_exp_f32 v68, v68\n"
"v_mfma_f32_16x16x32_bf16 v[44:47], a[132:135], v[92:95], v[44:47]\n"
"ds_read_b128 v[116:119], v5 offset:19584\n"
"v_mul_f32 v32, v32, v36\n"
"v_mfma_f32_16x16x32_bf16 v[48:51], a[136:139], v[92:95], v[48:51]\n"
"v_exp_f32 v69, v69\n"
"v_mfma_f32_16x16x32_bf16 v[48:51], a[124:127], v[88:91], v[48:51]\n"
"buffer_load_dwordx4 v2, s[32:35], s63 offen offset:3072 sc1 lds\n"
"v_mul_f32 v33, v33, v37\n"
"v_mfma_f32_16x16x32_bf16 v[48:51], a[112:115], v[84:87], v[48:51]\n"
"ds_read_b128 v[120:123], v5 offset:20736\n"
"v_mul_f32 v34, v34, v38\n"
"v_mfma_f32_16x16x32_bf16 v[48:51], a[100:103], v[80:83], v[48:51]\n"
"v_mul_f32 v35, v35, v39\n"
"v_mul_f32 v40, v40, v42\n"
"v_mfma_f32_16x16x32_bf16 v[52:55], a[104:107], v[80:83], v[52:55]\n"
"v_mul_f32 v41, v41, v43\n"
"v_fma_f32 v64, v64, v62, v62\n"
"v_mfma_f32_16x16x32_bf16 v[52:55], a[116:119], v[84:87], v[52:55]\n"
"ds_read_b128 v[124:127], v5 offset:21888\n"
"s_add_u32 m0, s64, 40960\n"
"v_mfma_f32_16x16x32_bf16 v[52:55], a[128:131], v[88:91], v[52:55]\n"
"v_rcp_f32 v64, v64\n"
"v_mfma_f32_16x16x32_bf16 v[52:55], a[140:143], v[92:95], v[52:55]\n"
"v_fma_f32 v65, v65, v62, v62\n"
"s_waitcnt lgkmcnt(4)\n"
"v_mfma_f32_16x16x32_bf16 v[52:55], a[152:155], v[96:99], v[52:55]\n"
"ds_read_b128 v[76:79], v5 offset:23040\n"
"v_fma_f32 v66, v66, v62, v62\n"
"v_mfma_f32_16x16x32_bf16 v[52:55], a[164:167], v[100:103], v[52:55]\n"
"buffer_load_dwordx4 v6, s[32:35], s63 offen offset:0 sc1 lds\n"
"v_fma_f32 v67, v67, v62, v62\n"
"v_mfma_f32_16x16x32_bf16 v[52:55], a[176:179], v[104:107], v[52:55]\n"
"v_rcp_f32 v65, v65\n"
"v_mfma_f32_16x16x32_bf16 v[52:55], a[188:191], v[108:111], v[52:55]\n"
"ds_read_b128 v[80:83], v5 offset:24192\n"
"v_fma_f32 v68, v68, v62, v62\n"
"v_mfma_f32_16x16x32_bf16 v[48:51], a[184:187], v[108:111], v[48:51]\n"
"v_rcp_f32 v66, v66\n"
"v_mfma_f32_16x16x32_bf16 v[48:51], a[172:175], v[104:107], v[48:51]\n"
"buffer_load_dwordx4 v6, s[32:35], s63 offen offset:1024 sc1 lds\n"
"v_fma_f32 v69, v69, v62, v62\n"
"v_mfma_f32_16x16x32_bf16 v[48:51], a[160:163], v[100:103], v[48:51]\n"
"ds_read_b128 v[84:87], v5 offset:25344\n"
"v_mul_f32 v32, v32, v64\n"
"v_mfma_f32_16x16x32_bf16 v[48:51], a[148:151], v[96:99], v[48:51]\n"
"v_rcp_f32 v67, v67\n"
"v_mfma_f32_16x16x32_bf16 v[44:47], a[144:147], v[96:99], v[44:47]\n"
"v_rcp_f32 v68, v68\n"
"v_mfma_f32_16x16x32_bf16 v[44:47], a[156:159], v[100:103], v[44:47]\n"
"buffer_load_dwordx2 v[12:13], v14, s[32:35], s66 offen sc1\n"
"ds_read_b128 v[88:91], v5 offset:26496\n"
"v_mfma_f32_16x16x32_bf16 v[44:47], a[168:171], v[104:107], v[44:47]\n"
"v_rcp_f32 v69, v69\n"
"v_mfma_f32_16x16x32_bf16 v[44:47], a[180:183], v[108:111], v[44:47]\n"
"v_mul_f32 v33, v33, v65\n"
"s_waitcnt lgkmcnt(4)\n"
"v_mfma_f32_16x16x32_bf16 v[44:47], a[192:195], v[112:115], v[44:47]\n"
"ds_read_b128 v[92:95], v5 offset:27648\n"
"v_mul_f32 v34, v34, v66\n"
"v_mfma_f32_16x16x32_bf16 v[44:47], a[204:207], v[116:119], v[44:47]\n"
"buffer_load_dwordx4 v6, s[32:35], s63 offen offset:2048 sc1 lds\n"
"v_mul_f32 v35, v35, v67\n"
"v_mfma_f32_16x16x32_bf16 v[44:47], a[216:219], v[120:123], v[44:47]\n"
"v_mul_f32 v40, v40, v68\n"
"v_mul_f32 v41, v41, v69\n"
"v_mfma_f32_16x16x32_bf16 v[44:47], a[228:231], v[124:127], v[44:47]\n"
"ds_read_b128 v[96:99], v5 offset:28800\n"
"v_cvt_pk_bf16_f32 v74, v40, v41\n"
"v_mfma_f32_16x16x32_bf16 v[48:51], a[232:235], v[124:127], v[48:51]\n"
"v_cvt_pk_bf16_f32 v72, v32, v33\n"
"v_cvt_pk_bf16_f32 v73, v34, v35\n"
"v_mfma_f32_16x16x32_bf16 v[48:51], a[220:223], v[120:123], v[48:51]\n"
"buffer_load_dwordx4 v6, s[32:35], s63 offen offset:3072 sc1 lds\n"
"v_mfma_f32_16x16x32_bf16 v[48:51], a[208:211], v[116:119], v[48:51]\n"
"ds_read_b128 v[100:103], v5 offset:29952\n"
"ds_read2st64_b32 v[56:57], v8 offset0:0 offset1:36\n"
"v_mfma_f32_16x16x32_bf16 v[48:51], a[196:199], v[112:115], v[48:51]\n"
"ds_read2st64_b32 v[58:59], v8 offset0:72 offset1:108\n"
"v_mfma_f32_16x16x32_bf16 v[52:55], a[200:203], v[112:115], v[52:55]\n"
"v_permlane32_swap_b32 v74, v75\n"
"v_mfma_f32_16x16x32_bf16 v[52:55], a[212:215], v[116:119], v[52:55]\n"
"ds_read_b128 v[104:107], v5 offset:31104\n"
"v_mfma_f32_16x16x32_bf16 v[52:55], a[224:227], v[120:123], v[52:55]\n"
"v_permlane16_swap_b32 v72, v74\n"
"v_mfma_f32_16x16x32_bf16 v[52:55], a[236:239], v[124:127], v[52:55]\n"
"s_add_u32 m0, s64, 45056\n"
"s_waitcnt lgkmcnt(6)\n"
"v_mfma_f32_16x16x32_bf16 v[52:55], a[248:251], v[76:79], v[52:55]\n"
"ds_read_b128 v[108:111], v5 offset:32256\n"
"v_mfma_f32_16x16x32_bf16 v[52:55], v[132:135], v[80:83], v[52:55]\n"
"buffer_load_dwordx4 v7, s[32:35], s63 offen offset:0 sc1 lds\n"
"s_add_u32 s63, s63, 890880\n"
"v_mfma_f32_16x16x32_bf16 v[52:55], v[144:147], v[84:87], v[52:55]\n"
"s_xor_b32 s64, s64, s65\n"
"v_mfma_f32_16x16x32_bf16 v[52:55], v[156:159], v[88:91], v[52:55]\n"
"ds_read_b128 v[112:115], v5 offset:33408\n"
"v_mfma_f32_16x16x32_bf16 v[48:51], v[152:155], v[88:91], v[48:51]\n"
"v_permlane16_swap_b32 v73, v75\n"
"v_mfma_f32_16x16x32_bf16 v[48:51], v[140:143], v[84:87], v[48:51]\n"
"buffer_store_dwordx4 v[72:75], v15, s[32:35], s68 offen sc1\n"
"ds_read_b32 v62, v11 offset:128\n"
"v_mfma_f32_16x16x32_bf16 v[48:51], v[128:131], v[80:83], v[48:51]\n"
"ds_read_b128 v[116:119], v5 offset:34560\n"
"s_waitcnt lgkmcnt(5)\n"
"v_add_f32 v56, v56, v57\n"
"v_mfma_f32_16x16x32_bf16 v[48:51], a[244:247], v[76:79], v[48:51]\n"
"v_add_f32 v58, v58, v59\n"
"v_mfma_f32_16x16x32_bf16 v[44:47], a[240:243], v[76:79], v[44:47]\n"
"v_add_f32 v56, v56, v58\n"
"v_mfma_f32_16x16x32_bf16 v[44:47], a[252:255], v[80:83], v[44:47]\n"
"ds_read_b128 v[120:123], v5 offset:35712\n"
"v_add_f32_dpp v56, v56, v56 row_ror:8 row_mask:0xf bank_mask:0xf\n"
"v_mfma_f32_16x16x32_bf16 v[44:47], v[136:139], v[84:87], v[44:47]\n"
"v_xor_b32 v5, 73728, v5\n"
"v_add_f32_dpp v56, v56, v56 row_ror:4 row_mask:0xf bank_mask:0xf\n"
"v_mfma_f32_16x16x32_bf16 v[44:47], v[148:151], v[88:91], v[44:47]\n"
"s_waitcnt lgkmcnt(2)\n"
"v_mfma_f32_16x16x32_bf16 v[44:47], v[160:163], v[92:95], v[44:47]\n"
"ds_read_b128 v[76:79], v4 offset:0\n"
"v_lshlrev_b32 v60, 16, v62\n"
"v_mfma_f32_16x16x32_bf16 v[44:47], v[172:175], v[96:99], v[44:47]\n"
"v_and_b32 v62, 0xffff0000, v62\n"
"v_add_f32_dpp v56, v56, v56 row_ror:2 row_mask:0xf bank_mask:0xf\n"
"v_mfma_f32_16x16x32_bf16 v[44:47], v[184:187], v[100:103], v[44:47]\n"
"v_rcp_f32 v61, v62\n"
"v_mfma_f32_16x16x32_bf16 v[44:47], v[196:199], v[104:107], v[44:47]\n"
"ds_read_b128 v[80:83], v4 offset:1152\n"
"v_mul_f32 v62, v62, v62\n"
"v_mfma_f32_16x16x32_bf16 v[48:51], v[200:203], v[104:107], v[48:51]\n"
"v_mul_f32 v61, 0xbfb8aa3b, v61\n"
"v_mul_f32 v56, 0x3a800000, v56\n"
"v_mfma_f32_16x16x32_bf16 v[48:51], v[188:191], v[100:103], v[48:51]\n"
"v_mul_f32 v57, v56, v56\n"
"v_mfma_f32_16x16x32_bf16 v[48:51], v[176:179], v[96:99], v[48:51]\n"
"ds_read_b128 v[84:87], v4 offset:2304\n"
"v_sub_f32_dpp v57, v56, v57 quad_perm:[1,1,3,3] row_mask:0xf bank_mask:0xf\n"
"v_mfma_f32_16x16x32_bf16 v[48:51], v[164:167], v[92:95], v[48:51]\n"
"v_max_f32 v57, 0, v57\n"
"v_mfma_f32_16x16x32_bf16 v[52:55], v[168:171], v[92:95], v[52:55]\n"
"v_cvt_pk_bf16_f32 v57, 0, v57\n"
"v_mfma_f32_16x16x32_bf16 v[52:55], v[180:183], v[96:99], v[52:55]\n"
"ds_read_b128 v[88:91], v4 offset:3456\n"
"v_add_f32 v57, 0x3727c5ac, v57\n"
"v_mfma_f32_16x16x32_bf16 v[52:55], v[192:195], v[100:103], v[52:55]\n"
"v_cvt_pk_bf16_f32 v57, 0, v57\n"
"v_mfma_f32_16x16x32_bf16 v[52:55], v[204:207], v[104:107], v[52:55]\n"
"s_waitcnt lgkmcnt(4)\n"
"v_mfma_f32_16x16x32_bf16 v[52:55], v[216:219], v[108:111], v[52:55]\n"
"ds_read_b128 v[92:95], v4 offset:4608\n"
"v_mfma_f32_16x16x32_bf16 v[52:55], v[228:231], v[112:115], v[52:55]\n"
"v_sqrt_f32 v57, v57\n"
"v_mfma_f32_16x16x32_bf16 v[52:55], v[240:243], v[116:119], v[52:55]\n"
"v_cvt_pk_bf16_f32 v57, v56, v57\n"
"v_mfma_f32_16x16x32_bf16 v[52:55], v[252:255], v[120:123], v[52:55]\n"
"ds_read_b128 v[96:99], v4 offset:5760\n"
"ds_write_b32 v10, v57 offset:0\n"
"v_mfma_f32_16x16x32_bf16 v[48:51], v[248:251], v[120:123], v[48:51]\n"
"v_mfma_f32_16x16x32_bf16 v[48:51], v[236:239], v[116:119], v[48:51]\n"
"v_mfma_f32_16x16x32_bf16 v[48:51], v[224:227], v[112:115], v[48:51]\n"
"ds_read_b128 v[100:103], v4 offset:6912\n"
"v_mfma_f32_16x16x32_bf16 v[48:51], v[212:215], v[108:111], v[48:51]\n"
"v_mfma_f32_16x16x32_bf16 v[44:47], v[208:211], v[108:111], v[44:47]\n"
"v_mfma_f32_16x16x32_bf16 v[44:47], v[220:223], v[112:115], v[44:47]\n"
"ds_read_b128 v[104:107], v4 offset:8064\n"
"v_mfma_f32_16x16x32_bf16 v[44:47], v[232:235], v[116:119], v[44:47]\n"
"s_sub_u32 s62, s62, 1\n"
"v_mfma_f32_16x16x32_bf16 v[44:47], v[244:247], v[120:123], v[44:47]\n"
"s_cbranch_scc0 .L_gl_1\n"
"s_nop 1\n"
"s_add_u32 s68, s68, 890880\n"
"s_waitcnt vmcnt(0)\n"
"s_barrier\n"
"buffer_store_dwordx2 v[16:17], v2, s[92:95], s68 idxen sc1\n"
"v_fma_f32 v52, -v60, v28, v52\n"
"v_fma_f32 v53, -v60, v29, v53\n"
"v_fma_f32 v54, -v60, v30, v54\n"
"v_fma_f32 v55, -v60, v31, v55\n"
"v_fma_f32 v48, -v60, v24, v48\n"
"v_fma_f32 v49, -v60, v25, v49\n"
"v_fma_f32 v50, -v60, v26, v50\n"
"v_fma_f32 v51, -v60, v27, v51\n"
"v_fma_f32 v44, -v60, v20, v44\n"
"v_fma_f32 v45, -v60, v21, v45\n"
"v_fma_f32 v46, -v60, v22, v46\n"
"v_fma_f32 v47, -v60, v23, v47\n"
"v_permlane32_swap_b32 v52, v54\n"
"v_permlane32_swap_b32 v53, v55\n"
"v_mul_f32 v64, v44, v61\n"
"v_mul_f32 v65, v45, v61\n"
"v_mul_f32 v66, v46, v61\n"
"v_mul_f32 v67, v47, v61\n"
"v_mul_f32 v68, v52, v61\n"
"v_mul_f32 v69, v53, v61\n"
"v_exp_f32 v64, v64\n"
"v_exp_f32 v65, v65\n"
"v_mul_f32 v44, v44, v48\n"
"v_mul_f32 v45, v45, v49\n"
"v_exp_f32 v66, v66\n"
"v_exp_f32 v67, v67\n"
"v_mul_f32 v46, v46, v50\n"
"v_mul_f32 v47, v47, v51\n"
"v_exp_f32 v68, v68\n"
"v_exp_f32 v69, v69\n"
"v_mul_f32 v52, v52, v54\n"
"v_mul_f32 v53, v53, v55\n"
"v_fma_f32 v64, v64, v62, v62\n"
"v_fma_f32 v65, v65, v62, v62\n"
"v_rcp_f32 v64, v64\n"
"v_rcp_f32 v65, v65\n"
"v_fma_f32 v66, v66, v62, v62\n"
"v_fma_f32 v67, v67, v62, v62\n"
"v_rcp_f32 v66, v66\n"
"v_rcp_f32 v67, v67\n"
"v_fma_f32 v68, v68, v62, v62\n"
"v_fma_f32 v69, v69, v62, v62\n"
"v_rcp_f32 v68, v68\n"
"v_rcp_f32 v69, v69\n"
"v_mul_f32 v44, v44, v64\n"
"v_mul_f32 v45, v45, v65\n"
"v_mul_f32 v46, v46, v66\n"
"v_mul_f32 v47, v47, v67\n"
"v_mul_f32 v52, v52, v68\n"
"v_mul_f32 v53, v53, v69\n"
"v_cvt_pk_bf16_f32 v74, v52, v53\n"
"v_cvt_pk_bf16_f32 v72, v44, v45\n"
"v_cvt_pk_bf16_f32 v73, v46, v47\n"
"v_permlane32_swap_b32 v74, v75\n"
"s_nop 1\n"
"v_permlane16_swap_b32 v72, v74\n"
"v_permlane16_swap_b32 v73, v75\n"
"buffer_store_dwordx4 v[72:75], v15, s[32:35], s68 offen sc1\n"
"s_add_u32 s68, s68, 890880\n"
"s_waitcnt vmcnt(0)\n"
"s_barrier\n"
"buffer_store_dwordx2 v[16:17], v2, s[92:95], s68 idxen sc1\n"
"s_branch .L_end\n"
".L_gt0_1:\n"
"s_nop 1\n"
"s_add_u32 s68, s68, 890880\n"
"s_waitcnt vmcnt(0)\n"
"s_barrier\n"
"buffer_store_dwordx2 v[16:17], v2, s[92:95], s68 idxen sc1\n"
"v_fma_f32 v40, -v60, v28, v40\n"
"v_fma_f32 v41, -v60, v29, v41\n"
"v_fma_f32 v42, -v60, v30, v42\n"
"v_fma_f32 v43, -v60, v31, v43\n"
"v_fma_f32 v36, -v60, v24, v36\n"
"v_fma_f32 v37, -v60, v25, v37\n"
"v_fma_f32 v38, -v60, v26, v38\n"
"v_fma_f32 v39, -v60, v27, v39\n"
"v_fma_f32 v32, -v60, v20, v32\n"
"v_fma_f32 v33, -v60, v21, v33\n"
"v_fma_f32 v34, -v60, v22, v34\n"
"v_fma_f32 v35, -v60, v23, v35\n"
"v_permlane32_swap_b32 v40, v42\n"
"v_permlane32_swap_b32 v41, v43\n"
"v_mul_f32 v64, v32, v61\n"
"v_mul_f32 v65, v33, v61\n"
"v_mul_f32 v66, v34, v61\n"
"v_mul_f32 v67, v35, v61\n"
"v_mul_f32 v68, v40, v61\n"
"v_mul_f32 v69, v41, v61\n"
"v_exp_f32 v64, v64\n"
"v_exp_f32 v65, v65\n"
"v_mul_f32 v32, v32, v36\n"
"v_mul_f32 v33, v33, v37\n"
"v_exp_f32 v66, v66\n"
"v_exp_f32 v67, v67\n"
"v_mul_f32 v34, v34, v38\n"
"v_mul_f32 v35, v35, v39\n"
"v_exp_f32 v68, v68\n"
"v_exp_f32 v69, v69\n"
"v_mul_f32 v40, v40, v42\n"
"v_mul_f32 v41, v41, v43\n"
"v_fma_f32 v64, v64, v62, v62\n"
"v_fma_f32 v65, v65, v62, v62\n"
"v_rcp_f32 v64, v64\n"
"v_rcp_f32 v65, v65\n"
"v_fma_f32 v66, v66, v62, v62\n"
"v_fma_f32 v67, v67, v62, v62\n"
"v_rcp_f32 v66, v66\n"
"v_rcp_f32 v67, v67\n"
"v_fma_f32 v68, v68, v62, v62\n"
"v_fma_f32 v69, v69, v62, v62\n"
"v_rcp_f32 v68, v68\n"
"v_rcp_f32 v69, v69\n"
"v_mul_f32 v32, v32, v64\n"
"v_mul_f32 v33, v33, v65\n"
"v_mul_f32 v34, v34, v66\n"
"v_mul_f32 v35, v35, v67\n"
"v_mul_f32 v40, v40, v68\n"
"v_mul_f32 v41, v41, v69\n"
"v_cvt_pk_bf16_f32 v74, v40, v41\n"
"v_cvt_pk_bf16_f32 v72, v32, v33\n"
"v_cvt_pk_bf16_f32 v73, v34, v35\n"
"v_permlane32_swap_b32 v74, v75\n"
"s_nop 1\n"
"v_permlane16_swap_b32 v72, v74\n"
"v_permlane16_swap_b32 v73, v75\n"
"buffer_store_dwordx4 v[72:75], v15, s[32:35], s68 offen sc1\n"
"s_add_u32 s68, s68, 890880\n"
"s_waitcnt vmcnt(0)\n"
"s_barrier\n"
"buffer_store_dwordx2 v[16:17], v2, s[92:95], s68 idxen sc1\n"
"s_branch .L_end\n"
".p2align 6\n"
".L_gv2:\n"
".L_gl_2:\n"
"s_waitcnt lgkmcnt(5)\n"
"v_mfma_f32_16x16x32_bf16 v[32:35], a[0:3], v[76:79], 0\n"
"ds_read_b128 v[108:111], v4 offset:9216\n"
"s_add_u32 s68, s68, 890880\n"
"v_mfma_f32_16x16x32_bf16 v[32:35], a[12:15], v[80:83], v[32:35]\n"
"v_fma_f32 v52, -v60, v28, v52\n"
"v_fma_f32 v53, -v60, v29, v53\n"
"v_mfma_f32_16x16x32_bf16 v[32:35], a[24:27], v[84:87], v[32:35]\n"
"v_fma_f32 v54, -v60, v30, v54\n"
"v_fma_f32 v55, -v60, v31, v55\n"
"v_mfma_f32_16x16x32_bf16 v[32:35], a[36:39], v[88:91], v[32:35]\n"
"ds_read_b128 v[112:115], v4 offset:10368\n"
"v_fma_f32 v48, -v60, v24, v48\n"
"v_mfma_f32_16x16x32_bf16 v[36:39], a[40:43], v[88:91], 0\n"
"v_permlane32_swap_b32 v52, v54\n"
"v_mfma_f32_16x16x32_bf16 v[36:39], a[28:31], v[84:87], v[36:39]\n"
"s_waitcnt vmcnt(4)\n"
"v_cmp_ne_u64 vcc, v[16:17], v[12:13]\n"
"v_mfma_f32_16x16x32_bf16 v[36:39], a[16:19], v[80:83], v[36:39]\n"
"ds_read_b128 v[116:119], v4 offset:11520\n"
"v_fma_f32 v49, -v60, v25, v49\n"
"v_mfma_f32_16x16x32_bf16 v[36:39], a[4:7], v[76:79], v[36:39]\n"
"s_cbranch_vccnz .L_rtg0_2\n"
".L_bkg0_2:\n"
"s_add_u32 s66, s66, 890880\n"
"v_mfma_f32_16x16x32_bf16 v[40:43], a[8:11], v[76:79], 0\n"
"s_add_u32 m0, s64, 0\n"
"v_fma_f32 v50, -v60, v26, v50\n"
"v_mfma_f32_16x16x32_bf16 v[40:43], a[20:23], v[80:83], v[40:43]\n"
"buffer_load_dwordx4 v2, s[32:35], s63 offen offset:0 sc1 lds\n"
"ds_read_b128 v[120:123], v4 offset:12672\n"
"v_mfma_f32_16x16x32_bf16 v[40:43], a[32:35], v[84:87], v[40:43]\n"
"v_permlane32_swap_b32 v53, v55\n"
"v_mfma_f32_16x16x32_bf16 v[40:43], a[44:47], v[88:91], v[40:43]\n"
"v_fma_f32 v51, -v60, v27, v51\n"
"s_waitcnt lgkmcnt(4)\n"
"v_mfma_f32_16x16x32_bf16 v[40:43], a[56:59], v[92:95], v[40:43]\n"
"ds_read_b128 v[124:127], v4 offset:13824\n"
"v_fma_f32 v44, -v60, v20, v44\n"
"v_mfma_f32_16x16x32_bf16 v[40:43], a[68:71], v[96:99], v[40:43]\n"
"v_fma_f32 v45, -v60, v21, v45\n"
"v_fma_f32 v46, -v60, v22, v46\n"
"v_mfma_f32_16x16x32_bf16 v[40:43], a[80:83], v[100:103], v[40:43]\n"
"v_fma_f32 v47, -v60, v23, v47\n"
"v_mul_f32 v64, v44, v61\n"
"v_mfma_f32_16x16x32_bf16 v[40:43], a[92:95], v[104:107], v[40:43]\n"
"ds_read_b128 v[76:79], v4 offset:14976\n"
"v_mul_f32 v65, v45, v61\n"
"v_mfma_f32_16x16x32_bf16 v[36:39], a[88:91], v[104:107], v[36:39]\n"
"v_exp_f32 v64, v64\n"
"v_mfma_f32_16x16x32_bf16 v[36:39], a[76:79], v[100:103], v[36:39]\n"
"s_waitcnt vmcnt(2)\n"
"s_waitcnt vmcnt(1)\n"
"s_barrier\n"
"v_mfma_f32_16x16x32_bf16 v[36:39], a[64:67], v[96:99], v[36:39]\n"
"ds_read_b128 v[80:83], v4 offset:16128\n"
"v_mul_f32 v66, v46, v61\n"
"v_mfma_f32_16x16x32_bf16 v[36:39], a[52:55], v[92:95], v[36:39]\n"
"v_exp_f32 v65, v65\n"
"v_mfma_f32_16x16x32_bf16 v[32:35], a[48:51], v[92:95], v[32:35]\n"
"v_exp_f32 v66, v66\n"
"v_mfma_f32_16x16x32_bf16 v[32:35], a[60:63], v[96:99], v[32:35]\n"
"buffer_load_dwordx4 v2, s[32:35], s63 offen offset:1024 sc1 lds\n"
"ds_read_b128 v[84:87], v4 offset:17280\n"
"v_mfma_f32_16x16x32_bf16 v[32:35], a[72:75], v[100:103], v[32:35]\n"
"v_mul_f32 v67, v47, v61\n"
"v_mul_f32 v68, v52, v61\n"
"v_mfma_f32_16x16x32_bf16 v[32:35], a[84:87], v[104:107], v[32:35]\n"
"v_mul_f32 v69, v53, v61\n"
"s_waitcnt lgkmcnt(4)\n"
"v_mfma_f32_16x16x32_bf16 v[32:35], a[96:99], v[108:111], v[32:35]\n"
"ds_read_b128 v[88:91], v4 offset:18432\n"
"v_mul_f32 v44, v44, v48\n"
"v_mfma_f32_16x16x32_bf16 v[32:35], a[108:111], v[112:115], v[32:35]\n"
"buffer_load_dwordx4 v2, s[32:35], s63 offen offset:2048 sc1 lds\n"
"v_mul_f32 v45, v45, v49\n"
"v_mfma_f32_16x16x32_bf16 v[32:35], a[120:123], v[116:119], v[32:35]\n"
"v_exp_f32 v67, v67\n"
"v_mfma_f32_16x16x32_bf16 v[32:35], a[132:135], v[120:123], v[32:35]\n"
"ds_read_b128 v[92:95], v4 offset:19584\n"
"v_mul_f32 v46, v46, v50\n"
"v_mfma_f32_16x16x32_bf16 v[36:39], a[136:139], v[120:123], v[36:39]\n"
"v_exp_f32 v68, v68\n"
"v_mfma_f32_16x16x32_bf16 v[36:39], a[124:127], v[116:119], v[36:39]\n"
"buffer_load_dwordx4 v2, s[32:35], s63 offen offset:3072 sc1 lds\n"
"v_mul_f32 v47, v47, v51\n"
"v_mfma_f32_16x16x32_bf16 v[36:39], a[112:115], v[112:115], v[36:39]\n"
"ds_read_b128 v[96:99], v4 offset:20736\n"
"v_mul_f32 v52, v52, v54\n"
"v_mfma_f32_16x16x32_bf16 v[36:39], a[100:103], v[108:111], v[36:39]\n"
"v_exp_f32 v69, v69\n"
"v_mfma_f32_16x16x32_bf16 v[40:43], a[104:107], v[108:111], v[40:43]\n"
"v_mul_f32 v53, v53, v55\n"
"v_fma_f32 v64, v64, v62, v62\n"
"v_mfma_f32_16x16x32_bf16 v[40:43], a[116:119], v[112:115], v[40:43]\n"
"ds_read_b128 v[100:103], v4 offset:21888\n"
"s_add_u32 m0, s64, 4096\n"
"v_mfma_f32_16x16x32_bf16 v[40:43], a[128:131], v[116:119], v[40:43]\n"
"v_rcp_f32 v64, v64\n"
"v_mfma_f32_16x16x32_bf16 v[40:43], a[140:143], v[120:123], v[40:43]\n"
"v_fma_f32 v65, v65, v62, v62\n"
"s_waitcnt lgkmcnt(4)\n"
"v_mfma_f32_16x16x32_bf16 v[40:43], a[152:155], v[124:127], v[40:43]\n"
"ds_read_b128 v[104:107], v4 offset:23040\n"
"v_fma_f32 v66, v66, v62, v62\n"
"v_mfma_f32_16x16x32_bf16 v[40:43], a[164:167], v[76:79], v[40:43]\n"
"buffer_load_dwordx4 v6, s[32:35], s63 offen offset:0 sc1 lds\n"
"v_fma_f32 v67, v67, v62, v62\n"
"v_mfma_f32_16x16x32_bf16 v[40:43], a[176:179], v[80:83], v[40:43]\n"
"v_rcp_f32 v65, v65\n"
"v_mfma_f32_16x16x32_bf16 v[40:43], a[188:191], v[84:87], v[40:43]\n"
"ds_read_b128 v[108:111], v4 offset:24192\n"
"v_fma_f32 v68, v68, v62, v62\n"
"v_mfma_f32_16x16x32_bf16 v[36:39], a[184:187], v[84:87], v[36:39]\n"
"v_rcp_f32 v66, v66\n"
"v_mfma_f32_16x16x32_bf16 v[36:39], a[172:175], v[80:83], v[36:39]\n"
"buffer_load_dwordx4 v6, s[32:35], s63 offen offset:1024 sc1 lds\n"
"v_fma_f32 v69, v69, v62, v62\n"
"v_mfma_f32_16x16x32_bf16 v[36:39], a[160:163], v[76:79], v[36:39]\n"
"ds_read_b128 v[112:115], v4 offset:25344\n"
"v_mul_f32 v44, v44, v64\n"
"v_mfma_f32_16x16x32_bf16 v[36:39], a[148:151], v[124:127], v[36:39]\n"
"v_rcp_f32 v67, v67\n"
"v_mfma_f32_16x16x32_bf16 v[32:35], a[144:147], v[124:127], v[32:35]\n"
"v_rcp_f32 v68, v68\n"
"v_mfma_f32_16x16x32_bf16 v[32:35], a[156:159], v[76:79], v[32:35]\n"
"buffer_load_dwordx2 v[12:13], v14, s[32:35], s66 offen sc1\n"
"ds_read_b128 v[116:119], v4 offset:26496\n"
"v_mfma_f32_16x16x32_bf16 v[32:35], a[168:171], v[80:83], v[32:35]\n"
"v_rcp_f32 v69, v69\n"
"v_mfma_f32_16x16x32_bf16 v[32:35], a[180:183], v[84:87], v[32:35]\n"
"v_mul_f32 v45, v45, v65\n"
"s_waitcnt lgkmcnt(4)\n"
"v_mfma_f32_16x16x32_bf16 v[32:35], a[192:195], v[88:91], v[32:35]\n"
"ds_read_b128 v[120:123], v4 offset:27648\n"
"v_mul_f32 v46, v46, v66\n"
"v_mfma_f32_16x16x32_bf16 v[32:35], a[204:207], v[92:95], v[32:35]\n"
"buffer_load_dwordx4 v6, s[32:35], s63 offen offset:2048 sc1 lds\n"
"v_mul_f32 v47, v47, v67\n"
"v_mfma_f32_16x16x32_bf16 v[32:35], a[216:219], v[96:99], v[32:35]\n"
"v_mul_f32 v52, v52, v68\n"
"v_mul_f32 v53, v53, v69\n"
"v_mfma_f32_16x16x32_bf16 v[32:35], a[228:231], v[100:103], v[32:35]\n"
"ds_read_b128 v[124:127], v4 offset:28800\n"
"v_cvt_pk_bf16_f32 v74, v52, v53\n"
"v_mfma_f32_16x16x32_bf16 v[36:39], a[232:235], v[100:103], v[36:39]\n"
"v_cvt_pk_bf16_f32 v72, v44, v45\n"
"v_cvt_pk_bf16_f32 v73, v46, v47\n"
"v_mfma_f32_16x16x32_bf16 v[36:39], a[220:223], v[96:99], v[36:39]\n"
"buffer_load_dwordx4 v6, s[32:35], s63 offen offset:3072 sc1 lds\n"
"v_mfma_f32_16x16x32_bf16 v[36:39], a[208:211], v[92:95], v[36:39]\n"
"ds_read_b128 v[76:79], v4 offset:29952\n"
"ds_read2st64_b32 v[56:57], v8 offset0:144 offset1:180\n"
"v_mfma_f32_16x16x32_bf16 v[36:39], a[196:199], v[88:91], v[36:39]\n"
"ds_read2st64_b32 v[58:59], v8 offset0:216 offset1:252\n"
"v_xor_b32 v8, v9, v8\n"
"v_mfma_f32_16x16x32_bf16 v[40:43], a[200:203], v[88:91], v[40:43]\n"
"v_permlane32_swap_b32 v74, v75\n"
"v_mfma_f32_16x16x32_bf16 v[40:43], a[212:215], v[92:95], v[40:43]\n"
"ds_read_b128 v[80:83], v4 offset:31104\n"
"v_mfma_f32_16x16x32_bf16 v[40:43], a[224:227], v[96:99], v[40:43]\n"
"v_permlane16_swap_b32 v72, v74\n"
"v_mfma_f32_16x16x32_bf16 v[40:43], a[236:239], v[100:103], v[40:43]\n"
"s_add_u32 m0, s64, 8192\n"
"s_waitcnt lgkmcnt(6)\n"
"v_mfma_f32_16x16x32_bf16 v[40:43], a[248:251], v[104:107], v[40:43]\n"
"ds_read_b128 v[84:87], v4 offset:32256\n"
"v_mfma_f32_16x16x32_bf16 v[40:43], v[132:135], v[108:111], v[40:43]\n"
"buffer_load_dwordx4 v7, s[32:35], s63 offen offset:0 sc1 lds\n"
"s_add_u32 s63, s63, 890880\n"
"v_mfma_f32_16x16x32_bf16 v[40:43], v[144:147], v[112:115], v[40:43]\n"
"v_permlane16_swap_b32 v73, v75\n"
"v_mfma_f32_16x16x32_bf16 v[40:43], v[156:159], v[116:119], v[40:43]\n"
"ds_read_b128 v[88:91], v4 offset:33408\n"
"v_mfma_f32_16x16x32_bf16 v[36:39], v[152:155], v[116:119], v[36:39]\n"
"v_mfma_f32_16x16x32_bf16 v[36:39], v[140:143], v[112:115], v[36:39]\n"
"buffer_store_dwordx4 v[72:75], v15, s[32:35], s68 offen sc1\n"
"ds_read_b32 v62, v11 offset:0\n"
"v_mfma_f32_16x16x32_bf16 v[36:39], v[128:131], v[108:111], v[36:39]\n"
"ds_read_b128 v[92:95], v4 offset:34560\n"
"s_waitcnt lgkmcnt(5)\n"
"v_add_f32 v56, v56, v57\n"
"v_mfma_f32_16x16x32_bf16 v[36:39], a[244:247], v[104:107], v[36:39]\n"
"v_add_f32 v58, v58, v59\n"
"v_mfma_f32_16x16x32_bf16 v[32:35], a[240:243], v[104:107], v[32:35]\n"
"v_add_f32 v56, v56, v58\n"
"v_mfma_f32_16x16x32_bf16 v[32:35], a[252:255], v[108:111], v[32:35]\n"
"ds_read_b128 v[96:99], v4 offset:35712\n"
"v_add_f32_dpp v56, v56, v56 row_ror:8 row_mask:0xf bank_mask:0xf\n"
"v_mfma_f32_16x16x32_bf16 v[32:35], v[136:139], v[112:115], v[32:35]\n"
"v_xor_b32 v4, 73728, v4\n"
"v_add_f32_dpp v56, v56, v56 row_ror:4 row_mask:0xf bank_mask:0xf\n"
"v_mfma_f32_16x16x32_bf16 v[32:35], v[148:151], v[116:119], v[32:35]\n"
"s_waitcnt lgkmcnt(2)\n"
"v_mfma_f32_16x16x32_bf16 v[32:35], v[160:163], v[120:123], v[32:35]\n"
"ds_read_b128 v[100:103], v5 offset:0\n"
"v_lshlrev_b32 v60, 16, v62\n"
"v_mfma_f32_16x16x32_bf16 v[32:35], v[172:175], v[124:127], v[32:35]\n"
"v_and_b32 v62, 0xffff0000, v62\n"
"v_add_f32_dpp v56, v56, v56 row_ror:2 row_mask:0xf bank_mask:0xf\n"
"v_mfma_f32_16x16x32_bf16 v[32:35], v[184:187], v[76:79], v[32:35]\n"
"v_rcp_f32 v61, v62\n"
"v_mfma_f32_16x16x32_bf16 v[32:35], v[196:199], v[80:83], v[32:35]\n"
"ds_read_b128 v[104:107], v5 offset:1152\n"
"v_mul_f32 v62, v62, v62\n"
"v_mfma_f32_16x16x32_bf16 v[36:39], v[200:203], v[80:83], v[36:39]\n"
"v_mul_f32 v61, 0xbfb8aa3b, v61\n"
"v_mul_f32 v56, 0x3a800000, v56\n"
"v_mfma_f32_16x16x32_bf16 v[36:39], v[188:191], v[76:79], v[36:39]\n"
"v_mul_f32 v57, v56, v56\n"
"v_mfma_f32_16x16x32_bf16 v[36:39], v[176:179], v[124:127], v[36:39]\n"
"ds_read_b128 v[108:111], v5 offset:2304\n"
"v_sub_f32_dpp v57, v56, v57 quad_perm:[1,1,3,3] row_mask:0xf bank_mask:0xf\n"
"v_mfma_f32_16x16x32_bf16 v[36:39], v[164:167], v[120:123], v[36:39]\n"
"v_max_f32 v57, 0, v57\n"
"v_mfma_f32_16x16x32_bf16 v[40:43], v[168:171], v[120:123], v[40:43]\n"
"v_cvt_pk_bf16_f32 v57, 0, v57\n"
"v_mfma_f32_16x16x32_bf16 v[40:43], v[180:183], v[124:127], v[40:43]\n"
"ds_read_b128 v[112:115], v5 offset:3456\n"
"v_add_f32 v57, 0x3727c5ac, v57\n"
"v_mfma_f32_16x16x32_bf16 v[40:43], v[192:195], v[76:79], v[40:43]\n"
"v_cvt_pk_bf16_f32 v57, 0, v57\n"
"v_mfma_f32_16x16x32_bf16 v[40:43], v[204:207], v[80:83], v[40:43]\n"
"s_waitcnt lgkmcnt(4)\n"
"v_mfma_f32_16x16x32_bf16 v[40:43], v[216:219], v[84:87], v[40:43]\n"
"ds_read_b128 v[116:119], v5 offset:4608\n"
"v_mfma_f32_16x16x32_bf16 v[40:43], v[228:231], v[88:91], v[40:43]\n"
"v_sqrt_f32 v57, v57\n"
"v_mfma_f32_16x16x32_bf16 v[40:43], v[240:243], v[92:95], v[40:43]\n"
"v_cvt_pk_bf16_f32 v57, v56, v57\n"
"v_mfma_f32_16x16x32_bf16 v[40:43], v[252:255], v[96:99], v[40:43]\n"
"ds_read_b128 v[120:123], v5 offset:5760\n"
"ds_write_b32 v10, v57 offset:128\n"
"v_mfma_f32_16x16x32_bf16 v[36:39], v[248:251], v[96:99], v[36:39]\n"
"v_mfma_f32_16x16x32_bf16 v[36:39], v[236:239], v[92:95], v[36:39]\n"
"v_mfma_f32_16x16x32_bf16 v[36:39], v[224:227], v[88:91], v[36:39]\n"
"ds_read_b128 v[124:127], v5 offset:6912\n"
"v_mfma_f32_16x16x32_bf16 v[36:39], v[212:215], v[84:87], v[36:39]\n"
"v_mfma_f32_16x16x32_bf16 v[32:35], v[208:211], v[84:87], v[32:35]\n"
"v_mfma_f32_16x16x32_bf16 v[32:35], v[220:223], v[88:91], v[32:35]\n"
"ds_read_b128 v[76:79], v5 offset:8064\n"
"v_mfma_f32_16x16x32_bf16 v[32:35], v[232:235], v[92:95], v[32:35]\n"
"s_sub_u32 s62, s62, 1\n"
"v_mfma_f32_16x16x32_bf16 v[32:35], v[244:247], v[96:99], v[32:35]\n"
"s_cbranch_scc1 .L_gt0_2\n"
"s_waitcnt lgkmcnt(5)\n"
"v_mfma_f32_16x16x32_bf16 v[44:47], a[0:3], v[100:103], 0\n"
"ds_read_b128 v[80:83], v5 offset:9216\n"
"s_add_u32 s68, s68, 890880\n"
"v_mfma_f32_16x16x32_bf16 v[44:47], a[12:15], v[104:107], v[44:47]\n"
"v_fma_f32 v40, -v60, v28, v40\n"
"v_fma_f32 v41, -v60, v29, v41\n"
"v_mfma_f32_16x16x32_bf16 v[44:47], a[24:27], v[108:111], v[44:47]\n"
"v_fma_f32 v42, -v60, v30, v42\n"
"v_fma_f32 v43, -v60, v31, v43\n"
"v_mfma_f32_16x16x32_bf16 v[44:47], a[36:39], v[112:115], v[44:47]\n"
"ds_read_b128 v[84:87], v5 offset:10368\n"
"v_fma_f32 v36, -v60, v24, v36\n"
"v_mfma_f32_16x16x32_bf16 v[48:51], a[40:43], v[112:115], 0\n"
"v_permlane32_swap_b32 v40, v42\n"
"v_mfma_f32_16x16x32_bf16 v[48:51], a[28:31], v[108:111], v[48:51]\n"
"s_waitcnt vmcnt(4)\n"
"v_cmp_ne_u64 vcc, v[16:17], v[12:13]\n"
"v_mfma_f32_16x16x32_bf16 v[48:51], a[16:19], v[104:107], v[48:51]\n"
"ds_read_b128 v[88:91], v5 offset:11520\n"
"v_fma_f32 v37, -v60, v25, v37\n"
"v_mfma_f32_16x16x32_bf16 v[48:51], a[4:7], v[100:103], v[48:51]\n"
"s_cbranch_vccnz .L_rtg1_2\n"
".L_bkg1_2:\n"
"s_add_u32 s66, s66, 890880\n"
"v_mfma_f32_16x16x32_bf16 v[52:55], a[8:11], v[100:103], 0\n"
"s_add_u32 m0, s64, 36864\n"
"v_fma_f32 v38, -v60, v26, v38\n"
"v_mfma_f32_16x16x32_bf16 v[52:55], a[20:23], v[104:107], v[52:55]\n"
"buffer_load_dwordx4 v2, s[32:35], s63 offen offset:0 sc1 lds\n"
"ds_read_b128 v[92:95], v5 offset:12672\n"
"v_mfma_f32_16x16x32_bf16 v[52:55], a[32:35], v[108:111], v[52:55]\n"
"v_permlane32_swap_b32 v41, v43\n"
"v_mfma_f32_16x16x32_bf16 v[52:55], a[44:47], v[112:115], v[52:55]\n"
"v_fma_f32 v39, -v60, v27, v39\n"
"s_waitcnt lgkmcnt(4)\n"
"v_mfma_f32_16x16x32_bf16 v[52:55], a[56:59], v[116:119], v[52:55]\n"
"ds_read_b128 v[96:99], v5 offset:13824\n"
"v_fma_f32 v32, -v60, v20, v32\n"
"v_mfma_f32_16x16x32_bf16 v[52:55], a[68:71], v[120:123], v[52:55]\n"
"v_fma_f32 v33, -v60, v21, v33\n"
"v_fma_f32 v34, -v60, v22, v34\n"
"v_mfma_f32_16x16x32_bf16 v[52:55], a[80:83], v[124:127], v[52:55]\n"
"v_fma_f32 v35, -v60, v23, v35\n"
"v_mul_f32 v64, v32, v61\n"
"v_mfma_f32_16x16x32_bf16 v[52:55], a[92:95], v[76:79], v[52:55]\n"
"ds_read_b128 v[100:103], v5 offset:14976\n"
"v_mul_f32 v65, v33, v61\n"
"v_mfma_f32_16x16x32_bf16 v[48:51], a[88:91], v[76:79], v[48:51]\n"
"v_exp_f32 v64, v64\n"
"v_mfma_f32_16x16x32_bf16 v[48:51], a[76:79], v[124:127], v[48:51]\n"
"s_waitcnt vmcnt(2)\n"
"s_waitcnt vmcnt(1)\n"
"s_barrier\n"
"v_mfma_f32_16x16x32_bf16 v[48:51], a[64:67], v[120:123], v[48:51]\n"
"ds_read_b128 v[104:107], v5 offset:16128\n"
"v_mul_f32 v66, v34, v61\n"
"v_mfma_f32_16x16x32_bf16 v[48:51], a[52:55], v[116:119], v[48:51]\n"
"v_exp_f32 v65, v65\n"
"v_mfma_f32_16x16x32_bf16 v[44:47], a[48:51], v[116:119], v[44:47]\n"
"v_exp_f32 v66, v66\n"
"v_mfma_f32_16x16x32_bf16 v[44:47], a[60:63], v[120:123], v[44:47]\n"
"buffer_load_dwordx4 v2, s[32:35], s63 offen offset:1024 sc1 lds\n"
"ds_read_b128 v[108:111], v5 offset:17280\n"
"v_mfma_f32_16x16x32_bf16 v[44:47], a[72:75], v[124:127], v[44:47]\n"
"v_mul_f32 v67, v35, v61\n"
"v_mul_f32 v68, v40, v61\n"
"v_mfma_f32_16x16x32_bf16 v[44:47], a[84:87], v[76:79], v[44:47]\n"
"v_mul_f32 v69, v41, v61\n"
"s_waitcnt lgkmcnt(4)\n"
"v_mfma_f32_16x16x32_bf16 v[44:47], a[96:99], v[80:83], v[44:47]\n"
"ds_read_b128 v[112:115], v5 offset:18432\n"
"v_mul_f32 v32, v32, v36\n"
"v_mfma_f32_16x16x32_bf16 v[44:47], a[108:111], v[84:87], v[44:47]\n"
"buffer_load_dwordx4 v2, s[32:35], s63 offen offset:2048 sc1 lds\n"
"v_mul_f32 v33, v33, v37\n"
"v_mfma_f32_16x16x32_bf16 v[44:47], a[120:123], v[88:91], v[44:47]\n"
"v_exp_f32 v67, v67\n"
"v_mfma_f32_16x16x32_bf16 v[44:47], a[132:135], v[92:95], v[44:47]\n"
"ds_read_b128 v[116:119], v5 offset:19584\n"
"v_mul_f32 v34, v34, v38\n"
"v_mfma_f32_16x16x32_bf16 v[48:51], a[136:139], v[92:95], v[48:51]\n"
"v_exp_f32 v68, v68\n"
"v_mfma_f32_16x16x32_bf16 v[48:51], a[124:127], v[88:91], v[48:51]\n"
"buffer_load_dwordx4 v2, s[32:35], s63 offen offset:3072 sc1 lds\n"
"v_mul_f32 v35, v35, v39\n"
"v_mfma_f32_16x16x32_bf16 v[48:51], a[112:115], v[84:87], v[48:51]\n"
"ds_read_b128 v[120:123], v5 offset:20736\n"
"v_mul_f32 v40, v40, v42\n"
"v_mfma_f32_16x16x32_bf16 v[48:51], a[100:103], v[80:83], v[48:51]\n"
"v_exp_f32 v69, v69\n"
"v_mfma_f32_16x16x32_bf16 v[52:55], a[104:107], v[80:83], v[52:55]\n"
"v_mul_f32 v41, v41, v43\n"
"v_fma_f32 v64, v64, v62, v62\n"
"v_mfma_f32_16x16x32_bf16 v[52:55], a[116:119], v[84:87], v[52:55]\n"
"ds_read_b128 v[124:127], v5 offset:21888\n"
"s_add_u32 m0, s64, 40960\n"
"v_mfma_f32_16x16x32_bf16 v[52:55], a[128:131], v[88:91], v[52:55]\n"
"v_rcp_f32 v64, v64\n"
"v_mfma_f32_16x16x32_bf16 v[52:55], a[140:143], v[92:95], v[52:55]\n"
"v_fma_f32 v65, v65, v62, v62\n"
"s_waitcnt lgkmcnt(4)\n"
"v_mfma_f32_16x16x32_bf16 v[52:55], a[152:155], v[96:99], v[52:55]\n"
"ds_read_b128 v[76:79], v5 offset:23040\n"
"v_fma_f32 v66, v66, v62, v62\n"
"v_mfma_f32_16x16x32_bf16 v[52:55], a[164:167], v[100:103], v[52:55]\n"
"buffer_load_dwordx4 v6, s[32:35], s63 offen offset:0 sc1 lds\n"
"v_fma_f32 v67, v67, v62, v62\n"
"v_mfma_f32_16x16x32_bf16 v[52:55], a[176:179], v[104:107], v[52:55]\n"
"v_rcp_f32 v65, v65\n"
"v_mfma_f32_16x16x32_bf16 v[52:55], a[188:191], v[108:111], v[52:55]\n"
"ds_read_b128 v[80:83], v5 offset:24192\n"
"v_fma_f32 v68, v68, v62, v62\n"
"v_mfma_f32_16x16x32_bf16 v[48:51], a[184:187], v[108:111], v[48:51]\n"
"v_rcp_f32 v66, v66\n"
"v_mfma_f32_16x16x32_bf16 v[48:51], a[172:175], v[104:107], v[48:51]\n"
"buffer_load_dwordx4 v6, s[32:35], s63 offen offset:1024 sc1 lds\n"
"v_fma_f32 v69, v69, v62, v62\n"
"v_mfma_f32_16x16x32_bf16 v[48:51], a[160:163], v[100:103], v[48:51]\n"
"ds_read_b128 v[84:87], v5 offset:25344\n"
"v_mul_f32 v32, v32, v64\n"
"v_mfma_f32_16x16x32_bf16 v[48:51], a[148:151], v[96:99], v[48:51]\n"
"v_rcp_f32 v67, v67\n"
"v_mfma_f32_16x16x32_bf16 v[44:47], a[144:147], v[96:99], v[44:47]\n"
"v_rcp_f32 v68, v68\n"
"v_mfma_f32_16x16x32_bf16 v[44:47], a[156:159], v[100:103], v[44:47]\n"
"buffer_load_dwordx2 v[12:13], v14, s[32:35], s66 offen sc1\n"
"ds_read_b128 v[88:91], v5 offset:26496\n"
"v_mfma_f32_16x16x32_bf16 v[44:47], a[168:171], v[104:107], v[44:47]\n"
"v_rcp_f32 v69, v69\n"
"v_mfma_f32_16x16x32_bf16 v[44:47], a[180:183], v[108:111], v[44:47]\n"
"v_mul_f32 v33, v33, v65\n"
"s_waitcnt lgkmcnt(4)\n"
"v_mfma_f32_16x16x32_bf16 v[44:47], a[192:195], v[112:115], v[44:47]\n"
"ds_read_b128 v[92:95], v5 offset:27648\n"
"v_mul_f32 v34, v34, v66\n"
"v_mfma_f32_16x16x32_bf16 v[44:47], a[204:207], v[116:119], v[44:47]\n"
"buffer_load_dwordx4 v6, s[32:35], s63 offen offset:2048 sc1 lds\n"
"v_mul_f32 v35, v35, v67\n"
"v_mfma_f32_16x16x32_bf16 v[44:47], a[216:219], v[120:123], v[44:47]\n"
"v_mul_f32 v40, v40, v68\n"
"v_mul_f32 v41, v41, v69\n"
"v_mfma_f32_16x16x32_bf16 v[44:47], a[228:231], v[124:127], v[44:47]\n"
"ds_read_b128 v[96:99], v5 offset:28800\n"
"v_cvt_pk_bf16_f32 v74, v40, v41\n"
"v_mfma_f32_16x16x32_bf16 v[48:51], a[232:235], v[124:127], v[48:51]\n"
"v_cvt_pk_bf16_f32 v72, v32, v33\n"
"v_cvt_pk_bf16_f32 v73, v34, v35\n"
"v_mfma_f32_16x16x32_bf16 v[48:51], a[220:223], v[120:123], v[48:51]\n"
"buffer_load_dwordx4 v6, s[32:35], s63 offen offset:3072 sc1 lds\n"
"v_mfma_f32_16x16x32_bf16 v[48:51], a[208:211], v[116:119], v[48:51]\n"
"ds_read_b128 v[100:103], v5 offset:29952\n"
"ds_read2st64_b32 v[56:57], v8 offset0:0 offset1:36\n"
"v_mfma_f32_16x16x32_bf16 v[48:51], a[196:199], v[112:115], v[48:51]\n"
"ds_read2st64_b32 v[58:59], v8 offset0:72 offset1:108\n"
"v_mfma_f32_16x16x32_bf16 v[52:55], a[200:203], v[112:115], v[52:55]\n"
"v_permlane32_swap_b32 v74, v75\n"
"v_mfma_f32_16x16x32_bf16 v[52:55], a[212:215], v[116:119], v[52:55]\n"
"ds_read_b128 v[104:107], v5 offset:31104\n"
"v_mfma_f32_16x16x32_bf16 v[52:55], a[224:227], v[120:123], v[52:55]\n"
"v_permlane16_swap_b32 v72, v74\n"
"v_mfma_f32_16x16x32_bf16 v[52:55], a[236:239], v[124:127], v[52:55]\n"
"s_add_u32 m0, s64, 45056\n"
"s_waitcnt lgkmcnt(6)\n"
"v_mfma_f32_16x16x32_bf16 v[52:55], a[248:251], v[76:79], v[52:55]\n"
"ds_read_b128 v[108:111], v5 offset:32256\n"
"v_mfma_f32_16x16x32_bf16 v[52:55], v[132:135], v[80:83], v[52:55]\n"
"buffer_load_dwordx4 v7, s[32:35], s63 offen offset:0 sc1 lds\n"
"s_add_u32 s63, s63, 890880\n"
"v_mfma_f32_16x16x32_bf16 v[52:55], v[144:147], v[84:87], v[52:55]\n"
"s_xor_b32 s64, s64, s65\n"
"v_mfma_f32_16x16x32_bf16 v[52:55], v[156:159], v[88:91], v[52:55]\n"
"ds_read_b128 v[112:115], v5 offset:33408\n"
"v_mfma_f32_16x16x32_bf16 v[48:51], v[152:155], v[88:91], v[48:51]\n"
"v_permlane16_swap_b32 v73, v75\n"
"v_mfma_f32_16x16x32_bf16 v[48:51], v[140:143], v[84:87], v[48:51]\n"
"buffer_store_dwordx4 v[72:75], v15, s[32:35], s68 offen sc1\n"
"ds_read_b32 v62, v11 offset:128\n"
"v_mfma_f32_16x16x32_bf16 v[48:51], v[128:131], v[80:83], v[48:51]\n"
"ds_read_b128 v[116:119], v5 offset:34560\n"
"s_waitcnt lgkmcnt(5)\n"
"v_add_f32 v56, v56, v57\n"
"v_mfma_f32_16x16x32_bf16 v[48:51], a[244:247], v[76:79], v[48:51]\n"
"v_add_f32 v58, v58, v59\n"
"v_mfma_f32_16x16x32_bf16 v[44:47], a[240:243], v[76:79], v[44:47]\n"
"v_add_f32 v56, v56, v58\n"
"v_mfma_f32_16x16x32_bf16 v[44:47], a[252:255], v[80:83], v[44:47]\n"
"ds_read_b128 v[120:123], v5 offset:35712\n"
"v_add_f32_dpp v56, v56, v56 row_ror:8 row_mask:0xf bank_mask:0xf\n"
"v_mfma_f32_16x16x32_bf16 v[44:47], v[136:139], v[84:87], v[44:47]\n"
"v_xor_b32 v5, 73728, v5\n"
"v_add_f32_dpp v56, v56, v56 row_ror:4 row_mask:0xf bank_mask:0xf\n"
"v_mfma_f32_16x16x32_bf16 v[44:47], v[148:151], v[88:91], v[44:47]\n"
"s_waitcnt lgkmcnt(2)\n"
"v_mfma_f32_16x16x32_bf16 v[44:47], v[160:163], v[92:95], v[44:47]\n"
"ds_read_b128 v[76:79], v4 offset:0\n"
"v_lshlrev_b32 v60, 16, v62\n"
"v_mfma_f32_16x16x32_bf16 v[44:47], v[172:175], v[96:99], v[44:47]\n"
"v_and_b32 v62, 0xffff0000, v62\n"
"v_add_f32_dpp v56, v56, v56 row_ror:2 row_mask:0xf bank_mask:0xf\n"
"v_mfma_f32_16x16x32_bf16 v[44:47], v[184:187], v[100:103], v[44:47]\n"
"v_rcp_f32 v61, v62\n"
"v_mfma_f32_16x16x32_bf16 v[44:47], v[196:199], v[104:107], v[44:47]\n"
"ds_read_b128 v[80:83], v4 offset:1152\n"
"v_mul_f32 v62, v62, v62\n"
"v_mfma_f32_16x16x32_bf16 v[48:51], v[200:203], v[104:107], v[48:51]\n"
"v_mul_f32 v61, 0xbfb8aa3b, v61\n"
"v_mul_f32 v56, 0x3a800000, v56\n"
"v_mfma_f32_16x16x32_bf16 v[48:51], v[188:191], v[100:103], v[48:51]\n"
"v_mul_f32 v57, v56, v56\n"
"v_mfma_f32_16x16x32_bf16 v[48:51], v[176:179], v[96:99], v[48:51]\n"
"ds_read_b128 v[84:87], v4 offset:2304\n"
"v_sub_f32_dpp v57, v56, v57 quad_perm:[1,1,3,3] row_mask:0xf bank_mask:0xf\n"
"v_mfma_f32_16x16x32_bf16 v[48:51], v[164:167], v[92:95], v[48:51]\n"
"v_max_f32 v57, 0, v57\n"
"v_mfma_f32_16x16x32_bf16 v[52:55], v[168:171], v[92:95], v[52:55]\n"
"v_cvt_pk_bf16_f32 v57, 0, v57\n"
"v_mfma_f32_16x16x32_bf16 v[52:55], v[180:183], v[96:99], v[52:55]\n"
"ds_read_b128 v[88:91], v4 offset:3456\n"
"v_add_f32 v57, 0x3727c5ac, v57\n"
"v_mfma_f32_16x16x32_bf16 v[52:55], v[192:195], v[100:103], v[52:55]\n"
"v_cvt_pk_bf16_f32 v57, 0, v57\n"
"v_mfma_f32_16x16x32_bf16 v[52:55], v[204:207], v[104:107], v[52:55]\n"
"s_waitcnt lgkmcnt(4)\n"
"v_mfma_f32_16x16x32_bf16 v[52:55], v[216:219], v[108:111], v[52:55]\n"
"ds_read_b128 v[92:95], v4 offset:4608\n"
"v_mfma_f32_16x16x32_bf16 v[52:55], v[228:231], v[112:115], v[52:55]\n"
"v_sqrt_f32 v57, v57\n"
"v_mfma_f32_16x16x32_bf16 v[52:55], v[240:243], v[116:119], v[52:55]\n"
"v_cvt_pk_bf16_f32 v57, v56, v57\n"
"v_mfma_f32_16x16x32_bf16 v[52:55], v[252:255], v[120:123], v[52:55]\n"
"ds_read_b128 v[96:99], v4 offset:5760\n"
"ds_write_b32 v10, v57 offset:0\n"
"v_mfma_f32_16x16x32_bf16 v[48:51], v[248:251], v[120:123], v[48:51]\n"
"v_mfma_f32_16x16x32_bf16 v[48:51], v[236:239], v[116:119], v[48:51]\n"
"v_mfma_f32_16x16x32_bf16 v[48:51], v[224:227], v[112:115], v[48:51]\n"
"ds_read_b128 v[100:103], v4 offset:6912\n"
"v_mfma_f32_16x16x32_bf16 v[48:51], v[212:215], v[108:111], v[48:51]\n"
"v_mfma_f32_16x16x32_bf16 v[44:47], v[208:211], v[108:111], v[44:47]\n"
"v_mfma_f32_16x16x32_bf16 v[44:47], v[220:223], v[112:115], v[44:47]\n"
"ds_read_b128 v[104:107], v4 offset:8064\n"
"v_mfma_f32_16x16x32_bf16 v[44:47], v[232:235], v[116:119], v[44:47]\n"
"s_sub_u32 s62, s62, 1\n"
"v_mfma_f32_16x16x32_bf16 v[44:47], v[244:247], v[120:123], v[44:47]\n"
"s_cbranch_scc0 .L_gl_2\n"
"s_nop 1\n"
"s_add_u32 s68, s68, 890880\n"
"s_waitcnt vmcnt(0)\n"
"s_barrier\n"
"buffer_store_dwordx2 v[16:17], v2, s[92:95], s68 idxen sc1\n"
"v_fma_f32 v52, -v60, v28, v52\n"
"v_fma_f32 v53, -v60, v29, v53\n"
"v_fma_f32 v54, -v60, v30, v54\n"
"v_fma_f32 v55, -v60, v31, v55\n"
"v_fma_f32 v48, -v60, v24, v48\n"
"v_fma_f32 v49, -v60, v25, v49\n"
"v_fma_f32 v50, -v60, v26, v50\n"
"v_fma_f32 v51, -v60, v27, v51\n"
"v_fma_f32 v44, -v60, v20, v44\n"
"v_fma_f32 v45, -v60, v21, v45\n"
"v_fma_f32 v46, -v60, v22, v46\n"
"v_fma_f32 v47, -v60, v23, v47\n"
"v_permlane32_swap_b32 v52, v54\n"
"v_permlane32_swap_b32 v53, v55\n"
"v_mul_f32 v64, v44, v61\n"
"v_mul_f32 v65, v45, v61\n"
"v_mul_f32 v66, v46, v61\n"
"v_mul_f32 v67, v47, v61\n"
"v_mul_f32 v68, v52, v61\n"
"v_mul_f32 v69, v53, v61\n"
"v_exp_f32 v64, v64\n"
"v_exp_f32 v65, v65\n"
"v_mul_f32 v44, v44, v48\n"
"v_mul_f32 v45, v45, v49\n"
"v_exp_f32 v66, v66\n"
"v_exp_f32 v67, v67\n"
"v_mul_f32 v46, v46, v50\n"
"v_mul_f32 v47, v47, v51\n"
"v_exp_f32 v68, v68\n"
"v_exp_f32 v69, v69\n"
"v_mul_f32 v52, v52, v54\n"
"v_mul_f32 v53, v53, v55\n"
"v_fma_f32 v64, v64, v62, v62\n"
"v_fma_f32 v65, v65, v62, v62\n"
"v_rcp_f32 v64, v64\n"
"v_rcp_f32 v65, v65\n"
"v_fma_f32 v66, v66, v62, v62\n"
"v_fma_f32 v67, v67, v62, v62\n"
"v_rcp_f32 v66, v66\n"
"v_rcp_f32 v67, v67\n"
"v_fma_f32 v68, v68, v62, v62\n"
"v_fma_f32 v69, v69, v62, v62\n"
"v_rcp_f32 v68, v68\n"
"v_rcp_f32 v69, v69\n"
"v_mul_f32 v44, v44, v64\n"
"v_mul_f32 v45, v45, v65\n"
"v_mul_f32 v46, v46, v66\n"
"v_mul_f32 v47, v47, v67\n"
"v_mul_f32 v52, v52, v68\n"
"v_mul_f32 v53, v53, v69\n"
"v_cvt_pk_bf16_f32 v74, v52, v53\n"
"v_cvt_pk_bf16_f32 v72, v44, v45\n"
"v_cvt_pk_bf16_f32 v73, v46, v47\n"
"v_permlane32_swap_b32 v74, v75\n"
"s_nop 1\n"
"v_permlane16_swap_b32 v72, v74\n"
"v_permlane16_swap_b32 v73, v75\n"
"buffer_store_dwordx4 v[72:75], v15, s[32:35], s68 offen sc1\n"
"s_add_u32 s68, s68, 890880\n"
"s_waitcnt vmcnt(0)\n"
"s_barrier\n"
"buffer_store_dwordx2 v[16:17], v2, s[92:95], s68 idxen sc1\n"
"s_branch .L_end\n"
".L_gt0_2:\n"
"s_nop 1\n"
"s_add_u32 s68, s68, 890880\n"
"s_waitcnt vmcnt(0)\n"
"s_barrier\n"
"buffer_store_dwordx2 v[16:17], v2, s[92:95], s68 idxen sc1\n"
"v_fma_f32 v40, -v60, v28, v40\n"
"v_fma_f32 v41, -v60, v29, v41\n"
"v_fma_f32 v42, -v60, v30, v42\n"
"v_fma_f32 v43, -v60, v31, v43\n"
"v_fma_f32 v36, -v60, v24, v36\n"
"v_fma_f32 v37, -v60, v25, v37\n"
"v_fma_f32 v38, -v60, v26, v38\n"
"v_fma_f32 v39, -v60, v27, v39\n"
"v_fma_f32 v32, -v60, v20, v32\n"
"v_fma_f32 v33, -v60, v21, v33\n"
"v_fma_f32 v34, -v60, v22, v34\n"
"v_fma_f32 v35, -v60, v23, v35\n"
"v_permlane32_swap_b32 v40, v42\n"
"v_permlane32_swap_b32 v41, v43\n"
"v_mul_f32 v64, v32, v61\n"
"v_mul_f32 v65, v33, v61\n"
"v_mul_f32 v66, v34, v61\n"
"v_mul_f32 v67, v35, v61\n"
"v_mul_f32 v68, v40, v61\n"
"v_mul_f32 v69, v41, v61\n"
"v_exp_f32 v64, v64\n"
"v_exp_f32 v65, v65\n"
"v_mul_f32 v32, v32, v36\n"
"v_mul_f32 v33, v33, v37\n"
"v_exp_f32 v66, v66\n"
"v_exp_f32 v67, v67\n"
"v_mul_f32 v34, v34, v38\n"
"v_mul_f32 v35, v35, v39\n"
"v_exp_f32 v68, v68\n"
"v_exp_f32 v69, v69\n"
"v_mul_f32 v40, v40, v42\n"
"v_mul_f32 v41, v41, v43\n"
"v_fma_f32 v64, v64, v62, v62\n"
"v_fma_f32 v65, v65, v62, v62\n"
"v_rcp_f32 v64, v64\n"
"v_rcp_f32 v65, v65\n"
"v_fma_f32 v66, v66, v62, v62\n"
"v_fma_f32 v67, v67, v62, v62\n"
"v_rcp_f32 v66, v66\n"
"v_rcp_f32 v67, v67\n"
"v_fma_f32 v68, v68, v62, v62\n"
"v_fma_f32 v69, v69, v62, v62\n"
"v_rcp_f32 v68, v68\n"
"v_rcp_f32 v69, v69\n"
"v_mul_f32 v32, v32, v64\n"
"v_mul_f32 v33, v33, v65\n"
"v_mul_f32 v34, v34, v66\n"
"v_mul_f32 v35, v35, v67\n"
"v_mul_f32 v40, v40, v68\n"
"v_mul_f32 v41, v41, v69\n"
"v_cvt_pk_bf16_f32 v74, v40, v41\n"
"v_cvt_pk_bf16_f32 v72, v32, v33\n"
"v_cvt_pk_bf16_f32 v73, v34, v35\n"
"v_permlane32_swap_b32 v74, v75\n"
"s_nop 1\n"
"v_permlane16_swap_b32 v72, v74\n"
"v_permlane16_swap_b32 v73, v75\n"
"buffer_store_dwordx4 v[72:75], v15, s[32:35], s68 offen sc1\n"
"s_add_u32 s68, s68, 890880\n"
"s_waitcnt vmcnt(0)\n"
"s_barrier\n"
"buffer_store_dwordx2 v[16:17], v2, s[92:95], s68 idxen sc1\n"
"s_branch .L_end\n"
".p2align 6\n"
".L_gv3:\n"
".L_gl_3:\n"
"s_waitcnt lgkmcnt(5)\n"
"v_mfma_f32_16x16x32_bf16 v[32:35], a[0:3], v[76:79], 0\n"
"ds_read_b128 v[108:111], v4 offset:9216\n"
"s_add_u32 s68, s68, 890880\n"
"v_mfma_f32_16x16x32_bf16 v[32:35], a[12:15], v[80:83], v[32:35]\n"
"v_fma_f32 v52, -v60, v28, v52\n"
"v_fma_f32 v53, -v60, v29, v53\n"
"v_mfma_f32_16x16x32_bf16 v[32:35], a[24:27], v[84:87], v[32:35]\n"
"v_fma_f32 v54, -v60, v30, v54\n"
"v_fma_f32 v55, -v60, v31, v55\n"
"v_mfma_f32_16x16x32_bf16 v[32:35], a[36:39], v[88:91], v[32:35]\n"
"ds_read_b128 v[112:115], v4 offset:10368\n"
"v_fma_f32 v48, -v60, v24, v48\n"
"v_mfma_f32_16x16x32_bf16 v[36:39], a[40:43], v[88:91], 0\n"
"v_permlane32_swap_b32 v52, v54\n"
"v_mfma_f32_16x16x32_bf16 v[36:39], a[28:31], v[84:87], v[36:39]\n"
"s_waitcnt vmcnt(4)\n"
"v_cmp_ne_u64 vcc, v[16:17], v[12:13]\n"
"v_mfma_f32_16x16x32_bf16 v[36:39], a[16:19], v[80:83], v[36:39]\n"
"ds_read_b128 v[116:119], v4 offset:11520\n"
"v_fma_f32 v49, -v60, v25, v49\n"
"v_mfma_f32_16x16x32_bf16 v[36:39], a[4:7], v[76:79], v[36:39]\n"
"s_cbranch_vccnz .L_rtg0_3\n"
".L_bkg0_3:\n"
"s_add_u32 s66, s66, 890880\n"
"v_mfma_f32_16x16x32_bf16 v[40:43], a[8:11], v[76:79], 0\n"
"s_add_u32 m0, s64, 0\n"
"v_fma_f32 v50, -v60, v26, v50\n"
"v_mfma_f32_16x16x32_bf16 v[40:43], a[20:23], v[80:83], v[40:43]\n"
"buffer_load_dwordx4 v2, s[32:35], s63 offen offset:0 sc1 lds\n"
"ds_read_b128 v[120:123], v4 offset:12672\n"
"v_mfma_f32_16x16x32_bf16 v[40:43], a[32:35], v[84:87], v[40:43]\n"
"v_permlane32_swap_b32 v53, v55\n"
"v_mfma_f32_16x16x32_bf16 v[40:43], a[44:47], v[88:91], v[40:43]\n"
"v_fma_f32 v51, -v60, v27, v51\n"
"s_waitcnt lgkmcnt(4)\n"
"v_mfma_f32_16x16x32_bf16 v[40:43], a[56:59], v[92:95], v[40:43]\n"
"ds_read_b128 v[124:127], v4 offset:13824\n"
"v_fma_f32 v44, -v60, v20, v44\n"
"v_mfma_f32_16x16x32_bf16 v[40:43], a[68:71], v[96:99], v[40:43]\n"
"v_fma_f32 v45, -v60, v21, v45\n"
"v_fma_f32 v46, -v60, v22, v46\n"
"v_mfma_f32_16x16x32_bf16 v[40:43], a[80:83], v[100:103], v[40:43]\n"
"v_fma_f32 v47, -v60, v23, v47\n"
"v_mul_f32 v64, v44, v61\n"
"v_mfma_f32_16x16x32_bf16 v[40:43], a[92:95], v[104:107], v[40:43]\n"
"ds_read_b128 v[76:79], v4 offset:14976\n"
"v_mul_f32 v65, v45, v61\n"
"v_mfma_f32_16x16x32_bf16 v[36:39], a[88:91], v[104:107], v[36:39]\n"
"s_waitcnt vmcnt(2)\n"
"s_waitcnt vmcnt(1)\n"
"s_barrier\n"
"v_mfma_f32_16x16x32_bf16 v[36:39], a[76:79], v[100:103], v[36:39]\n"
"buffer_load_dwordx4 v2, s[32:35], s63 offen offset:1024 sc1 lds\n"
"v_mul_f32 v66, v46, v61\n"
"v_mfma_f32_16x16x32_bf16 v[36:39], a[64:67], v[96:99], v[36:39]\n"
"ds_read_b128 v[80:83], v4 offset:16128\n"
"v_mul_f32 v67, v47, v61\n"
"v_mfma_f32_16x16x32_bf16 v[36:39], a[52:55], v[92:95], v[36:39]\n"
"v_exp_f32 v64, v64\n"
"v_mfma_f32_16x16x32_bf16 v[32:35], a[48:51], v[92:95], v[32:35]\n"
"v_exp_f32 v65, v65\n"
"v_mfma_f32_16x16x32_bf16 v[32:35], a[60:63], v[96:99], v[32:35]\n"
"buffer_store_dwordx2 v[16:17], v2, s[92:95], s68 idxen sc1\n"
"ds_read_b128 v[84:87], v4 offset:17280\n"
"v_mfma_f32_16x16x32_bf16 v[32:35], a[72:75], v[100:103], v[32:35]\n"
"v_exp_f32 v66, v66\n"
"v_mfma_f32_16x16x32_bf16 v[32:35], a[84:87], v[104:107], v[32:35]\n"
"v_mul_f32 v68, v52, v61\n"
"s_waitcnt lgkmcnt(4)\n"
"v_mfma_f32_16x16x32_bf16 v[32:35], a[96:99], v[108:111], v[32:35]\n"
"ds_read_b128 v[88:91], v4 offset:18432\n"
"v_mul_f32 v69, v53, v61\n"
"v_mfma_f32_16x16x32_bf16 v[32:35], a[108:111], v[112:115], v[32:35]\n"
"buffer_load_dwordx4 v2, s[32:35], s63 offen offset:2048 sc1 lds\n"
"v_mul_f32 v44, v44, v48\n"
"v_mfma_f32_16x16x32_bf16 v[32:35], a[120:123], v[116:119], v[32:35]\n"
"v_exp_f32 v67, v67\n"
"v_mfma_f32_16x16x32_bf16 v[32:35], a[132:135], v[120:123], v[32:35]\n"
"ds_read_b128 v[92:95], v4 offset:19584\n"
"v_mul_f32 v45, v45, v49\n"
"v_mfma_f32_16x16x32_bf16 v[36:39], a[136:139], v[120:123], v[36:39]\n"
"v_exp_f32 v68, v68\n"
"v_mfma_f32_16x16x32_bf16 v[36:39], a[124:127], v[116:119], v[36:39]\n"
"buffer_load_dwordx4 v2, s[32:35], s63 offen offset:3072 sc1 lds\n"
"v_mul_f32 v46, v46, v50\n"
"v_mfma_f32_16x16x32_bf16 v[36:39], a[112:115], v[112:115], v[36:39]\n"
"ds_read_b128 v[96:99], v4 offset:20736\n"
"v_mul_f32 v47, v47, v51\n"
"v_mfma_f32_16x16x32_bf16 v[36:39], a[100:103], v[108:111], v[36:39]\n"
"v_exp_f32 v69, v69\n"
"v_mfma_f32_16x16x32_bf16 v[40:43], a[104:107], v[108:111], v[40:43]\n"
"v_mul_f32 v52, v52, v54\n"
"v_mul_f32 v53, v53, v55\n"
"v_mfma_f32_16x16x32_bf16 v[40:43], a[116:119], v[112:115], v[40:43]\n"
"ds_read_b128 v[100:103], v4 offset:21888\n"
"s_add_u32 m0, s64, 4096\n"
"v_mfma_f32_16x16x32_bf16 v[40:43], a[128:131], v[116:119], v[40:43]\n"
"v_fma_f32 v64, v64, v62, v62\n"
"v_fma_f32 v65, v65, v62, v62\n"
"v_mfma_f32_16x16x32_bf16 v[40:43], a[140:143], v[120:123], v[40:43]\n"
"v_fma_f32 v66, v66, v62, v62\n"
"s_waitcnt lgkmcnt(4)\n"
"v_mfma_f32_16x16x32_bf16 v[40:43], a[152:155], v[124:127], v[40:43]\n"
"ds_read_b128 v[104:107], v4 offset:23040\n"
"v_fma_f32 v67, v67, v62, v62\n"
"v_mfma_f32_16x16x32_bf16 v[40:43], a[164:167], v[76:79], v[40:43]\n"
"buffer_load_dwordx4 v6, s[32:35], s63 offen offset:0 sc1 lds\n"
"v_fma_f32 v68, v68, v62, v62\n"
"v_mfma_f32_16x16x32_bf16 v[40:43], a[176:179], v[80:83], v[40:43]\n"
"v_rcp_f32 v64, v64\n"
"v_mfma_f32_16x16x32_bf16 v[40:43], a[188:191], v[84:87], v[40:43]\n"
"ds_read_b128 v[108:111], v4 offset:24192\n"
"v_fma_f32 v69, v69, v62, v62\n"
"v_mfma_f32_16x16x32_bf16 v[36:39], a[184:187], v[84:87], v[36:39]\n"
"v_rcp_f32 v65, v65\n"
"v_mfma_f32_16x16x32_bf16 v[36:39], a[172:175], v[80:83], v[36:39]\n"
"buffer_load_dwordx4 v6, s[32:35], s63 offen offset:1024 sc1 lds\n"
"v_mul_f32 v44, v44, v64\n"
"v_mfma_f32_16x16x32_bf16 v[36:39], a[160:163], v[76:79], v[36:39]\n"
"ds_read_b128 v[112:115], v4 offset:25344\n"
"v_mul_f32 v45, v45, v65\n"
"v_mfma_f32_16x16x32_bf16 v[36:39], a[148:151], v[124:127], v[36:39]\n"
"v_rcp_f32 v66, v66\n"
"v_mfma_f32_16x16x32_bf16 v[32:35], a[144:147], v[124:127], v[32:35]\n"
"v_rcp_f32 v67, v67\n"
"v_mfma_f32_16x16x32_bf16 v[32:35], a[156:159], v[76:79], v[32:35]\n"
"buffer_load_dwordx2 v[12:13], v14, s[32:35], s66 offen sc1\n"
"ds_read_b128 v[116:119], v4 offset:26496\n"
"v_mfma_f32_16x16x32_bf16 v[32:35], a[168:171], v[80:83], v[32:35]\n"
"v_rcp_f32 v68, v68\n"
"v_mfma_f32_16x16x32_bf16 v[32:35], a[180:183], v[84:87], v[32:35]\n"
"v_mul_f32 v46, v46, v66\n"
"s_waitcnt lgkmcnt(4)\n"
"v_mfma_f32_16x16x32_bf16 v[32:35], a[192:195], v[88:91], v[32:35]\n"
"ds_read_b128 v[120:123], v4 offset:27648\n"
"v_mul_f32 v47, v47, v67\n"
"v_mfma_f32_16x16x32_bf16 v[32:35], a[204:207], v[92:95], v[32:35]\n"
"buffer_load_dwordx4 v6, s[32:35], s63 offen offset:2048 sc1 lds\n"
"v_mul_f32 v52, v52, v68\n"
"v_mfma_f32_16x16x32_bf16 v[32:35], a[216:219], v[96:99], v[32:35]\n"
"v_rcp_f32 v69, v69\n"
"v_mfma_f32_16x16x32_bf16 v[32:35], a[228:231], v[100:103], v[32:35]\n"
"ds_read_b128 v[124:127], v4 offset:28800\n"
"v_mul_f32 v53, v53, v69\n"
"v_mfma_f32_16x16x32_bf16 v[36:39], a[232:235], v[100:103], v[36:39]\n"
"v_cvt_pk_bf16_f32 v74, v52, v53\n"
"v_cvt_pk_bf16_f32 v72, v44, v45\n"
"v_mfma_f32_16x16x32_bf16 v[36:39], a[220:223], v[96:99], v[36:39]\n"
"buffer_load_dwordx4 v6, s[32:35], s63 offen offset:3072 sc1 lds\n"
"v_cvt_pk_bf16_f32 v73, v46, v47\n"
"v_mfma_f32_16x16x32_bf16 v[36:39], a[208:211], v[92:95], v[36:39]\n"
"ds_read_b128 v[76:79], v4 offset:29952\n"
"ds_read2st64_b32 v[56:57], v8 offset0:144 offset1:180\n"
"v_mfma_f32_16x16x32_bf16 v[36:39], a[196:199], v[88:91], v[36:39]\n"
"ds_read2st64_b32 v[58:59], v8 offset0:216 offset1:252\n"
"v_xor_b32 v8, v9, v8\n"
"v_mfma_f32_16x16x32_bf16 v[40:43], a[200:203], v[88:91], v[40:43]\n"
"v_permlane32_swap_b32 v74, v75\n"
"v_mfma_f32_16x16x32_bf16 v[40:43], a[212:215], v[92:95], v[40:43]\n"
"ds_read_b128 v[80:83], v4 offset:31104\n"
"v_mfma_f32_16x16x32_bf16 v[40:43], a[224:227], v[96:99], v[40:43]\n"
"v_permlane16_swap_b32 v72, v74\n"
"v_mfma_f32_16x16x32_bf16 v[40:43], a[236:239], v[100:103], v[40:43]\n"
"s_add_u32 m0, s64, 8192\n"
"s_waitcnt lgkmcnt(6)\n"
"v_mfma_f32_16x16x32_bf16 v[40:43], a[248:251], v[104:107], v[40:43]\n"
"ds_read_b128 v[84:87], v4 offset:32256\n"
"v_mfma_f32_16x16x32_bf16 v[40:43], v[132:135], v[108:111], v[40:43]\n"
"buffer_load_dwordx4 v7, s[32:35], s63 offen offset:0 sc1 lds\n"
"s_add_u32 s63, s63, 890880\n"
"v_mfma_f32_16x16x32_bf16 v[40:43], v[144:147], v[112:115], v[40:43]\n"
"v_permlane16_swap_b32 v73, v75\n"
"v_mfma_f32_16x16x32_bf16 v[40:43], v[156:159], v[116:119], v[40:43]\n"
"ds_read_b128 v[88:91], v4 offset:33408\n"
"v_mfma_f32_16x16x32_bf16 v[36:39], v[152:155], v[116:119], v[36:39]\n"
"v_mfma_f32_16x16x32_bf16 v[36:39], v[140:143], v[112:115], v[36:39]\n"
"buffer_store_dwordx4 v[72:75], v15, s[32:35], s68 offen sc1\n"
"ds_read_b32 v62, v11 offset:0\n"
"v_mfma_f32_16x16x32_bf16 v[36:39], v[128:131], v[108:111], v[36:39]\n"
"ds_read_b128 v[92:95], v4 offset:34560\n"
"s_waitcnt lgkmcnt(5)\n"
"v_add_f32 v56, v56, v57\n"
"v_mfma_f32_16x16x32_bf16 v[36:39], a[244:247], v[104:107], v[36:39]\n"
"v_add_f32 v58, v58, v59\n"
"v_mfma_f32_16x16x32_bf16 v[32:35], a[240:243], v[104:107], v[32:35]\n"
"v_add_f32 v56, v56, v58\n"
"v_mfma_f32_16x16x32_bf16 v[32:35], a[252:255], v[108:111], v[32:35]\n"
"ds_read_b128 v[96:99], v4 offset:35712\n"
"v_add_f32_dpp v56, v56, v56 row_ror:8 row_mask:0xf bank_mask:0xf\n"
"v_mfma_f32_16x16x32_bf16 v[32:35], v[136:139], v[112:115], v[32:35]\n"
"v_xor_b32 v4, 73728, v4\n"
"v_add_f32_dpp v56, v56, v56 row_ror:4 row_mask:0xf bank_mask:0xf\n"
"v_mfma_f32_16x16x32_bf16 v[32:35], v[148:151], v[116:119], v[32:35]\n"
"s_waitcnt lgkmcnt(2)\n"
"v_mfma_f32_16x16x32_bf16 v[32:35], v[160:163], v[120:123], v[32:35]\n"
"ds_read_b128 v[100:103], v5 offset:0\n"
"v_lshlrev_b32 v60, 16, v62\n"
"v_mfma_f32_16x16x32_bf16 v[32:35], v[172:175], v[124:127], v[32:35]\n"
"v_and_b32 v62, 0xffff0000, v62\n"
"v_add_f32_dpp v56, v56, v56 row_ror:2 row_mask:0xf bank_mask:0xf\n"
"v_mfma_f32_16x16x32_bf16 v[32:35], v[184:187], v[76:79], v[32:35]\n"
"v_rcp_f32 v61, v62\n"
"v_mfma_f32_16x16x32_bf16 v[32:35], v[196:199], v[80:83], v[32:35]\n"
"ds_read_b128 v[104:107], v5 offset:1152\n"
"v_mul_f32 v62, v62, v62\n"
"v_mfma_f32_16x16x32_bf16 v[36:39], v[200:203], v[80:83], v[36:39]\n"
"v_mul_f32 v61, 0xbfb8aa3b, v61\n"
"v_mul_f32 v56, 0x3a800000, v56\n"
"v_mfma_f32_16x16x32_bf16 v[36:39], v[188:191], v[76:79], v[36:39]\n"
"v_mul_f32 v57, v56, v56\n"
"v_mfma_f32_16x16x32_bf16 v[36:39], v[176:179], v[124:127], v[36:39]\n"
"ds_read_b128 v[108:111], v5 offset:2304\n"
"v_sub_f32_dpp v57, v56, v57 quad_perm:[1,1,3,3] row_mask:0xf bank_mask:0xf\n"
"v_mfma_f32_16x16x32_bf16 v[36:39], v[164:167], v[120:123], v[36:39]\n"
"v_max_f32 v57, 0, v57\n"
"v_mfma_f32_16x16x32_bf16 v[40:43], v[168:171], v[120:123], v[40:43]\n"
"v_cvt_pk_bf16_f32 v57, 0, v57\n"
"v_mfma_f32_16x16x32_bf16 v[40:43], v[180:183], v[124:127], v[40:43]\n"
"ds_read_b128 v[112:115], v5 offset:3456\n"
"v_add_f32 v57, 0x3727c5ac, v57\n"
"v_mfma_f32_16x16x32_bf16 v[40:43], v[192:195], v[76:79], v[40:43]\n"
"v_cvt_pk_bf16_f32 v57, 0, v57\n"
"v_mfma_f32_16x16x32_bf16 v[40:43], v[204:207], v[80:83], v[40:43]\n"
"s_waitcnt lgkmcnt(4)\n"
"v_mfma_f32_16x16x32_bf16 v[40:43], v[216:219], v[84:87], v[40:43]\n"
"ds_read_b128 v[116:119], v5 offset:4608\n"
"v_mfma_f32_16x16x32_bf16 v[40:43], v[228:231], v[88:91], v[40:43]\n"
"v_sqrt_f32 v57, v57\n"
"v_mfma_f32_16x16x32_bf16 v[40:43], v[240:243], v[92:95], v[40:43]\n"
"v_cvt_pk_bf16_f32 v57, v56, v57\n"
"v_mfma_f32_16x16x32_bf16 v[40:43], v[252:255], v[96:99], v[40:43]\n"
"ds_read_b128 v[120:123], v5 offset:5760\n"
"ds_write_b32 v10, v57 offset:128\n"
"v_mfma_f32_16x16x32_bf16 v[36:39], v[248:251], v[96:99], v[36:39]\n"
"v_mfma_f32_16x16x32_bf16 v[36:39], v[236:239], v[92:95], v[36:39]\n"
"v_mfma_f32_16x16x32_bf16 v[36:39], v[224:227], v[88:91], v[36:39]\n"
"ds_read_b128 v[124:127], v5 offset:6912\n"
"v_mfma_f32_16x16x32_bf16 v[36:39], v[212:215], v[84:87], v[36:39]\n"
"v_mfma_f32_16x16x32_bf16 v[32:35], v[208:211], v[84:87], v[32:35]\n"
"v_mfma_f32_16x16x32_bf16 v[32:35], v[220:223], v[88:91], v[32:35]\n"
"ds_read_b128 v[76:79], v5 offset:8064\n"
"v_mfma_f32_16x16x32_bf16 v[32:35], v[232:235], v[92:95], v[32:35]\n"
"s_sub_u32 s62, s62, 1\n"
"v_mfma_f32_16x16x32_bf16 v[32:35], v[244:247], v[96:99], v[32:35]\n"
"s_cbranch_scc1 .L_gt0_3\n"
"s_waitcnt lgkmcnt(5)\n"
"v_mfma_f32_16x16x32_bf16 v[44:47], a[0:3], v[100:103], 0\n"
"ds_read_b128 v[80:83], v5 offset:9216\n"
"s_add_u32 s68, s68, 890880\n"
"v_mfma_f32_16x16x32_bf16 v[44:47], a[12:15], v[104:107], v[44:47]\n"
"v_fma_f32 v40, -v60, v28, v40\n"
"v_fma_f32 v41, -v60, v29, v41\n"
"v_mfma_f32_16x16x32_bf16 v[44:47], a[24:27], v[108:111], v[44:47]\n"
"v_fma_f32 v42, -v60, v30, v42\n"
"v_fma_f32 v43, -v60, v31, v43\n"
"v_mfma_f32_16x16x32_bf16 v[44:47], a[36:39], v[112:115], v[44:47]\n"
"ds_read_b128 v[84:87], v5 offset:10368\n"
"v_fma_f32 v36, -v60, v24, v36\n"
"v_mfma_f32_16x16x32_bf16 v[48:51], a[40:43], v[112:115], 0\n"
"v_permlane32_swap_b32 v40, v42\n"
"v_mfma_f32_16x16x32_bf16 v[48:51], a[28:31], v[108:111], v[48:51]\n"
"s_waitcnt vmcnt(4)\n"
"v_cmp_ne_u64 vcc, v[16:17], v[12:13]\n"
"v_mfma_f32_16x16x32_bf16 v[48:51], a[16:19], v[104:107], v[48:51]\n"
"ds_read_b128 v[88:91], v5 offset:11520\n"
"v_fma_f32 v37, -v60, v25, v37\n"
"v_mfma_f32_16x16x32_bf16 v[48:51], a[4:7], v[100:103], v[48:51]\n"
"s_cbranch_vccnz .L_rtg1_3\n"
".L_bkg1_3:\n"
"s_add_u32 s66, s66, 890880\n"
"v_mfma_f32_16x16x32_bf16 v[52:55], a[8:11], v[100:103], 0\n"
"s_add_u32 m0, s64, 36864\n"
"v_fma_f32 v38, -v60, v26, v38\n"
"v_mfma_f32_16x16x32_bf16 v[52:55], a[20:23], v[104:107], v[52:55]\n"
"buffer_load_dwordx4 v2, s[32:35], s63 offen offset:0 sc1 lds\n"
"ds_read_b128 v[92:95], v5 offset:12672\n"
"v_mfma_f32_16x16x32_bf16 v[52:55], a[32:35], v[108:111], v[52:55]\n"
"v_permlane32_swap_b32 v41, v43\n"
"v_mfma_f32_16x16x32_bf16 v[52:55], a[44:47], v[112:115], v[52:55]\n"
"v_fma_f32 v39, -v60, v27, v39\n"
"s_waitcnt lgkmcnt(4)\n"
"v_mfma_f32_16x16x32_bf16 v[52:55], a[56:59], v[116:119], v[52:55]\n"
"ds_read_b128 v[96:99], v5 offset:13824\n"
"v_fma_f32 v32, -v60, v20, v32\n"
"v_mfma_f32_16x16x32_bf16 v[52:55], a[68:71], v[120:123], v[52:55]\n"
"v_fma_f32 v33, -v60, v21, v33\n"
"v_fma_f32 v34, -v60, v22, v34\n"
"v_mfma_f32_16x16x32_bf16 v[52:55], a[80:83], v[124:127], v[52:55]\n"
"v_fma_f32 v35, -v60, v23, v35\n"
"v_mul_f32 v64, v32, v61\n"
"v_mfma_f32_16x16x32_bf16 v[52:55], a[92:95], v[76:79], v[52:55]\n"
"ds_read_b128 v[100:103], v5 offset:14976\n"
"v_mul_f32 v65, v33, v61\n"
"v_mfma_f32_16x16x32_bf16 v[48:51], a[88:91], v[76:79], v[48:51]\n"
"s_waitcnt vmcnt(2)\n"
"s_waitcnt vmcnt(1)\n"
"s_barrier\n"
"v_mfma_f32_16x16x32_bf16 v[48:51], a[76:79], v[124:127], v[48:51]\n"
"buffer_load_dwordx4 v2, s[32:35], s63 offen offset:1024 sc1 lds\n"
"v_mul_f32 v66, v34, v61\n"
"v_mfma_f32_16x16x32_bf16 v[48:51], a[64:67], v[120:123], v[48:51]\n"
"ds_read_b128 v[104:107], v5 offset:16128\n"
"v_mul_f32 v67, v35, v61\n"
"v_mfma_f32_16x16x32_bf16 v[48:51], a[52:55], v[116:119], v[48:51]\n"
"v_exp_f32 v64, v64\n"
"v_mfma_f32_16x16x32_bf16 v[44:47], a[48:51], v[116:119], v[44:47]\n"
"v_exp_f32 v65, v65\n"
"v_mfma_f32_16x16x32_bf16 v[44:47], a[60:63], v[120:123], v[44:47]\n"
"buffer_store_dwordx2 v[16:17], v2, s[92:95], s68 idxen sc1\n"
"ds_read_b128 v[108:111], v5 offset:17280\n"
"v_mfma_f32_16x16x32_bf16 v[44:47], a[72:75], v[124:127], v[44:47]\n"
"v_exp_f32 v66, v66\n"
"v_mfma_f32_16x16x32_bf16 v[44:47], a[84:87], v[76:79], v[44:47]\n"
"v_mul_f32 v68, v40, v61\n"
"s_waitcnt lgkmcnt(4)\n"
"v_mfma_f32_16x16x32_bf16 v[44:47], a[96:99], v[80:83], v[44:47]\n"
"ds_read_b128 v[112:115], v5 offset:18432\n"
"v_mul_f32 v69, v41, v61\n"
"v_mfma_f32_16x16x32_bf16 v[44:47], a[108:111], v[84:87], v[44:47]\n"
"buffer_load_dwordx4 v2, s[32:35], s63 offen offset:2048 sc1 lds\n"
"v_mul_f32 v32, v32, v36\n"
"v_mfma_f32_16x16x32_bf16 v[44:47], a[120:123], v[88:91], v[44:47]\n"
"v_exp_f32 v67, v67\n"
"v_mfma_f32_16x16x32_bf16 v[44:47], a[132:135], v[92:95], v[44:47]\n"
"ds_read_b128 v[116:119], v5 offset:19584\n"
"v_mul_f32 v33, v33, v37\n"
"v_mfma_f32_16x16x32_bf16 v[48:51], a[136:139], v[92:95], v[48:51]\n"
"v_exp_f32 v68, v68\n"
"v_mfma_f32_16x16x32_bf16 v[48:51], a[124:127], v[88:91], v[48:51]\n"
"buffer_load_dwordx4 v2, s[32:35], s63 offen offset:3072 sc1 lds\n"
"v_mul_f32 v34, v34, v38\n"
"v_mfma_f32_16x16x32_bf16 v[48:51], a[112:115], v[84:87], v[48:51]\n"
"ds_read_b128 v[120:123], v5 offset:20736\n"
"v_mul_f32 v35, v35, v39\n"
"v_mfma_f32_16x16x32_bf16 v[48:51], a[100:103], v[80:83], v[48:51]\n"
"v_exp_f32 v69, v69\n"
"v_mfma_f32_16x16x32_bf16 v[52:55], a[104:107], v[80:83], v[52:55]\n"
"v_mul_f32 v40, v40, v42\n"
"v_mul_f32 v41, v41, v43\n"
"v_mfma_f32_16x16x32_bf16 v[52:55], a[116:119], v[84:87], v[52:55]\n"
"ds_read_b128 v[124:127], v5 offset:21888\n"
"s_add_u32 m0, s64, 40960\n"
"v_mfma_f32_16x16x32_bf16 v[52:55], a[128:131], v[88:91], v[52:55]\n"
"v_fma_f32 v64, v64, v62, v62\n"
"v_fma_f32 v65, v65, v62, v62\n"
"v_mfma_f32_16x16x32_bf16 v[52:55], a[140:143], v[92:95], v[52:55]\n"
"v_fma_f32 v66, v66, v62, v62\n"
"s_waitcnt lgkmcnt(4)\n"
"v_mfma_f32_16x16x32_bf16 v[52:55], a[152:155], v[96:99], v[52:55]\n"
"ds_read_b128 v[76:79], v5 offset:23040\n"
"v_fma_f32 v67, v67, v62, v62\n"
"v_mfma_f32_16x16x32_bf16 v[52:55], a[164:167], v[100:103], v[52:55]\n"
"buffer_load_dwordx4 v6, s[32:35], s63 offen offset:0 sc1 lds\n"
"v_fma_f32 v68, v68, v62, v62\n"
"v_mfma_f32_16x16x32_bf16 v[52:55], a[176:179], v[104:107], v[52:55]\n"
"v_rcp_f32 v64, v64\n"
"v_mfma_f32_16x16x32_bf16 v[52:55], a[188:191], v[108:111], v[52:55]\n"
"ds_read_b128 v[80:83], v5 offset:24192\n"
"v_fma_f32 v69, v69, v62, v62\n"
"v_mfma_f32_16x16x32_bf16 v[48:51], a[184:187], v[108:111], v[48:51]\n"
"v_rcp_f32 v65, v65\n"
"v_mfma_f32_16x16x32_bf16 v[48:51], a[172:175], v[104:107], v[48:51]\n"
"buffer_load_dwordx4 v6, s[32:35], s63 offen offset:1024 sc1 lds\n"
"v_mul_f32 v32, v32, v64\n"
"v_mfma_f32_16x16x32_bf16 v[48:51], a[160:163], v[100:103], v[48:51]\n"
"ds_read_b128 v[84:87], v5 offset:25344\n"
"v_mul_f32 v33, v33, v65\n"
"v_mfma_f32_16x16x32_bf16 v[48:51], a[148:151], v[96:99], v[48:51]\n"
"v_rcp_f32 v66, v66\n"
"v_mfma_f32_16x16x32_bf16 v[44:47], a[144:147], v[96:99], v[44:47]\n"
"v_rcp_f32 v67, v67\n"
"v_mfma_f32_16x16x32_bf16 v[44:47], a[156:159], v[100:103], v[44:47]\n"
"buffer_load_dwordx2 v[12:13], v14, s[32:35], s66 offen sc1\n"
"ds_read_b128 v[88:91], v5 offset:26496\n"
"v_mfma_f32_16x16x32_bf16 v[44:47], a[168:171], v[104:107], v[44:47]\n"
"v_rcp_f32 v68, v68\n"
"v_mfma_f32_16x16x32_bf16 v[44:47], a[180:183], v[108:111], v[44:47]\n"
"v_mul_f32 v34, v34, v66\n"
"s_waitcnt lgkmcnt(4)\n"
"v_mfma_f32_16x16x32_bf16 v[44:47], a[192:195], v[112:115], v[44:47]\n"
"ds_read_b128 v[92:95], v5 offset:27648\n"
"v_mul_f32 v35, v35, v67\n"
"v_mfma_f32_16x16x32_bf16 v[44:47], a[204:207], v[116:119], v[44:47]\n"
"buffer_load_dwordx4 v6, s[32:35], s63 offen offset:2048 sc1 lds\n"
"v_mul_f32 v40, v40, v68\n"
"v_mfma_f32_16x16x32_bf16 v[44:47], a[216:219], v[120:123], v[44:47]\n"
"v_rcp_f32 v69, v69\n"
"v_mfma_f32_16x16x32_bf16 v[44:47], a[228:231], v[124:127], v[44:47]\n"
"ds_read_b128 v[96:99], v5 offset:28800\n"
"v_mul_f32 v41, v41, v69\n"
"v_mfma_f32_16x16x32_bf16 v[48:51], a[232:235], v[124:127], v[48:51]\n"
"v_cvt_pk_bf16_f32 v74, v40, v41\n"
"v_cvt_pk_bf16_f32 v72, v32, v33\n"
"v_mfma_f32_16x16x32_bf16 v[48:51], a[220:223], v[120:123], v[48:51]\n"
"buffer_load_dwordx4 v6, s[32:35], s63 offen offset:3072 sc1 lds\n"
"v_cvt_pk_bf16_f32 v73, v34, v35\n"
"v_mfma_f32_16x16x32_bf16 v[48:51], a[208:211], v[116:119], v[48:51]\n"
"ds_read_b128 v[100:103], v5 offset:29952\n"
"ds_read2st64_b32 v[56:57], v8 offset0:0 offset1:36\n"
"v_mfma_f32_16x16x32_bf16 v[48:51], a[196:199], v[112:115], v[48:51]\n"
"ds_read2st64_b32 v[58:59], v8 offset0:72 offset1:108\n"
"v_mfma_f32_16x16x32_bf16 v[52:55], a[200:203], v[112:115], v[52:55]\n"
"v_permlane32_swap_b32 v74, v75\n"
"v_mfma_f32_16x16x32_bf16 v[52:55], a[212:215], v[116:119], v[52:55]\n"
"ds_read_b128 v[104:107], v5 offset:31104\n"
"v_mfma_f32_16x16x32_bf16 v[52:55], a[224:227], v[120:123], v[52:55]\n"
"v_permlane16_swap_b32 v72, v74\n"
"v_mfma_f32_16x16x32_bf16 v[52:55], a[236:239], v[124:127], v[52:55]\n"
"s_add_u32 m0, s64, 45056\n"
"s_waitcnt lgkmcnt(6)\n"
"v_mfma_f32_16x16x32_bf16 v[52:55], a[248:251], v[76:79], v[52:55]\n"
"ds_read_b128 v[108:111], v5 offset:32256\n"
"v_mfma_f32_16x16x32_bf16 v[52:55], v[132:135], v[80:83], v[52:55]\n"
"buffer_load_dwordx4 v7, s[32:35], s63 offen offset:0 sc1 lds\n"
"s_add_u32 s63, s63, 890880\n"
"v_mfma_f32_16x16x32_bf16 v[52:55], v[144:147], v[84:87], v[52:55]\n"
"s_xor_b32 s64, s64, s65\n"
"v_mfma_f32_16x16x32_bf16 v[52:55], v[156:159], v[88:91], v[52:55]\n"
"ds_read_b128 v[112:115], v5 offset:33408\n"
"v_mfma_f32_16x16x32_bf16 v[48:51], v[152:155], v[88:91], v[48:51]\n"
"v_permlane16_swap_b32 v73, v75\n"
"v_mfma_f32_16x16x32_bf16 v[48:51], v[140:143], v[84:87], v[48:51]\n"
"buffer_store_dwordx4 v[72:75], v15, s[32:35], s68 offen sc1\n"
"ds_read_b32 v62, v11 offset:128\n"
"v_mfma_f32_16x16x32_bf16 v[48:51], v[128:131], v[80:83], v[48:51]\n"
"ds_read_b128 v[116:119], v5 offset:34560\n"
"s_waitcnt lgkmcnt(5)\n"
"v_add_f32 v56, v56, v57\n"
"v_mfma_f32_16x16x32_bf16 v[48:51], a[244:247], v[76:79], v[48:51]\n"
"v_add_f32 v58, v58, v59\n"
"v_mfma_f32_16x16x32_bf16 v[44:47], a[240:243], v[76:79], v[44:47]\n"
"v_add_f32 v56, v56, v58\n"
"v_mfma_f32_16x16x32_bf16 v[44:47], a[252:255], v[80:83], v[44:47]\n"
"ds_read_b128 v[120:123], v5 offset:35712\n"
"v_add_f32_dpp v56, v56, v56 row_ror:8 row_mask:0xf bank_mask:0xf\n"
"v_mfma_f32_16x16x32_bf16 v[44:47], v[136:139], v[84:87], v[44:47]\n"
"v_xor_b32 v5, 73728, v5\n"
"v_add_f32_dpp v56, v56, v56 row_ror:4 row_mask:0xf bank_mask:0xf\n"
"v_mfma_f32_16x16x32_bf16 v[44:47], v[148:151], v[88:91], v[44:47]\n"
"s_waitcnt lgkmcnt(2)\n"
"v_mfma_f32_16x16x32_bf16 v[44:47], v[160:163], v[92:95], v[44:47]\n"
"ds_read_b128 v[76:79], v4 offset:0\n"
"v_lshlrev_b32 v60, 16, v62\n"
"v_mfma_f32_16x16x32_bf16 v[44:47], v[172:175], v[96:99], v[44:47]\n"
"v_and_b32 v62, 0xffff0000, v62\n"
"v_add_f32_dpp v56, v56, v56 row_ror:2 row_mask:0xf bank_mask:0xf\n"
"v_mfma_f32_16x16x32_bf16 v[44:47], v[184:187], v[100:103], v[44:47]\n"
"v_rcp_f32 v61, v62\n"
"v_mfma_f32_16x16x32_bf16 v[44:47], v[196:199], v[104:107], v[44:47]\n"
"ds_read_b128 v[80:83], v4 offset:1152\n"
"v_mul_f32 v62, v62, v62\n"
"v_mfma_f32_16x16x32_bf16 v[48:51], v[200:203], v[104:107], v[48:51]\n"
"v_mul_f32 v61, 0xbfb8aa3b, v61\n"
"v_mul_f32 v56, 0x3a800000, v56\n"
"v_mfma_f32_16x16x32_bf16 v[48:51], v[188:191], v[100:103], v[48:51]\n"
"v_mul_f32 v57, v56, v56\n"
"v_mfma_f32_16x16x32_bf16 v[48:51], v[176:179], v[96:99], v[48:51]\n"
"ds_read_b128 v[84:87], v4 offset:2304\n"
"v_sub_f32_dpp v57, v56, v57 quad_perm:[1,1,3,3] row_mask:0xf bank_mask:0xf\n"
"v_mfma_f32_16x16x32_bf16 v[48:51], v[164:167], v[92:95], v[48:51]\n"
"v_max_f32 v57, 0, v57\n"
"v_mfma_f32_16x16x32_bf16 v[52:55], v[168:171], v[92:95], v[52:55]\n"
"v_cvt_pk_bf16_f32 v57, 0, v57\n"
"v_mfma_f32_16x16x32_bf16 v[52:55], v[180:183], v[96:99], v[52:55]\n"
"ds_read_b128 v[88:91], v4 offset:3456\n"
"v_add_f32 v57, 0x3727c5ac, v57\n"
"v_mfma_f32_16x16x32_bf16 v[52:55], v[192:195], v[100:103], v[52:55]\n"
"v_cvt_pk_bf16_f32 v57, 0, v57\n"
"v_mfma_f32_16x16x32_bf16 v[52:55], v[204:207], v[104:107], v[52:55]\n"
"s_waitcnt lgkmcnt(4)\n"
"v_mfma_f32_16x16x32_bf16 v[52:55], v[216:219], v[108:111], v[52:55]\n"
"ds_read_b128 v[92:95], v4 offset:4608\n"
"v_mfma_f32_16x16x32_bf16 v[52:55], v[228:231], v[112:115], v[52:55]\n"
"v_sqrt_f32 v57, v57\n"
"v_mfma_f32_16x16x32_bf16 v[52:55], v[240:243], v[116:119], v[52:55]\n"
"v_cvt_pk_bf16_f32 v57, v56, v57\n"
"v_mfma_f32_16x16x32_bf16 v[52:55], v[252:255], v[120:123], v[52:55]\n"
"ds_read_b128 v[96:99], v4 offset:5760\n"
"ds_write_b32 v10, v57 offset:0\n"
"v_mfma_f32_16x16x32_bf16 v[48:51], v[248:251], v[120:123], v[48:51]\n"
"v_mfma_f32_16x16x32_bf16 v[48:51], v[236:239], v[116:119], v[48:51]\n"
"v_mfma_f32_16x16x32_bf16 v[48:51], v[224:227], v[112:115], v[48:51]\n"
"ds_read_b128 v[100:103], v4 offset:6912\n"
"v_mfma_f32_16x16x32_bf16 v[48:51], v[212:215], v[108:111], v[48:51]\n"
"v_mfma_f32_16x16x32_bf16 v[44:47], v[208:211], v[108:111], v[44:47]\n"
"v_mfma_f32_16x16x32_bf16 v[44:47], v[220:223], v[112:115], v[44:47]\n"
"ds_read_b128 v[104:107], v4 offset:8064\n"
"v_mfma_f32_16x16x32_bf16 v[44:47], v[232:235], v[116:119], v[44:47]\n"
"s_sub_u32 s62, s62, 1\n"
"v_mfma_f32_16x16x32_bf16 v[44:47], v[244:247], v[120:123], v[44:47]\n"
"s_cbranch_scc0 .L_gl_3\n"
"s_nop 1\n"
"s_add_u32 s68, s68, 890880\n"
"s_waitcnt vmcnt(0)\n"
"s_barrier\n"
"buffer_store_dwordx2 v[16:17], v2, s[92:95], s68 idxen sc1\n"
"v_fma_f32 v52, -v60, v28, v52\n"
"v_fma_f32 v53, -v60, v29, v53\n"
"v_fma_f32 v54, -v60, v30, v54\n"
"v_fma_f32 v55, -v60, v31, v55\n"
"v_fma_f32 v48, -v60, v24, v48\n"
"v_fma_f32 v49, -v60, v25, v49\n"
"v_fma_f32 v50, -v60, v26, v50\n"
"v_fma_f32 v51, -v60, v27, v51\n"
"v_fma_f32 v44, -v60, v20, v44\n"
"v_fma_f32 v45, -v60, v21, v45\n"
"v_fma_f32 v46, -v60, v22, v46\n"
"v_fma_f32 v47, -v60, v23, v47\n"
"v_permlane32_swap_b32 v52, v54\n"
"v_permlane32_swap_b32 v53, v55\n"
"v_mul_f32 v64, v44, v61\n"
"v_mul_f32 v65, v45, v61\n"
"v_mul_f32 v66, v46, v61\n"
"v_mul_f32 v67, v47, v61\n"
"v_mul_f32 v68, v52, v61\n"
"v_mul_f32 v69, v53, v61\n"
"v_exp_f32 v64, v64\n"
"v_exp_f32 v65, v65\n"
"v_mul_f32 v44, v44, v48\n"
"v_mul_f32 v45, v45, v49\n"
"v_exp_f32 v66, v66\n"
"v_exp_f32 v67, v67\n"
"v_mul_f32 v46, v46, v50\n"
"v_mul_f32 v47, v47, v51\n"
"v_exp_f32 v68, v68\n"
"v_exp_f32 v69, v69\n"
"v_mul_f32 v52, v52, v54\n"
"v_mul_f32 v53, v53, v55\n"
"v_fma_f32 v64, v64, v62, v62\n"
"v_fma_f32 v65, v65, v62, v62\n"
"v_rcp_f32 v64, v64\n"
"v_rcp_f32 v65, v65\n"
"v_fma_f32 v66, v66, v62, v62\n"
"v_fma_f32 v67, v67, v62, v62\n"
"v_rcp_f32 v66, v66\n"
"v_rcp_f32 v67, v67\n"
"v_fma_f32 v68, v68, v62, v62\n"
"v_fma_f32 v69, v69, v62, v62\n"
"v_rcp_f32 v68, v68\n"
"v_rcp_f32 v69, v69\n"
"v_mul_f32 v44, v44, v64\n"
"v_mul_f32 v45, v45, v65\n"
"v_mul_f32 v46, v46, v66\n"
"v_mul_f32 v47, v47, v67\n"
"v_mul_f32 v52, v52, v68\n"
"v_mul_f32 v53, v53, v69\n"
"v_cvt_pk_bf16_f32 v74, v52, v53\n"
"v_cvt_pk_bf16_f32 v72, v44, v45\n"
"v_cvt_pk_bf16_f32 v73, v46, v47\n"
"v_permlane32_swap_b32 v74, v75\n"
"s_nop 1\n"
"v_permlane16_swap_b32 v72, v74\n"
"v_permlane16_swap_b32 v73, v75\n"
"buffer_store_dwordx4 v[72:75], v15, s[32:35], s68 offen sc1\n"
"s_add_u32 s68, s68, 890880\n"
"s_waitcnt vmcnt(0)\n"
"s_barrier\n"
"buffer_store_dwordx2 v[16:17], v2, s[92:95], s68 idxen sc1\n"
"s_branch .L_end\n"
".L_gt0_3:\n"
"s_nop 1\n"
"s_add_u32 s68, s68, 890880\n"
"s_waitcnt vmcnt(0)\n"
"s_barrier\n"
"buffer_store_dwordx2 v[16:17], v2, s[92:95], s68 idxen sc1\n"
"v_fma_f32 v40, -v60, v28, v40\n"
"v_fma_f32 v41, -v60, v29, v41\n"
"v_fma_f32 v42, -v60, v30, v42\n"
"v_fma_f32 v43, -v60, v31, v43\n"
"v_fma_f32 v36, -v60, v24, v36\n"
"v_fma_f32 v37, -v60, v25, v37\n"
"v_fma_f32 v38, -v60, v26, v38\n"
"v_fma_f32 v39, -v60, v27, v39\n"
"v_fma_f32 v32, -v60, v20, v32\n"
"v_fma_f32 v33, -v60, v21, v33\n"
"v_fma_f32 v34, -v60, v22, v34\n"
"v_fma_f32 v35, -v60, v23, v35\n"
"v_permlane32_swap_b32 v40, v42\n"
"v_permlane32_swap_b32 v41, v43\n"
"v_mul_f32 v64, v32, v61\n"
"v_mul_f32 v65, v33, v61\n"
"v_mul_f32 v66, v34, v61\n"
"v_mul_f32 v67, v35, v61\n"
"v_mul_f32 v68, v40, v61\n"
"v_mul_f32 v69, v41, v61\n"
"v_exp_f32 v64, v64\n"
"v_exp_f32 v65, v65\n"
"v_mul_f32 v32, v32, v36\n"
"v_mul_f32 v33, v33, v37\n"
"v_exp_f32 v66, v66\n"
"v_exp_f32 v67, v67\n"
"v_mul_f32 v34, v34, v38\n"
"v_mul_f32 v35, v35, v39\n"
"v_exp_f32 v68, v68\n"
"v_exp_f32 v69, v69\n"
"v_mul_f32 v40, v40, v42\n"
"v_mul_f32 v41, v41, v43\n"
"v_fma_f32 v64, v64, v62, v62\n"
"v_fma_f32 v65, v65, v62, v62\n"
"v_rcp_f32 v64, v64\n"
"v_rcp_f32 v65, v65\n"
"v_fma_f32 v66, v66, v62, v62\n"
"v_fma_f32 v67, v67, v62, v62\n"
"v_rcp_f32 v66, v66\n"
"v_rcp_f32 v67, v67\n"
"v_fma_f32 v68, v68, v62, v62\n"
"v_fma_f32 v69, v69, v62, v62\n"
"v_rcp_f32 v68, v68\n"
"v_rcp_f32 v69, v69\n"
"v_mul_f32 v32, v32, v64\n"
"v_mul_f32 v33, v33, v65\n"
"v_mul_f32 v34, v34, v66\n"
"v_mul_f32 v35, v35, v67\n"
"v_mul_f32 v40, v40, v68\n"
"v_mul_f32 v41, v41, v69\n"
"v_cvt_pk_bf16_f32 v74, v40, v41\n"
"v_cvt_pk_bf16_f32 v72, v32, v33\n"
"v_cvt_pk_bf16_f32 v73, v34, v35\n"
"v_permlane32_swap_b32 v74, v75\n"
"s_nop 1\n"
"v_permlane16_swap_b32 v72, v74\n"
"v_permlane16_swap_b32 v73, v75\n"
"buffer_store_dwordx4 v[72:75], v15, s[32:35], s68 offen sc1\n"
"s_add_u32 s68, s68, 890880\n"
"s_waitcnt vmcnt(0)\n"
"s_barrier\n"
"buffer_store_dwordx2 v[16:17], v2, s[92:95], s68 idxen sc1\n"
"s_branch .L_end\n"
".L_q:\n"
"v_mov_b32 v16, 0x3f803f80\n"
"v_mov_b32 v17, 0x3f803f80\n"
"v_mov_b32 v18, 0x3f803f80\n"
"v_mov_b32 v19, 0x3f803f80\n"
"s_add_u32 s36, s10, 0x0\n"
"s_addc_u32 s37, s11, 0x0\n"
"s_and_b32 s37, s37, 0xffff\n"
"s_mov_b32 s38, 0x600000\n"
"s_mov_b32 s39, 0x20000\n"
"s_add_u32 s40, s10, 0x400\n"
"s_addc_u32 s41, s11, 0x0\n"
"s_and_b32 s41, s41, 0xffff\n"
"s_mov_b32 s42, 0x5ffc00\n"
"s_mov_b32 s43, 0x20000\n"
"s_add_u32 s44, s10, 0x800\n"
"s_addc_u32 s45, s11, 0x0\n"
"s_and_b32 s45, s45, 0xffff\n"
"s_mov_b32 s46, 0x5ff800\n"
"s_mov_b32 s47, 0x20000\n"
"s_lshl_b32 s96, s27, 7\n"
"v_and_b32 v68, 3, v1\n"
"v_mul_u32_u24 v68, 8, v68\n"
"v_and_b32 v72, 1, v1\n"
"v_bfe_u32 v3, v1, 1, 1\n"
"v_lshl_add_u32 v72, v72, 1, v3\n"
"v_mul_u32_u24 v72, 8, v72\n"
"s_lshl_b32 s48, s26, 5\n"
"v_add_u32 v68, s48, v68\n"
"v_add_u32 v72, s48, v72\n"
"s_add_u32 s12, s8, 0x0\n"
"s_addc_u32 s13, s9, 0x0\n"
"s_and_b32 s13, s13, 0xffff\n"
"s_mov_b32 s14, 0x400000\n"
"s_mov_b32 s15, 0x20000\n"
"v_lshrrev_b32 v57, 3, v1\n"
"s_and_b32 s48, s26, 1\n"
"v_xor_b32 v3, v57, v1\n"
"v_xor_b32 v3, s48, v3\n"
"v_and_b32 v3, 7, v3\n"
"v_lshlrev_b32 v57, 11, v57\n"
"v_lshl_add_u32 v57, v3, 4, v57\n"
"s_lshl_b32 s16, s28, 20\n"
"s_sub_u32 s16, s16, 65536\n"
"s_and_b32 s16, s16, 0x3fffff\n"
"s_lshl_b32 s17, s26, 14\n"
"s_add_u32 s16, s16, s17\n"
"s_lshl_b32 s17, s26, 10\n"
"v_lshrrev_b32 v64, 3, v1\n"
"v_bfe_u32 v65, v1, 4, 1\n"
"s_and_b32 s48, s26, 1\n"
"s_lshl_b32 s48, s48, 1\n"
"v_or_b32 v65, s48, v65\n"
"v_lshlrev_b32 v65, 1, v65\n"
"v_and_b32 v66, 7, v1\n"
"v_xor_b32 v65, v65, v66\n"
"v_mul_u32_u24 v64, 6144, v64\n"
"v_lshl_add_u32 v67, v65, 4, v64\n"
"s_mul_i32 s48, s26, 49152\n"
"s_add_u32 s96, s96, s48\n"
"s_mul_i32 s97, s26, 3072\n"
"s_add_u32 m0, s17, 24576\n"
"s_nop 0\n"
"buffer_load_dwordx4 v57, s[12:15], s16 offen offset:0 lds\n"
"s_add_u32 m0, s97, 0\n"
"s_nop 0\n"
"buffer_load_dwordx4 v67, s[36:39], s96 offen offset:0 lds\n"
"buffer_load_dwordx4 v67, s[40:43], s96 offen offset:1024 lds\n"
"buffer_load_dwordx4 v67, s[44:47], s96 offen offset:2048 lds\n"
"s_add_u32 s96, s96, 196608\n"
"s_add_u32 m0, s97, 12288\n"
"s_nop 0\n"
"buffer_load_dwordx4 v67, s[36:39], s96 offen offset:0 lds\n"
"buffer_load_dwordx4 v67, s[40:43], s96 offen offset:1024 lds\n"
"buffer_load_dwordx4 v67, s[44:47], s96 offen offset:2048 lds\n"
"s_add_u32 s96, s96, 196608\n"
"s_add_u32 m0, s17, 28544\n"
"s_nop 0\n"
"buffer_load_dwordx4 v57, s[12:15], s16 offen offset:128 lds\n"
"s_add_u32 m0, s97, 139264\n"
"s_nop 0\n"
"buffer_load_dwordx4 v67, s[36:39], s96 offen offset:0 lds\n"
"buffer_load_dwordx4 v67, s[40:43], s96 offen offset:1024 lds\n"
"buffer_load_dwordx4 v67, s[44:47], s96 offen offset:2048 lds\n"
"s_add_u32 s96, s96, 196608\n"
"s_add_u32 m0, s97, 151552\n"
"s_nop 0\n"
"buffer_load_dwordx4 v67, s[36:39], s96 offen offset:0 lds\n"
"buffer_load_dwordx4 v67, s[40:43], s96 offen offset:1024 lds\n"
"buffer_load_dwordx4 v67, s[44:47], s96 offen offset:2048 lds\n"
"s_add_u32 s96, s96, 196608\n"
"s_add_u32 m0, s17, 32512\n"
"s_nop 0\n"
"buffer_load_dwordx4 v57, s[12:15], s16 offen offset:256 lds\n"
"s_add_u32 m0, s97, 90112\n"
"s_nop 0\n"
"buffer_load_dwordx4 v67, s[36:39], s96 offen offset:0 lds\n"
"buffer_load_dwordx4 v67, s[40:43], s96 offen offset:1024 lds\n"
"buffer_load_dwordx4 v67, s[44:47], s96 offen offset:2048 lds\n"
"s_add_u32 s96, s96, 196608\n"
"s_add_u32 m0, s97, 102400\n"
"s_nop 0\n"
"buffer_load_dwordx4 v67, s[36:39], s96 offen offset:0 lds\n"
"buffer_load_dwordx4 v67, s[40:43], s96 offen offset:1024 lds\n"
"buffer_load_dwordx4 v67, s[44:47], s96 offen offset:2048 lds\n"
"s_add_u32 s96, s96, 196608\n"
"v_lshrrev_b32 v65, 4, v1\n"
"v_bfe_u32 v64, v1, 3, 1\n"
"v_and_b32 v66, 1, v65\n"
"v_lshl_add_u32 v64, v66, 1, v64\n"
"v_lshlrev_b32 v64, 5, v64\n"
"v_mul_u32_u24 v65, 3072, v65\n"
"v_bfe_u32 v66, v1, 2, 2\n"
"v_lshl_add_u32 v65, v66, 7, v65\n"
"v_xor_b32 v68, v64, v68\n"
"v_add_u32 v68, v65, v68\n"
"v_add_u32 v69, 139264, v68\n"
"v_add_u32 v70, 90112, v68\n"
"v_add_u32 v71, 114688, v68\n"
"v_xor_b32 v72, v64, v72\n"
"v_add_u32 v72, v65, v72\n"
"v_add_u32 v73, 139264, v72\n"
"v_add_u32 v74, 90112, v72\n"
"v_add_u32 v75, 114688, v72\n"
"s_lshl2_add_u32 s48, s27, s26\n"
"s_lshl_b32 s48, s48, 9\n"
"s_add_u32 s68, s48, 4648960\n"
"s_lshl2_add_u32 s49, s27, 32\n"
"s_waitcnt lgkmcnt(0)\n"
"s_add_u32 s54, s52, 0x7f4a7c15\n"
"s_addc_u32 s55, s53, 0x9e3779b9\n"
"s_lshl_b32 s67, s49, 5\n"
"s_add_u32 s67, s67, 4898816\n"
"s_sub_u32 s51, s67, s68\n"
"s_add_u32 s92, s32, s51\n"
"s_addc_u32 s93, s33, 0x10000\n"
"s_cmp_eq_u32 s26, 0\n"
"s_cselect_b32 s94, 1, 0\n"
"s_mov_b32 s95, 0x20000\n"
"v_mov_b32 v30, s54\n"
"v_mov_b32 v31, s55\n"
"v_cmp_gt_u32 vcc, 12, v1\n"
"v_mul_u32_u24 v3, 296960, v1\n"
"v_mov_b32 v12, 0x7fff0000\n"
"v_cndmask_b32 v3, v12, v3, vcc\n"
"s_add_u32 s51, s67, 76021760\n"
"buffer_store_dwordx2 v[30:31], v3, s[32:35], s51 offen sc1\n"
"s_sub_u32 s67, s68, 296960\n"
"s_mul_i32 s49, s28, 296960\n"
"s_add_u32 s68, s68, s49\n"
"s_sub_u32 s68, s68, 2375680\n"
"s_mov_b32 s62, 31\n"
"s_add_u32 s48, s28, 3\n"
"s_and_b32 s48, s48, 3\n"
"s_mul_i32 s48, s48, 296960\n"
"s_add_u32 s66, s48, 41721856\n"
"s_sub_u32 s48, s48, s49\n"
"s_add_u32 s69, s48, 38010880\n"
"s_sub_u32 s70, 0xfdce2000, s48\n"
"s_mov_b32 s72, s70\n"
"s_mov_b32 s63, 0xfffba000\n"
"s_mul_i32 s64, s26, 9216\n"
"s_add_u32 s65, s64, 73728\n"
"s_xor_b32 s65, s65, s64\n"
"s_add_u32 s63, s63, s64\n"
"v_mov_b32 v4, v2\n"
"v_add_u32 v5, 36864, v2\n"
"s_mov_b32 s71, s63\n"
"v_bfe_u32 v3, v1, 1, 3\n"
"v_mul_u32_u24 v6, 1152, v3\n"
"v_lshrrev_b32 v3, 1, v3\n"
"v_add_u32 v3, s26, v3\n"
"v_lshrrev_b32 v7, 4, v1\n"
"v_lshl_add_u32 v3, v3, 2, v7\n"
"v_and_b32 v3, 15, v3\n"
"v_lshl_add_u32 v6, v3, 2, v6\n"
"v_and_b32 v3, 1, v1\n"
"v_lshl_add_u32 v6, v3, 6, v6\n"
"v_add_u32 v6, 1024, v6\n"
"s_lshl_b32 s48, s26, 2\n"
"v_add_u32 v8, s48, v7\n"
"v_and_b32 v9, 15, v1\n"
"v_add_u32 v3, 64, v1\n"
"v_cmp_eq_u32 vcc, 0, v9\n"
"v_add_u32 v7, 73728, v6\n"
"v_xor_b32 v7, v7, v6\n"
"v_cndmask_b32 v8, v3, v8, vcc\n"
"v_lshlrev_b32 v8, 2, v8\n"
"v_add_u32 v8, 147456, v8\n"
"v_lshlrev_b32 v9, 2, v9\n"
"v_add_u32 v9, 147456, v9\n"
"v_lshrrev_b32 v10, 4, v1\n"
"v_and_b32 v32, 15, v1\n"
"v_and_b32 v33, 1, v1\n"
"v_lshl_add_u32 v34, v10, 2, v33\n"
"v_lshlrev_b32 v33, 4, v33\n"
"v_mov_b32 v35, 0xffff\n"
"v_lshlrev_b32 v33, v33, v35\n"
"v_cmp_le_u32 vcc, v34, v32\n"
"v_add_u32 v34, 2, v34\n"
"s_nop 0\n"
"v_cndmask_b32 v124, 0, v33, vcc\n"
"v_xor_b32 v126, v124, v33\n"
"v_cmp_le_u32 vcc, v34, v32\n"
"s_nop 1\n"
"v_cndmask_b32 v125, 0, v33, vcc\n"
"v_xor_b32 v127, v125, v33\n"
"v_lshlrev_b32 v10, 4, v10\n"
"v_add_u32 v10, 147456, v10\n"
"s_lshl_b32 s48, s26, 10\n"
"s_add_u32 s48, s48, 148480\n"
"v_lshlrev_b32 v11, 3, v1\n"
"v_add_u32 v11, s48, v11\n"
"s_lshl_b32 s48, s26, 3\n"
"s_add_u32 s48, s48, 0\n"
"v_add_u32 v0, s48, v1\n"
"v_lshrrev_b32 v0, 2, v0\n"
"v_lshlrev_b32 v0, 7, v0\n"
"v_cmp_gt_u32 vcc, 8, v1\n"
"v_mov_b32 v3, 0x7fff0000\n"
"s_nop 0\n"
"v_cndmask_b32 v0, v3, v0, vcc\n"
"v_cndmask_b32 v30, 0, v30, vcc\n"
"v_cndmask_b32 v31, 0, v31, vcc\n"
"v_mov_b32 v116, 0xff61b1e6\n"
"v_mov_b32 v117, 0xff61b1e6\n"
"v_mov_b32 v118, 0xff61b1e6\n"
"v_mov_b32 v119, 0xff61b1e6\n"
"v_mov_b32 v120, 0xff61b1e6\n"
"v_mov_b32 v121, 0xff61b1e6\n"
"v_mov_b32 v122, 0xff61b1e6\n"
"v_mov_b32 v123, 0xff61b1e6\n"
"v_cmp_ne_u32 vcc, 0xffff, v124\n"
"s_nop 1\n"
"v_cndmask_b32 v116, 0, v116, vcc\n"
"v_cmp_ne_u32 vcc, 0xffff0000, v124\n"
"s_nop 1\n"
"v_cndmask_b32 v117, 0, v117, vcc\n"
"v_cmp_ne_u32 vcc, 0xffff, v125\n"
"s_nop 1\n"
"v_cndmask_b32 v118, 0, v118, vcc\n"
"v_cmp_ne_u32 vcc, 0xffff0000, v125\n"
"s_nop 1\n"
"v_cndmask_b32 v119, 0, v119, vcc\n"
"v_cmp_ne_u32 vcc, 0xffff, v126\n"
"s_nop 1\n"
"v_cndmask_b32 v120, 0, v120, vcc\n"
"v_cmp_ne_u32 vcc, 0xffff0000, v126\n"
"s_nop 1\n"
"v_cndmask_b32 v121, 0, v121, vcc\n"
"v_cmp_ne_u32 vcc, 0xffff, v127\n"
"s_nop 1\n"
"v_cndmask_b32 v122, 0, v122, vcc\n"
"v_cmp_ne_u32 vcc, 0xffff0000, v127\n"
"s_nop 1\n"
"v_cndmask_b32 v123, 0, v123, vcc\n"
"v_bfe_u32 v3, v1, 3, 2\n"
"v_mul_u32_u24 v56, 1187840, v3\n"
"v_cmp_eq_u32 vcc, 3, v3\n"
"s_sub_u32 s48, 0, s70\n"
"v_mov_b32 v3, s48\n"
"v_cndmask_b32 v56, v56, v3, vcc\n"
"v_and_b32 v3, 7, v1\n"
"v_lshl_add_u32 v56, v3, 7, v56\n"
"v_bfe_u32 v3, v1, 3, 1\n"
"v_lshrrev_b32 v58, 4, v1\n"
"v_xor_b32 v3, v3, v58\n"
"v_xor_b32 v3, v3, v1\n"
"v_and_b32 v3, 7, v3\n"
"v_and_b32 v58, 15, v1\n"
"v_lshlrev_b32 v58, 7, v58\n"
"v_lshl_add_u32 v58, v3, 4, v58\n"
"v_xor_b32 v59, 64, v58\n"
"v_add_u32 v58, 24576, v58\n"
"v_add_u32 v59, 24576, v59\n"
"v_cmp_gt_u32 vcc, 32, v1\n"
"v_mov_b32 v1, 0x7fff0000\n"
"s_nop 0\n"
"v_cndmask_b32 v1, v1, v2, vcc\n"
"s_mov_b32 s58, 0x4003c000\n"
"s_mov_b32 s59, 0x0\n"
"s_mov_b32 s60, 0x80028000\n"
"s_mov_b32 s61, 0x0\n"
"v_subrev_u32 v3, 148480, v11\n"
"v_xor_b32 v3, 1024, v3\n"
"v_add_u32 v3, 148480, v3\n"
"s_add_u32 s74, s66, s72\n"
"s_sub_u32 s74, s66, 286720\n"
"s_add_u32 s66, s66, s72\n"
"s_mov_b32 s72, 1187840\n"
"s_waitcnt vmcnt(15)\n"
"s_barrier\n"
"s_add_u32 m0, s17, 36480\n"
"s_nop 0\n"
"buffer_load_dwordx4 v57, s[12:15], s16 offen offset:384 lds\n"
"ds_read_b64_tr_b16 a[0:1], v68 offset:0\n"
"ds_read_b64_tr_b16 a[2:3], v68 offset:512\n"
"s_add_u32 m0, s97, 114688\n"
"s_nop 0\n"
"buffer_load_dwordx4 v67, s[36:39], s96 offen offset:0 lds\n"
"ds_read_b64_tr_b16 a[4:5], v68 offset:1024\n"
"ds_read_b64_tr_b16 a[6:7], v68 offset:1536\n"
"buffer_load_dwordx4 v67, s[40:43], s96 offen offset:1024 lds\n"
"ds_read_b64_tr_b16 a[8:9], v72 offset:2048\n"
"ds_read_b64_tr_b16 a[10:11], v72 offset:2560\n"
"buffer_load_dwordx4 v67, s[44:47], s96 offen offset:2048 lds\n"
"ds_read_b64_tr_b16 a[12:13], v68 offset:12288\n"
"ds_read_b64_tr_b16 a[14:15], v68 offset:12800\n"
"ds_read_b64_tr_b16 a[16:17], v68 offset:13312\n"
"s_add_u32 s96, s96, 196608\n"
"s_add_u32 m0, s97, 126976\n"
"s_nop 0\n"
"buffer_load_dwordx4 v67, s[36:39], s96 offen offset:0 lds\n"
"ds_read_b64_tr_b16 a[18:19], v68 offset:13824\n"
"ds_read_b64_tr_b16 a[20:21], v72 offset:14336\n"
"buffer_load_dwordx4 v67, s[40:43], s96 offen offset:1024 lds\n"
"ds_read_b64_tr_b16 a[22:23], v72 offset:14848\n"
"ds_read_b128 v[76:79], v58 offset:0\n"
"buffer_load_dwordx4 v67, s[44:47], s96 offen offset:2048 lds\n"
"ds_read_b128 v[80:83], v58 offset:2048\n"
"ds_read_b128 v[84:87], v59 offset:0\n"
"ds_read_b128 v[88:91], v59 offset:2048\n"
"s_add_u32 s96, s96, 196608\n"
"s_waitcnt lgkmcnt(0)\n"
"s_waitcnt vmcnt(15)\n"
"s_barrier\n"
"s_add_u32 m0, s17, 40448\n"
"s_nop 0\n"
"buffer_load_dwordx4 v57, s[12:15], s16 offen offset:512 lds\n"
"ds_read_b64_tr_b16 a[24:25], v69 offset:0\n"
"ds_read_b64_tr_b16 a[26:27], v69 offset:512\n"
"v_mfma_f32_16x16x32_bf16 v[20:23], a[0:3], v[16:19], 0\n"
"v_mfma_f32_16x16x32_bf16 v[24:27], a[4:7], v[16:19], 0\n"
"s_add_u32 m0, s97, 0\n"
"s_nop 0\n"
"buffer_load_dwordx4 v67, s[36:39], s96 offen offset:0 lds\n"
"ds_read_b64_tr_b16 a[28:29], v69 offset:1024\n"
"ds_read_b64_tr_b16 a[30:31], v69 offset:1536\n"
"v_mfma_f32_16x16x32_bf16 v[32:35], v[16:19], a[8:11], 0\n"
"v_mfma_f32_16x16x32_bf16 v[36:39], a[4:7], v[76:79], 0\n"
"buffer_load_dwordx4 v67, s[40:43], s96 offen offset:1024 lds\n"
"ds_read_b64_tr_b16 a[32:33], v73 offset:2048\n"
"ds_read_b64_tr_b16 a[34:35], v73 offset:2560\n"
"v_mfma_f32_16x16x32_bf16 v[40:43], v[76:79], a[8:11], 0\n"
"v_mfma_f32_16x16x32_bf16 v[44:47], a[0:3], v[80:83], 0\n"
"buffer_load_dwordx4 v67, s[44:47], s96 offen offset:2048 lds\n"
"ds_read_b64_tr_b16 a[36:37], v69 offset:12288\n"
"ds_read_b64_tr_b16 a[38:39], v69 offset:12800\n"
"ds_read_b64_tr_b16 a[40:41], v69 offset:13312\n"
"v_mfma_f32_16x16x32_bf16 v[48:51], a[4:7], v[80:83], 0\n"
"v_mfma_f32_16x16x32_bf16 v[52:55], v[80:83], a[8:11], 0\n"
"v_mfma_f32_16x16x32_bf16 v[20:23], a[12:15], v[16:19], v[20:23]\n"
"s_add_u32 s96, s96, 196608\n"
"s_add_u32 m0, s97, 12288\n"
"s_nop 0\n"
"buffer_load_dwordx4 v67, s[36:39], s96 offen offset:0 lds\n"
"ds_read_b64_tr_b16 a[42:43], v69 offset:13824\n"
"ds_read_b64_tr_b16 a[44:45], v73 offset:14336\n"
"v_mfma_f32_16x16x32_bf16 v[24:27], a[16:19], v[16:19], v[24:27]\n"
"v_mfma_f32_16x16x32_bf16 v[32:35], v[16:19], a[20:23], v[32:35]\n"
"buffer_load_dwordx4 v67, s[40:43], s96 offen offset:1024 lds\n"
"ds_read_b64_tr_b16 a[46:47], v73 offset:14848\n"
"ds_read_b128 v[92:95], v58 offset:4096\n"
"v_mfma_f32_16x16x32_bf16 v[36:39], a[16:19], v[84:87], v[36:39]\n"
"v_mfma_f32_16x16x32_bf16 v[40:43], v[84:87], a[20:23], v[40:43]\n"
"buffer_load_dwordx4 v67, s[44:47], s96 offen offset:2048 lds\n"
"ds_read_b128 v[96:99], v58 offset:6144\n"
"ds_read_b128 v[100:103], v59 offset:4096\n"
"ds_read_b128 v[104:107], v59 offset:6144\n"
"v_mfma_f32_16x16x32_bf16 v[44:47], a[12:15], v[88:91], v[44:47]\n"
"v_mfma_f32_16x16x32_bf16 v[48:51], a[16:19], v[88:91], v[48:51]\n"
"v_mfma_f32_16x16x32_bf16 v[52:55], v[88:91], a[20:23], v[52:55]\n"
"s_add_u32 s96, s96, 196608\n"
"s_waitcnt lgkmcnt(0)\n"
"s_waitcnt vmcnt(15)\n"
"s_barrier\n"
"s_add_u32 m0, s17, 44416\n"
"s_nop 0\n"
"buffer_load_dwordx4 v57, s[12:15], s16 offen offset:640 lds\n"
"ds_read_b64_tr_b16 a[48:49], v70 offset:0\n"
"ds_read_b64_tr_b16 a[50:51], v70 offset:512\n"
"v_mfma_f32_16x16x32_bf16 v[20:23], a[24:27], v[16:19], v[20:23]\n"
"v_mfma_f32_16x16x32_bf16 v[24:27], a[28:31], v[16:19], v[24:27]\n"
"s_add_u32 m0, s97, 139264\n"
"s_nop 0\n"
"buffer_load_dwordx4 v67, s[36:39], s96 offen offset:0 lds\n"
"ds_read_b64_tr_b16 a[52:53], v70 offset:1024\n"
"ds_read_b64_tr_b16 a[54:55], v70 offset:1536\n"
"v_mfma_f32_16x16x32_bf16 v[32:35], v[16:19], a[32:35], v[32:35]\n"
"v_mfma_f32_16x16x32_bf16 v[36:39], a[28:31], v[92:95], v[36:39]\n"
"buffer_load_dwordx4 v67, s[40:43], s96 offen offset:1024 lds\n"
"ds_read_b64_tr_b16 a[56:57], v74 offset:2048\n"
"ds_read_b64_tr_b16 a[58:59], v74 offset:2560\n"
"v_mfma_f32_16x16x32_bf16 v[40:43], v[92:95], a[32:35], v[40:43]\n"
"v_mfma_f32_16x16x32_bf16 v[44:47], a[24:27], v[96:99], v[44:47]\n"
"buffer_load_dwordx4 v67, s[44:47], s96 offen offset:2048 lds\n"
"ds_read_b64_tr_b16 a[60:61], v70 offset:12288\n"
"ds_read_b64_tr_b16 a[62:63], v70 offset:12800\n"
"ds_read_b64_tr_b16 a[64:65], v70 offset:13312\n"
"v_mfma_f32_16x16x32_bf16 v[48:51], a[28:31], v[96:99], v[48:51]\n"
"v_mfma_f32_16x16x32_bf16 v[52:55], v[96:99], a[32:35], v[52:55]\n"
"v_mfma_f32_16x16x32_bf16 v[20:23], a[36:39], v[16:19], v[20:23]\n"
"s_add_u32 s96, s96, 196608\n"
"s_add_u32 m0, s97, 151552\n"
"s_nop 0\n"
"buffer_load_dwordx4 v67, s[36:39], s96 offen offset:0 lds\n"
"ds_read_b64_tr_b16 a[66:67], v70 offset:13824\n"
"ds_read_b64_tr_b16 a[68:69], v74 offset:14336\n"
"v_mfma_f32_16x16x32_bf16 v[24:27], a[40:43], v[16:19], v[24:27]\n"
"v_mfma_f32_16x16x32_bf16 v[32:35], v[16:19], a[44:47], v[32:35]\n"
"buffer_load_dwordx4 v67, s[40:43], s96 offen offset:1024 lds\n"
"ds_read_b64_tr_b16 a[70:71], v74 offset:14848\n"
"ds_read_b128 v[76:79], v58 offset:8192\n"
"v_mfma_f32_16x16x32_bf16 v[36:39], a[40:43], v[100:103], v[36:39]\n"
"v_mfma_f32_16x16x32_bf16 v[40:43], v[100:103], a[44:47], v[40:43]\n"
"buffer_load_dwordx4 v67, s[44:47], s96 offen offset:2048 lds\n"
"ds_read_b128 v[80:83], v58 offset:10240\n"
"ds_read_b128 v[84:87], v59 offset:8192\n"
"ds_read_b128 v[88:91], v59 offset:10240\n"
"v_mfma_f32_16x16x32_bf16 v[44:47], a[36:39], v[104:107], v[44:47]\n"
"v_mfma_f32_16x16x32_bf16 v[48:51], a[40:43], v[104:107], v[48:51]\n"
"v_mfma_f32_16x16x32_bf16 v[52:55], v[104:107], a[44:47], v[52:55]\n"
"s_add_u32 s96, s96, 196608\n"
"s_waitcnt lgkmcnt(0)\n"
"s_waitcnt vmcnt(14)\n"
"s_barrier\n"
"s_add_u32 m0, s17, 48384\n"
"s_nop 0\n"
"buffer_load_dwordx4 v57, s[12:15], s16 offen offset:768 lds\n"
"ds_read_b64_tr_b16 a[72:73], v71 offset:0\n"
"ds_read_b64_tr_b16 a[74:75], v71 offset:512\n"
"v_mfma_f32_16x16x32_bf16 v[20:23], a[48:51], v[16:19], v[20:23]\n"
"v_mfma_f32_16x16x32_bf16 v[24:27], a[52:55], v[16:19], v[24:27]\n"
"s_add_u32 m0, s97, 90112\n"
"s_nop 0\n"
"buffer_load_dwordx4 v67, s[36:39], s96 offen offset:0 lds\n"
"ds_read_b64_tr_b16 a[76:77], v71 offset:1024\n"
"ds_read_b64_tr_b16 a[78:79], v71 offset:1536\n"
"v_mfma_f32_16x16x32_bf16 v[32:35], v[16:19], a[56:59], v[32:35]\n"
"v_mfma_f32_16x16x32_bf16 v[36:39], a[52:55], v[76:79], v[36:39]\n"
"buffer_load_dwordx4 v67, s[40:43], s96 offen offset:1024 lds\n"
"ds_read_b64_tr_b16 a[80:81], v75 offset:2048\n"
"ds_read_b64_tr_b16 a[82:83], v75 offset:2560\n"
"v_mfma_f32_16x16x32_bf16 v[40:43], v[76:79], a[56:59], v[40:43]\n"
"v_mfma_f32_16x16x32_bf16 v[44:47], a[48:51], v[80:83], v[44:47]\n"
"buffer_load_dwordx4 v67, s[44:47], s96 offen offset:2048 lds\n"
"ds_read_b64_tr_b16 a[84:85], v71 offset:12288\n"
"ds_read_b64_tr_b16 a[86:87], v71 offset:12800\n"
"ds_read_b64_tr_b16 a[88:89], v71 offset:13312\n"
"v_mfma_f32_16x16x32_bf16 v[48:51], a[52:55], v[80:83], v[48:51]\n"
"v_mfma_f32_16x16x32_bf16 v[52:55], v[80:83], a[56:59], v[52:55]\n"
"v_mfma_f32_16x16x32_bf16 v[20:23], a[60:63], v[16:19], v[20:23]\n"
"s_add_u32 s96, s96, 196608\n"
"s_add_u32 m0, s97, 102400\n"
"s_nop 0\n"
"buffer_load_dwordx4 v67, s[36:39], s96 offen offset:0 lds\n"
"ds_read_b64_tr_b16 a[90:91], v71 offset:13824\n"
"ds_read_b64_tr_b16 a[92:93], v75 offset:14336\n"
"v_mfma_f32_16x16x32_bf16 v[24:27], a[64:67], v[16:19], v[24:27]\n"
"v_mfma_f32_16x16x32_bf16 v[32:35], v[16:19], a[68:71], v[32:35]\n"
"buffer_load_dwordx4 v67, s[40:43], s96 offen offset:1024 lds\n"
"ds_read_b64_tr_b16 a[94:95], v75 offset:14848\n"
"ds_read_b128 v[92:95], v58 offset:12288\n"
"v_mfma_f32_16x16x32_bf16 v[36:39], a[64:67], v[84:87], v[36:39]\n"
"v_mfma_f32_16x16x32_bf16 v[40:43], v[84:87], a[68:71], v[40:43]\n"
"buffer_load_dwordx4 v67, s[44:47], s96 offen offset:2048 lds\n"
"ds_read_b128 v[96:99], v58 offset:14336\n"
"ds_read_b128 v[100:103], v59 offset:12288\n"
"ds_read_b128 v[104:107], v59 offset:14336\n"
"v_mfma_f32_16x16x32_bf16 v[44:47], a[60:63], v[88:91], v[44:47]\n"
"v_mfma_f32_16x16x32_bf16 v[48:51], a[64:67], v[88:91], v[48:51]\n"
"v_mfma_f32_16x16x32_bf16 v[52:55], v[88:91], a[68:71], v[52:55]\n"
"s_add_u32 s96, s96, 196608\n"
"s_waitcnt lgkmcnt(0)\n"
"s_waitcnt vmcnt(14)\n"
"s_barrier\n"
"s_add_u32 m0, s17, 52352\n"
"s_nop 0\n"
"buffer_load_dwordx4 v57, s[12:15], s16 offen offset:896 lds\n"
"ds_read_b64_tr_b16 a[96:97], v68 offset:0\n"
"ds_read_b64_tr_b16 a[98:99], v68 offset:512\n"
"v_mfma_f32_16x16x32_bf16 v[20:23], a[72:75], v[16:19], v[20:23]\n"
"v_mfma_f32_16x16x32_bf16 v[24:27], a[76:79], v[16:19], v[24:27]\n"
"s_add_u32 m0, s97, 114688\n"
"s_nop 0\n"
"buffer_load_dwordx4 v67, s[36:39], s96 offen offset:0 lds\n"
"ds_read_b64_tr_b16 a[100:101], v68 offset:1024\n"
"ds_read_b64_tr_b16 a[102:103], v68 offset:1536\n"
"v_mfma_f32_16x16x32_bf16 v[32:35], v[16:19], a[80:83], v[32:35]\n"
"v_mfma_f32_16x16x32_bf16 v[36:39], a[76:79], v[92:95], v[36:39]\n"
"buffer_load_dwordx4 v67, s[40:43], s96 offen offset:1024 lds\n"
"ds_read_b64_tr_b16 a[104:105], v72 offset:2048\n"
"ds_read_b64_tr_b16 a[106:107], v72 offset:2560\n"
"v_mfma_f32_16x16x32_bf16 v[40:43], v[92:95], a[80:83], v[40:43]\n"
"v_mfma_f32_16x16x32_bf16 v[44:47], a[72:75], v[96:99], v[44:47]\n"
"buffer_load_dwordx4 v67, s[44:47], s96 offen offset:2048 lds\n"
"ds_read_b64_tr_b16 a[108:109], v68 offset:12288\n"
"ds_read_b64_tr_b16 a[110:111], v68 offset:12800\n"
"ds_read_b64_tr_b16 a[112:113], v68 offset:13312\n"
"v_mfma_f32_16x16x32_bf16 v[48:51], a[76:79], v[96:99], v[48:51]\n"
"v_mfma_f32_16x16x32_bf16 v[52:55], v[96:99], a[80:83], v[52:55]\n"
"v_mfma_f32_16x16x32_bf16 v[20:23], a[84:87], v[16:19], v[20:23]\n"
"s_add_u32 s96, s96, 196608\n"
"s_add_u32 m0, s97, 126976\n"
"s_nop 0\n"
"buffer_load_dwordx4 v67, s[36:39], s96 offen offset:0 lds\n"
"ds_read_b64_tr_b16 a[114:115], v68 offset:13824\n"
"ds_read_b64_tr_b16 a[116:117], v72 offset:14336\n"
"v_mfma_f32_16x16x32_bf16 v[24:27], a[88:91], v[16:19], v[24:27]\n"
"v_mfma_f32_16x16x32_bf16 v[32:35], v[16:19], a[92:95], v[32:35]\n"
"buffer_load_dwordx4 v67, s[40:43], s96 offen offset:1024 lds\n"
"ds_read_b64_tr_b16 a[118:119], v72 offset:14848\n"
"ds_read_b128 v[76:79], v58 offset:16384\n"
"v_mfma_f32_16x16x32_bf16 v[36:39], a[88:91], v[100:103], v[36:39]\n"
"v_mfma_f32_16x16x32_bf16 v[40:43], v[100:103], a[92:95], v[40:43]\n"
"buffer_load_dwordx4 v67, s[44:47], s96 offen offset:2048 lds\n"
"ds_read_b128 v[80:83], v58 offset:18432\n"
"ds_read_b128 v[84:87], v59 offset:16384\n"
"ds_read_b128 v[88:91], v59 offset:18432\n"
"v_mfma_f32_16x16x32_bf16 v[44:47], a[84:87], v[104:107], v[44:47]\n"
"v_mfma_f32_16x16x32_bf16 v[48:51], a[88:91], v[104:107], v[48:51]\n"
"v_mfma_f32_16x16x32_bf16 v[52:55], v[104:107], a[92:95], v[52:55]\n"
"s_add_u32 s96, s96, 196608\n"
"s_waitcnt lgkmcnt(0)\n"
"s_waitcnt vmcnt(14)\n"
"s_barrier\n"
"s_add_u32 m0, s17, 56320\n"
"s_nop 0\n"
"buffer_load_dwordx4 v57, s[12:15], s16 offen offset:1024 lds\n"
"ds_read_b64_tr_b16 a[120:121], v69 offset:0\n"
"ds_read_b64_tr_b16 a[122:123], v69 offset:512\n"
"v_mfma_f32_16x16x32_bf16 v[20:23], a[96:99], v[16:19], v[20:23]\n"
"v_mfma_f32_16x16x32_bf16 v[24:27], a[100:103], v[16:19], v[24:27]\n"
"s_add_u32 m0, s97, 0\n"
"s_nop 0\n"
"buffer_load_dwordx4 v67, s[36:39], s96 offen offset:0 lds\n"
"ds_read_b64_tr_b16 a[124:125], v69 offset:1024\n"
"ds_read_b64_tr_b16 a[126:127], v69 offset:1536\n"
"v_mfma_f32_16x16x32_bf16 v[32:35], v[16:19], a[104:107], v[32:35]\n"
"v_mfma_f32_16x16x32_bf16 v[36:39], a[100:103], v[76:79], v[36:39]\n"
"buffer_load_dwordx4 v67, s[40:43], s96 offen offset:1024 lds\n"
"ds_read_b64_tr_b16 a[128:129], v73 offset:2048\n"
"ds_read_b64_tr_b16 a[130:131], v73 offset:2560\n"
"v_mfma_f32_16x16x32_bf16 v[40:43], v[76:79], a[104:107], v[40:43]\n"
"v_mfma_f32_16x16x32_bf16 v[44:47], a[96:99], v[80:83], v[44:47]\n"
"buffer_load_dwordx4 v67, s[44:47], s96 offen offset:2048 lds\n"
"ds_read_b64_tr_b16 a[132:133], v69 offset:12288\n"
"ds_read_b64_tr_b16 a[134:135], v69 offset:12800\n"
"ds_read_b64_tr_b16 a[136:137], v69 offset:13312\n"
"v_mfma_f32_16x16x32_bf16 v[48:51], a[100:103], v[80:83], v[48:51]\n"
"v_mfma_f32_16x16x32_bf16 v[52:55], v[80:83], a[104:107], v[52:55]\n"
"v_mfma_f32_16x16x32_bf16 v[20:23], a[108:111], v[16:19], v[20:23]\n"
"s_add_u32 s96, s96, 196608\n"
"s_add_u32 m0, s97, 12288\n"
"s_nop 0\n"
"buffer_load_dwordx4 v67, s[36:39], s96 offen offset:0 lds\n"
"ds_read_b64_tr_b16 a[138:139], v69 offset:13824\n"
"ds_read_b64_tr_b16 a[140:141], v73 offset:14336\n"
"v_mfma_f32_16x16x32_bf16 v[24:27], a[112:115], v[16:19], v[24:27]\n"
"v_mfma_f32_16x16x32_bf16 v[32:35], v[16:19], a[116:119], v[32:35]\n"
"buffer_load_dwordx4 v67, s[40:43], s96 offen offset:1024 lds\n"
"ds_read_b64_tr_b16 a[142:143], v73 offset:14848\n"
"ds_read_b128 v[92:95], v58 offset:20480\n"
"v_mfma_f32_16x16x32_bf16 v[36:39], a[112:115], v[84:87], v[36:39]\n"
"v_mfma_f32_16x16x32_bf16 v[40:43], v[84:87], a[116:119], v[40:43]\n"
"buffer_load_dwordx4 v67, s[44:47], s96 offen offset:2048 lds\n"
"ds_read_b128 v[96:99], v58 offset:22528\n"
"ds_read_b128 v[100:103], v59 offset:20480\n"
"ds_read_b128 v[104:107], v59 offset:22528\n"
"v_mfma_f32_16x16x32_bf16 v[44:47], a[108:111], v[88:91], v[44:47]\n"
"v_mfma_f32_16x16x32_bf16 v[48:51], a[112:115], v[88:91], v[48:51]\n"
"v_mfma_f32_16x16x32_bf16 v[52:55], v[88:91], a[116:119], v[52:55]\n"
"s_add_u32 s96, s96, 196608\n"
"s_waitcnt lgkmcnt(0)\n"
"s_waitcnt vmcnt(14)\n"
"s_barrier\n"
"s_add_u32 m0, s17, 60288\n"
"s_nop 0\n"
"buffer_load_dwordx4 v57, s[12:15], s16 offen offset:1152 lds\n"
"ds_read_b64_tr_b16 a[144:145], v70 offset:0\n"
"ds_read_b64_tr_b16 a[146:147], v70 offset:512\n"
"v_mfma_f32_16x16x32_bf16 v[20:23], a[120:123], v[16:19], v[20:23]\n"
"v_mfma_f32_16x16x32_bf16 v[24:27], a[124:127], v[16:19], v[24:27]\n"
"s_add_u32 m0, s97, 139264\n"
"s_nop 0\n"
"buffer_load_dwordx4 v67, s[36:39], s96 offen offset:0 lds\n"
"ds_read_b64_tr_b16 a[148:149], v70 offset:1024\n"
"ds_read_b64_tr_b16 a[150:151], v70 offset:1536\n"
"v_mfma_f32_16x16x32_bf16 v[32:35], v[16:19], a[128:131], v[32:35]\n"
"v_mfma_f32_16x16x32_bf16 v[36:39], a[124:127], v[92:95], v[36:39]\n"
"buffer_load_dwordx4 v67, s[40:43], s96 offen offset:1024 lds\n"
"ds_read_b64_tr_b16 a[152:153], v74 offset:2048\n"
"ds_read_b64_tr_b16 a[154:155], v74 offset:2560\n"
"v_mfma_f32_16x16x32_bf16 v[40:43], v[92:95], a[128:131], v[40:43]\n"
"v_mfma_f32_16x16x32_bf16 v[44:47], a[120:123], v[96:99], v[44:47]\n"
"buffer_load_dwordx4 v67, s[44:47], s96 offen offset:2048 lds\n"
"ds_read_b64_tr_b16 a[156:157], v70 offset:12288\n"
"ds_read_b64_tr_b16 a[158:159], v70 offset:12800\n"
"ds_read_b64_tr_b16 a[160:161], v70 offset:13312\n"
"v_mfma_f32_16x16x32_bf16 v[48:51], a[124:127], v[96:99], v[48:51]\n"
"v_mfma_f32_16x16x32_bf16 v[52:55], v[96:99], a[128:131], v[52:55]\n"
"v_mfma_f32_16x16x32_bf16 v[20:23], a[132:135], v[16:19], v[20:23]\n"
"s_add_u32 s96, s96, 196608\n"
"s_add_u32 m0, s97, 151552\n"
"s_nop 0\n"
"buffer_load_dwordx4 v67, s[36:39], s96 offen offset:0 lds\n"
"ds_read_b64_tr_b16 a[162:163], v70 offset:13824\n"
"ds_read_b64_tr_b16 a[164:165], v74 offset:14336\n"
"v_mfma_f32_16x16x32_bf16 v[24:27], a[136:139], v[16:19], v[24:27]\n"
"v_mfma_f32_16x16x32_bf16 v[32:35], v[16:19], a[140:143], v[32:35]\n"
"buffer_load_dwordx4 v67, s[40:43], s96 offen offset:1024 lds\n"
"ds_read_b64_tr_b16 a[166:167], v74 offset:14848\n"
"ds_read_b128 v[76:79], v58 offset:24576\n"
"v_mfma_f32_16x16x32_bf16 v[36:39], a[136:139], v[100:103], v[36:39]\n"
"v_mfma_f32_16x16x32_bf16 v[40:43], v[100:103], a[140:143], v[40:43]\n"
"buffer_load_dwordx4 v67, s[44:47], s96 offen offset:2048 lds\n"
"ds_read_b128 v[80:83], v58 offset:26624\n"
"ds_read_b128 v[84:87], v59 offset:24576\n"
"ds_read_b128 v[88:91], v59 offset:26624\n"
"v_mfma_f32_16x16x32_bf16 v[44:47], a[132:135], v[104:107], v[44:47]\n"
"v_mfma_f32_16x16x32_bf16 v[48:51], a[136:139], v[104:107], v[48:51]\n"
"v_mfma_f32_16x16x32_bf16 v[52:55], v[104:107], a[140:143], v[52:55]\n"
"s_add_u32 s96, s96, 196608\n"
"s_waitcnt lgkmcnt(0)\n"
"s_waitcnt vmcnt(14)\n"
"s_barrier\n"
"s_add_u32 m0, s17, 64256\n"
"s_nop 0\n"
"buffer_load_dwordx4 v57, s[12:15], s16 offen offset:1280 lds\n"
"ds_read_b64_tr_b16 a[168:169], v71 offset:0\n"
"ds_read_b64_tr_b16 a[170:171], v71 offset:512\n"
"v_mfma_f32_16x16x32_bf16 v[20:23], a[144:147], v[16:19], v[20:23]\n"
"v_mfma_f32_16x16x32_bf16 v[24:27], a[148:151], v[16:19], v[24:27]\n"
"s_add_u32 m0, s97, 90112\n"
"s_nop 0\n"
"buffer_load_dwordx4 v67, s[36:39], s96 offen offset:0 lds\n"
"ds_read_b64_tr_b16 a[172:173], v71 offset:1024\n"
"ds_read_b64_tr_b16 a[174:175], v71 offset:1536\n"
"v_mfma_f32_16x16x32_bf16 v[32:35], v[16:19], a[152:155], v[32:35]\n"
"v_mfma_f32_16x16x32_bf16 v[36:39], a[148:151], v[76:79], v[36:39]\n"
"buffer_load_dwordx4 v67, s[40:43], s96 offen offset:1024 lds\n"
"ds_read_b64_tr_b16 a[176:177], v75 offset:2048\n"
"ds_read_b64_tr_b16 a[178:179], v75 offset:2560\n"
"v_mfma_f32_16x16x32_bf16 v[40:43], v[76:79], a[152:155], v[40:43]\n"
"v_mfma_f32_16x16x32_bf16 v[44:47], a[144:147], v[80:83], v[44:47]\n"
"buffer_load_dwordx4 v67, s[44:47], s96 offen offset:2048 lds\n"
"ds_read_b64_tr_b16 a[180:181], v71 offset:12288\n"
"ds_read_b64_tr_b16 a[182:183], v71 offset:12800\n"
"v_mfma_f32_16x16x32_bf16 v[48:51], a[148:151], v[80:83], v[48:51]\n"
"v_mfma_f32_16x16x32_bf16 v[52:55], v[80:83], a[152:155], v[52:55]\n"
"s_add_u32 s96, s96, 196608\n"
"s_add_u32 m0, s97, 102400\n"
"s_nop 0\n"
"buffer_load_dwordx4 v67, s[36:39], s96 offen offset:0 lds\n"
"ds_read_b64_tr_b16 a[184:185], v71 offset:13312\n"
"ds_read_b64_tr_b16 a[186:187], v71 offset:13824\n"
"v_mfma_f32_16x16x32_bf16 v[20:23], a[156:159], v[16:19], v[20:23]\n"
"v_mfma_f32_16x16x32_bf16 v[24:27], a[160:163], v[16:19], v[24:27]\n"
"buffer_load_dwordx4 v67, s[40:43], s96 offen offset:1024 lds\n"
"ds_read_b64_tr_b16 a[188:189], v75 offset:14336\n"
"ds_read_b64_tr_b16 a[190:191], v75 offset:14848\n"
"v_mfma_f32_16x16x32_bf16 v[32:35], v[16:19], a[164:167], v[32:35]\n"
"v_mfma_f32_16x16x32_bf16 v[36:39], a[160:163], v[84:87], v[36:39]\n"
"buffer_load_dwordx4 v67, s[44:47], s96 offen offset:2048 lds\n"
"ds_read_b128 v[92:95], v58 offset:28672\n"
"ds_read_b128 v[96:99], v58 offset:30720\n"
"v_mfma_f32_16x16x32_bf16 v[40:43], v[84:87], a[164:167], v[40:43]\n"
"v_mfma_f32_16x16x32_bf16 v[44:47], a[156:159], v[88:91], v[44:47]\n"
"s_add_u32 s96, s96, 196608\n"
"buffer_load_dwordx2 v[60:61], v56, s[32:35], s66 offen sc1\n"
"ds_read_b128 v[100:103], v59 offset:28672\n"
"ds_read_b128 v[104:107], v59 offset:30720\n"
"v_mfma_f32_16x16x32_bf16 v[48:51], a[160:163], v[88:91], v[48:51]\n"
"v_mfma_f32_16x16x32_bf16 v[52:55], v[88:91], a[164:167], v[52:55]\n"
"s_waitcnt lgkmcnt(0)\n"
"s_waitcnt vmcnt(15)\n"
"s_barrier\n"
"s_add_u32 m0, s17, 68224\n"
"s_nop 0\n"
"buffer_load_dwordx4 v57, s[12:15], s16 offen offset:1408 lds\n"
"ds_read_b64_tr_b16 a[192:193], v68 offset:0\n"
"ds_read_b64_tr_b16 a[194:195], v68 offset:512\n"
"v_mfma_f32_16x16x32_bf16 v[20:23], a[168:171], v[16:19], v[20:23]\n"
"v_mfma_f32_16x16x32_bf16 v[24:27], a[172:175], v[16:19], v[24:27]\n"
"s_add_u32 m0, s97, 114688\n"
"s_nop 0\n"
"buffer_load_dwordx4 v67, s[36:39], s96 offen offset:0 lds\n"
"ds_read_b64_tr_b16 a[196:197], v68 offset:1024\n"
"ds_read_b64_tr_b16 a[198:199], v68 offset:1536\n"
"v_mfma_f32_16x16x32_bf16 v[32:35], v[16:19], a[176:179], v[32:35]\n"
"v_mfma_f32_16x16x32_bf16 v[36:39], a[172:175], v[92:95], v[36:39]\n"
"buffer_load_dwordx4 v67, s[40:43], s96 offen offset:1024 lds\n"
"ds_read_b64_tr_b16 a[200:201], v72 offset:2048\n"
"ds_read_b64_tr_b16 a[202:203], v72 offset:2560\n"
"v_mfma_f32_16x16x32_bf16 v[40:43], v[92:95], a[176:179], v[40:43]\n"
"v_mfma_f32_16x16x32_bf16 v[44:47], a[168:171], v[96:99], v[44:47]\n"
"buffer_load_dwordx4 v67, s[44:47], s96 offen offset:2048 lds\n"
"ds_read_b64_tr_b16 a[204:205], v68 offset:12288\n"
"ds_read_b64_tr_b16 a[206:207], v68 offset:12800\n"
"ds_read_b64_tr_b16 a[208:209], v68 offset:13312\n"
"v_mfma_f32_16x16x32_bf16 v[48:51], a[172:175], v[96:99], v[48:51]\n"
"v_mfma_f32_16x16x32_bf16 v[52:55], v[96:99], a[176:179], v[52:55]\n"
"v_mfma_f32_16x16x32_bf16 v[20:23], a[180:183], v[16:19], v[20:23]\n"
"s_add_u32 s96, s96, 196608\n"
"s_add_u32 m0, s97, 126976\n"
"s_nop 0\n"
"buffer_load_dwordx4 v67, s[36:39], s96 offen offset:0 lds\n"
"ds_read_b64_tr_b16 a[210:211], v68 offset:13824\n"
"ds_read_b64_tr_b16 a[212:213], v72 offset:14336\n"
"v_mfma_f32_16x16x32_bf16 v[24:27], a[184:187], v[16:19], v[24:27]\n"
"v_mfma_f32_16x16x32_bf16 v[32:35], v[16:19], a[188:191], v[32:35]\n"
"buffer_load_dwordx4 v67, s[40:43], s96 offen offset:1024 lds\n"
"ds_read_b64_tr_b16 a[214:215], v72 offset:14848\n"
"ds_read_b128 v[76:79], v58 offset:32768\n"
"v_mfma_f32_16x16x32_bf16 v[36:39], a[184:187], v[100:103], v[36:39]\n"
"v_mfma_f32_16x16x32_bf16 v[40:43], v[100:103], a[188:191], v[40:43]\n"
"buffer_load_dwordx4 v67, s[44:47], s96 offen offset:2048 lds\n"
"ds_read_b128 v[80:83], v58 offset:34816\n"
"ds_read_b128 v[84:87], v59 offset:32768\n"
"ds_read_b128 v[88:91], v59 offset:34816\n"
"v_mfma_f32_16x16x32_bf16 v[44:47], a[180:183], v[104:107], v[44:47]\n"
"v_mfma_f32_16x16x32_bf16 v[48:51], a[184:187], v[104:107], v[48:51]\n"
"v_mfma_f32_16x16x32_bf16 v[52:55], v[104:107], a[188:191], v[52:55]\n"
"s_add_u32 s96, s96, 196608\n"
"s_waitcnt lgkmcnt(0)\n"
"s_waitcnt vmcnt(15)\n"
"s_barrier\n"
"s_add_u32 m0, s17, 72192\n"
"s_nop 0\n"
"buffer_load_dwordx4 v57, s[12:15], s16 offen offset:1536 lds\n"
"ds_read_b64_tr_b16 a[216:217], v69 offset:0\n"
"ds_read_b64_tr_b16 a[218:219], v69 offset:512\n"
"v_mfma_f32_16x16x32_bf16 v[20:23], a[192:195], v[16:19], v[20:23]\n"
"v_mfma_f32_16x16x32_bf16 v[24:27], a[196:199], v[16:19], v[24:27]\n"
"s_add_u32 m0, s97, 0\n"
"s_nop 0\n"
"buffer_load_dwordx4 v67, s[36:39], s96 offen offset:0 lds\n"
"ds_read_b64_tr_b16 a[220:221], v69 offset:1024\n"
"ds_read_b64_tr_b16 a[222:223], v69 offset:1536\n"
"v_mfma_f32_16x16x32_bf16 v[32:35], v[16:19], a[200:203], v[32:35]\n"
"v_mfma_f32_16x16x32_bf16 v[36:39], a[196:199], v[76:79], v[36:39]\n"
"buffer_load_dwordx4 v67, s[40:43], s96 offen offset:1024 lds\n"
"ds_read_b64_tr_b16 a[224:225], v73 offset:2048\n"
"ds_read_b64_tr_b16 a[226:227], v73 offset:2560\n"
"v_mfma_f32_16x16x32_bf16 v[40:43], v[76:79], a[200:203], v[40:43]\n"
"v_mfma_f32_16x16x32_bf16 v[44:47], a[192:195], v[80:83], v[44:47]\n"
"buffer_load_dwordx4 v67, s[44:47], s96 offen offset:2048 lds\n"
"ds_read_b64_tr_b16 a[228:229], v69 offset:12288\n"
"ds_read_b64_tr_b16 a[230:231], v69 offset:12800\n"
"ds_read_b64_tr_b16 a[232:233], v69 offset:13312\n"
"v_mfma_f32_16x16x32_bf16 v[48:51], a[196:199], v[80:83], v[48:51]\n"
"v_mfma_f32_16x16x32_bf16 v[52:55], v[80:83], a[200:203], v[52:55]\n"
"v_mfma_f32_16x16x32_bf16 v[20:23], a[204:207], v[16:19], v[20:23]\n"
"s_add_u32 s96, s96, 196608\n"
"s_add_u32 m0, s97, 12288\n"
"s_nop 0\n"
"buffer_load_dwordx4 v67, s[36:39], s96 offen offset:0 lds\n"
"ds_read_b64_tr_b16 a[234:235], v69 offset:13824\n"
"ds_read_b64_tr_b16 a[236:237], v73 offset:14336\n"
"v_mfma_f32_16x16x32_bf16 v[24:27], a[208:211], v[16:19], v[24:27]\n"
"v_mfma_f32_16x16x32_bf16 v[32:35], v[16:19], a[212:215], v[32:35]\n"
"buffer_load_dwordx4 v67, s[40:43], s96 offen offset:1024 lds\n"
"ds_read_b64_tr_b16 a[238:239], v73 offset:14848\n"
"ds_read_b128 v[92:95], v58 offset:36864\n"
"v_mfma_f32_16x16x32_bf16 v[36:39], a[208:211], v[84:87], v[36:39]\n"
"v_mfma_f32_16x16x32_bf16 v[40:43], v[84:87], a[212:215], v[40:43]\n"
"buffer_load_dwordx4 v67, s[44:47], s96 offen offset:2048 lds\n"
"ds_read_b128 v[96:99], v58 offset:38912\n"
"ds_read_b128 v[100:103], v59 offset:36864\n"
"ds_read_b128 v[104:107], v59 offset:38912\n"
"v_mfma_f32_16x16x32_bf16 v[44:47], a[204:207], v[88:91], v[44:47]\n"
"v_mfma_f32_16x16x32_bf16 v[48:51], a[208:211], v[88:91], v[48:51]\n"
"v_mfma_f32_16x16x32_bf16 v[52:55], v[88:91], a[212:215], v[52:55]\n"
"s_add_u32 s96, s96, 196608\n"
"s_waitcnt lgkmcnt(0)\n"
"s_waitcnt vmcnt(15)\n"
"s_barrier\n"
"s_add_u32 m0, s17, 76160\n"
"s_nop 0\n"
"buffer_load_dwordx4 v57, s[12:15], s16 offen offset:1664 lds\n"
"ds_read_b64_tr_b16 a[240:241], v70 offset:0\n"
"ds_read_b64_tr_b16 a[242:243], v70 offset:512\n"
"v_mfma_f32_16x16x32_bf16 v[20:23], a[216:219], v[16:19], v[20:23]\n"
"v_mfma_f32_16x16x32_bf16 v[24:27], a[220:223], v[16:19], v[24:27]\n"
"s_add_u32 m0, s97, 139264\n"
"s_nop 0\n"
"buffer_load_dwordx4 v67, s[36:39], s96 offen offset:0 lds\n"
"ds_read_b64_tr_b16 a[244:245], v70 offset:1024\n"
"ds_read_b64_tr_b16 a[246:247], v70 offset:1536\n"
"v_mfma_f32_16x16x32_bf16 v[32:35], v[16:19], a[224:227], v[32:35]\n"
"v_mfma_f32_16x16x32_bf16 v[36:39], a[220:223], v[92:95], v[36:39]\n"
"buffer_load_dwordx4 v67, s[40:43], s96 offen offset:1024 lds\n"
"ds_read_b64_tr_b16 a[248:249], v74 offset:2048\n"
"ds_read_b64_tr_b16 a[250:251], v74 offset:2560\n"
"v_mfma_f32_16x16x32_bf16 v[40:43], v[92:95], a[224:227], v[40:43]\n"
"v_mfma_f32_16x16x32_bf16 v[44:47], a[216:219], v[96:99], v[44:47]\n"
"buffer_load_dwordx4 v67, s[44:47], s96 offen offset:2048 lds\n"
"ds_read_b64_tr_b16 a[252:253], v70 offset:12288\n"
"ds_read_b64_tr_b16 a[254:255], v70 offset:12800\n"
"ds_read_b64_tr_b16 v[128:129], v70 offset:13312\n"
"v_mfma_f32_16x16x32_bf16 v[48:51], a[220:223], v[96:99], v[48:51]\n"
"v_mfma_f32_16x16x32_bf16 v[52:55], v[96:99], a[224:227], v[52:55]\n"
"v_mfma_f32_16x16x32_bf16 v[20:23], a[228:231], v[16:19], v[20:23]\n"
"s_add_u32 s96, s96, 196608\n"
"s_add_u32 m0, s97, 151552\n"
"s_nop 0\n"
"buffer_load_dwordx4 v67, s[36:39], s96 offen offset:0 lds\n"
"ds_read_b64_tr_b16 v[130:131], v70 offset:13824\n"
"ds_read_b64_tr_b16 v[132:133], v74 offset:14336\n"
"v_mfma_f32_16x16x32_bf16 v[24:27], a[232:235], v[16:19], v[24:27]\n"
"v_mfma_f32_16x16x32_bf16 v[32:35], v[16:19], a[236:239], v[32:35]\n"
"buffer_load_dwordx4 v67, s[40:43], s96 offen offset:1024 lds\n"
"ds_read_b64_tr_b16 v[134:135], v74 offset:14848\n"
"ds_read_b128 v[76:79], v58 offset:40960\n"
"v_mfma_f32_16x16x32_bf16 v[36:39], a[232:235], v[100:103], v[36:39]\n"
"v_mfma_f32_16x16x32_bf16 v[40:43], v[100:103], a[236:239], v[40:43]\n"
"buffer_load_dwordx4 v67, s[44:47], s96 offen offset:2048 lds\n"
"ds_read_b128 v[80:83], v58 offset:43008\n"
"ds_read_b128 v[84:87], v59 offset:40960\n"
"ds_read_b128 v[88:91], v59 offset:43008\n"
"v_mfma_f32_16x16x32_bf16 v[44:47], a[228:231], v[104:107], v[44:47]\n"
"v_mfma_f32_16x16x32_bf16 v[48:51], a[232:235], v[104:107], v[48:51]\n"
"v_mfma_f32_16x16x32_bf16 v[52:55], v[104:107], a[236:239], v[52:55]\n"
"s_add_u32 s96, s96, 196608\n"
"s_waitcnt lgkmcnt(0)\n"
"s_waitcnt vmcnt(14)\n"
"s_barrier\n"
"s_add_u32 m0, s17, 80128\n"
"s_nop 0\n"
"buffer_load_dwordx4 v57, s[12:15], s16 offen offset:1792 lds\n"
"ds_read_b64_tr_b16 v[136:137], v71 offset:0\n"
"ds_read_b64_tr_b16 v[138:139], v71 offset:512\n"
"v_mfma_f32_16x16x32_bf16 v[20:23], a[240:243], v[16:19], v[20:23]\n"
"v_mfma_f32_16x16x32_bf16 v[24:27], a[244:247], v[16:19], v[24:27]\n"
"s_add_u32 m0, s97, 90112\n"
"s_nop 0\n"
"buffer_load_dwordx4 v67, s[36:39], s96 offen offset:0 lds\n"
"ds_read_b64_tr_b16 v[140:141], v71 offset:1024\n"
"ds_read_b64_tr_b16 v[142:143], v71 offset:1536\n"
"v_mfma_f32_16x16x32_bf16 v[32:35], v[16:19], a[248:251], v[32:35]\n"
"v_mfma_f32_16x16x32_bf16 v[36:39], a[244:247], v[76:79], v[36:39]\n"
"buffer_load_dwordx4 v67, s[40:43], s96 offen offset:1024 lds\n"
"ds_read_b64_tr_b16 v[144:145], v75 offset:2048\n"
"ds_read_b64_tr_b16 v[146:147], v75 offset:2560\n"
"v_mfma_f32_16x16x32_bf16 v[40:43], v[76:79], a[248:251], v[40:43]\n"
"v_mfma_f32_16x16x32_bf16 v[44:47], a[240:243], v[80:83], v[44:47]\n"
"buffer_load_dwordx4 v67, s[44:47], s96 offen offset:2048 lds\n"
"ds_read_b64_tr_b16 v[148:149], v71 offset:12288\n"
"ds_read_b64_tr_b16 v[150:151], v71 offset:12800\n"
"ds_read_b64_tr_b16 v[152:153], v71 offset:13312\n"
"v_mfma_f32_16x16x32_bf16 v[48:51], a[244:247], v[80:83], v[48:51]\n"
"v_mfma_f32_16x16x32_bf16 v[52:55], v[80:83], a[248:251], v[52:55]\n"
"v_mfma_f32_16x16x32_bf16 v[20:23], a[252:255], v[16:19], v[20:23]\n"
"s_add_u32 s96, s96, 196608\n"
"s_add_u32 m0, s97, 102400\n"
"s_nop 0\n"
"buffer_load_dwordx4 v67, s[36:39], s96 offen offset:0 lds\n"
"ds_read_b64_tr_b16 v[154:155], v71 offset:13824\n"
"ds_read_b64_tr_b16 v[156:157], v75 offset:14336\n"
"v_mfma_f32_16x16x32_bf16 v[24:27], v[128:131], v[16:19], v[24:27]\n"
"v_mfma_f32_16x16x32_bf16 v[32:35], v[16:19], v[132:135], v[32:35]\n"
"buffer_load_dwordx4 v67, s[40:43], s96 offen offset:1024 lds\n"
"ds_read_b64_tr_b16 v[158:159], v75 offset:14848\n"
"ds_read_b128 v[92:95], v58 offset:45056\n"
"v_mfma_f32_16x16x32_bf16 v[36:39], v[128:131], v[84:87], v[36:39]\n"
"v_mfma_f32_16x16x32_bf16 v[40:43], v[84:87], v[132:135], v[40:43]\n"
"buffer_load_dwordx4 v67, s[44:47], s96 offen offset:2048 lds\n"
"ds_read_b128 v[96:99], v58 offset:47104\n"
"ds_read_b128 v[100:103], v59 offset:45056\n"
"ds_read_b128 v[104:107], v59 offset:47104\n"
"v_mfma_f32_16x16x32_bf16 v[44:47], a[252:255], v[88:91], v[44:47]\n"
"v_mfma_f32_16x16x32_bf16 v[48:51], v[128:131], v[88:91], v[48:51]\n"
"v_mfma_f32_16x16x32_bf16 v[52:55], v[88:91], v[132:135], v[52:55]\n"
"s_add_u32 s96, s96, 196608\n"
"s_waitcnt lgkmcnt(0)\n"
"s_waitcnt vmcnt(14)\n"
"s_barrier\n"
"s_add_u32 m0, s17, 84096\n"
"s_nop 0\n"
"buffer_load_dwordx4 v57, s[12:15], s16 offen offset:1920 lds\n"
"ds_read_b64_tr_b16 v[160:161], v68 offset:0\n"
"v_mfma_f32_16x16x32_bf16 v[20:23], v[136:139], v[16:19], v[20:23]\n"
"s_add_u32 m0, s97, 114688\n"
"s_nop 0\n"
"buffer_load_dwordx4 v67, s[36:39], s96 offen offset:0 lds\n"
"ds_read_b64_tr_b16 v[162:163], v68 offset:512\n"
"v_mfma_f32_16x16x32_bf16 v[24:27], v[140:143], v[16:19], v[24:27]\n"
"buffer_load_dwordx4 v67, s[40:43], s96 offen offset:1024 lds\n"
"ds_read_b64_tr_b16 v[164:165], v68 offset:1024\n"
"v_mfma_f32_16x16x32_bf16 v[32:35], v[16:19], v[144:147], v[32:35]\n"
"buffer_load_dwordx4 v67, s[44:47], s96 offen offset:2048 lds\n"
"ds_read_b64_tr_b16 v[166:167], v68 offset:1536\n"
"v_mfma_f32_16x16x32_bf16 v[36:39], v[140:143], v[92:95], v[36:39]\n"
"s_add_u32 s96, s96, 196608\n"
"s_add_u32 m0, s97, 126976\n"
"s_nop 0\n"
"buffer_load_dwordx4 v67, s[36:39], s96 offen offset:0 lds\n"
"ds_read_b64_tr_b16 v[168:169], v72 offset:2048\n"
"v_mfma_f32_16x16x32_bf16 v[40:43], v[92:95], v[144:147], v[40:43]\n"
"buffer_load_dwordx4 v67, s[40:43], s96 offen offset:1024 lds\n"
"ds_read_b64_tr_b16 v[170:171], v72 offset:2560\n"
"v_mfma_f32_16x16x32_bf16 v[44:47], v[136:139], v[96:99], v[44:47]\n"
"buffer_load_dwordx4 v67, s[44:47], s96 offen offset:2048 lds\n"
"ds_read_b64_tr_b16 v[172:173], v68 offset:12288\n"
"v_mfma_f32_16x16x32_bf16 v[48:51], v[140:143], v[96:99], v[48:51]\n"
"s_add_u32 s96, s96, 196608\n"
"v_cmp_ne_u64 vcc, s[54:55], v[60:61]\n"
"s_cbranch_vccnz .L_rtqe\n"
".L_bkqe:\n"
"buffer_load_dword v108, v6, s[32:35], s74 offen sc1\n"
"ds_read_b64_tr_b16 v[174:175], v68 offset:12800\n"
"v_mfma_f32_16x16x32_bf16 v[52:55], v[96:99], v[144:147], v[52:55]\n"
"s_add_u32 s74, s74, 9216\n"
"buffer_load_dword v109, v6, s[32:35], s74 offen sc1\n"
"ds_read_b64_tr_b16 v[176:177], v68 offset:13312\n"
"v_mfma_f32_16x16x32_bf16 v[20:23], v[148:151], v[16:19], v[20:23]\n"
"s_add_u32 s74, s74, 9216\n"
"buffer_load_dword v110, v6, s[32:35], s74 offen sc1\n"
"ds_read_b64_tr_b16 v[178:179], v68 offset:13824\n"
"v_mfma_f32_16x16x32_bf16 v[24:27], v[152:155], v[16:19], v[24:27]\n"
"s_add_u32 s74, s74, 9216\n"
"buffer_load_dword v111, v6, s[32:35], s74 offen sc1\n"
"ds_read_b64_tr_b16 v[180:181], v72 offset:14336\n"
"v_mfma_f32_16x16x32_bf16 v[32:35], v[16:19], v[156:159], v[32:35]\n"
"s_sub_u32 s74, s66, 286720\n"
"buffer_load_dword v112, v6, s[32:35], s74 offen sc1\n"
"ds_read_b64_tr_b16 v[182:183], v72 offset:14848\n"
"v_mfma_f32_16x16x32_bf16 v[36:39], v[152:155], v[100:103], v[36:39]\n"
"s_add_u32 s74, s74, 9216\n"
"buffer_load_dword v113, v6, s[32:35], s74 offen sc1\n"
"ds_read_b128 v[76:79], v58 offset:49152\n"
"v_mfma_f32_16x16x32_bf16 v[40:43], v[100:103], v[156:159], v[40:43]\n"
"s_add_u32 s74, s74, 9216\n"
"buffer_load_dword v114, v6, s[32:35], s74 offen sc1\n"
"ds_read_b128 v[80:83], v58 offset:51200\n"
"v_mfma_f32_16x16x32_bf16 v[44:47], v[148:151], v[104:107], v[44:47]\n"
"s_add_u32 s74, s74, 9216\n"
"buffer_load_dword v115, v6, s[32:35], s74 offen sc1\n"
"ds_read_b128 v[84:87], v59 offset:49152\n"
"v_mfma_f32_16x16x32_bf16 v[48:51], v[152:155], v[104:107], v[48:51]\n"
"s_add_u32 s74, s74, 9216\n"
"s_add_u32 s66, s66, 1187840\n"
"s_add_u32 s63, s66, s71\n"
"s_add_u32 s66, s66, 2375680\n"
"buffer_load_dwordx2 v[12:13], v0, s[32:35], s66 offen sc1\n"
"ds_read_b128 v[88:91], v59 offset:51200\n"
"v_mfma_f32_16x16x32_bf16 v[52:55], v[104:107], v[156:159], v[52:55]\n"
"s_waitcnt lgkmcnt(0)\n"
"s_waitcnt vmcnt(23)\n"
"s_barrier\n"
"s_add_u32 m0, s64, 0\n"
"s_nop 0\n"
"buffer_load_dwordx4 v2, s[32:35], s63 offen offset:0 sc1 lds\n"
"ds_read_b64_tr_b16 v[184:185], v69 offset:0\n"
"v_mfma_f32_16x16x32_bf16 v[20:23], v[160:163], v[16:19], v[20:23]\n"
"buffer_load_dwordx4 v2, s[32:35], s63 offen offset:1024 sc1 lds\n"
"ds_read_b64_tr_b16 v[186:187], v69 offset:512\n"
"ds_read_b64_tr_b16 v[188:189], v69 offset:1024\n"
"v_mfma_f32_16x16x32_bf16 v[24:27], v[164:167], v[16:19], v[24:27]\n"
"v_mfma_f32_16x16x32_bf16 v[32:35], v[16:19], v[168:171], v[32:35]\n"
"buffer_load_dwordx4 v2, s[32:35], s63 offen offset:2048 sc1 lds\n"
"ds_read_b64_tr_b16 v[190:191], v69 offset:1536\n"
"ds_read_b64_tr_b16 v[192:193], v73 offset:2048\n"
"v_mfma_f32_16x16x32_bf16 v[36:39], v[164:167], v[76:79], v[36:39]\n"
"v_mfma_f32_16x16x32_bf16 v[40:43], v[76:79], v[168:171], v[40:43]\n"
"buffer_load_dwordx4 v2, s[32:35], s63 offen offset:3072 sc1 lds\n"
"ds_read_b64_tr_b16 v[194:195], v73 offset:2560\n"
"ds_read_b64_tr_b16 v[196:197], v69 offset:12288\n"
"v_mfma_f32_16x16x32_bf16 v[44:47], v[160:163], v[80:83], v[44:47]\n"
"v_mfma_f32_16x16x32_bf16 v[48:51], v[164:167], v[80:83], v[48:51]\n"
"s_add_u32 s63, s63, 4096\n"
"s_add_u32 m0, s64, 4096\n"
"s_nop 0\n"
"buffer_load_dwordx4 v2, s[32:35], s63 offen offset:0 sc1 lds\n"
"ds_read_b64_tr_b16 v[198:199], v69 offset:12800\n"
"v_mfma_f32_16x16x32_bf16 v[52:55], v[80:83], v[168:171], v[52:55]\n"
"buffer_load_dwordx4 v2, s[32:35], s63 offen offset:1024 sc1 lds\n"
"ds_read_b64_tr_b16 v[200:201], v69 offset:13312\n"
"ds_read_b64_tr_b16 v[202:203], v69 offset:13824\n"
"v_mfma_f32_16x16x32_bf16 v[20:23], v[172:175], v[16:19], v[20:23]\n"
"v_mfma_f32_16x16x32_bf16 v[24:27], v[176:179], v[16:19], v[24:27]\n"
"buffer_load_dwordx4 v2, s[32:35], s63 offen offset:2048 sc1 lds\n"
"ds_read_b64_tr_b16 v[204:205], v73 offset:14336\n"
"ds_read_b64_tr_b16 v[206:207], v73 offset:14848\n"
"v_mfma_f32_16x16x32_bf16 v[32:35], v[16:19], v[180:183], v[32:35]\n"
"v_mfma_f32_16x16x32_bf16 v[36:39], v[176:179], v[84:87], v[36:39]\n"
"buffer_load_dwordx4 v2, s[32:35], s63 offen offset:3072 sc1 lds\n"
"ds_read_b128 v[92:95], v58 offset:53248\n"
"ds_read_b128 v[96:99], v58 offset:55296\n"
"v_mfma_f32_16x16x32_bf16 v[40:43], v[84:87], v[180:183], v[40:43]\n"
"v_mfma_f32_16x16x32_bf16 v[44:47], v[172:175], v[88:91], v[44:47]\n"
"s_add_u32 s63, s63, 4096\n"
"s_add_u32 m0, s64, 8192\n"
"s_nop 0\n"
"buffer_load_dwordx4 v2, s[32:35], s63 offen offset:0 sc1 lds\n"
"ds_read_b128 v[100:103], v59 offset:53248\n"
"ds_read_b128 v[104:107], v59 offset:55296\n"
"v_mfma_f32_16x16x32_bf16 v[48:51], v[176:179], v[88:91], v[48:51]\n"
"v_mfma_f32_16x16x32_bf16 v[52:55], v[88:91], v[180:183], v[52:55]\n"
"s_waitcnt lgkmcnt(0)\n"
"s_waitcnt vmcnt(25)\n"
"s_barrier\n"
"s_add_u32 s63, s63, 1179648\n"
"s_add_u32 m0, s64, 36864\n"
"s_nop 0\n"
"buffer_load_dwordx4 v2, s[32:35], s63 offen offset:0 sc1 lds\n"
"ds_read_b64_tr_b16 v[208:209], v70 offset:0\n"
"ds_read_b64_tr_b16 v[210:211], v70 offset:512\n"
"ds_read_b64_tr_b16 v[212:213], v70 offset:1024\n"
"ds_read_b64_tr_b16 v[214:215], v70 offset:1536\n"
"v_mfma_f32_16x16x32_bf16 v[20:23], v[184:187], v[16:19], v[20:23]\n"
"v_mfma_f32_16x16x32_bf16 v[24:27], v[188:191], v[16:19], v[24:27]\n"
"v_mfma_f32_16x16x32_bf16 v[32:35], v[16:19], v[192:195], v[32:35]\n"
"v_mfma_f32_16x16x32_bf16 v[36:39], v[188:191], v[92:95], v[36:39]\n"
"buffer_load_dwordx4 v2, s[32:35], s63 offen offset:1024 sc1 lds\n"
"ds_read_b64_tr_b16 v[216:217], v74 offset:2048\n"
"ds_read_b64_tr_b16 v[218:219], v74 offset:2560\n"
"ds_read_b64_tr_b16 v[220:221], v70 offset:12288\n"
"ds_read_b64_tr_b16 v[222:223], v70 offset:12800\n"
"v_mfma_f32_16x16x32_bf16 v[40:43], v[92:95], v[192:195], v[40:43]\n"
"v_mfma_f32_16x16x32_bf16 v[44:47], v[184:187], v[96:99], v[44:47]\n"
"v_mfma_f32_16x16x32_bf16 v[48:51], v[188:191], v[96:99], v[48:51]\n"
"v_mfma_f32_16x16x32_bf16 v[52:55], v[96:99], v[192:195], v[52:55]\n"
"buffer_load_dwordx4 v2, s[32:35], s63 offen offset:2048 sc1 lds\n"
"ds_read_b64_tr_b16 v[224:225], v70 offset:13312\n"
"ds_read_b64_tr_b16 v[226:227], v70 offset:13824\n"
"ds_read_b64_tr_b16 v[228:229], v74 offset:14336\n"
"ds_read_b64_tr_b16 v[230:231], v74 offset:14848\n"
"v_mfma_f32_16x16x32_bf16 v[20:23], v[196:199], v[16:19], v[20:23]\n"
"v_mfma_f32_16x16x32_bf16 v[24:27], v[200:203], v[16:19], v[24:27]\n"
"v_mfma_f32_16x16x32_bf16 v[32:35], v[16:19], v[204:207], v[32:35]\n"
"v_mfma_f32_16x16x32_bf16 v[36:39], v[200:203], v[100:103], v[36:39]\n"
"buffer_load_dwordx4 v2, s[32:35], s63 offen offset:3072 sc1 lds\n"
"ds_read_b128 v[76:79], v58 offset:57344\n"
"ds_read_b128 v[80:83], v58 offset:59392\n"
"ds_read_b128 v[84:87], v59 offset:57344\n"
"ds_read_b128 v[88:91], v59 offset:59392\n"
"v_mfma_f32_16x16x32_bf16 v[40:43], v[100:103], v[204:207], v[40:43]\n"
"v_mfma_f32_16x16x32_bf16 v[44:47], v[196:199], v[104:107], v[44:47]\n"
"v_mfma_f32_16x16x32_bf16 v[48:51], v[200:203], v[104:107], v[48:51]\n"
"v_mfma_f32_16x16x32_bf16 v[52:55], v[104:107], v[204:207], v[52:55]\n"
"s_waitcnt lgkmcnt(0)\n"
"s_waitcnt vmcnt(22)\n"
"s_barrier\n"
"s_add_u32 s63, s63, 4096\n"
"s_add_u32 m0, s64, 40960\n"
"s_nop 0\n"
"buffer_load_dwordx4 v2, s[32:35], s63 offen offset:0 sc1 lds\n"
"ds_read_b64_tr_b16 v[232:233], v71 offset:0\n"
"ds_read_b64_tr_b16 v[234:235], v71 offset:512\n"
"ds_read_b64_tr_b16 v[236:237], v71 offset:1024\n"
"v_mfma_f32_16x16x32_bf16 v[20:23], v[208:211], v[16:19], v[20:23]\n"
"v_mfma_f32_16x16x32_bf16 v[24:27], v[212:215], v[16:19], v[24:27]\n"
"v_mfma_f32_16x16x32_bf16 v[32:35], v[16:19], v[216:219], v[32:35]\n"
"buffer_load_dwordx4 v2, s[32:35], s63 offen offset:1024 sc1 lds\n"
"ds_read_b64_tr_b16 v[238:239], v71 offset:1536\n"
"ds_read_b64_tr_b16 v[240:241], v75 offset:2048\n"
"ds_read_b64_tr_b16 v[242:243], v75 offset:2560\n"
"v_mfma_f32_16x16x32_bf16 v[36:39], v[212:215], v[76:79], v[36:39]\n"
"v_mfma_f32_16x16x32_bf16 v[40:43], v[76:79], v[216:219], v[40:43]\n"
"v_mfma_f32_16x16x32_bf16 v[44:47], v[208:211], v[80:83], v[44:47]\n"
"buffer_load_dwordx4 v2, s[32:35], s63 offen offset:2048 sc1 lds\n"
"ds_read_b64_tr_b16 v[244:245], v71 offset:12288\n"
"ds_read_b64_tr_b16 v[246:247], v71 offset:12800\n"
"ds_read_b64_tr_b16 v[248:249], v71 offset:13312\n"
"v_mfma_f32_16x16x32_bf16 v[48:51], v[212:215], v[80:83], v[48:51]\n"
"v_mfma_f32_16x16x32_bf16 v[52:55], v[80:83], v[216:219], v[52:55]\n"
"v_mfma_f32_16x16x32_bf16 v[20:23], v[220:223], v[16:19], v[20:23]\n"
"buffer_load_dwordx4 v2, s[32:35], s63 offen offset:3072 sc1 lds\n"
"ds_read_b64_tr_b16 v[250:251], v71 offset:13824\n"
"ds_read_b64_tr_b16 v[252:253], v75 offset:14336\n"
"ds_read_b64_tr_b16 v[254:255], v75 offset:14848\n"
"v_mfma_f32_16x16x32_bf16 v[24:27], v[224:227], v[16:19], v[24:27]\n"
"v_mfma_f32_16x16x32_bf16 v[32:35], v[16:19], v[228:231], v[32:35]\n"
"v_mfma_f32_16x16x32_bf16 v[36:39], v[224:227], v[84:87], v[36:39]\n"
"s_add_u32 s63, s63, 4096\n"
"s_add_u32 m0, s64, 45056\n"
"s_nop 0\n"
"buffer_load_dwordx4 v2, s[32:35], s63 offen offset:0 sc1 lds\n"
"ds_read_b128 v[92:95], v58 offset:61440\n"
"ds_read_b128 v[96:99], v58 offset:63488\n"
"ds_read_b128 v[100:103], v59 offset:61440\n"
"ds_read_b128 v[104:107], v59 offset:63488\n"
"v_mfma_f32_16x16x32_bf16 v[40:43], v[84:87], v[228:231], v[40:43]\n"
"v_mfma_f32_16x16x32_bf16 v[44:47], v[220:223], v[88:91], v[44:47]\n"
"v_mfma_f32_16x16x32_bf16 v[48:51], v[224:227], v[88:91], v[48:51]\n"
"v_mfma_f32_16x16x32_bf16 v[52:55], v[88:91], v[228:231], v[52:55]\n"
"s_xor_b32 s64, s64, s65\n"
"s_waitcnt lgkmcnt(0)\n"
"v_mfma_f32_16x16x32_bf16 v[20:23], v[232:235], v[16:19], v[20:23]\n"
"v_mfma_f32_16x16x32_bf16 v[24:27], v[236:239], v[16:19], v[24:27]\n"
"v_mfma_f32_16x16x32_bf16 v[32:35], v[16:19], v[240:243], v[32:35]\n"
"v_mfma_f32_16x16x32_bf16 v[36:39], v[236:239], v[92:95], v[36:39]\n"
"v_mfma_f32_16x16x32_bf16 v[40:43], v[92:95], v[240:243], v[40:43]\n"
"v_mfma_f32_16x16x32_bf16 v[44:47], v[232:235], v[96:99], v[44:47]\n"
"v_mfma_f32_16x16x32_bf16 v[48:51], v[236:239], v[96:99], v[48:51]\n"
"v_mfma_f32_16x16x32_bf16 v[52:55], v[96:99], v[240:243], v[52:55]\n"
"v_mfma_f32_16x16x32_bf16 v[20:23], v[244:247], v[16:19], v[20:23]\n"
"v_mfma_f32_16x16x32_bf16 v[24:27], v[248:251], v[16:19], v[24:27]\n"
"v_mfma_f32_16x16x32_bf16 v[32:35], v[16:19], v[252:255], v[32:35]\n"
"v_mfma_f32_16x16x32_bf16 v[36:39], v[248:251], v[100:103], v[36:39]\n"
"v_mfma_f32_16x16x32_bf16 v[40:43], v[100:103], v[252:255], v[40:43]\n"
"v_mfma_f32_16x16x32_bf16 v[44:47], v[244:247], v[104:107], v[44:47]\n"
"v_mfma_f32_16x16x32_bf16 v[48:51], v[248:251], v[104:107], v[48:51]\n"
"v_mfma_f32_16x16x32_bf16 v[52:55], v[104:107], v[252:255], v[52:55]\n"
"s_nop 2\n"
"v_mov_b32 v28, v32\n"
"s_waitcnt vmcnt(19)\n"
"v_add_f32 v108, v108, v109\n"
"v_add_f32 v110, v110, v111\n"
"v_add_f32 v108, v108, v110\n"
"s_nop 1\n"
"v_add_f32_dpp v108, v108, v108 row_ror:8 row_mask:0xf bank_mask:0xf\n"
"s_nop 1\n"
"v_add_f32_dpp v108, v108, v108 row_ror:4 row_mask:0xf bank_mask:0xf\n"
"s_nop 1\n"
"v_add_f32_dpp v108, v108, v108 row_ror:2 row_mask:0xf bank_mask:0xf\n"
"v_mul_f32 v108, 0x3a800000, v108\n"
"v_mul_f32 v109, v108, v108\n"
"s_nop 1\n"
"v_sub_f32_dpp v109, v108, v109 quad_perm:[1,1,3,3] row_mask:0xf bank_mask:0xf\n"
"v_max_f32 v109, 0, v109\n"
"v_cvt_pk_bf16_f32 v109, 0, v109\n"
"v_add_f32 v109, 0x3727c5ac, v109\n"
"v_cvt_pk_bf16_f32 v109, 0, v109\n"
"v_sqrt_f32 v109, v109\n"
"v_cvt_pk_bf16_f32 v108, 0, v108\n"
"v_cvt_pk_bf16_f32 v109, 0, v109\n"
"v_rcp_f32 v109, v109\n"
"s_nop 0\n"
"ds_write2_b32 v8, v108, v109 offset0:0 offset1:16\n"
"v_add_f32 v112, v112, v113\n"
"v_add_f32 v114, v114, v115\n"
"v_add_f32 v112, v112, v114\n"
"s_nop 1\n"
"v_add_f32_dpp v112, v112, v112 row_ror:8 row_mask:0xf bank_mask:0xf\n"
"s_nop 1\n"
"v_add_f32_dpp v112, v112, v112 row_ror:4 row_mask:0xf bank_mask:0xf\n"
"s_nop 1\n"
"v_add_f32_dpp v112, v112, v112 row_ror:2 row_mask:0xf bank_mask:0xf\n"
"v_mul_f32 v112, 0x3a800000, v112\n"
"v_mul_f32 v113, v112, v112\n"
"s_nop 1\n"
"v_sub_f32_dpp v113, v112, v113 quad_perm:[1,1,3,3] row_mask:0xf bank_mask:0xf\n"
"v_max_f32 v113, 0, v113\n"
"v_cvt_pk_bf16_f32 v113, 0, v113\n"
"v_add_f32 v113, 0x3727c5ac, v113\n"
"v_cvt_pk_bf16_f32 v113, 0, v113\n"
"v_sqrt_f32 v113, v113\n"
"v_cvt_pk_bf16_f32 v112, 0, v112\n"
"v_cvt_pk_bf16_f32 v113, 0, v113\n"
"v_rcp_f32 v113, v113\n"
"s_nop 0\n"
"ds_write2_b32 v8, v112, v113 offset0:32 offset1:48\n"
"s_waitcnt lgkmcnt(0)\n"
"s_barrier\n"
"ds_read2_b32 v[14:15], v9 offset0:0 offset1:16\n"
"ds_read_b128 v[92:95], v10 offset:0\n"
"ds_read_b128 v[96:99], v10 offset:64\n"
"s_add_u32 s68, s68, 1187840\n"
"s_waitcnt lgkmcnt(0)\n"
"v_mul_f32 v29, 0x3e8293ee, v15\n"
"v_fma_f32 v36, -v14, v24, v36\n"
"v_fma_f32 v37, -v14, v25, v37\n"
"v_fma_f32 v38, -v14, v26, v38\n"
"v_fma_f32 v39, -v14, v27, v39\n"
"v_fma_f32 v32, -v14, v20, v32\n"
"v_fma_f32 v33, -v14, v21, v33\n"
"v_fma_f32 v34, -v14, v22, v34\n"
"v_fma_f32 v35, -v14, v23, v35\n"
"v_mul_f32 v36, v36, v15\n"
"v_mul_f32 v37, v37, v15\n"
"v_mul_f32 v38, v38, v15\n"
"v_mul_f32 v39, v39, v15\n"
"v_mul_f32 v32, v32, v29\n"
"v_mul_f32 v33, v33, v29\n"
"v_mul_f32 v34, v34, v29\n"
"v_mul_f32 v35, v35, v29\n"
"v_cvt_pk_bf16_f32 v104, v36, v37\n"
"v_cvt_pk_bf16_f32 v105, v38, v39\n"
"v_cvt_pk_bf16_f32 v100, v32, v33\n"
"v_cvt_pk_bf16_f32 v101, v34, v35\n"
"ds_write2st64_b64 v11, v[100:101], v[104:105] offset0:0 offset1:1\n"
"v_fma_f32 v40, -v92, v28, v40\n"
"v_fma_f32 v41, -v93, v28, v41\n"
"v_fma_f32 v42, -v94, v28, v42\n"
"v_fma_f32 v43, -v95, v28, v43\n"
"v_mul_f32 v40, v40, v96\n"
"v_mul_f32 v41, v41, v97\n"
"v_mul_f32 v42, v42, v98\n"
"v_mul_f32 v43, v43, v99\n"
"s_waitcnt lgkmcnt(0)\n"
"s_barrier\n"
"ds_read_b64 v[102:103], v3\n"
"ds_read_b64 v[106:107], v3 offset:512\n"
"v_cvt_pk_bf16_f32 v112, v40, v41\n"
"v_cvt_pk_bf16_f32 v113, v42, v43\n"
"s_waitcnt lgkmcnt(0)\n"
"s_waitcnt vmcnt(9)\n"
"s_barrier\n"
"ds_read_b128 v[56:59], v4 offset:0\n"
"ds_read_b128 v[60:63], v4 offset:1152\n"
"ds_read_b128 v[64:67], v4 offset:2304\n"
"ds_read_b128 v[68:71], v4 offset:3456\n"
"ds_read_b128 v[72:75], v4 offset:4608\n"
"ds_read_b128 v[76:79], v4 offset:5760\n"
".p2align 6\n"
".L_ql:\n"
"s_bitcmp1_b64 s[58:59], s62\n"
"s_cbranch_scc1 .L_qs0\n"
".L_qb0:\n"
"s_waitcnt lgkmcnt(2)\n"
"v_mfma_f32_16x16x32_bf16 v[32:35], a[0:3], v[56:59], 0\n"
"ds_read_b128 v[80:83], v4 offset:6912\n"
"ds_read2_b32 v[14:15], v9 offset0:32 offset1:48\n"
"v_mfma_f32_16x16x32_bf16 v[32:35], a[12:15], v[60:63], v[32:35]\n"
"ds_read_b128 v[92:95], v10 offset:128\n"
"ds_read_b128 v[96:99], v10 offset:192\n"
"v_mfma_f32_16x16x32_bf16 v[36:39], a[16:19], v[60:63], 0\n"
"s_add_u32 s68, s68, 1187840\n"
"v_mfma_f32_16x16x32_bf16 v[36:39], a[4:7], v[56:59], v[36:39]\n"
"ds_read_b128 v[84:87], v4 offset:8064\n"
"v_mfma_f32_16x16x32_bf16 v[40:43], v[56:59], a[8:11], 0\n"
"ds_read2st64_b32 v[88:89], v6 offset0:0 offset1:36\n"
"v_mfma_f32_16x16x32_bf16 v[40:43], v[60:63], a[20:23], v[40:43]\n"
"s_waitcnt vmcnt(4)\n"
"v_cmp_ne_u64 vcc, v[30:31], v[12:13]\n"
"v_mfma_f32_16x16x32_bf16 v[40:43], v[64:67], a[32:35], v[40:43]\n"
"ds_read_b128 v[56:59], v4 offset:9216\n"
"ds_read2st64_b32 v[90:91], v6 offset0:72 offset1:108\n"
"v_mfma_f32_16x16x32_bf16 v[40:43], v[68:71], a[44:47], v[40:43]\n"
"s_cbranch_vccnz .L_rtq0\n"
".L_bkq0:\n"
"s_add_u32 s63, s66, s71\n"
"v_mfma_f32_16x16x32_bf16 v[36:39], a[40:43], v[68:71], v[36:39]\n"
"s_add_u32 s66, s66, s72\n"
"s_add_u32 m0, s64, 0\n"
"v_mfma_f32_16x16x32_bf16 v[36:39], a[28:31], v[64:67], v[36:39]\n"
"ds_read_b128 v[60:63], v4 offset:10368\n"
"buffer_load_dwordx4 v2, s[32:35], s63 offen offset:0 sc1 lds\n"
"v_mfma_f32_16x16x32_bf16 v[32:35], a[24:27], v[64:67], v[32:35]\n"
"s_waitcnt lgkmcnt(5)\n"
"v_mul_f32 v29, 0x3e8293ee, v15\n"
"v_mfma_f32_16x16x32_bf16 v[32:35], a[36:39], v[68:71], v[32:35]\n"
"v_fma_f32 v48, -v14, v24, v48\n"
"s_waitcnt lgkmcnt(4)\n"
"v_mfma_f32_16x16x32_bf16 v[32:35], a[48:51], v[72:75], v[32:35]\n"
"ds_read_b128 v[64:67], v4 offset:11520\n"
"v_fma_f32 v49, -v14, v25, v49\n"
"v_mfma_f32_16x16x32_bf16 v[32:35], a[60:63], v[76:79], v[32:35]\n"
"v_fma_f32 v50, -v14, v26, v50\n"
"v_fma_f32 v51, -v14, v27, v51\n"
"v_mfma_f32_16x16x32_bf16 v[36:39], a[64:67], v[76:79], v[36:39]\n"
"v_fma_f32 v44, -v14, v20, v44\n"
"v_fma_f32 v45, -v14, v21, v45\n"
"v_mfma_f32_16x16x32_bf16 v[36:39], a[52:55], v[72:75], v[36:39]\n"
"ds_read_b128 v[68:71], v4 offset:12672\n"
"v_fma_f32 v46, -v14, v22, v46\n"
"v_mfma_f32_16x16x32_bf16 v[40:43], v[72:75], a[56:59], v[40:43]\n"
"buffer_load_dwordx4 v2, s[32:35], s63 offen offset:1024 sc1 lds\n"
"v_fma_f32 v47, -v14, v23, v47\n"
"v_mfma_f32_16x16x32_bf16 v[40:43], v[76:79], a[68:71], v[40:43]\n"
"v_mul_f32 v48, v48, v15\n"
"v_mul_f32 v49, v49, v15\n"
"v_mfma_f32_16x16x32_bf16 v[40:43], v[80:83], a[80:83], v[40:43]\n"
"ds_read_b128 v[72:75], v4 offset:13824\n"
"v_mul_f32 v50, v50, v15\n"
"v_mfma_f32_16x16x32_bf16 v[40:43], v[84:87], a[92:95], v[40:43]\n"
"v_mul_f32 v51, v51, v15\n"
"v_mul_f32 v44, v44, v29\n"
"v_mfma_f32_16x16x32_bf16 v[36:39], a[88:91], v[84:87], v[36:39]\n"
"v_mul_f32 v45, v45, v29\n"
"v_mul_f32 v46, v46, v29\n"
"v_mfma_f32_16x16x32_bf16 v[36:39], a[76:79], v[80:83], v[36:39]\n"
"ds_read_b128 v[76:79], v4 offset:14976\n"
"v_mul_f32 v47, v47, v29\n"
"v_mfma_f32_16x16x32_bf16 v[32:35], a[72:75], v[80:83], v[32:35]\n"
"v_cvt_pk_bf16_f32 v108, v48, v49\n"
"v_cvt_pk_bf16_f32 v109, v50, v51\n"
"v_mfma_f32_16x16x32_bf16 v[32:35], a[84:87], v[84:87], v[32:35]\n"
"v_cvt_pk_bf16_f32 v100, v44, v45\n"
"s_waitcnt lgkmcnt(2)\n"
"v_mfma_f32_16x16x32_bf16 v[32:35], a[96:99], v[56:59], v[32:35]\n"
"ds_read_b128 v[80:83], v4 offset:16128\n"
"buffer_load_dwordx4 v2, s[32:35], s63 offen offset:2048 sc1 lds\n"
"v_mfma_f32_16x16x32_bf16 v[32:35], a[108:111], v[60:63], v[32:35]\n"
"v_cvt_pk_bf16_f32 v101, v46, v47\n"
"v_mfma_f32_16x16x32_bf16 v[36:39], a[112:115], v[60:63], v[36:39]\n"
"ds_write2st64_b64 v11, v[100:101], v[108:109] offset0:0 offset1:1\n"
"v_fma_f32 v52, -v92, v28, v52\n"
"v_mfma_f32_16x16x32_bf16 v[36:39], a[100:103], v[56:59], v[36:39]\n"
"ds_read_b128 v[84:87], v4 offset:17280\n"
"v_fma_f32 v53, -v93, v28, v53\n"
"v_mfma_f32_16x16x32_bf16 v[40:43], v[56:59], a[104:107], v[40:43]\n"
"v_fma_f32 v54, -v94, v28, v54\n"
"v_fma_f32 v55, -v95, v28, v55\n"
"v_mfma_f32_16x16x32_bf16 v[40:43], v[60:63], a[116:119], v[40:43]\n"
"v_mul_f32 v52, v52, v96\n"
"v_mul_f32 v53, v53, v97\n"
"v_mfma_f32_16x16x32_bf16 v[40:43], v[64:67], a[128:131], v[40:43]\n"
"ds_read_b128 v[56:59], v4 offset:18432\n"
"v_mul_f32 v54, v54, v98\n"
"v_mfma_f32_16x16x32_bf16 v[40:43], v[68:71], a[140:143], v[40:43]\n"
"v_mul_f32 v55, v55, v99\n"
"s_waitcnt lgkmcnt(2)\n"
"v_mfma_f32_16x16x32_bf16 v[36:39], a[136:139], v[68:71], v[36:39]\n"
"buffer_load_dwordx4 v2, s[32:35], s63 offen offset:3072 sc1 lds\n"
"s_waitcnt vmcnt(4)\n"
"v_mfma_f32_16x16x32_bf16 v[36:39], a[124:127], v[64:67], v[36:39]\n"
"ds_read_b128 v[60:63], v4 offset:19584\n"
"s_barrier\n"
"v_mfma_f32_16x16x32_bf16 v[32:35], a[120:123], v[64:67], v[32:35]\n"
"ds_read_b64 v[102:103], v3\n"
"ds_read_b64 v[110:111], v3 offset:512\n"
"v_mfma_f32_16x16x32_bf16 v[32:35], a[132:135], v[68:71], v[32:35]\n"
"buffer_store_dwordx2 v[30:31], v2, s[92:95], s67 idxen sc1\n"
"s_waitcnt lgkmcnt(4)\n"
"v_mfma_f32_16x16x32_bf16 v[32:35], a[144:147], v[72:75], v[32:35]\n"
"ds_read_b128 v[64:67], v4 offset:20736\n"
"v_cvt_pk_bf16_f32 v114, v52, v53\n"
"v_mfma_f32_16x16x32_bf16 v[32:35], a[156:159], v[76:79], v[32:35]\n"
"v_cvt_pk_bf16_f32 v115, v54, v55\n"
"v_add_f32 v88, v88, v89\n"
"v_mfma_f32_16x16x32_bf16 v[36:39], a[160:163], v[76:79], v[36:39]\n"
"v_add_f32 v90, v90, v91\n"
"v_mfma_f32_16x16x32_bf16 v[36:39], a[148:151], v[72:75], v[36:39]\n"
"ds_read_b128 v[68:71], v4 offset:21888\n"
"s_add_u32 s63, s63, 4096\n"
"v_mfma_f32_16x16x32_bf16 v[40:43], v[72:75], a[152:155], v[40:43]\n"
"s_add_u32 m0, s64, 4096\n"
"v_add_f32 v88, v88, v90\n"
"v_mfma_f32_16x16x32_bf16 v[40:43], v[76:79], a[164:167], v[40:43]\n"
"buffer_load_dwordx4 v2, s[32:35], s63 offen offset:0 sc1 lds\n"
"v_add_f32_dpp v88, v88, v88 row_ror:8 row_mask:0xf bank_mask:0xf\n"
"v_mfma_f32_16x16x32_bf16 v[40:43], v[80:83], a[176:179], v[40:43]\n"
"ds_read_b128 v[72:75], v4 offset:23040\n"
"s_waitcnt lgkmcnt(3)\n"
"v_mfma_f32_16x16x32_bf16 v[48:51], v[104:107], v[100:103], v[120:123]\n"
"v_add_f32_dpp v88, v88, v88 row_ror:4 row_mask:0xf bank_mask:0xf\n"
"v_mfma_f32_16x16x32_bf16 v[44:47], v[108:111], v[100:103], v[116:119]\n"
"v_mfma_f32_16x16x32_bf16 v[40:43], v[84:87], a[188:191], v[40:43]\n"
"v_add_f32_dpp v88, v88, v88 row_ror:2 row_mask:0xf bank_mask:0xf\n"
"v_mfma_f32_16x16x32_bf16 v[36:39], a[184:187], v[84:87], v[36:39]\n"
"v_mul_f32 v88, 0x3a800000, v88\n"
"v_mfma_f32_16x16x32_bf16 v[36:39], a[172:175], v[80:83], v[36:39]\n"
"ds_read_b128 v[76:79], v4 offset:24192\n"
"v_mul_f32 v89, v88, v88\n"
"v_mfma_f32_16x16x32_bf16 v[32:35], a[168:171], v[80:83], v[32:35]\n"
"v_max3_f32 v44, v44, v45, v48\n"
"v_max3_f32 v46, v46, v47, v50\n"
"v_mfma_f32_16x16x32_bf16 v[32:35], a[180:183], v[84:87], v[32:35]\n"
"v_max_f32 v44, v44, v49\n"
"s_waitcnt lgkmcnt(2)\n"
"v_mfma_f32_16x16x32_bf16 v[32:35], a[192:195], v[56:59], v[32:35]\n"
"ds_read_b128 v[80:83], v4 offset:25344\n"
"buffer_load_dwordx4 v2, s[32:35], s63 offen offset:1024 sc1 lds\n"
"v_mfma_f32_16x16x32_bf16 v[32:35], a[204:207], v[60:63], v[32:35]\n"
"v_max_f32 v46, v46, v51\n"
"v_sub_f32_dpp v89, v88, v89 quad_perm:[1,1,3,3] row_mask:0xf bank_mask:0xf\n"
"v_mfma_f32_16x16x32_bf16 v[36:39], a[208:211], v[60:63], v[36:39]\n"
"buffer_load_dwordx2 v[12:13], v0, s[32:35], s66 offen sc1\n"
"v_max_f32 v96, v44, v46\n"
"v_mfma_f32_16x16x32_bf16 v[36:39], a[196:199], v[56:59], v[36:39]\n"
"ds_read_b128 v[84:87], v4 offset:26496\n"
"v_max_f32 v97, v44, v46\n"
"v_mfma_f32_16x16x32_bf16 v[40:43], v[56:59], a[200:203], v[40:43]\n"
"v_max_f32 v89, 0, v89\n"
"v_cvt_pk_bf16_f32 v88, 0, v88\n"
"v_mfma_f32_16x16x32_bf16 v[40:43], v[60:63], a[212:215], v[40:43]\n"
"v_permlane16_swap_b32 v96, v97\n"
"v_mfma_f32_16x16x32_bf16 v[40:43], v[64:67], a[224:227], v[40:43]\n"
"ds_read_b128 v[56:59], v4 offset:27648\n"
"v_max_f32 v98, v96, v97\n"
"v_mfma_f32_16x16x32_bf16 v[40:43], v[68:71], a[236:239], v[40:43]\n"
"v_max_f32 v99, v96, v97\n"
"v_cvt_pk_bf16_f32 v89, 0, v89\n"
"v_mfma_f32_16x16x32_bf16 v[36:39], a[232:235], v[68:71], v[36:39]\n"
"buffer_load_dwordx4 v2, s[32:35], s63 offen offset:2048 sc1 lds\n"
"v_add_f32 v89, 0x3727c5ac, v89\n"
"v_mfma_f32_16x16x32_bf16 v[36:39], a[220:223], v[64:67], v[36:39]\n"
"ds_read_b128 v[60:63], v4 offset:28800\n"
"v_cvt_pk_bf16_f32 v89, 0, v89\n"
"v_mfma_f32_16x16x32_bf16 v[32:35], a[216:219], v[64:67], v[32:35]\n"
"v_permlane32_swap_b32 v98, v99\n"
"v_mfma_f32_16x16x32_bf16 v[32:35], a[228:231], v[68:71], v[32:35]\n"
"v_max_f32 v96, v98, v99\n"
"s_waitcnt lgkmcnt(2)\n"
"v_mfma_f32_16x16x32_bf16 v[32:35], a[240:243], v[72:75], v[32:35]\n"
"ds_read_b128 v[64:67], v4 offset:29952\n"
"v_sub_f32 v44, v44, v96\n"
"v_mfma_f32_16x16x32_bf16 v[32:35], a[252:255], v[76:79], v[32:35]\n"
"v_exp_f32 v44, v44\n"
"v_mfma_f32_16x16x32_bf16 v[36:39], v[128:131], v[76:79], v[36:39]\n"
"v_sqrt_f32 v89, v89\n"
"v_mfma_f32_16x16x32_bf16 v[36:39], a[244:247], v[72:75], v[36:39]\n"
"ds_read_b128 v[68:71], v4 offset:31104\n"
"v_sub_f32 v46, v46, v96\n"
"v_mfma_f32_16x16x32_bf16 v[40:43], v[72:75], a[248:251], v[40:43]\n"
"buffer_load_dwordx4 v2, s[32:35], s63 offen offset:3072 sc1 lds\n"
"v_cvt_pk_bf16_f32 v44, v44, v44\n"
"v_mfma_f32_16x16x32_bf16 v[40:43], v[76:79], v[132:135], v[40:43]\n"
"v_exp_f32 v46, v46\n"
"v_mfma_f32_16x16x32_bf16 v[40:43], v[80:83], v[144:147], v[40:43]\n"
"ds_read_b128 v[72:75], v4 offset:32256\n"
"v_cvt_pk_bf16_f32 v46, v46, v46\n"
"v_mfma_f32_16x16x32_bf16 v[40:43], v[84:87], v[156:159], v[40:43]\n"
"v_and_b32 v94, v44, v124\n"
"v_and_b32 v92, v44, v126\n"
"v_mfma_f32_16x16x32_bf16 v[36:39], v[152:155], v[84:87], v[36:39]\n"
"v_and_b32 v95, v46, v125\n"
"v_and_b32 v93, v46, v127\n"
"v_mfma_f32_16x16x32_bf16 v[36:39], v[140:143], v[80:83], v[36:39]\n"
"ds_read_b128 v[76:79], v4 offset:33408\n"
"v_cvt_pk_bf16_f32 v89, 0, v89\n"
"v_mfma_f32_16x16x32_bf16 v[48:51], v[16:19], v[92:95], 0\n"
"v_rcp_f32 v89, v89\n"
"v_mfma_f32_16x16x32_bf16 v[52:55], v[112:115], v[92:95], 0\n"
"ds_write2_b32 v8, v88, v89 offset0:0 offset1:16\n"
"v_mfma_f32_16x16x32_bf16 v[32:35], v[136:139], v[80:83], v[32:35]\n"
"v_mfma_f32_16x16x32_bf16 v[32:35], v[148:151], v[84:87], v[32:35]\n"
"s_add_u32 s63, s63, 4096\n"
"s_waitcnt lgkmcnt(3)\n"
"v_mfma_f32_16x16x32_bf16 v[32:35], v[160:163], v[56:59], v[32:35]\n"
"ds_read_b128 v[80:83], v4 offset:34560\n"
"s_add_u32 m0, s64, 8192\n"
"v_mfma_f32_16x16x32_bf16 v[32:35], v[172:175], v[60:63], v[32:35]\n"
"buffer_load_dwordx4 v2, s[32:35], s63 offen offset:0 sc1 lds\n"
"v_mfma_f32_16x16x32_bf16 v[36:39], v[176:179], v[60:63], v[36:39]\n"
"v_rcp_f32 v48, v48\n"
"v_mfma_f32_16x16x32_bf16 v[36:39], v[164:167], v[56:59], v[36:39]\n"
"ds_read_b128 v[84:87], v4 offset:35712\n"
"v_mul_f32 v52, v52, v48\n"
"v_mfma_f32_16x16x32_bf16 v[40:43], v[56:59], v[168:171], v[40:43]\n"
"v_xor_b32 v4, 73728, v4\n"
"v_mul_f32 v53, v53, v48\n"
"v_mfma_f32_16x16x32_bf16 v[40:43], v[60:63], v[180:183], v[40:43]\n"
"s_waitcnt lgkmcnt(2)\n"
"s_barrier\n"
"v_mfma_f32_16x16x32_bf16 v[40:43], v[64:67], v[192:195], v[40:43]\n"
"ds_read_b128 v[56:59], v5 offset:0\n"
"v_mul_f32 v54, v54, v48\n"
"v_mfma_f32_16x16x32_bf16 v[40:43], v[68:71], v[204:207], v[40:43]\n"
"v_mul_f32 v55, v55, v48\n"
"v_cvt_pk_bf16_f32 v100, v52, v53\n"
"v_mfma_f32_16x16x32_bf16 v[36:39], v[200:203], v[68:71], v[36:39]\n"
"v_cvt_pk_bf16_f32 v101, v54, v55\n"
"v_mfma_f32_16x16x32_bf16 v[36:39], v[188:191], v[64:67], v[36:39]\n"
"ds_read_b128 v[60:63], v5 offset:1152\n"
"v_mfma_f32_16x16x32_bf16 v[32:35], v[184:187], v[64:67], v[32:35]\n"
"v_permlane32_swap_b32 v100, v102\n"
"v_mfma_f32_16x16x32_bf16 v[32:35], v[196:199], v[68:71], v[32:35]\n"
"s_waitcnt lgkmcnt(2)\n"
"v_mfma_f32_16x16x32_bf16 v[32:35], v[208:211], v[72:75], v[32:35]\n"
"ds_read_b128 v[64:67], v5 offset:2304\n"
"v_mfma_f32_16x16x32_bf16 v[32:35], v[220:223], v[76:79], v[32:35]\n"
"v_permlane32_swap_b32 v101, v103\n"
"v_mfma_f32_16x16x32_bf16 v[36:39], v[224:227], v[76:79], v[36:39]\n"
"buffer_store_dwordx4 v[100:103], v1, s[32:35], s68 offen sc1\n"
"s_mov_b32 s67, s68\n"
"v_mfma_f32_16x16x32_bf16 v[36:39], v[212:215], v[72:75], v[36:39]\n"
"ds_read_b128 v[68:71], v5 offset:3456\n"
"v_mfma_f32_16x16x32_bf16 v[40:43], v[72:75], v[216:219], v[40:43]\n"
"v_mfma_f32_16x16x32_bf16 v[40:43], v[76:79], v[228:231], v[40:43]\n"
"v_mfma_f32_16x16x32_bf16 v[40:43], v[80:83], v[240:243], v[40:43]\n"
"ds_read_b128 v[72:75], v5 offset:4608\n"
"v_mfma_f32_16x16x32_bf16 v[40:43], v[84:87], v[252:255], v[40:43]\n"
"v_mfma_f32_16x16x32_bf16 v[36:39], v[248:251], v[84:87], v[36:39]\n"
"v_mfma_f32_16x16x32_bf16 v[36:39], v[236:239], v[80:83], v[36:39]\n"
"ds_read_b128 v[76:79], v5 offset:5760\n"
"v_mfma_f32_16x16x32_bf16 v[32:35], v[232:235], v[80:83], v[32:35]\n"
"v_mfma_f32_16x16x32_bf16 v[32:35], v[244:247], v[84:87], v[32:35]\n"
"s_bitcmp1_b64 s[60:61], s62\n"
"s_cbranch_scc1 .L_qs1\n"
".L_qb1:\n"
"s_waitcnt lgkmcnt(2)\n"
"v_mfma_f32_16x16x32_bf16 v[44:47], a[0:3], v[56:59], 0\n"
"ds_read_b128 v[80:83], v5 offset:6912\n"
"ds_read2_b32 v[14:15], v9 offset0:0 offset1:16\n"
"v_mfma_f32_16x16x32_bf16 v[44:47], a[12:15], v[60:63], v[44:47]\n"
"ds_read_b128 v[92:95], v10 offset:0\n"
"ds_read_b128 v[96:99], v10 offset:64\n"
"v_mfma_f32_16x16x32_bf16 v[48:51], a[16:19], v[60:63], 0\n"
"s_add_u32 s68, s68, 1187840\n"
"v_mfma_f32_16x16x32_bf16 v[48:51], a[4:7], v[56:59], v[48:51]\n"
"ds_read_b128 v[84:87], v5 offset:8064\n"
"v_mfma_f32_16x16x32_bf16 v[52:55], v[56:59], a[8:11], 0\n"
"ds_read2st64_b32 v[88:89], v6 offset0:144 offset1:180\n"
"v_mfma_f32_16x16x32_bf16 v[52:55], v[60:63], a[20:23], v[52:55]\n"
"s_waitcnt vmcnt(4)\n"
"v_cmp_ne_u64 vcc, v[30:31], v[12:13]\n"
"v_mfma_f32_16x16x32_bf16 v[52:55], v[64:67], a[32:35], v[52:55]\n"
"ds_read_b128 v[56:59], v5 offset:9216\n"
"ds_read2st64_b32 v[90:91], v6 offset0:216 offset1:252\n"
"v_mfma_f32_16x16x32_bf16 v[52:55], v[68:71], a[44:47], v[52:55]\n"
"s_cbranch_vccnz .L_rtq1\n"
".L_bkq1:\n"
"s_add_u32 s63, s66, s71\n"
"v_mfma_f32_16x16x32_bf16 v[48:51], a[40:43], v[68:71], v[48:51]\n"
"s_add_u32 s66, s66, s72\n"
"s_add_u32 m0, s64, 36864\n"
"v_mfma_f32_16x16x32_bf16 v[48:51], a[28:31], v[64:67], v[48:51]\n"
"ds_read_b128 v[60:63], v5 offset:10368\n"
"buffer_load_dwordx4 v2, s[32:35], s63 offen offset:0 sc1 lds\n"
"v_mfma_f32_16x16x32_bf16 v[44:47], a[24:27], v[64:67], v[44:47]\n"
"v_xor_b32 v6, v7, v6\n"
"s_waitcnt lgkmcnt(5)\n"
"v_mfma_f32_16x16x32_bf16 v[44:47], a[36:39], v[68:71], v[44:47]\n"
"v_mul_f32 v29, 0x3e8293ee, v15\n"
"s_waitcnt lgkmcnt(4)\n"
"v_mfma_f32_16x16x32_bf16 v[44:47], a[48:51], v[72:75], v[44:47]\n"
"ds_read_b128 v[64:67], v5 offset:11520\n"
"v_fma_f32 v36, -v14, v24, v36\n"
"v_mfma_f32_16x16x32_bf16 v[44:47], a[60:63], v[76:79], v[44:47]\n"
"v_fma_f32 v37, -v14, v25, v37\n"
"v_fma_f32 v38, -v14, v26, v38\n"
"v_mfma_f32_16x16x32_bf16 v[48:51], a[64:67], v[76:79], v[48:51]\n"
"v_fma_f32 v39, -v14, v27, v39\n"
"v_fma_f32 v32, -v14, v20, v32\n"
"v_mfma_f32_16x16x32_bf16 v[48:51], a[52:55], v[72:75], v[48:51]\n"
"ds_read_b128 v[68:71], v5 offset:12672\n"
"v_fma_f32 v33, -v14, v21, v33\n"
"v_mfma_f32_16x16x32_bf16 v[52:55], v[72:75], a[56:59], v[52:55]\n"
"buffer_load_dwordx4 v2, s[32:35], s63 offen offset:1024 sc1 lds\n"
"v_fma_f32 v34, -v14, v22, v34\n"
"v_mfma_f32_16x16x32_bf16 v[52:55], v[76:79], a[68:71], v[52:55]\n"
"v_fma_f32 v35, -v14, v23, v35\n"
"v_mul_f32 v36, v36, v15\n"
"v_mfma_f32_16x16x32_bf16 v[52:55], v[80:83], a[80:83], v[52:55]\n"
"ds_read_b128 v[72:75], v5 offset:13824\n"
"v_mul_f32 v37, v37, v15\n"
"v_mfma_f32_16x16x32_bf16 v[52:55], v[84:87], a[92:95], v[52:55]\n"
"v_mul_f32 v38, v38, v15\n"
"v_mul_f32 v39, v39, v15\n"
"v_mfma_f32_16x16x32_bf16 v[48:51], a[88:91], v[84:87], v[48:51]\n"
"v_mul_f32 v32, v32, v29\n"
"v_mul_f32 v33, v33, v29\n"
"v_mfma_f32_16x16x32_bf16 v[48:51], a[76:79], v[80:83], v[48:51]\n"
"ds_read_b128 v[76:79], v5 offset:14976\n"
"v_mul_f32 v34, v34, v29\n"
"v_mfma_f32_16x16x32_bf16 v[44:47], a[72:75], v[80:83], v[44:47]\n"
"v_mul_f32 v35, v35, v29\n"
"v_cvt_pk_bf16_f32 v104, v36, v37\n"
"v_mfma_f32_16x16x32_bf16 v[44:47], a[84:87], v[84:87], v[44:47]\n"
"v_cvt_pk_bf16_f32 v105, v38, v39\n"
"s_waitcnt lgkmcnt(2)\n"
"v_mfma_f32_16x16x32_bf16 v[44:47], a[96:99], v[56:59], v[44:47]\n"
"ds_read_b128 v[80:83], v5 offset:16128\n"
"buffer_load_dwordx4 v2, s[32:35], s63 offen offset:2048 sc1 lds\n"
"v_mfma_f32_16x16x32_bf16 v[44:47], a[108:111], v[60:63], v[44:47]\n"
"v_cvt_pk_bf16_f32 v100, v32, v33\n"
"v_cvt_pk_bf16_f32 v101, v34, v35\n"
"v_mfma_f32_16x16x32_bf16 v[48:51], a[112:115], v[60:63], v[48:51]\n"
"ds_write2st64_b64 v11, v[100:101], v[104:105] offset0:0 offset1:1\n"
"v_fma_f32 v40, -v92, v28, v40\n"
"v_mfma_f32_16x16x32_bf16 v[48:51], a[100:103], v[56:59], v[48:51]\n"
"ds_read_b128 v[84:87], v5 offset:17280\n"
"v_fma_f32 v41, -v93, v28, v41\n"
"v_mfma_f32_16x16x32_bf16 v[52:55], v[56:59], a[104:107], v[52:55]\n"
"v_fma_f32 v42, -v94, v28, v42\n"
"v_fma_f32 v43, -v95, v28, v43\n"
"v_mfma_f32_16x16x32_bf16 v[52:55], v[60:63], a[116:119], v[52:55]\n"
"v_mul_f32 v40, v40, v96\n"
"v_mul_f32 v41, v41, v97\n"
"v_mfma_f32_16x16x32_bf16 v[52:55], v[64:67], a[128:131], v[52:55]\n"
"ds_read_b128 v[56:59], v5 offset:18432\n"
"v_mul_f32 v42, v42, v98\n"
"v_mfma_f32_16x16x32_bf16 v[52:55], v[68:71], a[140:143], v[52:55]\n"
"v_mul_f32 v43, v43, v99\n"
"s_waitcnt lgkmcnt(2)\n"
"v_mfma_f32_16x16x32_bf16 v[48:51], a[136:139], v[68:71], v[48:51]\n"
"buffer_load_dwordx4 v2, s[32:35], s63 offen offset:3072 sc1 lds\n"
"s_waitcnt vmcnt(4)\n"
"v_mfma_f32_16x16x32_bf16 v[48:51], a[124:127], v[64:67], v[48:51]\n"
"ds_read_b128 v[60:63], v5 offset:19584\n"
"s_barrier\n"
"v_mfma_f32_16x16x32_bf16 v[44:47], a[120:123], v[64:67], v[44:47]\n"
"ds_read_b64 v[102:103], v3\n"
"ds_read_b64 v[106:107], v3 offset:512\n"
"v_mfma_f32_16x16x32_bf16 v[44:47], a[132:135], v[68:71], v[44:47]\n"
"buffer_store_dwordx2 v[30:31], v2, s[92:95], s67 idxen sc1\n"
"s_waitcnt lgkmcnt(4)\n"
"v_mfma_f32_16x16x32_bf16 v[44:47], a[144:147], v[72:75], v[44:47]\n"
"ds_read_b128 v[64:67], v5 offset:20736\n"
"v_cvt_pk_bf16_f32 v112, v40, v41\n"
"v_mfma_f32_16x16x32_bf16 v[44:47], a[156:159], v[76:79], v[44:47]\n"
"v_cvt_pk_bf16_f32 v113, v42, v43\n"
"v_add_f32 v88, v88, v89\n"
"v_mfma_f32_16x16x32_bf16 v[48:51], a[160:163], v[76:79], v[48:51]\n"
"v_add_f32 v90, v90, v91\n"
"v_mfma_f32_16x16x32_bf16 v[48:51], a[148:151], v[72:75], v[48:51]\n"
"ds_read_b128 v[68:71], v5 offset:21888\n"
"s_add_u32 s63, s63, 4096\n"
"v_mfma_f32_16x16x32_bf16 v[52:55], v[72:75], a[152:155], v[52:55]\n"
"s_add_u32 m0, s64, 40960\n"
"v_add_f32 v88, v88, v90\n"
"v_mfma_f32_16x16x32_bf16 v[52:55], v[76:79], a[164:167], v[52:55]\n"
"buffer_load_dwordx4 v2, s[32:35], s63 offen offset:0 sc1 lds\n"
"v_add_f32_dpp v88, v88, v88 row_ror:8 row_mask:0xf bank_mask:0xf\n"
"v_mfma_f32_16x16x32_bf16 v[52:55], v[80:83], a[176:179], v[52:55]\n"
"ds_read_b128 v[72:75], v5 offset:23040\n"
"s_waitcnt lgkmcnt(3)\n"
"v_mfma_f32_16x16x32_bf16 v[36:39], v[108:111], v[100:103], v[120:123]\n"
"v_add_f32_dpp v88, v88, v88 row_ror:4 row_mask:0xf bank_mask:0xf\n"
"v_mfma_f32_16x16x32_bf16 v[32:35], v[104:107], v[100:103], v[116:119]\n"
"v_mfma_f32_16x16x32_bf16 v[52:55], v[84:87], a[188:191], v[52:55]\n"
"v_add_f32_dpp v88, v88, v88 row_ror:2 row_mask:0xf bank_mask:0xf\n"
"v_mfma_f32_16x16x32_bf16 v[48:51], a[184:187], v[84:87], v[48:51]\n"
"v_mul_f32 v88, 0x3a800000, v88\n"
"v_mfma_f32_16x16x32_bf16 v[48:51], a[172:175], v[80:83], v[48:51]\n"
"ds_read_b128 v[76:79], v5 offset:24192\n"
"v_mul_f32 v89, v88, v88\n"
"v_mfma_f32_16x16x32_bf16 v[44:47], a[168:171], v[80:83], v[44:47]\n"
"v_max3_f32 v32, v32, v33, v36\n"
"v_max3_f32 v34, v34, v35, v38\n"
"v_mfma_f32_16x16x32_bf16 v[44:47], a[180:183], v[84:87], v[44:47]\n"
"v_max_f32 v32, v32, v37\n"
"s_waitcnt lgkmcnt(2)\n"
"v_mfma_f32_16x16x32_bf16 v[44:47], a[192:195], v[56:59], v[44:47]\n"
"ds_read_b128 v[80:83], v5 offset:25344\n"
"buffer_load_dwordx4 v2, s[32:35], s63 offen offset:1024 sc1 lds\n"
"v_mfma_f32_16x16x32_bf16 v[44:47], a[204:207], v[60:63], v[44:47]\n"
"v_max_f32 v34, v34, v39\n"
"v_sub_f32_dpp v89, v88, v89 quad_perm:[1,1,3,3] row_mask:0xf bank_mask:0xf\n"
"v_mfma_f32_16x16x32_bf16 v[48:51], a[208:211], v[60:63], v[48:51]\n"
"buffer_load_dwordx2 v[12:13], v0, s[32:35], s66 offen sc1\n"
"v_max_f32 v96, v32, v34\n"
"v_mfma_f32_16x16x32_bf16 v[48:51], a[196:199], v[56:59], v[48:51]\n"
"ds_read_b128 v[84:87], v5 offset:26496\n"
"v_max_f32 v97, v32, v34\n"
"v_mfma_f32_16x16x32_bf16 v[52:55], v[56:59], a[200:203], v[52:55]\n"
"v_max_f32 v89, 0, v89\n"
"v_cvt_pk_bf16_f32 v88, 0, v88\n"
"v_mfma_f32_16x16x32_bf16 v[52:55], v[60:63], a[212:215], v[52:55]\n"
"v_permlane16_swap_b32 v96, v97\n"
"v_mfma_f32_16x16x32_bf16 v[52:55], v[64:67], a[224:227], v[52:55]\n"
"ds_read_b128 v[56:59], v5 offset:27648\n"
"v_max_f32 v98, v96, v97\n"
"v_mfma_f32_16x16x32_bf16 v[52:55], v[68:71], a[236:239], v[52:55]\n"
"v_max_f32 v99, v96, v97\n"
"v_cvt_pk_bf16_f32 v89, 0, v89\n"
"v_mfma_f32_16x16x32_bf16 v[48:51], a[232:235], v[68:71], v[48:51]\n"
"buffer_load_dwordx4 v2, s[32:35], s63 offen offset:2048 sc1 lds\n"
"v_add_f32 v89, 0x3727c5ac, v89\n"
"v_mfma_f32_16x16x32_bf16 v[48:51], a[220:223], v[64:67], v[48:51]\n"
"ds_read_b128 v[60:63], v5 offset:28800\n"
"v_cvt_pk_bf16_f32 v89, 0, v89\n"
"v_mfma_f32_16x16x32_bf16 v[44:47], a[216:219], v[64:67], v[44:47]\n"
"v_permlane32_swap_b32 v98, v99\n"
"v_mfma_f32_16x16x32_bf16 v[44:47], a[228:231], v[68:71], v[44:47]\n"
"v_max_f32 v96, v98, v99\n"
"s_waitcnt lgkmcnt(2)\n"
"v_mfma_f32_16x16x32_bf16 v[44:47], a[240:243], v[72:75], v[44:47]\n"
"ds_read_b128 v[64:67], v5 offset:29952\n"
"v_sub_f32 v32, v32, v96\n"
"v_mfma_f32_16x16x32_bf16 v[44:47], a[252:255], v[76:79], v[44:47]\n"
"v_exp_f32 v32, v32\n"
"v_mfma_f32_16x16x32_bf16 v[48:51], v[128:131], v[76:79], v[48:51]\n"
"v_sqrt_f32 v89, v89\n"
"v_mfma_f32_16x16x32_bf16 v[48:51], a[244:247], v[72:75], v[48:51]\n"
"ds_read_b128 v[68:71], v5 offset:31104\n"
"v_sub_f32 v34, v34, v96\n"
"v_mfma_f32_16x16x32_bf16 v[52:55], v[72:75], a[248:251], v[52:55]\n"
"buffer_load_dwordx4 v2, s[32:35], s63 offen offset:3072 sc1 lds\n"
"v_cvt_pk_bf16_f32 v32, v32, v32\n"
"v_mfma_f32_16x16x32_bf16 v[52:55], v[76:79], v[132:135], v[52:55]\n"
"v_exp_f32 v34, v34\n"
"v_mfma_f32_16x16x32_bf16 v[52:55], v[80:83], v[144:147], v[52:55]\n"
"ds_read_b128 v[72:75], v5 offset:32256\n"
"v_cvt_pk_bf16_f32 v34, v34, v34\n"
"v_mfma_f32_16x16x32_bf16 v[52:55], v[84:87], v[156:159], v[52:55]\n"
"v_and_b32 v92, v32, v124\n"
"v_and_b32 v94, v32, v126\n"
"v_mfma_f32_16x16x32_bf16 v[48:51], v[152:155], v[84:87], v[48:51]\n"
"v_and_b32 v93, v34, v125\n"
"v_and_b32 v95, v34, v127\n"
"v_mfma_f32_16x16x32_bf16 v[48:51], v[140:143], v[80:83], v[48:51]\n"
"ds_read_b128 v[76:79], v5 offset:33408\n"
"v_cvt_pk_bf16_f32 v89, 0, v89\n"
"v_mfma_f32_16x16x32_bf16 v[36:39], v[16:19], v[92:95], 0\n"
"v_rcp_f32 v89, v89\n"
"v_mfma_f32_16x16x32_bf16 v[40:43], v[112:115], v[92:95], 0\n"
"ds_write2_b32 v8, v88, v89 offset0:32 offset1:48\n"
"v_mfma_f32_16x16x32_bf16 v[44:47], v[136:139], v[80:83], v[44:47]\n"
"v_mfma_f32_16x16x32_bf16 v[44:47], v[148:151], v[84:87], v[44:47]\n"
"s_add_u32 s63, s63, 4096\n"
"s_waitcnt lgkmcnt(3)\n"
"v_mfma_f32_16x16x32_bf16 v[44:47], v[160:163], v[56:59], v[44:47]\n"
"ds_read_b128 v[80:83], v5 offset:34560\n"
"s_add_u32 m0, s64, 45056\n"
"v_mfma_f32_16x16x32_bf16 v[44:47], v[172:175], v[60:63], v[44:47]\n"
"buffer_load_dwordx4 v2, s[32:35], s63 offen offset:0 sc1 lds\n"
"s_xor_b32 s64, s64, s65\n"
"v_mfma_f32_16x16x32_bf16 v[48:51], v[176:179], v[60:63], v[48:51]\n"
"v_rcp_f32 v36, v36\n"
"v_mfma_f32_16x16x32_bf16 v[48:51], v[164:167], v[56:59], v[48:51]\n"
"ds_read_b128 v[84:87], v5 offset:35712\n"
"v_mul_f32 v40, v40, v36\n"
"v_mfma_f32_16x16x32_bf16 v[52:55], v[56:59], v[168:171], v[52:55]\n"
"v_xor_b32 v5, 73728, v5\n"
"v_mul_f32 v41, v41, v36\n"
"v_mfma_f32_16x16x32_bf16 v[52:55], v[60:63], v[180:183], v[52:55]\n"
"s_waitcnt lgkmcnt(2)\n"
"s_barrier\n"
"v_mfma_f32_16x16x32_bf16 v[52:55], v[64:67], v[192:195], v[52:55]\n"
"ds_read_b128 v[56:59], v4 offset:0\n"
"v_mul_f32 v42, v42, v36\n"
"v_mfma_f32_16x16x32_bf16 v[52:55], v[68:71], v[204:207], v[52:55]\n"
"v_mul_f32 v43, v43, v36\n"
"v_cvt_pk_bf16_f32 v100, v40, v41\n"
"v_mfma_f32_16x16x32_bf16 v[48:51], v[200:203], v[68:71], v[48:51]\n"
"v_cvt_pk_bf16_f32 v101, v42, v43\n"
"v_mfma_f32_16x16x32_bf16 v[48:51], v[188:191], v[64:67], v[48:51]\n"
"ds_read_b128 v[60:63], v4 offset:1152\n"
"v_mfma_f32_16x16x32_bf16 v[44:47], v[184:187], v[64:67], v[44:47]\n"
"v_permlane32_swap_b32 v100, v102\n"
"v_mfma_f32_16x16x32_bf16 v[44:47], v[196:199], v[68:71], v[44:47]\n"
"s_waitcnt lgkmcnt(2)\n"
"v_mfma_f32_16x16x32_bf16 v[44:47], v[208:211], v[72:75], v[44:47]\n"
"ds_read_b128 v[64:67], v4 offset:2304\n"
"v_mfma_f32_16x16x32_bf16 v[44:47], v[220:223], v[76:79], v[44:47]\n"
"v_permlane32_swap_b32 v101, v103\n"
"v_mfma_f32_16x16x32_bf16 v[48:51], v[224:227], v[76:79], v[48:51]\n"
"buffer_store_dwordx4 v[100:103], v1, s[32:35], s68 offen sc1\n"
"s_mov_b32 s67, s68\n"
"v_mfma_f32_16x16x32_bf16 v[48:51], v[212:215], v[72:75], v[48:51]\n"
"ds_read_b128 v[68:71], v4 offset:3456\n"
"v_mfma_f32_16x16x32_bf16 v[52:55], v[72:75], v[216:219], v[52:55]\n"
"v_mfma_f32_16x16x32_bf16 v[52:55], v[76:79], v[228:231], v[52:55]\n"
"v_mfma_f32_16x16x32_bf16 v[52:55], v[80:83], v[240:243], v[52:55]\n"
"ds_read_b128 v[72:75], v4 offset:4608\n"
"v_mfma_f32_16x16x32_bf16 v[52:55], v[84:87], v[252:255], v[52:55]\n"
"v_mfma_f32_16x16x32_bf16 v[48:51], v[248:251], v[84:87], v[48:51]\n"
"v_mfma_f32_16x16x32_bf16 v[48:51], v[236:239], v[80:83], v[48:51]\n"
"ds_read_b128 v[76:79], v4 offset:5760\n"
"v_mfma_f32_16x16x32_bf16 v[44:47], v[232:235], v[80:83], v[44:47]\n"
"s_sub_u32 s62, s62, 1\n"
"v_mfma_f32_16x16x32_bf16 v[44:47], v[244:247], v[84:87], v[44:47]\n"
"s_cbranch_scc0 .L_ql\n"
"s_nop 1\n"
"ds_read2_b32 v[14:15], v9 offset0:32 offset1:48\n"
"ds_read_b128 v[92:95], v10 offset:128\n"
"ds_read_b128 v[96:99], v10 offset:192\n"
"s_add_u32 s68, s68, 1187840\n"
"s_waitcnt lgkmcnt(0)\n"
"v_mul_f32 v29, 0x3e8293ee, v15\n"
"v_fma_f32 v48, -v14, v24, v48\n"
"v_fma_f32 v49, -v14, v25, v49\n"
"v_fma_f32 v50, -v14, v26, v50\n"
"v_fma_f32 v51, -v14, v27, v51\n"
"v_fma_f32 v44, -v14, v20, v44\n"
"v_fma_f32 v45, -v14, v21, v45\n"
"v_fma_f32 v46, -v14, v22, v46\n"
"v_fma_f32 v47, -v14, v23, v47\n"
"v_mul_f32 v48, v48, v15\n"
"v_mul_f32 v49, v49, v15\n"
"v_mul_f32 v50, v50, v15\n"
"v_mul_f32 v51, v51, v15\n"
"v_mul_f32 v44, v44, v29\n"
"v_mul_f32 v45, v45, v29\n"
"v_mul_f32 v46, v46, v29\n"
"v_mul_f32 v47, v47, v29\n"
"v_cvt_pk_bf16_f32 v108, v48, v49\n"
"v_cvt_pk_bf16_f32 v109, v50, v51\n"
"v_cvt_pk_bf16_f32 v100, v44, v45\n"
"v_cvt_pk_bf16_f32 v101, v46, v47\n"
"ds_write2st64_b64 v11, v[100:101], v[108:109] offset0:0 offset1:1\n"
"v_fma_f32 v52, -v92, v28, v52\n"
"v_fma_f32 v53, -v93, v28, v53\n"
"v_fma_f32 v54, -v94, v28, v54\n"
"v_fma_f32 v55, -v95, v28, v55\n"
"v_mul_f32 v52, v52, v96\n"
"v_mul_f32 v53, v53, v97\n"
"v_mul_f32 v54, v54, v98\n"
"v_mul_f32 v55, v55, v99\n"
"s_waitcnt lgkmcnt(0)\n"
"s_waitcnt vmcnt(0)\n"
"s_barrier\n"
"ds_read_b64 v[102:103], v3\n"
"ds_read_b64 v[110:111], v3 offset:512\n"
"buffer_store_dwordx2 v[30:31], v2, s[92:95], s67 idxen sc1\n"
"v_cvt_pk_bf16_f32 v114, v52, v53\n"
"v_cvt_pk_bf16_f32 v115, v54, v55\n"
"s_waitcnt lgkmcnt(0)\n"
"v_mfma_f32_16x16x32_bf16 v[48:51], v[104:107], v[100:103], v[120:123]\n"
"v_mfma_f32_16x16x32_bf16 v[44:47], v[108:111], v[100:103], v[116:119]\n"
"s_nop 7\n"
"v_max3_f32 v44, v44, v45, v48\n"
"v_max3_f32 v46, v46, v47, v50\n"
"v_max_f32 v44, v44, v49\n"
"v_max_f32 v46, v46, v51\n"
"v_max_f32 v96, v44, v46\n"
"v_max_f32 v97, v44, v46\n"
"s_nop 1\n"
"v_permlane16_swap_b32 v96, v97\n"
"v_max_f32 v98, v96, v97\n"
"v_max_f32 v99, v96, v97\n"
"s_nop 1\n"
"v_permlane32_swap_b32 v98, v99\n"
"v_max_f32 v96, v98, v99\n"
"v_sub_f32 v44, v44, v96\n"
"v_sub_f32 v46, v46, v96\n"
"v_exp_f32 v44, v44\n"
"v_exp_f32 v46, v46\n"
"v_cvt_pk_bf16_f32 v44, v44, v44\n"
"v_cvt_pk_bf16_f32 v46, v46, v46\n"
"v_and_b32 v94, v44, v124\n"
"v_and_b32 v92, v44, v126\n"
"v_and_b32 v95, v46, v125\n"
"v_and_b32 v93, v46, v127\n"
"s_nop 1\n"
"v_mfma_f32_16x16x32_bf16 v[48:51], v[16:19], v[92:95], 0\n"
"v_mfma_f32_16x16x32_bf16 v[52:55], v[112:115], v[92:95], 0\n"
"s_nop 6\n"
"v_rcp_f32 v48, v48\n"
"s_nop 0\n"
"v_mul_f32 v52, v52, v48\n"
"v_mul_f32 v53, v53, v48\n"
"v_mul_f32 v54, v54, v48\n"
"v_mul_f32 v55, v55, v48\n"
"v_cvt_pk_bf16_f32 v100, v52, v53\n"
"v_cvt_pk_bf16_f32 v101, v54, v55\n"
"s_nop 0\n"
"v_permlane32_swap_b32 v100, v102\n"
"v_permlane32_swap_b32 v101, v103\n"
"buffer_store_dwordx4 v[100:103], v1, s[32:35], s68 offen sc1\n"
"s_mov_b32 s67, s68\n"
"s_waitcnt vmcnt(0)\n"
"s_barrier\n"
"buffer_store_dwordx2 v[30:31], v2, s[92:95], s67 idxen sc1\n"
"s_branch .L_end\n"
".L_w:\n"
"s_mov_b32 s96, 0\n"
"s_add_u32 s36, s12, 0x0\n"
"s_addc_u32 s37, s13, 0x0\n"
"s_and_b32 s37, s37, 0xffff\n"
"s_mov_b32 s38, 0x200000\n"
"s_mov_b32 s39, 0x20000\n"
"s_add_u32 s40, s12, 0xfffffc80\n"
"s_addc_u32 s41, s13, 0xffffffff\n"
"s_and_b32 s41, s41, 0xffff\n"
"s_mov_b32 s42, 0x200380\n"
"s_mov_b32 s43, 0x20000\n"
"s_lshl_b32 s48, s27, 8\n"
"s_add_u32 s96, s96, s48\n"
"v_and_b32 v8, 3, v1\n"
"v_mul_u32_u24 v8, 16, v8\n"
"s_and_b32 s48, s26, 1\n"
"s_lshl_b32 s48, s48, 6\n"
"s_lshr_b32 s49, s26, 1\n"
"s_lshl_b32 s49, s49, 10\n"
"s_add_u32 s48, s48, s49\n"
"v_add_u32 v8, s48, v8\n"
"v_lshrrev_b32 v4, 3, v1\n"
"v_bfe_u32 v5, v1, 4, 1\n"
"s_and_b32 s48, s26, 1\n"
"s_lshl_b32 s48, s48, 1\n"
"v_or_b32 v5, s48, v5\n"
"v_lshlrev_b32 v5, 1, v5\n"
"v_and_b32 v6, 7, v1\n"
"v_xor_b32 v5, v5, v6\n"
"v_mul_u32_u24 v4, 2048, v4\n"
"v_lshl_add_u32 v7, v5, 4, v4\n"
"s_mul_i32 s48, s26, 16384\n"
"s_add_u32 s96, s96, s48\n"
"s_mul_i32 s97, s26, 2048\n"
"s_add_u32 m0, s97, 0\n"
"s_nop 0\n"
"buffer_load_dwordx4 v7, s[36:39], s96 offen offset:0 lds\n"
"buffer_load_dwordx4 v7, s[40:43], s96 offen offset:1024 lds\n"
"s_add_u32 s96, s96, 65536\n"
"s_add_u32 m0, s97, 8192\n"
"s_nop 0\n"
"buffer_load_dwordx4 v7, s[36:39], s96 offen offset:0 lds\n"
"buffer_load_dwordx4 v7, s[40:43], s96 offen offset:1024 lds\n"
"s_add_u32 s96, s96, 65536\n"
"s_add_u32 m0, s97, 16384\n"
"s_nop 0\n"
"buffer_load_dwordx4 v7, s[36:39], s96 offen offset:0 lds\n"
"buffer_load_dwordx4 v7, s[40:43], s96 offen offset:1024 lds\n"
"s_add_u32 s96, s96, 65536\n"
"s_add_u32 m0, s97, 24576\n"
"s_nop 0\n"
"buffer_load_dwordx4 v7, s[36:39], s96 offen offset:0 lds\n"
"buffer_load_dwordx4 v7, s[40:43], s96 offen offset:1024 lds\n"
"s_add_u32 s96, s96, 65536\n"
"s_add_u32 m0, s97, 32768\n"
"s_nop 0\n"
"buffer_load_dwordx4 v7, s[36:39], s96 offen offset:0 lds\n"
"buffer_load_dwordx4 v7, s[40:43], s96 offen offset:1024 lds\n"
"s_add_u32 s96, s96, 65536\n"
"s_add_u32 m0, s97, 40960\n"
"s_nop 0\n"
"buffer_load_dwordx4 v7, s[36:39], s96 offen offset:0 lds\n"
"buffer_load_dwordx4 v7, s[40:43], s96 offen offset:1024 lds\n"
"s_add_u32 s96, s96, 65536\n"
"s_add_u32 m0, s97, 49152\n"
"s_nop 0\n"
"buffer_load_dwordx4 v7, s[36:39], s96 offen offset:0 lds\n"
"buffer_load_dwordx4 v7, s[40:43], s96 offen offset:1024 lds\n"
"s_add_u32 s96, s96, 65536\n"
"s_add_u32 m0, s97, 57344\n"
"s_nop 0\n"
"buffer_load_dwordx4 v7, s[36:39], s96 offen offset:0 lds\n"
"buffer_load_dwordx4 v7, s[40:43], s96 offen offset:1024 lds\n"
"s_add_u32 s96, s96, 65536\n"
"s_add_u32 m0, s97, 65536\n"
"s_nop 0\n"
"buffer_load_dwordx4 v7, s[36:39], s96 offen offset:0 lds\n"
"buffer_load_dwordx4 v7, s[40:43], s96 offen offset:1024 lds\n"
"s_add_u32 s96, s96, 65536\n"
"s_add_u32 m0, s97, 73728\n"
"s_nop 0\n"
"buffer_load_dwordx4 v7, s[36:39], s96 offen offset:0 lds\n"
"buffer_load_dwordx4 v7, s[40:43], s96 offen offset:1024 lds\n"
"s_add_u32 s96, s96, 65536\n"
"s_add_u32 m0, s97, 81920\n"
"s_nop 0\n"
"buffer_load_dwordx4 v7, s[36:39], s96 offen offset:0 lds\n"
"buffer_load_dwordx4 v7, s[40:43], s96 offen offset:1024 lds\n"
"s_add_u32 s96, s96, 65536\n"
"s_add_u32 m0, s97, 90112\n"
"s_nop 0\n"
"buffer_load_dwordx4 v7, s[36:39], s96 offen offset:0 lds\n"
"buffer_load_dwordx4 v7, s[40:43], s96 offen offset:1024 lds\n"
"s_add_u32 s96, s96, 65536\n"
"s_add_u32 m0, s97, 98304\n"
"s_nop 0\n"
"buffer_load_dwordx4 v7, s[36:39], s96 offen offset:0 lds\n"
"buffer_load_dwordx4 v7, s[40:43], s96 offen offset:1024 lds\n"
"s_add_u32 s96, s96, 65536\n"
"s_add_u32 m0, s97, 106496\n"
"s_nop 0\n"
"buffer_load_dwordx4 v7, s[36:39], s96 offen offset:0 lds\n"
"buffer_load_dwordx4 v7, s[40:43], s96 offen offset:1024 lds\n"
"s_add_u32 s96, s96, 65536\n"
"v_lshrrev_b32 v5, 4, v1\n"
"v_bfe_u32 v4, v1, 3, 1\n"
"v_and_b32 v6, 1, v5\n"
"v_lshl_add_u32 v4, v6, 1, v4\n"
"v_lshlrev_b32 v4, 5, v4\n"
"v_mul_u32_u24 v5, 2048, v5\n"
"v_bfe_u32 v6, v1, 2, 2\n"
"v_lshl_add_u32 v5, v6, 7, v5\n"
"v_xor_b32 v8, v4, v8\n"
"v_add_u32 v8, v5, v8\n"
"v_add_u32 v9, 65536, v8\n"
"s_waitcnt vmcnt(24)\n"
"s_barrier\n"
"s_add_u32 m0, s97, 114688\n"
"s_nop 0\n"
"buffer_load_dwordx4 v7, s[36:39], s96 offen offset:0 lds\n"
"buffer_load_dwordx4 v7, s[40:43], s96 offen offset:1024 lds\n"
"s_add_u32 s96, s96, 65536\n"
"s_add_u32 m0, s97, 122880\n"
"s_nop 0\n"
"buffer_load_dwordx4 v7, s[36:39], s96 offen offset:0 lds\n"
"buffer_load_dwordx4 v7, s[40:43], s96 offen offset:1024 lds\n"
"s_add_u32 s96, s96, 65536\n"
"ds_read_b64_tr_b16 a[0:1], v8 offset:0\n"
"ds_read_b64_tr_b16 a[2:3], v8 offset:512\n"
"ds_read_b64_tr_b16 a[4:5], v8 offset:8\n"
"ds_read_b64_tr_b16 a[6:7], v8 offset:520\n"
"ds_read_b64_tr_b16 a[8:9], v8 offset:8192\n"
"ds_read_b64_tr_b16 a[10:11], v8 offset:8704\n"
"ds_read_b64_tr_b16 a[12:13], v8 offset:8200\n"
"ds_read_b64_tr_b16 a[14:15], v8 offset:8712\n"
"s_waitcnt lgkmcnt(0)\n"
"s_waitcnt vmcnt(24)\n"
"s_barrier\n"
"s_add_u32 m0, s97, 0\n"
"s_nop 0\n"
"buffer_load_dwordx4 v7, s[36:39], s96 offen offset:0 lds\n"
"buffer_load_dwordx4 v7, s[40:43], s96 offen offset:1024 lds\n"
"s_add_u32 s96, s96, 65536\n"
"s_add_u32 m0, s97, 8192\n"
"s_nop 0\n"
"buffer_load_dwordx4 v7, s[36:39], s96 offen offset:0 lds\n"
"buffer_load_dwordx4 v7, s[40:43], s96 offen offset:1024 lds\n"
"s_add_u32 s96, s96, 65536\n"
"ds_read_b64_tr_b16 a[16:17], v8 offset:16384\n"
"ds_read_b64_tr_b16 a[18:19], v8 offset:16896\n"
"ds_read_b64_tr_b16 a[20:21], v8 offset:16392\n"
"ds_read_b64_tr_b16 a[22:23], v8 offset:16904\n"
"ds_read_b64_tr_b16 a[24:25], v8 offset:24576\n"
"ds_read_b64_tr_b16 a[26:27], v8 offset:25088\n"
"ds_read_b64_tr_b16 a[28:29], v8 offset:24584\n"
"ds_read_b64_tr_b16 a[30:31], v8 offset:25096\n"
"s_waitcnt lgkmcnt(0)\n"
"s_waitcnt vmcnt(24)\n"
"s_barrier\n"
"s_add_u32 m0, s97, 16384\n"
"s_nop 0\n"
"buffer_load_dwordx4 v7, s[36:39], s96 offen offset:0 lds\n"
"buffer_load_dwordx4 v7, s[40:43], s96 offen offset:1024 lds\n"
"s_add_u32 s96, s96, 65536\n"
"s_add_u32 m0, s97, 24576\n"
"s_nop 0\n"
"buffer_load_dwordx4 v7, s[36:39], s96 offen offset:0 lds\n"
"buffer_load_dwordx4 v7, s[40:43], s96 offen offset:1024 lds\n"
"s_add_u32 s96, s96, 65536\n"
"ds_read_b64_tr_b16 a[32:33], v8 offset:32768\n"
"ds_read_b64_tr_b16 a[34:35], v8 offset:33280\n"
"ds_read_b64_tr_b16 a[36:37], v8 offset:32776\n"
"ds_read_b64_tr_b16 a[38:39], v8 offset:33288\n"
"ds_read_b64_tr_b16 a[40:41], v8 offset:40960\n"
"ds_read_b64_tr_b16 a[42:43], v8 offset:41472\n"
"ds_read_b64_tr_b16 a[44:45], v8 offset:40968\n"
"ds_read_b64_tr_b16 a[46:47], v8 offset:41480\n"
"s_waitcnt lgkmcnt(0)\n"
"s_waitcnt vmcnt(24)\n"
"s_barrier\n"
"s_add_u32 m0, s97, 32768\n"
"s_nop 0\n"
"buffer_load_dwordx4 v7, s[36:39], s96 offen offset:0 lds\n"
"buffer_load_dwordx4 v7, s[40:43], s96 offen offset:1024 lds\n"
"s_add_u32 s96, s96, 65536\n"
"s_add_u32 m0, s97, 40960\n"
"s_nop 0\n"
"buffer_load_dwordx4 v7, s[36:39], s96 offen offset:0 lds\n"
"buffer_load_dwordx4 v7, s[40:43], s96 offen offset:1024 lds\n"
"s_add_u32 s96, s96, 65536\n"
"ds_read_b64_tr_b16 a[48:49], v8 offset:49152\n"
"ds_read_b64_tr_b16 a[50:51], v8 offset:49664\n"
"ds_read_b64_tr_b16 a[52:53], v8 offset:49160\n"
"ds_read_b64_tr_b16 a[54:55], v8 offset:49672\n"
"ds_read_b64_tr_b16 a[56:57], v8 offset:57344\n"
"ds_read_b64_tr_b16 a[58:59], v8 offset:57856\n"
"ds_read_b64_tr_b16 a[60:61], v8 offset:57352\n"
"ds_read_b64_tr_b16 a[62:63], v8 offset:57864\n"
"s_waitcnt lgkmcnt(0)\n"
"s_waitcnt vmcnt(24)\n"
"s_barrier\n"
"s_add_u32 m0, s97, 49152\n"
"s_nop 0\n"
"buffer_load_dwordx4 v7, s[36:39], s96 offen offset:0 lds\n"
"buffer_load_dwordx4 v7, s[40:43], s96 offen offset:1024 lds\n"
"s_add_u32 s96, s96, 65536\n"
"s_add_u32 m0, s97, 57344\n"
"s_nop 0\n"
"buffer_load_dwordx4 v7, s[36:39], s96 offen offset:0 lds\n"
"buffer_load_dwordx4 v7, s[40:43], s96 offen offset:1024 lds\n"
"s_add_u32 s96, s96, 65536\n"
"ds_read_b64_tr_b16 a[64:65], v9 offset:0\n"
"ds_read_b64_tr_b16 a[66:67], v9 offset:512\n"
"ds_read_b64_tr_b16 a[68:69], v9 offset:8\n"
"ds_read_b64_tr_b16 a[70:71], v9 offset:520\n"
"ds_read_b64_tr_b16 a[72:73], v9 offset:8192\n"
"ds_read_b64_tr_b16 a[74:75], v9 offset:8704\n"
"ds_read_b64_tr_b16 a[76:77], v9 offset:8200\n"
"ds_read_b64_tr_b16 a[78:79], v9 offset:8712\n"
"s_waitcnt lgkmcnt(0)\n"
"s_waitcnt vmcnt(24)\n"
"s_barrier\n"
"s_add_u32 m0, s97, 65536\n"
"s_nop 0\n"
"buffer_load_dwordx4 v7, s[36:39], s96 offen offset:0 lds\n"
"buffer_load_dwordx4 v7, s[40:43], s96 offen offset:1024 lds\n"
"s_add_u32 s96, s96, 65536\n"
"s_add_u32 m0, s97, 73728\n"
"s_nop 0\n"
"buffer_load_dwordx4 v7, s[36:39], s96 offen offset:0 lds\n"
"buffer_load_dwordx4 v7, s[40:43], s96 offen offset:1024 lds\n"
"s_add_u32 s96, s96, 65536\n"
"ds_read_b64_tr_b16 a[80:81], v9 offset:16384\n"
"ds_read_b64_tr_b16 a[82:83], v9 offset:16896\n"
"ds_read_b64_tr_b16 a[84:85], v9 offset:16392\n"
"ds_read_b64_tr_b16 a[86:87], v9 offset:16904\n"
"ds_read_b64_tr_b16 a[88:89], v9 offset:24576\n"
"ds_read_b64_tr_b16 a[90:91], v9 offset:25088\n"
"ds_read_b64_tr_b16 a[92:93], v9 offset:24584\n"
"ds_read_b64_tr_b16 a[94:95], v9 offset:25096\n"
"s_waitcnt lgkmcnt(0)\n"
"s_waitcnt vmcnt(24)\n"
"s_barrier\n"
"s_add_u32 m0, s97, 81920\n"
"s_nop 0\n"
"buffer_load_dwordx4 v7, s[36:39], s96 offen offset:0 lds\n"
"buffer_load_dwordx4 v7, s[40:43], s96 offen offset:1024 lds\n"
"s_add_u32 s96, s96, 65536\n"
"s_add_u32 m0, s97, 90112\n"
"s_nop 0\n"
"buffer_load_dwordx4 v7, s[36:39], s96 offen offset:0 lds\n"
"buffer_load_dwordx4 v7, s[40:43], s96 offen offset:1024 lds\n"
"s_add_u32 s96, s96, 65536\n"
"ds_read_b64_tr_b16 a[96:97], v9 offset:32768\n"
"ds_read_b64_tr_b16 a[98:99], v9 offset:33280\n"
"ds_read_b64_tr_b16 a[100:101], v9 offset:32776\n"
"ds_read_b64_tr_b16 a[102:103], v9 offset:33288\n"
"ds_read_b64_tr_b16 a[104:105], v9 offset:40960\n"
"ds_read_b64_tr_b16 a[106:107], v9 offset:41472\n"
"ds_read_b64_tr_b16 a[108:109], v9 offset:40968\n"
"ds_read_b64_tr_b16 a[110:111], v9 offset:41480\n"
"s_waitcnt lgkmcnt(0)\n"
"s_waitcnt vmcnt(24)\n"
"s_barrier\n"
"s_add_u32 m0, s97, 98304\n"
"s_nop 0\n"
"buffer_load_dwordx4 v7, s[36:39], s96 offen offset:0 lds\n"
"buffer_load_dwordx4 v7, s[40:43], s96 offen offset:1024 lds\n"
"s_add_u32 s96, s96, 65536\n"
"s_add_u32 m0, s97, 106496\n"
"s_nop 0\n"
"buffer_load_dwordx4 v7, s[36:39], s96 offen offset:0 lds\n"
"buffer_load_dwordx4 v7, s[40:43], s96 offen offset:1024 lds\n"
"s_add_u32 s96, s96, 65536\n"
"ds_read_b64_tr_b16 a[112:113], v9 offset:49152\n"
"ds_read_b64_tr_b16 a[114:115], v9 offset:49664\n"
"ds_read_b64_tr_b16 a[116:117], v9 offset:49160\n"
"ds_read_b64_tr_b16 a[118:119], v9 offset:49672\n"
"ds_read_b64_tr_b16 a[120:121], v9 offset:57344\n"
"ds_read_b64_tr_b16 a[122:123], v9 offset:57856\n"
"ds_read_b64_tr_b16 a[124:125], v9 offset:57352\n"
"ds_read_b64_tr_b16 a[126:127], v9 offset:57864\n"
"s_waitcnt lgkmcnt(0)\n"
"s_waitcnt vmcnt(24)\n"
"s_barrier\n"
"s_add_u32 m0, s97, 114688\n"
"s_nop 0\n"
"buffer_load_dwordx4 v7, s[36:39], s96 offen offset:0 lds\n"
"buffer_load_dwordx4 v7, s[40:43], s96 offen offset:1024 lds\n"
"s_add_u32 s96, s96, 65536\n"
"s_add_u32 m0, s97, 122880\n"
"s_nop 0\n"
"buffer_load_dwordx4 v7, s[36:39], s96 offen offset:0 lds\n"
"buffer_load_dwordx4 v7, s[40:43], s96 offen offset:1024 lds\n"
"s_add_u32 s96, s96, 65536\n"
"ds_read_b64_tr_b16 a[128:129], v8 offset:0\n"
"ds_read_b64_tr_b16 a[130:131], v8 offset:512\n"
"ds_read_b64_tr_b16 a[132:133], v8 offset:8\n"
"ds_read_b64_tr_b16 a[134:135], v8 offset:520\n"
"ds_read_b64_tr_b16 a[136:137], v8 offset:8192\n"
"ds_read_b64_tr_b16 a[138:139], v8 offset:8704\n"
"ds_read_b64_tr_b16 a[140:141], v8 offset:8200\n"
"ds_read_b64_tr_b16 a[142:143], v8 offset:8712\n"
"s_waitcnt lgkmcnt(0)\n"
"s_waitcnt vmcnt(24)\n"
"s_barrier\n"
"ds_read_b64_tr_b16 a[144:145], v8 offset:16384\n"
"ds_read_b64_tr_b16 a[146:147], v8 offset:16896\n"
"ds_read_b64_tr_b16 a[148:149], v8 offset:16392\n"
"ds_read_b64_tr_b16 a[150:151], v8 offset:16904\n"
"ds_read_b64_tr_b16 a[152:153], v8 offset:24576\n"
"ds_read_b64_tr_b16 a[154:155], v8 offset:25088\n"
"ds_read_b64_tr_b16 a[156:157], v8 offset:24584\n"
"ds_read_b64_tr_b16 a[158:159], v8 offset:25096\n"
"s_waitcnt lgkmcnt(0)\n"
"s_waitcnt vmcnt(20)\n"
"s_barrier\n"
"ds_read_b64_tr_b16 a[160:161], v8 offset:32768\n"
"ds_read_b64_tr_b16 a[162:163], v8 offset:33280\n"
"ds_read_b64_tr_b16 a[164:165], v8 offset:32776\n"
"ds_read_b64_tr_b16 a[166:167], v8 offset:33288\n"
"ds_read_b64_tr_b16 a[168:169], v8 offset:40960\n"
"ds_read_b64_tr_b16 a[170:171], v8 offset:41472\n"
"ds_read_b64_tr_b16 a[172:173], v8 offset:40968\n"
"ds_read_b64_tr_b16 a[174:175], v8 offset:41480\n"
"s_waitcnt lgkmcnt(0)\n"
"s_waitcnt vmcnt(16)\n"
"s_barrier\n"
"ds_read_b64_tr_b16 a[176:177], v8 offset:49152\n"
"ds_read_b64_tr_b16 a[178:179], v8 offset:49664\n"
"ds_read_b64_tr_b16 a[180:181], v8 offset:49160\n"
"ds_read_b64_tr_b16 a[182:183], v8 offset:49672\n"
"ds_read_b64_tr_b16 a[184:185], v8 offset:57344\n"
"ds_read_b64_tr_b16 a[186:187], v8 offset:57856\n"
"ds_read_b64_tr_b16 a[188:189], v8 offset:57352\n"
"ds_read_b64_tr_b16 a[190:191], v8 offset:57864\n"
"s_waitcnt lgkmcnt(0)\n"
"s_waitcnt vmcnt(12)\n"
"s_barrier\n"
"ds_read_b64_tr_b16 a[192:193], v9 offset:0\n"
"ds_read_b64_tr_b16 a[194:195], v9 offset:512\n"
"ds_read_b64_tr_b16 a[196:197], v9 offset:8\n"
"ds_read_b64_tr_b16 a[198:199], v9 offset:520\n"
"ds_read_b64_tr_b16 a[200:201], v9 offset:8192\n"
"ds_read_b64_tr_b16 a[202:203], v9 offset:8704\n"
"ds_read_b64_tr_b16 a[204:205], v9 offset:8200\n"
"ds_read_b64_tr_b16 a[206:207], v9 offset:8712\n"
"s_waitcnt lgkmcnt(0)\n"
"s_waitcnt vmcnt(8)\n"
"s_barrier\n"
"ds_read_b64_tr_b16 a[208:209], v9 offset:16384\n"
"ds_read_b64_tr_b16 a[210:211], v9 offset:16896\n"
"ds_read_b64_tr_b16 a[212:213], v9 offset:16392\n"
"ds_read_b64_tr_b16 a[214:215], v9 offset:16904\n"
"ds_read_b64_tr_b16 a[216:217], v9 offset:24576\n"
"ds_read_b64_tr_b16 a[218:219], v9 offset:25088\n"
"ds_read_b64_tr_b16 a[220:221], v9 offset:24584\n"
"ds_read_b64_tr_b16 a[222:223], v9 offset:25096\n"
"s_waitcnt lgkmcnt(0)\n"
"s_waitcnt vmcnt(4)\n"
"s_barrier\n"
"ds_read_b64_tr_b16 a[224:225], v9 offset:32768\n"
"ds_read_b64_tr_b16 a[226:227], v9 offset:33280\n"
"ds_read_b64_tr_b16 a[228:229], v9 offset:32776\n"
"ds_read_b64_tr_b16 a[230:231], v9 offset:33288\n"
"ds_read_b64_tr_b16 a[232:233], v9 offset:40960\n"
"ds_read_b64_tr_b16 a[234:235], v9 offset:41472\n"
"ds_read_b64_tr_b16 a[236:237], v9 offset:40968\n"
"ds_read_b64_tr_b16 a[238:239], v9 offset:41480\n"
"s_waitcnt lgkmcnt(0)\n"
"s_waitcnt vmcnt(0)\n"
"s_barrier\n"
"ds_read_b64_tr_b16 a[240:241], v9 offset:49152\n"
"ds_read_b64_tr_b16 a[242:243], v9 offset:49664\n"
"ds_read_b64_tr_b16 a[244:245], v9 offset:49160\n"
"ds_read_b64_tr_b16 a[246:247], v9 offset:49672\n"
"ds_read_b64_tr_b16 a[248:249], v9 offset:57344\n"
"ds_read_b64_tr_b16 a[250:251], v9 offset:57856\n"
"ds_read_b64_tr_b16 a[252:253], v9 offset:57352\n"
"ds_read_b64_tr_b16 a[254:255], v9 offset:57864\n"
"s_waitcnt lgkmcnt(0)\n"
"s_barrier\n"
"s_lshl2_add_u32 s50, s27, s26\n"
"s_and_b32 s51, s50, 6\n"
"s_lshl_b32 s51, s51, 1\n"
"v_add_u32 v3, s51, v1\n"
"v_bfi_b32 v3, 15, v3, v1\n"
"v_lshlrev_b32 v3, 2, v3\n"
"v_cmp_gt_u32 vcc, 32, v1\n"
"v_mov_b32 v15, 0x7fff0000\n"
"s_nop 0\n"
"v_cndmask_b32 v15, v15, v3, vcc\n"
"s_mul_i32 s48, s50, 1152\n"
"s_add_u32 s68, s48, 4681728\n"
"s_lshl2_add_u32 s49, s27, 96\n"
"s_waitcnt lgkmcnt(0)\n"
"s_add_u32 s54, s52, 0x7f4a7c15\n"
"s_addc_u32 s55, s53, 0x9e3779b9\n"
"s_lshl_b32 s67, s49, 5\n"
"s_add_u32 s67, s67, 4898816\n"
"s_sub_u32 s51, s67, s68\n"
"s_add_u32 s92, s32, s51\n"
"s_addc_u32 s93, s33, 0x10000\n"
"s_sub_u32 s92, s92, 593920\n"
"s_subb_u32 s93, s93, 0\n"
"s_cmp_eq_u32 s26, 0\n"
"s_cselect_b32 s94, 1, 0\n"
"s_mov_b32 s95, 0x20000\n"
"v_mov_b32 v16, s54\n"
"v_mov_b32 v17, s55\n"
"v_cmp_gt_u32 vcc, 12, v1\n"
"v_mul_u32_u24 v3, 296960, v1\n"
"v_mov_b32 v12, 0x7fff0000\n"
"v_cndmask_b32 v3, v12, v3, vcc\n"
"s_add_u32 s51, s67, 76021760\n"
"buffer_store_dwordx2 v[16:17], v3, s[32:35], s51 offen sc1\n"
"s_sub_u32 s67, s68, 296960\n"
"s_mul_i32 s49, s28, 296960\n"
"s_add_u32 s48, s48, s49\n"
"s_add_u32 s71, s48, 4612096\n"
"s_add_u32 s68, s48, 3493888\n"
"s_mov_b32 s62, 63\n"
"s_add_u32 s63, s49, 4648960\n"
"s_add_u32 s66, s49, 4898816\n"
"s_mul_i32 s64, s26, 8192\n"
"s_add_u32 s65, s64, 65536\n"
"s_xor_b32 s65, s65, s64\n"
"s_add_u32 s63, s63, s64\n"
"v_mov_b32 v4, v2\n"
"v_add_u32 v6, 4096, v2\n"
"v_and_b32 v14, 15, v1\n"
"v_lshlrev_b32 v14, 2, v14\n"
"s_mov_b32 s48, 32\n"
"v_add_u32 v14, s48, v14\n"
"v_lshrrev_b32 v14, 2, v14\n"
"v_lshlrev_b32 v14, 7, v14\n"
"v_lshrrev_b32 v3, 4, v1\n"
"v_mul_u32_u24 v3, 593920, v3\n"
"v_add_u32 v14, v3, v14\n"
"v_cmp_gt_u32 vcc, 32, v1\n"
"v_mov_b32 v3, 0x7fff0000\n"
"s_nop 0\n"
"v_cndmask_b32 v14, v3, v14, vcc\n"
"v_cndmask_b32 v16, 0, v16, vcc\n"
"v_cndmask_b32 v17, 0, v17, vcc\n"
"s_mov_b32 s48, 20000\n"
"s_branch .L_rfwa\n"
".L_rlwa:\n"
"s_sleep 8\n"
".L_rfwa:\n"
"buffer_load_dwordx2 v[12:13], v14, s[32:35], s66 offen sc1\n"
"s_sub_u32 s48, s48, 1\n"
"s_cbranch_scc1 .L_end\n"
"s_waitcnt vmcnt(0)\n"
"v_cmp_ne_u64 vcc, v[16:17], v[12:13]\n"
"s_cbranch_vccnz .L_rlwa\n"
"s_add_u32 s66, s66, 1187840\n"
"s_add_u32 m0, s64, 0\n"
"s_nop 0\n"
"buffer_load_dwordx4 v2, s[32:35], s63 offen offset:0 sc1 lds\n"
"buffer_load_dwordx4 v2, s[32:35], s63 offen offset:1024 sc1 lds\n"
"buffer_load_dwordx4 v2, s[32:35], s63 offen offset:2048 sc1 lds\n"
"buffer_load_dwordx4 v2, s[32:35], s63 offen offset:3072 sc1 lds\n"
"s_add_u32 m0, s64, 4096\n"
"s_nop 0\n"
"buffer_load_dwordx4 v6, s[32:35], s63 offen offset:0 sc1 lds\n"
"buffer_load_dwordx4 v6, s[32:35], s63 offen offset:1024 sc1 lds\n"
"buffer_load_dwordx4 v6, s[32:35], s63 offen offset:2048 sc1 lds\n"
"buffer_load_dwordx4 v6, s[32:35], s63 offen offset:3072 sc1 lds\n"
"s_add_u32 s63, s63, 593920\n"
"buffer_load_dwordx4 v[48:51], v2, s[32:35], s71 offen sc1\n"
"s_add_u32 s71, s71, 593920\n"
"buffer_load_dwordx4 v[52:55], v2, s[32:35], s71 offen sc1\n"
"s_add_u32 s71, s71, 593920\n"
"s_add_u32 m0, s64, 32768\n"
"s_nop 0\n"
"buffer_load_dwordx4 v2, s[32:35], s63 offen offset:0 sc1 lds\n"
"buffer_load_dwordx4 v2, s[32:35], s63 offen offset:1024 sc1 lds\n"
"buffer_load_dwordx4 v2, s[32:35], s63 offen offset:2048 sc1 lds\n"
"buffer_load_dwordx4 v2, s[32:35], s63 offen offset:3072 sc1 lds\n"
"s_add_u32 m0, s64, 36864\n"
"s_nop 0\n"
"buffer_load_dwordx4 v6, s[32:35], s63 offen offset:0 sc1 lds\n"
"buffer_load_dwordx4 v6, s[32:35], s63 offen offset:1024 sc1 lds\n"
"buffer_load_dwordx4 v6, s[32:35], s63 offen offset:2048 sc1 lds\n"
"buffer_load_dwordx4 v6, s[32:35], s63 offen offset:3072 sc1 lds\n"
"s_add_u32 s63, s63, 593920\n"
"s_xor_b32 s64, s64, s65\n"
"s_mov_b32 s48, 20000\n"
"s_branch .L_rfwc\n"
".L_rlwc:\n"
"s_sleep 8\n"
".L_rfwc:\n"
"buffer_load_dwordx2 v[12:13], v14, s[32:35], s66 offen sc1\n"
"s_sub_u32 s48, s48, 1\n"
"s_cbranch_scc1 .L_end\n"
"s_waitcnt vmcnt(0)\n"
"v_cmp_ne_u64 vcc, v[16:17], v[12:13]\n"
"s_cbranch_vccnz .L_rlwc\n"
"s_add_u32 s66, s66, 1187840\n"
"s_add_u32 m0, s64, 0\n"
"s_nop 0\n"
"buffer_load_dwordx4 v2, s[32:35], s63 offen offset:0 sc1 lds\n"
"buffer_load_dwordx4 v2, s[32:35], s63 offen offset:1024 sc1 lds\n"
"buffer_load_dwordx4 v2, s[32:35], s63 offen offset:2048 sc1 lds\n"
"buffer_load_dwordx4 v2, s[32:35], s63 offen offset:3072 sc1 lds\n"
"s_add_u32 m0, s64, 4096\n"
"s_nop 0\n"
"buffer_load_dwordx4 v6, s[32:35], s63 offen offset:0 sc1 lds\n"
"s_barrier\n"
"ds_read_b128 v[128:131], v4 offset:0\n"
"ds_read_b128 v[132:135], v4 offset:1024\n"
"ds_read_b128 v[136:139], v4 offset:2048\n"
"ds_read_b128 v[140:143], v4 offset:3072\n"
"ds_read_b128 v[144:147], v4 offset:4096\n"
"ds_read_b128 v[148:151], v4 offset:5120\n"
"ds_read_b128 v[152:155], v4 offset:6144\n"
"ds_read_b128 v[156:159], v4 offset:7168\n"
"ds_read_b128 v[160:163], v4 offset:8192\n"
"ds_read_b128 v[164:167], v4 offset:9216\n"
"ds_read_b128 v[168:171], v4 offset:10240\n"
"ds_read_b128 v[172:175], v4 offset:11264\n"
"v_lshlrev_b32 v32, 16, v48\n"
"v_and_b32 v33, 0xffff0000, v48\n"
"v_lshlrev_b32 v34, 16, v49\n"
"v_and_b32 v35, 0xffff0000, v49\n"
"v_lshlrev_b32 v36, 16, v50\n"
"v_and_b32 v37, 0xffff0000, v50\n"
"v_lshlrev_b32 v38, 16, v51\n"
"v_and_b32 v39, 0xffff0000, v51\n"
"s_cmp_eq_u32 s26, 1\n"
"s_cbranch_scc1 .L_wv1\n"
"s_cmp_eq_u32 s26, 2\n"
"s_cbranch_scc1 .L_wv2\n"
"s_cmp_eq_u32 s26, 3\n"
"s_cbranch_scc1 .L_wv3\n"
".p2align 6\n"
".L_wv0:\n"
".L_wl_0:\n"
"s_waitcnt lgkmcnt(4)\n"
"v_mfma_f32_16x16x32_bf16 v[32:35], a[0:3], v[128:131], v[32:35]\n"
"ds_read_b128 v[176:179], v4 offset:12288\n"
"v_mfma_f32_16x16x32_bf16 v[32:35], a[8:11], v[132:135], v[32:35]\n"
"buffer_load_dwordx4 v6, s[32:35], s63 offen offset:1024 sc1 lds\n"
"v_mfma_f32_16x16x32_bf16 v[32:35], a[16:19], v[136:139], v[32:35]\n"
"ds_read_b128 v[180:183], v4 offset:13312\n"
"s_add_u32 s68, s68, 593920\n"
"v_mfma_f32_16x16x32_bf16 v[32:35], a[24:27], v[140:143], v[32:35]\n"
"v_cvt_pk_bf16_f32 v56, v40, v41\n"
"v_cvt_pk_bf16_f32 v57, v42, v43\n"
"v_mfma_f32_16x16x32_bf16 v[36:39], a[28:31], v[140:143], v[36:39]\n"
"ds_read_b128 v[184:187], v4 offset:14336\n"
"v_cvt_pk_bf16_f32 v58, v44, v45\n"
"v_mfma_f32_16x16x32_bf16 v[36:39], a[20:23], v[136:139], v[36:39]\n"
"v_cvt_pk_bf16_f32 v59, v46, v47\n"
"v_add_f32 v60, v40, v41\n"
"v_mfma_f32_16x16x32_bf16 v[36:39], a[12:15], v[132:135], v[36:39]\n"
"ds_read_b128 v[188:191], v4 offset:15360\n"
"v_mul_f32 v61, v40, v40\n"
"v_mfma_f32_16x16x32_bf16 v[36:39], a[4:7], v[128:131], v[36:39]\n"
"v_fmac_f32 v61, v41, v41\n"
"v_add_f32 v60, v60, v42\n"
"v_mfma_f32_16x16x32_bf16 v[36:39], a[36:39], v[144:147], v[36:39]\n"
"ds_read_b128 v[128:131], v4 offset:16384\n"
"v_fmac_f32 v61, v42, v42\n"
"v_mfma_f32_16x16x32_bf16 v[36:39], a[44:47], v[148:151], v[36:39]\n"
"buffer_store_dwordx4 v[56:59], v2, s[32:35], s68 offen sc1\n"
"v_mfma_f32_16x16x32_bf16 v[36:39], a[52:55], v[152:155], v[36:39]\n"
"ds_read_b128 v[132:135], v4 offset:17408\n"
"v_add_f32 v60, v60, v43\n"
"v_mfma_f32_16x16x32_bf16 v[36:39], a[60:63], v[156:159], v[36:39]\n"
"v_fmac_f32 v61, v43, v43\n"
"v_add_f32 v60, v60, v44\n"
"v_mfma_f32_16x16x32_bf16 v[32:35], a[56:59], v[156:159], v[32:35]\n"
"ds_read_b128 v[136:139], v4 offset:18432\n"
"v_fmac_f32 v61, v44, v44\n"
"v_mfma_f32_16x16x32_bf16 v[32:35], a[48:51], v[152:155], v[32:35]\n"
"v_add_f32 v60, v60, v45\n"
"v_fmac_f32 v61, v45, v45\n"
"v_mfma_f32_16x16x32_bf16 v[32:35], a[40:43], v[148:151], v[32:35]\n"
"ds_read_b128 v[140:143], v4 offset:19456\n"
"v_add_f32 v60, v60, v46\n"
"v_mfma_f32_16x16x32_bf16 v[32:35], a[32:35], v[144:147], v[32:35]\n"
"v_fmac_f32 v61, v46, v46\n"
"s_waitcnt lgkmcnt(4)\n"
"v_mfma_f32_16x16x32_bf16 v[32:35], a[64:67], v[160:163], v[32:35]\n"
"ds_read_b128 v[144:147], v4 offset:20480\n"
"v_add_f32 v60, v60, v47\n"
"v_mfma_f32_16x16x32_bf16 v[32:35], a[72:75], v[164:167], v[32:35]\n"
"v_fmac_f32 v61, v47, v47\n"
"v_mfma_f32_16x16x32_bf16 v[32:35], a[80:83], v[168:171], v[32:35]\n"
"ds_read_b128 v[148:151], v4 offset:21504\n"
"v_mfma_f32_16x16x32_bf16 v[32:35], a[88:91], v[172:175], v[32:35]\n"
"v_permlane16_swap_b32 v60, v61\n"
"v_mfma_f32_16x16x32_bf16 v[36:39], a[92:95], v[172:175], v[36:39]\n"
"ds_read_b128 v[152:155], v4 offset:22528\n"
"v_add_f32 v60, v60, v61\n"
"v_mfma_f32_16x16x32_bf16 v[36:39], a[84:87], v[168:171], v[36:39]\n"
"buffer_load_dwordx4 v[48:51], v2, s[32:35], s71 offen sc1\n"
"v_mfma_f32_16x16x32_bf16 v[36:39], a[76:79], v[164:167], v[36:39]\n"
"ds_read_b128 v[156:159], v4 offset:23552\n"
"s_add_u32 s71, s71, 593920\n"
"v_mfma_f32_16x16x32_bf16 v[36:39], a[68:71], v[160:163], v[36:39]\n"
"v_permlane32_swap_b32 v60, v61\n"
"v_mfma_f32_16x16x32_bf16 v[36:39], a[100:103], v[176:179], v[36:39]\n"
"ds_read_b128 v[160:163], v4 offset:24576\n"
"v_add_f32 v60, v60, v61\n"
"v_mfma_f32_16x16x32_bf16 v[36:39], a[108:111], v[180:183], v[36:39]\n"
"buffer_store_dword v60, v15, s[32:35], s68 offen offset:1024 sc1\n"
"v_mfma_f32_16x16x32_bf16 v[36:39], a[116:119], v[184:187], v[36:39]\n"
"ds_read_b128 v[164:167], v4 offset:25600\n"
"v_mfma_f32_16x16x32_bf16 v[36:39], a[124:127], v[188:191], v[36:39]\n"
"v_mfma_f32_16x16x32_bf16 v[32:35], a[120:123], v[188:191], v[32:35]\n"
"ds_read_b128 v[168:171], v4 offset:26624\n"
"v_mfma_f32_16x16x32_bf16 v[32:35], a[112:115], v[184:187], v[32:35]\n"
"buffer_load_dwordx4 v6, s[32:35], s63 offen offset:2048 sc1 lds\n"
"v_mfma_f32_16x16x32_bf16 v[32:35], a[104:107], v[180:183], v[32:35]\n"
"ds_read_b128 v[172:175], v4 offset:27648\n"
"v_mfma_f32_16x16x32_bf16 v[32:35], a[96:99], v[176:179], v[32:35]\n"
"s_waitcnt lgkmcnt(4)\n"
"v_mfma_f32_16x16x32_bf16 v[32:35], a[128:131], v[128:131], v[32:35]\n"
"ds_read_b128 v[176:179], v4 offset:28672\n"
"v_mfma_f32_16x16x32_bf16 v[32:35], a[136:139], v[132:135], v[32:35]\n"
"buffer_load_dwordx4 v6, s[32:35], s63 offen offset:3072 sc1 lds\n"
"v_mfma_f32_16x16x32_bf16 v[32:35], a[144:147], v[136:139], v[32:35]\n"
"ds_read_b128 v[180:183], v4 offset:29696\n"
"s_add_u32 s63, s63, 593920\n"
"v_mfma_f32_16x16x32_bf16 v[32:35], a[152:155], v[140:143], v[32:35]\n"
"v_mfma_f32_16x16x32_bf16 v[36:39], a[156:159], v[140:143], v[36:39]\n"
"ds_read_b128 v[184:187], v4 offset:30720\n"
"v_mfma_f32_16x16x32_bf16 v[36:39], a[148:151], v[136:139], v[36:39]\n"
"v_mfma_f32_16x16x32_bf16 v[36:39], a[140:143], v[132:135], v[36:39]\n"
"ds_read_b128 v[188:191], v4 offset:31744\n"
"v_mfma_f32_16x16x32_bf16 v[36:39], a[132:135], v[128:131], v[36:39]\n"
"s_waitcnt vmcnt(12)\n"
"s_barrier\n"
"v_mfma_f32_16x16x32_bf16 v[36:39], a[164:167], v[144:147], v[36:39]\n"
"ds_read_b128 v[128:131], v4 offset:32768\n"
"v_lshlrev_b32 v40, 16, v52\n"
"v_mfma_f32_16x16x32_bf16 v[36:39], a[172:175], v[148:151], v[36:39]\n"
"buffer_store_dwordx2 v[16:17], v2, s[92:95], s68 idxen sc1\n"
"v_mfma_f32_16x16x32_bf16 v[36:39], a[180:183], v[152:155], v[36:39]\n"
"ds_read_b128 v[132:135], v4 offset:33792\n"
"v_and_b32 v41, 0xffff0000, v52\n"
"v_mfma_f32_16x16x32_bf16 v[36:39], a[188:191], v[156:159], v[36:39]\n"
"v_lshlrev_b32 v42, 16, v53\n"
"v_and_b32 v43, 0xffff0000, v53\n"
"v_mfma_f32_16x16x32_bf16 v[32:35], a[184:187], v[156:159], v[32:35]\n"
"ds_read_b128 v[136:139], v4 offset:34816\n"
"s_add_u32 m0, s64, 32768\n"
"v_mfma_f32_16x16x32_bf16 v[32:35], a[176:179], v[152:155], v[32:35]\n"
"buffer_load_dwordx4 v2, s[32:35], s63 offen offset:0 sc1 lds\n"
"v_mfma_f32_16x16x32_bf16 v[32:35], a[168:171], v[148:151], v[32:35]\n"
"ds_read_b128 v[140:143], v4 offset:35840\n"
"v_lshlrev_b32 v44, 16, v54\n"
"v_mfma_f32_16x16x32_bf16 v[32:35], a[160:163], v[144:147], v[32:35]\n"
"v_and_b32 v45, 0xffff0000, v54\n"
"s_waitcnt lgkmcnt(4)\n"
"v_mfma_f32_16x16x32_bf16 v[32:35], a[192:195], v[160:163], v[32:35]\n"
"ds_read_b128 v[144:147], v4 offset:36864\n"
"v_lshlrev_b32 v46, 16, v55\n"
"v_mfma_f32_16x16x32_bf16 v[32:35], a[200:203], v[164:167], v[32:35]\n"
"buffer_load_dwordx4 v2, s[32:35], s63 offen offset:1024 sc1 lds\n"
"v_mfma_f32_16x16x32_bf16 v[32:35], a[208:211], v[168:171], v[32:35]\n"
"ds_read_b128 v[148:151], v4 offset:37888\n"
"v_and_b32 v47, 0xffff0000, v55\n"
"v_mfma_f32_16x16x32_bf16 v[32:35], a[216:219], v[172:175], v[32:35]\n"
"v_mfma_f32_16x16x32_bf16 v[36:39], a[220:223], v[172:175], v[36:39]\n"
"ds_read_b128 v[152:155], v4 offset:38912\n"
"v_mfma_f32_16x16x32_bf16 v[36:39], a[212:215], v[168:171], v[36:39]\n"
"buffer_load_dwordx4 v2, s[32:35], s63 offen offset:2048 sc1 lds\n"
"v_mfma_f32_16x16x32_bf16 v[36:39], a[204:207], v[164:167], v[36:39]\n"
"ds_read_b128 v[156:159], v4 offset:39936\n"
"v_mfma_f32_16x16x32_bf16 v[36:39], a[196:199], v[160:163], v[36:39]\n"
"v_mfma_f32_16x16x32_bf16 v[36:39], a[228:231], v[176:179], v[36:39]\n"
"ds_read_b128 v[160:163], v4 offset:40960\n"
"v_mfma_f32_16x16x32_bf16 v[36:39], a[236:239], v[180:183], v[36:39]\n"
"buffer_load_dwordx4 v2, s[32:35], s63 offen offset:3072 sc1 lds\n"
"v_mfma_f32_16x16x32_bf16 v[36:39], a[244:247], v[184:187], v[36:39]\n"
"ds_read_b128 v[164:167], v4 offset:41984\n"
"v_mfma_f32_16x16x32_bf16 v[36:39], a[252:255], v[188:191], v[36:39]\n"
"v_mfma_f32_16x16x32_bf16 v[32:35], a[248:251], v[188:191], v[32:35]\n"
"ds_read_b128 v[168:171], v4 offset:43008\n"
"s_add_u32 m0, s64, 36864\n"
"v_mfma_f32_16x16x32_bf16 v[32:35], a[240:243], v[184:187], v[32:35]\n"
"buffer_load_dwordx4 v6, s[32:35], s63 offen offset:0 sc1 lds\n"
"v_mfma_f32_16x16x32_bf16 v[32:35], a[232:235], v[180:183], v[32:35]\n"
"ds_read_b128 v[172:175], v4 offset:44032\n"
"v_mfma_f32_16x16x32_bf16 v[32:35], a[224:227], v[176:179], v[32:35]\n"
"s_waitcnt lgkmcnt(4)\n"
"v_mfma_f32_16x16x32_bf16 v[40:43], a[0:3], v[128:131], v[40:43]\n"
"ds_read_b128 v[176:179], v4 offset:45056\n"
"v_mfma_f32_16x16x32_bf16 v[40:43], a[8:11], v[132:135], v[40:43]\n"
"buffer_load_dwordx4 v6, s[32:35], s63 offen offset:1024 sc1 lds\n"
"v_mfma_f32_16x16x32_bf16 v[40:43], a[16:19], v[136:139], v[40:43]\n"
"ds_read_b128 v[180:183], v4 offset:46080\n"
"s_add_u32 s68, s68, 593920\n"
"v_mfma_f32_16x16x32_bf16 v[40:43], a[24:27], v[140:143], v[40:43]\n"
"v_cvt_pk_bf16_f32 v56, v32, v33\n"
"v_cvt_pk_bf16_f32 v57, v34, v35\n"
"v_mfma_f32_16x16x32_bf16 v[44:47], a[28:31], v[140:143], v[44:47]\n"
"ds_read_b128 v[184:187], v4 offset:47104\n"
"v_cvt_pk_bf16_f32 v58, v36, v37\n"
"v_mfma_f32_16x16x32_bf16 v[44:47], a[20:23], v[136:139], v[44:47]\n"
"v_cvt_pk_bf16_f32 v59, v38, v39\n"
"v_add_f32 v60, v32, v33\n"
"v_mfma_f32_16x16x32_bf16 v[44:47], a[12:15], v[132:135], v[44:47]\n"
"ds_read_b128 v[188:191], v4 offset:48128\n"
"v_mul_f32 v61, v32, v32\n"
"v_mfma_f32_16x16x32_bf16 v[44:47], a[4:7], v[128:131], v[44:47]\n"
"v_fmac_f32 v61, v33, v33\n"
"v_add_f32 v60, v60, v34\n"
"v_mfma_f32_16x16x32_bf16 v[44:47], a[36:39], v[144:147], v[44:47]\n"
"ds_read_b128 v[128:131], v4 offset:49152\n"
"v_fmac_f32 v61, v34, v34\n"
"v_mfma_f32_16x16x32_bf16 v[44:47], a[44:47], v[148:151], v[44:47]\n"
"buffer_store_dwordx4 v[56:59], v2, s[32:35], s68 offen sc1\n"
"v_mfma_f32_16x16x32_bf16 v[44:47], a[52:55], v[152:155], v[44:47]\n"
"ds_read_b128 v[132:135], v4 offset:50176\n"
"v_add_f32 v60, v60, v35\n"
"v_mfma_f32_16x16x32_bf16 v[44:47], a[60:63], v[156:159], v[44:47]\n"
"v_fmac_f32 v61, v35, v35\n"
"v_add_f32 v60, v60, v36\n"
"v_mfma_f32_16x16x32_bf16 v[40:43], a[56:59], v[156:159], v[40:43]\n"
"ds_read_b128 v[136:139], v4 offset:51200\n"
"v_fmac_f32 v61, v36, v36\n"
"v_mfma_f32_16x16x32_bf16 v[40:43], a[48:51], v[152:155], v[40:43]\n"
"v_add_f32 v60, v60, v37\n"
"v_fmac_f32 v61, v37, v37\n"
"v_mfma_f32_16x16x32_bf16 v[40:43], a[40:43], v[148:151], v[40:43]\n"
"ds_read_b128 v[140:143], v4 offset:52224\n"
"v_add_f32 v60, v60, v38\n"
"v_mfma_f32_16x16x32_bf16 v[40:43], a[32:35], v[144:147], v[40:43]\n"
"v_fmac_f32 v61, v38, v38\n"
"s_waitcnt lgkmcnt(4)\n"
"v_mfma_f32_16x16x32_bf16 v[40:43], a[64:67], v[160:163], v[40:43]\n"
"ds_read_b128 v[144:147], v4 offset:53248\n"
"v_add_f32 v60, v60, v39\n"
"v_mfma_f32_16x16x32_bf16 v[40:43], a[72:75], v[164:167], v[40:43]\n"
"v_fmac_f32 v61, v39, v39\n"
"v_mfma_f32_16x16x32_bf16 v[40:43], a[80:83], v[168:171], v[40:43]\n"
"ds_read_b128 v[148:151], v4 offset:54272\n"
"v_mfma_f32_16x16x32_bf16 v[40:43], a[88:91], v[172:175], v[40:43]\n"
"v_permlane16_swap_b32 v60, v61\n"
"v_mfma_f32_16x16x32_bf16 v[44:47], a[92:95], v[172:175], v[44:47]\n"
"ds_read_b128 v[152:155], v4 offset:55296\n"
"v_add_f32 v60, v60, v61\n"
"v_mfma_f32_16x16x32_bf16 v[44:47], a[84:87], v[168:171], v[44:47]\n"
"buffer_load_dwordx4 v[52:55], v2, s[32:35], s71 offen sc1\n"
"v_mfma_f32_16x16x32_bf16 v[44:47], a[76:79], v[164:167], v[44:47]\n"
"ds_read_b128 v[156:159], v4 offset:56320\n"
"s_add_u32 s71, s71, 593920\n"
"v_mfma_f32_16x16x32_bf16 v[44:47], a[68:71], v[160:163], v[44:47]\n"
"v_permlane32_swap_b32 v60, v61\n"
"v_mfma_f32_16x16x32_bf16 v[44:47], a[100:103], v[176:179], v[44:47]\n"
"ds_read_b128 v[160:163], v4 offset:57344\n"
"v_add_f32 v60, v60, v61\n"
"v_mfma_f32_16x16x32_bf16 v[44:47], a[108:111], v[180:183], v[44:47]\n"
"buffer_store_dword v60, v15, s[32:35], s68 offen offset:1024 sc1\n"
"v_mfma_f32_16x16x32_bf16 v[44:47], a[116:119], v[184:187], v[44:47]\n"
"ds_read_b128 v[164:167], v4 offset:58368\n"
"v_mfma_f32_16x16x32_bf16 v[44:47], a[124:127], v[188:191], v[44:47]\n"
"v_mfma_f32_16x16x32_bf16 v[40:43], a[120:123], v[188:191], v[40:43]\n"
"ds_read_b128 v[168:171], v4 offset:59392\n"
"v_mfma_f32_16x16x32_bf16 v[40:43], a[112:115], v[184:187], v[40:43]\n"
"buffer_load_dwordx4 v6, s[32:35], s63 offen offset:2048 sc1 lds\n"
"v_mfma_f32_16x16x32_bf16 v[40:43], a[104:107], v[180:183], v[40:43]\n"
"ds_read_b128 v[172:175], v4 offset:60416\n"
"v_mfma_f32_16x16x32_bf16 v[40:43], a[96:99], v[176:179], v[40:43]\n"
"s_waitcnt lgkmcnt(4)\n"
"v_mfma_f32_16x16x32_bf16 v[40:43], a[128:131], v[128:131], v[40:43]\n"
"ds_read_b128 v[176:179], v4 offset:61440\n"
"v_mfma_f32_16x16x32_bf16 v[40:43], a[136:139], v[132:135], v[40:43]\n"
"buffer_load_dwordx4 v6, s[32:35], s63 offen offset:3072 sc1 lds\n"
"v_mfma_f32_16x16x32_bf16 v[40:43], a[144:147], v[136:139], v[40:43]\n"
"ds_read_b128 v[180:183], v4 offset:62464\n"
"s_add_u32 s63, s63, 593920\n"
"v_mfma_f32_16x16x32_bf16 v[40:43], a[152:155], v[140:143], v[40:43]\n"
"s_xor_b32 s64, s64, s65\n"
"v_mfma_f32_16x16x32_bf16 v[44:47], a[156:159], v[140:143], v[44:47]\n"
"ds_read_b128 v[184:187], v4 offset:63488\n"
"v_mfma_f32_16x16x32_bf16 v[44:47], a[148:151], v[136:139], v[44:47]\n"
"v_mfma_f32_16x16x32_bf16 v[44:47], a[140:143], v[132:135], v[44:47]\n"
"ds_read_b128 v[188:191], v4 offset:64512\n"
"v_xor_b32 v4, 65536, v4\n"
"v_mfma_f32_16x16x32_bf16 v[44:47], a[132:135], v[128:131], v[44:47]\n"
"s_waitcnt vmcnt(12)\n"
"s_barrier\n"
"v_mfma_f32_16x16x32_bf16 v[44:47], a[164:167], v[144:147], v[44:47]\n"
"ds_read_b128 v[128:131], v4 offset:0\n"
"v_lshlrev_b32 v32, 16, v48\n"
"v_mfma_f32_16x16x32_bf16 v[44:47], a[172:175], v[148:151], v[44:47]\n"
"buffer_store_dwordx2 v[16:17], v2, s[92:95], s68 idxen sc1\n"
"v_mfma_f32_16x16x32_bf16 v[44:47], a[180:183], v[152:155], v[44:47]\n"
"ds_read_b128 v[132:135], v4 offset:1024\n"
"v_and_b32 v33, 0xffff0000, v48\n"
"v_mfma_f32_16x16x32_bf16 v[44:47], a[188:191], v[156:159], v[44:47]\n"
"v_lshlrev_b32 v34, 16, v49\n"
"v_and_b32 v35, 0xffff0000, v49\n"
"v_mfma_f32_16x16x32_bf16 v[40:43], a[184:187], v[156:159], v[40:43]\n"
"ds_read_b128 v[136:139], v4 offset:2048\n"
"s_add_u32 m0, s64, 0\n"
"v_mfma_f32_16x16x32_bf16 v[40:43], a[176:179], v[152:155], v[40:43]\n"
"buffer_load_dwordx4 v2, s[32:35], s63 offen offset:0 sc1 lds\n"
"v_mfma_f32_16x16x32_bf16 v[40:43], a[168:171], v[148:151], v[40:43]\n"
"ds_read_b128 v[140:143], v4 offset:3072\n"
"v_lshlrev_b32 v36, 16, v50\n"
"v_mfma_f32_16x16x32_bf16 v[40:43], a[160:163], v[144:147], v[40:43]\n"
"v_and_b32 v37, 0xffff0000, v50\n"
"s_waitcnt lgkmcnt(4)\n"
"v_mfma_f32_16x16x32_bf16 v[40:43], a[192:195], v[160:163], v[40:43]\n"
"ds_read_b128 v[144:147], v4 offset:4096\n"
"v_lshlrev_b32 v38, 16, v51\n"
"v_mfma_f32_16x16x32_bf16 v[40:43], a[200:203], v[164:167], v[40:43]\n"
"buffer_load_dwordx4 v2, s[32:35], s63 offen offset:1024 sc1 lds\n"
"v_mfma_f32_16x16x32_bf16 v[40:43], a[208:211], v[168:171], v[40:43]\n"
"ds_read_b128 v[148:151], v4 offset:5120\n"
"v_and_b32 v39, 0xffff0000, v51\n"
"v_mfma_f32_16x16x32_bf16 v[40:43], a[216:219], v[172:175], v[40:43]\n"
"v_mfma_f32_16x16x32_bf16 v[44:47], a[220:223], v[172:175], v[44:47]\n"
"ds_read_b128 v[152:155], v4 offset:6144\n"
"v_mfma_f32_16x16x32_bf16 v[44:47], a[212:215], v[168:171], v[44:47]\n"
"buffer_load_dwordx4 v2, s[32:35], s63 offen offset:2048 sc1 lds\n"
"v_mfma_f32_16x16x32_bf16 v[44:47], a[204:207], v[164:167], v[44:47]\n"
"ds_read_b128 v[156:159], v4 offset:7168\n"
"v_mfma_f32_16x16x32_bf16 v[44:47], a[196:199], v[160:163], v[44:47]\n"
"v_mfma_f32_16x16x32_bf16 v[44:47], a[228:231], v[176:179], v[44:47]\n"
"ds_read_b128 v[160:163], v4 offset:8192\n"
"v_mfma_f32_16x16x32_bf16 v[44:47], a[236:239], v[180:183], v[44:47]\n"
"buffer_load_dwordx4 v2, s[32:35], s63 offen offset:3072 sc1 lds\n"
"v_mfma_f32_16x16x32_bf16 v[44:47], a[244:247], v[184:187], v[44:47]\n"
"ds_read_b128 v[164:167], v4 offset:9216\n"
"v_mfma_f32_16x16x32_bf16 v[44:47], a[252:255], v[188:191], v[44:47]\n"
"v_mfma_f32_16x16x32_bf16 v[40:43], a[248:251], v[188:191], v[40:43]\n"
"ds_read_b128 v[168:171], v4 offset:10240\n"
"s_add_u32 m0, s64, 4096\n"
"v_mfma_f32_16x16x32_bf16 v[40:43], a[240:243], v[184:187], v[40:43]\n"
"buffer_load_dwordx4 v6, s[32:35], s63 offen offset:0 sc1 lds\n"
"v_mfma_f32_16x16x32_bf16 v[40:43], a[232:235], v[180:183], v[40:43]\n"
"ds_read_b128 v[172:175], v4 offset:11264\n"
"s_sub_u32 s62, s62, 1\n"
"v_mfma_f32_16x16x32_bf16 v[40:43], a[224:227], v[176:179], v[40:43]\n"
"s_cbranch_scc0 .L_wl_0\n"
"s_nop 1\n"
"s_add_u32 s68, s68, 593920\n"
"s_nop 3\n"
"v_cvt_pk_bf16_f32 v56, v40, v41\n"
"v_cvt_pk_bf16_f32 v57, v42, v43\n"
"v_cvt_pk_bf16_f32 v58, v44, v45\n"
"v_cvt_pk_bf16_f32 v59, v46, v47\n"
"buffer_store_dwordx4 v[56:59], v2, s[32:35], s68 offen sc1\n"
"v_add_f32 v60, v40, v41\n"
"v_mul_f32 v61, v40, v40\n"
"v_fmac_f32 v61, v41, v41\n"
"v_add_f32 v60, v60, v42\n"
"v_fmac_f32 v61, v42, v42\n"
"v_add_f32 v60, v60, v43\n"
"v_fmac_f32 v61, v43, v43\n"
"v_add_f32 v60, v60, v44\n"
"v_fmac_f32 v61, v44, v44\n"
"v_add_f32 v60, v60, v45\n"
"v_fmac_f32 v61, v45, v45\n"
"v_add_f32 v60, v60, v46\n"
"v_fmac_f32 v61, v46, v46\n"
"v_add_f32 v60, v60, v47\n"
"v_fmac_f32 v61, v47, v47\n"
"s_nop 1\n"
"v_permlane16_swap_b32 v60, v61\n"
"v_add_f32 v60, v60, v61\n"
"s_nop 1\n"
"v_permlane32_swap_b32 v60, v61\n"
"v_add_f32 v60, v60, v61\n"
"buffer_store_dword v60, v15, s[32:35], s68 offen offset:1024 sc1\n"
"s_waitcnt vmcnt(10)\n"
"s_barrier\n"
"buffer_store_dwordx2 v[16:17], v2, s[92:95], s68 idxen sc1\n"
"s_add_u32 s68, s68, 593920\n"
"s_waitcnt vmcnt(1)\n"
"s_barrier\n"
"buffer_store_dwordx2 v[16:17], v2, s[92:95], s68 idxen sc1\n"
"s_branch .L_end\n"
".p2align 6\n"
".L_wv1:\n"
".L_wl_1:\n"
"s_waitcnt lgkmcnt(4)\n"
"v_mfma_f32_16x16x32_bf16 v[32:35], a[0:3], v[128:131], v[32:35]\n"
"ds_read_b128 v[176:179], v4 offset:12288\n"
"v_mfma_f32_16x16x32_bf16 v[32:35], a[8:11], v[132:135], v[32:35]\n"
"buffer_load_dwordx4 v6, s[32:35], s63 offen offset:1024 sc1 lds\n"
"v_mfma_f32_16x16x32_bf16 v[32:35], a[16:19], v[136:139], v[32:35]\n"
"ds_read_b128 v[180:183], v4 offset:13312\n"
"s_add_u32 s68, s68, 593920\n"
"v_mfma_f32_16x16x32_bf16 v[32:35], a[24:27], v[140:143], v[32:35]\n"
"v_cvt_pk_bf16_f32 v56, v40, v41\n"
"v_cvt_pk_bf16_f32 v57, v42, v43\n"
"v_mfma_f32_16x16x32_bf16 v[36:39], a[28:31], v[140:143], v[36:39]\n"
"ds_read_b128 v[184:187], v4 offset:14336\n"
"v_cvt_pk_bf16_f32 v58, v44, v45\n"
"v_mfma_f32_16x16x32_bf16 v[36:39], a[20:23], v[136:139], v[36:39]\n"
"v_cvt_pk_bf16_f32 v59, v46, v47\n"
"v_add_f32 v60, v40, v41\n"
"v_mfma_f32_16x16x32_bf16 v[36:39], a[12:15], v[132:135], v[36:39]\n"
"ds_read_b128 v[188:191], v4 offset:15360\n"
"v_mul_f32 v61, v40, v40\n"
"v_mfma_f32_16x16x32_bf16 v[36:39], a[4:7], v[128:131], v[36:39]\n"
"v_fmac_f32 v61, v41, v41\n"
"v_add_f32 v60, v60, v42\n"
"v_mfma_f32_16x16x32_bf16 v[36:39], a[36:39], v[144:147], v[36:39]\n"
"ds_read_b128 v[128:131], v4 offset:16384\n"
"v_fmac_f32 v61, v42, v42\n"
"v_mfma_f32_16x16x32_bf16 v[36:39], a[44:47], v[148:151], v[36:39]\n"
"buffer_store_dwordx4 v[56:59], v2, s[32:35], s68 offen sc1\n"
"v_mfma_f32_16x16x32_bf16 v[36:39], a[52:55], v[152:155], v[36:39]\n"
"ds_read_b128 v[132:135], v4 offset:17408\n"
"v_add_f32 v60, v60, v43\n"
"v_mfma_f32_16x16x32_bf16 v[36:39], a[60:63], v[156:159], v[36:39]\n"
"v_fmac_f32 v61, v43, v43\n"
"v_add_f32 v60, v60, v44\n"
"v_mfma_f32_16x16x32_bf16 v[32:35], a[56:59], v[156:159], v[32:35]\n"
"ds_read_b128 v[136:139], v4 offset:18432\n"
"v_fmac_f32 v61, v44, v44\n"
"v_mfma_f32_16x16x32_bf16 v[32:35], a[48:51], v[152:155], v[32:35]\n"
"v_add_f32 v60, v60, v45\n"
"v_fmac_f32 v61, v45, v45\n"
"v_mfma_f32_16x16x32_bf16 v[32:35], a[40:43], v[148:151], v[32:35]\n"
"ds_read_b128 v[140:143], v4 offset:19456\n"
"v_add_f32 v60, v60, v46\n"
"v_mfma_f32_16x16x32_bf16 v[32:35], a[32:35], v[144:147], v[32:35]\n"
"v_fmac_f32 v61, v46, v46\n"
"s_waitcnt lgkmcnt(4)\n"
"v_mfma_f32_16x16x32_bf16 v[32:35], a[64:67], v[160:163], v[32:35]\n"
"ds_read_b128 v[144:147], v4 offset:20480\n"
"v_add_f32 v60, v60, v47\n"
"v_mfma_f32_16x16x32_bf16 v[32:35], a[72:75], v[164:167], v[32:35]\n"
"v_fmac_f32 v61, v47, v47\n"
"v_mfma_f32_16x16x32_bf16 v[32:35], a[80:83], v[168:171], v[32:35]\n"
"ds_read_b128 v[148:151], v4 offset:21504\n"
"v_mfma_f32_16x16x32_bf16 v[32:35], a[88:91], v[172:175], v[32:35]\n"
"v_permlane16_swap_b32 v60, v61\n"
"v_mfma_f32_16x16x32_bf16 v[36:39], a[92:95], v[172:175], v[36:39]\n"
"ds_read_b128 v[152:155], v4 offset:22528\n"
"v_add_f32 v60, v60, v61\n"
"v_mfma_f32_16x16x32_bf16 v[36:39], a[84:87], v[168:171], v[36:39]\n"
"buffer_load_dwordx4 v[48:51], v2, s[32:35], s71 offen sc1\n"
"v_mfma_f32_16x16x32_bf16 v[36:39], a[76:79], v[164:167], v[36:39]\n"
"ds_read_b128 v[156:159], v4 offset:23552\n"
"s_add_u32 s71, s71, 593920\n"
"v_mfma_f32_16x16x32_bf16 v[36:39], a[68:71], v[160:163], v[36:39]\n"
"v_permlane32_swap_b32 v60, v61\n"
"v_mfma_f32_16x16x32_bf16 v[36:39], a[100:103], v[176:179], v[36:39]\n"
"ds_read_b128 v[160:163], v4 offset:24576\n"
"v_add_f32 v60, v60, v61\n"
"v_mfma_f32_16x16x32_bf16 v[36:39], a[108:111], v[180:183], v[36:39]\n"
"buffer_store_dword v60, v15, s[32:35], s68 offen offset:1024 sc1\n"
"v_mfma_f32_16x16x32_bf16 v[36:39], a[116:119], v[184:187], v[36:39]\n"
"ds_read_b128 v[164:167], v4 offset:25600\n"
"v_mfma_f32_16x16x32_bf16 v[36:39], a[124:127], v[188:191], v[36:39]\n"
"v_mfma_f32_16x16x32_bf16 v[32:35], a[120:123], v[188:191], v[32:35]\n"
"ds_read_b128 v[168:171], v4 offset:26624\n"
"v_mfma_f32_16x16x32_bf16 v[32:35], a[112:115], v[184:187], v[32:35]\n"
"buffer_load_dwordx4 v6, s[32:35], s63 offen offset:2048 sc1 lds\n"
"v_mfma_f32_16x16x32_bf16 v[32:35], a[104:107], v[180:183], v[32:35]\n"
"ds_read_b128 v[172:175], v4 offset:27648\n"
"v_mfma_f32_16x16x32_bf16 v[32:35], a[96:99], v[176:179], v[32:35]\n"
"s_waitcnt lgkmcnt(4)\n"
"v_mfma_f32_16x16x32_bf16 v[32:35], a[128:131], v[128:131], v[32:35]\n"
"ds_read_b128 v[176:179], v4 offset:28672\n"
"v_mfma_f32_16x16x32_bf16 v[32:35], a[136:139], v[132:135], v[32:35]\n"
"buffer_load_dwordx4 v6, s[32:35], s63 offen offset:3072 sc1 lds\n"
"v_mfma_f32_16x16x32_bf16 v[32:35], a[144:147], v[136:139], v[32:35]\n"
"ds_read_b128 v[180:183], v4 offset:29696\n"
"s_add_u32 s63, s63, 593920\n"
"v_mfma_f32_16x16x32_bf16 v[32:35], a[152:155], v[140:143], v[32:35]\n"
"v_mfma_f32_16x16x32_bf16 v[36:39], a[156:159], v[140:143], v[36:39]\n"
"ds_read_b128 v[184:187], v4 offset:30720\n"
"v_mfma_f32_16x16x32_bf16 v[36:39], a[148:151], v[136:139], v[36:39]\n"
"buffer_load_dwordx2 v[12:13], v14, s[32:35], s66 offen sc1\n"
"v_mfma_f32_16x16x32_bf16 v[36:39], a[140:143], v[132:135], v[36:39]\n"
"s_waitcnt vmcnt(12)\n"
"s_barrier\n"
"v_mfma_f32_16x16x32_bf16 v[36:39], a[132:135], v[128:131], v[36:39]\n"
"ds_read_b128 v[188:191], v4 offset:31744\n"
"v_mfma_f32_16x16x32_bf16 v[36:39], a[164:167], v[144:147], v[36:39]\n"
"ds_read_b128 v[128:131], v4 offset:32768\n"
"v_lshlrev_b32 v40, 16, v52\n"
"v_mfma_f32_16x16x32_bf16 v[36:39], a[172:175], v[148:151], v[36:39]\n"
"v_and_b32 v41, 0xffff0000, v52\n"
"v_lshlrev_b32 v42, 16, v53\n"
"v_mfma_f32_16x16x32_bf16 v[36:39], a[180:183], v[152:155], v[36:39]\n"
"ds_read_b128 v[132:135], v4 offset:33792\n"
"v_and_b32 v43, 0xffff0000, v53\n"
"v_mfma_f32_16x16x32_bf16 v[36:39], a[188:191], v[156:159], v[36:39]\n"
"v_lshlrev_b32 v44, 16, v54\n"
"v_and_b32 v45, 0xffff0000, v54\n"
"v_mfma_f32_16x16x32_bf16 v[32:35], a[184:187], v[156:159], v[32:35]\n"
"ds_read_b128 v[136:139], v4 offset:34816\n"
"s_add_u32 m0, s64, 32768\n"
"v_mfma_f32_16x16x32_bf16 v[32:35], a[176:179], v[152:155], v[32:35]\n"
"buffer_load_dwordx4 v2, s[32:35], s63 offen offset:0 sc1 lds\n"
"v_mfma_f32_16x16x32_bf16 v[32:35], a[168:171], v[148:151], v[32:35]\n"
"ds_read_b128 v[140:143], v4 offset:35840\n"
"v_lshlrev_b32 v46, 16, v55\n"
"v_mfma_f32_16x16x32_bf16 v[32:35], a[160:163], v[144:147], v[32:35]\n"
"v_and_b32 v47, 0xffff0000, v55\n"
"s_waitcnt lgkmcnt(4)\n"
"v_mfma_f32_16x16x32_bf16 v[32:35], a[192:195], v[160:163], v[32:35]\n"
"ds_read_b128 v[144:147], v4 offset:36864\n"
"v_mfma_f32_16x16x32_bf16 v[32:35], a[200:203], v[164:167], v[32:35]\n"
"buffer_load_dwordx4 v2, s[32:35], s63 offen offset:1024 sc1 lds\n"
"v_mfma_f32_16x16x32_bf16 v[32:35], a[208:211], v[168:171], v[32:35]\n"
"ds_read_b128 v[148:151], v4 offset:37888\n"
"v_mfma_f32_16x16x32_bf16 v[32:35], a[216:219], v[172:175], v[32:35]\n"
"v_mfma_f32_16x16x32_bf16 v[36:39], a[220:223], v[172:175], v[36:39]\n"
"ds_read_b128 v[152:155], v4 offset:38912\n"
"v_mfma_f32_16x16x32_bf16 v[36:39], a[212:215], v[168:171], v[36:39]\n"
"buffer_load_dwordx4 v2, s[32:35], s63 offen offset:2048 sc1 lds\n"
"v_mfma_f32_16x16x32_bf16 v[36:39], a[204:207], v[164:167], v[36:39]\n"
"ds_read_b128 v[156:159], v4 offset:39936\n"
"v_mfma_f32_16x16x32_bf16 v[36:39], a[196:199], v[160:163], v[36:39]\n"
"v_mfma_f32_16x16x32_bf16 v[36:39], a[228:231], v[176:179], v[36:39]\n"
"ds_read_b128 v[160:163], v4 offset:40960\n"
"v_mfma_f32_16x16x32_bf16 v[36:39], a[236:239], v[180:183], v[36:39]\n"
"buffer_load_dwordx4 v2, s[32:35], s63 offen offset:3072 sc1 lds\n"
"v_mfma_f32_16x16x32_bf16 v[36:39], a[244:247], v[184:187], v[36:39]\n"
"ds_read_b128 v[164:167], v4 offset:41984\n"
"v_mfma_f32_16x16x32_bf16 v[36:39], a[252:255], v[188:191], v[36:39]\n"
"v_mfma_f32_16x16x32_bf16 v[32:35], a[248:251], v[188:191], v[32:35]\n"
"ds_read_b128 v[168:171], v4 offset:43008\n"
"s_add_u32 m0, s64, 36864\n"
"v_mfma_f32_16x16x32_bf16 v[32:35], a[240:243], v[184:187], v[32:35]\n"
"buffer_load_dwordx4 v6, s[32:35], s63 offen offset:0 sc1 lds\n"
"v_mfma_f32_16x16x32_bf16 v[32:35], a[232:235], v[180:183], v[32:35]\n"
"ds_read_b128 v[172:175], v4 offset:44032\n"
"v_mfma_f32_16x16x32_bf16 v[32:35], a[224:227], v[176:179], v[32:35]\n"
"s_waitcnt lgkmcnt(4)\n"
"v_mfma_f32_16x16x32_bf16 v[40:43], a[0:3], v[128:131], v[40:43]\n"
"ds_read_b128 v[176:179], v4 offset:45056\n"
"v_mfma_f32_16x16x32_bf16 v[40:43], a[8:11], v[132:135], v[40:43]\n"
"buffer_load_dwordx4 v6, s[32:35], s63 offen offset:1024 sc1 lds\n"
"v_mfma_f32_16x16x32_bf16 v[40:43], a[16:19], v[136:139], v[40:43]\n"
"ds_read_b128 v[180:183], v4 offset:46080\n"
"s_add_u32 s68, s68, 593920\n"
"v_mfma_f32_16x16x32_bf16 v[40:43], a[24:27], v[140:143], v[40:43]\n"
"v_cvt_pk_bf16_f32 v56, v32, v33\n"
"v_cvt_pk_bf16_f32 v57, v34, v35\n"
"v_mfma_f32_16x16x32_bf16 v[44:47], a[28:31], v[140:143], v[44:47]\n"
"ds_read_b128 v[184:187], v4 offset:47104\n"
"v_cvt_pk_bf16_f32 v58, v36, v37\n"
"v_mfma_f32_16x16x32_bf16 v[44:47], a[20:23], v[136:139], v[44:47]\n"
"v_cvt_pk_bf16_f32 v59, v38, v39\n"
"v_add_f32 v60, v32, v33\n"
"v_mfma_f32_16x16x32_bf16 v[44:47], a[12:15], v[132:135], v[44:47]\n"
"ds_read_b128 v[188:191], v4 offset:48128\n"
"v_mul_f32 v61, v32, v32\n"
"v_mfma_f32_16x16x32_bf16 v[44:47], a[4:7], v[128:131], v[44:47]\n"
"v_fmac_f32 v61, v33, v33\n"
"v_add_f32 v60, v60, v34\n"
"v_mfma_f32_16x16x32_bf16 v[44:47], a[36:39], v[144:147], v[44:47]\n"
"ds_read_b128 v[128:131], v4 offset:49152\n"
"v_fmac_f32 v61, v34, v34\n"
"v_mfma_f32_16x16x32_bf16 v[44:47], a[44:47], v[148:151], v[44:47]\n"
"buffer_store_dwordx4 v[56:59], v2, s[32:35], s68 offen sc1\n"
"v_mfma_f32_16x16x32_bf16 v[44:47], a[52:55], v[152:155], v[44:47]\n"
"ds_read_b128 v[132:135], v4 offset:50176\n"
"v_add_f32 v60, v60, v35\n"
"v_mfma_f32_16x16x32_bf16 v[44:47], a[60:63], v[156:159], v[44:47]\n"
"v_fmac_f32 v61, v35, v35\n"
"v_add_f32 v60, v60, v36\n"
"v_mfma_f32_16x16x32_bf16 v[40:43], a[56:59], v[156:159], v[40:43]\n"
"ds_read_b128 v[136:139], v4 offset:51200\n"
"v_fmac_f32 v61, v36, v36\n"
"v_mfma_f32_16x16x32_bf16 v[40:43], a[48:51], v[152:155], v[40:43]\n"
"v_add_f32 v60, v60, v37\n"
"v_fmac_f32 v61, v37, v37\n"
"v_mfma_f32_16x16x32_bf16 v[40:43], a[40:43], v[148:151], v[40:43]\n"
"ds_read_b128 v[140:143], v4 offset:52224\n"
"v_add_f32 v60, v60, v38\n"
"v_mfma_f32_16x16x32_bf16 v[40:43], a[32:35], v[144:147], v[40:43]\n"
"v_fmac_f32 v61, v38, v38\n"
"s_waitcnt lgkmcnt(4)\n"
"v_mfma_f32_16x16x32_bf16 v[40:43], a[64:67], v[160:163], v[40:43]\n"
"ds_read_b128 v[144:147], v4 offset:53248\n"
"v_add_f32 v60, v60, v39\n"
"v_mfma_f32_16x16x32_bf16 v[40:43], a[72:75], v[164:167], v[40:43]\n"
"v_fmac_f32 v61, v39, v39\n"
"v_mfma_f32_16x16x32_bf16 v[40:43], a[80:83], v[168:171], v[40:43]\n"
"ds_read_b128 v[148:151], v4 offset:54272\n"
"v_mfma_f32_16x16x32_bf16 v[40:43], a[88:91], v[172:175], v[40:43]\n"
"v_permlane16_swap_b32 v60, v61\n"
"v_mfma_f32_16x16x32_bf16 v[44:47], a[92:95], v[172:175], v[44:47]\n"
"ds_read_b128 v[152:155], v4 offset:55296\n"
"v_add_f32 v60, v60, v61\n"
"v_mfma_f32_16x16x32_bf16 v[44:47], a[84:87], v[168:171], v[44:47]\n"
"buffer_load_dwordx4 v[52:55], v2, s[32:35], s71 offen sc1\n"
"v_mfma_f32_16x16x32_bf16 v[44:47], a[76:79], v[164:167], v[44:47]\n"
"ds_read_b128 v[156:159], v4 offset:56320\n"
"s_add_u32 s71, s71, 593920\n"
"v_mfma_f32_16x16x32_bf16 v[44:47], a[68:71], v[160:163], v[44:47]\n"
"v_permlane32_swap_b32 v60, v61\n"
"v_mfma_f32_16x16x32_bf16 v[44:47], a[100:103], v[176:179], v[44:47]\n"
"ds_read_b128 v[160:163], v4 offset:57344\n"
"v_add_f32 v60, v60, v61\n"
"v_mfma_f32_16x16x32_bf16 v[44:47], a[108:111], v[180:183], v[44:47]\n"
"buffer_store_dword v60, v15, s[32:35], s68 offen offset:1024 sc1\n"
"v_mfma_f32_16x16x32_bf16 v[44:47], a[116:119], v[184:187], v[44:47]\n"
"ds_read_b128 v[164:167], v4 offset:58368\n"
"v_mfma_f32_16x16x32_bf16 v[44:47], a[124:127], v[188:191], v[44:47]\n"
"v_mfma_f32_16x16x32_bf16 v[40:43], a[120:123], v[188:191], v[40:43]\n"
"ds_read_b128 v[168:171], v4 offset:59392\n"
"v_mfma_f32_16x16x32_bf16 v[40:43], a[112:115], v[184:187], v[40:43]\n"
"buffer_load_dwordx4 v6, s[32:35], s63 offen offset:2048 sc1 lds\n"
"v_mfma_f32_16x16x32_bf16 v[40:43], a[104:107], v[180:183], v[40:43]\n"
"ds_read_b128 v[172:175], v4 offset:60416\n"
"v_mfma_f32_16x16x32_bf16 v[40:43], a[96:99], v[176:179], v[40:43]\n"
"s_waitcnt lgkmcnt(4)\n"
"v_mfma_f32_16x16x32_bf16 v[40:43], a[128:131], v[128:131], v[40:43]\n"
"ds_read_b128 v[176:179], v4 offset:61440\n"
"v_mfma_f32_16x16x32_bf16 v[40:43], a[136:139], v[132:135], v[40:43]\n"
"buffer_load_dwordx4 v6, s[32:35], s63 offen offset:3072 sc1 lds\n"
"v_mfma_f32_16x16x32_bf16 v[40:43], a[144:147], v[136:139], v[40:43]\n"
"ds_read_b128 v[180:183], v4 offset:62464\n"
"s_waitcnt vmcnt(11)\n"
"v_mfma_f32_16x16x32_bf16 v[40:43], a[152:155], v[140:143], v[40:43]\n"
"v_cmp_ne_u64 vcc, v[16:17], v[12:13]\n"
"s_add_u32 s63, s63, 593920\n"
"v_mfma_f32_16x16x32_bf16 v[44:47], a[156:159], v[140:143], v[44:47]\n"
"ds_read_b128 v[184:187], v4 offset:63488\n"
"s_cbranch_vccnz .L_rtw1_1\n"
".L_bkw1_1:\n"
"v_mfma_f32_16x16x32_bf16 v[44:47], a[148:151], v[136:139], v[44:47]\n"
"s_add_u32 s66, s66, 1187840\n"
"s_xor_b32 s64, s64, s65\n"
"v_mfma_f32_16x16x32_bf16 v[44:47], a[140:143], v[132:135], v[44:47]\n"
"ds_read_b128 v[188:191], v4 offset:64512\n"
"s_barrier\n"
"v_mfma_f32_16x16x32_bf16 v[44:47], a[132:135], v[128:131], v[44:47]\n"
"v_xor_b32 v4, 65536, v4\n"
"v_mfma_f32_16x16x32_bf16 v[44:47], a[164:167], v[144:147], v[44:47]\n"
"ds_read_b128 v[128:131], v4 offset:0\n"
"v_lshlrev_b32 v32, 16, v48\n"
"v_mfma_f32_16x16x32_bf16 v[44:47], a[172:175], v[148:151], v[44:47]\n"
"v_and_b32 v33, 0xffff0000, v48\n"
"v_lshlrev_b32 v34, 16, v49\n"
"v_mfma_f32_16x16x32_bf16 v[44:47], a[180:183], v[152:155], v[44:47]\n"
"ds_read_b128 v[132:135], v4 offset:1024\n"
"v_and_b32 v35, 0xffff0000, v49\n"
"v_mfma_f32_16x16x32_bf16 v[44:47], a[188:191], v[156:159], v[44:47]\n"
"v_lshlrev_b32 v36, 16, v50\n"
"v_and_b32 v37, 0xffff0000, v50\n"
"v_mfma_f32_16x16x32_bf16 v[40:43], a[184:187], v[156:159], v[40:43]\n"
"ds_read_b128 v[136:139], v4 offset:2048\n"
"s_add_u32 m0, s64, 0\n"
"v_mfma_f32_16x16x32_bf16 v[40:43], a[176:179], v[152:155], v[40:43]\n"
"buffer_load_dwordx4 v2, s[32:35], s63 offen offset:0 sc1 lds\n"
"v_mfma_f32_16x16x32_bf16 v[40:43], a[168:171], v[148:151], v[40:43]\n"
"ds_read_b128 v[140:143], v4 offset:3072\n"
"v_lshlrev_b32 v38, 16, v51\n"
"v_mfma_f32_16x16x32_bf16 v[40:43], a[160:163], v[144:147], v[40:43]\n"
"v_and_b32 v39, 0xffff0000, v51\n"
"s_waitcnt lgkmcnt(4)\n"
"v_mfma_f32_16x16x32_bf16 v[40:43], a[192:195], v[160:163], v[40:43]\n"
"ds_read_b128 v[144:147], v4 offset:4096\n"
"v_mfma_f32_16x16x32_bf16 v[40:43], a[200:203], v[164:167], v[40:43]\n"
"buffer_load_dwordx4 v2, s[32:35], s63 offen offset:1024 sc1 lds\n"
"v_mfma_f32_16x16x32_bf16 v[40:43], a[208:211], v[168:171], v[40:43]\n"
"ds_read_b128 v[148:151], v4 offset:5120\n"
"v_mfma_f32_16x16x32_bf16 v[40:43], a[216:219], v[172:175], v[40:43]\n"
"v_mfma_f32_16x16x32_bf16 v[44:47], a[220:223], v[172:175], v[44:47]\n"
"ds_read_b128 v[152:155], v4 offset:6144\n"
"v_mfma_f32_16x16x32_bf16 v[44:47], a[212:215], v[168:171], v[44:47]\n"
"buffer_load_dwordx4 v2, s[32:35], s63 offen offset:2048 sc1 lds\n"
"v_mfma_f32_16x16x32_bf16 v[44:47], a[204:207], v[164:167], v[44:47]\n"
"ds_read_b128 v[156:159], v4 offset:7168\n"
"v_mfma_f32_16x16x32_bf16 v[44:47], a[196:199], v[160:163], v[44:47]\n"
"v_mfma_f32_16x16x32_bf16 v[44:47], a[228:231], v[176:179], v[44:47]\n"
"ds_read_b128 v[160:163], v4 offset:8192\n"
"v_mfma_f32_16x16x32_bf16 v[44:47], a[236:239], v[180:183], v[44:47]\n"
"buffer_load_dwordx4 v2, s[32:35], s63 offen offset:3072 sc1 lds\n"
"v_mfma_f32_16x16x32_bf16 v[44:47], a[244:247], v[184:187], v[44:47]\n"
"ds_read_b128 v[164:167], v4 offset:9216\n"
"v_mfma_f32_16x16x32_bf16 v[44:47], a[252:255], v[188:191], v[44:47]\n"
"v_mfma_f32_16x16x32_bf16 v[40:43], a[248:251], v[188:191], v[40:43]\n"
"ds_read_b128 v[168:171], v4 offset:10240\n"
"s_add_u32 m0, s64, 4096\n"
"v_mfma_f32_16x16x32_bf16 v[40:43], a[240:243], v[184:187], v[40:43]\n"
"buffer_load_dwordx4 v6, s[32:35], s63 offen offset:0 sc1 lds\n"
"v_mfma_f32_16x16x32_bf16 v[40:43], a[232:235], v[180:183], v[40:43]\n"
"ds_read_b128 v[172:175], v4 offset:11264\n"
"s_sub_u32 s62, s62, 1\n"
"v_mfma_f32_16x16x32_bf16 v[40:43], a[224:227], v[176:179], v[40:43]\n"
"s_cbranch_scc0 .L_wl_1\n"
"s_nop 1\n"
"s_add_u32 s68, s68, 593920\n"
"s_nop 3\n"
"v_cvt_pk_bf16_f32 v56, v40, v41\n"
"v_cvt_pk_bf16_f32 v57, v42, v43\n"
"v_cvt_pk_bf16_f32 v58, v44, v45\n"
"v_cvt_pk_bf16_f32 v59, v46, v47\n"
"buffer_store_dwordx4 v[56:59], v2, s[32:35], s68 offen sc1\n"
"v_add_f32 v60, v40, v41\n"
"v_mul_f32 v61, v40, v40\n"
"v_fmac_f32 v61, v41, v41\n"
"v_add_f32 v60, v60, v42\n"
"v_fmac_f32 v61, v42, v42\n"
"v_add_f32 v60, v60, v43\n"
"v_fmac_f32 v61, v43, v43\n"
"v_add_f32 v60, v60, v44\n"
"v_fmac_f32 v61, v44, v44\n"
"v_add_f32 v60, v60, v45\n"
"v_fmac_f32 v61, v45, v45\n"
"v_add_f32 v60, v60, v46\n"
"v_fmac_f32 v61, v46, v46\n"
"v_add_f32 v60, v60, v47\n"
"v_fmac_f32 v61, v47, v47\n"
"s_nop 1\n"
"v_permlane16_swap_b32 v60, v61\n"
"v_add_f32 v60, v60, v61\n"
"s_nop 1\n"
"v_permlane32_swap_b32 v60, v61\n"
"v_add_f32 v60, v60, v61\n"
"buffer_store_dword v60, v15, s[32:35], s68 offen offset:1024 sc1\n"
"s_waitcnt vmcnt(9)\n"
"s_barrier\n"
"buffer_store_dwordx2 v[16:17], v2, s[92:95], s68 idxen sc1\n"
"s_add_u32 s68, s68, 593920\n"
"s_waitcnt vmcnt(1)\n"
"s_barrier\n"
"buffer_store_dwordx2 v[16:17], v2, s[92:95], s68 idxen sc1\n"
"s_branch .L_end\n"
".p2align 6\n"
".L_wv2:\n"
".L_wl_2:\n"
"s_waitcnt lgkmcnt(4)\n"
"v_mfma_f32_16x16x32_bf16 v[32:35], a[0:3], v[128:131], v[32:35]\n"
"ds_read_b128 v[176:179], v4 offset:12288\n"
"v_mfma_f32_16x16x32_bf16 v[32:35], a[8:11], v[132:135], v[32:35]\n"
"buffer_load_dwordx4 v6, s[32:35], s63 offen offset:1024 sc1 lds\n"
"v_mfma_f32_16x16x32_bf16 v[32:35], a[16:19], v[136:139], v[32:35]\n"
"ds_read_b128 v[180:183], v4 offset:13312\n"
"s_add_u32 s68, s68, 593920\n"
"v_mfma_f32_16x16x32_bf16 v[32:35], a[24:27], v[140:143], v[32:35]\n"
"v_cvt_pk_bf16_f32 v56, v40, v41\n"
"v_cvt_pk_bf16_f32 v57, v42, v43\n"
"v_mfma_f32_16x16x32_bf16 v[36:39], a[28:31], v[140:143], v[36:39]\n"
"ds_read_b128 v[184:187], v4 offset:14336\n"
"v_cvt_pk_bf16_f32 v58, v44, v45\n"
"v_mfma_f32_16x16x32_bf16 v[36:39], a[20:23], v[136:139], v[36:39]\n"
"v_cvt_pk_bf16_f32 v59, v46, v47\n"
"v_add_f32 v60, v40, v41\n"
"v_mfma_f32_16x16x32_bf16 v[36:39], a[12:15], v[132:135], v[36:39]\n"
"ds_read_b128 v[188:191], v4 offset:15360\n"
"v_mul_f32 v61, v40, v40\n"
"v_mfma_f32_16x16x32_bf16 v[36:39], a[4:7], v[128:131], v[36:39]\n"
"v_fmac_f32 v61, v41, v41\n"
"v_add_f32 v60, v60, v42\n"
"v_mfma_f32_16x16x32_bf16 v[36:39], a[36:39], v[144:147], v[36:39]\n"
"ds_read_b128 v[128:131], v4 offset:16384\n"
"v_fmac_f32 v61, v42, v42\n"
"v_mfma_f32_16x16x32_bf16 v[36:39], a[44:47], v[148:151], v[36:39]\n"
"buffer_store_dwordx4 v[56:59], v2, s[32:35], s68 offen sc1\n"
"v_mfma_f32_16x16x32_bf16 v[36:39], a[52:55], v[152:155], v[36:39]\n"
"ds_read_b128 v[132:135], v4 offset:17408\n"
"v_add_f32 v60, v60, v43\n"
"v_mfma_f32_16x16x32_bf16 v[36:39], a[60:63], v[156:159], v[36:39]\n"
"v_fmac_f32 v61, v43, v43\n"
"v_add_f32 v60, v60, v44\n"
"v_mfma_f32_16x16x32_bf16 v[32:35], a[56:59], v[156:159], v[32:35]\n"
"ds_read_b128 v[136:139], v4 offset:18432\n"
"v_fmac_f32 v61, v44, v44\n"
"v_mfma_f32_16x16x32_bf16 v[32:35], a[48:51], v[152:155], v[32:35]\n"
"v_add_f32 v60, v60, v45\n"
"v_fmac_f32 v61, v45, v45\n"
"v_mfma_f32_16x16x32_bf16 v[32:35], a[40:43], v[148:151], v[32:35]\n"
"ds_read_b128 v[140:143], v4 offset:19456\n"
"v_add_f32 v60, v60, v46\n"
"v_mfma_f32_16x16x32_bf16 v[32:35], a[32:35], v[144:147], v[32:35]\n"
"v_fmac_f32 v61, v46, v46\n"
"s_waitcnt lgkmcnt(4)\n"
"v_mfma_f32_16x16x32_bf16 v[32:35], a[64:67], v[160:163], v[32:35]\n"
"ds_read_b128 v[144:147], v4 offset:20480\n"
"v_add_f32 v60, v60, v47\n"
"v_mfma_f32_16x16x32_bf16 v[32:35], a[72:75], v[164:167], v[32:35]\n"
"v_fmac_f32 v61, v47, v47\n"
"v_mfma_f32_16x16x32_bf16 v[32:35], a[80:83], v[168:171], v[32:35]\n"
"ds_read_b128 v[148:151], v4 offset:21504\n"
"v_mfma_f32_16x16x32_bf16 v[32:35], a[88:91], v[172:175], v[32:35]\n"
"v_permlane16_swap_b32 v60, v61\n"
"v_mfma_f32_16x16x32_bf16 v[36:39], a[92:95], v[172:175], v[36:39]\n"
"ds_read_b128 v[152:155], v4 offset:22528\n"
"v_add_f32 v60, v60, v61\n"
"v_mfma_f32_16x16x32_bf16 v[36:39], a[84:87], v[168:171], v[36:39]\n"
"buffer_load_dwordx4 v[48:51], v2, s[32:35], s71 offen sc1\n"
"v_mfma_f32_16x16x32_bf16 v[36:39], a[76:79], v[164:167], v[36:39]\n"
"ds_read_b128 v[156:159], v4 offset:23552\n"
"s_add_u32 s71, s71, 593920\n"
"v_mfma_f32_16x16x32_bf16 v[36:39], a[68:71], v[160:163], v[36:39]\n"
"v_permlane32_swap_b32 v60, v61\n"
"v_mfma_f32_16x16x32_bf16 v[36:39], a[100:103], v[176:179], v[36:39]\n"
"ds_read_b128 v[160:163], v4 offset:24576\n"
"v_add_f32 v60, v60, v61\n"
"v_mfma_f32_16x16x32_bf16 v[36:39], a[108:111], v[180:183], v[36:39]\n"
"buffer_store_dword v60, v15, s[32:35], s68 offen offset:1024 sc1\n"
"v_mfma_f32_16x16x32_bf16 v[36:39], a[116:119], v[184:187], v[36:39]\n"
"ds_read_b128 v[164:167], v4 offset:25600\n"
"v_mfma_f32_16x16x32_bf16 v[36:39], a[124:127], v[188:191], v[36:39]\n"
"v_mfma_f32_16x16x32_bf16 v[32:35], a[120:123], v[188:191], v[32:35]\n"
"ds_read_b128 v[168:171], v4 offset:26624\n"
"v_mfma_f32_16x16x32_bf16 v[32:35], a[112:115], v[184:187], v[32:35]\n"
"buffer_load_dwordx4 v6, s[32:35], s63 offen offset:2048 sc1 lds\n"
"v_mfma_f32_16x16x32_bf16 v[32:35], a[104:107], v[180:183], v[32:35]\n"
"ds_read_b128 v[172:175], v4 offset:27648\n"
"v_mfma_f32_16x16x32_bf16 v[32:35], a[96:99], v[176:179], v[32:35]\n"
"s_waitcnt lgkmcnt(4)\n"
"v_mfma_f32_16x16x32_bf16 v[32:35], a[128:131], v[128:131], v[32:35]\n"
"ds_read_b128 v[176:179], v4 offset:28672\n"
"v_mfma_f32_16x16x32_bf16 v[32:35], a[136:139], v[132:135], v[32:35]\n"
"buffer_load_dwordx4 v6, s[32:35], s63 offen offset:3072 sc1 lds\n"
"v_mfma_f32_16x16x32_bf16 v[32:35], a[144:147], v[136:139], v[32:35]\n"
"ds_read_b128 v[180:183], v4 offset:29696\n"
"s_add_u32 s63, s63, 593920\n"
"v_mfma_f32_16x16x32_bf16 v[32:35], a[152:155], v[140:143], v[32:35]\n"
"v_mfma_f32_16x16x32_bf16 v[36:39], a[156:159], v[140:143], v[36:39]\n"
"ds_read_b128 v[184:187], v4 offset:30720\n"
"v_mfma_f32_16x16x32_bf16 v[36:39], a[148:151], v[136:139], v[36:39]\n"
"s_waitcnt vmcnt(11)\n"
"s_barrier\n"
"v_mfma_f32_16x16x32_bf16 v[36:39], a[140:143], v[132:135], v[36:39]\n"
"ds_read_b128 v[188:191], v4 offset:31744\n"
"v_mfma_f32_16x16x32_bf16 v[36:39], a[132:135], v[128:131], v[36:39]\n"
"v_mfma_f32_16x16x32_bf16 v[36:39], a[164:167], v[144:147], v[36:39]\n"
"ds_read_b128 v[128:131], v4 offset:32768\n"
"v_lshlrev_b32 v40, 16, v52\n"
"v_mfma_f32_16x16x32_bf16 v[36:39], a[172:175], v[148:151], v[36:39]\n"
"v_and_b32 v41, 0xffff0000, v52\n"
"v_lshlrev_b32 v42, 16, v53\n"
"v_mfma_f32_16x16x32_bf16 v[36:39], a[180:183], v[152:155], v[36:39]\n"
"ds_read_b128 v[132:135], v4 offset:33792\n"
"v_and_b32 v43, 0xffff0000, v53\n"
"v_mfma_f32_16x16x32_bf16 v[36:39], a[188:191], v[156:159], v[36:39]\n"
"v_lshlrev_b32 v44, 16, v54\n"
"v_and_b32 v45, 0xffff0000, v54\n"
"v_mfma_f32_16x16x32_bf16 v[32:35], a[184:187], v[156:159], v[32:35]\n"
"ds_read_b128 v[136:139], v4 offset:34816\n"
"s_add_u32 m0, s64, 32768\n"
"v_mfma_f32_16x16x32_bf16 v[32:35], a[176:179], v[152:155], v[32:35]\n"
"buffer_load_dwordx4 v2, s[32:35], s63 offen offset:0 sc1 lds\n"
"v_mfma_f32_16x16x32_bf16 v[32:35], a[168:171], v[148:151], v[32:35]\n"
"ds_read_b128 v[140:143], v4 offset:35840\n"
"v_lshlrev_b32 v46, 16, v55\n"
"v_mfma_f32_16x16x32_bf16 v[32:35], a[160:163], v[144:147], v[32:35]\n"
"v_and_b32 v47, 0xffff0000, v55\n"
"s_waitcnt lgkmcnt(4)\n"
"v_mfma_f32_16x16x32_bf16 v[32:35], a[192:195], v[160:163], v[32:35]\n"
"ds_read_b128 v[144:147], v4 offset:36864\n"
"v_mfma_f32_16x16x32_bf16 v[32:35], a[200:203], v[164:167], v[32:35]\n"
"buffer_load_dwordx4 v2, s[32:35], s63 offen offset:1024 sc1 lds\n"
"v_mfma_f32_16x16x32_bf16 v[32:35], a[208:211], v[168:171], v[32:35]\n"
"ds_read_b128 v[148:151], v4 offset:37888\n"
"v_mfma_f32_16x16x32_bf16 v[32:35], a[216:219], v[172:175], v[32:35]\n"
"v_mfma_f32_16x16x32_bf16 v[36:39], a[220:223], v[172:175], v[36:39]\n"
"ds_read_b128 v[152:155], v4 offset:38912\n"
"v_mfma_f32_16x16x32_bf16 v[36:39], a[212:215], v[168:171], v[36:39]\n"
"buffer_load_dwordx4 v2, s[32:35], s63 offen offset:2048 sc1 lds\n"
"v_mfma_f32_16x16x32_bf16 v[36:39], a[204:207], v[164:167], v[36:39]\n"
"ds_read_b128 v[156:159], v4 offset:39936\n"
"v_mfma_f32_16x16x32_bf16 v[36:39], a[196:199], v[160:163], v[36:39]\n"
"v_mfma_f32_16x16x32_bf16 v[36:39], a[228:231], v[176:179], v[36:39]\n"
"ds_read_b128 v[160:163], v4 offset:40960\n"
"v_mfma_f32_16x16x32_bf16 v[36:39], a[236:239], v[180:183], v[36:39]\n"
"buffer_load_dwordx4 v2, s[32:35], s63 offen offset:3072 sc1 lds\n"
"v_mfma_f32_16x16x32_bf16 v[36:39], a[244:247], v[184:187], v[36:39]\n"
"ds_read_b128 v[164:167], v4 offset:41984\n"
"v_mfma_f32_16x16x32_bf16 v[36:39], a[252:255], v[188:191], v[36:39]\n"
"v_mfma_f32_16x16x32_bf16 v[32:35], a[248:251], v[188:191], v[32:35]\n"
"ds_read_b128 v[168:171], v4 offset:43008\n"
"s_add_u32 m0, s64, 36864\n"
"v_mfma_f32_16x16x32_bf16 v[32:35], a[240:243], v[184:187], v[32:35]\n"
"buffer_load_dwordx4 v6, s[32:35], s63 offen offset:0 sc1 lds\n"
"v_mfma_f32_16x16x32_bf16 v[32:35], a[232:235], v[180:183], v[32:35]\n"
"ds_read_b128 v[172:175], v4 offset:44032\n"
"v_mfma_f32_16x16x32_bf16 v[32:35], a[224:227], v[176:179], v[32:35]\n"
"s_waitcnt lgkmcnt(4)\n"
"v_mfma_f32_16x16x32_bf16 v[40:43], a[0:3], v[128:131], v[40:43]\n"
"ds_read_b128 v[176:179], v4 offset:45056\n"
"v_mfma_f32_16x16x32_bf16 v[40:43], a[8:11], v[132:135], v[40:43]\n"
"buffer_load_dwordx4 v6, s[32:35], s63 offen offset:1024 sc1 lds\n"
"v_mfma_f32_16x16x32_bf16 v[40:43], a[16:19], v[136:139], v[40:43]\n"
"ds_read_b128 v[180:183], v4 offset:46080\n"
"s_add_u32 s68, s68, 593920\n"
"v_mfma_f32_16x16x32_bf16 v[40:43], a[24:27], v[140:143], v[40:43]\n"
"v_cvt_pk_bf16_f32 v56, v32, v33\n"
"v_cvt_pk_bf16_f32 v57, v34, v35\n"
"v_mfma_f32_16x16x32_bf16 v[44:47], a[28:31], v[140:143], v[44:47]\n"
"ds_read_b128 v[184:187], v4 offset:47104\n"
"v_cvt_pk_bf16_f32 v58, v36, v37\n"
"v_mfma_f32_16x16x32_bf16 v[44:47], a[20:23], v[136:139], v[44:47]\n"
"v_cvt_pk_bf16_f32 v59, v38, v39\n"
"v_add_f32 v60, v32, v33\n"
"v_mfma_f32_16x16x32_bf16 v[44:47], a[12:15], v[132:135], v[44:47]\n"
"ds_read_b128 v[188:191], v4 offset:48128\n"
"v_mul_f32 v61, v32, v32\n"
"v_mfma_f32_16x16x32_bf16 v[44:47], a[4:7], v[128:131], v[44:47]\n"
"v_fmac_f32 v61, v33, v33\n"
"v_add_f32 v60, v60, v34\n"
"v_mfma_f32_16x16x32_bf16 v[44:47], a[36:39], v[144:147], v[44:47]\n"
"ds_read_b128 v[128:131], v4 offset:49152\n"
"v_fmac_f32 v61, v34, v34\n"
"v_mfma_f32_16x16x32_bf16 v[44:47], a[44:47], v[148:151], v[44:47]\n"
"buffer_store_dwordx4 v[56:59], v2, s[32:35], s68 offen sc1\n"
"v_mfma_f32_16x16x32_bf16 v[44:47], a[52:55], v[152:155], v[44:47]\n"
"ds_read_b128 v[132:135], v4 offset:50176\n"
"v_add_f32 v60, v60, v35\n"
"v_mfma_f32_16x16x32_bf16 v[44:47], a[60:63], v[156:159], v[44:47]\n"
"v_fmac_f32 v61, v35, v35\n"
"v_add_f32 v60, v60, v36\n"
"v_mfma_f32_16x16x32_bf16 v[40:43], a[56:59], v[156:159], v[40:43]\n"
"ds_read_b128 v[136:139], v4 offset:51200\n"
"v_fmac_f32 v61, v36, v36\n"
"v_mfma_f32_16x16x32_bf16 v[40:43], a[48:51], v[152:155], v[40:43]\n"
"v_add_f32 v60, v60, v37\n"
"v_fmac_f32 v61, v37, v37\n"
"v_mfma_f32_16x16x32_bf16 v[40:43], a[40:43], v[148:151], v[40:43]\n"
"ds_read_b128 v[140:143], v4 offset:52224\n"
"v_add_f32 v60, v60, v38\n"
"v_mfma_f32_16x16x32_bf16 v[40:43], a[32:35], v[144:147], v[40:43]\n"
"v_fmac_f32 v61, v38, v38\n"
"s_waitcnt lgkmcnt(4)\n"
"v_mfma_f32_16x16x32_bf16 v[40:43], a[64:67], v[160:163], v[40:43]\n"
"ds_read_b128 v[144:147], v4 offset:53248\n"
"v_add_f32 v60, v60, v39\n"
"v_mfma_f32_16x16x32_bf16 v[40:43], a[72:75], v[164:167], v[40:43]\n"
"v_fmac_f32 v61, v39, v39\n"
"v_mfma_f32_16x16x32_bf16 v[40:43], a[80:83], v[168:171], v[40:43]\n"
"ds_read_b128 v[148:151], v4 offset:54272\n"
"v_mfma_f32_16x16x32_bf16 v[40:43], a[88:91], v[172:175], v[40:43]\n"
"v_permlane16_swap_b32 v60, v61\n"
"v_mfma_f32_16x16x32_bf16 v[44:47], a[92:95], v[172:175], v[44:47]\n"
"ds_read_b128 v[152:155], v4 offset:55296\n"
"v_add_f32 v60, v60, v61\n"
"v_mfma_f32_16x16x32_bf16 v[44:47], a[84:87], v[168:171], v[44:47]\n"
"buffer_load_dwordx4 v[52:55], v2, s[32:35], s71 offen sc1\n"
"v_mfma_f32_16x16x32_bf16 v[44:47], a[76:79], v[164:167], v[44:47]\n"
"ds_read_b128 v[156:159], v4 offset:56320\n"
"s_add_u32 s71, s71, 593920\n"
"v_mfma_f32_16x16x32_bf16 v[44:47], a[68:71], v[160:163], v[44:47]\n"
"v_permlane32_swap_b32 v60, v61\n"
"v_mfma_f32_16x16x32_bf16 v[44:47], a[100:103], v[176:179], v[44:47]\n"
"ds_read_b128 v[160:163], v4 offset:57344\n"
"v_add_f32 v60, v60, v61\n"
"v_mfma_f32_16x16x32_bf16 v[44:47], a[108:111], v[180:183], v[44:47]\n"
"buffer_store_dword v60, v15, s[32:35], s68 offen offset:1024 sc1\n"
"v_mfma_f32_16x16x32_bf16 v[44:47], a[116:119], v[184:187], v[44:47]\n"
"ds_read_b128 v[164:167], v4 offset:58368\n"
"v_mfma_f32_16x16x32_bf16 v[44:47], a[124:127], v[188:191], v[44:47]\n"
"v_mfma_f32_16x16x32_bf16 v[40:43], a[120:123], v[188:191], v[40:43]\n"
"ds_read_b128 v[168:171], v4 offset:59392\n"
"v_mfma_f32_16x16x32_bf16 v[40:43], a[112:115], v[184:187], v[40:43]\n"
"buffer_load_dwordx4 v6, s[32:35], s63 offen offset:2048 sc1 lds\n"
"v_mfma_f32_16x16x32_bf16 v[40:43], a[104:107], v[180:183], v[40:43]\n"
"ds_read_b128 v[172:175], v4 offset:60416\n"
"v_mfma_f32_16x16x32_bf16 v[40:43], a[96:99], v[176:179], v[40:43]\n"
"s_waitcnt lgkmcnt(4)\n"
"v_mfma_f32_16x16x32_bf16 v[40:43], a[128:131], v[128:131], v[40:43]\n"
"ds_read_b128 v[176:179], v4 offset:61440\n"
"v_mfma_f32_16x16x32_bf16 v[40:43], a[136:139], v[132:135], v[40:43]\n"
"buffer_load_dwordx4 v6, s[32:35], s63 offen offset:3072 sc1 lds\n"
"v_mfma_f32_16x16x32_bf16 v[40:43], a[144:147], v[136:139], v[40:43]\n"
"ds_read_b128 v[180:183], v4 offset:62464\n"
"s_add_u32 s63, s63, 593920\n"
"v_mfma_f32_16x16x32_bf16 v[40:43], a[152:155], v[140:143], v[40:43]\n"
"s_xor_b32 s64, s64, s65\n"
"v_mfma_f32_16x16x32_bf16 v[44:47], a[156:159], v[140:143], v[44:47]\n"
"ds_read_b128 v[184:187], v4 offset:63488\n"
"v_mfma_f32_16x16x32_bf16 v[44:47], a[148:151], v[136:139], v[44:47]\n"
"s_waitcnt vmcnt(11)\n"
"s_barrier\n"
"v_mfma_f32_16x16x32_bf16 v[44:47], a[140:143], v[132:135], v[44:47]\n"
"ds_read_b128 v[188:191], v4 offset:64512\n"
"v_xor_b32 v4, 65536, v4\n"
"v_mfma_f32_16x16x32_bf16 v[44:47], a[132:135], v[128:131], v[44:47]\n"
"v_mfma_f32_16x16x32_bf16 v[44:47], a[164:167], v[144:147], v[44:47]\n"
"ds_read_b128 v[128:131], v4 offset:0\n"
"v_lshlrev_b32 v32, 16, v48\n"
"v_mfma_f32_16x16x32_bf16 v[44:47], a[172:175], v[148:151], v[44:47]\n"
"v_and_b32 v33, 0xffff0000, v48\n"
"v_lshlrev_b32 v34, 16, v49\n"
"v_mfma_f32_16x16x32_bf16 v[44:47], a[180:183], v[152:155], v[44:47]\n"
"ds_read_b128 v[132:135], v4 offset:1024\n"
"v_and_b32 v35, 0xffff0000, v49\n"
"v_mfma_f32_16x16x32_bf16 v[44:47], a[188:191], v[156:159], v[44:47]\n"
"v_lshlrev_b32 v36, 16, v50\n"
"v_and_b32 v37, 0xffff0000, v50\n"
"v_mfma_f32_16x16x32_bf16 v[40:43], a[184:187], v[156:159], v[40:43]\n"
"ds_read_b128 v[136:139], v4 offset:2048\n"
"s_add_u32 m0, s64, 0\n"
"v_mfma_f32_16x16x32_bf16 v[40:43], a[176:179], v[152:155], v[40:43]\n"
"buffer_load_dwordx4 v2, s[32:35], s63 offen offset:0 sc1 lds\n"
"v_mfma_f32_16x16x32_bf16 v[40:43], a[168:171], v[148:151], v[40:43]\n"
"ds_read_b128 v[140:143], v4 offset:3072\n"
"v_lshlrev_b32 v38, 16, v51\n"
"v_mfma_f32_16x16x32_bf16 v[40:43], a[160:163], v[144:147], v[40:43]\n"
"v_and_b32 v39, 0xffff0000, v51\n"
"s_waitcnt lgkmcnt(4)\n"
"v_mfma_f32_16x16x32_bf16 v[40:43], a[192:195], v[160:163], v[40:43]\n"
"ds_read_b128 v[144:147], v4 offset:4096\n"
"v_mfma_f32_16x16x32_bf16 v[40:43], a[200:203], v[164:167], v[40:43]\n"
"buffer_load_dwordx4 v2, s[32:35], s63 offen offset:1024 sc1 lds\n"
"v_mfma_f32_16x16x32_bf16 v[40:43], a[208:211], v[168:171], v[40:43]\n"
"ds_read_b128 v[148:151], v4 offset:5120\n"
"v_mfma_f32_16x16x32_bf16 v[40:43], a[216:219], v[172:175], v[40:43]\n"
"v_mfma_f32_16x16x32_bf16 v[44:47], a[220:223], v[172:175], v[44:47]\n"
"ds_read_b128 v[152:155], v4 offset:6144\n"
"v_mfma_f32_16x16x32_bf16 v[44:47], a[212:215], v[168:171], v[44:47]\n"
"buffer_load_dwordx4 v2, s[32:35], s63 offen offset:2048 sc1 lds\n"
"v_mfma_f32_16x16x32_bf16 v[44:47], a[204:207], v[164:167], v[44:47]\n"
"ds_read_b128 v[156:159], v4 offset:7168\n"
"v_mfma_f32_16x16x32_bf16 v[44:47], a[196:199], v[160:163], v[44:47]\n"
"v_mfma_f32_16x16x32_bf16 v[44:47], a[228:231], v[176:179], v[44:47]\n"
"ds_read_b128 v[160:163], v4 offset:8192\n"
"v_mfma_f32_16x16x32_bf16 v[44:47], a[236:239], v[180:183], v[44:47]\n"
"buffer_load_dwordx4 v2, s[32:35], s63 offen offset:3072 sc1 lds\n"
"v_mfma_f32_16x16x32_bf16 v[44:47], a[244:247], v[184:187], v[44:47]\n"
"ds_read_b128 v[164:167], v4 offset:9216\n"
"v_mfma_f32_16x16x32_bf16 v[44:47], a[252:255], v[188:191], v[44:47]\n"
"v_mfma_f32_16x16x32_bf16 v[40:43], a[248:251], v[188:191], v[40:43]\n"
"ds_read_b128 v[168:171], v4 offset:10240\n"
"s_add_u32 m0, s64, 4096\n"
"v_mfma_f32_16x16x32_bf16 v[40:43], a[240:243], v[184:187], v[40:43]\n"
"buffer_load_dwordx4 v6, s[32:35], s63 offen offset:0 sc1 lds\n"
"v_mfma_f32_16x16x32_bf16 v[40:43], a[232:235], v[180:183], v[40:43]\n"
"ds_read_b128 v[172:175], v4 offset:11264\n"
"s_sub_u32 s62, s62, 1\n"
"v_mfma_f32_16x16x32_bf16 v[40:43], a[224:227], v[176:179], v[40:43]\n"
"s_cbranch_scc0 .L_wl_2\n"
"s_nop 1\n"
"s_add_u32 s68, s68, 593920\n"
"s_nop 3\n"
"v_cvt_pk_bf16_f32 v56, v40, v41\n"
"v_cvt_pk_bf16_f32 v57, v42, v43\n"
"v_cvt_pk_bf16_f32 v58, v44, v45\n"
"v_cvt_pk_bf16_f32 v59, v46, v47\n"
"buffer_store_dwordx4 v[56:59], v2, s[32:35], s68 offen sc1\n"
"v_add_f32 v60, v40, v41\n"
"v_mul_f32 v61, v40, v40\n"
"v_fmac_f32 v61, v41, v41\n"
"v_add_f32 v60, v60, v42\n"
"v_fmac_f32 v61, v42, v42\n"
"v_add_f32 v60, v60, v43\n"
"v_fmac_f32 v61, v43, v43\n"
"v_add_f32 v60, v60, v44\n"
"v_fmac_f32 v61, v44, v44\n"
"v_add_f32 v60, v60, v45\n"
"v_fmac_f32 v61, v45, v45\n"
"v_add_f32 v60, v60, v46\n"
"v_fmac_f32 v61, v46, v46\n"
"v_add_f32 v60, v60, v47\n"
"v_fmac_f32 v61, v47, v47\n"
"s_nop 1\n"
"v_permlane16_swap_b32 v60, v61\n"
"v_add_f32 v60, v60, v61\n"
"s_nop 1\n"
"v_permlane32_swap_b32 v60, v61\n"
"v_add_f32 v60, v60, v61\n"
"buffer_store_dword v60, v15, s[32:35], s68 offen offset:1024 sc1\n"
"s_waitcnt vmcnt(9)\n"
"s_barrier\n"
"buffer_store_dwordx2 v[16:17], v2, s[92:95], s68 idxen sc1\n"
"s_add_u32 s68, s68, 593920\n"
"s_waitcnt vmcnt(1)\n"
"s_barrier\n"
"buffer_store_dwordx2 v[16:17], v2, s[92:95], s68 idxen sc1\n"
"s_branch .L_end\n"
".p2align 6\n"
".L_wv3:\n"
".L_wl_3:\n"
"s_waitcnt lgkmcnt(4)\n"
"v_mfma_f32_16x16x32_bf16 v[32:35], a[0:3], v[128:131], v[32:35]\n"
"ds_read_b128 v[176:179], v4 offset:12288\n"
"v_mfma_f32_16x16x32_bf16 v[32:35], a[8:11], v[132:135], v[32:35]\n"
"buffer_load_dwordx4 v6, s[32:35], s63 offen offset:1024 sc1 lds\n"
"v_mfma_f32_16x16x32_bf16 v[32:35], a[16:19], v[136:139], v[32:35]\n"
"ds_read_b128 v[180:183], v4 offset:13312\n"
"s_add_u32 s68, s68, 593920\n"
"v_mfma_f32_16x16x32_bf16 v[32:35], a[24:27], v[140:143], v[32:35]\n"
"v_cvt_pk_bf16_f32 v56, v40, v41\n"
"v_cvt_pk_bf16_f32 v57, v42, v43\n"
"v_mfma_f32_16x16x32_bf16 v[36:39], a[28:31], v[140:143], v[36:39]\n"
"ds_read_b128 v[184:187], v4 offset:14336\n"
"v_cvt_pk_bf16_f32 v58, v44, v45\n"
"v_mfma_f32_16x16x32_bf16 v[36:39], a[20:23], v[136:139], v[36:39]\n"
"v_cvt_pk_bf16_f32 v59, v46, v47\n"
"v_add_f32 v60, v40, v41\n"
"v_mfma_f32_16x16x32_bf16 v[36:39], a[12:15], v[132:135], v[36:39]\n"
"ds_read_b128 v[188:191], v4 offset:15360\n"
"v_mul_f32 v61, v40, v40\n"
"v_mfma_f32_16x16x32_bf16 v[36:39], a[4:7], v[128:131], v[36:39]\n"
"v_fmac_f32 v61, v41, v41\n"
"v_add_f32 v60, v60, v42\n"
"v_mfma_f32_16x16x32_bf16 v[36:39], a[36:39], v[144:147], v[36:39]\n"
"ds_read_b128 v[128:131], v4 offset:16384\n"
"v_fmac_f32 v61, v42, v42\n"
"v_mfma_f32_16x16x32_bf16 v[36:39], a[44:47], v[148:151], v[36:39]\n"
"buffer_store_dwordx4 v[56:59], v2, s[32:35], s68 offen sc1\n"
"v_mfma_f32_16x16x32_bf16 v[36:39], a[52:55], v[152:155], v[36:39]\n"
"ds_read_b128 v[132:135], v4 offset:17408\n"
"v_add_f32 v60, v60, v43\n"
"v_mfma_f32_16x16x32_bf16 v[36:39], a[60:63], v[156:159], v[36:39]\n"
"v_fmac_f32 v61, v43, v43\n"
"v_add_f32 v60, v60, v44\n"
"v_mfma_f32_16x16x32_bf16 v[32:35], a[56:59], v[156:159], v[32:35]\n"
"ds_read_b128 v[136:139], v4 offset:18432\n"
"v_fmac_f32 v61, v44, v44\n"
"v_mfma_f32_16x16x32_bf16 v[32:35], a[48:51], v[152:155], v[32:35]\n"
"v_add_f32 v60, v60, v45\n"
"v_fmac_f32 v61, v45, v45\n"
"v_mfma_f32_16x16x32_bf16 v[32:35], a[40:43], v[148:151], v[32:35]\n"
"ds_read_b128 v[140:143], v4 offset:19456\n"
"v_add_f32 v60, v60, v46\n"
"v_mfma_f32_16x16x32_bf16 v[32:35], a[32:35], v[144:147], v[32:35]\n"
"v_fmac_f32 v61, v46, v46\n"
"s_waitcnt lgkmcnt(4)\n"
"v_mfma_f32_16x16x32_bf16 v[32:35], a[64:67], v[160:163], v[32:35]\n"
"ds_read_b128 v[144:147], v4 offset:20480\n"
"v_add_f32 v60, v60, v47\n"
"v_mfma_f32_16x16x32_bf16 v[32:35], a[72:75], v[164:167], v[32:35]\n"
"v_fmac_f32 v61, v47, v47\n"
"v_mfma_f32_16x16x32_bf16 v[32:35], a[80:83], v[168:171], v[32:35]\n"
"ds_read_b128 v[148:151], v4 offset:21504\n"
"v_mfma_f32_16x16x32_bf16 v[32:35], a[88:91], v[172:175], v[32:35]\n"
"v_permlane16_swap_b32 v60, v61\n"
"v_mfma_f32_16x16x32_bf16 v[36:39], a[92:95], v[172:175], v[36:39]\n"
"ds_read_b128 v[152:155], v4 offset:22528\n"
"v_add_f32 v60, v60, v61\n"
"v_mfma_f32_16x16x32_bf16 v[36:39], a[84:87], v[168:171], v[36:39]\n"
"buffer_load_dwordx4 v[48:51], v2, s[32:35], s71 offen sc1\n"
"v_mfma_f32_16x16x32_bf16 v[36:39], a[76:79], v[164:167], v[36:39]\n"
"ds_read_b128 v[156:159], v4 offset:23552\n"
"s_add_u32 s71, s71, 593920\n"
"v_mfma_f32_16x16x32_bf16 v[36:39], a[68:71], v[160:163], v[36:39]\n"
"v_permlane32_swap_b32 v60, v61\n"
"v_mfma_f32_16x16x32_bf16 v[36:39], a[100:103], v[176:179], v[36:39]\n"
"ds_read_b128 v[160:163], v4 offset:24576\n"
"v_add_f32 v60, v60, v61\n"
"v_mfma_f32_16x16x32_bf16 v[36:39], a[108:111], v[180:183], v[36:39]\n"
"buffer_store_dword v60, v15, s[32:35], s68 offen offset:1024 sc1\n"
"v_mfma_f32_16x16x32_bf16 v[36:39], a[116:119], v[184:187], v[36:39]\n"
"ds_read_b128 v[164:167], v4 offset:25600\n"
"v_mfma_f32_16x16x32_bf16 v[36:39], a[124:127], v[188:191], v[36:39]\n"
"v_mfma_f32_16x16x32_bf16 v[32:35], a[120:123], v[188:191], v[32:35]\n"
"ds_read_b128 v[168:171], v4 offset:26624\n"
"v_mfma_f32_16x16x32_bf16 v[32:35], a[112:115], v[184:187], v[32:35]\n"
"buffer_load_dwordx4 v6, s[32:35], s63 offen offset:2048 sc1 lds\n"
"v_mfma_f32_16x16x32_bf16 v[32:35], a[104:107], v[180:183], v[32:35]\n"
"ds_read_b128 v[172:175], v4 offset:27648\n"
"v_mfma_f32_16x16x32_bf16 v[32:35], a[96:99], v[176:179], v[32:35]\n"
"s_waitcnt lgkmcnt(4)\n"
"v_mfma_f32_16x16x32_bf16 v[32:35], a[128:131], v[128:131], v[32:35]\n"
"ds_read_b128 v[176:179], v4 offset:28672\n"
"v_mfma_f32_16x16x32_bf16 v[32:35], a[136:139], v[132:135], v[32:35]\n"
"buffer_load_dwordx4 v6, s[32:35], s63 offen offset:3072 sc1 lds\n"
"v_mfma_f32_16x16x32_bf16 v[32:35], a[144:147], v[136:139], v[32:35]\n"
"ds_read_b128 v[180:183], v4 offset:29696\n"
"s_add_u32 s63, s63, 593920\n"
"v_mfma_f32_16x16x32_bf16 v[32:35], a[152:155], v[140:143], v[32:35]\n"
"v_mfma_f32_16x16x32_bf16 v[36:39], a[156:159], v[140:143], v[36:39]\n"
"s_waitcnt vmcnt(11)\n"
"s_barrier\n"
"v_mfma_f32_16x16x32_bf16 v[36:39], a[148:151], v[136:139], v[36:39]\n"
"ds_read_b128 v[184:187], v4 offset:30720\n"
"v_mfma_f32_16x16x32_bf16 v[36:39], a[140:143], v[132:135], v[36:39]\n"
"ds_read_b128 v[188:191], v4 offset:31744\n"
"v_mfma_f32_16x16x32_bf16 v[36:39], a[132:135], v[128:131], v[36:39]\n"
"v_mfma_f32_16x16x32_bf16 v[36:39], a[164:167], v[144:147], v[36:39]\n"
"ds_read_b128 v[128:131], v4 offset:32768\n"
"v_lshlrev_b32 v40, 16, v52\n"
"v_mfma_f32_16x16x32_bf16 v[36:39], a[172:175], v[148:151], v[36:39]\n"
"v_and_b32 v41, 0xffff0000, v52\n"
"v_lshlrev_b32 v42, 16, v53\n"
"v_mfma_f32_16x16x32_bf16 v[36:39], a[180:183], v[152:155], v[36:39]\n"
"ds_read_b128 v[132:135], v4 offset:33792\n"
"v_and_b32 v43, 0xffff0000, v53\n"
"v_mfma_f32_16x16x32_bf16 v[36:39], a[188:191], v[156:159], v[36:39]\n"
"v_lshlrev_b32 v44, 16, v54\n"
"v_and_b32 v45, 0xffff0000, v54\n"
"v_mfma_f32_16x16x32_bf16 v[32:35], a[184:187], v[156:159], v[32:35]\n"
"ds_read_b128 v[136:139], v4 offset:34816\n"
"s_add_u32 m0, s64, 32768\n"
"v_mfma_f32_16x16x32_bf16 v[32:35], a[176:179], v[152:155], v[32:35]\n"
"buffer_load_dwordx4 v2, s[32:35], s63 offen offset:0 sc1 lds\n"
"v_mfma_f32_16x16x32_bf16 v[32:35], a[168:171], v[148:151], v[32:35]\n"
"ds_read_b128 v[140:143], v4 offset:35840\n"
"v_lshlrev_b32 v46, 16, v55\n"
"v_mfma_f32_16x16x32_bf16 v[32:35], a[160:163], v[144:147], v[32:35]\n"
"v_and_b32 v47, 0xffff0000, v55\n"
"s_waitcnt lgkmcnt(4)\n"
"v_mfma_f32_16x16x32_bf16 v[32:35], a[192:195], v[160:163], v[32:35]\n"
"ds_read_b128 v[144:147], v4 offset:36864\n"
"v_mfma_f32_16x16x32_bf16 v[32:35], a[200:203], v[164:167], v[32:35]\n"
"buffer_load_dwordx4 v2, s[32:35], s63 offen offset:1024 sc1 lds\n"
"v_mfma_f32_16x16x32_bf16 v[32:35], a[208:211], v[168:171], v[32:35]\n"
"ds_read_b128 v[148:151], v4 offset:37888\n"
"v_mfma_f32_16x16x32_bf16 v[32:35], a[216:219], v[172:175], v[32:35]\n"
"v_mfma_f32_16x16x32_bf16 v[36:39], a[220:223], v[172:175], v[36:39]\n"
"ds_read_b128 v[152:155], v4 offset:38912\n"
"v_mfma_f32_16x16x32_bf16 v[36:39], a[212:215], v[168:171], v[36:39]\n"
"buffer_load_dwordx4 v2, s[32:35], s63 offen offset:2048 sc1 lds\n"
"v_mfma_f32_16x16x32_bf16 v[36:39], a[204:207], v[164:167], v[36:39]\n"
"ds_read_b128 v[156:159], v4 offset:39936\n"
"v_mfma_f32_16x16x32_bf16 v[36:39], a[196:199], v[160:163], v[36:39]\n"
"v_mfma_f32_16x16x32_bf16 v[36:39], a[228:231], v[176:179], v[36:39]\n"
"ds_read_b128 v[160:163], v4 offset:40960\n"
"v_mfma_f32_16x16x32_bf16 v[36:39], a[236:239], v[180:183], v[36:39]\n"
"buffer_load_dwordx4 v2, s[32:35], s63 offen offset:3072 sc1 lds\n"
"v_mfma_f32_16x16x32_bf16 v[36:39], a[244:247], v[184:187], v[36:39]\n"
"ds_read_b128 v[164:167], v4 offset:41984\n"
"v_mfma_f32_16x16x32_bf16 v[36:39], a[252:255], v[188:191], v[36:39]\n"
"v_mfma_f32_16x16x32_bf16 v[32:35], a[248:251], v[188:191], v[32:35]\n"
"ds_read_b128 v[168:171], v4 offset:43008\n"
"s_add_u32 m0, s64, 36864\n"
"v_mfma_f32_16x16x32_bf16 v[32:35], a[240:243], v[184:187], v[32:35]\n"
"buffer_load_dwordx4 v6, s[32:35], s63 offen offset:0 sc1 lds\n"
"v_mfma_f32_16x16x32_bf16 v[32:35], a[232:235], v[180:183], v[32:35]\n"
"ds_read_b128 v[172:175], v4 offset:44032\n"
"v_mfma_f32_16x16x32_bf16 v[32:35], a[224:227], v[176:179], v[32:35]\n"
"s_waitcnt lgkmcnt(4)\n"
"v_mfma_f32_16x16x32_bf16 v[40:43], a[0:3], v[128:131], v[40:43]\n"
"ds_read_b128 v[176:179], v4 offset:45056\n"
"v_mfma_f32_16x16x32_bf16 v[40:43], a[8:11], v[132:135], v[40:43]\n"
"buffer_load_dwordx4 v6, s[32:35], s63 offen offset:1024 sc1 lds\n"
"v_mfma_f32_16x16x32_bf16 v[40:43], a[16:19], v[136:139], v[40:43]\n"
"ds_read_b128 v[180:183], v4 offset:46080\n"
"s_add_u32 s68, s68, 593920\n"
"v_mfma_f32_16x16x32_bf16 v[40:43], a[24:27], v[140:143], v[40:43]\n"
"v_cvt_pk_bf16_f32 v56, v32, v33\n"
"v_cvt_pk_bf16_f32 v57, v34, v35\n"
"v_mfma_f32_16x16x32_bf16 v[44:47], a[28:31], v[140:143], v[44:47]\n"
"ds_read_b128 v[184:187], v4 offset:47104\n"
"v_cvt_pk_bf16_f32 v58, v36, v37\n"
"v_mfma_f32_16x16x32_bf16 v[44:47], a[20:23], v[136:139], v[44:47]\n"
"v_cvt_pk_bf16_f32 v59, v38, v39\n"
"v_add_f32 v60, v32, v33\n"
"v_mfma_f32_16x16x32_bf16 v[44:47], a[12:15], v[132:135], v[44:47]\n"
"ds_read_b128 v[188:191], v4 offset:48128\n"
"v_mul_f32 v61, v32, v32\n"
"v_mfma_f32_16x16x32_bf16 v[44:47], a[4:7], v[128:131], v[44:47]\n"
"v_fmac_f32 v61, v33, v33\n"
"v_add_f32 v60, v60, v34\n"
"v_mfma_f32_16x16x32_bf16 v[44:47], a[36:39], v[144:147], v[44:47]\n"
"ds_read_b128 v[128:131], v4 offset:49152\n"
"v_fmac_f32 v61, v34, v34\n"
"v_mfma_f32_16x16x32_bf16 v[44:47], a[44:47], v[148:151], v[44:47]\n"
"buffer_store_dwordx4 v[56:59], v2, s[32:35], s68 offen sc1\n"
"v_mfma_f32_16x16x32_bf16 v[44:47], a[52:55], v[152:155], v[44:47]\n"
"ds_read_b128 v[132:135], v4 offset:50176\n"
"v_add_f32 v60, v60, v35\n"
"v_mfma_f32_16x16x32_bf16 v[44:47], a[60:63], v[156:159], v[44:47]\n"
"v_fmac_f32 v61, v35, v35\n"
"v_add_f32 v60, v60, v36\n"
"v_mfma_f32_16x16x32_bf16 v[40:43], a[56:59], v[156:159], v[40:43]\n"
"ds_read_b128 v[136:139], v4 offset:51200\n"
"v_fmac_f32 v61, v36, v36\n"
"v_mfma_f32_16x16x32_bf16 v[40:43], a[48:51], v[152:155], v[40:43]\n"
"v_add_f32 v60, v60, v37\n"
"v_fmac_f32 v61, v37, v37\n"
"v_mfma_f32_16x16x32_bf16 v[40:43], a[40:43], v[148:151], v[40:43]\n"
"ds_read_b128 v[140:143], v4 offset:52224\n"
"v_add_f32 v60, v60, v38\n"
"v_mfma_f32_16x16x32_bf16 v[40:43], a[32:35], v[144:147], v[40:43]\n"
"v_fmac_f32 v61, v38, v38\n"
"s_waitcnt lgkmcnt(4)\n"
"v_mfma_f32_16x16x32_bf16 v[40:43], a[64:67], v[160:163], v[40:43]\n"
"ds_read_b128 v[144:147], v4 offset:53248\n"
"v_add_f32 v60, v60, v39\n"
"v_mfma_f32_16x16x32_bf16 v[40:43], a[72:75], v[164:167], v[40:43]\n"
"v_fmac_f32 v61, v39, v39\n"
"v_mfma_f32_16x16x32_bf16 v[40:43], a[80:83], v[168:171], v[40:43]\n"
"ds_read_b128 v[148:151], v4 offset:54272\n"
"v_mfma_f32_16x16x32_bf16 v[40:43], a[88:91], v[172:175], v[40:43]\n"
"v_permlane16_swap_b32 v60, v61\n"
"v_mfma_f32_16x16x32_bf16 v[44:47], a[92:95], v[172:175], v[44:47]\n"
"ds_read_b128 v[152:155], v4 offset:55296\n"
"v_add_f32 v60, v60, v61\n"
"v_mfma_f32_16x16x32_bf16 v[44:47], a[84:87], v[168:171], v[44:47]\n"
"buffer_load_dwordx4 v[52:55], v2, s[32:35], s71 offen sc1\n"
"v_mfma_f32_16x16x32_bf16 v[44:47], a[76:79], v[164:167], v[44:47]\n"
"ds_read_b128 v[156:159], v4 offset:56320\n"
"s_add_u32 s71, s71, 593920\n"
"v_mfma_f32_16x16x32_bf16 v[44:47], a[68:71], v[160:163], v[44:47]\n"
"v_permlane32_swap_b32 v60, v61\n"
"v_mfma_f32_16x16x32_bf16 v[44:47], a[100:103], v[176:179], v[44:47]\n"
"ds_read_b128 v[160:163], v4 offset:57344\n"
"v_add_f32 v60, v60, v61\n"
"v_mfma_f32_16x16x32_bf16 v[44:47], a[108:111], v[180:183], v[44:47]\n"
"buffer_store_dword v60, v15, s[32:35], s68 offen offset:1024 sc1\n"
"v_mfma_f32_16x16x32_bf16 v[44:47], a[116:119], v[184:187], v[44:47]\n"
"ds_read_b128 v[164:167], v4 offset:58368\n"
"v_mfma_f32_16x16x32_bf16 v[44:47], a[124:127], v[188:191], v[44:47]\n"
"v_mfma_f32_16x16x32_bf16 v[40:43], a[120:123], v[188:191], v[40:43]\n"
"ds_read_b128 v[168:171], v4 offset:59392\n"
"v_mfma_f32_16x16x32_bf16 v[40:43], a[112:115], v[184:187], v[40:43]\n"
"buffer_load_dwordx4 v6, s[32:35], s63 offen offset:2048 sc1 lds\n"
"v_mfma_f32_16x16x32_bf16 v[40:43], a[104:107], v[180:183], v[40:43]\n"
"ds_read_b128 v[172:175], v4 offset:60416\n"
"v_mfma_f32_16x16x32_bf16 v[40:43], a[96:99], v[176:179], v[40:43]\n"
"s_waitcnt lgkmcnt(4)\n"
"v_mfma_f32_16x16x32_bf16 v[40:43], a[128:131], v[128:131], v[40:43]\n"
"ds_read_b128 v[176:179], v4 offset:61440\n"
"v_mfma_f32_16x16x32_bf16 v[40:43], a[136:139], v[132:135], v[40:43]\n"
"buffer_load_dwordx4 v6, s[32:35], s63 offen offset:3072 sc1 lds\n"
"v_mfma_f32_16x16x32_bf16 v[40:43], a[144:147], v[136:139], v[40:43]\n"
"ds_read_b128 v[180:183], v4 offset:62464\n"
"s_add_u32 s63, s63, 593920\n"
"v_mfma_f32_16x16x32_bf16 v[40:43], a[152:155], v[140:143], v[40:43]\n"
"s_xor_b32 s64, s64, s65\n"
"v_mfma_f32_16x16x32_bf16 v[44:47], a[156:159], v[140:143], v[44:47]\n"
"s_waitcnt vmcnt(11)\n"
"s_barrier\n"
"v_mfma_f32_16x16x32_bf16 v[44:47], a[148:151], v[136:139], v[44:47]\n"
"ds_read_b128 v[184:187], v4 offset:63488\n"
"v_mfma_f32_16x16x32_bf16 v[44:47], a[140:143], v[132:135], v[44:47]\n"
"ds_read_b128 v[188:191], v4 offset:64512\n"
"v_xor_b32 v4, 65536, v4\n"
"v_mfma_f32_16x16x32_bf16 v[44:47], a[132:135], v[128:131], v[44:47]\n"
"v_mfma_f32_16x16x32_bf16 v[44:47], a[164:167], v[144:147], v[44:47]\n"
"ds_read_b128 v[128:131], v4 offset:0\n"
"v_lshlrev_b32 v32, 16, v48\n"
"v_mfma_f32_16x16x32_bf16 v[44:47], a[172:175], v[148:151], v[44:47]\n"
"v_and_b32 v33, 0xffff0000, v48\n"
"v_lshlrev_b32 v34, 16, v49\n"
"v_mfma_f32_16x16x32_bf16 v[44:47], a[180:183], v[152:155], v[44:47]\n"
"ds_read_b128 v[132:135], v4 offset:1024\n"
"v_and_b32 v35, 0xffff0000, v49\n"
"v_mfma_f32_16x16x32_bf16 v[44:47], a[188:191], v[156:159], v[44:47]\n"
"v_lshlrev_b32 v36, 16, v50\n"
"v_and_b32 v37, 0xffff0000, v50\n"
"v_mfma_f32_16x16x32_bf16 v[40:43], a[184:187], v[156:159], v[40:43]\n"
"ds_read_b128 v[136:139], v4 offset:2048\n"
"s_add_u32 m0, s64, 0\n"
"v_mfma_f32_16x16x32_bf16 v[40:43], a[176:179], v[152:155], v[40:43]\n"
"buffer_load_dwordx4 v2, s[32:35], s63 offen offset:0 sc1 lds\n"
"v_mfma_f32_16x16x32_bf16 v[40:43], a[168:171], v[148:151], v[40:43]\n"
"ds_read_b128 v[140:143], v4 offset:3072\n"
"v_lshlrev_b32 v38, 16, v51\n"
"v_mfma_f32_16x16x32_bf16 v[40:43], a[160:163], v[144:147], v[40:43]\n"
"v_and_b32 v39, 0xffff0000, v51\n"
"s_waitcnt lgkmcnt(4)\n"
"v_mfma_f32_16x16x32_bf16 v[40:43], a[192:195], v[160:163], v[40:43]\n"
"ds_read_b128 v[144:147], v4 offset:4096\n"
"v_mfma_f32_16x16x32_bf16 v[40:43], a[200:203], v[164:167], v[40:43]\n"
"buffer_load_dwordx4 v2, s[32:35], s63 offen offset:1024 sc1 lds\n"
"v_mfma_f32_16x16x32_bf16 v[40:43], a[208:211], v[168:171], v[40:43]\n"
"ds_read_b128 v[148:151], v4 offset:5120\n"
"v_mfma_f32_16x16x32_bf16 v[40:43], a[216:219], v[172:175], v[40:43]\n"
"v_mfma_f32_16x16x32_bf16 v[44:47], a[220:223], v[172:175], v[44:47]\n"
"ds_read_b128 v[152:155], v4 offset:6144\n"
"v_mfma_f32_16x16x32_bf16 v[44:47], a[212:215], v[168:171], v[44:47]\n"
"buffer_load_dwordx4 v2, s[32:35], s63 offen offset:2048 sc1 lds\n"
"v_mfma_f32_16x16x32_bf16 v[44:47], a[204:207], v[164:167], v[44:47]\n"
"ds_read_b128 v[156:159], v4 offset:7168\n"
"v_mfma_f32_16x16x32_bf16 v[44:47], a[196:199], v[160:163], v[44:47]\n"
"v_mfma_f32_16x16x32_bf16 v[44:47], a[228:231], v[176:179], v[44:47]\n"
"ds_read_b128 v[160:163], v4 offset:8192\n"
"v_mfma_f32_16x16x32_bf16 v[44:47], a[236:239], v[180:183], v[44:47]\n"
"buffer_load_dwordx4 v2, s[32:35], s63 offen offset:3072 sc1 lds\n"
"v_mfma_f32_16x16x32_bf16 v[44:47], a[244:247], v[184:187], v[44:47]\n"
"ds_read_b128 v[164:167], v4 offset:9216\n"
"v_mfma_f32_16x16x32_bf16 v[44:47], a[252:255], v[188:191], v[44:47]\n"
"v_mfma_f32_16x16x32_bf16 v[40:43], a[248:251], v[188:191], v[40:43]\n"
"ds_read_b128 v[168:171], v4 offset:10240\n"
"s_add_u32 m0, s64, 4096\n"
"v_mfma_f32_16x16x32_bf16 v[40:43], a[240:243], v[184:187], v[40:43]\n"
"buffer_load_dwordx4 v6, s[32:35], s63 offen offset:0 sc1 lds\n"
"v_mfma_f32_16x16x32_bf16 v[40:43], a[232:235], v[180:183], v[40:43]\n"
"ds_read_b128 v[172:175], v4 offset:11264\n"
"s_sub_u32 s62, s62, 1\n"
"v_mfma_f32_16x16x32_bf16 v[40:43], a[224:227], v[176:179], v[40:43]\n"
"s_cbranch_scc0 .L_wl_3\n"
"s_nop 1\n"
"s_add_u32 s68, s68, 593920\n"
"s_nop 3\n"
"v_cvt_pk_bf16_f32 v56, v40, v41\n"
"v_cvt_pk_bf16_f32 v57, v42, v43\n"
"v_cvt_pk_bf16_f32 v58, v44, v45\n"
"v_cvt_pk_bf16_f32 v59, v46, v47\n"
"buffer_store_dwordx4 v[56:59], v2, s[32:35], s68 offen sc1\n"
"v_add_f32 v60, v40, v41\n"
"v_mul_f32 v61, v40, v40\n"
"v_fmac_f32 v61, v41, v41\n"
"v_add_f32 v60, v60, v42\n"
"v_fmac_f32 v61, v42, v42\n"
"v_add_f32 v60, v60, v43\n"
"v_fmac_f32 v61, v43, v43\n"
"v_add_f32 v60, v60, v44\n"
"v_fmac_f32 v61, v44, v44\n"
"v_add_f32 v60, v60, v45\n"
"v_fmac_f32 v61, v45, v45\n"
"v_add_f32 v60, v60, v46\n"
"v_fmac_f32 v61, v46, v46\n"
"v_add_f32 v60, v60, v47\n"
"v_fmac_f32 v61, v47, v47\n"
"s_nop 1\n"
"v_permlane16_swap_b32 v60, v61\n"
"v_add_f32 v60, v60, v61\n"
"s_nop 1\n"
"v_permlane32_swap_b32 v60, v61\n"
"v_add_f32 v60, v60, v61\n"
"buffer_store_dword v60, v15, s[32:35], s68 offen offset:1024 sc1\n"
"s_waitcnt vmcnt(9)\n"
"s_barrier\n"
"buffer_store_dwordx2 v[16:17], v2, s[92:95], s68 idxen sc1\n"
"s_add_u32 s68, s68, 593920\n"
"s_waitcnt vmcnt(1)\n"
"s_barrier\n"
"buffer_store_dwordx2 v[16:17], v2, s[92:95], s68 idxen sc1\n"
"s_branch .L_end\n"
".L_h:\n"
"v_mov_b32 v16, 0x3f803f80\n"
"v_mov_b32 v17, 0x3f803f80\n"
"v_mov_b32 v18, 0x3f803f80\n"
"v_mov_b32 v19, 0x3f803f80\n"
"s_add_u32 s36, s14, 0x0\n"
"s_addc_u32 s37, s15, 0x0\n"
"s_and_b32 s37, s37, 0xffff\n"
"s_mov_b32 s38, 0x700000\n"
"s_mov_b32 s39, 0x20000\n"
"s_add_u32 s40, s16, 0xfffffc00\n"
"s_addc_u32 s41, s17, 0xffffffff\n"
"s_and_b32 s41, s41, 0xffff\n"
"s_mov_b32 s42, 0x700400\n"
"s_mov_b32 s43, 0x20000\n"
"s_lshl_b32 s96, s27, 7\n"
"s_add_u32 s96, s96, 6912\n"
"v_and_b32 v8, 1, v1\n"
"v_bfe_u32 v3, v1, 1, 1\n"
"v_lshl_add_u32 v8, v8, 1, v3\n"
"v_mul_u32_u24 v8, 8, v8\n"
"s_lshl_b32 s48, s26, 5\n"
"v_add_u32 v8, s48, v8\n"
"v_bfe_u32 v3, v1, 4, 1\n"
"v_lshl_add_u32 v8, v3, 10, v8\n"
"v_lshrrev_b32 v4, 3, v1\n"
"v_bfe_u32 v5, v1, 4, 1\n"
"s_and_b32 s48, s26, 1\n"
"s_lshl_b32 s48, s48, 1\n"
"v_or_b32 v5, s48, v5\n"
"v_lshlrev_b32 v5, 1, v5\n"
"v_and_b32 v6, 7, v1\n"
"v_xor_b32 v5, v5, v6\n"
"v_mul_u32_u24 v4, 7168, v4\n"
"v_lshl_add_u32 v7, v5, 4, v4\n"
"s_mul_i32 s48, s26, 57344\n"
"s_add_u32 s96, s96, s48\n"
"s_mul_i32 s97, s26, 2048\n"
"s_add_u32 m0, s97, 0\n"
"s_nop 0\n"
"buffer_load_dwordx4 v7, s[36:39], s96 offen offset:0 lds\n"
"buffer_load_dwordx4 v7, s[40:43], s96 offen offset:1024 lds\n"
"s_add_u32 s96, s96, 229376\n"
"s_add_u32 m0, s97, 8192\n"
"s_nop 0\n"
"buffer_load_dwordx4 v7, s[36:39], s96 offen offset:0 lds\n"
"buffer_load_dwordx4 v7, s[40:43], s96 offen offset:1024 lds\n"
"s_add_u32 s96, s96, 229376\n"
"s_add_u32 m0, s97, 16384\n"
"s_nop 0\n"
"buffer_load_dwordx4 v7, s[36:39], s96 offen offset:0 lds\n"
"buffer_load_dwordx4 v7, s[40:43], s96 offen offset:1024 lds\n"
"s_add_u32 s96, s96, 229376\n"
"s_add_u32 m0, s97, 24576\n"
"s_nop 0\n"
"buffer_load_dwordx4 v7, s[36:39], s96 offen offset:0 lds\n"
"buffer_load_dwordx4 v7, s[40:43], s96 offen offset:1024 lds\n"
"s_add_u32 s96, s96, 229376\n"
"s_add_u32 m0, s97, 32768\n"
"s_nop 0\n"
"buffer_load_dwordx4 v7, s[36:39], s96 offen offset:0 lds\n"
"buffer_load_dwordx4 v7, s[40:43], s96 offen offset:1024 lds\n"
"s_add_u32 s96, s96, 229376\n"
"s_add_u32 m0, s97, 40960\n"
"s_nop 0\n"
"buffer_load_dwordx4 v7, s[36:39], s96 offen offset:0 lds\n"
"buffer_load_dwordx4 v7, s[40:43], s96 offen offset:1024 lds\n"
"s_add_u32 s96, s96, 229376\n"
"s_add_u32 m0, s97, 49152\n"
"s_nop 0\n"
"buffer_load_dwordx4 v7, s[36:39], s96 offen offset:0 lds\n"
"buffer_load_dwordx4 v7, s[40:43], s96 offen offset:1024 lds\n"
"s_add_u32 s96, s96, 229376\n"
"s_add_u32 m0, s97, 57344\n"
"s_nop 0\n"
"buffer_load_dwordx4 v7, s[36:39], s96 offen offset:0 lds\n"
"buffer_load_dwordx4 v7, s[40:43], s96 offen offset:1024 lds\n"
"s_add_u32 s96, s96, 229376\n"
"s_add_u32 m0, s97, 65536\n"
"s_nop 0\n"
"buffer_load_dwordx4 v7, s[36:39], s96 offen offset:0 lds\n"
"buffer_load_dwordx4 v7, s[40:43], s96 offen offset:1024 lds\n"
"s_add_u32 s96, s96, 229376\n"
"s_add_u32 m0, s97, 73728\n"
"s_nop 0\n"
"buffer_load_dwordx4 v7, s[36:39], s96 offen offset:0 lds\n"
"buffer_load_dwordx4 v7, s[40:43], s96 offen offset:1024 lds\n"
"s_add_u32 s96, s96, 229376\n"
"s_add_u32 m0, s97, 81920\n"
"s_nop 0\n"
"buffer_load_dwordx4 v7, s[36:39], s96 offen offset:0 lds\n"
"buffer_load_dwordx4 v7, s[40:43], s96 offen offset:1024 lds\n"
"s_add_u32 s96, s96, 229376\n"
"s_add_u32 m0, s97, 90112\n"
"s_nop 0\n"
"buffer_load_dwordx4 v7, s[36:39], s96 offen offset:0 lds\n"
"buffer_load_dwordx4 v7, s[40:43], s96 offen offset:1024 lds\n"
"s_add_u32 s96, s96, 229376\n"
"s_add_u32 m0, s97, 98304\n"
"s_nop 0\n"
"buffer_load_dwordx4 v7, s[36:39], s96 offen offset:0 lds\n"
"buffer_load_dwordx4 v7, s[40:43], s96 offen offset:1024 lds\n"
"s_add_u32 s96, s96, 229376\n"
"s_add_u32 m0, s97, 106496\n"
"s_nop 0\n"
"buffer_load_dwordx4 v7, s[36:39], s96 offen offset:0 lds\n"
"buffer_load_dwordx4 v7, s[40:43], s96 offen offset:1024 lds\n"
"s_add_u32 s96, s96, 229376\n"
"v_lshrrev_b32 v5, 5, v1\n"
"v_bfe_u32 v4, v1, 3, 1\n"
"v_and_b32 v6, 1, v5\n"
"v_lshl_add_u32 v4, v6, 1, v4\n"
"v_lshlrev_b32 v4, 5, v4\n"
"v_mul_u32_u24 v5, 2048, v5\n"
"v_bfe_u32 v6, v1, 2, 2\n"
"v_lshl_add_u32 v5, v6, 7, v5\n"
"v_xor_b32 v8, v4, v8\n"
"v_add_u32 v8, v5, v8\n"
"v_add_u32 v9, 65536, v8\n"
"s_waitcnt vmcnt(24)\n"
"s_barrier\n"
"s_add_u32 m0, s97, 114688\n"
"s_nop 0\n"
"buffer_load_dwordx4 v7, s[36:39], s96 offen offset:0 lds\n"
"buffer_load_dwordx4 v7, s[40:43], s96 offen offset:1024 lds\n"
"s_add_u32 s96, s96, 229376\n"
"s_add_u32 m0, s97, 122880\n"
"s_nop 0\n"
"buffer_load_dwordx4 v7, s[36:39], s96 offen offset:0 lds\n"
"buffer_load_dwordx4 v7, s[40:43], s96 offen offset:1024 lds\n"
"s_add_u32 s96, s96, 229376\n"
"ds_read_b64_tr_b16 a[0:1], v8 offset:0\n"
"ds_read_b64_tr_b16 a[2:3], v8 offset:512\n"
"ds_read_b64_tr_b16 a[4:5], v8 offset:4096\n"
"ds_read_b64_tr_b16 a[6:7], v8 offset:4608\n"
"ds_read_b64_tr_b16 a[32:33], v8 offset:8192\n"
"ds_read_b64_tr_b16 a[34:35], v8 offset:8704\n"
"ds_read_b64_tr_b16 a[36:37], v8 offset:12288\n"
"ds_read_b64_tr_b16 a[38:39], v8 offset:12800\n"
"s_waitcnt lgkmcnt(0)\n"
"v_mfma_f32_32x32x16_bf16 v[112:127], a[0:3], v[16:19], 0\n"
"v_mfma_f32_32x32x16_bf16 v[112:127], a[4:7], v[16:19], v[112:127]\n"
"v_mfma_f32_32x32x16_bf16 v[112:127], a[32:35], v[16:19], v[112:127]\n"
"v_mfma_f32_32x32x16_bf16 v[112:127], a[36:39], v[16:19], v[112:127]\n"
"s_waitcnt vmcnt(24)\n"
"s_barrier\n"
"s_add_u32 m0, s97, 0\n"
"s_nop 0\n"
"buffer_load_dwordx4 v7, s[36:39], s96 offen offset:0 lds\n"
"buffer_load_dwordx4 v7, s[40:43], s96 offen offset:1024 lds\n"
"s_add_u32 s96, s96, 229376\n"
"s_add_u32 m0, s97, 8192\n"
"s_nop 0\n"
"buffer_load_dwordx4 v7, s[36:39], s96 offen offset:0 lds\n"
"buffer_load_dwordx4 v7, s[40:43], s96 offen offset:1024 lds\n"
"s_add_u32 s96, s96, 229376\n"
"ds_read_b64_tr_b16 a[64:65], v8 offset:16384\n"
"ds_read_b64_tr_b16 a[66:67], v8 offset:16896\n"
"ds_read_b64_tr_b16 a[68:69], v8 offset:20480\n"
"ds_read_b64_tr_b16 a[70:71], v8 offset:20992\n"
"ds_read_b64_tr_b16 a[96:97], v8 offset:24576\n"
"ds_read_b64_tr_b16 a[98:99], v8 offset:25088\n"
"ds_read_b64_tr_b16 a[100:101], v8 offset:28672\n"
"ds_read_b64_tr_b16 a[102:103], v8 offset:29184\n"
"s_waitcnt lgkmcnt(0)\n"
"v_mfma_f32_32x32x16_bf16 v[112:127], a[64:67], v[16:19], v[112:127]\n"
"v_mfma_f32_32x32x16_bf16 v[112:127], a[68:71], v[16:19], v[112:127]\n"
"v_mfma_f32_32x32x16_bf16 v[112:127], a[96:99], v[16:19], v[112:127]\n"
"v_mfma_f32_32x32x16_bf16 v[112:127], a[100:103], v[16:19], v[112:127]\n"
"s_waitcnt vmcnt(24)\n"
"s_barrier\n"
"s_add_u32 m0, s97, 16384\n"
"s_nop 0\n"
"buffer_load_dwordx4 v7, s[36:39], s96 offen offset:0 lds\n"
"buffer_load_dwordx4 v7, s[40:43], s96 offen offset:1024 lds\n"
"s_add_u32 s96, s96, 229376\n"
"s_add_u32 m0, s97, 24576\n"
"s_nop 0\n"
"buffer_load_dwordx4 v7, s[36:39], s96 offen offset:0 lds\n"
"buffer_load_dwordx4 v7, s[40:43], s96 offen offset:1024 lds\n"
"s_add_u32 s96, s96, 229376\n"
"ds_read_b64_tr_b16 a[128:129], v8 offset:32768\n"
"ds_read_b64_tr_b16 a[130:131], v8 offset:33280\n"
"ds_read_b64_tr_b16 a[132:133], v8 offset:36864\n"
"ds_read_b64_tr_b16 a[134:135], v8 offset:37376\n"
"ds_read_b64_tr_b16 a[160:161], v8 offset:40960\n"
"ds_read_b64_tr_b16 a[162:163], v8 offset:41472\n"
"ds_read_b64_tr_b16 a[164:165], v8 offset:45056\n"
"ds_read_b64_tr_b16 a[166:167], v8 offset:45568\n"
"s_waitcnt lgkmcnt(0)\n"
"v_mfma_f32_32x32x16_bf16 v[112:127], a[128:131], v[16:19], v[112:127]\n"
"v_mfma_f32_32x32x16_bf16 v[112:127], a[132:135], v[16:19], v[112:127]\n"
"v_mfma_f32_32x32x16_bf16 v[112:127], a[160:163], v[16:19], v[112:127]\n"
"v_mfma_f32_32x32x16_bf16 v[112:127], a[164:167], v[16:19], v[112:127]\n"
"s_waitcnt vmcnt(24)\n"
"s_barrier\n"
"s_add_u32 m0, s97, 32768\n"
"s_nop 0\n"
"buffer_load_dwordx4 v7, s[36:39], s96 offen offset:0 lds\n"
"buffer_load_dwordx4 v7, s[40:43], s96 offen offset:1024 lds\n"
"s_add_u32 s96, s96, 229376\n"
"s_add_u32 m0, s97, 40960\n"
"s_nop 0\n"
"buffer_load_dwordx4 v7, s[36:39], s96 offen offset:0 lds\n"
"buffer_load_dwordx4 v7, s[40:43], s96 offen offset:1024 lds\n"
"s_add_u32 s96, s96, 229376\n"
"ds_read_b64_tr_b16 a[192:193], v8 offset:49152\n"
"ds_read_b64_tr_b16 a[194:195], v8 offset:49664\n"
"ds_read_b64_tr_b16 a[196:197], v8 offset:53248\n"
"ds_read_b64_tr_b16 a[198:199], v8 offset:53760\n"
"ds_read_b64_tr_b16 a[224:225], v8 offset:57344\n"
"ds_read_b64_tr_b16 a[226:227], v8 offset:57856\n"
"ds_read_b64_tr_b16 a[228:229], v8 offset:61440\n"
"ds_read_b64_tr_b16 a[230:231], v8 offset:61952\n"
"s_waitcnt lgkmcnt(0)\n"
"v_mfma_f32_32x32x16_bf16 v[112:127], a[192:195], v[16:19], v[112:127]\n"
"v_mfma_f32_32x32x16_bf16 v[112:127], a[196:199], v[16:19], v[112:127]\n"
"v_mfma_f32_32x32x16_bf16 v[112:127], a[224:227], v[16:19], v[112:127]\n"
"v_mfma_f32_32x32x16_bf16 v[112:127], a[228:231], v[16:19], v[112:127]\n"
"s_waitcnt vmcnt(24)\n"
"s_barrier\n"
"s_add_u32 m0, s97, 49152\n"
"s_nop 0\n"
"buffer_load_dwordx4 v7, s[36:39], s96 offen offset:0 lds\n"
"buffer_load_dwordx4 v7, s[40:43], s96 offen offset:1024 lds\n"
"s_add_u32 s96, s96, 229376\n"
"s_add_u32 m0, s97, 57344\n"
"s_nop 0\n"
"buffer_load_dwordx4 v7, s[36:39], s96 offen offset:0 lds\n"
"buffer_load_dwordx4 v7, s[40:43], s96 offen offset:1024 lds\n"
"s_add_u32 s96, s96, 229376\n"
"ds_read_b64_tr_b16 a[8:9], v9 offset:0\n"
"ds_read_b64_tr_b16 a[10:11], v9 offset:512\n"
"ds_read_b64_tr_b16 a[12:13], v9 offset:4096\n"
"ds_read_b64_tr_b16 a[14:15], v9 offset:4608\n"
"ds_read_b64_tr_b16 a[40:41], v9 offset:8192\n"
"ds_read_b64_tr_b16 a[42:43], v9 offset:8704\n"
"ds_read_b64_tr_b16 a[44:45], v9 offset:12288\n"
"ds_read_b64_tr_b16 a[46:47], v9 offset:12800\n"
"s_waitcnt lgkmcnt(0)\n"
"v_mfma_f32_32x32x16_bf16 v[112:127], a[8:11], v[16:19], v[112:127]\n"
"v_mfma_f32_32x32x16_bf16 v[112:127], a[12:15], v[16:19], v[112:127]\n"
"v_mfma_f32_32x32x16_bf16 v[112:127], a[40:43], v[16:19], v[112:127]\n"
"v_mfma_f32_32x32x16_bf16 v[112:127], a[44:47], v[16:19], v[112:127]\n"
"s_waitcnt vmcnt(24)\n"
"s_barrier\n"
"s_add_u32 m0, s97, 65536\n"
"s_nop 0\n"
"buffer_load_dwordx4 v7, s[36:39], s96 offen offset:0 lds\n"
"buffer_load_dwordx4 v7, s[40:43], s96 offen offset:1024 lds\n"
"s_add_u32 s96, s96, 229376\n"
"s_add_u32 m0, s97, 73728\n"
"s_nop 0\n"
"buffer_load_dwordx4 v7, s[36:39], s96 offen offset:0 lds\n"
"buffer_load_dwordx4 v7, s[40:43], s96 offen offset:1024 lds\n"
"s_add_u32 s96, s96, 229376\n"
"ds_read_b64_tr_b16 a[72:73], v9 offset:16384\n"
"ds_read_b64_tr_b16 a[74:75], v9 offset:16896\n"
"ds_read_b64_tr_b16 a[76:77], v9 offset:20480\n"
"ds_read_b64_tr_b16 a[78:79], v9 offset:20992\n"
"ds_read_b64_tr_b16 a[104:105], v9 offset:24576\n"
"ds_read_b64_tr_b16 a[106:107], v9 offset:25088\n"
"ds_read_b64_tr_b16 a[108:109], v9 offset:28672\n"
"ds_read_b64_tr_b16 a[110:111], v9 offset:29184\n"
"s_waitcnt lgkmcnt(0)\n"
"v_mfma_f32_32x32x16_bf16 v[112:127], a[72:75], v[16:19], v[112:127]\n"
"v_mfma_f32_32x32x16_bf16 v[112:127], a[76:79], v[16:19], v[112:127]\n"
"v_mfma_f32_32x32x16_bf16 v[112:127], a[104:107], v[16:19], v[112:127]\n"
"v_mfma_f32_32x32x16_bf16 v[112:127], a[108:111], v[16:19], v[112:127]\n"
"s_waitcnt vmcnt(24)\n"
"s_barrier\n"
"s_add_u32 m0, s97, 81920\n"
"s_nop 0\n"
"buffer_load_dwordx4 v7, s[36:39], s96 offen offset:0 lds\n"
"buffer_load_dwordx4 v7, s[40:43], s96 offen offset:1024 lds\n"
"s_add_u32 s96, s96, 229376\n"
"s_add_u32 m0, s97, 90112\n"
"s_nop 0\n"
"buffer_load_dwordx4 v7, s[36:39], s96 offen offset:0 lds\n"
"buffer_load_dwordx4 v7, s[40:43], s96 offen offset:1024 lds\n"
"s_add_u32 s96, s96, 229376\n"
"ds_read_b64_tr_b16 a[136:137], v9 offset:32768\n"
"ds_read_b64_tr_b16 a[138:139], v9 offset:33280\n"
"ds_read_b64_tr_b16 a[140:141], v9 offset:36864\n"
"ds_read_b64_tr_b16 a[142:143], v9 offset:37376\n"
"ds_read_b64_tr_b16 a[168:169], v9 offset:40960\n"
"ds_read_b64_tr_b16 a[170:171], v9 offset:41472\n"
"ds_read_b64_tr_b16 a[172:173], v9 offset:45056\n"
"ds_read_b64_tr_b16 a[174:175], v9 offset:45568\n"
"s_waitcnt lgkmcnt(0)\n"
"v_mfma_f32_32x32x16_bf16 v[112:127], a[136:139], v[16:19], v[112:127]\n"
"v_mfma_f32_32x32x16_bf16 v[112:127], a[140:143], v[16:19], v[112:127]\n"
"v_mfma_f32_32x32x16_bf16 v[112:127], a[168:171], v[16:19], v[112:127]\n"
"v_mfma_f32_32x32x16_bf16 v[112:127], a[172:175], v[16:19], v[112:127]\n"
"s_waitcnt vmcnt(24)\n"
"s_barrier\n"
"s_add_u32 m0, s97, 98304\n"
"s_nop 0\n"
"buffer_load_dwordx4 v7, s[36:39], s96 offen offset:0 lds\n"
"buffer_load_dwordx4 v7, s[40:43], s96 offen offset:1024 lds\n"
"s_add_u32 s96, s96, 229376\n"
"s_add_u32 m0, s97, 106496\n"
"s_nop 0\n"
"buffer_load_dwordx4 v7, s[36:39], s96 offen offset:0 lds\n"
"buffer_load_dwordx4 v7, s[40:43], s96 offen offset:1024 lds\n"
"s_add_u32 s96, s96, 229376\n"
"ds_read_b64_tr_b16 a[200:201], v9 offset:49152\n"
"ds_read_b64_tr_b16 a[202:203], v9 offset:49664\n"
"ds_read_b64_tr_b16 a[204:205], v9 offset:53248\n"
"ds_read_b64_tr_b16 a[206:207], v9 offset:53760\n"
"ds_read_b64_tr_b16 a[232:233], v9 offset:57344\n"
"ds_read_b64_tr_b16 a[234:235], v9 offset:57856\n"
"ds_read_b64_tr_b16 a[236:237], v9 offset:61440\n"
"ds_read_b64_tr_b16 a[238:239], v9 offset:61952\n"
"s_waitcnt lgkmcnt(0)\n"
"v_mfma_f32_32x32x16_bf16 v[112:127], a[200:203], v[16:19], v[112:127]\n"
"v_mfma_f32_32x32x16_bf16 v[112:127], a[204:207], v[16:19], v[112:127]\n"
"v_mfma_f32_32x32x16_bf16 v[112:127], a[232:235], v[16:19], v[112:127]\n"
"v_mfma_f32_32x32x16_bf16 v[112:127], a[236:239], v[16:19], v[112:127]\n"
"s_waitcnt vmcnt(24)\n"
"s_barrier\n"
"s_add_u32 m0, s97, 114688\n"
"s_nop 0\n"
"buffer_load_dwordx4 v7, s[36:39], s96 offen offset:0 lds\n"
"buffer_load_dwordx4 v7, s[40:43], s96 offen offset:1024 lds\n"
"s_add_u32 s96, s96, 229376\n"
"s_add_u32 m0, s97, 122880\n"
"s_nop 0\n"
"buffer_load_dwordx4 v7, s[36:39], s96 offen offset:0 lds\n"
"buffer_load_dwordx4 v7, s[40:43], s96 offen offset:1024 lds\n"
"s_add_u32 s96, s96, 229376\n"
"ds_read_b64_tr_b16 a[16:17], v8 offset:0\n"
"ds_read_b64_tr_b16 a[18:19], v8 offset:512\n"
"ds_read_b64_tr_b16 a[20:21], v8 offset:4096\n"
"ds_read_b64_tr_b16 a[22:23], v8 offset:4608\n"
"ds_read_b64_tr_b16 a[48:49], v8 offset:8192\n"
"ds_read_b64_tr_b16 a[50:51], v8 offset:8704\n"
"ds_read_b64_tr_b16 a[52:53], v8 offset:12288\n"
"ds_read_b64_tr_b16 a[54:55], v8 offset:12800\n"
"s_waitcnt lgkmcnt(0)\n"
"v_mfma_f32_32x32x16_bf16 v[112:127], a[16:19], v[16:19], v[112:127]\n"
"v_mfma_f32_32x32x16_bf16 v[112:127], a[20:23], v[16:19], v[112:127]\n"
"v_mfma_f32_32x32x16_bf16 v[112:127], a[48:51], v[16:19], v[112:127]\n"
"v_mfma_f32_32x32x16_bf16 v[112:127], a[52:55], v[16:19], v[112:127]\n"
"s_waitcnt vmcnt(24)\n"
"s_barrier\n"
"ds_read_b64_tr_b16 a[80:81], v8 offset:16384\n"
"ds_read_b64_tr_b16 a[82:83], v8 offset:16896\n"
"ds_read_b64_tr_b16 a[84:85], v8 offset:20480\n"
"ds_read_b64_tr_b16 a[86:87], v8 offset:20992\n"
"ds_read_b64_tr_b16 a[112:113], v8 offset:24576\n"
"ds_read_b64_tr_b16 a[114:115], v8 offset:25088\n"
"ds_read_b64_tr_b16 a[116:117], v8 offset:28672\n"
"ds_read_b64_tr_b16 a[118:119], v8 offset:29184\n"
"s_waitcnt lgkmcnt(0)\n"
"v_mfma_f32_32x32x16_bf16 v[112:127], a[80:83], v[16:19], v[112:127]\n"
"v_mfma_f32_32x32x16_bf16 v[112:127], a[84:87], v[16:19], v[112:127]\n"
"v_mfma_f32_32x32x16_bf16 v[112:127], a[112:115], v[16:19], v[112:127]\n"
"v_mfma_f32_32x32x16_bf16 v[112:127], a[116:119], v[16:19], v[112:127]\n"
"s_waitcnt vmcnt(20)\n"
"s_barrier\n"
"ds_read_b64_tr_b16 a[144:145], v8 offset:32768\n"
"ds_read_b64_tr_b16 a[146:147], v8 offset:33280\n"
"ds_read_b64_tr_b16 a[148:149], v8 offset:36864\n"
"ds_read_b64_tr_b16 a[150:151], v8 offset:37376\n"
"ds_read_b64_tr_b16 a[176:177], v8 offset:40960\n"
"ds_read_b64_tr_b16 a[178:179], v8 offset:41472\n"
"ds_read_b64_tr_b16 a[180:181], v8 offset:45056\n"
"ds_read_b64_tr_b16 a[182:183], v8 offset:45568\n"
"s_waitcnt lgkmcnt(0)\n"
"v_mfma_f32_32x32x16_bf16 v[112:127], a[144:147], v[16:19], v[112:127]\n"
"v_mfma_f32_32x32x16_bf16 v[112:127], a[148:151], v[16:19], v[112:127]\n"
"v_mfma_f32_32x32x16_bf16 v[112:127], a[176:179], v[16:19], v[112:127]\n"
"v_mfma_f32_32x32x16_bf16 v[112:127], a[180:183], v[16:19], v[112:127]\n"
"s_waitcnt vmcnt(16)\n"
"s_barrier\n"
"ds_read_b64_tr_b16 a[208:209], v8 offset:49152\n"
"ds_read_b64_tr_b16 a[210:211], v8 offset:49664\n"
"ds_read_b64_tr_b16 a[212:213], v8 offset:53248\n"
"ds_read_b64_tr_b16 a[214:215], v8 offset:53760\n"
"ds_read_b64_tr_b16 a[240:241], v8 offset:57344\n"
"ds_read_b64_tr_b16 a[242:243], v8 offset:57856\n"
"ds_read_b64_tr_b16 a[244:245], v8 offset:61440\n"
"ds_read_b64_tr_b16 a[246:247], v8 offset:61952\n"
"s_waitcnt lgkmcnt(0)\n"
"v_mfma_f32_32x32x16_bf16 v[112:127], a[208:211], v[16:19], v[112:127]\n"
"v_mfma_f32_32x32x16_bf16 v[112:127], a[212:215], v[16:19], v[112:127]\n"
"v_mfma_f32_32x32x16_bf16 v[112:127], a[240:243], v[16:19], v[112:127]\n"
"v_mfma_f32_32x32x16_bf16 v[112:127], a[244:247], v[16:19], v[112:127]\n"
"s_waitcnt vmcnt(12)\n"
"s_barrier\n"
"ds_read_b64_tr_b16 a[24:25], v9 offset:0\n"
"ds_read_b64_tr_b16 a[26:27], v9 offset:512\n"
"ds_read_b64_tr_b16 a[28:29], v9 offset:4096\n"
"ds_read_b64_tr_b16 a[30:31], v9 offset:4608\n"
"ds_read_b64_tr_b16 a[56:57], v9 offset:8192\n"
"ds_read_b64_tr_b16 a[58:59], v9 offset:8704\n"
"ds_read_b64_tr_b16 a[60:61], v9 offset:12288\n"
"ds_read_b64_tr_b16 a[62:63], v9 offset:12800\n"
"s_waitcnt lgkmcnt(0)\n"
"v_mfma_f32_32x32x16_bf16 v[112:127], a[24:27], v[16:19], v[112:127]\n"
"v_mfma_f32_32x32x16_bf16 v[112:127], a[28:31], v[16:19], v[112:127]\n"
"v_mfma_f32_32x32x16_bf16 v[112:127], a[56:59], v[16:19], v[112:127]\n"
"v_mfma_f32_32x32x16_bf16 v[112:127], a[60:63], v[16:19], v[112:127]\n"
"s_waitcnt vmcnt(8)\n"
"s_barrier\n"
"ds_read_b64_tr_b16 a[88:89], v9 offset:16384\n"
"ds_read_b64_tr_b16 a[90:91], v9 offset:16896\n"
"ds_read_b64_tr_b16 a[92:93], v9 offset:20480\n"
"ds_read_b64_tr_b16 a[94:95], v9 offset:20992\n"
"ds_read_b64_tr_b16 a[120:121], v9 offset:24576\n"
"ds_read_b64_tr_b16 a[122:123], v9 offset:25088\n"
"ds_read_b64_tr_b16 a[124:125], v9 offset:28672\n"
"ds_read_b64_tr_b16 a[126:127], v9 offset:29184\n"
"s_waitcnt lgkmcnt(0)\n"
"v_mfma_f32_32x32x16_bf16 v[112:127], a[88:91], v[16:19], v[112:127]\n"
"v_mfma_f32_32x32x16_bf16 v[112:127], a[92:95], v[16:19], v[112:127]\n"
"v_mfma_f32_32x32x16_bf16 v[112:127], a[120:123], v[16:19], v[112:127]\n"
"v_mfma_f32_32x32x16_bf16 v[112:127], a[124:127], v[16:19], v[112:127]\n"
"s_waitcnt vmcnt(4)\n"
"s_barrier\n"
"ds_read_b64_tr_b16 a[152:153], v9 offset:32768\n"
"ds_read_b64_tr_b16 a[154:155], v9 offset:33280\n"
"ds_read_b64_tr_b16 a[156:157], v9 offset:36864\n"
"ds_read_b64_tr_b16 a[158:159], v9 offset:37376\n"
"ds_read_b64_tr_b16 a[184:185], v9 offset:40960\n"
"ds_read_b64_tr_b16 a[186:187], v9 offset:41472\n"
"ds_read_b64_tr_b16 a[188:189], v9 offset:45056\n"
"ds_read_b64_tr_b16 a[190:191], v9 offset:45568\n"
"s_waitcnt lgkmcnt(0)\n"
"v_mfma_f32_32x32x16_bf16 v[112:127], a[152:155], v[16:19], v[112:127]\n"
"v_mfma_f32_32x32x16_bf16 v[112:127], a[156:159], v[16:19], v[112:127]\n"
"v_mfma_f32_32x32x16_bf16 v[112:127], a[184:187], v[16:19], v[112:127]\n"
"v_mfma_f32_32x32x16_bf16 v[112:127], a[188:191], v[16:19], v[112:127]\n"
"s_waitcnt vmcnt(0)\n"
"s_barrier\n"
"ds_read_b64_tr_b16 a[216:217], v9 offset:49152\n"
"ds_read_b64_tr_b16 a[218:219], v9 offset:49664\n"
"ds_read_b64_tr_b16 a[220:221], v9 offset:53248\n"
"ds_read_b64_tr_b16 a[222:223], v9 offset:53760\n"
"ds_read_b64_tr_b16 a[248:249], v9 offset:57344\n"
"ds_read_b64_tr_b16 a[250:251], v9 offset:57856\n"
"ds_read_b64_tr_b16 a[252:253], v9 offset:61440\n"
"ds_read_b64_tr_b16 a[254:255], v9 offset:61952\n"
"s_waitcnt lgkmcnt(0)\n"
"v_mfma_f32_32x32x16_bf16 v[112:127], a[216:219], v[16:19], v[112:127]\n"
"v_mfma_f32_32x32x16_bf16 v[112:127], a[220:223], v[16:19], v[112:127]\n"
"v_mfma_f32_32x32x16_bf16 v[112:127], a[248:251], v[16:19], v[112:127]\n"
"v_mfma_f32_32x32x16_bf16 v[112:127], a[252:255], v[16:19], v[112:127]\n"
"s_barrier\n"
"s_lshl2_add_u32 s49, s27, 272\n"
"s_lshl2_add_u32 s68, s27, s26\n"
"s_lshl_b32 s68, s68, 9\n"
"s_add_u32 s68, s68, 4829184\n"
"s_mov_b32 s63, 4681728\n"
"s_waitcnt lgkmcnt(0)\n"
"s_add_u32 s54, s52, 0x7f4a7c15\n"
"s_addc_u32 s55, s53, 0x9e3779b9\n"
"s_lshl_b32 s67, s49, 5\n"
"s_add_u32 s67, s67, 4898816\n"
"s_sub_u32 s51, s67, s68\n"
"s_add_u32 s92, s32, s51\n"
"s_addc_u32 s93, s33, 0x10000\n"
"s_cmp_eq_u32 s26, 0\n"
"s_cselect_b32 s94, 1, 0\n"
"s_mov_b32 s95, 0x20000\n"
"v_mov_b32 v16, s54\n"
"v_mov_b32 v17, s55\n"
"v_cmp_gt_u32 vcc, 12, v1\n"
"v_mul_u32_u24 v3, 296960, v1\n"
"v_mov_b32 v12, 0x7fff0000\n"
"v_cndmask_b32 v3, v12, v3, vcc\n"
"s_add_u32 s51, s67, 76021760\n"
"buffer_store_dwordx2 v[16:17], v3, s[32:35], s51 offen sc1\n"
"s_sub_u32 s67, s68, 296960\n"
"s_sub_u32 s51, s51, 76021760\n"
"s_sub_u32 s51, s51, s68\n"
"v_mul_u32_u24 v3, 296960, v1\n"
"v_add_u32 v3, s51, v3\n"
"v_cmp_gt_u32 vcc, 2, v0\n"
"v_mov_b32 v18, 0x7fff0000\n"
"s_nop 0\n"
"v_cndmask_b32 v18, v18, v3, vcc\n"
"s_mul_i32 s48, s28, 593920\n"
"s_add_u32 s68, s68, s48\n"
"s_sub_u32 s68, s68, 3563520\n"
"s_add_u32 s63, s63, s48\n"
"s_add_u32 s66, s48, 4898816\n"
"s_mul_i32 s64, s26, 9216\n"
"s_add_u32 s63, s63, s64\n"
"s_mov_b32 s62, 31\n"
"v_and_b32 v4, 0xf0, v2\n"
"v_lshrrev_b32 v3, 5, v1\n"
"v_lshl_add_u32 v4, v3, 8, v4\n"
"v_bfe_u32 v3, v1, 4, 1\n"
"v_mul_u32_u24 v15, 296960, v3\n"
"v_add_u32 v15, v15, v4\n"
"v_add_u32 v15, 1187840, v15\n"
"v_mul_u32_u24 v3, 36864, v3\n"
"v_add_u32 v4, v3, v4\n"
"v_add_u32 v5, 73728, v4\n"
"v_add_u32 v20, 0, v2\n"
"v_add_u32 v21, 296960, v2\n"
"v_add_u32 v22, 4096, v2\n"
"v_add_u32 v23, 301056, v2\n"
"v_add_u32 v24, 5120, v2\n"
"v_add_u32 v25, 302080, v2\n"
"v_and_b32 v14, 7, v1\n"
"s_mul_i32 s48, s26, 8\n"
"s_add_u32 s48, s48, 96\n"
"v_add_u32 v14, s48, v14\n"
"v_lshrrev_b32 v14, 2, v14\n"
"v_lshlrev_b32 v14, 7, v14\n"
"v_lshrrev_b32 v3, 4, v1\n"
"v_mul_u32_u24 v3, 296960, v3\n"
"v_add_u32 v14, v3, v14\n"
"v_cmp_gt_u32 vcc, 32, v1\n"
"v_mov_b32 v3, 0x7fff0000\n"
"s_nop 0\n"
"v_cndmask_b32 v14, v3, v14, vcc\n"
"v_cndmask_b32 v16, 0, v16, vcc\n"
"v_cndmask_b32 v17, 0, v17, vcc\n"
"v_bfe_u32 v3, v1, 1, 3\n"
"v_mul_u32_u24 v8, 1152, v3\n"
"v_lshrrev_b32 v3, 1, v3\n"
"v_add_u32 v3, s26, v3\n"
"v_lshrrev_b32 v9, 4, v1\n"
"v_lshl_add_u32 v3, v3, 2, v9\n"
"v_and_b32 v3, 15, v3\n"
"v_lshl_add_u32 v8, v3, 2, v8\n"
"v_and_b32 v3, 1, v1\n"
"v_lshl_add_u32 v8, v3, 6, v8\n"
"v_add_u32 v8, 1024, v8\n"
"s_lshl_b32 s48, s26, 2\n"
"v_add_u32 v10, s48, v9\n"
"v_and_b32 v11, 15, v1\n"
"v_add_u32 v3, 64, v1\n"
"v_cmp_eq_u32 vcc, 0, v11\n"
"v_add_u32 v9, 73728, v8\n"
"v_xor_b32 v9, v9, v8\n"
"v_cndmask_b32 v10, v3, v10, vcc\n"
"v_lshlrev_b32 v10, 2, v10\n"
"v_add_u32 v10, 147456, v10\n"
"v_lshlrev_b32 v11, 2, v11\n"
"v_add_u32 v11, 147456, v11\n"
"v_and_b32 v3, 15, v1\n"
"v_cmp_eq_u32 vcc, 8, v3\n"
"v_lshrrev_b32 v3, 4, v1\n"
"s_lshl2_add_u32 s48, s26, 36880\n"
"v_add_lshl_u32 v3, v3, s48, 2\n"
"v_cndmask_b32 v10, v10, v3, vcc\n"
"v_and_b32 v3, 16, v1\n"
"v_lshl_add_u32 v11, v3, 2, v11\n"
"s_mov_b32 s48, 20000\n"
"s_branch .L_rfha\n"
".L_rlha:\n"
"s_sleep 8\n"
".L_rfha:\n"
"buffer_load_dwordx2 v[12:13], v14, s[32:35], s66 offen sc1\n"
"s_sub_u32 s48, s48, 1\n"
"s_cbranch_scc1 .L_end\n"
"s_waitcnt vmcnt(0)\n"
"v_cmp_ne_u64 vcc, v[16:17], v[12:13]\n"
"s_cbranch_vccnz .L_rlha\n"
"s_add_u32 s66, s66, 1187840\n"
"s_add_u32 m0, s64, 0\n"
"s_nop 0\n"
"buffer_load_dwordx4 v20, s[32:35], s63 offen offset:0 sc1 lds\n"
"buffer_load_dwordx4 v20, s[32:35], s63 offen offset:1024 sc1 lds\n"
"buffer_load_dwordx4 v20, s[32:35], s63 offen offset:2048 sc1 lds\n"
"buffer_load_dwordx4 v20, s[32:35], s63 offen offset:3072 sc1 lds\n"
"s_add_u32 m0, s64, 36864\n"
"s_nop 0\n"
"buffer_load_dwordx4 v21, s[32:35], s63 offen offset:0 sc1 lds\n"
"buffer_load_dwordx4 v21, s[32:35], s63 offen offset:1024 sc1 lds\n"
"buffer_load_dwordx4 v21, s[32:35], s63 offen offset:2048 sc1 lds\n"
"buffer_load_dwordx4 v21, s[32:35], s63 offen offset:3072 sc1 lds\n"
"s_add_u32 m0, s64, 4096\n"
"s_nop 0\n"
"buffer_load_dwordx4 v22, s[32:35], s63 offen offset:0 sc1 lds\n"
"s_add_u32 m0, s64, 40960\n"
"s_nop 0\n"
"buffer_load_dwordx4 v23, s[32:35], s63 offen offset:0 sc1 lds\n"
"s_add_u32 m0, s64, 5120\n"
"s_nop 0\n"
"buffer_load_dwordx4 v24, s[32:35], s63 offen offset:0 sc1 lds\n"
"buffer_load_dwordx4 v24, s[32:35], s63 offen offset:1024 sc1 lds\n"
"buffer_load_dwordx4 v24, s[32:35], s63 offen offset:2048 sc1 lds\n"
"buffer_load_dwordx4 v24, s[32:35], s63 offen offset:3072 sc1 lds\n"
"s_add_u32 m0, s64, 41984\n"
"s_nop 0\n"
"buffer_load_dwordx4 v25, s[32:35], s63 offen offset:0 sc1 lds\n"
"buffer_load_dwordx4 v25, s[32:35], s63 offen offset:1024 sc1 lds\n"
"buffer_load_dwordx4 v25, s[32:35], s63 offen offset:2048 sc1 lds\n"
"buffer_load_dwordx4 v25, s[32:35], s63 offen offset:3072 sc1 lds\n"
"s_add_u32 s63, s63, 1187840\n"
"s_waitcnt vmcnt(8)\n"
"s_barrier\n"
"ds_read_b128 v[128:131], v4 offset:0\n"
"ds_read_b128 v[132:135], v4 offset:512\n"
"ds_read_b128 v[136:139], v4 offset:9216\n"
"ds_read_b128 v[140:143], v4 offset:9728\n"
"ds_read_b128 v[144:147], v4 offset:18432\n"
"ds_read_b128 v[148:151], v4 offset:18944\n"
"ds_read_b128 v[152:155], v4 offset:27648\n"
"ds_read_b128 v[156:159], v4 offset:28160\n"
"s_mov_b32 s48, 20000\n"
"s_branch .L_rfhb\n"
".L_rlhb:\n"
"s_sleep 8\n"
".L_rfhb:\n"
"buffer_load_dwordx2 v[12:13], v14, s[32:35], s66 offen sc1\n"
"s_sub_u32 s48, s48, 1\n"
"s_cbranch_scc1 .L_end\n"
"s_waitcnt vmcnt(0)\n"
"v_cmp_ne_u64 vcc, v[16:17], v[12:13]\n"
"s_cbranch_vccnz .L_rlhb\n"
".p2align 6\n"
".L_hl:\n"
"s_waitcnt lgkmcnt(4)\n"
"v_mfma_f32_32x32x16_bf16 v[32:47], a[0:3], v[128:131], 0\n"
"ds_read_b128 v[160:163], v4 offset:1152\n"
"s_waitcnt vmcnt(10)\n"
"v_cmp_ne_u64 vcc, v[16:17], v[12:13]\n"
"ds_read_b32 v74, v11 offset:128\n"
"s_add_u32 s68, s68, 1187840\n"
"v_mfma_f32_32x32x16_bf16 v[32:47], a[4:7], v[132:135], v[32:47]\n"
"ds_read_b128 v[164:167], v4 offset:1664\n"
"s_cbranch_vccnz .L_rth0\n"
".L_bkh0:\n"
"s_add_u32 s66, s66, 1187840\n"
"v_mfma_f32_32x32x16_bf16 v[32:47], a[8:11], v[136:139], v[32:47]\n"
"ds_read_b128 v[168:171], v4 offset:10368\n"
"s_add_u32 m0, s64, 73728\n"
"v_mfma_f32_32x32x16_bf16 v[32:47], a[12:15], v[140:143], v[32:47]\n"
"ds_read_b128 v[172:175], v4 offset:10880\n"
"buffer_load_dwordx4 v20, s[32:35], s63 offen offset:0 sc1 lds\n"
"s_waitcnt lgkmcnt(3)\n"
"v_mfma_f32_32x32x16_bf16 v[32:47], a[16:19], v[144:147], v[32:47]\n"
"ds_read_b128 v[176:179], v4 offset:19584\n"
"buffer_load_dwordx4 v20, s[32:35], s63 offen offset:1024 sc1 lds\n"
"v_lshlrev_b32 v72, 16, v74\n"
"v_and_b32 v74, 0xffff0000, v74\n"
"v_mfma_f32_32x32x16_bf16 v[32:47], a[20:23], v[148:151], v[32:47]\n"
"ds_read_b128 v[180:183], v4 offset:20096\n"
"v_rcp_f32 v73, v74\n"
"v_mul_f32 v74, v74, v74\n"
"v_fma_f32 v48, -v72, v112, v48\n"
"v_fma_f32 v49, -v72, v113, v49\n"
"v_mfma_f32_32x32x16_bf16 v[32:47], a[24:27], v[152:155], v[32:47]\n"
"ds_read_b128 v[184:187], v4 offset:28800\n"
"buffer_load_dwordx4 v20, s[32:35], s63 offen offset:2048 sc1 lds\n"
"v_mul_f32 v73, 0xbfb8aa3b, v73\n"
"v_fma_f32 v50, -v72, v114, v50\n"
"v_mfma_f32_32x32x16_bf16 v[32:47], a[28:31], v[156:159], v[32:47]\n"
"ds_read_b128 v[188:191], v4 offset:29312\n"
"v_fma_f32 v51, -v72, v115, v51\n"
"v_fma_f32 v52, -v72, v116, v52\n"
"v_fma_f32 v53, -v72, v117, v53\n"
"v_fma_f32 v54, -v72, v118, v54\n"
"s_waitcnt lgkmcnt(4)\n"
"v_mfma_f32_32x32x16_bf16 v[32:47], a[32:35], v[160:163], v[32:47]\n"
"ds_read_b128 v[128:131], v4 offset:2304\n"
"buffer_load_dwordx4 v20, s[32:35], s63 offen offset:3072 sc1 lds\n"
"v_fma_f32 v55, -v72, v119, v55\n"
"v_fma_f32 v56, -v72, v120, v56\n"
"v_mfma_f32_32x32x16_bf16 v[32:47], a[36:39], v[164:167], v[32:47]\n"
"ds_read_b128 v[132:135], v4 offset:2816\n"
"v_fma_f32 v57, -v72, v121, v57\n"
"v_fma_f32 v58, -v72, v122, v58\n"
"v_fma_f32 v59, -v72, v123, v59\n"
"v_fma_f32 v60, -v72, v124, v60\n"
"v_fma_f32 v61, -v72, v125, v61\n"
"v_mfma_f32_32x32x16_bf16 v[32:47], a[40:43], v[168:171], v[32:47]\n"
"ds_read_b128 v[136:139], v4 offset:11520\n"
"s_add_u32 m0, s64, 110592\n"
"v_fma_f32 v62, -v72, v126, v62\n"
"buffer_load_dwordx4 v21, s[32:35], s63 offen offset:0 sc1 lds\n"
"v_mfma_f32_32x32x16_bf16 v[32:47], a[44:47], v[172:175], v[32:47]\n"
"ds_read_b128 v[140:143], v4 offset:12032\n"
"v_fma_f32 v63, -v72, v127, v63\n"
"v_mul_f32 v76, v48, v73\n"
"v_mul_f32 v77, v49, v73\n"
"v_mul_f32 v78, v50, v73\n"
"s_waitcnt lgkmcnt(4)\n"
"v_mfma_f32_32x32x16_bf16 v[32:47], a[48:51], v[176:179], v[32:47]\n"
"ds_read_b128 v[144:147], v4 offset:20736\n"
"buffer_load_dwordx4 v21, s[32:35], s63 offen offset:1024 sc1 lds\n"
"v_exp_f32 v76, v76\n"
"v_mfma_f32_32x32x16_bf16 v[32:47], a[52:55], v[180:183], v[32:47]\n"
"ds_read_b128 v[148:151], v4 offset:21248\n"
"v_exp_f32 v77, v77\n"
"v_exp_f32 v78, v78\n"
"v_mul_f32 v79, v51, v73\n"
"v_mfma_f32_32x32x16_bf16 v[32:47], a[56:59], v[184:187], v[32:47]\n"
"ds_read_b128 v[152:155], v4 offset:29952\n"
"buffer_load_dwordx4 v21, s[32:35], s63 offen offset:2048 sc1 lds\n"
"v_exp_f32 v79, v79\n"
"v_mfma_f32_32x32x16_bf16 v[32:47], a[60:63], v[188:191], v[32:47]\n"
"ds_read_b128 v[156:159], v4 offset:30464\n"
"v_mul_f32 v80, v52, v73\n"
"v_mul_f32 v81, v53, v73\n"
"v_exp_f32 v80, v80\n"
"s_waitcnt lgkmcnt(4)\n"
"v_mfma_f32_32x32x16_bf16 v[32:47], a[64:67], v[128:131], v[32:47]\n"
"ds_read_b128 v[160:163], v4 offset:3456\n"
"buffer_load_dwordx4 v21, s[32:35], s63 offen offset:3072 sc1 lds\n"
"v_exp_f32 v81, v81\n"
"v_mfma_f32_32x32x16_bf16 v[32:47], a[68:71], v[132:135], v[32:47]\n"
"ds_read_b128 v[164:167], v4 offset:3968\n"
"v_mul_f32 v82, v54, v73\n"
"v_mul_f32 v83, v55, v73\n"
"v_exp_f32 v82, v82\n"
"v_mul_f32 v48, v48, v56\n"
"v_mfma_f32_32x32x16_bf16 v[32:47], a[72:75], v[136:139], v[32:47]\n"
"ds_read_b128 v[168:171], v4 offset:12672\n"
"s_add_u32 m0, s64, 77824\n"
"v_exp_f32 v83, v83\n"
"v_mul_f32 v49, v49, v57\n"
"v_mul_f32 v50, v50, v58\n"
"v_mfma_f32_32x32x16_bf16 v[32:47], a[76:79], v[140:143], v[32:47]\n"
"ds_read_b128 v[172:175], v4 offset:13184\n"
"buffer_load_dwordx4 v22, s[32:35], s63 offen offset:0 sc1 lds\n"
"v_mul_f32 v51, v51, v59\n"
"s_waitcnt lgkmcnt(4)\n"
"v_mfma_f32_32x32x16_bf16 v[32:47], a[80:83], v[144:147], v[32:47]\n"
"ds_read_b128 v[176:179], v4 offset:21888\n"
"s_add_u32 m0, s64, 114688\n"
"v_mul_f32 v52, v52, v60\n"
"buffer_load_dwordx4 v23, s[32:35], s63 offen offset:0 sc1 lds\n"
"v_mfma_f32_32x32x16_bf16 v[32:47], a[84:87], v[148:151], v[32:47]\n"
"ds_read_b128 v[180:183], v4 offset:22400\n"
"v_mul_f32 v53, v53, v61\n"
"v_mul_f32 v54, v54, v62\n"
"v_mul_f32 v55, v55, v63\n"
"v_fma_f32 v76, v76, v74, v74\n"
"v_fma_f32 v77, v77, v74, v74\n"
"v_mfma_f32_32x32x16_bf16 v[32:47], a[88:91], v[152:155], v[32:47]\n"
"ds_read_b128 v[184:187], v4 offset:31104\n"
"buffer_load_dwordx2 v[12:13], v14, s[32:35], s66 offen sc1\n"
"v_rcp_f32 v76, v76\n"
"v_mfma_f32_32x32x16_bf16 v[32:47], a[92:95], v[156:159], v[32:47]\n"
"ds_read_b128 v[188:191], v4 offset:31616\n"
"v_rcp_f32 v77, v77\n"
"v_fma_f32 v78, v78, v74, v74\n"
"v_fma_f32 v79, v79, v74, v74\n"
"s_waitcnt lgkmcnt(4)\n"
"v_mfma_f32_32x32x16_bf16 v[32:47], a[96:99], v[160:163], v[32:47]\n"
"s_waitcnt vmcnt(11)\n"
"s_barrier\n"
"ds_read_b128 v[128:131], v4 offset:4608\n"
"v_rcp_f32 v78, v78\n"
"v_fma_f32 v80, v80, v74, v74\n"
"v_mfma_f32_32x32x16_bf16 v[32:47], a[100:103], v[164:167], v[32:47]\n"
"ds_read_b128 v[132:135], v4 offset:5120\n"
"buffer_store_dwordx2 v[16:17], v18, s[32:35], s68 offen sc1\n"
"ds_read2st64_b32 v[64:65], v8 offset0:0 offset1:36\n"
"ds_read2st64_b32 v[66:67], v8 offset0:72 offset1:108\n"
"v_mfma_f32_32x32x16_bf16 v[32:47], a[104:107], v[168:171], v[32:47]\n"
"ds_read_b128 v[136:139], v4 offset:13824\n"
"s_add_u32 m0, s64, 78848\n"
"ds_read2st64_b32 v[68:69], v8 offset0:144 offset1:180\n"
"buffer_load_dwordx4 v24, s[32:35], s63 offen offset:0 sc1 lds\n"
"v_mfma_f32_32x32x16_bf16 v[32:47], a[108:111], v[172:175], v[32:47]\n"
"ds_read_b128 v[140:143], v4 offset:14336\n"
"ds_read2st64_b32 v[70:71], v8 offset0:216 offset1:252\n"
"v_xor_b32 v8, v9, v8\n"
"v_rcp_f32 v79, v79\n"
"s_waitcnt lgkmcnt(8)\n"
"v_mfma_f32_32x32x16_bf16 v[32:47], a[112:115], v[176:179], v[32:47]\n"
"ds_read_b128 v[144:147], v4 offset:23040\n"
"v_rcp_f32 v80, v80\n"
"v_fma_f32 v81, v81, v74, v74\n"
"v_fma_f32 v82, v82, v74, v74\n"
"v_fma_f32 v83, v83, v74, v74\n"
"v_mfma_f32_32x32x16_bf16 v[32:47], a[116:119], v[180:183], v[32:47]\n"
"ds_read_b128 v[148:151], v4 offset:23552\n"
"buffer_load_dwordx4 v24, s[32:35], s63 offen offset:1024 sc1 lds\n"
"v_rcp_f32 v81, v81\n"
"v_mfma_f32_32x32x16_bf16 v[32:47], a[120:123], v[184:187], v[32:47]\n"
"ds_read_b128 v[152:155], v4 offset:32256\n"
"v_rcp_f32 v82, v82\n"
"v_rcp_f32 v83, v83\n"
"v_mul_f32 v48, v48, v76\n"
"v_mfma_f32_32x32x16_bf16 v[32:47], a[124:127], v[188:191], v[32:47]\n"
"ds_read_b128 v[156:159], v4 offset:32768\n"
"s_waitcnt lgkmcnt(4)\n"
"v_add_f32 v64, v64, v65\n"
"v_add_f32 v66, v66, v67\n"
"v_add_f32 v68, v68, v69\n"
"v_mfma_f32_32x32x16_bf16 v[32:47], a[128:131], v[128:131], v[32:47]\n"
"ds_read_b128 v[160:163], v4 offset:5760\n"
"buffer_load_dwordx4 v24, s[32:35], s63 offen offset:2048 sc1 lds\n"
"v_add_f32 v70, v70, v71\n"
"v_add_f32 v66, v64, v66\n"
"v_mfma_f32_32x32x16_bf16 v[32:47], a[132:135], v[132:135], v[32:47]\n"
"ds_read_b128 v[164:167], v4 offset:6272\n"
"v_add_f32 v68, v68, v70\n"
"v_add_f32_dpp v64, v66, v66 row_ror:8 row_mask:0xf bank_mask:0x3\n"
"v_mul_f32 v49, v49, v77\n"
"v_mul_f32 v50, v50, v78\n"
"v_add_f32_dpp v64, v68, v68 row_ror:8 row_mask:0xf bank_mask:0xc\n"
"v_mfma_f32_32x32x16_bf16 v[32:47], a[136:139], v[136:139], v[32:47]\n"
"ds_read_b128 v[168:171], v4 offset:14976\n"
"v_add_f32_dpp v64, v64, v64 row_ror:12 row_mask:0xf bank_mask:0xf\n"
"v_mul_f32 v51, v51, v79\n"
"v_mul_f32 v52, v52, v80\n"
"v_add_f32_dpp v64, v64, v64 quad_perm:[2,3,0,1] row_mask:0xf bank_mask:0xf\n"
"v_mul_f32 v53, v53, v81\n"
"v_mfma_f32_32x32x16_bf16 v[32:47], a[140:143], v[140:143], v[32:47]\n"
"ds_read_b128 v[172:175], v4 offset:15488\n"
"v_mul_f32 v64, 0x3a800000, v64\n"
"v_mul_f32 v54, v54, v82\n"
"v_mul_f32 v65, v64, v64\n"
"v_mul_f32 v55, v55, v83\n"
"s_waitcnt lgkmcnt(4)\n"
"v_mfma_f32_32x32x16_bf16 v[32:47], a[144:147], v[144:147], v[32:47]\n"
"ds_read_b128 v[176:179], v4 offset:24192\n"
"buffer_load_dwordx4 v24, s[32:35], s63 offen offset:3072 sc1 lds\n"
"v_sub_f32_dpp v65, v64, v65 quad_perm:[1,1,3,3] row_mask:0xf bank_mask:0xf\n"
"v_cvt_pk_bf16_f32 v84, v48, v49\n"
"v_mfma_f32_32x32x16_bf16 v[32:47], a[148:151], v[148:151], v[32:47]\n"
"ds_read_b128 v[180:183], v4 offset:24704\n"
"v_max_f32 v65, 0, v65\n"
"v_cvt_pk_bf16_f32 v85, v50, v51\n"
"v_cvt_pk_bf16_f32 v65, 0, v65\n"
"v_cvt_pk_bf16_f32 v86, v52, v53\n"
"v_add_f32 v65, 0x3727c5ac, v65\n"
"v_mfma_f32_32x32x16_bf16 v[32:47], a[152:155], v[152:155], v[32:47]\n"
"ds_read_b128 v[184:187], v4 offset:33408\n"
"v_cvt_pk_bf16_f32 v65, 0, v65\n"
"v_cvt_pk_bf16_f32 v87, v54, v55\n"
"v_sqrt_f32 v65, v65\n"
"v_mfma_f32_32x32x16_bf16 v[32:47], a[156:159], v[156:159], v[32:47]\n"
"ds_read_b128 v[188:191], v4 offset:33920\n"
"s_add_u32 m0, s64, 115712\n"
"v_cvt_pk_bf16_f32 v65, v64, v65\n"
"s_waitcnt lgkmcnt(4)\n"
"v_mfma_f32_32x32x16_bf16 v[32:47], a[160:163], v[160:163], v[32:47]\n"
"ds_read_b128 v[128:131], v4 offset:6912\n"
"buffer_load_dwordx4 v25, s[32:35], s63 offen offset:0 sc1 lds\n"
"ds_write_b32 v10, v65 offset:0\n"
"v_mfma_f32_32x32x16_bf16 v[32:47], a[164:167], v[164:167], v[32:47]\n"
"ds_read_b128 v[132:135], v4 offset:7424\n"
"buffer_store_dwordx4 v[84:87], v15, s[32:35], s68 offen sc1\n"
"v_mfma_f32_32x32x16_bf16 v[32:47], a[168:171], v[168:171], v[32:47]\n"
"ds_read_b128 v[136:139], v4 offset:16128\n"
"v_mfma_f32_32x32x16_bf16 v[32:47], a[172:175], v[172:175], v[32:47]\n"
"ds_read_b128 v[140:143], v4 offset:16640\n"
"buffer_load_dwordx4 v25, s[32:35], s63 offen offset:1024 sc1 lds\n"
"s_waitcnt lgkmcnt(5)\n"
"v_mfma_f32_32x32x16_bf16 v[32:47], a[176:179], v[176:179], v[32:47]\n"
"ds_read_b128 v[144:147], v4 offset:25344\n"
"v_mfma_f32_32x32x16_bf16 v[32:47], a[180:183], v[180:183], v[32:47]\n"
"ds_read_b128 v[148:151], v4 offset:25856\n"
"v_mfma_f32_32x32x16_bf16 v[32:47], a[184:187], v[184:187], v[32:47]\n"
"ds_read_b128 v[152:155], v4 offset:34560\n"
"buffer_load_dwordx4 v25, s[32:35], s63 offen offset:2048 sc1 lds\n"
"v_mfma_f32_32x32x16_bf16 v[32:47], a[188:191], v[188:191], v[32:47]\n"
"ds_read_b128 v[156:159], v4 offset:35072\n"
"s_waitcnt lgkmcnt(4)\n"
"v_mfma_f32_32x32x16_bf16 v[32:47], a[192:195], v[128:131], v[32:47]\n"
"ds_read_b128 v[160:163], v4 offset:8064\n"
"v_mfma_f32_32x32x16_bf16 v[32:47], a[196:199], v[132:135], v[32:47]\n"
"ds_read_b128 v[164:167], v4 offset:8576\n"
"v_mfma_f32_32x32x16_bf16 v[32:47], a[200:203], v[136:139], v[32:47]\n"
"ds_read_b128 v[168:171], v4 offset:17280\n"
"buffer_load_dwordx4 v25, s[32:35], s63 offen offset:3072 sc1 lds\n"
"s_add_u32 s63, s63, 1187840\n"
"v_mfma_f32_32x32x16_bf16 v[32:47], a[204:207], v[140:143], v[32:47]\n"
"ds_read_b128 v[172:175], v4 offset:17792\n"
"s_waitcnt lgkmcnt(4)\n"
"v_mfma_f32_32x32x16_bf16 v[32:47], a[208:211], v[144:147], v[32:47]\n"
"ds_read_b128 v[176:179], v4 offset:26496\n"
"v_mfma_f32_32x32x16_bf16 v[32:47], a[212:215], v[148:151], v[32:47]\n"
"ds_read_b128 v[180:183], v4 offset:27008\n"
"v_mfma_f32_32x32x16_bf16 v[32:47], a[216:219], v[152:155], v[32:47]\n"
"ds_read_b128 v[184:187], v4 offset:35712\n"
"v_mfma_f32_32x32x16_bf16 v[32:47], a[220:223], v[156:159], v[32:47]\n"
"ds_read_b128 v[188:191], v4 offset:36224\n"
"s_waitcnt lgkmcnt(4)\n"
"v_mfma_f32_32x32x16_bf16 v[32:47], a[224:227], v[160:163], v[32:47]\n"
"s_waitcnt vmcnt(11)\n"
"s_barrier\n"
"ds_read_b128 v[128:131], v5 offset:0\n"
"v_mfma_f32_32x32x16_bf16 v[32:47], a[228:231], v[164:167], v[32:47]\n"
"ds_read_b128 v[132:135], v5 offset:512\n"
"v_mfma_f32_32x32x16_bf16 v[32:47], a[232:235], v[168:171], v[32:47]\n"
"ds_read_b128 v[136:139], v5 offset:9216\n"
"v_mfma_f32_32x32x16_bf16 v[32:47], a[236:239], v[172:175], v[32:47]\n"
"ds_read_b128 v[140:143], v5 offset:9728\n"
"s_waitcnt lgkmcnt(4)\n"
"v_mfma_f32_32x32x16_bf16 v[32:47], a[240:243], v[176:179], v[32:47]\n"
"ds_read_b128 v[144:147], v5 offset:18432\n"
"v_mfma_f32_32x32x16_bf16 v[32:47], a[244:247], v[180:183], v[32:47]\n"
"ds_read_b128 v[148:151], v5 offset:18944\n"
"v_mfma_f32_32x32x16_bf16 v[32:47], a[248:251], v[184:187], v[32:47]\n"
"ds_read_b128 v[152:155], v5 offset:27648\n"
"v_mfma_f32_32x32x16_bf16 v[32:47], a[252:255], v[188:191], v[32:47]\n"
"ds_read_b128 v[156:159], v5 offset:28160\n"
"s_waitcnt lgkmcnt(4)\n"
"v_mfma_f32_32x32x16_bf16 v[48:63], a[0:3], v[128:131], 0\n"
"ds_read_b128 v[160:163], v5 offset:1152\n"
"s_waitcnt vmcnt(10)\n"
"v_cmp_ne_u64 vcc, v[16:17], v[12:13]\n"
"ds_read_b32 v74, v11 offset:0\n"
"s_add_u32 s68, s68, 1187840\n"
"v_mfma_f32_32x32x16_bf16 v[48:63], a[4:7], v[132:135], v[48:63]\n"
"ds_read_b128 v[164:167], v5 offset:1664\n"
"s_cbranch_vccnz .L_rth1\n"
".L_bkh1:\n"
"s_add_u32 s66, s66, 1187840\n"
"v_mfma_f32_32x32x16_bf16 v[48:63], a[8:11], v[136:139], v[48:63]\n"
"ds_read_b128 v[168:171], v5 offset:10368\n"
"s_add_u32 m0, s64, 0\n"
"v_mfma_f32_32x32x16_bf16 v[48:63], a[12:15], v[140:143], v[48:63]\n"
"ds_read_b128 v[172:175], v5 offset:10880\n"
"buffer_load_dwordx4 v20, s[32:35], s63 offen offset:0 sc1 lds\n"
"s_waitcnt lgkmcnt(3)\n"
"v_mfma_f32_32x32x16_bf16 v[48:63], a[16:19], v[144:147], v[48:63]\n"
"ds_read_b128 v[176:179], v5 offset:19584\n"
"buffer_load_dwordx4 v20, s[32:35], s63 offen offset:1024 sc1 lds\n"
"v_lshlrev_b32 v72, 16, v74\n"
"v_and_b32 v74, 0xffff0000, v74\n"
"v_mfma_f32_32x32x16_bf16 v[48:63], a[20:23], v[148:151], v[48:63]\n"
"ds_read_b128 v[180:183], v5 offset:20096\n"
"v_rcp_f32 v73, v74\n"
"v_mul_f32 v74, v74, v74\n"
"v_fma_f32 v32, -v72, v112, v32\n"
"v_fma_f32 v33, -v72, v113, v33\n"
"v_mfma_f32_32x32x16_bf16 v[48:63], a[24:27], v[152:155], v[48:63]\n"
"ds_read_b128 v[184:187], v5 offset:28800\n"
"buffer_load_dwordx4 v20, s[32:35], s63 offen offset:2048 sc1 lds\n"
"v_mul_f32 v73, 0xbfb8aa3b, v73\n"
"v_fma_f32 v34, -v72, v114, v34\n"
"v_mfma_f32_32x32x16_bf16 v[48:63], a[28:31], v[156:159], v[48:63]\n"
"ds_read_b128 v[188:191], v5 offset:29312\n"
"v_fma_f32 v35, -v72, v115, v35\n"
"v_fma_f32 v36, -v72, v116, v36\n"
"v_fma_f32 v37, -v72, v117, v37\n"
"v_fma_f32 v38, -v72, v118, v38\n"
"s_waitcnt lgkmcnt(4)\n"
"v_mfma_f32_32x32x16_bf16 v[48:63], a[32:35], v[160:163], v[48:63]\n"
"ds_read_b128 v[128:131], v5 offset:2304\n"
"buffer_load_dwordx4 v20, s[32:35], s63 offen offset:3072 sc1 lds\n"
"v_fma_f32 v39, -v72, v119, v39\n"
"v_fma_f32 v40, -v72, v120, v40\n"
"v_mfma_f32_32x32x16_bf16 v[48:63], a[36:39], v[164:167], v[48:63]\n"
"ds_read_b128 v[132:135], v5 offset:2816\n"
"v_fma_f32 v41, -v72, v121, v41\n"
"v_fma_f32 v42, -v72, v122, v42\n"
"v_fma_f32 v43, -v72, v123, v43\n"
"v_fma_f32 v44, -v72, v124, v44\n"
"v_fma_f32 v45, -v72, v125, v45\n"
"v_mfma_f32_32x32x16_bf16 v[48:63], a[40:43], v[168:171], v[48:63]\n"
"ds_read_b128 v[136:139], v5 offset:11520\n"
"s_add_u32 m0, s64, 36864\n"
"v_fma_f32 v46, -v72, v126, v46\n"
"buffer_load_dwordx4 v21, s[32:35], s63 offen offset:0 sc1 lds\n"
"v_mfma_f32_32x32x16_bf16 v[48:63], a[44:47], v[172:175], v[48:63]\n"
"ds_read_b128 v[140:143], v5 offset:12032\n"
"v_fma_f32 v47, -v72, v127, v47\n"
"v_mul_f32 v76, v32, v73\n"
"v_mul_f32 v77, v33, v73\n"
"v_mul_f32 v78, v34, v73\n"
"s_waitcnt lgkmcnt(4)\n"
"v_mfma_f32_32x32x16_bf16 v[48:63], a[48:51], v[176:179], v[48:63]\n"
"ds_read_b128 v[144:147], v5 offset:20736\n"
"buffer_load_dwordx4 v21, s[32:35], s63 offen offset:1024 sc1 lds\n"
"v_exp_f32 v76, v76\n"
"v_mfma_f32_32x32x16_bf16 v[48:63], a[52:55], v[180:183], v[48:63]\n"
"ds_read_b128 v[148:151], v5 offset:21248\n"
"v_exp_f32 v77, v77\n"
"v_exp_f32 v78, v78\n"
"v_mul_f32 v79, v35, v73\n"
"v_mfma_f32_32x32x16_bf16 v[48:63], a[56:59], v[184:187], v[48:63]\n"
"ds_read_b128 v[152:155], v5 offset:29952\n"
"buffer_load_dwordx4 v21, s[32:35], s63 offen offset:2048 sc1 lds\n"
"v_exp_f32 v79, v79\n"
"v_mfma_f32_32x32x16_bf16 v[48:63], a[60:63], v[188:191], v[48:63]\n"
"ds_read_b128 v[156:159], v5 offset:30464\n"
"v_mul_f32 v80, v36, v73\n"
"v_mul_f32 v81, v37, v73\n"
"v_exp_f32 v80, v80\n"
"s_waitcnt lgkmcnt(4)\n"
"v_mfma_f32_32x32x16_bf16 v[48:63], a[64:67], v[128:131], v[48:63]\n"
"ds_read_b128 v[160:163], v5 offset:3456\n"
"buffer_load_dwordx4 v21, s[32:35], s63 offen offset:3072 sc1 lds\n"
"v_exp_f32 v81, v81\n"
"v_mfma_f32_32x32x16_bf16 v[48:63], a[68:71], v[132:135], v[48:63]\n"
"ds_read_b128 v[164:167], v5 offset:3968\n"
"v_mul_f32 v82, v38, v73\n"
"v_mul_f32 v83, v39, v73\n"
"v_exp_f32 v82, v82\n"
"v_mul_f32 v32, v32, v40\n"
"v_mfma_f32_32x32x16_bf16 v[48:63], a[72:75], v[136:139], v[48:63]\n"
"ds_read_b128 v[168:171], v5 offset:12672\n"
"s_add_u32 m0, s64, 4096\n"
"v_exp_f32 v83, v83\n"
"v_mul_f32 v33, v33, v41\n"
"v_mul_f32 v34, v34, v42\n"
"v_mfma_f32_32x32x16_bf16 v[48:63], a[76:79], v[140:143], v[48:63]\n"
"ds_read_b128 v[172:175], v5 offset:13184\n"
"buffer_load_dwordx4 v22, s[32:35], s63 offen offset:0 sc1 lds\n"
"v_mul_f32 v35, v35, v43\n"
"s_waitcnt lgkmcnt(4)\n"
"v_mfma_f32_32x32x16_bf16 v[48:63], a[80:83], v[144:147], v[48:63]\n"
"ds_read_b128 v[176:179], v5 offset:21888\n"
"s_add_u32 m0, s64, 40960\n"
"v_mul_f32 v36, v36, v44\n"
"buffer_load_dwordx4 v23, s[32:35], s63 offen offset:0 sc1 lds\n"
"v_mfma_f32_32x32x16_bf16 v[48:63], a[84:87], v[148:151], v[48:63]\n"
"ds_read_b128 v[180:183], v5 offset:22400\n"
"v_mul_f32 v37, v37, v45\n"
"v_mul_f32 v38, v38, v46\n"
"v_mul_f32 v39, v39, v47\n"
"v_fma_f32 v76, v76, v74, v74\n"
"v_fma_f32 v77, v77, v74, v74\n"
"v_mfma_f32_32x32x16_bf16 v[48:63], a[88:91], v[152:155], v[48:63]\n"
"ds_read_b128 v[184:187], v5 offset:31104\n"
"buffer_load_dwordx2 v[12:13], v14, s[32:35], s66 offen sc1\n"
"v_rcp_f32 v76, v76\n"
"v_mfma_f32_32x32x16_bf16 v[48:63], a[92:95], v[156:159], v[48:63]\n"
"ds_read_b128 v[188:191], v5 offset:31616\n"
"v_rcp_f32 v77, v77\n"
"v_fma_f32 v78, v78, v74, v74\n"
"v_fma_f32 v79, v79, v74, v74\n"
"s_waitcnt lgkmcnt(4)\n"
"v_mfma_f32_32x32x16_bf16 v[48:63], a[96:99], v[160:163], v[48:63]\n"
"s_waitcnt vmcnt(11)\n"
"s_barrier\n"
"ds_read_b128 v[128:131], v5 offset:4608\n"
"v_rcp_f32 v78, v78\n"
"v_fma_f32 v80, v80, v74, v74\n"
"v_mfma_f32_32x32x16_bf16 v[48:63], a[100:103], v[164:167], v[48:63]\n"
"ds_read_b128 v[132:135], v5 offset:5120\n"
"buffer_store_dwordx2 v[16:17], v18, s[32:35], s68 offen sc1\n"
"ds_read2st64_b32 v[64:65], v8 offset0:0 offset1:36\n"
"ds_read2st64_b32 v[66:67], v8 offset0:72 offset1:108\n"
"v_mfma_f32_32x32x16_bf16 v[48:63], a[104:107], v[168:171], v[48:63]\n"
"ds_read_b128 v[136:139], v5 offset:13824\n"
"s_add_u32 m0, s64, 5120\n"
"ds_read2st64_b32 v[68:69], v8 offset0:144 offset1:180\n"
"buffer_load_dwordx4 v24, s[32:35], s63 offen offset:0 sc1 lds\n"
"v_mfma_f32_32x32x16_bf16 v[48:63], a[108:111], v[172:175], v[48:63]\n"
"ds_read_b128 v[140:143], v5 offset:14336\n"
"ds_read2st64_b32 v[70:71], v8 offset0:216 offset1:252\n"
"v_xor_b32 v8, v9, v8\n"
"v_rcp_f32 v79, v79\n"
"s_waitcnt lgkmcnt(8)\n"
"v_mfma_f32_32x32x16_bf16 v[48:63], a[112:115], v[176:179], v[48:63]\n"
"ds_read_b128 v[144:147], v5 offset:23040\n"
"v_rcp_f32 v80, v80\n"
"v_fma_f32 v81, v81, v74, v74\n"
"v_fma_f32 v82, v82, v74, v74\n"
"v_fma_f32 v83, v83, v74, v74\n"
"v_mfma_f32_32x32x16_bf16 v[48:63], a[116:119], v[180:183], v[48:63]\n"
"ds_read_b128 v[148:151], v5 offset:23552\n"
"buffer_load_dwordx4 v24, s[32:35], s63 offen offset:1024 sc1 lds\n"
"v_rcp_f32 v81, v81\n"
"v_mfma_f32_32x32x16_bf16 v[48:63], a[120:123], v[184:187], v[48:63]\n"
"ds_read_b128 v[152:155], v5 offset:32256\n"
"v_rcp_f32 v82, v82\n"
"v_rcp_f32 v83, v83\n"
"v_mul_f32 v32, v32, v76\n"
"v_mfma_f32_32x32x16_bf16 v[48:63], a[124:127], v[188:191], v[48:63]\n"
"ds_read_b128 v[156:159], v5 offset:32768\n"
"s_waitcnt lgkmcnt(4)\n"
"v_add_f32 v64, v64, v65\n"
"v_add_f32 v66, v66, v67\n"
"v_add_f32 v68, v68, v69\n"
"v_mfma_f32_32x32x16_bf16 v[48:63], a[128:131], v[128:131], v[48:63]\n"
"ds_read_b128 v[160:163], v5 offset:5760\n"
"buffer_load_dwordx4 v24, s[32:35], s63 offen offset:2048 sc1 lds\n"
"v_add_f32 v70, v70, v71\n"
"v_add_f32 v66, v64, v66\n"
"v_mfma_f32_32x32x16_bf16 v[48:63], a[132:135], v[132:135], v[48:63]\n"
"ds_read_b128 v[164:167], v5 offset:6272\n"
"v_add_f32 v68, v68, v70\n"
"v_add_f32_dpp v64, v66, v66 row_ror:8 row_mask:0xf bank_mask:0x3\n"
"v_mul_f32 v33, v33, v77\n"
"v_mul_f32 v34, v34, v78\n"
"v_add_f32_dpp v64, v68, v68 row_ror:8 row_mask:0xf bank_mask:0xc\n"
"v_mfma_f32_32x32x16_bf16 v[48:63], a[136:139], v[136:139], v[48:63]\n"
"ds_read_b128 v[168:171], v5 offset:14976\n"
"v_add_f32_dpp v64, v64, v64 row_ror:12 row_mask:0xf bank_mask:0xf\n"
"v_mul_f32 v35, v35, v79\n"
"v_mul_f32 v36, v36, v80\n"
"v_add_f32_dpp v64, v64, v64 quad_perm:[2,3,0,1] row_mask:0xf bank_mask:0xf\n"
"v_mul_f32 v37, v37, v81\n"
"v_mfma_f32_32x32x16_bf16 v[48:63], a[140:143], v[140:143], v[48:63]\n"
"ds_read_b128 v[172:175], v5 offset:15488\n"
"v_mul_f32 v64, 0x3a800000, v64\n"
"v_mul_f32 v38, v38, v82\n"
"v_mul_f32 v65, v64, v64\n"
"v_mul_f32 v39, v39, v83\n"
"s_waitcnt lgkmcnt(4)\n"
"v_mfma_f32_32x32x16_bf16 v[48:63], a[144:147], v[144:147], v[48:63]\n"
"ds_read_b128 v[176:179], v5 offset:24192\n"
"buffer_load_dwordx4 v24, s[32:35], s63 offen offset:3072 sc1 lds\n"
"v_sub_f32_dpp v65, v64, v65 quad_perm:[1,1,3,3] row_mask:0xf bank_mask:0xf\n"
"v_cvt_pk_bf16_f32 v84, v32, v33\n"
"v_mfma_f32_32x32x16_bf16 v[48:63], a[148:151], v[148:151], v[48:63]\n"
"ds_read_b128 v[180:183], v5 offset:24704\n"
"v_max_f32 v65, 0, v65\n"
"v_cvt_pk_bf16_f32 v85, v34, v35\n"
"v_cvt_pk_bf16_f32 v65, 0, v65\n"
"v_cvt_pk_bf16_f32 v86, v36, v37\n"
"v_add_f32 v65, 0x3727c5ac, v65\n"
"v_mfma_f32_32x32x16_bf16 v[48:63], a[152:155], v[152:155], v[48:63]\n"
"ds_read_b128 v[184:187], v5 offset:33408\n"
"v_cvt_pk_bf16_f32 v65, 0, v65\n"
"v_cvt_pk_bf16_f32 v87, v38, v39\n"
"v_sqrt_f32 v65, v65\n"
"v_mfma_f32_32x32x16_bf16 v[48:63], a[156:159], v[156:159], v[48:63]\n"
"ds_read_b128 v[188:191], v5 offset:33920\n"
"s_add_u32 m0, s64, 41984\n"
"v_cvt_pk_bf16_f32 v65, v64, v65\n"
"s_waitcnt lgkmcnt(4)\n"
"v_mfma_f32_32x32x16_bf16 v[48:63], a[160:163], v[160:163], v[48:63]\n"
"ds_read_b128 v[128:131], v5 offset:6912\n"
"buffer_load_dwordx4 v25, s[32:35], s63 offen offset:0 sc1 lds\n"
"ds_write_b32 v10, v65 offset:128\n"
"v_mfma_f32_32x32x16_bf16 v[48:63], a[164:167], v[164:167], v[48:63]\n"
"ds_read_b128 v[132:135], v5 offset:7424\n"
"buffer_store_dwordx4 v[84:87], v15, s[32:35], s68 offen sc1\n"
"v_mfma_f32_32x32x16_bf16 v[48:63], a[168:171], v[168:171], v[48:63]\n"
"ds_read_b128 v[136:139], v5 offset:16128\n"
"v_mfma_f32_32x32x16_bf16 v[48:63], a[172:175], v[172:175], v[48:63]\n"
"ds_read_b128 v[140:143], v5 offset:16640\n"
"buffer_load_dwordx4 v25, s[32:35], s63 offen offset:1024 sc1 lds\n"
"s_waitcnt lgkmcnt(5)\n"
"v_mfma_f32_32x32x16_bf16 v[48:63], a[176:179], v[176:179], v[48:63]\n"
"ds_read_b128 v[144:147], v5 offset:25344\n"
"v_mfma_f32_32x32x16_bf16 v[48:63], a[180:183], v[180:183], v[48:63]\n"
"ds_read_b128 v[148:151], v5 offset:25856\n"
"v_mfma_f32_32x32x16_bf16 v[48:63], a[184:187], v[184:187], v[48:63]\n"
"ds_read_b128 v[152:155], v5 offset:34560\n"
"buffer_load_dwordx4 v25, s[32:35], s63 offen offset:2048 sc1 lds\n"
"v_mfma_f32_32x32x16_bf16 v[48:63], a[188:191], v[188:191], v[48:63]\n"
"ds_read_b128 v[156:159], v5 offset:35072\n"
"s_waitcnt lgkmcnt(4)\n"
"v_mfma_f32_32x32x16_bf16 v[48:63], a[192:195], v[128:131], v[48:63]\n"
"ds_read_b128 v[160:163], v5 offset:8064\n"
"v_mfma_f32_32x32x16_bf16 v[48:63], a[196:199], v[132:135], v[48:63]\n"
"ds_read_b128 v[164:167], v5 offset:8576\n"
"v_mfma_f32_32x32x16_bf16 v[48:63], a[200:203], v[136:139], v[48:63]\n"
"ds_read_b128 v[168:171], v5 offset:17280\n"
"buffer_load_dwordx4 v25, s[32:35], s63 offen offset:3072 sc1 lds\n"
"s_add_u32 s63, s63, 1187840\n"
"v_mfma_f32_32x32x16_bf16 v[48:63], a[204:207], v[140:143], v[48:63]\n"
"ds_read_b128 v[172:175], v5 offset:17792\n"
"s_waitcnt lgkmcnt(4)\n"
"v_mfma_f32_32x32x16_bf16 v[48:63], a[208:211], v[144:147], v[48:63]\n"
"ds_read_b128 v[176:179], v5 offset:26496\n"
"v_mfma_f32_32x32x16_bf16 v[48:63], a[212:215], v[148:151], v[48:63]\n"
"ds_read_b128 v[180:183], v5 offset:27008\n"
"v_mfma_f32_32x32x16_bf16 v[48:63], a[216:219], v[152:155], v[48:63]\n"
"ds_read_b128 v[184:187], v5 offset:35712\n"
"v_mfma_f32_32x32x16_bf16 v[48:63], a[220:223], v[156:159], v[48:63]\n"
"ds_read_b128 v[188:191], v5 offset:36224\n"
"s_waitcnt lgkmcnt(4)\n"
"v_mfma_f32_32x32x16_bf16 v[48:63], a[224:227], v[160:163], v[48:63]\n"
"s_waitcnt vmcnt(11)\n"
"s_barrier\n"
"ds_read_b128 v[128:131], v4 offset:0\n"
"v_mfma_f32_32x32x16_bf16 v[48:63], a[228:231], v[164:167], v[48:63]\n"
"ds_read_b128 v[132:135], v4 offset:512\n"
"v_mfma_f32_32x32x16_bf16 v[48:63], a[232:235], v[168:171], v[48:63]\n"
"ds_read_b128 v[136:139], v4 offset:9216\n"
"v_mfma_f32_32x32x16_bf16 v[48:63], a[236:239], v[172:175], v[48:63]\n"
"ds_read_b128 v[140:143], v4 offset:9728\n"
"s_waitcnt lgkmcnt(4)\n"
"v_mfma_f32_32x32x16_bf16 v[48:63], a[240:243], v[176:179], v[48:63]\n"
"ds_read_b128 v[144:147], v4 offset:18432\n"
"v_mfma_f32_32x32x16_bf16 v[48:63], a[244:247], v[180:183], v[48:63]\n"
"ds_read_b128 v[148:151], v4 offset:18944\n"
"v_mfma_f32_32x32x16_bf16 v[48:63], a[248:251], v[184:187], v[48:63]\n"
"ds_read_b128 v[152:155], v4 offset:27648\n"
"s_sub_u32 s62, s62, 1\n"
"v_mfma_f32_32x32x16_bf16 v[48:63], a[252:255], v[188:191], v[48:63]\n"
"ds_read_b128 v[156:159], v4 offset:28160\n"
"s_cbranch_scc0 .L_hl\n"
"s_nop 1\n"
"ds_read_b32 v74, v11 offset:128\n"
"s_add_u32 s68, s68, 1187840\n"
"s_waitcnt lgkmcnt(0)\n"
"v_lshlrev_b32 v72, 16, v74\n"
"v_and_b32 v74, 0xffff0000, v74\n"
"v_rcp_f32 v73, v74\n"
"v_mul_f32 v74, v74, v74\n"
"v_mul_f32 v73, 0xbfb8aa3b, v73\n"
"v_fma_f32 v48, -v72, v112, v48\n"
"v_fma_f32 v49, -v72, v113, v49\n"
"v_fma_f32 v50, -v72, v114, v50\n"
"v_fma_f32 v51, -v72, v115, v51\n"
"v_fma_f32 v52, -v72, v116, v52\n"
"v_fma_f32 v53, -v72, v117, v53\n"
"v_fma_f32 v54, -v72, v118, v54\n"
"v_fma_f32 v55, -v72, v119, v55\n"
"v_fma_f32 v56, -v72, v120, v56\n"
"v_fma_f32 v57, -v72, v121, v57\n"
"v_fma_f32 v58, -v72, v122, v58\n"
"v_fma_f32 v59, -v72, v123, v59\n"
"v_fma_f32 v60, -v72, v124, v60\n"
"v_fma_f32 v61, -v72, v125, v61\n"
"v_fma_f32 v62, -v72, v126, v62\n"
"v_fma_f32 v63, -v72, v127, v63\n"
"v_mul_f32 v76, v48, v73\n"
"v_mul_f32 v77, v49, v73\n"
"v_mul_f32 v78, v50, v73\n"
"v_mul_f32 v79, v51, v73\n"
"v_mul_f32 v80, v52, v73\n"
"v_mul_f32 v81, v53, v73\n"
"v_mul_f32 v82, v54, v73\n"
"v_mul_f32 v83, v55, v73\n"
"v_exp_f32 v76, v76\n"
"v_exp_f32 v77, v77\n"
"v_mul_f32 v48, v48, v56\n"
"v_mul_f32 v49, v49, v57\n"
"v_exp_f32 v78, v78\n"
"v_exp_f32 v79, v79\n"
"v_mul_f32 v50, v50, v58\n"
"v_mul_f32 v51, v51, v59\n"
"v_exp_f32 v80, v80\n"
"v_exp_f32 v81, v81\n"
"v_mul_f32 v52, v52, v60\n"
"v_mul_f32 v53, v53, v61\n"
"v_exp_f32 v82, v82\n"
"v_exp_f32 v83, v83\n"
"v_mul_f32 v54, v54, v62\n"
"v_mul_f32 v55, v55, v63\n"
"v_fma_f32 v76, v76, v74, v74\n"
"v_fma_f32 v77, v77, v74, v74\n"
"v_rcp_f32 v76, v76\n"
"v_rcp_f32 v77, v77\n"
"v_fma_f32 v78, v78, v74, v74\n"
"v_fma_f32 v79, v79, v74, v74\n"
"v_rcp_f32 v78, v78\n"
"v_rcp_f32 v79, v79\n"
"v_fma_f32 v80, v80, v74, v74\n"
"v_fma_f32 v81, v81, v74, v74\n"
"v_rcp_f32 v80, v80\n"
"v_rcp_f32 v81, v81\n"
"v_fma_f32 v82, v82, v74, v74\n"
"v_fma_f32 v83, v83, v74, v74\n"
"v_rcp_f32 v82, v82\n"
"v_rcp_f32 v83, v83\n"
"v_mul_f32 v48, v48, v76\n"
"v_mul_f32 v49, v49, v77\n"
"v_mul_f32 v50, v50, v78\n"
"v_mul_f32 v51, v51, v79\n"
"v_mul_f32 v52, v52, v80\n"
"v_mul_f32 v53, v53, v81\n"
"v_mul_f32 v54, v54, v82\n"
"v_mul_f32 v55, v55, v83\n"
"v_cvt_pk_bf16_f32 v84, v48, v49\n"
"v_cvt_pk_bf16_f32 v85, v50, v51\n"
"v_cvt_pk_bf16_f32 v86, v52, v53\n"
"v_cvt_pk_bf16_f32 v87, v54, v55\n"
"buffer_store_dwordx4 v[84:87], v15, s[32:35], s68 offen sc1\n"
"s_waitcnt vmcnt(4)\n"
"s_barrier\n"
"buffer_store_dwordx2 v[16:17], v18, s[32:35], s68 offen sc1\n"
"s_add_u32 s68, s68, 1187840\n"
"s_waitcnt vmcnt(1)\n"
"s_barrier\n"
"buffer_store_dwordx2 v[16:17], v18, s[32:35], s68 offen sc1\n"
"s_branch .L_end\n"
".L_d:\n"
"s_lshl_b32 s65, s50, 11\n"
"s_mul_i32 s48, s50, 1152\n"
"s_add_u32 s48, s48, 4612096\n"
"s_add_u32 s65, s65, 4833280\n"
"s_cmp_eq_u32 s29, 0\n"
"s_cselect_b32 s48, s65, s48\n"
"s_cselect_b32 s49, 280, 0\n"
"s_lshl2_add_u32 s49, s27, s49\n"
"s_waitcnt lgkmcnt(0)\n"
"s_add_u32 s54, s52, 0x7f4a7c15\n"
"s_addc_u32 s55, s53, 0x9e3779b9\n"
"s_lshl_b32 s67, s49, 5\n"
"s_add_u32 s67, s67, 4898816\n"
"s_sub_u32 s51, s67, s48\n"
"s_add_u32 s92, s32, s51\n"
"s_addc_u32 s93, s33, 0x10000\n"
"s_cmp_eq_u32 s26, 0\n"
"s_cselect_b32 s94, 1, 0\n"
"s_mov_b32 s95, 0x20000\n"
"v_mov_b32 v8, s54\n"
"v_mov_b32 v9, s55\n"
"v_cmp_gt_u32 vcc, 12, v1\n"
"v_mul_u32_u24 v3, 296960, v1\n"
"v_mov_b32 v6, 0x7fff0000\n"
"v_cndmask_b32 v3, v6, v3, vcc\n"
"s_add_u32 s51, s67, 76021760\n"
"buffer_store_dwordx2 v[8:9], v3, s[32:35], s51 offen sc1\n"
"s_sub_u32 s67, s48, 296960\n"
"s_mul_i32 s48, s28, 296960\n"
"s_add_u32 s65, s65, s48\n"
"s_mul_i32 s96, s29, 3670016\n"
"s_add_u32 s36, s18, 0x0\n"
"s_addc_u32 s37, s19, 0x0\n"
"s_and_b32 s37, s37, 0xffff\n"
"s_mov_b32 s38, 0x700000\n"
"s_mov_b32 s39, 0x20000\n"
"s_add_u32 s40, s18, 0xfffffc80\n"
"s_addc_u32 s41, s19, 0xffffffff\n"
"s_and_b32 s41, s41, 0xffff\n"
"s_mov_b32 s42, 0x700380\n"
"s_mov_b32 s43, 0x20000\n"
"s_lshl_b32 s48, s27, 8\n"
"s_add_u32 s96, s96, s48\n"
"v_and_b32 v8, 3, v1\n"
"v_mul_u32_u24 v8, 16, v8\n"
"s_and_b32 s48, s26, 1\n"
"s_lshl_b32 s48, s48, 6\n"
"s_lshr_b32 s49, s26, 1\n"
"s_lshl_b32 s49, s49, 10\n"
"s_add_u32 s48, s48, s49\n"
"v_add_u32 v8, s48, v8\n"
"v_lshrrev_b32 v4, 3, v1\n"
"v_bfe_u32 v5, v1, 4, 1\n"
"s_and_b32 s48, s26, 1\n"
"s_lshl_b32 s48, s48, 1\n"
"v_or_b32 v5, s48, v5\n"
"v_lshlrev_b32 v5, 1, v5\n"
"v_and_b32 v6, 7, v1\n"
"v_xor_b32 v5, v5, v6\n"
"v_mul_u32_u24 v4, 2048, v4\n"
"v_lshl_add_u32 v7, v5, 4, v4\n"
"s_mul_i32 s48, s26, 16384\n"
"s_add_u32 s96, s96, s48\n"
"s_mul_i32 s97, s26, 2048\n"
"s_add_u32 m0, s97, 0\n"
"s_nop 0\n"
"buffer_load_dwordx4 v7, s[36:39], s96 offen offset:0 lds\n"
"buffer_load_dwordx4 v7, s[40:43], s96 offen offset:1024 lds\n"
"s_add_u32 s96, s96, 65536\n"
"s_add_u32 m0, s97, 8192\n"
"s_nop 0\n"
"buffer_load_dwordx4 v7, s[36:39], s96 offen offset:0 lds\n"
"buffer_load_dwordx4 v7, s[40:43], s96 offen offset:1024 lds\n"
"s_add_u32 s96, s96, 65536\n"
"s_add_u32 m0, s97, 16384\n"
"s_nop 0\n"
"buffer_load_dwordx4 v7, s[36:39], s96 offen offset:0 lds\n"
"buffer_load_dwordx4 v7, s[40:43], s96 offen offset:1024 lds\n"
"s_add_u32 s96, s96, 65536\n"
"s_add_u32 m0, s97, 24576\n"
"s_nop 0\n"
"buffer_load_dwordx4 v7, s[36:39], s96 offen offset:0 lds\n"
"buffer_load_dwordx4 v7, s[40:43], s96 offen offset:1024 lds\n"
"s_add_u32 s96, s96, 65536\n"
"s_add_u32 m0, s97, 32768\n"
"s_nop 0\n"
"buffer_load_dwordx4 v7, s[36:39], s96 offen offset:0 lds\n"
"buffer_load_dwordx4 v7, s[40:43], s96 offen offset:1024 lds\n"
"s_add_u32 s96, s96, 65536\n"
"s_add_u32 m0, s97, 40960\n"
"s_nop 0\n"
"buffer_load_dwordx4 v7, s[36:39], s96 offen offset:0 lds\n"
"buffer_load_dwordx4 v7, s[40:43], s96 offen offset:1024 lds\n"
"s_add_u32 s96, s96, 65536\n"
"s_add_u32 m0, s97, 49152\n"
"s_nop 0\n"
"buffer_load_dwordx4 v7, s[36:39], s96 offen offset:0 lds\n"
"buffer_load_dwordx4 v7, s[40:43], s96 offen offset:1024 lds\n"
"s_add_u32 s96, s96, 65536\n"
"s_add_u32 m0, s97, 57344\n"
"s_nop 0\n"
"buffer_load_dwordx4 v7, s[36:39], s96 offen offset:0 lds\n"
"buffer_load_dwordx4 v7, s[40:43], s96 offen offset:1024 lds\n"
"s_add_u32 s96, s96, 65536\n"
"s_add_u32 m0, s97, 65536\n"
"s_nop 0\n"
"buffer_load_dwordx4 v7, s[36:39], s96 offen offset:0 lds\n"
"buffer_load_dwordx4 v7, s[40:43], s96 offen offset:1024 lds\n"
"s_add_u32 s96, s96, 65536\n"
"s_add_u32 m0, s97, 73728\n"
"s_nop 0\n"
"buffer_load_dwordx4 v7, s[36:39], s96 offen offset:0 lds\n"
"buffer_load_dwordx4 v7, s[40:43], s96 offen offset:1024 lds\n"
"s_add_u32 s96, s96, 65536\n"
"s_add_u32 m0, s97, 81920\n"
"s_nop 0\n"
"buffer_load_dwordx4 v7, s[36:39], s96 offen offset:0 lds\n"
"buffer_load_dwordx4 v7, s[40:43], s96 offen offset:1024 lds\n"
"s_add_u32 s96, s96, 65536\n"
"s_add_u32 m0, s97, 90112\n"
"s_nop 0\n"
"buffer_load_dwordx4 v7, s[36:39], s96 offen offset:0 lds\n"
"buffer_load_dwordx4 v7, s[40:43], s96 offen offset:1024 lds\n"
"s_add_u32 s96, s96, 65536\n"
"s_add_u32 m0, s97, 98304\n"
"s_nop 0\n"
"buffer_load_dwordx4 v7, s[36:39], s96 offen offset:0 lds\n"
"buffer_load_dwordx4 v7, s[40:43], s96 offen offset:1024 lds\n"
"s_add_u32 s96, s96, 65536\n"
"s_add_u32 m0, s97, 106496\n"
"s_nop 0\n"
"buffer_load_dwordx4 v7, s[36:39], s96 offen offset:0 lds\n"
"buffer_load_dwordx4 v7, s[40:43], s96 offen offset:1024 lds\n"
"s_add_u32 s96, s96, 65536\n"
"v_lshrrev_b32 v5, 4, v1\n"
"v_bfe_u32 v4, v1, 3, 1\n"
"v_and_b32 v6, 1, v5\n"
"v_lshl_add_u32 v4, v6, 1, v4\n"
"v_lshlrev_b32 v4, 5, v4\n"
"v_mul_u32_u24 v5, 2048, v5\n"
"v_bfe_u32 v6, v1, 2, 2\n"
"v_lshl_add_u32 v5, v6, 7, v5\n"
"v_xor_b32 v8, v4, v8\n"
"v_add_u32 v8, v5, v8\n"
"v_add_u32 v9, 65536, v8\n"
"s_waitcnt vmcnt(24)\n"
"s_barrier\n"
"s_add_u32 m0, s97, 114688\n"
"s_nop 0\n"
"buffer_load_dwordx4 v7, s[36:39], s96 offen offset:0 lds\n"
"buffer_load_dwordx4 v7, s[40:43], s96 offen offset:1024 lds\n"
"s_add_u32 s96, s96, 65536\n"
"s_add_u32 m0, s97, 122880\n"
"s_nop 0\n"
"buffer_load_dwordx4 v7, s[36:39], s96 offen offset:0 lds\n"
"buffer_load_dwordx4 v7, s[40:43], s96 offen offset:1024 lds\n"
"s_add_u32 s96, s96, 65536\n"
"ds_read_b64_tr_b16 a[0:1], v8 offset:0\n"
"ds_read_b64_tr_b16 a[2:3], v8 offset:512\n"
"ds_read_b64_tr_b16 a[4:5], v8 offset:8\n"
"ds_read_b64_tr_b16 a[6:7], v8 offset:520\n"
"ds_read_b64_tr_b16 a[8:9], v8 offset:8192\n"
"ds_read_b64_tr_b16 a[10:11], v8 offset:8704\n"
"ds_read_b64_tr_b16 a[12:13], v8 offset:8200\n"
"ds_read_b64_tr_b16 a[14:15], v8 offset:8712\n"
"s_waitcnt lgkmcnt(0)\n"
"s_waitcnt vmcnt(24)\n"
"s_barrier\n"
"s_add_u32 m0, s97, 0\n"
"s_nop 0\n"
"buffer_load_dwordx4 v7, s[36:39], s96 offen offset:0 lds\n"
"buffer_load_dwordx4 v7, s[40:43], s96 offen offset:1024 lds\n"
"s_add_u32 s96, s96, 65536\n"
"s_add_u32 m0, s97, 8192\n"
"s_nop 0\n"
"buffer_load_dwordx4 v7, s[36:39], s96 offen offset:0 lds\n"
"buffer_load_dwordx4 v7, s[40:43], s96 offen offset:1024 lds\n"
"s_add_u32 s96, s96, 65536\n"
"ds_read_b64_tr_b16 a[16:17], v8 offset:16384\n"
"ds_read_b64_tr_b16 a[18:19], v8 offset:16896\n"
"ds_read_b64_tr_b16 a[20:21], v8 offset:16392\n"
"ds_read_b64_tr_b16 a[22:23], v8 offset:16904\n"
"ds_read_b64_tr_b16 a[24:25], v8 offset:24576\n"
"ds_read_b64_tr_b16 a[26:27], v8 offset:25088\n"
"ds_read_b64_tr_b16 a[28:29], v8 offset:24584\n"
"ds_read_b64_tr_b16 a[30:31], v8 offset:25096\n"
"s_waitcnt lgkmcnt(0)\n"
"s_waitcnt vmcnt(24)\n"
"s_barrier\n"
"s_add_u32 m0, s97, 16384\n"
"s_nop 0\n"
"buffer_load_dwordx4 v7, s[36:39], s96 offen offset:0 lds\n"
"buffer_load_dwordx4 v7, s[40:43], s96 offen offset:1024 lds\n"
"s_add_u32 s96, s96, 65536\n"
"s_add_u32 m0, s97, 24576\n"
"s_nop 0\n"
"buffer_load_dwordx4 v7, s[36:39], s96 offen offset:0 lds\n"
"buffer_load_dwordx4 v7, s[40:43], s96 offen offset:1024 lds\n"
"s_add_u32 s96, s96, 65536\n"
"ds_read_b64_tr_b16 a[32:33], v8 offset:32768\n"
"ds_read_b64_tr_b16 a[34:35], v8 offset:33280\n"
"ds_read_b64_tr_b16 a[36:37], v8 offset:32776\n"
"ds_read_b64_tr_b16 a[38:39], v8 offset:33288\n"
"ds_read_b64_tr_b16 a[40:41], v8 offset:40960\n"
"ds_read_b64_tr_b16 a[42:43], v8 offset:41472\n"
"ds_read_b64_tr_b16 a[44:45], v8 offset:40968\n"
"ds_read_b64_tr_b16 a[46:47], v8 offset:41480\n"
"s_waitcnt lgkmcnt(0)\n"
"s_waitcnt vmcnt(24)\n"
"s_barrier\n"
"s_add_u32 m0, s97, 32768\n"
"s_nop 0\n"
"buffer_load_dwordx4 v7, s[36:39], s96 offen offset:0 lds\n"
"buffer_load_dwordx4 v7, s[40:43], s96 offen offset:1024 lds\n"
"s_add_u32 s96, s96, 65536\n"
"s_add_u32 m0, s97, 40960\n"
"s_nop 0\n"
"buffer_load_dwordx4 v7, s[36:39], s96 offen offset:0 lds\n"
"buffer_load_dwordx4 v7, s[40:43], s96 offen offset:1024 lds\n"
"s_add_u32 s96, s96, 65536\n"
"ds_read_b64_tr_b16 a[48:49], v8 offset:49152\n"
"ds_read_b64_tr_b16 a[50:51], v8 offset:49664\n"
"ds_read_b64_tr_b16 a[52:53], v8 offset:49160\n"
"ds_read_b64_tr_b16 a[54:55], v8 offset:49672\n"
"ds_read_b64_tr_b16 a[56:57], v8 offset:57344\n"
"ds_read_b64_tr_b16 a[58:59], v8 offset:57856\n"
"ds_read_b64_tr_b16 a[60:61], v8 offset:57352\n"
"ds_read_b64_tr_b16 a[62:63], v8 offset:57864\n"
"s_waitcnt lgkmcnt(0)\n"
"s_waitcnt vmcnt(24)\n"
"s_barrier\n"
"s_add_u32 m0, s97, 49152\n"
"s_nop 0\n"
"buffer_load_dwordx4 v7, s[36:39], s96 offen offset:0 lds\n"
"buffer_load_dwordx4 v7, s[40:43], s96 offen offset:1024 lds\n"
"s_add_u32 s96, s96, 65536\n"
"s_add_u32 m0, s97, 57344\n"
"s_nop 0\n"
"buffer_load_dwordx4 v7, s[36:39], s96 offen offset:0 lds\n"
"buffer_load_dwordx4 v7, s[40:43], s96 offen offset:1024 lds\n"
"s_add_u32 s96, s96, 65536\n"
"ds_read_b64_tr_b16 a[64:65], v9 offset:0\n"
"ds_read_b64_tr_b16 a[66:67], v9 offset:512\n"
"ds_read_b64_tr_b16 a[68:69], v9 offset:8\n"
"ds_read_b64_tr_b16 a[70:71], v9 offset:520\n"
"ds_read_b64_tr_b16 a[72:73], v9 offset:8192\n"
"ds_read_b64_tr_b16 a[74:75], v9 offset:8704\n"
"ds_read_b64_tr_b16 a[76:77], v9 offset:8200\n"
"ds_read_b64_tr_b16 a[78:79], v9 offset:8712\n"
"s_waitcnt lgkmcnt(0)\n"
"s_waitcnt vmcnt(24)\n"
"s_barrier\n"
"s_add_u32 m0, s97, 65536\n"
"s_nop 0\n"
"buffer_load_dwordx4 v7, s[36:39], s96 offen offset:0 lds\n"
"buffer_load_dwordx4 v7, s[40:43], s96 offen offset:1024 lds\n"
"s_add_u32 s96, s96, 65536\n"
"s_add_u32 m0, s97, 73728\n"
"s_nop 0\n"
"buffer_load_dwordx4 v7, s[36:39], s96 offen offset:0 lds\n"
"buffer_load_dwordx4 v7, s[40:43], s96 offen offset:1024 lds\n"
"s_add_u32 s96, s96, 65536\n"
"ds_read_b64_tr_b16 a[80:81], v9 offset:16384\n"
"ds_read_b64_tr_b16 a[82:83], v9 offset:16896\n"
"ds_read_b64_tr_b16 a[84:85], v9 offset:16392\n"
"ds_read_b64_tr_b16 a[86:87], v9 offset:16904\n"
"ds_read_b64_tr_b16 a[88:89], v9 offset:24576\n"
"ds_read_b64_tr_b16 a[90:91], v9 offset:25088\n"
"ds_read_b64_tr_b16 a[92:93], v9 offset:24584\n"
"ds_read_b64_tr_b16 a[94:95], v9 offset:25096\n"
"s_waitcnt lgkmcnt(0)\n"
"s_waitcnt vmcnt(24)\n"
"s_barrier\n"
"s_add_u32 m0, s97, 81920\n"
"s_nop 0\n"
"buffer_load_dwordx4 v7, s[36:39], s96 offen offset:0 lds\n"
"buffer_load_dwordx4 v7, s[40:43], s96 offen offset:1024 lds\n"
"s_add_u32 s96, s96, 65536\n"
"s_add_u32 m0, s97, 90112\n"
"s_nop 0\n"
"buffer_load_dwordx4 v7, s[36:39], s96 offen offset:0 lds\n"
"buffer_load_dwordx4 v7, s[40:43], s96 offen offset:1024 lds\n"
"s_add_u32 s96, s96, 65536\n"
"ds_read_b64_tr_b16 a[96:97], v9 offset:32768\n"
"ds_read_b64_tr_b16 a[98:99], v9 offset:33280\n"
"ds_read_b64_tr_b16 a[100:101], v9 offset:32776\n"
"ds_read_b64_tr_b16 a[102:103], v9 offset:33288\n"
"ds_read_b64_tr_b16 a[104:105], v9 offset:40960\n"
"ds_read_b64_tr_b16 a[106:107], v9 offset:41472\n"
"ds_read_b64_tr_b16 a[108:109], v9 offset:40968\n"
"ds_read_b64_tr_b16 a[110:111], v9 offset:41480\n"
"s_waitcnt lgkmcnt(0)\n"
"s_waitcnt vmcnt(24)\n"
"s_barrier\n"
"s_add_u32 m0, s97, 98304\n"
"s_nop 0\n"
"buffer_load_dwordx4 v7, s[36:39], s96 offen offset:0 lds\n"
"buffer_load_dwordx4 v7, s[40:43], s96 offen offset:1024 lds\n"
"s_add_u32 s96, s96, 65536\n"
"s_add_u32 m0, s97, 106496\n"
"s_nop 0\n"
"buffer_load_dwordx4 v7, s[36:39], s96 offen offset:0 lds\n"
"buffer_load_dwordx4 v7, s[40:43], s96 offen offset:1024 lds\n"
"s_add_u32 s96, s96, 65536\n"
"ds_read_b64_tr_b16 a[112:113], v9 offset:49152\n"
"ds_read_b64_tr_b16 a[114:115], v9 offset:49664\n"
"ds_read_b64_tr_b16 a[116:117], v9 offset:49160\n"
"ds_read_b64_tr_b16 a[118:119], v9 offset:49672\n"
"ds_read_b64_tr_b16 a[120:121], v9 offset:57344\n"
"ds_read_b64_tr_b16 a[122:123], v9 offset:57856\n"
"ds_read_b64_tr_b16 a[124:125], v9 offset:57352\n"
"ds_read_b64_tr_b16 a[126:127], v9 offset:57864\n"
"s_waitcnt lgkmcnt(0)\n"
"s_waitcnt vmcnt(24)\n"
"s_barrier\n"
"s_add_u32 m0, s97, 114688\n"
"s_nop 0\n"
"buffer_load_dwordx4 v7, s[36:39], s96 offen offset:0 lds\n"
"buffer_load_dwordx4 v7, s[40:43], s96 offen offset:1024 lds\n"
"s_add_u32 s96, s96, 65536\n"
"s_add_u32 m0, s97, 122880\n"
"s_nop 0\n"
"buffer_load_dwordx4 v7, s[36:39], s96 offen offset:0 lds\n"
"buffer_load_dwordx4 v7, s[40:43], s96 offen offset:1024 lds\n"
"s_add_u32 s96, s96, 65536\n"
"ds_read_b64_tr_b16 a[128:129], v8 offset:0\n"
"ds_read_b64_tr_b16 a[130:131], v8 offset:512\n"
"ds_read_b64_tr_b16 a[132:133], v8 offset:8\n"
"ds_read_b64_tr_b16 a[134:135], v8 offset:520\n"
"ds_read_b64_tr_b16 a[136:137], v8 offset:8192\n"
"ds_read_b64_tr_b16 a[138:139], v8 offset:8704\n"
"ds_read_b64_tr_b16 a[140:141], v8 offset:8200\n"
"ds_read_b64_tr_b16 a[142:143], v8 offset:8712\n"
"s_waitcnt lgkmcnt(0)\n"
"s_waitcnt vmcnt(24)\n"
"s_barrier\n"
"s_add_u32 m0, s97, 0\n"
"s_nop 0\n"
"buffer_load_dwordx4 v7, s[36:39], s96 offen offset:0 lds\n"
"buffer_load_dwordx4 v7, s[40:43], s96 offen offset:1024 lds\n"
"s_add_u32 s96, s96, 65536\n"
"s_add_u32 m0, s97, 8192\n"
"s_nop 0\n"
"buffer_load_dwordx4 v7, s[36:39], s96 offen offset:0 lds\n"
"buffer_load_dwordx4 v7, s[40:43], s96 offen offset:1024 lds\n"
"s_add_u32 s96, s96, 65536\n"
"ds_read_b64_tr_b16 a[144:145], v8 offset:16384\n"
"ds_read_b64_tr_b16 a[146:147], v8 offset:16896\n"
"ds_read_b64_tr_b16 a[148:149], v8 offset:16392\n"
"ds_read_b64_tr_b16 a[150:151], v8 offset:16904\n"
"ds_read_b64_tr_b16 a[152:153], v8 offset:24576\n"
"ds_read_b64_tr_b16 a[154:155], v8 offset:25088\n"
"ds_read_b64_tr_b16 a[156:157], v8 offset:24584\n"
"ds_read_b64_tr_b16 a[158:159], v8 offset:25096\n"
"s_waitcnt lgkmcnt(0)\n"
"s_waitcnt vmcnt(24)\n"
"s_barrier\n"
"s_add_u32 m0, s97, 16384\n"
"s_nop 0\n"
"buffer_load_dwordx4 v7, s[36:39], s96 offen offset:0 lds\n"
"buffer_load_dwordx4 v7, s[40:43], s96 offen offset:1024 lds\n"
"s_add_u32 s96, s96, 65536\n"
"s_add_u32 m0, s97, 24576\n"
"s_nop 0\n"
"buffer_load_dwordx4 v7, s[36:39], s96 offen offset:0 lds\n"
"buffer_load_dwordx4 v7, s[40:43], s96 offen offset:1024 lds\n"
"s_add_u32 s96, s96, 65536\n"
"ds_read_b64_tr_b16 a[160:161], v8 offset:32768\n"
"ds_read_b64_tr_b16 a[162:163], v8 offset:33280\n"
"ds_read_b64_tr_b16 a[164:165], v8 offset:32776\n"
"ds_read_b64_tr_b16 a[166:167], v8 offset:33288\n"
"ds_read_b64_tr_b16 a[168:169], v8 offset:40960\n"
"ds_read_b64_tr_b16 a[170:171], v8 offset:41472\n"
"ds_read_b64_tr_b16 a[172:173], v8 offset:40968\n"
"ds_read_b64_tr_b16 a[174:175], v8 offset:41480\n"
"s_waitcnt lgkmcnt(0)\n"
"s_waitcnt vmcnt(24)\n"
"s_barrier\n"
"s_add_u32 m0, s97, 32768\n"
"s_nop 0\n"
"buffer_load_dwordx4 v7, s[36:39], s96 offen offset:0 lds\n"
"buffer_load_dwordx4 v7, s[40:43], s96 offen offset:1024 lds\n"
"s_add_u32 s96, s96, 65536\n"
"s_add_u32 m0, s97, 40960\n"
"s_nop 0\n"
"buffer_load_dwordx4 v7, s[36:39], s96 offen offset:0 lds\n"
"buffer_load_dwordx4 v7, s[40:43], s96 offen offset:1024 lds\n"
"s_add_u32 s96, s96, 65536\n"
"ds_read_b64_tr_b16 a[176:177], v8 offset:49152\n"
"ds_read_b64_tr_b16 a[178:179], v8 offset:49664\n"
"ds_read_b64_tr_b16 a[180:181], v8 offset:49160\n"
"ds_read_b64_tr_b16 a[182:183], v8 offset:49672\n"
"ds_read_b64_tr_b16 a[184:185], v8 offset:57344\n"
"ds_read_b64_tr_b16 a[186:187], v8 offset:57856\n"
"ds_read_b64_tr_b16 a[188:189], v8 offset:57352\n"
"ds_read_b64_tr_b16 a[190:191], v8 offset:57864\n"
"s_waitcnt lgkmcnt(0)\n"
"s_waitcnt vmcnt(24)\n"
"s_barrier\n"
"s_add_u32 m0, s97, 49152\n"
"s_nop 0\n"
"buffer_load_dwordx4 v7, s[36:39], s96 offen offset:0 lds\n"
"buffer_load_dwordx4 v7, s[40:43], s96 offen offset:1024 lds\n"
"s_add_u32 s96, s96, 65536\n"
"s_add_u32 m0, s97, 57344\n"
"s_nop 0\n"
"buffer_load_dwordx4 v7, s[36:39], s96 offen offset:0 lds\n"
"buffer_load_dwordx4 v7, s[40:43], s96 offen offset:1024 lds\n"
"s_add_u32 s96, s96, 65536\n"
"ds_read_b64_tr_b16 a[192:193], v9 offset:0\n"
"ds_read_b64_tr_b16 a[194:195], v9 offset:512\n"
"ds_read_b64_tr_b16 a[196:197], v9 offset:8\n"
"ds_read_b64_tr_b16 a[198:199], v9 offset:520\n"
"ds_read_b64_tr_b16 a[200:201], v9 offset:8192\n"
"ds_read_b64_tr_b16 a[202:203], v9 offset:8704\n"
"ds_read_b64_tr_b16 a[204:205], v9 offset:8200\n"
"ds_read_b64_tr_b16 a[206:207], v9 offset:8712\n"
"s_waitcnt lgkmcnt(0)\n"
"s_waitcnt vmcnt(24)\n"
"s_barrier\n"
"s_add_u32 m0, s97, 65536\n"
"s_nop 0\n"
"buffer_load_dwordx4 v7, s[36:39], s96 offen offset:0 lds\n"
"buffer_load_dwordx4 v7, s[40:43], s96 offen offset:1024 lds\n"
"s_add_u32 s96, s96, 65536\n"
"s_add_u32 m0, s97, 73728\n"
"s_nop 0\n"
"buffer_load_dwordx4 v7, s[36:39], s96 offen offset:0 lds\n"
"buffer_load_dwordx4 v7, s[40:43], s96 offen offset:1024 lds\n"
"s_add_u32 s96, s96, 65536\n"
"ds_read_b64_tr_b16 a[208:209], v9 offset:16384\n"
"ds_read_b64_tr_b16 a[210:211], v9 offset:16896\n"
"ds_read_b64_tr_b16 a[212:213], v9 offset:16392\n"
"ds_read_b64_tr_b16 a[214:215], v9 offset:16904\n"
"ds_read_b64_tr_b16 a[216:217], v9 offset:24576\n"
"ds_read_b64_tr_b16 a[218:219], v9 offset:25088\n"
"ds_read_b64_tr_b16 a[220:221], v9 offset:24584\n"
"ds_read_b64_tr_b16 a[222:223], v9 offset:25096\n"
"s_waitcnt lgkmcnt(0)\n"
"s_waitcnt vmcnt(24)\n"
"s_barrier\n"
"s_add_u32 m0, s97, 81920\n"
"s_nop 0\n"
"buffer_load_dwordx4 v7, s[36:39], s96 offen offset:0 lds\n"
"buffer_load_dwordx4 v7, s[40:43], s96 offen offset:1024 lds\n"
"s_add_u32 s96, s96, 65536\n"
"s_add_u32 m0, s97, 90112\n"
"s_nop 0\n"
"buffer_load_dwordx4 v7, s[36:39], s96 offen offset:0 lds\n"
"buffer_load_dwordx4 v7, s[40:43], s96 offen offset:1024 lds\n"
"s_add_u32 s96, s96, 65536\n"
"ds_read_b64_tr_b16 a[224:225], v9 offset:32768\n"
"ds_read_b64_tr_b16 a[226:227], v9 offset:33280\n"
"ds_read_b64_tr_b16 a[228:229], v9 offset:32776\n"
"ds_read_b64_tr_b16 a[230:231], v9 offset:33288\n"
"ds_read_b64_tr_b16 a[232:233], v9 offset:40960\n"
"ds_read_b64_tr_b16 a[234:235], v9 offset:41472\n"
"ds_read_b64_tr_b16 a[236:237], v9 offset:40968\n"
"ds_read_b64_tr_b16 a[238:239], v9 offset:41480\n"
"s_waitcnt lgkmcnt(0)\n"
"s_waitcnt vmcnt(24)\n"
"s_barrier\n"
"s_add_u32 m0, s97, 98304\n"
"s_nop 0\n"
"buffer_load_dwordx4 v7, s[36:39], s96 offen offset:0 lds\n"
"buffer_load_dwordx4 v7, s[40:43], s96 offen offset:1024 lds\n"
"s_add_u32 s96, s96, 65536\n"
"s_add_u32 m0, s97, 106496\n"
"s_nop 0\n"
"buffer_load_dwordx4 v7, s[36:39], s96 offen offset:0 lds\n"
"buffer_load_dwordx4 v7, s[40:43], s96 offen offset:1024 lds\n"
"s_add_u32 s96, s96, 65536\n"
"ds_read_b64_tr_b16 a[240:241], v9 offset:49152\n"
"ds_read_b64_tr_b16 a[242:243], v9 offset:49664\n"
"ds_read_b64_tr_b16 a[244:245], v9 offset:49160\n"
"ds_read_b64_tr_b16 a[246:247], v9 offset:49672\n"
"ds_read_b64_tr_b16 a[248:249], v9 offset:57344\n"
"ds_read_b64_tr_b16 a[250:251], v9 offset:57856\n"
"ds_read_b64_tr_b16 a[252:253], v9 offset:57352\n"
"ds_read_b64_tr_b16 a[254:255], v9 offset:57864\n"
"s_waitcnt lgkmcnt(0)\n"
"s_waitcnt vmcnt(24)\n"
"s_barrier\n"
"s_add_u32 m0, s97, 114688\n"
"s_nop 0\n"
"buffer_load_dwordx4 v7, s[36:39], s96 offen offset:0 lds\n"
"buffer_load_dwordx4 v7, s[40:43], s96 offen offset:1024 lds\n"
"s_add_u32 s96, s96, 65536\n"
"s_add_u32 m0, s97, 122880\n"
"s_nop 0\n"
"buffer_load_dwordx4 v7, s[36:39], s96 offen offset:0 lds\n"
"buffer_load_dwordx4 v7, s[40:43], s96 offen offset:1024 lds\n"
"s_add_u32 s96, s96, 65536\n"
"ds_read_b64_tr_b16 v[64:65], v8 offset:0\n"
"ds_read_b64_tr_b16 v[66:67], v8 offset:512\n"
"ds_read_b64_tr_b16 v[68:69], v8 offset:8\n"
"ds_read_b64_tr_b16 v[70:71], v8 offset:520\n"
"ds_read_b64_tr_b16 v[72:73], v8 offset:8192\n"
"ds_read_b64_tr_b16 v[74:75], v8 offset:8704\n"
"ds_read_b64_tr_b16 v[76:77], v8 offset:8200\n"
"ds_read_b64_tr_b16 v[78:79], v8 offset:8712\n"
"s_waitcnt lgkmcnt(0)\n"
"s_waitcnt vmcnt(24)\n"
"s_barrier\n"
"s_add_u32 m0, s97, 0\n"
"s_nop 0\n"
"buffer_load_dwordx4 v7, s[36:39], s96 offen offset:0 lds\n"
"buffer_load_dwordx4 v7, s[40:43], s96 offen offset:1024 lds\n"
"s_add_u32 s96, s96, 65536\n"
"s_add_u32 m0, s97, 8192\n"
"s_nop 0\n"
"buffer_load_dwordx4 v7, s[36:39], s96 offen offset:0 lds\n"
"buffer_load_dwordx4 v7, s[40:43], s96 offen offset:1024 lds\n"
"s_add_u32 s96, s96, 65536\n"
"ds_read_b64_tr_b16 v[80:81], v8 offset:16384\n"
"ds_read_b64_tr_b16 v[82:83], v8 offset:16896\n"
"ds_read_b64_tr_b16 v[84:85], v8 offset:16392\n"
"ds_read_b64_tr_b16 v[86:87], v8 offset:16904\n"
"ds_read_b64_tr_b16 v[88:89], v8 offset:24576\n"
"ds_read_b64_tr_b16 v[90:91], v8 offset:25088\n"
"ds_read_b64_tr_b16 v[92:93], v8 offset:24584\n"
"ds_read_b64_tr_b16 v[94:95], v8 offset:25096\n"
"s_waitcnt lgkmcnt(0)\n"
"s_waitcnt vmcnt(24)\n"
"s_barrier\n"
"s_add_u32 m0, s97, 16384\n"
"s_nop 0\n"
"buffer_load_dwordx4 v7, s[36:39], s96 offen offset:0 lds\n"
"buffer_load_dwordx4 v7, s[40:43], s96 offen offset:1024 lds\n"
"s_add_u32 s96, s96, 65536\n"
"s_add_u32 m0, s97, 24576\n"
"s_nop 0\n"
"buffer_load_dwordx4 v7, s[36:39], s96 offen offset:0 lds\n"
"buffer_load_dwordx4 v7, s[40:43], s96 offen offset:1024 lds\n"
"s_add_u32 s96, s96, 65536\n"
"ds_read_b64_tr_b16 v[96:97], v8 offset:32768\n"
"ds_read_b64_tr_b16 v[98:99], v8 offset:33280\n"
"ds_read_b64_tr_b16 v[100:101], v8 offset:32776\n"
"ds_read_b64_tr_b16 v[102:103], v8 offset:33288\n"
"ds_read_b64_tr_b16 v[104:105], v8 offset:40960\n"
"ds_read_b64_tr_b16 v[106:107], v8 offset:41472\n"
"ds_read_b64_tr_b16 v[108:109], v8 offset:40968\n"
"ds_read_b64_tr_b16 v[110:111], v8 offset:41480\n"
"s_waitcnt lgkmcnt(0)\n"
"s_waitcnt vmcnt(24)\n"
"s_barrier\n"
"s_add_u32 m0, s97, 32768\n"
"s_nop 0\n"
"buffer_load_dwordx4 v7, s[36:39], s96 offen offset:0 lds\n"
"buffer_load_dwordx4 v7, s[40:43], s96 offen offset:1024 lds\n"
"s_add_u32 s96, s96, 65536\n"
"s_add_u32 m0, s97, 40960\n"
"s_nop 0\n"
"buffer_load_dwordx4 v7, s[36:39], s96 offen offset:0 lds\n"
"buffer_load_dwordx4 v7, s[40:43], s96 offen offset:1024 lds\n"
"s_add_u32 s96, s96, 65536\n"
"ds_read_b64_tr_b16 v[112:113], v8 offset:49152\n"
"ds_read_b64_tr_b16 v[114:115], v8 offset:49664\n"
"ds_read_b64_tr_b16 v[116:117], v8 offset:49160\n"
"ds_read_b64_tr_b16 v[118:119], v8 offset:49672\n"
"ds_read_b64_tr_b16 v[120:121], v8 offset:57344\n"
"ds_read_b64_tr_b16 v[122:123], v8 offset:57856\n"
"ds_read_b64_tr_b16 v[124:125], v8 offset:57352\n"
"ds_read_b64_tr_b16 v[126:127], v8 offset:57864\n"
"s_waitcnt lgkmcnt(0)\n"
"s_waitcnt vmcnt(24)\n"
"s_barrier\n"
"s_add_u32 m0, s97, 49152\n"
"s_nop 0\n"
"buffer_load_dwordx4 v7, s[36:39], s96 offen offset:0 lds\n"
"buffer_load_dwordx4 v7, s[40:43], s96 offen offset:1024 lds\n"
"s_add_u32 s96, s96, 65536\n"
"s_add_u32 m0, s97, 57344\n"
"s_nop 0\n"
"buffer_load_dwordx4 v7, s[36:39], s96 offen offset:0 lds\n"
"buffer_load_dwordx4 v7, s[40:43], s96 offen offset:1024 lds\n"
"s_add_u32 s96, s96, 65536\n"
"ds_read_b64_tr_b16 v[128:129], v9 offset:0\n"
"ds_read_b64_tr_b16 v[130:131], v9 offset:512\n"
"ds_read_b64_tr_b16 v[132:133], v9 offset:8\n"
"ds_read_b64_tr_b16 v[134:135], v9 offset:520\n"
"ds_read_b64_tr_b16 v[136:137], v9 offset:8192\n"
"ds_read_b64_tr_b16 v[138:139], v9 offset:8704\n"
"ds_read_b64_tr_b16 v[140:141], v9 offset:8200\n"
"ds_read_b64_tr_b16 v[142:143], v9 offset:8712\n"
"s_waitcnt lgkmcnt(0)\n"
"s_waitcnt vmcnt(24)\n"
"s_barrier\n"
"ds_read_b64_tr_b16 v[144:145], v9 offset:16384\n"
"ds_read_b64_tr_b16 v[146:147], v9 offset:16896\n"
"ds_read_b64_tr_b16 v[148:149], v9 offset:16392\n"
"ds_read_b64_tr_b16 v[150:151], v9 offset:16904\n"
"ds_read_b64_tr_b16 v[152:153], v9 offset:24576\n"
"ds_read_b64_tr_b16 v[154:155], v9 offset:25088\n"
"ds_read_b64_tr_b16 v[156:157], v9 offset:24584\n"
"ds_read_b64_tr_b16 v[158:159], v9 offset:25096\n"
"s_waitcnt lgkmcnt(0)\n"
"s_waitcnt vmcnt(20)\n"
"s_barrier\n"
"ds_read_b64_tr_b16 v[160:161], v9 offset:32768\n"
"ds_read_b64_tr_b16 v[162:163], v9 offset:33280\n"
"ds_read_b64_tr_b16 v[164:165], v9 offset:32776\n"
"ds_read_b64_tr_b16 v[166:167], v9 offset:33288\n"
"ds_read_b64_tr_b16 v[168:169], v9 offset:40960\n"
"ds_read_b64_tr_b16 v[170:171], v9 offset:41472\n"
"ds_read_b64_tr_b16 v[172:173], v9 offset:40968\n"
"ds_read_b64_tr_b16 v[174:175], v9 offset:41480\n"
"s_waitcnt lgkmcnt(0)\n"
"s_waitcnt vmcnt(16)\n"
"s_barrier\n"
"ds_read_b64_tr_b16 v[176:177], v9 offset:49152\n"
"ds_read_b64_tr_b16 v[178:179], v9 offset:49664\n"
"ds_read_b64_tr_b16 v[180:181], v9 offset:49160\n"
"ds_read_b64_tr_b16 v[182:183], v9 offset:49672\n"
"ds_read_b64_tr_b16 v[184:185], v9 offset:57344\n"
"ds_read_b64_tr_b16 v[186:187], v9 offset:57856\n"
"ds_read_b64_tr_b16 v[188:189], v9 offset:57352\n"
"ds_read_b64_tr_b16 v[190:191], v9 offset:57864\n"
"s_waitcnt lgkmcnt(0)\n"
"s_waitcnt vmcnt(12)\n"
"s_barrier\n"
"ds_read_b64_tr_b16 v[192:193], v8 offset:0\n"
"ds_read_b64_tr_b16 v[194:195], v8 offset:512\n"
"ds_read_b64_tr_b16 v[196:197], v8 offset:8\n"
"ds_read_b64_tr_b16 v[198:199], v8 offset:520\n"
"ds_read_b64_tr_b16 v[200:201], v8 offset:8192\n"
"ds_read_b64_tr_b16 v[202:203], v8 offset:8704\n"
"ds_read_b64_tr_b16 v[204:205], v8 offset:8200\n"
"ds_read_b64_tr_b16 v[206:207], v8 offset:8712\n"
"s_waitcnt lgkmcnt(0)\n"
"s_waitcnt vmcnt(8)\n"
"s_barrier\n"
"ds_read_b64_tr_b16 v[208:209], v8 offset:16384\n"
"ds_read_b64_tr_b16 v[210:211], v8 offset:16896\n"
"ds_read_b64_tr_b16 v[212:213], v8 offset:16392\n"
"ds_read_b64_tr_b16 v[214:215], v8 offset:16904\n"
"ds_read_b64_tr_b16 v[216:217], v8 offset:24576\n"
"ds_read_b64_tr_b16 v[218:219], v8 offset:25088\n"
"ds_read_b64_tr_b16 v[220:221], v8 offset:24584\n"
"ds_read_b64_tr_b16 v[222:223], v8 offset:25096\n"
"s_waitcnt lgkmcnt(0)\n"
"s_waitcnt vmcnt(4)\n"
"s_barrier\n"
"ds_read_b64_tr_b16 v[224:225], v8 offset:32768\n"
"ds_read_b64_tr_b16 v[226:227], v8 offset:33280\n"
"ds_read_b64_tr_b16 v[228:229], v8 offset:32776\n"
"ds_read_b64_tr_b16 v[230:231], v8 offset:33288\n"
"ds_read_b64_tr_b16 v[232:233], v8 offset:40960\n"
"ds_read_b64_tr_b16 v[234:235], v8 offset:41472\n"
"ds_read_b64_tr_b16 v[236:237], v8 offset:40968\n"
"ds_read_b64_tr_b16 v[238:239], v8 offset:41480\n"
"s_waitcnt lgkmcnt(0)\n"
"s_waitcnt vmcnt(0)\n"
"s_barrier\n"
"ds_read_b64_tr_b16 v[240:241], v8 offset:49152\n"
"ds_read_b64_tr_b16 v[242:243], v8 offset:49664\n"
"ds_read_b64_tr_b16 v[244:245], v8 offset:49160\n"
"ds_read_b64_tr_b16 v[246:247], v8 offset:49672\n"
"ds_read_b64_tr_b16 v[248:249], v8 offset:57344\n"
"ds_read_b64_tr_b16 v[250:251], v8 offset:57856\n"
"ds_read_b64_tr_b16 v[252:253], v8 offset:57352\n"
"ds_read_b64_tr_b16 v[254:255], v8 offset:57864\n"
"s_waitcnt lgkmcnt(0)\n"
"s_barrier\n"
"v_mov_b32 v8, s54\n"
"v_mov_b32 v9, s55\n"
"s_lshl2_add_u32 s48, s29, s26\n"
"s_mul_i32 s63, s48, 14336\n"
"s_add_u32 s48, s29, 18\n"
"s_mul_i32 s49, s29, 72\n"
"s_add_u32 s49, s49, 128\n"
"v_lshl_add_u32 v0, v1, 2, s49\n"
"s_add_u32 s49, s50, 280\n"
"v_writelane_b32 v0, s49, 63\n"
"v_lshrrev_b32 v0, 2, v0\n"
"v_lshlrev_b32 v0, 7, v0\n"
"v_mov_b32 v3, 1187840\n"
"v_writelane_b32 v3, 0, 63\n"
"v_add_u32 v0, v3, v0\n"
"v_cmp_ge_u32 vcc, s48, v1\n"
"s_lshl_b32 s49, s29, 31\n"
"s_nop 4\n"
"s_or_b32 vcc_hi, vcc_hi, s49\n"
"s_nop 4\n"
"v_mov_b32 v3, 0x7fff0000\n"
"v_cndmask_b32 v0, v3, v0, vcc\n"
"v_cndmask_b32 v8, 0, v8, vcc\n"
"v_cndmask_b32 v9, 0, v9, vcc\n"
"v_mov_b32 v3, v21\n"
"s_mul_i32 s48, s28, 296960\n"
"s_add_u32 s63, s63, s48\n"
"s_add_u32 s63, s63, 4718592\n"
"s_add_u32 s66, s48, 3710976\n"
"s_mul_i32 s64, s26, 14336\n"
"s_lshl_b32 s74, s26, 11\n"
"s_add_u32 s74, s74, 114688\n"
"v_add_u32 v5, s74, v2\n"
"v_add_u32 v4, 57344, v2\n"
"s_add_u32 s44, s20, 0x0\n"
"s_addc_u32 s45, s21, 0x0\n"
"s_and_b32 s45, s45, 0xffff\n"
"s_mov_b32 s46, 0x400000\n"
"s_mov_b32 s47, 0x20000\n"
"s_mov_b32 s62, 16\n"
"s_cmp_lg_u32 s29, 0\n"
"s_cbranch_scc1 .L_dh\n"
"v_lshrrev_b32 v10, 4, v1\n"
"v_and_b32 v1, 15, v1\n"
"s_lshl_b32 s48, s50, 6\n"
"v_lshl_add_u32 v1, v1, 2, s48\n"
"v_lshl_add_u32 v1, v10, 12, v1\n"
"s_add_u32 s71, s70, 4681728\n"
"s_sub_u32 s68, s65, 2375680\n"
"s_lshl_b32 s65, s28, 20\n"
"s_mov_b32 s48, 20000\n"
"s_branch .L_rfdl\n"
".L_rldl:\n"
"s_sleep 8\n"
".L_rfdl:\n"
"buffer_load_dwordx2 v[6:7], v0, s[32:35], s66 offen sc1\n"
"s_sub_u32 s48, s48, 1\n"
"s_cbranch_scc1 .L_end\n"
"s_waitcnt vmcnt(0)\n"
"v_cmp_ne_u64 vcc, v[8:9], v[6:7]\n"
"s_cbranch_vccnz .L_rldl\n"
"s_add_u32 s66, s66, 1187840\n"
"s_add_u32 m0, s64, 0\n"
"s_nop 0\n"
"buffer_load_dwordx4 v2, s[32:35], s63 offen offset:0 sc1 lds\n"
"buffer_load_dwordx4 v2, s[32:35], s63 offen offset:1024 sc1 lds\n"
"buffer_load_dwordx4 v2, s[32:35], s63 offen offset:2048 sc1 lds\n"
"buffer_load_dwordx4 v2, s[32:35], s63 offen offset:3072 sc1 lds\n"
"s_add_u32 s63, s63, 4096\n"
"s_add_u32 m0, s64, 4096\n"
"s_nop 0\n"
"buffer_load_dwordx4 v2, s[32:35], s63 offen offset:0 sc1 lds\n"
"buffer_load_dwordx4 v2, s[32:35], s63 offen offset:1024 sc1 lds\n"
"buffer_load_dwordx4 v2, s[32:35], s63 offen offset:2048 sc1 lds\n"
"buffer_load_dwordx4 v2, s[32:35], s63 offen offset:3072 sc1 lds\n"
"s_add_u32 s63, s63, 4096\n"
"s_add_u32 m0, s64, 8192\n"
"s_nop 0\n"
"buffer_load_dwordx4 v2, s[32:35], s63 offen offset:0 sc1 lds\n"
"buffer_load_dwordx4 v2, s[32:35], s63 offen offset:1024 sc1 lds\n"
"buffer_load_dwordx4 v2, s[32:35], s63 offen offset:2048 sc1 lds\n"
"buffer_load_dwordx4 v2, s[32:35], s63 offen offset:3072 sc1 lds\n"
"s_add_u32 s63, s63, 4096\n"
"s_add_u32 m0, s64, 12288\n"
"s_nop 0\n"
"buffer_load_dwordx4 v2, s[32:35], s63 offen offset:0 sc1 lds\n"
"buffer_load_dwordx4 v2, s[32:35], s63 offen offset:1024 sc1 lds\n"
"s_add_u32 s63, s63, 1175552\n"
"buffer_load_dwordx4 v[12:15], v2, s[32:35], s71 offen sc1\n"
"s_add_u32 s71, s71, 1187840\n"
"buffer_load_dwordx2 v[6:7], v0, s[32:35], s66 offen sc1\n"
"s_waitcnt vmcnt(0)\n"
"s_barrier\n"
"v_lshlrev_b32 v16, 16, v12\n"
"v_and_b32 v17, 0xffff0000, v12\n"
"v_lshlrev_b32 v18, 16, v13\n"
"v_and_b32 v19, 0xffff0000, v13\n"
"v_lshlrev_b32 v20, 16, v14\n"
"v_and_b32 v21, 0xffff0000, v14\n"
"v_lshlrev_b32 v22, 16, v15\n"
"v_and_b32 v23, 0xffff0000, v15\n"
"ds_read_b128 v[32:35], v2 offset:0\n"
"ds_read_b128 v[36:39], v2 offset:14336\n"
"ds_read_b128 v[40:43], v2 offset:28672\n"
"ds_read_b128 v[44:47], v2 offset:43008\n"
"ds_read_b128 v[48:51], v2 offset:1024\n"
"ds_read_b128 v[52:55], v2 offset:15360\n"
"s_cmp_eq_u32 s26, 1\n"
"s_cbranch_scc1 .L_dlv1\n"
"s_cmp_eq_u32 s26, 2\n"
"s_cbranch_scc1 .L_dlv2\n"
"s_cmp_eq_u32 s26, 3\n"
"s_cbranch_scc1 .L_dlv3\n"
".p2align 6\n"
".L_dlv0:\n"
".L_dll_0:\n"
"s_waitcnt lgkmcnt(4)\n"
"v_mfma_f32_16x16x32_bf16 v[16:19], a[0:3], v[32:35], v[16:19]\n"
"ds_read_b128 v[56:59], v2 offset:29696\n"
"v_mfma_f32_16x16x32_bf16 v[16:19], a[112:115], v[36:39], v[16:19]\n"
"v_mfma_f32_16x16x32_bf16 v[20:23], a[116:119], v[36:39], v[20:23]\n"
"ds_read_b128 v[60:63], v2 offset:44032\n"
"v_mfma_f32_16x16x32_bf16 v[20:23], a[4:7], v[32:35], v[20:23]\n"
"s_add_u32 s68, s68, 1187840\n"
"s_waitcnt lgkmcnt(4)\n"
"v_mfma_f32_16x16x32_bf16 v[20:23], a[228:231], v[40:43], v[20:23]\n"
"ds_read_b128 v[32:35], v2 offset:2048\n"
"v_mfma_f32_16x16x32_bf16 v[20:23], v[148:151], v[44:47], v[20:23]\n"
"buffer_store_dwordx4 v[28:31], v2, s[32:35], s68 offen offset:1024 sc1\n"
"v_mfma_f32_16x16x32_bf16 v[16:19], v[144:147], v[44:47], v[16:19]\n"
"ds_read_b128 v[36:39], v2 offset:16384\n"
"v_mfma_f32_16x16x32_bf16 v[16:19], a[224:227], v[40:43], v[16:19]\n"
"s_waitcnt lgkmcnt(4)\n"
"v_mfma_f32_16x16x32_bf16 v[16:19], a[8:11], v[48:51], v[16:19]\n"
"ds_read_b128 v[40:43], v2 offset:30720\n"
"v_mfma_f32_16x16x32_bf16 v[16:19], a[120:123], v[52:55], v[16:19]\n"
"buffer_store_dwordx4 v[24:27], v2, s[32:35], s68 offen sc1\n"
"s_mov_b32 s67, s68\n"
"v_mfma_f32_16x16x32_bf16 v[20:23], a[124:127], v[52:55], v[20:23]\n"
"ds_read_b128 v[44:47], v2 offset:45056\n"
"v_mfma_f32_16x16x32_bf16 v[20:23], a[12:15], v[48:51], v[20:23]\n"
"s_waitcnt lgkmcnt(4)\n"
"v_mfma_f32_16x16x32_bf16 v[20:23], a[236:239], v[56:59], v[20:23]\n"
"ds_read_b128 v[48:51], v2 offset:3072\n"
"v_mfma_f32_16x16x32_bf16 v[20:23], v[156:159], v[60:63], v[20:23]\n"
"v_mfma_f32_16x16x32_bf16 v[16:19], v[152:155], v[60:63], v[16:19]\n"
"ds_read_b128 v[52:55], v2 offset:17408\n"
"v_mfma_f32_16x16x32_bf16 v[16:19], a[232:235], v[56:59], v[16:19]\n"
"s_waitcnt lgkmcnt(4)\n"
"v_mfma_f32_16x16x32_bf16 v[16:19], a[16:19], v[32:35], v[16:19]\n"
"ds_read_b128 v[56:59], v2 offset:31744\n"
"v_mfma_f32_16x16x32_bf16 v[16:19], a[128:131], v[36:39], v[16:19]\n"
"v_mfma_f32_16x16x32_bf16 v[20:23], a[132:135], v[36:39], v[20:23]\n"
"ds_read_b128 v[60:63], v2 offset:46080\n"
"v_mfma_f32_16x16x32_bf16 v[20:23], a[20:23], v[32:35], v[20:23]\n"
"s_waitcnt lgkmcnt(4)\n"
"s_waitcnt vmcnt(3)\n"
"s_barrier\n"
"v_mfma_f32_16x16x32_bf16 v[20:23], a[244:247], v[40:43], v[20:23]\n"
"ds_read_b128 v[32:35], v2 offset:4096\n"
"s_add_u32 m0, s64, 57344\n"
"v_mfma_f32_16x16x32_bf16 v[20:23], v[164:167], v[44:47], v[20:23]\n"
"buffer_load_dwordx4 v2, s[32:35], s63 offen offset:0 sc1 lds\n"
"v_mfma_f32_16x16x32_bf16 v[16:19], v[160:163], v[44:47], v[16:19]\n"
"ds_read_b128 v[36:39], v2 offset:18432\n"
"v_mfma_f32_16x16x32_bf16 v[16:19], a[240:243], v[40:43], v[16:19]\n"
"s_waitcnt lgkmcnt(4)\n"
"v_mfma_f32_16x16x32_bf16 v[16:19], a[24:27], v[48:51], v[16:19]\n"
"ds_read_b128 v[40:43], v2 offset:32768\n"
"v_mfma_f32_16x16x32_bf16 v[16:19], a[136:139], v[52:55], v[16:19]\n"
"v_mfma_f32_16x16x32_bf16 v[20:23], a[140:143], v[52:55], v[20:23]\n"
"ds_read_b128 v[44:47], v2 offset:47104\n"
"v_mfma_f32_16x16x32_bf16 v[20:23], a[28:31], v[48:51], v[20:23]\n"
"s_waitcnt lgkmcnt(4)\n"
"v_mfma_f32_16x16x32_bf16 v[20:23], a[252:255], v[56:59], v[20:23]\n"
"ds_read_b128 v[48:51], v2 offset:5120\n"
"v_mfma_f32_16x16x32_bf16 v[20:23], v[172:175], v[60:63], v[20:23]\n"
"buffer_load_dwordx4 v2, s[32:35], s63 offen offset:1024 sc1 lds\n"
"v_mfma_f32_16x16x32_bf16 v[16:19], v[168:171], v[60:63], v[16:19]\n"
"ds_read_b128 v[52:55], v2 offset:19456\n"
"v_mfma_f32_16x16x32_bf16 v[16:19], a[248:251], v[56:59], v[16:19]\n"
"s_waitcnt lgkmcnt(4)\n"
"v_mfma_f32_16x16x32_bf16 v[16:19], a[32:35], v[32:35], v[16:19]\n"
"ds_read_b128 v[56:59], v2 offset:33792\n"
"v_mfma_f32_16x16x32_bf16 v[16:19], a[144:147], v[36:39], v[16:19]\n"
"buffer_load_dwordx4 v2, s[32:35], s63 offen offset:2048 sc1 lds\n"
"v_mfma_f32_16x16x32_bf16 v[20:23], a[148:151], v[36:39], v[20:23]\n"
"ds_read_b128 v[60:63], v2 offset:48128\n"
"v_mfma_f32_16x16x32_bf16 v[20:23], a[36:39], v[32:35], v[20:23]\n"
"s_waitcnt lgkmcnt(4)\n"
"v_mfma_f32_16x16x32_bf16 v[20:23], v[68:71], v[40:43], v[20:23]\n"
"ds_read_b128 v[32:35], v2 offset:6144\n"
"v_mfma_f32_16x16x32_bf16 v[20:23], v[180:183], v[44:47], v[20:23]\n"
"buffer_load_dwordx4 v2, s[32:35], s63 offen offset:3072 sc1 lds\n"
"v_mfma_f32_16x16x32_bf16 v[16:19], v[176:179], v[44:47], v[16:19]\n"
"ds_read_b128 v[36:39], v2 offset:20480\n"
"v_mfma_f32_16x16x32_bf16 v[16:19], v[64:67], v[40:43], v[16:19]\n"
"s_waitcnt lgkmcnt(4)\n"
"v_mfma_f32_16x16x32_bf16 v[16:19], a[40:43], v[48:51], v[16:19]\n"
"ds_read_b128 v[40:43], v2 offset:34816\n"
"v_mfma_f32_16x16x32_bf16 v[16:19], a[152:155], v[52:55], v[16:19]\n"
"buffer_load_dwordx4 v[12:15], v2, s[32:35], s71 offen sc1\n"
"s_add_u32 s71, s71, 1187840\n"
"v_mfma_f32_16x16x32_bf16 v[20:23], a[156:159], v[52:55], v[20:23]\n"
"ds_read_b128 v[44:47], v2 offset:49152\n"
"s_add_u32 s63, s63, 4096\n"
"v_mfma_f32_16x16x32_bf16 v[20:23], a[44:47], v[48:51], v[20:23]\n"
"s_add_u32 m0, s64, 61440\n"
"s_waitcnt lgkmcnt(4)\n"
"v_mfma_f32_16x16x32_bf16 v[20:23], v[76:79], v[56:59], v[20:23]\n"
"ds_read_b128 v[48:51], v2 offset:7168\n"
"v_mfma_f32_16x16x32_bf16 v[20:23], v[188:191], v[60:63], v[20:23]\n"
"buffer_load_dwordx4 v2, s[32:35], s63 offen offset:0 sc1 lds\n"
"v_mfma_f32_16x16x32_bf16 v[16:19], v[184:187], v[60:63], v[16:19]\n"
"ds_read_b128 v[52:55], v2 offset:21504\n"
"v_mfma_f32_16x16x32_bf16 v[16:19], v[72:75], v[56:59], v[16:19]\n"
"s_waitcnt lgkmcnt(4)\n"
"v_mfma_f32_16x16x32_bf16 v[16:19], a[48:51], v[32:35], v[16:19]\n"
"ds_read_b128 v[56:59], v2 offset:35840\n"
"v_mfma_f32_16x16x32_bf16 v[16:19], a[160:163], v[36:39], v[16:19]\n"
"buffer_load_dwordx4 v2, s[32:35], s63 offen offset:1024 sc1 lds\n"
"v_mfma_f32_16x16x32_bf16 v[20:23], a[164:167], v[36:39], v[20:23]\n"
"ds_read_b128 v[60:63], v2 offset:50176\n"
"v_mfma_f32_16x16x32_bf16 v[20:23], a[52:55], v[32:35], v[20:23]\n"
"s_waitcnt lgkmcnt(4)\n"
"v_mfma_f32_16x16x32_bf16 v[20:23], v[84:87], v[40:43], v[20:23]\n"
"ds_read_b128 v[32:35], v2 offset:8192\n"
"v_mfma_f32_16x16x32_bf16 v[20:23], v[196:199], v[44:47], v[20:23]\n"
"buffer_load_dwordx4 v2, s[32:35], s63 offen offset:2048 sc1 lds\n"
"v_mfma_f32_16x16x32_bf16 v[16:19], v[192:195], v[44:47], v[16:19]\n"
"ds_read_b128 v[36:39], v2 offset:22528\n"
"v_mfma_f32_16x16x32_bf16 v[16:19], v[80:83], v[40:43], v[16:19]\n"
"s_waitcnt lgkmcnt(4)\n"
"v_mfma_f32_16x16x32_bf16 v[16:19], a[56:59], v[48:51], v[16:19]\n"
"ds_read_b128 v[40:43], v2 offset:36864\n"
"v_mfma_f32_16x16x32_bf16 v[16:19], a[168:171], v[52:55], v[16:19]\n"
"buffer_load_dwordx4 v2, s[32:35], s63 offen offset:3072 sc1 lds\n"
"v_mfma_f32_16x16x32_bf16 v[20:23], a[172:175], v[52:55], v[20:23]\n"
"ds_read_b128 v[44:47], v2 offset:51200\n"
"v_mfma_f32_16x16x32_bf16 v[20:23], a[60:63], v[48:51], v[20:23]\n"
"s_waitcnt lgkmcnt(4)\n"
"v_mfma_f32_16x16x32_bf16 v[20:23], v[92:95], v[56:59], v[20:23]\n"
"ds_read_b128 v[48:51], v2 offset:9216\n"
"v_mfma_f32_16x16x32_bf16 v[20:23], v[204:207], v[60:63], v[20:23]\n"
"s_add_u32 s63, s63, 4096\n"
"s_add_u32 m0, s64, 65536\n"
"v_mfma_f32_16x16x32_bf16 v[16:19], v[200:203], v[60:63], v[16:19]\n"
"ds_read_b128 v[52:55], v2 offset:23552\n"
"v_mfma_f32_16x16x32_bf16 v[16:19], v[88:91], v[56:59], v[16:19]\n"
"s_waitcnt lgkmcnt(4)\n"
"v_mfma_f32_16x16x32_bf16 v[16:19], a[64:67], v[32:35], v[16:19]\n"
"ds_read_b128 v[56:59], v2 offset:37888\n"
"v_mfma_f32_16x16x32_bf16 v[16:19], a[176:179], v[36:39], v[16:19]\n"
"buffer_load_dwordx4 v2, s[32:35], s63 offen offset:0 sc1 lds\n"
"v_mfma_f32_16x16x32_bf16 v[20:23], a[180:183], v[36:39], v[20:23]\n"
"ds_read_b128 v[60:63], v2 offset:52224\n"
"v_mfma_f32_16x16x32_bf16 v[20:23], a[68:71], v[32:35], v[20:23]\n"
"s_waitcnt lgkmcnt(4)\n"
"v_mfma_f32_16x16x32_bf16 v[20:23], v[100:103], v[40:43], v[20:23]\n"
"ds_read_b128 v[32:35], v2 offset:10240\n"
"v_mfma_f32_16x16x32_bf16 v[20:23], v[212:215], v[44:47], v[20:23]\n"
"buffer_load_dwordx4 v2, s[32:35], s63 offen offset:1024 sc1 lds\n"
"v_mfma_f32_16x16x32_bf16 v[16:19], v[208:211], v[44:47], v[16:19]\n"
"ds_read_b128 v[36:39], v2 offset:24576\n"
"v_mfma_f32_16x16x32_bf16 v[16:19], v[96:99], v[40:43], v[16:19]\n"
"s_waitcnt lgkmcnt(4)\n"
"v_mfma_f32_16x16x32_bf16 v[16:19], a[72:75], v[48:51], v[16:19]\n"
"ds_read_b128 v[40:43], v2 offset:38912\n"
"v_mfma_f32_16x16x32_bf16 v[16:19], a[184:187], v[52:55], v[16:19]\n"
"buffer_load_dwordx4 v2, s[32:35], s63 offen offset:2048 sc1 lds\n"
"v_mfma_f32_16x16x32_bf16 v[20:23], a[188:191], v[52:55], v[20:23]\n"
"ds_read_b128 v[44:47], v2 offset:53248\n"
"v_mfma_f32_16x16x32_bf16 v[20:23], a[76:79], v[48:51], v[20:23]\n"
"s_waitcnt lgkmcnt(4)\n"
"v_mfma_f32_16x16x32_bf16 v[20:23], v[108:111], v[56:59], v[20:23]\n"
"ds_read_b128 v[48:51], v2 offset:11264\n"
"v_mfma_f32_16x16x32_bf16 v[20:23], v[220:223], v[60:63], v[20:23]\n"
"v_mfma_f32_16x16x32_bf16 v[16:19], v[216:219], v[60:63], v[16:19]\n"
"ds_read_b128 v[52:55], v2 offset:25600\n"
"v_mfma_f32_16x16x32_bf16 v[16:19], v[104:107], v[56:59], v[16:19]\n"
"s_waitcnt lgkmcnt(4)\n"
"v_mfma_f32_16x16x32_bf16 v[16:19], a[80:83], v[32:35], v[16:19]\n"
"ds_read_b128 v[56:59], v2 offset:39936\n"
"v_mfma_f32_16x16x32_bf16 v[16:19], a[192:195], v[36:39], v[16:19]\n"
"buffer_load_dwordx4 v2, s[32:35], s63 offen offset:3072 sc1 lds\n"
"s_add_u32 s63, s63, 4096\n"
"v_mfma_f32_16x16x32_bf16 v[20:23], a[196:199], v[36:39], v[20:23]\n"
"ds_read_b128 v[60:63], v2 offset:54272\n"
"s_add_u32 m0, s64, 69632\n"
"v_mfma_f32_16x16x32_bf16 v[20:23], a[84:87], v[32:35], v[20:23]\n"
"s_waitcnt lgkmcnt(4)\n"
"v_mfma_f32_16x16x32_bf16 v[20:23], v[116:119], v[40:43], v[20:23]\n"
"ds_read_b128 v[32:35], v2 offset:12288\n"
"v_mfma_f32_16x16x32_bf16 v[20:23], v[228:231], v[44:47], v[20:23]\n"
"buffer_load_dwordx4 v2, s[32:35], s63 offen offset:0 sc1 lds\n"
"v_mfma_f32_16x16x32_bf16 v[16:19], v[224:227], v[44:47], v[16:19]\n"
"ds_read_b128 v[36:39], v2 offset:26624\n"
"v_mfma_f32_16x16x32_bf16 v[16:19], v[112:115], v[40:43], v[16:19]\n"
"s_waitcnt lgkmcnt(4)\n"
"v_mfma_f32_16x16x32_bf16 v[16:19], a[88:91], v[48:51], v[16:19]\n"
"ds_read_b128 v[40:43], v2 offset:40960\n"
"v_mfma_f32_16x16x32_bf16 v[16:19], a[200:203], v[52:55], v[16:19]\n"
"buffer_load_dwordx4 v2, s[32:35], s63 offen offset:1024 sc1 lds\n"
"s_add_u32 s63, s63, 1175552\n"
"v_mfma_f32_16x16x32_bf16 v[20:23], a[204:207], v[52:55], v[20:23]\n"
"ds_read_b128 v[44:47], v2 offset:55296\n"
"v_mfma_f32_16x16x32_bf16 v[20:23], a[92:95], v[48:51], v[20:23]\n"
"s_waitcnt lgkmcnt(4)\n"
"v_mfma_f32_16x16x32_bf16 v[20:23], v[124:127], v[56:59], v[20:23]\n"
"ds_read_b128 v[48:51], v2 offset:13312\n"
"s_waitcnt vmcnt(10)\n"
"v_mfma_f32_16x16x32_bf16 v[20:23], v[236:239], v[60:63], v[20:23]\n"
"v_lshlrev_b32 v24, 16, v12\n"
"v_and_b32 v25, 0xffff0000, v12\n"
"v_mfma_f32_16x16x32_bf16 v[16:19], v[232:235], v[60:63], v[16:19]\n"
"ds_read_b128 v[52:55], v2 offset:27648\n"
"v_lshlrev_b32 v26, 16, v13\n"
"v_mfma_f32_16x16x32_bf16 v[16:19], v[120:123], v[56:59], v[16:19]\n"
"v_and_b32 v27, 0xffff0000, v13\n"
"s_waitcnt lgkmcnt(4)\n"
"v_mfma_f32_16x16x32_bf16 v[16:19], a[96:99], v[32:35], v[16:19]\n"
"ds_read_b128 v[56:59], v2 offset:41984\n"
"v_lshlrev_b32 v28, 16, v14\n"
"v_mfma_f32_16x16x32_bf16 v[16:19], a[208:211], v[36:39], v[16:19]\n"
"v_and_b32 v29, 0xffff0000, v14\n"
"v_lshlrev_b32 v30, 16, v15\n"
"v_mfma_f32_16x16x32_bf16 v[20:23], a[212:215], v[36:39], v[20:23]\n"
"ds_read_b128 v[60:63], v2 offset:56320\n"
"v_and_b32 v31, 0xffff0000, v15\n"
"v_mfma_f32_16x16x32_bf16 v[20:23], a[100:103], v[32:35], v[20:23]\n"
"s_waitcnt lgkmcnt(4)\n"
"s_barrier\n"
"v_mfma_f32_16x16x32_bf16 v[20:23], v[132:135], v[40:43], v[20:23]\n"
"ds_read_b128 v[32:35], v4 offset:0\n"
"v_mfma_f32_16x16x32_bf16 v[20:23], v[244:247], v[44:47], v[20:23]\n"
"v_mfma_f32_16x16x32_bf16 v[16:19], v[240:243], v[44:47], v[16:19]\n"
"ds_read_b128 v[36:39], v4 offset:14336\n"
"v_mfma_f32_16x16x32_bf16 v[16:19], v[128:131], v[40:43], v[16:19]\n"
"s_waitcnt lgkmcnt(4)\n"
"v_mfma_f32_16x16x32_bf16 v[16:19], a[104:107], v[48:51], v[16:19]\n"
"ds_read_b128 v[40:43], v4 offset:28672\n"
"v_mfma_f32_16x16x32_bf16 v[16:19], a[216:219], v[52:55], v[16:19]\n"
"buffer_store_dwordx2 v[8:9], v2, s[92:95], s67 idxen sc1\n"
"v_mfma_f32_16x16x32_bf16 v[20:23], a[220:223], v[52:55], v[20:23]\n"
"ds_read_b128 v[44:47], v4 offset:43008\n"
"v_mfma_f32_16x16x32_bf16 v[20:23], a[108:111], v[48:51], v[20:23]\n"
"s_waitcnt lgkmcnt(4)\n"
"v_mfma_f32_16x16x32_bf16 v[20:23], v[140:143], v[56:59], v[20:23]\n"
"ds_read_b128 v[48:51], v4 offset:1024\n"
"v_mfma_f32_16x16x32_bf16 v[20:23], v[252:255], v[60:63], v[20:23]\n"
"v_mfma_f32_16x16x32_bf16 v[16:19], v[248:251], v[60:63], v[16:19]\n"
"ds_read_b128 v[52:55], v4 offset:15360\n"
"s_sub_u32 s62, s62, 1\n"
"v_mfma_f32_16x16x32_bf16 v[16:19], v[136:139], v[56:59], v[16:19]\n"
"s_cbranch_scc1 .L_dlb_0\n"
"s_waitcnt lgkmcnt(4)\n"
"v_mfma_f32_16x16x32_bf16 v[24:27], a[0:3], v[32:35], v[24:27]\n"
"ds_read_b128 v[56:59], v4 offset:29696\n"
"v_mfma_f32_16x16x32_bf16 v[24:27], a[112:115], v[36:39], v[24:27]\n"
"v_mfma_f32_16x16x32_bf16 v[28:31], a[116:119], v[36:39], v[28:31]\n"
"ds_read_b128 v[60:63], v4 offset:44032\n"
"v_mfma_f32_16x16x32_bf16 v[28:31], a[4:7], v[32:35], v[28:31]\n"
"s_add_u32 s68, s68, 1187840\n"
"s_waitcnt lgkmcnt(4)\n"
"v_mfma_f32_16x16x32_bf16 v[28:31], a[228:231], v[40:43], v[28:31]\n"
"ds_read_b128 v[32:35], v4 offset:2048\n"
"v_mfma_f32_16x16x32_bf16 v[28:31], v[148:151], v[44:47], v[28:31]\n"
"buffer_store_dwordx4 v[20:23], v2, s[32:35], s68 offen offset:1024 sc1\n"
"v_mfma_f32_16x16x32_bf16 v[24:27], v[144:147], v[44:47], v[24:27]\n"
"ds_read_b128 v[36:39], v4 offset:16384\n"
"v_mfma_f32_16x16x32_bf16 v[24:27], a[224:227], v[40:43], v[24:27]\n"
"s_waitcnt lgkmcnt(4)\n"
"v_mfma_f32_16x16x32_bf16 v[24:27], a[8:11], v[48:51], v[24:27]\n"
"ds_read_b128 v[40:43], v4 offset:30720\n"
"v_mfma_f32_16x16x32_bf16 v[24:27], a[120:123], v[52:55], v[24:27]\n"
"buffer_store_dwordx4 v[16:19], v2, s[32:35], s68 offen sc1\n"
"s_mov_b32 s67, s68\n"
"v_mfma_f32_16x16x32_bf16 v[28:31], a[124:127], v[52:55], v[28:31]\n"
"ds_read_b128 v[44:47], v4 offset:45056\n"
"v_mfma_f32_16x16x32_bf16 v[28:31], a[12:15], v[48:51], v[28:31]\n"
"s_waitcnt lgkmcnt(4)\n"
"v_mfma_f32_16x16x32_bf16 v[28:31], a[236:239], v[56:59], v[28:31]\n"
"ds_read_b128 v[48:51], v4 offset:3072\n"
"v_mfma_f32_16x16x32_bf16 v[28:31], v[156:159], v[60:63], v[28:31]\n"
"v_mfma_f32_16x16x32_bf16 v[24:27], v[152:155], v[60:63], v[24:27]\n"
"ds_read_b128 v[52:55], v4 offset:17408\n"
"v_mfma_f32_16x16x32_bf16 v[24:27], a[232:235], v[56:59], v[24:27]\n"
"s_waitcnt lgkmcnt(4)\n"
"v_mfma_f32_16x16x32_bf16 v[24:27], a[16:19], v[32:35], v[24:27]\n"
"ds_read_b128 v[56:59], v4 offset:31744\n"
"v_mfma_f32_16x16x32_bf16 v[24:27], a[128:131], v[36:39], v[24:27]\n"
"v_mfma_f32_16x16x32_bf16 v[28:31], a[132:135], v[36:39], v[28:31]\n"
"ds_read_b128 v[60:63], v4 offset:46080\n"
"v_mfma_f32_16x16x32_bf16 v[28:31], a[20:23], v[32:35], v[28:31]\n"
"s_waitcnt lgkmcnt(4)\n"
"s_waitcnt vmcnt(3)\n"
"s_barrier\n"
"v_mfma_f32_16x16x32_bf16 v[28:31], a[244:247], v[40:43], v[28:31]\n"
"ds_read_b128 v[32:35], v4 offset:4096\n"
"s_add_u32 m0, s64, 0\n"
"v_mfma_f32_16x16x32_bf16 v[28:31], v[164:167], v[44:47], v[28:31]\n"
"buffer_load_dwordx4 v2, s[32:35], s63 offen offset:0 sc1 lds\n"
"v_mfma_f32_16x16x32_bf16 v[24:27], v[160:163], v[44:47], v[24:27]\n"
"ds_read_b128 v[36:39], v4 offset:18432\n"
"v_mfma_f32_16x16x32_bf16 v[24:27], a[240:243], v[40:43], v[24:27]\n"
"s_waitcnt lgkmcnt(4)\n"
"v_mfma_f32_16x16x32_bf16 v[24:27], a[24:27], v[48:51], v[24:27]\n"
"ds_read_b128 v[40:43], v4 offset:32768\n"
"v_mfma_f32_16x16x32_bf16 v[24:27], a[136:139], v[52:55], v[24:27]\n"
"v_mfma_f32_16x16x32_bf16 v[28:31], a[140:143], v[52:55], v[28:31]\n"
"ds_read_b128 v[44:47], v4 offset:47104\n"
"v_mfma_f32_16x16x32_bf16 v[28:31], a[28:31], v[48:51], v[28:31]\n"
"s_waitcnt lgkmcnt(4)\n"
"v_mfma_f32_16x16x32_bf16 v[28:31], a[252:255], v[56:59], v[28:31]\n"
"ds_read_b128 v[48:51], v4 offset:5120\n"
"v_mfma_f32_16x16x32_bf16 v[28:31], v[172:175], v[60:63], v[28:31]\n"
"buffer_load_dwordx4 v2, s[32:35], s63 offen offset:1024 sc1 lds\n"
"v_mfma_f32_16x16x32_bf16 v[24:27], v[168:171], v[60:63], v[24:27]\n"
"ds_read_b128 v[52:55], v4 offset:19456\n"
"v_mfma_f32_16x16x32_bf16 v[24:27], a[248:251], v[56:59], v[24:27]\n"
"s_waitcnt lgkmcnt(4)\n"
"v_mfma_f32_16x16x32_bf16 v[24:27], a[32:35], v[32:35], v[24:27]\n"
"ds_read_b128 v[56:59], v4 offset:33792\n"
"v_mfma_f32_16x16x32_bf16 v[24:27], a[144:147], v[36:39], v[24:27]\n"
"buffer_load_dwordx4 v2, s[32:35], s63 offen offset:2048 sc1 lds\n"
"v_mfma_f32_16x16x32_bf16 v[28:31], a[148:151], v[36:39], v[28:31]\n"
"ds_read_b128 v[60:63], v4 offset:48128\n"
"v_mfma_f32_16x16x32_bf16 v[28:31], a[36:39], v[32:35], v[28:31]\n"
"s_waitcnt lgkmcnt(4)\n"
"v_mfma_f32_16x16x32_bf16 v[28:31], v[68:71], v[40:43], v[28:31]\n"
"ds_read_b128 v[32:35], v4 offset:6144\n"
"v_mfma_f32_16x16x32_bf16 v[28:31], v[180:183], v[44:47], v[28:31]\n"
"buffer_load_dwordx4 v2, s[32:35], s63 offen offset:3072 sc1 lds\n"
"v_mfma_f32_16x16x32_bf16 v[24:27], v[176:179], v[44:47], v[24:27]\n"
"ds_read_b128 v[36:39], v4 offset:20480\n"
"v_mfma_f32_16x16x32_bf16 v[24:27], v[64:67], v[40:43], v[24:27]\n"
"s_waitcnt lgkmcnt(4)\n"
"v_mfma_f32_16x16x32_bf16 v[24:27], a[40:43], v[48:51], v[24:27]\n"
"ds_read_b128 v[40:43], v4 offset:34816\n"
"v_mfma_f32_16x16x32_bf16 v[24:27], a[152:155], v[52:55], v[24:27]\n"
"buffer_load_dwordx4 v[12:15], v2, s[32:35], s71 offen sc1\n"
"s_add_u32 s71, s71, 1187840\n"
"v_mfma_f32_16x16x32_bf16 v[28:31], a[156:159], v[52:55], v[28:31]\n"
"ds_read_b128 v[44:47], v4 offset:49152\n"
"s_add_u32 s63, s63, 4096\n"
"v_mfma_f32_16x16x32_bf16 v[28:31], a[44:47], v[48:51], v[28:31]\n"
"s_add_u32 m0, s64, 4096\n"
"s_waitcnt lgkmcnt(4)\n"
"v_mfma_f32_16x16x32_bf16 v[28:31], v[76:79], v[56:59], v[28:31]\n"
"ds_read_b128 v[48:51], v4 offset:7168\n"
"v_mfma_f32_16x16x32_bf16 v[28:31], v[188:191], v[60:63], v[28:31]\n"
"buffer_load_dwordx4 v2, s[32:35], s63 offen offset:0 sc1 lds\n"
"v_mfma_f32_16x16x32_bf16 v[24:27], v[184:187], v[60:63], v[24:27]\n"
"ds_read_b128 v[52:55], v4 offset:21504\n"
"v_mfma_f32_16x16x32_bf16 v[24:27], v[72:75], v[56:59], v[24:27]\n"
"s_waitcnt lgkmcnt(4)\n"
"v_mfma_f32_16x16x32_bf16 v[24:27], a[48:51], v[32:35], v[24:27]\n"
"ds_read_b128 v[56:59], v4 offset:35840\n"
"v_mfma_f32_16x16x32_bf16 v[24:27], a[160:163], v[36:39], v[24:27]\n"
"buffer_load_dwordx4 v2, s[32:35], s63 offen offset:1024 sc1 lds\n"
"v_mfma_f32_16x16x32_bf16 v[28:31], a[164:167], v[36:39], v[28:31]\n"
"ds_read_b128 v[60:63], v4 offset:50176\n"
"v_mfma_f32_16x16x32_bf16 v[28:31], a[52:55], v[32:35], v[28:31]\n"
"s_waitcnt lgkmcnt(4)\n"
"v_mfma_f32_16x16x32_bf16 v[28:31], v[84:87], v[40:43], v[28:31]\n"
"ds_read_b128 v[32:35], v4 offset:8192\n"
"v_mfma_f32_16x16x32_bf16 v[28:31], v[196:199], v[44:47], v[28:31]\n"
"buffer_load_dwordx4 v2, s[32:35], s63 offen offset:2048 sc1 lds\n"
"v_mfma_f32_16x16x32_bf16 v[24:27], v[192:195], v[44:47], v[24:27]\n"
"ds_read_b128 v[36:39], v4 offset:22528\n"
"v_mfma_f32_16x16x32_bf16 v[24:27], v[80:83], v[40:43], v[24:27]\n"
"s_waitcnt lgkmcnt(4)\n"
"v_mfma_f32_16x16x32_bf16 v[24:27], a[56:59], v[48:51], v[24:27]\n"
"ds_read_b128 v[40:43], v4 offset:36864\n"
"v_mfma_f32_16x16x32_bf16 v[24:27], a[168:171], v[52:55], v[24:27]\n"
"buffer_load_dwordx4 v2, s[32:35], s63 offen offset:3072 sc1 lds\n"
"v_mfma_f32_16x16x32_bf16 v[28:31], a[172:175], v[52:55], v[28:31]\n"
"ds_read_b128 v[44:47], v4 offset:51200\n"
"v_mfma_f32_16x16x32_bf16 v[28:31], a[60:63], v[48:51], v[28:31]\n"
"s_waitcnt lgkmcnt(4)\n"
"v_mfma_f32_16x16x32_bf16 v[28:31], v[92:95], v[56:59], v[28:31]\n"
"ds_read_b128 v[48:51], v4 offset:9216\n"
"v_mfma_f32_16x16x32_bf16 v[28:31], v[204:207], v[60:63], v[28:31]\n"
"s_add_u32 s63, s63, 4096\n"
"s_add_u32 m0, s64, 8192\n"
"v_mfma_f32_16x16x32_bf16 v[24:27], v[200:203], v[60:63], v[24:27]\n"
"ds_read_b128 v[52:55], v4 offset:23552\n"
"v_mfma_f32_16x16x32_bf16 v[24:27], v[88:91], v[56:59], v[24:27]\n"
"s_waitcnt lgkmcnt(4)\n"
"v_mfma_f32_16x16x32_bf16 v[24:27], a[64:67], v[32:35], v[24:27]\n"
"ds_read_b128 v[56:59], v4 offset:37888\n"
"v_mfma_f32_16x16x32_bf16 v[24:27], a[176:179], v[36:39], v[24:27]\n"
"buffer_load_dwordx4 v2, s[32:35], s63 offen offset:0 sc1 lds\n"
"v_mfma_f32_16x16x32_bf16 v[28:31], a[180:183], v[36:39], v[28:31]\n"
"ds_read_b128 v[60:63], v4 offset:52224\n"
"v_mfma_f32_16x16x32_bf16 v[28:31], a[68:71], v[32:35], v[28:31]\n"
"s_waitcnt lgkmcnt(4)\n"
"v_mfma_f32_16x16x32_bf16 v[28:31], v[100:103], v[40:43], v[28:31]\n"
"ds_read_b128 v[32:35], v4 offset:10240\n"
"v_mfma_f32_16x16x32_bf16 v[28:31], v[212:215], v[44:47], v[28:31]\n"
"buffer_load_dwordx4 v2, s[32:35], s63 offen offset:1024 sc1 lds\n"
"v_mfma_f32_16x16x32_bf16 v[24:27], v[208:211], v[44:47], v[24:27]\n"
"ds_read_b128 v[36:39], v4 offset:24576\n"
"v_mfma_f32_16x16x32_bf16 v[24:27], v[96:99], v[40:43], v[24:27]\n"
"s_waitcnt lgkmcnt(4)\n"
"v_mfma_f32_16x16x32_bf16 v[24:27], a[72:75], v[48:51], v[24:27]\n"
"ds_read_b128 v[40:43], v4 offset:38912\n"
"v_mfma_f32_16x16x32_bf16 v[24:27], a[184:187], v[52:55], v[24:27]\n"
"buffer_load_dwordx4 v2, s[32:35], s63 offen offset:2048 sc1 lds\n"
"v_mfma_f32_16x16x32_bf16 v[28:31], a[188:191], v[52:55], v[28:31]\n"
"ds_read_b128 v[44:47], v4 offset:53248\n"
"v_mfma_f32_16x16x32_bf16 v[28:31], a[76:79], v[48:51], v[28:31]\n"
"s_waitcnt lgkmcnt(4)\n"
"v_mfma_f32_16x16x32_bf16 v[28:31], v[108:111], v[56:59], v[28:31]\n"
"ds_read_b128 v[48:51], v4 offset:11264\n"
"v_mfma_f32_16x16x32_bf16 v[28:31], v[220:223], v[60:63], v[28:31]\n"
"v_mfma_f32_16x16x32_bf16 v[24:27], v[216:219], v[60:63], v[24:27]\n"
"ds_read_b128 v[52:55], v4 offset:25600\n"
"v_mfma_f32_16x16x32_bf16 v[24:27], v[104:107], v[56:59], v[24:27]\n"
"s_waitcnt lgkmcnt(4)\n"
"v_mfma_f32_16x16x32_bf16 v[24:27], a[80:83], v[32:35], v[24:27]\n"
"ds_read_b128 v[56:59], v4 offset:39936\n"
"v_mfma_f32_16x16x32_bf16 v[24:27], a[192:195], v[36:39], v[24:27]\n"
"buffer_load_dwordx4 v2, s[32:35], s63 offen offset:3072 sc1 lds\n"
"s_add_u32 s63, s63, 4096\n"
"v_mfma_f32_16x16x32_bf16 v[28:31], a[196:199], v[36:39], v[28:31]\n"
"ds_read_b128 v[60:63], v4 offset:54272\n"
"s_add_u32 m0, s64, 12288\n"
"v_mfma_f32_16x16x32_bf16 v[28:31], a[84:87], v[32:35], v[28:31]\n"
"s_waitcnt lgkmcnt(4)\n"
"v_mfma_f32_16x16x32_bf16 v[28:31], v[116:119], v[40:43], v[28:31]\n"
"ds_read_b128 v[32:35], v4 offset:12288\n"
"v_mfma_f32_16x16x32_bf16 v[28:31], v[228:231], v[44:47], v[28:31]\n"
"buffer_load_dwordx4 v2, s[32:35], s63 offen offset:0 sc1 lds\n"
"v_mfma_f32_16x16x32_bf16 v[24:27], v[224:227], v[44:47], v[24:27]\n"
"ds_read_b128 v[36:39], v4 offset:26624\n"
"v_mfma_f32_16x16x32_bf16 v[24:27], v[112:115], v[40:43], v[24:27]\n"
"s_waitcnt lgkmcnt(4)\n"
"v_mfma_f32_16x16x32_bf16 v[24:27], a[88:91], v[48:51], v[24:27]\n"
"ds_read_b128 v[40:43], v4 offset:40960\n"
"v_mfma_f32_16x16x32_bf16 v[24:27], a[200:203], v[52:55], v[24:27]\n"
"buffer_load_dwordx4 v2, s[32:35], s63 offen offset:1024 sc1 lds\n"
"s_add_u32 s63, s63, 1175552\n"
"v_mfma_f32_16x16x32_bf16 v[28:31], a[204:207], v[52:55], v[28:31]\n"
"ds_read_b128 v[44:47], v4 offset:55296\n"
"v_mfma_f32_16x16x32_bf16 v[28:31], a[92:95], v[48:51], v[28:31]\n"
"s_waitcnt lgkmcnt(4)\n"
"v_mfma_f32_16x16x32_bf16 v[28:31], v[124:127], v[56:59], v[28:31]\n"
"ds_read_b128 v[48:51], v4 offset:13312\n"
"s_waitcnt vmcnt(10)\n"
"v_mfma_f32_16x16x32_bf16 v[28:31], v[236:239], v[60:63], v[28:31]\n"
"v_lshlrev_b32 v16, 16, v12\n"
"v_and_b32 v17, 0xffff0000, v12\n"
"v_mfma_f32_16x16x32_bf16 v[24:27], v[232:235], v[60:63], v[24:27]\n"
"ds_read_b128 v[52:55], v4 offset:27648\n"
"v_lshlrev_b32 v18, 16, v13\n"
"v_mfma_f32_16x16x32_bf16 v[24:27], v[120:123], v[56:59], v[24:27]\n"
"v_and_b32 v19, 0xffff0000, v13\n"
"s_waitcnt lgkmcnt(4)\n"
"v_mfma_f32_16x16x32_bf16 v[24:27], a[96:99], v[32:35], v[24:27]\n"
"ds_read_b128 v[56:59], v4 offset:41984\n"
"v_lshlrev_b32 v20, 16, v14\n"
"v_mfma_f32_16x16x32_bf16 v[24:27], a[208:211], v[36:39], v[24:27]\n"
"v_and_b32 v21, 0xffff0000, v14\n"
"v_lshlrev_b32 v22, 16, v15\n"
"v_mfma_f32_16x16x32_bf16 v[28:31], a[212:215], v[36:39], v[28:31]\n"
"ds_read_b128 v[60:63], v4 offset:56320\n"
"v_and_b32 v23, 0xffff0000, v15\n"
"v_mfma_f32_16x16x32_bf16 v[28:31], a[100:103], v[32:35], v[28:31]\n"
"s_waitcnt lgkmcnt(4)\n"
"s_barrier\n"
"v_mfma_f32_16x16x32_bf16 v[28:31], v[132:135], v[40:43], v[28:31]\n"
"ds_read_b128 v[32:35], v2 offset:0\n"
"v_mfma_f32_16x16x32_bf16 v[28:31], v[244:247], v[44:47], v[28:31]\n"
"v_mfma_f32_16x16x32_bf16 v[24:27], v[240:243], v[44:47], v[24:27]\n"
"ds_read_b128 v[36:39], v2 offset:14336\n"
"v_mfma_f32_16x16x32_bf16 v[24:27], v[128:131], v[40:43], v[24:27]\n"
"s_waitcnt lgkmcnt(4)\n"
"v_mfma_f32_16x16x32_bf16 v[24:27], a[104:107], v[48:51], v[24:27]\n"
"ds_read_b128 v[40:43], v2 offset:28672\n"
"v_mfma_f32_16x16x32_bf16 v[24:27], a[216:219], v[52:55], v[24:27]\n"
"buffer_store_dwordx2 v[8:9], v2, s[92:95], s67 idxen sc1\n"
"v_mfma_f32_16x16x32_bf16 v[28:31], a[220:223], v[52:55], v[28:31]\n"
"ds_read_b128 v[44:47], v2 offset:43008\n"
"v_mfma_f32_16x16x32_bf16 v[28:31], a[108:111], v[48:51], v[28:31]\n"
"s_waitcnt lgkmcnt(4)\n"
"v_mfma_f32_16x16x32_bf16 v[28:31], v[140:143], v[56:59], v[28:31]\n"
"ds_read_b128 v[48:51], v2 offset:1024\n"
"v_mfma_f32_16x16x32_bf16 v[28:31], v[252:255], v[60:63], v[28:31]\n"
"v_mfma_f32_16x16x32_bf16 v[24:27], v[248:251], v[60:63], v[24:27]\n"
"ds_read_b128 v[52:55], v2 offset:15360\n"
"v_mfma_f32_16x16x32_bf16 v[24:27], v[136:139], v[56:59], v[24:27]\n"
"s_branch .L_dll_0\n"
".L_dlb_0:\n"
"s_mov_b32 s62, 15\n"
"s_add_u32 s74, s74, 8192\n"
"v_lshrrev_b32 v5, 8, v2\n"
"v_lshl_add_u32 v5, v5, 4, s74\n"
"v_and_b32 v3, 128, v2\n"
"v_lshl_add_u32 v5, v3, 2, v5\n"
"v_and_b32 v3, 16, v2\n"
"v_lshl_add_u32 v5, v3, 4, v5\n"
"v_and_b32 v3, 96, v2\n"
"v_lshl_add_u32 v5, v3, 1, v5\n"
"v_lshrrev_b32 v3, 2, v2\n"
"v_add_u32 v3, s74, v3\n"
".p2align 6\n"
".L_dml_0:\n"
"s_waitcnt lgkmcnt(4)\n"
"v_mfma_f32_16x16x32_bf16 v[24:27], a[0:3], v[32:35], v[24:27]\n"
"ds_read_b128 v[56:59], v4 offset:29696\n"
"v_mfma_f32_16x16x32_bf16 v[24:27], a[112:115], v[36:39], v[24:27]\n"
"v_mfma_f32_16x16x32_bf16 v[28:31], a[116:119], v[36:39], v[28:31]\n"
"ds_read_b128 v[60:63], v4 offset:44032\n"
"v_mfma_f32_16x16x32_bf16 v[28:31], a[4:7], v[32:35], v[28:31]\n"
"v_cvt_pk_bf16_f32 v15, v22, v23\n"
"s_waitcnt lgkmcnt(4)\n"
"v_mfma_f32_16x16x32_bf16 v[28:31], a[228:231], v[40:43], v[28:31]\n"
"ds_read_b128 v[32:35], v4 offset:2048\n"
"v_cvt_pk_bf16_f32 v14, v20, v21\n"
"v_mfma_f32_16x16x32_bf16 v[28:31], v[148:151], v[44:47], v[28:31]\n"
"v_cvt_pk_bf16_f32 v13, v18, v19\n"
"v_cvt_pk_bf16_f32 v12, v16, v17\n"
"v_mfma_f32_16x16x32_bf16 v[24:27], v[144:147], v[44:47], v[24:27]\n"
"ds_read_b128 v[36:39], v4 offset:16384\n"
"ds_write_b128 v5, v[12:15]\n"
"v_mfma_f32_16x16x32_bf16 v[24:27], a[224:227], v[40:43], v[24:27]\n"
"ds_read2st64_b32 v[12:13], v3 offset0:0 offset1:1\n"
"s_waitcnt lgkmcnt(6)\n"
"v_mfma_f32_16x16x32_bf16 v[24:27], a[8:11], v[48:51], v[24:27]\n"
"ds_read_b128 v[40:43], v4 offset:30720\n"
"ds_read2st64_b32 v[14:15], v3 offset0:2 offset1:3\n"
"v_mfma_f32_16x16x32_bf16 v[24:27], a[120:123], v[52:55], v[24:27]\n"
"v_mfma_f32_16x16x32_bf16 v[28:31], a[124:127], v[52:55], v[28:31]\n"
"ds_read_b128 v[44:47], v4 offset:45056\n"
"v_mfma_f32_16x16x32_bf16 v[28:31], a[12:15], v[48:51], v[28:31]\n"
"s_waitcnt lgkmcnt(7)\n"
"v_mfma_f32_16x16x32_bf16 v[28:31], a[236:239], v[56:59], v[28:31]\n"
"ds_read_b128 v[48:51], v4 offset:3072\n"
"v_mfma_f32_16x16x32_bf16 v[28:31], v[156:159], v[60:63], v[28:31]\n"
"s_waitcnt lgkmcnt(2)\n"
"buffer_atomic_pk_add_bf16 v12, v1, s[44:47], s65 offen offset:0 sc1\n"
"v_mfma_f32_16x16x32_bf16 v[24:27], v[152:155], v[60:63], v[24:27]\n"
"ds_read_b128 v[52:55], v4 offset:17408\n"
"v_mfma_f32_16x16x32_bf16 v[24:27], a[232:235], v[56:59], v[24:27]\n"
"buffer_atomic_pk_add_bf16 v13, v1, s[44:47], s65 offen offset:2048 sc1\n"
"v_mfma_f32_16x16x32_bf16 v[24:27], a[16:19], v[32:35], v[24:27]\n"
"ds_read_b128 v[56:59], v4 offset:31744\n"
"s_add_u32 s65, s65, 16384\n"
"v_mfma_f32_16x16x32_bf16 v[24:27], a[128:131], v[36:39], v[24:27]\n"
"buffer_atomic_pk_add_bf16 v14, v1, s[44:47], s65 offen offset:0 sc1\n"
"v_mfma_f32_16x16x32_bf16 v[28:31], a[132:135], v[36:39], v[28:31]\n"
"ds_read_b128 v[60:63], v4 offset:46080\n"
"v_mfma_f32_16x16x32_bf16 v[28:31], a[20:23], v[32:35], v[28:31]\n"
"s_waitcnt lgkmcnt(4)\n"
"s_waitcnt vmcnt(3)\n"
"s_barrier\n"
"v_mfma_f32_16x16x32_bf16 v[28:31], a[244:247], v[40:43], v[28:31]\n"
"ds_read_b128 v[32:35], v4 offset:4096\n"
"s_add_u32 m0, s64, 0\n"
"v_mfma_f32_16x16x32_bf16 v[28:31], v[164:167], v[44:47], v[28:31]\n"
"buffer_atomic_pk_add_bf16 v15, v1, s[44:47], s65 offen offset:2048 sc1\n"
"s_add_u32 s65, s65, 16384\n"
"v_mfma_f32_16x16x32_bf16 v[24:27], v[160:163], v[44:47], v[24:27]\n"
"ds_read_b128 v[36:39], v4 offset:18432\n"
"v_mfma_f32_16x16x32_bf16 v[24:27], a[240:243], v[40:43], v[24:27]\n"
"s_waitcnt lgkmcnt(4)\n"
"v_mfma_f32_16x16x32_bf16 v[24:27], a[24:27], v[48:51], v[24:27]\n"
"ds_read_b128 v[40:43], v4 offset:32768\n"
"v_mfma_f32_16x16x32_bf16 v[24:27], a[136:139], v[52:55], v[24:27]\n"
"buffer_load_dwordx4 v2, s[32:35], s63 offen offset:0 sc1 lds\n"
"v_mfma_f32_16x16x32_bf16 v[28:31], a[140:143], v[52:55], v[28:31]\n"
"ds_read_b128 v[44:47], v4 offset:47104\n"
"v_mfma_f32_16x16x32_bf16 v[28:31], a[28:31], v[48:51], v[28:31]\n"
"s_waitcnt lgkmcnt(4)\n"
"v_mfma_f32_16x16x32_bf16 v[28:31], a[252:255], v[56:59], v[28:31]\n"
"ds_read_b128 v[48:51], v4 offset:5120\n"
"v_mfma_f32_16x16x32_bf16 v[28:31], v[172:175], v[60:63], v[28:31]\n"
"buffer_load_dwordx4 v2, s[32:35], s63 offen offset:1024 sc1 lds\n"
"v_mfma_f32_16x16x32_bf16 v[24:27], v[168:171], v[60:63], v[24:27]\n"
"ds_read_b128 v[52:55], v4 offset:19456\n"
"v_mfma_f32_16x16x32_bf16 v[24:27], a[248:251], v[56:59], v[24:27]\n"
"s_waitcnt lgkmcnt(4)\n"
"v_mfma_f32_16x16x32_bf16 v[24:27], a[32:35], v[32:35], v[24:27]\n"
"ds_read_b128 v[56:59], v4 offset:33792\n"
"v_mfma_f32_16x16x32_bf16 v[24:27], a[144:147], v[36:39], v[24:27]\n"
"buffer_load_dwordx4 v2, s[32:35], s63 offen offset:2048 sc1 lds\n"
"v_mfma_f32_16x16x32_bf16 v[28:31], a[148:151], v[36:39], v[28:31]\n"
"ds_read_b128 v[60:63], v4 offset:48128\n"
"v_mfma_f32_16x16x32_bf16 v[28:31], a[36:39], v[32:35], v[28:31]\n"
"s_waitcnt lgkmcnt(4)\n"
"v_mfma_f32_16x16x32_bf16 v[28:31], v[68:71], v[40:43], v[28:31]\n"
"ds_read_b128 v[32:35], v4 offset:6144\n"
"v_mfma_f32_16x16x32_bf16 v[28:31], v[180:183], v[44:47], v[28:31]\n"
"buffer_load_dwordx4 v2, s[32:35], s63 offen offset:3072 sc1 lds\n"
"v_mfma_f32_16x16x32_bf16 v[24:27], v[176:179], v[44:47], v[24:27]\n"
"ds_read_b128 v[36:39], v4 offset:20480\n"
"v_mfma_f32_16x16x32_bf16 v[24:27], v[64:67], v[40:43], v[24:27]\n"
"s_waitcnt lgkmcnt(4)\n"
"v_mfma_f32_16x16x32_bf16 v[24:27], a[40:43], v[48:51], v[24:27]\n"
"ds_read_b128 v[40:43], v4 offset:34816\n"
"v_mfma_f32_16x16x32_bf16 v[24:27], a[152:155], v[52:55], v[24:27]\n"
"buffer_load_dwordx4 v[12:15], v2, s[32:35], s71 offen sc1\n"
"s_add_u32 s71, s71, 1187840\n"
"v_mfma_f32_16x16x32_bf16 v[28:31], a[156:159], v[52:55], v[28:31]\n"
"ds_read_b128 v[44:47], v4 offset:49152\n"
"s_add_u32 s63, s63, 4096\n"
"v_mfma_f32_16x16x32_bf16 v[28:31], a[44:47], v[48:51], v[28:31]\n"
"s_add_u32 m0, s64, 4096\n"
"s_waitcnt lgkmcnt(4)\n"
"v_mfma_f32_16x16x32_bf16 v[28:31], v[76:79], v[56:59], v[28:31]\n"
"ds_read_b128 v[48:51], v4 offset:7168\n"
"v_mfma_f32_16x16x32_bf16 v[28:31], v[188:191], v[60:63], v[28:31]\n"
"buffer_load_dwordx4 v2, s[32:35], s63 offen offset:0 sc1 lds\n"
"v_mfma_f32_16x16x32_bf16 v[24:27], v[184:187], v[60:63], v[24:27]\n"
"ds_read_b128 v[52:55], v4 offset:21504\n"
"v_mfma_f32_16x16x32_bf16 v[24:27], v[72:75], v[56:59], v[24:27]\n"
"s_waitcnt lgkmcnt(4)\n"
"v_mfma_f32_16x16x32_bf16 v[24:27], a[48:51], v[32:35], v[24:27]\n"
"ds_read_b128 v[56:59], v4 offset:35840\n"
"v_mfma_f32_16x16x32_bf16 v[24:27], a[160:163], v[36:39], v[24:27]\n"
"buffer_load_dwordx4 v2, s[32:35], s63 offen offset:1024 sc1 lds\n"
"v_mfma_f32_16x16x32_bf16 v[28:31], a[164:167], v[36:39], v[28:31]\n"
"ds_read_b128 v[60:63], v4 offset:50176\n"
"v_mfma_f32_16x16x32_bf16 v[28:31], a[52:55], v[32:35], v[28:31]\n"
"s_waitcnt lgkmcnt(4)\n"
"v_mfma_f32_16x16x32_bf16 v[28:31], v[84:87], v[40:43], v[28:31]\n"
"ds_read_b128 v[32:35], v4 offset:8192\n"
"v_mfma_f32_16x16x32_bf16 v[28:31], v[196:199], v[44:47], v[28:31]\n"
"buffer_load_dwordx4 v2, s[32:35], s63 offen offset:2048 sc1 lds\n"
"v_mfma_f32_16x16x32_bf16 v[24:27], v[192:195], v[44:47], v[24:27]\n"
"ds_read_b128 v[36:39], v4 offset:22528\n"
"v_mfma_f32_16x16x32_bf16 v[24:27], v[80:83], v[40:43], v[24:27]\n"
"s_waitcnt lgkmcnt(4)\n"
"v_mfma_f32_16x16x32_bf16 v[24:27], a[56:59], v[48:51], v[24:27]\n"
"ds_read_b128 v[40:43], v4 offset:36864\n"
"v_mfma_f32_16x16x32_bf16 v[24:27], a[168:171], v[52:55], v[24:27]\n"
"buffer_load_dwordx4 v2, s[32:35], s63 offen offset:3072 sc1 lds\n"
"v_mfma_f32_16x16x32_bf16 v[28:31], a[172:175], v[52:55], v[28:31]\n"
"ds_read_b128 v[44:47], v4 offset:51200\n"
"v_mfma_f32_16x16x32_bf16 v[28:31], a[60:63], v[48:51], v[28:31]\n"
"s_waitcnt lgkmcnt(4)\n"
"v_mfma_f32_16x16x32_bf16 v[28:31], v[92:95], v[56:59], v[28:31]\n"
"ds_read_b128 v[48:51], v4 offset:9216\n"
"v_mfma_f32_16x16x32_bf16 v[28:31], v[204:207], v[60:63], v[28:31]\n"
"s_add_u32 s63, s63, 4096\n"
"s_add_u32 m0, s64, 8192\n"
"v_mfma_f32_16x16x32_bf16 v[24:27], v[200:203], v[60:63], v[24:27]\n"
"ds_read_b128 v[52:55], v4 offset:23552\n"
"v_mfma_f32_16x16x32_bf16 v[24:27], v[88:91], v[56:59], v[24:27]\n"
"s_waitcnt lgkmcnt(4)\n"
"v_mfma_f32_16x16x32_bf16 v[24:27], a[64:67], v[32:35], v[24:27]\n"
"ds_read_b128 v[56:59], v4 offset:37888\n"
"v_mfma_f32_16x16x32_bf16 v[24:27], a[176:179], v[36:39], v[24:27]\n"
"buffer_load_dwordx4 v2, s[32:35], s63 offen offset:0 sc1 lds\n"
"v_mfma_f32_16x16x32_bf16 v[28:31], a[180:183], v[36:39], v[28:31]\n"
"ds_read_b128 v[60:63], v4 offset:52224\n"
"v_mfma_f32_16x16x32_bf16 v[28:31], a[68:71], v[32:35], v[28:31]\n"
"s_waitcnt lgkmcnt(4)\n"
"v_mfma_f32_16x16x32_bf16 v[28:31], v[100:103], v[40:43], v[28:31]\n"
"ds_read_b128 v[32:35], v4 offset:10240\n"
"v_mfma_f32_16x16x32_bf16 v[28:31], v[212:215], v[44:47], v[28:31]\n"
"buffer_load_dwordx4 v2, s[32:35], s63 offen offset:1024 sc1 lds\n"
"v_mfma_f32_16x16x32_bf16 v[24:27], v[208:211], v[44:47], v[24:27]\n"
"ds_read_b128 v[36:39], v4 offset:24576\n"
"v_mfma_f32_16x16x32_bf16 v[24:27], v[96:99], v[40:43], v[24:27]\n"
"s_waitcnt lgkmcnt(4)\n"
"v_mfma_f32_16x16x32_bf16 v[24:27], a[72:75], v[48:51], v[24:27]\n"
"ds_read_b128 v[40:43], v4 offset:38912\n"
"v_mfma_f32_16x16x32_bf16 v[24:27], a[184:187], v[52:55], v[24:27]\n"
"buffer_load_dwordx4 v2, s[32:35], s63 offen offset:2048 sc1 lds\n"
"v_mfma_f32_16x16x32_bf16 v[28:31], a[188:191], v[52:55], v[28:31]\n"
"ds_read_b128 v[44:47], v4 offset:53248\n"
"v_mfma_f32_16x16x32_bf16 v[28:31], a[76:79], v[48:51], v[28:31]\n"
"s_waitcnt lgkmcnt(4)\n"
"v_mfma_f32_16x16x32_bf16 v[28:31], v[108:111], v[56:59], v[28:31]\n"
"ds_read_b128 v[48:51], v4 offset:11264\n"
"v_mfma_f32_16x16x32_bf16 v[28:31], v[220:223], v[60:63], v[28:31]\n"
"v_mfma_f32_16x16x32_bf16 v[24:27], v[216:219], v[60:63], v[24:27]\n"
"ds_read_b128 v[52:55], v4 offset:25600\n"
"v_mfma_f32_16x16x32_bf16 v[24:27], v[104:107], v[56:59], v[24:27]\n"
"s_waitcnt lgkmcnt(4)\n"
"v_mfma_f32_16x16x32_bf16 v[24:27], a[80:83], v[32:35], v[24:27]\n"
"ds_read_b128 v[56:59], v4 offset:39936\n"
"v_mfma_f32_16x16x32_bf16 v[24:27], a[192:195], v[36:39], v[24:27]\n"
"buffer_load_dwordx4 v2, s[32:35], s63 offen offset:3072 sc1 lds\n"
"s_add_u32 s63, s63, 4096\n"
"v_mfma_f32_16x16x32_bf16 v[28:31], a[196:199], v[36:39], v[28:31]\n"
"ds_read_b128 v[60:63], v4 offset:54272\n"
"s_add_u32 m0, s64, 12288\n"
"v_mfma_f32_16x16x32_bf16 v[28:31], a[84:87], v[32:35], v[28:31]\n"
"s_waitcnt lgkmcnt(4)\n"
"v_mfma_f32_16x16x32_bf16 v[28:31], v[116:119], v[40:43], v[28:31]\n"
"ds_read_b128 v[32:35], v4 offset:12288\n"
"v_mfma_f32_16x16x32_bf16 v[28:31], v[228:231], v[44:47], v[28:31]\n"
"buffer_load_dwordx4 v2, s[32:35], s63 offen offset:0 sc1 lds\n"
"v_mfma_f32_16x16x32_bf16 v[24:27], v[224:227], v[44:47], v[24:27]\n"
"ds_read_b128 v[36:39], v4 offset:26624\n"
"v_mfma_f32_16x16x32_bf16 v[24:27], v[112:115], v[40:43], v[24:27]\n"
"s_waitcnt lgkmcnt(4)\n"
"v_mfma_f32_16x16x32_bf16 v[24:27], a[88:91], v[48:51], v[24:27]\n"
"ds_read_b128 v[40:43], v4 offset:40960\n"
"v_mfma_f32_16x16x32_bf16 v[24:27], a[200:203], v[52:55], v[24:27]\n"
"buffer_load_dwordx4 v2, s[32:35], s63 offen offset:1024 sc1 lds\n"
"s_add_u32 s63, s63, 1175552\n"
"v_mfma_f32_16x16x32_bf16 v[28:31], a[204:207], v[52:55], v[28:31]\n"
"ds_read_b128 v[44:47], v4 offset:55296\n"
"v_mfma_f32_16x16x32_bf16 v[28:31], a[92:95], v[48:51], v[28:31]\n"
"s_waitcnt lgkmcnt(4)\n"
"v_mfma_f32_16x16x32_bf16 v[28:31], v[124:127], v[56:59], v[28:31]\n"
"ds_read_b128 v[48:51], v4 offset:13312\n"
"s_waitcnt vmcnt(10)\n"
"v_mfma_f32_16x16x32_bf16 v[28:31], v[236:239], v[60:63], v[28:31]\n"
"v_lshlrev_b32 v16, 16, v12\n"
"v_and_b32 v17, 0xffff0000, v12\n"
"v_mfma_f32_16x16x32_bf16 v[24:27], v[232:235], v[60:63], v[24:27]\n"
"ds_read_b128 v[52:55], v4 offset:27648\n"
"v_lshlrev_b32 v18, 16, v13\n"
"v_mfma_f32_16x16x32_bf16 v[24:27], v[120:123], v[56:59], v[24:27]\n"
"v_and_b32 v19, 0xffff0000, v13\n"
"s_waitcnt lgkmcnt(4)\n"
"v_mfma_f32_16x16x32_bf16 v[24:27], a[96:99], v[32:35], v[24:27]\n"
"ds_read_b128 v[56:59], v4 offset:41984\n"
"v_lshlrev_b32 v20, 16, v14\n"
"v_mfma_f32_16x16x32_bf16 v[24:27], a[208:211], v[36:39], v[24:27]\n"
"v_and_b32 v21, 0xffff0000, v14\n"
"v_lshlrev_b32 v22, 16, v15\n"
"v_mfma_f32_16x16x32_bf16 v[28:31], a[212:215], v[36:39], v[28:31]\n"
"ds_read_b128 v[60:63], v4 offset:56320\n"
"v_and_b32 v23, 0xffff0000, v15\n"
"v_mfma_f32_16x16x32_bf16 v[28:31], a[100:103], v[32:35], v[28:31]\n"
"s_waitcnt lgkmcnt(4)\n"
"s_barrier\n"
"v_mfma_f32_16x16x32_bf16 v[28:31], v[132:135], v[40:43], v[28:31]\n"
"ds_read_b128 v[32:35], v2 offset:0\n"
"v_mfma_f32_16x16x32_bf16 v[28:31], v[244:247], v[44:47], v[28:31]\n"
"v_mfma_f32_16x16x32_bf16 v[24:27], v[240:243], v[44:47], v[24:27]\n"
"ds_read_b128 v[36:39], v2 offset:14336\n"
"v_mfma_f32_16x16x32_bf16 v[24:27], v[128:131], v[40:43], v[24:27]\n"
"s_waitcnt lgkmcnt(4)\n"
"v_mfma_f32_16x16x32_bf16 v[24:27], a[104:107], v[48:51], v[24:27]\n"
"ds_read_b128 v[40:43], v2 offset:28672\n"
"v_mfma_f32_16x16x32_bf16 v[24:27], a[216:219], v[52:55], v[24:27]\n"
"v_mfma_f32_16x16x32_bf16 v[28:31], a[220:223], v[52:55], v[28:31]\n"
"ds_read_b128 v[44:47], v2 offset:43008\n"
"v_mfma_f32_16x16x32_bf16 v[28:31], a[108:111], v[48:51], v[28:31]\n"
"s_waitcnt lgkmcnt(4)\n"
"v_mfma_f32_16x16x32_bf16 v[28:31], v[140:143], v[56:59], v[28:31]\n"
"ds_read_b128 v[48:51], v2 offset:1024\n"
"v_mfma_f32_16x16x32_bf16 v[28:31], v[252:255], v[60:63], v[28:31]\n"
"v_mfma_f32_16x16x32_bf16 v[24:27], v[248:251], v[60:63], v[24:27]\n"
"ds_read_b128 v[52:55], v2 offset:15360\n"
"s_sub_u32 s62, s62, 1\n"
"v_mfma_f32_16x16x32_bf16 v[24:27], v[136:139], v[56:59], v[24:27]\n"
"s_cbranch_scc1 .L_dmt_0\n"
"s_waitcnt lgkmcnt(4)\n"
"v_mfma_f32_16x16x32_bf16 v[16:19], a[0:3], v[32:35], v[16:19]\n"
"ds_read_b128 v[56:59], v2 offset:29696\n"
"v_mfma_f32_16x16x32_bf16 v[16:19], a[112:115], v[36:39], v[16:19]\n"
"v_mfma_f32_16x16x32_bf16 v[20:23], a[116:119], v[36:39], v[20:23]\n"
"ds_read_b128 v[60:63], v2 offset:44032\n"
"v_mfma_f32_16x16x32_bf16 v[20:23], a[4:7], v[32:35], v[20:23]\n"
"v_cvt_pk_bf16_f32 v15, v30, v31\n"
"s_waitcnt lgkmcnt(4)\n"
"v_mfma_f32_16x16x32_bf16 v[20:23], a[228:231], v[40:43], v[20:23]\n"
"ds_read_b128 v[32:35], v2 offset:2048\n"
"v_cvt_pk_bf16_f32 v14, v28, v29\n"
"v_mfma_f32_16x16x32_bf16 v[20:23], v[148:151], v[44:47], v[20:23]\n"
"v_cvt_pk_bf16_f32 v13, v26, v27\n"
"v_cvt_pk_bf16_f32 v12, v24, v25\n"
"v_mfma_f32_16x16x32_bf16 v[16:19], v[144:147], v[44:47], v[16:19]\n"
"ds_read_b128 v[36:39], v2 offset:16384\n"
"ds_write_b128 v5, v[12:15]\n"
"v_mfma_f32_16x16x32_bf16 v[16:19], a[224:227], v[40:43], v[16:19]\n"
"ds_read2st64_b32 v[12:13], v3 offset0:0 offset1:1\n"
"s_waitcnt lgkmcnt(6)\n"
"v_mfma_f32_16x16x32_bf16 v[16:19], a[8:11], v[48:51], v[16:19]\n"
"ds_read_b128 v[40:43], v2 offset:30720\n"
"ds_read2st64_b32 v[14:15], v3 offset0:2 offset1:3\n"
"v_mfma_f32_16x16x32_bf16 v[16:19], a[120:123], v[52:55], v[16:19]\n"
"v_mfma_f32_16x16x32_bf16 v[20:23], a[124:127], v[52:55], v[20:23]\n"
"ds_read_b128 v[44:47], v2 offset:45056\n"
"v_mfma_f32_16x16x32_bf16 v[20:23], a[12:15], v[48:51], v[20:23]\n"
"s_waitcnt lgkmcnt(7)\n"
"v_mfma_f32_16x16x32_bf16 v[20:23], a[236:239], v[56:59], v[20:23]\n"
"ds_read_b128 v[48:51], v2 offset:3072\n"
"v_mfma_f32_16x16x32_bf16 v[20:23], v[156:159], v[60:63], v[20:23]\n"
"s_waitcnt lgkmcnt(2)\n"
"buffer_atomic_pk_add_bf16 v12, v1, s[44:47], s65 offen offset:0 sc1\n"
"v_mfma_f32_16x16x32_bf16 v[16:19], v[152:155], v[60:63], v[16:19]\n"
"ds_read_b128 v[52:55], v2 offset:17408\n"
"v_mfma_f32_16x16x32_bf16 v[16:19], a[232:235], v[56:59], v[16:19]\n"
"buffer_atomic_pk_add_bf16 v13, v1, s[44:47], s65 offen offset:2048 sc1\n"
"v_mfma_f32_16x16x32_bf16 v[16:19], a[16:19], v[32:35], v[16:19]\n"
"ds_read_b128 v[56:59], v2 offset:31744\n"
"s_add_u32 s65, s65, 16384\n"
"v_mfma_f32_16x16x32_bf16 v[16:19], a[128:131], v[36:39], v[16:19]\n"
"buffer_atomic_pk_add_bf16 v14, v1, s[44:47], s65 offen offset:0 sc1\n"
"v_mfma_f32_16x16x32_bf16 v[20:23], a[132:135], v[36:39], v[20:23]\n"
"ds_read_b128 v[60:63], v2 offset:46080\n"
"v_mfma_f32_16x16x32_bf16 v[20:23], a[20:23], v[32:35], v[20:23]\n"
"s_waitcnt lgkmcnt(4)\n"
"s_waitcnt vmcnt(3)\n"
"s_barrier\n"
"v_mfma_f32_16x16x32_bf16 v[20:23], a[244:247], v[40:43], v[20:23]\n"
"ds_read_b128 v[32:35], v2 offset:4096\n"
"s_add_u32 m0, s64, 57344\n"
"v_mfma_f32_16x16x32_bf16 v[20:23], v[164:167], v[44:47], v[20:23]\n"
"buffer_atomic_pk_add_bf16 v15, v1, s[44:47], s65 offen offset:2048 sc1\n"
"s_add_u32 s65, s65, 16384\n"
"v_mfma_f32_16x16x32_bf16 v[16:19], v[160:163], v[44:47], v[16:19]\n"
"ds_read_b128 v[36:39], v2 offset:18432\n"
"v_mfma_f32_16x16x32_bf16 v[16:19], a[240:243], v[40:43], v[16:19]\n"
"s_waitcnt lgkmcnt(4)\n"
"v_mfma_f32_16x16x32_bf16 v[16:19], a[24:27], v[48:51], v[16:19]\n"
"ds_read_b128 v[40:43], v2 offset:32768\n"
"v_mfma_f32_16x16x32_bf16 v[16:19], a[136:139], v[52:55], v[16:19]\n"
"buffer_load_dwordx4 v2, s[32:35], s63 offen offset:0 sc1 lds\n"
"v_mfma_f32_16x16x32_bf16 v[20:23], a[140:143], v[52:55], v[20:23]\n"
"ds_read_b128 v[44:47], v2 offset:47104\n"
"v_mfma_f32_16x16x32_bf16 v[20:23], a[28:31], v[48:51], v[20:23]\n"
"s_waitcnt lgkmcnt(4)\n"
"v_mfma_f32_16x16x32_bf16 v[20:23], a[252:255], v[56:59], v[20:23]\n"
"ds_read_b128 v[48:51], v2 offset:5120\n"
"v_mfma_f32_16x16x32_bf16 v[20:23], v[172:175], v[60:63], v[20:23]\n"
"buffer_load_dwordx4 v2, s[32:35], s63 offen offset:1024 sc1 lds\n"
"v_mfma_f32_16x16x32_bf16 v[16:19], v[168:171], v[60:63], v[16:19]\n"
"ds_read_b128 v[52:55], v2 offset:19456\n"
"v_mfma_f32_16x16x32_bf16 v[16:19], a[248:251], v[56:59], v[16:19]\n"
"s_waitcnt lgkmcnt(4)\n"
"v_mfma_f32_16x16x32_bf16 v[16:19], a[32:35], v[32:35], v[16:19]\n"
"ds_read_b128 v[56:59], v2 offset:33792\n"
"v_mfma_f32_16x16x32_bf16 v[16:19], a[144:147], v[36:39], v[16:19]\n"
"buffer_load_dwordx4 v2, s[32:35], s63 offen offset:2048 sc1 lds\n"
"v_mfma_f32_16x16x32_bf16 v[20:23], a[148:151], v[36:39], v[20:23]\n"
"ds_read_b128 v[60:63], v2 offset:48128\n"
"v_mfma_f32_16x16x32_bf16 v[20:23], a[36:39], v[32:35], v[20:23]\n"
"s_waitcnt lgkmcnt(4)\n"
"v_mfma_f32_16x16x32_bf16 v[20:23], v[68:71], v[40:43], v[20:23]\n"
"ds_read_b128 v[32:35], v2 offset:6144\n"
"v_mfma_f32_16x16x32_bf16 v[20:23], v[180:183], v[44:47], v[20:23]\n"
"buffer_load_dwordx4 v2, s[32:35], s63 offen offset:3072 sc1 lds\n"
"v_mfma_f32_16x16x32_bf16 v[16:19], v[176:179], v[44:47], v[16:19]\n"
"ds_read_b128 v[36:39], v2 offset:20480\n"
"v_mfma_f32_16x16x32_bf16 v[16:19], v[64:67], v[40:43], v[16:19]\n"
"s_waitcnt lgkmcnt(4)\n"
"v_mfma_f32_16x16x32_bf16 v[16:19], a[40:43], v[48:51], v[16:19]\n"
"ds_read_b128 v[40:43], v2 offset:34816\n"
"v_mfma_f32_16x16x32_bf16 v[16:19], a[152:155], v[52:55], v[16:19]\n"
"buffer_load_dwordx4 v[12:15], v2, s[32:35], s71 offen sc1\n"
"s_add_u32 s71, s71, 1187840\n"
"v_mfma_f32_16x16x32_bf16 v[20:23], a[156:159], v[52:55], v[20:23]\n"
"ds_read_b128 v[44:47], v2 offset:49152\n"
"s_add_u32 s63, s63, 4096\n"
"v_mfma_f32_16x16x32_bf16 v[20:23], a[44:47], v[48:51], v[20:23]\n"
"s_add_u32 m0, s64, 61440\n"
"s_waitcnt lgkmcnt(4)\n"
"v_mfma_f32_16x16x32_bf16 v[20:23], v[76:79], v[56:59], v[20:23]\n"
"ds_read_b128 v[48:51], v2 offset:7168\n"
"v_mfma_f32_16x16x32_bf16 v[20:23], v[188:191], v[60:63], v[20:23]\n"
"buffer_load_dwordx4 v2, s[32:35], s63 offen offset:0 sc1 lds\n"
"v_mfma_f32_16x16x32_bf16 v[16:19], v[184:187], v[60:63], v[16:19]\n"
"ds_read_b128 v[52:55], v2 offset:21504\n"
"v_mfma_f32_16x16x32_bf16 v[16:19], v[72:75], v[56:59], v[16:19]\n"
"s_waitcnt lgkmcnt(4)\n"
"v_mfma_f32_16x16x32_bf16 v[16:19], a[48:51], v[32:35], v[16:19]\n"
"ds_read_b128 v[56:59], v2 offset:35840\n"
"v_mfma_f32_16x16x32_bf16 v[16:19], a[160:163], v[36:39], v[16:19]\n"
"buffer_load_dwordx4 v2, s[32:35], s63 offen offset:1024 sc1 lds\n"
"v_mfma_f32_16x16x32_bf16 v[20:23], a[164:167], v[36:39], v[20:23]\n"
"ds_read_b128 v[60:63], v2 offset:50176\n"
"v_mfma_f32_16x16x32_bf16 v[20:23], a[52:55], v[32:35], v[20:23]\n"
"s_waitcnt lgkmcnt(4)\n"
"v_mfma_f32_16x16x32_bf16 v[20:23], v[84:87], v[40:43], v[20:23]\n"
"ds_read_b128 v[32:35], v2 offset:8192\n"
"v_mfma_f32_16x16x32_bf16 v[20:23], v[196:199], v[44:47], v[20:23]\n"
"buffer_load_dwordx4 v2, s[32:35], s63 offen offset:2048 sc1 lds\n"
"v_mfma_f32_16x16x32_bf16 v[16:19], v[192:195], v[44:47], v[16:19]\n"
"ds_read_b128 v[36:39], v2 offset:22528\n"
"v_mfma_f32_16x16x32_bf16 v[16:19], v[80:83], v[40:43], v[16:19]\n"
"s_waitcnt lgkmcnt(4)\n"
"v_mfma_f32_16x16x32_bf16 v[16:19], a[56:59], v[48:51], v[16:19]\n"
"ds_read_b128 v[40:43], v2 offset:36864\n"
"v_mfma_f32_16x16x32_bf16 v[16:19], a[168:171], v[52:55], v[16:19]\n"
"buffer_load_dwordx4 v2, s[32:35], s63 offen offset:3072 sc1 lds\n"
"v_mfma_f32_16x16x32_bf16 v[20:23], a[172:175], v[52:55], v[20:23]\n"
"ds_read_b128 v[44:47], v2 offset:51200\n"
"v_mfma_f32_16x16x32_bf16 v[20:23], a[60:63], v[48:51], v[20:23]\n"
"s_waitcnt lgkmcnt(4)\n"
"v_mfma_f32_16x16x32_bf16 v[20:23], v[92:95], v[56:59], v[20:23]\n"
"ds_read_b128 v[48:51], v2 offset:9216\n"
"v_mfma_f32_16x16x32_bf16 v[20:23], v[204:207], v[60:63], v[20:23]\n"
"s_add_u32 s63, s63, 4096\n"
"s_add_u32 m0, s64, 65536\n"
"v_mfma_f32_16x16x32_bf16 v[16:19], v[200:203], v[60:63], v[16:19]\n"
"ds_read_b128 v[52:55], v2 offset:23552\n"
"v_mfma_f32_16x16x32_bf16 v[16:19], v[88:91], v[56:59], v[16:19]\n"
"s_waitcnt lgkmcnt(4)\n"
"v_mfma_f32_16x16x32_bf16 v[16:19], a[64:67], v[32:35], v[16:19]\n"
"ds_read_b128 v[56:59], v2 offset:37888\n"
"v_mfma_f32_16x16x32_bf16 v[16:19], a[176:179], v[36:39], v[16:19]\n"
"buffer_load_dwordx4 v2, s[32:35], s63 offen offset:0 sc1 lds\n"
"v_mfma_f32_16x16x32_bf16 v[20:23], a[180:183], v[36:39], v[20:23]\n"
"ds_read_b128 v[60:63], v2 offset:52224\n"
"v_mfma_f32_16x16x32_bf16 v[20:23], a[68:71], v[32:35], v[20:23]\n"
"s_waitcnt lgkmcnt(4)\n"
"v_mfma_f32_16x16x32_bf16 v[20:23], v[100:103], v[40:43], v[20:23]\n"
"ds_read_b128 v[32:35], v2 offset:10240\n"
"v_mfma_f32_16x16x32_bf16 v[20:23], v[212:215], v[44:47], v[20:23]\n"
"buffer_load_dwordx4 v2, s[32:35], s63 offen offset:1024 sc1 lds\n"
"v_mfma_f32_16x16x32_bf16 v[16:19], v[208:211], v[44:47], v[16:19]\n"
"ds_read_b128 v[36:39], v2 offset:24576\n"
"v_mfma_f32_16x16x32_bf16 v[16:19], v[96:99], v[40:43], v[16:19]\n"
"s_waitcnt lgkmcnt(4)\n"
"v_mfma_f32_16x16x32_bf16 v[16:19], a[72:75], v[48:51], v[16:19]\n"
"ds_read_b128 v[40:43], v2 offset:38912\n"
"v_mfma_f32_16x16x32_bf16 v[16:19], a[184:187], v[52:55], v[16:19]\n"
"buffer_load_dwordx4 v2, s[32:35], s63 offen offset:2048 sc1 lds\n"
"v_mfma_f32_16x16x32_bf16 v[20:23], a[188:191], v[52:55], v[20:23]\n"
"ds_read_b128 v[44:47], v2 offset:53248\n"
"v_mfma_f32_16x16x32_bf16 v[20:23], a[76:79], v[48:51], v[20:23]\n"
"s_waitcnt lgkmcnt(4)\n"
"v_mfma_f32_16x16x32_bf16 v[20:23], v[108:111], v[56:59], v[20:23]\n"
"ds_read_b128 v[48:51], v2 offset:11264\n"
"v_mfma_f32_16x16x32_bf16 v[20:23], v[220:223], v[60:63], v[20:23]\n"
"v_mfma_f32_16x16x32_bf16 v[16:19], v[216:219], v[60:63], v[16:19]\n"
"ds_read_b128 v[52:55], v2 offset:25600\n"
"v_mfma_f32_16x16x32_bf16 v[16:19], v[104:107], v[56:59], v[16:19]\n"
"s_waitcnt lgkmcnt(4)\n"
"v_mfma_f32_16x16x32_bf16 v[16:19], a[80:83], v[32:35], v[16:19]\n"
"ds_read_b128 v[56:59], v2 offset:39936\n"
"v_mfma_f32_16x16x32_bf16 v[16:19], a[192:195], v[36:39], v[16:19]\n"
"buffer_load_dwordx4 v2, s[32:35], s63 offen offset:3072 sc1 lds\n"
"s_add_u32 s63, s63, 4096\n"
"v_mfma_f32_16x16x32_bf16 v[20:23], a[196:199], v[36:39], v[20:23]\n"
"ds_read_b128 v[60:63], v2 offset:54272\n"
"s_add_u32 m0, s64, 69632\n"
"v_mfma_f32_16x16x32_bf16 v[20:23], a[84:87], v[32:35], v[20:23]\n"
"s_waitcnt lgkmcnt(4)\n"
"v_mfma_f32_16x16x32_bf16 v[20:23], v[116:119], v[40:43], v[20:23]\n"
"ds_read_b128 v[32:35], v2 offset:12288\n"
"v_mfma_f32_16x16x32_bf16 v[20:23], v[228:231], v[44:47], v[20:23]\n"
"buffer_load_dwordx4 v2, s[32:35], s63 offen offset:0 sc1 lds\n"
"v_mfma_f32_16x16x32_bf16 v[16:19], v[224:227], v[44:47], v[16:19]\n"
"ds_read_b128 v[36:39], v2 offset:26624\n"
"v_mfma_f32_16x16x32_bf16 v[16:19], v[112:115], v[40:43], v[16:19]\n"
"s_waitcnt lgkmcnt(4)\n"
"v_mfma_f32_16x16x32_bf16 v[16:19], a[88:91], v[48:51], v[16:19]\n"
"ds_read_b128 v[40:43], v2 offset:40960\n"
"v_mfma_f32_16x16x32_bf16 v[16:19], a[200:203], v[52:55], v[16:19]\n"
"buffer_load_dwordx4 v2, s[32:35], s63 offen offset:1024 sc1 lds\n"
"s_add_u32 s63, s63, 1175552\n"
"v_mfma_f32_16x16x32_bf16 v[20:23], a[204:207], v[52:55], v[20:23]\n"
"ds_read_b128 v[44:47], v2 offset:55296\n"
"v_mfma_f32_16x16x32_bf16 v[20:23], a[92:95], v[48:51], v[20:23]\n"
"s_waitcnt lgkmcnt(4)\n"
"v_mfma_f32_16x16x32_bf16 v[20:23], v[124:127], v[56:59], v[20:23]\n"
"ds_read_b128 v[48:51], v2 offset:13312\n"
"s_waitcnt vmcnt(10)\n"
"v_mfma_f32_16x16x32_bf16 v[20:23], v[236:239], v[60:63], v[20:23]\n"
"v_lshlrev_b32 v24, 16, v12\n"
"v_and_b32 v25, 0xffff0000, v12\n"
"v_mfma_f32_16x16x32_bf16 v[16:19], v[232:235], v[60:63], v[16:19]\n"
"ds_read_b128 v[52:55], v2 offset:27648\n"
"v_lshlrev_b32 v26, 16, v13\n"
"v_mfma_f32_16x16x32_bf16 v[16:19], v[120:123], v[56:59], v[16:19]\n"
"v_and_b32 v27, 0xffff0000, v13\n"
"s_waitcnt lgkmcnt(4)\n"
"v_mfma_f32_16x16x32_bf16 v[16:19], a[96:99], v[32:35], v[16:19]\n"
"ds_read_b128 v[56:59], v2 offset:41984\n"
"v_lshlrev_b32 v28, 16, v14\n"
"v_mfma_f32_16x16x32_bf16 v[16:19], a[208:211], v[36:39], v[16:19]\n"
"v_and_b32 v29, 0xffff0000, v14\n"
"v_lshlrev_b32 v30, 16, v15\n"
"v_mfma_f32_16x16x32_bf16 v[20:23], a[212:215], v[36:39], v[20:23]\n"
"ds_read_b128 v[60:63], v2 offset:56320\n"
"v_and_b32 v31, 0xffff0000, v15\n"
"v_mfma_f32_16x16x32_bf16 v[20:23], a[100:103], v[32:35], v[20:23]\n"
"s_waitcnt lgkmcnt(4)\n"
"s_barrier\n"
"v_mfma_f32_16x16x32_bf16 v[20:23], v[132:135], v[40:43], v[20:23]\n"
"ds_read_b128 v[32:35], v4 offset:0\n"
"v_mfma_f32_16x16x32_bf16 v[20:23], v[244:247], v[44:47], v[20:23]\n"
"v_mfma_f32_16x16x32_bf16 v[16:19], v[240:243], v[44:47], v[16:19]\n"
"ds_read_b128 v[36:39], v4 offset:14336\n"
"v_mfma_f32_16x16x32_bf16 v[16:19], v[128:131], v[40:43], v[16:19]\n"
"s_waitcnt lgkmcnt(4)\n"
"v_mfma_f32_16x16x32_bf16 v[16:19], a[104:107], v[48:51], v[16:19]\n"
"ds_read_b128 v[40:43], v4 offset:28672\n"
"v_mfma_f32_16x16x32_bf16 v[16:19], a[216:219], v[52:55], v[16:19]\n"
"v_mfma_f32_16x16x32_bf16 v[20:23], a[220:223], v[52:55], v[20:23]\n"
"ds_read_b128 v[44:47], v4 offset:43008\n"
"v_mfma_f32_16x16x32_bf16 v[20:23], a[108:111], v[48:51], v[20:23]\n"
"s_waitcnt lgkmcnt(4)\n"
"v_mfma_f32_16x16x32_bf16 v[20:23], v[140:143], v[56:59], v[20:23]\n"
"ds_read_b128 v[48:51], v4 offset:1024\n"
"v_mfma_f32_16x16x32_bf16 v[20:23], v[252:255], v[60:63], v[20:23]\n"
"v_mfma_f32_16x16x32_bf16 v[16:19], v[248:251], v[60:63], v[16:19]\n"
"ds_read_b128 v[52:55], v4 offset:15360\n"
"v_mfma_f32_16x16x32_bf16 v[16:19], v[136:139], v[56:59], v[16:19]\n"
"s_branch .L_dml_0\n"
".L_dmt_0:\n"
"s_nop 1\n"
"s_nop 0\n"
"v_cvt_pk_bf16_f32 v15, v30, v31\n"
"v_cvt_pk_bf16_f32 v14, v28, v29\n"
"s_nop 1\n"
"v_cvt_pk_bf16_f32 v13, v26, v27\n"
"v_cvt_pk_bf16_f32 v12, v24, v25\n"
"ds_write_b128 v5, v[12:15]\n"
"ds_read2st64_b32 v[12:13], v3 offset0:0 offset1:1\n"
"ds_read2st64_b32 v[14:15], v3 offset0:2 offset1:3\n"
"s_waitcnt lgkmcnt(0)\n"
"buffer_atomic_pk_add_bf16 v12, v1, s[44:47], s65 offen offset:0 sc1\n"
"buffer_atomic_pk_add_bf16 v13, v1, s[44:47], s65 offen offset:2048 sc1\n"
"s_add_u32 s65, s65, 16384\n"
"buffer_atomic_pk_add_bf16 v14, v1, s[44:47], s65 offen offset:0 sc1\n"
"buffer_atomic_pk_add_bf16 v15, v1, s[44:47], s65 offen offset:2048 sc1\n"
"s_add_u32 s65, s65, 16384\n"
"s_branch .L_end\n"
".p2align 6\n"
".L_dlv1:\n"
".L_dll_1:\n"
"s_waitcnt lgkmcnt(4)\n"
"v_mfma_f32_16x16x32_bf16 v[16:19], a[0:3], v[32:35], v[16:19]\n"
"ds_read_b128 v[56:59], v2 offset:29696\n"
"v_mfma_f32_16x16x32_bf16 v[16:19], a[112:115], v[36:39], v[16:19]\n"
"v_mfma_f32_16x16x32_bf16 v[20:23], a[116:119], v[36:39], v[20:23]\n"
"ds_read_b128 v[60:63], v2 offset:44032\n"
"v_mfma_f32_16x16x32_bf16 v[20:23], a[4:7], v[32:35], v[20:23]\n"
"s_add_u32 s68, s68, 1187840\n"
"s_waitcnt lgkmcnt(4)\n"
"v_mfma_f32_16x16x32_bf16 v[20:23], a[228:231], v[40:43], v[20:23]\n"
"ds_read_b128 v[32:35], v2 offset:2048\n"
"v_mfma_f32_16x16x32_bf16 v[20:23], v[148:151], v[44:47], v[20:23]\n"
"buffer_store_dwordx4 v[28:31], v2, s[32:35], s68 offen offset:1024 sc1\n"
"v_mfma_f32_16x16x32_bf16 v[16:19], v[144:147], v[44:47], v[16:19]\n"
"ds_read_b128 v[36:39], v2 offset:16384\n"
"v_mfma_f32_16x16x32_bf16 v[16:19], a[224:227], v[40:43], v[16:19]\n"
"s_waitcnt lgkmcnt(4)\n"
"v_mfma_f32_16x16x32_bf16 v[16:19], a[8:11], v[48:51], v[16:19]\n"
"ds_read_b128 v[40:43], v2 offset:30720\n"
"v_mfma_f32_16x16x32_bf16 v[16:19], a[120:123], v[52:55], v[16:19]\n"
"buffer_store_dwordx4 v[24:27], v2, s[32:35], s68 offen sc1\n"
"s_mov_b32 s67, s68\n"
"v_mfma_f32_16x16x32_bf16 v[20:23], a[124:127], v[52:55], v[20:23]\n"
"ds_read_b128 v[44:47], v2 offset:45056\n"
"v_mfma_f32_16x16x32_bf16 v[20:23], a[12:15], v[48:51], v[20:23]\n"
"s_waitcnt lgkmcnt(4)\n"
"v_mfma_f32_16x16x32_bf16 v[20:23], a[236:239], v[56:59], v[20:23]\n"
"ds_read_b128 v[48:51], v2 offset:3072\n"
"v_mfma_f32_16x16x32_bf16 v[20:23], v[156:159], v[60:63], v[20:23]\n"
"v_mfma_f32_16x16x32_bf16 v[16:19], v[152:155], v[60:63], v[16:19]\n"
"ds_read_b128 v[52:55], v2 offset:17408\n"
"s_waitcnt vmcnt(5)\n"
"v_mfma_f32_16x16x32_bf16 v[16:19], a[232:235], v[56:59], v[16:19]\n"
"v_cmp_ne_u64 vcc, v[8:9], v[6:7]\n"
"s_waitcnt lgkmcnt(4)\n"
"v_mfma_f32_16x16x32_bf16 v[16:19], a[16:19], v[32:35], v[16:19]\n"
"ds_read_b128 v[56:59], v2 offset:31744\n"
"s_cbranch_vccnz .L_rtl0_1\n"
".L_bkl0_1:\n"
"v_mfma_f32_16x16x32_bf16 v[16:19], a[128:131], v[36:39], v[16:19]\n"
"s_add_u32 s66, s66, 1187840\n"
"v_mfma_f32_16x16x32_bf16 v[20:23], a[132:135], v[36:39], v[20:23]\n"
"s_waitcnt vmcnt(2)\n"
"s_barrier\n"
"v_mfma_f32_16x16x32_bf16 v[20:23], a[20:23], v[32:35], v[20:23]\n"
"ds_read_b128 v[60:63], v2 offset:46080\n"
"s_waitcnt lgkmcnt(4)\n"
"v_mfma_f32_16x16x32_bf16 v[20:23], a[244:247], v[40:43], v[20:23]\n"
"ds_read_b128 v[32:35], v2 offset:4096\n"
"s_add_u32 m0, s64, 57344\n"
"v_mfma_f32_16x16x32_bf16 v[20:23], v[164:167], v[44:47], v[20:23]\n"
"buffer_load_dwordx4 v2, s[32:35], s63 offen offset:0 sc1 lds\n"
"v_mfma_f32_16x16x32_bf16 v[16:19], v[160:163], v[44:47], v[16:19]\n"
"ds_read_b128 v[36:39], v2 offset:18432\n"
"v_mfma_f32_16x16x32_bf16 v[16:19], a[240:243], v[40:43], v[16:19]\n"
"s_waitcnt lgkmcnt(4)\n"
"v_mfma_f32_16x16x32_bf16 v[16:19], a[24:27], v[48:51], v[16:19]\n"
"ds_read_b128 v[40:43], v2 offset:32768\n"
"v_mfma_f32_16x16x32_bf16 v[16:19], a[136:139], v[52:55], v[16:19]\n"
"v_mfma_f32_16x16x32_bf16 v[20:23], a[140:143], v[52:55], v[20:23]\n"
"ds_read_b128 v[44:47], v2 offset:47104\n"
"v_mfma_f32_16x16x32_bf16 v[20:23], a[28:31], v[48:51], v[20:23]\n"
"s_waitcnt lgkmcnt(4)\n"
"v_mfma_f32_16x16x32_bf16 v[20:23], a[252:255], v[56:59], v[20:23]\n"
"ds_read_b128 v[48:51], v2 offset:5120\n"
"v_mfma_f32_16x16x32_bf16 v[20:23], v[172:175], v[60:63], v[20:23]\n"
"buffer_load_dwordx4 v2, s[32:35], s63 offen offset:1024 sc1 lds\n"
"v_mfma_f32_16x16x32_bf16 v[16:19], v[168:171], v[60:63], v[16:19]\n"
"ds_read_b128 v[52:55], v2 offset:19456\n"
"v_mfma_f32_16x16x32_bf16 v[16:19], a[248:251], v[56:59], v[16:19]\n"
"s_waitcnt lgkmcnt(4)\n"
"v_mfma_f32_16x16x32_bf16 v[16:19], a[32:35], v[32:35], v[16:19]\n"
"ds_read_b128 v[56:59], v2 offset:33792\n"
"v_mfma_f32_16x16x32_bf16 v[16:19], a[144:147], v[36:39], v[16:19]\n"
"buffer_load_dwordx4 v2, s[32:35], s63 offen offset:2048 sc1 lds\n"
"v_mfma_f32_16x16x32_bf16 v[20:23], a[148:151], v[36:39], v[20:23]\n"
"ds_read_b128 v[60:63], v2 offset:48128\n"
"v_mfma_f32_16x16x32_bf16 v[20:23], a[36:39], v[32:35], v[20:23]\n"
"s_waitcnt lgkmcnt(4)\n"
"v_mfma_f32_16x16x32_bf16 v[20:23], v[68:71], v[40:43], v[20:23]\n"
"ds_read_b128 v[32:35], v2 offset:6144\n"
"v_mfma_f32_16x16x32_bf16 v[20:23], v[180:183], v[44:47], v[20:23]\n"
"buffer_load_dwordx4 v2, s[32:35], s63 offen offset:3072 sc1 lds\n"
"v_mfma_f32_16x16x32_bf16 v[16:19], v[176:179], v[44:47], v[16:19]\n"
"ds_read_b128 v[36:39], v2 offset:20480\n"
"v_mfma_f32_16x16x32_bf16 v[16:19], v[64:67], v[40:43], v[16:19]\n"
"s_waitcnt lgkmcnt(4)\n"
"v_mfma_f32_16x16x32_bf16 v[16:19], a[40:43], v[48:51], v[16:19]\n"
"ds_read_b128 v[40:43], v2 offset:34816\n"
"v_mfma_f32_16x16x32_bf16 v[16:19], a[152:155], v[52:55], v[16:19]\n"
"buffer_load_dwordx4 v[12:15], v2, s[32:35], s71 offen sc1\n"
"s_add_u32 s71, s71, 1187840\n"
"v_mfma_f32_16x16x32_bf16 v[20:23], a[156:159], v[52:55], v[20:23]\n"
"ds_read_b128 v[44:47], v2 offset:49152\n"
"s_add_u32 s63, s63, 4096\n"
"v_mfma_f32_16x16x32_bf16 v[20:23], a[44:47], v[48:51], v[20:23]\n"
"s_add_u32 m0, s64, 61440\n"
"s_waitcnt lgkmcnt(4)\n"
"v_mfma_f32_16x16x32_bf16 v[20:23], v[76:79], v[56:59], v[20:23]\n"
"ds_read_b128 v[48:51], v2 offset:7168\n"
"v_mfma_f32_16x16x32_bf16 v[20:23], v[188:191], v[60:63], v[20:23]\n"
"buffer_load_dwordx4 v2, s[32:35], s63 offen offset:0 sc1 lds\n"
"v_mfma_f32_16x16x32_bf16 v[16:19], v[184:187], v[60:63], v[16:19]\n"
"ds_read_b128 v[52:55], v2 offset:21504\n"
"v_mfma_f32_16x16x32_bf16 v[16:19], v[72:75], v[56:59], v[16:19]\n"
"s_waitcnt lgkmcnt(4)\n"
"v_mfma_f32_16x16x32_bf16 v[16:19], a[48:51], v[32:35], v[16:19]\n"
"ds_read_b128 v[56:59], v2 offset:35840\n"
"v_mfma_f32_16x16x32_bf16 v[16:19], a[160:163], v[36:39], v[16:19]\n"
"buffer_load_dwordx4 v2, s[32:35], s63 offen offset:1024 sc1 lds\n"
"v_mfma_f32_16x16x32_bf16 v[20:23], a[164:167], v[36:39], v[20:23]\n"
"ds_read_b128 v[60:63], v2 offset:50176\n"
"v_mfma_f32_16x16x32_bf16 v[20:23], a[52:55], v[32:35], v[20:23]\n"
"s_waitcnt lgkmcnt(4)\n"
"v_mfma_f32_16x16x32_bf16 v[20:23], v[84:87], v[40:43], v[20:23]\n"
"ds_read_b128 v[32:35], v2 offset:8192\n"
"v_mfma_f32_16x16x32_bf16 v[20:23], v[196:199], v[44:47], v[20:23]\n"
"buffer_load_dwordx4 v2, s[32:35], s63 offen offset:2048 sc1 lds\n"
"v_mfma_f32_16x16x32_bf16 v[16:19], v[192:195], v[44:47], v[16:19]\n"
"ds_read_b128 v[36:39], v2 offset:22528\n"
"v_mfma_f32_16x16x32_bf16 v[16:19], v[80:83], v[40:43], v[16:19]\n"
"s_waitcnt lgkmcnt(4)\n"
"v_mfma_f32_16x16x32_bf16 v[16:19], a[56:59], v[48:51], v[16:19]\n"
"ds_read_b128 v[40:43], v2 offset:36864\n"
"v_mfma_f32_16x16x32_bf16 v[16:19], a[168:171], v[52:55], v[16:19]\n"
"buffer_load_dwordx4 v2, s[32:35], s63 offen offset:3072 sc1 lds\n"
"v_mfma_f32_16x16x32_bf16 v[20:23], a[172:175], v[52:55], v[20:23]\n"
"ds_read_b128 v[44:47], v2 offset:51200\n"
"v_mfma_f32_16x16x32_bf16 v[20:23], a[60:63], v[48:51], v[20:23]\n"
"s_waitcnt lgkmcnt(4)\n"
"v_mfma_f32_16x16x32_bf16 v[20:23], v[92:95], v[56:59], v[20:23]\n"
"ds_read_b128 v[48:51], v2 offset:9216\n"
"v_mfma_f32_16x16x32_bf16 v[20:23], v[204:207], v[60:63], v[20:23]\n"
"s_add_u32 s63, s63, 4096\n"
"s_add_u32 m0, s64, 65536\n"
"v_mfma_f32_16x16x32_bf16 v[16:19], v[200:203], v[60:63], v[16:19]\n"
"ds_read_b128 v[52:55], v2 offset:23552\n"
"v_mfma_f32_16x16x32_bf16 v[16:19], v[88:91], v[56:59], v[16:19]\n"
"s_waitcnt lgkmcnt(4)\n"
"v_mfma_f32_16x16x32_bf16 v[16:19], a[64:67], v[32:35], v[16:19]\n"
"ds_read_b128 v[56:59], v2 offset:37888\n"
"v_mfma_f32_16x16x32_bf16 v[16:19], a[176:179], v[36:39], v[16:19]\n"
"buffer_load_dwordx4 v2, s[32:35], s63 offen offset:0 sc1 lds\n"
"v_mfma_f32_16x16x32_bf16 v[20:23], a[180:183], v[36:39], v[20:23]\n"
"ds_read_b128 v[60:63], v2 offset:52224\n"
"v_mfma_f32_16x16x32_bf16 v[20:23], a[68:71], v[32:35], v[20:23]\n"
"s_waitcnt lgkmcnt(4)\n"
"v_mfma_f32_16x16x32_bf16 v[20:23], v[100:103], v[40:43], v[20:23]\n"
"ds_read_b128 v[32:35], v2 offset:10240\n"
"v_mfma_f32_16x16x32_bf16 v[20:23], v[212:215], v[44:47], v[20:23]\n"
"buffer_load_dwordx4 v2, s[32:35], s63 offen offset:1024 sc1 lds\n"
"v_mfma_f32_16x16x32_bf16 v[16:19], v[208:211], v[44:47], v[16:19]\n"
"ds_read_b128 v[36:39], v2 offset:24576\n"
"v_mfma_f32_16x16x32_bf16 v[16:19], v[96:99], v[40:43], v[16:19]\n"
"s_waitcnt lgkmcnt(4)\n"
"v_mfma_f32_16x16x32_bf16 v[16:19], a[72:75], v[48:51], v[16:19]\n"
"ds_read_b128 v[40:43], v2 offset:38912\n"
"v_mfma_f32_16x16x32_bf16 v[16:19], a[184:187], v[52:55], v[16:19]\n"
"buffer_load_dwordx4 v2, s[32:35], s63 offen offset:2048 sc1 lds\n"
"v_mfma_f32_16x16x32_bf16 v[20:23], a[188:191], v[52:55], v[20:23]\n"
"ds_read_b128 v[44:47], v2 offset:53248\n"
"v_mfma_f32_16x16x32_bf16 v[20:23], a[76:79], v[48:51], v[20:23]\n"
"s_waitcnt lgkmcnt(4)\n"
"v_mfma_f32_16x16x32_bf16 v[20:23], v[108:111], v[56:59], v[20:23]\n"
"ds_read_b128 v[48:51], v2 offset:11264\n"
"v_mfma_f32_16x16x32_bf16 v[20:23], v[220:223], v[60:63], v[20:23]\n"
"buffer_load_dwordx2 v[6:7], v0, s[32:35], s66 offen sc1\n"
"v_mfma_f32_16x16x32_bf16 v[16:19], v[216:219], v[60:63], v[16:19]\n"
"ds_read_b128 v[52:55], v2 offset:25600\n"
"v_mfma_f32_16x16x32_bf16 v[16:19], v[104:107], v[56:59], v[16:19]\n"
"s_waitcnt lgkmcnt(4)\n"
"v_mfma_f32_16x16x32_bf16 v[16:19], a[80:83], v[32:35], v[16:19]\n"
"ds_read_b128 v[56:59], v2 offset:39936\n"
"v_mfma_f32_16x16x32_bf16 v[16:19], a[192:195], v[36:39], v[16:19]\n"
"buffer_load_dwordx4 v2, s[32:35], s63 offen offset:3072 sc1 lds\n"
"s_add_u32 s63, s63, 4096\n"
"v_mfma_f32_16x16x32_bf16 v[20:23], a[196:199], v[36:39], v[20:23]\n"
"ds_read_b128 v[60:63], v2 offset:54272\n"
"s_add_u32 m0, s64, 69632\n"
"v_mfma_f32_16x16x32_bf16 v[20:23], a[84:87], v[32:35], v[20:23]\n"
"s_waitcnt lgkmcnt(4)\n"
"v_mfma_f32_16x16x32_bf16 v[20:23], v[116:119], v[40:43], v[20:23]\n"
"ds_read_b128 v[32:35], v2 offset:12288\n"
"v_mfma_f32_16x16x32_bf16 v[20:23], v[228:231], v[44:47], v[20:23]\n"
"buffer_load_dwordx4 v2, s[32:35], s63 offen offset:0 sc1 lds\n"
"v_mfma_f32_16x16x32_bf16 v[16:19], v[224:227], v[44:47], v[16:19]\n"
"ds_read_b128 v[36:39], v2 offset:26624\n"
"v_mfma_f32_16x16x32_bf16 v[16:19], v[112:115], v[40:43], v[16:19]\n"
"s_waitcnt lgkmcnt(4)\n"
"v_mfma_f32_16x16x32_bf16 v[16:19], a[88:91], v[48:51], v[16:19]\n"
"ds_read_b128 v[40:43], v2 offset:40960\n"
"v_mfma_f32_16x16x32_bf16 v[16:19], a[200:203], v[52:55], v[16:19]\n"
"buffer_load_dwordx4 v2, s[32:35], s63 offen offset:1024 sc1 lds\n"
"s_add_u32 s63, s63, 1175552\n"
"v_mfma_f32_16x16x32_bf16 v[20:23], a[204:207], v[52:55], v[20:23]\n"
"ds_read_b128 v[44:47], v2 offset:55296\n"
"v_mfma_f32_16x16x32_bf16 v[20:23], a[92:95], v[48:51], v[20:23]\n"
"s_waitcnt lgkmcnt(4)\n"
"v_mfma_f32_16x16x32_bf16 v[20:23], v[124:127], v[56:59], v[20:23]\n"
"ds_read_b128 v[48:51], v2 offset:13312\n"
"s_waitcnt vmcnt(11)\n"
"v_mfma_f32_16x16x32_bf16 v[20:23], v[236:239], v[60:63], v[20:23]\n"
"v_lshlrev_b32 v24, 16, v12\n"
"v_and_b32 v25, 0xffff0000, v12\n"
"v_mfma_f32_16x16x32_bf16 v[16:19], v[232:235], v[60:63], v[16:19]\n"
"ds_read_b128 v[52:55], v2 offset:27648\n"
"v_lshlrev_b32 v26, 16, v13\n"
"v_mfma_f32_16x16x32_bf16 v[16:19], v[120:123], v[56:59], v[16:19]\n"
"v_and_b32 v27, 0xffff0000, v13\n"
"s_waitcnt lgkmcnt(4)\n"
"v_mfma_f32_16x16x32_bf16 v[16:19], a[96:99], v[32:35], v[16:19]\n"
"ds_read_b128 v[56:59], v2 offset:41984\n"
"v_lshlrev_b32 v28, 16, v14\n"
"v_mfma_f32_16x16x32_bf16 v[16:19], a[208:211], v[36:39], v[16:19]\n"
"v_and_b32 v29, 0xffff0000, v14\n"
"v_lshlrev_b32 v30, 16, v15\n"
"v_mfma_f32_16x16x32_bf16 v[20:23], a[212:215], v[36:39], v[20:23]\n"
"s_barrier\n"
"v_mfma_f32_16x16x32_bf16 v[20:23], a[100:103], v[32:35], v[20:23]\n"
"ds_read_b128 v[60:63], v2 offset:56320\n"
"s_waitcnt lgkmcnt(4)\n"
"v_mfma_f32_16x16x32_bf16 v[20:23], v[132:135], v[40:43], v[20:23]\n"
"ds_read_b128 v[32:35], v4 offset:0\n"
"v_and_b32 v31, 0xffff0000, v15\n"
"v_mfma_f32_16x16x32_bf16 v[20:23], v[244:247], v[44:47], v[20:23]\n"
"v_mfma_f32_16x16x32_bf16 v[16:19], v[240:243], v[44:47], v[16:19]\n"
"ds_read_b128 v[36:39], v4 offset:14336\n"
"v_mfma_f32_16x16x32_bf16 v[16:19], v[128:131], v[40:43], v[16:19]\n"
"s_waitcnt lgkmcnt(4)\n"
"v_mfma_f32_16x16x32_bf16 v[16:19], a[104:107], v[48:51], v[16:19]\n"
"ds_read_b128 v[40:43], v4 offset:28672\n"
"v_mfma_f32_16x16x32_bf16 v[16:19], a[216:219], v[52:55], v[16:19]\n"
"v_mfma_f32_16x16x32_bf16 v[20:23], a[220:223], v[52:55], v[20:23]\n"
"ds_read_b128 v[44:47], v4 offset:43008\n"
"v_mfma_f32_16x16x32_bf16 v[20:23], a[108:111], v[48:51], v[20:23]\n"
"s_waitcnt lgkmcnt(4)\n"
"v_mfma_f32_16x16x32_bf16 v[20:23], v[140:143], v[56:59], v[20:23]\n"
"ds_read_b128 v[48:51], v4 offset:1024\n"
"v_mfma_f32_16x16x32_bf16 v[20:23], v[252:255], v[60:63], v[20:23]\n"
"v_mfma_f32_16x16x32_bf16 v[16:19], v[248:251], v[60:63], v[16:19]\n"
"ds_read_b128 v[52:55], v4 offset:15360\n"
"s_sub_u32 s62, s62, 1\n"
"v_mfma_f32_16x16x32_bf16 v[16:19], v[136:139], v[56:59], v[16:19]\n"
"s_cbranch_scc1 .L_dlb_1\n"
"s_waitcnt lgkmcnt(4)\n"
"v_mfma_f32_16x16x32_bf16 v[24:27], a[0:3], v[32:35], v[24:27]\n"
"ds_read_b128 v[56:59], v4 offset:29696\n"
"v_mfma_f32_16x16x32_bf16 v[24:27], a[112:115], v[36:39], v[24:27]\n"
"v_mfma_f32_16x16x32_bf16 v[28:31], a[116:119], v[36:39], v[28:31]\n"
"ds_read_b128 v[60:63], v4 offset:44032\n"
"v_mfma_f32_16x16x32_bf16 v[28:31], a[4:7], v[32:35], v[28:31]\n"
"s_add_u32 s68, s68, 1187840\n"
"s_waitcnt lgkmcnt(4)\n"
"v_mfma_f32_16x16x32_bf16 v[28:31], a[228:231], v[40:43], v[28:31]\n"
"ds_read_b128 v[32:35], v4 offset:2048\n"
"v_mfma_f32_16x16x32_bf16 v[28:31], v[148:151], v[44:47], v[28:31]\n"
"buffer_store_dwordx4 v[20:23], v2, s[32:35], s68 offen offset:1024 sc1\n"
"v_mfma_f32_16x16x32_bf16 v[24:27], v[144:147], v[44:47], v[24:27]\n"
"ds_read_b128 v[36:39], v4 offset:16384\n"
"v_mfma_f32_16x16x32_bf16 v[24:27], a[224:227], v[40:43], v[24:27]\n"
"s_waitcnt lgkmcnt(4)\n"
"v_mfma_f32_16x16x32_bf16 v[24:27], a[8:11], v[48:51], v[24:27]\n"
"ds_read_b128 v[40:43], v4 offset:30720\n"
"v_mfma_f32_16x16x32_bf16 v[24:27], a[120:123], v[52:55], v[24:27]\n"
"buffer_store_dwordx4 v[16:19], v2, s[32:35], s68 offen sc1\n"
"s_mov_b32 s67, s68\n"
"v_mfma_f32_16x16x32_bf16 v[28:31], a[124:127], v[52:55], v[28:31]\n"
"ds_read_b128 v[44:47], v4 offset:45056\n"
"v_mfma_f32_16x16x32_bf16 v[28:31], a[12:15], v[48:51], v[28:31]\n"
"s_waitcnt lgkmcnt(4)\n"
"v_mfma_f32_16x16x32_bf16 v[28:31], a[236:239], v[56:59], v[28:31]\n"
"ds_read_b128 v[48:51], v4 offset:3072\n"
"v_mfma_f32_16x16x32_bf16 v[28:31], v[156:159], v[60:63], v[28:31]\n"
"v_mfma_f32_16x16x32_bf16 v[24:27], v[152:155], v[60:63], v[24:27]\n"
"ds_read_b128 v[52:55], v4 offset:17408\n"
"s_waitcnt vmcnt(5)\n"
"v_mfma_f32_16x16x32_bf16 v[24:27], a[232:235], v[56:59], v[24:27]\n"
"v_cmp_ne_u64 vcc, v[8:9], v[6:7]\n"
"s_waitcnt lgkmcnt(4)\n"
"v_mfma_f32_16x16x32_bf16 v[24:27], a[16:19], v[32:35], v[24:27]\n"
"ds_read_b128 v[56:59], v4 offset:31744\n"
"s_cbranch_vccnz .L_rtl1_1\n"
".L_bkl1_1:\n"
"v_mfma_f32_16x16x32_bf16 v[24:27], a[128:131], v[36:39], v[24:27]\n"
"s_add_u32 s66, s66, 1187840\n"
"v_mfma_f32_16x16x32_bf16 v[28:31], a[132:135], v[36:39], v[28:31]\n"
"s_waitcnt vmcnt(2)\n"
"s_barrier\n"
"v_mfma_f32_16x16x32_bf16 v[28:31], a[20:23], v[32:35], v[28:31]\n"
"ds_read_b128 v[60:63], v4 offset:46080\n"
"s_waitcnt lgkmcnt(4)\n"
"v_mfma_f32_16x16x32_bf16 v[28:31], a[244:247], v[40:43], v[28:31]\n"
"ds_read_b128 v[32:35], v4 offset:4096\n"
"s_add_u32 m0, s64, 0\n"
"v_mfma_f32_16x16x32_bf16 v[28:31], v[164:167], v[44:47], v[28:31]\n"
"buffer_load_dwordx4 v2, s[32:35], s63 offen offset:0 sc1 lds\n"
"v_mfma_f32_16x16x32_bf16 v[24:27], v[160:163], v[44:47], v[24:27]\n"
"ds_read_b128 v[36:39], v4 offset:18432\n"
"v_mfma_f32_16x16x32_bf16 v[24:27], a[240:243], v[40:43], v[24:27]\n"
"s_waitcnt lgkmcnt(4)\n"
"v_mfma_f32_16x16x32_bf16 v[24:27], a[24:27], v[48:51], v[24:27]\n"
"ds_read_b128 v[40:43], v4 offset:32768\n"
"v_mfma_f32_16x16x32_bf16 v[24:27], a[136:139], v[52:55], v[24:27]\n"
"v_mfma_f32_16x16x32_bf16 v[28:31], a[140:143], v[52:55], v[28:31]\n"
"ds_read_b128 v[44:47], v4 offset:47104\n"
"v_mfma_f32_16x16x32_bf16 v[28:31], a[28:31], v[48:51], v[28:31]\n"
"s_waitcnt lgkmcnt(4)\n"
"v_mfma_f32_16x16x32_bf16 v[28:31], a[252:255], v[56:59], v[28:31]\n"
"ds_read_b128 v[48:51], v4 offset:5120\n"
"v_mfma_f32_16x16x32_bf16 v[28:31], v[172:175], v[60:63], v[28:31]\n"
"buffer_load_dwordx4 v2, s[32:35], s63 offen offset:1024 sc1 lds\n"
"v_mfma_f32_16x16x32_bf16 v[24:27], v[168:171], v[60:63], v[24:27]\n"
"ds_read_b128 v[52:55], v4 offset:19456\n"
"v_mfma_f32_16x16x32_bf16 v[24:27], a[248:251], v[56:59], v[24:27]\n"
"s_waitcnt lgkmcnt(4)\n"
"v_mfma_f32_16x16x32_bf16 v[24:27], a[32:35], v[32:35], v[24:27]\n"
"ds_read_b128 v[56:59], v4 offset:33792\n"
"v_mfma_f32_16x16x32_bf16 v[24:27], a[144:147], v[36:39], v[24:27]\n"
"buffer_load_dwordx4 v2, s[32:35], s63 offen offset:2048 sc1 lds\n"
"v_mfma_f32_16x16x32_bf16 v[28:31], a[148:151], v[36:39], v[28:31]\n"
"ds_read_b128 v[60:63], v4 offset:48128\n"
"v_mfma_f32_16x16x32_bf16 v[28:31], a[36:39], v[32:35], v[28:31]\n"
"s_waitcnt lgkmcnt(4)\n"
"v_mfma_f32_16x16x32_bf16 v[28:31], v[68:71], v[40:43], v[28:31]\n"
"ds_read_b128 v[32:35], v4 offset:6144\n"
"v_mfma_f32_16x16x32_bf16 v[28:31], v[180:183], v[44:47], v[28:31]\n"
"buffer_load_dwordx4 v2, s[32:35], s63 offen offset:3072 sc1 lds\n"
"v_mfma_f32_16x16x32_bf16 v[24:27], v[176:179], v[44:47], v[24:27]\n"
"ds_read_b128 v[36:39], v4 offset:20480\n"
"v_mfma_f32_16x16x32_bf16 v[24:27], v[64:67], v[40:43], v[24:27]\n"
"s_waitcnt lgkmcnt(4)\n"
"v_mfma_f32_16x16x32_bf16 v[24:27], a[40:43], v[48:51], v[24:27]\n"
"ds_read_b128 v[40:43], v4 offset:34816\n"
"v_mfma_f32_16x16x32_bf16 v[24:27], a[152:155], v[52:55], v[24:27]\n"
"buffer_load_dwordx4 v[12:15], v2, s[32:35], s71 offen sc1\n"
"s_add_u32 s71, s71, 1187840\n"
"v_mfma_f32_16x16x32_bf16 v[28:31], a[156:159], v[52:55], v[28:31]\n"
"ds_read_b128 v[44:47], v4 offset:49152\n"
"s_add_u32 s63, s63, 4096\n"
"v_mfma_f32_16x16x32_bf16 v[28:31], a[44:47], v[48:51], v[28:31]\n"
"s_add_u32 m0, s64, 4096\n"
"s_waitcnt lgkmcnt(4)\n"
"v_mfma_f32_16x16x32_bf16 v[28:31], v[76:79], v[56:59], v[28:31]\n"
"ds_read_b128 v[48:51], v4 offset:7168\n"
"v_mfma_f32_16x16x32_bf16 v[28:31], v[188:191], v[60:63], v[28:31]\n"
"buffer_load_dwordx4 v2, s[32:35], s63 offen offset:0 sc1 lds\n"
"v_mfma_f32_16x16x32_bf16 v[24:27], v[184:187], v[60:63], v[24:27]\n"
"ds_read_b128 v[52:55], v4 offset:21504\n"
"v_mfma_f32_16x16x32_bf16 v[24:27], v[72:75], v[56:59], v[24:27]\n"
"s_waitcnt lgkmcnt(4)\n"
"v_mfma_f32_16x16x32_bf16 v[24:27], a[48:51], v[32:35], v[24:27]\n"
"ds_read_b128 v[56:59], v4 offset:35840\n"
"v_mfma_f32_16x16x32_bf16 v[24:27], a[160:163], v[36:39], v[24:27]\n"
"buffer_load_dwordx4 v2, s[32:35], s63 offen offset:1024 sc1 lds\n"
"v_mfma_f32_16x16x32_bf16 v[28:31], a[164:167], v[36:39], v[28:31]\n"
"ds_read_b128 v[60:63], v4 offset:50176\n"
"v_mfma_f32_16x16x32_bf16 v[28:31], a[52:55], v[32:35], v[28:31]\n"
"s_waitcnt lgkmcnt(4)\n"
"v_mfma_f32_16x16x32_bf16 v[28:31], v[84:87], v[40:43], v[28:31]\n"
"ds_read_b128 v[32:35], v4 offset:8192\n"
"v_mfma_f32_16x16x32_bf16 v[28:31], v[196:199], v[44:47], v[28:31]\n"
"buffer_load_dwordx4 v2, s[32:35], s63 offen offset:2048 sc1 lds\n"
"v_mfma_f32_16x16x32_bf16 v[24:27], v[192:195], v[44:47], v[24:27]\n"
"ds_read_b128 v[36:39], v4 offset:22528\n"
"v_mfma_f32_16x16x32_bf16 v[24:27], v[80:83], v[40:43], v[24:27]\n"
"s_waitcnt lgkmcnt(4)\n"
"v_mfma_f32_16x16x32_bf16 v[24:27], a[56:59], v[48:51], v[24:27]\n"
"ds_read_b128 v[40:43], v4 offset:36864\n"
"v_mfma_f32_16x16x32_bf16 v[24:27], a[168:171], v[52:55], v[24:27]\n"
"buffer_load_dwordx4 v2, s[32:35], s63 offen offset:3072 sc1 lds\n"
"v_mfma_f32_16x16x32_bf16 v[28:31], a[172:175], v[52:55], v[28:31]\n"
"ds_read_b128 v[44:47], v4 offset:51200\n"
"v_mfma_f32_16x16x32_bf16 v[28:31], a[60:63], v[48:51], v[28:31]\n"
"s_waitcnt lgkmcnt(4)\n"
"v_mfma_f32_16x16x32_bf16 v[28:31], v[92:95], v[56:59], v[28:31]\n"
"ds_read_b128 v[48:51], v4 offset:9216\n"
"v_mfma_f32_16x16x32_bf16 v[28:31], v[204:207], v[60:63], v[28:31]\n"
"s_add_u32 s63, s63, 4096\n"
"s_add_u32 m0, s64, 8192\n"
"v_mfma_f32_16x16x32_bf16 v[24:27], v[200:203], v[60:63], v[24:27]\n"
"ds_read_b128 v[52:55], v4 offset:23552\n"
"v_mfma_f32_16x16x32_bf16 v[24:27], v[88:91], v[56:59], v[24:27]\n"
"s_waitcnt lgkmcnt(4)\n"
"v_mfma_f32_16x16x32_bf16 v[24:27], a[64:67], v[32:35], v[24:27]\n"
"ds_read_b128 v[56:59], v4 offset:37888\n"
"v_mfma_f32_16x16x32_bf16 v[24:27], a[176:179], v[36:39], v[24:27]\n"
"buffer_load_dwordx4 v2, s[32:35], s63 offen offset:0 sc1 lds\n"
"v_mfma_f32_16x16x32_bf16 v[28:31], a[180:183], v[36:39], v[28:31]\n"
"ds_read_b128 v[60:63], v4 offset:52224\n"
"v_mfma_f32_16x16x32_bf16 v[28:31], a[68:71], v[32:35], v[28:31]\n"
"s_waitcnt lgkmcnt(4)\n"
"v_mfma_f32_16x16x32_bf16 v[28:31], v[100:103], v[40:43], v[28:31]\n"
"ds_read_b128 v[32:35], v4 offset:10240\n"
"v_mfma_f32_16x16x32_bf16 v[28:31], v[212:215], v[44:47], v[28:31]\n"
"buffer_load_dwordx4 v2, s[32:35], s63 offen offset:1024 sc1 lds\n"
"v_mfma_f32_16x16x32_bf16 v[24:27], v[208:211], v[44:47], v[24:27]\n"
"ds_read_b128 v[36:39], v4 offset:24576\n"
"v_mfma_f32_16x16x32_bf16 v[24:27], v[96:99], v[40:43], v[24:27]\n"
"s_waitcnt lgkmcnt(4)\n"
"v_mfma_f32_16x16x32_bf16 v[24:27], a[72:75], v[48:51], v[24:27]\n"
"ds_read_b128 v[40:43], v4 offset:38912\n"
"v_mfma_f32_16x16x32_bf16 v[24:27], a[184:187], v[52:55], v[24:27]\n"
"buffer_load_dwordx4 v2, s[32:35], s63 offen offset:2048 sc1 lds\n"
"v_mfma_f32_16x16x32_bf16 v[28:31], a[188:191], v[52:55], v[28:31]\n"
"ds_read_b128 v[44:47], v4 offset:53248\n"
"v_mfma_f32_16x16x32_bf16 v[28:31], a[76:79], v[48:51], v[28:31]\n"
"s_waitcnt lgkmcnt(4)\n"
"v_mfma_f32_16x16x32_bf16 v[28:31], v[108:111], v[56:59], v[28:31]\n"
"ds_read_b128 v[48:51], v4 offset:11264\n"
"v_mfma_f32_16x16x32_bf16 v[28:31], v[220:223], v[60:63], v[28:31]\n"
"buffer_load_dwordx2 v[6:7], v0, s[32:35], s66 offen sc1\n"
"v_mfma_f32_16x16x32_bf16 v[24:27], v[216:219], v[60:63], v[24:27]\n"
"ds_read_b128 v[52:55], v4 offset:25600\n"
"v_mfma_f32_16x16x32_bf16 v[24:27], v[104:107], v[56:59], v[24:27]\n"
"s_waitcnt lgkmcnt(4)\n"
"v_mfma_f32_16x16x32_bf16 v[24:27], a[80:83], v[32:35], v[24:27]\n"
"ds_read_b128 v[56:59], v4 offset:39936\n"
"v_mfma_f32_16x16x32_bf16 v[24:27], a[192:195], v[36:39], v[24:27]\n"
"buffer_load_dwordx4 v2, s[32:35], s63 offen offset:3072 sc1 lds\n"
"s_add_u32 s63, s63, 4096\n"
"v_mfma_f32_16x16x32_bf16 v[28:31], a[196:199], v[36:39], v[28:31]\n"
"ds_read_b128 v[60:63], v4 offset:54272\n"
"s_add_u32 m0, s64, 12288\n"
"v_mfma_f32_16x16x32_bf16 v[28:31], a[84:87], v[32:35], v[28:31]\n"
"s_waitcnt lgkmcnt(4)\n"
"v_mfma_f32_16x16x32_bf16 v[28:31], v[116:119], v[40:43], v[28:31]\n"
"ds_read_b128 v[32:35], v4 offset:12288\n"
"v_mfma_f32_16x16x32_bf16 v[28:31], v[228:231], v[44:47], v[28:31]\n"
"buffer_load_dwordx4 v2, s[32:35], s63 offen offset:0 sc1 lds\n"
"v_mfma_f32_16x16x32_bf16 v[24:27], v[224:227], v[44:47], v[24:27]\n"
"ds_read_b128 v[36:39], v4 offset:26624\n"
"v_mfma_f32_16x16x32_bf16 v[24:27], v[112:115], v[40:43], v[24:27]\n"
"s_waitcnt lgkmcnt(4)\n"
"v_mfma_f32_16x16x32_bf16 v[24:27], a[88:91], v[48:51], v[24:27]\n"
"ds_read_b128 v[40:43], v4 offset:40960\n"
"v_mfma_f32_16x16x32_bf16 v[24:27], a[200:203], v[52:55], v[24:27]\n"
"buffer_load_dwordx4 v2, s[32:35], s63 offen offset:1024 sc1 lds\n"
"s_add_u32 s63, s63, 1175552\n"
"v_mfma_f32_16x16x32_bf16 v[28:31], a[204:207], v[52:55], v[28:31]\n"
"ds_read_b128 v[44:47], v4 offset:55296\n"
"v_mfma_f32_16x16x32_bf16 v[28:31], a[92:95], v[48:51], v[28:31]\n"
"s_waitcnt lgkmcnt(4)\n"
"v_mfma_f32_16x16x32_bf16 v[28:31], v[124:127], v[56:59], v[28:31]\n"
"ds_read_b128 v[48:51], v4 offset:13312\n"
"s_waitcnt vmcnt(11)\n"
"v_mfma_f32_16x16x32_bf16 v[28:31], v[236:239], v[60:63], v[28:31]\n"
"v_lshlrev_b32 v16, 16, v12\n"
"v_and_b32 v17, 0xffff0000, v12\n"
"v_mfma_f32_16x16x32_bf16 v[24:27], v[232:235], v[60:63], v[24:27]\n"
"ds_read_b128 v[52:55], v4 offset:27648\n"
"v_lshlrev_b32 v18, 16, v13\n"
"v_mfma_f32_16x16x32_bf16 v[24:27], v[120:123], v[56:59], v[24:27]\n"
"v_and_b32 v19, 0xffff0000, v13\n"
"s_waitcnt lgkmcnt(4)\n"
"v_mfma_f32_16x16x32_bf16 v[24:27], a[96:99], v[32:35], v[24:27]\n"
"ds_read_b128 v[56:59], v4 offset:41984\n"
"v_lshlrev_b32 v20, 16, v14\n"
"v_mfma_f32_16x16x32_bf16 v[24:27], a[208:211], v[36:39], v[24:27]\n"
"v_and_b32 v21, 0xffff0000, v14\n"
"v_lshlrev_b32 v22, 16, v15\n"
"v_mfma_f32_16x16x32_bf16 v[28:31], a[212:215], v[36:39], v[28:31]\n"
"s_barrier\n"
"v_mfma_f32_16x16x32_bf16 v[28:31], a[100:103], v[32:35], v[28:31]\n"
"ds_read_b128 v[60:63], v4 offset:56320\n"
"s_waitcnt lgkmcnt(4)\n"
"v_mfma_f32_16x16x32_bf16 v[28:31], v[132:135], v[40:43], v[28:31]\n"
"ds_read_b128 v[32:35], v2 offset:0\n"
"v_and_b32 v23, 0xffff0000, v15\n"
"v_mfma_f32_16x16x32_bf16 v[28:31], v[244:247], v[44:47], v[28:31]\n"
"v_mfma_f32_16x16x32_bf16 v[24:27], v[240:243], v[44:47], v[24:27]\n"
"ds_read_b128 v[36:39], v2 offset:14336\n"
"v_mfma_f32_16x16x32_bf16 v[24:27], v[128:131], v[40:43], v[24:27]\n"
"s_waitcnt lgkmcnt(4)\n"
"v_mfma_f32_16x16x32_bf16 v[24:27], a[104:107], v[48:51], v[24:27]\n"
"ds_read_b128 v[40:43], v2 offset:28672\n"
"v_mfma_f32_16x16x32_bf16 v[24:27], a[216:219], v[52:55], v[24:27]\n"
"v_mfma_f32_16x16x32_bf16 v[28:31], a[220:223], v[52:55], v[28:31]\n"
"ds_read_b128 v[44:47], v2 offset:43008\n"
"v_mfma_f32_16x16x32_bf16 v[28:31], a[108:111], v[48:51], v[28:31]\n"
"s_waitcnt lgkmcnt(4)\n"
"v_mfma_f32_16x16x32_bf16 v[28:31], v[140:143], v[56:59], v[28:31]\n"
"ds_read_b128 v[48:51], v2 offset:1024\n"
"v_mfma_f32_16x16x32_bf16 v[28:31], v[252:255], v[60:63], v[28:31]\n"
"v_mfma_f32_16x16x32_bf16 v[24:27], v[248:251], v[60:63], v[24:27]\n"
"ds_read_b128 v[52:55], v2 offset:15360\n"
"v_mfma_f32_16x16x32_bf16 v[24:27], v[136:139], v[56:59], v[24:27]\n"
"s_branch .L_dll_1\n"
".L_dlb_1:\n"
"s_mov_b32 s62, 15\n"
"s_add_u32 s74, s74, 8192\n"
"v_lshrrev_b32 v5, 8, v2\n"
"v_lshl_add_u32 v5, v5, 4, s74\n"
"v_and_b32 v3, 128, v2\n"
"v_lshl_add_u32 v5, v3, 2, v5\n"
"v_and_b32 v3, 16, v2\n"
"v_lshl_add_u32 v5, v3, 4, v5\n"
"v_and_b32 v3, 96, v2\n"
"v_lshl_add_u32 v5, v3, 1, v5\n"
"v_lshrrev_b32 v3, 2, v2\n"
"v_add_u32 v3, s74, v3\n"
".p2align 6\n"
".L_dml_1:\n"
"s_waitcnt lgkmcnt(4)\n"
"v_mfma_f32_16x16x32_bf16 v[24:27], a[0:3], v[32:35], v[24:27]\n"
"ds_read_b128 v[56:59], v4 offset:29696\n"
"v_mfma_f32_16x16x32_bf16 v[24:27], a[112:115], v[36:39], v[24:27]\n"
"v_mfma_f32_16x16x32_bf16 v[28:31], a[116:119], v[36:39], v[28:31]\n"
"ds_read_b128 v[60:63], v4 offset:44032\n"
"v_mfma_f32_16x16x32_bf16 v[28:31], a[4:7], v[32:35], v[28:31]\n"
"v_cvt_pk_bf16_f32 v15, v22, v23\n"
"s_waitcnt lgkmcnt(4)\n"
"v_mfma_f32_16x16x32_bf16 v[28:31], a[228:231], v[40:43], v[28:31]\n"
"ds_read_b128 v[32:35], v4 offset:2048\n"
"v_cvt_pk_bf16_f32 v14, v20, v21\n"
"v_mfma_f32_16x16x32_bf16 v[28:31], v[148:151], v[44:47], v[28:31]\n"
"v_cvt_pk_bf16_f32 v13, v18, v19\n"
"v_cvt_pk_bf16_f32 v12, v16, v17\n"
"v_mfma_f32_16x16x32_bf16 v[24:27], v[144:147], v[44:47], v[24:27]\n"
"ds_read_b128 v[36:39], v4 offset:16384\n"
"ds_write_b128 v5, v[12:15]\n"
"v_mfma_f32_16x16x32_bf16 v[24:27], a[224:227], v[40:43], v[24:27]\n"
"ds_read2st64_b32 v[12:13], v3 offset0:0 offset1:1\n"
"s_waitcnt lgkmcnt(6)\n"
"v_mfma_f32_16x16x32_bf16 v[24:27], a[8:11], v[48:51], v[24:27]\n"
"ds_read_b128 v[40:43], v4 offset:30720\n"
"ds_read2st64_b32 v[14:15], v3 offset0:2 offset1:3\n"
"v_mfma_f32_16x16x32_bf16 v[24:27], a[120:123], v[52:55], v[24:27]\n"
"v_mfma_f32_16x16x32_bf16 v[28:31], a[124:127], v[52:55], v[28:31]\n"
"ds_read_b128 v[44:47], v4 offset:45056\n"
"v_mfma_f32_16x16x32_bf16 v[28:31], a[12:15], v[48:51], v[28:31]\n"
"s_waitcnt lgkmcnt(7)\n"
"v_mfma_f32_16x16x32_bf16 v[28:31], a[236:239], v[56:59], v[28:31]\n"
"ds_read_b128 v[48:51], v4 offset:3072\n"
"v_mfma_f32_16x16x32_bf16 v[28:31], v[156:159], v[60:63], v[28:31]\n"
"s_waitcnt lgkmcnt(2)\n"
"buffer_atomic_pk_add_bf16 v12, v1, s[44:47], s65 offen offset:0 sc1\n"
"v_mfma_f32_16x16x32_bf16 v[24:27], v[152:155], v[60:63], v[24:27]\n"
"ds_read_b128 v[52:55], v4 offset:17408\n"
"s_waitcnt vmcnt(4)\n"
"v_mfma_f32_16x16x32_bf16 v[24:27], a[232:235], v[56:59], v[24:27]\n"
"buffer_atomic_pk_add_bf16 v13, v1, s[44:47], s65 offen offset:2048 sc1\n"
"v_mfma_f32_16x16x32_bf16 v[24:27], a[16:19], v[32:35], v[24:27]\n"
"ds_read_b128 v[56:59], v4 offset:31744\n"
"v_cmp_ne_u64 vcc, v[8:9], v[6:7]\n"
"v_mfma_f32_16x16x32_bf16 v[24:27], a[128:131], v[36:39], v[24:27]\n"
"s_cbranch_vccnz .L_rtm1_1\n"
".L_bkm1_1:\n"
"s_add_u32 s66, s66, 1187840\n"
"v_mfma_f32_16x16x32_bf16 v[28:31], a[132:135], v[36:39], v[28:31]\n"
"s_waitcnt vmcnt(2)\n"
"s_barrier\n"
"v_mfma_f32_16x16x32_bf16 v[28:31], a[20:23], v[32:35], v[28:31]\n"
"ds_read_b128 v[60:63], v4 offset:46080\n"
"s_waitcnt lgkmcnt(4)\n"
"v_mfma_f32_16x16x32_bf16 v[28:31], a[244:247], v[40:43], v[28:31]\n"
"ds_read_b128 v[32:35], v4 offset:4096\n"
"s_add_u32 m0, s64, 0\n"
"v_mfma_f32_16x16x32_bf16 v[28:31], v[164:167], v[44:47], v[28:31]\n"
"buffer_load_dwordx4 v2, s[32:35], s63 offen offset:0 sc1 lds\n"
"s_add_u32 s65, s65, 16384\n"
"v_mfma_f32_16x16x32_bf16 v[24:27], v[160:163], v[44:47], v[24:27]\n"
"ds_read_b128 v[36:39], v4 offset:18432\n"
"v_mfma_f32_16x16x32_bf16 v[24:27], a[240:243], v[40:43], v[24:27]\n"
"buffer_atomic_pk_add_bf16 v14, v1, s[44:47], s65 offen offset:0 sc1\n"
"s_waitcnt lgkmcnt(4)\n"
"v_mfma_f32_16x16x32_bf16 v[24:27], a[24:27], v[48:51], v[24:27]\n"
"ds_read_b128 v[40:43], v4 offset:32768\n"
"v_mfma_f32_16x16x32_bf16 v[24:27], a[136:139], v[52:55], v[24:27]\n"
"buffer_atomic_pk_add_bf16 v15, v1, s[44:47], s65 offen offset:2048 sc1\n"
"s_add_u32 s65, s65, 16384\n"
"v_mfma_f32_16x16x32_bf16 v[28:31], a[140:143], v[52:55], v[28:31]\n"
"ds_read_b128 v[44:47], v4 offset:47104\n"
"v_mfma_f32_16x16x32_bf16 v[28:31], a[28:31], v[48:51], v[28:31]\n"
"s_waitcnt lgkmcnt(4)\n"
"v_mfma_f32_16x16x32_bf16 v[28:31], a[252:255], v[56:59], v[28:31]\n"
"ds_read_b128 v[48:51], v4 offset:5120\n"
"v_mfma_f32_16x16x32_bf16 v[28:31], v[172:175], v[60:63], v[28:31]\n"
"buffer_load_dwordx4 v2, s[32:35], s63 offen offset:1024 sc1 lds\n"
"v_mfma_f32_16x16x32_bf16 v[24:27], v[168:171], v[60:63], v[24:27]\n"
"ds_read_b128 v[52:55], v4 offset:19456\n"
"v_mfma_f32_16x16x32_bf16 v[24:27], a[248:251], v[56:59], v[24:27]\n"
"s_waitcnt lgkmcnt(4)\n"
"v_mfma_f32_16x16x32_bf16 v[24:27], a[32:35], v[32:35], v[24:27]\n"
"ds_read_b128 v[56:59], v4 offset:33792\n"
"v_mfma_f32_16x16x32_bf16 v[24:27], a[144:147], v[36:39], v[24:27]\n"
"buffer_load_dwordx4 v2, s[32:35], s63 offen offset:2048 sc1 lds\n"
"v_mfma_f32_16x16x32_bf16 v[28:31], a[148:151], v[36:39], v[28:31]\n"
"ds_read_b128 v[60:63], v4 offset:48128\n"
"v_mfma_f32_16x16x32_bf16 v[28:31], a[36:39], v[32:35], v[28:31]\n"
"s_waitcnt lgkmcnt(4)\n"
"v_mfma_f32_16x16x32_bf16 v[28:31], v[68:71], v[40:43], v[28:31]\n"
"ds_read_b128 v[32:35], v4 offset:6144\n"
"v_mfma_f32_16x16x32_bf16 v[28:31], v[180:183], v[44:47], v[28:31]\n"
"buffer_load_dwordx4 v2, s[32:35], s63 offen offset:3072 sc1 lds\n"
"v_mfma_f32_16x16x32_bf16 v[24:27], v[176:179], v[44:47], v[24:27]\n"
"ds_read_b128 v[36:39], v4 offset:20480\n"
"v_mfma_f32_16x16x32_bf16 v[24:27], v[64:67], v[40:43], v[24:27]\n"
"s_waitcnt lgkmcnt(4)\n"
"v_mfma_f32_16x16x32_bf16 v[24:27], a[40:43], v[48:51], v[24:27]\n"
"ds_read_b128 v[40:43], v4 offset:34816\n"
"v_mfma_f32_16x16x32_bf16 v[24:27], a[152:155], v[52:55], v[24:27]\n"
"buffer_load_dwordx4 v[12:15], v2, s[32:35], s71 offen sc1\n"
"s_add_u32 s71, s71, 1187840\n"
"v_mfma_f32_16x16x32_bf16 v[28:31], a[156:159], v[52:55], v[28:31]\n"
"ds_read_b128 v[44:47], v4 offset:49152\n"
"s_add_u32 s63, s63, 4096\n"
"v_mfma_f32_16x16x32_bf16 v[28:31], a[44:47], v[48:51], v[28:31]\n"
"s_add_u32 m0, s64, 4096\n"
"s_waitcnt lgkmcnt(4)\n"
"v_mfma_f32_16x16x32_bf16 v[28:31], v[76:79], v[56:59], v[28:31]\n"
"ds_read_b128 v[48:51], v4 offset:7168\n"
"v_mfma_f32_16x16x32_bf16 v[28:31], v[188:191], v[60:63], v[28:31]\n"
"buffer_load_dwordx4 v2, s[32:35], s63 offen offset:0 sc1 lds\n"
"v_mfma_f32_16x16x32_bf16 v[24:27], v[184:187], v[60:63], v[24:27]\n"
"ds_read_b128 v[52:55], v4 offset:21504\n"
"v_mfma_f32_16x16x32_bf16 v[24:27], v[72:75], v[56:59], v[24:27]\n"
"s_waitcnt lgkmcnt(4)\n"
"v_mfma_f32_16x16x32_bf16 v[24:27], a[48:51], v[32:35], v[24:27]\n"
"ds_read_b128 v[56:59], v4 offset:35840\n"
"v_mfma_f32_16x16x32_bf16 v[24:27], a[160:163], v[36:39], v[24:27]\n"
"buffer_load_dwordx4 v2, s[32:35], s63 offen offset:1024 sc1 lds\n"
"v_mfma_f32_16x16x32_bf16 v[28:31], a[164:167], v[36:39], v[28:31]\n"
"ds_read_b128 v[60:63], v4 offset:50176\n"
"v_mfma_f32_16x16x32_bf16 v[28:31], a[52:55], v[32:35], v[28:31]\n"
"s_waitcnt lgkmcnt(4)\n"
"v_mfma_f32_16x16x32_bf16 v[28:31], v[84:87], v[40:43], v[28:31]\n"
"ds_read_b128 v[32:35], v4 offset:8192\n"
"v_mfma_f32_16x16x32_bf16 v[28:31], v[196:199], v[44:47], v[28:31]\n"
"buffer_load_dwordx4 v2, s[32:35], s63 offen offset:2048 sc1 lds\n"
"v_mfma_f32_16x16x32_bf16 v[24:27], v[192:195], v[44:47], v[24:27]\n"
"ds_read_b128 v[36:39], v4 offset:22528\n"
"v_mfma_f32_16x16x32_bf16 v[24:27], v[80:83], v[40:43], v[24:27]\n"
"s_waitcnt lgkmcnt(4)\n"
"v_mfma_f32_16x16x32_bf16 v[24:27], a[56:59], v[48:51], v[24:27]\n"
"ds_read_b128 v[40:43], v4 offset:36864\n"
"v_mfma_f32_16x16x32_bf16 v[24:27], a[168:171], v[52:55], v[24:27]\n"
"buffer_load_dwordx4 v2, s[32:35], s63 offen offset:3072 sc1 lds\n"
"v_mfma_f32_16x16x32_bf16 v[28:31], a[172:175], v[52:55], v[28:31]\n"
"ds_read_b128 v[44:47], v4 offset:51200\n"
"v_mfma_f32_16x16x32_bf16 v[28:31], a[60:63], v[48:51], v[28:31]\n"
"s_waitcnt lgkmcnt(4)\n"
"v_mfma_f32_16x16x32_bf16 v[28:31], v[92:95], v[56:59], v[28:31]\n"
"ds_read_b128 v[48:51], v4 offset:9216\n"
"v_mfma_f32_16x16x32_bf16 v[28:31], v[204:207], v[60:63], v[28:31]\n"
"s_add_u32 s63, s63, 4096\n"
"s_add_u32 m0, s64, 8192\n"
"v_mfma_f32_16x16x32_bf16 v[24:27], v[200:203], v[60:63], v[24:27]\n"
"ds_read_b128 v[52:55], v4 offset:23552\n"
"v_mfma_f32_16x16x32_bf16 v[24:27], v[88:91], v[56:59], v[24:27]\n"
"s_waitcnt lgkmcnt(4)\n"
"v_mfma_f32_16x16x32_bf16 v[24:27], a[64:67], v[32:35], v[24:27]\n"
"ds_read_b128 v[56:59], v4 offset:37888\n"
"v_mfma_f32_16x16x32_bf16 v[24:27], a[176:179], v[36:39], v[24:27]\n"
"buffer_load_dwordx4 v2, s[32:35], s63 offen offset:0 sc1 lds\n"
"v_mfma_f32_16x16x32_bf16 v[28:31], a[180:183], v[36:39], v[28:31]\n"
"ds_read_b128 v[60:63], v4 offset:52224\n"
"v_mfma_f32_16x16x32_bf16 v[28:31], a[68:71], v[32:35], v[28:31]\n"
"s_waitcnt lgkmcnt(4)\n"
"v_mfma_f32_16x16x32_bf16 v[28:31], v[100:103], v[40:43], v[28:31]\n"
"ds_read_b128 v[32:35], v4 offset:10240\n"
"v_mfma_f32_16x16x32_bf16 v[28:31], v[212:215], v[44:47], v[28:31]\n"
"buffer_load_dwordx4 v2, s[32:35], s63 offen offset:1024 sc1 lds\n"
"v_mfma_f32_16x16x32_bf16 v[24:27], v[208:211], v[44:47], v[24:27]\n"
"ds_read_b128 v[36:39], v4 offset:24576\n"
"v_mfma_f32_16x16x32_bf16 v[24:27], v[96:99], v[40:43], v[24:27]\n"
"s_waitcnt lgkmcnt(4)\n"
"v_mfma_f32_16x16x32_bf16 v[24:27], a[72:75], v[48:51], v[24:27]\n"
"ds_read_b128 v[40:43], v4 offset:38912\n"
"v_mfma_f32_16x16x32_bf16 v[24:27], a[184:187], v[52:55], v[24:27]\n"
"buffer_load_dwordx4 v2, s[32:35], s63 offen offset:2048 sc1 lds\n"
"v_mfma_f32_16x16x32_bf16 v[28:31], a[188:191], v[52:55], v[28:31]\n"
"ds_read_b128 v[44:47], v4 offset:53248\n"
"v_mfma_f32_16x16x32_bf16 v[28:31], a[76:79], v[48:51], v[28:31]\n"
"s_waitcnt lgkmcnt(4)\n"
"v_mfma_f32_16x16x32_bf16 v[28:31], v[108:111], v[56:59], v[28:31]\n"
"ds_read_b128 v[48:51], v4 offset:11264\n"
"v_mfma_f32_16x16x32_bf16 v[28:31], v[220:223], v[60:63], v[28:31]\n"
"buffer_load_dwordx2 v[6:7], v0, s[32:35], s66 offen sc1\n"
"v_mfma_f32_16x16x32_bf16 v[24:27], v[216:219], v[60:63], v[24:27]\n"
"ds_read_b128 v[52:55], v4 offset:25600\n"
"v_mfma_f32_16x16x32_bf16 v[24:27], v[104:107], v[56:59], v[24:27]\n"
"s_waitcnt lgkmcnt(4)\n"
"v_mfma_f32_16x16x32_bf16 v[24:27], a[80:83], v[32:35], v[24:27]\n"
"ds_read_b128 v[56:59], v4 offset:39936\n"
"v_mfma_f32_16x16x32_bf16 v[24:27], a[192:195], v[36:39], v[24:27]\n"
"buffer_load_dwordx4 v2, s[32:35], s63 offen offset:3072 sc1 lds\n"
"s_add_u32 s63, s63, 4096\n"
"v_mfma_f32_16x16x32_bf16 v[28:31], a[196:199], v[36:39], v[28:31]\n"
"ds_read_b128 v[60:63], v4 offset:54272\n"
"s_add_u32 m0, s64, 12288\n"
"v_mfma_f32_16x16x32_bf16 v[28:31], a[84:87], v[32:35], v[28:31]\n"
"s_waitcnt lgkmcnt(4)\n"
"v_mfma_f32_16x16x32_bf16 v[28:31], v[116:119], v[40:43], v[28:31]\n"
"ds_read_b128 v[32:35], v4 offset:12288\n"
"v_mfma_f32_16x16x32_bf16 v[28:31], v[228:231], v[44:47], v[28:31]\n"
"buffer_load_dwordx4 v2, s[32:35], s63 offen offset:0 sc1 lds\n"
"v_mfma_f32_16x16x32_bf16 v[24:27], v[224:227], v[44:47], v[24:27]\n"
"ds_read_b128 v[36:39], v4 offset:26624\n"
"v_mfma_f32_16x16x32_bf16 v[24:27], v[112:115], v[40:43], v[24:27]\n"
"s_waitcnt lgkmcnt(4)\n"
"v_mfma_f32_16x16x32_bf16 v[24:27], a[88:91], v[48:51], v[24:27]\n"
"ds_read_b128 v[40:43], v4 offset:40960\n"
"v_mfma_f32_16x16x32_bf16 v[24:27], a[200:203], v[52:55], v[24:27]\n"
"buffer_load_dwordx4 v2, s[32:35], s63 offen offset:1024 sc1 lds\n"
"s_add_u32 s63, s63, 1175552\n"
"v_mfma_f32_16x16x32_bf16 v[28:31], a[204:207], v[52:55], v[28:31]\n"
"ds_read_b128 v[44:47], v4 offset:55296\n"
"v_mfma_f32_16x16x32_bf16 v[28:31], a[92:95], v[48:51], v[28:31]\n"
"s_waitcnt lgkmcnt(4)\n"
"v_mfma_f32_16x16x32_bf16 v[28:31], v[124:127], v[56:59], v[28:31]\n"
"ds_read_b128 v[48:51], v4 offset:13312\n"
"s_waitcnt vmcnt(11)\n"
"v_mfma_f32_16x16x32_bf16 v[28:31], v[236:239], v[60:63], v[28:31]\n"
"v_lshlrev_b32 v16, 16, v12\n"
"v_and_b32 v17, 0xffff0000, v12\n"
"v_mfma_f32_16x16x32_bf16 v[24:27], v[232:235], v[60:63], v[24:27]\n"
"ds_read_b128 v[52:55], v4 offset:27648\n"
"v_lshlrev_b32 v18, 16, v13\n"
"v_mfma_f32_16x16x32_bf16 v[24:27], v[120:123], v[56:59], v[24:27]\n"
"v_and_b32 v19, 0xffff0000, v13\n"
"s_waitcnt lgkmcnt(4)\n"
"v_mfma_f32_16x16x32_bf16 v[24:27], a[96:99], v[32:35], v[24:27]\n"
"ds_read_b128 v[56:59], v4 offset:41984\n"
"v_lshlrev_b32 v20, 16, v14\n"
"v_mfma_f32_16x16x32_bf16 v[24:27], a[208:211], v[36:39], v[24:27]\n"
"v_and_b32 v21, 0xffff0000, v14\n"
"v_lshlrev_b32 v22, 16, v15\n"
"v_mfma_f32_16x16x32_bf16 v[28:31], a[212:215], v[36:39], v[28:31]\n"
"s_barrier\n"
"v_mfma_f32_16x16x32_bf16 v[28:31], a[100:103], v[32:35], v[28:31]\n"
"ds_read_b128 v[60:63], v4 offset:56320\n"
"s_waitcnt lgkmcnt(4)\n"
"v_mfma_f32_16x16x32_bf16 v[28:31], v[132:135], v[40:43], v[28:31]\n"
"ds_read_b128 v[32:35], v2 offset:0\n"
"v_and_b32 v23, 0xffff0000, v15\n"
"v_mfma_f32_16x16x32_bf16 v[28:31], v[244:247], v[44:47], v[28:31]\n"
"v_mfma_f32_16x16x32_bf16 v[24:27], v[240:243], v[44:47], v[24:27]\n"
"ds_read_b128 v[36:39], v2 offset:14336\n"
"v_mfma_f32_16x16x32_bf16 v[24:27], v[128:131], v[40:43], v[24:27]\n"
"s_waitcnt lgkmcnt(4)\n"
"v_mfma_f32_16x16x32_bf16 v[24:27], a[104:107], v[48:51], v[24:27]\n"
"ds_read_b128 v[40:43], v2 offset:28672\n"
"v_mfma_f32_16x16x32_bf16 v[24:27], a[216:219], v[52:55], v[24:27]\n"
"v_mfma_f32_16x16x32_bf16 v[28:31], a[220:223], v[52:55], v[28:31]\n"
"ds_read_b128 v[44:47], v2 offset:43008\n"
"v_mfma_f32_16x16x32_bf16 v[28:31], a[108:111], v[48:51], v[28:31]\n"
"s_waitcnt lgkmcnt(4)\n"
"v_mfma_f32_16x16x32_bf16 v[28:31], v[140:143], v[56:59], v[28:31]\n"
"ds_read_b128 v[48:51], v2 offset:1024\n"
"v_mfma_f32_16x16x32_bf16 v[28:31], v[252:255], v[60:63], v[28:31]\n"
"v_mfma_f32_16x16x32_bf16 v[24:27], v[248:251], v[60:63], v[24:27]\n"
"ds_read_b128 v[52:55], v2 offset:15360\n"
"s_sub_u32 s62, s62, 1\n"
"v_mfma_f32_16x16x32_bf16 v[24:27], v[136:139], v[56:59], v[24:27]\n"
"s_cbranch_scc1 .L_dmt_1\n"
"s_waitcnt lgkmcnt(4)\n"
"v_mfma_f32_16x16x32_bf16 v[16:19], a[0:3], v[32:35], v[16:19]\n"
"ds_read_b128 v[56:59], v2 offset:29696\n"
"v_mfma_f32_16x16x32_bf16 v[16:19], a[112:115], v[36:39], v[16:19]\n"
"v_mfma_f32_16x16x32_bf16 v[20:23], a[116:119], v[36:39], v[20:23]\n"
"ds_read_b128 v[60:63], v2 offset:44032\n"
"v_mfma_f32_16x16x32_bf16 v[20:23], a[4:7], v[32:35], v[20:23]\n"
"v_cvt_pk_bf16_f32 v15, v30, v31\n"
"s_waitcnt lgkmcnt(4)\n"
"v_mfma_f32_16x16x32_bf16 v[20:23], a[228:231], v[40:43], v[20:23]\n"
"ds_read_b128 v[32:35], v2 offset:2048\n"
"v_cvt_pk_bf16_f32 v14, v28, v29\n"
"v_mfma_f32_16x16x32_bf16 v[20:23], v[148:151], v[44:47], v[20:23]\n"
"v_cvt_pk_bf16_f32 v13, v26, v27\n"
"v_cvt_pk_bf16_f32 v12, v24, v25\n"
"v_mfma_f32_16x16x32_bf16 v[16:19], v[144:147], v[44:47], v[16:19]\n"
"ds_read_b128 v[36:39], v2 offset:16384\n"
"ds_write_b128 v5, v[12:15]\n"
"v_mfma_f32_16x16x32_bf16 v[16:19], a[224:227], v[40:43], v[16:19]\n"
"ds_read2st64_b32 v[12:13], v3 offset0:0 offset1:1\n"
"s_waitcnt lgkmcnt(6)\n"
"v_mfma_f32_16x16x32_bf16 v[16:19], a[8:11], v[48:51], v[16:19]\n"
"ds_read_b128 v[40:43], v2 offset:30720\n"
"ds_read2st64_b32 v[14:15], v3 offset0:2 offset1:3\n"
"v_mfma_f32_16x16x32_bf16 v[16:19], a[120:123], v[52:55], v[16:19]\n"
"v_mfma_f32_16x16x32_bf16 v[20:23], a[124:127], v[52:55], v[20:23]\n"
"ds_read_b128 v[44:47], v2 offset:45056\n"
"v_mfma_f32_16x16x32_bf16 v[20:23], a[12:15], v[48:51], v[20:23]\n"
"s_waitcnt lgkmcnt(7)\n"
"v_mfma_f32_16x16x32_bf16 v[20:23], a[236:239], v[56:59], v[20:23]\n"
"ds_read_b128 v[48:51], v2 offset:3072\n"
"v_mfma_f32_16x16x32_bf16 v[20:23], v[156:159], v[60:63], v[20:23]\n"
"s_waitcnt lgkmcnt(2)\n"
"buffer_atomic_pk_add_bf16 v12, v1, s[44:47], s65 offen offset:0 sc1\n"
"v_mfma_f32_16x16x32_bf16 v[16:19], v[152:155], v[60:63], v[16:19]\n"
"ds_read_b128 v[52:55], v2 offset:17408\n"
"s_waitcnt vmcnt(4)\n"
"v_mfma_f32_16x16x32_bf16 v[16:19], a[232:235], v[56:59], v[16:19]\n"
"buffer_atomic_pk_add_bf16 v13, v1, s[44:47], s65 offen offset:2048 sc1\n"
"v_mfma_f32_16x16x32_bf16 v[16:19], a[16:19], v[32:35], v[16:19]\n"
"ds_read_b128 v[56:59], v2 offset:31744\n"
"v_cmp_ne_u64 vcc, v[8:9], v[6:7]\n"
"v_mfma_f32_16x16x32_bf16 v[16:19], a[128:131], v[36:39], v[16:19]\n"
"s_cbranch_vccnz .L_rtm0_1\n"
".L_bkm0_1:\n"
"s_add_u32 s66, s66, 1187840\n"
"v_mfma_f32_16x16x32_bf16 v[20:23], a[132:135], v[36:39], v[20:23]\n"
"s_waitcnt vmcnt(2)\n"
"s_barrier\n"
"v_mfma_f32_16x16x32_bf16 v[20:23], a[20:23], v[32:35], v[20:23]\n"
"ds_read_b128 v[60:63], v2 offset:46080\n"
"s_waitcnt lgkmcnt(4)\n"
"v_mfma_f32_16x16x32_bf16 v[20:23], a[244:247], v[40:43], v[20:23]\n"
"ds_read_b128 v[32:35], v2 offset:4096\n"
"s_add_u32 m0, s64, 57344\n"
"v_mfma_f32_16x16x32_bf16 v[20:23], v[164:167], v[44:47], v[20:23]\n"
"buffer_load_dwordx4 v2, s[32:35], s63 offen offset:0 sc1 lds\n"
"s_add_u32 s65, s65, 16384\n"
"v_mfma_f32_16x16x32_bf16 v[16:19], v[160:163], v[44:47], v[16:19]\n"
"ds_read_b128 v[36:39], v2 offset:18432\n"
"v_mfma_f32_16x16x32_bf16 v[16:19], a[240:243], v[40:43], v[16:19]\n"
"buffer_atomic_pk_add_bf16 v14, v1, s[44:47], s65 offen offset:0 sc1\n"
"s_waitcnt lgkmcnt(4)\n"
"v_mfma_f32_16x16x32_bf16 v[16:19], a[24:27], v[48:51], v[16:19]\n"
"ds_read_b128 v[40:43], v2 offset:32768\n"
"v_mfma_f32_16x16x32_bf16 v[16:19], a[136:139], v[52:55], v[16:19]\n"
"buffer_atomic_pk_add_bf16 v15, v1, s[44:47], s65 offen offset:2048 sc1\n"
"s_add_u32 s65, s65, 16384\n"
"v_mfma_f32_16x16x32_bf16 v[20:23], a[140:143], v[52:55], v[20:23]\n"
"ds_read_b128 v[44:47], v2 offset:47104\n"
"v_mfma_f32_16x16x32_bf16 v[20:23], a[28:31], v[48:51], v[20:23]\n"
"s_waitcnt lgkmcnt(4)\n"
"v_mfma_f32_16x16x32_bf16 v[20:23], a[252:255], v[56:59], v[20:23]\n"
"ds_read_b128 v[48:51], v2 offset:5120\n"
"v_mfma_f32_16x16x32_bf16 v[20:23], v[172:175], v[60:63], v[20:23]\n"
"buffer_load_dwordx4 v2, s[32:35], s63 offen offset:1024 sc1 lds\n"
"v_mfma_f32_16x16x32_bf16 v[16:19], v[168:171], v[60:63], v[16:19]\n"
"ds_read_b128 v[52:55], v2 offset:19456\n"
"v_mfma_f32_16x16x32_bf16 v[16:19], a[248:251], v[56:59], v[16:19]\n"
"s_waitcnt lgkmcnt(4)\n"
"v_mfma_f32_16x16x32_bf16 v[16:19], a[32:35], v[32:35], v[16:19]\n"
"ds_read_b128 v[56:59], v2 offset:33792\n"
"v_mfma_f32_16x16x32_bf16 v[16:19], a[144:147], v[36:39], v[16:19]\n"
"buffer_load_dwordx4 v2, s[32:35], s63 offen offset:2048 sc1 lds\n"
"v_mfma_f32_16x16x32_bf16 v[20:23], a[148:151], v[36:39], v[20:23]\n"
"ds_read_b128 v[60:63], v2 offset:48128\n"
"v_mfma_f32_16x16x32_bf16 v[20:23], a[36:39], v[32:35], v[20:23]\n"
"s_waitcnt lgkmcnt(4)\n"
"v_mfma_f32_16x16x32_bf16 v[20:23], v[68:71], v[40:43], v[20:23]\n"
"ds_read_b128 v[32:35], v2 offset:6144\n"
"v_mfma_f32_16x16x32_bf16 v[20:23], v[180:183], v[44:47], v[20:23]\n"
"buffer_load_dwordx4 v2, s[32:35], s63 offen offset:3072 sc1 lds\n"
"v_mfma_f32_16x16x32_bf16 v[16:19], v[176:179], v[44:47], v[16:19]\n"
"ds_read_b128 v[36:39], v2 offset:20480\n"
"v_mfma_f32_16x16x32_bf16 v[16:19], v[64:67], v[40:43], v[16:19]\n"
"s_waitcnt lgkmcnt(4)\n"
"v_mfma_f32_16x16x32_bf16 v[16:19], a[40:43], v[48:51], v[16:19]\n"
"ds_read_b128 v[40:43], v2 offset:34816\n"
"v_mfma_f32_16x16x32_bf16 v[16:19], a[152:155], v[52:55], v[16:19]\n"
"buffer_load_dwordx4 v[12:15], v2, s[32:35], s71 offen sc1\n"
"s_add_u32 s71, s71, 1187840\n"
"v_mfma_f32_16x16x32_bf16 v[20:23], a[156:159], v[52:55], v[20:23]\n"
"ds_read_b128 v[44:47], v2 offset:49152\n"
"s_add_u32 s63, s63, 4096\n"
"v_mfma_f32_16x16x32_bf16 v[20:23], a[44:47], v[48:51], v[20:23]\n"
"s_add_u32 m0, s64, 61440\n"
"s_waitcnt lgkmcnt(4)\n"
"v_mfma_f32_16x16x32_bf16 v[20:23], v[76:79], v[56:59], v[20:23]\n"
"ds_read_b128 v[48:51], v2 offset:7168\n"
"v_mfma_f32_16x16x32_bf16 v[20:23], v[188:191], v[60:63], v[20:23]\n"
"buffer_load_dwordx4 v2, s[32:35], s63 offen offset:0 sc1 lds\n"
"v_mfma_f32_16x16x32_bf16 v[16:19], v[184:187], v[60:63], v[16:19]\n"
"ds_read_b128 v[52:55], v2 offset:21504\n"
"v_mfma_f32_16x16x32_bf16 v[16:19], v[72:75], v[56:59], v[16:19]\n"
"s_waitcnt lgkmcnt(4)\n"
"v_mfma_f32_16x16x32_bf16 v[16:19], a[48:51], v[32:35], v[16:19]\n"
"ds_read_b128 v[56:59], v2 offset:35840\n"
"v_mfma_f32_16x16x32_bf16 v[16:19], a[160:163], v[36:39], v[16:19]\n"
"buffer_load_dwordx4 v2, s[32:35], s63 offen offset:1024 sc1 lds\n"
"v_mfma_f32_16x16x32_bf16 v[20:23], a[164:167], v[36:39], v[20:23]\n"
"ds_read_b128 v[60:63], v2 offset:50176\n"
"v_mfma_f32_16x16x32_bf16 v[20:23], a[52:55], v[32:35], v[20:23]\n"
"s_waitcnt lgkmcnt(4)\n"
"v_mfma_f32_16x16x32_bf16 v[20:23], v[84:87], v[40:43], v[20:23]\n"
"ds_read_b128 v[32:35], v2 offset:8192\n"
"v_mfma_f32_16x16x32_bf16 v[20:23], v[196:199], v[44:47], v[20:23]\n"
"buffer_load_dwordx4 v2, s[32:35], s63 offen offset:2048 sc1 lds\n"
"v_mfma_f32_16x16x32_bf16 v[16:19], v[192:195], v[44:47], v[16:19]\n"
"ds_read_b128 v[36:39], v2 offset:22528\n"
"v_mfma_f32_16x16x32_bf16 v[16:19], v[80:83], v[40:43], v[16:19]\n"
"s_waitcnt lgkmcnt(4)\n"
"v_mfma_f32_16x16x32_bf16 v[16:19], a[56:59], v[48:51], v[16:19]\n"
"ds_read_b128 v[40:43], v2 offset:36864\n"
"v_mfma_f32_16x16x32_bf16 v[16:19], a[168:171], v[52:55], v[16:19]\n"
"buffer_load_dwordx4 v2, s[32:35], s63 offen offset:3072 sc1 lds\n"
"v_mfma_f32_16x16x32_bf16 v[20:23], a[172:175], v[52:55], v[20:23]\n"
"ds_read_b128 v[44:47], v2 offset:51200\n"
"v_mfma_f32_16x16x32_bf16 v[20:23], a[60:63], v[48:51], v[20:23]\n"
"s_waitcnt lgkmcnt(4)\n"
"v_mfma_f32_16x16x32_bf16 v[20:23], v[92:95], v[56:59], v[20:23]\n"
"ds_read_b128 v[48:51], v2 offset:9216\n"
"v_mfma_f32_16x16x32_bf16 v[20:23], v[204:207], v[60:63], v[20:23]\n"
"s_add_u32 s63, s63, 4096\n"
"s_add_u32 m0, s64, 65536\n"
"v_mfma_f32_16x16x32_bf16 v[16:19], v[200:203], v[60:63], v[16:19]\n"
"ds_read_b128 v[52:55], v2 offset:23552\n"
"v_mfma_f32_16x16x32_bf16 v[16:19], v[88:91], v[56:59], v[16:19]\n"
"s_waitcnt lgkmcnt(4)\n"
"v_mfma_f32_16x16x32_bf16 v[16:19], a[64:67], v[32:35], v[16:19]\n"
"ds_read_b128 v[56:59], v2 offset:37888\n"
"v_mfma_f32_16x16x32_bf16 v[16:19], a[176:179], v[36:39], v[16:19]\n"
"buffer_load_dwordx4 v2, s[32:35], s63 offen offset:0 sc1 lds\n"
"v_mfma_f32_16x16x32_bf16 v[20:23], a[180:183], v[36:39], v[20:23]\n"
"ds_read_b128 v[60:63], v2 offset:52224\n"
"v_mfma_f32_16x16x32_bf16 v[20:23], a[68:71], v[32:35], v[20:23]\n"
"s_waitcnt lgkmcnt(4)\n"
"v_mfma_f32_16x16x32_bf16 v[20:23], v[100:103], v[40:43], v[20:23]\n"
"ds_read_b128 v[32:35], v2 offset:10240\n"
"v_mfma_f32_16x16x32_bf16 v[20:23], v[212:215], v[44:47], v[20:23]\n"
"buffer_load_dwordx4 v2, s[32:35], s63 offen offset:1024 sc1 lds\n"
"v_mfma_f32_16x16x32_bf16 v[16:19], v[208:211], v[44:47], v[16:19]\n"
"ds_read_b128 v[36:39], v2 offset:24576\n"
"v_mfma_f32_16x16x32_bf16 v[16:19], v[96:99], v[40:43], v[16:19]\n"
"s_waitcnt lgkmcnt(4)\n"
"v_mfma_f32_16x16x32_bf16 v[16:19], a[72:75], v[48:51], v[16:19]\n"
"ds_read_b128 v[40:43], v2 offset:38912\n"
"v_mfma_f32_16x16x32_bf16 v[16:19], a[184:187], v[52:55], v[16:19]\n"
"buffer_load_dwordx4 v2, s[32:35], s63 offen offset:2048 sc1 lds\n"
"v_mfma_f32_16x16x32_bf16 v[20:23], a[188:191], v[52:55], v[20:23]\n"
"ds_read_b128 v[44:47], v2 offset:53248\n"
"v_mfma_f32_16x16x32_bf16 v[20:23], a[76:79], v[48:51], v[20:23]\n"
"s_waitcnt lgkmcnt(4)\n"
"v_mfma_f32_16x16x32_bf16 v[20:23], v[108:111], v[56:59], v[20:23]\n"
"ds_read_b128 v[48:51], v2 offset:11264\n"
"v_mfma_f32_16x16x32_bf16 v[20:23], v[220:223], v[60:63], v[20:23]\n"
"buffer_load_dwordx2 v[6:7], v0, s[32:35], s66 offen sc1\n"
"v_mfma_f32_16x16x32_bf16 v[16:19], v[216:219], v[60:63], v[16:19]\n"
"ds_read_b128 v[52:55], v2 offset:25600\n"
"v_mfma_f32_16x16x32_bf16 v[16:19], v[104:107], v[56:59], v[16:19]\n"
"s_waitcnt lgkmcnt(4)\n"
"v_mfma_f32_16x16x32_bf16 v[16:19], a[80:83], v[32:35], v[16:19]\n"
"ds_read_b128 v[56:59], v2 offset:39936\n"
"v_mfma_f32_16x16x32_bf16 v[16:19], a[192:195], v[36:39], v[16:19]\n"
"buffer_load_dwordx4 v2, s[32:35], s63 offen offset:3072 sc1 lds\n"
"s_add_u32 s63, s63, 4096\n"
"v_mfma_f32_16x16x32_bf16 v[20:23], a[196:199], v[36:39], v[20:23]\n"
"ds_read_b128 v[60:63], v2 offset:54272\n"
"s_add_u32 m0, s64, 69632\n"
"v_mfma_f32_16x16x32_bf16 v[20:23], a[84:87], v[32:35], v[20:23]\n"
"s_waitcnt lgkmcnt(4)\n"
"v_mfma_f32_16x16x32_bf16 v[20:23], v[116:119], v[40:43], v[20:23]\n"
"ds_read_b128 v[32:35], v2 offset:12288\n"
"v_mfma_f32_16x16x32_bf16 v[20:23], v[228:231], v[44:47], v[20:23]\n"
"buffer_load_dwordx4 v2, s[32:35], s63 offen offset:0 sc1 lds\n"
"v_mfma_f32_16x16x32_bf16 v[16:19], v[224:227], v[44:47], v[16:19]\n"
"ds_read_b128 v[36:39], v2 offset:26624\n"
"v_mfma_f32_16x16x32_bf16 v[16:19], v[112:115], v[40:43], v[16:19]\n"
"s_waitcnt lgkmcnt(4)\n"
"v_mfma_f32_16x16x32_bf16 v[16:19], a[88:91], v[48:51], v[16:19]\n"
"ds_read_b128 v[40:43], v2 offset:40960\n"
"v_mfma_f32_16x16x32_bf16 v[16:19], a[200:203], v[52:55], v[16:19]\n"
"buffer_load_dwordx4 v2, s[32:35], s63 offen offset:1024 sc1 lds\n"
"s_add_u32 s63, s63, 1175552\n"
"v_mfma_f32_16x16x32_bf16 v[20:23], a[204:207], v[52:55], v[20:23]\n"
"ds_read_b128 v[44:47], v2 offset:55296\n"
"v_mfma_f32_16x16x32_bf16 v[20:23], a[92:95], v[48:51], v[20:23]\n"
"s_waitcnt lgkmcnt(4)\n"
"v_mfma_f32_16x16x32_bf16 v[20:23], v[124:127], v[56:59], v[20:23]\n"
"ds_read_b128 v[48:51], v2 offset:13312\n"
"s_waitcnt vmcnt(11)\n"
"v_mfma_f32_16x16x32_bf16 v[20:23], v[236:239], v[60:63], v[20:23]\n"
"v_lshlrev_b32 v24, 16, v12\n"
"v_and_b32 v25, 0xffff0000, v12\n"
"v_mfma_f32_16x16x32_bf16 v[16:19], v[232:235], v[60:63], v[16:19]\n"
"ds_read_b128 v[52:55], v2 offset:27648\n"
"v_lshlrev_b32 v26, 16, v13\n"
"v_mfma_f32_16x16x32_bf16 v[16:19], v[120:123], v[56:59], v[16:19]\n"
"v_and_b32 v27, 0xffff0000, v13\n"
"s_waitcnt lgkmcnt(4)\n"
"v_mfma_f32_16x16x32_bf16 v[16:19], a[96:99], v[32:35], v[16:19]\n"
"ds_read_b128 v[56:59], v2 offset:41984\n"
"v_lshlrev_b32 v28, 16, v14\n"
"v_mfma_f32_16x16x32_bf16 v[16:19], a[208:211], v[36:39], v[16:19]\n"
"v_and_b32 v29, 0xffff0000, v14\n"
"v_lshlrev_b32 v30, 16, v15\n"
"v_mfma_f32_16x16x32_bf16 v[20:23], a[212:215], v[36:39], v[20:23]\n"
"s_barrier\n"
"v_mfma_f32_16x16x32_bf16 v[20:23], a[100:103], v[32:35], v[20:23]\n"
"ds_read_b128 v[60:63], v2 offset:56320\n"
"s_waitcnt lgkmcnt(4)\n"
"v_mfma_f32_16x16x32_bf16 v[20:23], v[132:135], v[40:43], v[20:23]\n"
"ds_read_b128 v[32:35], v4 offset:0\n"
"v_and_b32 v31, 0xffff0000, v15\n"
"v_mfma_f32_16x16x32_bf16 v[20:23], v[244:247], v[44:47], v[20:23]\n"
"v_mfma_f32_16x16x32_bf16 v[16:19], v[240:243], v[44:47], v[16:19]\n"
"ds_read_b128 v[36:39], v4 offset:14336\n"
"v_mfma_f32_16x16x32_bf16 v[16:19], v[128:131], v[40:43], v[16:19]\n"
"s_waitcnt lgkmcnt(4)\n"
"v_mfma_f32_16x16x32_bf16 v[16:19], a[104:107], v[48:51], v[16:19]\n"
"ds_read_b128 v[40:43], v4 offset:28672\n"
"v_mfma_f32_16x16x32_bf16 v[16:19], a[216:219], v[52:55], v[16:19]\n"
"v_mfma_f32_16x16x32_bf16 v[20:23], a[220:223], v[52:55], v[20:23]\n"
"ds_read_b128 v[44:47], v4 offset:43008\n"
"v_mfma_f32_16x16x32_bf16 v[20:23], a[108:111], v[48:51], v[20:23]\n"
"s_waitcnt lgkmcnt(4)\n"
"v_mfma_f32_16x16x32_bf16 v[20:23], v[140:143], v[56:59], v[20:23]\n"
"ds_read_b128 v[48:51], v4 offset:1024\n"
"v_mfma_f32_16x16x32_bf16 v[20:23], v[252:255], v[60:63], v[20:23]\n"
"v_mfma_f32_16x16x32_bf16 v[16:19], v[248:251], v[60:63], v[16:19]\n"
"ds_read_b128 v[52:55], v4 offset:15360\n"
"v_mfma_f32_16x16x32_bf16 v[16:19], v[136:139], v[56:59], v[16:19]\n"
"s_branch .L_dml_1\n"
".L_dmt_1:\n"
"s_nop 1\n"
"s_nop 0\n"
"v_cvt_pk_bf16_f32 v15, v30, v31\n"
"v_cvt_pk_bf16_f32 v14, v28, v29\n"
"s_nop 1\n"
"v_cvt_pk_bf16_f32 v13, v26, v27\n"
"v_cvt_pk_bf16_f32 v12, v24, v25\n"
"ds_write_b128 v5, v[12:15]\n"
"ds_read2st64_b32 v[12:13], v3 offset0:0 offset1:1\n"
"ds_read2st64_b32 v[14:15], v3 offset0:2 offset1:3\n"
"s_waitcnt lgkmcnt(0)\n"
"buffer_atomic_pk_add_bf16 v12, v1, s[44:47], s65 offen offset:0 sc1\n"
"buffer_atomic_pk_add_bf16 v13, v1, s[44:47], s65 offen offset:2048 sc1\n"
"s_add_u32 s65, s65, 16384\n"
"buffer_atomic_pk_add_bf16 v14, v1, s[44:47], s65 offen offset:0 sc1\n"
"buffer_atomic_pk_add_bf16 v15, v1, s[44:47], s65 offen offset:2048 sc1\n"
"s_add_u32 s65, s65, 16384\n"
"s_branch .L_end\n"
".p2align 6\n"
".L_dlv2:\n"
".L_dll_2:\n"
"s_waitcnt lgkmcnt(4)\n"
"v_mfma_f32_16x16x32_bf16 v[16:19], a[0:3], v[32:35], v[16:19]\n"
"ds_read_b128 v[56:59], v2 offset:29696\n"
"v_mfma_f32_16x16x32_bf16 v[16:19], a[112:115], v[36:39], v[16:19]\n"
"v_mfma_f32_16x16x32_bf16 v[20:23], a[116:119], v[36:39], v[20:23]\n"
"ds_read_b128 v[60:63], v2 offset:44032\n"
"v_mfma_f32_16x16x32_bf16 v[20:23], a[4:7], v[32:35], v[20:23]\n"
"s_add_u32 s68, s68, 1187840\n"
"s_waitcnt lgkmcnt(4)\n"
"v_mfma_f32_16x16x32_bf16 v[20:23], a[228:231], v[40:43], v[20:23]\n"
"ds_read_b128 v[32:35], v2 offset:2048\n"
"v_mfma_f32_16x16x32_bf16 v[20:23], v[148:151], v[44:47], v[20:23]\n"
"buffer_store_dwordx4 v[28:31], v2, s[32:35], s68 offen offset:1024 sc1\n"
"v_mfma_f32_16x16x32_bf16 v[16:19], v[144:147], v[44:47], v[16:19]\n"
"ds_read_b128 v[36:39], v2 offset:16384\n"
"v_mfma_f32_16x16x32_bf16 v[16:19], a[224:227], v[40:43], v[16:19]\n"
"s_waitcnt lgkmcnt(4)\n"
"v_mfma_f32_16x16x32_bf16 v[16:19], a[8:11], v[48:51], v[16:19]\n"
"ds_read_b128 v[40:43], v2 offset:30720\n"
"v_mfma_f32_16x16x32_bf16 v[16:19], a[120:123], v[52:55], v[16:19]\n"
"buffer_store_dwordx4 v[24:27], v2, s[32:35], s68 offen sc1\n"
"s_mov_b32 s67, s68\n"
"v_mfma_f32_16x16x32_bf16 v[20:23], a[124:127], v[52:55], v[20:23]\n"
"ds_read_b128 v[44:47], v2 offset:45056\n"
"v_mfma_f32_16x16x32_bf16 v[20:23], a[12:15], v[48:51], v[20:23]\n"
"s_waitcnt lgkmcnt(4)\n"
"v_mfma_f32_16x16x32_bf16 v[20:23], a[236:239], v[56:59], v[20:23]\n"
"ds_read_b128 v[48:51], v2 offset:3072\n"
"v_mfma_f32_16x16x32_bf16 v[20:23], v[156:159], v[60:63], v[20:23]\n"
"v_mfma_f32_16x16x32_bf16 v[16:19], v[152:155], v[60:63], v[16:19]\n"
"ds_read_b128 v[52:55], v2 offset:17408\n"
"v_mfma_f32_16x16x32_bf16 v[16:19], a[232:235], v[56:59], v[16:19]\n"
"s_waitcnt lgkmcnt(4)\n"
"v_mfma_f32_16x16x32_bf16 v[16:19], a[16:19], v[32:35], v[16:19]\n"
"ds_read_b128 v[56:59], v2 offset:31744\n"
"v_mfma_f32_16x16x32_bf16 v[16:19], a[128:131], v[36:39], v[16:19]\n"
"s_waitcnt vmcnt(2)\n"
"s_barrier\n"
"v_mfma_f32_16x16x32_bf16 v[20:23], a[132:135], v[36:39], v[20:23]\n"
"ds_read_b128 v[60:63], v2 offset:46080\n"
"v_mfma_f32_16x16x32_bf16 v[20:23], a[20:23], v[32:35], v[20:23]\n"
"s_waitcnt lgkmcnt(4)\n"
"v_mfma_f32_16x16x32_bf16 v[20:23], a[244:247], v[40:43], v[20:23]\n"
"ds_read_b128 v[32:35], v2 offset:4096\n"
"s_add_u32 m0, s64, 57344\n"
"v_mfma_f32_16x16x32_bf16 v[20:23], v[164:167], v[44:47], v[20:23]\n"
"buffer_load_dwordx4 v2, s[32:35], s63 offen offset:0 sc1 lds\n"
"v_mfma_f32_16x16x32_bf16 v[16:19], v[160:163], v[44:47], v[16:19]\n"
"ds_read_b128 v[36:39], v2 offset:18432\n"
"v_mfma_f32_16x16x32_bf16 v[16:19], a[240:243], v[40:43], v[16:19]\n"
"s_waitcnt lgkmcnt(4)\n"
"v_mfma_f32_16x16x32_bf16 v[16:19], a[24:27], v[48:51], v[16:19]\n"
"ds_read_b128 v[40:43], v2 offset:32768\n"
"v_mfma_f32_16x16x32_bf16 v[16:19], a[136:139], v[52:55], v[16:19]\n"
"v_mfma_f32_16x16x32_bf16 v[20:23], a[140:143], v[52:55], v[20:23]\n"
"ds_read_b128 v[44:47], v2 offset:47104\n"
"v_mfma_f32_16x16x32_bf16 v[20:23], a[28:31], v[48:51], v[20:23]\n"
"s_waitcnt lgkmcnt(4)\n"
"v_mfma_f32_16x16x32_bf16 v[20:23], a[252:255], v[56:59], v[20:23]\n"
"ds_read_b128 v[48:51], v2 offset:5120\n"
"v_mfma_f32_16x16x32_bf16 v[20:23], v[172:175], v[60:63], v[20:23]\n"
"buffer_load_dwordx4 v2, s[32:35], s63 offen offset:1024 sc1 lds\n"
"v_mfma_f32_16x16x32_bf16 v[16:19], v[168:171], v[60:63], v[16:19]\n"
"ds_read_b128 v[52:55], v2 offset:19456\n"
"v_mfma_f32_16x16x32_bf16 v[16:19], a[248:251], v[56:59], v[16:19]\n"
"s_waitcnt lgkmcnt(4)\n"
"v_mfma_f32_16x16x32_bf16 v[16:19], a[32:35], v[32:35], v[16:19]\n"
"ds_read_b128 v[56:59], v2 offset:33792\n"
"v_mfma_f32_16x16x32_bf16 v[16:19], a[144:147], v[36:39], v[16:19]\n"
"buffer_load_dwordx4 v2, s[32:35], s63 offen offset:2048 sc1 lds\n"
"v_mfma_f32_16x16x32_bf16 v[20:23], a[148:151], v[36:39], v[20:23]\n"
"ds_read_b128 v[60:63], v2 offset:48128\n"
"v_mfma_f32_16x16x32_bf16 v[20:23], a[36:39], v[32:35], v[20:23]\n"
"s_waitcnt lgkmcnt(4)\n"
"v_mfma_f32_16x16x32_bf16 v[20:23], v[68:71], v[40:43], v[20:23]\n"
"ds_read_b128 v[32:35], v2 offset:6144\n"
"v_mfma_f32_16x16x32_bf16 v[20:23], v[180:183], v[44:47], v[20:23]\n"
"buffer_load_dwordx4 v2, s[32:35], s63 offen offset:3072 sc1 lds\n"
"v_mfma_f32_16x16x32_bf16 v[16:19], v[176:179], v[44:47], v[16:19]\n"
"ds_read_b128 v[36:39], v2 offset:20480\n"
"v_mfma_f32_16x16x32_bf16 v[16:19], v[64:67], v[40:43], v[16:19]\n"
"s_waitcnt lgkmcnt(4)\n"
"v_mfma_f32_16x16x32_bf16 v[16:19], a[40:43], v[48:51], v[16:19]\n"
"ds_read_b128 v[40:43], v2 offset:34816\n"
"v_mfma_f32_16x16x32_bf16 v[16:19], a[152:155], v[52:55], v[16:19]\n"
"buffer_load_dwordx4 v[12:15], v2, s[32:35], s71 offen sc1\n"
"s_add_u32 s71, s71, 1187840\n"
"v_mfma_f32_16x16x32_bf16 v[20:23], a[156:159], v[52:55], v[20:23]\n"
"ds_read_b128 v[44:47], v2 offset:49152\n"
"s_add_u32 s63, s63, 4096\n"
"v_mfma_f32_16x16x32_bf16 v[20:23], a[44:47], v[48:51], v[20:23]\n"
"s_add_u32 m0, s64, 61440\n"
"s_waitcnt lgkmcnt(4)\n"
"v_mfma_f32_16x16x32_bf16 v[20:23], v[76:79], v[56:59], v[20:23]\n"
"ds_read_b128 v[48:51], v2 offset:7168\n"
"v_mfma_f32_16x16x32_bf16 v[20:23], v[188:191], v[60:63], v[20:23]\n"
"buffer_load_dwordx4 v2, s[32:35], s63 offen offset:0 sc1 lds\n"
"v_mfma_f32_16x16x32_bf16 v[16:19], v[184:187], v[60:63], v[16:19]\n"
"ds_read_b128 v[52:55], v2 offset:21504\n"
"v_mfma_f32_16x16x32_bf16 v[16:19], v[72:75], v[56:59], v[16:19]\n"
"s_waitcnt lgkmcnt(4)\n"
"v_mfma_f32_16x16x32_bf16 v[16:19], a[48:51], v[32:35], v[16:19]\n"
"ds_read_b128 v[56:59], v2 offset:35840\n"
"v_mfma_f32_16x16x32_bf16 v[16:19], a[160:163], v[36:39], v[16:19]\n"
"buffer_load_dwordx4 v2, s[32:35], s63 offen offset:1024 sc1 lds\n"
"v_mfma_f32_16x16x32_bf16 v[20:23], a[164:167], v[36:39], v[20:23]\n"
"ds_read_b128 v[60:63], v2 offset:50176\n"
"v_mfma_f32_16x16x32_bf16 v[20:23], a[52:55], v[32:35], v[20:23]\n"
"s_waitcnt lgkmcnt(4)\n"
"v_mfma_f32_16x16x32_bf16 v[20:23], v[84:87], v[40:43], v[20:23]\n"
"ds_read_b128 v[32:35], v2 offset:8192\n"
"v_mfma_f32_16x16x32_bf16 v[20:23], v[196:199], v[44:47], v[20:23]\n"
"buffer_load_dwordx4 v2, s[32:35], s63 offen offset:2048 sc1 lds\n"
"v_mfma_f32_16x16x32_bf16 v[16:19], v[192:195], v[44:47], v[16:19]\n"
"ds_read_b128 v[36:39], v2 offset:22528\n"
"v_mfma_f32_16x16x32_bf16 v[16:19], v[80:83], v[40:43], v[16:19]\n"
"s_waitcnt lgkmcnt(4)\n"
"v_mfma_f32_16x16x32_bf16 v[16:19], a[56:59], v[48:51], v[16:19]\n"
"ds_read_b128 v[40:43], v2 offset:36864\n"
"v_mfma_f32_16x16x32_bf16 v[16:19], a[168:171], v[52:55], v[16:19]\n"
"buffer_load_dwordx4 v2, s[32:35], s63 offen offset:3072 sc1 lds\n"
"v_mfma_f32_16x16x32_bf16 v[20:23], a[172:175], v[52:55], v[20:23]\n"
"ds_read_b128 v[44:47], v2 offset:51200\n"
"v_mfma_f32_16x16x32_bf16 v[20:23], a[60:63], v[48:51], v[20:23]\n"
"s_waitcnt lgkmcnt(4)\n"
"v_mfma_f32_16x16x32_bf16 v[20:23], v[92:95], v[56:59], v[20:23]\n"
"ds_read_b128 v[48:51], v2 offset:9216\n"
"v_mfma_f32_16x16x32_bf16 v[20:23], v[204:207], v[60:63], v[20:23]\n"
"s_add_u32 s63, s63, 4096\n"
"s_add_u32 m0, s64, 65536\n"
"v_mfma_f32_16x16x32_bf16 v[16:19], v[200:203], v[60:63], v[16:19]\n"
"ds_read_b128 v[52:55], v2 offset:23552\n"
"v_mfma_f32_16x16x32_bf16 v[16:19], v[88:91], v[56:59], v[16:19]\n"
"s_waitcnt lgkmcnt(4)\n"
"v_mfma_f32_16x16x32_bf16 v[16:19], a[64:67], v[32:35], v[16:19]\n"
"ds_read_b128 v[56:59], v2 offset:37888\n"
"v_mfma_f32_16x16x32_bf16 v[16:19], a[176:179], v[36:39], v[16:19]\n"
"buffer_load_dwordx4 v2, s[32:35], s63 offen offset:0 sc1 lds\n"
"v_mfma_f32_16x16x32_bf16 v[20:23], a[180:183], v[36:39], v[20:23]\n"
"ds_read_b128 v[60:63], v2 offset:52224\n"
"v_mfma_f32_16x16x32_bf16 v[20:23], a[68:71], v[32:35], v[20:23]\n"
"s_waitcnt lgkmcnt(4)\n"
"v_mfma_f32_16x16x32_bf16 v[20:23], v[100:103], v[40:43], v[20:23]\n"
"ds_read_b128 v[32:35], v2 offset:10240\n"
"v_mfma_f32_16x16x32_bf16 v[20:23], v[212:215], v[44:47], v[20:23]\n"
"buffer_load_dwordx4 v2, s[32:35], s63 offen offset:1024 sc1 lds\n"
"v_mfma_f32_16x16x32_bf16 v[16:19], v[208:211], v[44:47], v[16:19]\n"
"ds_read_b128 v[36:39], v2 offset:24576\n"
"v_mfma_f32_16x16x32_bf16 v[16:19], v[96:99], v[40:43], v[16:19]\n"
"s_waitcnt lgkmcnt(4)\n"
"v_mfma_f32_16x16x32_bf16 v[16:19], a[72:75], v[48:51], v[16:19]\n"
"ds_read_b128 v[40:43], v2 offset:38912\n"
"v_mfma_f32_16x16x32_bf16 v[16:19], a[184:187], v[52:55], v[16:19]\n"
"buffer_load_dwordx4 v2, s[32:35], s63 offen offset:2048 sc1 lds\n"
"v_mfma_f32_16x16x32_bf16 v[20:23], a[188:191], v[52:55], v[20:23]\n"
"ds_read_b128 v[44:47], v2 offset:53248\n"
"v_mfma_f32_16x16x32_bf16 v[20:23], a[76:79], v[48:51], v[20:23]\n"
"s_waitcnt lgkmcnt(4)\n"
"v_mfma_f32_16x16x32_bf16 v[20:23], v[108:111], v[56:59], v[20:23]\n"
"ds_read_b128 v[48:51], v2 offset:11264\n"
"v_mfma_f32_16x16x32_bf16 v[20:23], v[220:223], v[60:63], v[20:23]\n"
"v_mfma_f32_16x16x32_bf16 v[16:19], v[216:219], v[60:63], v[16:19]\n"
"ds_read_b128 v[52:55], v2 offset:25600\n"
"v_mfma_f32_16x16x32_bf16 v[16:19], v[104:107], v[56:59], v[16:19]\n"
"s_waitcnt lgkmcnt(4)\n"
"v_mfma_f32_16x16x32_bf16 v[16:19], a[80:83], v[32:35], v[16:19]\n"
"ds_read_b128 v[56:59], v2 offset:39936\n"
"v_mfma_f32_16x16x32_bf16 v[16:19], a[192:195], v[36:39], v[16:19]\n"
"buffer_load_dwordx4 v2, s[32:35], s63 offen offset:3072 sc1 lds\n"
"s_add_u32 s63, s63, 4096\n"
"v_mfma_f32_16x16x32_bf16 v[20:23], a[196:199], v[36:39], v[20:23]\n"
"ds_read_b128 v[60:63], v2 offset:54272\n"
"s_add_u32 m0, s64, 69632\n"
"v_mfma_f32_16x16x32_bf16 v[20:23], a[84:87], v[32:35], v[20:23]\n"
"s_waitcnt lgkmcnt(4)\n"
"v_mfma_f32_16x16x32_bf16 v[20:23], v[116:119], v[40:43], v[20:23]\n"
"ds_read_b128 v[32:35], v2 offset:12288\n"
"v_mfma_f32_16x16x32_bf16 v[20:23], v[228:231], v[44:47], v[20:23]\n"
"buffer_load_dwordx4 v2, s[32:35], s63 offen offset:0 sc1 lds\n"
"v_mfma_f32_16x16x32_bf16 v[16:19], v[224:227], v[44:47], v[16:19]\n"
"ds_read_b128 v[36:39], v2 offset:26624\n"
"v_mfma_f32_16x16x32_bf16 v[16:19], v[112:115], v[40:43], v[16:19]\n"
"s_waitcnt lgkmcnt(4)\n"
"v_mfma_f32_16x16x32_bf16 v[16:19], a[88:91], v[48:51], v[16:19]\n"
"ds_read_b128 v[40:43], v2 offset:40960\n"
"v_mfma_f32_16x16x32_bf16 v[16:19], a[200:203], v[52:55], v[16:19]\n"
"buffer_load_dwordx4 v2, s[32:35], s63 offen offset:1024 sc1 lds\n"
"s_add_u32 s63, s63, 1175552\n"
"v_mfma_f32_16x16x32_bf16 v[20:23], a[204:207], v[52:55], v[20:23]\n"
"ds_read_b128 v[44:47], v2 offset:55296\n"
"v_mfma_f32_16x16x32_bf16 v[20:23], a[92:95], v[48:51], v[20:23]\n"
"s_waitcnt lgkmcnt(4)\n"
"v_mfma_f32_16x16x32_bf16 v[20:23], v[124:127], v[56:59], v[20:23]\n"
"ds_read_b128 v[48:51], v2 offset:13312\n"
"s_waitcnt vmcnt(10)\n"
"v_mfma_f32_16x16x32_bf16 v[20:23], v[236:239], v[60:63], v[20:23]\n"
"v_lshlrev_b32 v24, 16, v12\n"
"v_and_b32 v25, 0xffff0000, v12\n"
"v_mfma_f32_16x16x32_bf16 v[16:19], v[232:235], v[60:63], v[16:19]\n"
"ds_read_b128 v[52:55], v2 offset:27648\n"
"v_lshlrev_b32 v26, 16, v13\n"
"v_mfma_f32_16x16x32_bf16 v[16:19], v[120:123], v[56:59], v[16:19]\n"
"v_and_b32 v27, 0xffff0000, v13\n"
"s_waitcnt lgkmcnt(4)\n"
"v_mfma_f32_16x16x32_bf16 v[16:19], a[96:99], v[32:35], v[16:19]\n"
"ds_read_b128 v[56:59], v2 offset:41984\n"
"v_lshlrev_b32 v28, 16, v14\n"
"v_mfma_f32_16x16x32_bf16 v[16:19], a[208:211], v[36:39], v[16:19]\n"
"s_barrier\n"
"v_mfma_f32_16x16x32_bf16 v[20:23], a[212:215], v[36:39], v[20:23]\n"
"ds_read_b128 v[60:63], v2 offset:56320\n"
"v_and_b32 v29, 0xffff0000, v14\n"
"v_mfma_f32_16x16x32_bf16 v[20:23], a[100:103], v[32:35], v[20:23]\n"
"v_lshlrev_b32 v30, 16, v15\n"
"s_waitcnt lgkmcnt(4)\n"
"v_mfma_f32_16x16x32_bf16 v[20:23], v[132:135], v[40:43], v[20:23]\n"
"ds_read_b128 v[32:35], v4 offset:0\n"
"v_and_b32 v31, 0xffff0000, v15\n"
"v_mfma_f32_16x16x32_bf16 v[20:23], v[244:247], v[44:47], v[20:23]\n"
"v_mfma_f32_16x16x32_bf16 v[16:19], v[240:243], v[44:47], v[16:19]\n"
"ds_read_b128 v[36:39], v4 offset:14336\n"
"v_mfma_f32_16x16x32_bf16 v[16:19], v[128:131], v[40:43], v[16:19]\n"
"s_waitcnt lgkmcnt(4)\n"
"v_mfma_f32_16x16x32_bf16 v[16:19], a[104:107], v[48:51], v[16:19]\n"
"ds_read_b128 v[40:43], v4 offset:28672\n"
"v_mfma_f32_16x16x32_bf16 v[16:19], a[216:219], v[52:55], v[16:19]\n"
"v_mfma_f32_16x16x32_bf16 v[20:23], a[220:223], v[52:55], v[20:23]\n"
"ds_read_b128 v[44:47], v4 offset:43008\n"
"v_mfma_f32_16x16x32_bf16 v[20:23], a[108:111], v[48:51], v[20:23]\n"
"s_waitcnt lgkmcnt(4)\n"
"v_mfma_f32_16x16x32_bf16 v[20:23], v[140:143], v[56:59], v[20:23]\n"
"ds_read_b128 v[48:51], v4 offset:1024\n"
"v_mfma_f32_16x16x32_bf16 v[20:23], v[252:255], v[60:63], v[20:23]\n"
"v_mfma_f32_16x16x32_bf16 v[16:19], v[248:251], v[60:63], v[16:19]\n"
"ds_read_b128 v[52:55], v4 offset:15360\n"
"s_sub_u32 s62, s62, 1\n"
"v_mfma_f32_16x16x32_bf16 v[16:19], v[136:139], v[56:59], v[16:19]\n"
"s_cbranch_scc1 .L_dlb_2\n"
"s_waitcnt lgkmcnt(4)\n"
"v_mfma_f32_16x16x32_bf16 v[24:27], a[0:3], v[32:35], v[24:27]\n"
"ds_read_b128 v[56:59], v4 offset:29696\n"
"v_mfma_f32_16x16x32_bf16 v[24:27], a[112:115], v[36:39], v[24:27]\n"
"v_mfma_f32_16x16x32_bf16 v[28:31], a[116:119], v[36:39], v[28:31]\n"
"ds_read_b128 v[60:63], v4 offset:44032\n"
"v_mfma_f32_16x16x32_bf16 v[28:31], a[4:7], v[32:35], v[28:31]\n"
"s_add_u32 s68, s68, 1187840\n"
"s_waitcnt lgkmcnt(4)\n"
"v_mfma_f32_16x16x32_bf16 v[28:31], a[228:231], v[40:43], v[28:31]\n"
"ds_read_b128 v[32:35], v4 offset:2048\n"
"v_mfma_f32_16x16x32_bf16 v[28:31], v[148:151], v[44:47], v[28:31]\n"
"buffer_store_dwordx4 v[20:23], v2, s[32:35], s68 offen offset:1024 sc1\n"
"v_mfma_f32_16x16x32_bf16 v[24:27], v[144:147], v[44:47], v[24:27]\n"
"ds_read_b128 v[36:39], v4 offset:16384\n"
"v_mfma_f32_16x16x32_bf16 v[24:27], a[224:227], v[40:43], v[24:27]\n"
"s_waitcnt lgkmcnt(4)\n"
"v_mfma_f32_16x16x32_bf16 v[24:27], a[8:11], v[48:51], v[24:27]\n"
"ds_read_b128 v[40:43], v4 offset:30720\n"
"v_mfma_f32_16x16x32_bf16 v[24:27], a[120:123], v[52:55], v[24:27]\n"
"buffer_store_dwordx4 v[16:19], v2, s[32:35], s68 offen sc1\n"
"s_mov_b32 s67, s68\n"
"v_mfma_f32_16x16x32_bf16 v[28:31], a[124:127], v[52:55], v[28:31]\n"
"ds_read_b128 v[44:47], v4 offset:45056\n"
"v_mfma_f32_16x16x32_bf16 v[28:31], a[12:15], v[48:51], v[28:31]\n"
"s_waitcnt lgkmcnt(4)\n"
"v_mfma_f32_16x16x32_bf16 v[28:31], a[236:239], v[56:59], v[28:31]\n"
"ds_read_b128 v[48:51], v4 offset:3072\n"
"v_mfma_f32_16x16x32_bf16 v[28:31], v[156:159], v[60:63], v[28:31]\n"
"v_mfma_f32_16x16x32_bf16 v[24:27], v[152:155], v[60:63], v[24:27]\n"
"ds_read_b128 v[52:55], v4 offset:17408\n"
"v_mfma_f32_16x16x32_bf16 v[24:27], a[232:235], v[56:59], v[24:27]\n"
"s_waitcnt lgkmcnt(4)\n"
"v_mfma_f32_16x16x32_bf16 v[24:27], a[16:19], v[32:35], v[24:27]\n"
"ds_read_b128 v[56:59], v4 offset:31744\n"
"v_mfma_f32_16x16x32_bf16 v[24:27], a[128:131], v[36:39], v[24:27]\n"
"s_waitcnt vmcnt(2)\n"
"s_barrier\n"
"v_mfma_f32_16x16x32_bf16 v[28:31], a[132:135], v[36:39], v[28:31]\n"
"ds_read_b128 v[60:63], v4 offset:46080\n"
"v_mfma_f32_16x16x32_bf16 v[28:31], a[20:23], v[32:35], v[28:31]\n"
"s_waitcnt lgkmcnt(4)\n"
"v_mfma_f32_16x16x32_bf16 v[28:31], a[244:247], v[40:43], v[28:31]\n"
"ds_read_b128 v[32:35], v4 offset:4096\n"
"s_add_u32 m0, s64, 0\n"
"v_mfma_f32_16x16x32_bf16 v[28:31], v[164:167], v[44:47], v[28:31]\n"
"buffer_load_dwordx4 v2, s[32:35], s63 offen offset:0 sc1 lds\n"
"v_mfma_f32_16x16x32_bf16 v[24:27], v[160:163], v[44:47], v[24:27]\n"
"ds_read_b128 v[36:39], v4 offset:18432\n"
"v_mfma_f32_16x16x32_bf16 v[24:27], a[240:243], v[40:43], v[24:27]\n"
"s_waitcnt lgkmcnt(4)\n"
"v_mfma_f32_16x16x32_bf16 v[24:27], a[24:27], v[48:51], v[24:27]\n"
"ds_read_b128 v[40:43], v4 offset:32768\n"
"v_mfma_f32_16x16x32_bf16 v[24:27], a[136:139], v[52:55], v[24:27]\n"
"v_mfma_f32_16x16x32_bf16 v[28:31], a[140:143], v[52:55], v[28:31]\n"
"ds_read_b128 v[44:47], v4 offset:47104\n"
"v_mfma_f32_16x16x32_bf16 v[28:31], a[28:31], v[48:51], v[28:31]\n"
"s_waitcnt lgkmcnt(4)\n"
"v_mfma_f32_16x16x32_bf16 v[28:31], a[252:255], v[56:59], v[28:31]\n"
"ds_read_b128 v[48:51], v4 offset:5120\n"
"v_mfma_f32_16x16x32_bf16 v[28:31], v[172:175], v[60:63], v[28:31]\n"
"buffer_load_dwordx4 v2, s[32:35], s63 offen offset:1024 sc1 lds\n"
"v_mfma_f32_16x16x32_bf16 v[24:27], v[168:171], v[60:63], v[24:27]\n"
"ds_read_b128 v[52:55], v4 offset:19456\n"
"v_mfma_f32_16x16x32_bf16 v[24:27], a[248:251], v[56:59], v[24:27]\n"
"s_waitcnt lgkmcnt(4)\n"
"v_mfma_f32_16x16x32_bf16 v[24:27], a[32:35], v[32:35], v[24:27]\n"
"ds_read_b128 v[56:59], v4 offset:33792\n"
"v_mfma_f32_16x16x32_bf16 v[24:27], a[144:147], v[36:39], v[24:27]\n"
"buffer_load_dwordx4 v2, s[32:35], s63 offen offset:2048 sc1 lds\n"
"v_mfma_f32_16x16x32_bf16 v[28:31], a[148:151], v[36:39], v[28:31]\n"
"ds_read_b128 v[60:63], v4 offset:48128\n"
"v_mfma_f32_16x16x32_bf16 v[28:31], a[36:39], v[32:35], v[28:31]\n"
"s_waitcnt lgkmcnt(4)\n"
"v_mfma_f32_16x16x32_bf16 v[28:31], v[68:71], v[40:43], v[28:31]\n"
"ds_read_b128 v[32:35], v4 offset:6144\n"
"v_mfma_f32_16x16x32_bf16 v[28:31], v[180:183], v[44:47], v[28:31]\n"
"buffer_load_dwordx4 v2, s[32:35], s63 offen offset:3072 sc1 lds\n"
"v_mfma_f32_16x16x32_bf16 v[24:27], v[176:179], v[44:47], v[24:27]\n"
"ds_read_b128 v[36:39], v4 offset:20480\n"
"v_mfma_f32_16x16x32_bf16 v[24:27], v[64:67], v[40:43], v[24:27]\n"
"s_waitcnt lgkmcnt(4)\n"
"v_mfma_f32_16x16x32_bf16 v[24:27], a[40:43], v[48:51], v[24:27]\n"
"ds_read_b128 v[40:43], v4 offset:34816\n"
"v_mfma_f32_16x16x32_bf16 v[24:27], a[152:155], v[52:55], v[24:27]\n"
"buffer_load_dwordx4 v[12:15], v2, s[32:35], s71 offen sc1\n"
"s_add_u32 s71, s71, 1187840\n"
"v_mfma_f32_16x16x32_bf16 v[28:31], a[156:159], v[52:55], v[28:31]\n"
"ds_read_b128 v[44:47], v4 offset:49152\n"
"s_add_u32 s63, s63, 4096\n"
"v_mfma_f32_16x16x32_bf16 v[28:31], a[44:47], v[48:51], v[28:31]\n"
"s_add_u32 m0, s64, 4096\n"
"s_waitcnt lgkmcnt(4)\n"
"v_mfma_f32_16x16x32_bf16 v[28:31], v[76:79], v[56:59], v[28:31]\n"
"ds_read_b128 v[48:51], v4 offset:7168\n"
"v_mfma_f32_16x16x32_bf16 v[28:31], v[188:191], v[60:63], v[28:31]\n"
"buffer_load_dwordx4 v2, s[32:35], s63 offen offset:0 sc1 lds\n"
"v_mfma_f32_16x16x32_bf16 v[24:27], v[184:187], v[60:63], v[24:27]\n"
"ds_read_b128 v[52:55], v4 offset:21504\n"
"v_mfma_f32_16x16x32_bf16 v[24:27], v[72:75], v[56:59], v[24:27]\n"
"s_waitcnt lgkmcnt(4)\n"
"v_mfma_f32_16x16x32_bf16 v[24:27], a[48:51], v[32:35], v[24:27]\n"
"ds_read_b128 v[56:59], v4 offset:35840\n"
"v_mfma_f32_16x16x32_bf16 v[24:27], a[160:163], v[36:39], v[24:27]\n"
"buffer_load_dwordx4 v2, s[32:35], s63 offen offset:1024 sc1 lds\n"
"v_mfma_f32_16x16x32_bf16 v[28:31], a[164:167], v[36:39], v[28:31]\n"
"ds_read_b128 v[60:63], v4 offset:50176\n"
"v_mfma_f32_16x16x32_bf16 v[28:31], a[52:55], v[32:35], v[28:31]\n"
"s_waitcnt lgkmcnt(4)\n"
"v_mfma_f32_16x16x32_bf16 v[28:31], v[84:87], v[40:43], v[28:31]\n"
"ds_read_b128 v[32:35], v4 offset:8192\n"
"v_mfma_f32_16x16x32_bf16 v[28:31], v[196:199], v[44:47], v[28:31]\n"
"buffer_load_dwordx4 v2, s[32:35], s63 offen offset:2048 sc1 lds\n"
"v_mfma_f32_16x16x32_bf16 v[24:27], v[192:195], v[44:47], v[24:27]\n"
"ds_read_b128 v[36:39], v4 offset:22528\n"
"v_mfma_f32_16x16x32_bf16 v[24:27], v[80:83], v[40:43], v[24:27]\n"
"s_waitcnt lgkmcnt(4)\n"
"v_mfma_f32_16x16x32_bf16 v[24:27], a[56:59], v[48:51], v[24:27]\n"
"ds_read_b128 v[40:43], v4 offset:36864\n"
"v_mfma_f32_16x16x32_bf16 v[24:27], a[168:171], v[52:55], v[24:27]\n"
"buffer_load_dwordx4 v2, s[32:35], s63 offen offset:3072 sc1 lds\n"
"v_mfma_f32_16x16x32_bf16 v[28:31], a[172:175], v[52:55], v[28:31]\n"
"ds_read_b128 v[44:47], v4 offset:51200\n"
"v_mfma_f32_16x16x32_bf16 v[28:31], a[60:63], v[48:51], v[28:31]\n"
"s_waitcnt lgkmcnt(4)\n"
"v_mfma_f32_16x16x32_bf16 v[28:31], v[92:95], v[56:59], v[28:31]\n"
"ds_read_b128 v[48:51], v4 offset:9216\n"
"v_mfma_f32_16x16x32_bf16 v[28:31], v[204:207], v[60:63], v[28:31]\n"
"s_add_u32 s63, s63, 4096\n"
"s_add_u32 m0, s64, 8192\n"
"v_mfma_f32_16x16x32_bf16 v[24:27], v[200:203], v[60:63], v[24:27]\n"
"ds_read_b128 v[52:55], v4 offset:23552\n"
"v_mfma_f32_16x16x32_bf16 v[24:27], v[88:91], v[56:59], v[24:27]\n"
"s_waitcnt lgkmcnt(4)\n"
"v_mfma_f32_16x16x32_bf16 v[24:27], a[64:67], v[32:35], v[24:27]\n"
"ds_read_b128 v[56:59], v4 offset:37888\n"
"v_mfma_f32_16x16x32_bf16 v[24:27], a[176:179], v[36:39], v[24:27]\n"
"buffer_load_dwordx4 v2, s[32:35], s63 offen offset:0 sc1 lds\n"
"v_mfma_f32_16x16x32_bf16 v[28:31], a[180:183], v[36:39], v[28:31]\n"
"ds_read_b128 v[60:63], v4 offset:52224\n"
"v_mfma_f32_16x16x32_bf16 v[28:31], a[68:71], v[32:35], v[28:31]\n"
"s_waitcnt lgkmcnt(4)\n"
"v_mfma_f32_16x16x32_bf16 v[28:31], v[100:103], v[40:43], v[28:31]\n"
"ds_read_b128 v[32:35], v4 offset:10240\n"
"v_mfma_f32_16x16x32_bf16 v[28:31], v[212:215], v[44:47], v[28:31]\n"
"buffer_load_dwordx4 v2, s[32:35], s63 offen offset:1024 sc1 lds\n"
"v_mfma_f32_16x16x32_bf16 v[24:27], v[208:211], v[44:47], v[24:27]\n"
"ds_read_b128 v[36:39], v4 offset:24576\n"
"v_mfma_f32_16x16x32_bf16 v[24:27], v[96:99], v[40:43], v[24:27]\n"
"s_waitcnt lgkmcnt(4)\n"
"v_mfma_f32_16x16x32_bf16 v[24:27], a[72:75], v[48:51], v[24:27]\n"
"ds_read_b128 v[40:43], v4 offset:38912\n"
"v_mfma_f32_16x16x32_bf16 v[24:27], a[184:187], v[52:55], v[24:27]\n"
"buffer_load_dwordx4 v2, s[32:35], s63 offen offset:2048 sc1 lds\n"
"v_mfma_f32_16x16x32_bf16 v[28:31], a[188:191], v[52:55], v[28:31]\n"
"ds_read_b128 v[44:47], v4 offset:53248\n"
"v_mfma_f32_16x16x32_bf16 v[28:31], a[76:79], v[48:51], v[28:31]\n"
"s_waitcnt lgkmcnt(4)\n"
"v_mfma_f32_16x16x32_bf16 v[28:31], v[108:111], v[56:59], v[28:31]\n"
"ds_read_b128 v[48:51], v4 offset:11264\n"
"v_mfma_f32_16x16x32_bf16 v[28:31], v[220:223], v[60:63], v[28:31]\n"
"v_mfma_f32_16x16x32_bf16 v[24:27], v[216:219], v[60:63], v[24:27]\n"
"ds_read_b128 v[52:55], v4 offset:25600\n"
"v_mfma_f32_16x16x32_bf16 v[24:27], v[104:107], v[56:59], v[24:27]\n"
"s_waitcnt lgkmcnt(4)\n"
"v_mfma_f32_16x16x32_bf16 v[24:27], a[80:83], v[32:35], v[24:27]\n"
"ds_read_b128 v[56:59], v4 offset:39936\n"
"v_mfma_f32_16x16x32_bf16 v[24:27], a[192:195], v[36:39], v[24:27]\n"
"buffer_load_dwordx4 v2, s[32:35], s63 offen offset:3072 sc1 lds\n"
"s_add_u32 s63, s63, 4096\n"
"v_mfma_f32_16x16x32_bf16 v[28:31], a[196:199], v[36:39], v[28:31]\n"
"ds_read_b128 v[60:63], v4 offset:54272\n"
"s_add_u32 m0, s64, 12288\n"
"v_mfma_f32_16x16x32_bf16 v[28:31], a[84:87], v[32:35], v[28:31]\n"
"s_waitcnt lgkmcnt(4)\n"
"v_mfma_f32_16x16x32_bf16 v[28:31], v[116:119], v[40:43], v[28:31]\n"
"ds_read_b128 v[32:35], v4 offset:12288\n"
"v_mfma_f32_16x16x32_bf16 v[28:31], v[228:231], v[44:47], v[28:31]\n"
"buffer_load_dwordx4 v2, s[32:35], s63 offen offset:0 sc1 lds\n"
"v_mfma_f32_16x16x32_bf16 v[24:27], v[224:227], v[44:47], v[24:27]\n"
"ds_read_b128 v[36:39], v4 offset:26624\n"
"v_mfma_f32_16x16x32_bf16 v[24:27], v[112:115], v[40:43], v[24:27]\n"
"s_waitcnt lgkmcnt(4)\n"
"v_mfma_f32_16x16x32_bf16 v[24:27], a[88:91], v[48:51], v[24:27]\n"
"ds_read_b128 v[40:43], v4 offset:40960\n"
"v_mfma_f32_16x16x32_bf16 v[24:27], a[200:203], v[52:55], v[24:27]\n"
"buffer_load_dwordx4 v2, s[32:35], s63 offen offset:1024 sc1 lds\n"
"s_add_u32 s63, s63, 1175552\n"
"v_mfma_f32_16x16x32_bf16 v[28:31], a[204:207], v[52:55], v[28:31]\n"
"ds_read_b128 v[44:47], v4 offset:55296\n"
"v_mfma_f32_16x16x32_bf16 v[28:31], a[92:95], v[48:51], v[28:31]\n"
"s_waitcnt lgkmcnt(4)\n"
"v_mfma_f32_16x16x32_bf16 v[28:31], v[124:127], v[56:59], v[28:31]\n"
"ds_read_b128 v[48:51], v4 offset:13312\n"
"s_waitcnt vmcnt(10)\n"
"v_mfma_f32_16x16x32_bf16 v[28:31], v[236:239], v[60:63], v[28:31]\n"
"v_lshlrev_b32 v16, 16, v12\n"
"v_and_b32 v17, 0xffff0000, v12\n"
"v_mfma_f32_16x16x32_bf16 v[24:27], v[232:235], v[60:63], v[24:27]\n"
"ds_read_b128 v[52:55], v4 offset:27648\n"
"v_lshlrev_b32 v18, 16, v13\n"
"v_mfma_f32_16x16x32_bf16 v[24:27], v[120:123], v[56:59], v[24:27]\n"
"v_and_b32 v19, 0xffff0000, v13\n"
"s_waitcnt lgkmcnt(4)\n"
"v_mfma_f32_16x16x32_bf16 v[24:27], a[96:99], v[32:35], v[24:27]\n"
"ds_read_b128 v[56:59], v4 offset:41984\n"
"v_lshlrev_b32 v20, 16, v14\n"
"v_mfma_f32_16x16x32_bf16 v[24:27], a[208:211], v[36:39], v[24:27]\n"
"s_barrier\n"
"v_mfma_f32_16x16x32_bf16 v[28:31], a[212:215], v[36:39], v[28:31]\n"
"ds_read_b128 v[60:63], v4 offset:56320\n"
"v_and_b32 v21, 0xffff0000, v14\n"
"v_mfma_f32_16x16x32_bf16 v[28:31], a[100:103], v[32:35], v[28:31]\n"
"v_lshlrev_b32 v22, 16, v15\n"
"s_waitcnt lgkmcnt(4)\n"
"v_mfma_f32_16x16x32_bf16 v[28:31], v[132:135], v[40:43], v[28:31]\n"
"ds_read_b128 v[32:35], v2 offset:0\n"
"v_and_b32 v23, 0xffff0000, v15\n"
"v_mfma_f32_16x16x32_bf16 v[28:31], v[244:247], v[44:47], v[28:31]\n"
"v_mfma_f32_16x16x32_bf16 v[24:27], v[240:243], v[44:47], v[24:27]\n"
"ds_read_b128 v[36:39], v2 offset:14336\n"
"v_mfma_f32_16x16x32_bf16 v[24:27], v[128:131], v[40:43], v[24:27]\n"
"s_waitcnt lgkmcnt(4)\n"
"v_mfma_f32_16x16x32_bf16 v[24:27], a[104:107], v[48:51], v[24:27]\n"
"ds_read_b128 v[40:43], v2 offset:28672\n"
"v_mfma_f32_16x16x32_bf16 v[24:27], a[216:219], v[52:55], v[24:27]\n"
"v_mfma_f32_16x16x32_bf16 v[28:31], a[220:223], v[52:55], v[28:31]\n"
"ds_read_b128 v[44:47], v2 offset:43008\n"
"v_mfma_f32_16x16x32_bf16 v[28:31], a[108:111], v[48:51], v[28:31]\n"
"s_waitcnt lgkmcnt(4)\n"
"v_mfma_f32_16x16x32_bf16 v[28:31], v[140:143], v[56:59], v[28:31]\n"
"ds_read_b128 v[48:51], v2 offset:1024\n"
"v_mfma_f32_16x16x32_bf16 v[28:31], v[252:255], v[60:63], v[28:31]\n"
"v_mfma_f32_16x16x32_bf16 v[24:27], v[248:251], v[60:63], v[24:27]\n"
"ds_read_b128 v[52:55], v2 offset:15360\n"
"v_mfma_f32_16x16x32_bf16 v[24:27], v[136:139], v[56:59], v[24:27]\n"
"s_branch .L_dll_2\n"
".L_dlb_2:\n"
"s_mov_b32 s62, 15\n"
"s_add_u32 s74, s74, 8192\n"
"v_lshrrev_b32 v5, 8, v2\n"
"v_lshl_add_u32 v5, v5, 4, s74\n"
"v_and_b32 v3, 128, v2\n"
"v_lshl_add_u32 v5, v3, 2, v5\n"
"v_and_b32 v3, 16, v2\n"
"v_lshl_add_u32 v5, v3, 4, v5\n"
"v_and_b32 v3, 96, v2\n"
"v_lshl_add_u32 v5, v3, 1, v5\n"
"v_lshrrev_b32 v3, 2, v2\n"
"v_add_u32 v3, s74, v3\n"
".p2align 6\n"
".L_dml_2:\n"
"s_waitcnt lgkmcnt(4)\n"
"v_mfma_f32_16x16x32_bf16 v[24:27], a[0:3], v[32:35], v[24:27]\n"
"ds_read_b128 v[56:59], v4 offset:29696\n"
"v_mfma_f32_16x16x32_bf16 v[24:27], a[112:115], v[36:39], v[24:27]\n"
"v_mfma_f32_16x16x32_bf16 v[28:31], a[116:119], v[36:39], v[28:31]\n"
"ds_read_b128 v[60:63], v4 offset:44032\n"
"v_mfma_f32_16x16x32_bf16 v[28:31], a[4:7], v[32:35], v[28:31]\n"
"v_cvt_pk_bf16_f32 v15, v22, v23\n"
"s_waitcnt lgkmcnt(4)\n"
"v_mfma_f32_16x16x32_bf16 v[28:31], a[228:231], v[40:43], v[28:31]\n"
"ds_read_b128 v[32:35], v4 offset:2048\n"
"v_cvt_pk_bf16_f32 v14, v20, v21\n"
"v_mfma_f32_16x16x32_bf16 v[28:31], v[148:151], v[44:47], v[28:31]\n"
"v_cvt_pk_bf16_f32 v13, v18, v19\n"
"v_cvt_pk_bf16_f32 v12, v16, v17\n"
"v_mfma_f32_16x16x32_bf16 v[24:27], v[144:147], v[44:47], v[24:27]\n"
"ds_read_b128 v[36:39], v4 offset:16384\n"
"ds_write_b128 v5, v[12:15]\n"
"v_mfma_f32_16x16x32_bf16 v[24:27], a[224:227], v[40:43], v[24:27]\n"
"ds_read2st64_b32 v[12:13], v3 offset0:0 offset1:1\n"
"s_waitcnt lgkmcnt(6)\n"
"v_mfma_f32_16x16x32_bf16 v[24:27], a[8:11], v[48:51], v[24:27]\n"
"ds_read_b128 v[40:43], v4 offset:30720\n"
"ds_read2st64_b32 v[14:15], v3 offset0:2 offset1:3\n"
"v_mfma_f32_16x16x32_bf16 v[24:27], a[120:123], v[52:55], v[24:27]\n"
"v_mfma_f32_16x16x32_bf16 v[28:31], a[124:127], v[52:55], v[28:31]\n"
"ds_read_b128 v[44:47], v4 offset:45056\n"
"v_mfma_f32_16x16x32_bf16 v[28:31], a[12:15], v[48:51], v[28:31]\n"
"s_waitcnt lgkmcnt(7)\n"
"v_mfma_f32_16x16x32_bf16 v[28:31], a[236:239], v[56:59], v[28:31]\n"
"ds_read_b128 v[48:51], v4 offset:3072\n"
"v_mfma_f32_16x16x32_bf16 v[28:31], v[156:159], v[60:63], v[28:31]\n"
"s_waitcnt lgkmcnt(2)\n"
"buffer_atomic_pk_add_bf16 v12, v1, s[44:47], s65 offen offset:0 sc1\n"
"v_mfma_f32_16x16x32_bf16 v[24:27], v[152:155], v[60:63], v[24:27]\n"
"ds_read_b128 v[52:55], v4 offset:17408\n"
"v_mfma_f32_16x16x32_bf16 v[24:27], a[232:235], v[56:59], v[24:27]\n"
"buffer_atomic_pk_add_bf16 v13, v1, s[44:47], s65 offen offset:2048 sc1\n"
"v_mfma_f32_16x16x32_bf16 v[24:27], a[16:19], v[32:35], v[24:27]\n"
"ds_read_b128 v[56:59], v4 offset:31744\n"
"s_add_u32 s65, s65, 16384\n"
"v_mfma_f32_16x16x32_bf16 v[24:27], a[128:131], v[36:39], v[24:27]\n"
"s_waitcnt vmcnt(2)\n"
"s_barrier\n"
"v_mfma_f32_16x16x32_bf16 v[28:31], a[132:135], v[36:39], v[28:31]\n"
"ds_read_b128 v[60:63], v4 offset:46080\n"
"v_mfma_f32_16x16x32_bf16 v[28:31], a[20:23], v[32:35], v[28:31]\n"
"buffer_atomic_pk_add_bf16 v14, v1, s[44:47], s65 offen offset:0 sc1\n"
"s_waitcnt lgkmcnt(4)\n"
"v_mfma_f32_16x16x32_bf16 v[28:31], a[244:247], v[40:43], v[28:31]\n"
"ds_read_b128 v[32:35], v4 offset:4096\n"
"s_add_u32 m0, s64, 0\n"
"v_mfma_f32_16x16x32_bf16 v[28:31], v[164:167], v[44:47], v[28:31]\n"
"buffer_atomic_pk_add_bf16 v15, v1, s[44:47], s65 offen offset:2048 sc1\n"
"s_add_u32 s65, s65, 16384\n"
"v_mfma_f32_16x16x32_bf16 v[24:27], v[160:163], v[44:47], v[24:27]\n"
"ds_read_b128 v[36:39], v4 offset:18432\n"
"v_mfma_f32_16x16x32_bf16 v[24:27], a[240:243], v[40:43], v[24:27]\n"
"s_waitcnt lgkmcnt(4)\n"
"v_mfma_f32_16x16x32_bf16 v[24:27], a[24:27], v[48:51], v[24:27]\n"
"ds_read_b128 v[40:43], v4 offset:32768\n"
"v_mfma_f32_16x16x32_bf16 v[24:27], a[136:139], v[52:55], v[24:27]\n"
"buffer_load_dwordx4 v2, s[32:35], s63 offen offset:0 sc1 lds\n"
"v_mfma_f32_16x16x32_bf16 v[28:31], a[140:143], v[52:55], v[28:31]\n"
"ds_read_b128 v[44:47], v4 offset:47104\n"
"v_mfma_f32_16x16x32_bf16 v[28:31], a[28:31], v[48:51], v[28:31]\n"
"s_waitcnt lgkmcnt(4)\n"
"v_mfma_f32_16x16x32_bf16 v[28:31], a[252:255], v[56:59], v[28:31]\n"
"ds_read_b128 v[48:51], v4 offset:5120\n"
"v_mfma_f32_16x16x32_bf16 v[28:31], v[172:175], v[60:63], v[28:31]\n"
"buffer_load_dwordx4 v2, s[32:35], s63 offen offset:1024 sc1 lds\n"
"v_mfma_f32_16x16x32_bf16 v[24:27], v[168:171], v[60:63], v[24:27]\n"
"ds_read_b128 v[52:55], v4 offset:19456\n"
"v_mfma_f32_16x16x32_bf16 v[24:27], a[248:251], v[56:59], v[24:27]\n"
"s_waitcnt lgkmcnt(4)\n"
"v_mfma_f32_16x16x32_bf16 v[24:27], a[32:35], v[32:35], v[24:27]\n"
"ds_read_b128 v[56:59], v4 offset:33792\n"
"v_mfma_f32_16x16x32_bf16 v[24:27], a[144:147], v[36:39], v[24:27]\n"
"buffer_load_dwordx4 v2, s[32:35], s63 offen offset:2048 sc1 lds\n"
"v_mfma_f32_16x16x32_bf16 v[28:31], a[148:151], v[36:39], v[28:31]\n"
"ds_read_b128 v[60:63], v4 offset:48128\n"
"v_mfma_f32_16x16x32_bf16 v[28:31], a[36:39], v[32:35], v[28:31]\n"
"s_waitcnt lgkmcnt(4)\n"
"v_mfma_f32_16x16x32_bf16 v[28:31], v[68:71], v[40:43], v[28:31]\n"
"ds_read_b128 v[32:35], v4 offset:6144\n"
"v_mfma_f32_16x16x32_bf16 v[28:31], v[180:183], v[44:47], v[28:31]\n"
"buffer_load_dwordx4 v2, s[32:35], s63 offen offset:3072 sc1 lds\n"
"v_mfma_f32_16x16x32_bf16 v[24:27], v[176:179], v[44:47], v[24:27]\n"
"ds_read_b128 v[36:39], v4 offset:20480\n"
"v_mfma_f32_16x16x32_bf16 v[24:27], v[64:67], v[40:43], v[24:27]\n"
"s_waitcnt lgkmcnt(4)\n"
"v_mfma_f32_16x16x32_bf16 v[24:27], a[40:43], v[48:51], v[24:27]\n"
"ds_read_b128 v[40:43], v4 offset:34816\n"
"v_mfma_f32_16x16x32_bf16 v[24:27], a[152:155], v[52:55], v[24:27]\n"
"buffer_load_dwordx4 v[12:15], v2, s[32:35], s71 offen sc1\n"
"s_add_u32 s71, s71, 1187840\n"
"v_mfma_f32_16x16x32_bf16 v[28:31], a[156:159], v[52:55], v[28:31]\n"
"ds_read_b128 v[44:47], v4 offset:49152\n"
"s_add_u32 s63, s63, 4096\n"
"v_mfma_f32_16x16x32_bf16 v[28:31], a[44:47], v[48:51], v[28:31]\n"
"s_add_u32 m0, s64, 4096\n"
"s_waitcnt lgkmcnt(4)\n"
"v_mfma_f32_16x16x32_bf16 v[28:31], v[76:79], v[56:59], v[28:31]\n"
"ds_read_b128 v[48:51], v4 offset:7168\n"
"v_mfma_f32_16x16x32_bf16 v[28:31], v[188:191], v[60:63], v[28:31]\n"
"buffer_load_dwordx4 v2, s[32:35], s63 offen offset:0 sc1 lds\n"
"v_mfma_f32_16x16x32_bf16 v[24:27], v[184:187], v[60:63], v[24:27]\n"
"ds_read_b128 v[52:55], v4 offset:21504\n"
"v_mfma_f32_16x16x32_bf16 v[24:27], v[72:75], v[56:59], v[24:27]\n"
"s_waitcnt lgkmcnt(4)\n"
"v_mfma_f32_16x16x32_bf16 v[24:27], a[48:51], v[32:35], v[24:27]\n"
"ds_read_b128 v[56:59], v4 offset:35840\n"
"v_mfma_f32_16x16x32_bf16 v[24:27], a[160:163], v[36:39], v[24:27]\n"
"buffer_load_dwordx4 v2, s[32:35], s63 offen offset:1024 sc1 lds\n"
"v_mfma_f32_16x16x32_bf16 v[28:31], a[164:167], v[36:39], v[28:31]\n"
"ds_read_b128 v[60:63], v4 offset:50176\n"
"v_mfma_f32_16x16x32_bf16 v[28:31], a[52:55], v[32:35], v[28:31]\n"
"s_waitcnt lgkmcnt(4)\n"
"v_mfma_f32_16x16x32_bf16 v[28:31], v[84:87], v[40:43], v[28:31]\n"
"ds_read_b128 v[32:35], v4 offset:8192\n"
"v_mfma_f32_16x16x32_bf16 v[28:31], v[196:199], v[44:47], v[28:31]\n"
"buffer_load_dwordx4 v2, s[32:35], s63 offen offset:2048 sc1 lds\n"
"v_mfma_f32_16x16x32_bf16 v[24:27], v[192:195], v[44:47], v[24:27]\n"
"ds_read_b128 v[36:39], v4 offset:22528\n"
"v_mfma_f32_16x16x32_bf16 v[24:27], v[80:83], v[40:43], v[24:27]\n"
"s_waitcnt lgkmcnt(4)\n"
"v_mfma_f32_16x16x32_bf16 v[24:27], a[56:59], v[48:51], v[24:27]\n"
"ds_read_b128 v[40:43], v4 offset:36864\n"
"v_mfma_f32_16x16x32_bf16 v[24:27], a[168:171], v[52:55], v[24:27]\n"
"buffer_load_dwordx4 v2, s[32:35], s63 offen offset:3072 sc1 lds\n"
"v_mfma_f32_16x16x32_bf16 v[28:31], a[172:175], v[52:55], v[28:31]\n"
"ds_read_b128 v[44:47], v4 offset:51200\n"
"v_mfma_f32_16x16x32_bf16 v[28:31], a[60:63], v[48:51], v[28:31]\n"
"s_waitcnt lgkmcnt(4)\n"
"v_mfma_f32_16x16x32_bf16 v[28:31], v[92:95], v[56:59], v[28:31]\n"
"ds_read_b128 v[48:51], v4 offset:9216\n"
"v_mfma_f32_16x16x32_bf16 v[28:31], v[204:207], v[60:63], v[28:31]\n"
"s_add_u32 s63, s63, 4096\n"
"s_add_u32 m0, s64, 8192\n"
"v_mfma_f32_16x16x32_bf16 v[24:27], v[200:203], v[60:63], v[24:27]\n"
"ds_read_b128 v[52:55], v4 offset:23552\n"
"v_mfma_f32_16x16x32_bf16 v[24:27], v[88:91], v[56:59], v[24:27]\n"
"s_waitcnt lgkmcnt(4)\n"
"v_mfma_f32_16x16x32_bf16 v[24:27], a[64:67], v[32:35], v[24:27]\n"
"ds_read_b128 v[56:59], v4 offset:37888\n"
"v_mfma_f32_16x16x32_bf16 v[24:27], a[176:179], v[36:39], v[24:27]\n"
"buffer_load_dwordx4 v2, s[32:35], s63 offen offset:0 sc1 lds\n"
"v_mfma_f32_16x16x32_bf16 v[28:31], a[180:183], v[36:39], v[28:31]\n"
"ds_read_b128 v[60:63], v4 offset:52224\n"
"v_mfma_f32_16x16x32_bf16 v[28:31], a[68:71], v[32:35], v[28:31]\n"
"s_waitcnt lgkmcnt(4)\n"
"v_mfma_f32_16x16x32_bf16 v[28:31], v[100:103], v[40:43], v[28:31]\n"
"ds_read_b128 v[32:35], v4 offset:10240\n"
"v_mfma_f32_16x16x32_bf16 v[28:31], v[212:215], v[44:47], v[28:31]\n"
"buffer_load_dwordx4 v2, s[32:35], s63 offen offset:1024 sc1 lds\n"
"v_mfma_f32_16x16x32_bf16 v[24:27], v[208:211], v[44:47], v[24:27]\n"
"ds_read_b128 v[36:39], v4 offset:24576\n"
"v_mfma_f32_16x16x32_bf16 v[24:27], v[96:99], v[40:43], v[24:27]\n"
"s_waitcnt lgkmcnt(4)\n"
"v_mfma_f32_16x16x32_bf16 v[24:27], a[72:75], v[48:51], v[24:27]\n"
"ds_read_b128 v[40:43], v4 offset:38912\n"
"v_mfma_f32_16x16x32_bf16 v[24:27], a[184:187], v[52:55], v[24:27]\n"
"buffer_load_dwordx4 v2, s[32:35], s63 offen offset:2048 sc1 lds\n"
"v_mfma_f32_16x16x32_bf16 v[28:31], a[188:191], v[52:55], v[28:31]\n"
"ds_read_b128 v[44:47], v4 offset:53248\n"
"v_mfma_f32_16x16x32_bf16 v[28:31], a[76:79], v[48:51], v[28:31]\n"
"s_waitcnt lgkmcnt(4)\n"
"v_mfma_f32_16x16x32_bf16 v[28:31], v[108:111], v[56:59], v[28:31]\n"
"ds_read_b128 v[48:51], v4 offset:11264\n"
"v_mfma_f32_16x16x32_bf16 v[28:31], v[220:223], v[60:63], v[28:31]\n"
"v_mfma_f32_16x16x32_bf16 v[24:27], v[216:219], v[60:63], v[24:27]\n"
"ds_read_b128 v[52:55], v4 offset:25600\n"
"v_mfma_f32_16x16x32_bf16 v[24:27], v[104:107], v[56:59], v[24:27]\n"
"s_waitcnt lgkmcnt(4)\n"
"v_mfma_f32_16x16x32_bf16 v[24:27], a[80:83], v[32:35], v[24:27]\n"
"ds_read_b128 v[56:59], v4 offset:39936\n"
"v_mfma_f32_16x16x32_bf16 v[24:27], a[192:195], v[36:39], v[24:27]\n"
"buffer_load_dwordx4 v2, s[32:35], s63 offen offset:3072 sc1 lds\n"
"s_add_u32 s63, s63, 4096\n"
"v_mfma_f32_16x16x32_bf16 v[28:31], a[196:199], v[36:39], v[28:31]\n"
"ds_read_b128 v[60:63], v4 offset:54272\n"
"s_add_u32 m0, s64, 12288\n"
"v_mfma_f32_16x16x32_bf16 v[28:31], a[84:87], v[32:35], v[28:31]\n"
"s_waitcnt lgkmcnt(4)\n"
"v_mfma_f32_16x16x32_bf16 v[28:31], v[116:119], v[40:43], v[28:31]\n"
"ds_read_b128 v[32:35], v4 offset:12288\n"
"v_mfma_f32_16x16x32_bf16 v[28:31], v[228:231], v[44:47], v[28:31]\n"
"buffer_load_dwordx4 v2, s[32:35], s63 offen offset:0 sc1 lds\n"
"v_mfma_f32_16x16x32_bf16 v[24:27], v[224:227], v[44:47], v[24:27]\n"
"ds_read_b128 v[36:39], v4 offset:26624\n"
"v_mfma_f32_16x16x32_bf16 v[24:27], v[112:115], v[40:43], v[24:27]\n"
"s_waitcnt lgkmcnt(4)\n"
"v_mfma_f32_16x16x32_bf16 v[24:27], a[88:91], v[48:51], v[24:27]\n"
"ds_read_b128 v[40:43], v4 offset:40960\n"
"v_mfma_f32_16x16x32_bf16 v[24:27], a[200:203], v[52:55], v[24:27]\n"
"buffer_load_dwordx4 v2, s[32:35], s63 offen offset:1024 sc1 lds\n"
"s_add_u32 s63, s63, 1175552\n"
"v_mfma_f32_16x16x32_bf16 v[28:31], a[204:207], v[52:55], v[28:31]\n"
"ds_read_b128 v[44:47], v4 offset:55296\n"
"v_mfma_f32_16x16x32_bf16 v[28:31], a[92:95], v[48:51], v[28:31]\n"
"s_waitcnt lgkmcnt(4)\n"
"v_mfma_f32_16x16x32_bf16 v[28:31], v[124:127], v[56:59], v[28:31]\n"
"ds_read_b128 v[48:51], v4 offset:13312\n"
"s_waitcnt vmcnt(10)\n"
"v_mfma_f32_16x16x32_bf16 v[28:31], v[236:239], v[60:63], v[28:31]\n"
"v_lshlrev_b32 v16, 16, v12\n"
"v_and_b32 v17, 0xffff0000, v12\n"
"v_mfma_f32_16x16x32_bf16 v[24:27], v[232:235], v[60:63], v[24:27]\n"
"ds_read_b128 v[52:55], v4 offset:27648\n"
"v_lshlrev_b32 v18, 16, v13\n"
"v_mfma_f32_16x16x32_bf16 v[24:27], v[120:123], v[56:59], v[24:27]\n"
"v_and_b32 v19, 0xffff0000, v13\n"
"s_waitcnt lgkmcnt(4)\n"
"v_mfma_f32_16x16x32_bf16 v[24:27], a[96:99], v[32:35], v[24:27]\n"
"ds_read_b128 v[56:59], v4 offset:41984\n"
"v_lshlrev_b32 v20, 16, v14\n"
"v_mfma_f32_16x16x32_bf16 v[24:27], a[208:211], v[36:39], v[24:27]\n"
"s_barrier\n"
"v_mfma_f32_16x16x32_bf16 v[28:31], a[212:215], v[36:39], v[28:31]\n"
"ds_read_b128 v[60:63], v4 offset:56320\n"
"v_and_b32 v21, 0xffff0000, v14\n"
"v_mfma_f32_16x16x32_bf16 v[28:31], a[100:103], v[32:35], v[28:31]\n"
"v_lshlrev_b32 v22, 16, v15\n"
"s_waitcnt lgkmcnt(4)\n"
"v_mfma_f32_16x16x32_bf16 v[28:31], v[132:135], v[40:43], v[28:31]\n"
"ds_read_b128 v[32:35], v2 offset:0\n"
"v_and_b32 v23, 0xffff0000, v15\n"
"v_mfma_f32_16x16x32_bf16 v[28:31], v[244:247], v[44:47], v[28:31]\n"
"v_mfma_f32_16x16x32_bf16 v[24:27], v[240:243], v[44:47], v[24:27]\n"
"ds_read_b128 v[36:39], v2 offset:14336\n"
"v_mfma_f32_16x16x32_bf16 v[24:27], v[128:131], v[40:43], v[24:27]\n"
"s_waitcnt lgkmcnt(4)\n"
"v_mfma_f32_16x16x32_bf16 v[24:27], a[104:107], v[48:51], v[24:27]\n"
"ds_read_b128 v[40:43], v2 offset:28672\n"
"v_mfma_f32_16x16x32_bf16 v[24:27], a[216:219], v[52:55], v[24:27]\n"
"v_mfma_f32_16x16x32_bf16 v[28:31], a[220:223], v[52:55], v[28:31]\n"
"ds_read_b128 v[44:47], v2 offset:43008\n"
"v_mfma_f32_16x16x32_bf16 v[28:31], a[108:111], v[48:51], v[28:31]\n"
"s_waitcnt lgkmcnt(4)\n"
"v_mfma_f32_16x16x32_bf16 v[28:31], v[140:143], v[56:59], v[28:31]\n"
"ds_read_b128 v[48:51], v2 offset:1024\n"
"v_mfma_f32_16x16x32_bf16 v[28:31], v[252:255], v[60:63], v[28:31]\n"
"v_mfma_f32_16x16x32_bf16 v[24:27], v[248:251], v[60:63], v[24:27]\n"
"ds_read_b128 v[52:55], v2 offset:15360\n"
"s_sub_u32 s62, s62, 1\n"
"v_mfma_f32_16x16x32_bf16 v[24:27], v[136:139], v[56:59], v[24:27]\n"
"s_cbranch_scc1 .L_dmt_2\n"
"s_waitcnt lgkmcnt(4)\n"
"v_mfma_f32_16x16x32_bf16 v[16:19], a[0:3], v[32:35], v[16:19]\n"
"ds_read_b128 v[56:59], v2 offset:29696\n"
"v_mfma_f32_16x16x32_bf16 v[16:19], a[112:115], v[36:39], v[16:19]\n"
"v_mfma_f32_16x16x32_bf16 v[20:23], a[116:119], v[36:39], v[20:23]\n"
"ds_read_b128 v[60:63], v2 offset:44032\n"
"v_mfma_f32_16x16x32_bf16 v[20:23], a[4:7], v[32:35], v[20:23]\n"
"v_cvt_pk_bf16_f32 v15, v30, v31\n"
"s_waitcnt lgkmcnt(4)\n"
"v_mfma_f32_16x16x32_bf16 v[20:23], a[228:231], v[40:43], v[20:23]\n"
"ds_read_b128 v[32:35], v2 offset:2048\n"
"v_cvt_pk_bf16_f32 v14, v28, v29\n"
"v_mfma_f32_16x16x32_bf16 v[20:23], v[148:151], v[44:47], v[20:23]\n"
"v_cvt_pk_bf16_f32 v13, v26, v27\n"
"v_cvt_pk_bf16_f32 v12, v24, v25\n"
"v_mfma_f32_16x16x32_bf16 v[16:19], v[144:147], v[44:47], v[16:19]\n"
"ds_read_b128 v[36:39], v2 offset:16384\n"
"ds_write_b128 v5, v[12:15]\n"
"v_mfma_f32_16x16x32_bf16 v[16:19], a[224:227], v[40:43], v[16:19]\n"
"ds_read2st64_b32 v[12:13], v3 offset0:0 offset1:1\n"
"s_waitcnt lgkmcnt(6)\n"
"v_mfma_f32_16x16x32_bf16 v[16:19], a[8:11], v[48:51], v[16:19]\n"
"ds_read_b128 v[40:43], v2 offset:30720\n"
"ds_read2st64_b32 v[14:15], v3 offset0:2 offset1:3\n"
"v_mfma_f32_16x16x32_bf16 v[16:19], a[120:123], v[52:55], v[16:19]\n"
"v_mfma_f32_16x16x32_bf16 v[20:23], a[124:127], v[52:55], v[20:23]\n"
"ds_read_b128 v[44:47], v2 offset:45056\n"
"v_mfma_f32_16x16x32_bf16 v[20:23], a[12:15], v[48:51], v[20:23]\n"
"s_waitcnt lgkmcnt(7)\n"
"v_mfma_f32_16x16x32_bf16 v[20:23], a[236:239], v[56:59], v[20:23]\n"
"ds_read_b128 v[48:51], v2 offset:3072\n"
"v_mfma_f32_16x16x32_bf16 v[20:23], v[156:159], v[60:63], v[20:23]\n"
"s_waitcnt lgkmcnt(2)\n"
"buffer_atomic_pk_add_bf16 v12, v1, s[44:47], s65 offen offset:0 sc1\n"
"v_mfma_f32_16x16x32_bf16 v[16:19], v[152:155], v[60:63], v[16:19]\n"
"ds_read_b128 v[52:55], v2 offset:17408\n"
"v_mfma_f32_16x16x32_bf16 v[16:19], a[232:235], v[56:59], v[16:19]\n"
"buffer_atomic_pk_add_bf16 v13, v1, s[44:47], s65 offen offset:2048 sc1\n"
"v_mfma_f32_16x16x32_bf16 v[16:19], a[16:19], v[32:35], v[16:19]\n"
"ds_read_b128 v[56:59], v2 offset:31744\n"
"s_add_u32 s65, s65, 16384\n"
"v_mfma_f32_16x16x32_bf16 v[16:19], a[128:131], v[36:39], v[16:19]\n"
"s_waitcnt vmcnt(2)\n"
"s_barrier\n"
"v_mfma_f32_16x16x32_bf16 v[20:23], a[132:135], v[36:39], v[20:23]\n"
"ds_read_b128 v[60:63], v2 offset:46080\n"
"v_mfma_f32_16x16x32_bf16 v[20:23], a[20:23], v[32:35], v[20:23]\n"
"buffer_atomic_pk_add_bf16 v14, v1, s[44:47], s65 offen offset:0 sc1\n"
"s_waitcnt lgkmcnt(4)\n"
"v_mfma_f32_16x16x32_bf16 v[20:23], a[244:247], v[40:43], v[20:23]\n"
"ds_read_b128 v[32:35], v2 offset:4096\n"
"s_add_u32 m0, s64, 57344\n"
"v_mfma_f32_16x16x32_bf16 v[20:23], v[164:167], v[44:47], v[20:23]\n"
"buffer_atomic_pk_add_bf16 v15, v1, s[44:47], s65 offen offset:2048 sc1\n"
"s_add_u32 s65, s65, 16384\n"
"v_mfma_f32_16x16x32_bf16 v[16:19], v[160:163], v[44:47], v[16:19]\n"
"ds_read_b128 v[36:39], v2 offset:18432\n"
"v_mfma_f32_16x16x32_bf16 v[16:19], a[240:243], v[40:43], v[16:19]\n"
"s_waitcnt lgkmcnt(4)\n"
"v_mfma_f32_16x16x32_bf16 v[16:19], a[24:27], v[48:51], v[16:19]\n"
"ds_read_b128 v[40:43], v2 offset:32768\n"
"v_mfma_f32_16x16x32_bf16 v[16:19], a[136:139], v[52:55], v[16:19]\n"
"buffer_load_dwordx4 v2, s[32:35], s63 offen offset:0 sc1 lds\n"
"v_mfma_f32_16x16x32_bf16 v[20:23], a[140:143], v[52:55], v[20:23]\n"
"ds_read_b128 v[44:47], v2 offset:47104\n"
"v_mfma_f32_16x16x32_bf16 v[20:23], a[28:31], v[48:51], v[20:23]\n"
"s_waitcnt lgkmcnt(4)\n"
"v_mfma_f32_16x16x32_bf16 v[20:23], a[252:255], v[56:59], v[20:23]\n"
"ds_read_b128 v[48:51], v2 offset:5120\n"
"v_mfma_f32_16x16x32_bf16 v[20:23], v[172:175], v[60:63], v[20:23]\n"
"buffer_load_dwordx4 v2, s[32:35], s63 offen offset:1024 sc1 lds\n"
"v_mfma_f32_16x16x32_bf16 v[16:19], v[168:171], v[60:63], v[16:19]\n"
"ds_read_b128 v[52:55], v2 offset:19456\n"
"v_mfma_f32_16x16x32_bf16 v[16:19], a[248:251], v[56:59], v[16:19]\n"
"s_waitcnt lgkmcnt(4)\n"
"v_mfma_f32_16x16x32_bf16 v[16:19], a[32:35], v[32:35], v[16:19]\n"
"ds_read_b128 v[56:59], v2 offset:33792\n"
"v_mfma_f32_16x16x32_bf16 v[16:19], a[144:147], v[36:39], v[16:19]\n"
"buffer_load_dwordx4 v2, s[32:35], s63 offen offset:2048 sc1 lds\n"
"v_mfma_f32_16x16x32_bf16 v[20:23], a[148:151], v[36:39], v[20:23]\n"
"ds_read_b128 v[60:63], v2 offset:48128\n"
"v_mfma_f32_16x16x32_bf16 v[20:23], a[36:39], v[32:35], v[20:23]\n"
"s_waitcnt lgkmcnt(4)\n"
"v_mfma_f32_16x16x32_bf16 v[20:23], v[68:71], v[40:43], v[20:23]\n"
"ds_read_b128 v[32:35], v2 offset:6144\n"
"v_mfma_f32_16x16x32_bf16 v[20:23], v[180:183], v[44:47], v[20:23]\n"
"buffer_load_dwordx4 v2, s[32:35], s63 offen offset:3072 sc1 lds\n"
"v_mfma_f32_16x16x32_bf16 v[16:19], v[176:179], v[44:47], v[16:19]\n"
"ds_read_b128 v[36:39], v2 offset:20480\n"
"v_mfma_f32_16x16x32_bf16 v[16:19], v[64:67], v[40:43], v[16:19]\n"
"s_waitcnt lgkmcnt(4)\n"
"v_mfma_f32_16x16x32_bf16 v[16:19], a[40:43], v[48:51], v[16:19]\n"
"ds_read_b128 v[40:43], v2 offset:34816\n"
"v_mfma_f32_16x16x32_bf16 v[16:19], a[152:155], v[52:55], v[16:19]\n"
"buffer_load_dwordx4 v[12:15], v2, s[32:35], s71 offen sc1\n"
"s_add_u32 s71, s71, 1187840\n"
"v_mfma_f32_16x16x32_bf16 v[20:23], a[156:159], v[52:55], v[20:23]\n"
"ds_read_b128 v[44:47], v2 offset:49152\n"
"s_add_u32 s63, s63, 4096\n"
"v_mfma_f32_16x16x32_bf16 v[20:23], a[44:47], v[48:51], v[20:23]\n"
"s_add_u32 m0, s64, 61440\n"
"s_waitcnt lgkmcnt(4)\n"
"v_mfma_f32_16x16x32_bf16 v[20:23], v[76:79], v[56:59], v[20:23]\n"
"ds_read_b128 v[48:51], v2 offset:7168\n"
"v_mfma_f32_16x16x32_bf16 v[20:23], v[188:191], v[60:63], v[20:23]\n"
"buffer_load_dwordx4 v2, s[32:35], s63 offen offset:0 sc1 lds\n"
"v_mfma_f32_16x16x32_bf16 v[16:19], v[184:187], v[60:63], v[16:19]\n"
"ds_read_b128 v[52:55], v2 offset:21504\n"
"v_mfma_f32_16x16x32_bf16 v[16:19], v[72:75], v[56:59], v[16:19]\n"
"s_waitcnt lgkmcnt(4)\n"
"v_mfma_f32_16x16x32_bf16 v[16:19], a[48:51], v[32:35], v[16:19]\n"
"ds_read_b128 v[56:59], v2 offset:35840\n"
"v_mfma_f32_16x16x32_bf16 v[16:19], a[160:163], v[36:39], v[16:19]\n"
"buffer_load_dwordx4 v2, s[32:35], s63 offen offset:1024 sc1 lds\n"
"v_mfma_f32_16x16x32_bf16 v[20:23], a[164:167], v[36:39], v[20:23]\n"
"ds_read_b128 v[60:63], v2 offset:50176\n"
"v_mfma_f32_16x16x32_bf16 v[20:23], a[52:55], v[32:35], v[20:23]\n"
"s_waitcnt lgkmcnt(4)\n"
"v_mfma_f32_16x16x32_bf16 v[20:23], v[84:87], v[40:43], v[20:23]\n"
"ds_read_b128 v[32:35], v2 offset:8192\n"
"v_mfma_f32_16x16x32_bf16 v[20:23], v[196:199], v[44:47], v[20:23]\n"
"buffer_load_dwordx4 v2, s[32:35], s63 offen offset:2048 sc1 lds\n"
"v_mfma_f32_16x16x32_bf16 v[16:19], v[192:195], v[44:47], v[16:19]\n"
"ds_read_b128 v[36:39], v2 offset:22528\n"
"v_mfma_f32_16x16x32_bf16 v[16:19], v[80:83], v[40:43], v[16:19]\n"
"s_waitcnt lgkmcnt(4)\n"
"v_mfma_f32_16x16x32_bf16 v[16:19], a[56:59], v[48:51], v[16:19]\n"
"ds_read_b128 v[40:43], v2 offset:36864\n"
"v_mfma_f32_16x16x32_bf16 v[16:19], a[168:171], v[52:55], v[16:19]\n"
"buffer_load_dwordx4 v2, s[32:35], s63 offen offset:3072 sc1 lds\n"
"v_mfma_f32_16x16x32_bf16 v[20:23], a[172:175], v[52:55], v[20:23]\n"
"ds_read_b128 v[44:47], v2 offset:51200\n"
"v_mfma_f32_16x16x32_bf16 v[20:23], a[60:63], v[48:51], v[20:23]\n"
"s_waitcnt lgkmcnt(4)\n"
"v_mfma_f32_16x16x32_bf16 v[20:23], v[92:95], v[56:59], v[20:23]\n"
"ds_read_b128 v[48:51], v2 offset:9216\n"
"v_mfma_f32_16x16x32_bf16 v[20:23], v[204:207], v[60:63], v[20:23]\n"
"s_add_u32 s63, s63, 4096\n"
"s_add_u32 m0, s64, 65536\n"
"v_mfma_f32_16x16x32_bf16 v[16:19], v[200:203], v[60:63], v[16:19]\n"
"ds_read_b128 v[52:55], v2 offset:23552\n"
"v_mfma_f32_16x16x32_bf16 v[16:19], v[88:91], v[56:59], v[16:19]\n"
"s_waitcnt lgkmcnt(4)\n"
"v_mfma_f32_16x16x32_bf16 v[16:19], a[64:67], v[32:35], v[16:19]\n"
"ds_read_b128 v[56:59], v2 offset:37888\n"
"v_mfma_f32_16x16x32_bf16 v[16:19], a[176:179], v[36:39], v[16:19]\n"
"buffer_load_dwordx4 v2, s[32:35], s63 offen offset:0 sc1 lds\n"
"v_mfma_f32_16x16x32_bf16 v[20:23], a[180:183], v[36:39], v[20:23]\n"
"ds_read_b128 v[60:63], v2 offset:52224\n"
"v_mfma_f32_16x16x32_bf16 v[20:23], a[68:71], v[32:35], v[20:23]\n"
"s_waitcnt lgkmcnt(4)\n"
"v_mfma_f32_16x16x32_bf16 v[20:23], v[100:103], v[40:43], v[20:23]\n"
"ds_read_b128 v[32:35], v2 offset:10240\n"
"v_mfma_f32_16x16x32_bf16 v[20:23], v[212:215], v[44:47], v[20:23]\n"
"buffer_load_dwordx4 v2, s[32:35], s63 offen offset:1024 sc1 lds\n"
"v_mfma_f32_16x16x32_bf16 v[16:19], v[208:211], v[44:47], v[16:19]\n"
"ds_read_b128 v[36:39], v2 offset:24576\n"
"v_mfma_f32_16x16x32_bf16 v[16:19], v[96:99], v[40:43], v[16:19]\n"
"s_waitcnt lgkmcnt(4)\n"
"v_mfma_f32_16x16x32_bf16 v[16:19], a[72:75], v[48:51], v[16:19]\n"
"ds_read_b128 v[40:43], v2 offset:38912\n"
"v_mfma_f32_16x16x32_bf16 v[16:19], a[184:187], v[52:55], v[16:19]\n"
"buffer_load_dwordx4 v2, s[32:35], s63 offen offset:2048 sc1 lds\n"
"v_mfma_f32_16x16x32_bf16 v[20:23], a[188:191], v[52:55], v[20:23]\n"
"ds_read_b128 v[44:47], v2 offset:53248\n"
"v_mfma_f32_16x16x32_bf16 v[20:23], a[76:79], v[48:51], v[20:23]\n"
"s_waitcnt lgkmcnt(4)\n"
"v_mfma_f32_16x16x32_bf16 v[20:23], v[108:111], v[56:59], v[20:23]\n"
"ds_read_b128 v[48:51], v2 offset:11264\n"
"v_mfma_f32_16x16x32_bf16 v[20:23], v[220:223], v[60:63], v[20:23]\n"
"v_mfma_f32_16x16x32_bf16 v[16:19], v[216:219], v[60:63], v[16:19]\n"
"ds_read_b128 v[52:55], v2 offset:25600\n"
"v_mfma_f32_16x16x32_bf16 v[16:19], v[104:107], v[56:59], v[16:19]\n"
"s_waitcnt lgkmcnt(4)\n"
"v_mfma_f32_16x16x32_bf16 v[16:19], a[80:83], v[32:35], v[16:19]\n"
"ds_read_b128 v[56:59], v2 offset:39936\n"
"v_mfma_f32_16x16x32_bf16 v[16:19], a[192:195], v[36:39], v[16:19]\n"
"buffer_load_dwordx4 v2, s[32:35], s63 offen offset:3072 sc1 lds\n"
"s_add_u32 s63, s63, 4096\n"
"v_mfma_f32_16x16x32_bf16 v[20:23], a[196:199], v[36:39], v[20:23]\n"
"ds_read_b128 v[60:63], v2 offset:54272\n"
"s_add_u32 m0, s64, 69632\n"
"v_mfma_f32_16x16x32_bf16 v[20:23], a[84:87], v[32:35], v[20:23]\n"
"s_waitcnt lgkmcnt(4)\n"
"v_mfma_f32_16x16x32_bf16 v[20:23], v[116:119], v[40:43], v[20:23]\n"
"ds_read_b128 v[32:35], v2 offset:12288\n"
"v_mfma_f32_16x16x32_bf16 v[20:23], v[228:231], v[44:47], v[20:23]\n"
"buffer_load_dwordx4 v2, s[32:35], s63 offen offset:0 sc1 lds\n"
"v_mfma_f32_16x16x32_bf16 v[16:19], v[224:227], v[44:47], v[16:19]\n"
"ds_read_b128 v[36:39], v2 offset:26624\n"
"v_mfma_f32_16x16x32_bf16 v[16:19], v[112:115], v[40:43], v[16:19]\n"
"s_waitcnt lgkmcnt(4)\n"
"v_mfma_f32_16x16x32_bf16 v[16:19], a[88:91], v[48:51], v[16:19]\n"
"ds_read_b128 v[40:43], v2 offset:40960\n"
"v_mfma_f32_16x16x32_bf16 v[16:19], a[200:203], v[52:55], v[16:19]\n"
"buffer_load_dwordx4 v2, s[32:35], s63 offen offset:1024 sc1 lds\n"
"s_add_u32 s63, s63, 1175552\n"
"v_mfma_f32_16x16x32_bf16 v[20:23], a[204:207], v[52:55], v[20:23]\n"
"ds_read_b128 v[44:47], v2 offset:55296\n"
"v_mfma_f32_16x16x32_bf16 v[20:23], a[92:95], v[48:51], v[20:23]\n"
"s_waitcnt lgkmcnt(4)\n"
"v_mfma_f32_16x16x32_bf16 v[20:23], v[124:127], v[56:59], v[20:23]\n"
"ds_read_b128 v[48:51], v2 offset:13312\n"
"s_waitcnt vmcnt(10)\n"
"v_mfma_f32_16x16x32_bf16 v[20:23], v[236:239], v[60:63], v[20:23]\n"
"v_lshlrev_b32 v24, 16, v12\n"
"v_and_b32 v25, 0xffff0000, v12\n"
"v_mfma_f32_16x16x32_bf16 v[16:19], v[232:235], v[60:63], v[16:19]\n"
"ds_read_b128 v[52:55], v2 offset:27648\n"
"v_lshlrev_b32 v26, 16, v13\n"
"v_mfma_f32_16x16x32_bf16 v[16:19], v[120:123], v[56:59], v[16:19]\n"
"v_and_b32 v27, 0xffff0000, v13\n"
"s_waitcnt lgkmcnt(4)\n"
"v_mfma_f32_16x16x32_bf16 v[16:19], a[96:99], v[32:35], v[16:19]\n"
"ds_read_b128 v[56:59], v2 offset:41984\n"
"v_lshlrev_b32 v28, 16, v14\n"
"v_mfma_f32_16x16x32_bf16 v[16:19], a[208:211], v[36:39], v[16:19]\n"
"s_barrier\n"
"v_mfma_f32_16x16x32_bf16 v[20:23], a[212:215], v[36:39], v[20:23]\n"
"ds_read_b128 v[60:63], v2 offset:56320\n"
"v_and_b32 v29, 0xffff0000, v14\n"
"v_mfma_f32_16x16x32_bf16 v[20:23], a[100:103], v[32:35], v[20:23]\n"
"v_lshlrev_b32 v30, 16, v15\n"
"s_waitcnt lgkmcnt(4)\n"
"v_mfma_f32_16x16x32_bf16 v[20:23], v[132:135], v[40:43], v[20:23]\n"
"ds_read_b128 v[32:35], v4 offset:0\n"
"v_and_b32 v31, 0xffff0000, v15\n"
"v_mfma_f32_16x16x32_bf16 v[20:23], v[244:247], v[44:47], v[20:23]\n"
"v_mfma_f32_16x16x32_bf16 v[16:19], v[240:243], v[44:47], v[16:19]\n"
"ds_read_b128 v[36:39], v4 offset:14336\n"
"v_mfma_f32_16x16x32_bf16 v[16:19], v[128:131], v[40:43], v[16:19]\n"
"s_waitcnt lgkmcnt(4)\n"
"v_mfma_f32_16x16x32_bf16 v[16:19], a[104:107], v[48:51], v[16:19]\n"
"ds_read_b128 v[40:43], v4 offset:28672\n"
"v_mfma_f32_16x16x32_bf16 v[16:19], a[216:219], v[52:55], v[16:19]\n"
"v_mfma_f32_16x16x32_bf16 v[20:23], a[220:223], v[52:55], v[20:23]\n"
"ds_read_b128 v[44:47], v4 offset:43008\n"
"v_mfma_f32_16x16x32_bf16 v[20:23], a[108:111], v[48:51], v[20:23]\n"
"s_waitcnt lgkmcnt(4)\n"
"v_mfma_f32_16x16x32_bf16 v[20:23], v[140:143], v[56:59], v[20:23]\n"
"ds_read_b128 v[48:51], v4 offset:1024\n"
"v_mfma_f32_16x16x32_bf16 v[20:23], v[252:255], v[60:63], v[20:23]\n"
"v_mfma_f32_16x16x32_bf16 v[16:19], v[248:251], v[60:63], v[16:19]\n"
"ds_read_b128 v[52:55], v4 offset:15360\n"
"v_mfma_f32_16x16x32_bf16 v[16:19], v[136:139], v[56:59], v[16:19]\n"
"s_branch .L_dml_2\n"
".L_dmt_2:\n"
"s_nop 1\n"
"s_nop 0\n"
"v_cvt_pk_bf16_f32 v15, v30, v31\n"
"v_cvt_pk_bf16_f32 v14, v28, v29\n"
"s_nop 1\n"
"v_cvt_pk_bf16_f32 v13, v26, v27\n"
"v_cvt_pk_bf16_f32 v12, v24, v25\n"
"ds_write_b128 v5, v[12:15]\n"
"ds_read2st64_b32 v[12:13], v3 offset0:0 offset1:1\n"
"ds_read2st64_b32 v[14:15], v3 offset0:2 offset1:3\n"
"s_waitcnt lgkmcnt(0)\n"
"buffer_atomic_pk_add_bf16 v12, v1, s[44:47], s65 offen offset:0 sc1\n"
"buffer_atomic_pk_add_bf16 v13, v1, s[44:47], s65 offen offset:2048 sc1\n"
"s_add_u32 s65, s65, 16384\n"
"buffer_atomic_pk_add_bf16 v14, v1, s[44:47], s65 offen offset:0 sc1\n"
"buffer_atomic_pk_add_bf16 v15, v1, s[44:47], s65 offen offset:2048 sc1\n"
"s_add_u32 s65, s65, 16384\n"
"s_branch .L_end\n"
".p2align 6\n"
".L_dlv3:\n"
".L_dll_3:\n"
"s_waitcnt lgkmcnt(4)\n"
"v_mfma_f32_16x16x32_bf16 v[16:19], a[0:3], v[32:35], v[16:19]\n"
"ds_read_b128 v[56:59], v2 offset:29696\n"
"v_mfma_f32_16x16x32_bf16 v[16:19], a[112:115], v[36:39], v[16:19]\n"
"v_mfma_f32_16x16x32_bf16 v[20:23], a[116:119], v[36:39], v[20:23]\n"
"ds_read_b128 v[60:63], v2 offset:44032\n"
"v_mfma_f32_16x16x32_bf16 v[20:23], a[4:7], v[32:35], v[20:23]\n"
"s_add_u32 s68, s68, 1187840\n"
"s_waitcnt lgkmcnt(4)\n"
"v_mfma_f32_16x16x32_bf16 v[20:23], a[228:231], v[40:43], v[20:23]\n"
"ds_read_b128 v[32:35], v2 offset:2048\n"
"v_mfma_f32_16x16x32_bf16 v[20:23], v[148:151], v[44:47], v[20:23]\n"
"buffer_store_dwordx4 v[28:31], v2, s[32:35], s68 offen offset:1024 sc1\n"
"v_mfma_f32_16x16x32_bf16 v[16:19], v[144:147], v[44:47], v[16:19]\n"
"ds_read_b128 v[36:39], v2 offset:16384\n"
"v_mfma_f32_16x16x32_bf16 v[16:19], a[224:227], v[40:43], v[16:19]\n"
"s_waitcnt lgkmcnt(4)\n"
"v_mfma_f32_16x16x32_bf16 v[16:19], a[8:11], v[48:51], v[16:19]\n"
"ds_read_b128 v[40:43], v2 offset:30720\n"
"v_mfma_f32_16x16x32_bf16 v[16:19], a[120:123], v[52:55], v[16:19]\n"
"buffer_store_dwordx4 v[24:27], v2, s[32:35], s68 offen sc1\n"
"s_mov_b32 s67, s68\n"
"v_mfma_f32_16x16x32_bf16 v[20:23], a[124:127], v[52:55], v[20:23]\n"
"ds_read_b128 v[44:47], v2 offset:45056\n"
"v_mfma_f32_16x16x32_bf16 v[20:23], a[12:15], v[48:51], v[20:23]\n"
"s_waitcnt lgkmcnt(4)\n"
"v_mfma_f32_16x16x32_bf16 v[20:23], a[236:239], v[56:59], v[20:23]\n"
"ds_read_b128 v[48:51], v2 offset:3072\n"
"v_mfma_f32_16x16x32_bf16 v[20:23], v[156:159], v[60:63], v[20:23]\n"
"v_mfma_f32_16x16x32_bf16 v[16:19], v[152:155], v[60:63], v[16:19]\n"
"ds_read_b128 v[52:55], v2 offset:17408\n"
"v_mfma_f32_16x16x32_bf16 v[16:19], a[232:235], v[56:59], v[16:19]\n"
"s_waitcnt lgkmcnt(4)\n"
"v_mfma_f32_16x16x32_bf16 v[16:19], a[16:19], v[32:35], v[16:19]\n"
"s_waitcnt vmcnt(2)\n"
"s_barrier\n"
"v_mfma_f32_16x16x32_bf16 v[16:19], a[128:131], v[36:39], v[16:19]\n"
"ds_read_b128 v[56:59], v2 offset:31744\n"
"v_mfma_f32_16x16x32_bf16 v[20:23], a[132:135], v[36:39], v[20:23]\n"
"ds_read_b128 v[60:63], v2 offset:46080\n"
"v_mfma_f32_16x16x32_bf16 v[20:23], a[20:23], v[32:35], v[20:23]\n"
"s_waitcnt lgkmcnt(4)\n"
"v_mfma_f32_16x16x32_bf16 v[20:23], a[244:247], v[40:43], v[20:23]\n"
"ds_read_b128 v[32:35], v2 offset:4096\n"
"s_add_u32 m0, s64, 57344\n"
"v_mfma_f32_16x16x32_bf16 v[20:23], v[164:167], v[44:47], v[20:23]\n"
"buffer_load_dwordx4 v2, s[32:35], s63 offen offset:0 sc1 lds\n"
"v_mfma_f32_16x16x32_bf16 v[16:19], v[160:163], v[44:47], v[16:19]\n"
"ds_read_b128 v[36:39], v2 offset:18432\n"
"v_mfma_f32_16x16x32_bf16 v[16:19], a[240:243], v[40:43], v[16:19]\n"
"s_waitcnt lgkmcnt(4)\n"
"v_mfma_f32_16x16x32_bf16 v[16:19], a[24:27], v[48:51], v[16:19]\n"
"ds_read_b128 v[40:43], v2 offset:32768\n"
"v_mfma_f32_16x16x32_bf16 v[16:19], a[136:139], v[52:55], v[16:19]\n"
"v_mfma_f32_16x16x32_bf16 v[20:23], a[140:143], v[52:55], v[20:23]\n"
"ds_read_b128 v[44:47], v2 offset:47104\n"
"v_mfma_f32_16x16x32_bf16 v[20:23], a[28:31], v[48:51], v[20:23]\n"
"s_waitcnt lgkmcnt(4)\n"
"v_mfma_f32_16x16x32_bf16 v[20:23], a[252:255], v[56:59], v[20:23]\n"
"ds_read_b128 v[48:51], v2 offset:5120\n"
"v_mfma_f32_16x16x32_bf16 v[20:23], v[172:175], v[60:63], v[20:23]\n"
"buffer_load_dwordx4 v2, s[32:35], s63 offen offset:1024 sc1 lds\n"
"v_mfma_f32_16x16x32_bf16 v[16:19], v[168:171], v[60:63], v[16:19]\n"
"ds_read_b128 v[52:55], v2 offset:19456\n"
"v_mfma_f32_16x16x32_bf16 v[16:19], a[248:251], v[56:59], v[16:19]\n"
"s_waitcnt lgkmcnt(4)\n"
"v_mfma_f32_16x16x32_bf16 v[16:19], a[32:35], v[32:35], v[16:19]\n"
"ds_read_b128 v[56:59], v2 offset:33792\n"
"v_mfma_f32_16x16x32_bf16 v[16:19], a[144:147], v[36:39], v[16:19]\n"
"buffer_load_dwordx4 v2, s[32:35], s63 offen offset:2048 sc1 lds\n"
"v_mfma_f32_16x16x32_bf16 v[20:23], a[148:151], v[36:39], v[20:23]\n"
"ds_read_b128 v[60:63], v2 offset:48128\n"
"v_mfma_f32_16x16x32_bf16 v[20:23], a[36:39], v[32:35], v[20:23]\n"
"s_waitcnt lgkmcnt(4)\n"
"v_mfma_f32_16x16x32_bf16 v[20:23], v[68:71], v[40:43], v[20:23]\n"
"ds_read_b128 v[32:35], v2 offset:6144\n"
"v_mfma_f32_16x16x32_bf16 v[20:23], v[180:183], v[44:47], v[20:23]\n"
"buffer_load_dwordx4 v2, s[32:35], s63 offen offset:3072 sc1 lds\n"
"v_mfma_f32_16x16x32_bf16 v[16:19], v[176:179], v[44:47], v[16:19]\n"
"ds_read_b128 v[36:39], v2 offset:20480\n"
"v_mfma_f32_16x16x32_bf16 v[16:19], v[64:67], v[40:43], v[16:19]\n"
"s_waitcnt lgkmcnt(4)\n"
"v_mfma_f32_16x16x32_bf16 v[16:19], a[40:43], v[48:51], v[16:19]\n"
"ds_read_b128 v[40:43], v2 offset:34816\n"
"v_mfma_f32_16x16x32_bf16 v[16:19], a[152:155], v[52:55], v[16:19]\n"
"buffer_load_dwordx4 v[12:15], v2, s[32:35], s71 offen sc1\n"
"s_add_u32 s71, s71, 1187840\n"
"v_mfma_f32_16x16x32_bf16 v[20:23], a[156:159], v[52:55], v[20:23]\n"
"ds_read_b128 v[44:47], v2 offset:49152\n"
"s_add_u32 s63, s63, 4096\n"
"v_mfma_f32_16x16x32_bf16 v[20:23], a[44:47], v[48:51], v[20:23]\n"
"s_add_u32 m0, s64, 61440\n"
"s_waitcnt lgkmcnt(4)\n"
"v_mfma_f32_16x16x32_bf16 v[20:23], v[76:79], v[56:59], v[20:23]\n"
"ds_read_b128 v[48:51], v2 offset:7168\n"
"v_mfma_f32_16x16x32_bf16 v[20:23], v[188:191], v[60:63], v[20:23]\n"
"buffer_load_dwordx4 v2, s[32:35], s63 offen offset:0 sc1 lds\n"
"v_mfma_f32_16x16x32_bf16 v[16:19], v[184:187], v[60:63], v[16:19]\n"
"ds_read_b128 v[52:55], v2 offset:21504\n"
"v_mfma_f32_16x16x32_bf16 v[16:19], v[72:75], v[56:59], v[16:19]\n"
"s_waitcnt lgkmcnt(4)\n"
"v_mfma_f32_16x16x32_bf16 v[16:19], a[48:51], v[32:35], v[16:19]\n"
"ds_read_b128 v[56:59], v2 offset:35840\n"
"v_mfma_f32_16x16x32_bf16 v[16:19], a[160:163], v[36:39], v[16:19]\n"
"buffer_load_dwordx4 v2, s[32:35], s63 offen offset:1024 sc1 lds\n"
"v_mfma_f32_16x16x32_bf16 v[20:23], a[164:167], v[36:39], v[20:23]\n"
"ds_read_b128 v[60:63], v2 offset:50176\n"
"v_mfma_f32_16x16x32_bf16 v[20:23], a[52:55], v[32:35], v[20:23]\n"
"s_waitcnt lgkmcnt(4)\n"
"v_mfma_f32_16x16x32_bf16 v[20:23], v[84:87], v[40:43], v[20:23]\n"
"ds_read_b128 v[32:35], v2 offset:8192\n"
"v_mfma_f32_16x16x32_bf16 v[20:23], v[196:199], v[44:47], v[20:23]\n"
"buffer_load_dwordx4 v2, s[32:35], s63 offen offset:2048 sc1 lds\n"
"v_mfma_f32_16x16x32_bf16 v[16:19], v[192:195], v[44:47], v[16:19]\n"
"ds_read_b128 v[36:39], v2 offset:22528\n"
"v_mfma_f32_16x16x32_bf16 v[16:19], v[80:83], v[40:43], v[16:19]\n"
"s_waitcnt lgkmcnt(4)\n"
"v_mfma_f32_16x16x32_bf16 v[16:19], a[56:59], v[48:51], v[16:19]\n"
"ds_read_b128 v[40:43], v2 offset:36864\n"
"v_mfma_f32_16x16x32_bf16 v[16:19], a[168:171], v[52:55], v[16:19]\n"
"buffer_load_dwordx4 v2, s[32:35], s63 offen offset:3072 sc1 lds\n"
"v_mfma_f32_16x16x32_bf16 v[20:23], a[172:175], v[52:55], v[20:23]\n"
"ds_read_b128 v[44:47], v2 offset:51200\n"
"v_mfma_f32_16x16x32_bf16 v[20:23], a[60:63], v[48:51], v[20:23]\n"
"s_waitcnt lgkmcnt(4)\n"
"v_mfma_f32_16x16x32_bf16 v[20:23], v[92:95], v[56:59], v[20:23]\n"
"ds_read_b128 v[48:51], v2 offset:9216\n"
"v_mfma_f32_16x16x32_bf16 v[20:23], v[204:207], v[60:63], v[20:23]\n"
"s_add_u32 s63, s63, 4096\n"
"s_add_u32 m0, s64, 65536\n"
"v_mfma_f32_16x16x32_bf16 v[16:19], v[200:203], v[60:63], v[16:19]\n"
"ds_read_b128 v[52:55], v2 offset:23552\n"
"v_mfma_f32_16x16x32_bf16 v[16:19], v[88:91], v[56:59], v[16:19]\n"
"s_waitcnt lgkmcnt(4)\n"
"v_mfma_f32_16x16x32_bf16 v[16:19], a[64:67], v[32:35], v[16:19]\n"
"ds_read_b128 v[56:59], v2 offset:37888\n"
"v_mfma_f32_16x16x32_bf16 v[16:19], a[176:179], v[36:39], v[16:19]\n"
"buffer_load_dwordx4 v2, s[32:35], s63 offen offset:0 sc1 lds\n"
"v_mfma_f32_16x16x32_bf16 v[20:23], a[180:183], v[36:39], v[20:23]\n"
"ds_read_b128 v[60:63], v2 offset:52224\n"
"v_mfma_f32_16x16x32_bf16 v[20:23], a[68:71], v[32:35], v[20:23]\n"
"s_waitcnt lgkmcnt(4)\n"
"v_mfma_f32_16x16x32_bf16 v[20:23], v[100:103], v[40:43], v[20:23]\n"
"ds_read_b128 v[32:35], v2 offset:10240\n"
"v_mfma_f32_16x16x32_bf16 v[20:23], v[212:215], v[44:47], v[20:23]\n"
"buffer_load_dwordx4 v2, s[32:35], s63 offen offset:1024 sc1 lds\n"
"v_mfma_f32_16x16x32_bf16 v[16:19], v[208:211], v[44:47], v[16:19]\n"
"ds_read_b128 v[36:39], v2 offset:24576\n"
"v_mfma_f32_16x16x32_bf16 v[16:19], v[96:99], v[40:43], v[16:19]\n"
"s_waitcnt lgkmcnt(4)\n"
"v_mfma_f32_16x16x32_bf16 v[16:19], a[72:75], v[48:51], v[16:19]\n"
"ds_read_b128 v[40:43], v2 offset:38912\n"
"v_mfma_f32_16x16x32_bf16 v[16:19], a[184:187], v[52:55], v[16:19]\n"
"buffer_load_dwordx4 v2, s[32:35], s63 offen offset:2048 sc1 lds\n"
"v_mfma_f32_16x16x32_bf16 v[20:23], a[188:191], v[52:55], v[20:23]\n"
"ds_read_b128 v[44:47], v2 offset:53248\n"
"v_mfma_f32_16x16x32_bf16 v[20:23], a[76:79], v[48:51], v[20:23]\n"
"s_waitcnt lgkmcnt(4)\n"
"v_mfma_f32_16x16x32_bf16 v[20:23], v[108:111], v[56:59], v[20:23]\n"
"ds_read_b128 v[48:51], v2 offset:11264\n"
"v_mfma_f32_16x16x32_bf16 v[20:23], v[220:223], v[60:63], v[20:23]\n"
"v_mfma_f32_16x16x32_bf16 v[16:19], v[216:219], v[60:63], v[16:19]\n"
"ds_read_b128 v[52:55], v2 offset:25600\n"
"v_mfma_f32_16x16x32_bf16 v[16:19], v[104:107], v[56:59], v[16:19]\n"
"s_waitcnt lgkmcnt(4)\n"
"v_mfma_f32_16x16x32_bf16 v[16:19], a[80:83], v[32:35], v[16:19]\n"
"ds_read_b128 v[56:59], v2 offset:39936\n"
"v_mfma_f32_16x16x32_bf16 v[16:19], a[192:195], v[36:39], v[16:19]\n"
"buffer_load_dwordx4 v2, s[32:35], s63 offen offset:3072 sc1 lds\n"
"s_add_u32 s63, s63, 4096\n"
"v_mfma_f32_16x16x32_bf16 v[20:23], a[196:199], v[36:39], v[20:23]\n"
"ds_read_b128 v[60:63], v2 offset:54272\n"
"s_add_u32 m0, s64, 69632\n"
"v_mfma_f32_16x16x32_bf16 v[20:23], a[84:87], v[32:35], v[20:23]\n"
"s_waitcnt lgkmcnt(4)\n"
"v_mfma_f32_16x16x32_bf16 v[20:23], v[116:119], v[40:43], v[20:23]\n"
"ds_read_b128 v[32:35], v2 offset:12288\n"
"v_mfma_f32_16x16x32_bf16 v[20:23], v[228:231], v[44:47], v[20:23]\n"
"buffer_load_dwordx4 v2, s[32:35], s63 offen offset:0 sc1 lds\n"
"v_mfma_f32_16x16x32_bf16 v[16:19], v[224:227], v[44:47], v[16:19]\n"
"ds_read_b128 v[36:39], v2 offset:26624\n"
"v_mfma_f32_16x16x32_bf16 v[16:19], v[112:115], v[40:43], v[16:19]\n"
"s_waitcnt lgkmcnt(4)\n"
"v_mfma_f32_16x16x32_bf16 v[16:19], a[88:91], v[48:51], v[16:19]\n"
"ds_read_b128 v[40:43], v2 offset:40960\n"
"v_mfma_f32_16x16x32_bf16 v[16:19], a[200:203], v[52:55], v[16:19]\n"
"buffer_load_dwordx4 v2, s[32:35], s63 offen offset:1024 sc1 lds\n"
"s_add_u32 s63, s63, 1175552\n"
"v_mfma_f32_16x16x32_bf16 v[20:23], a[204:207], v[52:55], v[20:23]\n"
"ds_read_b128 v[44:47], v2 offset:55296\n"
"v_mfma_f32_16x16x32_bf16 v[20:23], a[92:95], v[48:51], v[20:23]\n"
"s_waitcnt lgkmcnt(4)\n"
"v_mfma_f32_16x16x32_bf16 v[20:23], v[124:127], v[56:59], v[20:23]\n"
"ds_read_b128 v[48:51], v2 offset:13312\n"
"s_waitcnt vmcnt(10)\n"
"v_mfma_f32_16x16x32_bf16 v[20:23], v[236:239], v[60:63], v[20:23]\n"
"v_lshlrev_b32 v24, 16, v12\n"
"v_and_b32 v25, 0xffff0000, v12\n"
"v_mfma_f32_16x16x32_bf16 v[16:19], v[232:235], v[60:63], v[16:19]\n"
"ds_read_b128 v[52:55], v2 offset:27648\n"
"v_lshlrev_b32 v26, 16, v13\n"
"v_mfma_f32_16x16x32_bf16 v[16:19], v[120:123], v[56:59], v[16:19]\n"
"v_and_b32 v27, 0xffff0000, v13\n"
"s_waitcnt lgkmcnt(4)\n"
"v_mfma_f32_16x16x32_bf16 v[16:19], a[96:99], v[32:35], v[16:19]\n"
"s_barrier\n"
"v_mfma_f32_16x16x32_bf16 v[16:19], a[208:211], v[36:39], v[16:19]\n"
"ds_read_b128 v[56:59], v2 offset:41984\n"
"v_lshlrev_b32 v28, 16, v14\n"
"v_mfma_f32_16x16x32_bf16 v[20:23], a[212:215], v[36:39], v[20:23]\n"
"ds_read_b128 v[60:63], v2 offset:56320\n"
"v_and_b32 v29, 0xffff0000, v14\n"
"v_mfma_f32_16x16x32_bf16 v[20:23], a[100:103], v[32:35], v[20:23]\n"
"v_lshlrev_b32 v30, 16, v15\n"
"s_waitcnt lgkmcnt(4)\n"
"v_mfma_f32_16x16x32_bf16 v[20:23], v[132:135], v[40:43], v[20:23]\n"
"ds_read_b128 v[32:35], v4 offset:0\n"
"v_and_b32 v31, 0xffff0000, v15\n"
"v_mfma_f32_16x16x32_bf16 v[20:23], v[244:247], v[44:47], v[20:23]\n"
"v_mfma_f32_16x16x32_bf16 v[16:19], v[240:243], v[44:47], v[16:19]\n"
"ds_read_b128 v[36:39], v4 offset:14336\n"
"v_mfma_f32_16x16x32_bf16 v[16:19], v[128:131], v[40:43], v[16:19]\n"
"s_waitcnt lgkmcnt(4)\n"
"v_mfma_f32_16x16x32_bf16 v[16:19], a[104:107], v[48:51], v[16:19]\n"
"ds_read_b128 v[40:43], v4 offset:28672\n"
"v_mfma_f32_16x16x32_bf16 v[16:19], a[216:219], v[52:55], v[16:19]\n"
"v_mfma_f32_16x16x32_bf16 v[20:23], a[220:223], v[52:55], v[20:23]\n"
"ds_read_b128 v[44:47], v4 offset:43008\n"
"v_mfma_f32_16x16x32_bf16 v[20:23], a[108:111], v[48:51], v[20:23]\n"
"s_waitcnt lgkmcnt(4)\n"
"v_mfma_f32_16x16x32_bf16 v[20:23], v[140:143], v[56:59], v[20:23]\n"
"ds_read_b128 v[48:51], v4 offset:1024\n"
"v_mfma_f32_16x16x32_bf16 v[20:23], v[252:255], v[60:63], v[20:23]\n"
"v_mfma_f32_16x16x32_bf16 v[16:19], v[248:251], v[60:63], v[16:19]\n"
"ds_read_b128 v[52:55], v4 offset:15360\n"
"s_sub_u32 s62, s62, 1\n"
"v_mfma_f32_16x16x32_bf16 v[16:19], v[136:139], v[56:59], v[16:19]\n"
"s_cbranch_scc1 .L_dlb_3\n"
"s_waitcnt lgkmcnt(4)\n"
"v_mfma_f32_16x16x32_bf16 v[24:27], a[0:3], v[32:35], v[24:27]\n"
"ds_read_b128 v[56:59], v4 offset:29696\n"
"v_mfma_f32_16x16x32_bf16 v[24:27], a[112:115], v[36:39], v[24:27]\n"
"v_mfma_f32_16x16x32_bf16 v[28:31], a[116:119], v[36:39], v[28:31]\n"
"ds_read_b128 v[60:63], v4 offset:44032\n"
"v_mfma_f32_16x16x32_bf16 v[28:31], a[4:7], v[32:35], v[28:31]\n"
"s_add_u32 s68, s68, 1187840\n"
"s_waitcnt lgkmcnt(4)\n"
"v_mfma_f32_16x16x32_bf16 v[28:31], a[228:231], v[40:43], v[28:31]\n"
"ds_read_b128 v[32:35], v4 offset:2048\n"
"v_mfma_f32_16x16x32_bf16 v[28:31], v[148:151], v[44:47], v[28:31]\n"
"buffer_store_dwordx4 v[20:23], v2, s[32:35], s68 offen offset:1024 sc1\n"
"v_mfma_f32_16x16x32_bf16 v[24:27], v[144:147], v[44:47], v[24:27]\n"
"ds_read_b128 v[36:39], v4 offset:16384\n"
"v_mfma_f32_16x16x32_bf16 v[24:27], a[224:227], v[40:43], v[24:27]\n"
"s_waitcnt lgkmcnt(4)\n"
"v_mfma_f32_16x16x32_bf16 v[24:27], a[8:11], v[48:51], v[24:27]\n"
"ds_read_b128 v[40:43], v4 offset:30720\n"
"v_mfma_f32_16x16x32_bf16 v[24:27], a[120:123], v[52:55], v[24:27]\n"
"buffer_store_dwordx4 v[16:19], v2, s[32:35], s68 offen sc1\n"
"s_mov_b32 s67, s68\n"
"v_mfma_f32_16x16x32_bf16 v[28:31], a[124:127], v[52:55], v[28:31]\n"
"ds_read_b128 v[44:47], v4 offset:45056\n"
"v_mfma_f32_16x16x32_bf16 v[28:31], a[12:15], v[48:51], v[28:31]\n"
"s_waitcnt lgkmcnt(4)\n"
"v_mfma_f32_16x16x32_bf16 v[28:31], a[236:239], v[56:59], v[28:31]\n"
"ds_read_b128 v[48:51], v4 offset:3072\n"
"v_mfma_f32_16x16x32_bf16 v[28:31], v[156:159], v[60:63], v[28:31]\n"
"v_mfma_f32_16x16x32_bf16 v[24:27], v[152:155], v[60:63], v[24:27]\n"
"ds_read_b128 v[52:55], v4 offset:17408\n"
"v_mfma_f32_16x16x32_bf16 v[24:27], a[232:235], v[56:59], v[24:27]\n"
"s_waitcnt lgkmcnt(4)\n"
"v_mfma_f32_16x16x32_bf16 v[24:27], a[16:19], v[32:35], v[24:27]\n"
"s_waitcnt vmcnt(2)\n"
"s_barrier\n"
"v_mfma_f32_16x16x32_bf16 v[24:27], a[128:131], v[36:39], v[24:27]\n"
"ds_read_b128 v[56:59], v4 offset:31744\n"
"v_mfma_f32_16x16x32_bf16 v[28:31], a[132:135], v[36:39], v[28:31]\n"
"ds_read_b128 v[60:63], v4 offset:46080\n"
"v_mfma_f32_16x16x32_bf16 v[28:31], a[20:23], v[32:35], v[28:31]\n"
"s_waitcnt lgkmcnt(4)\n"
"v_mfma_f32_16x16x32_bf16 v[28:31], a[244:247], v[40:43], v[28:31]\n"
"ds_read_b128 v[32:35], v4 offset:4096\n"
"s_add_u32 m0, s64, 0\n"
"v_mfma_f32_16x16x32_bf16 v[28:31], v[164:167], v[44:47], v[28:31]\n"
"buffer_load_dwordx4 v2, s[32:35], s63 offen offset:0 sc1 lds\n"
"v_mfma_f32_16x16x32_bf16 v[24:27], v[160:163], v[44:47], v[24:27]\n"
"ds_read_b128 v[36:39], v4 offset:18432\n"
"v_mfma_f32_16x16x32_bf16 v[24:27], a[240:243], v[40:43], v[24:27]\n"
"s_waitcnt lgkmcnt(4)\n"
"v_mfma_f32_16x16x32_bf16 v[24:27], a[24:27], v[48:51], v[24:27]\n"
"ds_read_b128 v[40:43], v4 offset:32768\n"
"v_mfma_f32_16x16x32_bf16 v[24:27], a[136:139], v[52:55], v[24:27]\n"
"v_mfma_f32_16x16x32_bf16 v[28:31], a[140:143], v[52:55], v[28:31]\n"
"ds_read_b128 v[44:47], v4 offset:47104\n"
"v_mfma_f32_16x16x32_bf16 v[28:31], a[28:31], v[48:51], v[28:31]\n"
"s_waitcnt lgkmcnt(4)\n"
"v_mfma_f32_16x16x32_bf16 v[28:31], a[252:255], v[56:59], v[28:31]\n"
"ds_read_b128 v[48:51], v4 offset:5120\n"
"v_mfma_f32_16x16x32_bf16 v[28:31], v[172:175], v[60:63], v[28:31]\n"
"buffer_load_dwordx4 v2, s[32:35], s63 offen offset:1024 sc1 lds\n"
"v_mfma_f32_16x16x32_bf16 v[24:27], v[168:171], v[60:63], v[24:27]\n"
"ds_read_b128 v[52:55], v4 offset:19456\n"
"v_mfma_f32_16x16x32_bf16 v[24:27], a[248:251], v[56:59], v[24:27]\n"
"s_waitcnt lgkmcnt(4)\n"
"v_mfma_f32_16x16x32_bf16 v[24:27], a[32:35], v[32:35], v[24:27]\n"
"ds_read_b128 v[56:59], v4 offset:33792\n"
"v_mfma_f32_16x16x32_bf16 v[24:27], a[144:147], v[36:39], v[24:27]\n"
"buffer_load_dwordx4 v2, s[32:35], s63 offen offset:2048 sc1 lds\n"
"v_mfma_f32_16x16x32_bf16 v[28:31], a[148:151], v[36:39], v[28:31]\n"
"ds_read_b128 v[60:63], v4 offset:48128\n"
"v_mfma_f32_16x16x32_bf16 v[28:31], a[36:39], v[32:35], v[28:31]\n"
"s_waitcnt lgkmcnt(4)\n"
"v_mfma_f32_16x16x32_bf16 v[28:31], v[68:71], v[40:43], v[28:31]\n"
"ds_read_b128 v[32:35], v4 offset:6144\n"
"v_mfma_f32_16x16x32_bf16 v[28:31], v[180:183], v[44:47], v[28:31]\n"
"buffer_load_dwordx4 v2, s[32:35], s63 offen offset:3072 sc1 lds\n"
"v_mfma_f32_16x16x32_bf16 v[24:27], v[176:179], v[44:47], v[24:27]\n"
"ds_read_b128 v[36:39], v4 offset:20480\n"
"v_mfma_f32_16x16x32_bf16 v[24:27], v[64:67], v[40:43], v[24:27]\n"
"s_waitcnt lgkmcnt(4)\n"
"v_mfma_f32_16x16x32_bf16 v[24:27], a[40:43], v[48:51], v[24:27]\n"
"ds_read_b128 v[40:43], v4 offset:34816\n"
"v_mfma_f32_16x16x32_bf16 v[24:27], a[152:155], v[52:55], v[24:27]\n"
"buffer_load_dwordx4 v[12:15], v2, s[32:35], s71 offen sc1\n"
"s_add_u32 s71, s71, 1187840\n"
"v_mfma_f32_16x16x32_bf16 v[28:31], a[156:159], v[52:55], v[28:31]\n"
"ds_read_b128 v[44:47], v4 offset:49152\n"
"s_add_u32 s63, s63, 4096\n"
"v_mfma_f32_16x16x32_bf16 v[28:31], a[44:47], v[48:51], v[28:31]\n"
"s_add_u32 m0, s64, 4096\n"
"s_waitcnt lgkmcnt(4)\n"
"v_mfma_f32_16x16x32_bf16 v[28:31], v[76:79], v[56:59], v[28:31]\n"
"ds_read_b128 v[48:51], v4 offset:7168\n"
"v_mfma_f32_16x16x32_bf16 v[28:31], v[188:191], v[60:63], v[28:31]\n"
"buffer_load_dwordx4 v2, s[32:35], s63 offen offset:0 sc1 lds\n"
"v_mfma_f32_16x16x32_bf16 v[24:27], v[184:187], v[60:63], v[24:27]\n"
"ds_read_b128 v[52:55], v4 offset:21504\n"
"v_mfma_f32_16x16x32_bf16 v[24:27], v[72:75], v[56:59], v[24:27]\n"
"s_waitcnt lgkmcnt(4)\n"
"v_mfma_f32_16x16x32_bf16 v[24:27], a[48:51], v[32:35], v[24:27]\n"
"ds_read_b128 v[56:59], v4 offset:35840\n"
"v_mfma_f32_16x16x32_bf16 v[24:27], a[160:163], v[36:39], v[24:27]\n"
"buffer_load_dwordx4 v2, s[32:35], s63 offen offset:1024 sc1 lds\n"
"v_mfma_f32_16x16x32_bf16 v[28:31], a[164:167], v[36:39], v[28:31]\n"
"ds_read_b128 v[60:63], v4 offset:50176\n"
"v_mfma_f32_16x16x32_bf16 v[28:31], a[52:55], v[32:35], v[28:31]\n"
"s_waitcnt lgkmcnt(4)\n"
"v_mfma_f32_16x16x32_bf16 v[28:31], v[84:87], v[40:43], v[28:31]\n"
"ds_read_b128 v[32:35], v4 offset:8192\n"
"v_mfma_f32_16x16x32_bf16 v[28:31], v[196:199], v[44:47], v[28:31]\n"
"buffer_load_dwordx4 v2, s[32:35], s63 offen offset:2048 sc1 lds\n"
"v_mfma_f32_16x16x32_bf16 v[24:27], v[192:195], v[44:47], v[24:27]\n"
"ds_read_b128 v[36:39], v4 offset:22528\n"
"v_mfma_f32_16x16x32_bf16 v[24:27], v[80:83], v[40:43], v[24:27]\n"
"s_waitcnt lgkmcnt(4)\n"
"v_mfma_f32_16x16x32_bf16 v[24:27], a[56:59], v[48:51], v[24:27]\n"
"ds_read_b128 v[40:43], v4 offset:36864\n"
"v_mfma_f32_16x16x32_bf16 v[24:27], a[168:171], v[52:55], v[24:27]\n"
"buffer_load_dwordx4 v2, s[32:35], s63 offen offset:3072 sc1 lds\n"
"v_mfma_f32_16x16x32_bf16 v[28:31], a[172:175], v[52:55], v[28:31]\n"
"ds_read_b128 v[44:47], v4 offset:51200\n"
"v_mfma_f32_16x16x32_bf16 v[28:31], a[60:63], v[48:51], v[28:31]\n"
"s_waitcnt lgkmcnt(4)\n"
"v_mfma_f32_16x16x32_bf16 v[28:31], v[92:95], v[56:59], v[28:31]\n"
"ds_read_b128 v[48:51], v4 offset:9216\n"
"v_mfma_f32_16x16x32_bf16 v[28:31], v[204:207], v[60:63], v[28:31]\n"
"s_add_u32 s63, s63, 4096\n"
"s_add_u32 m0, s64, 8192\n"
"v_mfma_f32_16x16x32_bf16 v[24:27], v[200:203], v[60:63], v[24:27]\n"
"ds_read_b128 v[52:55], v4 offset:23552\n"
"v_mfma_f32_16x16x32_bf16 v[24:27], v[88:91], v[56:59], v[24:27]\n"
"s_waitcnt lgkmcnt(4)\n"
"v_mfma_f32_16x16x32_bf16 v[24:27], a[64:67], v[32:35], v[24:27]\n"
"ds_read_b128 v[56:59], v4 offset:37888\n"
"v_mfma_f32_16x16x32_bf16 v[24:27], a[176:179], v[36:39], v[24:27]\n"
"buffer_load_dwordx4 v2, s[32:35], s63 offen offset:0 sc1 lds\n"
"v_mfma_f32_16x16x32_bf16 v[28:31], a[180:183], v[36:39], v[28:31]\n"
"ds_read_b128 v[60:63], v4 offset:52224\n"
"v_mfma_f32_16x16x32_bf16 v[28:31], a[68:71], v[32:35], v[28:31]\n"
"s_waitcnt lgkmcnt(4)\n"
"v_mfma_f32_16x16x32_bf16 v[28:31], v[100:103], v[40:43], v[28:31]\n"
"ds_read_b128 v[32:35], v4 offset:10240\n"
"v_mfma_f32_16x16x32_bf16 v[28:31], v[212:215], v[44:47], v[28:31]\n"
"buffer_load_dwordx4 v2, s[32:35], s63 offen offset:1024 sc1 lds\n"
"v_mfma_f32_16x16x32_bf16 v[24:27], v[208:211], v[44:47], v[24:27]\n"
"ds_read_b128 v[36:39], v4 offset:24576\n"
"v_mfma_f32_16x16x32_bf16 v[24:27], v[96:99], v[40:43], v[24:27]\n"
"s_waitcnt lgkmcnt(4)\n"
"v_mfma_f32_16x16x32_bf16 v[24:27], a[72:75], v[48:51], v[24:27]\n"
"ds_read_b128 v[40:43], v4 offset:38912\n"
"v_mfma_f32_16x16x32_bf16 v[24:27], a[184:187], v[52:55], v[24:27]\n"
"buffer_load_dwordx4 v2, s[32:35], s63 offen offset:2048 sc1 lds\n"
"v_mfma_f32_16x16x32_bf16 v[28:31], a[188:191], v[52:55], v[28:31]\n"
"ds_read_b128 v[44:47], v4 offset:53248\n"
"v_mfma_f32_16x16x32_bf16 v[28:31], a[76:79], v[48:51], v[28:31]\n"
"s_waitcnt lgkmcnt(4)\n"
"v_mfma_f32_16x16x32_bf16 v[28:31], v[108:111], v[56:59], v[28:31]\n"
"ds_read_b128 v[48:51], v4 offset:11264\n"
"v_mfma_f32_16x16x32_bf16 v[28:31], v[220:223], v[60:63], v[28:31]\n"
"v_mfma_f32_16x16x32_bf16 v[24:27], v[216:219], v[60:63], v[24:27]\n"
"ds_read_b128 v[52:55], v4 offset:25600\n"
"v_mfma_f32_16x16x32_bf16 v[24:27], v[104:107], v[56:59], v[24:27]\n"
"s_waitcnt lgkmcnt(4)\n"
"v_mfma_f32_16x16x32_bf16 v[24:27], a[80:83], v[32:35], v[24:27]\n"
"ds_read_b128 v[56:59], v4 offset:39936\n"
"v_mfma_f32_16x16x32_bf16 v[24:27], a[192:195], v[36:39], v[24:27]\n"
"buffer_load_dwordx4 v2, s[32:35], s63 offen offset:3072 sc1 lds\n"
"s_add_u32 s63, s63, 4096\n"
"v_mfma_f32_16x16x32_bf16 v[28:31], a[196:199], v[36:39], v[28:31]\n"
"ds_read_b128 v[60:63], v4 offset:54272\n"
"s_add_u32 m0, s64, 12288\n"
"v_mfma_f32_16x16x32_bf16 v[28:31], a[84:87], v[32:35], v[28:31]\n"
"s_waitcnt lgkmcnt(4)\n"
"v_mfma_f32_16x16x32_bf16 v[28:31], v[116:119], v[40:43], v[28:31]\n"
"ds_read_b128 v[32:35], v4 offset:12288\n"
"v_mfma_f32_16x16x32_bf16 v[28:31], v[228:231], v[44:47], v[28:31]\n"
"buffer_load_dwordx4 v2, s[32:35], s63 offen offset:0 sc1 lds\n"
"v_mfma_f32_16x16x32_bf16 v[24:27], v[224:227], v[44:47], v[24:27]\n"
"ds_read_b128 v[36:39], v4 offset:26624\n"
"v_mfma_f32_16x16x32_bf16 v[24:27], v[112:115], v[40:43], v[24:27]\n"
"s_waitcnt lgkmcnt(4)\n"
"v_mfma_f32_16x16x32_bf16 v[24:27], a[88:91], v[48:51], v[24:27]\n"
"ds_read_b128 v[40:43], v4 offset:40960\n"
"v_mfma_f32_16x16x32_bf16 v[24:27], a[200:203], v[52:55], v[24:27]\n"
"buffer_load_dwordx4 v2, s[32:35], s63 offen offset:1024 sc1 lds\n"
"s_add_u32 s63, s63, 1175552\n"
"v_mfma_f32_16x16x32_bf16 v[28:31], a[204:207], v[52:55], v[28:31]\n"
"ds_read_b128 v[44:47], v4 offset:55296\n"
"v_mfma_f32_16x16x32_bf16 v[28:31], a[92:95], v[48:51], v[28:31]\n"
"s_waitcnt lgkmcnt(4)\n"
"v_mfma_f32_16x16x32_bf16 v[28:31], v[124:127], v[56:59], v[28:31]\n"
"ds_read_b128 v[48:51], v4 offset:13312\n"
"s_waitcnt vmcnt(10)\n"
"v_mfma_f32_16x16x32_bf16 v[28:31], v[236:239], v[60:63], v[28:31]\n"
"v_lshlrev_b32 v16, 16, v12\n"
"v_and_b32 v17, 0xffff0000, v12\n"
"v_mfma_f32_16x16x32_bf16 v[24:27], v[232:235], v[60:63], v[24:27]\n"
"ds_read_b128 v[52:55], v4 offset:27648\n"
"v_lshlrev_b32 v18, 16, v13\n"
"v_mfma_f32_16x16x32_bf16 v[24:27], v[120:123], v[56:59], v[24:27]\n"
"v_and_b32 v19, 0xffff0000, v13\n"
"s_waitcnt lgkmcnt(4)\n"
"v_mfma_f32_16x16x32_bf16 v[24:27], a[96:99], v[32:35], v[24:27]\n"
"s_barrier\n"
"v_mfma_f32_16x16x32_bf16 v[24:27], a[208:211], v[36:39], v[24:27]\n"
"ds_read_b128 v[56:59], v4 offset:41984\n"
"v_lshlrev_b32 v20, 16, v14\n"
"v_mfma_f32_16x16x32_bf16 v[28:31], a[212:215], v[36:39], v[28:31]\n"
"ds_read_b128 v[60:63], v4 offset:56320\n"
"v_and_b32 v21, 0xffff0000, v14\n"
"v_mfma_f32_16x16x32_bf16 v[28:31], a[100:103], v[32:35], v[28:31]\n"
"v_lshlrev_b32 v22, 16, v15\n"
"s_waitcnt lgkmcnt(4)\n"
"v_mfma_f32_16x16x32_bf16 v[28:31], v[132:135], v[40:43], v[28:31]\n"
"ds_read_b128 v[32:35], v2 offset:0\n"
"v_and_b32 v23, 0xffff0000, v15\n"
"v_mfma_f32_16x16x32_bf16 v[28:31], v[244:247], v[44:47], v[28:31]\n"
"v_mfma_f32_16x16x32_bf16 v[24:27], v[240:243], v[44:47], v[24:27]\n"
"ds_read_b128 v[36:39], v2 offset:14336\n"
"v_mfma_f32_16x16x32_bf16 v[24:27], v[128:131], v[40:43], v[24:27]\n"
"s_waitcnt lgkmcnt(4)\n"
"v_mfma_f32_16x16x32_bf16 v[24:27], a[104:107], v[48:51], v[24:27]\n"
"ds_read_b128 v[40:43], v2 offset:28672\n"
"v_mfma_f32_16x16x32_bf16 v[24:27], a[216:219], v[52:55], v[24:27]\n"
"v_mfma_f32_16x16x32_bf16 v[28:31], a[220:223], v[52:55], v[28:31]\n"
"ds_read_b128 v[44:47], v2 offset:43008\n"
"v_mfma_f32_16x16x32_bf16 v[28:31], a[108:111], v[48:51], v[28:31]\n"
"s_waitcnt lgkmcnt(4)\n"
"v_mfma_f32_16x16x32_bf16 v[28:31], v[140:143], v[56:59], v[28:31]\n"
"ds_read_b128 v[48:51], v2 offset:1024\n"
"v_mfma_f32_16x16x32_bf16 v[28:31], v[252:255], v[60:63], v[28:31]\n"
"v_mfma_f32_16x16x32_bf16 v[24:27], v[248:251], v[60:63], v[24:27]\n"
"ds_read_b128 v[52:55], v2 offset:15360\n"
"v_mfma_f32_16x16x32_bf16 v[24:27], v[136:139], v[56:59], v[24:27]\n"
"s_branch .L_dll_3\n"
".L_dlb_3:\n"
"s_mov_b32 s62, 15\n"
"s_add_u32 s74, s74, 8192\n"
"v_lshrrev_b32 v5, 8, v2\n"
"v_lshl_add_u32 v5, v5, 4, s74\n"
"v_and_b32 v3, 128, v2\n"
"v_lshl_add_u32 v5, v3, 2, v5\n"
"v_and_b32 v3, 16, v2\n"
"v_lshl_add_u32 v5, v3, 4, v5\n"
"v_and_b32 v3, 96, v2\n"
"v_lshl_add_u32 v5, v3, 1, v5\n"
"v_lshrrev_b32 v3, 2, v2\n"
"v_add_u32 v3, s74, v3\n"
".p2align 6\n"
".L_dml_3:\n"
"s_waitcnt lgkmcnt(4)\n"
"v_mfma_f32_16x16x32_bf16 v[24:27], a[0:3], v[32:35], v[24:27]\n"
"ds_read_b128 v[56:59], v4 offset:29696\n"
"v_mfma_f32_16x16x32_bf16 v[24:27], a[112:115], v[36:39], v[24:27]\n"
"v_mfma_f32_16x16x32_bf16 v[28:31], a[116:119], v[36:39], v[28:31]\n"
"ds_read_b128 v[60:63], v4 offset:44032\n"
"v_mfma_f32_16x16x32_bf16 v[28:31], a[4:7], v[32:35], v[28:31]\n"
"v_cvt_pk_bf16_f32 v15, v22, v23\n"
"s_waitcnt lgkmcnt(4)\n"
"v_mfma_f32_16x16x32_bf16 v[28:31], a[228:231], v[40:43], v[28:31]\n"
"ds_read_b128 v[32:35], v4 offset:2048\n"
"v_cvt_pk_bf16_f32 v14, v20, v21\n"
"v_mfma_f32_16x16x32_bf16 v[28:31], v[148:151], v[44:47], v[28:31]\n"
"v_cvt_pk_bf16_f32 v13, v18, v19\n"
"v_cvt_pk_bf16_f32 v12, v16, v17\n"
"v_mfma_f32_16x16x32_bf16 v[24:27], v[144:147], v[44:47], v[24:27]\n"
"ds_read_b128 v[36:39], v4 offset:16384\n"
"ds_write_b128 v5, v[12:15]\n"
"v_mfma_f32_16x16x32_bf16 v[24:27], a[224:227], v[40:43], v[24:27]\n"
"ds_read2st64_b32 v[12:13], v3 offset0:0 offset1:1\n"
"s_waitcnt lgkmcnt(6)\n"
"v_mfma_f32_16x16x32_bf16 v[24:27], a[8:11], v[48:51], v[24:27]\n"
"ds_read_b128 v[40:43], v4 offset:30720\n"
"ds_read2st64_b32 v[14:15], v3 offset0:2 offset1:3\n"
"v_mfma_f32_16x16x32_bf16 v[24:27], a[120:123], v[52:55], v[24:27]\n"
"v_mfma_f32_16x16x32_bf16 v[28:31], a[124:127], v[52:55], v[28:31]\n"
"ds_read_b128 v[44:47], v4 offset:45056\n"
"v_mfma_f32_16x16x32_bf16 v[28:31], a[12:15], v[48:51], v[28:31]\n"
"s_waitcnt lgkmcnt(7)\n"
"v_mfma_f32_16x16x32_bf16 v[28:31], a[236:239], v[56:59], v[28:31]\n"
"ds_read_b128 v[48:51], v4 offset:3072\n"
"v_mfma_f32_16x16x32_bf16 v[28:31], v[156:159], v[60:63], v[28:31]\n"
"s_waitcnt lgkmcnt(2)\n"
"buffer_atomic_pk_add_bf16 v12, v1, s[44:47], s65 offen offset:0 sc1\n"
"v_mfma_f32_16x16x32_bf16 v[24:27], v[152:155], v[60:63], v[24:27]\n"
"ds_read_b128 v[52:55], v4 offset:17408\n"
"v_mfma_f32_16x16x32_bf16 v[24:27], a[232:235], v[56:59], v[24:27]\n"
"buffer_atomic_pk_add_bf16 v13, v1, s[44:47], s65 offen offset:2048 sc1\n"
"v_mfma_f32_16x16x32_bf16 v[24:27], a[16:19], v[32:35], v[24:27]\n"
"s_waitcnt vmcnt(2)\n"
"s_barrier\n"
"v_mfma_f32_16x16x32_bf16 v[24:27], a[128:131], v[36:39], v[24:27]\n"
"ds_read_b128 v[56:59], v4 offset:31744\n"
"s_add_u32 s65, s65, 16384\n"
"v_mfma_f32_16x16x32_bf16 v[28:31], a[132:135], v[36:39], v[28:31]\n"
"ds_read_b128 v[60:63], v4 offset:46080\n"
"v_mfma_f32_16x16x32_bf16 v[28:31], a[20:23], v[32:35], v[28:31]\n"
"buffer_atomic_pk_add_bf16 v14, v1, s[44:47], s65 offen offset:0 sc1\n"
"s_waitcnt lgkmcnt(4)\n"
"v_mfma_f32_16x16x32_bf16 v[28:31], a[244:247], v[40:43], v[28:31]\n"
"ds_read_b128 v[32:35], v4 offset:4096\n"
"s_add_u32 m0, s64, 0\n"
"v_mfma_f32_16x16x32_bf16 v[28:31], v[164:167], v[44:47], v[28:31]\n"
"buffer_atomic_pk_add_bf16 v15, v1, s[44:47], s65 offen offset:2048 sc1\n"
"s_add_u32 s65, s65, 16384\n"
"v_mfma_f32_16x16x32_bf16 v[24:27], v[160:163], v[44:47], v[24:27]\n"
"ds_read_b128 v[36:39], v4 offset:18432\n"
"v_mfma_f32_16x16x32_bf16 v[24:27], a[240:243], v[40:43], v[24:27]\n"
"s_waitcnt lgkmcnt(4)\n"
"v_mfma_f32_16x16x32_bf16 v[24:27], a[24:27], v[48:51], v[24:27]\n"
"ds_read_b128 v[40:43], v4 offset:32768\n"
"v_mfma_f32_16x16x32_bf16 v[24:27], a[136:139], v[52:55], v[24:27]\n"
"buffer_load_dwordx4 v2, s[32:35], s63 offen offset:0 sc1 lds\n"
"v_mfma_f32_16x16x32_bf16 v[28:31], a[140:143], v[52:55], v[28:31]\n"
"ds_read_b128 v[44:47], v4 offset:47104\n"
"v_mfma_f32_16x16x32_bf16 v[28:31], a[28:31], v[48:51], v[28:31]\n"
"s_waitcnt lgkmcnt(4)\n"
"v_mfma_f32_16x16x32_bf16 v[28:31], a[252:255], v[56:59], v[28:31]\n"
"ds_read_b128 v[48:51], v4 offset:5120\n"
"v_mfma_f32_16x16x32_bf16 v[28:31], v[172:175], v[60:63], v[28:31]\n"
"buffer_load_dwordx4 v2, s[32:35], s63 offen offset:1024 sc1 lds\n"
"v_mfma_f32_16x16x32_bf16 v[24:27], v[168:171], v[60:63], v[24:27]\n"
"ds_read_b128 v[52:55], v4 offset:19456\n"
"v_mfma_f32_16x16x32_bf16 v[24:27], a[248:251], v[56:59], v[24:27]\n"
"s_waitcnt lgkmcnt(4)\n"
"v_mfma_f32_16x16x32_bf16 v[24:27], a[32:35], v[32:35], v[24:27]\n"
"ds_read_b128 v[56:59], v4 offset:33792\n"
"v_mfma_f32_16x16x32_bf16 v[24:27], a[144:147], v[36:39], v[24:27]\n"
"buffer_load_dwordx4 v2, s[32:35], s63 offen offset:2048 sc1 lds\n"
"v_mfma_f32_16x16x32_bf16 v[28:31], a[148:151], v[36:39], v[28:31]\n"
"ds_read_b128 v[60:63], v4 offset:48128\n"
"v_mfma_f32_16x16x32_bf16 v[28:31], a[36:39], v[32:35], v[28:31]\n"
"s_waitcnt lgkmcnt(4)\n"
"v_mfma_f32_16x16x32_bf16 v[28:31], v[68:71], v[40:43], v[28:31]\n"
"ds_read_b128 v[32:35], v4 offset:6144\n"
"v_mfma_f32_16x16x32_bf16 v[28:31], v[180:183], v[44:47], v[28:31]\n"
"buffer_load_dwordx4 v2, s[32:35], s63 offen offset:3072 sc1 lds\n"
"v_mfma_f32_16x16x32_bf16 v[24:27], v[176:179], v[44:47], v[24:27]\n"
"ds_read_b128 v[36:39], v4 offset:20480\n"
"v_mfma_f32_16x16x32_bf16 v[24:27], v[64:67], v[40:43], v[24:27]\n"
"s_waitcnt lgkmcnt(4)\n"
"v_mfma_f32_16x16x32_bf16 v[24:27], a[40:43], v[48:51], v[24:27]\n"
"ds_read_b128 v[40:43], v4 offset:34816\n"
"v_mfma_f32_16x16x32_bf16 v[24:27], a[152:155], v[52:55], v[24:27]\n"
"buffer_load_dwordx4 v[12:15], v2, s[32:35], s71 offen sc1\n"
"s_add_u32 s71, s71, 1187840\n"
"v_mfma_f32_16x16x32_bf16 v[28:31], a[156:159], v[52:55], v[28:31]\n"
"ds_read_b128 v[44:47], v4 offset:49152\n"
"s_add_u32 s63, s63, 4096\n"
"v_mfma_f32_16x16x32_bf16 v[28:31], a[44:47], v[48:51], v[28:31]\n"
"s_add_u32 m0, s64, 4096\n"
"s_waitcnt lgkmcnt(4)\n"
"v_mfma_f32_16x16x32_bf16 v[28:31], v[76:79], v[56:59], v[28:31]\n"
"ds_read_b128 v[48:51], v4 offset:7168\n"
"v_mfma_f32_16x16x32_bf16 v[28:31], v[188:191], v[60:63], v[28:31]\n"
"buffer_load_dwordx4 v2, s[32:35], s63 offen offset:0 sc1 lds\n"
"v_mfma_f32_16x16x32_bf16 v[24:27], v[184:187], v[60:63], v[24:27]\n"
"ds_read_b128 v[52:55], v4 offset:21504\n"
"v_mfma_f32_16x16x32_bf16 v[24:27], v[72:75], v[56:59], v[24:27]\n"
"s_waitcnt lgkmcnt(4)\n"
"v_mfma_f32_16x16x32_bf16 v[24:27], a[48:51], v[32:35], v[24:27]\n"
"ds_read_b128 v[56:59], v4 offset:35840\n"
"v_mfma_f32_16x16x32_bf16 v[24:27], a[160:163], v[36:39], v[24:27]\n"
"buffer_load_dwordx4 v2, s[32:35], s63 offen offset:1024 sc1 lds\n"
"v_mfma_f32_16x16x32_bf16 v[28:31], a[164:167], v[36:39], v[28:31]\n"
"ds_read_b128 v[60:63], v4 offset:50176\n"
"v_mfma_f32_16x16x32_bf16 v[28:31], a[52:55], v[32:35], v[28:31]\n"
"s_waitcnt lgkmcnt(4)\n"
"v_mfma_f32_16x16x32_bf16 v[28:31], v[84:87], v[40:43], v[28:31]\n"
"ds_read_b128 v[32:35], v4 offset:8192\n"
"v_mfma_f32_16x16x32_bf16 v[28:31], v[196:199], v[44:47], v[28:31]\n"
"buffer_load_dwordx4 v2, s[32:35], s63 offen offset:2048 sc1 lds\n"
"v_mfma_f32_16x16x32_bf16 v[24:27], v[192:195], v[44:47], v[24:27]\n"
"ds_read_b128 v[36:39], v4 offset:22528\n"
"v_mfma_f32_16x16x32_bf16 v[24:27], v[80:83], v[40:43], v[24:27]\n"
"s_waitcnt lgkmcnt(4)\n"
"v_mfma_f32_16x16x32_bf16 v[24:27], a[56:59], v[48:51], v[24:27]\n"
"ds_read_b128 v[40:43], v4 offset:36864\n"
"v_mfma_f32_16x16x32_bf16 v[24:27], a[168:171], v[52:55], v[24:27]\n"
"buffer_load_dwordx4 v2, s[32:35], s63 offen offset:3072 sc1 lds\n"
"v_mfma_f32_16x16x32_bf16 v[28:31], a[172:175], v[52:55], v[28:31]\n"
"ds_read_b128 v[44:47], v4 offset:51200\n"
"v_mfma_f32_16x16x32_bf16 v[28:31], a[60:63], v[48:51], v[28:31]\n"
"s_waitcnt lgkmcnt(4)\n"
"v_mfma_f32_16x16x32_bf16 v[28:31], v[92:95], v[56:59], v[28:31]\n"
"ds_read_b128 v[48:51], v4 offset:9216\n"
"v_mfma_f32_16x16x32_bf16 v[28:31], v[204:207], v[60:63], v[28:31]\n"
"s_add_u32 s63, s63, 4096\n"
"s_add_u32 m0, s64, 8192\n"
"v_mfma_f32_16x16x32_bf16 v[24:27], v[200:203], v[60:63], v[24:27]\n"
"ds_read_b128 v[52:55], v4 offset:23552\n"
"v_mfma_f32_16x16x32_bf16 v[24:27], v[88:91], v[56:59], v[24:27]\n"
"s_waitcnt lgkmcnt(4)\n"
"v_mfma_f32_16x16x32_bf16 v[24:27], a[64:67], v[32:35], v[24:27]\n"
"ds_read_b128 v[56:59], v4 offset:37888\n"
"v_mfma_f32_16x16x32_bf16 v[24:27], a[176:179], v[36:39], v[24:27]\n"
"buffer_load_dwordx4 v2, s[32:35], s63 offen offset:0 sc1 lds\n"
"v_mfma_f32_16x16x32_bf16 v[28:31], a[180:183], v[36:39], v[28:31]\n"
"ds_read_b128 v[60:63], v4 offset:52224\n"
"v_mfma_f32_16x16x32_bf16 v[28:31], a[68:71], v[32:35], v[28:31]\n"
"s_waitcnt lgkmcnt(4)\n"
"v_mfma_f32_16x16x32_bf16 v[28:31], v[100:103], v[40:43], v[28:31]\n"
"ds_read_b128 v[32:35], v4 offset:10240\n"
"v_mfma_f32_16x16x32_bf16 v[28:31], v[212:215], v[44:47], v[28:31]\n"
"buffer_load_dwordx4 v2, s[32:35], s63 offen offset:1024 sc1 lds\n"
"v_mfma_f32_16x16x32_bf16 v[24:27], v[208:211], v[44:47], v[24:27]\n"
"ds_read_b128 v[36:39], v4 offset:24576\n"
"v_mfma_f32_16x16x32_bf16 v[24:27], v[96:99], v[40:43], v[24:27]\n"
"s_waitcnt lgkmcnt(4)\n"
"v_mfma_f32_16x16x32_bf16 v[24:27], a[72:75], v[48:51], v[24:27]\n"
"ds_read_b128 v[40:43], v4 offset:38912\n"
"v_mfma_f32_16x16x32_bf16 v[24:27], a[184:187], v[52:55], v[24:27]\n"
"buffer_load_dwordx4 v2, s[32:35], s63 offen offset:2048 sc1 lds\n"
"v_mfma_f32_16x16x32_bf16 v[28:31], a[188:191], v[52:55], v[28:31]\n"
"ds_read_b128 v[44:47], v4 offset:53248\n"
"v_mfma_f32_16x16x32_bf16 v[28:31], a[76:79], v[48:51], v[28:31]\n"
"s_waitcnt lgkmcnt(4)\n"
"v_mfma_f32_16x16x32_bf16 v[28:31], v[108:111], v[56:59], v[28:31]\n"
"ds_read_b128 v[48:51], v4 offset:11264\n"
"v_mfma_f32_16x16x32_bf16 v[28:31], v[220:223], v[60:63], v[28:31]\n"
"v_mfma_f32_16x16x32_bf16 v[24:27], v[216:219], v[60:63], v[24:27]\n"
"ds_read_b128 v[52:55], v4 offset:25600\n"
"v_mfma_f32_16x16x32_bf16 v[24:27], v[104:107], v[56:59], v[24:27]\n"
"s_waitcnt lgkmcnt(4)\n"
"v_mfma_f32_16x16x32_bf16 v[24:27], a[80:83], v[32:35], v[24:27]\n"
"ds_read_b128 v[56:59], v4 offset:39936\n"
"v_mfma_f32_16x16x32_bf16 v[24:27], a[192:195], v[36:39], v[24:27]\n"
"buffer_load_dwordx4 v2, s[32:35], s63 offen offset:3072 sc1 lds\n"
"s_add_u32 s63, s63, 4096\n"
"v_mfma_f32_16x16x32_bf16 v[28:31], a[196:199], v[36:39], v[28:31]\n"
"ds_read_b128 v[60:63], v4 offset:54272\n"
"s_add_u32 m0, s64, 12288\n"
"v_mfma_f32_16x16x32_bf16 v[28:31], a[84:87], v[32:35], v[28:31]\n"
"s_waitcnt lgkmcnt(4)\n"
"v_mfma_f32_16x16x32_bf16 v[28:31], v[116:119], v[40:43], v[28:31]\n"
"ds_read_b128 v[32:35], v4 offset:12288\n"
"v_mfma_f32_16x16x32_bf16 v[28:31], v[228:231], v[44:47], v[28:31]\n"
"buffer_load_dwordx4 v2, s[32:35], s63 offen offset:0 sc1 lds\n"
"v_mfma_f32_16x16x32_bf16 v[24:27], v[224:227], v[44:47], v[24:27]\n"
"ds_read_b128 v[36:39], v4 offset:26624\n"
"v_mfma_f32_16x16x32_bf16 v[24:27], v[112:115], v[40:43], v[24:27]\n"
"s_waitcnt lgkmcnt(4)\n"
"v_mfma_f32_16x16x32_bf16 v[24:27], a[88:91], v[48:51], v[24:27]\n"
"ds_read_b128 v[40:43], v4 offset:40960\n"
"v_mfma_f32_16x16x32_bf16 v[24:27], a[200:203], v[52:55], v[24:27]\n"
"buffer_load_dwordx4 v2, s[32:35], s63 offen offset:1024 sc1 lds\n"
"s_add_u32 s63, s63, 1175552\n"
"v_mfma_f32_16x16x32_bf16 v[28:31], a[204:207], v[52:55], v[28:31]\n"
"ds_read_b128 v[44:47], v4 offset:55296\n"
"v_mfma_f32_16x16x32_bf16 v[28:31], a[92:95], v[48:51], v[28:31]\n"
"s_waitcnt lgkmcnt(4)\n"
"v_mfma_f32_16x16x32_bf16 v[28:31], v[124:127], v[56:59], v[28:31]\n"
"ds_read_b128 v[48:51], v4 offset:13312\n"
"s_waitcnt vmcnt(10)\n"
"v_mfma_f32_16x16x32_bf16 v[28:31], v[236:239], v[60:63], v[28:31]\n"
"v_lshlrev_b32 v16, 16, v12\n"
"v_and_b32 v17, 0xffff0000, v12\n"
"v_mfma_f32_16x16x32_bf16 v[24:27], v[232:235], v[60:63], v[24:27]\n"
"ds_read_b128 v[52:55], v4 offset:27648\n"
"v_lshlrev_b32 v18, 16, v13\n"
"v_mfma_f32_16x16x32_bf16 v[24:27], v[120:123], v[56:59], v[24:27]\n"
"v_and_b32 v19, 0xffff0000, v13\n"
"s_waitcnt lgkmcnt(4)\n"
"v_mfma_f32_16x16x32_bf16 v[24:27], a[96:99], v[32:35], v[24:27]\n"
"s_barrier\n"
"v_mfma_f32_16x16x32_bf16 v[24:27], a[208:211], v[36:39], v[24:27]\n"
"ds_read_b128 v[56:59], v4 offset:41984\n"
"v_lshlrev_b32 v20, 16, v14\n"
"v_mfma_f32_16x16x32_bf16 v[28:31], a[212:215], v[36:39], v[28:31]\n"
"ds_read_b128 v[60:63], v4 offset:56320\n"
"v_and_b32 v21, 0xffff0000, v14\n"
"v_mfma_f32_16x16x32_bf16 v[28:31], a[100:103], v[32:35], v[28:31]\n"
"v_lshlrev_b32 v22, 16, v15\n"
"s_waitcnt lgkmcnt(4)\n"
"v_mfma_f32_16x16x32_bf16 v[28:31], v[132:135], v[40:43], v[28:31]\n"
"ds_read_b128 v[32:35], v2 offset:0\n"
"v_and_b32 v23, 0xffff0000, v15\n"
"v_mfma_f32_16x16x32_bf16 v[28:31], v[244:247], v[44:47], v[28:31]\n"
"v_mfma_f32_16x16x32_bf16 v[24:27], v[240:243], v[44:47], v[24:27]\n"
"ds_read_b128 v[36:39], v2 offset:14336\n"
"v_mfma_f32_16x16x32_bf16 v[24:27], v[128:131], v[40:43], v[24:27]\n"
"s_waitcnt lgkmcnt(4)\n"
"v_mfma_f32_16x16x32_bf16 v[24:27], a[104:107], v[48:51], v[24:27]\n"
"ds_read_b128 v[40:43], v2 offset:28672\n"
"v_mfma_f32_16x16x32_bf16 v[24:27], a[216:219], v[52:55], v[24:27]\n"
"v_mfma_f32_16x16x32_bf16 v[28:31], a[220:223], v[52:55], v[28:31]\n"
"ds_read_b128 v[44:47], v2 offset:43008\n"
"v_mfma_f32_16x16x32_bf16 v[28:31], a[108:111], v[48:51], v[28:31]\n"
"s_waitcnt lgkmcnt(4)\n"
"v_mfma_f32_16x16x32_bf16 v[28:31], v[140:143], v[56:59], v[28:31]\n"
"ds_read_b128 v[48:51], v2 offset:1024\n"
"v_mfma_f32_16x16x32_bf16 v[28:31], v[252:255], v[60:63], v[28:31]\n"
"v_mfma_f32_16x16x32_bf16 v[24:27], v[248:251], v[60:63], v[24:27]\n"
"ds_read_b128 v[52:55], v2 offset:15360\n"
"s_sub_u32 s62, s62, 1\n"
"v_mfma_f32_16x16x32_bf16 v[24:27], v[136:139], v[56:59], v[24:27]\n"
"s_cbranch_scc1 .L_dmt_3\n"
"s_waitcnt lgkmcnt(4)\n"
"v_mfma_f32_16x16x32_bf16 v[16:19], a[0:3], v[32:35], v[16:19]\n"
"ds_read_b128 v[56:59], v2 offset:29696\n"
"v_mfma_f32_16x16x32_bf16 v[16:19], a[112:115], v[36:39], v[16:19]\n"
"v_mfma_f32_16x16x32_bf16 v[20:23], a[116:119], v[36:39], v[20:23]\n"
"ds_read_b128 v[60:63], v2 offset:44032\n"
"v_mfma_f32_16x16x32_bf16 v[20:23], a[4:7], v[32:35], v[20:23]\n"
"v_cvt_pk_bf16_f32 v15, v30, v31\n"
"s_waitcnt lgkmcnt(4)\n"
"v_mfma_f32_16x16x32_bf16 v[20:23], a[228:231], v[40:43], v[20:23]\n"
"ds_read_b128 v[32:35], v2 offset:2048\n"
"v_cvt_pk_bf16_f32 v14, v28, v29\n"
"v_mfma_f32_16x16x32_bf16 v[20:23], v[148:151], v[44:47], v[20:23]\n"
"v_cvt_pk_bf16_f32 v13, v26, v27\n"
"v_cvt_pk_bf16_f32 v12, v24, v25\n"
"v_mfma_f32_16x16x32_bf16 v[16:19], v[144:147], v[44:47], v[16:19]\n"
"ds_read_b128 v[36:39], v2 offset:16384\n"
"ds_write_b128 v5, v[12:15]\n"
"v_mfma_f32_16x16x32_bf16 v[16:19], a[224:227], v[40:43], v[16:19]\n"
"ds_read2st64_b32 v[12:13], v3 offset0:0 offset1:1\n"
"s_waitcnt lgkmcnt(6)\n"
"v_mfma_f32_16x16x32_bf16 v[16:19], a[8:11], v[48:51], v[16:19]\n"
"ds_read_b128 v[40:43], v2 offset:30720\n"
"ds_read2st64_b32 v[14:15], v3 offset0:2 offset1:3\n"
"v_mfma_f32_16x16x32_bf16 v[16:19], a[120:123], v[52:55], v[16:19]\n"
"v_mfma_f32_16x16x32_bf16 v[20:23], a[124:127], v[52:55], v[20:23]\n"
"ds_read_b128 v[44:47], v2 offset:45056\n"
"v_mfma_f32_16x16x32_bf16 v[20:23], a[12:15], v[48:51], v[20:23]\n"
"s_waitcnt lgkmcnt(7)\n"
"v_mfma_f32_16x16x32_bf16 v[20:23], a[236:239], v[56:59], v[20:23]\n"
"ds_read_b128 v[48:51], v2 offset:3072\n"
"v_mfma_f32_16x16x32_bf16 v[20:23], v[156:159], v[60:63], v[20:23]\n"
"s_waitcnt lgkmcnt(2)\n"
"buffer_atomic_pk_add_bf16 v12, v1, s[44:47], s65 offen offset:0 sc1\n"
"v_mfma_f32_16x16x32_bf16 v[16:19], v[152:155], v[60:63], v[16:19]\n"
"ds_read_b128 v[52:55], v2 offset:17408\n"
"v_mfma_f32_16x16x32_bf16 v[16:19], a[232:235], v[56:59], v[16:19]\n"
"buffer_atomic_pk_add_bf16 v13, v1, s[44:47], s65 offen offset:2048 sc1\n"
"v_mfma_f32_16x16x32_bf16 v[16:19], a[16:19], v[32:35], v[16:19]\n"
"s_waitcnt vmcnt(2)\n"
"s_barrier\n"
"v_mfma_f32_16x16x32_bf16 v[16:19], a[128:131], v[36:39], v[16:19]\n"
"ds_read_b128 v[56:59], v2 offset:31744\n"
"s_add_u32 s65, s65, 16384\n"
"v_mfma_f32_16x16x32_bf16 v[20:23], a[132:135], v[36:39], v[20:23]\n"
"ds_read_b128 v[60:63], v2 offset:46080\n"
"v_mfma_f32_16x16x32_bf16 v[20:23], a[20:23], v[32:35], v[20:23]\n"
"buffer_atomic_pk_add_bf16 v14, v1, s[44:47], s65 offen offset:0 sc1\n"
"s_waitcnt lgkmcnt(4)\n"
"v_mfma_f32_16x16x32_bf16 v[20:23], a[244:247], v[40:43], v[20:23]\n"
"ds_read_b128 v[32:35], v2 offset:4096\n"
"s_add_u32 m0, s64, 57344\n"
"v_mfma_f32_16x16x32_bf16 v[20:23], v[164:167], v[44:47], v[20:23]\n"
"buffer_atomic_pk_add_bf16 v15, v1, s[44:47], s65 offen offset:2048 sc1\n"
"s_add_u32 s65, s65, 16384\n"
"v_mfma_f32_16x16x32_bf16 v[16:19], v[160:163], v[44:47], v[16:19]\n"
"ds_read_b128 v[36:39], v2 offset:18432\n"
"v_mfma_f32_16x16x32_bf16 v[16:19], a[240:243], v[40:43], v[16:19]\n"
"s_waitcnt lgkmcnt(4)\n"
"v_mfma_f32_16x16x32_bf16 v[16:19], a[24:27], v[48:51], v[16:19]\n"
"ds_read_b128 v[40:43], v2 offset:32768\n"
"v_mfma_f32_16x16x32_bf16 v[16:19], a[136:139], v[52:55], v[16:19]\n"
"buffer_load_dwordx4 v2, s[32:35], s63 offen offset:0 sc1 lds\n"
"v_mfma_f32_16x16x32_bf16 v[20:23], a[140:143], v[52:55], v[20:23]\n"
"ds_read_b128 v[44:47], v2 offset:47104\n"
"v_mfma_f32_16x16x32_bf16 v[20:23], a[28:31], v[48:51], v[20:23]\n"
"s_waitcnt lgkmcnt(4)\n"
"v_mfma_f32_16x16x32_bf16 v[20:23], a[252:255], v[56:59], v[20:23]\n"
"ds_read_b128 v[48:51], v2 offset:5120\n"
"v_mfma_f32_16x16x32_bf16 v[20:23], v[172:175], v[60:63], v[20:23]\n"
"buffer_load_dwordx4 v2, s[32:35], s63 offen offset:1024 sc1 lds\n"
"v_mfma_f32_16x16x32_bf16 v[16:19], v[168:171], v[60:63], v[16:19]\n"
"ds_read_b128 v[52:55], v2 offset:19456\n"
"v_mfma_f32_16x16x32_bf16 v[16:19], a[248:251], v[56:59], v[16:19]\n"
"s_waitcnt lgkmcnt(4)\n"
"v_mfma_f32_16x16x32_bf16 v[16:19], a[32:35], v[32:35], v[16:19]\n"
"ds_read_b128 v[56:59], v2 offset:33792\n"
"v_mfma_f32_16x16x32_bf16 v[16:19], a[144:147], v[36:39], v[16:19]\n"
"buffer_load_dwordx4 v2, s[32:35], s63 offen offset:2048 sc1 lds\n"
"v_mfma_f32_16x16x32_bf16 v[20:23], a[148:151], v[36:39], v[20:23]\n"
"ds_read_b128 v[60:63], v2 offset:48128\n"
"v_mfma_f32_16x16x32_bf16 v[20:23], a[36:39], v[32:35], v[20:23]\n"
"s_waitcnt lgkmcnt(4)\n"
"v_mfma_f32_16x16x32_bf16 v[20:23], v[68:71], v[40:43], v[20:23]\n"
"ds_read_b128 v[32:35], v2 offset:6144\n"
"v_mfma_f32_16x16x32_bf16 v[20:23], v[180:183], v[44:47], v[20:23]\n"
"buffer_load_dwordx4 v2, s[32:35], s63 offen offset:3072 sc1 lds\n"
"v_mfma_f32_16x16x32_bf16 v[16:19], v[176:179], v[44:47], v[16:19]\n"
"ds_read_b128 v[36:39], v2 offset:20480\n"
"v_mfma_f32_16x16x32_bf16 v[16:19], v[64:67], v[40:43], v[16:19]\n"
"s_waitcnt lgkmcnt(4)\n"
"v_mfma_f32_16x16x32_bf16 v[16:19], a[40:43], v[48:51], v[16:19]\n"
"ds_read_b128 v[40:43], v2 offset:34816\n"
"v_mfma_f32_16x16x32_bf16 v[16:19], a[152:155], v[52:55], v[16:19]\n"
"buffer_load_dwordx4 v[12:15], v2, s[32:35], s71 offen sc1\n"
"s_add_u32 s71, s71, 1187840\n"
"v_mfma_f32_16x16x32_bf16 v[20:23], a[156:159], v[52:55], v[20:23]\n"
"ds_read_b128 v[44:47], v2 offset:49152\n"
"s_add_u32 s63, s63, 4096\n"
"v_mfma_f32_16x16x32_bf16 v[20:23], a[44:47], v[48:51], v[20:23]\n"
"s_add_u32 m0, s64, 61440\n"
"s_waitcnt lgkmcnt(4)\n"
"v_mfma_f32_16x16x32_bf16 v[20:23], v[76:79], v[56:59], v[20:23]\n"
"ds_read_b128 v[48:51], v2 offset:7168\n"
"v_mfma_f32_16x16x32_bf16 v[20:23], v[188:191], v[60:63], v[20:23]\n"
"buffer_load_dwordx4 v2, s[32:35], s63 offen offset:0 sc1 lds\n"
"v_mfma_f32_16x16x32_bf16 v[16:19], v[184:187], v[60:63], v[16:19]\n"
"ds_read_b128 v[52:55], v2 offset:21504\n"
"v_mfma_f32_16x16x32_bf16 v[16:19], v[72:75], v[56:59], v[16:19]\n"
"s_waitcnt lgkmcnt(4)\n"
"v_mfma_f32_16x16x32_bf16 v[16:19], a[48:51], v[32:35], v[16:19]\n"
"ds_read_b128 v[56:59], v2 offset:35840\n"
"v_mfma_f32_16x16x32_bf16 v[16:19], a[160:163], v[36:39], v[16:19]\n"
"buffer_load_dwordx4 v2, s[32:35], s63 offen offset:1024 sc1 lds\n"
"v_mfma_f32_16x16x32_bf16 v[20:23], a[164:167], v[36:39], v[20:23]\n"
"ds_read_b128 v[60:63], v2 offset:50176\n"
"v_mfma_f32_16x16x32_bf16 v[20:23], a[52:55], v[32:35], v[20:23]\n"
"s_waitcnt lgkmcnt(4)\n"
"v_mfma_f32_16x16x32_bf16 v[20:23], v[84:87], v[40:43], v[20:23]\n"
"ds_read_b128 v[32:35], v2 offset:8192\n"
"v_mfma_f32_16x16x32_bf16 v[20:23], v[196:199], v[44:47], v[20:23]\n"
"buffer_load_dwordx4 v2, s[32:35], s63 offen offset:2048 sc1 lds\n"
"v_mfma_f32_16x16x32_bf16 v[16:19], v[192:195], v[44:47], v[16:19]\n"
"ds_read_b128 v[36:39], v2 offset:22528\n"
"v_mfma_f32_16x16x32_bf16 v[16:19], v[80:83], v[40:43], v[16:19]\n"
"s_waitcnt lgkmcnt(4)\n"
"v_mfma_f32_16x16x32_bf16 v[16:19], a[56:59], v[48:51], v[16:19]\n"
"ds_read_b128 v[40:43], v2 offset:36864\n"
"v_mfma_f32_16x16x32_bf16 v[16:19], a[168:171], v[52:55], v[16:19]\n"
"buffer_load_dwordx4 v2, s[32:35], s63 offen offset:3072 sc1 lds\n"
"v_mfma_f32_16x16x32_bf16 v[20:23], a[172:175], v[52:55], v[20:23]\n"
"ds_read_b128 v[44:47], v2 offset:51200\n"
"v_mfma_f32_16x16x32_bf16 v[20:23], a[60:63], v[48:51], v[20:23]\n"
"s_waitcnt lgkmcnt(4)\n"
"v_mfma_f32_16x16x32_bf16 v[20:23], v[92:95], v[56:59], v[20:23]\n"
"ds_read_b128 v[48:51], v2 offset:9216\n"
"v_mfma_f32_16x16x32_bf16 v[20:23], v[204:207], v[60:63], v[20:23]\n"
"s_add_u32 s63, s63, 4096\n"
"s_add_u32 m0, s64, 65536\n"
"v_mfma_f32_16x16x32_bf16 v[16:19], v[200:203], v[60:63], v[16:19]\n"
"ds_read_b128 v[52:55], v2 offset:23552\n"
"v_mfma_f32_16x16x32_bf16 v[16:19], v[88:91], v[56:59], v[16:19]\n"
"s_waitcnt lgkmcnt(4)\n"
"v_mfma_f32_16x16x32_bf16 v[16:19], a[64:67], v[32:35], v[16:19]\n"
"ds_read_b128 v[56:59], v2 offset:37888\n"
"v_mfma_f32_16x16x32_bf16 v[16:19], a[176:179], v[36:39], v[16:19]\n"
"buffer_load_dwordx4 v2, s[32:35], s63 offen offset:0 sc1 lds\n"
"v_mfma_f32_16x16x32_bf16 v[20:23], a[180:183], v[36:39], v[20:23]\n"
"ds_read_b128 v[60:63], v2 offset:52224\n"
"v_mfma_f32_16x16x32_bf16 v[20:23], a[68:71], v[32:35], v[20:23]\n"
"s_waitcnt lgkmcnt(4)\n"
"v_mfma_f32_16x16x32_bf16 v[20:23], v[100:103], v[40:43], v[20:23]\n"
"ds_read_b128 v[32:35], v2 offset:10240\n"
"v_mfma_f32_16x16x32_bf16 v[20:23], v[212:215], v[44:47], v[20:23]\n"
"buffer_load_dwordx4 v2, s[32:35], s63 offen offset:1024 sc1 lds\n"
"v_mfma_f32_16x16x32_bf16 v[16:19], v[208:211], v[44:47], v[16:19]\n"
"ds_read_b128 v[36:39], v2 offset:24576\n"
"v_mfma_f32_16x16x32_bf16 v[16:19], v[96:99], v[40:43], v[16:19]\n"
"s_waitcnt lgkmcnt(4)\n"
"v_mfma_f32_16x16x32_bf16 v[16:19], a[72:75], v[48:51], v[16:19]\n"
"ds_read_b128 v[40:43], v2 offset:38912\n"
"v_mfma_f32_16x16x32_bf16 v[16:19], a[184:187], v[52:55], v[16:19]\n"
"buffer_load_dwordx4 v2, s[32:35], s63 offen offset:2048 sc1 lds\n"
"v_mfma_f32_16x16x32_bf16 v[20:23], a[188:191], v[52:55], v[20:23]\n"
"ds_read_b128 v[44:47], v2 offset:53248\n"
"v_mfma_f32_16x16x32_bf16 v[20:23], a[76:79], v[48:51], v[20:23]\n"
"s_waitcnt lgkmcnt(4)\n"
"v_mfma_f32_16x16x32_bf16 v[20:23], v[108:111], v[56:59], v[20:23]\n"
"ds_read_b128 v[48:51], v2 offset:11264\n"
"v_mfma_f32_16x16x32_bf16 v[20:23], v[220:223], v[60:63], v[20:23]\n"
"v_mfma_f32_16x16x32_bf16 v[16:19], v[216:219], v[60:63], v[16:19]\n"
"ds_read_b128 v[52:55], v2 offset:25600\n"
"v_mfma_f32_16x16x32_bf16 v[16:19], v[104:107], v[56:59], v[16:19]\n"
"s_waitcnt lgkmcnt(4)\n"
"v_mfma_f32_16x16x32_bf16 v[16:19], a[80:83], v[32:35], v[16:19]\n"
"ds_read_b128 v[56:59], v2 offset:39936\n"
"v_mfma_f32_16x16x32_bf16 v[16:19], a[192:195], v[36:39], v[16:19]\n"
"buffer_load_dwordx4 v2, s[32:35], s63 offen offset:3072 sc1 lds\n"
"s_add_u32 s63, s63, 4096\n"
"v_mfma_f32_16x16x32_bf16 v[20:23], a[196:199], v[36:39], v[20:23]\n"
"ds_read_b128 v[60:63], v2 offset:54272\n"
"s_add_u32 m0, s64, 69632\n"
"v_mfma_f32_16x16x32_bf16 v[20:23], a[84:87], v[32:35], v[20:23]\n"
"s_waitcnt lgkmcnt(4)\n"
"v_mfma_f32_16x16x32_bf16 v[20:23], v[116:119], v[40:43], v[20:23]\n"
"ds_read_b128 v[32:35], v2 offset:12288\n"
"v_mfma_f32_16x16x32_bf16 v[20:23], v[228:231], v[44:47], v[20:23]\n"
"buffer_load_dwordx4 v2, s[32:35], s63 offen offset:0 sc1 lds\n"
"v_mfma_f32_16x16x32_bf16 v[16:19], v[224:227], v[44:47], v[16:19]\n"
"ds_read_b128 v[36:39], v2 offset:26624\n"
"v_mfma_f32_16x16x32_bf16 v[16:19], v[112:115], v[40:43], v[16:19]\n"
"s_waitcnt lgkmcnt(4)\n"
"v_mfma_f32_16x16x32_bf16 v[16:19], a[88:91], v[48:51], v[16:19]\n"
"ds_read_b128 v[40:43], v2 offset:40960\n"
"v_mfma_f32_16x16x32_bf16 v[16:19], a[200:203], v[52:55], v[16:19]\n"
"buffer_load_dwordx4 v2, s[32:35], s63 offen offset:1024 sc1 lds\n"
"s_add_u32 s63, s63, 1175552\n"
"v_mfma_f32_16x16x32_bf16 v[20:23], a[204:207], v[52:55], v[20:23]\n"
"ds_read_b128 v[44:47], v2 offset:55296\n"
"v_mfma_f32_16x16x32_bf16 v[20:23], a[92:95], v[48:51], v[20:23]\n"
"s_waitcnt lgkmcnt(4)\n"
"v_mfma_f32_16x16x32_bf16 v[20:23], v[124:127], v[56:59], v[20:23]\n"
"ds_read_b128 v[48:51], v2 offset:13312\n"
"s_waitcnt vmcnt(10)\n"
"v_mfma_f32_16x16x32_bf16 v[20:23], v[236:239], v[60:63], v[20:23]\n"
"v_lshlrev_b32 v24, 16, v12\n"
"v_and_b32 v25, 0xffff0000, v12\n"
"v_mfma_f32_16x16x32_bf16 v[16:19], v[232:235], v[60:63], v[16:19]\n"
"ds_read_b128 v[52:55], v2 offset:27648\n"
"v_lshlrev_b32 v26, 16, v13\n"
"v_mfma_f32_16x16x32_bf16 v[16:19], v[120:123], v[56:59], v[16:19]\n"
"v_and_b32 v27, 0xffff0000, v13\n"
"s_waitcnt lgkmcnt(4)\n"
"v_mfma_f32_16x16x32_bf16 v[16:19], a[96:99], v[32:35], v[16:19]\n"
"s_barrier\n"
"v_mfma_f32_16x16x32_bf16 v[16:19], a[208:211], v[36:39], v[16:19]\n"
"ds_read_b128 v[56:59], v2 offset:41984\n"
"v_lshlrev_b32 v28, 16, v14\n"
"v_mfma_f32_16x16x32_bf16 v[20:23], a[212:215], v[36:39], v[20:23]\n"
"ds_read_b128 v[60:63], v2 offset:56320\n"
"v_and_b32 v29, 0xffff0000, v14\n"
"v_mfma_f32_16x16x32_bf16 v[20:23], a[100:103], v[32:35], v[20:23]\n"
"v_lshlrev_b32 v30, 16, v15\n"
"s_waitcnt lgkmcnt(4)\n"
"v_mfma_f32_16x16x32_bf16 v[20:23], v[132:135], v[40:43], v[20:23]\n"
"ds_read_b128 v[32:35], v4 offset:0\n"
"v_and_b32 v31, 0xffff0000, v15\n"
"v_mfma_f32_16x16x32_bf16 v[20:23], v[244:247], v[44:47], v[20:23]\n"
"v_mfma_f32_16x16x32_bf16 v[16:19], v[240:243], v[44:47], v[16:19]\n"
"ds_read_b128 v[36:39], v4 offset:14336\n"
"v_mfma_f32_16x16x32_bf16 v[16:19], v[128:131], v[40:43], v[16:19]\n"
"s_waitcnt lgkmcnt(4)\n"
"v_mfma_f32_16x16x32_bf16 v[16:19], a[104:107], v[48:51], v[16:19]\n"
"ds_read_b128 v[40:43], v4 offset:28672\n"
"v_mfma_f32_16x16x32_bf16 v[16:19], a[216:219], v[52:55], v[16:19]\n"
"v_mfma_f32_16x16x32_bf16 v[20:23], a[220:223], v[52:55], v[20:23]\n"
"ds_read_b128 v[44:47], v4 offset:43008\n"
"v_mfma_f32_16x16x32_bf16 v[20:23], a[108:111], v[48:51], v[20:23]\n"
"s_waitcnt lgkmcnt(4)\n"
"v_mfma_f32_16x16x32_bf16 v[20:23], v[140:143], v[56:59], v[20:23]\n"
"ds_read_b128 v[48:51], v4 offset:1024\n"
"v_mfma_f32_16x16x32_bf16 v[20:23], v[252:255], v[60:63], v[20:23]\n"
"v_mfma_f32_16x16x32_bf16 v[16:19], v[248:251], v[60:63], v[16:19]\n"
"ds_read_b128 v[52:55], v4 offset:15360\n"
"v_mfma_f32_16x16x32_bf16 v[16:19], v[136:139], v[56:59], v[16:19]\n"
"s_branch .L_dml_3\n"
".L_dmt_3:\n"
"s_nop 1\n"
"s_nop 0\n"
"v_cvt_pk_bf16_f32 v15, v30, v31\n"
"v_cvt_pk_bf16_f32 v14, v28, v29\n"
"s_nop 1\n"
"v_cvt_pk_bf16_f32 v13, v26, v27\n"
"v_cvt_pk_bf16_f32 v12, v24, v25\n"
"ds_write_b128 v5, v[12:15]\n"
"ds_read2st64_b32 v[12:13], v3 offset0:0 offset1:1\n"
"ds_read2st64_b32 v[14:15], v3 offset0:2 offset1:3\n"
"s_waitcnt lgkmcnt(0)\n"
"buffer_atomic_pk_add_bf16 v12, v1, s[44:47], s65 offen offset:0 sc1\n"
"buffer_atomic_pk_add_bf16 v13, v1, s[44:47], s65 offen offset:2048 sc1\n"
"s_add_u32 s65, s65, 16384\n"
"buffer_atomic_pk_add_bf16 v14, v1, s[44:47], s65 offen offset:0 sc1\n"
"buffer_atomic_pk_add_bf16 v15, v1, s[44:47], s65 offen offset:2048 sc1\n"
"s_add_u32 s65, s65, 16384\n"
"s_branch .L_end\n"
".L_dh:\n"
"v_mov_b32 v12, 0\n"
"v_mov_b32 v13, 0\n"
"v_mov_b32 v14, 0\n"
"v_mov_b32 v15, 0\n"
"v_lshrrev_b32 v10, 4, v1\n"
"v_and_b32 v11, 0xf0, v2\n"
"v_lshl_add_u32 v10, v10, 11, v11\n"
"s_lshl2_add_u32 s48, s28, s26\n"
"s_lshl_b32 s48, s48, 10\n"
"s_add_u32 s48, s48, s27\n"
"s_lshl_b32 s48, s48, 8\n"
"s_mov_b32 s49, 31\n"
".L_dz:\n"
"buffer_store_dwordx4 v[12:15], v10, s[44:47], s48 offen sc1\n"
"s_add_u32 s48, s48, 8192\n"
"s_sub_u32 s49, s49, 1\n"
"s_cbranch_scc0 .L_dz\n"
"v_lshrrev_b32 v10, 4, v1\n"
"v_and_b32 v1, 15, v1\n"
"s_lshl_b32 s48, s50, 6\n"
"v_lshl_add_u32 v1, v1, 2, s48\n"
"v_lshl_add_u32 v1, v10, 12, v1\n"
"s_mov_b32 s71, s65\n"
"s_add_u32 s69, s69, 38010880\n"
"s_add_u32 s68, s70, 2236416\n"
"s_add_u32 s70, s70, 42622976\n"
"s_lshl_b32 s65, s28, 20\n"
"s_mov_b32 s48, 20000\n"
"s_branch .L_rfda\n"
".L_rlda:\n"
"s_sleep 8\n"
".L_rfda:\n"
"buffer_load_dwordx2 v[6:7], v0, s[32:35], s66 offen sc1\n"
"s_sub_u32 s48, s48, 1\n"
"s_cbranch_scc1 .L_end\n"
"s_waitcnt vmcnt(0)\n"
"v_cmp_ne_u64 vcc, v[8:9], v[6:7]\n"
"s_cbranch_vccnz .L_rlda\n"
"s_add_u32 s66, s66, 1187840\n"
"s_add_u32 m0, s64, 0\n"
"s_nop 0\n"
"buffer_load_dwordx4 v2, s[32:35], s63 offen offset:0 sc1 lds\n"
"buffer_load_dwordx4 v2, s[32:35], s63 offen offset:1024 sc1 lds\n"
"buffer_load_dwordx4 v2, s[32:35], s63 offen offset:2048 sc1 lds\n"
"buffer_load_dwordx4 v2, s[32:35], s63 offen offset:3072 sc1 lds\n"
"s_add_u32 s63, s63, 4096\n"
"s_add_u32 m0, s64, 4096\n"
"s_nop 0\n"
"buffer_load_dwordx4 v2, s[32:35], s63 offen offset:0 sc1 lds\n"
"buffer_load_dwordx4 v2, s[32:35], s63 offen offset:1024 sc1 lds\n"
"buffer_load_dwordx4 v2, s[32:35], s63 offen offset:2048 sc1 lds\n"
"buffer_load_dwordx4 v2, s[32:35], s63 offen offset:3072 sc1 lds\n"
"s_add_u32 s63, s63, 4096\n"
"s_add_u32 m0, s64, 8192\n"
"s_nop 0\n"
"buffer_load_dwordx4 v2, s[32:35], s63 offen offset:0 sc1 lds\n"
"buffer_load_dwordx4 v2, s[32:35], s63 offen offset:1024 sc1 lds\n"
"buffer_load_dwordx4 v2, s[32:35], s63 offen offset:2048 sc1 lds\n"
"buffer_load_dwordx4 v2, s[32:35], s63 offen offset:3072 sc1 lds\n"
"s_add_u32 s63, s63, 4096\n"
"s_add_u32 m0, s64, 12288\n"
"s_nop 0\n"
"buffer_load_dwordx4 v2, s[32:35], s63 offen offset:0 sc1 lds\n"
"buffer_load_dwordx4 v2, s[32:35], s63 offen offset:1024 sc1 lds\n"
"s_add_u32 s63, s63, 1175552\n"
"buffer_load_dwordx2 v[6:7], v0, s[32:35], s66 offen sc1\n"
"s_waitcnt vmcnt(0)\n"
"s_barrier\n"
"ds_read_b128 v[32:35], v2 offset:0\n"
"ds_read_b128 v[36:39], v2 offset:14336\n"
"ds_read_b128 v[40:43], v2 offset:28672\n"
"ds_read_b128 v[44:47], v2 offset:43008\n"
"ds_read_b128 v[48:51], v2 offset:1024\n"
"ds_read_b128 v[52:55], v2 offset:15360\n"
"s_cmp_eq_u32 s26, 1\n"
"s_cbranch_scc1 .L_dhv1\n"
"s_cmp_eq_u32 s26, 2\n"
"s_cbranch_scc1 .L_dhv2\n"
"s_cmp_eq_u32 s26, 3\n"
"s_cbranch_scc1 .L_dhv3\n"
".p2align 6\n"
".L_dhv0:\n"
".L_dal_0:\n"
"s_waitcnt lgkmcnt(4)\n"
"v_mfma_f32_16x16x32_bf16 v[16:19], a[0:3], v[32:35], 0\n"
"ds_read_b128 v[56:59], v2 offset:29696\n"
"ds_read_b128 v[12:15], v5 offset:8192\n"
"v_mfma_f32_16x16x32_bf16 v[16:19], a[112:115], v[36:39], v[16:19]\n"
"v_mfma_f32_16x16x32_bf16 v[20:23], a[116:119], v[36:39], 0\n"
"ds_read_b128 v[60:63], v2 offset:44032\n"
"v_mfma_f32_16x16x32_bf16 v[20:23], a[4:7], v[32:35], v[20:23]\n"
"s_waitcnt lgkmcnt(5)\n"
"v_mfma_f32_16x16x32_bf16 v[20:23], a[228:231], v[40:43], v[20:23]\n"
"ds_read_b128 v[32:35], v2 offset:2048\n"
"v_mfma_f32_16x16x32_bf16 v[20:23], v[148:151], v[44:47], v[20:23]\n"
"s_waitcnt lgkmcnt(2)\n"
"v_add_f32 v24, v24, v12\n"
"v_mfma_f32_16x16x32_bf16 v[16:19], v[144:147], v[44:47], v[16:19]\n"
"ds_read_b128 v[36:39], v2 offset:16384\n"
"v_add_f32 v25, v25, v13\n"
"v_mfma_f32_16x16x32_bf16 v[16:19], a[224:227], v[40:43], v[16:19]\n"
"v_add_f32 v26, v26, v14\n"
"v_mfma_f32_16x16x32_bf16 v[16:19], a[8:11], v[48:51], v[16:19]\n"
"ds_read_b128 v[40:43], v2 offset:30720\n"
"v_add_f32 v27, v27, v15\n"
"v_mfma_f32_16x16x32_bf16 v[16:19], a[120:123], v[52:55], v[16:19]\n"
"ds_read_b128 v[12:15], v5 offset:9216\n"
"v_mfma_f32_16x16x32_bf16 v[20:23], a[124:127], v[52:55], v[20:23]\n"
"ds_read_b128 v[44:47], v2 offset:45056\n"
"v_mfma_f32_16x16x32_bf16 v[20:23], a[12:15], v[48:51], v[20:23]\n"
"s_waitcnt lgkmcnt(5)\n"
"v_mfma_f32_16x16x32_bf16 v[20:23], a[236:239], v[56:59], v[20:23]\n"
"ds_read_b128 v[48:51], v2 offset:3072\n"
"v_mfma_f32_16x16x32_bf16 v[20:23], v[156:159], v[60:63], v[20:23]\n"
"v_mfma_f32_16x16x32_bf16 v[16:19], v[152:155], v[60:63], v[16:19]\n"
"ds_read_b128 v[52:55], v2 offset:17408\n"
"s_waitcnt lgkmcnt(3)\n"
"v_mfma_f32_16x16x32_bf16 v[16:19], a[232:235], v[56:59], v[16:19]\n"
"v_add_f32 v28, v28, v12\n"
"v_mfma_f32_16x16x32_bf16 v[16:19], a[16:19], v[32:35], v[16:19]\n"
"ds_read_b128 v[56:59], v2 offset:31744\n"
"v_add_f32 v29, v29, v13\n"
"v_mfma_f32_16x16x32_bf16 v[16:19], a[128:131], v[36:39], v[16:19]\n"
"v_add_f32 v30, v30, v14\n"
"v_add_f32 v31, v31, v15\n"
"v_mfma_f32_16x16x32_bf16 v[20:23], a[132:135], v[36:39], v[20:23]\n"
"ds_read_b128 v[60:63], v2 offset:46080\n"
"v_cvt_pk_bf16_f32 v12, v24, v25\n"
"v_mfma_f32_16x16x32_bf16 v[20:23], a[20:23], v[32:35], v[20:23]\n"
"s_waitcnt lgkmcnt(4)\n"
"s_waitcnt vmcnt(1)\n"
"s_barrier\n"
"v_mfma_f32_16x16x32_bf16 v[20:23], a[244:247], v[40:43], v[20:23]\n"
"ds_read_b128 v[32:35], v2 offset:4096\n"
"s_add_u32 m0, s64, 57344\n"
"v_mfma_f32_16x16x32_bf16 v[20:23], v[164:167], v[44:47], v[20:23]\n"
"buffer_load_dwordx4 v2, s[32:35], s63 offen offset:0 sc1 lds\n"
"v_cvt_pk_bf16_f32 v13, v26, v27\n"
"v_mfma_f32_16x16x32_bf16 v[16:19], v[160:163], v[44:47], v[16:19]\n"
"ds_read_b128 v[36:39], v2 offset:18432\n"
"v_cvt_pk_bf16_f32 v14, v28, v29\n"
"v_mfma_f32_16x16x32_bf16 v[16:19], a[240:243], v[40:43], v[16:19]\n"
"v_cvt_pk_bf16_f32 v15, v30, v31\n"
"s_waitcnt lgkmcnt(4)\n"
"v_mfma_f32_16x16x32_bf16 v[16:19], a[24:27], v[48:51], v[16:19]\n"
"ds_read_b128 v[40:43], v2 offset:32768\n"
"v_mfma_f32_16x16x32_bf16 v[16:19], a[136:139], v[52:55], v[16:19]\n"
"buffer_store_dwordx4 v[12:15], v2, s[32:35], s68 offen sc1\n"
"v_add_f32 v10, v24, v25\n"
"v_mfma_f32_16x16x32_bf16 v[20:23], a[140:143], v[52:55], v[20:23]\n"
"ds_read_b128 v[44:47], v2 offset:47104\n"
"v_mul_f32 v11, v24, v24\n"
"v_mfma_f32_16x16x32_bf16 v[20:23], a[28:31], v[48:51], v[20:23]\n"
"v_fmac_f32 v11, v25, v25\n"
"s_waitcnt lgkmcnt(4)\n"
"v_mfma_f32_16x16x32_bf16 v[20:23], a[252:255], v[56:59], v[20:23]\n"
"ds_read_b128 v[48:51], v2 offset:5120\n"
"v_add_f32 v10, v10, v26\n"
"v_mfma_f32_16x16x32_bf16 v[20:23], v[172:175], v[60:63], v[20:23]\n"
"buffer_load_dwordx4 v2, s[32:35], s63 offen offset:1024 sc1 lds\n"
"v_fmac_f32 v11, v26, v26\n"
"v_mfma_f32_16x16x32_bf16 v[16:19], v[168:171], v[60:63], v[16:19]\n"
"ds_read_b128 v[52:55], v2 offset:19456\n"
"v_add_f32 v10, v10, v27\n"
"v_mfma_f32_16x16x32_bf16 v[16:19], a[248:251], v[56:59], v[16:19]\n"
"v_fmac_f32 v11, v27, v27\n"
"s_waitcnt lgkmcnt(4)\n"
"v_mfma_f32_16x16x32_bf16 v[16:19], a[32:35], v[32:35], v[16:19]\n"
"ds_read_b128 v[56:59], v2 offset:33792\n"
"v_add_f32 v10, v10, v28\n"
"v_mfma_f32_16x16x32_bf16 v[16:19], a[144:147], v[36:39], v[16:19]\n"
"buffer_load_dwordx4 v2, s[32:35], s63 offen offset:2048 sc1 lds\n"
"v_fmac_f32 v11, v28, v28\n"
"v_mfma_f32_16x16x32_bf16 v[20:23], a[148:151], v[36:39], v[20:23]\n"
"ds_read_b128 v[60:63], v2 offset:48128\n"
"v_add_f32 v10, v10, v29\n"
"v_mfma_f32_16x16x32_bf16 v[20:23], a[36:39], v[32:35], v[20:23]\n"
"v_fmac_f32 v11, v29, v29\n"
"s_waitcnt lgkmcnt(4)\n"
"v_mfma_f32_16x16x32_bf16 v[20:23], v[68:71], v[40:43], v[20:23]\n"
"ds_read_b128 v[32:35], v2 offset:6144\n"
"v_add_f32 v10, v10, v30\n"
"v_mfma_f32_16x16x32_bf16 v[20:23], v[180:183], v[44:47], v[20:23]\n"
"buffer_load_dwordx4 v2, s[32:35], s63 offen offset:3072 sc1 lds\n"
"s_add_u32 m0, s74, 0\n"
"v_mfma_f32_16x16x32_bf16 v[16:19], v[176:179], v[44:47], v[16:19]\n"
"ds_read_b128 v[36:39], v2 offset:20480\n"
"v_fmac_f32 v11, v30, v30\n"
"v_mfma_f32_16x16x32_bf16 v[16:19], v[64:67], v[40:43], v[16:19]\n"
"v_add_f32 v10, v10, v31\n"
"s_waitcnt lgkmcnt(4)\n"
"v_mfma_f32_16x16x32_bf16 v[16:19], a[40:43], v[48:51], v[16:19]\n"
"ds_read_b128 v[40:43], v2 offset:34816\n"
"v_fmac_f32 v11, v31, v31\n"
"v_mfma_f32_16x16x32_bf16 v[16:19], a[152:155], v[52:55], v[16:19]\n"
"buffer_load_dwordx4 v2, s[32:35], s71 offen offset:0 sc1 lds\n"
"v_permlane16_swap_b32 v10, v11\n"
"v_mfma_f32_16x16x32_bf16 v[20:23], a[156:159], v[52:55], v[20:23]\n"
"ds_read_b128 v[44:47], v2 offset:49152\n"
"v_add_f32 v10, v10, v11\n"
"v_mfma_f32_16x16x32_bf16 v[20:23], a[44:47], v[48:51], v[20:23]\n"
"s_waitcnt lgkmcnt(4)\n"
"v_mfma_f32_16x16x32_bf16 v[20:23], v[76:79], v[56:59], v[20:23]\n"
"ds_read_b128 v[48:51], v2 offset:7168\n"
"v_permlane32_swap_b32 v10, v11\n"
"v_mfma_f32_16x16x32_bf16 v[20:23], v[188:191], v[60:63], v[20:23]\n"
"buffer_load_dwordx4 v2, s[32:35], s71 offen offset:1024 sc1 lds\n"
"s_add_u32 s71, s71, 1187840\n"
"v_mfma_f32_16x16x32_bf16 v[16:19], v[184:187], v[60:63], v[16:19]\n"
"ds_read_b128 v[52:55], v2 offset:21504\n"
"s_add_u32 s63, s63, 4096\n"
"v_mfma_f32_16x16x32_bf16 v[16:19], v[72:75], v[56:59], v[16:19]\n"
"s_add_u32 m0, s64, 61440\n"
"s_waitcnt lgkmcnt(4)\n"
"v_mfma_f32_16x16x32_bf16 v[16:19], a[48:51], v[32:35], v[16:19]\n"
"ds_read_b128 v[56:59], v2 offset:35840\n"
"v_add_f32 v10, v10, v11\n"
"v_mfma_f32_16x16x32_bf16 v[16:19], a[160:163], v[36:39], v[16:19]\n"
"buffer_store_dword v10, v3, s[32:35], s68 offen offset:1024 sc1\n"
"s_mov_b32 s67, s68\n"
"v_mfma_f32_16x16x32_bf16 v[20:23], a[164:167], v[36:39], v[20:23]\n"
"ds_read_b128 v[60:63], v2 offset:50176\n"
"s_mov_b32 s68, s70\n"
"v_mfma_f32_16x16x32_bf16 v[20:23], a[52:55], v[32:35], v[20:23]\n"
"s_add_u32 s70, s70, 1187840\n"
"s_waitcnt lgkmcnt(4)\n"
"v_mfma_f32_16x16x32_bf16 v[20:23], v[84:87], v[40:43], v[20:23]\n"
"ds_read_b128 v[32:35], v2 offset:8192\n"
"v_mfma_f32_16x16x32_bf16 v[20:23], v[196:199], v[44:47], v[20:23]\n"
"buffer_load_dwordx4 v2, s[32:35], s63 offen offset:0 sc1 lds\n"
"v_mfma_f32_16x16x32_bf16 v[16:19], v[192:195], v[44:47], v[16:19]\n"
"ds_read_b128 v[36:39], v2 offset:22528\n"
"v_mfma_f32_16x16x32_bf16 v[16:19], v[80:83], v[40:43], v[16:19]\n"
"s_waitcnt lgkmcnt(4)\n"
"v_mfma_f32_16x16x32_bf16 v[16:19], a[56:59], v[48:51], v[16:19]\n"
"ds_read_b128 v[40:43], v2 offset:36864\n"
"v_mfma_f32_16x16x32_bf16 v[16:19], a[168:171], v[52:55], v[16:19]\n"
"buffer_load_dwordx4 v2, s[32:35], s63 offen offset:1024 sc1 lds\n"
"v_mfma_f32_16x16x32_bf16 v[20:23], a[172:175], v[52:55], v[20:23]\n"
"ds_read_b128 v[44:47], v2 offset:51200\n"
"v_mfma_f32_16x16x32_bf16 v[20:23], a[60:63], v[48:51], v[20:23]\n"
"s_waitcnt lgkmcnt(4)\n"
"v_mfma_f32_16x16x32_bf16 v[20:23], v[92:95], v[56:59], v[20:23]\n"
"ds_read_b128 v[48:51], v2 offset:9216\n"
"v_mfma_f32_16x16x32_bf16 v[20:23], v[204:207], v[60:63], v[20:23]\n"
"buffer_load_dwordx4 v2, s[32:35], s63 offen offset:2048 sc1 lds\n"
"v_mfma_f32_16x16x32_bf16 v[16:19], v[200:203], v[60:63], v[16:19]\n"
"ds_read_b128 v[52:55], v2 offset:23552\n"
"v_mfma_f32_16x16x32_bf16 v[16:19], v[88:91], v[56:59], v[16:19]\n"
"s_waitcnt lgkmcnt(4)\n"
"v_mfma_f32_16x16x32_bf16 v[16:19], a[64:67], v[32:35], v[16:19]\n"
"ds_read_b128 v[56:59], v2 offset:37888\n"
"v_mfma_f32_16x16x32_bf16 v[16:19], a[176:179], v[36:39], v[16:19]\n"
"buffer_load_dwordx4 v2, s[32:35], s63 offen offset:3072 sc1 lds\n"
"s_add_u32 s63, s63, 4096\n"
"v_mfma_f32_16x16x32_bf16 v[20:23], a[180:183], v[36:39], v[20:23]\n"
"ds_read_b128 v[60:63], v2 offset:52224\n"
"s_add_u32 m0, s64, 65536\n"
"v_mfma_f32_16x16x32_bf16 v[20:23], a[68:71], v[32:35], v[20:23]\n"
"s_waitcnt lgkmcnt(4)\n"
"v_mfma_f32_16x16x32_bf16 v[20:23], v[100:103], v[40:43], v[20:23]\n"
"ds_read_b128 v[32:35], v2 offset:10240\n"
"v_mfma_f32_16x16x32_bf16 v[20:23], v[212:215], v[44:47], v[20:23]\n"
"buffer_load_dwordx4 v2, s[32:35], s63 offen offset:0 sc1 lds\n"
"v_mfma_f32_16x16x32_bf16 v[16:19], v[208:211], v[44:47], v[16:19]\n"
"ds_read_b128 v[36:39], v2 offset:24576\n"
"v_mfma_f32_16x16x32_bf16 v[16:19], v[96:99], v[40:43], v[16:19]\n"
"s_waitcnt lgkmcnt(4)\n"
"v_mfma_f32_16x16x32_bf16 v[16:19], a[72:75], v[48:51], v[16:19]\n"
"ds_read_b128 v[40:43], v2 offset:38912\n"
"v_mfma_f32_16x16x32_bf16 v[16:19], a[184:187], v[52:55], v[16:19]\n"
"buffer_load_dwordx4 v2, s[32:35], s63 offen offset:1024 sc1 lds\n"
"v_mfma_f32_16x16x32_bf16 v[20:23], a[188:191], v[52:55], v[20:23]\n"
"ds_read_b128 v[44:47], v2 offset:53248\n"
"v_mfma_f32_16x16x32_bf16 v[20:23], a[76:79], v[48:51], v[20:23]\n"
"s_waitcnt lgkmcnt(4)\n"
"v_mfma_f32_16x16x32_bf16 v[20:23], v[108:111], v[56:59], v[20:23]\n"
"ds_read_b128 v[48:51], v2 offset:11264\n"
"v_mfma_f32_16x16x32_bf16 v[20:23], v[220:223], v[60:63], v[20:23]\n"
"buffer_load_dwordx4 v2, s[32:35], s63 offen offset:2048 sc1 lds\n"
"v_mfma_f32_16x16x32_bf16 v[16:19], v[216:219], v[60:63], v[16:19]\n"
"ds_read_b128 v[52:55], v2 offset:25600\n"
"v_mfma_f32_16x16x32_bf16 v[16:19], v[104:107], v[56:59], v[16:19]\n"
"s_waitcnt lgkmcnt(4)\n"
"v_mfma_f32_16x16x32_bf16 v[16:19], a[80:83], v[32:35], v[16:19]\n"
"ds_read_b128 v[56:59], v2 offset:39936\n"
"v_mfma_f32_16x16x32_bf16 v[16:19], a[192:195], v[36:39], v[16:19]\n"
"buffer_load_dwordx4 v2, s[32:35], s63 offen offset:3072 sc1 lds\n"
"s_add_u32 s63, s63, 4096\n"
"v_mfma_f32_16x16x32_bf16 v[20:23], a[196:199], v[36:39], v[20:23]\n"
"ds_read_b128 v[60:63], v2 offset:54272\n"
"s_add_u32 m0, s64, 69632\n"
"v_mfma_f32_16x16x32_bf16 v[20:23], a[84:87], v[32:35], v[20:23]\n"
"s_waitcnt lgkmcnt(4)\n"
"v_mfma_f32_16x16x32_bf16 v[20:23], v[116:119], v[40:43], v[20:23]\n"
"ds_read_b128 v[32:35], v2 offset:12288\n"
"v_mfma_f32_16x16x32_bf16 v[20:23], v[228:231], v[44:47], v[20:23]\n"
"buffer_load_dwordx4 v2, s[32:35], s63 offen offset:0 sc1 lds\n"
"v_mfma_f32_16x16x32_bf16 v[16:19], v[224:227], v[44:47], v[16:19]\n"
"ds_read_b128 v[36:39], v2 offset:26624\n"
"v_mfma_f32_16x16x32_bf16 v[16:19], v[112:115], v[40:43], v[16:19]\n"
"s_waitcnt lgkmcnt(4)\n"
"v_mfma_f32_16x16x32_bf16 v[16:19], a[88:91], v[48:51], v[16:19]\n"
"ds_read_b128 v[40:43], v2 offset:40960\n"
"v_mfma_f32_16x16x32_bf16 v[16:19], a[200:203], v[52:55], v[16:19]\n"
"buffer_load_dwordx4 v2, s[32:35], s63 offen offset:1024 sc1 lds\n"
"s_add_u32 s63, s63, 1175552\n"
"v_mfma_f32_16x16x32_bf16 v[20:23], a[204:207], v[52:55], v[20:23]\n"
"ds_read_b128 v[44:47], v2 offset:55296\n"
"v_mfma_f32_16x16x32_bf16 v[20:23], a[92:95], v[48:51], v[20:23]\n"
"s_waitcnt lgkmcnt(4)\n"
"v_mfma_f32_16x16x32_bf16 v[20:23], v[124:127], v[56:59], v[20:23]\n"
"ds_read_b128 v[48:51], v2 offset:13312\n"
"v_mfma_f32_16x16x32_bf16 v[20:23], v[236:239], v[60:63], v[20:23]\n"
"v_mfma_f32_16x16x32_bf16 v[16:19], v[232:235], v[60:63], v[16:19]\n"
"ds_read_b128 v[52:55], v2 offset:27648\n"
"v_mfma_f32_16x16x32_bf16 v[16:19], v[120:123], v[56:59], v[16:19]\n"
"s_waitcnt lgkmcnt(4)\n"
"v_mfma_f32_16x16x32_bf16 v[16:19], a[96:99], v[32:35], v[16:19]\n"
"ds_read_b128 v[56:59], v2 offset:41984\n"
"v_mfma_f32_16x16x32_bf16 v[16:19], a[208:211], v[36:39], v[16:19]\n"
"v_mfma_f32_16x16x32_bf16 v[20:23], a[212:215], v[36:39], v[20:23]\n"
"ds_read_b128 v[60:63], v2 offset:56320\n"
"v_mfma_f32_16x16x32_bf16 v[20:23], a[100:103], v[32:35], v[20:23]\n"
"s_waitcnt lgkmcnt(4)\n"
"s_waitcnt vmcnt(13)\n"
"s_waitcnt vmcnt(10)\n"
"s_barrier\n"
"v_mfma_f32_16x16x32_bf16 v[20:23], v[132:135], v[40:43], v[20:23]\n"
"ds_read_b128 v[32:35], v4 offset:0\n"
"v_mfma_f32_16x16x32_bf16 v[20:23], v[244:247], v[44:47], v[20:23]\n"
"v_mfma_f32_16x16x32_bf16 v[16:19], v[240:243], v[44:47], v[16:19]\n"
"ds_read_b128 v[36:39], v4 offset:14336\n"
"v_mfma_f32_16x16x32_bf16 v[16:19], v[128:131], v[40:43], v[16:19]\n"
"s_waitcnt lgkmcnt(4)\n"
"v_mfma_f32_16x16x32_bf16 v[16:19], a[104:107], v[48:51], v[16:19]\n"
"ds_read_b128 v[40:43], v4 offset:28672\n"
"v_mfma_f32_16x16x32_bf16 v[16:19], a[216:219], v[52:55], v[16:19]\n"
"buffer_store_dwordx2 v[8:9], v2, s[92:95], s67 idxen sc1\n"
"v_mfma_f32_16x16x32_bf16 v[20:23], a[220:223], v[52:55], v[20:23]\n"
"ds_read_b128 v[44:47], v4 offset:43008\n"
"v_mfma_f32_16x16x32_bf16 v[20:23], a[108:111], v[48:51], v[20:23]\n"
"s_waitcnt lgkmcnt(4)\n"
"v_mfma_f32_16x16x32_bf16 v[20:23], v[140:143], v[56:59], v[20:23]\n"
"ds_read_b128 v[48:51], v4 offset:1024\n"
"v_mfma_f32_16x16x32_bf16 v[20:23], v[252:255], v[60:63], v[20:23]\n"
"v_mfma_f32_16x16x32_bf16 v[16:19], v[248:251], v[60:63], v[16:19]\n"
"ds_read_b128 v[52:55], v4 offset:15360\n"
"s_sub_u32 s62, s62, 1\n"
"v_mfma_f32_16x16x32_bf16 v[16:19], v[136:139], v[56:59], v[16:19]\n"
"s_cbranch_scc1 .L_dab_0\n"
"s_waitcnt lgkmcnt(4)\n"
"v_mfma_f32_16x16x32_bf16 v[24:27], a[0:3], v[32:35], 0\n"
"ds_read_b128 v[56:59], v4 offset:29696\n"
"ds_read_b128 v[12:15], v5 offset:0\n"
"v_mfma_f32_16x16x32_bf16 v[24:27], a[112:115], v[36:39], v[24:27]\n"
"v_mfma_f32_16x16x32_bf16 v[28:31], a[116:119], v[36:39], 0\n"
"ds_read_b128 v[60:63], v4 offset:44032\n"
"v_mfma_f32_16x16x32_bf16 v[28:31], a[4:7], v[32:35], v[28:31]\n"
"s_waitcnt lgkmcnt(5)\n"
"v_mfma_f32_16x16x32_bf16 v[28:31], a[228:231], v[40:43], v[28:31]\n"
"ds_read_b128 v[32:35], v4 offset:2048\n"
"v_mfma_f32_16x16x32_bf16 v[28:31], v[148:151], v[44:47], v[28:31]\n"
"s_waitcnt lgkmcnt(2)\n"
"v_add_f32 v16, v16, v12\n"
"v_mfma_f32_16x16x32_bf16 v[24:27], v[144:147], v[44:47], v[24:27]\n"
"ds_read_b128 v[36:39], v4 offset:16384\n"
"v_add_f32 v17, v17, v13\n"
"v_mfma_f32_16x16x32_bf16 v[24:27], a[224:227], v[40:43], v[24:27]\n"
"v_add_f32 v18, v18, v14\n"
"v_mfma_f32_16x16x32_bf16 v[24:27], a[8:11], v[48:51], v[24:27]\n"
"ds_read_b128 v[40:43], v4 offset:30720\n"
"v_add_f32 v19, v19, v15\n"
"v_mfma_f32_16x16x32_bf16 v[24:27], a[120:123], v[52:55], v[24:27]\n"
"ds_read_b128 v[12:15], v5 offset:1024\n"
"v_mfma_f32_16x16x32_bf16 v[28:31], a[124:127], v[52:55], v[28:31]\n"
"ds_read_b128 v[44:47], v4 offset:45056\n"
"v_mfma_f32_16x16x32_bf16 v[28:31], a[12:15], v[48:51], v[28:31]\n"
"s_waitcnt lgkmcnt(5)\n"
"v_mfma_f32_16x16x32_bf16 v[28:31], a[236:239], v[56:59], v[28:31]\n"
"ds_read_b128 v[48:51], v4 offset:3072\n"
"v_mfma_f32_16x16x32_bf16 v[28:31], v[156:159], v[60:63], v[28:31]\n"
"v_mfma_f32_16x16x32_bf16 v[24:27], v[152:155], v[60:63], v[24:27]\n"
"ds_read_b128 v[52:55], v4 offset:17408\n"
"s_waitcnt lgkmcnt(3)\n"
"v_mfma_f32_16x16x32_bf16 v[24:27], a[232:235], v[56:59], v[24:27]\n"
"v_add_f32 v20, v20, v12\n"
"v_mfma_f32_16x16x32_bf16 v[24:27], a[16:19], v[32:35], v[24:27]\n"
"ds_read_b128 v[56:59], v4 offset:31744\n"
"v_add_f32 v21, v21, v13\n"
"v_mfma_f32_16x16x32_bf16 v[24:27], a[128:131], v[36:39], v[24:27]\n"
"v_add_f32 v22, v22, v14\n"
"v_add_f32 v23, v23, v15\n"
"v_mfma_f32_16x16x32_bf16 v[28:31], a[132:135], v[36:39], v[28:31]\n"
"ds_read_b128 v[60:63], v4 offset:46080\n"
"v_cvt_pk_bf16_f32 v12, v16, v17\n"
"v_mfma_f32_16x16x32_bf16 v[28:31], a[20:23], v[32:35], v[28:31]\n"
"s_waitcnt lgkmcnt(4)\n"
"s_waitcnt vmcnt(1)\n"
"s_barrier\n"
"v_mfma_f32_16x16x32_bf16 v[28:31], a[244:247], v[40:43], v[28:31]\n"
"ds_read_b128 v[32:35], v4 offset:4096\n"
"s_add_u32 m0, s64, 0\n"
"v_mfma_f32_16x16x32_bf16 v[28:31], v[164:167], v[44:47], v[28:31]\n"
"buffer_load_dwordx4 v2, s[32:35], s63 offen offset:0 sc1 lds\n"
"v_cvt_pk_bf16_f32 v13, v18, v19\n"
"v_mfma_f32_16x16x32_bf16 v[24:27], v[160:163], v[44:47], v[24:27]\n"
"ds_read_b128 v[36:39], v4 offset:18432\n"
"v_cvt_pk_bf16_f32 v14, v20, v21\n"
"v_mfma_f32_16x16x32_bf16 v[24:27], a[240:243], v[40:43], v[24:27]\n"
"v_cvt_pk_bf16_f32 v15, v22, v23\n"
"s_waitcnt lgkmcnt(4)\n"
"v_mfma_f32_16x16x32_bf16 v[24:27], a[24:27], v[48:51], v[24:27]\n"
"ds_read_b128 v[40:43], v4 offset:32768\n"
"v_mfma_f32_16x16x32_bf16 v[24:27], a[136:139], v[52:55], v[24:27]\n"
"buffer_store_dwordx4 v[12:15], v2, s[32:35], s69 offen sc1\n"
"v_add_f32 v10, v16, v17\n"
"v_mfma_f32_16x16x32_bf16 v[28:31], a[140:143], v[52:55], v[28:31]\n"
"ds_read_b128 v[44:47], v4 offset:47104\n"
"v_mul_f32 v11, v16, v16\n"
"v_mfma_f32_16x16x32_bf16 v[28:31], a[28:31], v[48:51], v[28:31]\n"
"v_fmac_f32 v11, v17, v17\n"
"s_waitcnt lgkmcnt(4)\n"
"v_mfma_f32_16x16x32_bf16 v[28:31], a[252:255], v[56:59], v[28:31]\n"
"ds_read_b128 v[48:51], v4 offset:5120\n"
"v_add_f32 v10, v10, v18\n"
"v_mfma_f32_16x16x32_bf16 v[28:31], v[172:175], v[60:63], v[28:31]\n"
"buffer_load_dwordx4 v2, s[32:35], s63 offen offset:1024 sc1 lds\n"
"v_fmac_f32 v11, v18, v18\n"
"v_mfma_f32_16x16x32_bf16 v[24:27], v[168:171], v[60:63], v[24:27]\n"
"ds_read_b128 v[52:55], v4 offset:19456\n"
"v_add_f32 v10, v10, v19\n"
"v_mfma_f32_16x16x32_bf16 v[24:27], a[248:251], v[56:59], v[24:27]\n"
"v_fmac_f32 v11, v19, v19\n"
"s_waitcnt lgkmcnt(4)\n"
"v_mfma_f32_16x16x32_bf16 v[24:27], a[32:35], v[32:35], v[24:27]\n"
"ds_read_b128 v[56:59], v4 offset:33792\n"
"v_add_f32 v10, v10, v20\n"
"v_mfma_f32_16x16x32_bf16 v[24:27], a[144:147], v[36:39], v[24:27]\n"
"buffer_load_dwordx4 v2, s[32:35], s63 offen offset:2048 sc1 lds\n"
"v_fmac_f32 v11, v20, v20\n"
"v_mfma_f32_16x16x32_bf16 v[28:31], a[148:151], v[36:39], v[28:31]\n"
"ds_read_b128 v[60:63], v4 offset:48128\n"
"v_add_f32 v10, v10, v21\n"
"v_mfma_f32_16x16x32_bf16 v[28:31], a[36:39], v[32:35], v[28:31]\n"
"v_fmac_f32 v11, v21, v21\n"
"s_waitcnt lgkmcnt(4)\n"
"v_mfma_f32_16x16x32_bf16 v[28:31], v[68:71], v[40:43], v[28:31]\n"
"ds_read_b128 v[32:35], v4 offset:6144\n"
"v_add_f32 v10, v10, v22\n"
"v_mfma_f32_16x16x32_bf16 v[28:31], v[180:183], v[44:47], v[28:31]\n"
"buffer_load_dwordx4 v2, s[32:35], s63 offen offset:3072 sc1 lds\n"
"s_add_u32 m0, s74, 8192\n"
"v_mfma_f32_16x16x32_bf16 v[24:27], v[176:179], v[44:47], v[24:27]\n"
"ds_read_b128 v[36:39], v4 offset:20480\n"
"v_fmac_f32 v11, v22, v22\n"
"v_mfma_f32_16x16x32_bf16 v[24:27], v[64:67], v[40:43], v[24:27]\n"
"v_add_f32 v10, v10, v23\n"
"s_waitcnt lgkmcnt(4)\n"
"v_mfma_f32_16x16x32_bf16 v[24:27], a[40:43], v[48:51], v[24:27]\n"
"ds_read_b128 v[40:43], v4 offset:34816\n"
"v_fmac_f32 v11, v23, v23\n"
"v_mfma_f32_16x16x32_bf16 v[24:27], a[152:155], v[52:55], v[24:27]\n"
"buffer_load_dwordx4 v2, s[32:35], s71 offen offset:0 sc1 lds\n"
"v_permlane16_swap_b32 v10, v11\n"
"v_mfma_f32_16x16x32_bf16 v[28:31], a[156:159], v[52:55], v[28:31]\n"
"ds_read_b128 v[44:47], v4 offset:49152\n"
"v_add_f32 v10, v10, v11\n"
"v_mfma_f32_16x16x32_bf16 v[28:31], a[44:47], v[48:51], v[28:31]\n"
"s_waitcnt lgkmcnt(4)\n"
"v_mfma_f32_16x16x32_bf16 v[28:31], v[76:79], v[56:59], v[28:31]\n"
"ds_read_b128 v[48:51], v4 offset:7168\n"
"v_permlane32_swap_b32 v10, v11\n"
"v_mfma_f32_16x16x32_bf16 v[28:31], v[188:191], v[60:63], v[28:31]\n"
"buffer_load_dwordx4 v2, s[32:35], s71 offen offset:1024 sc1 lds\n"
"s_add_u32 s71, s71, 1187840\n"
"v_mfma_f32_16x16x32_bf16 v[24:27], v[184:187], v[60:63], v[24:27]\n"
"ds_read_b128 v[52:55], v4 offset:21504\n"
"s_add_u32 s63, s63, 4096\n"
"v_mfma_f32_16x16x32_bf16 v[24:27], v[72:75], v[56:59], v[24:27]\n"
"s_add_u32 m0, s64, 4096\n"
"s_waitcnt lgkmcnt(4)\n"
"v_mfma_f32_16x16x32_bf16 v[24:27], a[48:51], v[32:35], v[24:27]\n"
"ds_read_b128 v[56:59], v4 offset:35840\n"
"v_add_f32 v10, v10, v11\n"
"v_mfma_f32_16x16x32_bf16 v[24:27], a[160:163], v[36:39], v[24:27]\n"
"buffer_store_dword v10, v3, s[32:35], s69 offen offset:1024 sc1\n"
"s_mov_b32 s67, s69\n"
"v_mfma_f32_16x16x32_bf16 v[28:31], a[164:167], v[36:39], v[28:31]\n"
"ds_read_b128 v[60:63], v4 offset:50176\n"
"s_mov_b32 s69, s70\n"
"v_mfma_f32_16x16x32_bf16 v[28:31], a[52:55], v[32:35], v[28:31]\n"
"s_add_u32 s70, s70, 1187840\n"
"s_waitcnt lgkmcnt(4)\n"
"v_mfma_f32_16x16x32_bf16 v[28:31], v[84:87], v[40:43], v[28:31]\n"
"ds_read_b128 v[32:35], v4 offset:8192\n"
"v_mfma_f32_16x16x32_bf16 v[28:31], v[196:199], v[44:47], v[28:31]\n"
"buffer_load_dwordx4 v2, s[32:35], s63 offen offset:0 sc1 lds\n"
"v_mfma_f32_16x16x32_bf16 v[24:27], v[192:195], v[44:47], v[24:27]\n"
"ds_read_b128 v[36:39], v4 offset:22528\n"
"v_mfma_f32_16x16x32_bf16 v[24:27], v[80:83], v[40:43], v[24:27]\n"
"s_waitcnt lgkmcnt(4)\n"
"v_mfma_f32_16x16x32_bf16 v[24:27], a[56:59], v[48:51], v[24:27]\n"
"ds_read_b128 v[40:43], v4 offset:36864\n"
"v_mfma_f32_16x16x32_bf16 v[24:27], a[168:171], v[52:55], v[24:27]\n"
"buffer_load_dwordx4 v2, s[32:35], s63 offen offset:1024 sc1 lds\n"
"v_mfma_f32_16x16x32_bf16 v[28:31], a[172:175], v[52:55], v[28:31]\n"
"ds_read_b128 v[44:47], v4 offset:51200\n"
"v_mfma_f32_16x16x32_bf16 v[28:31], a[60:63], v[48:51], v[28:31]\n"
"s_waitcnt lgkmcnt(4)\n"
"v_mfma_f32_16x16x32_bf16 v[28:31], v[92:95], v[56:59], v[28:31]\n"
"ds_read_b128 v[48:51], v4 offset:9216\n"
"v_mfma_f32_16x16x32_bf16 v[28:31], v[204:207], v[60:63], v[28:31]\n"
"buffer_load_dwordx4 v2, s[32:35], s63 offen offset:2048 sc1 lds\n"
"v_mfma_f32_16x16x32_bf16 v[24:27], v[200:203], v[60:63], v[24:27]\n"
"ds_read_b128 v[52:55], v4 offset:23552\n"
"v_mfma_f32_16x16x32_bf16 v[24:27], v[88:91], v[56:59], v[24:27]\n"
"s_waitcnt lgkmcnt(4)\n"
"v_mfma_f32_16x16x32_bf16 v[24:27], a[64:67], v[32:35], v[24:27]\n"
"ds_read_b128 v[56:59], v4 offset:37888\n"
"v_mfma_f32_16x16x32_bf16 v[24:27], a[176:179], v[36:39], v[24:27]\n"
"buffer_load_dwordx4 v2, s[32:35], s63 offen offset:3072 sc1 lds\n"
"s_add_u32 s63, s63, 4096\n"
"v_mfma_f32_16x16x32_bf16 v[28:31], a[180:183], v[36:39], v[28:31]\n"
"ds_read_b128 v[60:63], v4 offset:52224\n"
"s_add_u32 m0, s64, 8192\n"
"v_mfma_f32_16x16x32_bf16 v[28:31], a[68:71], v[32:35], v[28:31]\n"
"s_waitcnt lgkmcnt(4)\n"
"v_mfma_f32_16x16x32_bf16 v[28:31], v[100:103], v[40:43], v[28:31]\n"
"ds_read_b128 v[32:35], v4 offset:10240\n"
"v_mfma_f32_16x16x32_bf16 v[28:31], v[212:215], v[44:47], v[28:31]\n"
"buffer_load_dwordx4 v2, s[32:35], s63 offen offset:0 sc1 lds\n"
"v_mfma_f32_16x16x32_bf16 v[24:27], v[208:211], v[44:47], v[24:27]\n"
"ds_read_b128 v[36:39], v4 offset:24576\n"
"v_mfma_f32_16x16x32_bf16 v[24:27], v[96:99], v[40:43], v[24:27]\n"
"s_waitcnt lgkmcnt(4)\n"
"v_mfma_f32_16x16x32_bf16 v[24:27], a[72:75], v[48:51], v[24:27]\n"
"ds_read_b128 v[40:43], v4 offset:38912\n"
"v_mfma_f32_16x16x32_bf16 v[24:27], a[184:187], v[52:55], v[24:27]\n"
"buffer_load_dwordx4 v2, s[32:35], s63 offen offset:1024 sc1 lds\n"
"v_mfma_f32_16x16x32_bf16 v[28:31], a[188:191], v[52:55], v[28:31]\n"
"ds_read_b128 v[44:47], v4 offset:53248\n"
"v_mfma_f32_16x16x32_bf16 v[28:31], a[76:79], v[48:51], v[28:31]\n"
"s_waitcnt lgkmcnt(4)\n"
"v_mfma_f32_16x16x32_bf16 v[28:31], v[108:111], v[56:59], v[28:31]\n"
"ds_read_b128 v[48:51], v4 offset:11264\n"
"v_mfma_f32_16x16x32_bf16 v[28:31], v[220:223], v[60:63], v[28:31]\n"
"buffer_load_dwordx4 v2, s[32:35], s63 offen offset:2048 sc1 lds\n"
"v_mfma_f32_16x16x32_bf16 v[24:27], v[216:219], v[60:63], v[24:27]\n"
"ds_read_b128 v[52:55], v4 offset:25600\n"
"v_mfma_f32_16x16x32_bf16 v[24:27], v[104:107], v[56:59], v[24:27]\n"
"s_waitcnt lgkmcnt(4)\n"
"v_mfma_f32_16x16x32_bf16 v[24:27], a[80:83], v[32:35], v[24:27]\n"
"ds_read_b128 v[56:59], v4 offset:39936\n"
"v_mfma_f32_16x16x32_bf16 v[24:27], a[192:195], v[36:39], v[24:27]\n"
"buffer_load_dwordx4 v2, s[32:35], s63 offen offset:3072 sc1 lds\n"
"s_add_u32 s63, s63, 4096\n"
"v_mfma_f32_16x16x32_bf16 v[28:31], a[196:199], v[36:39], v[28:31]\n"
"ds_read_b128 v[60:63], v4 offset:54272\n"
"s_add_u32 m0, s64, 12288\n"
"v_mfma_f32_16x16x32_bf16 v[28:31], a[84:87], v[32:35], v[28:31]\n"
"s_waitcnt lgkmcnt(4)\n"
"v_mfma_f32_16x16x32_bf16 v[28:31], v[116:119], v[40:43], v[28:31]\n"
"ds_read_b128 v[32:35], v4 offset:12288\n"
"v_mfma_f32_16x16x32_bf16 v[28:31], v[228:231], v[44:47], v[28:31]\n"
"buffer_load_dwordx4 v2, s[32:35], s63 offen offset:0 sc1 lds\n"
"v_mfma_f32_16x16x32_bf16 v[24:27], v[224:227], v[44:47], v[24:27]\n"
"ds_read_b128 v[36:39], v4 offset:26624\n"
"v_mfma_f32_16x16x32_bf16 v[24:27], v[112:115], v[40:43], v[24:27]\n"
"s_waitcnt lgkmcnt(4)\n"
"v_mfma_f32_16x16x32_bf16 v[24:27], a[88:91], v[48:51], v[24:27]\n"
"ds_read_b128 v[40:43], v4 offset:40960\n"
"v_mfma_f32_16x16x32_bf16 v[24:27], a[200:203], v[52:55], v[24:27]\n"
"buffer_load_dwordx4 v2, s[32:35], s63 offen offset:1024 sc1 lds\n"
"s_add_u32 s63, s63, 1175552\n"
"v_mfma_f32_16x16x32_bf16 v[28:31], a[204:207], v[52:55], v[28:31]\n"
"ds_read_b128 v[44:47], v4 offset:55296\n"
"v_mfma_f32_16x16x32_bf16 v[28:31], a[92:95], v[48:51], v[28:31]\n"
"s_waitcnt lgkmcnt(4)\n"
"v_mfma_f32_16x16x32_bf16 v[28:31], v[124:127], v[56:59], v[28:31]\n"
"ds_read_b128 v[48:51], v4 offset:13312\n"
"v_mfma_f32_16x16x32_bf16 v[28:31], v[236:239], v[60:63], v[28:31]\n"
"v_mfma_f32_16x16x32_bf16 v[24:27], v[232:235], v[60:63], v[24:27]\n"
"ds_read_b128 v[52:55], v4 offset:27648\n"
"v_mfma_f32_16x16x32_bf16 v[24:27], v[120:123], v[56:59], v[24:27]\n"
"s_waitcnt lgkmcnt(4)\n"
"v_mfma_f32_16x16x32_bf16 v[24:27], a[96:99], v[32:35], v[24:27]\n"
"ds_read_b128 v[56:59], v4 offset:41984\n"
"v_mfma_f32_16x16x32_bf16 v[24:27], a[208:211], v[36:39], v[24:27]\n"
"v_mfma_f32_16x16x32_bf16 v[28:31], a[212:215], v[36:39], v[28:31]\n"
"ds_read_b128 v[60:63], v4 offset:56320\n"
"v_mfma_f32_16x16x32_bf16 v[28:31], a[100:103], v[32:35], v[28:31]\n"
"s_waitcnt lgkmcnt(4)\n"
"s_waitcnt vmcnt(13)\n"
"s_waitcnt vmcnt(10)\n"
"s_barrier\n"
"v_mfma_f32_16x16x32_bf16 v[28:31], v[132:135], v[40:43], v[28:31]\n"
"ds_read_b128 v[32:35], v2 offset:0\n"
"v_mfma_f32_16x16x32_bf16 v[28:31], v[244:247], v[44:47], v[28:31]\n"
"v_mfma_f32_16x16x32_bf16 v[24:27], v[240:243], v[44:47], v[24:27]\n"
"ds_read_b128 v[36:39], v2 offset:14336\n"
"v_mfma_f32_16x16x32_bf16 v[24:27], v[128:131], v[40:43], v[24:27]\n"
"s_waitcnt lgkmcnt(4)\n"
"v_mfma_f32_16x16x32_bf16 v[24:27], a[104:107], v[48:51], v[24:27]\n"
"ds_read_b128 v[40:43], v2 offset:28672\n"
"v_mfma_f32_16x16x32_bf16 v[24:27], a[216:219], v[52:55], v[24:27]\n"
"buffer_store_dwordx2 v[8:9], v2, s[92:95], s67 idxen sc1\n"
"v_mfma_f32_16x16x32_bf16 v[28:31], a[220:223], v[52:55], v[28:31]\n"
"ds_read_b128 v[44:47], v2 offset:43008\n"
"v_mfma_f32_16x16x32_bf16 v[28:31], a[108:111], v[48:51], v[28:31]\n"
"s_waitcnt lgkmcnt(4)\n"
"v_mfma_f32_16x16x32_bf16 v[28:31], v[140:143], v[56:59], v[28:31]\n"
"ds_read_b128 v[48:51], v2 offset:1024\n"
"v_mfma_f32_16x16x32_bf16 v[28:31], v[252:255], v[60:63], v[28:31]\n"
"v_mfma_f32_16x16x32_bf16 v[24:27], v[248:251], v[60:63], v[24:27]\n"
"ds_read_b128 v[52:55], v2 offset:15360\n"
"s_cmp_eq_u32 s62, 0\n"
"v_mfma_f32_16x16x32_bf16 v[24:27], v[136:139], v[56:59], v[24:27]\n"
"s_cbranch_scc0 .L_dal_0\n"
"s_mov_b32 s48, 0x7fff0000\n"
"v_writelane_b32 v8, 0, 63\n"
"v_writelane_b32 v9, 0, 63\n"
"v_writelane_b32 v6, 0, 63\n"
"v_writelane_b32 v7, 0, 63\n"
"v_writelane_b32 v0, s48, 63\n"
"s_branch .L_dal_0\n"
".L_dab_0:\n"
"s_mov_b32 s62, 15\n"
"s_add_u32 s74, s74, 8192\n"
"v_lshrrev_b32 v5, 8, v2\n"
"v_lshl_add_u32 v5, v5, 4, s74\n"
"v_and_b32 v3, 128, v2\n"
"v_lshl_add_u32 v5, v3, 2, v5\n"
"v_and_b32 v3, 16, v2\n"
"v_lshl_add_u32 v5, v3, 4, v5\n"
"v_and_b32 v3, 96, v2\n"
"v_lshl_add_u32 v5, v3, 1, v5\n"
"v_lshrrev_b32 v3, 2, v2\n"
"v_add_u32 v3, s74, v3\n"
".p2align 6\n"
".L_dbl_0:\n"
"s_waitcnt lgkmcnt(4)\n"
"v_mfma_f32_16x16x32_bf16 v[24:27], a[0:3], v[32:35], 0\n"
"ds_read_b128 v[56:59], v4 offset:29696\n"
"v_mfma_f32_16x16x32_bf16 v[24:27], a[112:115], v[36:39], v[24:27]\n"
"v_mfma_f32_16x16x32_bf16 v[28:31], a[116:119], v[36:39], 0\n"
"ds_read_b128 v[60:63], v4 offset:44032\n"
"v_mfma_f32_16x16x32_bf16 v[28:31], a[4:7], v[32:35], v[28:31]\n"
"v_cvt_pk_bf16_f32 v15, v22, v23\n"
"s_waitcnt lgkmcnt(4)\n"
"v_mfma_f32_16x16x32_bf16 v[28:31], a[228:231], v[40:43], v[28:31]\n"
"ds_read_b128 v[32:35], v4 offset:2048\n"
"v_cvt_pk_bf16_f32 v14, v20, v21\n"
"v_mfma_f32_16x16x32_bf16 v[28:31], v[148:151], v[44:47], v[28:31]\n"
"v_cvt_pk_bf16_f32 v13, v18, v19\n"
"v_cvt_pk_bf16_f32 v12, v16, v17\n"
"v_mfma_f32_16x16x32_bf16 v[24:27], v[144:147], v[44:47], v[24:27]\n"
"ds_read_b128 v[36:39], v4 offset:16384\n"
"ds_write_b128 v5, v[12:15]\n"
"v_mfma_f32_16x16x32_bf16 v[24:27], a[224:227], v[40:43], v[24:27]\n"
"ds_read2st64_b32 v[12:13], v3 offset0:0 offset1:1\n"
"s_waitcnt lgkmcnt(6)\n"
"v_mfma_f32_16x16x32_bf16 v[24:27], a[8:11], v[48:51], v[24:27]\n"
"ds_read_b128 v[40:43], v4 offset:30720\n"
"ds_read2st64_b32 v[14:15], v3 offset0:2 offset1:3\n"
"v_mfma_f32_16x16x32_bf16 v[24:27], a[120:123], v[52:55], v[24:27]\n"
"v_mfma_f32_16x16x32_bf16 v[28:31], a[124:127], v[52:55], v[28:31]\n"
"ds_read_b128 v[44:47], v4 offset:45056\n"
"v_mfma_f32_16x16x32_bf16 v[28:31], a[12:15], v[48:51], v[28:31]\n"
"s_waitcnt lgkmcnt(7)\n"
"v_mfma_f32_16x16x32_bf16 v[28:31], a[236:239], v[56:59], v[28:31]\n"
"ds_read_b128 v[48:51], v4 offset:3072\n"
"v_mfma_f32_16x16x32_bf16 v[28:31], v[156:159], v[60:63], v[28:31]\n"
"s_waitcnt lgkmcnt(2)\n"
"buffer_atomic_pk_add_bf16 v12, v1, s[44:47], s65 offen offset:0 sc1\n"
"v_mfma_f32_16x16x32_bf16 v[24:27], v[152:155], v[60:63], v[24:27]\n"
"ds_read_b128 v[52:55], v4 offset:17408\n"
"v_mfma_f32_16x16x32_bf16 v[24:27], a[232:235], v[56:59], v[24:27]\n"
"buffer_atomic_pk_add_bf16 v13, v1, s[44:47], s65 offen offset:2048 sc1\n"
"v_mfma_f32_16x16x32_bf16 v[24:27], a[16:19], v[32:35], v[24:27]\n"
"ds_read_b128 v[56:59], v4 offset:31744\n"
"s_add_u32 s65, s65, 16384\n"
"v_mfma_f32_16x16x32_bf16 v[24:27], a[128:131], v[36:39], v[24:27]\n"
"buffer_atomic_pk_add_bf16 v14, v1, s[44:47], s65 offen offset:0 sc1\n"
"v_mfma_f32_16x16x32_bf16 v[28:31], a[132:135], v[36:39], v[28:31]\n"
"ds_read_b128 v[60:63], v4 offset:46080\n"
"v_mfma_f32_16x16x32_bf16 v[28:31], a[20:23], v[32:35], v[28:31]\n"
"s_waitcnt lgkmcnt(4)\n"
"s_waitcnt vmcnt(3)\n"
"s_barrier\n"
"v_mfma_f32_16x16x32_bf16 v[28:31], a[244:247], v[40:43], v[28:31]\n"
"ds_read_b128 v[32:35], v4 offset:4096\n"
"s_add_u32 m0, s64, 0\n"
"v_mfma_f32_16x16x32_bf16 v[28:31], v[164:167], v[44:47], v[28:31]\n"
"buffer_atomic_pk_add_bf16 v15, v1, s[44:47], s65 offen offset:2048 sc1\n"
"s_add_u32 s65, s65, 16384\n"
"v_mfma_f32_16x16x32_bf16 v[24:27], v[160:163], v[44:47], v[24:27]\n"
"ds_read_b128 v[36:39], v4 offset:18432\n"
"v_mfma_f32_16x16x32_bf16 v[24:27], a[240:243], v[40:43], v[24:27]\n"
"s_waitcnt lgkmcnt(4)\n"
"v_mfma_f32_16x16x32_bf16 v[24:27], a[24:27], v[48:51], v[24:27]\n"
"ds_read_b128 v[40:43], v4 offset:32768\n"
"v_mfma_f32_16x16x32_bf16 v[24:27], a[136:139], v[52:55], v[24:27]\n"
"buffer_load_dwordx4 v2, s[32:35], s63 offen offset:0 sc1 lds\n"
"v_mfma_f32_16x16x32_bf16 v[28:31], a[140:143], v[52:55], v[28:31]\n"
"ds_read_b128 v[44:47], v4 offset:47104\n"
"v_mfma_f32_16x16x32_bf16 v[28:31], a[28:31], v[48:51], v[28:31]\n"
"s_waitcnt lgkmcnt(4)\n"
"v_mfma_f32_16x16x32_bf16 v[28:31], a[252:255], v[56:59], v[28:31]\n"
"ds_read_b128 v[48:51], v4 offset:5120\n"
"v_mfma_f32_16x16x32_bf16 v[28:31], v[172:175], v[60:63], v[28:31]\n"
"buffer_load_dwordx4 v2, s[32:35], s63 offen offset:1024 sc1 lds\n"
"v_mfma_f32_16x16x32_bf16 v[24:27], v[168:171], v[60:63], v[24:27]\n"
"ds_read_b128 v[52:55], v4 offset:19456\n"
"v_mfma_f32_16x16x32_bf16 v[24:27], a[248:251], v[56:59], v[24:27]\n"
"s_waitcnt lgkmcnt(4)\n"
"v_mfma_f32_16x16x32_bf16 v[24:27], a[32:35], v[32:35], v[24:27]\n"
"ds_read_b128 v[56:59], v4 offset:33792\n"
"v_mfma_f32_16x16x32_bf16 v[24:27], a[144:147], v[36:39], v[24:27]\n"
"buffer_load_dwordx4 v2, s[32:35], s63 offen offset:2048 sc1 lds\n"
"v_mfma_f32_16x16x32_bf16 v[28:31], a[148:151], v[36:39], v[28:31]\n"
"ds_read_b128 v[60:63], v4 offset:48128\n"
"v_mfma_f32_16x16x32_bf16 v[28:31], a[36:39], v[32:35], v[28:31]\n"
"s_waitcnt lgkmcnt(4)\n"
"v_mfma_f32_16x16x32_bf16 v[28:31], v[68:71], v[40:43], v[28:31]\n"
"ds_read_b128 v[32:35], v4 offset:6144\n"
"v_mfma_f32_16x16x32_bf16 v[28:31], v[180:183], v[44:47], v[28:31]\n"
"buffer_load_dwordx4 v2, s[32:35], s63 offen offset:3072 sc1 lds\n"
"v_mfma_f32_16x16x32_bf16 v[24:27], v[176:179], v[44:47], v[24:27]\n"
"ds_read_b128 v[36:39], v4 offset:20480\n"
"v_mfma_f32_16x16x32_bf16 v[24:27], v[64:67], v[40:43], v[24:27]\n"
"s_waitcnt lgkmcnt(4)\n"
"v_mfma_f32_16x16x32_bf16 v[24:27], a[40:43], v[48:51], v[24:27]\n"
"ds_read_b128 v[40:43], v4 offset:34816\n"
"s_add_u32 s63, s63, 4096\n"
"v_mfma_f32_16x16x32_bf16 v[24:27], a[152:155], v[52:55], v[24:27]\n"
"s_add_u32 m0, s64, 4096\n"
"v_mfma_f32_16x16x32_bf16 v[28:31], a[156:159], v[52:55], v[28:31]\n"
"ds_read_b128 v[44:47], v4 offset:49152\n"
"v_mfma_f32_16x16x32_bf16 v[28:31], a[44:47], v[48:51], v[28:31]\n"
"s_waitcnt lgkmcnt(4)\n"
"v_mfma_f32_16x16x32_bf16 v[28:31], v[76:79], v[56:59], v[28:31]\n"
"ds_read_b128 v[48:51], v4 offset:7168\n"
"v_mfma_f32_16x16x32_bf16 v[28:31], v[188:191], v[60:63], v[28:31]\n"
"buffer_load_dwordx4 v2, s[32:35], s63 offen offset:0 sc1 lds\n"
"v_mfma_f32_16x16x32_bf16 v[24:27], v[184:187], v[60:63], v[24:27]\n"
"ds_read_b128 v[52:55], v4 offset:21504\n"
"v_mfma_f32_16x16x32_bf16 v[24:27], v[72:75], v[56:59], v[24:27]\n"
"s_waitcnt lgkmcnt(4)\n"
"v_mfma_f32_16x16x32_bf16 v[24:27], a[48:51], v[32:35], v[24:27]\n"
"ds_read_b128 v[56:59], v4 offset:35840\n"
"v_mfma_f32_16x16x32_bf16 v[24:27], a[160:163], v[36:39], v[24:27]\n"
"buffer_load_dwordx4 v2, s[32:35], s63 offen offset:1024 sc1 lds\n"
"v_mfma_f32_16x16x32_bf16 v[28:31], a[164:167], v[36:39], v[28:31]\n"
"ds_read_b128 v[60:63], v4 offset:50176\n"
"v_mfma_f32_16x16x32_bf16 v[28:31], a[52:55], v[32:35], v[28:31]\n"
"s_waitcnt lgkmcnt(4)\n"
"v_mfma_f32_16x16x32_bf16 v[28:31], v[84:87], v[40:43], v[28:31]\n"
"ds_read_b128 v[32:35], v4 offset:8192\n"
"v_mfma_f32_16x16x32_bf16 v[28:31], v[196:199], v[44:47], v[28:31]\n"
"buffer_load_dwordx4 v2, s[32:35], s63 offen offset:2048 sc1 lds\n"
"v_mfma_f32_16x16x32_bf16 v[24:27], v[192:195], v[44:47], v[24:27]\n"
"ds_read_b128 v[36:39], v4 offset:22528\n"
"v_mfma_f32_16x16x32_bf16 v[24:27], v[80:83], v[40:43], v[24:27]\n"
"s_waitcnt lgkmcnt(4)\n"
"v_mfma_f32_16x16x32_bf16 v[24:27], a[56:59], v[48:51], v[24:27]\n"
"ds_read_b128 v[40:43], v4 offset:36864\n"
"v_mfma_f32_16x16x32_bf16 v[24:27], a[168:171], v[52:55], v[24:27]\n"
"buffer_load_dwordx4 v2, s[32:35], s63 offen offset:3072 sc1 lds\n"
"v_mfma_f32_16x16x32_bf16 v[28:31], a[172:175], v[52:55], v[28:31]\n"
"ds_read_b128 v[44:47], v4 offset:51200\n"
"v_mfma_f32_16x16x32_bf16 v[28:31], a[60:63], v[48:51], v[28:31]\n"
"s_waitcnt lgkmcnt(4)\n"
"v_mfma_f32_16x16x32_bf16 v[28:31], v[92:95], v[56:59], v[28:31]\n"
"ds_read_b128 v[48:51], v4 offset:9216\n"
"v_mfma_f32_16x16x32_bf16 v[28:31], v[204:207], v[60:63], v[28:31]\n"
"s_add_u32 s63, s63, 4096\n"
"s_add_u32 m0, s64, 8192\n"
"v_mfma_f32_16x16x32_bf16 v[24:27], v[200:203], v[60:63], v[24:27]\n"
"ds_read_b128 v[52:55], v4 offset:23552\n"
"v_mfma_f32_16x16x32_bf16 v[24:27], v[88:91], v[56:59], v[24:27]\n"
"s_waitcnt lgkmcnt(4)\n"
"v_mfma_f32_16x16x32_bf16 v[24:27], a[64:67], v[32:35], v[24:27]\n"
"ds_read_b128 v[56:59], v4 offset:37888\n"
"v_mfma_f32_16x16x32_bf16 v[24:27], a[176:179], v[36:39], v[24:27]\n"
"buffer_load_dwordx4 v2, s[32:35], s63 offen offset:0 sc1 lds\n"
"v_mfma_f32_16x16x32_bf16 v[28:31], a[180:183], v[36:39], v[28:31]\n"
"ds_read_b128 v[60:63], v4 offset:52224\n"
"v_mfma_f32_16x16x32_bf16 v[28:31], a[68:71], v[32:35], v[28:31]\n"
"s_waitcnt lgkmcnt(4)\n"
"v_mfma_f32_16x16x32_bf16 v[28:31], v[100:103], v[40:43], v[28:31]\n"
"ds_read_b128 v[32:35], v4 offset:10240\n"
"v_mfma_f32_16x16x32_bf16 v[28:31], v[212:215], v[44:47], v[28:31]\n"
"buffer_load_dwordx4 v2, s[32:35], s63 offen offset:1024 sc1 lds\n"
"v_mfma_f32_16x16x32_bf16 v[24:27], v[208:211], v[44:47], v[24:27]\n"
"ds_read_b128 v[36:39], v4 offset:24576\n"
"v_mfma_f32_16x16x32_bf16 v[24:27], v[96:99], v[40:43], v[24:27]\n"
"s_waitcnt lgkmcnt(4)\n"
"v_mfma_f32_16x16x32_bf16 v[24:27], a[72:75], v[48:51], v[24:27]\n"
"ds_read_b128 v[40:43], v4 offset:38912\n"
"v_mfma_f32_16x16x32_bf16 v[24:27], a[184:187], v[52:55], v[24:27]\n"
"buffer_load_dwordx4 v2, s[32:35], s63 offen offset:2048 sc1 lds\n"
"v_mfma_f32_16x16x32_bf16 v[28:31], a[188:191], v[52:55], v[28:31]\n"
"ds_read_b128 v[44:47], v4 offset:53248\n"
"v_mfma_f32_16x16x32_bf16 v[28:31], a[76:79], v[48:51], v[28:31]\n"
"s_waitcnt lgkmcnt(4)\n"
"v_mfma_f32_16x16x32_bf16 v[28:31], v[108:111], v[56:59], v[28:31]\n"
"ds_read_b128 v[48:51], v4 offset:11264\n"
"v_mfma_f32_16x16x32_bf16 v[28:31], v[220:223], v[60:63], v[28:31]\n"
"v_mfma_f32_16x16x32_bf16 v[24:27], v[216:219], v[60:63], v[24:27]\n"
"ds_read_b128 v[52:55], v4 offset:25600\n"
"v_mfma_f32_16x16x32_bf16 v[24:27], v[104:107], v[56:59], v[24:27]\n"
"s_waitcnt lgkmcnt(4)\n"
"v_mfma_f32_16x16x32_bf16 v[24:27], a[80:83], v[32:35], v[24:27]\n"
"ds_read_b128 v[56:59], v4 offset:39936\n"
"v_mfma_f32_16x16x32_bf16 v[24:27], a[192:195], v[36:39], v[24:27]\n"
"buffer_load_dwordx4 v2, s[32:35], s63 offen offset:3072 sc1 lds\n"
"s_add_u32 s63, s63, 4096\n"
"v_mfma_f32_16x16x32_bf16 v[28:31], a[196:199], v[36:39], v[28:31]\n"
"ds_read_b128 v[60:63], v4 offset:54272\n"
"s_add_u32 m0, s64, 12288\n"
"v_mfma_f32_16x16x32_bf16 v[28:31], a[84:87], v[32:35], v[28:31]\n"
"s_waitcnt lgkmcnt(4)\n"
"v_mfma_f32_16x16x32_bf16 v[28:31], v[116:119], v[40:43], v[28:31]\n"
"ds_read_b128 v[32:35], v4 offset:12288\n"
"v_mfma_f32_16x16x32_bf16 v[28:31], v[228:231], v[44:47], v[28:31]\n"
"buffer_load_dwordx4 v2, s[32:35], s63 offen offset:0 sc1 lds\n"
"v_mfma_f32_16x16x32_bf16 v[24:27], v[224:227], v[44:47], v[24:27]\n"
"ds_read_b128 v[36:39], v4 offset:26624\n"
"v_mfma_f32_16x16x32_bf16 v[24:27], v[112:115], v[40:43], v[24:27]\n"
"s_waitcnt lgkmcnt(4)\n"
"v_mfma_f32_16x16x32_bf16 v[24:27], a[88:91], v[48:51], v[24:27]\n"
"ds_read_b128 v[40:43], v4 offset:40960\n"
"v_mfma_f32_16x16x32_bf16 v[24:27], a[200:203], v[52:55], v[24:27]\n"
"buffer_load_dwordx4 v2, s[32:35], s63 offen offset:1024 sc1 lds\n"
"s_add_u32 s63, s63, 1175552\n"
"v_mfma_f32_16x16x32_bf16 v[28:31], a[204:207], v[52:55], v[28:31]\n"
"ds_read_b128 v[44:47], v4 offset:55296\n"
"v_mfma_f32_16x16x32_bf16 v[28:31], a[92:95], v[48:51], v[28:31]\n"
"s_waitcnt lgkmcnt(4)\n"
"v_mfma_f32_16x16x32_bf16 v[28:31], v[124:127], v[56:59], v[28:31]\n"
"ds_read_b128 v[48:51], v4 offset:13312\n"
"v_mfma_f32_16x16x32_bf16 v[28:31], v[236:239], v[60:63], v[28:31]\n"
"v_mfma_f32_16x16x32_bf16 v[24:27], v[232:235], v[60:63], v[24:27]\n"
"ds_read_b128 v[52:55], v4 offset:27648\n"
"v_mfma_f32_16x16x32_bf16 v[24:27], v[120:123], v[56:59], v[24:27]\n"
"s_waitcnt lgkmcnt(4)\n"
"v_mfma_f32_16x16x32_bf16 v[24:27], a[96:99], v[32:35], v[24:27]\n"
"ds_read_b128 v[56:59], v4 offset:41984\n"
"v_mfma_f32_16x16x32_bf16 v[24:27], a[208:211], v[36:39], v[24:27]\n"
"v_mfma_f32_16x16x32_bf16 v[28:31], a[212:215], v[36:39], v[28:31]\n"
"ds_read_b128 v[60:63], v4 offset:56320\n"
"v_mfma_f32_16x16x32_bf16 v[28:31], a[100:103], v[32:35], v[28:31]\n"
"s_waitcnt lgkmcnt(4)\n"
"s_waitcnt vmcnt(10)\n"
"s_barrier\n"
"v_mfma_f32_16x16x32_bf16 v[28:31], v[132:135], v[40:43], v[28:31]\n"
"ds_read_b128 v[32:35], v2 offset:0\n"
"v_mfma_f32_16x16x32_bf16 v[28:31], v[244:247], v[44:47], v[28:31]\n"
"v_mfma_f32_16x16x32_bf16 v[24:27], v[240:243], v[44:47], v[24:27]\n"
"ds_read_b128 v[36:39], v2 offset:14336\n"
"v_mfma_f32_16x16x32_bf16 v[24:27], v[128:131], v[40:43], v[24:27]\n"
"s_waitcnt lgkmcnt(4)\n"
"v_mfma_f32_16x16x32_bf16 v[24:27], a[104:107], v[48:51], v[24:27]\n"
"ds_read_b128 v[40:43], v2 offset:28672\n"
"v_mfma_f32_16x16x32_bf16 v[24:27], a[216:219], v[52:55], v[24:27]\n"
"v_mfma_f32_16x16x32_bf16 v[28:31], a[220:223], v[52:55], v[28:31]\n"
"ds_read_b128 v[44:47], v2 offset:43008\n"
"v_mfma_f32_16x16x32_bf16 v[28:31], a[108:111], v[48:51], v[28:31]\n"
"s_waitcnt lgkmcnt(4)\n"
"v_mfma_f32_16x16x32_bf16 v[28:31], v[140:143], v[56:59], v[28:31]\n"
"ds_read_b128 v[48:51], v2 offset:1024\n"
"v_mfma_f32_16x16x32_bf16 v[28:31], v[252:255], v[60:63], v[28:31]\n"
"v_mfma_f32_16x16x32_bf16 v[24:27], v[248:251], v[60:63], v[24:27]\n"
"ds_read_b128 v[52:55], v2 offset:15360\n"
"s_sub_u32 s62, s62, 1\n"
"v_mfma_f32_16x16x32_bf16 v[24:27], v[136:139], v[56:59], v[24:27]\n"
"s_cbranch_scc1 .L_dbt_0\n"
"s_waitcnt lgkmcnt(4)\n"
"v_mfma_f32_16x16x32_bf16 v[16:19], a[0:3], v[32:35], 0\n"
"ds_read_b128 v[56:59], v2 offset:29696\n"
"v_mfma_f32_16x16x32_bf16 v[16:19], a[112:115], v[36:39], v[16:19]\n"
"v_mfma_f32_16x16x32_bf16 v[20:23], a[116:119], v[36:39], 0\n"
"ds_read_b128 v[60:63], v2 offset:44032\n"
"v_mfma_f32_16x16x32_bf16 v[20:23], a[4:7], v[32:35], v[20:23]\n"
"v_cvt_pk_bf16_f32 v15, v30, v31\n"
"s_waitcnt lgkmcnt(4)\n"
"v_mfma_f32_16x16x32_bf16 v[20:23], a[228:231], v[40:43], v[20:23]\n"
"ds_read_b128 v[32:35], v2 offset:2048\n"
"v_cvt_pk_bf16_f32 v14, v28, v29\n"
"v_mfma_f32_16x16x32_bf16 v[20:23], v[148:151], v[44:47], v[20:23]\n"
"v_cvt_pk_bf16_f32 v13, v26, v27\n"
"v_cvt_pk_bf16_f32 v12, v24, v25\n"
"v_mfma_f32_16x16x32_bf16 v[16:19], v[144:147], v[44:47], v[16:19]\n"
"ds_read_b128 v[36:39], v2 offset:16384\n"
"ds_write_b128 v5, v[12:15]\n"
"v_mfma_f32_16x16x32_bf16 v[16:19], a[224:227], v[40:43], v[16:19]\n"
"ds_read2st64_b32 v[12:13], v3 offset0:0 offset1:1\n"
"s_waitcnt lgkmcnt(6)\n"
"v_mfma_f32_16x16x32_bf16 v[16:19], a[8:11], v[48:51], v[16:19]\n"
"ds_read_b128 v[40:43], v2 offset:30720\n"
"ds_read2st64_b32 v[14:15], v3 offset0:2 offset1:3\n"
"v_mfma_f32_16x16x32_bf16 v[16:19], a[120:123], v[52:55], v[16:19]\n"
"v_mfma_f32_16x16x32_bf16 v[20:23], a[124:127], v[52:55], v[20:23]\n"
"ds_read_b128 v[44:47], v2 offset:45056\n"
"v_mfma_f32_16x16x32_bf16 v[20:23], a[12:15], v[48:51], v[20:23]\n"
"s_waitcnt lgkmcnt(7)\n"
"v_mfma_f32_16x16x32_bf16 v[20:23], a[236:239], v[56:59], v[20:23]\n"
"ds_read_b128 v[48:51], v2 offset:3072\n"
"v_mfma_f32_16x16x32_bf16 v[20:23], v[156:159], v[60:63], v[20:23]\n"
"s_waitcnt lgkmcnt(2)\n"
"buffer_atomic_pk_add_bf16 v12, v1, s[44:47], s65 offen offset:0 sc1\n"
"v_mfma_f32_16x16x32_bf16 v[16:19], v[152:155], v[60:63], v[16:19]\n"
"ds_read_b128 v[52:55], v2 offset:17408\n"
"v_mfma_f32_16x16x32_bf16 v[16:19], a[232:235], v[56:59], v[16:19]\n"
"buffer_atomic_pk_add_bf16 v13, v1, s[44:47], s65 offen offset:2048 sc1\n"
"v_mfma_f32_16x16x32_bf16 v[16:19], a[16:19], v[32:35], v[16:19]\n"
"ds_read_b128 v[56:59], v2 offset:31744\n"
"s_add_u32 s65, s65, 16384\n"
"v_mfma_f32_16x16x32_bf16 v[16:19], a[128:131], v[36:39], v[16:19]\n"
"buffer_atomic_pk_add_bf16 v14, v1, s[44:47], s65 offen offset:0 sc1\n"
"v_mfma_f32_16x16x32_bf16 v[20:23], a[132:135], v[36:39], v[20:23]\n"
"ds_read_b128 v[60:63], v2 offset:46080\n"
"v_mfma_f32_16x16x32_bf16 v[20:23], a[20:23], v[32:35], v[20:23]\n"
"s_waitcnt lgkmcnt(4)\n"
"s_waitcnt vmcnt(3)\n"
"s_barrier\n"
"v_mfma_f32_16x16x32_bf16 v[20:23], a[244:247], v[40:43], v[20:23]\n"
"ds_read_b128 v[32:35], v2 offset:4096\n"
"s_add_u32 m0, s64, 57344\n"
"v_mfma_f32_16x16x32_bf16 v[20:23], v[164:167], v[44:47], v[20:23]\n"
"buffer_atomic_pk_add_bf16 v15, v1, s[44:47], s65 offen offset:2048 sc1\n"
"s_add_u32 s65, s65, 16384\n"
"v_mfma_f32_16x16x32_bf16 v[16:19], v[160:163], v[44:47], v[16:19]\n"
"ds_read_b128 v[36:39], v2 offset:18432\n"
"v_mfma_f32_16x16x32_bf16 v[16:19], a[240:243], v[40:43], v[16:19]\n"
"s_waitcnt lgkmcnt(4)\n"
"v_mfma_f32_16x16x32_bf16 v[16:19], a[24:27], v[48:51], v[16:19]\n"
"ds_read_b128 v[40:43], v2 offset:32768\n"
"v_mfma_f32_16x16x32_bf16 v[16:19], a[136:139], v[52:55], v[16:19]\n"
"buffer_load_dwordx4 v2, s[32:35], s63 offen offset:0 sc1 lds\n"
"v_mfma_f32_16x16x32_bf16 v[20:23], a[140:143], v[52:55], v[20:23]\n"
"ds_read_b128 v[44:47], v2 offset:47104\n"
"v_mfma_f32_16x16x32_bf16 v[20:23], a[28:31], v[48:51], v[20:23]\n"
"s_waitcnt lgkmcnt(4)\n"
"v_mfma_f32_16x16x32_bf16 v[20:23], a[252:255], v[56:59], v[20:23]\n"
"ds_read_b128 v[48:51], v2 offset:5120\n"
"v_mfma_f32_16x16x32_bf16 v[20:23], v[172:175], v[60:63], v[20:23]\n"
"buffer_load_dwordx4 v2, s[32:35], s63 offen offset:1024 sc1 lds\n"
"v_mfma_f32_16x16x32_bf16 v[16:19], v[168:171], v[60:63], v[16:19]\n"
"ds_read_b128 v[52:55], v2 offset:19456\n"
"v_mfma_f32_16x16x32_bf16 v[16:19], a[248:251], v[56:59], v[16:19]\n"
"s_waitcnt lgkmcnt(4)\n"
"v_mfma_f32_16x16x32_bf16 v[16:19], a[32:35], v[32:35], v[16:19]\n"
"ds_read_b128 v[56:59], v2 offset:33792\n"
"v_mfma_f32_16x16x32_bf16 v[16:19], a[144:147], v[36:39], v[16:19]\n"
"buffer_load_dwordx4 v2, s[32:35], s63 offen offset:2048 sc1 lds\n"
"v_mfma_f32_16x16x32_bf16 v[20:23], a[148:151], v[36:39], v[20:23]\n"
"ds_read_b128 v[60:63], v2 offset:48128\n"
"v_mfma_f32_16x16x32_bf16 v[20:23], a[36:39], v[32:35], v[20:23]\n"
"s_waitcnt lgkmcnt(4)\n"
"v_mfma_f32_16x16x32_bf16 v[20:23], v[68:71], v[40:43], v[20:23]\n"
"ds_read_b128 v[32:35], v2 offset:6144\n"
"v_mfma_f32_16x16x32_bf16 v[20:23], v[180:183], v[44:47], v[20:23]\n"
"buffer_load_dwordx4 v2, s[32:35], s63 offen offset:3072 sc1 lds\n"
"v_mfma_f32_16x16x32_bf16 v[16:19], v[176:179], v[44:47], v[16:19]\n"
"ds_read_b128 v[36:39], v2 offset:20480\n"
"v_mfma_f32_16x16x32_bf16 v[16:19], v[64:67], v[40:43], v[16:19]\n"
"s_waitcnt lgkmcnt(4)\n"
"v_mfma_f32_16x16x32_bf16 v[16:19], a[40:43], v[48:51], v[16:19]\n"
"ds_read_b128 v[40:43], v2 offset:34816\n"
"s_add_u32 s63, s63, 4096\n"
"v_mfma_f32_16x16x32_bf16 v[16:19], a[152:155], v[52:55], v[16:19]\n"
"s_add_u32 m0, s64, 61440\n"
"v_mfma_f32_16x16x32_bf16 v[20:23], a[156:159], v[52:55], v[20:23]\n"
"ds_read_b128 v[44:47], v2 offset:49152\n"
"v_mfma_f32_16x16x32_bf16 v[20:23], a[44:47], v[48:51], v[20:23]\n"
"s_waitcnt lgkmcnt(4)\n"
"v_mfma_f32_16x16x32_bf16 v[20:23], v[76:79], v[56:59], v[20:23]\n"
"ds_read_b128 v[48:51], v2 offset:7168\n"
"v_mfma_f32_16x16x32_bf16 v[20:23], v[188:191], v[60:63], v[20:23]\n"
"buffer_load_dwordx4 v2, s[32:35], s63 offen offset:0 sc1 lds\n"
"v_mfma_f32_16x16x32_bf16 v[16:19], v[184:187], v[60:63], v[16:19]\n"
"ds_read_b128 v[52:55], v2 offset:21504\n"
"v_mfma_f32_16x16x32_bf16 v[16:19], v[72:75], v[56:59], v[16:19]\n"
"s_waitcnt lgkmcnt(4)\n"
"v_mfma_f32_16x16x32_bf16 v[16:19], a[48:51], v[32:35], v[16:19]\n"
"ds_read_b128 v[56:59], v2 offset:35840\n"
"v_mfma_f32_16x16x32_bf16 v[16:19], a[160:163], v[36:39], v[16:19]\n"
"buffer_load_dwordx4 v2, s[32:35], s63 offen offset:1024 sc1 lds\n"
"v_mfma_f32_16x16x32_bf16 v[20:23], a[164:167], v[36:39], v[20:23]\n"
"ds_read_b128 v[60:63], v2 offset:50176\n"
"v_mfma_f32_16x16x32_bf16 v[20:23], a[52:55], v[32:35], v[20:23]\n"
"s_waitcnt lgkmcnt(4)\n"
"v_mfma_f32_16x16x32_bf16 v[20:23], v[84:87], v[40:43], v[20:23]\n"
"ds_read_b128 v[32:35], v2 offset:8192\n"
"v_mfma_f32_16x16x32_bf16 v[20:23], v[196:199], v[44:47], v[20:23]\n"
"buffer_load_dwordx4 v2, s[32:35], s63 offen offset:2048 sc1 lds\n"
"v_mfma_f32_16x16x32_bf16 v[16:19], v[192:195], v[44:47], v[16:19]\n"
"ds_read_b128 v[36:39], v2 offset:22528\n"
"v_mfma_f32_16x16x32_bf16 v[16:19], v[80:83], v[40:43], v[16:19]\n"
"s_waitcnt lgkmcnt(4)\n"
"v_mfma_f32_16x16x32_bf16 v[16:19], a[56:59], v[48:51], v[16:19]\n"
"ds_read_b128 v[40:43], v2 offset:36864\n"
"v_mfma_f32_16x16x32_bf16 v[16:19], a[168:171], v[52:55], v[16:19]\n"
"buffer_load_dwordx4 v2, s[32:35], s63 offen offset:3072 sc1 lds\n"
"v_mfma_f32_16x16x32_bf16 v[20:23], a[172:175], v[52:55], v[20:23]\n"
"ds_read_b128 v[44:47], v2 offset:51200\n"
"v_mfma_f32_16x16x32_bf16 v[20:23], a[60:63], v[48:51], v[20:23]\n"
"s_waitcnt lgkmcnt(4)\n"
"v_mfma_f32_16x16x32_bf16 v[20:23], v[92:95], v[56:59], v[20:23]\n"
"ds_read_b128 v[48:51], v2 offset:9216\n"
"v_mfma_f32_16x16x32_bf16 v[20:23], v[204:207], v[60:63], v[20:23]\n"
"s_add_u32 s63, s63, 4096\n"
"s_add_u32 m0, s64, 65536\n"
"v_mfma_f32_16x16x32_bf16 v[16:19], v[200:203], v[60:63], v[16:19]\n"
"ds_read_b128 v[52:55], v2 offset:23552\n"
"v_mfma_f32_16x16x32_bf16 v[16:19], v[88:91], v[56:59], v[16:19]\n"
"s_waitcnt lgkmcnt(4)\n"
"v_mfma_f32_16x16x32_bf16 v[16:19], a[64:67], v[32:35], v[16:19]\n"
"ds_read_b128 v[56:59], v2 offset:37888\n"
"v_mfma_f32_16x16x32_bf16 v[16:19], a[176:179], v[36:39], v[16:19]\n"
"buffer_load_dwordx4 v2, s[32:35], s63 offen offset:0 sc1 lds\n"
"v_mfma_f32_16x16x32_bf16 v[20:23], a[180:183], v[36:39], v[20:23]\n"
"ds_read_b128 v[60:63], v2 offset:52224\n"
"v_mfma_f32_16x16x32_bf16 v[20:23], a[68:71], v[32:35], v[20:23]\n"
"s_waitcnt lgkmcnt(4)\n"
"v_mfma_f32_16x16x32_bf16 v[20:23], v[100:103], v[40:43], v[20:23]\n"
"ds_read_b128 v[32:35], v2 offset:10240\n"
"v_mfma_f32_16x16x32_bf16 v[20:23], v[212:215], v[44:47], v[20:23]\n"
"buffer_load_dwordx4 v2, s[32:35], s63 offen offset:1024 sc1 lds\n"
"v_mfma_f32_16x16x32_bf16 v[16:19], v[208:211], v[44:47], v[16:19]\n"
"ds_read_b128 v[36:39], v2 offset:24576\n"
"v_mfma_f32_16x16x32_bf16 v[16:19], v[96:99], v[40:43], v[16:19]\n"
"s_waitcnt lgkmcnt(4)\n"
"v_mfma_f32_16x16x32_bf16 v[16:19], a[72:75], v[48:51], v[16:19]\n"
"ds_read_b128 v[40:43], v2 offset:38912\n"
"v_mfma_f32_16x16x32_bf16 v[16:19], a[184:187], v[52:55], v[16:19]\n"
"buffer_load_dwordx4 v2, s[32:35], s63 offen offset:2048 sc1 lds\n"
"v_mfma_f32_16x16x32_bf16 v[20:23], a[188:191], v[52:55], v[20:23]\n"
"ds_read_b128 v[44:47], v2 offset:53248\n"
"v_mfma_f32_16x16x32_bf16 v[20:23], a[76:79], v[48:51], v[20:23]\n"
"s_waitcnt lgkmcnt(4)\n"
"v_mfma_f32_16x16x32_bf16 v[20:23], v[108:111], v[56:59], v[20:23]\n"
"ds_read_b128 v[48:51], v2 offset:11264\n"
"v_mfma_f32_16x16x32_bf16 v[20:23], v[220:223], v[60:63], v[20:23]\n"
"v_mfma_f32_16x16x32_bf16 v[16:19], v[216:219], v[60:63], v[16:19]\n"
"ds_read_b128 v[52:55], v2 offset:25600\n"
"v_mfma_f32_16x16x32_bf16 v[16:19], v[104:107], v[56:59], v[16:19]\n"
"s_waitcnt lgkmcnt(4)\n"
"v_mfma_f32_16x16x32_bf16 v[16:19], a[80:83], v[32:35], v[16:19]\n"
"ds_read_b128 v[56:59], v2 offset:39936\n"
"v_mfma_f32_16x16x32_bf16 v[16:19], a[192:195], v[36:39], v[16:19]\n"
"buffer_load_dwordx4 v2, s[32:35], s63 offen offset:3072 sc1 lds\n"
"s_add_u32 s63, s63, 4096\n"
"v_mfma_f32_16x16x32_bf16 v[20:23], a[196:199], v[36:39], v[20:23]\n"
"ds_read_b128 v[60:63], v2 offset:54272\n"
"s_add_u32 m0, s64, 69632\n"
"v_mfma_f32_16x16x32_bf16 v[20:23], a[84:87], v[32:35], v[20:23]\n"
"s_waitcnt lgkmcnt(4)\n"
"v_mfma_f32_16x16x32_bf16 v[20:23], v[116:119], v[40:43], v[20:23]\n"
"ds_read_b128 v[32:35], v2 offset:12288\n"
"v_mfma_f32_16x16x32_bf16 v[20:23], v[228:231], v[44:47], v[20:23]\n"
"buffer_load_dwordx4 v2, s[32:35], s63 offen offset:0 sc1 lds\n"
"v_mfma_f32_16x16x32_bf16 v[16:19], v[224:227], v[44:47], v[16:19]\n"
"ds_read_b128 v[36:39], v2 offset:26624\n"
"v_mfma_f32_16x16x32_bf16 v[16:19], v[112:115], v[40:43], v[16:19]\n"
"s_waitcnt lgkmcnt(4)\n"
"v_mfma_f32_16x16x32_bf16 v[16:19], a[88:91], v[48:51], v[16:19]\n"
"ds_read_b128 v[40:43], v2 offset:40960\n"
"v_mfma_f32_16x16x32_bf16 v[16:19], a[200:203], v[52:55], v[16:19]\n"
"buffer_load_dwordx4 v2, s[32:35], s63 offen offset:1024 sc1 lds\n"
"s_add_u32 s63, s63, 1175552\n"
"v_mfma_f32_16x16x32_bf16 v[20:23], a[204:207], v[52:55], v[20:23]\n"
"ds_read_b128 v[44:47], v2 offset:55296\n"
"v_mfma_f32_16x16x32_bf16 v[20:23], a[92:95], v[48:51], v[20:23]\n"
"s_waitcnt lgkmcnt(4)\n"
"v_mfma_f32_16x16x32_bf16 v[20:23], v[124:127], v[56:59], v[20:23]\n"
"ds_read_b128 v[48:51], v2 offset:13312\n"
"v_mfma_f32_16x16x32_bf16 v[20:23], v[236:239], v[60:63], v[20:23]\n"
"v_mfma_f32_16x16x32_bf16 v[16:19], v[232:235], v[60:63], v[16:19]\n"
"ds_read_b128 v[52:55], v2 offset:27648\n"
"v_mfma_f32_16x16x32_bf16 v[16:19], v[120:123], v[56:59], v[16:19]\n"
"s_waitcnt lgkmcnt(4)\n"
"v_mfma_f32_16x16x32_bf16 v[16:19], a[96:99], v[32:35], v[16:19]\n"
"ds_read_b128 v[56:59], v2 offset:41984\n"
"v_mfma_f32_16x16x32_bf16 v[16:19], a[208:211], v[36:39], v[16:19]\n"
"v_mfma_f32_16x16x32_bf16 v[20:23], a[212:215], v[36:39], v[20:23]\n"
"ds_read_b128 v[60:63], v2 offset:56320\n"
"v_mfma_f32_16x16x32_bf16 v[20:23], a[100:103], v[32:35], v[20:23]\n"
"s_waitcnt lgkmcnt(4)\n"
"s_waitcnt vmcnt(10)\n"
"s_barrier\n"
"v_mfma_f32_16x16x32_bf16 v[20:23], v[132:135], v[40:43], v[20:23]\n"
"ds_read_b128 v[32:35], v4 offset:0\n"
"v_mfma_f32_16x16x32_bf16 v[20:23], v[244:247], v[44:47], v[20:23]\n"
"v_mfma_f32_16x16x32_bf16 v[16:19], v[240:243], v[44:47], v[16:19]\n"
"ds_read_b128 v[36:39], v4 offset:14336\n"
"v_mfma_f32_16x16x32_bf16 v[16:19], v[128:131], v[40:43], v[16:19]\n"
"s_waitcnt lgkmcnt(4)\n"
"v_mfma_f32_16x16x32_bf16 v[16:19], a[104:107], v[48:51], v[16:19]\n"
"ds_read_b128 v[40:43], v4 offset:28672\n"
"v_mfma_f32_16x16x32_bf16 v[16:19], a[216:219], v[52:55], v[16:19]\n"
"v_mfma_f32_16x16x32_bf16 v[20:23], a[220:223], v[52:55], v[20:23]\n"
"ds_read_b128 v[44:47], v4 offset:43008\n"
"v_mfma_f32_16x16x32_bf16 v[20:23], a[108:111], v[48:51], v[20:23]\n"
"s_waitcnt lgkmcnt(4)\n"
"v_mfma_f32_16x16x32_bf16 v[20:23], v[140:143], v[56:59], v[20:23]\n"
"ds_read_b128 v[48:51], v4 offset:1024\n"
"v_mfma_f32_16x16x32_bf16 v[20:23], v[252:255], v[60:63], v[20:23]\n"
"v_mfma_f32_16x16x32_bf16 v[16:19], v[248:251], v[60:63], v[16:19]\n"
"ds_read_b128 v[52:55], v4 offset:15360\n"
"v_mfma_f32_16x16x32_bf16 v[16:19], v[136:139], v[56:59], v[16:19]\n"
"s_branch .L_dbl_0\n"
".L_dbt_0:\n"
"s_nop 1\n"
"s_nop 0\n"
"v_cvt_pk_bf16_f32 v15, v30, v31\n"
"v_cvt_pk_bf16_f32 v14, v28, v29\n"
"s_nop 1\n"
"v_cvt_pk_bf16_f32 v13, v26, v27\n"
"v_cvt_pk_bf16_f32 v12, v24, v25\n"
"ds_write_b128 v5, v[12:15]\n"
"ds_read2st64_b32 v[12:13], v3 offset0:0 offset1:1\n"
"ds_read2st64_b32 v[14:15], v3 offset0:2 offset1:3\n"
"s_waitcnt lgkmcnt(0)\n"
"buffer_atomic_pk_add_bf16 v12, v1, s[44:47], s65 offen offset:0 sc1\n"
"buffer_atomic_pk_add_bf16 v13, v1, s[44:47], s65 offen offset:2048 sc1\n"
"s_add_u32 s65, s65, 16384\n"
"buffer_atomic_pk_add_bf16 v14, v1, s[44:47], s65 offen offset:0 sc1\n"
"buffer_atomic_pk_add_bf16 v15, v1, s[44:47], s65 offen offset:2048 sc1\n"
"s_add_u32 s65, s65, 16384\n"
"s_or_b32 s48, s28, s27\n"
"s_or_b32 s48, s48, s26\n"
"s_cbranch_scc1 .L_end\n"
"v_mov_b32 v8, s54\n"
"v_mov_b32 v9, s55\n"
"buffer_store_dwordx2 v[8:9], off, s[32:35], 0 sc1\n"
"s_branch .L_end\n"
".p2align 6\n"
".L_dhv1:\n"
".L_dal_1:\n"
"s_waitcnt lgkmcnt(4)\n"
"v_mfma_f32_16x16x32_bf16 v[16:19], a[0:3], v[32:35], 0\n"
"ds_read_b128 v[56:59], v2 offset:29696\n"
"ds_read_b128 v[12:15], v5 offset:8192\n"
"v_mfma_f32_16x16x32_bf16 v[16:19], a[112:115], v[36:39], v[16:19]\n"
"v_mfma_f32_16x16x32_bf16 v[20:23], a[116:119], v[36:39], 0\n"
"ds_read_b128 v[60:63], v2 offset:44032\n"
"v_mfma_f32_16x16x32_bf16 v[20:23], a[4:7], v[32:35], v[20:23]\n"
"s_waitcnt lgkmcnt(5)\n"
"v_mfma_f32_16x16x32_bf16 v[20:23], a[228:231], v[40:43], v[20:23]\n"
"ds_read_b128 v[32:35], v2 offset:2048\n"
"v_mfma_f32_16x16x32_bf16 v[20:23], v[148:151], v[44:47], v[20:23]\n"
"s_waitcnt lgkmcnt(2)\n"
"v_add_f32 v24, v24, v12\n"
"v_mfma_f32_16x16x32_bf16 v[16:19], v[144:147], v[44:47], v[16:19]\n"
"ds_read_b128 v[36:39], v2 offset:16384\n"
"v_add_f32 v25, v25, v13\n"
"v_mfma_f32_16x16x32_bf16 v[16:19], a[224:227], v[40:43], v[16:19]\n"
"v_add_f32 v26, v26, v14\n"
"v_mfma_f32_16x16x32_bf16 v[16:19], a[8:11], v[48:51], v[16:19]\n"
"ds_read_b128 v[40:43], v2 offset:30720\n"
"v_add_f32 v27, v27, v15\n"
"v_mfma_f32_16x16x32_bf16 v[16:19], a[120:123], v[52:55], v[16:19]\n"
"ds_read_b128 v[12:15], v5 offset:9216\n"
"v_mfma_f32_16x16x32_bf16 v[20:23], a[124:127], v[52:55], v[20:23]\n"
"ds_read_b128 v[44:47], v2 offset:45056\n"
"v_mfma_f32_16x16x32_bf16 v[20:23], a[12:15], v[48:51], v[20:23]\n"
"s_waitcnt lgkmcnt(5)\n"
"v_mfma_f32_16x16x32_bf16 v[20:23], a[236:239], v[56:59], v[20:23]\n"
"ds_read_b128 v[48:51], v2 offset:3072\n"
"v_mfma_f32_16x16x32_bf16 v[20:23], v[156:159], v[60:63], v[20:23]\n"
"v_mfma_f32_16x16x32_bf16 v[16:19], v[152:155], v[60:63], v[16:19]\n"
"ds_read_b128 v[52:55], v2 offset:17408\n"
"s_waitcnt vmcnt(5)\n"
"v_mfma_f32_16x16x32_bf16 v[16:19], a[232:235], v[56:59], v[16:19]\n"
"v_cmp_ne_u64 vcc, v[8:9], v[6:7]\n"
"s_waitcnt lgkmcnt(5)\n"
"v_mfma_f32_16x16x32_bf16 v[16:19], a[16:19], v[32:35], v[16:19]\n"
"ds_read_b128 v[56:59], v2 offset:31744\n"
"s_cbranch_vccnz .L_rta0_1\n"
".L_bka0_1:\n"
"v_mfma_f32_16x16x32_bf16 v[16:19], a[128:131], v[36:39], v[16:19]\n"
"s_add_u32 s66, s66, 1187840\n"
"s_waitcnt lgkmcnt(4)\n"
"v_mfma_f32_16x16x32_bf16 v[20:23], a[132:135], v[36:39], v[20:23]\n"
"s_waitcnt vmcnt(0)\n"
"s_barrier\n"
"v_mfma_f32_16x16x32_bf16 v[20:23], a[20:23], v[32:35], v[20:23]\n"
"ds_read_b128 v[60:63], v2 offset:46080\n"
"s_waitcnt lgkmcnt(4)\n"
"v_mfma_f32_16x16x32_bf16 v[20:23], a[244:247], v[40:43], v[20:23]\n"
"ds_read_b128 v[32:35], v2 offset:4096\n"
"s_add_u32 m0, s64, 57344\n"
"v_mfma_f32_16x16x32_bf16 v[20:23], v[164:167], v[44:47], v[20:23]\n"
"buffer_load_dwordx4 v2, s[32:35], s63 offen offset:0 sc1 lds\n"
"v_add_f32 v28, v28, v12\n"
"v_mfma_f32_16x16x32_bf16 v[16:19], v[160:163], v[44:47], v[16:19]\n"
"ds_read_b128 v[36:39], v2 offset:18432\n"
"v_add_f32 v29, v29, v13\n"
"v_mfma_f32_16x16x32_bf16 v[16:19], a[240:243], v[40:43], v[16:19]\n"
"v_add_f32 v30, v30, v14\n"
"s_waitcnt lgkmcnt(4)\n"
"v_mfma_f32_16x16x32_bf16 v[16:19], a[24:27], v[48:51], v[16:19]\n"
"ds_read_b128 v[40:43], v2 offset:32768\n"
"v_add_f32 v31, v31, v15\n"
"v_mfma_f32_16x16x32_bf16 v[16:19], a[136:139], v[52:55], v[16:19]\n"
"v_cvt_pk_bf16_f32 v12, v24, v25\n"
"v_cvt_pk_bf16_f32 v13, v26, v27\n"
"v_mfma_f32_16x16x32_bf16 v[20:23], a[140:143], v[52:55], v[20:23]\n"
"ds_read_b128 v[44:47], v2 offset:47104\n"
"v_cvt_pk_bf16_f32 v14, v28, v29\n"
"v_mfma_f32_16x16x32_bf16 v[20:23], a[28:31], v[48:51], v[20:23]\n"
"v_cvt_pk_bf16_f32 v15, v30, v31\n"
"s_waitcnt lgkmcnt(4)\n"
"v_mfma_f32_16x16x32_bf16 v[20:23], a[252:255], v[56:59], v[20:23]\n"
"ds_read_b128 v[48:51], v2 offset:5120\n"
"v_mfma_f32_16x16x32_bf16 v[20:23], v[172:175], v[60:63], v[20:23]\n"
"buffer_store_dwordx4 v[12:15], v2, s[32:35], s68 offen sc1\n"
"v_add_f32 v10, v24, v25\n"
"v_mfma_f32_16x16x32_bf16 v[16:19], v[168:171], v[60:63], v[16:19]\n"
"ds_read_b128 v[52:55], v2 offset:19456\n"
"v_mul_f32 v11, v24, v24\n"
"v_mfma_f32_16x16x32_bf16 v[16:19], a[248:251], v[56:59], v[16:19]\n"
"v_fmac_f32 v11, v25, v25\n"
"s_waitcnt lgkmcnt(4)\n"
"v_mfma_f32_16x16x32_bf16 v[16:19], a[32:35], v[32:35], v[16:19]\n"
"ds_read_b128 v[56:59], v2 offset:33792\n"
"v_add_f32 v10, v10, v26\n"
"v_mfma_f32_16x16x32_bf16 v[16:19], a[144:147], v[36:39], v[16:19]\n"
"buffer_load_dwordx4 v2, s[32:35], s63 offen offset:1024 sc1 lds\n"
"v_fmac_f32 v11, v26, v26\n"
"v_mfma_f32_16x16x32_bf16 v[20:23], a[148:151], v[36:39], v[20:23]\n"
"ds_read_b128 v[60:63], v2 offset:48128\n"
"v_add_f32 v10, v10, v27\n"
"v_mfma_f32_16x16x32_bf16 v[20:23], a[36:39], v[32:35], v[20:23]\n"
"v_fmac_f32 v11, v27, v27\n"
"s_waitcnt lgkmcnt(4)\n"
"v_mfma_f32_16x16x32_bf16 v[20:23], v[68:71], v[40:43], v[20:23]\n"
"ds_read_b128 v[32:35], v2 offset:6144\n"
"v_add_f32 v10, v10, v28\n"
"v_mfma_f32_16x16x32_bf16 v[20:23], v[180:183], v[44:47], v[20:23]\n"
"buffer_load_dwordx4 v2, s[32:35], s63 offen offset:2048 sc1 lds\n"
"v_fmac_f32 v11, v28, v28\n"
"v_mfma_f32_16x16x32_bf16 v[16:19], v[176:179], v[44:47], v[16:19]\n"
"ds_read_b128 v[36:39], v2 offset:20480\n"
"v_add_f32 v10, v10, v29\n"
"v_mfma_f32_16x16x32_bf16 v[16:19], v[64:67], v[40:43], v[16:19]\n"
"v_fmac_f32 v11, v29, v29\n"
"s_waitcnt lgkmcnt(4)\n"
"v_mfma_f32_16x16x32_bf16 v[16:19], a[40:43], v[48:51], v[16:19]\n"
"ds_read_b128 v[40:43], v2 offset:34816\n"
"v_add_f32 v10, v10, v30\n"
"v_mfma_f32_16x16x32_bf16 v[16:19], a[152:155], v[52:55], v[16:19]\n"
"buffer_load_dwordx4 v2, s[32:35], s63 offen offset:3072 sc1 lds\n"
"s_add_u32 m0, s74, 0\n"
"v_mfma_f32_16x16x32_bf16 v[20:23], a[156:159], v[52:55], v[20:23]\n"
"ds_read_b128 v[44:47], v2 offset:49152\n"
"v_fmac_f32 v11, v30, v30\n"
"v_mfma_f32_16x16x32_bf16 v[20:23], a[44:47], v[48:51], v[20:23]\n"
"v_add_f32 v10, v10, v31\n"
"s_waitcnt lgkmcnt(4)\n"
"v_mfma_f32_16x16x32_bf16 v[20:23], v[76:79], v[56:59], v[20:23]\n"
"ds_read_b128 v[48:51], v2 offset:7168\n"
"v_fmac_f32 v11, v31, v31\n"
"v_mfma_f32_16x16x32_bf16 v[20:23], v[188:191], v[60:63], v[20:23]\n"
"buffer_load_dwordx4 v2, s[32:35], s71 offen offset:0 sc1 lds\n"
"v_permlane16_swap_b32 v10, v11\n"
"v_mfma_f32_16x16x32_bf16 v[16:19], v[184:187], v[60:63], v[16:19]\n"
"ds_read_b128 v[52:55], v2 offset:21504\n"
"v_add_f32 v10, v10, v11\n"
"v_mfma_f32_16x16x32_bf16 v[16:19], v[72:75], v[56:59], v[16:19]\n"
"s_waitcnt lgkmcnt(4)\n"
"v_mfma_f32_16x16x32_bf16 v[16:19], a[48:51], v[32:35], v[16:19]\n"
"ds_read_b128 v[56:59], v2 offset:35840\n"
"v_permlane32_swap_b32 v10, v11\n"
"v_mfma_f32_16x16x32_bf16 v[16:19], a[160:163], v[36:39], v[16:19]\n"
"buffer_load_dwordx4 v2, s[32:35], s71 offen offset:1024 sc1 lds\n"
"s_add_u32 s71, s71, 1187840\n"
"v_mfma_f32_16x16x32_bf16 v[20:23], a[164:167], v[36:39], v[20:23]\n"
"ds_read_b128 v[60:63], v2 offset:50176\n"
"s_add_u32 s63, s63, 4096\n"
"v_mfma_f32_16x16x32_bf16 v[20:23], a[52:55], v[32:35], v[20:23]\n"
"s_add_u32 m0, s64, 61440\n"
"s_waitcnt lgkmcnt(4)\n"
"v_mfma_f32_16x16x32_bf16 v[20:23], v[84:87], v[40:43], v[20:23]\n"
"ds_read_b128 v[32:35], v2 offset:8192\n"
"v_add_f32 v10, v10, v11\n"
"v_mfma_f32_16x16x32_bf16 v[20:23], v[196:199], v[44:47], v[20:23]\n"
"buffer_store_dword v10, v3, s[32:35], s68 offen offset:1024 sc1\n"
"s_mov_b32 s67, s68\n"
"v_mfma_f32_16x16x32_bf16 v[16:19], v[192:195], v[44:47], v[16:19]\n"
"ds_read_b128 v[36:39], v2 offset:22528\n"
"s_mov_b32 s68, s70\n"
"v_mfma_f32_16x16x32_bf16 v[16:19], v[80:83], v[40:43], v[16:19]\n"
"s_add_u32 s70, s70, 1187840\n"
"s_waitcnt lgkmcnt(4)\n"
"v_mfma_f32_16x16x32_bf16 v[16:19], a[56:59], v[48:51], v[16:19]\n"
"ds_read_b128 v[40:43], v2 offset:36864\n"
"v_mfma_f32_16x16x32_bf16 v[16:19], a[168:171], v[52:55], v[16:19]\n"
"buffer_load_dwordx4 v2, s[32:35], s63 offen offset:0 sc1 lds\n"
"v_mfma_f32_16x16x32_bf16 v[20:23], a[172:175], v[52:55], v[20:23]\n"
"ds_read_b128 v[44:47], v2 offset:51200\n"
"v_mfma_f32_16x16x32_bf16 v[20:23], a[60:63], v[48:51], v[20:23]\n"
"s_waitcnt lgkmcnt(4)\n"
"v_mfma_f32_16x16x32_bf16 v[20:23], v[92:95], v[56:59], v[20:23]\n"
"ds_read_b128 v[48:51], v2 offset:9216\n"
"v_mfma_f32_16x16x32_bf16 v[20:23], v[204:207], v[60:63], v[20:23]\n"
"buffer_load_dwordx4 v2, s[32:35], s63 offen offset:1024 sc1 lds\n"
"v_mfma_f32_16x16x32_bf16 v[16:19], v[200:203], v[60:63], v[16:19]\n"
"ds_read_b128 v[52:55], v2 offset:23552\n"
"v_mfma_f32_16x16x32_bf16 v[16:19], v[88:91], v[56:59], v[16:19]\n"
"s_waitcnt lgkmcnt(4)\n"
"v_mfma_f32_16x16x32_bf16 v[16:19], a[64:67], v[32:35], v[16:19]\n"
"ds_read_b128 v[56:59], v2 offset:37888\n"
"v_mfma_f32_16x16x32_bf16 v[16:19], a[176:179], v[36:39], v[16:19]\n"
"buffer_load_dwordx4 v2, s[32:35], s63 offen offset:2048 sc1 lds\n"
"v_mfma_f32_16x16x32_bf16 v[20:23], a[180:183], v[36:39], v[20:23]\n"
"ds_read_b128 v[60:63], v2 offset:52224\n"
"v_mfma_f32_16x16x32_bf16 v[20:23], a[68:71], v[32:35], v[20:23]\n"
"s_waitcnt lgkmcnt(4)\n"
"v_mfma_f32_16x16x32_bf16 v[20:23], v[100:103], v[40:43], v[20:23]\n"
"ds_read_b128 v[32:35], v2 offset:10240\n"
"v_mfma_f32_16x16x32_bf16 v[20:23], v[212:215], v[44:47], v[20:23]\n"
"buffer_load_dwordx4 v2, s[32:35], s63 offen offset:3072 sc1 lds\n"
"s_add_u32 s63, s63, 4096\n"
"v_mfma_f32_16x16x32_bf16 v[16:19], v[208:211], v[44:47], v[16:19]\n"
"ds_read_b128 v[36:39], v2 offset:24576\n"
"s_add_u32 m0, s64, 65536\n"
"v_mfma_f32_16x16x32_bf16 v[16:19], v[96:99], v[40:43], v[16:19]\n"
"s_waitcnt lgkmcnt(4)\n"
"v_mfma_f32_16x16x32_bf16 v[16:19], a[72:75], v[48:51], v[16:19]\n"
"ds_read_b128 v[40:43], v2 offset:38912\n"
"v_mfma_f32_16x16x32_bf16 v[16:19], a[184:187], v[52:55], v[16:19]\n"
"buffer_load_dwordx4 v2, s[32:35], s63 offen offset:0 sc1 lds\n"
"v_mfma_f32_16x16x32_bf16 v[20:23], a[188:191], v[52:55], v[20:23]\n"
"ds_read_b128 v[44:47], v2 offset:53248\n"
"v_mfma_f32_16x16x32_bf16 v[20:23], a[76:79], v[48:51], v[20:23]\n"
"s_waitcnt lgkmcnt(4)\n"
"v_mfma_f32_16x16x32_bf16 v[20:23], v[108:111], v[56:59], v[20:23]\n"
"ds_read_b128 v[48:51], v2 offset:11264\n"
"v_mfma_f32_16x16x32_bf16 v[20:23], v[220:223], v[60:63], v[20:23]\n"
"buffer_load_dwordx2 v[6:7], v0, s[32:35], s66 offen sc1\n"
"v_mfma_f32_16x16x32_bf16 v[16:19], v[216:219], v[60:63], v[16:19]\n"
"ds_read_b128 v[52:55], v2 offset:25600\n"
"v_mfma_f32_16x16x32_bf16 v[16:19], v[104:107], v[56:59], v[16:19]\n"
"s_waitcnt lgkmcnt(4)\n"
"v_mfma_f32_16x16x32_bf16 v[16:19], a[80:83], v[32:35], v[16:19]\n"
"ds_read_b128 v[56:59], v2 offset:39936\n"
"v_mfma_f32_16x16x32_bf16 v[16:19], a[192:195], v[36:39], v[16:19]\n"
"buffer_load_dwordx4 v2, s[32:35], s63 offen offset:1024 sc1 lds\n"
"v_mfma_f32_16x16x32_bf16 v[20:23], a[196:199], v[36:39], v[20:23]\n"
"ds_read_b128 v[60:63], v2 offset:54272\n"
"v_mfma_f32_16x16x32_bf16 v[20:23], a[84:87], v[32:35], v[20:23]\n"
"s_waitcnt lgkmcnt(4)\n"
"v_mfma_f32_16x16x32_bf16 v[20:23], v[116:119], v[40:43], v[20:23]\n"
"ds_read_b128 v[32:35], v2 offset:12288\n"
"v_mfma_f32_16x16x32_bf16 v[20:23], v[228:231], v[44:47], v[20:23]\n"
"buffer_load_dwordx4 v2, s[32:35], s63 offen offset:2048 sc1 lds\n"
"v_mfma_f32_16x16x32_bf16 v[16:19], v[224:227], v[44:47], v[16:19]\n"
"ds_read_b128 v[36:39], v2 offset:26624\n"
"v_mfma_f32_16x16x32_bf16 v[16:19], v[112:115], v[40:43], v[16:19]\n"
"s_waitcnt lgkmcnt(4)\n"
"v_mfma_f32_16x16x32_bf16 v[16:19], a[88:91], v[48:51], v[16:19]\n"
"ds_read_b128 v[40:43], v2 offset:40960\n"
"v_mfma_f32_16x16x32_bf16 v[16:19], a[200:203], v[52:55], v[16:19]\n"
"buffer_load_dwordx4 v2, s[32:35], s63 offen offset:3072 sc1 lds\n"
"s_add_u32 s63, s63, 4096\n"
"v_mfma_f32_16x16x32_bf16 v[20:23], a[204:207], v[52:55], v[20:23]\n"
"ds_read_b128 v[44:47], v2 offset:55296\n"
"s_add_u32 m0, s64, 69632\n"
"v_mfma_f32_16x16x32_bf16 v[20:23], a[92:95], v[48:51], v[20:23]\n"
"s_waitcnt lgkmcnt(4)\n"
"v_mfma_f32_16x16x32_bf16 v[20:23], v[124:127], v[56:59], v[20:23]\n"
"ds_read_b128 v[48:51], v2 offset:13312\n"
"v_mfma_f32_16x16x32_bf16 v[20:23], v[236:239], v[60:63], v[20:23]\n"
"buffer_load_dwordx4 v2, s[32:35], s63 offen offset:0 sc1 lds\n"
"v_mfma_f32_16x16x32_bf16 v[16:19], v[232:235], v[60:63], v[16:19]\n"
"ds_read_b128 v[52:55], v2 offset:27648\n"
"v_mfma_f32_16x16x32_bf16 v[16:19], v[120:123], v[56:59], v[16:19]\n"
"s_waitcnt lgkmcnt(4)\n"
"v_mfma_f32_16x16x32_bf16 v[16:19], a[96:99], v[32:35], v[16:19]\n"
"ds_read_b128 v[56:59], v2 offset:41984\n"
"v_mfma_f32_16x16x32_bf16 v[16:19], a[208:211], v[36:39], v[16:19]\n"
"buffer_load_dwordx4 v2, s[32:35], s63 offen offset:1024 sc1 lds\n"
"s_add_u32 s63, s63, 1175552\n"
"v_mfma_f32_16x16x32_bf16 v[20:23], a[212:215], v[36:39], v[20:23]\n"
"s_waitcnt vmcnt(14)\n"
"s_waitcnt vmcnt(11)\n"
"s_barrier\n"
"v_mfma_f32_16x16x32_bf16 v[20:23], a[100:103], v[32:35], v[20:23]\n"
"ds_read_b128 v[60:63], v2 offset:56320\n"
"s_waitcnt lgkmcnt(4)\n"
"v_mfma_f32_16x16x32_bf16 v[20:23], v[132:135], v[40:43], v[20:23]\n"
"ds_read_b128 v[32:35], v4 offset:0\n"
"v_mfma_f32_16x16x32_bf16 v[20:23], v[244:247], v[44:47], v[20:23]\n"
"v_mfma_f32_16x16x32_bf16 v[16:19], v[240:243], v[44:47], v[16:19]\n"
"ds_read_b128 v[36:39], v4 offset:14336\n"
"v_mfma_f32_16x16x32_bf16 v[16:19], v[128:131], v[40:43], v[16:19]\n"
"s_waitcnt lgkmcnt(4)\n"
"v_mfma_f32_16x16x32_bf16 v[16:19], a[104:107], v[48:51], v[16:19]\n"
"ds_read_b128 v[40:43], v4 offset:28672\n"
"v_mfma_f32_16x16x32_bf16 v[16:19], a[216:219], v[52:55], v[16:19]\n"
"v_mfma_f32_16x16x32_bf16 v[20:23], a[220:223], v[52:55], v[20:23]\n"
"ds_read_b128 v[44:47], v4 offset:43008\n"
"v_mfma_f32_16x16x32_bf16 v[20:23], a[108:111], v[48:51], v[20:23]\n"
"s_waitcnt lgkmcnt(4)\n"
"v_mfma_f32_16x16x32_bf16 v[20:23], v[140:143], v[56:59], v[20:23]\n"
"ds_read_b128 v[48:51], v4 offset:1024\n"
"v_mfma_f32_16x16x32_bf16 v[20:23], v[252:255], v[60:63], v[20:23]\n"
"v_mfma_f32_16x16x32_bf16 v[16:19], v[248:251], v[60:63], v[16:19]\n"
"ds_read_b128 v[52:55], v4 offset:15360\n"
"s_sub_u32 s62, s62, 1\n"
"v_mfma_f32_16x16x32_bf16 v[16:19], v[136:139], v[56:59], v[16:19]\n"
"s_cbranch_scc1 .L_dab_1\n"
"s_waitcnt lgkmcnt(4)\n"
"v_mfma_f32_16x16x32_bf16 v[24:27], a[0:3], v[32:35], 0\n"
"ds_read_b128 v[56:59], v4 offset:29696\n"
"ds_read_b128 v[12:15], v5 offset:0\n"
"v_mfma_f32_16x16x32_bf16 v[24:27], a[112:115], v[36:39], v[24:27]\n"
"v_mfma_f32_16x16x32_bf16 v[28:31], a[116:119], v[36:39], 0\n"
"ds_read_b128 v[60:63], v4 offset:44032\n"
"v_mfma_f32_16x16x32_bf16 v[28:31], a[4:7], v[32:35], v[28:31]\n"
"s_waitcnt lgkmcnt(5)\n"
"v_mfma_f32_16x16x32_bf16 v[28:31], a[228:231], v[40:43], v[28:31]\n"
"ds_read_b128 v[32:35], v4 offset:2048\n"
"v_mfma_f32_16x16x32_bf16 v[28:31], v[148:151], v[44:47], v[28:31]\n"
"s_waitcnt lgkmcnt(2)\n"
"v_add_f32 v16, v16, v12\n"
"v_mfma_f32_16x16x32_bf16 v[24:27], v[144:147], v[44:47], v[24:27]\n"
"ds_read_b128 v[36:39], v4 offset:16384\n"
"v_add_f32 v17, v17, v13\n"
"v_mfma_f32_16x16x32_bf16 v[24:27], a[224:227], v[40:43], v[24:27]\n"
"v_add_f32 v18, v18, v14\n"
"v_mfma_f32_16x16x32_bf16 v[24:27], a[8:11], v[48:51], v[24:27]\n"
"ds_read_b128 v[40:43], v4 offset:30720\n"
"v_add_f32 v19, v19, v15\n"
"v_mfma_f32_16x16x32_bf16 v[24:27], a[120:123], v[52:55], v[24:27]\n"
"ds_read_b128 v[12:15], v5 offset:1024\n"
"v_mfma_f32_16x16x32_bf16 v[28:31], a[124:127], v[52:55], v[28:31]\n"
"ds_read_b128 v[44:47], v4 offset:45056\n"
"v_mfma_f32_16x16x32_bf16 v[28:31], a[12:15], v[48:51], v[28:31]\n"
"s_waitcnt lgkmcnt(5)\n"
"v_mfma_f32_16x16x32_bf16 v[28:31], a[236:239], v[56:59], v[28:31]\n"
"ds_read_b128 v[48:51], v4 offset:3072\n"
"v_mfma_f32_16x16x32_bf16 v[28:31], v[156:159], v[60:63], v[28:31]\n"
"v_mfma_f32_16x16x32_bf16 v[24:27], v[152:155], v[60:63], v[24:27]\n"
"ds_read_b128 v[52:55], v4 offset:17408\n"
"s_waitcnt vmcnt(5)\n"
"v_mfma_f32_16x16x32_bf16 v[24:27], a[232:235], v[56:59], v[24:27]\n"
"v_cmp_ne_u64 vcc, v[8:9], v[6:7]\n"
"s_waitcnt lgkmcnt(5)\n"
"v_mfma_f32_16x16x32_bf16 v[24:27], a[16:19], v[32:35], v[24:27]\n"
"ds_read_b128 v[56:59], v4 offset:31744\n"
"s_cbranch_vccnz .L_rta1_1\n"
".L_bka1_1:\n"
"v_mfma_f32_16x16x32_bf16 v[24:27], a[128:131], v[36:39], v[24:27]\n"
"s_add_u32 s66, s66, 1187840\n"
"s_waitcnt lgkmcnt(4)\n"
"v_mfma_f32_16x16x32_bf16 v[28:31], a[132:135], v[36:39], v[28:31]\n"
"s_waitcnt vmcnt(0)\n"
"s_barrier\n"
"v_mfma_f32_16x16x32_bf16 v[28:31], a[20:23], v[32:35], v[28:31]\n"
"ds_read_b128 v[60:63], v4 offset:46080\n"
"s_waitcnt lgkmcnt(4)\n"
"v_mfma_f32_16x16x32_bf16 v[28:31], a[244:247], v[40:43], v[28:31]\n"
"ds_read_b128 v[32:35], v4 offset:4096\n"
"s_add_u32 m0, s64, 0\n"
"v_mfma_f32_16x16x32_bf16 v[28:31], v[164:167], v[44:47], v[28:31]\n"
"buffer_load_dwordx4 v2, s[32:35], s63 offen offset:0 sc1 lds\n"
"v_add_f32 v20, v20, v12\n"
"v_mfma_f32_16x16x32_bf16 v[24:27], v[160:163], v[44:47], v[24:27]\n"
"ds_read_b128 v[36:39], v4 offset:18432\n"
"v_add_f32 v21, v21, v13\n"
"v_mfma_f32_16x16x32_bf16 v[24:27], a[240:243], v[40:43], v[24:27]\n"
"v_add_f32 v22, v22, v14\n"
"s_waitcnt lgkmcnt(4)\n"
"v_mfma_f32_16x16x32_bf16 v[24:27], a[24:27], v[48:51], v[24:27]\n"
"ds_read_b128 v[40:43], v4 offset:32768\n"
"v_add_f32 v23, v23, v15\n"
"v_mfma_f32_16x16x32_bf16 v[24:27], a[136:139], v[52:55], v[24:27]\n"
"v_cvt_pk_bf16_f32 v12, v16, v17\n"
"v_cvt_pk_bf16_f32 v13, v18, v19\n"
"v_mfma_f32_16x16x32_bf16 v[28:31], a[140:143], v[52:55], v[28:31]\n"
"ds_read_b128 v[44:47], v4 offset:47104\n"
"v_cvt_pk_bf16_f32 v14, v20, v21\n"
"v_mfma_f32_16x16x32_bf16 v[28:31], a[28:31], v[48:51], v[28:31]\n"
"v_cvt_pk_bf16_f32 v15, v22, v23\n"
"s_waitcnt lgkmcnt(4)\n"
"v_mfma_f32_16x16x32_bf16 v[28:31], a[252:255], v[56:59], v[28:31]\n"
"ds_read_b128 v[48:51], v4 offset:5120\n"
"v_mfma_f32_16x16x32_bf16 v[28:31], v[172:175], v[60:63], v[28:31]\n"
"buffer_store_dwordx4 v[12:15], v2, s[32:35], s69 offen sc1\n"
"v_add_f32 v10, v16, v17\n"
"v_mfma_f32_16x16x32_bf16 v[24:27], v[168:171], v[60:63], v[24:27]\n"
"ds_read_b128 v[52:55], v4 offset:19456\n"
"v_mul_f32 v11, v16, v16\n"
"v_mfma_f32_16x16x32_bf16 v[24:27], a[248:251], v[56:59], v[24:27]\n"
"v_fmac_f32 v11, v17, v17\n"
"s_waitcnt lgkmcnt(4)\n"
"v_mfma_f32_16x16x32_bf16 v[24:27], a[32:35], v[32:35], v[24:27]\n"
"ds_read_b128 v[56:59], v4 offset:33792\n"
"v_add_f32 v10, v10, v18\n"
"v_mfma_f32_16x16x32_bf16 v[24:27], a[144:147], v[36:39], v[24:27]\n"
"buffer_load_dwordx4 v2, s[32:35], s63 offen offset:1024 sc1 lds\n"
"v_fmac_f32 v11, v18, v18\n"
"v_mfma_f32_16x16x32_bf16 v[28:31], a[148:151], v[36:39], v[28:31]\n"
"ds_read_b128 v[60:63], v4 offset:48128\n"
"v_add_f32 v10, v10, v19\n"
"v_mfma_f32_16x16x32_bf16 v[28:31], a[36:39], v[32:35], v[28:31]\n"
"v_fmac_f32 v11, v19, v19\n"
"s_waitcnt lgkmcnt(4)\n"
"v_mfma_f32_16x16x32_bf16 v[28:31], v[68:71], v[40:43], v[28:31]\n"
"ds_read_b128 v[32:35], v4 offset:6144\n"
"v_add_f32 v10, v10, v20\n"
"v_mfma_f32_16x16x32_bf16 v[28:31], v[180:183], v[44:47], v[28:31]\n"
"buffer_load_dwordx4 v2, s[32:35], s63 offen offset:2048 sc1 lds\n"
"v_fmac_f32 v11, v20, v20\n"
"v_mfma_f32_16x16x32_bf16 v[24:27], v[176:179], v[44:47], v[24:27]\n"
"ds_read_b128 v[36:39], v4 offset:20480\n"
"v_add_f32 v10, v10, v21\n"
"v_mfma_f32_16x16x32_bf16 v[24:27], v[64:67], v[40:43], v[24:27]\n"
"v_fmac_f32 v11, v21, v21\n"
"s_waitcnt lgkmcnt(4)\n"
"v_mfma_f32_16x16x32_bf16 v[24:27], a[40:43], v[48:51], v[24:27]\n"
"ds_read_b128 v[40:43], v4 offset:34816\n"
"v_add_f32 v10, v10, v22\n"
"v_mfma_f32_16x16x32_bf16 v[24:27], a[152:155], v[52:55], v[24:27]\n"
"buffer_load_dwordx4 v2, s[32:35], s63 offen offset:3072 sc1 lds\n"
"s_add_u32 m0, s74, 8192\n"
"v_mfma_f32_16x16x32_bf16 v[28:31], a[156:159], v[52:55], v[28:31]\n"
"ds_read_b128 v[44:47], v4 offset:49152\n"
"v_fmac_f32 v11, v22, v22\n"
"v_mfma_f32_16x16x32_bf16 v[28:31], a[44:47], v[48:51], v[28:31]\n"
"v_add_f32 v10, v10, v23\n"
"s_waitcnt lgkmcnt(4)\n"
"v_mfma_f32_16x16x32_bf16 v[28:31], v[76:79], v[56:59], v[28:31]\n"
"ds_read_b128 v[48:51], v4 offset:7168\n"
"v_fmac_f32 v11, v23, v23\n"
"v_mfma_f32_16x16x32_bf16 v[28:31], v[188:191], v[60:63], v[28:31]\n"
"buffer_load_dwordx4 v2, s[32:35], s71 offen offset:0 sc1 lds\n"
"v_permlane16_swap_b32 v10, v11\n"
"v_mfma_f32_16x16x32_bf16 v[24:27], v[184:187], v[60:63], v[24:27]\n"
"ds_read_b128 v[52:55], v4 offset:21504\n"
"v_add_f32 v10, v10, v11\n"
"v_mfma_f32_16x16x32_bf16 v[24:27], v[72:75], v[56:59], v[24:27]\n"
"s_waitcnt lgkmcnt(4)\n"
"v_mfma_f32_16x16x32_bf16 v[24:27], a[48:51], v[32:35], v[24:27]\n"
"ds_read_b128 v[56:59], v4 offset:35840\n"
"v_permlane32_swap_b32 v10, v11\n"
"v_mfma_f32_16x16x32_bf16 v[24:27], a[160:163], v[36:39], v[24:27]\n"
"buffer_load_dwordx4 v2, s[32:35], s71 offen offset:1024 sc1 lds\n"
"s_add_u32 s71, s71, 1187840\n"
"v_mfma_f32_16x16x32_bf16 v[28:31], a[164:167], v[36:39], v[28:31]\n"
"ds_read_b128 v[60:63], v4 offset:50176\n"
"s_add_u32 s63, s63, 4096\n"
"v_mfma_f32_16x16x32_bf16 v[28:31], a[52:55], v[32:35], v[28:31]\n"
"s_add_u32 m0, s64, 4096\n"
"s_waitcnt lgkmcnt(4)\n"
"v_mfma_f32_16x16x32_bf16 v[28:31], v[84:87], v[40:43], v[28:31]\n"
"ds_read_b128 v[32:35], v4 offset:8192\n"
"v_add_f32 v10, v10, v11\n"
"v_mfma_f32_16x16x32_bf16 v[28:31], v[196:199], v[44:47], v[28:31]\n"
"buffer_store_dword v10, v3, s[32:35], s69 offen offset:1024 sc1\n"
"s_mov_b32 s67, s69\n"
"v_mfma_f32_16x16x32_bf16 v[24:27], v[192:195], v[44:47], v[24:27]\n"
"ds_read_b128 v[36:39], v4 offset:22528\n"
"s_mov_b32 s69, s70\n"
"v_mfma_f32_16x16x32_bf16 v[24:27], v[80:83], v[40:43], v[24:27]\n"
"s_add_u32 s70, s70, 1187840\n"
"s_waitcnt lgkmcnt(4)\n"
"v_mfma_f32_16x16x32_bf16 v[24:27], a[56:59], v[48:51], v[24:27]\n"
"ds_read_b128 v[40:43], v4 offset:36864\n"
"v_mfma_f32_16x16x32_bf16 v[24:27], a[168:171], v[52:55], v[24:27]\n"
"buffer_load_dwordx4 v2, s[32:35], s63 offen offset:0 sc1 lds\n"
"v_mfma_f32_16x16x32_bf16 v[28:31], a[172:175], v[52:55], v[28:31]\n"
"ds_read_b128 v[44:47], v4 offset:51200\n"
"v_mfma_f32_16x16x32_bf16 v[28:31], a[60:63], v[48:51], v[28:31]\n"
"s_waitcnt lgkmcnt(4)\n"
"v_mfma_f32_16x16x32_bf16 v[28:31], v[92:95], v[56:59], v[28:31]\n"
"ds_read_b128 v[48:51], v4 offset:9216\n"
"v_mfma_f32_16x16x32_bf16 v[28:31], v[204:207], v[60:63], v[28:31]\n"
"buffer_load_dwordx4 v2, s[32:35], s63 offen offset:1024 sc1 lds\n"
"v_mfma_f32_16x16x32_bf16 v[24:27], v[200:203], v[60:63], v[24:27]\n"
"ds_read_b128 v[52:55], v4 offset:23552\n"
"v_mfma_f32_16x16x32_bf16 v[24:27], v[88:91], v[56:59], v[24:27]\n"
"s_waitcnt lgkmcnt(4)\n"
"v_mfma_f32_16x16x32_bf16 v[24:27], a[64:67], v[32:35], v[24:27]\n"
"ds_read_b128 v[56:59], v4 offset:37888\n"
"v_mfma_f32_16x16x32_bf16 v[24:27], a[176:179], v[36:39], v[24:27]\n"
"buffer_load_dwordx4 v2, s[32:35], s63 offen offset:2048 sc1 lds\n"
"v_mfma_f32_16x16x32_bf16 v[28:31], a[180:183], v[36:39], v[28:31]\n"
"ds_read_b128 v[60:63], v4 offset:52224\n"
"v_mfma_f32_16x16x32_bf16 v[28:31], a[68:71], v[32:35], v[28:31]\n"
"s_waitcnt lgkmcnt(4)\n"
"v_mfma_f32_16x16x32_bf16 v[28:31], v[100:103], v[40:43], v[28:31]\n"
"ds_read_b128 v[32:35], v4 offset:10240\n"
"v_mfma_f32_16x16x32_bf16 v[28:31], v[212:215], v[44:47], v[28:31]\n"
"buffer_load_dwordx4 v2, s[32:35], s63 offen offset:3072 sc1 lds\n"
"s_add_u32 s63, s63, 4096\n"
"v_mfma_f32_16x16x32_bf16 v[24:27], v[208:211], v[44:47], v[24:27]\n"
"ds_read_b128 v[36:39], v4 offset:24576\n"
"s_add_u32 m0, s64, 8192\n"
"v_mfma_f32_16x16x32_bf16 v[24:27], v[96:99], v[40:43], v[24:27]\n"
"s_waitcnt lgkmcnt(4)\n"
"v_mfma_f32_16x16x32_bf16 v[24:27], a[72:75], v[48:51], v[24:27]\n"
"ds_read_b128 v[40:43], v4 offset:38912\n"
"v_mfma_f32_16x16x32_bf16 v[24:27], a[184:187], v[52:55], v[24:27]\n"
"buffer_load_dwordx4 v2, s[32:35], s63 offen offset:0 sc1 lds\n"
"v_mfma_f32_16x16x32_bf16 v[28:31], a[188:191], v[52:55], v[28:31]\n"
"ds_read_b128 v[44:47], v4 offset:53248\n"
"v_mfma_f32_16x16x32_bf16 v[28:31], a[76:79], v[48:51], v[28:31]\n"
"s_waitcnt lgkmcnt(4)\n"
"v_mfma_f32_16x16x32_bf16 v[28:31], v[108:111], v[56:59], v[28:31]\n"
"ds_read_b128 v[48:51], v4 offset:11264\n"
"v_mfma_f32_16x16x32_bf16 v[28:31], v[220:223], v[60:63], v[28:31]\n"
"buffer_load_dwordx2 v[6:7], v0, s[32:35], s66 offen sc1\n"
"v_mfma_f32_16x16x32_bf16 v[24:27], v[216:219], v[60:63], v[24:27]\n"
"ds_read_b128 v[52:55], v4 offset:25600\n"
"v_mfma_f32_16x16x32_bf16 v[24:27], v[104:107], v[56:59], v[24:27]\n"
"s_waitcnt lgkmcnt(4)\n"
"v_mfma_f32_16x16x32_bf16 v[24:27], a[80:83], v[32:35], v[24:27]\n"
"ds_read_b128 v[56:59], v4 offset:39936\n"
"v_mfma_f32_16x16x32_bf16 v[24:27], a[192:195], v[36:39], v[24:27]\n"
"buffer_load_dwordx4 v2, s[32:35], s63 offen offset:1024 sc1 lds\n"
"v_mfma_f32_16x16x32_bf16 v[28:31], a[196:199], v[36:39], v[28:31]\n"
"ds_read_b128 v[60:63], v4 offset:54272\n"
"v_mfma_f32_16x16x32_bf16 v[28:31], a[84:87], v[32:35], v[28:31]\n"
"s_waitcnt lgkmcnt(4)\n"
"v_mfma_f32_16x16x32_bf16 v[28:31], v[116:119], v[40:43], v[28:31]\n"
"ds_read_b128 v[32:35], v4 offset:12288\n"
"v_mfma_f32_16x16x32_bf16 v[28:31], v[228:231], v[44:47], v[28:31]\n"
"buffer_load_dwordx4 v2, s[32:35], s63 offen offset:2048 sc1 lds\n"
"v_mfma_f32_16x16x32_bf16 v[24:27], v[224:227], v[44:47], v[24:27]\n"
"ds_read_b128 v[36:39], v4 offset:26624\n"
"v_mfma_f32_16x16x32_bf16 v[24:27], v[112:115], v[40:43], v[24:27]\n"
"s_waitcnt lgkmcnt(4)\n"
"v_mfma_f32_16x16x32_bf16 v[24:27], a[88:91], v[48:51], v[24:27]\n"
"ds_read_b128 v[40:43], v4 offset:40960\n"
"v_mfma_f32_16x16x32_bf16 v[24:27], a[200:203], v[52:55], v[24:27]\n"
"buffer_load_dwordx4 v2, s[32:35], s63 offen offset:3072 sc1 lds\n"
"s_add_u32 s63, s63, 4096\n"
"v_mfma_f32_16x16x32_bf16 v[28:31], a[204:207], v[52:55], v[28:31]\n"
"ds_read_b128 v[44:47], v4 offset:55296\n"
"s_add_u32 m0, s64, 12288\n"
"v_mfma_f32_16x16x32_bf16 v[28:31], a[92:95], v[48:51], v[28:31]\n"
"s_waitcnt lgkmcnt(4)\n"
"v_mfma_f32_16x16x32_bf16 v[28:31], v[124:127], v[56:59], v[28:31]\n"
"ds_read_b128 v[48:51], v4 offset:13312\n"
"v_mfma_f32_16x16x32_bf16 v[28:31], v[236:239], v[60:63], v[28:31]\n"
"buffer_load_dwordx4 v2, s[32:35], s63 offen offset:0 sc1 lds\n"
"v_mfma_f32_16x16x32_bf16 v[24:27], v[232:235], v[60:63], v[24:27]\n"
"ds_read_b128 v[52:55], v4 offset:27648\n"
"v_mfma_f32_16x16x32_bf16 v[24:27], v[120:123], v[56:59], v[24:27]\n"
"s_waitcnt lgkmcnt(4)\n"
"v_mfma_f32_16x16x32_bf16 v[24:27], a[96:99], v[32:35], v[24:27]\n"
"ds_read_b128 v[56:59], v4 offset:41984\n"
"v_mfma_f32_16x16x32_bf16 v[24:27], a[208:211], v[36:39], v[24:27]\n"
"buffer_load_dwordx4 v2, s[32:35], s63 offen offset:1024 sc1 lds\n"
"s_add_u32 s63, s63, 1175552\n"
"v_mfma_f32_16x16x32_bf16 v[28:31], a[212:215], v[36:39], v[28:31]\n"
"s_waitcnt vmcnt(14)\n"
"s_waitcnt vmcnt(11)\n"
"s_barrier\n"
"v_mfma_f32_16x16x32_bf16 v[28:31], a[100:103], v[32:35], v[28:31]\n"
"ds_read_b128 v[60:63], v4 offset:56320\n"
"s_waitcnt lgkmcnt(4)\n"
"v_mfma_f32_16x16x32_bf16 v[28:31], v[132:135], v[40:43], v[28:31]\n"
"ds_read_b128 v[32:35], v2 offset:0\n"
"v_mfma_f32_16x16x32_bf16 v[28:31], v[244:247], v[44:47], v[28:31]\n"
"v_mfma_f32_16x16x32_bf16 v[24:27], v[240:243], v[44:47], v[24:27]\n"
"ds_read_b128 v[36:39], v2 offset:14336\n"
"v_mfma_f32_16x16x32_bf16 v[24:27], v[128:131], v[40:43], v[24:27]\n"
"s_waitcnt lgkmcnt(4)\n"
"v_mfma_f32_16x16x32_bf16 v[24:27], a[104:107], v[48:51], v[24:27]\n"
"ds_read_b128 v[40:43], v2 offset:28672\n"
"v_mfma_f32_16x16x32_bf16 v[24:27], a[216:219], v[52:55], v[24:27]\n"
"v_mfma_f32_16x16x32_bf16 v[28:31], a[220:223], v[52:55], v[28:31]\n"
"ds_read_b128 v[44:47], v2 offset:43008\n"
"v_mfma_f32_16x16x32_bf16 v[28:31], a[108:111], v[48:51], v[28:31]\n"
"s_waitcnt lgkmcnt(4)\n"
"v_mfma_f32_16x16x32_bf16 v[28:31], v[140:143], v[56:59], v[28:31]\n"
"ds_read_b128 v[48:51], v2 offset:1024\n"
"v_mfma_f32_16x16x32_bf16 v[28:31], v[252:255], v[60:63], v[28:31]\n"
"v_mfma_f32_16x16x32_bf16 v[24:27], v[248:251], v[60:63], v[24:27]\n"
"ds_read_b128 v[52:55], v2 offset:15360\n"
"s_cmp_eq_u32 s62, 0\n"
"v_mfma_f32_16x16x32_bf16 v[24:27], v[136:139], v[56:59], v[24:27]\n"
"s_cbranch_scc0 .L_dal_1\n"
"s_mov_b32 s48, 0x7fff0000\n"
"s_waitcnt vmcnt(5)\n"
"v_writelane_b32 v8, 0, 63\n"
"v_writelane_b32 v9, 0, 63\n"
"v_writelane_b32 v6, 0, 63\n"
"v_writelane_b32 v7, 0, 63\n"
"v_writelane_b32 v0, s48, 63\n"
"s_branch .L_dal_1\n"
".L_dab_1:\n"
"s_mov_b32 s62, 15\n"
"s_add_u32 s74, s74, 8192\n"
"v_lshrrev_b32 v5, 8, v2\n"
"v_lshl_add_u32 v5, v5, 4, s74\n"
"v_and_b32 v3, 128, v2\n"
"v_lshl_add_u32 v5, v3, 2, v5\n"
"v_and_b32 v3, 16, v2\n"
"v_lshl_add_u32 v5, v3, 4, v5\n"
"v_and_b32 v3, 96, v2\n"
"v_lshl_add_u32 v5, v3, 1, v5\n"
"v_lshrrev_b32 v3, 2, v2\n"
"v_add_u32 v3, s74, v3\n"
".p2align 6\n"
".L_dbl_1:\n"
"s_waitcnt lgkmcnt(4)\n"
"v_mfma_f32_16x16x32_bf16 v[24:27], a[0:3], v[32:35], 0\n"
"ds_read_b128 v[56:59], v4 offset:29696\n"
"v_mfma_f32_16x16x32_bf16 v[24:27], a[112:115], v[36:39], v[24:27]\n"
"v_mfma_f32_16x16x32_bf16 v[28:31], a[116:119], v[36:39], 0\n"
"ds_read_b128 v[60:63], v4 offset:44032\n"
"v_mfma_f32_16x16x32_bf16 v[28:31], a[4:7], v[32:35], v[28:31]\n"
"v_cvt_pk_bf16_f32 v15, v22, v23\n"
"s_waitcnt lgkmcnt(4)\n"
"v_mfma_f32_16x16x32_bf16 v[28:31], a[228:231], v[40:43], v[28:31]\n"
"ds_read_b128 v[32:35], v4 offset:2048\n"
"v_cvt_pk_bf16_f32 v14, v20, v21\n"
"v_mfma_f32_16x16x32_bf16 v[28:31], v[148:151], v[44:47], v[28:31]\n"
"v_cvt_pk_bf16_f32 v13, v18, v19\n"
"v_cvt_pk_bf16_f32 v12, v16, v17\n"
"v_mfma_f32_16x16x32_bf16 v[24:27], v[144:147], v[44:47], v[24:27]\n"
"ds_read_b128 v[36:39], v4 offset:16384\n"
"ds_write_b128 v5, v[12:15]\n"
"v_mfma_f32_16x16x32_bf16 v[24:27], a[224:227], v[40:43], v[24:27]\n"
"ds_read2st64_b32 v[12:13], v3 offset0:0 offset1:1\n"
"s_waitcnt lgkmcnt(6)\n"
"v_mfma_f32_16x16x32_bf16 v[24:27], a[8:11], v[48:51], v[24:27]\n"
"ds_read_b128 v[40:43], v4 offset:30720\n"
"ds_read2st64_b32 v[14:15], v3 offset0:2 offset1:3\n"
"v_mfma_f32_16x16x32_bf16 v[24:27], a[120:123], v[52:55], v[24:27]\n"
"v_mfma_f32_16x16x32_bf16 v[28:31], a[124:127], v[52:55], v[28:31]\n"
"ds_read_b128 v[44:47], v4 offset:45056\n"
"v_mfma_f32_16x16x32_bf16 v[28:31], a[12:15], v[48:51], v[28:31]\n"
"s_waitcnt lgkmcnt(7)\n"
"v_mfma_f32_16x16x32_bf16 v[28:31], a[236:239], v[56:59], v[28:31]\n"
"ds_read_b128 v[48:51], v4 offset:3072\n"
"v_mfma_f32_16x16x32_bf16 v[28:31], v[156:159], v[60:63], v[28:31]\n"
"s_waitcnt lgkmcnt(2)\n"
"buffer_atomic_pk_add_bf16 v12, v1, s[44:47], s65 offen offset:0 sc1\n"
"v_mfma_f32_16x16x32_bf16 v[24:27], v[152:155], v[60:63], v[24:27]\n"
"ds_read_b128 v[52:55], v4 offset:17408\n"
"s_waitcnt vmcnt(4)\n"
"v_mfma_f32_16x16x32_bf16 v[24:27], a[232:235], v[56:59], v[24:27]\n"
"buffer_atomic_pk_add_bf16 v13, v1, s[44:47], s65 offen offset:2048 sc1\n"
"v_mfma_f32_16x16x32_bf16 v[24:27], a[16:19], v[32:35], v[24:27]\n"
"ds_read_b128 v[56:59], v4 offset:31744\n"
"v_cmp_ne_u64 vcc, v[8:9], v[6:7]\n"
"v_mfma_f32_16x16x32_bf16 v[24:27], a[128:131], v[36:39], v[24:27]\n"
"s_cbranch_vccnz .L_rtb1_1\n"
".L_bkb1_1:\n"
"s_add_u32 s66, s66, 1187840\n"
"v_mfma_f32_16x16x32_bf16 v[28:31], a[132:135], v[36:39], v[28:31]\n"
"s_waitcnt vmcnt(2)\n"
"s_barrier\n"
"v_mfma_f32_16x16x32_bf16 v[28:31], a[20:23], v[32:35], v[28:31]\n"
"ds_read_b128 v[60:63], v4 offset:46080\n"
"s_waitcnt lgkmcnt(4)\n"
"v_mfma_f32_16x16x32_bf16 v[28:31], a[244:247], v[40:43], v[28:31]\n"
"ds_read_b128 v[32:35], v4 offset:4096\n"
"s_add_u32 m0, s64, 0\n"
"v_mfma_f32_16x16x32_bf16 v[28:31], v[164:167], v[44:47], v[28:31]\n"
"buffer_load_dwordx4 v2, s[32:35], s63 offen offset:0 sc1 lds\n"
"s_add_u32 s65, s65, 16384\n"
"v_mfma_f32_16x16x32_bf16 v[24:27], v[160:163], v[44:47], v[24:27]\n"
"ds_read_b128 v[36:39], v4 offset:18432\n"
"v_mfma_f32_16x16x32_bf16 v[24:27], a[240:243], v[40:43], v[24:27]\n"
"buffer_atomic_pk_add_bf16 v14, v1, s[44:47], s65 offen offset:0 sc1\n"
"s_waitcnt lgkmcnt(4)\n"
"v_mfma_f32_16x16x32_bf16 v[24:27], a[24:27], v[48:51], v[24:27]\n"
"ds_read_b128 v[40:43], v4 offset:32768\n"
"v_mfma_f32_16x16x32_bf16 v[24:27], a[136:139], v[52:55], v[24:27]\n"
"buffer_atomic_pk_add_bf16 v15, v1, s[44:47], s65 offen offset:2048 sc1\n"
"s_add_u32 s65, s65, 16384\n"
"v_mfma_f32_16x16x32_bf16 v[28:31], a[140:143], v[52:55], v[28:31]\n"
"ds_read_b128 v[44:47], v4 offset:47104\n"
"v_mfma_f32_16x16x32_bf16 v[28:31], a[28:31], v[48:51], v[28:31]\n"
"s_waitcnt lgkmcnt(4)\n"
"v_mfma_f32_16x16x32_bf16 v[28:31], a[252:255], v[56:59], v[28:31]\n"
"ds_read_b128 v[48:51], v4 offset:5120\n"
"v_mfma_f32_16x16x32_bf16 v[28:31], v[172:175], v[60:63], v[28:31]\n"
"buffer_load_dwordx4 v2, s[32:35], s63 offen offset:1024 sc1 lds\n"
"v_mfma_f32_16x16x32_bf16 v[24:27], v[168:171], v[60:63], v[24:27]\n"
"ds_read_b128 v[52:55], v4 offset:19456\n"
"v_mfma_f32_16x16x32_bf16 v[24:27], a[248:251], v[56:59], v[24:27]\n"
"s_waitcnt lgkmcnt(4)\n"
"v_mfma_f32_16x16x32_bf16 v[24:27], a[32:35], v[32:35], v[24:27]\n"
"ds_read_b128 v[56:59], v4 offset:33792\n"
"v_mfma_f32_16x16x32_bf16 v[24:27], a[144:147], v[36:39], v[24:27]\n"
"buffer_load_dwordx4 v2, s[32:35], s63 offen offset:2048 sc1 lds\n"
"v_mfma_f32_16x16x32_bf16 v[28:31], a[148:151], v[36:39], v[28:31]\n"
"ds_read_b128 v[60:63], v4 offset:48128\n"
"v_mfma_f32_16x16x32_bf16 v[28:31], a[36:39], v[32:35], v[28:31]\n"
"s_waitcnt lgkmcnt(4)\n"
"v_mfma_f32_16x16x32_bf16 v[28:31], v[68:71], v[40:43], v[28:31]\n"
"ds_read_b128 v[32:35], v4 offset:6144\n"
"v_mfma_f32_16x16x32_bf16 v[28:31], v[180:183], v[44:47], v[28:31]\n"
"buffer_load_dwordx4 v2, s[32:35], s63 offen offset:3072 sc1 lds\n"
"v_mfma_f32_16x16x32_bf16 v[24:27], v[176:179], v[44:47], v[24:27]\n"
"ds_read_b128 v[36:39], v4 offset:20480\n"
"v_mfma_f32_16x16x32_bf16 v[24:27], v[64:67], v[40:43], v[24:27]\n"
"s_waitcnt lgkmcnt(4)\n"
"v_mfma_f32_16x16x32_bf16 v[24:27], a[40:43], v[48:51], v[24:27]\n"
"ds_read_b128 v[40:43], v4 offset:34816\n"
"s_add_u32 s63, s63, 4096\n"
"v_mfma_f32_16x16x32_bf16 v[24:27], a[152:155], v[52:55], v[24:27]\n"
"s_add_u32 m0, s64, 4096\n"
"v_mfma_f32_16x16x32_bf16 v[28:31], a[156:159], v[52:55], v[28:31]\n"
"ds_read_b128 v[44:47], v4 offset:49152\n"
"v_mfma_f32_16x16x32_bf16 v[28:31], a[44:47], v[48:51], v[28:31]\n"
"s_waitcnt lgkmcnt(4)\n"
"v_mfma_f32_16x16x32_bf16 v[28:31], v[76:79], v[56:59], v[28:31]\n"
"ds_read_b128 v[48:51], v4 offset:7168\n"
"v_mfma_f32_16x16x32_bf16 v[28:31], v[188:191], v[60:63], v[28:31]\n"
"buffer_load_dwordx4 v2, s[32:35], s63 offen offset:0 sc1 lds\n"
"v_mfma_f32_16x16x32_bf16 v[24:27], v[184:187], v[60:63], v[24:27]\n"
"ds_read_b128 v[52:55], v4 offset:21504\n"
"v_mfma_f32_16x16x32_bf16 v[24:27], v[72:75], v[56:59], v[24:27]\n"
"s_waitcnt lgkmcnt(4)\n"
"v_mfma_f32_16x16x32_bf16 v[24:27], a[48:51], v[32:35], v[24:27]\n"
"ds_read_b128 v[56:59], v4 offset:35840\n"
"v_mfma_f32_16x16x32_bf16 v[24:27], a[160:163], v[36:39], v[24:27]\n"
"buffer_load_dwordx4 v2, s[32:35], s63 offen offset:1024 sc1 lds\n"
"v_mfma_f32_16x16x32_bf16 v[28:31], a[164:167], v[36:39], v[28:31]\n"
"ds_read_b128 v[60:63], v4 offset:50176\n"
"v_mfma_f32_16x16x32_bf16 v[28:31], a[52:55], v[32:35], v[28:31]\n"
"s_waitcnt lgkmcnt(4)\n"
"v_mfma_f32_16x16x32_bf16 v[28:31], v[84:87], v[40:43], v[28:31]\n"
"ds_read_b128 v[32:35], v4 offset:8192\n"
"v_mfma_f32_16x16x32_bf16 v[28:31], v[196:199], v[44:47], v[28:31]\n"
"buffer_load_dwordx4 v2, s[32:35], s63 offen offset:2048 sc1 lds\n"
"v_mfma_f32_16x16x32_bf16 v[24:27], v[192:195], v[44:47], v[24:27]\n"
"ds_read_b128 v[36:39], v4 offset:22528\n"
"v_mfma_f32_16x16x32_bf16 v[24:27], v[80:83], v[40:43], v[24:27]\n"
"s_waitcnt lgkmcnt(4)\n"
"v_mfma_f32_16x16x32_bf16 v[24:27], a[56:59], v[48:51], v[24:27]\n"
"ds_read_b128 v[40:43], v4 offset:36864\n"
"v_mfma_f32_16x16x32_bf16 v[24:27], a[168:171], v[52:55], v[24:27]\n"
"buffer_load_dwordx4 v2, s[32:35], s63 offen offset:3072 sc1 lds\n"
"v_mfma_f32_16x16x32_bf16 v[28:31], a[172:175], v[52:55], v[28:31]\n"
"ds_read_b128 v[44:47], v4 offset:51200\n"
"v_mfma_f32_16x16x32_bf16 v[28:31], a[60:63], v[48:51], v[28:31]\n"
"s_waitcnt lgkmcnt(4)\n"
"v_mfma_f32_16x16x32_bf16 v[28:31], v[92:95], v[56:59], v[28:31]\n"
"ds_read_b128 v[48:51], v4 offset:9216\n"
"v_mfma_f32_16x16x32_bf16 v[28:31], v[204:207], v[60:63], v[28:31]\n"
"s_add_u32 s63, s63, 4096\n"
"s_add_u32 m0, s64, 8192\n"
"v_mfma_f32_16x16x32_bf16 v[24:27], v[200:203], v[60:63], v[24:27]\n"
"ds_read_b128 v[52:55], v4 offset:23552\n"
"v_mfma_f32_16x16x32_bf16 v[24:27], v[88:91], v[56:59], v[24:27]\n"
"s_waitcnt lgkmcnt(4)\n"
"v_mfma_f32_16x16x32_bf16 v[24:27], a[64:67], v[32:35], v[24:27]\n"
"ds_read_b128 v[56:59], v4 offset:37888\n"
"v_mfma_f32_16x16x32_bf16 v[24:27], a[176:179], v[36:39], v[24:27]\n"
"buffer_load_dwordx4 v2, s[32:35], s63 offen offset:0 sc1 lds\n"
"v_mfma_f32_16x16x32_bf16 v[28:31], a[180:183], v[36:39], v[28:31]\n"
"ds_read_b128 v[60:63], v4 offset:52224\n"
"v_mfma_f32_16x16x32_bf16 v[28:31], a[68:71], v[32:35], v[28:31]\n"
"s_waitcnt lgkmcnt(4)\n"
"v_mfma_f32_16x16x32_bf16 v[28:31], v[100:103], v[40:43], v[28:31]\n"
"ds_read_b128 v[32:35], v4 offset:10240\n"
"v_mfma_f32_16x16x32_bf16 v[28:31], v[212:215], v[44:47], v[28:31]\n"
"buffer_load_dwordx4 v2, s[32:35], s63 offen offset:1024 sc1 lds\n"
"v_mfma_f32_16x16x32_bf16 v[24:27], v[208:211], v[44:47], v[24:27]\n"
"ds_read_b128 v[36:39], v4 offset:24576\n"
"v_mfma_f32_16x16x32_bf16 v[24:27], v[96:99], v[40:43], v[24:27]\n"
"s_waitcnt lgkmcnt(4)\n"
"v_mfma_f32_16x16x32_bf16 v[24:27], a[72:75], v[48:51], v[24:27]\n"
"ds_read_b128 v[40:43], v4 offset:38912\n"
"v_mfma_f32_16x16x32_bf16 v[24:27], a[184:187], v[52:55], v[24:27]\n"
"buffer_load_dwordx4 v2, s[32:35], s63 offen offset:2048 sc1 lds\n"
"v_mfma_f32_16x16x32_bf16 v[28:31], a[188:191], v[52:55], v[28:31]\n"
"ds_read_b128 v[44:47], v4 offset:53248\n"
"v_mfma_f32_16x16x32_bf16 v[28:31], a[76:79], v[48:51], v[28:31]\n"
"s_waitcnt lgkmcnt(4)\n"
"v_mfma_f32_16x16x32_bf16 v[28:31], v[108:111], v[56:59], v[28:31]\n"
"ds_read_b128 v[48:51], v4 offset:11264\n"
"v_mfma_f32_16x16x32_bf16 v[28:31], v[220:223], v[60:63], v[28:31]\n"
"buffer_load_dwordx2 v[6:7], v0, s[32:35], s66 offen sc1\n"
"v_mfma_f32_16x16x32_bf16 v[24:27], v[216:219], v[60:63], v[24:27]\n"
"ds_read_b128 v[52:55], v4 offset:25600\n"
"v_mfma_f32_16x16x32_bf16 v[24:27], v[104:107], v[56:59], v[24:27]\n"
"s_waitcnt lgkmcnt(4)\n"
"v_mfma_f32_16x16x32_bf16 v[24:27], a[80:83], v[32:35], v[24:27]\n"
"ds_read_b128 v[56:59], v4 offset:39936\n"
"v_mfma_f32_16x16x32_bf16 v[24:27], a[192:195], v[36:39], v[24:27]\n"
"buffer_load_dwordx4 v2, s[32:35], s63 offen offset:3072 sc1 lds\n"
"s_add_u32 s63, s63, 4096\n"
"v_mfma_f32_16x16x32_bf16 v[28:31], a[196:199], v[36:39], v[28:31]\n"
"ds_read_b128 v[60:63], v4 offset:54272\n"
"s_add_u32 m0, s64, 12288\n"
"v_mfma_f32_16x16x32_bf16 v[28:31], a[84:87], v[32:35], v[28:31]\n"
"s_waitcnt lgkmcnt(4)\n"
"v_mfma_f32_16x16x32_bf16 v[28:31], v[116:119], v[40:43], v[28:31]\n"
"ds_read_b128 v[32:35], v4 offset:12288\n"
"v_mfma_f32_16x16x32_bf16 v[28:31], v[228:231], v[44:47], v[28:31]\n"
"buffer_load_dwordx4 v2, s[32:35], s63 offen offset:0 sc1 lds\n"
"v_mfma_f32_16x16x32_bf16 v[24:27], v[224:227], v[44:47], v[24:27]\n"
"ds_read_b128 v[36:39], v4 offset:26624\n"
"v_mfma_f32_16x16x32_bf16 v[24:27], v[112:115], v[40:43], v[24:27]\n"
"s_waitcnt lgkmcnt(4)\n"
"v_mfma_f32_16x16x32_bf16 v[24:27], a[88:91], v[48:51], v[24:27]\n"
"ds_read_b128 v[40:43], v4 offset:40960\n"
"v_mfma_f32_16x16x32_bf16 v[24:27], a[200:203], v[52:55], v[24:27]\n"
"buffer_load_dwordx4 v2, s[32:35], s63 offen offset:1024 sc1 lds\n"
"s_add_u32 s63, s63, 1175552\n"
"v_mfma_f32_16x16x32_bf16 v[28:31], a[204:207], v[52:55], v[28:31]\n"
"ds_read_b128 v[44:47], v4 offset:55296\n"
"v_mfma_f32_16x16x32_bf16 v[28:31], a[92:95], v[48:51], v[28:31]\n"
"s_waitcnt lgkmcnt(4)\n"
"v_mfma_f32_16x16x32_bf16 v[28:31], v[124:127], v[56:59], v[28:31]\n"
"ds_read_b128 v[48:51], v4 offset:13312\n"
"v_mfma_f32_16x16x32_bf16 v[28:31], v[236:239], v[60:63], v[28:31]\n"
"v_mfma_f32_16x16x32_bf16 v[24:27], v[232:235], v[60:63], v[24:27]\n"
"ds_read_b128 v[52:55], v4 offset:27648\n"
"v_mfma_f32_16x16x32_bf16 v[24:27], v[120:123], v[56:59], v[24:27]\n"
"s_waitcnt lgkmcnt(4)\n"
"v_mfma_f32_16x16x32_bf16 v[24:27], a[96:99], v[32:35], v[24:27]\n"
"ds_read_b128 v[56:59], v4 offset:41984\n"
"v_mfma_f32_16x16x32_bf16 v[24:27], a[208:211], v[36:39], v[24:27]\n"
"v_mfma_f32_16x16x32_bf16 v[28:31], a[212:215], v[36:39], v[28:31]\n"
"s_waitcnt vmcnt(11)\n"
"s_barrier\n"
"v_mfma_f32_16x16x32_bf16 v[28:31], a[100:103], v[32:35], v[28:31]\n"
"ds_read_b128 v[60:63], v4 offset:56320\n"
"s_waitcnt lgkmcnt(4)\n"
"v_mfma_f32_16x16x32_bf16 v[28:31], v[132:135], v[40:43], v[28:31]\n"
"ds_read_b128 v[32:35], v2 offset:0\n"
"v_mfma_f32_16x16x32_bf16 v[28:31], v[244:247], v[44:47], v[28:31]\n"
"v_mfma_f32_16x16x32_bf16 v[24:27], v[240:243], v[44:47], v[24:27]\n"
"ds_read_b128 v[36:39], v2 offset:14336\n"
"v_mfma_f32_16x16x32_bf16 v[24:27], v[128:131], v[40:43], v[24:27]\n"
"s_waitcnt lgkmcnt(4)\n"
"v_mfma_f32_16x16x32_bf16 v[24:27], a[104:107], v[48:51], v[24:27]\n"
"ds_read_b128 v[40:43], v2 offset:28672\n"
"v_mfma_f32_16x16x32_bf16 v[24:27], a[216:219], v[52:55], v[24:27]\n"
"v_mfma_f32_16x16x32_bf16 v[28:31], a[220:223], v[52:55], v[28:31]\n"
"ds_read_b128 v[44:47], v2 offset:43008\n"
"v_mfma_f32_16x16x32_bf16 v[28:31], a[108:111], v[48:51], v[28:31]\n"
"s_waitcnt lgkmcnt(4)\n"
"v_mfma_f32_16x16x32_bf16 v[28:31], v[140:143], v[56:59], v[28:31]\n"
"ds_read_b128 v[48:51], v2 offset:1024\n"
"v_mfma_f32_16x16x32_bf16 v[28:31], v[252:255], v[60:63], v[28:31]\n"
"v_mfma_f32_16x16x32_bf16 v[24:27], v[248:251], v[60:63], v[24:27]\n"
"ds_read_b128 v[52:55], v2 offset:15360\n"
"s_sub_u32 s62, s62, 1\n"
"v_mfma_f32_16x16x32_bf16 v[24:27], v[136:139], v[56:59], v[24:27]\n"
"s_cbranch_scc1 .L_dbt_1\n"
"s_waitcnt lgkmcnt(4)\n"
"v_mfma_f32_16x16x32_bf16 v[16:19], a[0:3], v[32:35], 0\n"
"ds_read_b128 v[56:59], v2 offset:29696\n"
"v_mfma_f32_16x16x32_bf16 v[16:19], a[112:115], v[36:39], v[16:19]\n"
"v_mfma_f32_16x16x32_bf16 v[20:23], a[116:119], v[36:39], 0\n"
"ds_read_b128 v[60:63], v2 offset:44032\n"
"v_mfma_f32_16x16x32_bf16 v[20:23], a[4:7], v[32:35], v[20:23]\n"
"v_cvt_pk_bf16_f32 v15, v30, v31\n"
"s_waitcnt lgkmcnt(4)\n"
"v_mfma_f32_16x16x32_bf16 v[20:23], a[228:231], v[40:43], v[20:23]\n"
"ds_read_b128 v[32:35], v2 offset:2048\n"
"v_cvt_pk_bf16_f32 v14, v28, v29\n"
"v_mfma_f32_16x16x32_bf16 v[20:23], v[148:151], v[44:47], v[20:23]\n"
"v_cvt_pk_bf16_f32 v13, v26, v27\n"
"v_cvt_pk_bf16_f32 v12, v24, v25\n"
"v_mfma_f32_16x16x32_bf16 v[16:19], v[144:147], v[44:47], v[16:19]\n"
"ds_read_b128 v[36:39], v2 offset:16384\n"
"ds_write_b128 v5, v[12:15]\n"
"v_mfma_f32_16x16x32_bf16 v[16:19], a[224:227], v[40:43], v[16:19]\n"
"ds_read2st64_b32 v[12:13], v3 offset0:0 offset1:1\n"
"s_waitcnt lgkmcnt(6)\n"
"v_mfma_f32_16x16x32_bf16 v[16:19], a[8:11], v[48:51], v[16:19]\n"
"ds_read_b128 v[40:43], v2 offset:30720\n"
"ds_read2st64_b32 v[14:15], v3 offset0:2 offset1:3\n"
"v_mfma_f32_16x16x32_bf16 v[16:19], a[120:123], v[52:55], v[16:19]\n"
"v_mfma_f32_16x16x32_bf16 v[20:23], a[124:127], v[52:55], v[20:23]\n"
"ds_read_b128 v[44:47], v2 offset:45056\n"
"v_mfma_f32_16x16x32_bf16 v[20:23], a[12:15], v[48:51], v[20:23]\n"
"s_waitcnt lgkmcnt(7)\n"
"v_mfma_f32_16x16x32_bf16 v[20:23], a[236:239], v[56:59], v[20:23]\n"
"ds_read_b128 v[48:51], v2 offset:3072\n"
"v_mfma_f32_16x16x32_bf16 v[20:23], v[156:159], v[60:63], v[20:23]\n"
"s_waitcnt lgkmcnt(2)\n"
"buffer_atomic_pk_add_bf16 v12, v1, s[44:47], s65 offen offset:0 sc1\n"
"v_mfma_f32_16x16x32_bf16 v[16:19], v[152:155], v[60:63], v[16:19]\n"
"ds_read_b128 v[52:55], v2 offset:17408\n"
"s_waitcnt vmcnt(4)\n"
"v_mfma_f32_16x16x32_bf16 v[16:19], a[232:235], v[56:59], v[16:19]\n"
"buffer_atomic_pk_add_bf16 v13, v1, s[44:47], s65 offen offset:2048 sc1\n"
"v_mfma_f32_16x16x32_bf16 v[16:19], a[16:19], v[32:35], v[16:19]\n"
"ds_read_b128 v[56:59], v2 offset:31744\n"
"v_cmp_ne_u64 vcc, v[8:9], v[6:7]\n"
"v_mfma_f32_16x16x32_bf16 v[16:19], a[128:131], v[36:39], v[16:19]\n"
"s_cbranch_vccnz .L_rtb0_1\n"
".L_bkb0_1:\n"
"s_add_u32 s66, s66, 1187840\n"
"v_mfma_f32_16x16x32_bf16 v[20:23], a[132:135], v[36:39], v[20:23]\n"
"s_waitcnt vmcnt(2)\n"
"s_barrier\n"
"v_mfma_f32_16x16x32_bf16 v[20:23], a[20:23], v[32:35], v[20:23]\n"
"ds_read_b128 v[60:63], v2 offset:46080\n"
"s_waitcnt lgkmcnt(4)\n"
"v_mfma_f32_16x16x32_bf16 v[20:23], a[244:247], v[40:43], v[20:23]\n"
"ds_read_b128 v[32:35], v2 offset:4096\n"
"s_add_u32 m0, s64, 57344\n"
"v_mfma_f32_16x16x32_bf16 v[20:23], v[164:167], v[44:47], v[20:23]\n"
"buffer_load_dwordx4 v2, s[32:35], s63 offen offset:0 sc1 lds\n"
"s_add_u32 s65, s65, 16384\n"
"v_mfma_f32_16x16x32_bf16 v[16:19], v[160:163], v[44:47], v[16:19]\n"
"ds_read_b128 v[36:39], v2 offset:18432\n"
"v_mfma_f32_16x16x32_bf16 v[16:19], a[240:243], v[40:43], v[16:19]\n"
"buffer_atomic_pk_add_bf16 v14, v1, s[44:47], s65 offen offset:0 sc1\n"
"s_waitcnt lgkmcnt(4)\n"
"v_mfma_f32_16x16x32_bf16 v[16:19], a[24:27], v[48:51], v[16:19]\n"
"ds_read_b128 v[40:43], v2 offset:32768\n"
"v_mfma_f32_16x16x32_bf16 v[16:19], a[136:139], v[52:55], v[16:19]\n"
"buffer_atomic_pk_add_bf16 v15, v1, s[44:47], s65 offen offset:2048 sc1\n"
"s_add_u32 s65, s65, 16384\n"
"v_mfma_f32_16x16x32_bf16 v[20:23], a[140:143], v[52:55], v[20:23]\n"
"ds_read_b128 v[44:47], v2 offset:47104\n"
"v_mfma_f32_16x16x32_bf16 v[20:23], a[28:31], v[48:51], v[20:23]\n"
"s_waitcnt lgkmcnt(4)\n"
"v_mfma_f32_16x16x32_bf16 v[20:23], a[252:255], v[56:59], v[20:23]\n"
"ds_read_b128 v[48:51], v2 offset:5120\n"
"v_mfma_f32_16x16x32_bf16 v[20:23], v[172:175], v[60:63], v[20:23]\n"
"buffer_load_dwordx4 v2, s[32:35], s63 offen offset:1024 sc1 lds\n"
"v_mfma_f32_16x16x32_bf16 v[16:19], v[168:171], v[60:63], v[16:19]\n"
"ds_read_b128 v[52:55], v2 offset:19456\n"
"v_mfma_f32_16x16x32_bf16 v[16:19], a[248:251], v[56:59], v[16:19]\n"
"s_waitcnt lgkmcnt(4)\n"
"v_mfma_f32_16x16x32_bf16 v[16:19], a[32:35], v[32:35], v[16:19]\n"
"ds_read_b128 v[56:59], v2 offset:33792\n"
"v_mfma_f32_16x16x32_bf16 v[16:19], a[144:147], v[36:39], v[16:19]\n"
"buffer_load_dwordx4 v2, s[32:35], s63 offen offset:2048 sc1 lds\n"
"v_mfma_f32_16x16x32_bf16 v[20:23], a[148:151], v[36:39], v[20:23]\n"
"ds_read_b128 v[60:63], v2 offset:48128\n"
"v_mfma_f32_16x16x32_bf16 v[20:23], a[36:39], v[32:35], v[20:23]\n"
"s_waitcnt lgkmcnt(4)\n"
"v_mfma_f32_16x16x32_bf16 v[20:23], v[68:71], v[40:43], v[20:23]\n"
"ds_read_b128 v[32:35], v2 offset:6144\n"
"v_mfma_f32_16x16x32_bf16 v[20:23], v[180:183], v[44:47], v[20:23]\n"
"buffer_load_dwordx4 v2, s[32:35], s63 offen offset:3072 sc1 lds\n"
"v_mfma_f32_16x16x32_bf16 v[16:19], v[176:179], v[44:47], v[16:19]\n"
"ds_read_b128 v[36:39], v2 offset:20480\n"
"v_mfma_f32_16x16x32_bf16 v[16:19], v[64:67], v[40:43], v[16:19]\n"
"s_waitcnt lgkmcnt(4)\n"
"v_mfma_f32_16x16x32_bf16 v[16:19], a[40:43], v[48:51], v[16:19]\n"
"ds_read_b128 v[40:43], v2 offset:34816\n"
"s_add_u32 s63, s63, 4096\n"
"v_mfma_f32_16x16x32_bf16 v[16:19], a[152:155], v[52:55], v[16:19]\n"
"s_add_u32 m0, s64, 61440\n"
"v_mfma_f32_16x16x32_bf16 v[20:23], a[156:159], v[52:55], v[20:23]\n"
"ds_read_b128 v[44:47], v2 offset:49152\n"
"v_mfma_f32_16x16x32_bf16 v[20:23], a[44:47], v[48:51], v[20:23]\n"
"s_waitcnt lgkmcnt(4)\n"
"v_mfma_f32_16x16x32_bf16 v[20:23], v[76:79], v[56:59], v[20:23]\n"
"ds_read_b128 v[48:51], v2 offset:7168\n"
"v_mfma_f32_16x16x32_bf16 v[20:23], v[188:191], v[60:63], v[20:23]\n"
"buffer_load_dwordx4 v2, s[32:35], s63 offen offset:0 sc1 lds\n"
"v_mfma_f32_16x16x32_bf16 v[16:19], v[184:187], v[60:63], v[16:19]\n"
"ds_read_b128 v[52:55], v2 offset:21504\n"
"v_mfma_f32_16x16x32_bf16 v[16:19], v[72:75], v[56:59], v[16:19]\n"
"s_waitcnt lgkmcnt(4)\n"
"v_mfma_f32_16x16x32_bf16 v[16:19], a[48:51], v[32:35], v[16:19]\n"
"ds_read_b128 v[56:59], v2 offset:35840\n"
"v_mfma_f32_16x16x32_bf16 v[16:19], a[160:163], v[36:39], v[16:19]\n"
"buffer_load_dwordx4 v2, s[32:35], s63 offen offset:1024 sc1 lds\n"
"v_mfma_f32_16x16x32_bf16 v[20:23], a[164:167], v[36:39], v[20:23]\n"
"ds_read_b128 v[60:63], v2 offset:50176\n"
"v_mfma_f32_16x16x32_bf16 v[20:23], a[52:55], v[32:35], v[20:23]\n"
"s_waitcnt lgkmcnt(4)\n"
"v_mfma_f32_16x16x32_bf16 v[20:23], v[84:87], v[40:43], v[20:23]\n"
"ds_read_b128 v[32:35], v2 offset:8192\n"
"v_mfma_f32_16x16x32_bf16 v[20:23], v[196:199], v[44:47], v[20:23]\n"
"buffer_load_dwordx4 v2, s[32:35], s63 offen offset:2048 sc1 lds\n"
"v_mfma_f32_16x16x32_bf16 v[16:19], v[192:195], v[44:47], v[16:19]\n"
"ds_read_b128 v[36:39], v2 offset:22528\n"
"v_mfma_f32_16x16x32_bf16 v[16:19], v[80:83], v[40:43], v[16:19]\n"
"s_waitcnt lgkmcnt(4)\n"
"v_mfma_f32_16x16x32_bf16 v[16:19], a[56:59], v[48:51], v[16:19]\n"
"ds_read_b128 v[40:43], v2 offset:36864\n"
"v_mfma_f32_16x16x32_bf16 v[16:19], a[168:171], v[52:55], v[16:19]\n"
"buffer_load_dwordx4 v2, s[32:35], s63 offen offset:3072 sc1 lds\n"
"v_mfma_f32_16x16x32_bf16 v[20:23], a[172:175], v[52:55], v[20:23]\n"
"ds_read_b128 v[44:47], v2 offset:51200\n"
"v_mfma_f32_16x16x32_bf16 v[20:23], a[60:63], v[48:51], v[20:23]\n"
"s_waitcnt lgkmcnt(4)\n"
"v_mfma_f32_16x16x32_bf16 v[20:23], v[92:95], v[56:59], v[20:23]\n"
"ds_read_b128 v[48:51], v2 offset:9216\n"
"v_mfma_f32_16x16x32_bf16 v[20:23], v[204:207], v[60:63], v[20:23]\n"
"s_add_u32 s63, s63, 4096\n"
"s_add_u32 m0, s64, 65536\n"
"v_mfma_f32_16x16x32_bf16 v[16:19], v[200:203], v[60:63], v[16:19]\n"
"ds_read_b128 v[52:55], v2 offset:23552\n"
"v_mfma_f32_16x16x32_bf16 v[16:19], v[88:91], v[56:59], v[16:19]\n"
"s_waitcnt lgkmcnt(4)\n"
"v_mfma_f32_16x16x32_bf16 v[16:19], a[64:67], v[32:35], v[16:19]\n"
"ds_read_b128 v[56:59], v2 offset:37888\n"
"v_mfma_f32_16x16x32_bf16 v[16:19], a[176:179], v[36:39], v[16:19]\n"
"buffer_load_dwordx4 v2, s[32:35], s63 offen offset:0 sc1 lds\n"
"v_mfma_f32_16x16x32_bf16 v[20:23], a[180:183], v[36:39], v[20:23]\n"
"ds_read_b128 v[60:63], v2 offset:52224\n"
"v_mfma_f32_16x16x32_bf16 v[20:23], a[68:71], v[32:35], v[20:23]\n"
"s_waitcnt lgkmcnt(4)\n"
"v_mfma_f32_16x16x32_bf16 v[20:23], v[100:103], v[40:43], v[20:23]\n"
"ds_read_b128 v[32:35], v2 offset:10240\n"
"v_mfma_f32_16x16x32_bf16 v[20:23], v[212:215], v[44:47], v[20:23]\n"
"buffer_load_dwordx4 v2, s[32:35], s63 offen offset:1024 sc1 lds\n"
"v_mfma_f32_16x16x32_bf16 v[16:19], v[208:211], v[44:47], v[16:19]\n"
"ds_read_b128 v[36:39], v2 offset:24576\n"
"v_mfma_f32_16x16x32_bf16 v[16:19], v[96:99], v[40:43], v[16:19]\n"
"s_waitcnt lgkmcnt(4)\n"
"v_mfma_f32_16x16x32_bf16 v[16:19], a[72:75], v[48:51], v[16:19]\n"
"ds_read_b128 v[40:43], v2 offset:38912\n"
"v_mfma_f32_16x16x32_bf16 v[16:19], a[184:187], v[52:55], v[16:19]\n"
"buffer_load_dwordx4 v2, s[32:35], s63 offen offset:2048 sc1 lds\n"
"v_mfma_f32_16x16x32_bf16 v[20:23], a[188:191], v[52:55], v[20:23]\n"
"ds_read_b128 v[44:47], v2 offset:53248\n"
"v_mfma_f32_16x16x32_bf16 v[20:23], a[76:79], v[48:51], v[20:23]\n"
"s_waitcnt lgkmcnt(4)\n"
"v_mfma_f32_16x16x32_bf16 v[20:23], v[108:111], v[56:59], v[20:23]\n"
"ds_read_b128 v[48:51], v2 offset:11264\n"
"v_mfma_f32_16x16x32_bf16 v[20:23], v[220:223], v[60:63], v[20:23]\n"
"buffer_load_dwordx2 v[6:7], v0, s[32:35], s66 offen sc1\n"
"v_mfma_f32_16x16x32_bf16 v[16:19], v[216:219], v[60:63], v[16:19]\n"
"ds_read_b128 v[52:55], v2 offset:25600\n"
"v_mfma_f32_16x16x32_bf16 v[16:19], v[104:107], v[56:59], v[16:19]\n"
"s_waitcnt lgkmcnt(4)\n"
"v_mfma_f32_16x16x32_bf16 v[16:19], a[80:83], v[32:35], v[16:19]\n"
"ds_read_b128 v[56:59], v2 offset:39936\n"
"v_mfma_f32_16x16x32_bf16 v[16:19], a[192:195], v[36:39], v[16:19]\n"
"buffer_load_dwordx4 v2, s[32:35], s63 offen offset:3072 sc1 lds\n"
"s_add_u32 s63, s63, 4096\n"
"v_mfma_f32_16x16x32_bf16 v[20:23], a[196:199], v[36:39], v[20:23]\n"
"ds_read_b128 v[60:63], v2 offset:54272\n"
"s_add_u32 m0, s64, 69632\n"
"v_mfma_f32_16x16x32_bf16 v[20:23], a[84:87], v[32:35], v[20:23]\n"
"s_waitcnt lgkmcnt(4)\n"
"v_mfma_f32_16x16x32_bf16 v[20:23], v[116:119], v[40:43], v[20:23]\n"
"ds_read_b128 v[32:35], v2 offset:12288\n"
"v_mfma_f32_16x16x32_bf16 v[20:23], v[228:231], v[44:47], v[20:23]\n"
"buffer_load_dwordx4 v2, s[32:35], s63 offen offset:0 sc1 lds\n"
"v_mfma_f32_16x16x32_bf16 v[16:19], v[224:227], v[44:47], v[16:19]\n"
"ds_read_b128 v[36:39], v2 offset:26624\n"
"v_mfma_f32_16x16x32_bf16 v[16:19], v[112:115], v[40:43], v[16:19]\n"
"s_waitcnt lgkmcnt(4)\n"
"v_mfma_f32_16x16x32_bf16 v[16:19], a[88:91], v[48:51], v[16:19]\n"
"ds_read_b128 v[40:43], v2 offset:40960\n"
"v_mfma_f32_16x16x32_bf16 v[16:19], a[200:203], v[52:55], v[16:19]\n"
"buffer_load_dwordx4 v2, s[32:35], s63 offen offset:1024 sc1 lds\n"
"s_add_u32 s63, s63, 1175552\n"
"v_mfma_f32_16x16x32_bf16 v[20:23], a[204:207], v[52:55], v[20:23]\n"
"ds_read_b128 v[44:47], v2 offset:55296\n"
"v_mfma_f32_16x16x32_bf16 v[20:23], a[92:95], v[48:51], v[20:23]\n"
"s_waitcnt lgkmcnt(4)\n"
"v_mfma_f32_16x16x32_bf16 v[20:23], v[124:127], v[56:59], v[20:23]\n"
"ds_read_b128 v[48:51], v2 offset:13312\n"
"v_mfma_f32_16x16x32_bf16 v[20:23], v[236:239], v[60:63], v[20:23]\n"
"v_mfma_f32_16x16x32_bf16 v[16:19], v[232:235], v[60:63], v[16:19]\n"
"ds_read_b128 v[52:55], v2 offset:27648\n"
"v_mfma_f32_16x16x32_bf16 v[16:19], v[120:123], v[56:59], v[16:19]\n"
"s_waitcnt lgkmcnt(4)\n"
"v_mfma_f32_16x16x32_bf16 v[16:19], a[96:99], v[32:35], v[16:19]\n"
"ds_read_b128 v[56:59], v2 offset:41984\n"
"v_mfma_f32_16x16x32_bf16 v[16:19], a[208:211], v[36:39], v[16:19]\n"
"v_mfma_f32_16x16x32_bf16 v[20:23], a[212:215], v[36:39], v[20:23]\n"
"s_waitcnt vmcnt(11)\n"
"s_barrier\n"
"v_mfma_f32_16x16x32_bf16 v[20:23], a[100:103], v[32:35], v[20:23]\n"
"ds_read_b128 v[60:63], v2 offset:56320\n"
"s_waitcnt lgkmcnt(4)\n"
"v_mfma_f32_16x16x32_bf16 v[20:23], v[132:135], v[40:43], v[20:23]\n"
"ds_read_b128 v[32:35], v4 offset:0\n"
"v_mfma_f32_16x16x32_bf16 v[20:23], v[244:247], v[44:47], v[20:23]\n"
"v_mfma_f32_16x16x32_bf16 v[16:19], v[240:243], v[44:47], v[16:19]\n"
"ds_read_b128 v[36:39], v4 offset:14336\n"
"v_mfma_f32_16x16x32_bf16 v[16:19], v[128:131], v[40:43], v[16:19]\n"
"s_waitcnt lgkmcnt(4)\n"
"v_mfma_f32_16x16x32_bf16 v[16:19], a[104:107], v[48:51], v[16:19]\n"
"ds_read_b128 v[40:43], v4 offset:28672\n"
"v_mfma_f32_16x16x32_bf16 v[16:19], a[216:219], v[52:55], v[16:19]\n"
"v_mfma_f32_16x16x32_bf16 v[20:23], a[220:223], v[52:55], v[20:23]\n"
"ds_read_b128 v[44:47], v4 offset:43008\n"
"v_mfma_f32_16x16x32_bf16 v[20:23], a[108:111], v[48:51], v[20:23]\n"
"s_waitcnt lgkmcnt(4)\n"
"v_mfma_f32_16x16x32_bf16 v[20:23], v[140:143], v[56:59], v[20:23]\n"
"ds_read_b128 v[48:51], v4 offset:1024\n"
"v_mfma_f32_16x16x32_bf16 v[20:23], v[252:255], v[60:63], v[20:23]\n"
"v_mfma_f32_16x16x32_bf16 v[16:19], v[248:251], v[60:63], v[16:19]\n"
"ds_read_b128 v[52:55], v4 offset:15360\n"
"v_mfma_f32_16x16x32_bf16 v[16:19], v[136:139], v[56:59], v[16:19]\n"
"s_branch .L_dbl_1\n"
".L_dbt_1:\n"
"s_nop 1\n"
"s_nop 0\n"
"v_cvt_pk_bf16_f32 v15, v30, v31\n"
"v_cvt_pk_bf16_f32 v14, v28, v29\n"
"s_nop 1\n"
"v_cvt_pk_bf16_f32 v13, v26, v27\n"
"v_cvt_pk_bf16_f32 v12, v24, v25\n"
"ds_write_b128 v5, v[12:15]\n"
"ds_read2st64_b32 v[12:13], v3 offset0:0 offset1:1\n"
"ds_read2st64_b32 v[14:15], v3 offset0:2 offset1:3\n"
"s_waitcnt lgkmcnt(0)\n"
"buffer_atomic_pk_add_bf16 v12, v1, s[44:47], s65 offen offset:0 sc1\n"
"buffer_atomic_pk_add_bf16 v13, v1, s[44:47], s65 offen offset:2048 sc1\n"
"s_add_u32 s65, s65, 16384\n"
"buffer_atomic_pk_add_bf16 v14, v1, s[44:47], s65 offen offset:0 sc1\n"
"buffer_atomic_pk_add_bf16 v15, v1, s[44:47], s65 offen offset:2048 sc1\n"
"s_add_u32 s65, s65, 16384\n"
"s_or_b32 s48, s28, s27\n"
"s_or_b32 s48, s48, s26\n"
"s_cbranch_scc1 .L_end\n"
"v_mov_b32 v8, s54\n"
"v_mov_b32 v9, s55\n"
"buffer_store_dwordx2 v[8:9], off, s[32:35], 0 sc1\n"
"s_branch .L_end\n"
".p2align 6\n"
".L_dhv2:\n"
".L_dal_2:\n"
"s_waitcnt lgkmcnt(4)\n"
"v_mfma_f32_16x16x32_bf16 v[16:19], a[0:3], v[32:35], 0\n"
"ds_read_b128 v[56:59], v2 offset:29696\n"
"ds_read_b128 v[12:15], v5 offset:8192\n"
"v_mfma_f32_16x16x32_bf16 v[16:19], a[112:115], v[36:39], v[16:19]\n"
"v_mfma_f32_16x16x32_bf16 v[20:23], a[116:119], v[36:39], 0\n"
"ds_read_b128 v[60:63], v2 offset:44032\n"
"v_mfma_f32_16x16x32_bf16 v[20:23], a[4:7], v[32:35], v[20:23]\n"
"s_waitcnt lgkmcnt(5)\n"
"v_mfma_f32_16x16x32_bf16 v[20:23], a[228:231], v[40:43], v[20:23]\n"
"ds_read_b128 v[32:35], v2 offset:2048\n"
"v_mfma_f32_16x16x32_bf16 v[20:23], v[148:151], v[44:47], v[20:23]\n"
"s_waitcnt lgkmcnt(2)\n"
"v_add_f32 v24, v24, v12\n"
"v_mfma_f32_16x16x32_bf16 v[16:19], v[144:147], v[44:47], v[16:19]\n"
"ds_read_b128 v[36:39], v2 offset:16384\n"
"v_add_f32 v25, v25, v13\n"
"v_mfma_f32_16x16x32_bf16 v[16:19], a[224:227], v[40:43], v[16:19]\n"
"v_add_f32 v26, v26, v14\n"
"v_mfma_f32_16x16x32_bf16 v[16:19], a[8:11], v[48:51], v[16:19]\n"
"ds_read_b128 v[40:43], v2 offset:30720\n"
"v_add_f32 v27, v27, v15\n"
"v_mfma_f32_16x16x32_bf16 v[16:19], a[120:123], v[52:55], v[16:19]\n"
"ds_read_b128 v[12:15], v5 offset:9216\n"
"v_mfma_f32_16x16x32_bf16 v[20:23], a[124:127], v[52:55], v[20:23]\n"
"ds_read_b128 v[44:47], v2 offset:45056\n"
"v_mfma_f32_16x16x32_bf16 v[20:23], a[12:15], v[48:51], v[20:23]\n"
"s_waitcnt lgkmcnt(5)\n"
"v_mfma_f32_16x16x32_bf16 v[20:23], a[236:239], v[56:59], v[20:23]\n"
"ds_read_b128 v[48:51], v2 offset:3072\n"
"v_mfma_f32_16x16x32_bf16 v[20:23], v[156:159], v[60:63], v[20:23]\n"
"v_mfma_f32_16x16x32_bf16 v[16:19], v[152:155], v[60:63], v[16:19]\n"
"ds_read_b128 v[52:55], v2 offset:17408\n"
"s_waitcnt lgkmcnt(3)\n"
"v_mfma_f32_16x16x32_bf16 v[16:19], a[232:235], v[56:59], v[16:19]\n"
"v_add_f32 v28, v28, v12\n"
"v_mfma_f32_16x16x32_bf16 v[16:19], a[16:19], v[32:35], v[16:19]\n"
"ds_read_b128 v[56:59], v2 offset:31744\n"
"v_add_f32 v29, v29, v13\n"
"v_mfma_f32_16x16x32_bf16 v[16:19], a[128:131], v[36:39], v[16:19]\n"
"s_waitcnt vmcnt(0)\n"
"s_barrier\n"
"v_mfma_f32_16x16x32_bf16 v[20:23], a[132:135], v[36:39], v[20:23]\n"
"ds_read_b128 v[60:63], v2 offset:46080\n"
"v_add_f32 v30, v30, v14\n"
"v_mfma_f32_16x16x32_bf16 v[20:23], a[20:23], v[32:35], v[20:23]\n"
"v_add_f32 v31, v31, v15\n"
"s_waitcnt lgkmcnt(4)\n"
"v_mfma_f32_16x16x32_bf16 v[20:23], a[244:247], v[40:43], v[20:23]\n"
"ds_read_b128 v[32:35], v2 offset:4096\n"
"s_add_u32 m0, s64, 57344\n"
"v_mfma_f32_16x16x32_bf16 v[20:23], v[164:167], v[44:47], v[20:23]\n"
"buffer_load_dwordx4 v2, s[32:35], s63 offen offset:0 sc1 lds\n"
"v_cvt_pk_bf16_f32 v12, v24, v25\n"
"v_mfma_f32_16x16x32_bf16 v[16:19], v[160:163], v[44:47], v[16:19]\n"
"ds_read_b128 v[36:39], v2 offset:18432\n"
"v_cvt_pk_bf16_f32 v13, v26, v27\n"
"v_mfma_f32_16x16x32_bf16 v[16:19], a[240:243], v[40:43], v[16:19]\n"
"v_cvt_pk_bf16_f32 v14, v28, v29\n"
"s_waitcnt lgkmcnt(4)\n"
"v_mfma_f32_16x16x32_bf16 v[16:19], a[24:27], v[48:51], v[16:19]\n"
"ds_read_b128 v[40:43], v2 offset:32768\n"
"v_cvt_pk_bf16_f32 v15, v30, v31\n"
"v_mfma_f32_16x16x32_bf16 v[16:19], a[136:139], v[52:55], v[16:19]\n"
"buffer_store_dwordx4 v[12:15], v2, s[32:35], s68 offen sc1\n"
"v_add_f32 v10, v24, v25\n"
"v_mfma_f32_16x16x32_bf16 v[20:23], a[140:143], v[52:55], v[20:23]\n"
"ds_read_b128 v[44:47], v2 offset:47104\n"
"v_mul_f32 v11, v24, v24\n"
"v_mfma_f32_16x16x32_bf16 v[20:23], a[28:31], v[48:51], v[20:23]\n"
"v_fmac_f32 v11, v25, v25\n"
"s_waitcnt lgkmcnt(4)\n"
"v_mfma_f32_16x16x32_bf16 v[20:23], a[252:255], v[56:59], v[20:23]\n"
"ds_read_b128 v[48:51], v2 offset:5120\n"
"v_add_f32 v10, v10, v26\n"
"v_mfma_f32_16x16x32_bf16 v[20:23], v[172:175], v[60:63], v[20:23]\n"
"buffer_load_dwordx4 v2, s[32:35], s63 offen offset:1024 sc1 lds\n"
"v_fmac_f32 v11, v26, v26\n"
"v_mfma_f32_16x16x32_bf16 v[16:19], v[168:171], v[60:63], v[16:19]\n"
"ds_read_b128 v[52:55], v2 offset:19456\n"
"v_add_f32 v10, v10, v27\n"
"v_mfma_f32_16x16x32_bf16 v[16:19], a[248:251], v[56:59], v[16:19]\n"
"v_fmac_f32 v11, v27, v27\n"
"s_waitcnt lgkmcnt(4)\n"
"v_mfma_f32_16x16x32_bf16 v[16:19], a[32:35], v[32:35], v[16:19]\n"
"ds_read_b128 v[56:59], v2 offset:33792\n"
"v_add_f32 v10, v10, v28\n"
"v_mfma_f32_16x16x32_bf16 v[16:19], a[144:147], v[36:39], v[16:19]\n"
"buffer_load_dwordx4 v2, s[32:35], s63 offen offset:2048 sc1 lds\n"
"v_fmac_f32 v11, v28, v28\n"
"v_mfma_f32_16x16x32_bf16 v[20:23], a[148:151], v[36:39], v[20:23]\n"
"ds_read_b128 v[60:63], v2 offset:48128\n"
"v_add_f32 v10, v10, v29\n"
"v_mfma_f32_16x16x32_bf16 v[20:23], a[36:39], v[32:35], v[20:23]\n"
"v_fmac_f32 v11, v29, v29\n"
"s_waitcnt lgkmcnt(4)\n"
"v_mfma_f32_16x16x32_bf16 v[20:23], v[68:71], v[40:43], v[20:23]\n"
"ds_read_b128 v[32:35], v2 offset:6144\n"
"v_add_f32 v10, v10, v30\n"
"v_mfma_f32_16x16x32_bf16 v[20:23], v[180:183], v[44:47], v[20:23]\n"
"buffer_load_dwordx4 v2, s[32:35], s63 offen offset:3072 sc1 lds\n"
"s_add_u32 m0, s74, 0\n"
"v_mfma_f32_16x16x32_bf16 v[16:19], v[176:179], v[44:47], v[16:19]\n"
"ds_read_b128 v[36:39], v2 offset:20480\n"
"v_fmac_f32 v11, v30, v30\n"
"v_mfma_f32_16x16x32_bf16 v[16:19], v[64:67], v[40:43], v[16:19]\n"
"v_add_f32 v10, v10, v31\n"
"s_waitcnt lgkmcnt(4)\n"
"v_mfma_f32_16x16x32_bf16 v[16:19], a[40:43], v[48:51], v[16:19]\n"
"ds_read_b128 v[40:43], v2 offset:34816\n"
"v_fmac_f32 v11, v31, v31\n"
"v_mfma_f32_16x16x32_bf16 v[16:19], a[152:155], v[52:55], v[16:19]\n"
"buffer_load_dwordx4 v2, s[32:35], s71 offen offset:0 sc1 lds\n"
"v_permlane16_swap_b32 v10, v11\n"
"v_mfma_f32_16x16x32_bf16 v[20:23], a[156:159], v[52:55], v[20:23]\n"
"ds_read_b128 v[44:47], v2 offset:49152\n"
"v_add_f32 v10, v10, v11\n"
"v_mfma_f32_16x16x32_bf16 v[20:23], a[44:47], v[48:51], v[20:23]\n"
"s_waitcnt lgkmcnt(4)\n"
"v_mfma_f32_16x16x32_bf16 v[20:23], v[76:79], v[56:59], v[20:23]\n"
"ds_read_b128 v[48:51], v2 offset:7168\n"
"v_permlane32_swap_b32 v10, v11\n"
"v_mfma_f32_16x16x32_bf16 v[20:23], v[188:191], v[60:63], v[20:23]\n"
"buffer_load_dwordx4 v2, s[32:35], s71 offen offset:1024 sc1 lds\n"
"s_add_u32 s71, s71, 1187840\n"
"v_mfma_f32_16x16x32_bf16 v[16:19], v[184:187], v[60:63], v[16:19]\n"
"ds_read_b128 v[52:55], v2 offset:21504\n"
"s_add_u32 s63, s63, 4096\n"
"v_mfma_f32_16x16x32_bf16 v[16:19], v[72:75], v[56:59], v[16:19]\n"
"s_add_u32 m0, s64, 61440\n"
"s_waitcnt lgkmcnt(4)\n"
"v_mfma_f32_16x16x32_bf16 v[16:19], a[48:51], v[32:35], v[16:19]\n"
"ds_read_b128 v[56:59], v2 offset:35840\n"
"v_add_f32 v10, v10, v11\n"
"v_mfma_f32_16x16x32_bf16 v[16:19], a[160:163], v[36:39], v[16:19]\n"
"buffer_store_dword v10, v3, s[32:35], s68 offen offset:1024 sc1\n"
"s_mov_b32 s67, s68\n"
"v_mfma_f32_16x16x32_bf16 v[20:23], a[164:167], v[36:39], v[20:23]\n"
"ds_read_b128 v[60:63], v2 offset:50176\n"
"s_mov_b32 s68, s70\n"
"v_mfma_f32_16x16x32_bf16 v[20:23], a[52:55], v[32:35], v[20:23]\n"
"s_add_u32 s70, s70, 1187840\n"
"s_waitcnt lgkmcnt(4)\n"
"v_mfma_f32_16x16x32_bf16 v[20:23], v[84:87], v[40:43], v[20:23]\n"
"ds_read_b128 v[32:35], v2 offset:8192\n"
"v_mfma_f32_16x16x32_bf16 v[20:23], v[196:199], v[44:47], v[20:23]\n"
"buffer_load_dwordx4 v2, s[32:35], s63 offen offset:0 sc1 lds\n"
"v_mfma_f32_16x16x32_bf16 v[16:19], v[192:195], v[44:47], v[16:19]\n"
"ds_read_b128 v[36:39], v2 offset:22528\n"
"v_mfma_f32_16x16x32_bf16 v[16:19], v[80:83], v[40:43], v[16:19]\n"
"s_waitcnt lgkmcnt(4)\n"
"v_mfma_f32_16x16x32_bf16 v[16:19], a[56:59], v[48:51], v[16:19]\n"
"ds_read_b128 v[40:43], v2 offset:36864\n"
"v_mfma_f32_16x16x32_bf16 v[16:19], a[168:171], v[52:55], v[16:19]\n"
"buffer_load_dwordx4 v2, s[32:35], s63 offen offset:1024 sc1 lds\n"
"v_mfma_f32_16x16x32_bf16 v[20:23], a[172:175], v[52:55], v[20:23]\n"
"ds_read_b128 v[44:47], v2 offset:51200\n"
"v_mfma_f32_16x16x32_bf16 v[20:23], a[60:63], v[48:51], v[20:23]\n"
"s_waitcnt lgkmcnt(4)\n"
"v_mfma_f32_16x16x32_bf16 v[20:23], v[92:95], v[56:59], v[20:23]\n"
"ds_read_b128 v[48:51], v2 offset:9216\n"
"v_mfma_f32_16x16x32_bf16 v[20:23], v[204:207], v[60:63], v[20:23]\n"
"buffer_load_dwordx4 v2, s[32:35], s63 offen offset:2048 sc1 lds\n"
"v_mfma_f32_16x16x32_bf16 v[16:19], v[200:203], v[60:63], v[16:19]\n"
"ds_read_b128 v[52:55], v2 offset:23552\n"
"v_mfma_f32_16x16x32_bf16 v[16:19], v[88:91], v[56:59], v[16:19]\n"
"s_waitcnt lgkmcnt(4)\n"
"v_mfma_f32_16x16x32_bf16 v[16:19], a[64:67], v[32:35], v[16:19]\n"
"ds_read_b128 v[56:59], v2 offset:37888\n"
"v_mfma_f32_16x16x32_bf16 v[16:19], a[176:179], v[36:39], v[16:19]\n"
"buffer_load_dwordx4 v2, s[32:35], s63 offen offset:3072 sc1 lds\n"
"s_add_u32 s63, s63, 4096\n"
"v_mfma_f32_16x16x32_bf16 v[20:23], a[180:183], v[36:39], v[20:23]\n"
"ds_read_b128 v[60:63], v2 offset:52224\n"
"s_add_u32 m0, s64, 65536\n"
"v_mfma_f32_16x16x32_bf16 v[20:23], a[68:71], v[32:35], v[20:23]\n"
"s_waitcnt lgkmcnt(4)\n"
"v_mfma_f32_16x16x32_bf16 v[20:23], v[100:103], v[40:43], v[20:23]\n"
"ds_read_b128 v[32:35], v2 offset:10240\n"
"v_mfma_f32_16x16x32_bf16 v[20:23], v[212:215], v[44:47], v[20:23]\n"
"buffer_load_dwordx4 v2, s[32:35], s63 offen offset:0 sc1 lds\n"
"v_mfma_f32_16x16x32_bf16 v[16:19], v[208:211], v[44:47], v[16:19]\n"
"ds_read_b128 v[36:39], v2 offset:24576\n"
"v_mfma_f32_16x16x32_bf16 v[16:19], v[96:99], v[40:43], v[16:19]\n"
"s_waitcnt lgkmcnt(4)\n"
"v_mfma_f32_16x16x32_bf16 v[16:19], a[72:75], v[48:51], v[16:19]\n"
"ds_read_b128 v[40:43], v2 offset:38912\n"
"v_mfma_f32_16x16x32_bf16 v[16:19], a[184:187], v[52:55], v[16:19]\n"
"buffer_load_dwordx4 v2, s[32:35], s63 offen offset:1024 sc1 lds\n"
"v_mfma_f32_16x16x32_bf16 v[20:23], a[188:191], v[52:55], v[20:23]\n"
"ds_read_b128 v[44:47], v2 offset:53248\n"
"v_mfma_f32_16x16x32_bf16 v[20:23], a[76:79], v[48:51], v[20:23]\n"
"s_waitcnt lgkmcnt(4)\n"
"v_mfma_f32_16x16x32_bf16 v[20:23], v[108:111], v[56:59], v[20:23]\n"
"ds_read_b128 v[48:51], v2 offset:11264\n"
"v_mfma_f32_16x16x32_bf16 v[20:23], v[220:223], v[60:63], v[20:23]\n"
"buffer_load_dwordx4 v2, s[32:35], s63 offen offset:2048 sc1 lds\n"
"v_mfma_f32_16x16x32_bf16 v[16:19], v[216:219], v[60:63], v[16:19]\n"
"ds_read_b128 v[52:55], v2 offset:25600\n"
"v_mfma_f32_16x16x32_bf16 v[16:19], v[104:107], v[56:59], v[16:19]\n"
"s_waitcnt lgkmcnt(4)\n"
"v_mfma_f32_16x16x32_bf16 v[16:19], a[80:83], v[32:35], v[16:19]\n"
"ds_read_b128 v[56:59], v2 offset:39936\n"
"v_mfma_f32_16x16x32_bf16 v[16:19], a[192:195], v[36:39], v[16:19]\n"
"buffer_load_dwordx4 v2, s[32:35], s63 offen offset:3072 sc1 lds\n"
"s_add_u32 s63, s63, 4096\n"
"v_mfma_f32_16x16x32_bf16 v[20:23], a[196:199], v[36:39], v[20:23]\n"
"ds_read_b128 v[60:63], v2 offset:54272\n"
"s_add_u32 m0, s64, 69632\n"
"v_mfma_f32_16x16x32_bf16 v[20:23], a[84:87], v[32:35], v[20:23]\n"
"s_waitcnt lgkmcnt(4)\n"
"v_mfma_f32_16x16x32_bf16 v[20:23], v[116:119], v[40:43], v[20:23]\n"
"ds_read_b128 v[32:35], v2 offset:12288\n"
"v_mfma_f32_16x16x32_bf16 v[20:23], v[228:231], v[44:47], v[20:23]\n"
"buffer_load_dwordx4 v2, s[32:35], s63 offen offset:0 sc1 lds\n"
"v_mfma_f32_16x16x32_bf16 v[16:19], v[224:227], v[44:47], v[16:19]\n"
"ds_read_b128 v[36:39], v2 offset:26624\n"
"v_mfma_f32_16x16x32_bf16 v[16:19], v[112:115], v[40:43], v[16:19]\n"
"s_waitcnt lgkmcnt(4)\n"
"v_mfma_f32_16x16x32_bf16 v[16:19], a[88:91], v[48:51], v[16:19]\n"
"ds_read_b128 v[40:43], v2 offset:40960\n"
"v_mfma_f32_16x16x32_bf16 v[16:19], a[200:203], v[52:55], v[16:19]\n"
"buffer_load_dwordx4 v2, s[32:35], s63 offen offset:1024 sc1 lds\n"
"s_add_u32 s63, s63, 1175552\n"
"v_mfma_f32_16x16x32_bf16 v[20:23], a[204:207], v[52:55], v[20:23]\n"
"ds_read_b128 v[44:47], v2 offset:55296\n"
"v_mfma_f32_16x16x32_bf16 v[20:23], a[92:95], v[48:51], v[20:23]\n"
"s_waitcnt lgkmcnt(4)\n"
"v_mfma_f32_16x16x32_bf16 v[20:23], v[124:127], v[56:59], v[20:23]\n"
"ds_read_b128 v[48:51], v2 offset:13312\n"
"v_mfma_f32_16x16x32_bf16 v[20:23], v[236:239], v[60:63], v[20:23]\n"
"v_mfma_f32_16x16x32_bf16 v[16:19], v[232:235], v[60:63], v[16:19]\n"
"ds_read_b128 v[52:55], v2 offset:27648\n"
"v_mfma_f32_16x16x32_bf16 v[16:19], v[120:123], v[56:59], v[16:19]\n"
"s_waitcnt lgkmcnt(4)\n"
"v_mfma_f32_16x16x32_bf16 v[16:19], a[96:99], v[32:35], v[16:19]\n"
"ds_read_b128 v[56:59], v2 offset:41984\n"
"v_mfma_f32_16x16x32_bf16 v[16:19], a[208:211], v[36:39], v[16:19]\n"
"s_waitcnt vmcnt(13)\n"
"s_waitcnt vmcnt(10)\n"
"s_barrier\n"
"v_mfma_f32_16x16x32_bf16 v[20:23], a[212:215], v[36:39], v[20:23]\n"
"ds_read_b128 v[60:63], v2 offset:56320\n"
"v_mfma_f32_16x16x32_bf16 v[20:23], a[100:103], v[32:35], v[20:23]\n"
"s_waitcnt lgkmcnt(4)\n"
"v_mfma_f32_16x16x32_bf16 v[20:23], v[132:135], v[40:43], v[20:23]\n"
"ds_read_b128 v[32:35], v4 offset:0\n"
"v_mfma_f32_16x16x32_bf16 v[20:23], v[244:247], v[44:47], v[20:23]\n"
"v_mfma_f32_16x16x32_bf16 v[16:19], v[240:243], v[44:47], v[16:19]\n"
"ds_read_b128 v[36:39], v4 offset:14336\n"
"v_mfma_f32_16x16x32_bf16 v[16:19], v[128:131], v[40:43], v[16:19]\n"
"s_waitcnt lgkmcnt(4)\n"
"v_mfma_f32_16x16x32_bf16 v[16:19], a[104:107], v[48:51], v[16:19]\n"
"ds_read_b128 v[40:43], v4 offset:28672\n"
"v_mfma_f32_16x16x32_bf16 v[16:19], a[216:219], v[52:55], v[16:19]\n"
"v_mfma_f32_16x16x32_bf16 v[20:23], a[220:223], v[52:55], v[20:23]\n"
"ds_read_b128 v[44:47], v4 offset:43008\n"
"v_mfma_f32_16x16x32_bf16 v[20:23], a[108:111], v[48:51], v[20:23]\n"
"s_waitcnt lgkmcnt(4)\n"
"v_mfma_f32_16x16x32_bf16 v[20:23], v[140:143], v[56:59], v[20:23]\n"
"ds_read_b128 v[48:51], v4 offset:1024\n"
"v_mfma_f32_16x16x32_bf16 v[20:23], v[252:255], v[60:63], v[20:23]\n"
"v_mfma_f32_16x16x32_bf16 v[16:19], v[248:251], v[60:63], v[16:19]\n"
"ds_read_b128 v[52:55], v4 offset:15360\n"
"s_sub_u32 s62, s62, 1\n"
"v_mfma_f32_16x16x32_bf16 v[16:19], v[136:139], v[56:59], v[16:19]\n"
"s_cbranch_scc1 .L_dab_2\n"
"s_waitcnt lgkmcnt(4)\n"
"v_mfma_f32_16x16x32_bf16 v[24:27], a[0:3], v[32:35], 0\n"
"ds_read_b128 v[56:59], v4 offset:29696\n"
"ds_read_b128 v[12:15], v5 offset:0\n"
"v_mfma_f32_16x16x32_bf16 v[24:27], a[112:115], v[36:39], v[24:27]\n"
"v_mfma_f32_16x16x32_bf16 v[28:31], a[116:119], v[36:39], 0\n"
"ds_read_b128 v[60:63], v4 offset:44032\n"
"v_mfma_f32_16x16x32_bf16 v[28:31], a[4:7], v[32:35], v[28:31]\n"
"s_waitcnt lgkmcnt(5)\n"
"v_mfma_f32_16x16x32_bf16 v[28:31], a[228:231], v[40:43], v[28:31]\n"
"ds_read_b128 v[32:35], v4 offset:2048\n"
"v_mfma_f32_16x16x32_bf16 v[28:31], v[148:151], v[44:47], v[28:31]\n"
"s_waitcnt lgkmcnt(2)\n"
"v_add_f32 v16, v16, v12\n"
"v_mfma_f32_16x16x32_bf16 v[24:27], v[144:147], v[44:47], v[24:27]\n"
"ds_read_b128 v[36:39], v4 offset:16384\n"
"v_add_f32 v17, v17, v13\n"
"v_mfma_f32_16x16x32_bf16 v[24:27], a[224:227], v[40:43], v[24:27]\n"
"v_add_f32 v18, v18, v14\n"
"v_mfma_f32_16x16x32_bf16 v[24:27], a[8:11], v[48:51], v[24:27]\n"
"ds_read_b128 v[40:43], v4 offset:30720\n"
"v_add_f32 v19, v19, v15\n"
"v_mfma_f32_16x16x32_bf16 v[24:27], a[120:123], v[52:55], v[24:27]\n"
"ds_read_b128 v[12:15], v5 offset:1024\n"
"v_mfma_f32_16x16x32_bf16 v[28:31], a[124:127], v[52:55], v[28:31]\n"
"ds_read_b128 v[44:47], v4 offset:45056\n"
"v_mfma_f32_16x16x32_bf16 v[28:31], a[12:15], v[48:51], v[28:31]\n"
"s_waitcnt lgkmcnt(5)\n"
"v_mfma_f32_16x16x32_bf16 v[28:31], a[236:239], v[56:59], v[28:31]\n"
"ds_read_b128 v[48:51], v4 offset:3072\n"
"v_mfma_f32_16x16x32_bf16 v[28:31], v[156:159], v[60:63], v[28:31]\n"
"v_mfma_f32_16x16x32_bf16 v[24:27], v[152:155], v[60:63], v[24:27]\n"
"ds_read_b128 v[52:55], v4 offset:17408\n"
"s_waitcnt lgkmcnt(3)\n"
"v_mfma_f32_16x16x32_bf16 v[24:27], a[232:235], v[56:59], v[24:27]\n"
"v_add_f32 v20, v20, v12\n"
"v_mfma_f32_16x16x32_bf16 v[24:27], a[16:19], v[32:35], v[24:27]\n"
"ds_read_b128 v[56:59], v4 offset:31744\n"
"v_add_f32 v21, v21, v13\n"
"v_mfma_f32_16x16x32_bf16 v[24:27], a[128:131], v[36:39], v[24:27]\n"
"s_waitcnt vmcnt(0)\n"
"s_barrier\n"
"v_mfma_f32_16x16x32_bf16 v[28:31], a[132:135], v[36:39], v[28:31]\n"
"ds_read_b128 v[60:63], v4 offset:46080\n"
"v_add_f32 v22, v22, v14\n"
"v_mfma_f32_16x16x32_bf16 v[28:31], a[20:23], v[32:35], v[28:31]\n"
"v_add_f32 v23, v23, v15\n"
"s_waitcnt lgkmcnt(4)\n"
"v_mfma_f32_16x16x32_bf16 v[28:31], a[244:247], v[40:43], v[28:31]\n"
"ds_read_b128 v[32:35], v4 offset:4096\n"
"s_add_u32 m0, s64, 0\n"
"v_mfma_f32_16x16x32_bf16 v[28:31], v[164:167], v[44:47], v[28:31]\n"
"buffer_load_dwordx4 v2, s[32:35], s63 offen offset:0 sc1 lds\n"
"v_cvt_pk_bf16_f32 v12, v16, v17\n"
"v_mfma_f32_16x16x32_bf16 v[24:27], v[160:163], v[44:47], v[24:27]\n"
"ds_read_b128 v[36:39], v4 offset:18432\n"
"v_cvt_pk_bf16_f32 v13, v18, v19\n"
"v_mfma_f32_16x16x32_bf16 v[24:27], a[240:243], v[40:43], v[24:27]\n"
"v_cvt_pk_bf16_f32 v14, v20, v21\n"
"s_waitcnt lgkmcnt(4)\n"
"v_mfma_f32_16x16x32_bf16 v[24:27], a[24:27], v[48:51], v[24:27]\n"
"ds_read_b128 v[40:43], v4 offset:32768\n"
"v_cvt_pk_bf16_f32 v15, v22, v23\n"
"v_mfma_f32_16x16x32_bf16 v[24:27], a[136:139], v[52:55], v[24:27]\n"
"buffer_store_dwordx4 v[12:15], v2, s[32:35], s69 offen sc1\n"
"v_add_f32 v10, v16, v17\n"
"v_mfma_f32_16x16x32_bf16 v[28:31], a[140:143], v[52:55], v[28:31]\n"
"ds_read_b128 v[44:47], v4 offset:47104\n"
"v_mul_f32 v11, v16, v16\n"
"v_mfma_f32_16x16x32_bf16 v[28:31], a[28:31], v[48:51], v[28:31]\n"
"v_fmac_f32 v11, v17, v17\n"
"s_waitcnt lgkmcnt(4)\n"
"v_mfma_f32_16x16x32_bf16 v[28:31], a[252:255], v[56:59], v[28:31]\n"
"ds_read_b128 v[48:51], v4 offset:5120\n"
"v_add_f32 v10, v10, v18\n"
"v_mfma_f32_16x16x32_bf16 v[28:31], v[172:175], v[60:63], v[28:31]\n"
"buffer_load_dwordx4 v2, s[32:35], s63 offen offset:1024 sc1 lds\n"
"v_fmac_f32 v11, v18, v18\n"
"v_mfma_f32_16x16x32_bf16 v[24:27], v[168:171], v[60:63], v[24:27]\n"
"ds_read_b128 v[52:55], v4 offset:19456\n"
"v_add_f32 v10, v10, v19\n"
"v_mfma_f32_16x16x32_bf16 v[24:27], a[248:251], v[56:59], v[24:27]\n"
"v_fmac_f32 v11, v19, v19\n"
"s_waitcnt lgkmcnt(4)\n"
"v_mfma_f32_16x16x32_bf16 v[24:27], a[32:35], v[32:35], v[24:27]\n"
"ds_read_b128 v[56:59], v4 offset:33792\n"
"v_add_f32 v10, v10, v20\n"
"v_mfma_f32_16x16x32_bf16 v[24:27], a[144:147], v[36:39], v[24:27]\n"
"buffer_load_dwordx4 v2, s[32:35], s63 offen offset:2048 sc1 lds\n"
"v_fmac_f32 v11, v20, v20\n"
"v_mfma_f32_16x16x32_bf16 v[28:31], a[148:151], v[36:39], v[28:31]\n"
"ds_read_b128 v[60:63], v4 offset:48128\n"
"v_add_f32 v10, v10, v21\n"
"v_mfma_f32_16x16x32_bf16 v[28:31], a[36:39], v[32:35], v[28:31]\n"
"v_fmac_f32 v11, v21, v21\n"
"s_waitcnt lgkmcnt(4)\n"
"v_mfma_f32_16x16x32_bf16 v[28:31], v[68:71], v[40:43], v[28:31]\n"
"ds_read_b128 v[32:35], v4 offset:6144\n"
"v_add_f32 v10, v10, v22\n"
"v_mfma_f32_16x16x32_bf16 v[28:31], v[180:183], v[44:47], v[28:31]\n"
"buffer_load_dwordx4 v2, s[32:35], s63 offen offset:3072 sc1 lds\n"
"s_add_u32 m0, s74, 8192\n"
"v_mfma_f32_16x16x32_bf16 v[24:27], v[176:179], v[44:47], v[24:27]\n"
"ds_read_b128 v[36:39], v4 offset:20480\n"
"v_fmac_f32 v11, v22, v22\n"
"v_mfma_f32_16x16x32_bf16 v[24:27], v[64:67], v[40:43], v[24:27]\n"
"v_add_f32 v10, v10, v23\n"
"s_waitcnt lgkmcnt(4)\n"
"v_mfma_f32_16x16x32_bf16 v[24:27], a[40:43], v[48:51], v[24:27]\n"
"ds_read_b128 v[40:43], v4 offset:34816\n"
"v_fmac_f32 v11, v23, v23\n"
"v_mfma_f32_16x16x32_bf16 v[24:27], a[152:155], v[52:55], v[24:27]\n"
"buffer_load_dwordx4 v2, s[32:35], s71 offen offset:0 sc1 lds\n"
"v_permlane16_swap_b32 v10, v11\n"
"v_mfma_f32_16x16x32_bf16 v[28:31], a[156:159], v[52:55], v[28:31]\n"
"ds_read_b128 v[44:47], v4 offset:49152\n"
"v_add_f32 v10, v10, v11\n"
"v_mfma_f32_16x16x32_bf16 v[28:31], a[44:47], v[48:51], v[28:31]\n"
"s_waitcnt lgkmcnt(4)\n"
"v_mfma_f32_16x16x32_bf16 v[28:31], v[76:79], v[56:59], v[28:31]\n"
"ds_read_b128 v[48:51], v4 offset:7168\n"
"v_permlane32_swap_b32 v10, v11\n"
"v_mfma_f32_16x16x32_bf16 v[28:31], v[188:191], v[60:63], v[28:31]\n"
"buffer_load_dwordx4 v2, s[32:35], s71 offen offset:1024 sc1 lds\n"
"s_add_u32 s71, s71, 1187840\n"
"v_mfma_f32_16x16x32_bf16 v[24:27], v[184:187], v[60:63], v[24:27]\n"
"ds_read_b128 v[52:55], v4 offset:21504\n"
"s_add_u32 s63, s63, 4096\n"
"v_mfma_f32_16x16x32_bf16 v[24:27], v[72:75], v[56:59], v[24:27]\n"
"s_add_u32 m0, s64, 4096\n"
"s_waitcnt lgkmcnt(4)\n"
"v_mfma_f32_16x16x32_bf16 v[24:27], a[48:51], v[32:35], v[24:27]\n"
"ds_read_b128 v[56:59], v4 offset:35840\n"
"v_add_f32 v10, v10, v11\n"
"v_mfma_f32_16x16x32_bf16 v[24:27], a[160:163], v[36:39], v[24:27]\n"
"buffer_store_dword v10, v3, s[32:35], s69 offen offset:1024 sc1\n"
"s_mov_b32 s67, s69\n"
"v_mfma_f32_16x16x32_bf16 v[28:31], a[164:167], v[36:39], v[28:31]\n"
"ds_read_b128 v[60:63], v4 offset:50176\n"
"s_mov_b32 s69, s70\n"
"v_mfma_f32_16x16x32_bf16 v[28:31], a[52:55], v[32:35], v[28:31]\n"
"s_add_u32 s70, s70, 1187840\n"
"s_waitcnt lgkmcnt(4)\n"
"v_mfma_f32_16x16x32_bf16 v[28:31], v[84:87], v[40:43], v[28:31]\n"
"ds_read_b128 v[32:35], v4 offset:8192\n"
"v_mfma_f32_16x16x32_bf16 v[28:31], v[196:199], v[44:47], v[28:31]\n"
"buffer_load_dwordx4 v2, s[32:35], s63 offen offset:0 sc1 lds\n"
"v_mfma_f32_16x16x32_bf16 v[24:27], v[192:195], v[44:47], v[24:27]\n"
"ds_read_b128 v[36:39], v4 offset:22528\n"
"v_mfma_f32_16x16x32_bf16 v[24:27], v[80:83], v[40:43], v[24:27]\n"
"s_waitcnt lgkmcnt(4)\n"
"v_mfma_f32_16x16x32_bf16 v[24:27], a[56:59], v[48:51], v[24:27]\n"
"ds_read_b128 v[40:43], v4 offset:36864\n"
"v_mfma_f32_16x16x32_bf16 v[24:27], a[168:171], v[52:55], v[24:27]\n"
"buffer_load_dwordx4 v2, s[32:35], s63 offen offset:1024 sc1 lds\n"
"v_mfma_f32_16x16x32_bf16 v[28:31], a[172:175], v[52:55], v[28:31]\n"
"ds_read_b128 v[44:47], v4 offset:51200\n"
"v_mfma_f32_16x16x32_bf16 v[28:31], a[60:63], v[48:51], v[28:31]\n"
"s_waitcnt lgkmcnt(4)\n"
"v_mfma_f32_16x16x32_bf16 v[28:31], v[92:95], v[56:59], v[28:31]\n"
"ds_read_b128 v[48:51], v4 offset:9216\n"
"v_mfma_f32_16x16x32_bf16 v[28:31], v[204:207], v[60:63], v[28:31]\n"
"buffer_load_dwordx4 v2, s[32:35], s63 offen offset:2048 sc1 lds\n"
"v_mfma_f32_16x16x32_bf16 v[24:27], v[200:203], v[60:63], v[24:27]\n"
"ds_read_b128 v[52:55], v4 offset:23552\n"
"v_mfma_f32_16x16x32_bf16 v[24:27], v[88:91], v[56:59], v[24:27]\n"
"s_waitcnt lgkmcnt(4)\n"
"v_mfma_f32_16x16x32_bf16 v[24:27], a[64:67], v[32:35], v[24:27]\n"
"ds_read_b128 v[56:59], v4 offset:37888\n"
"v_mfma_f32_16x16x32_bf16 v[24:27], a[176:179], v[36:39], v[24:27]\n"
"buffer_load_dwordx4 v2, s[32:35], s63 offen offset:3072 sc1 lds\n"
"s_add_u32 s63, s63, 4096\n"
"v_mfma_f32_16x16x32_bf16 v[28:31], a[180:183], v[36:39], v[28:31]\n"
"ds_read_b128 v[60:63], v4 offset:52224\n"
"s_add_u32 m0, s64, 8192\n"
"v_mfma_f32_16x16x32_bf16 v[28:31], a[68:71], v[32:35], v[28:31]\n"
"s_waitcnt lgkmcnt(4)\n"
"v_mfma_f32_16x16x32_bf16 v[28:31], v[100:103], v[40:43], v[28:31]\n"
"ds_read_b128 v[32:35], v4 offset:10240\n"
"v_mfma_f32_16x16x32_bf16 v[28:31], v[212:215], v[44:47], v[28:31]\n"
"buffer_load_dwordx4 v2, s[32:35], s63 offen offset:0 sc1 lds\n"
"v_mfma_f32_16x16x32_bf16 v[24:27], v[208:211], v[44:47], v[24:27]\n"
"ds_read_b128 v[36:39], v4 offset:24576\n"
"v_mfma_f32_16x16x32_bf16 v[24:27], v[96:99], v[40:43], v[24:27]\n"
"s_waitcnt lgkmcnt(4)\n"
"v_mfma_f32_16x16x32_bf16 v[24:27], a[72:75], v[48:51], v[24:27]\n"
"ds_read_b128 v[40:43], v4 offset:38912\n"
"v_mfma_f32_16x16x32_bf16 v[24:27], a[184:187], v[52:55], v[24:27]\n"
"buffer_load_dwordx4 v2, s[32:35], s63 offen offset:1024 sc1 lds\n"
"v_mfma_f32_16x16x32_bf16 v[28:31], a[188:191], v[52:55], v[28:31]\n"
"ds_read_b128 v[44:47], v4 offset:53248\n"
"v_mfma_f32_16x16x32_bf16 v[28:31], a[76:79], v[48:51], v[28:31]\n"
"s_waitcnt lgkmcnt(4)\n"
"v_mfma_f32_16x16x32_bf16 v[28:31], v[108:111], v[56:59], v[28:31]\n"
"ds_read_b128 v[48:51], v4 offset:11264\n"
"v_mfma_f32_16x16x32_bf16 v[28:31], v[220:223], v[60:63], v[28:31]\n"
"buffer_load_dwordx4 v2, s[32:35], s63 offen offset:2048 sc1 lds\n"
"v_mfma_f32_16x16x32_bf16 v[24:27], v[216:219], v[60:63], v[24:27]\n"
"ds_read_b128 v[52:55], v4 offset:25600\n"
"v_mfma_f32_16x16x32_bf16 v[24:27], v[104:107], v[56:59], v[24:27]\n"
"s_waitcnt lgkmcnt(4)\n"
"v_mfma_f32_16x16x32_bf16 v[24:27], a[80:83], v[32:35], v[24:27]\n"
"ds_read_b128 v[56:59], v4 offset:39936\n"
"v_mfma_f32_16x16x32_bf16 v[24:27], a[192:195], v[36:39], v[24:27]\n"
"buffer_load_dwordx4 v2, s[32:35], s63 offen offset:3072 sc1 lds\n"
"s_add_u32 s63, s63, 4096\n"
"v_mfma_f32_16x16x32_bf16 v[28:31], a[196:199], v[36:39], v[28:31]\n"
"ds_read_b128 v[60:63], v4 offset:54272\n"
"s_add_u32 m0, s64, 12288\n"
"v_mfma_f32_16x16x32_bf16 v[28:31], a[84:87], v[32:35], v[28:31]\n"
"s_waitcnt lgkmcnt(4)\n"
"v_mfma_f32_16x16x32_bf16 v[28:31], v[116:119], v[40:43], v[28:31]\n"
"ds_read_b128 v[32:35], v4 offset:12288\n"
"v_mfma_f32_16x16x32_bf16 v[28:31], v[228:231], v[44:47], v[28:31]\n"
"buffer_load_dwordx4 v2, s[32:35], s63 offen offset:0 sc1 lds\n"
"v_mfma_f32_16x16x32_bf16 v[24:27], v[224:227], v[44:47], v[24:27]\n"
"ds_read_b128 v[36:39], v4 offset:26624\n"
"v_mfma_f32_16x16x32_bf16 v[24:27], v[112:115], v[40:43], v[24:27]\n"
"s_waitcnt lgkmcnt(4)\n"
"v_mfma_f32_16x16x32_bf16 v[24:27], a[88:91], v[48:51], v[24:27]\n"
"ds_read_b128 v[40:43], v4 offset:40960\n"
"v_mfma_f32_16x16x32_bf16 v[24:27], a[200:203], v[52:55], v[24:27]\n"
"buffer_load_dwordx4 v2, s[32:35], s63 offen offset:1024 sc1 lds\n"
"s_add_u32 s63, s63, 1175552\n"
"v_mfma_f32_16x16x32_bf16 v[28:31], a[204:207], v[52:55], v[28:31]\n"
"ds_read_b128 v[44:47], v4 offset:55296\n"
"v_mfma_f32_16x16x32_bf16 v[28:31], a[92:95], v[48:51], v[28:31]\n"
"s_waitcnt lgkmcnt(4)\n"
"v_mfma_f32_16x16x32_bf16 v[28:31], v[124:127], v[56:59], v[28:31]\n"
"ds_read_b128 v[48:51], v4 offset:13312\n"
"v_mfma_f32_16x16x32_bf16 v[28:31], v[236:239], v[60:63], v[28:31]\n"
"v_mfma_f32_16x16x32_bf16 v[24:27], v[232:235], v[60:63], v[24:27]\n"
"ds_read_b128 v[52:55], v4 offset:27648\n"
"v_mfma_f32_16x16x32_bf16 v[24:27], v[120:123], v[56:59], v[24:27]\n"
"s_waitcnt lgkmcnt(4)\n"
"v_mfma_f32_16x16x32_bf16 v[24:27], a[96:99], v[32:35], v[24:27]\n"
"ds_read_b128 v[56:59], v4 offset:41984\n"
"v_mfma_f32_16x16x32_bf16 v[24:27], a[208:211], v[36:39], v[24:27]\n"
"s_waitcnt vmcnt(13)\n"
"s_waitcnt vmcnt(10)\n"
"s_barrier\n"
"v_mfma_f32_16x16x32_bf16 v[28:31], a[212:215], v[36:39], v[28:31]\n"
"ds_read_b128 v[60:63], v4 offset:56320\n"
"v_mfma_f32_16x16x32_bf16 v[28:31], a[100:103], v[32:35], v[28:31]\n"
"s_waitcnt lgkmcnt(4)\n"
"v_mfma_f32_16x16x32_bf16 v[28:31], v[132:135], v[40:43], v[28:31]\n"
"ds_read_b128 v[32:35], v2 offset:0\n"
"v_mfma_f32_16x16x32_bf16 v[28:31], v[244:247], v[44:47], v[28:31]\n"
"v_mfma_f32_16x16x32_bf16 v[24:27], v[240:243], v[44:47], v[24:27]\n"
"ds_read_b128 v[36:39], v2 offset:14336\n"
"v_mfma_f32_16x16x32_bf16 v[24:27], v[128:131], v[40:43], v[24:27]\n"
"s_waitcnt lgkmcnt(4)\n"
"v_mfma_f32_16x16x32_bf16 v[24:27], a[104:107], v[48:51], v[24:27]\n"
"ds_read_b128 v[40:43], v2 offset:28672\n"
"v_mfma_f32_16x16x32_bf16 v[24:27], a[216:219], v[52:55], v[24:27]\n"
"v_mfma_f32_16x16x32_bf16 v[28:31], a[220:223], v[52:55], v[28:31]\n"
"ds_read_b128 v[44:47], v2 offset:43008\n"
"v_mfma_f32_16x16x32_bf16 v[28:31], a[108:111], v[48:51], v[28:31]\n"
"s_waitcnt lgkmcnt(4)\n"
"v_mfma_f32_16x16x32_bf16 v[28:31], v[140:143], v[56:59], v[28:31]\n"
"ds_read_b128 v[48:51], v2 offset:1024\n"
"v_mfma_f32_16x16x32_bf16 v[28:31], v[252:255], v[60:63], v[28:31]\n"
"v_mfma_f32_16x16x32_bf16 v[24:27], v[248:251], v[60:63], v[24:27]\n"
"ds_read_b128 v[52:55], v2 offset:15360\n"
"s_cmp_eq_u32 s62, 0\n"
"v_mfma_f32_16x16x32_bf16 v[24:27], v[136:139], v[56:59], v[24:27]\n"
"s_cbranch_scc0 .L_dal_2\n"
"s_mov_b32 s48, 0x7fff0000\n"
"v_writelane_b32 v8, 0, 63\n"
"v_writelane_b32 v9, 0, 63\n"
"v_writelane_b32 v6, 0, 63\n"
"v_writelane_b32 v7, 0, 63\n"
"v_writelane_b32 v0, s48, 63\n"
"s_branch .L_dal_2\n"
".L_dab_2:\n"
"s_mov_b32 s62, 15\n"
"s_add_u32 s74, s74, 8192\n"
"v_lshrrev_b32 v5, 8, v2\n"
"v_lshl_add_u32 v5, v5, 4, s74\n"
"v_and_b32 v3, 128, v2\n"
"v_lshl_add_u32 v5, v3, 2, v5\n"
"v_and_b32 v3, 16, v2\n"
"v_lshl_add_u32 v5, v3, 4, v5\n"
"v_and_b32 v3, 96, v2\n"
"v_lshl_add_u32 v5, v3, 1, v5\n"
"v_lshrrev_b32 v3, 2, v2\n"
"v_add_u32 v3, s74, v3\n"
".p2align 6\n"
".L_dbl_2:\n"
"s_waitcnt lgkmcnt(4)\n"
"v_mfma_f32_16x16x32_bf16 v[24:27], a[0:3], v[32:35], 0\n"
"ds_read_b128 v[56:59], v4 offset:29696\n"
"v_mfma_f32_16x16x32_bf16 v[24:27], a[112:115], v[36:39], v[24:27]\n"
"v_mfma_f32_16x16x32_bf16 v[28:31], a[116:119], v[36:39], 0\n"
"ds_read_b128 v[60:63], v4 offset:44032\n"
"v_mfma_f32_16x16x32_bf16 v[28:31], a[4:7], v[32:35], v[28:31]\n"
"v_cvt_pk_bf16_f32 v15, v22, v23\n"
"s_waitcnt lgkmcnt(4)\n"
"v_mfma_f32_16x16x32_bf16 v[28:31], a[228:231], v[40:43], v[28:31]\n"
"ds_read_b128 v[32:35], v4 offset:2048\n"
"v_cvt_pk_bf16_f32 v14, v20, v21\n"
"v_mfma_f32_16x16x32_bf16 v[28:31], v[148:151], v[44:47], v[28:31]\n"
"v_cvt_pk_bf16_f32 v13, v18, v19\n"
"v_cvt_pk_bf16_f32 v12, v16, v17\n"
"v_mfma_f32_16x16x32_bf16 v[24:27], v[144:147], v[44:47], v[24:27]\n"
"ds_read_b128 v[36:39], v4 offset:16384\n"
"ds_write_b128 v5, v[12:15]\n"
"v_mfma_f32_16x16x32_bf16 v[24:27], a[224:227], v[40:43], v[24:27]\n"
"ds_read2st64_b32 v[12:13], v3 offset0:0 offset1:1\n"
"s_waitcnt lgkmcnt(6)\n"
"v_mfma_f32_16x16x32_bf16 v[24:27], a[8:11], v[48:51], v[24:27]\n"
"ds_read_b128 v[40:43], v4 offset:30720\n"
"ds_read2st64_b32 v[14:15], v3 offset0:2 offset1:3\n"
"v_mfma_f32_16x16x32_bf16 v[24:27], a[120:123], v[52:55], v[24:27]\n"
"v_mfma_f32_16x16x32_bf16 v[28:31], a[124:127], v[52:55], v[28:31]\n"
"ds_read_b128 v[44:47], v4 offset:45056\n"
"v_mfma_f32_16x16x32_bf16 v[28:31], a[12:15], v[48:51], v[28:31]\n"
"s_waitcnt lgkmcnt(7)\n"
"v_mfma_f32_16x16x32_bf16 v[28:31], a[236:239], v[56:59], v[28:31]\n"
"ds_read_b128 v[48:51], v4 offset:3072\n"
"v_mfma_f32_16x16x32_bf16 v[28:31], v[156:159], v[60:63], v[28:31]\n"
"s_waitcnt lgkmcnt(2)\n"
"buffer_atomic_pk_add_bf16 v12, v1, s[44:47], s65 offen offset:0 sc1\n"
"v_mfma_f32_16x16x32_bf16 v[24:27], v[152:155], v[60:63], v[24:27]\n"
"ds_read_b128 v[52:55], v4 offset:17408\n"
"v_mfma_f32_16x16x32_bf16 v[24:27], a[232:235], v[56:59], v[24:27]\n"
"buffer_atomic_pk_add_bf16 v13, v1, s[44:47], s65 offen offset:2048 sc1\n"
"v_mfma_f32_16x16x32_bf16 v[24:27], a[16:19], v[32:35], v[24:27]\n"
"ds_read_b128 v[56:59], v4 offset:31744\n"
"s_add_u32 s65, s65, 16384\n"
"v_mfma_f32_16x16x32_bf16 v[24:27], a[128:131], v[36:39], v[24:27]\n"
"s_waitcnt vmcnt(2)\n"
"s_barrier\n"
"v_mfma_f32_16x16x32_bf16 v[28:31], a[132:135], v[36:39], v[28:31]\n"
"ds_read_b128 v[60:63], v4 offset:46080\n"
"v_mfma_f32_16x16x32_bf16 v[28:31], a[20:23], v[32:35], v[28:31]\n"
"buffer_atomic_pk_add_bf16 v14, v1, s[44:47], s65 offen offset:0 sc1\n"
"s_waitcnt lgkmcnt(4)\n"
"v_mfma_f32_16x16x32_bf16 v[28:31], a[244:247], v[40:43], v[28:31]\n"
"ds_read_b128 v[32:35], v4 offset:4096\n"
"s_add_u32 m0, s64, 0\n"
"v_mfma_f32_16x16x32_bf16 v[28:31], v[164:167], v[44:47], v[28:31]\n"
"buffer_atomic_pk_add_bf16 v15, v1, s[44:47], s65 offen offset:2048 sc1\n"
"s_add_u32 s65, s65, 16384\n"
"v_mfma_f32_16x16x32_bf16 v[24:27], v[160:163], v[44:47], v[24:27]\n"
"ds_read_b128 v[36:39], v4 offset:18432\n"
"v_mfma_f32_16x16x32_bf16 v[24:27], a[240:243], v[40:43], v[24:27]\n"
"s_waitcnt lgkmcnt(4)\n"
"v_mfma_f32_16x16x32_bf16 v[24:27], a[24:27], v[48:51], v[24:27]\n"
"ds_read_b128 v[40:43], v4 offset:32768\n"
"v_mfma_f32_16x16x32_bf16 v[24:27], a[136:139], v[52:55], v[24:27]\n"
"buffer_load_dwordx4 v2, s[32:35], s63 offen offset:0 sc1 lds\n"
"v_mfma_f32_16x16x32_bf16 v[28:31], a[140:143], v[52:55], v[28:31]\n"
"ds_read_b128 v[44:47], v4 offset:47104\n"
"v_mfma_f32_16x16x32_bf16 v[28:31], a[28:31], v[48:51], v[28:31]\n"
"s_waitcnt lgkmcnt(4)\n"
"v_mfma_f32_16x16x32_bf16 v[28:31], a[252:255], v[56:59], v[28:31]\n"
"ds_read_b128 v[48:51], v4 offset:5120\n"
"v_mfma_f32_16x16x32_bf16 v[28:31], v[172:175], v[60:63], v[28:31]\n"
"buffer_load_dwordx4 v2, s[32:35], s63 offen offset:1024 sc1 lds\n"
"v_mfma_f32_16x16x32_bf16 v[24:27], v[168:171], v[60:63], v[24:27]\n"
"ds_read_b128 v[52:55], v4 offset:19456\n"
"v_mfma_f32_16x16x32_bf16 v[24:27], a[248:251], v[56:59], v[24:27]\n"
"s_waitcnt lgkmcnt(4)\n"
"v_mfma_f32_16x16x32_bf16 v[24:27], a[32:35], v[32:35], v[24:27]\n"
"ds_read_b128 v[56:59], v4 offset:33792\n"
"v_mfma_f32_16x16x32_bf16 v[24:27], a[144:147], v[36:39], v[24:27]\n"
"buffer_load_dwordx4 v2, s[32:35], s63 offen offset:2048 sc1 lds\n"
"v_mfma_f32_16x16x32_bf16 v[28:31], a[148:151], v[36:39], v[28:31]\n"
"ds_read_b128 v[60:63], v4 offset:48128\n"
"v_mfma_f32_16x16x32_bf16 v[28:31], a[36:39], v[32:35], v[28:31]\n"
"s_waitcnt lgkmcnt(4)\n"
"v_mfma_f32_16x16x32_bf16 v[28:31], v[68:71], v[40:43], v[28:31]\n"
"ds_read_b128 v[32:35], v4 offset:6144\n"
"v_mfma_f32_16x16x32_bf16 v[28:31], v[180:183], v[44:47], v[28:31]\n"
"buffer_load_dwordx4 v2, s[32:35], s63 offen offset:3072 sc1 lds\n"
"v_mfma_f32_16x16x32_bf16 v[24:27], v[176:179], v[44:47], v[24:27]\n"
"ds_read_b128 v[36:39], v4 offset:20480\n"
"v_mfma_f32_16x16x32_bf16 v[24:27], v[64:67], v[40:43], v[24:27]\n"
"s_waitcnt lgkmcnt(4)\n"
"v_mfma_f32_16x16x32_bf16 v[24:27], a[40:43], v[48:51], v[24:27]\n"
"ds_read_b128 v[40:43], v4 offset:34816\n"
"s_add_u32 s63, s63, 4096\n"
"v_mfma_f32_16x16x32_bf16 v[24:27], a[152:155], v[52:55], v[24:27]\n"
"s_add_u32 m0, s64, 4096\n"
"v_mfma_f32_16x16x32_bf16 v[28:31], a[156:159], v[52:55], v[28:31]\n"
"ds_read_b128 v[44:47], v4 offset:49152\n"
"v_mfma_f32_16x16x32_bf16 v[28:31], a[44:47], v[48:51], v[28:31]\n"
"s_waitcnt lgkmcnt(4)\n"
"v_mfma_f32_16x16x32_bf16 v[28:31], v[76:79], v[56:59], v[28:31]\n"
"ds_read_b128 v[48:51], v4 offset:7168\n"
"v_mfma_f32_16x16x32_bf16 v[28:31], v[188:191], v[60:63], v[28:31]\n"
"buffer_load_dwordx4 v2, s[32:35], s63 offen offset:0 sc1 lds\n"
"v_mfma_f32_16x16x32_bf16 v[24:27], v[184:187], v[60:63], v[24:27]\n"
"ds_read_b128 v[52:55], v4 offset:21504\n"
"v_mfma_f32_16x16x32_bf16 v[24:27], v[72:75], v[56:59], v[24:27]\n"
"s_waitcnt lgkmcnt(4)\n"
"v_mfma_f32_16x16x32_bf16 v[24:27], a[48:51], v[32:35], v[24:27]\n"
"ds_read_b128 v[56:59], v4 offset:35840\n"
"v_mfma_f32_16x16x32_bf16 v[24:27], a[160:163], v[36:39], v[24:27]\n"
"buffer_load_dwordx4 v2, s[32:35], s63 offen offset:1024 sc1 lds\n"
"v_mfma_f32_16x16x32_bf16 v[28:31], a[164:167], v[36:39], v[28:31]\n"
"ds_read_b128 v[60:63], v4 offset:50176\n"
"v_mfma_f32_16x16x32_bf16 v[28:31], a[52:55], v[32:35], v[28:31]\n"
"s_waitcnt lgkmcnt(4)\n"
"v_mfma_f32_16x16x32_bf16 v[28:31], v[84:87], v[40:43], v[28:31]\n"
"ds_read_b128 v[32:35], v4 offset:8192\n"
"v_mfma_f32_16x16x32_bf16 v[28:31], v[196:199], v[44:47], v[28:31]\n"
"buffer_load_dwordx4 v2, s[32:35], s63 offen offset:2048 sc1 lds\n"
"v_mfma_f32_16x16x32_bf16 v[24:27], v[192:195], v[44:47], v[24:27]\n"
"ds_read_b128 v[36:39], v4 offset:22528\n"
"v_mfma_f32_16x16x32_bf16 v[24:27], v[80:83], v[40:43], v[24:27]\n"
"s_waitcnt lgkmcnt(4)\n"
"v_mfma_f32_16x16x32_bf16 v[24:27], a[56:59], v[48:51], v[24:27]\n"
"ds_read_b128 v[40:43], v4 offset:36864\n"
"v_mfma_f32_16x16x32_bf16 v[24:27], a[168:171], v[52:55], v[24:27]\n"
"buffer_load_dwordx4 v2, s[32:35], s63 offen offset:3072 sc1 lds\n"
"v_mfma_f32_16x16x32_bf16 v[28:31], a[172:175], v[52:55], v[28:31]\n"
"ds_read_b128 v[44:47], v4 offset:51200\n"
"v_mfma_f32_16x16x32_bf16 v[28:31], a[60:63], v[48:51], v[28:31]\n"
"s_waitcnt lgkmcnt(4)\n"
"v_mfma_f32_16x16x32_bf16 v[28:31], v[92:95], v[56:59], v[28:31]\n"
"ds_read_b128 v[48:51], v4 offset:9216\n"
"v_mfma_f32_16x16x32_bf16 v[28:31], v[204:207], v[60:63], v[28:31]\n"
"s_add_u32 s63, s63, 4096\n"
"s_add_u32 m0, s64, 8192\n"
"v_mfma_f32_16x16x32_bf16 v[24:27], v[200:203], v[60:63], v[24:27]\n"
"ds_read_b128 v[52:55], v4 offset:23552\n"
"v_mfma_f32_16x16x32_bf16 v[24:27], v[88:91], v[56:59], v[24:27]\n"
"s_waitcnt lgkmcnt(4)\n"
"v_mfma_f32_16x16x32_bf16 v[24:27], a[64:67], v[32:35], v[24:27]\n"
"ds_read_b128 v[56:59], v4 offset:37888\n"
"v_mfma_f32_16x16x32_bf16 v[24:27], a[176:179], v[36:39], v[24:27]\n"
"buffer_load_dwordx4 v2, s[32:35], s63 offen offset:0 sc1 lds\n"
"v_mfma_f32_16x16x32_bf16 v[28:31], a[180:183], v[36:39], v[28:31]\n"
"ds_read_b128 v[60:63], v4 offset:52224\n"
"v_mfma_f32_16x16x32_bf16 v[28:31], a[68:71], v[32:35], v[28:31]\n"
"s_waitcnt lgkmcnt(4)\n"
"v_mfma_f32_16x16x32_bf16 v[28:31], v[100:103], v[40:43], v[28:31]\n"
"ds_read_b128 v[32:35], v4 offset:10240\n"
"v_mfma_f32_16x16x32_bf16 v[28:31], v[212:215], v[44:47], v[28:31]\n"
"buffer_load_dwordx4 v2, s[32:35], s63 offen offset:1024 sc1 lds\n"
"v_mfma_f32_16x16x32_bf16 v[24:27], v[208:211], v[44:47], v[24:27]\n"
"ds_read_b128 v[36:39], v4 offset:24576\n"
"v_mfma_f32_16x16x32_bf16 v[24:27], v[96:99], v[40:43], v[24:27]\n"
"s_waitcnt lgkmcnt(4)\n"
"v_mfma_f32_16x16x32_bf16 v[24:27], a[72:75], v[48:51], v[24:27]\n"
"ds_read_b128 v[40:43], v4 offset:38912\n"
"v_mfma_f32_16x16x32_bf16 v[24:27], a[184:187], v[52:55], v[24:27]\n"
"buffer_load_dwordx4 v2, s[32:35], s63 offen offset:2048 sc1 lds\n"
"v_mfma_f32_16x16x32_bf16 v[28:31], a[188:191], v[52:55], v[28:31]\n"
"ds_read_b128 v[44:47], v4 offset:53248\n"
"v_mfma_f32_16x16x32_bf16 v[28:31], a[76:79], v[48:51], v[28:31]\n"
"s_waitcnt lgkmcnt(4)\n"
"v_mfma_f32_16x16x32_bf16 v[28:31], v[108:111], v[56:59], v[28:31]\n"
"ds_read_b128 v[48:51], v4 offset:11264\n"
"v_mfma_f32_16x16x32_bf16 v[28:31], v[220:223], v[60:63], v[28:31]\n"
"v_mfma_f32_16x16x32_bf16 v[24:27], v[216:219], v[60:63], v[24:27]\n"
"ds_read_b128 v[52:55], v4 offset:25600\n"
"v_mfma_f32_16x16x32_bf16 v[24:27], v[104:107], v[56:59], v[24:27]\n"
"s_waitcnt lgkmcnt(4)\n"
"v_mfma_f32_16x16x32_bf16 v[24:27], a[80:83], v[32:35], v[24:27]\n"
"ds_read_b128 v[56:59], v4 offset:39936\n"
"v_mfma_f32_16x16x32_bf16 v[24:27], a[192:195], v[36:39], v[24:27]\n"
"buffer_load_dwordx4 v2, s[32:35], s63 offen offset:3072 sc1 lds\n"
"s_add_u32 s63, s63, 4096\n"
"v_mfma_f32_16x16x32_bf16 v[28:31], a[196:199], v[36:39], v[28:31]\n"
"ds_read_b128 v[60:63], v4 offset:54272\n"
"s_add_u32 m0, s64, 12288\n"
"v_mfma_f32_16x16x32_bf16 v[28:31], a[84:87], v[32:35], v[28:31]\n"
"s_waitcnt lgkmcnt(4)\n"
"v_mfma_f32_16x16x32_bf16 v[28:31], v[116:119], v[40:43], v[28:31]\n"
"ds_read_b128 v[32:35], v4 offset:12288\n"
"v_mfma_f32_16x16x32_bf16 v[28:31], v[228:231], v[44:47], v[28:31]\n"
"buffer_load_dwordx4 v2, s[32:35], s63 offen offset:0 sc1 lds\n"
"v_mfma_f32_16x16x32_bf16 v[24:27], v[224:227], v[44:47], v[24:27]\n"
"ds_read_b128 v[36:39], v4 offset:26624\n"
"v_mfma_f32_16x16x32_bf16 v[24:27], v[112:115], v[40:43], v[24:27]\n"
"s_waitcnt lgkmcnt(4)\n"
"v_mfma_f32_16x16x32_bf16 v[24:27], a[88:91], v[48:51], v[24:27]\n"
"ds_read_b128 v[40:43], v4 offset:40960\n"
"v_mfma_f32_16x16x32_bf16 v[24:27], a[200:203], v[52:55], v[24:27]\n"
"buffer_load_dwordx4 v2, s[32:35], s63 offen offset:1024 sc1 lds\n"
"s_add_u32 s63, s63, 1175552\n"
"v_mfma_f32_16x16x32_bf16 v[28:31], a[204:207], v[52:55], v[28:31]\n"
"ds_read_b128 v[44:47], v4 offset:55296\n"
"v_mfma_f32_16x16x32_bf16 v[28:31], a[92:95], v[48:51], v[28:31]\n"
"s_waitcnt lgkmcnt(4)\n"
"v_mfma_f32_16x16x32_bf16 v[28:31], v[124:127], v[56:59], v[28:31]\n"
"ds_read_b128 v[48:51], v4 offset:13312\n"
"v_mfma_f32_16x16x32_bf16 v[28:31], v[236:239], v[60:63], v[28:31]\n"
"v_mfma_f32_16x16x32_bf16 v[24:27], v[232:235], v[60:63], v[24:27]\n"
"ds_read_b128 v[52:55], v4 offset:27648\n"
"v_mfma_f32_16x16x32_bf16 v[24:27], v[120:123], v[56:59], v[24:27]\n"
"s_waitcnt lgkmcnt(4)\n"
"v_mfma_f32_16x16x32_bf16 v[24:27], a[96:99], v[32:35], v[24:27]\n"
"ds_read_b128 v[56:59], v4 offset:41984\n"
"v_mfma_f32_16x16x32_bf16 v[24:27], a[208:211], v[36:39], v[24:27]\n"
"s_waitcnt vmcnt(10)\n"
"s_barrier\n"
"v_mfma_f32_16x16x32_bf16 v[28:31], a[212:215], v[36:39], v[28:31]\n"
"ds_read_b128 v[60:63], v4 offset:56320\n"
"v_mfma_f32_16x16x32_bf16 v[28:31], a[100:103], v[32:35], v[28:31]\n"
"s_waitcnt lgkmcnt(4)\n"
"v_mfma_f32_16x16x32_bf16 v[28:31], v[132:135], v[40:43], v[28:31]\n"
"ds_read_b128 v[32:35], v2 offset:0\n"
"v_mfma_f32_16x16x32_bf16 v[28:31], v[244:247], v[44:47], v[28:31]\n"
"v_mfma_f32_16x16x32_bf16 v[24:27], v[240:243], v[44:47], v[24:27]\n"
"ds_read_b128 v[36:39], v2 offset:14336\n"
"v_mfma_f32_16x16x32_bf16 v[24:27], v[128:131], v[40:43], v[24:27]\n"
"s_waitcnt lgkmcnt(4)\n"
"v_mfma_f32_16x16x32_bf16 v[24:27], a[104:107], v[48:51], v[24:27]\n"
"ds_read_b128 v[40:43], v2 offset:28672\n"
"v_mfma_f32_16x16x32_bf16 v[24:27], a[216:219], v[52:55], v[24:27]\n"
"v_mfma_f32_16x16x32_bf16 v[28:31], a[220:223], v[52:55], v[28:31]\n"
"ds_read_b128 v[44:47], v2 offset:43008\n"
"v_mfma_f32_16x16x32_bf16 v[28:31], a[108:111], v[48:51], v[28:31]\n"
"s_waitcnt lgkmcnt(4)\n"
"v_mfma_f32_16x16x32_bf16 v[28:31], v[140:143], v[56:59], v[28:31]\n"
"ds_read_b128 v[48:51], v2 offset:1024\n"
"v_mfma_f32_16x16x32_bf16 v[28:31], v[252:255], v[60:63], v[28:31]\n"
"v_mfma_f32_16x16x32_bf16 v[24:27], v[248:251], v[60:63], v[24:27]\n"
"ds_read_b128 v[52:55], v2 offset:15360\n"
"s_sub_u32 s62, s62, 1\n"
"v_mfma_f32_16x16x32_bf16 v[24:27], v[136:139], v[56:59], v[24:27]\n"
"s_cbranch_scc1 .L_dbt_2\n"
"s_waitcnt lgkmcnt(4)\n"
"v_mfma_f32_16x16x32_bf16 v[16:19], a[0:3], v[32:35], 0\n"
"ds_read_b128 v[56:59], v2 offset:29696\n"
"v_mfma_f32_16x16x32_bf16 v[16:19], a[112:115], v[36:39], v[16:19]\n"
"v_mfma_f32_16x16x32_bf16 v[20:23], a[116:119], v[36:39], 0\n"
"ds_read_b128 v[60:63], v2 offset:44032\n"
"v_mfma_f32_16x16x32_bf16 v[20:23], a[4:7], v[32:35], v[20:23]\n"
"v_cvt_pk_bf16_f32 v15, v30, v31\n"
"s_waitcnt lgkmcnt(4)\n"
"v_mfma_f32_16x16x32_bf16 v[20:23], a[228:231], v[40:43], v[20:23]\n"
"ds_read_b128 v[32:35], v2 offset:2048\n"
"v_cvt_pk_bf16_f32 v14, v28, v29\n"
"v_mfma_f32_16x16x32_bf16 v[20:23], v[148:151], v[44:47], v[20:23]\n"
"v_cvt_pk_bf16_f32 v13, v26, v27\n"
"v_cvt_pk_bf16_f32 v12, v24, v25\n"
"v_mfma_f32_16x16x32_bf16 v[16:19], v[144:147], v[44:47], v[16:19]\n"
"ds_read_b128 v[36:39], v2 offset:16384\n"
"ds_write_b128 v5, v[12:15]\n"
"v_mfma_f32_16x16x32_bf16 v[16:19], a[224:227], v[40:43], v[16:19]\n"
"ds_read2st64_b32 v[12:13], v3 offset0:0 offset1:1\n"
"s_waitcnt lgkmcnt(6)\n"
"v_mfma_f32_16x16x32_bf16 v[16:19], a[8:11], v[48:51], v[16:19]\n"
"ds_read_b128 v[40:43], v2 offset:30720\n"
"ds_read2st64_b32 v[14:15], v3 offset0:2 offset1:3\n"
"v_mfma_f32_16x16x32_bf16 v[16:19], a[120:123], v[52:55], v[16:19]\n"
"v_mfma_f32_16x16x32_bf16 v[20:23], a[124:127], v[52:55], v[20:23]\n"
"ds_read_b128 v[44:47], v2 offset:45056\n"
"v_mfma_f32_16x16x32_bf16 v[20:23], a[12:15], v[48:51], v[20:23]\n"
"s_waitcnt lgkmcnt(7)\n"
"v_mfma_f32_16x16x32_bf16 v[20:23], a[236:239], v[56:59], v[20:23]\n"
"ds_read_b128 v[48:51], v2 offset:3072\n"
"v_mfma_f32_16x16x32_bf16 v[20:23], v[156:159], v[60:63], v[20:23]\n"
"s_waitcnt lgkmcnt(2)\n"
"buffer_atomic_pk_add_bf16 v12, v1, s[44:47], s65 offen offset:0 sc1\n"
"v_mfma_f32_16x16x32_bf16 v[16:19], v[152:155], v[60:63], v[16:19]\n"
"ds_read_b128 v[52:55], v2 offset:17408\n"
"v_mfma_f32_16x16x32_bf16 v[16:19], a[232:235], v[56:59], v[16:19]\n"
"buffer_atomic_pk_add_bf16 v13, v1, s[44:47], s65 offen offset:2048 sc1\n"
"v_mfma_f32_16x16x32_bf16 v[16:19], a[16:19], v[32:35], v[16:19]\n"
"ds_read_b128 v[56:59], v2 offset:31744\n"
"s_add_u32 s65, s65, 16384\n"
"v_mfma_f32_16x16x32_bf16 v[16:19], a[128:131], v[36:39], v[16:19]\n"
"s_waitcnt vmcnt(2)\n"
"s_barrier\n"
"v_mfma_f32_16x16x32_bf16 v[20:23], a[132:135], v[36:39], v[20:23]\n"
"ds_read_b128 v[60:63], v2 offset:46080\n"
"v_mfma_f32_16x16x32_bf16 v[20:23], a[20:23], v[32:35], v[20:23]\n"
"buffer_atomic_pk_add_bf16 v14, v1, s[44:47], s65 offen offset:0 sc1\n"
"s_waitcnt lgkmcnt(4)\n"
"v_mfma_f32_16x16x32_bf16 v[20:23], a[244:247], v[40:43], v[20:23]\n"
"ds_read_b128 v[32:35], v2 offset:4096\n"
"s_add_u32 m0, s64, 57344\n"
"v_mfma_f32_16x16x32_bf16 v[20:23], v[164:167], v[44:47], v[20:23]\n"
"buffer_atomic_pk_add_bf16 v15, v1, s[44:47], s65 offen offset:2048 sc1\n"
"s_add_u32 s65, s65, 16384\n"
"v_mfma_f32_16x16x32_bf16 v[16:19], v[160:163], v[44:47], v[16:19]\n"
"ds_read_b128 v[36:39], v2 offset:18432\n"
"v_mfma_f32_16x16x32_bf16 v[16:19], a[240:243], v[40:43], v[16:19]\n"
"s_waitcnt lgkmcnt(4)\n"
"v_mfma_f32_16x16x32_bf16 v[16:19], a[24:27], v[48:51], v[16:19]\n"
"ds_read_b128 v[40:43], v2 offset:32768\n"
"v_mfma_f32_16x16x32_bf16 v[16:19], a[136:139], v[52:55], v[16:19]\n"
"buffer_load_dwordx4 v2, s[32:35], s63 offen offset:0 sc1 lds\n"
"v_mfma_f32_16x16x32_bf16 v[20:23], a[140:143], v[52:55], v[20:23]\n"
"ds_read_b128 v[44:47], v2 offset:47104\n"
"v_mfma_f32_16x16x32_bf16 v[20:23], a[28:31], v[48:51], v[20:23]\n"
"s_waitcnt lgkmcnt(4)\n"
"v_mfma_f32_16x16x32_bf16 v[20:23], a[252:255], v[56:59], v[20:23]\n"
"ds_read_b128 v[48:51], v2 offset:5120\n"
"v_mfma_f32_16x16x32_bf16 v[20:23], v[172:175], v[60:63], v[20:23]\n"
"buffer_load_dwordx4 v2, s[32:35], s63 offen offset:1024 sc1 lds\n"
"v_mfma_f32_16x16x32_bf16 v[16:19], v[168:171], v[60:63], v[16:19]\n"
"ds_read_b128 v[52:55], v2 offset:19456\n"
"v_mfma_f32_16x16x32_bf16 v[16:19], a[248:251], v[56:59], v[16:19]\n"
"s_waitcnt lgkmcnt(4)\n"
"v_mfma_f32_16x16x32_bf16 v[16:19], a[32:35], v[32:35], v[16:19]\n"
"ds_read_b128 v[56:59], v2 offset:33792\n"
"v_mfma_f32_16x16x32_bf16 v[16:19], a[144:147], v[36:39], v[16:19]\n"
"buffer_load_dwordx4 v2, s[32:35], s63 offen offset:2048 sc1 lds\n"
"v_mfma_f32_16x16x32_bf16 v[20:23], a[148:151], v[36:39], v[20:23]\n"
"ds_read_b128 v[60:63], v2 offset:48128\n"
"v_mfma_f32_16x16x32_bf16 v[20:23], a[36:39], v[32:35], v[20:23]\n"
"s_waitcnt lgkmcnt(4)\n"
"v_mfma_f32_16x16x32_bf16 v[20:23], v[68:71], v[40:43], v[20:23]\n"
"ds_read_b128 v[32:35], v2 offset:6144\n"
"v_mfma_f32_16x16x32_bf16 v[20:23], v[180:183], v[44:47], v[20:23]\n"
"buffer_load_dwordx4 v2, s[32:35], s63 offen offset:3072 sc1 lds\n"
"v_mfma_f32_16x16x32_bf16 v[16:19], v[176:179], v[44:47], v[16:19]\n"
"ds_read_b128 v[36:39], v2 offset:20480\n"
"v_mfma_f32_16x16x32_bf16 v[16:19], v[64:67], v[40:43], v[16:19]\n"
"s_waitcnt lgkmcnt(4)\n"
"v_mfma_f32_16x16x32_bf16 v[16:19], a[40:43], v[48:51], v[16:19]\n"
"ds_read_b128 v[40:43], v2 offset:34816\n"
"s_add_u32 s63, s63, 4096\n"
"v_mfma_f32_16x16x32_bf16 v[16:19], a[152:155], v[52:55], v[16:19]\n"
"s_add_u32 m0, s64, 61440\n"
"v_mfma_f32_16x16x32_bf16 v[20:23], a[156:159], v[52:55], v[20:23]\n"
"ds_read_b128 v[44:47], v2 offset:49152\n"
"v_mfma_f32_16x16x32_bf16 v[20:23], a[44:47], v[48:51], v[20:23]\n"
"s_waitcnt lgkmcnt(4)\n"
"v_mfma_f32_16x16x32_bf16 v[20:23], v[76:79], v[56:59], v[20:23]\n"
"ds_read_b128 v[48:51], v2 offset:7168\n"
"v_mfma_f32_16x16x32_bf16 v[20:23], v[188:191], v[60:63], v[20:23]\n"
"buffer_load_dwordx4 v2, s[32:35], s63 offen offset:0 sc1 lds\n"
"v_mfma_f32_16x16x32_bf16 v[16:19], v[184:187], v[60:63], v[16:19]\n"
"ds_read_b128 v[52:55], v2 offset:21504\n"
"v_mfma_f32_16x16x32_bf16 v[16:19], v[72:75], v[56:59], v[16:19]\n"
"s_waitcnt lgkmcnt(4)\n"
"v_mfma_f32_16x16x32_bf16 v[16:19], a[48:51], v[32:35], v[16:19]\n"
"ds_read_b128 v[56:59], v2 offset:35840\n"
"v_mfma_f32_16x16x32_bf16 v[16:19], a[160:163], v[36:39], v[16:19]\n"
"buffer_load_dwordx4 v2, s[32:35], s63 offen offset:1024 sc1 lds\n"
"v_mfma_f32_16x16x32_bf16 v[20:23], a[164:167], v[36:39], v[20:23]\n"
"ds_read_b128 v[60:63], v2 offset:50176\n"
"v_mfma_f32_16x16x32_bf16 v[20:23], a[52:55], v[32:35], v[20:23]\n"
"s_waitcnt lgkmcnt(4)\n"
"v_mfma_f32_16x16x32_bf16 v[20:23], v[84:87], v[40:43], v[20:23]\n"
"ds_read_b128 v[32:35], v2 offset:8192\n"
"v_mfma_f32_16x16x32_bf16 v[20:23], v[196:199], v[44:47], v[20:23]\n"
"buffer_load_dwordx4 v2, s[32:35], s63 offen offset:2048 sc1 lds\n"
"v_mfma_f32_16x16x32_bf16 v[16:19], v[192:195], v[44:47], v[16:19]\n"
"ds_read_b128 v[36:39], v2 offset:22528\n"
"v_mfma_f32_16x16x32_bf16 v[16:19], v[80:83], v[40:43], v[16:19]\n"
"s_waitcnt lgkmcnt(4)\n"
"v_mfma_f32_16x16x32_bf16 v[16:19], a[56:59], v[48:51], v[16:19]\n"
"ds_read_b128 v[40:43], v2 offset:36864\n"
"v_mfma_f32_16x16x32_bf16 v[16:19], a[168:171], v[52:55], v[16:19]\n"
"buffer_load_dwordx4 v2, s[32:35], s63 offen offset:3072 sc1 lds\n"
"v_mfma_f32_16x16x32_bf16 v[20:23], a[172:175], v[52:55], v[20:23]\n"
"ds_read_b128 v[44:47], v2 offset:51200\n"
"v_mfma_f32_16x16x32_bf16 v[20:23], a[60:63], v[48:51], v[20:23]\n"
"s_waitcnt lgkmcnt(4)\n"
"v_mfma_f32_16x16x32_bf16 v[20:23], v[92:95], v[56:59], v[20:23]\n"
"ds_read_b128 v[48:51], v2 offset:9216\n"
"v_mfma_f32_16x16x32_bf16 v[20:23], v[204:207], v[60:63], v[20:23]\n"
"s_add_u32 s63, s63, 4096\n"
"s_add_u32 m0, s64, 65536\n"
"v_mfma_f32_16x16x32_bf16 v[16:19], v[200:203], v[60:63], v[16:19]\n"
"ds_read_b128 v[52:55], v2 offset:23552\n"
"v_mfma_f32_16x16x32_bf16 v[16:19], v[88:91], v[56:59], v[16:19]\n"
"s_waitcnt lgkmcnt(4)\n"
"v_mfma_f32_16x16x32_bf16 v[16:19], a[64:67], v[32:35], v[16:19]\n"
"ds_read_b128 v[56:59], v2 offset:37888\n"
"v_mfma_f32_16x16x32_bf16 v[16:19], a[176:179], v[36:39], v[16:19]\n"
"buffer_load_dwordx4 v2, s[32:35], s63 offen offset:0 sc1 lds\n"
"v_mfma_f32_16x16x32_bf16 v[20:23], a[180:183], v[36:39], v[20:23]\n"
"ds_read_b128 v[60:63], v2 offset:52224\n"
"v_mfma_f32_16x16x32_bf16 v[20:23], a[68:71], v[32:35], v[20:23]\n"
"s_waitcnt lgkmcnt(4)\n"
"v_mfma_f32_16x16x32_bf16 v[20:23], v[100:103], v[40:43], v[20:23]\n"
"ds_read_b128 v[32:35], v2 offset:10240\n"
"v_mfma_f32_16x16x32_bf16 v[20:23], v[212:215], v[44:47], v[20:23]\n"
"buffer_load_dwordx4 v2, s[32:35], s63 offen offset:1024 sc1 lds\n"
"v_mfma_f32_16x16x32_bf16 v[16:19], v[208:211], v[44:47], v[16:19]\n"
"ds_read_b128 v[36:39], v2 offset:24576\n"
"v_mfma_f32_16x16x32_bf16 v[16:19], v[96:99], v[40:43], v[16:19]\n"
"s_waitcnt lgkmcnt(4)\n"
"v_mfma_f32_16x16x32_bf16 v[16:19], a[72:75], v[48:51], v[16:19]\n"
"ds_read_b128 v[40:43], v2 offset:38912\n"
"v_mfma_f32_16x16x32_bf16 v[16:19], a[184:187], v[52:55], v[16:19]\n"
"buffer_load_dwordx4 v2, s[32:35], s63 offen offset:2048 sc1 lds\n"
"v_mfma_f32_16x16x32_bf16 v[20:23], a[188:191], v[52:55], v[20:23]\n"
"ds_read_b128 v[44:47], v2 offset:53248\n"
"v_mfma_f32_16x16x32_bf16 v[20:23], a[76:79], v[48:51], v[20:23]\n"
"s_waitcnt lgkmcnt(4)\n"
"v_mfma_f32_16x16x32_bf16 v[20:23], v[108:111], v[56:59], v[20:23]\n"
"ds_read_b128 v[48:51], v2 offset:11264\n"
"v_mfma_f32_16x16x32_bf16 v[20:23], v[220:223], v[60:63], v[20:23]\n"
"v_mfma_f32_16x16x32_bf16 v[16:19], v[216:219], v[60:63], v[16:19]\n"
"ds_read_b128 v[52:55], v2 offset:25600\n"
"v_mfma_f32_16x16x32_bf16 v[16:19], v[104:107], v[56:59], v[16:19]\n"
"s_waitcnt lgkmcnt(4)\n"
"v_mfma_f32_16x16x32_bf16 v[16:19], a[80:83], v[32:35], v[16:19]\n"
"ds_read_b128 v[56:59], v2 offset:39936\n"
"v_mfma_f32_16x16x32_bf16 v[16:19], a[192:195], v[36:39], v[16:19]\n"
"buffer_load_dwordx4 v2, s[32:35], s63 offen offset:3072 sc1 lds\n"
"s_add_u32 s63, s63, 4096\n"
"v_mfma_f32_16x16x32_bf16 v[20:23], a[196:199], v[36:39], v[20:23]\n"
"ds_read_b128 v[60:63], v2 offset:54272\n"
"s_add_u32 m0, s64, 69632\n"
"v_mfma_f32_16x16x32_bf16 v[20:23], a[84:87], v[32:35], v[20:23]\n"
"s_waitcnt lgkmcnt(4)\n"
"v_mfma_f32_16x16x32_bf16 v[20:23], v[116:119], v[40:43], v[20:23]\n"
"ds_read_b128 v[32:35], v2 offset:12288\n"
"v_mfma_f32_16x16x32_bf16 v[20:23], v[228:231], v[44:47], v[20:23]\n"
"buffer_load_dwordx4 v2, s[32:35], s63 offen offset:0 sc1 lds\n"
"v_mfma_f32_16x16x32_bf16 v[16:19], v[224:227], v[44:47], v[16:19]\n"
"ds_read_b128 v[36:39], v2 offset:26624\n"
"v_mfma_f32_16x16x32_bf16 v[16:19], v[112:115], v[40:43], v[16:19]\n"
"s_waitcnt lgkmcnt(4)\n"
"v_mfma_f32_16x16x32_bf16 v[16:19], a[88:91], v[48:51], v[16:19]\n"
"ds_read_b128 v[40:43], v2 offset:40960\n"
"v_mfma_f32_16x16x32_bf16 v[16:19], a[200:203], v[52:55], v[16:19]\n"
"buffer_load_dwordx4 v2, s[32:35], s63 offen offset:1024 sc1 lds\n"
"s_add_u32 s63, s63, 1175552\n"
"v_mfma_f32_16x16x32_bf16 v[20:23], a[204:207], v[52:55], v[20:23]\n"
"ds_read_b128 v[44:47], v2 offset:55296\n"
"v_mfma_f32_16x16x32_bf16 v[20:23], a[92:95], v[48:51], v[20:23]\n"
"s_waitcnt lgkmcnt(4)\n"
"v_mfma_f32_16x16x32_bf16 v[20:23], v[124:127], v[56:59], v[20:23]\n"
"ds_read_b128 v[48:51], v2 offset:13312\n"
"v_mfma_f32_16x16x32_bf16 v[20:23], v[236:239], v[60:63], v[20:23]\n"
"v_mfma_f32_16x16x32_bf16 v[16:19], v[232:235], v[60:63], v[16:19]\n"
"ds_read_b128 v[52:55], v2 offset:27648\n"
"v_mfma_f32_16x16x32_bf16 v[16:19], v[120:123], v[56:59], v[16:19]\n"
"s_waitcnt lgkmcnt(4)\n"
"v_mfma_f32_16x16x32_bf16 v[16:19], a[96:99], v[32:35], v[16:19]\n"
"ds_read_b128 v[56:59], v2 offset:41984\n"
"v_mfma_f32_16x16x32_bf16 v[16:19], a[208:211], v[36:39], v[16:19]\n"
"s_waitcnt vmcnt(10)\n"
"s_barrier\n"
"v_mfma_f32_16x16x32_bf16 v[20:23], a[212:215], v[36:39], v[20:23]\n"
"ds_read_b128 v[60:63], v2 offset:56320\n"
"v_mfma_f32_16x16x32_bf16 v[20:23], a[100:103], v[32:35], v[20:23]\n"
"s_waitcnt lgkmcnt(4)\n"
"v_mfma_f32_16x16x32_bf16 v[20:23], v[132:135], v[40:43], v[20:23]\n"
"ds_read_b128 v[32:35], v4 offset:0\n"
"v_mfma_f32_16x16x32_bf16 v[20:23], v[244:247], v[44:47], v[20:23]\n"
"v_mfma_f32_16x16x32_bf16 v[16:19], v[240:243], v[44:47], v[16:19]\n"
"ds_read_b128 v[36:39], v4 offset:14336\n"
"v_mfma_f32_16x16x32_bf16 v[16:19], v[128:131], v[40:43], v[16:19]\n"
"s_waitcnt lgkmcnt(4)\n"
"v_mfma_f32_16x16x32_bf16 v[16:19], a[104:107], v[48:51], v[16:19]\n"
"ds_read_b128 v[40:43], v4 offset:28672\n"
"v_mfma_f32_16x16x32_bf16 v[16:19], a[216:219], v[52:55], v[16:19]\n"
"v_mfma_f32_16x16x32_bf16 v[20:23], a[220:223], v[52:55], v[20:23]\n"
"ds_read_b128 v[44:47], v4 offset:43008\n"
"v_mfma_f32_16x16x32_bf16 v[20:23], a[108:111], v[48:51], v[20:23]\n"
"s_waitcnt lgkmcnt(4)\n"
"v_mfma_f32_16x16x32_bf16 v[20:23], v[140:143], v[56:59], v[20:23]\n"
"ds_read_b128 v[48:51], v4 offset:1024\n"
"v_mfma_f32_16x16x32_bf16 v[20:23], v[252:255], v[60:63], v[20:23]\n"
"v_mfma_f32_16x16x32_bf16 v[16:19], v[248:251], v[60:63], v[16:19]\n"
"ds_read_b128 v[52:55], v4 offset:15360\n"
"v_mfma_f32_16x16x32_bf16 v[16:19], v[136:139], v[56:59], v[16:19]\n"
"s_branch .L_dbl_2\n"
".L_dbt_2:\n"
"s_nop 1\n"
"s_nop 0\n"
"v_cvt_pk_bf16_f32 v15, v30, v31\n"
"v_cvt_pk_bf16_f32 v14, v28, v29\n"
"s_nop 1\n"
"v_cvt_pk_bf16_f32 v13, v26, v27\n"
"v_cvt_pk_bf16_f32 v12, v24, v25\n"
"ds_write_b128 v5, v[12:15]\n"
"ds_read2st64_b32 v[12:13], v3 offset0:0 offset1:1\n"
"ds_read2st64_b32 v[14:15], v3 offset0:2 offset1:3\n"
"s_waitcnt lgkmcnt(0)\n"
"buffer_atomic_pk_add_bf16 v12, v1, s[44:47], s65 offen offset:0 sc1\n"
"buffer_atomic_pk_add_bf16 v13, v1, s[44:47], s65 offen offset:2048 sc1\n"
"s_add_u32 s65, s65, 16384\n"
"buffer_atomic_pk_add_bf16 v14, v1, s[44:47], s65 offen offset:0 sc1\n"
"buffer_atomic_pk_add_bf16 v15, v1, s[44:47], s65 offen offset:2048 sc1\n"
"s_add_u32 s65, s65, 16384\n"
"s_or_b32 s48, s28, s27\n"
"s_or_b32 s48, s48, s26\n"
"s_cbranch_scc1 .L_end\n"
"v_mov_b32 v8, s54\n"
"v_mov_b32 v9, s55\n"
"buffer_store_dwordx2 v[8:9], off, s[32:35], 0 sc1\n"
"s_branch .L_end\n"
".p2align 6\n"
".L_dhv3:\n"
".L_dal_3:\n"
"s_waitcnt lgkmcnt(4)\n"
"v_mfma_f32_16x16x32_bf16 v[16:19], a[0:3], v[32:35], 0\n"
"ds_read_b128 v[56:59], v2 offset:29696\n"
"ds_read_b128 v[12:15], v5 offset:8192\n"
"v_mfma_f32_16x16x32_bf16 v[16:19], a[112:115], v[36:39], v[16:19]\n"
"v_mfma_f32_16x16x32_bf16 v[20:23], a[116:119], v[36:39], 0\n"
"ds_read_b128 v[60:63], v2 offset:44032\n"
"v_mfma_f32_16x16x32_bf16 v[20:23], a[4:7], v[32:35], v[20:23]\n"
"s_waitcnt lgkmcnt(5)\n"
"v_mfma_f32_16x16x32_bf16 v[20:23], a[228:231], v[40:43], v[20:23]\n"
"ds_read_b128 v[32:35], v2 offset:2048\n"
"v_mfma_f32_16x16x32_bf16 v[20:23], v[148:151], v[44:47], v[20:23]\n"
"s_waitcnt lgkmcnt(2)\n"
"v_add_f32 v24, v24, v12\n"
"v_mfma_f32_16x16x32_bf16 v[16:19], v[144:147], v[44:47], v[16:19]\n"
"ds_read_b128 v[36:39], v2 offset:16384\n"
"v_add_f32 v25, v25, v13\n"
"v_mfma_f32_16x16x32_bf16 v[16:19], a[224:227], v[40:43], v[16:19]\n"
"v_add_f32 v26, v26, v14\n"
"v_mfma_f32_16x16x32_bf16 v[16:19], a[8:11], v[48:51], v[16:19]\n"
"ds_read_b128 v[40:43], v2 offset:30720\n"
"v_add_f32 v27, v27, v15\n"
"v_mfma_f32_16x16x32_bf16 v[16:19], a[120:123], v[52:55], v[16:19]\n"
"ds_read_b128 v[12:15], v5 offset:9216\n"
"v_mfma_f32_16x16x32_bf16 v[20:23], a[124:127], v[52:55], v[20:23]\n"
"ds_read_b128 v[44:47], v2 offset:45056\n"
"v_mfma_f32_16x16x32_bf16 v[20:23], a[12:15], v[48:51], v[20:23]\n"
"s_waitcnt lgkmcnt(5)\n"
"v_mfma_f32_16x16x32_bf16 v[20:23], a[236:239], v[56:59], v[20:23]\n"
"ds_read_b128 v[48:51], v2 offset:3072\n"
"v_mfma_f32_16x16x32_bf16 v[20:23], v[156:159], v[60:63], v[20:23]\n"
"v_mfma_f32_16x16x32_bf16 v[16:19], v[152:155], v[60:63], v[16:19]\n"
"ds_read_b128 v[52:55], v2 offset:17408\n"
"s_waitcnt lgkmcnt(3)\n"
"v_mfma_f32_16x16x32_bf16 v[16:19], a[232:235], v[56:59], v[16:19]\n"
"v_add_f32 v28, v28, v12\n"
"v_mfma_f32_16x16x32_bf16 v[16:19], a[16:19], v[32:35], v[16:19]\n"
"s_waitcnt vmcnt(0)\n"
"s_barrier\n"
"v_mfma_f32_16x16x32_bf16 v[16:19], a[128:131], v[36:39], v[16:19]\n"
"ds_read_b128 v[56:59], v2 offset:31744\n"
"v_add_f32 v29, v29, v13\n"
"v_mfma_f32_16x16x32_bf16 v[20:23], a[132:135], v[36:39], v[20:23]\n"
"ds_read_b128 v[60:63], v2 offset:46080\n"
"v_add_f32 v30, v30, v14\n"
"v_mfma_f32_16x16x32_bf16 v[20:23], a[20:23], v[32:35], v[20:23]\n"
"v_add_f32 v31, v31, v15\n"
"s_waitcnt lgkmcnt(4)\n"
"v_mfma_f32_16x16x32_bf16 v[20:23], a[244:247], v[40:43], v[20:23]\n"
"ds_read_b128 v[32:35], v2 offset:4096\n"
"s_add_u32 m0, s64, 57344\n"
"v_mfma_f32_16x16x32_bf16 v[20:23], v[164:167], v[44:47], v[20:23]\n"
"buffer_load_dwordx4 v2, s[32:35], s63 offen offset:0 sc1 lds\n"
"v_cvt_pk_bf16_f32 v12, v24, v25\n"
"v_mfma_f32_16x16x32_bf16 v[16:19], v[160:163], v[44:47], v[16:19]\n"
"ds_read_b128 v[36:39], v2 offset:18432\n"
"v_cvt_pk_bf16_f32 v13, v26, v27\n"
"v_mfma_f32_16x16x32_bf16 v[16:19], a[240:243], v[40:43], v[16:19]\n"
"v_cvt_pk_bf16_f32 v14, v28, v29\n"
"s_waitcnt lgkmcnt(4)\n"
"v_mfma_f32_16x16x32_bf16 v[16:19], a[24:27], v[48:51], v[16:19]\n"
"ds_read_b128 v[40:43], v2 offset:32768\n"
"v_cvt_pk_bf16_f32 v15, v30, v31\n"
"v_mfma_f32_16x16x32_bf16 v[16:19], a[136:139], v[52:55], v[16:19]\n"
"buffer_store_dwordx4 v[12:15], v2, s[32:35], s68 offen sc1\n"
"v_add_f32 v10, v24, v25\n"
"v_mfma_f32_16x16x32_bf16 v[20:23], a[140:143], v[52:55], v[20:23]\n"
"ds_read_b128 v[44:47], v2 offset:47104\n"
"v_mul_f32 v11, v24, v24\n"
"v_mfma_f32_16x16x32_bf16 v[20:23], a[28:31], v[48:51], v[20:23]\n"
"v_fmac_f32 v11, v25, v25\n"
"s_waitcnt lgkmcnt(4)\n"
"v_mfma_f32_16x16x32_bf16 v[20:23], a[252:255], v[56:59], v[20:23]\n"
"ds_read_b128 v[48:51], v2 offset:5120\n"
"v_add_f32 v10, v10, v26\n"
"v_mfma_f32_16x16x32_bf16 v[20:23], v[172:175], v[60:63], v[20:23]\n"
"buffer_load_dwordx4 v2, s[32:35], s63 offen offset:1024 sc1 lds\n"
"v_fmac_f32 v11, v26, v26\n"
"v_mfma_f32_16x16x32_bf16 v[16:19], v[168:171], v[60:63], v[16:19]\n"
"ds_read_b128 v[52:55], v2 offset:19456\n"
"v_add_f32 v10, v10, v27\n"
"v_mfma_f32_16x16x32_bf16 v[16:19], a[248:251], v[56:59], v[16:19]\n"
"v_fmac_f32 v11, v27, v27\n"
"s_waitcnt lgkmcnt(4)\n"
"v_mfma_f32_16x16x32_bf16 v[16:19], a[32:35], v[32:35], v[16:19]\n"
"ds_read_b128 v[56:59], v2 offset:33792\n"
"v_add_f32 v10, v10, v28\n"
"v_mfma_f32_16x16x32_bf16 v[16:19], a[144:147], v[36:39], v[16:19]\n"
"buffer_load_dwordx4 v2, s[32:35], s63 offen offset:2048 sc1 lds\n"
"v_fmac_f32 v11, v28, v28\n"
"v_mfma_f32_16x16x32_bf16 v[20:23], a[148:151], v[36:39], v[20:23]\n"
"ds_read_b128 v[60:63], v2 offset:48128\n"
"v_add_f32 v10, v10, v29\n"
"v_mfma_f32_16x16x32_bf16 v[20:23], a[36:39], v[32:35], v[20:23]\n"
"v_fmac_f32 v11, v29, v29\n"
"s_waitcnt lgkmcnt(4)\n"
"v_mfma_f32_16x16x32_bf16 v[20:23], v[68:71], v[40:43], v[20:23]\n"
"ds_read_b128 v[32:35], v2 offset:6144\n"
"v_add_f32 v10, v10, v30\n"
"v_mfma_f32_16x16x32_bf16 v[20:23], v[180:183], v[44:47], v[20:23]\n"
"buffer_load_dwordx4 v2, s[32:35], s63 offen offset:3072 sc1 lds\n"
"s_add_u32 m0, s74, 0\n"
"v_mfma_f32_16x16x32_bf16 v[16:19], v[176:179], v[44:47], v[16:19]\n"
"ds_read_b128 v[36:39], v2 offset:20480\n"
"v_fmac_f32 v11, v30, v30\n"
"v_mfma_f32_16x16x32_bf16 v[16:19], v[64:67], v[40:43], v[16:19]\n"
"v_add_f32 v10, v10, v31\n"
"s_waitcnt lgkmcnt(4)\n"
"v_mfma_f32_16x16x32_bf16 v[16:19], a[40:43], v[48:51], v[16:19]\n"
"ds_read_b128 v[40:43], v2 offset:34816\n"
"v_fmac_f32 v11, v31, v31\n"
"v_mfma_f32_16x16x32_bf16 v[16:19], a[152:155], v[52:55], v[16:19]\n"
"buffer_load_dwordx4 v2, s[32:35], s71 offen offset:0 sc1 lds\n"
"v_permlane16_swap_b32 v10, v11\n"
"v_mfma_f32_16x16x32_bf16 v[20:23], a[156:159], v[52:55], v[20:23]\n"
"ds_read_b128 v[44:47], v2 offset:49152\n"
"v_add_f32 v10, v10, v11\n"
"v_mfma_f32_16x16x32_bf16 v[20:23], a[44:47], v[48:51], v[20:23]\n"
"s_waitcnt lgkmcnt(4)\n"
"v_mfma_f32_16x16x32_bf16 v[20:23], v[76:79], v[56:59], v[20:23]\n"
"ds_read_b128 v[48:51], v2 offset:7168\n"
"v_permlane32_swap_b32 v10, v11\n"
"v_mfma_f32_16x16x32_bf16 v[20:23], v[188:191], v[60:63], v[20:23]\n"
"buffer_load_dwordx4 v2, s[32:35], s71 offen offset:1024 sc1 lds\n"
"s_add_u32 s71, s71, 1187840\n"
"v_mfma_f32_16x16x32_bf16 v[16:19], v[184:187], v[60:63], v[16:19]\n"
"ds_read_b128 v[52:55], v2 offset:21504\n"
"s_add_u32 s63, s63, 4096\n"
"v_mfma_f32_16x16x32_bf16 v[16:19], v[72:75], v[56:59], v[16:19]\n"
"s_add_u32 m0, s64, 61440\n"
"s_waitcnt lgkmcnt(4)\n"
"v_mfma_f32_16x16x32_bf16 v[16:19], a[48:51], v[32:35], v[16:19]\n"
"ds_read_b128 v[56:59], v2 offset:35840\n"
"v_add_f32 v10, v10, v11\n"
"v_mfma_f32_16x16x32_bf16 v[16:19], a[160:163], v[36:39], v[16:19]\n"
"buffer_store_dword v10, v3, s[32:35], s68 offen offset:1024 sc1\n"
"s_mov_b32 s67, s68\n"
"v_mfma_f32_16x16x32_bf16 v[20:23], a[164:167], v[36:39], v[20:23]\n"
"ds_read_b128 v[60:63], v2 offset:50176\n"
"s_mov_b32 s68, s70\n"
"v_mfma_f32_16x16x32_bf16 v[20:23], a[52:55], v[32:35], v[20:23]\n"
"s_add_u32 s70, s70, 1187840\n"
"s_waitcnt lgkmcnt(4)\n"
"v_mfma_f32_16x16x32_bf16 v[20:23], v[84:87], v[40:43], v[20:23]\n"
"ds_read_b128 v[32:35], v2 offset:8192\n"
"v_mfma_f32_16x16x32_bf16 v[20:23], v[196:199], v[44:47], v[20:23]\n"
"buffer_load_dwordx4 v2, s[32:35], s63 offen offset:0 sc1 lds\n"
"v_mfma_f32_16x16x32_bf16 v[16:19], v[192:195], v[44:47], v[16:19]\n"
"ds_read_b128 v[36:39], v2 offset:22528\n"
"v_mfma_f32_16x16x32_bf16 v[16:19], v[80:83], v[40:43], v[16:19]\n"
"s_waitcnt lgkmcnt(4)\n"
"v_mfma_f32_16x16x32_bf16 v[16:19], a[56:59], v[48:51], v[16:19]\n"
"ds_read_b128 v[40:43], v2 offset:36864\n"
"v_mfma_f32_16x16x32_bf16 v[16:19], a[168:171], v[52:55], v[16:19]\n"
"buffer_load_dwordx4 v2, s[32:35], s63 offen offset:1024 sc1 lds\n"
"v_mfma_f32_16x16x32_bf16 v[20:23], a[172:175], v[52:55], v[20:23]\n"
"ds_read_b128 v[44:47], v2 offset:51200\n"
"v_mfma_f32_16x16x32_bf16 v[20:23], a[60:63], v[48:51], v[20:23]\n"
"s_waitcnt lgkmcnt(4)\n"
"v_mfma_f32_16x16x32_bf16 v[20:23], v[92:95], v[56:59], v[20:23]\n"
"ds_read_b128 v[48:51], v2 offset:9216\n"
"v_mfma_f32_16x16x32_bf16 v[20:23], v[204:207], v[60:63], v[20:23]\n"
"buffer_load_dwordx4 v2, s[32:35], s63 offen offset:2048 sc1 lds\n"
"v_mfma_f32_16x16x32_bf16 v[16:19], v[200:203], v[60:63], v[16:19]\n"
"ds_read_b128 v[52:55], v2 offset:23552\n"
"v_mfma_f32_16x16x32_bf16 v[16:19], v[88:91], v[56:59], v[16:19]\n"
"s_waitcnt lgkmcnt(4)\n"
"v_mfma_f32_16x16x32_bf16 v[16:19], a[64:67], v[32:35], v[16:19]\n"
"ds_read_b128 v[56:59], v2 offset:37888\n"
"v_mfma_f32_16x16x32_bf16 v[16:19], a[176:179], v[36:39], v[16:19]\n"
"buffer_load_dwordx4 v2, s[32:35], s63 offen offset:3072 sc1 lds\n"
"s_add_u32 s63, s63, 4096\n"
"v_mfma_f32_16x16x32_bf16 v[20:23], a[180:183], v[36:39], v[20:23]\n"
"ds_read_b128 v[60:63], v2 offset:52224\n"
"s_add_u32 m0, s64, 65536\n"
"v_mfma_f32_16x16x32_bf16 v[20:23], a[68:71], v[32:35], v[20:23]\n"
"s_waitcnt lgkmcnt(4)\n"
"v_mfma_f32_16x16x32_bf16 v[20:23], v[100:103], v[40:43], v[20:23]\n"
"ds_read_b128 v[32:35], v2 offset:10240\n"
"v_mfma_f32_16x16x32_bf16 v[20:23], v[212:215], v[44:47], v[20:23]\n"
"buffer_load_dwordx4 v2, s[32:35], s63 offen offset:0 sc1 lds\n"
"v_mfma_f32_16x16x32_bf16 v[16:19], v[208:211], v[44:47], v[16:19]\n"
"ds_read_b128 v[36:39], v2 offset:24576\n"
"v_mfma_f32_16x16x32_bf16 v[16:19], v[96:99], v[40:43], v[16:19]\n"
"s_waitcnt lgkmcnt(4)\n"
"v_mfma_f32_16x16x32_bf16 v[16:19], a[72:75], v[48:51], v[16:19]\n"
"ds_read_b128 v[40:43], v2 offset:38912\n"
"v_mfma_f32_16x16x32_bf16 v[16:19], a[184:187], v[52:55], v[16:19]\n"
"buffer_load_dwordx4 v2, s[32:35], s63 offen offset:1024 sc1 lds\n"
"v_mfma_f32_16x16x32_bf16 v[20:23], a[188:191], v[52:55], v[20:23]\n"
"ds_read_b128 v[44:47], v2 offset:53248\n"
"v_mfma_f32_16x16x32_bf16 v[20:23], a[76:79], v[48:51], v[20:23]\n"
"s_waitcnt lgkmcnt(4)\n"
"v_mfma_f32_16x16x32_bf16 v[20:23], v[108:111], v[56:59], v[20:23]\n"
"ds_read_b128 v[48:51], v2 offset:11264\n"
"v_mfma_f32_16x16x32_bf16 v[20:23], v[220:223], v[60:63], v[20:23]\n"
"buffer_load_dwordx4 v2, s[32:35], s63 offen offset:2048 sc1 lds\n"
"v_mfma_f32_16x16x32_bf16 v[16:19], v[216:219], v[60:63], v[16:19]\n"
"ds_read_b128 v[52:55], v2 offset:25600\n"
"v_mfma_f32_16x16x32_bf16 v[16:19], v[104:107], v[56:59], v[16:19]\n"
"s_waitcnt lgkmcnt(4)\n"
"v_mfma_f32_16x16x32_bf16 v[16:19], a[80:83], v[32:35], v[16:19]\n"
"ds_read_b128 v[56:59], v2 offset:39936\n"
"v_mfma_f32_16x16x32_bf16 v[16:19], a[192:195], v[36:39], v[16:19]\n"
"buffer_load_dwordx4 v2, s[32:35], s63 offen offset:3072 sc1 lds\n"
"s_add_u32 s63, s63, 4096\n"
"v_mfma_f32_16x16x32_bf16 v[20:23], a[196:199], v[36:39], v[20:23]\n"
"ds_read_b128 v[60:63], v2 offset:54272\n"
"s_add_u32 m0, s64, 69632\n"
"v_mfma_f32_16x16x32_bf16 v[20:23], a[84:87], v[32:35], v[20:23]\n"
"s_waitcnt lgkmcnt(4)\n"
"v_mfma_f32_16x16x32_bf16 v[20:23], v[116:119], v[40:43], v[20:23]\n"
"ds_read_b128 v[32:35], v2 offset:12288\n"
"v_mfma_f32_16x16x32_bf16 v[20:23], v[228:231], v[44:47], v[20:23]\n"
"buffer_load_dwordx4 v2, s[32:35], s63 offen offset:0 sc1 lds\n"
"v_mfma_f32_16x16x32_bf16 v[16:19], v[224:227], v[44:47], v[16:19]\n"
"ds_read_b128 v[36:39], v2 offset:26624\n"
"v_mfma_f32_16x16x32_bf16 v[16:19], v[112:115], v[40:43], v[16:19]\n"
"s_waitcnt lgkmcnt(4)\n"
"v_mfma_f32_16x16x32_bf16 v[16:19], a[88:91], v[48:51], v[16:19]\n"
"ds_read_b128 v[40:43], v2 offset:40960\n"
"v_mfma_f32_16x16x32_bf16 v[16:19], a[200:203], v[52:55], v[16:19]\n"
"buffer_load_dwordx4 v2, s[32:35], s63 offen offset:1024 sc1 lds\n"
"s_add_u32 s63, s63, 1175552\n"
"v_mfma_f32_16x16x32_bf16 v[20:23], a[204:207], v[52:55], v[20:23]\n"
"ds_read_b128 v[44:47], v2 offset:55296\n"
"v_mfma_f32_16x16x32_bf16 v[20:23], a[92:95], v[48:51], v[20:23]\n"
"s_waitcnt lgkmcnt(4)\n"
"v_mfma_f32_16x16x32_bf16 v[20:23], v[124:127], v[56:59], v[20:23]\n"
"ds_read_b128 v[48:51], v2 offset:13312\n"
"v_mfma_f32_16x16x32_bf16 v[20:23], v[236:239], v[60:63], v[20:23]\n"
"v_mfma_f32_16x16x32_bf16 v[16:19], v[232:235], v[60:63], v[16:19]\n"
"ds_read_b128 v[52:55], v2 offset:27648\n"
"v_mfma_f32_16x16x32_bf16 v[16:19], v[120:123], v[56:59], v[16:19]\n"
"s_waitcnt lgkmcnt(4)\n"
"v_mfma_f32_16x16x32_bf16 v[16:19], a[96:99], v[32:35], v[16:19]\n"
"s_waitcnt vmcnt(13)\n"
"s_waitcnt vmcnt(10)\n"
"s_barrier\n"
"v_mfma_f32_16x16x32_bf16 v[16:19], a[208:211], v[36:39], v[16:19]\n"
"ds_read_b128 v[56:59], v2 offset:41984\n"
"v_mfma_f32_16x16x32_bf16 v[20:23], a[212:215], v[36:39], v[20:23]\n"
"ds_read_b128 v[60:63], v2 offset:56320\n"
"v_mfma_f32_16x16x32_bf16 v[20:23], a[100:103], v[32:35], v[20:23]\n"
"s_waitcnt lgkmcnt(4)\n"
"v_mfma_f32_16x16x32_bf16 v[20:23], v[132:135], v[40:43], v[20:23]\n"
"ds_read_b128 v[32:35], v4 offset:0\n"
"v_mfma_f32_16x16x32_bf16 v[20:23], v[244:247], v[44:47], v[20:23]\n"
"v_mfma_f32_16x16x32_bf16 v[16:19], v[240:243], v[44:47], v[16:19]\n"
"ds_read_b128 v[36:39], v4 offset:14336\n"
"v_mfma_f32_16x16x32_bf16 v[16:19], v[128:131], v[40:43], v[16:19]\n"
"s_waitcnt lgkmcnt(4)\n"
"v_mfma_f32_16x16x32_bf16 v[16:19], a[104:107], v[48:51], v[16:19]\n"
"ds_read_b128 v[40:43], v4 offset:28672\n"
"v_mfma_f32_16x16x32_bf16 v[16:19], a[216:219], v[52:55], v[16:19]\n"
"v_mfma_f32_16x16x32_bf16 v[20:23], a[220:223], v[52:55], v[20:23]\n"
"ds_read_b128 v[44:47], v4 offset:43008\n"
"v_mfma_f32_16x16x32_bf16 v[20:23], a[108:111], v[48:51], v[20:23]\n"
"s_waitcnt lgkmcnt(4)\n"
"v_mfma_f32_16x16x32_bf16 v[20:23], v[140:143], v[56:59], v[20:23]\n"
"ds_read_b128 v[48:51], v4 offset:1024\n"
"v_mfma_f32_16x16x32_bf16 v[20:23], v[252:255], v[60:63], v[20:23]\n"
"v_mfma_f32_16x16x32_bf16 v[16:19], v[248:251], v[60:63], v[16:19]\n"
"ds_read_b128 v[52:55], v4 offset:15360\n"
"s_sub_u32 s62, s62, 1\n"
"v_mfma_f32_16x16x32_bf16 v[16:19], v[136:139], v[56:59], v[16:19]\n"
"s_cbranch_scc1 .L_dab_3\n"
"s_waitcnt lgkmcnt(4)\n"
"v_mfma_f32_16x16x32_bf16 v[24:27], a[0:3], v[32:35], 0\n"
"ds_read_b128 v[56:59], v4 offset:29696\n"
"ds_read_b128 v[12:15], v5 offset:0\n"
"v_mfma_f32_16x16x32_bf16 v[24:27], a[112:115], v[36:39], v[24:27]\n"
"v_mfma_f32_16x16x32_bf16 v[28:31], a[116:119], v[36:39], 0\n"
"ds_read_b128 v[60:63], v4 offset:44032\n"
"v_mfma_f32_16x16x32_bf16 v[28:31], a[4:7], v[32:35], v[28:31]\n"
"s_waitcnt lgkmcnt(5)\n"
"v_mfma_f32_16x16x32_bf16 v[28:31], a[228:231], v[40:43], v[28:31]\n"
"ds_read_b128 v[32:35], v4 offset:2048\n"
"v_mfma_f32_16x16x32_bf16 v[28:31], v[148:151], v[44:47], v[28:31]\n"
"s_waitcnt lgkmcnt(2)\n"
"v_add_f32 v16, v16, v12\n"
"v_mfma_f32_16x16x32_bf16 v[24:27], v[144:147], v[44:47], v[24:27]\n"
"ds_read_b128 v[36:39], v4 offset:16384\n"
"v_add_f32 v17, v17, v13\n"
"v_mfma_f32_16x16x32_bf16 v[24:27], a[224:227], v[40:43], v[24:27]\n"
"v_add_f32 v18, v18, v14\n"
"v_mfma_f32_16x16x32_bf16 v[24:27], a[8:11], v[48:51], v[24:27]\n"
"ds_read_b128 v[40:43], v4 offset:30720\n"
"v_add_f32 v19, v19, v15\n"
"v_mfma_f32_16x16x32_bf16 v[24:27], a[120:123], v[52:55], v[24:27]\n"
"ds_read_b128 v[12:15], v5 offset:1024\n"
"v_mfma_f32_16x16x32_bf16 v[28:31], a[124:127], v[52:55], v[28:31]\n"
"ds_read_b128 v[44:47], v4 offset:45056\n"
"v_mfma_f32_16x16x32_bf16 v[28:31], a[12:15], v[48:51], v[28:31]\n"
"s_waitcnt lgkmcnt(5)\n"
"v_mfma_f32_16x16x32_bf16 v[28:31], a[236:239], v[56:59], v[28:31]\n"
"ds_read_b128 v[48:51], v4 offset:3072\n"
"v_mfma_f32_16x16x32_bf16 v[28:31], v[156:159], v[60:63], v[28:31]\n"
"v_mfma_f32_16x16x32_bf16 v[24:27], v[152:155], v[60:63], v[24:27]\n"
"ds_read_b128 v[52:55], v4 offset:17408\n"
"s_waitcnt lgkmcnt(3)\n"
"v_mfma_f32_16x16x32_bf16 v[24:27], a[232:235], v[56:59], v[24:27]\n"
"v_add_f32 v20, v20, v12\n"
"v_mfma_f32_16x16x32_bf16 v[24:27], a[16:19], v[32:35], v[24:27]\n"
"s_waitcnt vmcnt(0)\n"
"s_barrier\n"
"v_mfma_f32_16x16x32_bf16 v[24:27], a[128:131], v[36:39], v[24:27]\n"
"ds_read_b128 v[56:59], v4 offset:31744\n"
"v_add_f32 v21, v21, v13\n"
"v_mfma_f32_16x16x32_bf16 v[28:31], a[132:135], v[36:39], v[28:31]\n"
"ds_read_b128 v[60:63], v4 offset:46080\n"
"v_add_f32 v22, v22, v14\n"
"v_mfma_f32_16x16x32_bf16 v[28:31], a[20:23], v[32:35], v[28:31]\n"
"v_add_f32 v23, v23, v15\n"
"s_waitcnt lgkmcnt(4)\n"
"v_mfma_f32_16x16x32_bf16 v[28:31], a[244:247], v[40:43], v[28:31]\n"
"ds_read_b128 v[32:35], v4 offset:4096\n"
"s_add_u32 m0, s64, 0\n"
"v_mfma_f32_16x16x32_bf16 v[28:31], v[164:167], v[44:47], v[28:31]\n"
"buffer_load_dwordx4 v2, s[32:35], s63 offen offset:0 sc1 lds\n"
"v_cvt_pk_bf16_f32 v12, v16, v17\n"
"v_mfma_f32_16x16x32_bf16 v[24:27], v[160:163], v[44:47], v[24:27]\n"
"ds_read_b128 v[36:39], v4 offset:18432\n"
"v_cvt_pk_bf16_f32 v13, v18, v19\n"
"v_mfma_f32_16x16x32_bf16 v[24:27], a[240:243], v[40:43], v[24:27]\n"
"v_cvt_pk_bf16_f32 v14, v20, v21\n"
"s_waitcnt lgkmcnt(4)\n"
"v_mfma_f32_16x16x32_bf16 v[24:27], a[24:27], v[48:51], v[24:27]\n"
"ds_read_b128 v[40:43], v4 offset:32768\n"
"v_cvt_pk_bf16_f32 v15, v22, v23\n"
"v_mfma_f32_16x16x32_bf16 v[24:27], a[136:139], v[52:55], v[24:27]\n"
"buffer_store_dwordx4 v[12:15], v2, s[32:35], s69 offen sc1\n"
"v_add_f32 v10, v16, v17\n"
"v_mfma_f32_16x16x32_bf16 v[28:31], a[140:143], v[52:55], v[28:31]\n"
"ds_read_b128 v[44:47], v4 offset:47104\n"
"v_mul_f32 v11, v16, v16\n"
"v_mfma_f32_16x16x32_bf16 v[28:31], a[28:31], v[48:51], v[28:31]\n"
"v_fmac_f32 v11, v17, v17\n"
"s_waitcnt lgkmcnt(4)\n"
"v_mfma_f32_16x16x32_bf16 v[28:31], a[252:255], v[56:59], v[28:31]\n"
"ds_read_b128 v[48:51], v4 offset:5120\n"
"v_add_f32 v10, v10, v18\n"
"v_mfma_f32_16x16x32_bf16 v[28:31], v[172:175], v[60:63], v[28:31]\n"
"buffer_load_dwordx4 v2, s[32:35], s63 offen offset:1024 sc1 lds\n"
"v_fmac_f32 v11, v18, v18\n"
"v_mfma_f32_16x16x32_bf16 v[24:27], v[168:171], v[60:63], v[24:27]\n"
"ds_read_b128 v[52:55], v4 offset:19456\n"
"v_add_f32 v10, v10, v19\n"
"v_mfma_f32_16x16x32_bf16 v[24:27], a[248:251], v[56:59], v[24:27]\n"
"v_fmac_f32 v11, v19, v19\n"
"s_waitcnt lgkmcnt(4)\n"
"v_mfma_f32_16x16x32_bf16 v[24:27], a[32:35], v[32:35], v[24:27]\n"
"ds_read_b128 v[56:59], v4 offset:33792\n"
"v_add_f32 v10, v10, v20\n"
"v_mfma_f32_16x16x32_bf16 v[24:27], a[144:147], v[36:39], v[24:27]\n"
"buffer_load_dwordx4 v2, s[32:35], s63 offen offset:2048 sc1 lds\n"
"v_fmac_f32 v11, v20, v20\n"
"v_mfma_f32_16x16x32_bf16 v[28:31], a[148:151], v[36:39], v[28:31]\n"
"ds_read_b128 v[60:63], v4 offset:48128\n"
"v_add_f32 v10, v10, v21\n"
"v_mfma_f32_16x16x32_bf16 v[28:31], a[36:39], v[32:35], v[28:31]\n"
"v_fmac_f32 v11, v21, v21\n"
"s_waitcnt lgkmcnt(4)\n"
"v_mfma_f32_16x16x32_bf16 v[28:31], v[68:71], v[40:43], v[28:31]\n"
"ds_read_b128 v[32:35], v4 offset:6144\n"
"v_add_f32 v10, v10, v22\n"
"v_mfma_f32_16x16x32_bf16 v[28:31], v[180:183], v[44:47], v[28:31]\n"
"buffer_load_dwordx4 v2, s[32:35], s63 offen offset:3072 sc1 lds\n"
"s_add_u32 m0, s74, 8192\n"
"v_mfma_f32_16x16x32_bf16 v[24:27], v[176:179], v[44:47], v[24:27]\n"
"ds_read_b128 v[36:39], v4 offset:20480\n"
"v_fmac_f32 v11, v22, v22\n"
"v_mfma_f32_16x16x32_bf16 v[24:27], v[64:67], v[40:43], v[24:27]\n"
"v_add_f32 v10, v10, v23\n"
"s_waitcnt lgkmcnt(4)\n"
"v_mfma_f32_16x16x32_bf16 v[24:27], a[40:43], v[48:51], v[24:27]\n"
"ds_read_b128 v[40:43], v4 offset:34816\n"
"v_fmac_f32 v11, v23, v23\n"
"v_mfma_f32_16x16x32_bf16 v[24:27], a[152:155], v[52:55], v[24:27]\n"
"buffer_load_dwordx4 v2, s[32:35], s71 offen offset:0 sc1 lds\n"
"v_permlane16_swap_b32 v10, v11\n"
"v_mfma_f32_16x16x32_bf16 v[28:31], a[156:159], v[52:55], v[28:31]\n"
"ds_read_b128 v[44:47], v4 offset:49152\n"
"v_add_f32 v10, v10, v11\n"
"v_mfma_f32_16x16x32_bf16 v[28:31], a[44:47], v[48:51], v[28:31]\n"
"s_waitcnt lgkmcnt(4)\n"
"v_mfma_f32_16x16x32_bf16 v[28:31], v[76:79], v[56:59], v[28:31]\n"
"ds_read_b128 v[48:51], v4 offset:7168\n"
"v_permlane32_swap_b32 v10, v11\n"
"v_mfma_f32_16x16x32_bf16 v[28:31], v[188:191], v[60:63], v[28:31]\n"
"buffer_load_dwordx4 v2, s[32:35], s71 offen offset:1024 sc1 lds\n"
"s_add_u32 s71, s71, 1187840\n"
"v_mfma_f32_16x16x32_bf16 v[24:27], v[184:187], v[60:63], v[24:27]\n"
"ds_read_b128 v[52:55], v4 offset:21504\n"
"s_add_u32 s63, s63, 4096\n"
"v_mfma_f32_16x16x32_bf16 v[24:27], v[72:75], v[56:59], v[24:27]\n"
"s_add_u32 m0, s64, 4096\n"
"s_waitcnt lgkmcnt(4)\n"
"v_mfma_f32_16x16x32_bf16 v[24:27], a[48:51], v[32:35], v[24:27]\n"
"ds_read_b128 v[56:59], v4 offset:35840\n"
"v_add_f32 v10, v10, v11\n"
"v_mfma_f32_16x16x32_bf16 v[24:27], a[160:163], v[36:39], v[24:27]\n"
"buffer_store_dword v10, v3, s[32:35], s69 offen offset:1024 sc1\n"
"s_mov_b32 s67, s69\n"
"v_mfma_f32_16x16x32_bf16 v[28:31], a[164:167], v[36:39], v[28:31]\n"
"ds_read_b128 v[60:63], v4 offset:50176\n"
"s_mov_b32 s69, s70\n"
"v_mfma_f32_16x16x32_bf16 v[28:31], a[52:55], v[32:35], v[28:31]\n"
"s_add_u32 s70, s70, 1187840\n"
"s_waitcnt lgkmcnt(4)\n"
"v_mfma_f32_16x16x32_bf16 v[28:31], v[84:87], v[40:43], v[28:31]\n"
"ds_read_b128 v[32:35], v4 offset:8192\n"
"v_mfma_f32_16x16x32_bf16 v[28:31], v[196:199], v[44:47], v[28:31]\n"
"buffer_load_dwordx4 v2, s[32:35], s63 offen offset:0 sc1 lds\n"
"v_mfma_f32_16x16x32_bf16 v[24:27], v[192:195], v[44:47], v[24:27]\n"
"ds_read_b128 v[36:39], v4 offset:22528\n"
"v_mfma_f32_16x16x32_bf16 v[24:27], v[80:83], v[40:43], v[24:27]\n"
"s_waitcnt lgkmcnt(4)\n"
"v_mfma_f32_16x16x32_bf16 v[24:27], a[56:59], v[48:51], v[24:27]\n"
"ds_read_b128 v[40:43], v4 offset:36864\n"
"v_mfma_f32_16x16x32_bf16 v[24:27], a[168:171], v[52:55], v[24:27]\n"
"buffer_load_dwordx4 v2, s[32:35], s63 offen offset:1024 sc1 lds\n"
"v_mfma_f32_16x16x32_bf16 v[28:31], a[172:175], v[52:55], v[28:31]\n"
"ds_read_b128 v[44:47], v4 offset:51200\n"
"v_mfma_f32_16x16x32_bf16 v[28:31], a[60:63], v[48:51], v[28:31]\n"
"s_waitcnt lgkmcnt(4)\n"
"v_mfma_f32_16x16x32_bf16 v[28:31], v[92:95], v[56:59], v[28:31]\n"
"ds_read_b128 v[48:51], v4 offset:9216\n"
"v_mfma_f32_16x16x32_bf16 v[28:31], v[204:207], v[60:63], v[28:31]\n"
"buffer_load_dwordx4 v2, s[32:35], s63 offen offset:2048 sc1 lds\n"
"v_mfma_f32_16x16x32_bf16 v[24:27], v[200:203], v[60:63], v[24:27]\n"
"ds_read_b128 v[52:55], v4 offset:23552\n"
"v_mfma_f32_16x16x32_bf16 v[24:27], v[88:91], v[56:59], v[24:27]\n"
"s_waitcnt lgkmcnt(4)\n"
"v_mfma_f32_16x16x32_bf16 v[24:27], a[64:67], v[32:35], v[24:27]\n"
"ds_read_b128 v[56:59], v4 offset:37888\n"
"v_mfma_f32_16x16x32_bf16 v[24:27], a[176:179], v[36:39], v[24:27]\n"
"buffer_load_dwordx4 v2, s[32:35], s63 offen offset:3072 sc1 lds\n"
"s_add_u32 s63, s63, 4096\n"
"v_mfma_f32_16x16x32_bf16 v[28:31], a[180:183], v[36:39], v[28:31]\n"
"ds_read_b128 v[60:63], v4 offset:52224\n"
"s_add_u32 m0, s64, 8192\n"
"v_mfma_f32_16x16x32_bf16 v[28:31], a[68:71], v[32:35], v[28:31]\n"
"s_waitcnt lgkmcnt(4)\n"
"v_mfma_f32_16x16x32_bf16 v[28:31], v[100:103], v[40:43], v[28:31]\n"
"ds_read_b128 v[32:35], v4 offset:10240\n"
"v_mfma_f32_16x16x32_bf16 v[28:31], v[212:215], v[44:47], v[28:31]\n"
"buffer_load_dwordx4 v2, s[32:35], s63 offen offset:0 sc1 lds\n"
"v_mfma_f32_16x16x32_bf16 v[24:27], v[208:211], v[44:47], v[24:27]\n"
"ds_read_b128 v[36:39], v4 offset:24576\n"
"v_mfma_f32_16x16x32_bf16 v[24:27], v[96:99], v[40:43], v[24:27]\n"
"s_waitcnt lgkmcnt(4)\n"
"v_mfma_f32_16x16x32_bf16 v[24:27], a[72:75], v[48:51], v[24:27]\n"
"ds_read_b128 v[40:43], v4 offset:38912\n"
"v_mfma_f32_16x16x32_bf16 v[24:27], a[184:187], v[52:55], v[24:27]\n"
"buffer_load_dwordx4 v2, s[32:35], s63 offen offset:1024 sc1 lds\n"
"v_mfma_f32_16x16x32_bf16 v[28:31], a[188:191], v[52:55], v[28:31]\n"
"ds_read_b128 v[44:47], v4 offset:53248\n"
"v_mfma_f32_16x16x32_bf16 v[28:31], a[76:79], v[48:51], v[28:31]\n"
"s_waitcnt lgkmcnt(4)\n"
"v_mfma_f32_16x16x32_bf16 v[28:31], v[108:111], v[56:59], v[28:31]\n"
"ds_read_b128 v[48:51], v4 offset:11264\n"
"v_mfma_f32_16x16x32_bf16 v[28:31], v[220:223], v[60:63], v[28:31]\n"
"buffer_load_dwordx4 v2, s[32:35], s63 offen offset:2048 sc1 lds\n"
"v_mfma_f32_16x16x32_bf16 v[24:27], v[216:219], v[60:63], v[24:27]\n"
"ds_read_b128 v[52:55], v4 offset:25600\n"
"v_mfma_f32_16x16x32_bf16 v[24:27], v[104:107], v[56:59], v[24:27]\n"
"s_waitcnt lgkmcnt(4)\n"
"v_mfma_f32_16x16x32_bf16 v[24:27], a[80:83], v[32:35], v[24:27]\n"
"ds_read_b128 v[56:59], v4 offset:39936\n"
"v_mfma_f32_16x16x32_bf16 v[24:27], a[192:195], v[36:39], v[24:27]\n"
"buffer_load_dwordx4 v2, s[32:35], s63 offen offset:3072 sc1 lds\n"
"s_add_u32 s63, s63, 4096\n"
"v_mfma_f32_16x16x32_bf16 v[28:31], a[196:199], v[36:39], v[28:31]\n"
"ds_read_b128 v[60:63], v4 offset:54272\n"
"s_add_u32 m0, s64, 12288\n"
"v_mfma_f32_16x16x32_bf16 v[28:31], a[84:87], v[32:35], v[28:31]\n"
"s_waitcnt lgkmcnt(4)\n"
"v_mfma_f32_16x16x32_bf16 v[28:31], v[116:119], v[40:43], v[28:31]\n"
"ds_read_b128 v[32:35], v4 offset:12288\n"
"v_mfma_f32_16x16x32_bf16 v[28:31], v[228:231], v[44:47], v[28:31]\n"
"buffer_load_dwordx4 v2, s[32:35], s63 offen offset:0 sc1 lds\n"
"v_mfma_f32_16x16x32_bf16 v[24:27], v[224:227], v[44:47], v[24:27]\n"
"ds_read_b128 v[36:39], v4 offset:26624\n"
"v_mfma_f32_16x16x32_bf16 v[24:27], v[112:115], v[40:43], v[24:27]\n"
"s_waitcnt lgkmcnt(4)\n"
"v_mfma_f32_16x16x32_bf16 v[24:27], a[88:91], v[48:51], v[24:27]\n"
"ds_read_b128 v[40:43], v4 offset:40960\n"
"v_mfma_f32_16x16x32_bf16 v[24:27], a[200:203], v[52:55], v[24:27]\n"
"buffer_load_dwordx4 v2, s[32:35], s63 offen offset:1024 sc1 lds\n"
"s_add_u32 s63, s63, 1175552\n"
"v_mfma_f32_16x16x32_bf16 v[28:31], a[204:207], v[52:55], v[28:31]\n"
"ds_read_b128 v[44:47], v4 offset:55296\n"
"v_mfma_f32_16x16x32_bf16 v[28:31], a[92:95], v[48:51], v[28:31]\n"
"s_waitcnt lgkmcnt(4)\n"
"v_mfma_f32_16x16x32_bf16 v[28:31], v[124:127], v[56:59], v[28:31]\n"
"ds_read_b128 v[48:51], v4 offset:13312\n"
"v_mfma_f32_16x16x32_bf16 v[28:31], v[236:239], v[60:63], v[28:31]\n"
"v_mfma_f32_16x16x32_bf16 v[24:27], v[232:235], v[60:63], v[24:27]\n"
"ds_read_b128 v[52:55], v4 offset:27648\n"
"v_mfma_f32_16x16x32_bf16 v[24:27], v[120:123], v[56:59], v[24:27]\n"
"s_waitcnt lgkmcnt(4)\n"
"v_mfma_f32_16x16x32_bf16 v[24:27], a[96:99], v[32:35], v[24:27]\n"
"s_waitcnt vmcnt(13)\n"
"s_waitcnt vmcnt(10)\n"
"s_barrier\n"
"v_mfma_f32_16x16x32_bf16 v[24:27], a[208:211], v[36:39], v[24:27]\n"
"ds_read_b128 v[56:59], v4 offset:41984\n"
"v_mfma_f32_16x16x32_bf16 v[28:31], a[212:215], v[36:39], v[28:31]\n"
"ds_read_b128 v[60:63], v4 offset:56320\n"
"v_mfma_f32_16x16x32_bf16 v[28:31], a[100:103], v[32:35], v[28:31]\n"
"s_waitcnt lgkmcnt(4)\n"
"v_mfma_f32_16x16x32_bf16 v[28:31], v[132:135], v[40:43], v[28:31]\n"
"ds_read_b128 v[32:35], v2 offset:0\n"
"v_mfma_f32_16x16x32_bf16 v[28:31], v[244:247], v[44:47], v[28:31]\n"
"v_mfma_f32_16x16x32_bf16 v[24:27], v[240:243], v[44:47], v[24:27]\n"
"ds_read_b128 v[36:39], v2 offset:14336\n"
"v_mfma_f32_16x16x32_bf16 v[24:27], v[128:131], v[40:43], v[24:27]\n"
"s_waitcnt lgkmcnt(4)\n"
"v_mfma_f32_16x16x32_bf16 v[24:27], a[104:107], v[48:51], v[24:27]\n"
"ds_read_b128 v[40:43], v2 offset:28672\n"
"v_mfma_f32_16x16x32_bf16 v[24:27], a[216:219], v[52:55], v[24:27]\n"
"v_mfma_f32_16x16x32_bf16 v[28:31], a[220:223], v[52:55], v[28:31]\n"
"ds_read_b128 v[44:47], v2 offset:43008\n"
"v_mfma_f32_16x16x32_bf16 v[28:31], a[108:111], v[48:51], v[28:31]\n"
"s_waitcnt lgkmcnt(4)\n"
"v_mfma_f32_16x16x32_bf16 v[28:31], v[140:143], v[56:59], v[28:31]\n"
"ds_read_b128 v[48:51], v2 offset:1024\n"
"v_mfma_f32_16x16x32_bf16 v[28:31], v[252:255], v[60:63], v[28:31]\n"
"v_mfma_f32_16x16x32_bf16 v[24:27], v[248:251], v[60:63], v[24:27]\n"
"ds_read_b128 v[52:55], v2 offset:15360\n"
"s_cmp_eq_u32 s62, 0\n"
"v_mfma_f32_16x16x32_bf16 v[24:27], v[136:139], v[56:59], v[24:27]\n"
"s_cbranch_scc0 .L_dal_3\n"
"s_mov_b32 s48, 0x7fff0000\n"
"v_writelane_b32 v8, 0, 63\n"
"v_writelane_b32 v9, 0, 63\n"
"v_writelane_b32 v6, 0, 63\n"
"v_writelane_b32 v7, 0, 63\n"
"v_writelane_b32 v0, s48, 63\n"
"s_branch .L_dal_3\n"
".L_dab_3:\n"
"s_mov_b32 s62, 15\n"
"s_add_u32 s74, s74, 8192\n"
"v_lshrrev_b32 v5, 8, v2\n"
"v_lshl_add_u32 v5, v5, 4, s74\n"
"v_and_b32 v3, 128, v2\n"
"v_lshl_add_u32 v5, v3, 2, v5\n"
"v_and_b32 v3, 16, v2\n"
"v_lshl_add_u32 v5, v3, 4, v5\n"
"v_and_b32 v3, 96, v2\n"
"v_lshl_add_u32 v5, v3, 1, v5\n"
"v_lshrrev_b32 v3, 2, v2\n"
"v_add_u32 v3, s74, v3\n"
".p2align 6\n"
".L_dbl_3:\n"
"s_waitcnt lgkmcnt(4)\n"
"v_mfma_f32_16x16x32_bf16 v[24:27], a[0:3], v[32:35], 0\n"
"ds_read_b128 v[56:59], v4 offset:29696\n"
"v_mfma_f32_16x16x32_bf16 v[24:27], a[112:115], v[36:39], v[24:27]\n"
"v_mfma_f32_16x16x32_bf16 v[28:31], a[116:119], v[36:39], 0\n"
"ds_read_b128 v[60:63], v4 offset:44032\n"
"v_mfma_f32_16x16x32_bf16 v[28:31], a[4:7], v[32:35], v[28:31]\n"
"v_cvt_pk_bf16_f32 v15, v22, v23\n"
"s_waitcnt lgkmcnt(4)\n"
"v_mfma_f32_16x16x32_bf16 v[28:31], a[228:231], v[40:43], v[28:31]\n"
"ds_read_b128 v[32:35], v4 offset:2048\n"
"v_cvt_pk_bf16_f32 v14, v20, v21\n"
"v_mfma_f32_16x16x32_bf16 v[28:31], v[148:151], v[44:47], v[28:31]\n"
"v_cvt_pk_bf16_f32 v13, v18, v19\n"
"v_cvt_pk_bf16_f32 v12, v16, v17\n"
"v_mfma_f32_16x16x32_bf16 v[24:27], v[144:147], v[44:47], v[24:27]\n"
"ds_read_b128 v[36:39], v4 offset:16384\n"
"ds_write_b128 v5, v[12:15]\n"
"v_mfma_f32_16x16x32_bf16 v[24:27], a[224:227], v[40:43], v[24:27]\n"
"ds_read2st64_b32 v[12:13], v3 offset0:0 offset1:1\n"
"s_waitcnt lgkmcnt(6)\n"
"v_mfma_f32_16x16x32_bf16 v[24:27], a[8:11], v[48:51], v[24:27]\n"
"ds_read_b128 v[40:43], v4 offset:30720\n"
"ds_read2st64_b32 v[14:15], v3 offset0:2 offset1:3\n"
"v_mfma_f32_16x16x32_bf16 v[24:27], a[120:123], v[52:55], v[24:27]\n"
"v_mfma_f32_16x16x32_bf16 v[28:31], a[124:127], v[52:55], v[28:31]\n"
"ds_read_b128 v[44:47], v4 offset:45056\n"
"v_mfma_f32_16x16x32_bf16 v[28:31], a[12:15], v[48:51], v[28:31]\n"
"s_waitcnt lgkmcnt(7)\n"
"v_mfma_f32_16x16x32_bf16 v[28:31], a[236:239], v[56:59], v[28:31]\n"
"ds_read_b128 v[48:51], v4 offset:3072\n"
"v_mfma_f32_16x16x32_bf16 v[28:31], v[156:159], v[60:63], v[28:31]\n"
"s_waitcnt lgkmcnt(2)\n"
"buffer_atomic_pk_add_bf16 v12, v1, s[44:47], s65 offen offset:0 sc1\n"
"v_mfma_f32_16x16x32_bf16 v[24:27], v[152:155], v[60:63], v[24:27]\n"
"ds_read_b128 v[52:55], v4 offset:17408\n"
"v_mfma_f32_16x16x32_bf16 v[24:27], a[232:235], v[56:59], v[24:27]\n"
"buffer_atomic_pk_add_bf16 v13, v1, s[44:47], s65 offen offset:2048 sc1\n"
"v_mfma_f32_16x16x32_bf16 v[24:27], a[16:19], v[32:35], v[24:27]\n"
"s_waitcnt vmcnt(2)\n"
"s_barrier\n"
"v_mfma_f32_16x16x32_bf16 v[24:27], a[128:131], v[36:39], v[24:27]\n"
"ds_read_b128 v[56:59], v4 offset:31744\n"
"s_add_u32 s65, s65, 16384\n"
"v_mfma_f32_16x16x32_bf16 v[28:31], a[132:135], v[36:39], v[28:31]\n"
"ds_read_b128 v[60:63], v4 offset:46080\n"
"v_mfma_f32_16x16x32_bf16 v[28:31], a[20:23], v[32:35], v[28:31]\n"
"buffer_atomic_pk_add_bf16 v14, v1, s[44:47], s65 offen offset:0 sc1\n"
"s_waitcnt lgkmcnt(4)\n"
"v_mfma_f32_16x16x32_bf16 v[28:31], a[244:247], v[40:43], v[28:31]\n"
"ds_read_b128 v[32:35], v4 offset:4096\n"
"s_add_u32 m0, s64, 0\n"
"v_mfma_f32_16x16x32_bf16 v[28:31], v[164:167], v[44:47], v[28:31]\n"
"buffer_atomic_pk_add_bf16 v15, v1, s[44:47], s65 offen offset:2048 sc1\n"
"s_add_u32 s65, s65, 16384\n"
"v_mfma_f32_16x16x32_bf16 v[24:27], v[160:163], v[44:47], v[24:27]\n"
"ds_read_b128 v[36:39], v4 offset:18432\n"
"v_mfma_f32_16x16x32_bf16 v[24:27], a[240:243], v[40:43], v[24:27]\n"
"s_waitcnt lgkmcnt(4)\n"
"v_mfma_f32_16x16x32_bf16 v[24:27], a[24:27], v[48:51], v[24:27]\n"
"ds_read_b128 v[40:43], v4 offset:32768\n"
"v_mfma_f32_16x16x32_bf16 v[24:27], a[136:139], v[52:55], v[24:27]\n"
"buffer_load_dwordx4 v2, s[32:35], s63 offen offset:0 sc1 lds\n"
"v_mfma_f32_16x16x32_bf16 v[28:31], a[140:143], v[52:55], v[28:31]\n"
"ds_read_b128 v[44:47], v4 offset:47104\n"
"v_mfma_f32_16x16x32_bf16 v[28:31], a[28:31], v[48:51], v[28:31]\n"
"s_waitcnt lgkmcnt(4)\n"
"v_mfma_f32_16x16x32_bf16 v[28:31], a[252:255], v[56:59], v[28:31]\n"
"ds_read_b128 v[48:51], v4 offset:5120\n"
"v_mfma_f32_16x16x32_bf16 v[28:31], v[172:175], v[60:63], v[28:31]\n"
"buffer_load_dwordx4 v2, s[32:35], s63 offen offset:1024 sc1 lds\n"
"v_mfma_f32_16x16x32_bf16 v[24:27], v[168:171], v[60:63], v[24:27]\n"
"ds_read_b128 v[52:55], v4 offset:19456\n"
"v_mfma_f32_16x16x32_bf16 v[24:27], a[248:251], v[56:59], v[24:27]\n"
"s_waitcnt lgkmcnt(4)\n"
"v_mfma_f32_16x16x32_bf16 v[24:27], a[32:35], v[32:35], v[24:27]\n"
"ds_read_b128 v[56:59], v4 offset:33792\n"
"v_mfma_f32_16x16x32_bf16 v[24:27], a[144:147], v[36:39], v[24:27]\n"
"buffer_load_dwordx4 v2, s[32:35], s63 offen offset:2048 sc1 lds\n"
"v_mfma_f32_16x16x32_bf16 v[28:31], a[148:151], v[36:39], v[28:31]\n"
"ds_read_b128 v[60:63], v4 offset:48128\n"
"v_mfma_f32_16x16x32_bf16 v[28:31], a[36:39], v[32:35], v[28:31]\n"
"s_waitcnt lgkmcnt(4)\n"
"v_mfma_f32_16x16x32_bf16 v[28:31], v[68:71], v[40:43], v[28:31]\n"
"ds_read_b128 v[32:35], v4 offset:6144\n"
"v_mfma_f32_16x16x32_bf16 v[28:31], v[180:183], v[44:47], v[28:31]\n"
"buffer_load_dwordx4 v2, s[32:35], s63 offen offset:3072 sc1 lds\n"
"v_mfma_f32_16x16x32_bf16 v[24:27], v[176:179], v[44:47], v[24:27]\n"
"ds_read_b128 v[36:39], v4 offset:20480\n"
"v_mfma_f32_16x16x32_bf16 v[24:27], v[64:67], v[40:43], v[24:27]\n"
"s_waitcnt lgkmcnt(4)\n"
"v_mfma_f32_16x16x32_bf16 v[24:27], a[40:43], v[48:51], v[24:27]\n"
"ds_read_b128 v[40:43], v4 offset:34816\n"
"s_add_u32 s63, s63, 4096\n"
"v_mfma_f32_16x16x32_bf16 v[24:27], a[152:155], v[52:55], v[24:27]\n"
"s_add_u32 m0, s64, 4096\n"
"v_mfma_f32_16x16x32_bf16 v[28:31], a[156:159], v[52:55], v[28:31]\n"
"ds_read_b128 v[44:47], v4 offset:49152\n"
"v_mfma_f32_16x16x32_bf16 v[28:31], a[44:47], v[48:51], v[28:31]\n"
"s_waitcnt lgkmcnt(4)\n"
"v_mfma_f32_16x16x32_bf16 v[28:31], v[76:79], v[56:59], v[28:31]\n"
"ds_read_b128 v[48:51], v4 offset:7168\n"
"v_mfma_f32_16x16x32_bf16 v[28:31], v[188:191], v[60:63], v[28:31]\n"
"buffer_load_dwordx4 v2, s[32:35], s63 offen offset:0 sc1 lds\n"
"v_mfma_f32_16x16x32_bf16 v[24:27], v[184:187], v[60:63], v[24:27]\n"
"ds_read_b128 v[52:55], v4 offset:21504\n"
"v_mfma_f32_16x16x32_bf16 v[24:27], v[72:75], v[56:59], v[24:27]\n"
"s_waitcnt lgkmcnt(4)\n"
"v_mfma_f32_16x16x32_bf16 v[24:27], a[48:51], v[32:35], v[24:27]\n"
"ds_read_b128 v[56:59], v4 offset:35840\n"
"v_mfma_f32_16x16x32_bf16 v[24:27], a[160:163], v[36:39], v[24:27]\n"
"buffer_load_dwordx4 v2, s[32:35], s63 offen offset:1024 sc1 lds\n"
"v_mfma_f32_16x16x32_bf16 v[28:31], a[164:167], v[36:39], v[28:31]\n"
"ds_read_b128 v[60:63], v4 offset:50176\n"
"v_mfma_f32_16x16x32_bf16 v[28:31], a[52:55], v[32:35], v[28:31]\n"
"s_waitcnt lgkmcnt(4)\n"
"v_mfma_f32_16x16x32_bf16 v[28:31], v[84:87], v[40:43], v[28:31]\n"
"ds_read_b128 v[32:35], v4 offset:8192\n"
"v_mfma_f32_16x16x32_bf16 v[28:31], v[196:199], v[44:47], v[28:31]\n"
"buffer_load_dwordx4 v2, s[32:35], s63 offen offset:2048 sc1 lds\n"
"v_mfma_f32_16x16x32_bf16 v[24:27], v[192:195], v[44:47], v[24:27]\n"
"ds_read_b128 v[36:39], v4 offset:22528\n"
"v_mfma_f32_16x16x32_bf16 v[24:27], v[80:83], v[40:43], v[24:27]\n"
"s_waitcnt lgkmcnt(4)\n"
"v_mfma_f32_16x16x32_bf16 v[24:27], a[56:59], v[48:51], v[24:27]\n"
"ds_read_b128 v[40:43], v4 offset:36864\n"
"v_mfma_f32_16x16x32_bf16 v[24:27], a[168:171], v[52:55], v[24:27]\n"
"buffer_load_dwordx4 v2, s[32:35], s63 offen offset:3072 sc1 lds\n"
"v_mfma_f32_16x16x32_bf16 v[28:31], a[172:175], v[52:55], v[28:31]\n"
"ds_read_b128 v[44:47], v4 offset:51200\n"
"v_mfma_f32_16x16x32_bf16 v[28:31], a[60:63], v[48:51], v[28:31]\n"
"s_waitcnt lgkmcnt(4)\n"
"v_mfma_f32_16x16x32_bf16 v[28:31], v[92:95], v[56:59], v[28:31]\n"
"ds_read_b128 v[48:51], v4 offset:9216\n"
"v_mfma_f32_16x16x32_bf16 v[28:31], v[204:207], v[60:63], v[28:31]\n"
"s_add_u32 s63, s63, 4096\n"
"s_add_u32 m0, s64, 8192\n"
"v_mfma_f32_16x16x32_bf16 v[24:27], v[200:203], v[60:63], v[24:27]\n"
"ds_read_b128 v[52:55], v4 offset:23552\n"
"v_mfma_f32_16x16x32_bf16 v[24:27], v[88:91], v[56:59], v[24:27]\n"
"s_waitcnt lgkmcnt(4)\n"
"v_mfma_f32_16x16x32_bf16 v[24:27], a[64:67], v[32:35], v[24:27]\n"
"ds_read_b128 v[56:59], v4 offset:37888\n"
"v_mfma_f32_16x16x32_bf16 v[24:27], a[176:179], v[36:39], v[24:27]\n"
"buffer_load_dwordx4 v2, s[32:35], s63 offen offset:0 sc1 lds\n"
"v_mfma_f32_16x16x32_bf16 v[28:31], a[180:183], v[36:39], v[28:31]\n"
"ds_read_b128 v[60:63], v4 offset:52224\n"
"v_mfma_f32_16x16x32_bf16 v[28:31], a[68:71], v[32:35], v[28:31]\n"
"s_waitcnt lgkmcnt(4)\n"
"v_mfma_f32_16x16x32_bf16 v[28:31], v[100:103], v[40:43], v[28:31]\n"
"ds_read_b128 v[32:35], v4 offset:10240\n"
"v_mfma_f32_16x16x32_bf16 v[28:31], v[212:215], v[44:47], v[28:31]\n"
"buffer_load_dwordx4 v2, s[32:35], s63 offen offset:1024 sc1 lds\n"
"v_mfma_f32_16x16x32_bf16 v[24:27], v[208:211], v[44:47], v[24:27]\n"
"ds_read_b128 v[36:39], v4 offset:24576\n"
"v_mfma_f32_16x16x32_bf16 v[24:27], v[96:99], v[40:43], v[24:27]\n"
"s_waitcnt lgkmcnt(4)\n"
"v_mfma_f32_16x16x32_bf16 v[24:27], a[72:75], v[48:51], v[24:27]\n"
"ds_read_b128 v[40:43], v4 offset:38912\n"
"v_mfma_f32_16x16x32_bf16 v[24:27], a[184:187], v[52:55], v[24:27]\n"
"buffer_load_dwordx4 v2, s[32:35], s63 offen offset:2048 sc1 lds\n"
"v_mfma_f32_16x16x32_bf16 v[28:31], a[188:191], v[52:55], v[28:31]\n"
"ds_read_b128 v[44:47], v4 offset:53248\n"
"v_mfma_f32_16x16x32_bf16 v[28:31], a[76:79], v[48:51], v[28:31]\n"
"s_waitcnt lgkmcnt(4)\n"
"v_mfma_f32_16x16x32_bf16 v[28:31], v[108:111], v[56:59], v[28:31]\n"
"ds_read_b128 v[48:51], v4 offset:11264\n"
"v_mfma_f32_16x16x32_bf16 v[28:31], v[220:223], v[60:63], v[28:31]\n"
"v_mfma_f32_16x16x32_bf16 v[24:27], v[216:219], v[60:63], v[24:27]\n"
"ds_read_b128 v[52:55], v4 offset:25600\n"
"v_mfma_f32_16x16x32_bf16 v[24:27], v[104:107], v[56:59], v[24:27]\n"
"s_waitcnt lgkmcnt(4)\n"
"v_mfma_f32_16x16x32_bf16 v[24:27], a[80:83], v[32:35], v[24:27]\n"
"ds_read_b128 v[56:59], v4 offset:39936\n"
"v_mfma_f32_16x16x32_bf16 v[24:27], a[192:195], v[36:39], v[24:27]\n"
"buffer_load_dwordx4 v2, s[32:35], s63 offen offset:3072 sc1 lds\n"
"s_add_u32 s63, s63, 4096\n"
"v_mfma_f32_16x16x32_bf16 v[28:31], a[196:199], v[36:39], v[28:31]\n"
"ds_read_b128 v[60:63], v4 offset:54272\n"
"s_add_u32 m0, s64, 12288\n"
"v_mfma_f32_16x16x32_bf16 v[28:31], a[84:87], v[32:35], v[28:31]\n"
"s_waitcnt lgkmcnt(4)\n"
"v_mfma_f32_16x16x32_bf16 v[28:31], v[116:119], v[40:43], v[28:31]\n"
"ds_read_b128 v[32:35], v4 offset:12288\n"
"v_mfma_f32_16x16x32_bf16 v[28:31], v[228:231], v[44:47], v[28:31]\n"
"buffer_load_dwordx4 v2, s[32:35], s63 offen offset:0 sc1 lds\n"
"v_mfma_f32_16x16x32_bf16 v[24:27], v[224:227], v[44:47], v[24:27]\n"
"ds_read_b128 v[36:39], v4 offset:26624\n"
"v_mfma_f32_16x16x32_bf16 v[24:27], v[112:115], v[40:43], v[24:27]\n"
"s_waitcnt lgkmcnt(4)\n"
"v_mfma_f32_16x16x32_bf16 v[24:27], a[88:91], v[48:51], v[24:27]\n"
"ds_read_b128 v[40:43], v4 offset:40960\n"
"v_mfma_f32_16x16x32_bf16 v[24:27], a[200:203], v[52:55], v[24:27]\n"
"buffer_load_dwordx4 v2, s[32:35], s63 offen offset:1024 sc1 lds\n"
"s_add_u32 s63, s63, 1175552\n"
"v_mfma_f32_16x16x32_bf16 v[28:31], a[204:207], v[52:55], v[28:31]\n"
"ds_read_b128 v[44:47], v4 offset:55296\n"
"v_mfma_f32_16x16x32_bf16 v[28:31], a[92:95], v[48:51], v[28:31]\n"
"s_waitcnt lgkmcnt(4)\n"
"v_mfma_f32_16x16x32_bf16 v[28:31], v[124:127], v[56:59], v[28:31]\n"
"ds_read_b128 v[48:51], v4 offset:13312\n"
"v_mfma_f32_16x16x32_bf16 v[28:31], v[236:239], v[60:63], v[28:31]\n"
"v_mfma_f32_16x16x32_bf16 v[24:27], v[232:235], v[60:63], v[24:27]\n"
"ds_read_b128 v[52:55], v4 offset:27648\n"
"v_mfma_f32_16x16x32_bf16 v[24:27], v[120:123], v[56:59], v[24:27]\n"
"s_waitcnt lgkmcnt(4)\n"
"v_mfma_f32_16x16x32_bf16 v[24:27], a[96:99], v[32:35], v[24:27]\n"
"s_waitcnt vmcnt(10)\n"
"s_barrier\n"
"v_mfma_f32_16x16x32_bf16 v[24:27], a[208:211], v[36:39], v[24:27]\n"
"ds_read_b128 v[56:59], v4 offset:41984\n"
"v_mfma_f32_16x16x32_bf16 v[28:31], a[212:215], v[36:39], v[28:31]\n"
"ds_read_b128 v[60:63], v4 offset:56320\n"
"v_mfma_f32_16x16x32_bf16 v[28:31], a[100:103], v[32:35], v[28:31]\n"
"s_waitcnt lgkmcnt(4)\n"
"v_mfma_f32_16x16x32_bf16 v[28:31], v[132:135], v[40:43], v[28:31]\n"
"ds_read_b128 v[32:35], v2 offset:0\n"
"v_mfma_f32_16x16x32_bf16 v[28:31], v[244:247], v[44:47], v[28:31]\n"
"v_mfma_f32_16x16x32_bf16 v[24:27], v[240:243], v[44:47], v[24:27]\n"
"ds_read_b128 v[36:39], v2 offset:14336\n"
"v_mfma_f32_16x16x32_bf16 v[24:27], v[128:131], v[40:43], v[24:27]\n"
"s_waitcnt lgkmcnt(4)\n"
"v_mfma_f32_16x16x32_bf16 v[24:27], a[104:107], v[48:51], v[24:27]\n"
"ds_read_b128 v[40:43], v2 offset:28672\n"
"v_mfma_f32_16x16x32_bf16 v[24:27], a[216:219], v[52:55], v[24:27]\n"
"v_mfma_f32_16x16x32_bf16 v[28:31], a[220:223], v[52:55], v[28:31]\n"
"ds_read_b128 v[44:47], v2 offset:43008\n"
"v_mfma_f32_16x16x32_bf16 v[28:31], a[108:111], v[48:51], v[28:31]\n"
"s_waitcnt lgkmcnt(4)\n"
"v_mfma_f32_16x16x32_bf16 v[28:31], v[140:143], v[56:59], v[28:31]\n"
"ds_read_b128 v[48:51], v2 offset:1024\n"
"v_mfma_f32_16x16x32_bf16 v[28:31], v[252:255], v[60:63], v[28:31]\n"
"v_mfma_f32_16x16x32_bf16 v[24:27], v[248:251], v[60:63], v[24:27]\n"
"ds_read_b128 v[52:55], v2 offset:15360\n"
"s_sub_u32 s62, s62, 1\n"
"v_mfma_f32_16x16x32_bf16 v[24:27], v[136:139], v[56:59], v[24:27]\n"
"s_cbranch_scc1 .L_dbt_3\n"
"s_waitcnt lgkmcnt(4)\n"
"v_mfma_f32_16x16x32_bf16 v[16:19], a[0:3], v[32:35], 0\n"
"ds_read_b128 v[56:59], v2 offset:29696\n"
"v_mfma_f32_16x16x32_bf16 v[16:19], a[112:115], v[36:39], v[16:19]\n"
"v_mfma_f32_16x16x32_bf16 v[20:23], a[116:119], v[36:39], 0\n"
"ds_read_b128 v[60:63], v2 offset:44032\n"
"v_mfma_f32_16x16x32_bf16 v[20:23], a[4:7], v[32:35], v[20:23]\n"
"v_cvt_pk_bf16_f32 v15, v30, v31\n"
"s_waitcnt lgkmcnt(4)\n"
"v_mfma_f32_16x16x32_bf16 v[20:23], a[228:231], v[40:43], v[20:23]\n"
"ds_read_b128 v[32:35], v2 offset:2048\n"
"v_cvt_pk_bf16_f32 v14, v28, v29\n"
"v_mfma_f32_16x16x32_bf16 v[20:23], v[148:151], v[44:47], v[20:23]\n"
"v_cvt_pk_bf16_f32 v13, v26, v27\n"
"v_cvt_pk_bf16_f32 v12, v24, v25\n"
"v_mfma_f32_16x16x32_bf16 v[16:19], v[144:147], v[44:47], v[16:19]\n"
"ds_read_b128 v[36:39], v2 offset:16384\n"
"ds_write_b128 v5, v[12:15]\n"
"v_mfma_f32_16x16x32_bf16 v[16:19], a[224:227], v[40:43], v[16:19]\n"
"ds_read2st64_b32 v[12:13], v3 offset0:0 offset1:1\n"
"s_waitcnt lgkmcnt(6)\n"
"v_mfma_f32_16x16x32_bf16 v[16:19], a[8:11], v[48:51], v[16:19]\n"
"ds_read_b128 v[40:43], v2 offset:30720\n"
"ds_read2st64_b32 v[14:15], v3 offset0:2 offset1:3\n"
"v_mfma_f32_16x16x32_bf16 v[16:19], a[120:123], v[52:55], v[16:19]\n"
"v_mfma_f32_16x16x32_bf16 v[20:23], a[124:127], v[52:55], v[20:23]\n"
"ds_read_b128 v[44:47], v2 offset:45056\n"
"v_mfma_f32_16x16x32_bf16 v[20:23], a[12:15], v[48:51], v[20:23]\n"
"s_waitcnt lgkmcnt(7)\n"
"v_mfma_f32_16x16x32_bf16 v[20:23], a[236:239], v[56:59], v[20:23]\n"
"ds_read_b128 v[48:51], v2 offset:3072\n"
"v_mfma_f32_16x16x32_bf16 v[20:23], v[156:159], v[60:63], v[20:23]\n"
"s_waitcnt lgkmcnt(2)\n"
"buffer_atomic_pk_add_bf16 v12, v1, s[44:47], s65 offen offset:0 sc1\n"
"v_mfma_f32_16x16x32_bf16 v[16:19], v[152:155], v[60:63], v[16:19]\n"
"ds_read_b128 v[52:55], v2 offset:17408\n"
"v_mfma_f32_16x16x32_bf16 v[16:19], a[232:235], v[56:59], v[16:19]\n"
"buffer_atomic_pk_add_bf16 v13, v1, s[44:47], s65 offen offset:2048 sc1\n"
"v_mfma_f32_16x16x32_bf16 v[16:19], a[16:19], v[32:35], v[16:19]\n"
"s_waitcnt vmcnt(2)\n"
"s_barrier\n"
"v_mfma_f32_16x16x32_bf16 v[16:19], a[128:131], v[36:39], v[16:19]\n"
"ds_read_b128 v[56:59], v2 offset:31744\n"
"s_add_u32 s65, s65, 16384\n"
"v_mfma_f32_16x16x32_bf16 v[20:23], a[132:135], v[36:39], v[20:23]\n"
"ds_read_b128 v[60:63], v2 offset:46080\n"
"v_mfma_f32_16x16x32_bf16 v[20:23], a[20:23], v[32:35], v[20:23]\n"
"buffer_atomic_pk_add_bf16 v14, v1, s[44:47], s65 offen offset:0 sc1\n"
"s_waitcnt lgkmcnt(4)\n"
"v_mfma_f32_16x16x32_bf16 v[20:23], a[244:247], v[40:43], v[20:23]\n"
"ds_read_b128 v[32:35], v2 offset:4096\n"
"s_add_u32 m0, s64, 57344\n"
"v_mfma_f32_16x16x32_bf16 v[20:23], v[164:167], v[44:47], v[20:23]\n"
"buffer_atomic_pk_add_bf16 v15, v1, s[44:47], s65 offen offset:2048 sc1\n"
"s_add_u32 s65, s65, 16384\n"
"v_mfma_f32_16x16x32_bf16 v[16:19], v[160:163], v[44:47], v[16:19]\n"
"ds_read_b128 v[36:39], v2 offset:18432\n"
"v_mfma_f32_16x16x32_bf16 v[16:19], a[240:243], v[40:43], v[16:19]\n"
"s_waitcnt lgkmcnt(4)\n"
"v_mfma_f32_16x16x32_bf16 v[16:19], a[24:27], v[48:51], v[16:19]\n"
"ds_read_b128 v[40:43], v2 offset:32768\n"
"v_mfma_f32_16x16x32_bf16 v[16:19], a[136:139], v[52:55], v[16:19]\n"
"buffer_load_dwordx4 v2, s[32:35], s63 offen offset:0 sc1 lds\n"
"v_mfma_f32_16x16x32_bf16 v[20:23], a[140:143], v[52:55], v[20:23]\n"
"ds_read_b128 v[44:47], v2 offset:47104\n"
"v_mfma_f32_16x16x32_bf16 v[20:23], a[28:31], v[48:51], v[20:23]\n"
"s_waitcnt lgkmcnt(4)\n"
"v_mfma_f32_16x16x32_bf16 v[20:23], a[252:255], v[56:59], v[20:23]\n"
"ds_read_b128 v[48:51], v2 offset:5120\n"
"v_mfma_f32_16x16x32_bf16 v[20:23], v[172:175], v[60:63], v[20:23]\n"
"buffer_load_dwordx4 v2, s[32:35], s63 offen offset:1024 sc1 lds\n"
"v_mfma_f32_16x16x32_bf16 v[16:19], v[168:171], v[60:63], v[16:19]\n"
"ds_read_b128 v[52:55], v2 offset:19456\n"
"v_mfma_f32_16x16x32_bf16 v[16:19], a[248:251], v[56:59], v[16:19]\n"
"s_waitcnt lgkmcnt(4)\n"
"v_mfma_f32_16x16x32_bf16 v[16:19], a[32:35], v[32:35], v[16:19]\n"
"ds_read_b128 v[56:59], v2 offset:33792\n"
"v_mfma_f32_16x16x32_bf16 v[16:19], a[144:147], v[36:39], v[16:19]\n"
"buffer_load_dwordx4 v2, s[32:35], s63 offen offset:2048 sc1 lds\n"
"v_mfma_f32_16x16x32_bf16 v[20:23], a[148:151], v[36:39], v[20:23]\n"
"ds_read_b128 v[60:63], v2 offset:48128\n"
"v_mfma_f32_16x16x32_bf16 v[20:23], a[36:39], v[32:35], v[20:23]\n"
"s_waitcnt lgkmcnt(4)\n"
"v_mfma_f32_16x16x32_bf16 v[20:23], v[68:71], v[40:43], v[20:23]\n"
"ds_read_b128 v[32:35], v2 offset:6144\n"
"v_mfma_f32_16x16x32_bf16 v[20:23], v[180:183], v[44:47], v[20:23]\n"
"buffer_load_dwordx4 v2, s[32:35], s63 offen offset:3072 sc1 lds\n"
"v_mfma_f32_16x16x32_bf16 v[16:19], v[176:179], v[44:47], v[16:19]\n"
"ds_read_b128 v[36:39], v2 offset:20480\n"
"v_mfma_f32_16x16x32_bf16 v[16:19], v[64:67], v[40:43], v[16:19]\n"
"s_waitcnt lgkmcnt(4)\n"
"v_mfma_f32_16x16x32_bf16 v[16:19], a[40:43], v[48:51], v[16:19]\n"
"ds_read_b128 v[40:43], v2 offset:34816\n"
"s_add_u32 s63, s63, 4096\n"
"v_mfma_f32_16x16x32_bf16 v[16:19], a[152:155], v[52:55], v[16:19]\n"
"s_add_u32 m0, s64, 61440\n"
"v_mfma_f32_16x16x32_bf16 v[20:23], a[156:159], v[52:55], v[20:23]\n"
"ds_read_b128 v[44:47], v2 offset:49152\n"
"v_mfma_f32_16x16x32_bf16 v[20:23], a[44:47], v[48:51], v[20:23]\n"
"s_waitcnt lgkmcnt(4)\n"
"v_mfma_f32_16x16x32_bf16 v[20:23], v[76:79], v[56:59], v[20:23]\n"
"ds_read_b128 v[48:51], v2 offset:7168\n"
"v_mfma_f32_16x16x32_bf16 v[20:23], v[188:191], v[60:63], v[20:23]\n"
"buffer_load_dwordx4 v2, s[32:35], s63 offen offset:0 sc1 lds\n"
"v_mfma_f32_16x16x32_bf16 v[16:19], v[184:187], v[60:63], v[16:19]\n"
"ds_read_b128 v[52:55], v2 offset:21504\n"
"v_mfma_f32_16x16x32_bf16 v[16:19], v[72:75], v[56:59], v[16:19]\n"
"s_waitcnt lgkmcnt(4)\n"
"v_mfma_f32_16x16x32_bf16 v[16:19], a[48:51], v[32:35], v[16:19]\n"
"ds_read_b128 v[56:59], v2 offset:35840\n"
"v_mfma_f32_16x16x32_bf16 v[16:19], a[160:163], v[36:39], v[16:19]\n"
"buffer_load_dwordx4 v2, s[32:35], s63 offen offset:1024 sc1 lds\n"
"v_mfma_f32_16x16x32_bf16 v[20:23], a[164:167], v[36:39], v[20:23]\n"
"ds_read_b128 v[60:63], v2 offset:50176\n"
"v_mfma_f32_16x16x32_bf16 v[20:23], a[52:55], v[32:35], v[20:23]\n"
"s_waitcnt lgkmcnt(4)\n"
"v_mfma_f32_16x16x32_bf16 v[20:23], v[84:87], v[40:43], v[20:23]\n"
"ds_read_b128 v[32:35], v2 offset:8192\n"
"v_mfma_f32_16x16x32_bf16 v[20:23], v[196:199], v[44:47], v[20:23]\n"
"buffer_load_dwordx4 v2, s[32:35], s63 offen offset:2048 sc1 lds\n"
"v_mfma_f32_16x16x32_bf16 v[16:19], v[192:195], v[44:47], v[16:19]\n"
"ds_read_b128 v[36:39], v2 offset:22528\n"
"v_mfma_f32_16x16x32_bf16 v[16:19], v[80:83], v[40:43], v[16:19]\n"
"s_waitcnt lgkmcnt(4)\n"
"v_mfma_f32_16x16x32_bf16 v[16:19], a[56:59], v[48:51], v[16:19]\n"
"ds_read_b128 v[40:43], v2 offset:36864\n"
"v_mfma_f32_16x16x32_bf16 v[16:19], a[168:171], v[52:55], v[16:19]\n"
"buffer_load_dwordx4 v2, s[32:35], s63 offen offset:3072 sc1 lds\n"
"v_mfma_f32_16x16x32_bf16 v[20:23], a[172:175], v[52:55], v[20:23]\n"
"ds_read_b128 v[44:47], v2 offset:51200\n"
"v_mfma_f32_16x16x32_bf16 v[20:23], a[60:63], v[48:51], v[20:23]\n"
"s_waitcnt lgkmcnt(4)\n"
"v_mfma_f32_16x16x32_bf16 v[20:23], v[92:95], v[56:59], v[20:23]\n"
"ds_read_b128 v[48:51], v2 offset:9216\n"
"v_mfma_f32_16x16x32_bf16 v[20:23], v[204:207], v[60:63], v[20:23]\n"
"s_add_u32 s63, s63, 4096\n"
"s_add_u32 m0, s64, 65536\n"
"v_mfma_f32_16x16x32_bf16 v[16:19], v[200:203], v[60:63], v[16:19]\n"
"ds_read_b128 v[52:55], v2 offset:23552\n"
"v_mfma_f32_16x16x32_bf16 v[16:19], v[88:91], v[56:59], v[16:19]\n"
"s_waitcnt lgkmcnt(4)\n"
"v_mfma_f32_16x16x32_bf16 v[16:19], a[64:67], v[32:35], v[16:19]\n"
"ds_read_b128 v[56:59], v2 offset:37888\n"
"v_mfma_f32_16x16x32_bf16 v[16:19], a[176:179], v[36:39], v[16:19]\n"
"buffer_load_dwordx4 v2, s[32:35], s63 offen offset:0 sc1 lds\n"
"v_mfma_f32_16x16x32_bf16 v[20:23], a[180:183], v[36:39], v[20:23]\n"
"ds_read_b128 v[60:63], v2 offset:52224\n"
"v_mfma_f32_16x16x32_bf16 v[20:23], a[68:71], v[32:35], v[20:23]\n"
"s_waitcnt lgkmcnt(4)\n"
"v_mfma_f32_16x16x32_bf16 v[20:23], v[100:103], v[40:43], v[20:23]\n"
"ds_read_b128 v[32:35], v2 offset:10240\n"
"v_mfma_f32_16x16x32_bf16 v[20:23], v[212:215], v[44:47], v[20:23]\n"
"buffer_load_dwordx4 v2, s[32:35], s63 offen offset:1024 sc1 lds\n"
"v_mfma_f32_16x16x32_bf16 v[16:19], v[208:211], v[44:47], v[16:19]\n"
"ds_read_b128 v[36:39], v2 offset:24576\n"
"v_mfma_f32_16x16x32_bf16 v[16:19], v[96:99], v[40:43], v[16:19]\n"
"s_waitcnt lgkmcnt(4)\n"
"v_mfma_f32_16x16x32_bf16 v[16:19], a[72:75], v[48:51], v[16:19]\n"
"ds_read_b128 v[40:43], v2 offset:38912\n"
"v_mfma_f32_16x16x32_bf16 v[16:19], a[184:187], v[52:55], v[16:19]\n"
"buffer_load_dwordx4 v2, s[32:35], s63 offen offset:2048 sc1 lds\n"
"v_mfma_f32_16x16x32_bf16 v[20:23], a[188:191], v[52:55], v[20:23]\n"
"ds_read_b128 v[44:47], v2 offset:53248\n"
"v_mfma_f32_16x16x32_bf16 v[20:23], a[76:79], v[48:51], v[20:23]\n"
"s_waitcnt lgkmcnt(4)\n"
"v_mfma_f32_16x16x32_bf16 v[20:23], v[108:111], v[56:59], v[20:23]\n"
"ds_read_b128 v[48:51], v2 offset:11264\n"
"v_mfma_f32_16x16x32_bf16 v[20:23], v[220:223], v[60:63], v[20:23]\n"
"v_mfma_f32_16x16x32_bf16 v[16:19], v[216:219], v[60:63], v[16:19]\n"
"ds_read_b128 v[52:55], v2 offset:25600\n"
"v_mfma_f32_16x16x32_bf16 v[16:19], v[104:107], v[56:59], v[16:19]\n"
"s_waitcnt lgkmcnt(4)\n"
"v_mfma_f32_16x16x32_bf16 v[16:19], a[80:83], v[32:35], v[16:19]\n"
"ds_read_b128 v[56:59], v2 offset:39936\n"
"v_mfma_f32_16x16x32_bf16 v[16:19], a[192:195], v[36:39], v[16:19]\n"
"buffer_load_dwordx4 v2, s[32:35], s63 offen offset:3072 sc1 lds\n"
"s_add_u32 s63, s63, 4096\n"
"v_mfma_f32_16x16x32_bf16 v[20:23], a[196:199], v[36:39], v[20:23]\n"
"ds_read_b128 v[60:63], v2 offset:54272\n"
"s_add_u32 m0, s64, 69632\n"
"v_mfma_f32_16x16x32_bf16 v[20:23], a[84:87], v[32:35], v[20:23]\n"
"s_waitcnt lgkmcnt(4)\n"
"v_mfma_f32_16x16x32_bf16 v[20:23], v[116:119], v[40:43], v[20:23]\n"
"ds_read_b128 v[32:35], v2 offset:12288\n"
"v_mfma_f32_16x16x32_bf16 v[20:23], v[228:231], v[44:47], v[20:23]\n"
"buffer_load_dwordx4 v2, s[32:35], s63 offen offset:0 sc1 lds\n"
"v_mfma_f32_16x16x32_bf16 v[16:19], v[224:227], v[44:47], v[16:19]\n"
"ds_read_b128 v[36:39], v2 offset:26624\n"
"v_mfma_f32_16x16x32_bf16 v[16:19], v[112:115], v[40:43], v[16:19]\n"
"s_waitcnt lgkmcnt(4)\n"
"v_mfma_f32_16x16x32_bf16 v[16:19], a[88:91], v[48:51], v[16:19]\n"
"ds_read_b128 v[40:43], v2 offset:40960\n"
"v_mfma_f32_16x16x32_bf16 v[16:19], a[200:203], v[52:55], v[16:19]\n"
"buffer_load_dwordx4 v2, s[32:35], s63 offen offset:1024 sc1 lds\n"
"s_add_u32 s63, s63, 1175552\n"
"v_mfma_f32_16x16x32_bf16 v[20:23], a[204:207], v[52:55], v[20:23]\n"
"ds_read_b128 v[44:47], v2 offset:55296\n"
"v_mfma_f32_16x16x32_bf16 v[20:23], a[92:95], v[48:51], v[20:23]\n"
"s_waitcnt lgkmcnt(4)\n"
"v_mfma_f32_16x16x32_bf16 v[20:23], v[124:127], v[56:59], v[20:23]\n"
"ds_read_b128 v[48:51], v2 offset:13312\n"
"v_mfma_f32_16x16x32_bf16 v[20:23], v[236:239], v[60:63], v[20:23]\n"
"v_mfma_f32_16x16x32_bf16 v[16:19], v[232:235], v[60:63], v[16:19]\n"
"ds_read_b128 v[52:55], v2 offset:27648\n"
"v_mfma_f32_16x16x32_bf16 v[16:19], v[120:123], v[56:59], v[16:19]\n"
"s_waitcnt lgkmcnt(4)\n"
"v_mfma_f32_16x16x32_bf16 v[16:19], a[96:99], v[32:35], v[16:19]\n"
"s_waitcnt vmcnt(10)\n"
"s_barrier\n"
"v_mfma_f32_16x16x32_bf16 v[16:19], a[208:211], v[36:39], v[16:19]\n"
"ds_read_b128 v[56:59], v2 offset:41984\n"
"v_mfma_f32_16x16x32_bf16 v[20:23], a[212:215], v[36:39], v[20:23]\n"
"ds_read_b128 v[60:63], v2 offset:56320\n"
"v_mfma_f32_16x16x32_bf16 v[20:23], a[100:103], v[32:35], v[20:23]\n"
"s_waitcnt lgkmcnt(4)\n"
"v_mfma_f32_16x16x32_bf16 v[20:23], v[132:135], v[40:43], v[20:23]\n"
"ds_read_b128 v[32:35], v4 offset:0\n"
"v_mfma_f32_16x16x32_bf16 v[20:23], v[244:247], v[44:47], v[20:23]\n"
"v_mfma_f32_16x16x32_bf16 v[16:19], v[240:243], v[44:47], v[16:19]\n"
"ds_read_b128 v[36:39], v4 offset:14336\n"
"v_mfma_f32_16x16x32_bf16 v[16:19], v[128:131], v[40:43], v[16:19]\n"
"s_waitcnt lgkmcnt(4)\n"
"v_mfma_f32_16x16x32_bf16 v[16:19], a[104:107], v[48:51], v[16:19]\n"
"ds_read_b128 v[40:43], v4 offset:28672\n"
"v_mfma_f32_16x16x32_bf16 v[16:19], a[216:219], v[52:55], v[16:19]\n"
"v_mfma_f32_16x16x32_bf16 v[20:23], a[220:223], v[52:55], v[20:23]\n"
"ds_read_b128 v[44:47], v4 offset:43008\n"
"v_mfma_f32_16x16x32_bf16 v[20:23], a[108:111], v[48:51], v[20:23]\n"
"s_waitcnt lgkmcnt(4)\n"
"v_mfma_f32_16x16x32_bf16 v[20:23], v[140:143], v[56:59], v[20:23]\n"
"ds_read_b128 v[48:51], v4 offset:1024\n"
"v_mfma_f32_16x16x32_bf16 v[20:23], v[252:255], v[60:63], v[20:23]\n"
"v_mfma_f32_16x16x32_bf16 v[16:19], v[248:251], v[60:63], v[16:19]\n"
"ds_read_b128 v[52:55], v4 offset:15360\n"
"v_mfma_f32_16x16x32_bf16 v[16:19], v[136:139], v[56:59], v[16:19]\n"
"s_branch .L_dbl_3\n"
".L_dbt_3:\n"
"s_nop 1\n"
"s_nop 0\n"
"v_cvt_pk_bf16_f32 v15, v30, v31\n"
"v_cvt_pk_bf16_f32 v14, v28, v29\n"
"s_nop 1\n"
"v_cvt_pk_bf16_f32 v13, v26, v27\n"
"v_cvt_pk_bf16_f32 v12, v24, v25\n"
"ds_write_b128 v5, v[12:15]\n"
"ds_read2st64_b32 v[12:13], v3 offset0:0 offset1:1\n"
"ds_read2st64_b32 v[14:15], v3 offset0:2 offset1:3\n"
"s_waitcnt lgkmcnt(0)\n"
"buffer_atomic_pk_add_bf16 v12, v1, s[44:47], s65 offen offset:0 sc1\n"
"buffer_atomic_pk_add_bf16 v13, v1, s[44:47], s65 offen offset:2048 sc1\n"
"s_add_u32 s65, s65, 16384\n"
"buffer_atomic_pk_add_bf16 v14, v1, s[44:47], s65 offen offset:0 sc1\n"
"buffer_atomic_pk_add_bf16 v15, v1, s[44:47], s65 offen offset:2048 sc1\n"
"s_add_u32 s65, s65, 16384\n"
"s_or_b32 s48, s28, s27\n"
"s_or_b32 s48, s48, s26\n"
"s_cbranch_scc1 .L_end\n"
"v_mov_b32 v8, s54\n"
"v_mov_b32 v9, s55\n"
"buffer_store_dwordx2 v[8:9], off, s[32:35], 0 sc1\n"
"s_branch .L_end\n"
".L_rtg0_0:\n"
"s_mov_b32 s48, 20000\n"
".L_rlg0_0:\n"
"s_sleep 8\n"
".L_rfg0_0:\n"
"buffer_load_dwordx2 v[12:13], v14, s[32:35], s66 offen sc1\n"
"s_sub_u32 s48, s48, 1\n"
"s_cbranch_scc1 .L_end\n"
"s_waitcnt vmcnt(0)\n"
"v_cmp_ne_u64 vcc, v[16:17], v[12:13]\n"
"s_cbranch_vccnz .L_rlg0_0\n"
"s_branch .L_bkg0_0\n"
".L_rtg1_0:\n"
"s_mov_b32 s48, 20000\n"
".L_rlg1_0:\n"
"s_sleep 8\n"
".L_rfg1_0:\n"
"buffer_load_dwordx2 v[12:13], v14, s[32:35], s66 offen sc1\n"
"s_sub_u32 s48, s48, 1\n"
"s_cbranch_scc1 .L_end\n"
"s_waitcnt vmcnt(0)\n"
"v_cmp_ne_u64 vcc, v[16:17], v[12:13]\n"
"s_cbranch_vccnz .L_rlg1_0\n"
"s_branch .L_bkg1_0\n"
".L_rtg0_1:\n"
"s_mov_b32 s48, 20000\n"
".L_rlg0_1:\n"
"s_sleep 8\n"
".L_rfg0_1:\n"
"buffer_load_dwordx2 v[12:13], v14, s[32:35], s66 offen sc1\n"
"s_sub_u32 s48, s48, 1\n"
"s_cbranch_scc1 .L_end\n"
"s_waitcnt vmcnt(0)\n"
"v_cmp_ne_u64 vcc, v[16:17], v[12:13]\n"
"s_cbranch_vccnz .L_rlg0_1\n"
"s_branch .L_bkg0_1\n"
".L_rtg1_1:\n"
"s_mov_b32 s48, 20000\n"
".L_rlg1_1:\n"
"s_sleep 8\n"
".L_rfg1_1:\n"
"buffer_load_dwordx2 v[12:13], v14, s[32:35], s66 offen sc1\n"
"s_sub_u32 s48, s48, 1\n"
"s_cbranch_scc1 .L_end\n"
"s_waitcnt vmcnt(0)\n"
"v_cmp_ne_u64 vcc, v[16:17], v[12:13]\n"
"s_cbranch_vccnz .L_rlg1_1\n"
"s_branch .L_bkg1_1\n"
".L_rtg0_2:\n"
"s_mov_b32 s48, 20000\n"
".L_rlg0_2:\n"
"s_sleep 8\n"
".L_rfg0_2:\n"
"buffer_load_dwordx2 v[12:13], v14, s[32:35], s66 offen sc1\n"
"s_sub_u32 s48, s48, 1\n"
"s_cbranch_scc1 .L_end\n"
"s_waitcnt vmcnt(0)\n"
"v_cmp_ne_u64 vcc, v[16:17], v[12:13]\n"
"s_cbranch_vccnz .L_rlg0_2\n"
"s_branch .L_bkg0_2\n"
".L_rtg1_2:\n"
"s_mov_b32 s48, 20000\n"
".L_rlg1_2:\n"
"s_sleep 8\n"
".L_rfg1_2:\n"
"buffer_load_dwordx2 v[12:13], v14, s[32:35], s66 offen sc1\n"
"s_sub_u32 s48, s48, 1\n"
"s_cbranch_scc1 .L_end\n"
"s_waitcnt vmcnt(0)\n"
"v_cmp_ne_u64 vcc, v[16:17], v[12:13]\n"
"s_cbranch_vccnz .L_rlg1_2\n"
"s_branch .L_bkg1_2\n"
".L_rtg0_3:\n"
"s_mov_b32 s48, 20000\n"
".L_rlg0_3:\n"
"s_sleep 8\n"
".L_rfg0_3:\n"
"buffer_load_dwordx2 v[12:13], v14, s[32:35], s66 offen sc1\n"
"s_sub_u32 s48, s48, 1\n"
"s_cbranch_scc1 .L_end\n"
"s_waitcnt vmcnt(0)\n"
"v_cmp_ne_u64 vcc, v[16:17], v[12:13]\n"
"s_cbranch_vccnz .L_rlg0_3\n"
"s_branch .L_bkg0_3\n"
".L_rtg1_3:\n"
"s_mov_b32 s48, 20000\n"
".L_rlg1_3:\n"
"s_sleep 8\n"
".L_rfg1_3:\n"
"buffer_load_dwordx2 v[12:13], v14, s[32:35], s66 offen sc1\n"
"s_sub_u32 s48, s48, 1\n"
"s_cbranch_scc1 .L_end\n"
"s_waitcnt vmcnt(0)\n"
"v_cmp_ne_u64 vcc, v[16:17], v[12:13]\n"
"s_cbranch_vccnz .L_rlg1_3\n"
"s_branch .L_bkg1_3\n"
".L_rtqe:\n"
"s_mov_b32 s48, 20000\n"
"s_branch .L_rfqe\n"
".L_rlqe:\n"
"s_sleep 8\n"
".L_rfqe:\n"
"buffer_load_dwordx2 v[60:61], v56, s[32:35], s66 offen sc1\n"
"s_sub_u32 s48, s48, 1\n"
"s_cbranch_scc1 .L_end\n"
"s_waitcnt vmcnt(0)\n"
"v_cmp_ne_u64 vcc, s[54:55], v[60:61]\n"
"s_cbranch_vccnz .L_rlqe\n"
"s_branch .L_bkqe\n"
".L_rtq0:\n"
"s_mov_b32 s48, 20000\n"
".L_rlq0:\n"
"s_sleep 8\n"
".L_rfq0:\n"
"buffer_load_dwordx2 v[12:13], v0, s[32:35], s66 offen sc1\n"
"s_sub_u32 s48, s48, 1\n"
"s_cbranch_scc1 .L_end\n"
"s_waitcnt vmcnt(0)\n"
"v_cmp_ne_u64 vcc, v[30:31], v[12:13]\n"
"s_cbranch_vccnz .L_rlq0\n"
"s_branch .L_bkq0\n"
".L_rtq1:\n"
"s_mov_b32 s48, 20000\n"
".L_rlq1:\n"
"s_sleep 8\n"
".L_rfq1:\n"
"buffer_load_dwordx2 v[12:13], v0, s[32:35], s66 offen sc1\n"
"s_sub_u32 s48, s48, 1\n"
"s_cbranch_scc1 .L_end\n"
"s_waitcnt vmcnt(0)\n"
"v_cmp_ne_u64 vcc, v[30:31], v[12:13]\n"
"s_cbranch_vccnz .L_rlq1\n"
"s_branch .L_bkq1\n"
".L_qs0:\n"
"s_cmp_lg_u32 s62, 30\n"
"s_cbranch_scc1 .L_qn4\n"
"v_mov_b32 v120, 0xff61b1e6\n"
"v_mov_b32 v121, 0xff61b1e6\n"
"v_mov_b32 v122, 0xff61b1e6\n"
"v_mov_b32 v123, 0xff61b1e6\n"
"v_cmp_ne_u32 vcc, 0xffff, v126\n"
"s_nop 1\n"
"v_cndmask_b32 v120, 0, v120, vcc\n"
"v_cmp_ne_u32 vcc, 0xffff0000, v126\n"
"s_nop 1\n"
"v_cndmask_b32 v121, 0, v121, vcc\n"
"v_cmp_ne_u32 vcc, 0xffff, v127\n"
"s_nop 1\n"
"v_cndmask_b32 v122, 0, v122, vcc\n"
"v_cmp_ne_u32 vcc, 0xffff0000, v127\n"
"s_nop 1\n"
"v_cndmask_b32 v123, 0, v123, vcc\n"
"s_nop 1\n"
"s_branch .L_qb0\n"
".L_qn4:\n"
"s_cmp_lg_u32 s62, 17\n"
"s_cbranch_scc1 .L_qn30\n"
"s_mov_b32 s72, s69\n"
"s_nop 1\n"
"s_branch .L_qb0\n"
".L_qn30:\n"
"s_cmp_lg_u32 s62, 16\n"
"s_cbranch_scc1 .L_qn32\n"
"s_mov_b32 s72, 1187840\n"
"s_nop 1\n"
"s_branch .L_qb0\n"
".L_qn32:\n"
"s_cmp_lg_u32 s62, 15\n"
"s_cbranch_scc1 .L_qn34\n"
"s_sub_u32 s68, s68, 39198720\n"
"s_nop 1\n"
"s_branch .L_qb0\n"
".L_qn34:\n"
"s_cmp_lg_u32 s62, 14\n"
"s_cbranch_scc1 .L_qn36\n"
"v_mov_b32 v120, 0xff61b1e6\n"
"v_mov_b32 v121, 0xff61b1e6\n"
"v_mov_b32 v122, 0xff61b1e6\n"
"v_mov_b32 v123, 0xff61b1e6\n"
"v_cmp_ne_u32 vcc, 0xffff, v126\n"
"s_nop 1\n"
"v_cndmask_b32 v120, 0, v120, vcc\n"
"v_cmp_ne_u32 vcc, 0xffff0000, v126\n"
"s_nop 1\n"
"v_cndmask_b32 v121, 0, v121, vcc\n"
"v_cmp_ne_u32 vcc, 0xffff, v127\n"
"s_nop 1\n"
"v_cndmask_b32 v122, 0, v122, vcc\n"
"v_cmp_ne_u32 vcc, 0xffff0000, v127\n"
"s_nop 1\n"
"v_cndmask_b32 v123, 0, v123, vcc\n"
"s_nop 1\n"
"s_branch .L_qb0\n"
".L_qn36:\n"
"s_branch .L_qb0\n"
".L_qs1:\n"
"s_cmp_lg_u32 s62, 31\n"
"s_cbranch_scc1 .L_qn3\n"
"s_cmp_lg_u32 s28, 0\n"
"s_cbranch_scc1 .L_qb1\n"
"v_mov_b32 v120, 0xff61b1e6\n"
"v_mov_b32 v121, 0xff61b1e6\n"
"v_mov_b32 v122, 0xff61b1e6\n"
"v_mov_b32 v123, 0xff61b1e6\n"
"s_nop 1\n"
"s_branch .L_qb1\n"
".L_qn3:\n"
"s_cmp_lg_u32 s62, 17\n"
"s_cbranch_scc1 .L_qn31\n"
"s_mov_b32 s72, s70\n"
"s_nop 1\n"
"s_branch .L_qb1\n"
".L_qn31:\n"
"s_cmp_lg_u32 s62, 15\n"
"s_cbranch_scc1 .L_qn35\n"
"s_add_u32 s68, s68, 38010880\n"
"s_cmp_lg_u32 s28, 0\n"
"s_cbranch_scc1 .L_qb1\n"
"v_mov_b32 v120, 0xff61b1e6\n"
"v_mov_b32 v121, 0xff61b1e6\n"
"v_mov_b32 v122, 0xff61b1e6\n"
"v_mov_b32 v123, 0xff61b1e6\n"
"s_nop 1\n"
"s_branch .L_qb1\n"
".L_qn35:\n"
"s_branch .L_qb1\n"
".L_rtw1_1:\n"
"s_mov_b32 s48, 20000\n"
".L_rlw1_1:\n"
"s_sleep 8\n"
".L_rfw1_1:\n"
"buffer_load_dwordx2 v[12:13], v14, s[32:35], s66 offen sc1\n"
"s_sub_u32 s48, s48, 1\n"
"s_cbranch_scc1 .L_end\n"
"s_waitcnt vmcnt(0)\n"
"v_cmp_ne_u64 vcc, v[16:17], v[12:13]\n"
"s_cbranch_vccnz .L_rlw1_1\n"
"s_branch .L_bkw1_1\n"
".L_rth0:\n"
"s_mov_b32 s48, 20000\n"
".L_rlh0:\n"
"s_sleep 8\n"
".L_rfh0:\n"
"buffer_load_dwordx2 v[12:13], v14, s[32:35], s66 offen sc1\n"
"s_sub_u32 s48, s48, 1\n"
"s_cbranch_scc1 .L_end\n"
"s_waitcnt vmcnt(0)\n"
"v_cmp_ne_u64 vcc, v[16:17], v[12:13]\n"
"s_cbranch_vccnz .L_rlh0\n"
"s_branch .L_bkh0\n"
".L_rth1:\n"
"s_mov_b32 s48, 20000\n"
".L_rlh1:\n"
"s_sleep 8\n"
".L_rfh1:\n"
"buffer_load_dwordx2 v[12:13], v14, s[32:35], s66 offen sc1\n"
"s_sub_u32 s48, s48, 1\n"
"s_cbranch_scc1 .L_end\n"
"s_waitcnt vmcnt(0)\n"
"v_cmp_ne_u64 vcc, v[16:17], v[12:13]\n"
"s_cbranch_vccnz .L_rlh1\n"
"s_branch .L_bkh1\n"
".L_rtl0_1:\n"
"s_mov_b32 s48, 20000\n"
".L_rll0_1:\n"
"s_sleep 8\n"
".L_rfl0_1:\n"
"buffer_load_dwordx2 v[6:7], v0, s[32:35], s66 offen sc1\n"
"s_sub_u32 s48, s48, 1\n"
"s_cbranch_scc1 .L_end\n"
"s_waitcnt vmcnt(0)\n"
"v_cmp_ne_u64 vcc, v[8:9], v[6:7]\n"
"s_cbranch_vccnz .L_rll0_1\n"
"s_branch .L_bkl0_1\n"
".L_rtl1_1:\n"
"s_mov_b32 s48, 20000\n"
".L_rll1_1:\n"
"s_sleep 8\n"
".L_rfl1_1:\n"
"buffer_load_dwordx2 v[6:7], v0, s[32:35], s66 offen sc1\n"
"s_sub_u32 s48, s48, 1\n"
"s_cbranch_scc1 .L_end\n"
"s_waitcnt vmcnt(0)\n"
"v_cmp_ne_u64 vcc, v[8:9], v[6:7]\n"
"s_cbranch_vccnz .L_rll1_1\n"
"s_branch .L_bkl1_1\n"
".L_rtm1_1:\n"
"s_mov_b32 s48, 20000\n"
".L_rlm1_1:\n"
"s_sleep 8\n"
".L_rfm1_1:\n"
"buffer_load_dwordx2 v[6:7], v0, s[32:35], s66 offen sc1\n"
"s_sub_u32 s48, s48, 1\n"
"s_cbranch_scc1 .L_end\n"
"s_waitcnt vmcnt(0)\n"
"v_cmp_ne_u64 vcc, v[8:9], v[6:7]\n"
"s_cbranch_vccnz .L_rlm1_1\n"
"s_branch .L_bkm1_1\n"
".L_rtm0_1:\n"
"s_mov_b32 s48, 20000\n"
".L_rlm0_1:\n"
"s_sleep 8\n"
".L_rfm0_1:\n"
"buffer_load_dwordx2 v[6:7], v0, s[32:35], s66 offen sc1\n"
"s_sub_u32 s48, s48, 1\n"
"s_cbranch_scc1 .L_end\n"
"s_waitcnt vmcnt(0)\n"
"v_cmp_ne_u64 vcc, v[8:9], v[6:7]\n"
"s_cbranch_vccnz .L_rlm0_1\n"
"s_branch .L_bkm0_1\n"
".L_rta0_1:\n"
"s_mov_b32 s48, 20000\n"
".L_rla0_1:\n"
"s_sleep 8\n"
".L_rfa0_1:\n"
"buffer_load_dwordx2 v[6:7], v0, s[32:35], s66 offen sc1\n"
"s_sub_u32 s48, s48, 1\n"
"s_cbranch_scc1 .L_end\n"
"s_waitcnt vmcnt(0)\n"
"v_cmp_ne_u64 vcc, v[8:9], v[6:7]\n"
"s_cbranch_vccnz .L_rla0_1\n"
"s_branch .L_bka0_1\n"
".L_rta1_1:\n"
"s_mov_b32 s48, 20000\n"
".L_rla1_1:\n"
"s_sleep 8\n"
".L_rfa1_1:\n"
"buffer_load_dwordx2 v[6:7], v0, s[32:35], s66 offen sc1\n"
"s_sub_u32 s48, s48, 1\n"
"s_cbranch_scc1 .L_end\n"
"s_waitcnt vmcnt(0)\n"
"v_cmp_ne_u64 vcc, v[8:9], v[6:7]\n"
"s_cbranch_vccnz .L_rla1_1\n"
"s_branch .L_bka1_1\n"
".L_rtb1_1:\n"
"s_mov_b32 s48, 20000\n"
".L_rlb1_1:\n"
"s_sleep 8\n"
".L_rfb1_1:\n"
"buffer_load_dwordx2 v[6:7], v0, s[32:35], s66 offen sc1\n"
"s_sub_u32 s48, s48, 1\n"
"s_cbranch_scc1 .L_end\n"
"s_waitcnt vmcnt(0)\n"
"v_cmp_ne_u64 vcc, v[8:9], v[6:7]\n"
"s_cbranch_vccnz .L_rlb1_1\n"
"s_branch .L_bkb1_1\n"
".L_rtb0_1:\n"
"s_mov_b32 s48, 20000\n"
".L_rlb0_1:\n"
"s_sleep 8\n"
".L_rfb0_1:\n"
"buffer_load_dwordx2 v[6:7], v0, s[32:35], s66 offen sc1\n"
"s_sub_u32 s48, s48, 1\n"
"s_cbranch_scc1 .L_end\n"
"s_waitcnt vmcnt(0)\n"
"v_cmp_ne_u64 vcc, v[8:9], v[6:7]\n"
"s_cbranch_vccnz .L_rlb0_1\n"
"s_branch .L_bkb0_1\n"
".L_end:\n"
"s_waitcnt vmcnt(0) lgkmcnt(0)\n"
".L_last:\n"
    : "+{v0}"(tid)
    : "{s[8:9]}"(x), "{s[10:11]}"(wq), "{s[12:13]}"(wo), "{s[14:15]}"(wg), "{s[16:17]}"(wu), "{s[18:19]}"(wd),
      "{s[20:21]}"(out), "{s[22:23]}"(ws), "{s24}"(blockIdx.x)
    : "v1", "v2", "v3", "v4", "v5", "v6", "v7", "v8", "v9", "v10", "v11", "v12", "v13", "v14", "v15", "v16", "v17", "v18", "v19", "v20", "v21", "v22", "v23", "v24", "v25", "v26", "v27", "v28", "v29", "v30", "v31", "v32", "v33", "v34", "v35", "v36", "v37", "v38", "v39", "v40", "v41", "v42", "v43", "v44", "v45", "v46", "v47", "v48", "v49", "v50", "v51", "v52", "v53", "v54", "v55", "v56", "v57", "v58", "v59", "v60", "v61", "v62", "v63", "v64", "v65", "v66", "v67", "v68", "v69", "v70", "v71", "v72", "v73", "v74", "v75", "v76", "v77", "v78", "v79", "v80", "v81", "v82", "v83", "v84", "v85", "v86", "v87", "v88", "v89", "v90", "v91", "v92", "v93", "v94", "v95", "v96", "v97", "v98", "v99", "v100", "v101", "v102", "v103", "v104", "v105", "v106", "v107", "v108", "v109", "v110", "v111", "v112", "v113", "v114", "v115", "v116", "v117", "v118", "v119", "v120", "v121", "v122", "v123", "v124", "v125", "v126", "v127", "v128", "v129", "v130", "v131", "v132", "v133", "v134", "v135", "v136", "v137", "v138", "v139", "v140", "v141", "v142", "v143", "v144", "v145", "v146", "v147", "v148", "v149", "v150", "v151", "v152", "v153", "v154", "v155", "v156", "v157", "v158", "v159", "v160", "v161", "v162", "v163", "v164", "v165", "v166", "v167", "v168", "v169", "v170", "v171", "v172", "v173", "v174", "v175", "v176", "v177", "v178", "v179", "v180", "v181", "v182", "v183", "v184", "v185", "v186", "v187", "v188", "v189", "v190", "v191", "v192", "v193", "v194", "v195", "v196", "v197", "v198", "v199", "v200", "v201", "v202", "v203", "v204", "v205", "v206", "v207", "v208", "v209", "v210", "v211", "v212", "v213", "v214", "v215", "v216", "v217", "v218", "v219", "v220", "v221", "v222", "v223", "v224", "v225", "v226", "v227", "v228", "v229", "v230", "v231", "v232", "v233", "v234", "v235", "v236", "v237", "v238", "v239", "v240", "v241", "v242", "v243", "v244", "v245", "v246", "v247", "v248", "v249", "v250", "v251", "v252", "v253", "v254", "v255", "a0", "a1", "a2", "a3", "a4", "a5", "a6", "a7", "a8", "a9", "a10", "a11", "a12", "a13", "a14", "a15", "a16", "a17", "a18", "a19", "a20", "a21", "a22", "a23", "a24", "a25", "a26", "a27", "a28", "a29", "a30", "a31", "a32", "a33", "a34", "a35", "a36", "a37", "a38", "a39", "a40", "a41", "a42", "a43", "a44", "a45", "a46", "a47", "a48", "a49", "a50", "a51", "a52", "a53", "a54", "a55", "a56", "a57", "a58", "a59", "a60", "a61", "a62", "a63", "a64", "a65", "a66", "a67", "a68", "a69", "a70", "a71", "a72", "a73", "a74", "a75", "a76", "a77", "a78", "a79", "a80", "a81", "a82", "a83", "a84", "a85", "a86", "a87", "a88", "a89", "a90", "a91", "a92", "a93", "a94", "a95", "a96", "a97", "a98", "a99", "a100", "a101", "a102", "a103", "a104", "a105", "a106", "a107", "a108", "a109", "a110", "a111", "a112", "a113", "a114", "a115", "a116", "a117", "a118", "a119", "a120", "a121", "a122", "a123", "a124", "a125", "a126", "a127", "a128", "a129", "a130", "a131", "a132", "a133", "a134", "a135", "a136", "a137", "a138", "a139", "a140", "a141", "a142", "a143", "a144", "a145", "a146", "a147", "a148", "a149", "a150", "a151", "a152", "a153", "a154", "a155", "a156", "a157", "a158", "a159", "a160", "a161", "a162", "a163", "a164", "a165", "a166", "a167", "a168", "a169", "a170", "a171", "a172", "a173", "a174", "a175", "a176", "a177", "a178", "a179", "a180", "a181", "a182", "a183", "a184", "a185", "a186", "a187", "a188", "a189", "a190", "a191", "a192", "a193", "a194", "a195", "a196", "a197", "a198", "a199", "a200", "a201", "a202", "a203", "a204", "a205", "a206", "a207", "a208", "a209", "a210", "a211", "a212", "a213", "a214", "a215", "a216", "a217", "a218", "a219", "a220", "a221", "a222", "a223", "a224", "a225", "a226", "a227", "a228", "a229", "a230", "a231", "a232", "a233", "a234", "a235", "a236", "a237", "a238", "a239", "a240", "a241", "a242", "a243", "a244", "a245", "a246", "a247", "a248", "a249", "a250", "a251", "a252", "a253", "a254", "a255", "s25", "s26", "s27", "s28", "s29", "s30", "s31", "s32", "s33", "s34", "s35", "s36", "s37", "s38", "s39", "s40", "s41", "s42", "s43", "s44", "s45", "s46", "s47", "s48", "s49", "s50", "s51", "s52", "s53", "s54", "s55", "s56", "s57", "s58", "s59", "s60", "s61", "s62", "s63", "s64", "s65", "s66", "s67", "s68", "s69", "s70", "s71", "s72", "s73", "s74", "s75", "s76", "s77", "s78", "s79", "s80", "s81", "s82", "s83", "s84", "s85", "s86", "s87", "s88", "s89", "s90", "s91", "s92", "s93", "s94", "s95", "s96", "s97", "s98", "s99", "vcc", "scc", "m0", "memory");
}

extern "C" void kernel_launch(void* const* d_in, const int* in_sizes, int n_in, void* d_out, int out_size,
                              void* d_ws, size_t ws_size, hipStream_t stream) {
    constexpr int smem = 163840;
    (void)hipFuncSetAttribute((const void*)body_k, hipFuncAttributeMaxDynamicSharedMemorySize, smem);
    body_k<<<256, 256, smem, stream>>>(d_in[0], d_in[1], d_in[2], d_in[4], d_in[5], d_in[6], d_out, d_ws);
}
```

```cpp
#include <hip/hip_runtime.h>
#pragma clang diagnostic ignored "-Winline-asm"
#define W_XI 4612096
#define W_O 4648960
#define W_XA 4681728
#define W_H 4718592
#define W_P 4833280
#define W_EB 296960
#define W_MAP 1
extern "C" __global__ __launch_bounds__(256, 1) void body_k(const void* x, const void* wq, const void* wo, const void* wg, const void* wu,
                                                            const void* wd, void* out, void* ws) {
    unsigned tid = threadIdx.x;
    asm volatile(
R"#(
s_load_dwordx2 s[52:53],s[22:23],0x0
v_lshrrev_b32 v3,6,v0
v_and_b32 v1,63,v0
v_readfirstlane_b32 s26,v3
v_lshlrev_b32 v2,4,v1
s_add_u32 s32,s22,0x0
s_addc_u32 s33,s23,0x0
s_and_b32 s33,s33,0xffff
s_mov_b32 s34,0x504c000
s_mov_b32 s35,0x20000
s_mov_b32 s73,0
s_lshr_b32 s49,s24,3
s_cmp_lt_u32 s49,16
s_cbranch_scc1 .L_nc
s_bfe_u32 s28,s24,0x20001
s_and_b32 s27,s49,7
s_bfe_u32 s29,s49,0x10003
s_mov_b32 s62,0
s_mov_b32 s74,1
s_bitcmp1_b32 s24,0
s_cbranch_scc0 .L_nt
s_getpc_b64 s[40:41]
.L_ct16:
s_and_b32 s41,s41,0xffff
s_mov_b32 s42,.L_last-.L_ct16
s_mov_b32 s43,0x20000
s_sub_u32 s48,s49,16
s_lshl_b32 s48,s48,8
v_add_lshl_u32 v5,v0,s48,7
buffer_load_dword v10,v5,s[40:43],0 offen
s_add_u32 s36,s10,0x0
s_addc_u32 s37,s11,0x0
s_and_b32 s37,s37,0xffff
s_mov_b32 s38,0x600000
s_mov_b32 s39,0x20000
s_lshl_b32 s48,s49,8
s_sub_u32 s48,s48,4096
v_add_u32 v4,s48,v0
s_lshl_b32 s48,s28,9
s_add_u32 s44,s8,0x0
s_addc_u32 s45,s9,0x0
s_and_b32 s45,s45,0xffff
s_mov_b32 s46,0x400000
s_mov_b32 s47,0x20000
v_lshrrev_b32 v5,10,v4
v_bfe_u32 v6,v4,1,9
v_lshl_add_u32 v5,v5,13,v6
v_subrev_u32 v5,512,v5
v_and_b32 v5,0x7fff,v5
v_lshlrev_b32 v5,7,v5
buffer_load_dword v10,v5,s[44:47],0 offen
v_mul_u32_u24 v5,43691,v4
v_lshrrev_b32 v5,19,v5
v_mul_u32_u24 v6,12,v5
v_sub_u32 v6,v4,v6
v_mul_u32_u24 v5,48,v5
v_lshrrev_b32 v7,2,v6
v_mul_u32_u24 v7,12,v7
v_add3_u32 v5,v5,v6,v7
v_lshlrev_b32 v5,7,v5
v_add_u32 v4,4096,v4
buffer_load_dword v10,v5,s[36:39],s48 offen
v_mul_u32_u24 v5,43691,v4
v_lshrrev_b32 v5,19,v5
v_mul_u32_u24 v6,12,v5
v_sub_u32 v6,v4,v6
v_mul_u32_u24 v5,48,v5
v_lshrrev_b32 v7,2,v6
v_mul_u32_u24 v7,12,v7
v_add3_u32 v5,v5,v6,v7
v_lshlrev_b32 v5,7,v5
v_add_u32 v4,4096,v4
buffer_load_dword v10,v5,s[36:39],s48 offen
v_mul_u32_u24 v5,43691,v4
v_lshrrev_b32 v5,19,v5
v_mul_u32_u24 v6,12,v5
v_sub_u32 v6,v4,v6
v_mul_u32_u24 v5,48,v5
v_lshrrev_b32 v7,2,v6
v_mul_u32_u24 v7,12,v7
v_add3_u32 v5,v5,v6,v7
v_lshlrev_b32 v5,7,v5
v_add_u32 v4,4096,v4
buffer_load_dword v10,v5,s[36:39],s48 offen
s_mov_b32 s62,1
s_mov_b32 s74,4
.L_nt:
s_lshl2_add_u32 s50,s27,s26
s_mul_i32 s70,s50,1152
v_and_b32 v20,15,v1
v_lshlrev_b32 v20,11,v20
v_and_b32 v21,48,v1
s_lshl_b32 s48,s50,6
v_add3_u32 v20,v20,v21,s48
s_add_u32 s36,s8,0x0
s_addc_u32 s37,s9,0x0
s_and_b32 s37,s37,0xffff
s_mov_b32 s38,0x400000
s_mov_b32 s39,0x20000
s_lshl2_add_u32 s48,s28,s62
s_lshl_b32 s48,s48,3
s_sub_u32 s48,s48,s29
s_add_u32 s48,s48,127
s_lshl_b32 s66,s27,7
s_add_u32 s66,s66,286720
s_sub_u32 s66,s66,s70
s_add_u32 s49,s28,3
s_and_b32 s49,s49,3
s_mul_i32 s49,s49,296960
s_add_u32 s69,s70,s49
s_add_u32 s69,s69,41435136
s_mul_i32 s49,s28,296960
s_add_u32 s70,s70,s49
s_and_b32 s51,s50,6
s_lshl_b32 s51,s51,1
v_add_u32 v3,s51,v1
v_bfi_b32 v3,15,v3,v1
v_lshlrev_b32 v3,2,v3
v_cmp_gt_u32 vcc,32,v1
v_mov_b32 v21,0x7fff0000
s_nop 0
v_cndmask_b32 v21,v21,v3,vcc
s_mul_i32 s49,s29,1187840
s_sub_u32 s71,s70,s49
s_add_u32 s71,s71,4612096
v_mul_u32_u24 v24,2375680,v1
v_add_u32 v24,s71,v24
s_cmp_eq_u32 s29,0
s_cselect_b32 s68,s71,s69
v_writelane_b32 v24,s68,0
s_mul_i32 s49,s62,9502720
s_add_u32 s71,s71,s49
s_cmp_eq_u32 s62,0
s_cselect_b32 s68,s68,s71
s_add_u32 s71,s71,2375680
v_lshrrev_b32 v22,2,v0
v_mov_b32 v23,0x3f803f80
.L_dc:
s_and_b32 s49,s48,127
s_lshl_b32 s49,s49,15
s_add_u32 s48,s48,2
buffer_load_dwordx4 v[32:35],v20,s[36:39],s49 offen
s_and_b32 s49,s48,127
s_lshl_b32 s49,s49,15
s_add_u32 s48,s48,2
buffer_load_dwordx4 v[36:39],v20,s[36:39],s49 offen
s_and_b32 s49,s48,127
s_lshl_b32 s49,s49,15
s_add_u32 s48,s48,2
buffer_load_dwordx4 v[40:43],v20,s[36:39],s49 offen
s_and_b32 s49,s48,127
s_lshl_b32 s49,s49,15
s_add_u32 s48,s48,2
buffer_load_dwordx4 v[44:47],v20,s[36:39],s49 offen
v_cmp_eq_u32 vcc,s62,v22
v_mov_b32 v25,0x7fff0000
s_add_u32 s62,s62,1
v_cndmask_b32 v25,v25,v24,vcc
s_waitcnt lgkmcnt(0)
s_add_u32 s54,s52,0x7f4a7c15
s_addc_u32 s55,s53,0x9e3779b9
v_mov_b32 v8,s54
v_mov_b32 v9,s55
s_waitcnt vmcnt(0)
v_dot2_f32_bf16 v48,v32,v23,0
v_dot2_f32_bf16 v49,v32,v32,0
v_dot2_f32_bf16 v50,v36,v23,0
v_dot2_f32_bf16 v51,v36,v36,0
v_dot2_f32_bf16 v52,v40,v23,0
v_dot2_f32_bf16 v53,v40,v40,0
v_dot2_f32_bf16 v54,v44,v23,0
v_dot2_f32_bf16 v55,v44,v44,0
v_dot2_f32_bf16 v48,v33,v23,v48
v_dot2_f32_bf16 v49,v33,v33,v49
v_dot2_f32_bf16 v50,v37,v23,v50
v_dot2_f32_bf16 v51,v37,v37,v51
v_dot2_f32_bf16 v52,v41,v23,v52
v_dot2_f32_bf16 v53,v41,v41,v53
v_dot2_f32_bf16 v54,v45,v23,v54
v_dot2_f32_bf16 v55,v45,v45,v55
v_dot2_f32_bf16 v48,v34,v23,v48
v_dot2_f32_bf16 v49,v34,v34,v49
v_dot2_f32_bf16 v50,v38,v23,v50
v_dot2_f32_bf16 v51,v38,v38,v51
v_dot2_f32_bf16 v52,v42,v23,v52
v_dot2_f32_bf16 v53,v42,v42,v53
v_dot2_f32_bf16 v54,v46,v23,v54
v_dot2_f32_bf16 v55,v46,v46,v55
v_dot2_f32_bf16 v48,v35,v23,v48
v_dot2_f32_bf16 v49,v35,v35,v49
v_dot2_f32_bf16 v50,v39,v23,v50
v_dot2_f32_bf16 v51,v39,v39,v51
v_dot2_f32_bf16 v52,v43,v23,v52
v_dot2_f32_bf16 v53,v43,v43,v53
v_dot2_f32_bf16 v54,v47,v23,v54
v_dot2_f32_bf16 v55,v47,v47,v55
v_permlane16_swap_b32 v48,v49
v_permlane16_swap_b32 v50,v51
v_permlane16_swap_b32 v52,v53
v_permlane16_swap_b32 v54,v55
v_add_f32 v48,v48,v49
v_add_f32 v50,v50,v51
v_add_f32 v52,v52,v53
v_add_f32 v54,v54,v55
v_permlane32_swap_b32 v48,v49
v_permlane32_swap_b32 v50,v51
v_permlane32_swap_b32 v52,v53
v_permlane32_swap_b32 v54,v55
v_add_f32 v48,v48,v49
v_add_f32 v50,v50,v51
v_add_f32 v52,v52,v53
v_add_f32 v54,v54,v55
buffer_store_dwordx4 v[32:35],v2,s[32:35],s68 offen sc1
buffer_store_dword v48,v21,s[32:35],s68 offen offset:1024 sc1
s_mov_b32 s68,s71
s_add_u32 s71,s71,2375680
buffer_store_dwordx4 v[36:39],v2,s[32:35],s68 offen sc1
buffer_store_dword v50,v21,s[32:35],s68 offen offset:1024 sc1
s_mov_b32 s68,s71
s_add_u32 s71,s71,2375680
buffer_store_dwordx4 v[40:43],v2,s[32:35],s68 offen sc1
buffer_store_dword v52,v21,s[32:35],s68 offen offset:1024 sc1
s_mov_b32 s68,s71
s_add_u32 s71,s71,2375680
buffer_store_dwordx4 v[44:47],v2,s[32:35],s68 offen sc1
buffer_store_dword v54,v21,s[32:35],s68 offen offset:1024 sc1
s_mov_b32 s68,s71
s_add_u32 s71,s71,2375680
s_waitcnt vmcnt(0)
s_barrier
buffer_store_dwordx2 v[8:9],v25,s[32:35],s66 offen sc1
s_cmp_lt_u32 s62,s74
s_cbranch_scc1 .L_dc
s_bitcmp1_b32 s24,0
s_cbranch_scc1 .L_d
s_lshr_b32 s49,s24,3
.L_nc:
s_bfe_u32 s50,s24,0x20001
s_and_b32 s27,s49,3
s_lshl2_add_u32 s27,s50,s27
s_lshr_b32 s28,s49,2
s_bitcmp1_b32 s24,0
s_cbranch_scc1 .L_q
s_and_b32 s27,s49,1
s_lshl1_add_u32 s27,s50,s27
s_lshr_b32 s28,s49,1
s_cmp_lt_u32 s49,4
s_cbranch_scc1 .L_w
s_cmp_ge_u32 s49,16
s_cbranch_scc1 .L_ns
s_getpc_b64 s[40:41]
.L_ct5:
s_and_b32 s41,s41,0xffff
s_mov_b32 s42,.L_last-.L_ct5
s_mov_b32 s43,0x20000
s_sub_u32 s48,s49,5
s_lshl_b32 s48,s48,8
v_add_lshl_u32 v5,v0,s48,7
buffer_load_dword v120,v5,s[40:43],0 offen
s_lshl2_add_u32 s48,s49,s50
s_lshl_b32 s48,s48,8
s_sub_u32 s48,s48,4096
v_add_u32 v4,s48,v0
v_mul_u32_u24 v5,52429,v4
v_lshrrev_b32 v5,22,v5
v_mul_u32_u24 v5,2240,v5
v_add_u32 v5,v5,v4
v_lshlrev_b32 v5,7,v5
v_add_u32 v5,4898816,v5
v_add_u32 v4,12288,v4
buffer_load_dword v120,v5,s[32:35],0 offen sc1
v_mul_u32_u24 v5,52429,v4
v_lshrrev_b32 v5,22,v5
v_mul_u32_u24 v5,2240,v5
v_add_u32 v5,v5,v4
v_lshlrev_b32 v5,7,v5
v_add_u32 v5,4898816,v5
v_add_u32 v4,12288,v4
buffer_load_dword v120,v5,s[32:35],0 offen sc1
s_sleep 30
.L_ns:
s_lshr_b32 s27,s50,1
s_and_b32 s28,s50,1
s_cmp_eq_u32 s49,4
s_cbranch_scc1 .L_h
s_sub_u32 s27,s49,14
s_lshr_b32 s27,s27,1
s_and_b32 s28,s49,1
s_add_u32 s28,s28,1
s_sub_u32 s48,s49,5
s_cmp_lt_u32 s49,14
s_cselect_b32 s27,s48,s27
s_cselect_b32 s28,0,s28
s_mul_i32 s50,s50,9
s_add_u32 s27,s27,s50
.L_g:
v_mov_b32 v16,0x3f803f80
v_mov_b32 v17,0x3f803f80
v_mov_b32 v18,0x3f803f80
v_mov_b32 v19,0x3f803f80
s_add_u32 s36,s14,0x0
s_addc_u32 s37,s15,0x0
s_and_b32 s37,s37,0xffff
s_mov_b32 s38,0x700000
s_mov_b32 s39,0x20000
s_add_u32 s40,s16,0x0
s_addc_u32 s41,s17,0x0
s_and_b32 s41,s41,0xffff
s_mov_b32 s42,0x700000
s_mov_b32 s43,0x20000
v_add_u32 v8,0,v1
v_mul_u32_u24 v9,5462,v8
v_lshrrev_b32 v9,16,v9
v_mul_u32_u24 v10,12,v9
v_sub_u32 v10,v8,v10
v_mul_u32_u24 v9,7168,v9
v_lshl_add_u32 v9,v10,4,v9
v_subrev_u32 v4,0,v9
v_add_u32 v8,64,v1
v_mul_u32_u24 v9,5462,v8
v_lshrrev_b32 v9,16,v9
v_mul_u32_u24 v10,12,v9
v_sub_u32 v10,v8,v10
v_mul_u32_u24 v9,7168,v9
v_lshl_add_u32 v9,v10,4,v9
v_subrev_u32 v5,1024,v9
v_add_u32 v8,128,v1
v_mul_u32_u24 v9,5462,v8
v_lshrrev_b32 v9,16,v9
v_mul_u32_u24 v10,12,v9
v_sub_u32 v10,v8,v10
v_mul_u32_u24 v9,7168,v9
v_lshl_add_u32 v9,v10,4,v9
v_subrev_u32 v6,2048,v9
s_mul_i32 s48,s26,114688
s_mul_i32 s49,s27,192
s_add_u32 s63,s48,s49
s_mul_i32 s64,s26,3072
s_mul_i32 s48,s28,11
s_lshr_b32 s48,s48,1
s_mul_i32 s49,s48,458752
s_add_u32 s63,s63,s49
s_and_b32 s49,s48,3
s_mul_i32 s49,s49,24576
s_add_u32 s49,s49,s64
s_add_u32 s48,s48,1
s_add_u32 m0,s49,0
s_nop 0
buffer_load_dwordx4 v4,s[36:39],s63 offen offset:0 lds
buffer_load_dwordx4 v5,s[36:39],s63 offen offset:1024 lds
buffer_load_dwordx4 v6,s[36:39],s63 offen offset:2048 lds
s_add_u32 m0,s49,12288
s_nop 0
buffer_load_dwordx4 v4,s[40:43],s63 offen offset:0 lds
buffer_load_dwordx4 v5,s[40:43],s63 offen offset:1024 lds
buffer_load_dwordx4 v6,s[40:43],s63 offen offset:2048 lds
s_add_u32 s63,s63,458752
s_and_b32 s49,s48,3
s_mul_i32 s49,s49,24576
s_add_u32 s49,s49,s64
s_add_u32 s48,s48,1
s_add_u32 m0,s49,0
s_nop 0
buffer_load_dwordx4 v4,s[36:39],s63 offen offset:0 lds
buffer_load_dwordx4 v5,s[36:39],s63 offen offset:1024 lds
buffer_load_dwordx4 v6,s[36:39],s63 offen offset:2048 lds
s_add_u32 m0,s49,12288
s_nop 0
buffer_load_dwordx4 v4,s[40:43],s63 offen offset:0 lds
buffer_load_dwordx4 v5,s[40:43],s63 offen offset:1024 lds
buffer_load_dwordx4 v6,s[40:43],s63 offen offset:2048 lds
s_add_u32 s63,s63,458752
s_and_b32 s49,s48,3
s_mul_i32 s49,s49,24576
s_add_u32 s49,s49,s64
s_add_u32 s48,s48,1
s_add_u32 m0,s49,0
s_nop 0
buffer_load_dwordx4 v4,s[36:39],s63 offen offset:0 lds
buffer_load_dwordx4 v5,s[36:39],s63 offen offset:1024 lds
buffer_load_dwordx4 v6,s[36:39],s63 offen offset:2048 lds
s_add_u32 m0,s49,12288
s_nop 0
buffer_load_dwordx4 v4,s[40:43],s63 offen offset:0 lds
buffer_load_dwordx4 v5,s[40:43],s63 offen offset:1024 lds
buffer_load_dwordx4 v6,s[40:43],s63 offen offset:2048 lds
s_add_u32 s63,s63,458752
v_mov_b32 v20,0
v_mov_b32 v21,0
v_mov_b32 v22,0
v_mov_b32 v23,0
v_mov_b32 v24,0
v_mov_b32 v25,0
v_mov_b32 v26,0
v_mov_b32 v27,0
v_mov_b32 v28,0
v_mov_b32 v29,0
v_mov_b32 v30,0
v_mov_b32 v31,0
v_and_b32 v12,15,v1
v_lshrrev_b32 v13,4,v1
v_lshrrev_b32 v14,2,v12
v_and_b32 v15,3,v12
v_lshl_add_u32 v13,v13,3,v14
v_mul_u32_u24 v13,192,v13
s_mul_i32 s48,s26,48
v_add_u32 v13,s48,v13
v_and_b32 v14,1,v15
v_lshrrev_b32 v15,1,v15
v_lshl_add_u32 v13,v14,3,v13
v_lshl_add_u32 v8,v15,5,v13
v_mul_u32_u24 v15,12288,v15
v_add3_u32 v9,v13,v15,16
v_add_u32 v10,49152,v8
v_add_u32 v11,49152,v9
v_add_u32 v12,98304,v8
v_add_u32 v13,98304,v9
s_cmp_eq_u32 s28,1
s_cbranch_scc1 .L_gw5
s_cmp_eq_u32 s28,2
s_cbranch_scc1 .L_gw11
.L_gw0:
s_waitcnt vmcnt(12)
s_barrier
ds_read_b64_tr_b16 a[0:1],v8 offset:0
ds_read_b64_tr_b16 a[2:3],v8 offset:768
ds_read_b64_tr_b16 a[4:5],v8 offset:12288
ds_read_b64_tr_b16 a[6:7],v8 offset:13056
ds_read_b64_tr_b16 a[8:9],v9 offset:0
ds_read_b64_tr_b16 a[10:11],v9 offset:768
ds_read_b64_tr_b16 a[12:13],v8 offset:6144
ds_read_b64_tr_b16 a[14:15],v8 offset:6912
ds_read_b64_tr_b16 a[16:17],v8 offset:18432
ds_read_b64_tr_b16 a[18:19],v8 offset:19200
ds_read_b64_tr_b16 a[20:21],v9 offset:6144
ds_read_b64_tr_b16 a[22:23],v9 offset:6912
s_add_u32 m0,s64,73728
s_nop 0
buffer_load_dwordx4 v4,s[36:39],s63 offen offset:0 lds
buffer_load_dwordx4 v5,s[36:39],s63 offen offset:1024 lds
buffer_load_dwordx4 v6,s[36:39],s63 offen offset:2048 lds
s_add_u32 m0,s64,86016
s_nop 0
buffer_load_dwordx4 v4,s[40:43],s63 offen offset:0 lds
buffer_load_dwordx4 v5,s[40:43],s63 offen offset:1024 lds
buffer_load_dwordx4 v6,s[40:43],s63 offen offset:2048 lds
s_add_u32 s63,s63,458752
.L_gy0:
s_waitcnt lgkmcnt(0)
v_mfma_f32_16x16x32_bf16 v[20:23],a[0:3],v[16:19],v[20:23]
v_mfma_f32_16x16x32_bf16 v[24:27],a[4:7],v[16:19],v[24:27]
v_mfma_f32_16x16x32_bf16 v[28:31],a[8:11],v[16:19],v[28:31]
v_mfma_f32_16x16x32_bf16 v[20:23],a[12:15],v[16:19],v[20:23]
v_mfma_f32_16x16x32_bf16 v[24:27],a[16:19],v[16:19],v[24:27]
v_mfma_f32_16x16x32_bf16 v[28:31],a[20:23],v[16:19],v[28:31]
.L_gw1:
s_waitcnt vmcnt(12)
s_barrier
ds_read_b64_tr_b16 a[24:25],v8 offset:24576
ds_read_b64_tr_b16 a[26:27],v8 offset:25344
ds_read_b64_tr_b16 a[28:29],v8 offset:36864
ds_read_b64_tr_b16 a[30:31],v8 offset:37632
ds_read_b64_tr_b16 a[32:33],v9 offset:24576
ds_read_b64_tr_b16 a[34:35],v9 offset:25344
ds_read_b64_tr_b16 a[36:37],v8 offset:30720
ds_read_b64_tr_b16 a[38:39],v8 offset:31488
ds_read_b64_tr_b16 a[40:41],v8 offset:43008
ds_read_b64_tr_b16 a[42:43],v8 offset:43776
ds_read_b64_tr_b16 a[44:45],v9 offset:30720
ds_read_b64_tr_b16 a[46:47],v9 offset:31488
s_add_u32 m0,s64,0
s_nop 0
buffer_load_dwordx4 v4,s[36:39],s63 offen offset:0 lds
buffer_load_dwordx4 v5,s[36:39],s63 offen offset:1024 lds
buffer_load_dwordx4 v6,s[36:39],s63 offen offset:2048 lds
s_add_u32 m0,s64,12288
s_nop 0
buffer_load_dwordx4 v4,s[40:43],s63 offen offset:0 lds
buffer_load_dwordx4 v5,s[40:43],s63 offen offset:1024 lds
buffer_load_dwordx4 v6,s[40:43],s63 offen offset:2048 lds
s_add_u32 s63,s63,458752
.L_gy1:
s_waitcnt lgkmcnt(0)
v_mfma_f32_16x16x32_bf16 v[20:23],a[24:27],v[16:19],v[20:23]
v_mfma_f32_16x16x32_bf16 v[24:27],a[28:31],v[16:19],v[24:27]
v_mfma_f32_16x16x32_bf16 v[28:31],a[32:35],v[16:19],v[28:31]
v_mfma_f32_16x16x32_bf16 v[20:23],a[36:39],v[16:19],v[20:23]
v_mfma_f32_16x16x32_bf16 v[24:27],a[40:43],v[16:19],v[24:27]
v_mfma_f32_16x16x32_bf16 v[28:31],a[44:47],v[16:19],v[28:31]
.L_gw2:
s_waitcnt vmcnt(12)
s_cmp_eq_u32 s28,1
s_barrier
ds_read_b64_tr_b16 a[48:49],v10 offset:0
ds_read_b64_tr_b16 a[50:51],v10 offset:768
ds_read_b64_tr_b16 a[52:53],v10 offset:12288
ds_read_b64_tr_b16 a[54:55],v10 offset:13056
ds_read_b64_tr_b16 a[56:57],v11 offset:0
ds_read_b64_tr_b16 a[58:59],v11 offset:768
ds_read_b64_tr_b16 a[60:61],v10 offset:6144
)#"
R"#(
ds_read_b64_tr_b16 a[62:63],v10 offset:6912
ds_read_b64_tr_b16 a[64:65],v10 offset:18432
ds_read_b64_tr_b16 a[66:67],v10 offset:19200
ds_read_b64_tr_b16 a[68:69],v11 offset:6144
ds_read_b64_tr_b16 a[70:71],v11 offset:6912
s_cbranch_scc1 .L_gy2
s_add_u32 m0,s64,24576
s_nop 0
buffer_load_dwordx4 v4,s[36:39],s63 offen offset:0 lds
buffer_load_dwordx4 v5,s[36:39],s63 offen offset:1024 lds
buffer_load_dwordx4 v6,s[36:39],s63 offen offset:2048 lds
s_add_u32 m0,s64,36864
s_nop 0
buffer_load_dwordx4 v4,s[40:43],s63 offen offset:0 lds
buffer_load_dwordx4 v5,s[40:43],s63 offen offset:1024 lds
buffer_load_dwordx4 v6,s[40:43],s63 offen offset:2048 lds
s_add_u32 s63,s63,458752
.L_gy2:
s_waitcnt lgkmcnt(0)
v_mfma_f32_16x16x32_bf16 v[20:23],a[48:51],v[16:19],v[20:23]
v_mfma_f32_16x16x32_bf16 v[24:27],a[52:55],v[16:19],v[24:27]
v_mfma_f32_16x16x32_bf16 v[28:31],a[56:59],v[16:19],v[28:31]
v_mfma_f32_16x16x32_bf16 v[20:23],a[60:63],v[16:19],v[20:23]
v_mfma_f32_16x16x32_bf16 v[24:27],a[64:67],v[16:19],v[24:27]
v_mfma_f32_16x16x32_bf16 v[28:31],a[68:71],v[16:19],v[28:31]
.L_gw3:
s_waitcnt vmcnt(12)
s_cmp_eq_u32 s28,1
s_cbranch_scc0 .L_gx3
s_waitcnt vmcnt(6)
.L_gx3:
s_barrier
ds_read_b64_tr_b16 a[72:73],v10 offset:24576
ds_read_b64_tr_b16 a[74:75],v10 offset:25344
ds_read_b64_tr_b16 a[76:77],v10 offset:36864
ds_read_b64_tr_b16 a[78:79],v10 offset:37632
ds_read_b64_tr_b16 a[80:81],v11 offset:24576
ds_read_b64_tr_b16 a[82:83],v11 offset:25344
ds_read_b64_tr_b16 a[84:85],v10 offset:30720
ds_read_b64_tr_b16 a[86:87],v10 offset:31488
ds_read_b64_tr_b16 a[88:89],v10 offset:43008
ds_read_b64_tr_b16 a[90:91],v10 offset:43776
ds_read_b64_tr_b16 a[92:93],v11 offset:30720
ds_read_b64_tr_b16 a[94:95],v11 offset:31488
s_cbranch_scc1 .L_gy3
s_add_u32 m0,s64,49152
s_nop 0
buffer_load_dwordx4 v4,s[36:39],s63 offen offset:0 lds
buffer_load_dwordx4 v5,s[36:39],s63 offen offset:1024 lds
buffer_load_dwordx4 v6,s[36:39],s63 offen offset:2048 lds
s_add_u32 m0,s64,61440
s_nop 0
buffer_load_dwordx4 v4,s[40:43],s63 offen offset:0 lds
buffer_load_dwordx4 v5,s[40:43],s63 offen offset:1024 lds
buffer_load_dwordx4 v6,s[40:43],s63 offen offset:2048 lds
s_add_u32 s63,s63,458752
.L_gy3:
s_waitcnt lgkmcnt(0)
v_mfma_f32_16x16x32_bf16 v[20:23],a[72:75],v[16:19],v[20:23]
v_mfma_f32_16x16x32_bf16 v[24:27],a[76:79],v[16:19],v[24:27]
v_mfma_f32_16x16x32_bf16 v[28:31],a[80:83],v[16:19],v[28:31]
v_mfma_f32_16x16x32_bf16 v[20:23],a[84:87],v[16:19],v[20:23]
v_mfma_f32_16x16x32_bf16 v[24:27],a[88:91],v[16:19],v[24:27]
v_mfma_f32_16x16x32_bf16 v[28:31],a[92:95],v[16:19],v[28:31]
.L_gw4:
s_waitcnt vmcnt(12)
s_cmp_eq_u32 s28,1
s_cbranch_scc0 .L_gx4
s_waitcnt vmcnt(0)
.L_gx4:
s_barrier
ds_read_b64_tr_b16 a[96:97],v8 offset:0
ds_read_b64_tr_b16 a[98:99],v8 offset:768
ds_read_b64_tr_b16 a[100:101],v8 offset:12288
ds_read_b64_tr_b16 a[102:103],v8 offset:13056
ds_read_b64_tr_b16 a[104:105],v9 offset:0
ds_read_b64_tr_b16 a[106:107],v9 offset:768
ds_read_b64_tr_b16 a[108:109],v8 offset:6144
ds_read_b64_tr_b16 a[110:111],v8 offset:6912
ds_read_b64_tr_b16 a[112:113],v8 offset:18432
ds_read_b64_tr_b16 a[114:115],v8 offset:19200
ds_read_b64_tr_b16 a[116:117],v9 offset:6144
ds_read_b64_tr_b16 a[118:119],v9 offset:6912
s_cbranch_scc1 .L_gy4
s_add_u32 m0,s64,73728
s_nop 0
buffer_load_dwordx4 v4,s[36:39],s63 offen offset:0 lds
buffer_load_dwordx4 v5,s[36:39],s63 offen offset:1024 lds
buffer_load_dwordx4 v6,s[36:39],s63 offen offset:2048 lds
s_add_u32 m0,s64,86016
s_nop 0
buffer_load_dwordx4 v4,s[40:43],s63 offen offset:0 lds
buffer_load_dwordx4 v5,s[40:43],s63 offen offset:1024 lds
buffer_load_dwordx4 v6,s[40:43],s63 offen offset:2048 lds
s_add_u32 s63,s63,458752
.L_gy4:
s_waitcnt lgkmcnt(0)
v_mfma_f32_16x16x32_bf16 v[20:23],a[96:99],v[16:19],v[20:23]
v_mfma_f32_16x16x32_bf16 v[24:27],a[100:103],v[16:19],v[24:27]
v_mfma_f32_16x16x32_bf16 v[28:31],a[104:107],v[16:19],v[28:31]
v_mfma_f32_16x16x32_bf16 v[20:23],a[108:111],v[16:19],v[20:23]
v_mfma_f32_16x16x32_bf16 v[24:27],a[112:115],v[16:19],v[24:27]
v_mfma_f32_16x16x32_bf16 v[28:31],a[116:119],v[16:19],v[28:31]
s_cmp_eq_u32 s28,1
s_cbranch_scc1 .L_gz
.L_gw5:
s_waitcnt vmcnt(12)
s_barrier
ds_read_b64_tr_b16 a[120:121],v8 offset:24576
ds_read_b64_tr_b16 a[122:123],v8 offset:25344
ds_read_b64_tr_b16 a[124:125],v8 offset:36864
ds_read_b64_tr_b16 a[126:127],v8 offset:37632
ds_read_b64_tr_b16 a[128:129],v9 offset:24576
ds_read_b64_tr_b16 a[130:131],v9 offset:25344
ds_read_b64_tr_b16 a[132:133],v8 offset:30720
ds_read_b64_tr_b16 a[134:135],v8 offset:31488
ds_read_b64_tr_b16 a[136:137],v8 offset:43008
ds_read_b64_tr_b16 a[138:139],v8 offset:43776
ds_read_b64_tr_b16 a[140:141],v9 offset:30720
ds_read_b64_tr_b16 a[142:143],v9 offset:31488
s_add_u32 m0,s64,0
s_nop 0
buffer_load_dwordx4 v4,s[36:39],s63 offen offset:0 lds
buffer_load_dwordx4 v5,s[36:39],s63 offen offset:1024 lds
buffer_load_dwordx4 v6,s[36:39],s63 offen offset:2048 lds
s_add_u32 m0,s64,12288
s_nop 0
buffer_load_dwordx4 v4,s[40:43],s63 offen offset:0 lds
buffer_load_dwordx4 v5,s[40:43],s63 offen offset:1024 lds
buffer_load_dwordx4 v6,s[40:43],s63 offen offset:2048 lds
s_add_u32 s63,s63,458752
.L_gy5:
s_waitcnt lgkmcnt(0)
v_mfma_f32_16x16x32_bf16 v[20:23],a[120:123],v[16:19],v[20:23]
v_mfma_f32_16x16x32_bf16 v[24:27],a[124:127],v[16:19],v[24:27]
v_mfma_f32_16x16x32_bf16 v[28:31],a[128:131],v[16:19],v[28:31]
v_mfma_f32_16x16x32_bf16 v[20:23],a[132:135],v[16:19],v[20:23]
v_mfma_f32_16x16x32_bf16 v[24:27],a[136:139],v[16:19],v[24:27]
v_mfma_f32_16x16x32_bf16 v[28:31],a[140:143],v[16:19],v[28:31]
.L_gw6:
s_waitcnt vmcnt(12)
s_barrier
ds_read_b64_tr_b16 a[144:145],v10 offset:0
ds_read_b64_tr_b16 a[146:147],v10 offset:768
ds_read_b64_tr_b16 a[148:149],v10 offset:12288
ds_read_b64_tr_b16 a[150:151],v10 offset:13056
ds_read_b64_tr_b16 a[152:153],v11 offset:0
ds_read_b64_tr_b16 a[154:155],v11 offset:768
ds_read_b64_tr_b16 a[156:157],v10 offset:6144
ds_read_b64_tr_b16 a[158:159],v10 offset:6912
ds_read_b64_tr_b16 a[160:161],v10 offset:18432
ds_read_b64_tr_b16 a[162:163],v10 offset:19200
ds_read_b64_tr_b16 a[164:165],v11 offset:6144
ds_read_b64_tr_b16 a[166:167],v11 offset:6912
s_add_u32 m0,s64,24576
s_nop 0
buffer_load_dwordx4 v4,s[36:39],s63 offen offset:0 lds
buffer_load_dwordx4 v5,s[36:39],s63 offen offset:1024 lds
buffer_load_dwordx4 v6,s[36:39],s63 offen offset:2048 lds
s_add_u32 m0,s64,36864
s_nop 0
buffer_load_dwordx4 v4,s[40:43],s63 offen offset:0 lds
buffer_load_dwordx4 v5,s[40:43],s63 offen offset:1024 lds
buffer_load_dwordx4 v6,s[40:43],s63 offen offset:2048 lds
s_add_u32 s63,s63,458752
.L_gy6:
s_waitcnt lgkmcnt(0)
v_mfma_f32_16x16x32_bf16 v[20:23],a[144:147],v[16:19],v[20:23]
v_mfma_f32_16x16x32_bf16 v[24:27],a[148:151],v[16:19],v[24:27]
v_mfma_f32_16x16x32_bf16 v[28:31],a[152:155],v[16:19],v[28:31]
v_mfma_f32_16x16x32_bf16 v[20:23],a[156:159],v[16:19],v[20:23]
v_mfma_f32_16x16x32_bf16 v[24:27],a[160:163],v[16:19],v[24:27]
v_mfma_f32_16x16x32_bf16 v[28:31],a[164:167],v[16:19],v[28:31]
.L_gw7:
s_waitcnt vmcnt(12)
s_barrier
ds_read_b64_tr_b16 a[168:169],v10 offset:24576
ds_read_b64_tr_b16 a[170:171],v10 offset:25344
ds_read_b64_tr_b16 a[172:173],v10 offset:36864
ds_read_b64_tr_b16 a[174:175],v10 offset:37632
ds_read_b64_tr_b16 a[176:177],v11 offset:24576
ds_read_b64_tr_b16 a[178:179],v11 offset:25344
ds_read_b64_tr_b16 a[180:181],v10 offset:30720
ds_read_b64_tr_b16 a[182:183],v10 offset:31488
ds_read_b64_tr_b16 a[184:185],v10 offset:43008
ds_read_b64_tr_b16 a[186:187],v10 offset:43776
ds_read_b64_tr_b16 a[188:189],v11 offset:30720
ds_read_b64_tr_b16 a[190:191],v11 offset:31488
s_add_u32 m0,s64,49152
s_nop 0
buffer_load_dwordx4 v4,s[36:39],s63 offen offset:0 lds
buffer_load_dwordx4 v5,s[36:39],s63 offen offset:1024 lds
buffer_load_dwordx4 v6,s[36:39],s63 offen offset:2048 lds
s_add_u32 m0,s64,61440
s_nop 0
buffer_load_dwordx4 v4,s[40:43],s63 offen offset:0 lds
buffer_load_dwordx4 v5,s[40:43],s63 offen offset:1024 lds
buffer_load_dwordx4 v6,s[40:43],s63 offen offset:2048 lds
s_add_u32 s63,s63,458752
.L_gy7:
s_waitcnt lgkmcnt(0)
v_mfma_f32_16x16x32_bf16 v[20:23],a[168:171],v[16:19],v[20:23]
v_mfma_f32_16x16x32_bf16 v[24:27],a[172:175],v[16:19],v[24:27]
v_mfma_f32_16x16x32_bf16 v[28:31],a[176:179],v[16:19],v[28:31]
v_mfma_f32_16x16x32_bf16 v[20:23],a[180:183],v[16:19],v[20:23]
v_mfma_f32_16x16x32_bf16 v[24:27],a[184:187],v[16:19],v[24:27]
v_mfma_f32_16x16x32_bf16 v[28:31],a[188:191],v[16:19],v[28:31]
.L_gw8:
s_waitcnt vmcnt(12)
s_cmp_eq_u32 s28,2
s_barrier
ds_read_b64_tr_b16 a[192:193],v8 offset:0
ds_read_b64_tr_b16 a[194:195],v8 offset:768
ds_read_b64_tr_b16 a[196:197],v8 offset:12288
ds_read_b64_tr_b16 a[198:199],v8 offset:13056
ds_read_b64_tr_b16 a[200:201],v9 offset:0
ds_read_b64_tr_b16 a[202:203],v9 offset:768
ds_read_b64_tr_b16 a[204:205],v8 offset:6144
ds_read_b64_tr_b16 a[206:207],v8 offset:6912
ds_read_b64_tr_b16 a[208:209],v8 offset:18432
ds_read_b64_tr_b16 a[210:211],v8 offset:19200
ds_read_b64_tr_b16 a[212:213],v9 offset:6144
ds_read_b64_tr_b16 a[214:215],v9 offset:6912
s_cbranch_scc1 .L_gy8
s_add_u32 m0,s64,73728
s_nop 0
buffer_load_dwordx4 v4,s[36:39],s63 offen offset:0 lds
buffer_load_dwordx4 v5,s[36:39],s63 offen offset:1024 lds
buffer_load_dwordx4 v6,s[36:39],s63 offen offset:2048 lds
s_add_u32 m0,s64,86016
s_nop 0
buffer_load_dwordx4 v4,s[40:43],s63 offen offset:0 lds
buffer_load_dwordx4 v5,s[40:43],s63 offen offset:1024 lds
buffer_load_dwordx4 v6,s[40:43],s63 offen offset:2048 lds
s_add_u32 s63,s63,458752
.L_gy8:
s_waitcnt lgkmcnt(0)
v_mfma_f32_16x16x32_bf16 v[20:23],a[192:195],v[16:19],v[20:23]
v_mfma_f32_16x16x32_bf16 v[24:27],a[196:199],v[16:19],v[24:27]
v_mfma_f32_16x16x32_bf16 v[28:31],a[200:203],v[16:19],v[28:31]
v_mfma_f32_16x16x32_bf16 v[20:23],a[204:207],v[16:19],v[20:23]
v_mfma_f32_16x16x32_bf16 v[24:27],a[208:211],v[16:19],v[24:27]
v_mfma_f32_16x16x32_bf16 v[28:31],a[212:215],v[16:19],v[28:31]
.L_gw9:
s_waitcnt vmcnt(12)
s_cmp_eq_u32 s28,2
s_cbranch_scc0 .L_gx9
s_waitcnt vmcnt(6)
.L_gx9:
s_barrier
ds_read_b64_tr_b16 a[216:217],v8 offset:24576
ds_read_b64_tr_b16 a[218:219],v8 offset:25344
ds_read_b64_tr_b16 a[220:221],v8 offset:36864
ds_read_b64_tr_b16 a[222:223],v8 offset:37632
ds_read_b64_tr_b16 a[224:225],v9 offset:24576
ds_read_b64_tr_b16 a[226:227],v9 offset:25344
ds_read_b64_tr_b16 a[228:229],v8 offset:30720
ds_read_b64_tr_b16 a[230:231],v8 offset:31488
ds_read_b64_tr_b16 a[232:233],v8 offset:43008
ds_read_b64_tr_b16 a[234:235],v8 offset:43776
ds_read_b64_tr_b16 a[236:237],v9 offset:30720
ds_read_b64_tr_b16 a[238:239],v9 offset:31488
s_cbranch_scc1 .L_gy9
s_add_u32 m0,s64,0
s_nop 0
buffer_load_dwordx4 v4,s[36:39],s63 offen offset:0 lds
buffer_load_dwordx4 v5,s[36:39],s63 offen offset:1024 lds
buffer_load_dwordx4 v6,s[36:39],s63 offen offset:2048 lds
s_add_u32 m0,s64,12288
s_nop 0
buffer_load_dwordx4 v4,s[40:43],s63 offen offset:0 lds
buffer_load_dwordx4 v5,s[40:43],s63 offen offset:1024 lds
buffer_load_dwordx4 v6,s[40:43],s63 offen offset:2048 lds
s_add_u32 s63,s63,458752
.L_gy9:
s_waitcnt lgkmcnt(0)
v_mfma_f32_16x16x32_bf16 v[20:23],a[216:219],v[16:19],v[20:23]
v_mfma_f32_16x16x32_bf16 v[24:27],a[220:223],v[16:19],v[24:27]
v_mfma_f32_16x16x32_bf16 v[28:31],a[224:227],v[16:19],v[28:31]
v_mfma_f32_16x16x32_bf16 v[20:23],a[228:231],v[16:19],v[20:23]
v_mfma_f32_16x16x32_bf16 v[24:27],a[232:235],v[16:19],v[24:27]
v_mfma_f32_16x16x32_bf16 v[28:31],a[236:239],v[16:19],v[28:31]
.L_gw10:
s_waitcnt vmcnt(12)
s_cmp_eq_u32 s28,2
s_cbranch_scc0 .L_gx10
s_waitcnt vmcnt(0)
.L_gx10:
s_barrier
ds_read_b64_tr_b16 a[240:241],v10 offset:0
ds_read_b64_tr_b16 a[242:243],v10 offset:768
ds_read_b64_tr_b16 a[244:245],v10 offset:12288
ds_read_b64_tr_b16 a[246:247],v10 offset:13056
ds_read_b64_tr_b16 a[248:249],v11 offset:0
ds_read_b64_tr_b16 a[250:251],v11 offset:768
ds_read_b64_tr_b16 a[252:253],v10 offset:6144
ds_read_b64_tr_b16 a[254:255],v10 offset:6912
ds_read_b64_tr_b16 v[128:129],v10 offset:18432
ds_read_b64_tr_b16 v[130:131],v10 offset:19200
ds_read_b64_tr_b16 v[132:133],v11 offset:6144
ds_read_b64_tr_b16 v[134:135],v11 offset:6912
s_cbranch_scc1 .L_gy10
s_add_u32 m0,s64,24576
s_nop 0
buffer_load_dwordx4 v4,s[36:39],s63 offen offset:0 lds
buffer_load_dwordx4 v5,s[36:39],s63 offen offset:1024 lds
buffer_load_dwordx4 v6,s[36:39],s63 offen offset:2048 lds
s_add_u32 m0,s64,36864
s_nop 0
buffer_load_dwordx4 v4,s[40:43],s63 offen offset:0 lds
buffer_load_dwordx4 v5,s[40:43],s63 offen offset:1024 lds
buffer_load_dwordx4 v6,s[40:43],s63 offen offset:2048 lds
s_add_u32 s63,s63,458752
.L_gy10:
s_waitcnt lgkmcnt(0)
v_mfma_f32_16x16x32_bf16 v[20:23],a[240:243],v[16:19],v[20:23]
v_mfma_f32_16x16x32_bf16 v[24:27],a[244:247],v[16:19],v[24:27]
v_mfma_f32_16x16x32_bf16 v[28:31],a[248:251],v[16:19],v[28:31]
v_mfma_f32_16x16x32_bf16 v[20:23],a[252:255],v[16:19],v[20:23]
v_mfma_f32_16x16x32_bf16 v[24:27],v[128:131],v[16:19],v[24:27]
v_mfma_f32_16x16x32_bf16 v[28:31],v[132:135],v[16:19],v[28:31]
s_cmp_eq_u32 s28,2
s_cbranch_scc1 .L_gz
.L_gw11:
s_waitcnt vmcnt(12)
s_barrier
ds_read_b64_tr_b16 v[136:137],v10 offset:24576
ds_read_b64_tr_b16 v[138:139],v10 offset:25344
ds_read_b64_tr_b16 v[140:141],v10 offset:36864
ds_read_b64_tr_b16 v[142:143],v10 offset:37632
ds_read_b64_tr_b16 v[144:145],v11 offset:24576
ds_read_b64_tr_b16 v[146:147],v11 offset:25344
ds_read_b64_tr_b16 v[148:149],v10 offset:30720
ds_read_b64_tr_b16 v[150:151],v10 offset:31488
ds_read_b64_tr_b16 v[152:153],v10 offset:43008
ds_read_b64_tr_b16 v[154:155],v10 offset:43776
ds_read_b64_tr_b16 v[156:157],v11 offset:30720
ds_read_b64_tr_b16 v[158:159],v11 offset:31488
s_add_u32 m0,s64,49152
s_nop 0
buffer_load_dwordx4 v4,s[36:39],s63 offen offset:0 lds
buffer_load_dwordx4 v5,s[36:39],s63 offen offset:1024 lds
buffer_load_dwordx4 v6,s[36:39],s63 offen offset:2048 lds
s_add_u32 m0,s64,61440
s_nop 0
buffer_load_dwordx4 v4,s[40:43],s63 offen offset:0 lds
buffer_load_dwordx4 v5,s[40:43],s63 offen offset:1024 lds
buffer_load_dwordx4 v6,s[40:43],s63 offen offset:2048 lds
s_add_u32 s63,s63,458752
.L_gy11:
s_waitcnt lgkmcnt(0)
v_mfma_f32_16x16x32_bf16 v[20:23],v[136:139],v[16:19],v[20:23]
v_mfma_f32_16x16x32_bf16 v[24:27],v[140:143],v[16:19],v[24:27]
v_mfma_f32_16x16x32_bf16 v[28:31],v[144:147],v[16:19],v[28:31]
v_mfma_f32_16x16x32_bf16 v[20:23],v[148:151],v[16:19],v[20:23]
v_mfma_f32_16x16x32_bf16 v[24:27],v[152:155],v[16:19],v[24:27]
v_mfma_f32_16x16x32_bf16 v[28:31],v[156:159],v[16:19],v[28:31]
.L_gw12:
s_waitcnt vmcnt(12)
s_barrier
ds_read_b64_tr_b16 v[160:161],v8 offset:0
ds_read_b64_tr_b16 v[162:163],v8 offset:768
ds_read_b64_tr_b16 v[164:165],v8 offset:12288
ds_read_b64_tr_b16 v[166:167],v8 offset:13056
ds_read_b64_tr_b16 v[168:169],v9 offset:0
ds_read_b64_tr_b16 v[170:171],v9 offset:768
ds_read_b64_tr_b16 v[172:173],v8 offset:6144
ds_read_b64_tr_b16 v[174:175],v8 offset:6912
ds_read_b64_tr_b16 v[176:177],v8 offset:18432
ds_read_b64_tr_b16 v[178:179],v8 offset:19200
ds_read_b64_tr_b16 v[180:181],v9 offset:6144
ds_read_b64_tr_b16 v[182:183],v9 offset:6912
s_add_u32 m0,s64,73728
s_nop 0
buffer_load_dwordx4 v4,s[36:39],s63 offen offset:0 lds
buffer_load_dwordx4 v5,s[36:39],s63 offen offset:1024 lds
buffer_load_dwordx4 v6,s[36:39],s63 offen offset:2048 lds
s_add_u32 m0,s64,86016
s_nop 0
buffer_load_dwordx4 v4,s[40:43],s63 offen offset:0 lds
buffer_load_dwordx4 v5,s[40:43],s63 offen offset:1024 lds
buffer_load_dwordx4 v6,s[40:43],s63 offen offset:2048 lds
s_sub_u32 s63,s63,6881280
.L_gy12:
s_waitcnt lgkmcnt(0)
v_mfma_f32_16x16x32_bf16 v[20:23],v[160:163],v[16:19],v[20:23]
v_mfma_f32_16x16x32_bf16 v[24:27],v[164:167],v[16:19],v[24:27]
v_mfma_f32_16x16x32_bf16 v[28:31],v[168:171],v[16:19],v[28:31]
v_mfma_f32_16x16x32_bf16 v[20:23],v[172:175],v[16:19],v[20:23]
v_mfma_f32_16x16x32_bf16 v[24:27],v[176:179],v[16:19],v[24:27]
v_mfma_f32_16x16x32_bf16 v[28:31],v[180:183],v[16:19],v[28:31]
.L_gw13:
s_waitcnt vmcnt(12)
s_cmp_eq_u32 s28,0
s_barrier
ds_read_b64_tr_b16 v[184:185],v8 offset:24576
ds_read_b64_tr_b16 v[186:187],v8 offset:25344
ds_read_b64_tr_b16 v[188:189],v8 offset:36864
ds_read_b64_tr_b16 v[190:191],v8 offset:37632
ds_read_b64_tr_b16 v[192:193],v9 offset:24576
ds_read_b64_tr_b16 v[194:195],v9 offset:25344
ds_read_b64_tr_b16 v[196:197],v8 offset:30720
ds_read_b64_tr_b16 v[198:199],v8 offset:31488
ds_read_b64_tr_b16 v[200:201],v8 offset:43008
ds_read_b64_tr_b16 v[202:203],v8 offset:43776
ds_read_b64_tr_b16 v[204:205],v9 offset:30720
ds_read_b64_tr_b16 v[206:207],v9 offset:31488
s_cbranch_scc1 .L_gy13
s_add_u32 m0,s64,0
s_nop 0
buffer_load_dwordx4 v4,s[36:39],s63 offen offset:0 lds
buffer_load_dwordx4 v5,s[36:39],s63 offen offset:1024 lds
buffer_load_dwordx4 v6,s[36:39],s63 offen offset:2048 lds
s_add_u32 m0,s64,12288
s_nop 0
buffer_load_dwordx4 v4,s[40:43],s63 offen offset:0 lds
buffer_load_dwordx4 v5,s[40:43],s63 offen offset:1024 lds
buffer_load_dwordx4 v6,s[40:43],s63 offen offset:2048 lds
s_add_u32 s63,s63,458752
.L_gy13:
s_waitcnt lgkmcnt(0)
v_mfma_f32_16x16x32_bf16 v[20:23],v[184:187],v[16:19],v[20:23]
v_mfma_f32_16x16x32_bf16 v[24:27],v[188:191],v[16:19],v[24:27]
v_mfma_f32_16x16x32_bf16 v[28:31],v[192:195],v[16:19],v[28:31]
v_mfma_f32_16x16x32_bf16 v[20:23],v[196:199],v[16:19],v[20:23]
v_mfma_f32_16x16x32_bf16 v[24:27],v[200:203],v[16:19],v[24:27]
v_mfma_f32_16x16x32_bf16 v[28:31],v[204:207],v[16:19],v[28:31]
.L_gw14:
s_waitcnt vmcnt(12)
s_cmp_eq_u32 s28,0
s_cbranch_scc0 .L_gx14
s_waitcnt vmcnt(6)
.L_gx14:
s_barrier
ds_read_b64_tr_b16 v[208:209],v10 offset:0
ds_read_b64_tr_b16 v[210:211],v10 offset:768
ds_read_b64_tr_b16 v[212:213],v10 offset:12288
ds_read_b64_tr_b16 v[214:215],v10 offset:13056
ds_read_b64_tr_b16 v[216:217],v11 offset:0
ds_read_b64_tr_b16 v[218:219],v11 offset:768
ds_read_b64_tr_b16 v[220:221],v10 offset:6144
ds_read_b64_tr_b16 v[222:223],v10 offset:6912
ds_read_b64_tr_b16 v[224:225],v10 offset:18432
ds_read_b64_tr_b16 v[226:227],v10 offset:19200
ds_read_b64_tr_b16 v[228:229],v11 offset:6144
ds_read_b64_tr_b16 v[230:231],v11 offset:6912
s_cbranch_scc1 .L_gy14
s_add_u32 m0,s64,24576
s_nop 0
buffer_load_dwordx4 v4,s[36:39],s63 offen offset:0 lds
buffer_load_dwordx4 v5,s[36:39],s63 offen offset:1024 lds
buffer_load_dwordx4 v6,s[36:39],s63 offen offset:2048 lds
s_add_u32 m0,s64,36864
s_nop 0
buffer_load_dwordx4 v4,s[40:43],s63 offen offset:0 lds
buffer_load_dwordx4 v5,s[40:43],s63 offen offset:1024 lds
buffer_load_dwordx4 v6,s[40:43],s63 offen offset:2048 lds
s_add_u32 s63,s63,458752
.L_gy14:
s_waitcnt lgkmcnt(0)
v_mfma_f32_16x16x32_bf16 v[20:23],v[208:211],v[16:19],v[20:23]
v_mfma_f32_16x16x32_bf16 v[24:27],v[212:215],v[16:19],v[24:27]
v_mfma_f32_16x16x32_bf16 v[28:31],v[216:219],v[16:19],v[28:31]
v_mfma_f32_16x16x32_bf16 v[20:23],v[220:223],v[16:19],v[20:23]
v_mfma_f32_16x16x32_bf16 v[24:27],v[224:227],v[16:19],v[24:27]
v_mfma_f32_16x16x32_bf16 v[28:31],v[228:231],v[16:19],v[28:31]
.L_gw15:
s_waitcnt vmcnt(12)
s_cmp_eq_u32 s28,0
s_cbranch_scc0 .L_gx15
s_waitcnt vmcnt(0)
.L_gx15:
s_barrier
ds_read_b64_tr_b16 v[232:233],v10 offset:24576
ds_read_b64_tr_b16 v[234:235],v10 offset:25344
ds_read_b64_tr_b16 v[236:237],v10 offset:36864
ds_read_b64_tr_b16 v[238:239],v10 offset:37632
ds_read_b64_tr_b16 v[240:241],v11 offset:24576
ds_read_b64_tr_b16 v[242:243],v11 offset:25344
ds_read_b64_tr_b16 v[244:245],v10 offset:30720
ds_read_b64_tr_b16 v[246:247],v10 offset:31488
ds_read_b64_tr_b16 v[248:249],v10 offset:43008
ds_read_b64_tr_b16 v[250:251],v10 offset:43776
ds_read_b64_tr_b16 v[252:253],v11 offset:30720
ds_read_b64_tr_b16 v[254:255],v11 offset:31488
s_cbranch_scc1 .L_gy15
s_add_u32 m0,s64,49152
s_nop 0
buffer_load_dwordx4 v4,s[36:39],s63 offen offset:0 lds
buffer_load_dwordx4 v5,s[36:39],s63 offen offset:1024 lds
buffer_load_dwordx4 v6,s[36:39],s63 offen offset:2048 lds
s_add_u32 m0,s64,61440
s_nop 0
buffer_load_dwordx4 v4,s[40:43],s63 offen offset:0 lds
buffer_load_dwordx4 v5,s[40:43],s63 offen offset:1024 lds
buffer_load_dwordx4 v6,s[40:43],s63 offen offset:2048 lds
s_add_u32 s63,s63,458752
.L_gy15:
s_waitcnt lgkmcnt(0)
v_mfma_f32_16x16x32_bf16 v[20:23],v[232:235],v[16:19],v[20:23]
)#"
R"#(
v_mfma_f32_16x16x32_bf16 v[24:27],v[236:239],v[16:19],v[24:27]
v_mfma_f32_16x16x32_bf16 v[28:31],v[240:243],v[16:19],v[28:31]
v_mfma_f32_16x16x32_bf16 v[20:23],v[244:247],v[16:19],v[20:23]
v_mfma_f32_16x16x32_bf16 v[24:27],v[248:251],v[16:19],v[24:27]
v_mfma_f32_16x16x32_bf16 v[28:31],v[252:255],v[16:19],v[28:31]
s_cmp_eq_u32 s28,0
s_cbranch_scc0 .L_gw0
.L_gz:
s_barrier
s_lshl2_add_u32 s49,s27,128
s_mul_i32 s68,s27,3072
s_mul_i32 s48,s26,768
s_cmp_eq_u32 s28,0
s_addc_u32 s62,84,0
v_cmp_gt_u32 vcc,48,v1
v_mov_b32 v15,0x7fff0000
s_nop 0
v_cndmask_b32 v15,v15,v2,vcc
s_add_u32 s68,s68,s48
s_add_u32 s68,s68,4718592
s_waitcnt lgkmcnt(0)
s_add_u32 s54,s52,0x7f4a7c15
s_addc_u32 s55,s53,0x9e3779b9
s_lshl_b32 s67,s49,5
s_add_u32 s67,s67,4898816
s_sub_u32 s51,s67,s68
s_add_u32 s92,s32,s51
s_addc_u32 s93,s33,0x10000
s_sub_u32 s92,s92,890880
s_subb_u32 s93,s93,0
s_cmp_eq_u32 s26,3
s_cselect_b32 s94,1,0
s_mov_b32 s95,0x20000
v_mov_b32 v16,s54
v_mov_b32 v17,s55
v_cmp_gt_u32 vcc,12,v1
v_mul_u32_u24 v3,296960,v1
v_mov_b32 v12,0x7fff0000
v_cndmask_b32 v3,v12,v3,vcc
s_add_u32 s51,s67,76021760
buffer_store_dwordx2 v[16:17],v3,s[32:35],s51 offen sc1
s_sub_u32 s67,s68,296960
s_mul_i32 s48,s28,296960
s_add_u32 s68,s68,s48
s_sub_u32 s68,s68,1781760
s_add_u32 s63,s48,4681728
s_add_u32 s66,s48,4898816
s_mul_i32 s64,s26,9216
s_add_u32 s65,s64,73728
s_xor_b32 s65,s65,s64
s_add_u32 s63,s63,s64
v_mov_b32 v4,v2
v_add_u32 v5,36864,v2
v_add_u32 v6,4096,v2
v_add_u32 v7,8192,v2
v_bfe_u32 v3,v1,1,3
v_mul_u32_u24 v8,1152,v3
v_lshrrev_b32 v3,1,v3
v_add_u32 v3,s26,v3
v_lshrrev_b32 v9,4,v1
v_lshl_add_u32 v3,v3,2,v9
v_and_b32 v3,15,v3
v_lshl_add_u32 v8,v3,2,v8
v_and_b32 v3,1,v1
v_lshl_add_u32 v8,v3,6,v8
v_add_u32 v8,1024,v8
s_lshl_b32 s48,s26,2
v_add_u32 v10,s48,v9
v_and_b32 v11,15,v1
v_add_u32 v3,64,v1
v_cmp_eq_u32 vcc,0,v11
v_add_u32 v9,73728,v8
v_xor_b32 v9,v9,v8
v_cndmask_b32 v10,v3,v10,vcc
v_lshlrev_b32 v10,2,v10
v_add_u32 v10,147456,v10
v_lshlrev_b32 v11,2,v11
v_add_u32 v11,147456,v11
v_lshlrev_b32 v14,2,v1
v_add_u32 v14,96,v14
v_lshrrev_b32 v14,2,v14
v_lshlrev_b32 v14,7,v14
v_cmp_gt_u32 vcc,8,v1
v_mov_b32 v3,0x7fff0000
s_nop 0
v_cndmask_b32 v14,v3,v14,vcc
v_cndmask_b32 v16,0,v16,vcc
v_cndmask_b32 v17,0,v17,vcc
s_mov_b32 s48,20000
s_branch .L_rfga
.L_rlga:
s_sleep 8
.L_rfga:
buffer_load_dwordx2 v[12:13],v14,s[32:35],s66 offen sc1
s_sub_u32 s48,s48,1
s_cbranch_scc1 .L_end
s_waitcnt vmcnt(0)
v_cmp_ne_u64 vcc,v[16:17],v[12:13]
s_cbranch_vccnz .L_rlga
s_add_u32 s66,s66,890880
s_add_u32 m0,s64,0
s_nop 0
buffer_load_dwordx4 v2,s[32:35],s63 offen offset:0 sc1 lds
buffer_load_dwordx4 v2,s[32:35],s63 offen offset:1024 sc1 lds
buffer_load_dwordx4 v2,s[32:35],s63 offen offset:2048 sc1 lds
buffer_load_dwordx4 v2,s[32:35],s63 offen offset:3072 sc1 lds
s_add_u32 m0,s64,4096
s_nop 0
buffer_load_dwordx4 v6,s[32:35],s63 offen offset:0 sc1 lds
buffer_load_dwordx4 v6,s[32:35],s63 offen offset:1024 sc1 lds
buffer_load_dwordx4 v6,s[32:35],s63 offen offset:2048 sc1 lds
buffer_load_dwordx4 v6,s[32:35],s63 offen offset:3072 sc1 lds
s_add_u32 m0,s64,8192
s_nop 0
buffer_load_dwordx4 v7,s[32:35],s63 offen offset:0 sc1 lds
s_add_u32 s63,s63,890880
s_mov_b32 s48,20000
s_branch .L_rfgb
.L_rlgb:
s_sleep 8
.L_rfgb:
buffer_load_dwordx2 v[12:13],v14,s[32:35],s66 offen sc1
s_sub_u32 s48,s48,1
s_cbranch_scc1 .L_end
s_waitcnt vmcnt(0)
v_cmp_ne_u64 vcc,v[16:17],v[12:13]
s_cbranch_vccnz .L_rlgb
s_add_u32 s66,s66,890880
s_barrier
s_add_u32 m0,s64,36864
s_nop 0
buffer_load_dwordx4 v2,s[32:35],s63 offen offset:0 sc1 lds
buffer_load_dwordx4 v2,s[32:35],s63 offen offset:1024 sc1 lds
buffer_load_dwordx4 v2,s[32:35],s63 offen offset:2048 sc1 lds
buffer_load_dwordx4 v2,s[32:35],s63 offen offset:3072 sc1 lds
s_add_u32 m0,s64,40960
s_nop 0
buffer_load_dwordx4 v6,s[32:35],s63 offen offset:0 sc1 lds
buffer_load_dwordx4 v6,s[32:35],s63 offen offset:1024 sc1 lds
ds_read2st64_b32 v[56:57],v8 offset0:0 offset1:36
ds_read2st64_b32 v[58:59],v8 offset0:72 offset1:108
s_waitcnt lgkmcnt(0)
v_add_f32 v56,v56,v57
v_add_f32 v58,v58,v59
v_add_f32 v56,v56,v58
s_nop 1
v_add_f32_dpp v56,v56,v56 row_ror:8 row_mask:0xf bank_mask:0xf
s_nop 1
v_add_f32_dpp v56,v56,v56 row_ror:4 row_mask:0xf bank_mask:0xf
ds_read_b128 v[76:79],v4 offset:0
ds_read_b128 v[80:83],v4 offset:1152
ds_read_b128 v[84:87],v4 offset:2304
ds_read_b128 v[88:91],v4 offset:3456
ds_read_b128 v[92:95],v4 offset:4608
ds_read_b128 v[96:99],v4 offset:5760
ds_read_b128 v[100:103],v4 offset:6912
ds_read_b128 v[104:107],v4 offset:8064
s_waitcnt lgkmcnt(0)
s_cmp_eq_u32 s26,2
s_cselect_b32 s48,890880,0
s_add_u32 s66,s66,s48
.L_ggo:
s_cmp_eq_u32 s26,1
s_cbranch_scc1 .L_gv1
s_cmp_eq_u32 s26,2
s_cbranch_scc1 .L_gv2
s_cmp_eq_u32 s26,3
s_cbranch_scc1 .L_gv3
.p2align 6
.L_gv0:
.L_gl_0:
s_waitcnt lgkmcnt(4)
v_mfma_f32_16x16x32_bf16 v[32:35],a[0:3],v[76:79],0
ds_read_b128 v[108:111],v4 offset:9216
s_add_u32 s68,s68,890880
v_mfma_f32_16x16x32_bf16 v[32:35],a[12:15],v[80:83],v[32:35]
buffer_load_dwordx4 v6,s[32:35],s63 offen offset:2048 sc1 lds
v_fma_f32 v52,-v60,v28,v52
v_mfma_f32_16x16x32_bf16 v[32:35],a[24:27],v[84:87],v[32:35]
v_fma_f32 v53,-v60,v29,v53
v_fma_f32 v54,-v60,v30,v54
v_mfma_f32_16x16x32_bf16 v[32:35],a[36:39],v[88:91],v[32:35]
ds_read_b128 v[112:115],v4 offset:10368
v_fma_f32 v55,-v60,v31,v55
v_mfma_f32_16x16x32_bf16 v[36:39],a[40:43],v[88:91],0
v_permlane32_swap_b32 v52,v54
v_mfma_f32_16x16x32_bf16 v[36:39],a[28:31],v[84:87],v[36:39]
buffer_load_dwordx4 v6,s[32:35],s63 offen offset:3072 sc1 lds
v_fma_f32 v48,-v60,v24,v48
v_mfma_f32_16x16x32_bf16 v[36:39],a[16:19],v[80:83],v[36:39]
ds_read_b128 v[116:119],v4 offset:11520
v_fma_f32 v49,-v60,v25,v49
v_mfma_f32_16x16x32_bf16 v[36:39],a[4:7],v[76:79],v[36:39]
v_permlane32_swap_b32 v53,v55
v_mfma_f32_16x16x32_bf16 v[40:43],a[8:11],v[76:79],0
v_fma_f32 v50,-v60,v26,v50
v_fma_f32 v51,-v60,v27,v51
v_mfma_f32_16x16x32_bf16 v[40:43],a[20:23],v[80:83],v[40:43]
ds_read_b128 v[120:123],v4 offset:12672
v_fma_f32 v44,-v60,v20,v44
v_mfma_f32_16x16x32_bf16 v[40:43],a[32:35],v[84:87],v[40:43]
v_fma_f32 v45,-v60,v21,v45
v_fma_f32 v46,-v60,v22,v46
v_mfma_f32_16x16x32_bf16 v[40:43],a[44:47],v[88:91],v[40:43]
s_waitcnt lgkmcnt(4)
s_waitcnt vmcnt(8)
s_barrier
v_mfma_f32_16x16x32_bf16 v[40:43],a[56:59],v[92:95],v[40:43]
ds_read_b128 v[124:127],v4 offset:13824
s_add_u32 m0,s64,45056
v_mfma_f32_16x16x32_bf16 v[40:43],a[68:71],v[96:99],v[40:43]
buffer_load_dwordx4 v7,s[32:35],s63 offen offset:0 sc1 lds
s_add_u32 s63,s63,890880
v_mfma_f32_16x16x32_bf16 v[40:43],a[80:83],v[100:103],v[40:43]
s_xor_b32 s64,s64,s65
v_fma_f32 v47,-v60,v23,v47
v_mfma_f32_16x16x32_bf16 v[40:43],a[92:95],v[104:107],v[40:43]
ds_read_b128 v[76:79],v4 offset:14976
v_mul_f32 v64,v44,v61
v_mfma_f32_16x16x32_bf16 v[36:39],a[88:91],v[104:107],v[36:39]
v_exp_f32 v64,v64
v_mfma_f32_16x16x32_bf16 v[36:39],a[76:79],v[100:103],v[36:39]
v_mul_f32 v65,v45,v61
v_mul_f32 v66,v46,v61
v_mfma_f32_16x16x32_bf16 v[36:39],a[64:67],v[96:99],v[36:39]
ds_read_b128 v[80:83],v4 offset:16128
v_mul_f32 v67,v47,v61
v_mfma_f32_16x16x32_bf16 v[36:39],a[52:55],v[92:95],v[36:39]
v_exp_f32 v65,v65
v_mfma_f32_16x16x32_bf16 v[32:35],a[48:51],v[92:95],v[32:35]
v_exp_f32 v66,v66
v_mfma_f32_16x16x32_bf16 v[32:35],a[60:63],v[96:99],v[32:35]
ds_read_b128 v[84:87],v4 offset:17280
v_mul_f32 v68,v52,v61
v_mfma_f32_16x16x32_bf16 v[32:35],a[72:75],v[100:103],v[32:35]
v_exp_f32 v67,v67
v_mfma_f32_16x16x32_bf16 v[32:35],a[84:87],v[104:107],v[32:35]
v_mul_f32 v69,v53,v61
s_waitcnt lgkmcnt(4)
v_mfma_f32_16x16x32_bf16 v[32:35],a[96:99],v[108:111],v[32:35]
ds_read_b128 v[88:91],v4 offset:18432
v_mul_f32 v44,v44,v48
v_mfma_f32_16x16x32_bf16 v[32:35],a[108:111],v[112:115],v[32:35]
v_exp_f32 v68,v68
v_mfma_f32_16x16x32_bf16 v[32:35],a[120:123],v[116:119],v[32:35]
v_exp_f32 v69,v69
v_mfma_f32_16x16x32_bf16 v[32:35],a[132:135],v[120:123],v[32:35]
ds_read_b128 v[92:95],v4 offset:19584
v_mul_f32 v45,v45,v49
v_mfma_f32_16x16x32_bf16 v[36:39],a[136:139],v[120:123],v[36:39]
v_mul_f32 v46,v46,v50
v_mul_f32 v47,v47,v51
v_mfma_f32_16x16x32_bf16 v[36:39],a[124:127],v[116:119],v[36:39]
v_mul_f32 v52,v52,v54
v_mul_f32 v53,v53,v55
v_mfma_f32_16x16x32_bf16 v[36:39],a[112:115],v[112:115],v[36:39]
ds_read_b128 v[96:99],v4 offset:20736
v_fma_f32 v64,v64,v62,v62
v_mfma_f32_16x16x32_bf16 v[36:39],a[100:103],v[108:111],v[36:39]
v_rcp_f32 v64,v64
v_mfma_f32_16x16x32_bf16 v[40:43],a[104:107],v[108:111],v[40:43]
v_fma_f32 v65,v65,v62,v62
v_fma_f32 v66,v66,v62,v62
v_mfma_f32_16x16x32_bf16 v[40:43],a[116:119],v[112:115],v[40:43]
ds_read_b128 v[100:103],v4 offset:21888
v_fma_f32 v67,v67,v62,v62
v_mfma_f32_16x16x32_bf16 v[40:43],a[128:131],v[116:119],v[40:43]
v_rcp_f32 v65,v65
v_mfma_f32_16x16x32_bf16 v[40:43],a[140:143],v[120:123],v[40:43]
v_fma_f32 v68,v68,v62,v62
s_waitcnt lgkmcnt(4)
v_mfma_f32_16x16x32_bf16 v[40:43],a[152:155],v[124:127],v[40:43]
ds_read_b128 v[104:107],v4 offset:23040
v_fma_f32 v69,v69,v62,v62
v_mfma_f32_16x16x32_bf16 v[40:43],a[164:167],v[76:79],v[40:43]
v_rcp_f32 v66,v66
v_mfma_f32_16x16x32_bf16 v[40:43],a[176:179],v[80:83],v[40:43]
v_rcp_f32 v67,v67
v_mfma_f32_16x16x32_bf16 v[40:43],a[188:191],v[84:87],v[40:43]
ds_read_b128 v[108:111],v4 offset:24192
v_mul_f32 v44,v44,v64
v_mfma_f32_16x16x32_bf16 v[36:39],a[184:187],v[84:87],v[36:39]
v_rcp_f32 v68,v68
v_mfma_f32_16x16x32_bf16 v[36:39],a[172:175],v[80:83],v[36:39]
v_rcp_f32 v69,v69
v_mfma_f32_16x16x32_bf16 v[36:39],a[160:163],v[76:79],v[36:39]
ds_read_b128 v[112:115],v4 offset:25344
v_mul_f32 v45,v45,v65
v_mfma_f32_16x16x32_bf16 v[36:39],a[148:151],v[124:127],v[36:39]
v_mul_f32 v46,v46,v66
v_mul_f32 v47,v47,v67
v_mfma_f32_16x16x32_bf16 v[32:35],a[144:147],v[124:127],v[32:35]
v_mul_f32 v52,v52,v68
v_mul_f32 v53,v53,v69
v_mfma_f32_16x16x32_bf16 v[32:35],a[156:159],v[76:79],v[32:35]
ds_read_b128 v[116:119],v4 offset:26496
v_cvt_pk_bf16_f32 v74,v52,v53
v_mfma_f32_16x16x32_bf16 v[32:35],a[168:171],v[80:83],v[32:35]
v_cvt_pk_bf16_f32 v72,v44,v45
v_cvt_pk_bf16_f32 v73,v46,v47
v_mfma_f32_16x16x32_bf16 v[32:35],a[180:183],v[84:87],v[32:35]
v_add_f32_dpp v56,v56,v56 row_ror:2 row_mask:0xf bank_mask:0xf
s_waitcnt lgkmcnt(4)
v_mfma_f32_16x16x32_bf16 v[32:35],a[192:195],v[88:91],v[32:35]
ds_read_b128 v[120:123],v4 offset:27648
v_mul_f32 v56,0x3a800000,v56
v_mfma_f32_16x16x32_bf16 v[32:35],a[204:207],v[92:95],v[32:35]
v_permlane32_swap_b32 v74,v75
v_mfma_f32_16x16x32_bf16 v[32:35],a[216:219],v[96:99],v[32:35]
v_mul_f32 v57,v56,v56
v_mfma_f32_16x16x32_bf16 v[32:35],a[228:231],v[100:103],v[32:35]
ds_read_b128 v[124:127],v4 offset:28800
v_sub_f32_dpp v57,v56,v57 quad_perm:[1,1,3,3] row_mask:0xf bank_mask:0xf
v_mfma_f32_16x16x32_bf16 v[36:39],a[232:235],v[100:103],v[36:39]
v_permlane16_swap_b32 v72,v74
v_mfma_f32_16x16x32_bf16 v[36:39],a[220:223],v[96:99],v[36:39]
v_permlane16_swap_b32 v73,v75
v_mfma_f32_16x16x32_bf16 v[36:39],a[208:211],v[92:95],v[36:39]
buffer_store_dwordx4 v[72:75],v15,s[32:35],s68 offen sc1
ds_read_b128 v[76:79],v4 offset:29952
v_mfma_f32_16x16x32_bf16 v[36:39],a[196:199],v[88:91],v[36:39]
v_max_f32 v57,0,v57
v_mfma_f32_16x16x32_bf16 v[40:43],a[200:203],v[88:91],v[40:43]
v_cvt_pk_bf16_f32 v57,0,v57
v_mfma_f32_16x16x32_bf16 v[40:43],a[212:215],v[92:95],v[40:43]
ds_read_b128 v[80:83],v4 offset:31104
v_add_f32 v57,0x3727c5ac,v57
v_mfma_f32_16x16x32_bf16 v[40:43],a[224:227],v[96:99],v[40:43]
v_cvt_pk_bf16_f32 v57,0,v57
v_mfma_f32_16x16x32_bf16 v[40:43],a[236:239],v[100:103],v[40:43]
s_waitcnt lgkmcnt(4)
v_mfma_f32_16x16x32_bf16 v[40:43],a[248:251],v[104:107],v[40:43]
ds_read_b128 v[84:87],v4 offset:32256
v_mfma_f32_16x16x32_bf16 v[40:43],v[132:135],v[108:111],v[40:43]
v_sqrt_f32 v57,v57
v_mfma_f32_16x16x32_bf16 v[40:43],v[144:147],v[112:115],v[40:43]
v_cvt_pk_bf16_f32 v57,v56,v57
v_mfma_f32_16x16x32_bf16 v[40:43],v[156:159],v[116:119],v[40:43]
ds_read_b128 v[88:91],v4 offset:33408
ds_write_b32 v10,v57 offset:0
v_mfma_f32_16x16x32_bf16 v[36:39],v[152:155],v[116:119],v[36:39]
v_mfma_f32_16x16x32_bf16 v[36:39],v[140:143],v[112:115],v[36:39]
v_mfma_f32_16x16x32_bf16 v[36:39],v[128:131],v[108:111],v[36:39]
ds_read_b128 v[92:95],v4 offset:34560
v_mfma_f32_16x16x32_bf16 v[36:39],a[244:247],v[104:107],v[36:39]
v_mfma_f32_16x16x32_bf16 v[32:35],a[240:243],v[104:107],v[32:35]
v_mfma_f32_16x16x32_bf16 v[32:35],a[252:255],v[108:111],v[32:35]
ds_read_b128 v[96:99],v4 offset:35712
v_mfma_f32_16x16x32_bf16 v[32:35],v[136:139],v[112:115],v[32:35]
v_xor_b32 v4,73728,v4
v_mfma_f32_16x16x32_bf16 v[32:35],v[148:151],v[116:119],v[32:35]
s_waitcnt lgkmcnt(5)
s_waitcnt vmcnt(1)
s_waitcnt lgkmcnt(2)
s_barrier
v_mfma_f32_16x16x32_bf16 v[32:35],v[160:163],v[120:123],v[32:35]
ds_read_b128 v[100:103],v5 offset:0
s_add_u32 m0,s64,0
v_mfma_f32_16x16x32_bf16 v[32:35],v[172:175],v[124:127],v[32:35]
buffer_load_dwordx4 v2,s[32:35],s63 offen offset:0 sc1 lds
v_mfma_f32_16x16x32_bf16 v[32:35],v[184:187],v[76:79],v[32:35]
ds_read_b32 v62,v11 offset:0
v_mfma_f32_16x16x32_bf16 v[32:35],v[196:199],v[80:83],v[32:35]
ds_read_b128 v[104:107],v5 offset:1152
v_mfma_f32_16x16x32_bf16 v[36:39],v[200:203],v[80:83],v[36:39]
v_mfma_f32_16x16x32_bf16 v[36:39],v[188:191],v[76:79],v[36:39]
buffer_load_dwordx4 v2,s[32:35],s63 offen offset:1024 sc1 lds
v_mfma_f32_16x16x32_bf16 v[36:39],v[176:179],v[124:127],v[36:39]
ds_read_b128 v[108:111],v5 offset:2304
ds_read2st64_b32 v[56:57],v8 offset0:144 offset1:180
v_mfma_f32_16x16x32_bf16 v[36:39],v[164:167],v[120:123],v[36:39]
ds_read2st64_b32 v[58:59],v8 offset0:216 offset1:252
v_xor_b32 v8,v9,v8
v_mfma_f32_16x16x32_bf16 v[40:43],v[168:171],v[120:123],v[40:43]
s_waitcnt lgkmcnt(4)
v_lshlrev_b32 v60,16,v62
v_mfma_f32_16x16x32_bf16 v[40:43],v[180:183],v[124:127],v[40:43]
buffer_load_dwordx4 v2,s[32:35],s63 offen offset:2048 sc1 lds
ds_read_b128 v[112:115],v5 offset:3456
v_mfma_f32_16x16x32_bf16 v[40:43],v[192:195],v[76:79],v[40:43]
v_and_b32 v62,0xffff0000,v62
v_mfma_f32_16x16x32_bf16 v[40:43],v[204:207],v[80:83],v[40:43]
v_mfma_f32_16x16x32_bf16 v[40:43],v[216:219],v[84:87],v[40:43]
ds_read_b128 v[116:119],v5 offset:4608
v_mfma_f32_16x16x32_bf16 v[40:43],v[228:231],v[88:91],v[40:43]
buffer_load_dwordx4 v2,s[32:35],s63 offen offset:3072 sc1 lds
v_mfma_f32_16x16x32_bf16 v[40:43],v[240:243],v[92:95],v[40:43]
v_rcp_f32 v61,v62
v_mfma_f32_16x16x32_bf16 v[40:43],v[252:255],v[96:99],v[40:43]
ds_read_b128 v[120:123],v5 offset:5760
v_mul_f32 v62,v62,v62
v_mfma_f32_16x16x32_bf16 v[36:39],v[248:251],v[96:99],v[36:39]
s_add_u32 m0,s64,4096
s_waitcnt lgkmcnt(3)
v_add_f32 v56,v56,v57
v_mfma_f32_16x16x32_bf16 v[36:39],v[236:239],v[92:95],v[36:39]
buffer_load_dwordx4 v6,s[32:35],s63 offen offset:0 sc1 lds
v_add_f32 v58,v58,v59
v_mfma_f32_16x16x32_bf16 v[36:39],v[224:227],v[88:91],v[36:39]
ds_read_b128 v[124:127],v5 offset:6912
v_add_f32 v56,v56,v58
v_mfma_f32_16x16x32_bf16 v[36:39],v[212:215],v[84:87],v[36:39]
v_mul_f32 v61,0xbfb8aa3b,v61
v_add_f32_dpp v56,v56,v56 row_ror:8 row_mask:0xf bank_mask:0xf
v_mfma_f32_16x16x32_bf16 v[32:35],v[208:211],v[84:87],v[32:35]
v_mfma_f32_16x16x32_bf16 v[32:35],v[220:223],v[88:91],v[32:35]
buffer_load_dwordx4 v6,s[32:35],s63 offen offset:1024 sc1 lds
ds_read_b128 v[76:79],v5 offset:8064
v_mfma_f32_16x16x32_bf16 v[32:35],v[232:235],v[92:95],v[32:35]
v_add_f32_dpp v56,v56,v56 row_ror:4 row_mask:0xf bank_mask:0xf
s_sub_u32 s62,s62,1
v_mfma_f32_16x16x32_bf16 v[32:35],v[244:247],v[96:99],v[32:35]
s_cbranch_scc1 .L_gt0_0
s_waitcnt lgkmcnt(4)
v_mfma_f32_16x16x32_bf16 v[44:47],a[0:3],v[100:103],0
ds_read_b128 v[80:83],v5 offset:9216
s_add_u32 s68,s68,890880
v_mfma_f32_16x16x32_bf16 v[44:47],a[12:15],v[104:107],v[44:47]
buffer_load_dwordx4 v6,s[32:35],s63 offen offset:2048 sc1 lds
v_fma_f32 v40,-v60,v28,v40
v_mfma_f32_16x16x32_bf16 v[44:47],a[24:27],v[108:111],v[44:47]
v_fma_f32 v41,-v60,v29,v41
v_fma_f32 v42,-v60,v30,v42
v_mfma_f32_16x16x32_bf16 v[44:47],a[36:39],v[112:115],v[44:47]
ds_read_b128 v[84:87],v5 offset:10368
v_fma_f32 v43,-v60,v31,v43
v_mfma_f32_16x16x32_bf16 v[48:51],a[40:43],v[112:115],0
v_permlane32_swap_b32 v40,v42
v_mfma_f32_16x16x32_bf16 v[48:51],a[28:31],v[108:111],v[48:51]
buffer_load_dwordx4 v6,s[32:35],s63 offen offset:3072 sc1 lds
v_fma_f32 v36,-v60,v24,v36
v_mfma_f32_16x16x32_bf16 v[48:51],a[16:19],v[104:107],v[48:51]
ds_read_b128 v[88:91],v5 offset:11520
v_fma_f32 v37,-v60,v25,v37
v_mfma_f32_16x16x32_bf16 v[48:51],a[4:7],v[100:103],v[48:51]
v_permlane32_swap_b32 v41,v43
v_mfma_f32_16x16x32_bf16 v[52:55],a[8:11],v[100:103],0
v_fma_f32 v38,-v60,v26,v38
v_fma_f32 v39,-v60,v27,v39
v_mfma_f32_16x16x32_bf16 v[52:55],a[20:23],v[104:107],v[52:55]
ds_read_b128 v[92:95],v5 offset:12672
v_fma_f32 v32,-v60,v20,v32
v_mfma_f32_16x16x32_bf16 v[52:55],a[32:35],v[108:111],v[52:55]
v_fma_f32 v33,-v60,v21,v33
v_fma_f32 v34,-v60,v22,v34
v_mfma_f32_16x16x32_bf16 v[52:55],a[44:47],v[112:115],v[52:55]
s_waitcnt lgkmcnt(4)
s_waitcnt vmcnt(8)
s_barrier
v_mfma_f32_16x16x32_bf16 v[52:55],a[56:59],v[116:119],v[52:55]
ds_read_b128 v[96:99],v5 offset:13824
s_add_u32 m0,s64,8192
v_mfma_f32_16x16x32_bf16 v[52:55],a[68:71],v[120:123],v[52:55]
buffer_load_dwordx4 v7,s[32:35],s63 offen offset:0 sc1 lds
s_add_u32 s63,s63,890880
v_mfma_f32_16x16x32_bf16 v[52:55],a[80:83],v[124:127],v[52:55]
v_fma_f32 v35,-v60,v23,v35
v_mul_f32 v64,v32,v61
v_mfma_f32_16x16x32_bf16 v[52:55],a[92:95],v[76:79],v[52:55]
ds_read_b128 v[100:103],v5 offset:14976
v_mul_f32 v65,v33,v61
v_mfma_f32_16x16x32_bf16 v[48:51],a[88:91],v[76:79],v[48:51]
v_exp_f32 v64,v64
v_mfma_f32_16x16x32_bf16 v[48:51],a[76:79],v[124:127],v[48:51]
v_exp_f32 v65,v65
v_mfma_f32_16x16x32_bf16 v[48:51],a[64:67],v[120:123],v[48:51]
ds_read_b128 v[104:107],v5 offset:16128
v_mul_f32 v66,v34,v61
v_mfma_f32_16x16x32_bf16 v[48:51],a[52:55],v[116:119],v[48:51]
v_exp_f32 v66,v66
v_mfma_f32_16x16x32_bf16 v[44:47],a[48:51],v[116:119],v[44:47]
v_mul_f32 v67,v35,v61
v_mul_f32 v68,v40,v61
v_mfma_f32_16x16x32_bf16 v[44:47],a[60:63],v[120:123],v[44:47]
ds_read_b128 v[108:111],v5 offset:17280
v_mul_f32 v69,v41,v61
v_mfma_f32_16x16x32_bf16 v[44:47],a[72:75],v[124:127],v[44:47]
v_exp_f32 v67,v67
v_mfma_f32_16x16x32_bf16 v[44:47],a[84:87],v[76:79],v[44:47]
v_mul_f32 v32,v32,v36
s_waitcnt lgkmcnt(4)
v_mfma_f32_16x16x32_bf16 v[44:47],a[96:99],v[80:83],v[44:47]
ds_read_b128 v[112:115],v5 offset:18432
v_mul_f32 v33,v33,v37
v_mfma_f32_16x16x32_bf16 v[44:47],a[108:111],v[84:87],v[44:47]
v_exp_f32 v68,v68
v_mfma_f32_16x16x32_bf16 v[44:47],a[120:123],v[88:91],v[44:47]
v_exp_f32 v69,v69
v_mfma_f32_16x16x32_bf16 v[44:47],a[132:135],v[92:95],v[44:47]
ds_read_b128 v[116:119],v5 offset:19584
v_mul_f32 v34,v34,v38
v_mfma_f32_16x16x32_bf16 v[48:51],a[136:139],v[92:95],v[48:51]
v_mul_f32 v35,v35,v39
v_mul_f32 v40,v40,v42
v_mfma_f32_16x16x32_bf16 v[48:51],a[124:127],v[88:91],v[48:51]
v_mul_f32 v41,v41,v43
v_fma_f32 v64,v64,v62,v62
v_mfma_f32_16x16x32_bf16 v[48:51],a[112:115],v[84:87],v[48:51]
ds_read_b128 v[120:123],v5 offset:20736
v_fma_f32 v65,v65,v62,v62
)#"
R"#(
v_mfma_f32_16x16x32_bf16 v[48:51],a[100:103],v[80:83],v[48:51]
v_rcp_f32 v64,v64
v_mfma_f32_16x16x32_bf16 v[52:55],a[104:107],v[80:83],v[52:55]
v_rcp_f32 v65,v65
v_mfma_f32_16x16x32_bf16 v[52:55],a[116:119],v[84:87],v[52:55]
ds_read_b128 v[124:127],v5 offset:21888
v_fma_f32 v66,v66,v62,v62
v_mfma_f32_16x16x32_bf16 v[52:55],a[128:131],v[88:91],v[52:55]
v_rcp_f32 v66,v66
v_mfma_f32_16x16x32_bf16 v[52:55],a[140:143],v[92:95],v[52:55]
v_fma_f32 v67,v67,v62,v62
s_waitcnt lgkmcnt(4)
v_mfma_f32_16x16x32_bf16 v[52:55],a[152:155],v[96:99],v[52:55]
ds_read_b128 v[76:79],v5 offset:23040
v_fma_f32 v68,v68,v62,v62
v_mfma_f32_16x16x32_bf16 v[52:55],a[164:167],v[100:103],v[52:55]
v_rcp_f32 v67,v67
v_mfma_f32_16x16x32_bf16 v[52:55],a[176:179],v[104:107],v[52:55]
v_rcp_f32 v68,v68
v_mfma_f32_16x16x32_bf16 v[52:55],a[188:191],v[108:111],v[52:55]
ds_read_b128 v[80:83],v5 offset:24192
v_fma_f32 v69,v69,v62,v62
v_mfma_f32_16x16x32_bf16 v[48:51],a[184:187],v[108:111],v[48:51]
v_rcp_f32 v69,v69
v_mfma_f32_16x16x32_bf16 v[48:51],a[172:175],v[104:107],v[48:51]
v_mul_f32 v32,v32,v64
v_mul_f32 v33,v33,v65
v_mfma_f32_16x16x32_bf16 v[48:51],a[160:163],v[100:103],v[48:51]
ds_read_b128 v[84:87],v5 offset:25344
v_mul_f32 v34,v34,v66
v_mfma_f32_16x16x32_bf16 v[48:51],a[148:151],v[96:99],v[48:51]
v_mul_f32 v35,v35,v67
v_mul_f32 v40,v40,v68
v_mfma_f32_16x16x32_bf16 v[44:47],a[144:147],v[96:99],v[44:47]
v_mul_f32 v41,v41,v69
v_cvt_pk_bf16_f32 v72,v32,v33
v_mfma_f32_16x16x32_bf16 v[44:47],a[156:159],v[100:103],v[44:47]
ds_read_b128 v[88:91],v5 offset:26496
v_cvt_pk_bf16_f32 v74,v40,v41
v_mfma_f32_16x16x32_bf16 v[44:47],a[168:171],v[104:107],v[44:47]
v_cvt_pk_bf16_f32 v73,v34,v35
v_add_f32_dpp v56,v56,v56 row_ror:2 row_mask:0xf bank_mask:0xf
v_mfma_f32_16x16x32_bf16 v[44:47],a[180:183],v[108:111],v[44:47]
v_mul_f32 v56,0x3a800000,v56
s_waitcnt lgkmcnt(4)
v_mfma_f32_16x16x32_bf16 v[44:47],a[192:195],v[112:115],v[44:47]
ds_read_b128 v[92:95],v5 offset:27648
v_mul_f32 v57,v56,v56
v_mfma_f32_16x16x32_bf16 v[44:47],a[204:207],v[116:119],v[44:47]
v_permlane32_swap_b32 v74,v75
v_mfma_f32_16x16x32_bf16 v[44:47],a[216:219],v[120:123],v[44:47]
v_sub_f32_dpp v57,v56,v57 quad_perm:[1,1,3,3] row_mask:0xf bank_mask:0xf
v_mfma_f32_16x16x32_bf16 v[44:47],a[228:231],v[124:127],v[44:47]
ds_read_b128 v[96:99],v5 offset:28800
v_max_f32 v57,0,v57
v_mfma_f32_16x16x32_bf16 v[48:51],a[232:235],v[124:127],v[48:51]
v_permlane16_swap_b32 v72,v74
v_mfma_f32_16x16x32_bf16 v[48:51],a[220:223],v[120:123],v[48:51]
v_permlane16_swap_b32 v73,v75
v_mfma_f32_16x16x32_bf16 v[48:51],a[208:211],v[116:119],v[48:51]
buffer_store_dwordx4 v[72:75],v15,s[32:35],s68 offen sc1
ds_read_b128 v[100:103],v5 offset:29952
v_mfma_f32_16x16x32_bf16 v[48:51],a[196:199],v[112:115],v[48:51]
v_cvt_pk_bf16_f32 v57,0,v57
v_mfma_f32_16x16x32_bf16 v[52:55],a[200:203],v[112:115],v[52:55]
v_add_f32 v57,0x3727c5ac,v57
v_mfma_f32_16x16x32_bf16 v[52:55],a[212:215],v[116:119],v[52:55]
ds_read_b128 v[104:107],v5 offset:31104
v_cvt_pk_bf16_f32 v57,0,v57
v_mfma_f32_16x16x32_bf16 v[52:55],a[224:227],v[120:123],v[52:55]
v_sqrt_f32 v57,v57
v_mfma_f32_16x16x32_bf16 v[52:55],a[236:239],v[124:127],v[52:55]
v_cvt_pk_bf16_f32 v57,v56,v57
s_waitcnt lgkmcnt(4)
v_mfma_f32_16x16x32_bf16 v[52:55],a[248:251],v[76:79],v[52:55]
ds_read_b128 v[108:111],v5 offset:32256
ds_write_b32 v10,v57 offset:128
v_mfma_f32_16x16x32_bf16 v[52:55],v[132:135],v[80:83],v[52:55]
v_mfma_f32_16x16x32_bf16 v[52:55],v[144:147],v[84:87],v[52:55]
v_mfma_f32_16x16x32_bf16 v[52:55],v[156:159],v[88:91],v[52:55]
ds_read_b128 v[112:115],v5 offset:33408
v_mfma_f32_16x16x32_bf16 v[48:51],v[152:155],v[88:91],v[48:51]
v_mfma_f32_16x16x32_bf16 v[48:51],v[140:143],v[84:87],v[48:51]
v_mfma_f32_16x16x32_bf16 v[48:51],v[128:131],v[80:83],v[48:51]
ds_read_b128 v[116:119],v5 offset:34560
v_mfma_f32_16x16x32_bf16 v[48:51],a[244:247],v[76:79],v[48:51]
v_mfma_f32_16x16x32_bf16 v[44:47],a[240:243],v[76:79],v[44:47]
v_mfma_f32_16x16x32_bf16 v[44:47],a[252:255],v[80:83],v[44:47]
ds_read_b128 v[120:123],v5 offset:35712
v_mfma_f32_16x16x32_bf16 v[44:47],v[136:139],v[84:87],v[44:47]
v_xor_b32 v5,73728,v5
v_mfma_f32_16x16x32_bf16 v[44:47],v[148:151],v[88:91],v[44:47]
s_waitcnt lgkmcnt(5)
s_waitcnt vmcnt(1)
s_waitcnt lgkmcnt(3)
s_barrier
v_mfma_f32_16x16x32_bf16 v[44:47],v[160:163],v[92:95],v[44:47]
ds_read_b128 v[76:79],v4 offset:0
s_add_u32 m0,s64,36864
v_mfma_f32_16x16x32_bf16 v[44:47],v[172:175],v[96:99],v[44:47]
buffer_load_dwordx4 v2,s[32:35],s63 offen offset:0 sc1 lds
v_mfma_f32_16x16x32_bf16 v[44:47],v[184:187],v[100:103],v[44:47]
ds_read_b32 v62,v11 offset:128
v_mfma_f32_16x16x32_bf16 v[44:47],v[196:199],v[104:107],v[44:47]
ds_read_b128 v[80:83],v4 offset:1152
v_mfma_f32_16x16x32_bf16 v[48:51],v[200:203],v[104:107],v[48:51]
v_mfma_f32_16x16x32_bf16 v[48:51],v[188:191],v[100:103],v[48:51]
buffer_load_dwordx4 v2,s[32:35],s63 offen offset:1024 sc1 lds
v_mfma_f32_16x16x32_bf16 v[48:51],v[176:179],v[96:99],v[48:51]
ds_read_b128 v[84:87],v4 offset:2304
ds_read2st64_b32 v[56:57],v8 offset0:0 offset1:36
v_mfma_f32_16x16x32_bf16 v[48:51],v[164:167],v[92:95],v[48:51]
ds_read2st64_b32 v[58:59],v8 offset0:72 offset1:108
v_mfma_f32_16x16x32_bf16 v[52:55],v[168:171],v[92:95],v[52:55]
s_waitcnt lgkmcnt(4)
v_lshlrev_b32 v60,16,v62
v_mfma_f32_16x16x32_bf16 v[52:55],v[180:183],v[96:99],v[52:55]
buffer_load_dwordx4 v2,s[32:35],s63 offen offset:2048 sc1 lds
ds_read_b128 v[88:91],v4 offset:3456
v_mfma_f32_16x16x32_bf16 v[52:55],v[192:195],v[100:103],v[52:55]
v_and_b32 v62,0xffff0000,v62
v_mfma_f32_16x16x32_bf16 v[52:55],v[204:207],v[104:107],v[52:55]
v_mfma_f32_16x16x32_bf16 v[52:55],v[216:219],v[108:111],v[52:55]
ds_read_b128 v[92:95],v4 offset:4608
v_mfma_f32_16x16x32_bf16 v[52:55],v[228:231],v[112:115],v[52:55]
buffer_load_dwordx4 v2,s[32:35],s63 offen offset:3072 sc1 lds
v_mfma_f32_16x16x32_bf16 v[52:55],v[240:243],v[116:119],v[52:55]
v_rcp_f32 v61,v62
v_mfma_f32_16x16x32_bf16 v[52:55],v[252:255],v[120:123],v[52:55]
ds_read_b128 v[96:99],v4 offset:5760
v_mul_f32 v62,v62,v62
v_mfma_f32_16x16x32_bf16 v[48:51],v[248:251],v[120:123],v[48:51]
s_add_u32 m0,s64,40960
s_waitcnt lgkmcnt(3)
v_add_f32 v56,v56,v57
v_mfma_f32_16x16x32_bf16 v[48:51],v[236:239],v[116:119],v[48:51]
buffer_load_dwordx4 v6,s[32:35],s63 offen offset:0 sc1 lds
v_add_f32 v58,v58,v59
v_mfma_f32_16x16x32_bf16 v[48:51],v[224:227],v[112:115],v[48:51]
ds_read_b128 v[100:103],v4 offset:6912
v_add_f32 v56,v56,v58
v_mfma_f32_16x16x32_bf16 v[48:51],v[212:215],v[108:111],v[48:51]
v_mul_f32 v61,0xbfb8aa3b,v61
v_add_f32_dpp v56,v56,v56 row_ror:8 row_mask:0xf bank_mask:0xf
v_mfma_f32_16x16x32_bf16 v[44:47],v[208:211],v[108:111],v[44:47]
v_mfma_f32_16x16x32_bf16 v[44:47],v[220:223],v[112:115],v[44:47]
buffer_load_dwordx4 v6,s[32:35],s63 offen offset:1024 sc1 lds
ds_read_b128 v[104:107],v4 offset:8064
v_mfma_f32_16x16x32_bf16 v[44:47],v[232:235],v[116:119],v[44:47]
v_add_f32_dpp v56,v56,v56 row_ror:4 row_mask:0xf bank_mask:0xf
s_sub_u32 s62,s62,1
v_mfma_f32_16x16x32_bf16 v[44:47],v[244:247],v[120:123],v[44:47]
s_cbranch_scc0 .L_gl_0
s_nop 1
s_add_u32 s68,s68,890880
s_waitcnt vmcnt(6)
s_barrier
buffer_store_dwordx2 v[16:17],v2,s[92:95],s68 idxen sc1
v_fma_f32 v52,-v60,v28,v52
v_fma_f32 v53,-v60,v29,v53
v_fma_f32 v54,-v60,v30,v54
v_fma_f32 v55,-v60,v31,v55
v_fma_f32 v48,-v60,v24,v48
v_fma_f32 v49,-v60,v25,v49
v_fma_f32 v50,-v60,v26,v50
v_fma_f32 v51,-v60,v27,v51
v_fma_f32 v44,-v60,v20,v44
v_fma_f32 v45,-v60,v21,v45
v_fma_f32 v46,-v60,v22,v46
v_fma_f32 v47,-v60,v23,v47
v_permlane32_swap_b32 v52,v54
v_permlane32_swap_b32 v53,v55
v_mul_f32 v64,v44,v61
v_mul_f32 v65,v45,v61
v_mul_f32 v66,v46,v61
v_mul_f32 v67,v47,v61
v_mul_f32 v68,v52,v61
v_mul_f32 v69,v53,v61
v_exp_f32 v64,v64
v_exp_f32 v65,v65
v_mul_f32 v44,v44,v48
v_mul_f32 v45,v45,v49
v_exp_f32 v66,v66
v_exp_f32 v67,v67
v_mul_f32 v46,v46,v50
v_mul_f32 v47,v47,v51
v_exp_f32 v68,v68
v_exp_f32 v69,v69
v_mul_f32 v52,v52,v54
v_mul_f32 v53,v53,v55
v_fma_f32 v64,v64,v62,v62
v_fma_f32 v65,v65,v62,v62
v_rcp_f32 v64,v64
v_rcp_f32 v65,v65
v_fma_f32 v66,v66,v62,v62
v_fma_f32 v67,v67,v62,v62
v_rcp_f32 v66,v66
v_rcp_f32 v67,v67
v_fma_f32 v68,v68,v62,v62
v_fma_f32 v69,v69,v62,v62
v_rcp_f32 v68,v68
v_rcp_f32 v69,v69
v_mul_f32 v44,v44,v64
v_mul_f32 v45,v45,v65
v_mul_f32 v46,v46,v66
v_mul_f32 v47,v47,v67
v_mul_f32 v52,v52,v68
v_mul_f32 v53,v53,v69
v_cvt_pk_bf16_f32 v74,v52,v53
v_cvt_pk_bf16_f32 v72,v44,v45
v_cvt_pk_bf16_f32 v73,v46,v47
v_permlane32_swap_b32 v74,v75
s_nop 1
v_permlane16_swap_b32 v72,v74
v_permlane16_swap_b32 v73,v75
buffer_store_dwordx4 v[72:75],v15,s[32:35],s68 offen sc1
s_add_u32 s68,s68,890880
s_waitcnt vmcnt(0)
s_barrier
buffer_store_dwordx2 v[16:17],v2,s[92:95],s68 idxen sc1
s_branch .L_end
.L_gt0_0:
s_nop 1
s_add_u32 s68,s68,890880
s_waitcnt vmcnt(6)
s_barrier
buffer_store_dwordx2 v[16:17],v2,s[92:95],s68 idxen sc1
v_fma_f32 v40,-v60,v28,v40
v_fma_f32 v41,-v60,v29,v41
v_fma_f32 v42,-v60,v30,v42
v_fma_f32 v43,-v60,v31,v43
v_fma_f32 v36,-v60,v24,v36
v_fma_f32 v37,-v60,v25,v37
v_fma_f32 v38,-v60,v26,v38
v_fma_f32 v39,-v60,v27,v39
v_fma_f32 v32,-v60,v20,v32
v_fma_f32 v33,-v60,v21,v33
v_fma_f32 v34,-v60,v22,v34
v_fma_f32 v35,-v60,v23,v35
v_permlane32_swap_b32 v40,v42
v_permlane32_swap_b32 v41,v43
v_mul_f32 v64,v32,v61
v_mul_f32 v65,v33,v61
v_mul_f32 v66,v34,v61
v_mul_f32 v67,v35,v61
v_mul_f32 v68,v40,v61
v_mul_f32 v69,v41,v61
v_exp_f32 v64,v64
v_exp_f32 v65,v65
v_mul_f32 v32,v32,v36
v_mul_f32 v33,v33,v37
v_exp_f32 v66,v66
v_exp_f32 v67,v67
v_mul_f32 v34,v34,v38
v_mul_f32 v35,v35,v39
v_exp_f32 v68,v68
v_exp_f32 v69,v69
v_mul_f32 v40,v40,v42
v_mul_f32 v41,v41,v43
v_fma_f32 v64,v64,v62,v62
v_fma_f32 v65,v65,v62,v62
v_rcp_f32 v64,v64
v_rcp_f32 v65,v65
v_fma_f32 v66,v66,v62,v62
v_fma_f32 v67,v67,v62,v62
v_rcp_f32 v66,v66
v_rcp_f32 v67,v67
v_fma_f32 v68,v68,v62,v62
v_fma_f32 v69,v69,v62,v62
v_rcp_f32 v68,v68
v_rcp_f32 v69,v69
v_mul_f32 v32,v32,v64
v_mul_f32 v33,v33,v65
v_mul_f32 v34,v34,v66
v_mul_f32 v35,v35,v67
v_mul_f32 v40,v40,v68
v_mul_f32 v41,v41,v69
v_cvt_pk_bf16_f32 v74,v40,v41
v_cvt_pk_bf16_f32 v72,v32,v33
v_cvt_pk_bf16_f32 v73,v34,v35
v_permlane32_swap_b32 v74,v75
s_nop 1
v_permlane16_swap_b32 v72,v74
v_permlane16_swap_b32 v73,v75
buffer_store_dwordx4 v[72:75],v15,s[32:35],s68 offen sc1
s_add_u32 s68,s68,890880
s_waitcnt vmcnt(0)
s_barrier
buffer_store_dwordx2 v[16:17],v2,s[92:95],s68 idxen sc1
s_branch .L_end
.p2align 6
.L_gv1:
.L_gl_1:
s_waitcnt lgkmcnt(4)
v_mfma_f32_16x16x32_bf16 v[32:35],a[0:3],v[76:79],0
ds_read_b128 v[108:111],v4 offset:9216
s_add_u32 s68,s68,890880
v_mfma_f32_16x16x32_bf16 v[32:35],a[12:15],v[80:83],v[32:35]
buffer_load_dwordx4 v6,s[32:35],s63 offen offset:2048 sc1 lds
v_fma_f32 v52,-v60,v28,v52
v_mfma_f32_16x16x32_bf16 v[32:35],a[24:27],v[84:87],v[32:35]
v_fma_f32 v53,-v60,v29,v53
v_fma_f32 v54,-v60,v30,v54
v_mfma_f32_16x16x32_bf16 v[32:35],a[36:39],v[88:91],v[32:35]
ds_read_b128 v[112:115],v4 offset:10368
v_fma_f32 v55,-v60,v31,v55
v_mfma_f32_16x16x32_bf16 v[36:39],a[40:43],v[88:91],0
v_permlane32_swap_b32 v52,v54
v_mfma_f32_16x16x32_bf16 v[36:39],a[28:31],v[84:87],v[36:39]
buffer_load_dwordx4 v6,s[32:35],s63 offen offset:3072 sc1 lds
v_fma_f32 v48,-v60,v24,v48
v_mfma_f32_16x16x32_bf16 v[36:39],a[16:19],v[80:83],v[36:39]
ds_read_b128 v[116:119],v4 offset:11520
v_fma_f32 v49,-v60,v25,v49
v_mfma_f32_16x16x32_bf16 v[36:39],a[4:7],v[76:79],v[36:39]
v_permlane32_swap_b32 v53,v55
v_mfma_f32_16x16x32_bf16 v[40:43],a[8:11],v[76:79],0
v_fma_f32 v50,-v60,v26,v50
v_fma_f32 v51,-v60,v27,v51
v_mfma_f32_16x16x32_bf16 v[40:43],a[20:23],v[80:83],v[40:43]
ds_read_b128 v[120:123],v4 offset:12672
v_fma_f32 v44,-v60,v20,v44
v_mfma_f32_16x16x32_bf16 v[40:43],a[32:35],v[84:87],v[40:43]
s_waitcnt vmcnt(8)
s_barrier
v_mfma_f32_16x16x32_bf16 v[40:43],a[44:47],v[88:91],v[40:43]
v_fma_f32 v45,-v60,v21,v45
s_waitcnt lgkmcnt(4)
v_mfma_f32_16x16x32_bf16 v[40:43],a[56:59],v[92:95],v[40:43]
ds_read_b128 v[124:127],v4 offset:13824
s_add_u32 m0,s64,45056
v_mfma_f32_16x16x32_bf16 v[40:43],a[68:71],v[96:99],v[40:43]
buffer_load_dwordx4 v7,s[32:35],s63 offen offset:0 sc1 lds
s_add_u32 s63,s63,890880
v_mfma_f32_16x16x32_bf16 v[40:43],a[80:83],v[100:103],v[40:43]
s_xor_b32 s64,s64,s65
v_fma_f32 v46,-v60,v22,v46
v_mfma_f32_16x16x32_bf16 v[40:43],a[92:95],v[104:107],v[40:43]
ds_read_b128 v[76:79],v4 offset:14976
v_fma_f32 v47,-v60,v23,v47
v_mfma_f32_16x16x32_bf16 v[36:39],a[88:91],v[104:107],v[36:39]
v_mul_f32 v64,v44,v61
v_mul_f32 v65,v45,v61
v_mfma_f32_16x16x32_bf16 v[36:39],a[76:79],v[100:103],v[36:39]
buffer_load_dwordx2 v[12:13],v14,s[32:35],s66 offen sc1
v_mul_f32 v66,v46,v61
v_mfma_f32_16x16x32_bf16 v[36:39],a[64:67],v[96:99],v[36:39]
ds_read_b128 v[80:83],v4 offset:16128
v_mul_f32 v67,v47,v61
v_mfma_f32_16x16x32_bf16 v[36:39],a[52:55],v[92:95],v[36:39]
v_exp_f32 v64,v64
v_mfma_f32_16x16x32_bf16 v[32:35],a[48:51],v[92:95],v[32:35]
v_exp_f32 v65,v65
v_mfma_f32_16x16x32_bf16 v[32:35],a[60:63],v[96:99],v[32:35]
ds_read_b128 v[84:87],v4 offset:17280
v_mul_f32 v68,v52,v61
v_mfma_f32_16x16x32_bf16 v[32:35],a[72:75],v[100:103],v[32:35]
v_exp_f32 v66,v66
v_mfma_f32_16x16x32_bf16 v[32:35],a[84:87],v[104:107],v[32:35]
v_mul_f32 v69,v53,v61
s_waitcnt lgkmcnt(4)
v_mfma_f32_16x16x32_bf16 v[32:35],a[96:99],v[108:111],v[32:35]
ds_read_b128 v[88:91],v4 offset:18432
v_mul_f32 v44,v44,v48
v_mfma_f32_16x16x32_bf16 v[32:35],a[108:111],v[112:115],v[32:35]
v_exp_f32 v67,v67
v_mfma_f32_16x16x32_bf16 v[32:35],a[120:123],v[116:119],v[32:35]
v_exp_f32 v68,v68
v_mfma_f32_16x16x32_bf16 v[32:35],a[132:135],v[120:123],v[32:35]
ds_read_b128 v[92:95],v4 offset:19584
v_mul_f32 v45,v45,v49
v_mfma_f32_16x16x32_bf16 v[36:39],a[136:139],v[120:123],v[36:39]
v_exp_f32 v69,v69
v_mfma_f32_16x16x32_bf16 v[36:39],a[124:127],v[116:119],v[36:39]
v_mul_f32 v46,v46,v50
v_mul_f32 v47,v47,v51
v_mfma_f32_16x16x32_bf16 v[36:39],a[112:115],v[112:115],v[36:39]
ds_read_b128 v[96:99],v4 offset:20736
v_mul_f32 v52,v52,v54
v_mfma_f32_16x16x32_bf16 v[36:39],a[100:103],v[108:111],v[36:39]
v_mul_f32 v53,v53,v55
v_fma_f32 v64,v64,v62,v62
v_mfma_f32_16x16x32_bf16 v[40:43],a[104:107],v[108:111],v[40:43]
v_rcp_f32 v64,v64
v_mfma_f32_16x16x32_bf16 v[40:43],a[116:119],v[112:115],v[40:43]
ds_read_b128 v[100:103],v4 offset:21888
v_fma_f32 v65,v65,v62,v62
v_mfma_f32_16x16x32_bf16 v[40:43],a[128:131],v[116:119],v[40:43]
v_rcp_f32 v65,v65
v_mfma_f32_16x16x32_bf16 v[40:43],a[140:143],v[120:123],v[40:43]
v_fma_f32 v66,v66,v62,v62
s_waitcnt lgkmcnt(4)
v_mfma_f32_16x16x32_bf16 v[40:43],a[152:155],v[124:127],v[40:43]
ds_read_b128 v[104:107],v4 offset:23040
v_fma_f32 v67,v67,v62,v62
v_mfma_f32_16x16x32_bf16 v[40:43],a[164:167],v[76:79],v[40:43]
v_rcp_f32 v66,v66
v_mfma_f32_16x16x32_bf16 v[40:43],a[176:179],v[80:83],v[40:43]
v_rcp_f32 v67,v67
v_mfma_f32_16x16x32_bf16 v[40:43],a[188:191],v[84:87],v[40:43]
ds_read_b128 v[108:111],v4 offset:24192
v_fma_f32 v68,v68,v62,v62
v_mfma_f32_16x16x32_bf16 v[36:39],a[184:187],v[84:87],v[36:39]
v_rcp_f32 v68,v68
v_mfma_f32_16x16x32_bf16 v[36:39],a[172:175],v[80:83],v[36:39]
v_fma_f32 v69,v69,v62,v62
v_mul_f32 v44,v44,v64
v_mfma_f32_16x16x32_bf16 v[36:39],a[160:163],v[76:79],v[36:39]
ds_read_b128 v[112:115],v4 offset:25344
v_mul_f32 v45,v45,v65
v_mfma_f32_16x16x32_bf16 v[36:39],a[148:151],v[124:127],v[36:39]
v_rcp_f32 v69,v69
v_mfma_f32_16x16x32_bf16 v[32:35],a[144:147],v[124:127],v[32:35]
v_mul_f32 v46,v46,v66
v_mul_f32 v47,v47,v67
v_mfma_f32_16x16x32_bf16 v[32:35],a[156:159],v[76:79],v[32:35]
ds_read_b128 v[116:119],v4 offset:26496
v_mul_f32 v52,v52,v68
v_mfma_f32_16x16x32_bf16 v[32:35],a[168:171],v[80:83],v[32:35]
v_mul_f32 v53,v53,v69
v_cvt_pk_bf16_f32 v72,v44,v45
v_mfma_f32_16x16x32_bf16 v[32:35],a[180:183],v[84:87],v[32:35]
v_cvt_pk_bf16_f32 v74,v52,v53
s_waitcnt lgkmcnt(4)
v_mfma_f32_16x16x32_bf16 v[32:35],a[192:195],v[88:91],v[32:35]
ds_read_b128 v[120:123],v4 offset:27648
v_cvt_pk_bf16_f32 v73,v46,v47
v_mfma_f32_16x16x32_bf16 v[32:35],a[204:207],v[92:95],v[32:35]
v_permlane32_swap_b32 v74,v75
v_mfma_f32_16x16x32_bf16 v[32:35],a[216:219],v[96:99],v[32:35]
v_add_f32_dpp v56,v56,v56 row_ror:2 row_mask:0xf bank_mask:0xf
v_mfma_f32_16x16x32_bf16 v[32:35],a[228:231],v[100:103],v[32:35]
ds_read_b128 v[124:127],v4 offset:28800
v_mul_f32 v56,0x3a800000,v56
v_mfma_f32_16x16x32_bf16 v[36:39],a[232:235],v[100:103],v[36:39]
v_permlane16_swap_b32 v72,v74
v_mfma_f32_16x16x32_bf16 v[36:39],a[220:223],v[96:99],v[36:39]
v_permlane16_swap_b32 v73,v75
v_mfma_f32_16x16x32_bf16 v[36:39],a[208:211],v[92:95],v[36:39]
buffer_store_dwordx4 v[72:75],v15,s[32:35],s68 offen sc1
ds_read_b128 v[76:79],v4 offset:29952
v_mfma_f32_16x16x32_bf16 v[36:39],a[196:199],v[88:91],v[36:39]
v_mul_f32 v57,v56,v56
v_mfma_f32_16x16x32_bf16 v[40:43],a[200:203],v[88:91],v[40:43]
v_mfma_f32_16x16x32_bf16 v[40:43],a[212:215],v[92:95],v[40:43]
ds_read_b128 v[80:83],v4 offset:31104
v_sub_f32_dpp v57,v56,v57 quad_perm:[1,1,3,3] row_mask:0xf bank_mask:0xf
v_mfma_f32_16x16x32_bf16 v[40:43],a[224:227],v[96:99],v[40:43]
v_max_f32 v57,0,v57
v_mfma_f32_16x16x32_bf16 v[40:43],a[236:239],v[100:103],v[40:43]
v_cvt_pk_bf16_f32 v57,0,v57
s_waitcnt lgkmcnt(4)
v_mfma_f32_16x16x32_bf16 v[40:43],a[248:251],v[104:107],v[40:43]
ds_read_b128 v[84:87],v4 offset:32256
v_add_f32 v57,0x3727c5ac,v57
v_mfma_f32_16x16x32_bf16 v[40:43],v[132:135],v[108:111],v[40:43]
v_cvt_pk_bf16_f32 v57,0,v57
v_mfma_f32_16x16x32_bf16 v[40:43],v[144:147],v[112:115],v[40:43]
v_sqrt_f32 v57,v57
v_mfma_f32_16x16x32_bf16 v[40:43],v[156:159],v[116:119],v[40:43]
ds_read_b128 v[88:91],v4 offset:33408
v_cvt_pk_bf16_f32 v57,v56,v57
v_mfma_f32_16x16x32_bf16 v[36:39],v[152:155],v[116:119],v[36:39]
ds_write_b32 v10,v57 offset:0
v_mfma_f32_16x16x32_bf16 v[36:39],v[140:143],v[112:115],v[36:39]
v_mfma_f32_16x16x32_bf16 v[36:39],v[128:131],v[108:111],v[36:39]
ds_read_b128 v[92:95],v4 offset:34560
s_waitcnt vmcnt(1)
v_mfma_f32_16x16x32_bf16 v[36:39],a[244:247],v[104:107],v[36:39]
v_cmp_ne_u64 vcc,v[16:17],v[12:13]
v_mfma_f32_16x16x32_bf16 v[32:35],a[240:243],v[104:107],v[32:35]
s_cbranch_vccnz .L_rtg0_1
.L_bkg0_1:
s_add_u32 s66,s66,1781760
v_mfma_f32_16x16x32_bf16 v[32:35],a[252:255],v[108:111],v[32:35]
ds_read_b128 v[96:99],v4 offset:35712
v_mfma_f32_16x16x32_bf16 v[32:35],v[136:139],v[112:115],v[32:35]
v_xor_b32 v4,73728,v4
s_waitcnt lgkmcnt(2)
s_barrier
v_mfma_f32_16x16x32_bf16 v[32:35],v[148:151],v[116:119],v[32:35]
v_mfma_f32_16x16x32_bf16 v[32:35],v[160:163],v[120:123],v[32:35]
ds_read_b128 v[100:103],v5 offset:0
s_add_u32 m0,s64,0
v_mfma_f32_16x16x32_bf16 v[32:35],v[172:175],v[124:127],v[32:35]
buffer_load_dwordx4 v2,s[32:35],s63 offen offset:0 sc1 lds
v_mfma_f32_16x16x32_bf16 v[32:35],v[184:187],v[76:79],v[32:35]
ds_read_b32 v62,v11 offset:0
v_mfma_f32_16x16x32_bf16 v[32:35],v[196:199],v[80:83],v[32:35]
ds_read_b128 v[104:107],v5 offset:1152
v_mfma_f32_16x16x32_bf16 v[36:39],v[200:203],v[80:83],v[36:39]
v_mfma_f32_16x16x32_bf16 v[36:39],v[188:191],v[76:79],v[36:39]
buffer_load_dwordx4 v2,s[32:35],s63 offen offset:1024 sc1 lds
v_mfma_f32_16x16x32_bf16 v[36:39],v[176:179],v[124:127],v[36:39]
ds_read_b128 v[108:111],v5 offset:2304
ds_read2st64_b32 v[56:57],v8 offset0:144 offset1:180
v_mfma_f32_16x16x32_bf16 v[36:39],v[164:167],v[120:123],v[36:39]
ds_read2st64_b32 v[58:59],v8 offset0:216 offset1:252
v_xor_b32 v8,v9,v8
v_mfma_f32_16x16x32_bf16 v[40:43],v[168:171],v[120:123],v[40:43]
)#"
R"#(
s_waitcnt lgkmcnt(4)
v_lshlrev_b32 v60,16,v62
v_mfma_f32_16x16x32_bf16 v[40:43],v[180:183],v[124:127],v[40:43]
buffer_load_dwordx4 v2,s[32:35],s63 offen offset:2048 sc1 lds
ds_read_b128 v[112:115],v5 offset:3456
v_mfma_f32_16x16x32_bf16 v[40:43],v[192:195],v[76:79],v[40:43]
v_and_b32 v62,0xffff0000,v62
v_mfma_f32_16x16x32_bf16 v[40:43],v[204:207],v[80:83],v[40:43]
v_mfma_f32_16x16x32_bf16 v[40:43],v[216:219],v[84:87],v[40:43]
ds_read_b128 v[116:119],v5 offset:4608
v_mfma_f32_16x16x32_bf16 v[40:43],v[228:231],v[88:91],v[40:43]
buffer_load_dwordx4 v2,s[32:35],s63 offen offset:3072 sc1 lds
v_mfma_f32_16x16x32_bf16 v[40:43],v[240:243],v[92:95],v[40:43]
v_rcp_f32 v61,v62
v_mfma_f32_16x16x32_bf16 v[40:43],v[252:255],v[96:99],v[40:43]
ds_read_b128 v[120:123],v5 offset:5760
v_mul_f32 v62,v62,v62
v_mfma_f32_16x16x32_bf16 v[36:39],v[248:251],v[96:99],v[36:39]
s_add_u32 m0,s64,4096
s_waitcnt lgkmcnt(3)
v_add_f32 v56,v56,v57
v_mfma_f32_16x16x32_bf16 v[36:39],v[236:239],v[92:95],v[36:39]
buffer_load_dwordx4 v6,s[32:35],s63 offen offset:0 sc1 lds
v_add_f32 v58,v58,v59
v_mfma_f32_16x16x32_bf16 v[36:39],v[224:227],v[88:91],v[36:39]
ds_read_b128 v[124:127],v5 offset:6912
v_add_f32 v56,v56,v58
v_mfma_f32_16x16x32_bf16 v[36:39],v[212:215],v[84:87],v[36:39]
v_mul_f32 v61,0xbfb8aa3b,v61
v_add_f32_dpp v56,v56,v56 row_ror:8 row_mask:0xf bank_mask:0xf
v_mfma_f32_16x16x32_bf16 v[32:35],v[208:211],v[84:87],v[32:35]
v_mfma_f32_16x16x32_bf16 v[32:35],v[220:223],v[88:91],v[32:35]
buffer_load_dwordx4 v6,s[32:35],s63 offen offset:1024 sc1 lds
ds_read_b128 v[76:79],v5 offset:8064
v_mfma_f32_16x16x32_bf16 v[32:35],v[232:235],v[92:95],v[32:35]
v_add_f32_dpp v56,v56,v56 row_ror:4 row_mask:0xf bank_mask:0xf
s_sub_u32 s62,s62,1
v_mfma_f32_16x16x32_bf16 v[32:35],v[244:247],v[96:99],v[32:35]
s_cbranch_scc1 .L_gt0_1
s_waitcnt lgkmcnt(4)
v_mfma_f32_16x16x32_bf16 v[44:47],a[0:3],v[100:103],0
ds_read_b128 v[80:83],v5 offset:9216
s_add_u32 s68,s68,890880
v_mfma_f32_16x16x32_bf16 v[44:47],a[12:15],v[104:107],v[44:47]
buffer_load_dwordx4 v6,s[32:35],s63 offen offset:2048 sc1 lds
v_fma_f32 v40,-v60,v28,v40
v_mfma_f32_16x16x32_bf16 v[44:47],a[24:27],v[108:111],v[44:47]
v_fma_f32 v41,-v60,v29,v41
v_fma_f32 v42,-v60,v30,v42
v_mfma_f32_16x16x32_bf16 v[44:47],a[36:39],v[112:115],v[44:47]
ds_read_b128 v[84:87],v5 offset:10368
v_fma_f32 v43,-v60,v31,v43
v_mfma_f32_16x16x32_bf16 v[48:51],a[40:43],v[112:115],0
v_permlane32_swap_b32 v40,v42
v_mfma_f32_16x16x32_bf16 v[48:51],a[28:31],v[108:111],v[48:51]
buffer_load_dwordx4 v6,s[32:35],s63 offen offset:3072 sc1 lds
v_fma_f32 v36,-v60,v24,v36
v_mfma_f32_16x16x32_bf16 v[48:51],a[16:19],v[104:107],v[48:51]
ds_read_b128 v[88:91],v5 offset:11520
v_fma_f32 v37,-v60,v25,v37
v_mfma_f32_16x16x32_bf16 v[48:51],a[4:7],v[100:103],v[48:51]
v_permlane32_swap_b32 v41,v43
v_mfma_f32_16x16x32_bf16 v[52:55],a[8:11],v[100:103],0
v_fma_f32 v38,-v60,v26,v38
v_fma_f32 v39,-v60,v27,v39
v_mfma_f32_16x16x32_bf16 v[52:55],a[20:23],v[104:107],v[52:55]
ds_read_b128 v[92:95],v5 offset:12672
v_fma_f32 v32,-v60,v20,v32
v_mfma_f32_16x16x32_bf16 v[52:55],a[32:35],v[108:111],v[52:55]
s_waitcnt vmcnt(8)
s_barrier
v_mfma_f32_16x16x32_bf16 v[52:55],a[44:47],v[112:115],v[52:55]
v_fma_f32 v33,-v60,v21,v33
s_waitcnt lgkmcnt(4)
v_mfma_f32_16x16x32_bf16 v[52:55],a[56:59],v[116:119],v[52:55]
ds_read_b128 v[96:99],v5 offset:13824
s_add_u32 m0,s64,8192
v_mfma_f32_16x16x32_bf16 v[52:55],a[68:71],v[120:123],v[52:55]
buffer_load_dwordx4 v7,s[32:35],s63 offen offset:0 sc1 lds
s_add_u32 s63,s63,890880
v_mfma_f32_16x16x32_bf16 v[52:55],a[80:83],v[124:127],v[52:55]
v_fma_f32 v34,-v60,v22,v34
v_fma_f32 v35,-v60,v23,v35
v_mfma_f32_16x16x32_bf16 v[52:55],a[92:95],v[76:79],v[52:55]
ds_read_b128 v[100:103],v5 offset:14976
v_mul_f32 v64,v32,v61
v_mfma_f32_16x16x32_bf16 v[48:51],a[88:91],v[76:79],v[48:51]
v_exp_f32 v64,v64
v_mfma_f32_16x16x32_bf16 v[48:51],a[76:79],v[124:127],v[48:51]
v_mul_f32 v65,v33,v61
v_mul_f32 v66,v34,v61
v_mfma_f32_16x16x32_bf16 v[48:51],a[64:67],v[120:123],v[48:51]
ds_read_b128 v[104:107],v5 offset:16128
v_mul_f32 v67,v35,v61
v_mfma_f32_16x16x32_bf16 v[48:51],a[52:55],v[116:119],v[48:51]
v_exp_f32 v65,v65
v_mfma_f32_16x16x32_bf16 v[44:47],a[48:51],v[116:119],v[44:47]
v_exp_f32 v66,v66
v_mfma_f32_16x16x32_bf16 v[44:47],a[60:63],v[120:123],v[44:47]
ds_read_b128 v[108:111],v5 offset:17280
v_mul_f32 v68,v40,v61
v_mfma_f32_16x16x32_bf16 v[44:47],a[72:75],v[124:127],v[44:47]
v_exp_f32 v67,v67
v_mfma_f32_16x16x32_bf16 v[44:47],a[84:87],v[76:79],v[44:47]
v_mul_f32 v69,v41,v61
s_waitcnt lgkmcnt(4)
v_mfma_f32_16x16x32_bf16 v[44:47],a[96:99],v[80:83],v[44:47]
ds_read_b128 v[112:115],v5 offset:18432
v_mul_f32 v32,v32,v36
v_mfma_f32_16x16x32_bf16 v[44:47],a[108:111],v[84:87],v[44:47]
v_exp_f32 v68,v68
v_mfma_f32_16x16x32_bf16 v[44:47],a[120:123],v[88:91],v[44:47]
v_exp_f32 v69,v69
v_mfma_f32_16x16x32_bf16 v[44:47],a[132:135],v[92:95],v[44:47]
ds_read_b128 v[116:119],v5 offset:19584
v_mul_f32 v33,v33,v37
v_mfma_f32_16x16x32_bf16 v[48:51],a[136:139],v[92:95],v[48:51]
v_mul_f32 v34,v34,v38
v_mul_f32 v35,v35,v39
v_mfma_f32_16x16x32_bf16 v[48:51],a[124:127],v[88:91],v[48:51]
v_mul_f32 v40,v40,v42
v_mul_f32 v41,v41,v43
v_mfma_f32_16x16x32_bf16 v[48:51],a[112:115],v[84:87],v[48:51]
ds_read_b128 v[120:123],v5 offset:20736
v_fma_f32 v64,v64,v62,v62
v_mfma_f32_16x16x32_bf16 v[48:51],a[100:103],v[80:83],v[48:51]
v_rcp_f32 v64,v64
v_mfma_f32_16x16x32_bf16 v[52:55],a[104:107],v[80:83],v[52:55]
v_fma_f32 v65,v65,v62,v62
v_fma_f32 v66,v66,v62,v62
v_mfma_f32_16x16x32_bf16 v[52:55],a[116:119],v[84:87],v[52:55]
ds_read_b128 v[124:127],v5 offset:21888
v_fma_f32 v67,v67,v62,v62
v_mfma_f32_16x16x32_bf16 v[52:55],a[128:131],v[88:91],v[52:55]
v_rcp_f32 v65,v65
v_mfma_f32_16x16x32_bf16 v[52:55],a[140:143],v[92:95],v[52:55]
v_fma_f32 v68,v68,v62,v62
s_waitcnt lgkmcnt(4)
v_mfma_f32_16x16x32_bf16 v[52:55],a[152:155],v[96:99],v[52:55]
ds_read_b128 v[76:79],v5 offset:23040
v_fma_f32 v69,v69,v62,v62
v_mfma_f32_16x16x32_bf16 v[52:55],a[164:167],v[100:103],v[52:55]
v_rcp_f32 v66,v66
v_mfma_f32_16x16x32_bf16 v[52:55],a[176:179],v[104:107],v[52:55]
v_rcp_f32 v67,v67
v_mfma_f32_16x16x32_bf16 v[52:55],a[188:191],v[108:111],v[52:55]
ds_read_b128 v[80:83],v5 offset:24192
v_mul_f32 v32,v32,v64
v_mfma_f32_16x16x32_bf16 v[48:51],a[184:187],v[108:111],v[48:51]
v_rcp_f32 v68,v68
v_mfma_f32_16x16x32_bf16 v[48:51],a[172:175],v[104:107],v[48:51]
v_rcp_f32 v69,v69
v_mfma_f32_16x16x32_bf16 v[48:51],a[160:163],v[100:103],v[48:51]
ds_read_b128 v[84:87],v5 offset:25344
v_mul_f32 v33,v33,v65
v_mfma_f32_16x16x32_bf16 v[48:51],a[148:151],v[96:99],v[48:51]
v_mul_f32 v34,v34,v66
v_mul_f32 v35,v35,v67
v_mfma_f32_16x16x32_bf16 v[44:47],a[144:147],v[96:99],v[44:47]
v_mul_f32 v40,v40,v68
v_mul_f32 v41,v41,v69
v_mfma_f32_16x16x32_bf16 v[44:47],a[156:159],v[100:103],v[44:47]
ds_read_b128 v[88:91],v5 offset:26496
v_cvt_pk_bf16_f32 v74,v40,v41
v_mfma_f32_16x16x32_bf16 v[44:47],a[168:171],v[104:107],v[44:47]
v_cvt_pk_bf16_f32 v72,v32,v33
v_cvt_pk_bf16_f32 v73,v34,v35
v_mfma_f32_16x16x32_bf16 v[44:47],a[180:183],v[108:111],v[44:47]
v_add_f32_dpp v56,v56,v56 row_ror:2 row_mask:0xf bank_mask:0xf
s_waitcnt lgkmcnt(4)
v_mfma_f32_16x16x32_bf16 v[44:47],a[192:195],v[112:115],v[44:47]
ds_read_b128 v[92:95],v5 offset:27648
v_mul_f32 v56,0x3a800000,v56
v_mfma_f32_16x16x32_bf16 v[44:47],a[204:207],v[116:119],v[44:47]
v_permlane32_swap_b32 v74,v75
v_mfma_f32_16x16x32_bf16 v[44:47],a[216:219],v[120:123],v[44:47]
v_mul_f32 v57,v56,v56
v_mfma_f32_16x16x32_bf16 v[44:47],a[228:231],v[124:127],v[44:47]
ds_read_b128 v[96:99],v5 offset:28800
v_sub_f32_dpp v57,v56,v57 quad_perm:[1,1,3,3] row_mask:0xf bank_mask:0xf
v_mfma_f32_16x16x32_bf16 v[48:51],a[232:235],v[124:127],v[48:51]
v_permlane16_swap_b32 v72,v74
v_mfma_f32_16x16x32_bf16 v[48:51],a[220:223],v[120:123],v[48:51]
v_permlane16_swap_b32 v73,v75
v_mfma_f32_16x16x32_bf16 v[48:51],a[208:211],v[116:119],v[48:51]
buffer_store_dwordx4 v[72:75],v15,s[32:35],s68 offen sc1
ds_read_b128 v[100:103],v5 offset:29952
v_mfma_f32_16x16x32_bf16 v[48:51],a[196:199],v[112:115],v[48:51]
v_max_f32 v57,0,v57
v_mfma_f32_16x16x32_bf16 v[52:55],a[200:203],v[112:115],v[52:55]
v_cvt_pk_bf16_f32 v57,0,v57
v_mfma_f32_16x16x32_bf16 v[52:55],a[212:215],v[116:119],v[52:55]
ds_read_b128 v[104:107],v5 offset:31104
v_add_f32 v57,0x3727c5ac,v57
v_mfma_f32_16x16x32_bf16 v[52:55],a[224:227],v[120:123],v[52:55]
v_cvt_pk_bf16_f32 v57,0,v57
v_mfma_f32_16x16x32_bf16 v[52:55],a[236:239],v[124:127],v[52:55]
s_waitcnt lgkmcnt(4)
v_mfma_f32_16x16x32_bf16 v[52:55],a[248:251],v[76:79],v[52:55]
ds_read_b128 v[108:111],v5 offset:32256
v_mfma_f32_16x16x32_bf16 v[52:55],v[132:135],v[80:83],v[52:55]
v_sqrt_f32 v57,v57
v_mfma_f32_16x16x32_bf16 v[52:55],v[144:147],v[84:87],v[52:55]
v_cvt_pk_bf16_f32 v57,v56,v57
v_mfma_f32_16x16x32_bf16 v[52:55],v[156:159],v[88:91],v[52:55]
ds_read_b128 v[112:115],v5 offset:33408
ds_write_b32 v10,v57 offset:128
v_mfma_f32_16x16x32_bf16 v[48:51],v[152:155],v[88:91],v[48:51]
v_mfma_f32_16x16x32_bf16 v[48:51],v[140:143],v[84:87],v[48:51]
v_mfma_f32_16x16x32_bf16 v[48:51],v[128:131],v[80:83],v[48:51]
ds_read_b128 v[116:119],v5 offset:34560
v_mfma_f32_16x16x32_bf16 v[48:51],a[244:247],v[76:79],v[48:51]
v_mfma_f32_16x16x32_bf16 v[44:47],a[240:243],v[76:79],v[44:47]
v_mfma_f32_16x16x32_bf16 v[44:47],a[252:255],v[80:83],v[44:47]
ds_read_b128 v[120:123],v5 offset:35712
v_mfma_f32_16x16x32_bf16 v[44:47],v[136:139],v[84:87],v[44:47]
s_waitcnt vmcnt(1)
s_waitcnt lgkmcnt(2)
s_barrier
v_mfma_f32_16x16x32_bf16 v[44:47],v[148:151],v[88:91],v[44:47]
v_xor_b32 v5,73728,v5
v_mfma_f32_16x16x32_bf16 v[44:47],v[160:163],v[92:95],v[44:47]
ds_read_b128 v[76:79],v4 offset:0
s_add_u32 m0,s64,36864
v_mfma_f32_16x16x32_bf16 v[44:47],v[172:175],v[96:99],v[44:47]
buffer_load_dwordx4 v2,s[32:35],s63 offen offset:0 sc1 lds
v_mfma_f32_16x16x32_bf16 v[44:47],v[184:187],v[100:103],v[44:47]
ds_read_b32 v62,v11 offset:128
v_mfma_f32_16x16x32_bf16 v[44:47],v[196:199],v[104:107],v[44:47]
ds_read_b128 v[80:83],v4 offset:1152
v_mfma_f32_16x16x32_bf16 v[48:51],v[200:203],v[104:107],v[48:51]
v_mfma_f32_16x16x32_bf16 v[48:51],v[188:191],v[100:103],v[48:51]
buffer_load_dwordx4 v2,s[32:35],s63 offen offset:1024 sc1 lds
v_mfma_f32_16x16x32_bf16 v[48:51],v[176:179],v[96:99],v[48:51]
ds_read_b128 v[84:87],v4 offset:2304
ds_read2st64_b32 v[56:57],v8 offset0:0 offset1:36
v_mfma_f32_16x16x32_bf16 v[48:51],v[164:167],v[92:95],v[48:51]
ds_read2st64_b32 v[58:59],v8 offset0:72 offset1:108
v_mfma_f32_16x16x32_bf16 v[52:55],v[168:171],v[92:95],v[52:55]
s_waitcnt lgkmcnt(4)
v_lshlrev_b32 v60,16,v62
v_mfma_f32_16x16x32_bf16 v[52:55],v[180:183],v[96:99],v[52:55]
buffer_load_dwordx4 v2,s[32:35],s63 offen offset:2048 sc1 lds
ds_read_b128 v[88:91],v4 offset:3456
v_mfma_f32_16x16x32_bf16 v[52:55],v[192:195],v[100:103],v[52:55]
v_and_b32 v62,0xffff0000,v62
v_mfma_f32_16x16x32_bf16 v[52:55],v[204:207],v[104:107],v[52:55]
v_mfma_f32_16x16x32_bf16 v[52:55],v[216:219],v[108:111],v[52:55]
ds_read_b128 v[92:95],v4 offset:4608
v_mfma_f32_16x16x32_bf16 v[52:55],v[228:231],v[112:115],v[52:55]
buffer_load_dwordx4 v2,s[32:35],s63 offen offset:3072 sc1 lds
v_mfma_f32_16x16x32_bf16 v[52:55],v[240:243],v[116:119],v[52:55]
v_rcp_f32 v61,v62
v_mfma_f32_16x16x32_bf16 v[52:55],v[252:255],v[120:123],v[52:55]
ds_read_b128 v[96:99],v4 offset:5760
v_mul_f32 v62,v62,v62
v_mfma_f32_16x16x32_bf16 v[48:51],v[248:251],v[120:123],v[48:51]
s_add_u32 m0,s64,40960
s_waitcnt lgkmcnt(3)
v_add_f32 v56,v56,v57
v_mfma_f32_16x16x32_bf16 v[48:51],v[236:239],v[116:119],v[48:51]
buffer_load_dwordx4 v6,s[32:35],s63 offen offset:0 sc1 lds
v_add_f32 v58,v58,v59
v_mfma_f32_16x16x32_bf16 v[48:51],v[224:227],v[112:115],v[48:51]
ds_read_b128 v[100:103],v4 offset:6912
v_add_f32 v56,v56,v58
v_mfma_f32_16x16x32_bf16 v[48:51],v[212:215],v[108:111],v[48:51]
v_mul_f32 v61,0xbfb8aa3b,v61
v_add_f32_dpp v56,v56,v56 row_ror:8 row_mask:0xf bank_mask:0xf
v_mfma_f32_16x16x32_bf16 v[44:47],v[208:211],v[108:111],v[44:47]
v_mfma_f32_16x16x32_bf16 v[44:47],v[220:223],v[112:115],v[44:47]
buffer_load_dwordx4 v6,s[32:35],s63 offen offset:1024 sc1 lds
ds_read_b128 v[104:107],v4 offset:8064
v_mfma_f32_16x16x32_bf16 v[44:47],v[232:235],v[116:119],v[44:47]
v_add_f32_dpp v56,v56,v56 row_ror:4 row_mask:0xf bank_mask:0xf
s_sub_u32 s62,s62,1
v_mfma_f32_16x16x32_bf16 v[44:47],v[244:247],v[120:123],v[44:47]
s_cbranch_scc0 .L_gl_1
s_nop 1
s_add_u32 s68,s68,890880
s_waitcnt vmcnt(6)
s_barrier
buffer_store_dwordx2 v[16:17],v2,s[92:95],s68 idxen sc1
v_fma_f32 v52,-v60,v28,v52
v_fma_f32 v53,-v60,v29,v53
v_fma_f32 v54,-v60,v30,v54
v_fma_f32 v55,-v60,v31,v55
v_fma_f32 v48,-v60,v24,v48
v_fma_f32 v49,-v60,v25,v49
v_fma_f32 v50,-v60,v26,v50
v_fma_f32 v51,-v60,v27,v51
v_fma_f32 v44,-v60,v20,v44
v_fma_f32 v45,-v60,v21,v45
v_fma_f32 v46,-v60,v22,v46
v_fma_f32 v47,-v60,v23,v47
v_permlane32_swap_b32 v52,v54
v_permlane32_swap_b32 v53,v55
v_mul_f32 v64,v44,v61
v_mul_f32 v65,v45,v61
v_mul_f32 v66,v46,v61
v_mul_f32 v67,v47,v61
v_mul_f32 v68,v52,v61
v_mul_f32 v69,v53,v61
v_exp_f32 v64,v64
v_exp_f32 v65,v65
v_mul_f32 v44,v44,v48
v_mul_f32 v45,v45,v49
v_exp_f32 v66,v66
v_exp_f32 v67,v67
v_mul_f32 v46,v46,v50
v_mul_f32 v47,v47,v51
v_exp_f32 v68,v68
v_exp_f32 v69,v69
v_mul_f32 v52,v52,v54
v_mul_f32 v53,v53,v55
v_fma_f32 v64,v64,v62,v62
v_fma_f32 v65,v65,v62,v62
v_rcp_f32 v64,v64
v_rcp_f32 v65,v65
v_fma_f32 v66,v66,v62,v62
v_fma_f32 v67,v67,v62,v62
v_rcp_f32 v66,v66
v_rcp_f32 v67,v67
v_fma_f32 v68,v68,v62,v62
v_fma_f32 v69,v69,v62,v62
v_rcp_f32 v68,v68
v_rcp_f32 v69,v69
v_mul_f32 v44,v44,v64
v_mul_f32 v45,v45,v65
v_mul_f32 v46,v46,v66
v_mul_f32 v47,v47,v67
v_mul_f32 v52,v52,v68
v_mul_f32 v53,v53,v69
v_cvt_pk_bf16_f32 v74,v52,v53
v_cvt_pk_bf16_f32 v72,v44,v45
v_cvt_pk_bf16_f32 v73,v46,v47
v_permlane32_swap_b32 v74,v75
s_nop 1
v_permlane16_swap_b32 v72,v74
v_permlane16_swap_b32 v73,v75
buffer_store_dwordx4 v[72:75],v15,s[32:35],s68 offen sc1
s_add_u32 s68,s68,890880
s_waitcnt vmcnt(0)
s_barrier
buffer_store_dwordx2 v[16:17],v2,s[92:95],s68 idxen sc1
s_branch .L_end
.L_gt0_1:
s_nop 1
s_add_u32 s68,s68,890880
s_waitcnt vmcnt(6)
s_barrier
buffer_store_dwordx2 v[16:17],v2,s[92:95],s68 idxen sc1
v_fma_f32 v40,-v60,v28,v40
v_fma_f32 v41,-v60,v29,v41
v_fma_f32 v42,-v60,v30,v42
v_fma_f32 v43,-v60,v31,v43
v_fma_f32 v36,-v60,v24,v36
v_fma_f32 v37,-v60,v25,v37
v_fma_f32 v38,-v60,v26,v38
v_fma_f32 v39,-v60,v27,v39
v_fma_f32 v32,-v60,v20,v32
v_fma_f32 v33,-v60,v21,v33
v_fma_f32 v34,-v60,v22,v34
v_fma_f32 v35,-v60,v23,v35
v_permlane32_swap_b32 v40,v42
v_permlane32_swap_b32 v41,v43
v_mul_f32 v64,v32,v61
v_mul_f32 v65,v33,v61
v_mul_f32 v66,v34,v61
v_mul_f32 v67,v35,v61
v_mul_f32 v68,v40,v61
v_mul_f32 v69,v41,v61
v_exp_f32 v64,v64
v_exp_f32 v65,v65
v_mul_f32 v32,v32,v36
v_mul_f32 v33,v33,v37
v_exp_f32 v66,v66
v_exp_f32 v67,v67
v_mul_f32 v34,v34,v38
v_mul_f32 v35,v35,v39
v_exp_f32 v68,v68
v_exp_f32 v69,v69
v_mul_f32 v40,v40,v42
v_mul_f32 v41,v41,v43
v_fma_f32 v64,v64,v62,v62
v_fma_f32 v65,v65,v62,v62
v_rcp_f32 v64,v64
v_rcp_f32 v65,v65
v_fma_f32 v66,v66,v62,v62
v_fma_f32 v67,v67,v62,v62
v_rcp_f32 v66,v66
v_rcp_f32 v67,v67
v_fma_f32 v68,v68,v62,v62
v_fma_f32 v69,v69,v62,v62
v_rcp_f32 v68,v68
v_rcp_f32 v69,v69
v_mul_f32 v32,v32,v64
v_mul_f32 v33,v33,v65
v_mul_f32 v34,v34,v66
v_mul_f32 v35,v35,v67
v_mul_f32 v40,v40,v68
v_mul_f32 v41,v41,v69
v_cvt_pk_bf16_f32 v74,v40,v41
v_cvt_pk_bf16_f32 v72,v32,v33
v_cvt_pk_bf16_f32 v73,v34,v35
v_permlane32_swap_b32 v74,v75
s_nop 1
v_permlane16_swap_b32 v72,v74
v_permlane16_swap_b32 v73,v75
buffer_store_dwordx4 v[72:75],v15,s[32:35],s68 offen sc1
s_add_u32 s68,s68,890880
s_waitcnt vmcnt(0)
s_barrier
buffer_store_dwordx2 v[16:17],v2,s[92:95],s68 idxen sc1
s_branch .L_end
.p2align 6
.L_gv2:
.L_gl_2:
s_waitcnt lgkmcnt(4)
v_mfma_f32_16x16x32_bf16 v[32:35],a[0:3],v[76:79],0
ds_read_b128 v[108:111],v4 offset:9216
s_add_u32 s68,s68,890880
v_mfma_f32_16x16x32_bf16 v[32:35],a[12:15],v[80:83],v[32:35]
buffer_load_dwordx4 v6,s[32:35],s63 offen offset:2048 sc1 lds
v_fma_f32 v52,-v60,v28,v52
v_mfma_f32_16x16x32_bf16 v[32:35],a[24:27],v[84:87],v[32:35]
v_fma_f32 v53,-v60,v29,v53
v_fma_f32 v54,-v60,v30,v54
v_mfma_f32_16x16x32_bf16 v[32:35],a[36:39],v[88:91],v[32:35]
ds_read_b128 v[112:115],v4 offset:10368
v_fma_f32 v55,-v60,v31,v55
v_mfma_f32_16x16x32_bf16 v[36:39],a[40:43],v[88:91],0
v_permlane32_swap_b32 v52,v54
v_mfma_f32_16x16x32_bf16 v[36:39],a[28:31],v[84:87],v[36:39]
buffer_load_dwordx4 v6,s[32:35],s63 offen offset:3072 sc1 lds
v_fma_f32 v48,-v60,v24,v48
v_mfma_f32_16x16x32_bf16 v[36:39],a[16:19],v[80:83],v[36:39]
ds_read_b128 v[116:119],v4 offset:11520
v_fma_f32 v49,-v60,v25,v49
v_mfma_f32_16x16x32_bf16 v[36:39],a[4:7],v[76:79],v[36:39]
v_permlane32_swap_b32 v53,v55
v_mfma_f32_16x16x32_bf16 v[40:43],a[8:11],v[76:79],0
v_fma_f32 v50,-v60,v26,v50
v_fma_f32 v51,-v60,v27,v51
v_mfma_f32_16x16x32_bf16 v[40:43],a[20:23],v[80:83],v[40:43]
s_waitcnt vmcnt(8)
s_barrier
v_mfma_f32_16x16x32_bf16 v[40:43],a[32:35],v[84:87],v[40:43]
ds_read_b128 v[120:123],v4 offset:12672
v_fma_f32 v44,-v60,v20,v44
v_mfma_f32_16x16x32_bf16 v[40:43],a[44:47],v[88:91],v[40:43]
v_fma_f32 v45,-v60,v21,v45
s_waitcnt lgkmcnt(4)
v_mfma_f32_16x16x32_bf16 v[40:43],a[56:59],v[92:95],v[40:43]
ds_read_b128 v[124:127],v4 offset:13824
s_add_u32 m0,s64,45056
v_mfma_f32_16x16x32_bf16 v[40:43],a[68:71],v[96:99],v[40:43]
buffer_load_dwordx4 v7,s[32:35],s63 offen offset:0 sc1 lds
s_add_u32 s63,s63,890880
v_mfma_f32_16x16x32_bf16 v[40:43],a[80:83],v[100:103],v[40:43]
s_xor_b32 s64,s64,s65
v_fma_f32 v46,-v60,v22,v46
v_mfma_f32_16x16x32_bf16 v[40:43],a[92:95],v[104:107],v[40:43]
ds_read_b128 v[76:79],v4 offset:14976
v_fma_f32 v47,-v60,v23,v47
v_mfma_f32_16x16x32_bf16 v[36:39],a[88:91],v[104:107],v[36:39]
v_mul_f32 v64,v44,v61
v_mul_f32 v65,v45,v61
v_mfma_f32_16x16x32_bf16 v[36:39],a[76:79],v[100:103],v[36:39]
v_exp_f32 v64,v64
v_mfma_f32_16x16x32_bf16 v[36:39],a[64:67],v[96:99],v[36:39]
ds_read_b128 v[80:83],v4 offset:16128
v_mul_f32 v66,v46,v61
v_mfma_f32_16x16x32_bf16 v[36:39],a[52:55],v[92:95],v[36:39]
v_exp_f32 v65,v65
v_mfma_f32_16x16x32_bf16 v[32:35],a[48:51],v[92:95],v[32:35]
v_exp_f32 v66,v66
v_mfma_f32_16x16x32_bf16 v[32:35],a[60:63],v[96:99],v[32:35]
ds_read_b128 v[84:87],v4 offset:17280
v_mul_f32 v67,v47,v61
v_mfma_f32_16x16x32_bf16 v[32:35],a[72:75],v[100:103],v[32:35]
v_exp_f32 v67,v67
v_mfma_f32_16x16x32_bf16 v[32:35],a[84:87],v[104:107],v[32:35]
v_mul_f32 v68,v52,v61
s_waitcnt lgkmcnt(4)
v_mfma_f32_16x16x32_bf16 v[32:35],a[96:99],v[108:111],v[32:35]
ds_read_b128 v[88:91],v4 offset:18432
v_mul_f32 v69,v53,v61
v_mfma_f32_16x16x32_bf16 v[32:35],a[108:111],v[112:115],v[32:35]
v_exp_f32 v68,v68
v_mfma_f32_16x16x32_bf16 v[32:35],a[120:123],v[116:119],v[32:35]
v_exp_f32 v69,v69
v_mfma_f32_16x16x32_bf16 v[32:35],a[132:135],v[120:123],v[32:35]
ds_read_b128 v[92:95],v4 offset:19584
v_mul_f32 v44,v44,v48
v_mfma_f32_16x16x32_bf16 v[36:39],a[136:139],v[120:123],v[36:39]
v_mul_f32 v45,v45,v49
v_mul_f32 v46,v46,v50
v_mfma_f32_16x16x32_bf16 v[36:39],a[124:127],v[116:119],v[36:39]
v_mul_f32 v47,v47,v51
)#"
R"#(
v_mul_f32 v52,v52,v54
v_mfma_f32_16x16x32_bf16 v[36:39],a[112:115],v[112:115],v[36:39]
ds_read_b128 v[96:99],v4 offset:20736
v_mul_f32 v53,v53,v55
v_mfma_f32_16x16x32_bf16 v[36:39],a[100:103],v[108:111],v[36:39]
v_fma_f32 v64,v64,v62,v62
v_fma_f32 v65,v65,v62,v62
v_mfma_f32_16x16x32_bf16 v[40:43],a[104:107],v[108:111],v[40:43]
v_rcp_f32 v64,v64
v_mfma_f32_16x16x32_bf16 v[40:43],a[116:119],v[112:115],v[40:43]
ds_read_b128 v[100:103],v4 offset:21888
v_fma_f32 v66,v66,v62,v62
v_mfma_f32_16x16x32_bf16 v[40:43],a[128:131],v[116:119],v[40:43]
v_rcp_f32 v65,v65
v_mfma_f32_16x16x32_bf16 v[40:43],a[140:143],v[120:123],v[40:43]
v_fma_f32 v67,v67,v62,v62
s_waitcnt lgkmcnt(4)
v_mfma_f32_16x16x32_bf16 v[40:43],a[152:155],v[124:127],v[40:43]
ds_read_b128 v[104:107],v4 offset:23040
v_fma_f32 v68,v68,v62,v62
v_mfma_f32_16x16x32_bf16 v[40:43],a[164:167],v[76:79],v[40:43]
v_rcp_f32 v66,v66
v_mfma_f32_16x16x32_bf16 v[40:43],a[176:179],v[80:83],v[40:43]
v_rcp_f32 v67,v67
v_mfma_f32_16x16x32_bf16 v[40:43],a[188:191],v[84:87],v[40:43]
ds_read_b128 v[108:111],v4 offset:24192
v_fma_f32 v69,v69,v62,v62
v_mfma_f32_16x16x32_bf16 v[36:39],a[184:187],v[84:87],v[36:39]
v_rcp_f32 v68,v68
v_mfma_f32_16x16x32_bf16 v[36:39],a[172:175],v[80:83],v[36:39]
v_rcp_f32 v69,v69
v_mfma_f32_16x16x32_bf16 v[36:39],a[160:163],v[76:79],v[36:39]
ds_read_b128 v[112:115],v4 offset:25344
v_mul_f32 v44,v44,v64
v_mfma_f32_16x16x32_bf16 v[36:39],a[148:151],v[124:127],v[36:39]
v_mul_f32 v45,v45,v65
v_mul_f32 v46,v46,v66
v_mfma_f32_16x16x32_bf16 v[32:35],a[144:147],v[124:127],v[32:35]
v_mul_f32 v47,v47,v67
v_mul_f32 v52,v52,v68
v_mfma_f32_16x16x32_bf16 v[32:35],a[156:159],v[76:79],v[32:35]
ds_read_b128 v[116:119],v4 offset:26496
v_mul_f32 v53,v53,v69
v_mfma_f32_16x16x32_bf16 v[32:35],a[168:171],v[80:83],v[32:35]
v_cvt_pk_bf16_f32 v74,v52,v53
v_cvt_pk_bf16_f32 v72,v44,v45
v_mfma_f32_16x16x32_bf16 v[32:35],a[180:183],v[84:87],v[32:35]
v_cvt_pk_bf16_f32 v73,v46,v47
s_waitcnt lgkmcnt(4)
v_mfma_f32_16x16x32_bf16 v[32:35],a[192:195],v[88:91],v[32:35]
ds_read_b128 v[120:123],v4 offset:27648
v_add_f32_dpp v56,v56,v56 row_ror:2 row_mask:0xf bank_mask:0xf
v_mfma_f32_16x16x32_bf16 v[32:35],a[204:207],v[92:95],v[32:35]
v_permlane32_swap_b32 v74,v75
v_mfma_f32_16x16x32_bf16 v[32:35],a[216:219],v[96:99],v[32:35]
v_mul_f32 v56,0x3a800000,v56
v_mfma_f32_16x16x32_bf16 v[32:35],a[228:231],v[100:103],v[32:35]
ds_read_b128 v[124:127],v4 offset:28800
v_mul_f32 v57,v56,v56
v_mfma_f32_16x16x32_bf16 v[36:39],a[232:235],v[100:103],v[36:39]
v_permlane16_swap_b32 v72,v74
v_mfma_f32_16x16x32_bf16 v[36:39],a[220:223],v[96:99],v[36:39]
v_permlane16_swap_b32 v73,v75
v_mfma_f32_16x16x32_bf16 v[36:39],a[208:211],v[92:95],v[36:39]
buffer_store_dwordx4 v[72:75],v15,s[32:35],s68 offen sc1
ds_read_b128 v[76:79],v4 offset:29952
v_mfma_f32_16x16x32_bf16 v[36:39],a[196:199],v[88:91],v[36:39]
v_sub_f32_dpp v57,v56,v57 quad_perm:[1,1,3,3] row_mask:0xf bank_mask:0xf
v_mfma_f32_16x16x32_bf16 v[40:43],a[200:203],v[88:91],v[40:43]
v_max_f32 v57,0,v57
v_mfma_f32_16x16x32_bf16 v[40:43],a[212:215],v[92:95],v[40:43]
ds_read_b128 v[80:83],v4 offset:31104
v_cvt_pk_bf16_f32 v57,0,v57
v_mfma_f32_16x16x32_bf16 v[40:43],a[224:227],v[96:99],v[40:43]
v_add_f32 v57,0x3727c5ac,v57
v_mfma_f32_16x16x32_bf16 v[40:43],a[236:239],v[100:103],v[40:43]
v_cvt_pk_bf16_f32 v57,0,v57
s_waitcnt lgkmcnt(4)
v_mfma_f32_16x16x32_bf16 v[40:43],a[248:251],v[104:107],v[40:43]
ds_read_b128 v[84:87],v4 offset:32256
v_mfma_f32_16x16x32_bf16 v[40:43],v[132:135],v[108:111],v[40:43]
v_sqrt_f32 v57,v57
v_mfma_f32_16x16x32_bf16 v[40:43],v[144:147],v[112:115],v[40:43]
v_cvt_pk_bf16_f32 v57,v56,v57
v_mfma_f32_16x16x32_bf16 v[40:43],v[156:159],v[116:119],v[40:43]
ds_read_b128 v[88:91],v4 offset:33408
ds_write_b32 v10,v57 offset:0
v_mfma_f32_16x16x32_bf16 v[36:39],v[152:155],v[116:119],v[36:39]
v_mfma_f32_16x16x32_bf16 v[36:39],v[140:143],v[112:115],v[36:39]
v_mfma_f32_16x16x32_bf16 v[36:39],v[128:131],v[108:111],v[36:39]
ds_read_b128 v[92:95],v4 offset:34560
v_mfma_f32_16x16x32_bf16 v[36:39],a[244:247],v[104:107],v[36:39]
v_mfma_f32_16x16x32_bf16 v[32:35],a[240:243],v[104:107],v[32:35]
v_mfma_f32_16x16x32_bf16 v[32:35],a[252:255],v[108:111],v[32:35]
s_waitcnt vmcnt(1)
s_waitcnt lgkmcnt(1)
s_barrier
v_mfma_f32_16x16x32_bf16 v[32:35],v[136:139],v[112:115],v[32:35]
ds_read_b128 v[96:99],v4 offset:35712
v_xor_b32 v4,73728,v4
v_mfma_f32_16x16x32_bf16 v[32:35],v[148:151],v[116:119],v[32:35]
v_mfma_f32_16x16x32_bf16 v[32:35],v[160:163],v[120:123],v[32:35]
ds_read_b128 v[100:103],v5 offset:0
s_add_u32 m0,s64,0
v_mfma_f32_16x16x32_bf16 v[32:35],v[172:175],v[124:127],v[32:35]
buffer_load_dwordx4 v2,s[32:35],s63 offen offset:0 sc1 lds
v_mfma_f32_16x16x32_bf16 v[32:35],v[184:187],v[76:79],v[32:35]
ds_read_b32 v62,v11 offset:0
v_mfma_f32_16x16x32_bf16 v[32:35],v[196:199],v[80:83],v[32:35]
ds_read_b128 v[104:107],v5 offset:1152
v_mfma_f32_16x16x32_bf16 v[36:39],v[200:203],v[80:83],v[36:39]
v_mfma_f32_16x16x32_bf16 v[36:39],v[188:191],v[76:79],v[36:39]
buffer_load_dwordx4 v2,s[32:35],s63 offen offset:1024 sc1 lds
v_mfma_f32_16x16x32_bf16 v[36:39],v[176:179],v[124:127],v[36:39]
ds_read_b128 v[108:111],v5 offset:2304
ds_read2st64_b32 v[56:57],v8 offset0:144 offset1:180
v_mfma_f32_16x16x32_bf16 v[36:39],v[164:167],v[120:123],v[36:39]
ds_read2st64_b32 v[58:59],v8 offset0:216 offset1:252
v_xor_b32 v8,v9,v8
v_mfma_f32_16x16x32_bf16 v[40:43],v[168:171],v[120:123],v[40:43]
s_waitcnt lgkmcnt(4)
v_lshlrev_b32 v60,16,v62
v_mfma_f32_16x16x32_bf16 v[40:43],v[180:183],v[124:127],v[40:43]
buffer_load_dwordx4 v2,s[32:35],s63 offen offset:2048 sc1 lds
ds_read_b128 v[112:115],v5 offset:3456
v_mfma_f32_16x16x32_bf16 v[40:43],v[192:195],v[76:79],v[40:43]
v_and_b32 v62,0xffff0000,v62
v_mfma_f32_16x16x32_bf16 v[40:43],v[204:207],v[80:83],v[40:43]
v_mfma_f32_16x16x32_bf16 v[40:43],v[216:219],v[84:87],v[40:43]
ds_read_b128 v[116:119],v5 offset:4608
v_mfma_f32_16x16x32_bf16 v[40:43],v[228:231],v[88:91],v[40:43]
buffer_load_dwordx4 v2,s[32:35],s63 offen offset:3072 sc1 lds
v_mfma_f32_16x16x32_bf16 v[40:43],v[240:243],v[92:95],v[40:43]
v_rcp_f32 v61,v62
v_mfma_f32_16x16x32_bf16 v[40:43],v[252:255],v[96:99],v[40:43]
ds_read_b128 v[120:123],v5 offset:5760
v_mul_f32 v62,v62,v62
v_mfma_f32_16x16x32_bf16 v[36:39],v[248:251],v[96:99],v[36:39]
s_add_u32 m0,s64,4096
s_waitcnt lgkmcnt(3)
v_add_f32 v56,v56,v57
v_mfma_f32_16x16x32_bf16 v[36:39],v[236:239],v[92:95],v[36:39]
buffer_load_dwordx4 v6,s[32:35],s63 offen offset:0 sc1 lds
v_add_f32 v58,v58,v59
v_mfma_f32_16x16x32_bf16 v[36:39],v[224:227],v[88:91],v[36:39]
ds_read_b128 v[124:127],v5 offset:6912
v_add_f32 v56,v56,v58
v_mfma_f32_16x16x32_bf16 v[36:39],v[212:215],v[84:87],v[36:39]
v_mul_f32 v61,0xbfb8aa3b,v61
v_add_f32_dpp v56,v56,v56 row_ror:8 row_mask:0xf bank_mask:0xf
v_mfma_f32_16x16x32_bf16 v[32:35],v[208:211],v[84:87],v[32:35]
v_mfma_f32_16x16x32_bf16 v[32:35],v[220:223],v[88:91],v[32:35]
buffer_load_dwordx4 v6,s[32:35],s63 offen offset:1024 sc1 lds
ds_read_b128 v[76:79],v5 offset:8064
v_mfma_f32_16x16x32_bf16 v[32:35],v[232:235],v[92:95],v[32:35]
v_add_f32_dpp v56,v56,v56 row_ror:4 row_mask:0xf bank_mask:0xf
s_sub_u32 s62,s62,1
v_mfma_f32_16x16x32_bf16 v[32:35],v[244:247],v[96:99],v[32:35]
s_cbranch_scc1 .L_gt0_2
s_waitcnt lgkmcnt(4)
v_mfma_f32_16x16x32_bf16 v[44:47],a[0:3],v[100:103],0
ds_read_b128 v[80:83],v5 offset:9216
s_add_u32 s68,s68,890880
v_mfma_f32_16x16x32_bf16 v[44:47],a[12:15],v[104:107],v[44:47]
buffer_load_dwordx4 v6,s[32:35],s63 offen offset:2048 sc1 lds
v_fma_f32 v40,-v60,v28,v40
v_mfma_f32_16x16x32_bf16 v[44:47],a[24:27],v[108:111],v[44:47]
v_fma_f32 v41,-v60,v29,v41
v_fma_f32 v42,-v60,v30,v42
v_mfma_f32_16x16x32_bf16 v[44:47],a[36:39],v[112:115],v[44:47]
ds_read_b128 v[84:87],v5 offset:10368
v_fma_f32 v43,-v60,v31,v43
v_mfma_f32_16x16x32_bf16 v[48:51],a[40:43],v[112:115],0
v_permlane32_swap_b32 v40,v42
v_mfma_f32_16x16x32_bf16 v[48:51],a[28:31],v[108:111],v[48:51]
buffer_load_dwordx4 v6,s[32:35],s63 offen offset:3072 sc1 lds
v_fma_f32 v36,-v60,v24,v36
v_mfma_f32_16x16x32_bf16 v[48:51],a[16:19],v[104:107],v[48:51]
ds_read_b128 v[88:91],v5 offset:11520
v_fma_f32 v37,-v60,v25,v37
v_mfma_f32_16x16x32_bf16 v[48:51],a[4:7],v[100:103],v[48:51]
v_permlane32_swap_b32 v41,v43
v_mfma_f32_16x16x32_bf16 v[52:55],a[8:11],v[100:103],0
v_fma_f32 v38,-v60,v26,v38
v_fma_f32 v39,-v60,v27,v39
v_mfma_f32_16x16x32_bf16 v[52:55],a[20:23],v[104:107],v[52:55]
s_waitcnt vmcnt(8)
s_barrier
v_mfma_f32_16x16x32_bf16 v[52:55],a[32:35],v[108:111],v[52:55]
ds_read_b128 v[92:95],v5 offset:12672
v_fma_f32 v32,-v60,v20,v32
v_mfma_f32_16x16x32_bf16 v[52:55],a[44:47],v[112:115],v[52:55]
v_fma_f32 v33,-v60,v21,v33
s_waitcnt lgkmcnt(4)
v_mfma_f32_16x16x32_bf16 v[52:55],a[56:59],v[116:119],v[52:55]
ds_read_b128 v[96:99],v5 offset:13824
s_add_u32 m0,s64,8192
v_mfma_f32_16x16x32_bf16 v[52:55],a[68:71],v[120:123],v[52:55]
buffer_load_dwordx4 v7,s[32:35],s63 offen offset:0 sc1 lds
s_add_u32 s63,s63,890880
v_mfma_f32_16x16x32_bf16 v[52:55],a[80:83],v[124:127],v[52:55]
v_fma_f32 v34,-v60,v22,v34
v_fma_f32 v35,-v60,v23,v35
v_mfma_f32_16x16x32_bf16 v[52:55],a[92:95],v[76:79],v[52:55]
ds_read_b128 v[100:103],v5 offset:14976
v_mul_f32 v64,v32,v61
v_mfma_f32_16x16x32_bf16 v[48:51],a[88:91],v[76:79],v[48:51]
v_exp_f32 v64,v64
v_mfma_f32_16x16x32_bf16 v[48:51],a[76:79],v[124:127],v[48:51]
buffer_load_dwordx2 v[12:13],v14,s[32:35],s66 offen sc1
v_mul_f32 v65,v33,v61
v_mfma_f32_16x16x32_bf16 v[48:51],a[64:67],v[120:123],v[48:51]
ds_read_b128 v[104:107],v5 offset:16128
v_mul_f32 v66,v34,v61
v_mfma_f32_16x16x32_bf16 v[48:51],a[52:55],v[116:119],v[48:51]
v_exp_f32 v65,v65
v_mfma_f32_16x16x32_bf16 v[44:47],a[48:51],v[116:119],v[44:47]
v_exp_f32 v66,v66
v_mfma_f32_16x16x32_bf16 v[44:47],a[60:63],v[120:123],v[44:47]
ds_read_b128 v[108:111],v5 offset:17280
v_mul_f32 v67,v35,v61
v_mfma_f32_16x16x32_bf16 v[44:47],a[72:75],v[124:127],v[44:47]
v_exp_f32 v67,v67
v_mfma_f32_16x16x32_bf16 v[44:47],a[84:87],v[76:79],v[44:47]
v_mul_f32 v68,v40,v61
s_waitcnt lgkmcnt(4)
v_mfma_f32_16x16x32_bf16 v[44:47],a[96:99],v[80:83],v[44:47]
ds_read_b128 v[112:115],v5 offset:18432
v_mul_f32 v69,v41,v61
v_mfma_f32_16x16x32_bf16 v[44:47],a[108:111],v[84:87],v[44:47]
v_exp_f32 v68,v68
v_mfma_f32_16x16x32_bf16 v[44:47],a[120:123],v[88:91],v[44:47]
v_exp_f32 v69,v69
v_mfma_f32_16x16x32_bf16 v[44:47],a[132:135],v[92:95],v[44:47]
ds_read_b128 v[116:119],v5 offset:19584
v_mul_f32 v32,v32,v36
v_mfma_f32_16x16x32_bf16 v[48:51],a[136:139],v[92:95],v[48:51]
v_mul_f32 v33,v33,v37
v_mul_f32 v34,v34,v38
v_mfma_f32_16x16x32_bf16 v[48:51],a[124:127],v[88:91],v[48:51]
v_mul_f32 v35,v35,v39
v_mul_f32 v40,v40,v42
v_mfma_f32_16x16x32_bf16 v[48:51],a[112:115],v[84:87],v[48:51]
ds_read_b128 v[120:123],v5 offset:20736
v_mul_f32 v41,v41,v43
v_mfma_f32_16x16x32_bf16 v[48:51],a[100:103],v[80:83],v[48:51]
v_fma_f32 v64,v64,v62,v62
v_fma_f32 v65,v65,v62,v62
v_mfma_f32_16x16x32_bf16 v[52:55],a[104:107],v[80:83],v[52:55]
v_rcp_f32 v64,v64
v_mfma_f32_16x16x32_bf16 v[52:55],a[116:119],v[84:87],v[52:55]
ds_read_b128 v[124:127],v5 offset:21888
v_fma_f32 v66,v66,v62,v62
v_mfma_f32_16x16x32_bf16 v[52:55],a[128:131],v[88:91],v[52:55]
v_rcp_f32 v65,v65
v_mfma_f32_16x16x32_bf16 v[52:55],a[140:143],v[92:95],v[52:55]
v_fma_f32 v67,v67,v62,v62
s_waitcnt lgkmcnt(4)
v_mfma_f32_16x16x32_bf16 v[52:55],a[152:155],v[96:99],v[52:55]
ds_read_b128 v[76:79],v5 offset:23040
v_fma_f32 v68,v68,v62,v62
v_mfma_f32_16x16x32_bf16 v[52:55],a[164:167],v[100:103],v[52:55]
v_rcp_f32 v66,v66
v_mfma_f32_16x16x32_bf16 v[52:55],a[176:179],v[104:107],v[52:55]
v_rcp_f32 v67,v67
v_mfma_f32_16x16x32_bf16 v[52:55],a[188:191],v[108:111],v[52:55]
ds_read_b128 v[80:83],v5 offset:24192
v_fma_f32 v69,v69,v62,v62
v_mfma_f32_16x16x32_bf16 v[48:51],a[184:187],v[108:111],v[48:51]
v_rcp_f32 v68,v68
v_mfma_f32_16x16x32_bf16 v[48:51],a[172:175],v[104:107],v[48:51]
v_rcp_f32 v69,v69
v_mfma_f32_16x16x32_bf16 v[48:51],a[160:163],v[100:103],v[48:51]
ds_read_b128 v[84:87],v5 offset:25344
v_mul_f32 v32,v32,v64
v_mfma_f32_16x16x32_bf16 v[48:51],a[148:151],v[96:99],v[48:51]
v_mul_f32 v33,v33,v65
v_mul_f32 v34,v34,v66
v_mfma_f32_16x16x32_bf16 v[44:47],a[144:147],v[96:99],v[44:47]
v_mul_f32 v35,v35,v67
v_mul_f32 v40,v40,v68
v_mfma_f32_16x16x32_bf16 v[44:47],a[156:159],v[100:103],v[44:47]
ds_read_b128 v[88:91],v5 offset:26496
v_mul_f32 v41,v41,v69
v_mfma_f32_16x16x32_bf16 v[44:47],a[168:171],v[104:107],v[44:47]
v_cvt_pk_bf16_f32 v74,v40,v41
v_cvt_pk_bf16_f32 v72,v32,v33
v_mfma_f32_16x16x32_bf16 v[44:47],a[180:183],v[108:111],v[44:47]
v_cvt_pk_bf16_f32 v73,v34,v35
s_waitcnt lgkmcnt(4)
v_mfma_f32_16x16x32_bf16 v[44:47],a[192:195],v[112:115],v[44:47]
ds_read_b128 v[92:95],v5 offset:27648
v_add_f32_dpp v56,v56,v56 row_ror:2 row_mask:0xf bank_mask:0xf
v_mfma_f32_16x16x32_bf16 v[44:47],a[204:207],v[116:119],v[44:47]
v_permlane32_swap_b32 v74,v75
v_mfma_f32_16x16x32_bf16 v[44:47],a[216:219],v[120:123],v[44:47]
v_mul_f32 v56,0x3a800000,v56
v_mfma_f32_16x16x32_bf16 v[44:47],a[228:231],v[124:127],v[44:47]
ds_read_b128 v[96:99],v5 offset:28800
v_mul_f32 v57,v56,v56
v_mfma_f32_16x16x32_bf16 v[48:51],a[232:235],v[124:127],v[48:51]
v_permlane16_swap_b32 v72,v74
v_mfma_f32_16x16x32_bf16 v[48:51],a[220:223],v[120:123],v[48:51]
v_permlane16_swap_b32 v73,v75
v_mfma_f32_16x16x32_bf16 v[48:51],a[208:211],v[116:119],v[48:51]
buffer_store_dwordx4 v[72:75],v15,s[32:35],s68 offen sc1
ds_read_b128 v[100:103],v5 offset:29952
v_mfma_f32_16x16x32_bf16 v[48:51],a[196:199],v[112:115],v[48:51]
v_sub_f32_dpp v57,v56,v57 quad_perm:[1,1,3,3] row_mask:0xf bank_mask:0xf
v_mfma_f32_16x16x32_bf16 v[52:55],a[200:203],v[112:115],v[52:55]
v_max_f32 v57,0,v57
v_mfma_f32_16x16x32_bf16 v[52:55],a[212:215],v[116:119],v[52:55]
ds_read_b128 v[104:107],v5 offset:31104
v_cvt_pk_bf16_f32 v57,0,v57
v_mfma_f32_16x16x32_bf16 v[52:55],a[224:227],v[120:123],v[52:55]
v_add_f32 v57,0x3727c5ac,v57
v_mfma_f32_16x16x32_bf16 v[52:55],a[236:239],v[124:127],v[52:55]
v_cvt_pk_bf16_f32 v57,0,v57
s_waitcnt lgkmcnt(4)
v_mfma_f32_16x16x32_bf16 v[52:55],a[248:251],v[76:79],v[52:55]
ds_read_b128 v[108:111],v5 offset:32256
v_mfma_f32_16x16x32_bf16 v[52:55],v[132:135],v[80:83],v[52:55]
v_sqrt_f32 v57,v57
v_mfma_f32_16x16x32_bf16 v[52:55],v[144:147],v[84:87],v[52:55]
v_cvt_pk_bf16_f32 v57,v56,v57
v_mfma_f32_16x16x32_bf16 v[52:55],v[156:159],v[88:91],v[52:55]
ds_read_b128 v[112:115],v5 offset:33408
ds_write_b32 v10,v57 offset:128
v_mfma_f32_16x16x32_bf16 v[48:51],v[152:155],v[88:91],v[48:51]
v_mfma_f32_16x16x32_bf16 v[48:51],v[140:143],v[84:87],v[48:51]
s_waitcnt vmcnt(1)
v_cmp_ne_u64 vcc,v[16:17],v[12:13]
v_mfma_f32_16x16x32_bf16 v[48:51],v[128:131],v[80:83],v[48:51]
ds_read_b128 v[116:119],v5 offset:34560
v_mfma_f32_16x16x32_bf16 v[48:51],a[244:247],v[76:79],v[48:51]
s_cbranch_vccnz .L_rtg1_2
.L_bkg1_2:
s_add_u32 s66,s66,1781760
v_mfma_f32_16x16x32_bf16 v[44:47],a[240:243],v[76:79],v[44:47]
v_mfma_f32_16x16x32_bf16 v[44:47],a[252:255],v[80:83],v[44:47]
ds_read_b128 v[120:123],v5 offset:35712
s_waitcnt lgkmcnt(2)
s_barrier
v_mfma_f32_16x16x32_bf16 v[44:47],v[136:139],v[84:87],v[44:47]
v_xor_b32 v5,73728,v5
v_mfma_f32_16x16x32_bf16 v[44:47],v[148:151],v[88:91],v[44:47]
v_mfma_f32_16x16x32_bf16 v[44:47],v[160:163],v[92:95],v[44:47]
ds_read_b128 v[76:79],v4 offset:0
s_add_u32 m0,s64,36864
v_mfma_f32_16x16x32_bf16 v[44:47],v[172:175],v[96:99],v[44:47]
buffer_load_dwordx4 v2,s[32:35],s63 offen offset:0 sc1 lds
v_mfma_f32_16x16x32_bf16 v[44:47],v[184:187],v[100:103],v[44:47]
ds_read_b32 v62,v11 offset:128
v_mfma_f32_16x16x32_bf16 v[44:47],v[196:199],v[104:107],v[44:47]
ds_read_b128 v[80:83],v4 offset:1152
v_mfma_f32_16x16x32_bf16 v[48:51],v[200:203],v[104:107],v[48:51]
v_mfma_f32_16x16x32_bf16 v[48:51],v[188:191],v[100:103],v[48:51]
buffer_load_dwordx4 v2,s[32:35],s63 offen offset:1024 sc1 lds
v_mfma_f32_16x16x32_bf16 v[48:51],v[176:179],v[96:99],v[48:51]
ds_read_b128 v[84:87],v4 offset:2304
ds_read2st64_b32 v[56:57],v8 offset0:0 offset1:36
v_mfma_f32_16x16x32_bf16 v[48:51],v[164:167],v[92:95],v[48:51]
ds_read2st64_b32 v[58:59],v8 offset0:72 offset1:108
v_mfma_f32_16x16x32_bf16 v[52:55],v[168:171],v[92:95],v[52:55]
s_waitcnt lgkmcnt(4)
v_lshlrev_b32 v60,16,v62
v_mfma_f32_16x16x32_bf16 v[52:55],v[180:183],v[96:99],v[52:55]
buffer_load_dwordx4 v2,s[32:35],s63 offen offset:2048 sc1 lds
ds_read_b128 v[88:91],v4 offset:3456
v_mfma_f32_16x16x32_bf16 v[52:55],v[192:195],v[100:103],v[52:55]
v_and_b32 v62,0xffff0000,v62
v_mfma_f32_16x16x32_bf16 v[52:55],v[204:207],v[104:107],v[52:55]
v_mfma_f32_16x16x32_bf16 v[52:55],v[216:219],v[108:111],v[52:55]
ds_read_b128 v[92:95],v4 offset:4608
v_mfma_f32_16x16x32_bf16 v[52:55],v[228:231],v[112:115],v[52:55]
buffer_load_dwordx4 v2,s[32:35],s63 offen offset:3072 sc1 lds
v_mfma_f32_16x16x32_bf16 v[52:55],v[240:243],v[116:119],v[52:55]
v_rcp_f32 v61,v62
v_mfma_f32_16x16x32_bf16 v[52:55],v[252:255],v[120:123],v[52:55]
ds_read_b128 v[96:99],v4 offset:5760
v_mul_f32 v62,v62,v62
v_mfma_f32_16x16x32_bf16 v[48:51],v[248:251],v[120:123],v[48:51]
s_add_u32 m0,s64,40960
s_waitcnt lgkmcnt(3)
v_add_f32 v56,v56,v57
v_mfma_f32_16x16x32_bf16 v[48:51],v[236:239],v[116:119],v[48:51]
buffer_load_dwordx4 v6,s[32:35],s63 offen offset:0 sc1 lds
v_add_f32 v58,v58,v59
v_mfma_f32_16x16x32_bf16 v[48:51],v[224:227],v[112:115],v[48:51]
ds_read_b128 v[100:103],v4 offset:6912
v_add_f32 v56,v56,v58
v_mfma_f32_16x16x32_bf16 v[48:51],v[212:215],v[108:111],v[48:51]
v_mul_f32 v61,0xbfb8aa3b,v61
v_add_f32_dpp v56,v56,v56 row_ror:8 row_mask:0xf bank_mask:0xf
v_mfma_f32_16x16x32_bf16 v[44:47],v[208:211],v[108:111],v[44:47]
v_mfma_f32_16x16x32_bf16 v[44:47],v[220:223],v[112:115],v[44:47]
buffer_load_dwordx4 v6,s[32:35],s63 offen offset:1024 sc1 lds
ds_read_b128 v[104:107],v4 offset:8064
v_mfma_f32_16x16x32_bf16 v[44:47],v[232:235],v[116:119],v[44:47]
v_add_f32_dpp v56,v56,v56 row_ror:4 row_mask:0xf bank_mask:0xf
s_sub_u32 s62,s62,1
v_mfma_f32_16x16x32_bf16 v[44:47],v[244:247],v[120:123],v[44:47]
s_cbranch_scc0 .L_gl_2
s_nop 1
s_add_u32 s68,s68,890880
s_waitcnt vmcnt(6)
s_barrier
buffer_store_dwordx2 v[16:17],v2,s[92:95],s68 idxen sc1
v_fma_f32 v52,-v60,v28,v52
v_fma_f32 v53,-v60,v29,v53
v_fma_f32 v54,-v60,v30,v54
v_fma_f32 v55,-v60,v31,v55
v_fma_f32 v48,-v60,v24,v48
v_fma_f32 v49,-v60,v25,v49
v_fma_f32 v50,-v60,v26,v50
v_fma_f32 v51,-v60,v27,v51
v_fma_f32 v44,-v60,v20,v44
v_fma_f32 v45,-v60,v21,v45
v_fma_f32 v46,-v60,v22,v46
v_fma_f32 v47,-v60,v23,v47
v_permlane32_swap_b32 v52,v54
v_permlane32_swap_b32 v53,v55
v_mul_f32 v64,v44,v61
v_mul_f32 v65,v45,v61
v_mul_f32 v66,v46,v61
v_mul_f32 v67,v47,v61
v_mul_f32 v68,v52,v61
v_mul_f32 v69,v53,v61
v_exp_f32 v64,v64
v_exp_f32 v65,v65
v_mul_f32 v44,v44,v48
v_mul_f32 v45,v45,v49
v_exp_f32 v66,v66
v_exp_f32 v67,v67
v_mul_f32 v46,v46,v50
v_mul_f32 v47,v47,v51
v_exp_f32 v68,v68
v_exp_f32 v69,v69
v_mul_f32 v52,v52,v54
v_mul_f32 v53,v53,v55
v_fma_f32 v64,v64,v62,v62
v_fma_f32 v65,v65,v62,v62
v_rcp_f32 v64,v64
v_rcp_f32 v65,v65
v_fma_f32 v66,v66,v62,v62
v_fma_f32 v67,v67,v62,v62
v_rcp_f32 v66,v66
v_rcp_f32 v67,v67
v_fma_f32 v68,v68,v62,v62
v_fma_f32 v69,v69,v62,v62
v_rcp_f32 v68,v68
v_rcp_f32 v69,v69
v_mul_f32 v44,v44,v64
v_mul_f32 v45,v45,v65
v_mul_f32 v46,v46,v66
v_mul_f32 v47,v47,v67
v_mul_f32 v52,v52,v68
v_mul_f32 v53,v53,v69
v_cvt_pk_bf16_f32 v74,v52,v53
v_cvt_pk_bf16_f32 v72,v44,v45
v_cvt_pk_bf16_f32 v73,v46,v47
v_permlane32_swap_b32 v74,v75
s_nop 1
v_permlane16_swap_b32 v72,v74
v_permlane16_swap_b32 v73,v75
buffer_store_dwordx4 v[72:75],v15,s[32:35],s68 offen sc1
s_add_u32 s68,s68,890880
s_waitcnt vmcnt(0)
s_barrier
buffer_store_dwordx2 v[16:17],v2,s[92:95],s68 idxen sc1
s_branch .L_end
.L_gt0_2:
s_nop 1
s_add_u32 s68,s68,890880
s_waitcnt vmcnt(6)
s_barrier
buffer_store_dwordx2 v[16:17],v2,s[92:95],s68 idxen sc1
v_fma_f32 v40,-v60,v28,v40
v_fma_f32 v41,-v60,v29,v41
v_fma_f32 v42,-v60,v30,v42
v_fma_f32 v43,-v60,v31,v43
v_fma_f32 v36,-v60,v24,v36
v_fma_f32 v37,-v60,v25,v37
v_fma_f32 v38,-v60,v26,v38
v_fma_f32 v39,-v60,v27,v39
v_fma_f32 v32,-v60,v20,v32
v_fma_f32 v33,-v60,v21,v33
v_fma_f32 v34,-v60,v22,v34
v_fma_f32 v35,-v60,v23,v35
v_permlane32_swap_b32 v40,v42
v_permlane32_swap_b32 v41,v43
v_mul_f32 v64,v32,v61
v_mul_f32 v65,v33,v61
v_mul_f32 v66,v34,v61
v_mul_f32 v67,v35,v61
v_mul_f32 v68,v40,v61
v_mul_f32 v69,v41,v61
v_exp_f32 v64,v64
v_exp_f32 v65,v65
v_mul_f32 v32,v32,v36
)#"
R"#(
v_mul_f32 v33,v33,v37
v_exp_f32 v66,v66
v_exp_f32 v67,v67
v_mul_f32 v34,v34,v38
v_mul_f32 v35,v35,v39
v_exp_f32 v68,v68
v_exp_f32 v69,v69
v_mul_f32 v40,v40,v42
v_mul_f32 v41,v41,v43
v_fma_f32 v64,v64,v62,v62
v_fma_f32 v65,v65,v62,v62
v_rcp_f32 v64,v64
v_rcp_f32 v65,v65
v_fma_f32 v66,v66,v62,v62
v_fma_f32 v67,v67,v62,v62
v_rcp_f32 v66,v66
v_rcp_f32 v67,v67
v_fma_f32 v68,v68,v62,v62
v_fma_f32 v69,v69,v62,v62
v_rcp_f32 v68,v68
v_rcp_f32 v69,v69
v_mul_f32 v32,v32,v64
v_mul_f32 v33,v33,v65
v_mul_f32 v34,v34,v66
v_mul_f32 v35,v35,v67
v_mul_f32 v40,v40,v68
v_mul_f32 v41,v41,v69
v_cvt_pk_bf16_f32 v74,v40,v41
v_cvt_pk_bf16_f32 v72,v32,v33
v_cvt_pk_bf16_f32 v73,v34,v35
v_permlane32_swap_b32 v74,v75
s_nop 1
v_permlane16_swap_b32 v72,v74
v_permlane16_swap_b32 v73,v75
buffer_store_dwordx4 v[72:75],v15,s[32:35],s68 offen sc1
s_add_u32 s68,s68,890880
s_waitcnt vmcnt(0)
s_barrier
buffer_store_dwordx2 v[16:17],v2,s[92:95],s68 idxen sc1
s_branch .L_end
.p2align 6
.L_gv3:
.L_gl_3:
s_waitcnt lgkmcnt(4)
v_mfma_f32_16x16x32_bf16 v[32:35],a[0:3],v[76:79],0
ds_read_b128 v[108:111],v4 offset:9216
s_add_u32 s68,s68,890880
v_mfma_f32_16x16x32_bf16 v[32:35],a[12:15],v[80:83],v[32:35]
buffer_load_dwordx4 v6,s[32:35],s63 offen offset:2048 sc1 lds
v_fma_f32 v52,-v60,v28,v52
v_mfma_f32_16x16x32_bf16 v[32:35],a[24:27],v[84:87],v[32:35]
v_fma_f32 v53,-v60,v29,v53
v_fma_f32 v54,-v60,v30,v54
v_mfma_f32_16x16x32_bf16 v[32:35],a[36:39],v[88:91],v[32:35]
ds_read_b128 v[112:115],v4 offset:10368
v_fma_f32 v55,-v60,v31,v55
v_mfma_f32_16x16x32_bf16 v[36:39],a[40:43],v[88:91],0
v_permlane32_swap_b32 v52,v54
v_mfma_f32_16x16x32_bf16 v[36:39],a[28:31],v[84:87],v[36:39]
buffer_load_dwordx4 v6,s[32:35],s63 offen offset:3072 sc1 lds
v_fma_f32 v48,-v60,v24,v48
v_mfma_f32_16x16x32_bf16 v[36:39],a[16:19],v[80:83],v[36:39]
ds_read_b128 v[116:119],v4 offset:11520
v_fma_f32 v49,-v60,v25,v49
v_mfma_f32_16x16x32_bf16 v[36:39],a[4:7],v[76:79],v[36:39]
v_permlane32_swap_b32 v53,v55
v_mfma_f32_16x16x32_bf16 v[40:43],a[8:11],v[76:79],0
s_waitcnt vmcnt(8)
s_barrier
v_mfma_f32_16x16x32_bf16 v[40:43],a[20:23],v[80:83],v[40:43]
buffer_store_dwordx2 v[16:17],v2,s[92:95],s68 idxen sc1
ds_read_b128 v[120:123],v4 offset:12672
v_mfma_f32_16x16x32_bf16 v[40:43],a[32:35],v[84:87],v[40:43]
v_fma_f32 v50,-v60,v26,v50
v_fma_f32 v51,-v60,v27,v51
v_mfma_f32_16x16x32_bf16 v[40:43],a[44:47],v[88:91],v[40:43]
v_fma_f32 v44,-v60,v20,v44
s_waitcnt lgkmcnt(4)
v_mfma_f32_16x16x32_bf16 v[40:43],a[56:59],v[92:95],v[40:43]
ds_read_b128 v[124:127],v4 offset:13824
s_add_u32 m0,s64,45056
v_mfma_f32_16x16x32_bf16 v[40:43],a[68:71],v[96:99],v[40:43]
buffer_load_dwordx4 v7,s[32:35],s63 offen offset:0 sc1 lds
s_add_u32 s63,s63,890880
v_mfma_f32_16x16x32_bf16 v[40:43],a[80:83],v[100:103],v[40:43]
s_xor_b32 s64,s64,s65
v_fma_f32 v45,-v60,v21,v45
v_mfma_f32_16x16x32_bf16 v[40:43],a[92:95],v[104:107],v[40:43]
ds_read_b128 v[76:79],v4 offset:14976
v_fma_f32 v46,-v60,v22,v46
v_mfma_f32_16x16x32_bf16 v[36:39],a[88:91],v[104:107],v[36:39]
v_fma_f32 v47,-v60,v23,v47
v_mul_f32 v64,v44,v61
v_mfma_f32_16x16x32_bf16 v[36:39],a[76:79],v[100:103],v[36:39]
v_exp_f32 v64,v64
v_mfma_f32_16x16x32_bf16 v[36:39],a[64:67],v[96:99],v[36:39]
ds_read_b128 v[80:83],v4 offset:16128
v_mul_f32 v65,v45,v61
v_mfma_f32_16x16x32_bf16 v[36:39],a[52:55],v[92:95],v[36:39]
v_exp_f32 v65,v65
v_mfma_f32_16x16x32_bf16 v[32:35],a[48:51],v[92:95],v[32:35]
v_mul_f32 v66,v46,v61
v_mul_f32 v67,v47,v61
v_mfma_f32_16x16x32_bf16 v[32:35],a[60:63],v[96:99],v[32:35]
ds_read_b128 v[84:87],v4 offset:17280
v_mul_f32 v68,v52,v61
v_mfma_f32_16x16x32_bf16 v[32:35],a[72:75],v[100:103],v[32:35]
v_exp_f32 v66,v66
v_mfma_f32_16x16x32_bf16 v[32:35],a[84:87],v[104:107],v[32:35]
v_mul_f32 v69,v53,v61
s_waitcnt lgkmcnt(4)
v_mfma_f32_16x16x32_bf16 v[32:35],a[96:99],v[108:111],v[32:35]
ds_read_b128 v[88:91],v4 offset:18432
v_mul_f32 v44,v44,v48
v_mfma_f32_16x16x32_bf16 v[32:35],a[108:111],v[112:115],v[32:35]
v_exp_f32 v67,v67
v_mfma_f32_16x16x32_bf16 v[32:35],a[120:123],v[116:119],v[32:35]
v_exp_f32 v68,v68
v_mfma_f32_16x16x32_bf16 v[32:35],a[132:135],v[120:123],v[32:35]
ds_read_b128 v[92:95],v4 offset:19584
v_mul_f32 v45,v45,v49
v_mfma_f32_16x16x32_bf16 v[36:39],a[136:139],v[120:123],v[36:39]
v_exp_f32 v69,v69
v_mfma_f32_16x16x32_bf16 v[36:39],a[124:127],v[116:119],v[36:39]
v_mul_f32 v46,v46,v50
v_mul_f32 v47,v47,v51
v_mfma_f32_16x16x32_bf16 v[36:39],a[112:115],v[112:115],v[36:39]
ds_read_b128 v[96:99],v4 offset:20736
v_mul_f32 v52,v52,v54
v_mfma_f32_16x16x32_bf16 v[36:39],a[100:103],v[108:111],v[36:39]
v_mul_f32 v53,v53,v55
v_fma_f32 v64,v64,v62,v62
v_mfma_f32_16x16x32_bf16 v[40:43],a[104:107],v[108:111],v[40:43]
v_rcp_f32 v64,v64
v_mfma_f32_16x16x32_bf16 v[40:43],a[116:119],v[112:115],v[40:43]
ds_read_b128 v[100:103],v4 offset:21888
v_fma_f32 v65,v65,v62,v62
v_mfma_f32_16x16x32_bf16 v[40:43],a[128:131],v[116:119],v[40:43]
v_rcp_f32 v65,v65
v_mfma_f32_16x16x32_bf16 v[40:43],a[140:143],v[120:123],v[40:43]
v_fma_f32 v66,v66,v62,v62
s_waitcnt lgkmcnt(4)
v_mfma_f32_16x16x32_bf16 v[40:43],a[152:155],v[124:127],v[40:43]
ds_read_b128 v[104:107],v4 offset:23040
v_fma_f32 v67,v67,v62,v62
v_mfma_f32_16x16x32_bf16 v[40:43],a[164:167],v[76:79],v[40:43]
v_rcp_f32 v66,v66
v_mfma_f32_16x16x32_bf16 v[40:43],a[176:179],v[80:83],v[40:43]
v_rcp_f32 v67,v67
v_mfma_f32_16x16x32_bf16 v[40:43],a[188:191],v[84:87],v[40:43]
ds_read_b128 v[108:111],v4 offset:24192
v_fma_f32 v68,v68,v62,v62
v_mfma_f32_16x16x32_bf16 v[36:39],a[184:187],v[84:87],v[36:39]
v_rcp_f32 v68,v68
v_mfma_f32_16x16x32_bf16 v[36:39],a[172:175],v[80:83],v[36:39]
v_fma_f32 v69,v69,v62,v62
v_mul_f32 v44,v44,v64
v_mfma_f32_16x16x32_bf16 v[36:39],a[160:163],v[76:79],v[36:39]
ds_read_b128 v[112:115],v4 offset:25344
v_mul_f32 v45,v45,v65
v_mfma_f32_16x16x32_bf16 v[36:39],a[148:151],v[124:127],v[36:39]
v_rcp_f32 v69,v69
v_mfma_f32_16x16x32_bf16 v[32:35],a[144:147],v[124:127],v[32:35]
v_mul_f32 v46,v46,v66
v_mul_f32 v47,v47,v67
v_mfma_f32_16x16x32_bf16 v[32:35],a[156:159],v[76:79],v[32:35]
ds_read_b128 v[116:119],v4 offset:26496
v_mul_f32 v52,v52,v68
v_mfma_f32_16x16x32_bf16 v[32:35],a[168:171],v[80:83],v[32:35]
v_mul_f32 v53,v53,v69
v_cvt_pk_bf16_f32 v72,v44,v45
v_mfma_f32_16x16x32_bf16 v[32:35],a[180:183],v[84:87],v[32:35]
v_cvt_pk_bf16_f32 v74,v52,v53
s_waitcnt lgkmcnt(4)
v_mfma_f32_16x16x32_bf16 v[32:35],a[192:195],v[88:91],v[32:35]
ds_read_b128 v[120:123],v4 offset:27648
v_cvt_pk_bf16_f32 v73,v46,v47
v_mfma_f32_16x16x32_bf16 v[32:35],a[204:207],v[92:95],v[32:35]
v_permlane32_swap_b32 v74,v75
v_mfma_f32_16x16x32_bf16 v[32:35],a[216:219],v[96:99],v[32:35]
v_add_f32_dpp v56,v56,v56 row_ror:2 row_mask:0xf bank_mask:0xf
v_mfma_f32_16x16x32_bf16 v[32:35],a[228:231],v[100:103],v[32:35]
ds_read_b128 v[124:127],v4 offset:28800
v_mul_f32 v56,0x3a800000,v56
v_mfma_f32_16x16x32_bf16 v[36:39],a[232:235],v[100:103],v[36:39]
v_permlane16_swap_b32 v72,v74
v_mfma_f32_16x16x32_bf16 v[36:39],a[220:223],v[96:99],v[36:39]
v_permlane16_swap_b32 v73,v75
v_mfma_f32_16x16x32_bf16 v[36:39],a[208:211],v[92:95],v[36:39]
buffer_store_dwordx4 v[72:75],v15,s[32:35],s68 offen sc1
ds_read_b128 v[76:79],v4 offset:29952
v_mfma_f32_16x16x32_bf16 v[36:39],a[196:199],v[88:91],v[36:39]
v_mul_f32 v57,v56,v56
v_mfma_f32_16x16x32_bf16 v[40:43],a[200:203],v[88:91],v[40:43]
v_mfma_f32_16x16x32_bf16 v[40:43],a[212:215],v[92:95],v[40:43]
ds_read_b128 v[80:83],v4 offset:31104
v_sub_f32_dpp v57,v56,v57 quad_perm:[1,1,3,3] row_mask:0xf bank_mask:0xf
v_mfma_f32_16x16x32_bf16 v[40:43],a[224:227],v[96:99],v[40:43]
v_max_f32 v57,0,v57
v_mfma_f32_16x16x32_bf16 v[40:43],a[236:239],v[100:103],v[40:43]
v_cvt_pk_bf16_f32 v57,0,v57
s_waitcnt lgkmcnt(4)
v_mfma_f32_16x16x32_bf16 v[40:43],a[248:251],v[104:107],v[40:43]
ds_read_b128 v[84:87],v4 offset:32256
v_add_f32 v57,0x3727c5ac,v57
v_mfma_f32_16x16x32_bf16 v[40:43],v[132:135],v[108:111],v[40:43]
v_cvt_pk_bf16_f32 v57,0,v57
v_mfma_f32_16x16x32_bf16 v[40:43],v[144:147],v[112:115],v[40:43]
v_sqrt_f32 v57,v57
v_mfma_f32_16x16x32_bf16 v[40:43],v[156:159],v[116:119],v[40:43]
ds_read_b128 v[88:91],v4 offset:33408
v_cvt_pk_bf16_f32 v57,v56,v57
v_mfma_f32_16x16x32_bf16 v[36:39],v[152:155],v[116:119],v[36:39]
ds_write_b32 v10,v57 offset:0
v_mfma_f32_16x16x32_bf16 v[36:39],v[140:143],v[112:115],v[36:39]
v_mfma_f32_16x16x32_bf16 v[36:39],v[128:131],v[108:111],v[36:39]
ds_read_b128 v[92:95],v4 offset:34560
v_mfma_f32_16x16x32_bf16 v[36:39],a[244:247],v[104:107],v[36:39]
v_mfma_f32_16x16x32_bf16 v[32:35],a[240:243],v[104:107],v[32:35]
s_waitcnt vmcnt(1)
s_waitcnt lgkmcnt(1)
s_barrier
v_mfma_f32_16x16x32_bf16 v[32:35],a[252:255],v[108:111],v[32:35]
ds_read_b128 v[96:99],v4 offset:35712
v_mfma_f32_16x16x32_bf16 v[32:35],v[136:139],v[112:115],v[32:35]
v_xor_b32 v4,73728,v4
v_mfma_f32_16x16x32_bf16 v[32:35],v[148:151],v[116:119],v[32:35]
v_mfma_f32_16x16x32_bf16 v[32:35],v[160:163],v[120:123],v[32:35]
ds_read_b128 v[100:103],v5 offset:0
s_add_u32 m0,s64,0
v_mfma_f32_16x16x32_bf16 v[32:35],v[172:175],v[124:127],v[32:35]
buffer_load_dwordx4 v2,s[32:35],s63 offen offset:0 sc1 lds
v_mfma_f32_16x16x32_bf16 v[32:35],v[184:187],v[76:79],v[32:35]
ds_read_b32 v62,v11 offset:0
v_mfma_f32_16x16x32_bf16 v[32:35],v[196:199],v[80:83],v[32:35]
ds_read_b128 v[104:107],v5 offset:1152
v_mfma_f32_16x16x32_bf16 v[36:39],v[200:203],v[80:83],v[36:39]
v_mfma_f32_16x16x32_bf16 v[36:39],v[188:191],v[76:79],v[36:39]
buffer_load_dwordx4 v2,s[32:35],s63 offen offset:1024 sc1 lds
v_mfma_f32_16x16x32_bf16 v[36:39],v[176:179],v[124:127],v[36:39]
ds_read_b128 v[108:111],v5 offset:2304
ds_read2st64_b32 v[56:57],v8 offset0:144 offset1:180
v_mfma_f32_16x16x32_bf16 v[36:39],v[164:167],v[120:123],v[36:39]
ds_read2st64_b32 v[58:59],v8 offset0:216 offset1:252
v_xor_b32 v8,v9,v8
v_mfma_f32_16x16x32_bf16 v[40:43],v[168:171],v[120:123],v[40:43]
s_waitcnt lgkmcnt(4)
v_lshlrev_b32 v60,16,v62
v_mfma_f32_16x16x32_bf16 v[40:43],v[180:183],v[124:127],v[40:43]
buffer_load_dwordx4 v2,s[32:35],s63 offen offset:2048 sc1 lds
ds_read_b128 v[112:115],v5 offset:3456
v_mfma_f32_16x16x32_bf16 v[40:43],v[192:195],v[76:79],v[40:43]
v_and_b32 v62,0xffff0000,v62
v_mfma_f32_16x16x32_bf16 v[40:43],v[204:207],v[80:83],v[40:43]
v_mfma_f32_16x16x32_bf16 v[40:43],v[216:219],v[84:87],v[40:43]
ds_read_b128 v[116:119],v5 offset:4608
v_mfma_f32_16x16x32_bf16 v[40:43],v[228:231],v[88:91],v[40:43]
buffer_load_dwordx4 v2,s[32:35],s63 offen offset:3072 sc1 lds
v_mfma_f32_16x16x32_bf16 v[40:43],v[240:243],v[92:95],v[40:43]
v_rcp_f32 v61,v62
v_mfma_f32_16x16x32_bf16 v[40:43],v[252:255],v[96:99],v[40:43]
ds_read_b128 v[120:123],v5 offset:5760
v_mul_f32 v62,v62,v62
v_mfma_f32_16x16x32_bf16 v[36:39],v[248:251],v[96:99],v[36:39]
s_add_u32 m0,s64,4096
s_waitcnt lgkmcnt(3)
v_add_f32 v56,v56,v57
v_mfma_f32_16x16x32_bf16 v[36:39],v[236:239],v[92:95],v[36:39]
buffer_load_dwordx4 v6,s[32:35],s63 offen offset:0 sc1 lds
v_add_f32 v58,v58,v59
v_mfma_f32_16x16x32_bf16 v[36:39],v[224:227],v[88:91],v[36:39]
ds_read_b128 v[124:127],v5 offset:6912
v_add_f32 v56,v56,v58
v_mfma_f32_16x16x32_bf16 v[36:39],v[212:215],v[84:87],v[36:39]
v_mul_f32 v61,0xbfb8aa3b,v61
v_add_f32_dpp v56,v56,v56 row_ror:8 row_mask:0xf bank_mask:0xf
v_mfma_f32_16x16x32_bf16 v[32:35],v[208:211],v[84:87],v[32:35]
v_mfma_f32_16x16x32_bf16 v[32:35],v[220:223],v[88:91],v[32:35]
buffer_load_dwordx4 v6,s[32:35],s63 offen offset:1024 sc1 lds
ds_read_b128 v[76:79],v5 offset:8064
v_mfma_f32_16x16x32_bf16 v[32:35],v[232:235],v[92:95],v[32:35]
v_add_f32_dpp v56,v56,v56 row_ror:4 row_mask:0xf bank_mask:0xf
s_sub_u32 s62,s62,1
v_mfma_f32_16x16x32_bf16 v[32:35],v[244:247],v[96:99],v[32:35]
s_cbranch_scc1 .L_gt0_3
s_waitcnt lgkmcnt(4)
v_mfma_f32_16x16x32_bf16 v[44:47],a[0:3],v[100:103],0
ds_read_b128 v[80:83],v5 offset:9216
s_add_u32 s68,s68,890880
v_mfma_f32_16x16x32_bf16 v[44:47],a[12:15],v[104:107],v[44:47]
buffer_load_dwordx4 v6,s[32:35],s63 offen offset:2048 sc1 lds
v_fma_f32 v40,-v60,v28,v40
v_mfma_f32_16x16x32_bf16 v[44:47],a[24:27],v[108:111],v[44:47]
v_fma_f32 v41,-v60,v29,v41
v_fma_f32 v42,-v60,v30,v42
v_mfma_f32_16x16x32_bf16 v[44:47],a[36:39],v[112:115],v[44:47]
ds_read_b128 v[84:87],v5 offset:10368
v_fma_f32 v43,-v60,v31,v43
v_mfma_f32_16x16x32_bf16 v[48:51],a[40:43],v[112:115],0
v_permlane32_swap_b32 v40,v42
v_mfma_f32_16x16x32_bf16 v[48:51],a[28:31],v[108:111],v[48:51]
buffer_load_dwordx4 v6,s[32:35],s63 offen offset:3072 sc1 lds
v_fma_f32 v36,-v60,v24,v36
v_mfma_f32_16x16x32_bf16 v[48:51],a[16:19],v[104:107],v[48:51]
ds_read_b128 v[88:91],v5 offset:11520
v_fma_f32 v37,-v60,v25,v37
v_mfma_f32_16x16x32_bf16 v[48:51],a[4:7],v[100:103],v[48:51]
v_permlane32_swap_b32 v41,v43
v_mfma_f32_16x16x32_bf16 v[52:55],a[8:11],v[100:103],0
s_waitcnt vmcnt(8)
s_barrier
v_mfma_f32_16x16x32_bf16 v[52:55],a[20:23],v[104:107],v[52:55]
buffer_store_dwordx2 v[16:17],v2,s[92:95],s68 idxen sc1
ds_read_b128 v[92:95],v5 offset:12672
v_mfma_f32_16x16x32_bf16 v[52:55],a[32:35],v[108:111],v[52:55]
v_fma_f32 v38,-v60,v26,v38
v_fma_f32 v39,-v60,v27,v39
v_mfma_f32_16x16x32_bf16 v[52:55],a[44:47],v[112:115],v[52:55]
v_fma_f32 v32,-v60,v20,v32
s_waitcnt lgkmcnt(4)
v_mfma_f32_16x16x32_bf16 v[52:55],a[56:59],v[116:119],v[52:55]
ds_read_b128 v[96:99],v5 offset:13824
s_add_u32 m0,s64,8192
v_mfma_f32_16x16x32_bf16 v[52:55],a[68:71],v[120:123],v[52:55]
buffer_load_dwordx4 v7,s[32:35],s63 offen offset:0 sc1 lds
s_add_u32 s63,s63,890880
v_mfma_f32_16x16x32_bf16 v[52:55],a[80:83],v[124:127],v[52:55]
v_fma_f32 v33,-v60,v21,v33
v_fma_f32 v34,-v60,v22,v34
v_mfma_f32_16x16x32_bf16 v[52:55],a[92:95],v[76:79],v[52:55]
ds_read_b128 v[100:103],v5 offset:14976
v_fma_f32 v35,-v60,v23,v35
v_mfma_f32_16x16x32_bf16 v[48:51],a[88:91],v[76:79],v[48:51]
v_mul_f32 v64,v32,v61
v_mul_f32 v65,v33,v61
v_mfma_f32_16x16x32_bf16 v[48:51],a[76:79],v[124:127],v[48:51]
v_exp_f32 v64,v64
v_mfma_f32_16x16x32_bf16 v[48:51],a[64:67],v[120:123],v[48:51]
ds_read_b128 v[104:107],v5 offset:16128
v_mul_f32 v66,v34,v61
v_mfma_f32_16x16x32_bf16 v[48:51],a[52:55],v[116:119],v[48:51]
v_exp_f32 v65,v65
v_mfma_f32_16x16x32_bf16 v[44:47],a[48:51],v[116:119],v[44:47]
v_exp_f32 v66,v66
v_mfma_f32_16x16x32_bf16 v[44:47],a[60:63],v[120:123],v[44:47]
ds_read_b128 v[108:111],v5 offset:17280
v_mul_f32 v67,v35,v61
v_mfma_f32_16x16x32_bf16 v[44:47],a[72:75],v[124:127],v[44:47]
v_exp_f32 v67,v67
v_mfma_f32_16x16x32_bf16 v[44:47],a[84:87],v[76:79],v[44:47]
v_mul_f32 v68,v40,v61
s_waitcnt lgkmcnt(4)
v_mfma_f32_16x16x32_bf16 v[44:47],a[96:99],v[80:83],v[44:47]
ds_read_b128 v[112:115],v5 offset:18432
v_mul_f32 v69,v41,v61
v_mfma_f32_16x16x32_bf16 v[44:47],a[108:111],v[84:87],v[44:47]
v_exp_f32 v68,v68
v_mfma_f32_16x16x32_bf16 v[44:47],a[120:123],v[88:91],v[44:47]
v_exp_f32 v69,v69
v_mfma_f32_16x16x32_bf16 v[44:47],a[132:135],v[92:95],v[44:47]
ds_read_b128 v[116:119],v5 offset:19584
v_mul_f32 v32,v32,v36
v_mfma_f32_16x16x32_bf16 v[48:51],a[136:139],v[92:95],v[48:51]
v_mul_f32 v33,v33,v37
v_mul_f32 v34,v34,v38
v_mfma_f32_16x16x32_bf16 v[48:51],a[124:127],v[88:91],v[48:51]
v_mul_f32 v35,v35,v39
v_mul_f32 v40,v40,v42
v_mfma_f32_16x16x32_bf16 v[48:51],a[112:115],v[84:87],v[48:51]
ds_read_b128 v[120:123],v5 offset:20736
v_mul_f32 v41,v41,v43
v_mfma_f32_16x16x32_bf16 v[48:51],a[100:103],v[80:83],v[48:51]
v_fma_f32 v64,v64,v62,v62
v_fma_f32 v65,v65,v62,v62
v_mfma_f32_16x16x32_bf16 v[52:55],a[104:107],v[80:83],v[52:55]
v_rcp_f32 v64,v64
v_mfma_f32_16x16x32_bf16 v[52:55],a[116:119],v[84:87],v[52:55]
ds_read_b128 v[124:127],v5 offset:21888
v_fma_f32 v66,v66,v62,v62
v_mfma_f32_16x16x32_bf16 v[52:55],a[128:131],v[88:91],v[52:55]
v_rcp_f32 v65,v65
v_mfma_f32_16x16x32_bf16 v[52:55],a[140:143],v[92:95],v[52:55]
v_fma_f32 v67,v67,v62,v62
s_waitcnt lgkmcnt(4)
v_mfma_f32_16x16x32_bf16 v[52:55],a[152:155],v[96:99],v[52:55]
ds_read_b128 v[76:79],v5 offset:23040
v_fma_f32 v68,v68,v62,v62
v_mfma_f32_16x16x32_bf16 v[52:55],a[164:167],v[100:103],v[52:55]
v_rcp_f32 v66,v66
v_mfma_f32_16x16x32_bf16 v[52:55],a[176:179],v[104:107],v[52:55]
v_rcp_f32 v67,v67
v_mfma_f32_16x16x32_bf16 v[52:55],a[188:191],v[108:111],v[52:55]
ds_read_b128 v[80:83],v5 offset:24192
v_fma_f32 v69,v69,v62,v62
v_mfma_f32_16x16x32_bf16 v[48:51],a[184:187],v[108:111],v[48:51]
v_rcp_f32 v68,v68
v_mfma_f32_16x16x32_bf16 v[48:51],a[172:175],v[104:107],v[48:51]
v_rcp_f32 v69,v69
v_mfma_f32_16x16x32_bf16 v[48:51],a[160:163],v[100:103],v[48:51]
ds_read_b128 v[84:87],v5 offset:25344
v_mul_f32 v32,v32,v64
v_mfma_f32_16x16x32_bf16 v[48:51],a[148:151],v[96:99],v[48:51]
v_mul_f32 v33,v33,v65
v_mul_f32 v34,v34,v66
v_mfma_f32_16x16x32_bf16 v[44:47],a[144:147],v[96:99],v[44:47]
v_mul_f32 v35,v35,v67
v_mul_f32 v40,v40,v68
v_mfma_f32_16x16x32_bf16 v[44:47],a[156:159],v[100:103],v[44:47]
ds_read_b128 v[88:91],v5 offset:26496
v_mul_f32 v41,v41,v69
v_mfma_f32_16x16x32_bf16 v[44:47],a[168:171],v[104:107],v[44:47]
v_cvt_pk_bf16_f32 v74,v40,v41
v_cvt_pk_bf16_f32 v72,v32,v33
v_mfma_f32_16x16x32_bf16 v[44:47],a[180:183],v[108:111],v[44:47]
v_cvt_pk_bf16_f32 v73,v34,v35
s_waitcnt lgkmcnt(4)
v_mfma_f32_16x16x32_bf16 v[44:47],a[192:195],v[112:115],v[44:47]
ds_read_b128 v[92:95],v5 offset:27648
v_add_f32_dpp v56,v56,v56 row_ror:2 row_mask:0xf bank_mask:0xf
v_mfma_f32_16x16x32_bf16 v[44:47],a[204:207],v[116:119],v[44:47]
v_permlane32_swap_b32 v74,v75
v_mfma_f32_16x16x32_bf16 v[44:47],a[216:219],v[120:123],v[44:47]
v_mul_f32 v56,0x3a800000,v56
v_mfma_f32_16x16x32_bf16 v[44:47],a[228:231],v[124:127],v[44:47]
ds_read_b128 v[96:99],v5 offset:28800
v_mul_f32 v57,v56,v56
v_mfma_f32_16x16x32_bf16 v[48:51],a[232:235],v[124:127],v[48:51]
v_permlane16_swap_b32 v72,v74
v_mfma_f32_16x16x32_bf16 v[48:51],a[220:223],v[120:123],v[48:51]
v_permlane16_swap_b32 v73,v75
v_mfma_f32_16x16x32_bf16 v[48:51],a[208:211],v[116:119],v[48:51]
buffer_store_dwordx4 v[72:75],v15,s[32:35],s68 offen sc1
ds_read_b128 v[100:103],v5 offset:29952
v_mfma_f32_16x16x32_bf16 v[48:51],a[196:199],v[112:115],v[48:51]
v_sub_f32_dpp v57,v56,v57 quad_perm:[1,1,3,3] row_mask:0xf bank_mask:0xf
v_mfma_f32_16x16x32_bf16 v[52:55],a[200:203],v[112:115],v[52:55]
v_max_f32 v57,0,v57
v_mfma_f32_16x16x32_bf16 v[52:55],a[212:215],v[116:119],v[52:55]
ds_read_b128 v[104:107],v5 offset:31104
v_cvt_pk_bf16_f32 v57,0,v57
v_mfma_f32_16x16x32_bf16 v[52:55],a[224:227],v[120:123],v[52:55]
v_add_f32 v57,0x3727c5ac,v57
v_mfma_f32_16x16x32_bf16 v[52:55],a[236:239],v[124:127],v[52:55]
v_cvt_pk_bf16_f32 v57,0,v57
s_waitcnt lgkmcnt(4)
v_mfma_f32_16x16x32_bf16 v[52:55],a[248:251],v[76:79],v[52:55]
ds_read_b128 v[108:111],v5 offset:32256
v_mfma_f32_16x16x32_bf16 v[52:55],v[132:135],v[80:83],v[52:55]
v_sqrt_f32 v57,v57
v_mfma_f32_16x16x32_bf16 v[52:55],v[144:147],v[84:87],v[52:55]
v_cvt_pk_bf16_f32 v57,v56,v57
v_mfma_f32_16x16x32_bf16 v[52:55],v[156:159],v[88:91],v[52:55]
ds_read_b128 v[112:115],v5 offset:33408
ds_write_b32 v10,v57 offset:128
v_mfma_f32_16x16x32_bf16 v[48:51],v[152:155],v[88:91],v[48:51]
v_mfma_f32_16x16x32_bf16 v[48:51],v[140:143],v[84:87],v[48:51]
v_mfma_f32_16x16x32_bf16 v[48:51],v[128:131],v[80:83],v[48:51]
ds_read_b128 v[116:119],v5 offset:34560
v_mfma_f32_16x16x32_bf16 v[48:51],a[244:247],v[76:79],v[48:51]
v_mfma_f32_16x16x32_bf16 v[44:47],a[240:243],v[76:79],v[44:47]
s_waitcnt vmcnt(1)
s_waitcnt lgkmcnt(1)
s_barrier
v_mfma_f32_16x16x32_bf16 v[44:47],a[252:255],v[80:83],v[44:47]
ds_read_b128 v[120:123],v5 offset:35712
v_mfma_f32_16x16x32_bf16 v[44:47],v[136:139],v[84:87],v[44:47]
v_xor_b32 v5,73728,v5
v_mfma_f32_16x16x32_bf16 v[44:47],v[148:151],v[88:91],v[44:47]
v_mfma_f32_16x16x32_bf16 v[44:47],v[160:163],v[92:95],v[44:47]
ds_read_b128 v[76:79],v4 offset:0
s_add_u32 m0,s64,36864
v_mfma_f32_16x16x32_bf16 v[44:47],v[172:175],v[96:99],v[44:47]
buffer_load_dwordx4 v2,s[32:35],s63 offen offset:0 sc1 lds
v_mfma_f32_16x16x32_bf16 v[44:47],v[184:187],v[100:103],v[44:47]
ds_read_b32 v62,v11 offset:128
v_mfma_f32_16x16x32_bf16 v[44:47],v[196:199],v[104:107],v[44:47]
ds_read_b128 v[80:83],v4 offset:1152
v_mfma_f32_16x16x32_bf16 v[48:51],v[200:203],v[104:107],v[48:51]
v_mfma_f32_16x16x32_bf16 v[48:51],v[188:191],v[100:103],v[48:51]
buffer_load_dwordx4 v2,s[32:35],s63 offen offset:1024 sc1 lds
v_mfma_f32_16x16x32_bf16 v[48:51],v[176:179],v[96:99],v[48:51]
ds_read_b128 v[84:87],v4 offset:2304
ds_read2st64_b32 v[56:57],v8 offset0:0 offset1:36
v_mfma_f32_16x16x32_bf16 v[48:51],v[164:167],v[92:95],v[48:51]
ds_read2st64_b32 v[58:59],v8 offset0:72 offset1:108
v_mfma_f32_16x16x32_bf16 v[52:55],v[168:171],v[92:95],v[52:55]
s_waitcnt lgkmcnt(4)
v_lshlrev_b32 v60,16,v62
v_mfma_f32_16x16x32_bf16 v[52:55],v[180:183],v[96:99],v[52:55]
buffer_load_dwordx4 v2,s[32:35],s63 offen offset:2048 sc1 lds
ds_read_b128 v[88:91],v4 offset:3456
v_mfma_f32_16x16x32_bf16 v[52:55],v[192:195],v[100:103],v[52:55]
v_and_b32 v62,0xffff0000,v62
v_mfma_f32_16x16x32_bf16 v[52:55],v[204:207],v[104:107],v[52:55]
v_mfma_f32_16x16x32_bf16 v[52:55],v[216:219],v[108:111],v[52:55]
ds_read_b128 v[92:95],v4 offset:4608
v_mfma_f32_16x16x32_bf16 v[52:55],v[228:231],v[112:115],v[52:55]
buffer_load_dwordx4 v2,s[32:35],s63 offen offset:3072 sc1 lds
v_mfma_f32_16x16x32_bf16 v[52:55],v[240:243],v[116:119],v[52:55]
v_rcp_f32 v61,v62
)#"
R"#(
v_mfma_f32_16x16x32_bf16 v[52:55],v[252:255],v[120:123],v[52:55]
ds_read_b128 v[96:99],v4 offset:5760
v_mul_f32 v62,v62,v62
v_mfma_f32_16x16x32_bf16 v[48:51],v[248:251],v[120:123],v[48:51]
s_add_u32 m0,s64,40960
s_waitcnt lgkmcnt(3)
v_add_f32 v56,v56,v57
v_mfma_f32_16x16x32_bf16 v[48:51],v[236:239],v[116:119],v[48:51]
buffer_load_dwordx4 v6,s[32:35],s63 offen offset:0 sc1 lds
v_add_f32 v58,v58,v59
v_mfma_f32_16x16x32_bf16 v[48:51],v[224:227],v[112:115],v[48:51]
ds_read_b128 v[100:103],v4 offset:6912
v_add_f32 v56,v56,v58
v_mfma_f32_16x16x32_bf16 v[48:51],v[212:215],v[108:111],v[48:51]
v_mul_f32 v61,0xbfb8aa3b,v61
v_add_f32_dpp v56,v56,v56 row_ror:8 row_mask:0xf bank_mask:0xf
v_mfma_f32_16x16x32_bf16 v[44:47],v[208:211],v[108:111],v[44:47]
v_mfma_f32_16x16x32_bf16 v[44:47],v[220:223],v[112:115],v[44:47]
buffer_load_dwordx4 v6,s[32:35],s63 offen offset:1024 sc1 lds
ds_read_b128 v[104:107],v4 offset:8064
v_mfma_f32_16x16x32_bf16 v[44:47],v[232:235],v[116:119],v[44:47]
v_add_f32_dpp v56,v56,v56 row_ror:4 row_mask:0xf bank_mask:0xf
s_sub_u32 s62,s62,1
v_mfma_f32_16x16x32_bf16 v[44:47],v[244:247],v[120:123],v[44:47]
s_cbranch_scc0 .L_gl_3
s_nop 1
s_add_u32 s68,s68,890880
s_waitcnt vmcnt(6)
s_barrier
buffer_store_dwordx2 v[16:17],v2,s[92:95],s68 idxen sc1
v_fma_f32 v52,-v60,v28,v52
v_fma_f32 v53,-v60,v29,v53
v_fma_f32 v54,-v60,v30,v54
v_fma_f32 v55,-v60,v31,v55
v_fma_f32 v48,-v60,v24,v48
v_fma_f32 v49,-v60,v25,v49
v_fma_f32 v50,-v60,v26,v50
v_fma_f32 v51,-v60,v27,v51
v_fma_f32 v44,-v60,v20,v44
v_fma_f32 v45,-v60,v21,v45
v_fma_f32 v46,-v60,v22,v46
v_fma_f32 v47,-v60,v23,v47
v_permlane32_swap_b32 v52,v54
v_permlane32_swap_b32 v53,v55
v_mul_f32 v64,v44,v61
v_mul_f32 v65,v45,v61
v_mul_f32 v66,v46,v61
v_mul_f32 v67,v47,v61
v_mul_f32 v68,v52,v61
v_mul_f32 v69,v53,v61
v_exp_f32 v64,v64
v_exp_f32 v65,v65
v_mul_f32 v44,v44,v48
v_mul_f32 v45,v45,v49
v_exp_f32 v66,v66
v_exp_f32 v67,v67
v_mul_f32 v46,v46,v50
v_mul_f32 v47,v47,v51
v_exp_f32 v68,v68
v_exp_f32 v69,v69
v_mul_f32 v52,v52,v54
v_mul_f32 v53,v53,v55
v_fma_f32 v64,v64,v62,v62
v_fma_f32 v65,v65,v62,v62
v_rcp_f32 v64,v64
v_rcp_f32 v65,v65
v_fma_f32 v66,v66,v62,v62
v_fma_f32 v67,v67,v62,v62
v_rcp_f32 v66,v66
v_rcp_f32 v67,v67
v_fma_f32 v68,v68,v62,v62
v_fma_f32 v69,v69,v62,v62
v_rcp_f32 v68,v68
v_rcp_f32 v69,v69
v_mul_f32 v44,v44,v64
v_mul_f32 v45,v45,v65
v_mul_f32 v46,v46,v66
v_mul_f32 v47,v47,v67
v_mul_f32 v52,v52,v68
v_mul_f32 v53,v53,v69
v_cvt_pk_bf16_f32 v74,v52,v53
v_cvt_pk_bf16_f32 v72,v44,v45
v_cvt_pk_bf16_f32 v73,v46,v47
v_permlane32_swap_b32 v74,v75
s_nop 1
v_permlane16_swap_b32 v72,v74
v_permlane16_swap_b32 v73,v75
buffer_store_dwordx4 v[72:75],v15,s[32:35],s68 offen sc1
s_add_u32 s68,s68,890880
s_waitcnt vmcnt(0)
s_barrier
buffer_store_dwordx2 v[16:17],v2,s[92:95],s68 idxen sc1
s_branch .L_end
.L_gt0_3:
s_nop 1
s_add_u32 s68,s68,890880
s_waitcnt vmcnt(6)
s_barrier
buffer_store_dwordx2 v[16:17],v2,s[92:95],s68 idxen sc1
v_fma_f32 v40,-v60,v28,v40
v_fma_f32 v41,-v60,v29,v41
v_fma_f32 v42,-v60,v30,v42
v_fma_f32 v43,-v60,v31,v43
v_fma_f32 v36,-v60,v24,v36
v_fma_f32 v37,-v60,v25,v37
v_fma_f32 v38,-v60,v26,v38
v_fma_f32 v39,-v60,v27,v39
v_fma_f32 v32,-v60,v20,v32
v_fma_f32 v33,-v60,v21,v33
v_fma_f32 v34,-v60,v22,v34
v_fma_f32 v35,-v60,v23,v35
v_permlane32_swap_b32 v40,v42
v_permlane32_swap_b32 v41,v43
v_mul_f32 v64,v32,v61
v_mul_f32 v65,v33,v61
v_mul_f32 v66,v34,v61
v_mul_f32 v67,v35,v61
v_mul_f32 v68,v40,v61
v_mul_f32 v69,v41,v61
v_exp_f32 v64,v64
v_exp_f32 v65,v65
v_mul_f32 v32,v32,v36
v_mul_f32 v33,v33,v37
v_exp_f32 v66,v66
v_exp_f32 v67,v67
v_mul_f32 v34,v34,v38
v_mul_f32 v35,v35,v39
v_exp_f32 v68,v68
v_exp_f32 v69,v69
v_mul_f32 v40,v40,v42
v_mul_f32 v41,v41,v43
v_fma_f32 v64,v64,v62,v62
v_fma_f32 v65,v65,v62,v62
v_rcp_f32 v64,v64
v_rcp_f32 v65,v65
v_fma_f32 v66,v66,v62,v62
v_fma_f32 v67,v67,v62,v62
v_rcp_f32 v66,v66
v_rcp_f32 v67,v67
v_fma_f32 v68,v68,v62,v62
v_fma_f32 v69,v69,v62,v62
v_rcp_f32 v68,v68
v_rcp_f32 v69,v69
v_mul_f32 v32,v32,v64
v_mul_f32 v33,v33,v65
v_mul_f32 v34,v34,v66
v_mul_f32 v35,v35,v67
v_mul_f32 v40,v40,v68
v_mul_f32 v41,v41,v69
v_cvt_pk_bf16_f32 v74,v40,v41
v_cvt_pk_bf16_f32 v72,v32,v33
v_cvt_pk_bf16_f32 v73,v34,v35
v_permlane32_swap_b32 v74,v75
s_nop 1
v_permlane16_swap_b32 v72,v74
v_permlane16_swap_b32 v73,v75
buffer_store_dwordx4 v[72:75],v15,s[32:35],s68 offen sc1
s_add_u32 s68,s68,890880
s_waitcnt vmcnt(0)
s_barrier
buffer_store_dwordx2 v[16:17],v2,s[92:95],s68 idxen sc1
s_branch .L_end
.L_q:
v_mov_b32 v16,0x3f803f80
v_mov_b32 v17,0x3f803f80
v_mov_b32 v18,0x3f803f80
v_mov_b32 v19,0x3f803f80
s_add_u32 s36,s10,0x0
s_addc_u32 s37,s11,0x0
s_and_b32 s37,s37,0xffff
s_mov_b32 s38,0x600000
s_mov_b32 s39,0x20000
s_add_u32 s40,s10,0x400
s_addc_u32 s41,s11,0x0
s_and_b32 s41,s41,0xffff
s_mov_b32 s42,0x5ffc00
s_mov_b32 s43,0x20000
s_add_u32 s44,s10,0x800
s_addc_u32 s45,s11,0x0
s_and_b32 s45,s45,0xffff
s_mov_b32 s46,0x5ff800
s_mov_b32 s47,0x20000
s_lshl_b32 s96,s27,7
v_and_b32 v68,3,v1
v_mul_u32_u24 v68,8,v68
v_and_b32 v72,1,v1
v_bfe_u32 v3,v1,1,1
v_lshl_add_u32 v72,v72,1,v3
v_mul_u32_u24 v72,8,v72
s_lshl_b32 s48,s26,5
v_add_u32 v68,s48,v68
v_add_u32 v72,s48,v72
s_add_u32 s12,s8,0x0
s_addc_u32 s13,s9,0x0
s_and_b32 s13,s13,0xffff
s_mov_b32 s14,0x400000
s_mov_b32 s15,0x20000
v_lshrrev_b32 v57,3,v1
s_and_b32 s48,s26,1
v_xor_b32 v3,v57,v1
v_xor_b32 v3,s48,v3
v_and_b32 v3,7,v3
v_lshlrev_b32 v57,11,v57
v_lshl_add_u32 v57,v3,4,v57
s_lshl_b32 s16,s28,20
s_sub_u32 s16,s16,65536
s_and_b32 s16,s16,0x3fffff
s_lshl_b32 s17,s26,14
s_add_u32 s16,s16,s17
s_lshl_b32 s17,s26,10
v_lshrrev_b32 v64,3,v1
v_bfe_u32 v65,v1,4,1
s_and_b32 s48,s26,1
s_lshl_b32 s48,s48,1
v_or_b32 v65,s48,v65
v_lshlrev_b32 v65,1,v65
v_and_b32 v66,7,v1
v_xor_b32 v65,v65,v66
v_mul_u32_u24 v64,6144,v64
v_lshl_add_u32 v67,v65,4,v64
s_mul_i32 s48,s26,49152
s_add_u32 s96,s96,s48
s_mul_i32 s97,s26,3072
s_add_u32 m0,s17,24576
s_nop 0
buffer_load_dwordx4 v57,s[12:15],s16 offen offset:0 lds
s_add_u32 m0,s97,0
s_nop 0
buffer_load_dwordx4 v67,s[36:39],s96 offen offset:0 lds
buffer_load_dwordx4 v67,s[40:43],s96 offen offset:1024 lds
buffer_load_dwordx4 v67,s[44:47],s96 offen offset:2048 lds
s_add_u32 s96,s96,196608
s_add_u32 m0,s97,12288
s_nop 0
buffer_load_dwordx4 v67,s[36:39],s96 offen offset:0 lds
buffer_load_dwordx4 v67,s[40:43],s96 offen offset:1024 lds
buffer_load_dwordx4 v67,s[44:47],s96 offen offset:2048 lds
s_add_u32 s96,s96,196608
s_add_u32 m0,s17,28544
s_nop 0
buffer_load_dwordx4 v57,s[12:15],s16 offen offset:128 lds
s_add_u32 m0,s97,139264
s_nop 0
buffer_load_dwordx4 v67,s[36:39],s96 offen offset:0 lds
buffer_load_dwordx4 v67,s[40:43],s96 offen offset:1024 lds
buffer_load_dwordx4 v67,s[44:47],s96 offen offset:2048 lds
s_add_u32 s96,s96,196608
s_add_u32 m0,s97,151552
s_nop 0
buffer_load_dwordx4 v67,s[36:39],s96 offen offset:0 lds
buffer_load_dwordx4 v67,s[40:43],s96 offen offset:1024 lds
buffer_load_dwordx4 v67,s[44:47],s96 offen offset:2048 lds
s_add_u32 s96,s96,196608
s_add_u32 m0,s17,32512
s_nop 0
buffer_load_dwordx4 v57,s[12:15],s16 offen offset:256 lds
s_add_u32 m0,s97,90112
s_nop 0
buffer_load_dwordx4 v67,s[36:39],s96 offen offset:0 lds
buffer_load_dwordx4 v67,s[40:43],s96 offen offset:1024 lds
buffer_load_dwordx4 v67,s[44:47],s96 offen offset:2048 lds
s_add_u32 s96,s96,196608
s_add_u32 m0,s97,102400
s_nop 0
buffer_load_dwordx4 v67,s[36:39],s96 offen offset:0 lds
buffer_load_dwordx4 v67,s[40:43],s96 offen offset:1024 lds
buffer_load_dwordx4 v67,s[44:47],s96 offen offset:2048 lds
s_add_u32 s96,s96,196608
v_lshrrev_b32 v65,4,v1
v_bfe_u32 v64,v1,3,1
v_and_b32 v66,1,v65
v_lshl_add_u32 v64,v66,1,v64
v_lshlrev_b32 v64,5,v64
v_mul_u32_u24 v65,3072,v65
v_bfe_u32 v66,v1,2,2
v_lshl_add_u32 v65,v66,7,v65
v_xor_b32 v68,v64,v68
v_add_u32 v68,v65,v68
v_add_u32 v69,139264,v68
v_add_u32 v70,90112,v68
v_add_u32 v71,114688,v68
v_xor_b32 v72,v64,v72
v_add_u32 v72,v65,v72
v_add_u32 v73,139264,v72
v_add_u32 v74,90112,v72
v_add_u32 v75,114688,v72
s_lshl2_add_u32 s48,s27,s26
s_lshl_b32 s48,s48,9
s_add_u32 s68,s48,4648960
s_lshl2_add_u32 s49,s27,32
s_waitcnt lgkmcnt(0)
s_add_u32 s54,s52,0x7f4a7c15
s_addc_u32 s55,s53,0x9e3779b9
s_lshl_b32 s67,s49,5
s_add_u32 s67,s67,4898816
s_sub_u32 s51,s67,s68
s_add_u32 s92,s32,s51
s_addc_u32 s93,s33,0x10000
s_cmp_eq_u32 s26,0
s_cselect_b32 s94,1,0
s_mov_b32 s95,0x20000
v_mov_b32 v30,s54
v_mov_b32 v31,s55
v_cmp_gt_u32 vcc,12,v1
v_mul_u32_u24 v3,296960,v1
v_mov_b32 v12,0x7fff0000
v_cndmask_b32 v3,v12,v3,vcc
s_add_u32 s51,s67,76021760
buffer_store_dwordx2 v[30:31],v3,s[32:35],s51 offen sc1
s_sub_u32 s67,s68,296960
s_mul_i32 s49,s28,296960
s_add_u32 s68,s68,s49
s_sub_u32 s68,s68,2375680
s_mov_b32 s62,31
s_add_u32 s48,s28,3
s_and_b32 s48,s48,3
s_mul_i32 s48,s48,296960
s_add_u32 s66,s48,41721856
s_sub_u32 s48,s48,s49
s_add_u32 s69,s48,38010880
s_sub_u32 s70,0xfdce2000,s48
s_mov_b32 s72,s70
s_mov_b32 s63,0xfffba000
s_mul_i32 s64,s26,9216
s_add_u32 s65,s64,73728
s_xor_b32 s65,s65,s64
s_add_u32 s63,s63,s64
v_mov_b32 v4,v2
v_add_u32 v5,36864,v2
s_mov_b32 s71,s63
v_bfe_u32 v3,v1,1,3
v_mul_u32_u24 v6,1152,v3
v_lshrrev_b32 v3,1,v3
v_add_u32 v3,s26,v3
v_lshrrev_b32 v7,4,v1
v_lshl_add_u32 v3,v3,2,v7
v_and_b32 v3,15,v3
v_lshl_add_u32 v6,v3,2,v6
v_and_b32 v3,1,v1
v_lshl_add_u32 v6,v3,6,v6
v_add_u32 v6,1024,v6
s_lshl_b32 s48,s26,2
v_add_u32 v8,s48,v7
v_and_b32 v9,15,v1
v_add_u32 v3,64,v1
v_cmp_eq_u32 vcc,0,v9
v_add_u32 v7,73728,v6
v_xor_b32 v7,v7,v6
v_cndmask_b32 v8,v3,v8,vcc
v_lshlrev_b32 v8,2,v8
v_add_u32 v8,147456,v8
v_lshlrev_b32 v9,2,v9
v_add_u32 v9,147456,v9
v_lshrrev_b32 v10,4,v1
v_and_b32 v32,15,v1
v_and_b32 v33,1,v1
v_lshl_add_u32 v34,v10,2,v33
v_lshlrev_b32 v33,4,v33
v_mov_b32 v35,0xffff
v_lshlrev_b32 v33,v33,v35
v_cmp_le_u32 vcc,v34,v32
v_add_u32 v34,2,v34
s_nop 0
v_cndmask_b32 v124,0,v33,vcc
v_xor_b32 v126,v124,v33
v_cmp_le_u32 vcc,v34,v32
s_nop 1
v_cndmask_b32 v125,0,v33,vcc
v_xor_b32 v127,v125,v33
v_lshlrev_b32 v10,4,v10
v_add_u32 v10,147456,v10
s_lshl_b32 s48,s26,10
s_add_u32 s48,s48,148480
v_lshlrev_b32 v11,3,v1
v_add_u32 v11,s48,v11
s_lshl_b32 s48,s26,3
s_add_u32 s48,s48,0
v_add_u32 v0,s48,v1
v_lshrrev_b32 v0,2,v0
v_lshlrev_b32 v0,7,v0
v_cmp_gt_u32 vcc,8,v1
v_mov_b32 v3,0x7fff0000
s_nop 0
v_cndmask_b32 v0,v3,v0,vcc
v_cndmask_b32 v30,0,v30,vcc
v_cndmask_b32 v31,0,v31,vcc
v_mov_b32 v116,0xff61b1e6
v_mov_b32 v117,0xff61b1e6
v_mov_b32 v118,0xff61b1e6
v_mov_b32 v119,0xff61b1e6
v_mov_b32 v120,0xff61b1e6
v_mov_b32 v121,0xff61b1e6
v_mov_b32 v122,0xff61b1e6
v_mov_b32 v123,0xff61b1e6
v_cmp_ne_u32 vcc,0xffff,v124
s_nop 1
v_cndmask_b32 v116,0,v116,vcc
v_cmp_ne_u32 vcc,0xffff0000,v124
s_nop 1
v_cndmask_b32 v117,0,v117,vcc
v_cmp_ne_u32 vcc,0xffff,v125
s_nop 1
v_cndmask_b32 v118,0,v118,vcc
v_cmp_ne_u32 vcc,0xffff0000,v125
s_nop 1
v_cndmask_b32 v119,0,v119,vcc
v_cmp_ne_u32 vcc,0xffff,v126
s_nop 1
v_cndmask_b32 v120,0,v120,vcc
v_cmp_ne_u32 vcc,0xffff0000,v126
s_nop 1
v_cndmask_b32 v121,0,v121,vcc
v_cmp_ne_u32 vcc,0xffff,v127
s_nop 1
v_cndmask_b32 v122,0,v122,vcc
v_cmp_ne_u32 vcc,0xffff0000,v127
s_nop 1
v_cndmask_b32 v123,0,v123,vcc
v_bfe_u32 v3,v1,3,2
v_mul_u32_u24 v56,1187840,v3
v_cmp_eq_u32 vcc,3,v3
s_sub_u32 s48,0,s70
v_mov_b32 v3,s48
v_cndmask_b32 v56,v56,v3,vcc
v_and_b32 v3,7,v1
v_lshl_add_u32 v56,v3,7,v56
v_bfe_u32 v3,v1,3,1
v_lshrrev_b32 v58,4,v1
v_xor_b32 v3,v3,v58
v_xor_b32 v3,v3,v1
v_and_b32 v3,7,v3
v_and_b32 v58,15,v1
v_lshlrev_b32 v58,7,v58
v_lshl_add_u32 v58,v3,4,v58
v_xor_b32 v59,64,v58
v_add_u32 v58,24576,v58
v_add_u32 v59,24576,v59
v_cmp_gt_u32 vcc,32,v1
v_mov_b32 v1,0x7fff0000
s_nop 0
v_cndmask_b32 v1,v1,v2,vcc
s_mov_b32 s58,0x4003c000
s_mov_b32 s59,0x0
s_mov_b32 s60,0x80028000
s_mov_b32 s61,0x0
v_subrev_u32 v3,148480,v11
v_xor_b32 v3,1024,v3
v_add_u32 v3,148480,v3
s_add_u32 s74,s66,s72
s_sub_u32 s74,s66,286720
s_add_u32 s66,s66,s72
s_mov_b32 s72,1187840
s_waitcnt vmcnt(15)
s_barrier
s_add_u32 m0,s17,36480
s_nop 0
buffer_load_dwordx4 v57,s[12:15],s16 offen offset:384 lds
ds_read_b64_tr_b16 a[0:1],v68 offset:0
ds_read_b64_tr_b16 a[2:3],v68 offset:512
s_add_u32 m0,s97,114688
s_nop 0
buffer_load_dwordx4 v67,s[36:39],s96 offen offset:0 lds
ds_read_b64_tr_b16 a[4:5],v68 offset:1024
ds_read_b64_tr_b16 a[6:7],v68 offset:1536
buffer_load_dwordx4 v67,s[40:43],s96 offen offset:1024 lds
ds_read_b64_tr_b16 a[8:9],v72 offset:2048
ds_read_b64_tr_b16 a[10:11],v72 offset:2560
buffer_load_dwordx4 v67,s[44:47],s96 offen offset:2048 lds
ds_read_b64_tr_b16 a[12:13],v68 offset:12288
ds_read_b64_tr_b16 a[14:15],v68 offset:12800
ds_read_b64_tr_b16 a[16:17],v68 offset:13312
s_add_u32 s96,s96,196608
s_add_u32 m0,s97,126976
s_nop 0
buffer_load_dwordx4 v67,s[36:39],s96 offen offset:0 lds
ds_read_b64_tr_b16 a[18:19],v68 offset:13824
ds_read_b64_tr_b16 a[20:21],v72 offset:14336
buffer_load_dwordx4 v67,s[40:43],s96 offen offset:1024 lds
ds_read_b64_tr_b16 a[22:23],v72 offset:14848
ds_read_b128 v[76:79],v58 offset:0
buffer_load_dwordx4 v67,s[44:47],s96 offen offset:2048 lds
ds_read_b128 v[80:83],v58 offset:2048
ds_read_b128 v[84:87],v59 offset:0
ds_read_b128 v[88:91],v59 offset:2048
s_add_u32 s96,s96,196608
s_waitcnt lgkmcnt(0)
s_waitcnt vmcnt(15)
s_barrier
s_add_u32 m0,s17,40448
s_nop 0
buffer_load_dwordx4 v57,s[12:15],s16 offen offset:512 lds
ds_read_b64_tr_b16 a[24:25],v69 offset:0
ds_read_b64_tr_b16 a[26:27],v69 offset:512
v_mfma_f32_16x16x32_bf16 v[20:23],a[0:3],v[16:19],0
v_mfma_f32_16x16x32_bf16 v[24:27],a[4:7],v[16:19],0
s_add_u32 m0,s97,0
s_nop 0
buffer_load_dwordx4 v67,s[36:39],s96 offen offset:0 lds
ds_read_b64_tr_b16 a[28:29],v69 offset:1024
ds_read_b64_tr_b16 a[30:31],v69 offset:1536
)#"
R"#(
v_mfma_f32_16x16x32_bf16 v[32:35],v[16:19],a[8:11],0
v_mfma_f32_16x16x32_bf16 v[36:39],a[4:7],v[76:79],0
buffer_load_dwordx4 v67,s[40:43],s96 offen offset:1024 lds
ds_read_b64_tr_b16 a[32:33],v73 offset:2048
ds_read_b64_tr_b16 a[34:35],v73 offset:2560
v_mfma_f32_16x16x32_bf16 v[40:43],v[76:79],a[8:11],0
v_mfma_f32_16x16x32_bf16 v[44:47],a[0:3],v[80:83],0
buffer_load_dwordx4 v67,s[44:47],s96 offen offset:2048 lds
ds_read_b64_tr_b16 a[36:37],v69 offset:12288
ds_read_b64_tr_b16 a[38:39],v69 offset:12800
ds_read_b64_tr_b16 a[40:41],v69 offset:13312
v_mfma_f32_16x16x32_bf16 v[48:51],a[4:7],v[80:83],0
v_mfma_f32_16x16x32_bf16 v[52:55],v[80:83],a[8:11],0
v_mfma_f32_16x16x32_bf16 v[20:23],a[12:15],v[16:19],v[20:23]
s_add_u32 s96,s96,196608
s_add_u32 m0,s97,12288
s_nop 0
buffer_load_dwordx4 v67,s[36:39],s96 offen offset:0 lds
ds_read_b64_tr_b16 a[42:43],v69 offset:13824
ds_read_b64_tr_b16 a[44:45],v73 offset:14336
v_mfma_f32_16x16x32_bf16 v[24:27],a[16:19],v[16:19],v[24:27]
v_mfma_f32_16x16x32_bf16 v[32:35],v[16:19],a[20:23],v[32:35]
buffer_load_dwordx4 v67,s[40:43],s96 offen offset:1024 lds
ds_read_b64_tr_b16 a[46:47],v73 offset:14848
ds_read_b128 v[92:95],v58 offset:4096
v_mfma_f32_16x16x32_bf16 v[36:39],a[16:19],v[84:87],v[36:39]
v_mfma_f32_16x16x32_bf16 v[40:43],v[84:87],a[20:23],v[40:43]
buffer_load_dwordx4 v67,s[44:47],s96 offen offset:2048 lds
ds_read_b128 v[96:99],v58 offset:6144
ds_read_b128 v[100:103],v59 offset:4096
ds_read_b128 v[104:107],v59 offset:6144
v_mfma_f32_16x16x32_bf16 v[44:47],a[12:15],v[88:91],v[44:47]
v_mfma_f32_16x16x32_bf16 v[48:51],a[16:19],v[88:91],v[48:51]
v_mfma_f32_16x16x32_bf16 v[52:55],v[88:91],a[20:23],v[52:55]
s_add_u32 s96,s96,196608
s_waitcnt lgkmcnt(0)
s_waitcnt vmcnt(15)
s_barrier
s_add_u32 m0,s17,44416
s_nop 0
buffer_load_dwordx4 v57,s[12:15],s16 offen offset:640 lds
ds_read_b64_tr_b16 a[48:49],v70 offset:0
ds_read_b64_tr_b16 a[50:51],v70 offset:512
v_mfma_f32_16x16x32_bf16 v[20:23],a[24:27],v[16:19],v[20:23]
v_mfma_f32_16x16x32_bf16 v[24:27],a[28:31],v[16:19],v[24:27]
s_add_u32 m0,s97,139264
s_nop 0
buffer_load_dwordx4 v67,s[36:39],s96 offen offset:0 lds
ds_read_b64_tr_b16 a[52:53],v70 offset:1024
ds_read_b64_tr_b16 a[54:55],v70 offset:1536
v_mfma_f32_16x16x32_bf16 v[32:35],v[16:19],a[32:35],v[32:35]
v_mfma_f32_16x16x32_bf16 v[36:39],a[28:31],v[92:95],v[36:39]
buffer_load_dwordx4 v67,s[40:43],s96 offen offset:1024 lds
ds_read_b64_tr_b16 a[56:57],v74 offset:2048
ds_read_b64_tr_b16 a[58:59],v74 offset:2560
v_mfma_f32_16x16x32_bf16 v[40:43],v[92:95],a[32:35],v[40:43]
v_mfma_f32_16x16x32_bf16 v[44:47],a[24:27],v[96:99],v[44:47]
buffer_load_dwordx4 v67,s[44:47],s96 offen offset:2048 lds
ds_read_b64_tr_b16 a[60:61],v70 offset:12288
ds_read_b64_tr_b16 a[62:63],v70 offset:12800
ds_read_b64_tr_b16 a[64:65],v70 offset:13312
v_mfma_f32_16x16x32_bf16 v[48:51],a[28:31],v[96:99],v[48:51]
v_mfma_f32_16x16x32_bf16 v[52:55],v[96:99],a[32:35],v[52:55]
v_mfma_f32_16x16x32_bf16 v[20:23],a[36:39],v[16:19],v[20:23]
s_add_u32 s96,s96,196608
s_add_u32 m0,s97,151552
s_nop 0
buffer_load_dwordx4 v67,s[36:39],s96 offen offset:0 lds
ds_read_b64_tr_b16 a[66:67],v70 offset:13824
ds_read_b64_tr_b16 a[68:69],v74 offset:14336
v_mfma_f32_16x16x32_bf16 v[24:27],a[40:43],v[16:19],v[24:27]
v_mfma_f32_16x16x32_bf16 v[32:35],v[16:19],a[44:47],v[32:35]
buffer_load_dwordx4 v67,s[40:43],s96 offen offset:1024 lds
ds_read_b64_tr_b16 a[70:71],v74 offset:14848
ds_read_b128 v[76:79],v58 offset:8192
v_mfma_f32_16x16x32_bf16 v[36:39],a[40:43],v[100:103],v[36:39]
v_mfma_f32_16x16x32_bf16 v[40:43],v[100:103],a[44:47],v[40:43]
buffer_load_dwordx4 v67,s[44:47],s96 offen offset:2048 lds
ds_read_b128 v[80:83],v58 offset:10240
ds_read_b128 v[84:87],v59 offset:8192
ds_read_b128 v[88:91],v59 offset:10240
v_mfma_f32_16x16x32_bf16 v[44:47],a[36:39],v[104:107],v[44:47]
v_mfma_f32_16x16x32_bf16 v[48:51],a[40:43],v[104:107],v[48:51]
v_mfma_f32_16x16x32_bf16 v[52:55],v[104:107],a[44:47],v[52:55]
s_add_u32 s96,s96,196608
s_waitcnt lgkmcnt(0)
s_waitcnt vmcnt(14)
s_barrier
s_add_u32 m0,s17,48384
s_nop 0
buffer_load_dwordx4 v57,s[12:15],s16 offen offset:768 lds
ds_read_b64_tr_b16 a[72:73],v71 offset:0
ds_read_b64_tr_b16 a[74:75],v71 offset:512
v_mfma_f32_16x16x32_bf16 v[20:23],a[48:51],v[16:19],v[20:23]
v_mfma_f32_16x16x32_bf16 v[24:27],a[52:55],v[16:19],v[24:27]
s_add_u32 m0,s97,90112
s_nop 0
buffer_load_dwordx4 v67,s[36:39],s96 offen offset:0 lds
ds_read_b64_tr_b16 a[76:77],v71 offset:1024
ds_read_b64_tr_b16 a[78:79],v71 offset:1536
v_mfma_f32_16x16x32_bf16 v[32:35],v[16:19],a[56:59],v[32:35]
v_mfma_f32_16x16x32_bf16 v[36:39],a[52:55],v[76:79],v[36:39]
buffer_load_dwordx4 v67,s[40:43],s96 offen offset:1024 lds
ds_read_b64_tr_b16 a[80:81],v75 offset:2048
ds_read_b64_tr_b16 a[82:83],v75 offset:2560
v_mfma_f32_16x16x32_bf16 v[40:43],v[76:79],a[56:59],v[40:43]
v_mfma_f32_16x16x32_bf16 v[44:47],a[48:51],v[80:83],v[44:47]
buffer_load_dwordx4 v67,s[44:47],s96 offen offset:2048 lds
ds_read_b64_tr_b16 a[84:85],v71 offset:12288
ds_read_b64_tr_b16 a[86:87],v71 offset:12800
ds_read_b64_tr_b16 a[88:89],v71 offset:13312
v_mfma_f32_16x16x32_bf16 v[48:51],a[52:55],v[80:83],v[48:51]
v_mfma_f32_16x16x32_bf16 v[52:55],v[80:83],a[56:59],v[52:55]
v_mfma_f32_16x16x32_bf16 v[20:23],a[60:63],v[16:19],v[20:23]
s_add_u32 s96,s96,196608
s_add_u32 m0,s97,102400
s_nop 0
buffer_load_dwordx4 v67,s[36:39],s96 offen offset:0 lds
ds_read_b64_tr_b16 a[90:91],v71 offset:13824
ds_read_b64_tr_b16 a[92:93],v75 offset:14336
v_mfma_f32_16x16x32_bf16 v[24:27],a[64:67],v[16:19],v[24:27]
v_mfma_f32_16x16x32_bf16 v[32:35],v[16:19],a[68:71],v[32:35]
buffer_load_dwordx4 v67,s[40:43],s96 offen offset:1024 lds
ds_read_b64_tr_b16 a[94:95],v75 offset:14848
ds_read_b128 v[92:95],v58 offset:12288
v_mfma_f32_16x16x32_bf16 v[36:39],a[64:67],v[84:87],v[36:39]
v_mfma_f32_16x16x32_bf16 v[40:43],v[84:87],a[68:71],v[40:43]
buffer_load_dwordx4 v67,s[44:47],s96 offen offset:2048 lds
ds_read_b128 v[96:99],v58 offset:14336
ds_read_b128 v[100:103],v59 offset:12288
ds_read_b128 v[104:107],v59 offset:14336
v_mfma_f32_16x16x32_bf16 v[44:47],a[60:63],v[88:91],v[44:47]
v_mfma_f32_16x16x32_bf16 v[48:51],a[64:67],v[88:91],v[48:51]
v_mfma_f32_16x16x32_bf16 v[52:55],v[88:91],a[68:71],v[52:55]
s_add_u32 s96,s96,196608
s_waitcnt lgkmcnt(0)
s_waitcnt vmcnt(14)
s_barrier
s_add_u32 m0,s17,52352
s_nop 0
buffer_load_dwordx4 v57,s[12:15],s16 offen offset:896 lds
ds_read_b64_tr_b16 a[96:97],v68 offset:0
ds_read_b64_tr_b16 a[98:99],v68 offset:512
v_mfma_f32_16x16x32_bf16 v[20:23],a[72:75],v[16:19],v[20:23]
v_mfma_f32_16x16x32_bf16 v[24:27],a[76:79],v[16:19],v[24:27]
s_add_u32 m0,s97,114688
s_nop 0
buffer_load_dwordx4 v67,s[36:39],s96 offen offset:0 lds
ds_read_b64_tr_b16 a[100:101],v68 offset:1024
ds_read_b64_tr_b16 a[102:103],v68 offset:1536
v_mfma_f32_16x16x32_bf16 v[32:35],v[16:19],a[80:83],v[32:35]
v_mfma_f32_16x16x32_bf16 v[36:39],a[76:79],v[92:95],v[36:39]
buffer_load_dwordx4 v67,s[40:43],s96 offen offset:1024 lds
ds_read_b64_tr_b16 a[104:105],v72 offset:2048
ds_read_b64_tr_b16 a[106:107],v72 offset:2560
v_mfma_f32_16x16x32_bf16 v[40:43],v[92:95],a[80:83],v[40:43]
v_mfma_f32_16x16x32_bf16 v[44:47],a[72:75],v[96:99],v[44:47]
buffer_load_dwordx4 v67,s[44:47],s96 offen offset:2048 lds
ds_read_b64_tr_b16 a[108:109],v68 offset:12288
ds_read_b64_tr_b16 a[110:111],v68 offset:12800
ds_read_b64_tr_b16 a[112:113],v68 offset:13312
v_mfma_f32_16x16x32_bf16 v[48:51],a[76:79],v[96:99],v[48:51]
v_mfma_f32_16x16x32_bf16 v[52:55],v[96:99],a[80:83],v[52:55]
v_mfma_f32_16x16x32_bf16 v[20:23],a[84:87],v[16:19],v[20:23]
s_add_u32 s96,s96,196608
s_add_u32 m0,s97,126976
s_nop 0
buffer_load_dwordx4 v67,s[36:39],s96 offen offset:0 lds
ds_read_b64_tr_b16 a[114:115],v68 offset:13824
ds_read_b64_tr_b16 a[116:117],v72 offset:14336
v_mfma_f32_16x16x32_bf16 v[24:27],a[88:91],v[16:19],v[24:27]
v_mfma_f32_16x16x32_bf16 v[32:35],v[16:19],a[92:95],v[32:35]
buffer_load_dwordx4 v67,s[40:43],s96 offen offset:1024 lds
ds_read_b64_tr_b16 a[118:119],v72 offset:14848
ds_read_b128 v[76:79],v58 offset:16384
v_mfma_f32_16x16x32_bf16 v[36:39],a[88:91],v[100:103],v[36:39]
v_mfma_f32_16x16x32_bf16 v[40:43],v[100:103],a[92:95],v[40:43]
buffer_load_dwordx4 v67,s[44:47],s96 offen offset:2048 lds
ds_read_b128 v[80:83],v58 offset:18432
ds_read_b128 v[84:87],v59 offset:16384
ds_read_b128 v[88:91],v59 offset:18432
v_mfma_f32_16x16x32_bf16 v[44:47],a[84:87],v[104:107],v[44:47]
v_mfma_f32_16x16x32_bf16 v[48:51],a[88:91],v[104:107],v[48:51]
v_mfma_f32_16x16x32_bf16 v[52:55],v[104:107],a[92:95],v[52:55]
s_add_u32 s96,s96,196608
s_waitcnt lgkmcnt(0)
s_waitcnt vmcnt(14)
s_barrier
s_add_u32 m0,s17,56320
s_nop 0
buffer_load_dwordx4 v57,s[12:15],s16 offen offset:1024 lds
ds_read_b64_tr_b16 a[120:121],v69 offset:0
ds_read_b64_tr_b16 a[122:123],v69 offset:512
v_mfma_f32_16x16x32_bf16 v[20:23],a[96:99],v[16:19],v[20:23]
v_mfma_f32_16x16x32_bf16 v[24:27],a[100:103],v[16:19],v[24:27]
s_add_u32 m0,s97,0
s_nop 0
buffer_load_dwordx4 v67,s[36:39],s96 offen offset:0 lds
ds_read_b64_tr_b16 a[124:125],v69 offset:1024
ds_read_b64_tr_b16 a[126:127],v69 offset:1536
v_mfma_f32_16x16x32_bf16 v[32:35],v[16:19],a[104:107],v[32:35]
v_mfma_f32_16x16x32_bf16 v[36:39],a[100:103],v[76:79],v[36:39]
buffer_load_dwordx4 v67,s[40:43],s96 offen offset:1024 lds
ds_read_b64_tr_b16 a[128:129],v73 offset:2048
ds_read_b64_tr_b16 a[130:131],v73 offset:2560
v_mfma_f32_16x16x32_bf16 v[40:43],v[76:79],a[104:107],v[40:43]
v_mfma_f32_16x16x32_bf16 v[44:47],a[96:99],v[80:83],v[44:47]
buffer_load_dwordx4 v67,s[44:47],s96 offen offset:2048 lds
ds_read_b64_tr_b16 a[132:133],v69 offset:12288
ds_read_b64_tr_b16 a[134:135],v69 offset:12800
ds_read_b64_tr_b16 a[136:137],v69 offset:13312
v_mfma_f32_16x16x32_bf16 v[48:51],a[100:103],v[80:83],v[48:51]
v_mfma_f32_16x16x32_bf16 v[52:55],v[80:83],a[104:107],v[52:55]
v_mfma_f32_16x16x32_bf16 v[20:23],a[108:111],v[16:19],v[20:23]
s_add_u32 s96,s96,196608
s_add_u32 m0,s97,12288
s_nop 0
buffer_load_dwordx4 v67,s[36:39],s96 offen offset:0 lds
ds_read_b64_tr_b16 a[138:139],v69 offset:13824
ds_read_b64_tr_b16 a[140:141],v73 offset:14336
v_mfma_f32_16x16x32_bf16 v[24:27],a[112:115],v[16:19],v[24:27]
v_mfma_f32_16x16x32_bf16 v[32:35],v[16:19],a[116:119],v[32:35]
buffer_load_dwordx4 v67,s[40:43],s96 offen offset:1024 lds
ds_read_b64_tr_b16 a[142:143],v73 offset:14848
ds_read_b128 v[92:95],v58 offset:20480
v_mfma_f32_16x16x32_bf16 v[36:39],a[112:115],v[84:87],v[36:39]
v_mfma_f32_16x16x32_bf16 v[40:43],v[84:87],a[116:119],v[40:43]
buffer_load_dwordx4 v67,s[44:47],s96 offen offset:2048 lds
ds_read_b128 v[96:99],v58 offset:22528
ds_read_b128 v[100:103],v59 offset:20480
ds_read_b128 v[104:107],v59 offset:22528
v_mfma_f32_16x16x32_bf16 v[44:47],a[108:111],v[88:91],v[44:47]
v_mfma_f32_16x16x32_bf16 v[48:51],a[112:115],v[88:91],v[48:51]
v_mfma_f32_16x16x32_bf16 v[52:55],v[88:91],a[116:119],v[52:55]
s_add_u32 s96,s96,196608
s_waitcnt lgkmcnt(0)
s_waitcnt vmcnt(14)
s_barrier
s_add_u32 m0,s17,60288
s_nop 0
buffer_load_dwordx4 v57,s[12:15],s16 offen offset:1152 lds
ds_read_b64_tr_b16 a[144:145],v70 offset:0
ds_read_b64_tr_b16 a[146:147],v70 offset:512
v_mfma_f32_16x16x32_bf16 v[20:23],a[120:123],v[16:19],v[20:23]
v_mfma_f32_16x16x32_bf16 v[24:27],a[124:127],v[16:19],v[24:27]
s_add_u32 m0,s97,139264
s_nop 0
buffer_load_dwordx4 v67,s[36:39],s96 offen offset:0 lds
ds_read_b64_tr_b16 a[148:149],v70 offset:1024
ds_read_b64_tr_b16 a[150:151],v70 offset:1536
v_mfma_f32_16x16x32_bf16 v[32:35],v[16:19],a[128:131],v[32:35]
v_mfma_f32_16x16x32_bf16 v[36:39],a[124:127],v[92:95],v[36:39]
buffer_load_dwordx4 v67,s[40:43],s96 offen offset:1024 lds
ds_read_b64_tr_b16 a[152:153],v74 offset:2048
ds_read_b64_tr_b16 a[154:155],v74 offset:2560
v_mfma_f32_16x16x32_bf16 v[40:43],v[92:95],a[128:131],v[40:43]
v_mfma_f32_16x16x32_bf16 v[44:47],a[120:123],v[96:99],v[44:47]
buffer_load_dwordx4 v67,s[44:47],s96 offen offset:2048 lds
ds_read_b64_tr_b16 a[156:157],v70 offset:12288
ds_read_b64_tr_b16 a[158:159],v70 offset:12800
ds_read_b64_tr_b16 a[160:161],v70 offset:13312
v_mfma_f32_16x16x32_bf16 v[48:51],a[124:127],v[96:99],v[48:51]
v_mfma_f32_16x16x32_bf16 v[52:55],v[96:99],a[128:131],v[52:55]
v_mfma_f32_16x16x32_bf16 v[20:23],a[132:135],v[16:19],v[20:23]
s_add_u32 s96,s96,196608
s_add_u32 m0,s97,151552
s_nop 0
buffer_load_dwordx4 v67,s[36:39],s96 offen offset:0 lds
ds_read_b64_tr_b16 a[162:163],v70 offset:13824
ds_read_b64_tr_b16 a[164:165],v74 offset:14336
v_mfma_f32_16x16x32_bf16 v[24:27],a[136:139],v[16:19],v[24:27]
v_mfma_f32_16x16x32_bf16 v[32:35],v[16:19],a[140:143],v[32:35]
buffer_load_dwordx4 v67,s[40:43],s96 offen offset:1024 lds
ds_read_b64_tr_b16 a[166:167],v74 offset:14848
ds_read_b128 v[76:79],v58 offset:24576
v_mfma_f32_16x16x32_bf16 v[36:39],a[136:139],v[100:103],v[36:39]
v_mfma_f32_16x16x32_bf16 v[40:43],v[100:103],a[140:143],v[40:43]
buffer_load_dwordx4 v67,s[44:47],s96 offen offset:2048 lds
ds_read_b128 v[80:83],v58 offset:26624
ds_read_b128 v[84:87],v59 offset:24576
ds_read_b128 v[88:91],v59 offset:26624
v_mfma_f32_16x16x32_bf16 v[44:47],a[132:135],v[104:107],v[44:47]
v_mfma_f32_16x16x32_bf16 v[48:51],a[136:139],v[104:107],v[48:51]
v_mfma_f32_16x16x32_bf16 v[52:55],v[104:107],a[140:143],v[52:55]
s_add_u32 s96,s96,196608
s_waitcnt lgkmcnt(0)
s_waitcnt vmcnt(14)
s_barrier
s_add_u32 m0,s17,64256
s_nop 0
buffer_load_dwordx4 v57,s[12:15],s16 offen offset:1280 lds
ds_read_b64_tr_b16 a[168:169],v71 offset:0
ds_read_b64_tr_b16 a[170:171],v71 offset:512
v_mfma_f32_16x16x32_bf16 v[20:23],a[144:147],v[16:19],v[20:23]
v_mfma_f32_16x16x32_bf16 v[24:27],a[148:151],v[16:19],v[24:27]
s_add_u32 m0,s97,90112
s_nop 0
buffer_load_dwordx4 v67,s[36:39],s96 offen offset:0 lds
ds_read_b64_tr_b16 a[172:173],v71 offset:1024
ds_read_b64_tr_b16 a[174:175],v71 offset:1536
v_mfma_f32_16x16x32_bf16 v[32:35],v[16:19],a[152:155],v[32:35]
v_mfma_f32_16x16x32_bf16 v[36:39],a[148:151],v[76:79],v[36:39]
buffer_load_dwordx4 v67,s[40:43],s96 offen offset:1024 lds
ds_read_b64_tr_b16 a[176:177],v75 offset:2048
ds_read_b64_tr_b16 a[178:179],v75 offset:2560
v_mfma_f32_16x16x32_bf16 v[40:43],v[76:79],a[152:155],v[40:43]
v_mfma_f32_16x16x32_bf16 v[44:47],a[144:147],v[80:83],v[44:47]
buffer_load_dwordx4 v67,s[44:47],s96 offen offset:2048 lds
ds_read_b64_tr_b16 a[180:181],v71 offset:12288
ds_read_b64_tr_b16 a[182:183],v71 offset:12800
v_mfma_f32_16x16x32_bf16 v[48:51],a[148:151],v[80:83],v[48:51]
v_mfma_f32_16x16x32_bf16 v[52:55],v[80:83],a[152:155],v[52:55]
s_add_u32 s96,s96,196608
s_add_u32 m0,s97,102400
s_nop 0
buffer_load_dwordx4 v67,s[36:39],s96 offen offset:0 lds
ds_read_b64_tr_b16 a[184:185],v71 offset:13312
ds_read_b64_tr_b16 a[186:187],v71 offset:13824
v_mfma_f32_16x16x32_bf16 v[20:23],a[156:159],v[16:19],v[20:23]
v_mfma_f32_16x16x32_bf16 v[24:27],a[160:163],v[16:19],v[24:27]
buffer_load_dwordx4 v67,s[40:43],s96 offen offset:1024 lds
ds_read_b64_tr_b16 a[188:189],v75 offset:14336
ds_read_b64_tr_b16 a[190:191],v75 offset:14848
v_mfma_f32_16x16x32_bf16 v[32:35],v[16:19],a[164:167],v[32:35]
v_mfma_f32_16x16x32_bf16 v[36:39],a[160:163],v[84:87],v[36:39]
buffer_load_dwordx4 v67,s[44:47],s96 offen offset:2048 lds
ds_read_b128 v[92:95],v58 offset:28672
ds_read_b128 v[96:99],v58 offset:30720
v_mfma_f32_16x16x32_bf16 v[40:43],v[84:87],a[164:167],v[40:43]
v_mfma_f32_16x16x32_bf16 v[44:47],a[156:159],v[88:91],v[44:47]
s_add_u32 s96,s96,196608
buffer_load_dwordx2 v[60:61],v56,s[32:35],s66 offen sc1
ds_read_b128 v[100:103],v59 offset:28672
ds_read_b128 v[104:107],v59 offset:30720
v_mfma_f32_16x16x32_bf16 v[48:51],a[160:163],v[88:91],v[48:51]
v_mfma_f32_16x16x32_bf16 v[52:55],v[88:91],a[164:167],v[52:55]
s_waitcnt lgkmcnt(0)
s_waitcnt vmcnt(15)
s_barrier
s_add_u32 m0,s17,68224
s_nop 0
buffer_load_dwordx4 v57,s[12:15],s16 offen offset:1408 lds
ds_read_b64_tr_b16 a[192:193],v68 offset:0
ds_read_b64_tr_b16 a[194:195],v68 offset:512
v_mfma_f32_16x16x32_bf16 v[20:23],a[168:171],v[16:19],v[20:23]
v_mfma_f32_16x16x32_bf16 v[24:27],a[172:175],v[16:19],v[24:27]
s_add_u32 m0,s97,114688
s_nop 0
buffer_load_dwordx4 v67,s[36:39],s96 offen offset:0 lds
ds_read_b64_tr_b16 a[196:197],v68 offset:1024
ds_read_b64_tr_b16 a[198:199],v68 offset:1536
v_mfma_f32_16x16x32_bf16 v[32:35],v[16:19],a[176:179],v[32:35]
v_mfma_f32_16x16x32_bf16 v[36:39],a[172:175],v[92:95],v[36:39]
buffer_load_dwordx4 v67,s[40:43],s96 offen offset:1024 lds
ds_read_b64_tr_b16 a[200:201],v72 offset:2048
ds_read_b64_tr_b16 a[202:203],v72 offset:2560
v_mfma_f32_16x16x32_bf16 v[40:43],v[92:95],a[176:179],v[40:43]
v_mfma_f32_16x16x32_bf16 v[44:47],a[168:171],v[96:99],v[44:47]
buffer_load_dwordx4 v67,s[44:47],s96 offen offset:2048 lds
ds_read_b64_tr_b16 a[204:205],v68 offset:12288
ds_read_b64_tr_b16 a[206:207],v68 offset:12800
ds_read_b64_tr_b16 a[208:209],v68 offset:13312
v_mfma_f32_16x16x32_bf16 v[48:51],a[172:175],v[96:99],v[48:51]
v_mfma_f32_16x16x32_bf16 v[52:55],v[96:99],a[176:179],v[52:55]
v_mfma_f32_16x16x32_bf16 v[20:23],a[180:183],v[16:19],v[20:23]
s_add_u32 s96,s96,196608
s_add_u32 m0,s97,126976
s_nop 0
buffer_load_dwordx4 v67,s[36:39],s96 offen offset:0 lds
ds_read_b64_tr_b16 a[210:211],v68 offset:13824
ds_read_b64_tr_b16 a[212:213],v72 offset:14336
v_mfma_f32_16x16x32_bf16 v[24:27],a[184:187],v[16:19],v[24:27]
v_mfma_f32_16x16x32_bf16 v[32:35],v[16:19],a[188:191],v[32:35]
buffer_load_dwordx4 v67,s[40:43],s96 offen offset:1024 lds
ds_read_b64_tr_b16 a[214:215],v72 offset:14848
ds_read_b128 v[76:79],v58 offset:32768
v_mfma_f32_16x16x32_bf16 v[36:39],a[184:187],v[100:103],v[36:39]
v_mfma_f32_16x16x32_bf16 v[40:43],v[100:103],a[188:191],v[40:43]
buffer_load_dwordx4 v67,s[44:47],s96 offen offset:2048 lds
ds_read_b128 v[80:83],v58 offset:34816
ds_read_b128 v[84:87],v59 offset:32768
ds_read_b128 v[88:91],v59 offset:34816
v_mfma_f32_16x16x32_bf16 v[44:47],a[180:183],v[104:107],v[44:47]
v_mfma_f32_16x16x32_bf16 v[48:51],a[184:187],v[104:107],v[48:51]
v_mfma_f32_16x16x32_bf16 v[52:55],v[104:107],a[188:191],v[52:55]
s_add_u32 s96,s96,196608
s_waitcnt lgkmcnt(0)
s_waitcnt vmcnt(15)
s_barrier
s_add_u32 m0,s17,72192
s_nop 0
buffer_load_dwordx4 v57,s[12:15],s16 offen offset:1536 lds
ds_read_b64_tr_b16 a[216:217],v69 offset:0
ds_read_b64_tr_b16 a[218:219],v69 offset:512
v_mfma_f32_16x16x32_bf16 v[20:23],a[192:195],v[16:19],v[20:23]
v_mfma_f32_16x16x32_bf16 v[24:27],a[196:199],v[16:19],v[24:27]
s_add_u32 m0,s97,0
s_nop 0
buffer_load_dwordx4 v67,s[36:39],s96 offen offset:0 lds
ds_read_b64_tr_b16 a[220:221],v69 offset:1024
ds_read_b64_tr_b16 a[222:223],v69 offset:1536
v_mfma_f32_16x16x32_bf16 v[32:35],v[16:19],a[200:203],v[32:35]
v_mfma_f32_16x16x32_bf16 v[36:39],a[196:199],v[76:79],v[36:39]
buffer_load_dwordx4 v67,s[40:43],s96 offen offset:1024 lds
ds_read_b64_tr_b16 a[224:225],v73 offset:2048
ds_read_b64_tr_b16 a[226:227],v73 offset:2560
v_mfma_f32_16x16x32_bf16 v[40:43],v[76:79],a[200:203],v[40:43]
v_mfma_f32_16x16x32_bf16 v[44:47],a[192:195],v[80:83],v[44:47]
buffer_load_dwordx4 v67,s[44:47],s96 offen offset:2048 lds
ds_read_b64_tr_b16 a[228:229],v69 offset:12288
ds_read_b64_tr_b16 a[230:231],v69 offset:12800
ds_read_b64_tr_b16 a[232:233],v69 offset:13312
v_mfma_f32_16x16x32_bf16 v[48:51],a[196:199],v[80:83],v[48:51]
v_mfma_f32_16x16x32_bf16 v[52:55],v[80:83],a[200:203],v[52:55]
v_mfma_f32_16x16x32_bf16 v[20:23],a[204:207],v[16:19],v[20:23]
s_add_u32 s96,s96,196608
s_add_u32 m0,s97,12288
s_nop 0
buffer_load_dwordx4 v67,s[36:39],s96 offen offset:0 lds
ds_read_b64_tr_b16 a[234:235],v69 offset:13824
ds_read_b64_tr_b16 a[236:237],v73 offset:14336
v_mfma_f32_16x16x32_bf16 v[24:27],a[208:211],v[16:19],v[24:27]
v_mfma_f32_16x16x32_bf16 v[32:35],v[16:19],a[212:215],v[32:35]
buffer_load_dwordx4 v67,s[40:43],s96 offen offset:1024 lds
ds_read_b64_tr_b16 a[238:239],v73 offset:14848
ds_read_b128 v[92:95],v58 offset:36864
v_mfma_f32_16x16x32_bf16 v[36:39],a[208:211],v[84:87],v[36:39]
v_mfma_f32_16x16x32_bf16 v[40:43],v[84:87],a[212:215],v[40:43]
buffer_load_dwordx4 v67,s[44:47],s96 offen offset:2048 lds
ds_read_b128 v[96:99],v58 offset:38912
ds_read_b128 v[100:103],v59 offset:36864
ds_read_b128 v[104:107],v59 offset:38912
v_mfma_f32_16x16x32_bf16 v[44:47],a[204:207],v[88:91],v[44:47]
v_mfma_f32_16x16x32_bf16 v[48:51],a[208:211],v[88:91],v[48:51]
v_mfma_f32_16x16x32_bf16 v[52:55],v[88:91],a[212:215],v[52:55]
s_add_u32 s96,s96,196608
s_waitcnt lgkmcnt(0)
s_waitcnt vmcnt(15)
s_barrier
s_add_u32 m0,s17,76160
s_nop 0
buffer_load_dwordx4 v57,s[12:15],s16 offen offset:1664 lds
ds_read_b64_tr_b16 a[240:241],v70 offset:0
ds_read_b64_tr_b16 a[242:243],v70 offset:512
v_mfma_f32_16x16x32_bf16 v[20:23],a[216:219],v[16:19],v[20:23]
v_mfma_f32_16x16x32_bf16 v[24:27],a[220:223],v[16:19],v[24:27]
s_add_u32 m0,s97,139264
s_nop 0
buffer_load_dwordx4 v67,s[36:39],s96 offen offset:0 lds
ds_read_b64_tr_b16 a[244:245],v70 offset:1024
ds_read_b64_tr_b16 a[246:247],v70 offset:1536
v_mfma_f32_16x16x32_bf16 v[32:35],v[16:19],a[224:227],v[32:35]
v_mfma_f32_16x16x32_bf16 v[36:39],a[220:223],v[92:95],v[36:39]
buffer_load_dwordx4 v67,s[40:43],s96 offen offset:1024 lds
ds_read_b64_tr_b16 a[248:249],v74 offset:2048
ds_read_b64_tr_b16 a[250:251],v74 offset:2560
v_mfma_f32_16x16x32_bf16 v[40:43],v[92:95],a[224:227],v[40:43]
v_mfma_f32_16x16x32_bf16 v[44:47],a[216:219],v[96:99],v[44:47]
buffer_load_dwordx4 v67,s[44:47],s96 offen offset:2048 lds
ds_read_b64_tr_b16 a[252:253],v70 offset:12288
ds_read_b64_tr_b16 a[254:255],v70 offset:12800
ds_read_b64_tr_b16 v[128:129],v70 offset:13312
v_mfma_f32_16x16x32_bf16 v[48:51],a[220:223],v[96:99],v[48:51]
v_mfma_f32_16x16x32_bf16 v[52:55],v[96:99],a[224:227],v[52:55]
v_mfma_f32_16x16x32_bf16 v[20:23],a[228:231],v[16:19],v[20:23]
s_add_u32 s96,s96,196608
s_add_u32 m0,s97,151552
s_nop 0
buffer_load_dwordx4 v67,s[36:39],s96 offen offset:0 lds
ds_read_b64_tr_b16 v[130:131],v70 offset:13824
ds_read_b64_tr_b16 v[132:133],v74 offset:14336
v_mfma_f32_16x16x32_bf16 v[24:27],a[232:235],v[16:19],v[24:27]
v_mfma_f32_16x16x32_bf16 v[32:35],v[16:19],a[236:239],v[32:35]
buffer_load_dwordx4 v67,s[40:43],s96 offen offset:1024 lds
ds_read_b64_tr_b16 v[134:135],v74 offset:14848
ds_read_b128 v[76:79],v58 offset:40960
v_mfma_f32_16x16x32_bf16 v[36:39],a[232:235],v[100:103],v[36:39]
v_mfma_f32_16x16x32_bf16 v[40:43],v[100:103],a[236:239],v[40:43]
buffer_load_dwordx4 v67,s[44:47],s96 offen offset:2048 lds
ds_read_b128 v[80:83],v58 offset:43008
ds_read_b128 v[84:87],v59 offset:40960
ds_read_b128 v[88:91],v59 offset:43008
v_mfma_f32_16x16x32_bf16 v[44:47],a[228:231],v[104:107],v[44:47]
v_mfma_f32_16x16x32_bf16 v[48:51],a[232:235],v[104:107],v[48:51]
v_mfma_f32_16x16x32_bf16 v[52:55],v[104:107],a[236:239],v[52:55]
s_add_u32 s96,s96,196608
s_waitcnt lgkmcnt(0)
s_waitcnt vmcnt(14)
s_barrier
s_add_u32 m0,s17,80128
s_nop 0
buffer_load_dwordx4 v57,s[12:15],s16 offen offset:1792 lds
ds_read_b64_tr_b16 v[136:137],v71 offset:0
ds_read_b64_tr_b16 v[138:139],v71 offset:512
v_mfma_f32_16x16x32_bf16 v[20:23],a[240:243],v[16:19],v[20:23]
v_mfma_f32_16x16x32_bf16 v[24:27],a[244:247],v[16:19],v[24:27]
s_add_u32 m0,s97,90112
s_nop 0
buffer_load_dwordx4 v67,s[36:39],s96 offen offset:0 lds
ds_read_b64_tr_b16 v[140:141],v71 offset:1024
)#"
R"#(
ds_read_b64_tr_b16 v[142:143],v71 offset:1536
v_mfma_f32_16x16x32_bf16 v[32:35],v[16:19],a[248:251],v[32:35]
v_mfma_f32_16x16x32_bf16 v[36:39],a[244:247],v[76:79],v[36:39]
buffer_load_dwordx4 v67,s[40:43],s96 offen offset:1024 lds
ds_read_b64_tr_b16 v[144:145],v75 offset:2048
ds_read_b64_tr_b16 v[146:147],v75 offset:2560
v_mfma_f32_16x16x32_bf16 v[40:43],v[76:79],a[248:251],v[40:43]
v_mfma_f32_16x16x32_bf16 v[44:47],a[240:243],v[80:83],v[44:47]
buffer_load_dwordx4 v67,s[44:47],s96 offen offset:2048 lds
ds_read_b64_tr_b16 v[148:149],v71 offset:12288
ds_read_b64_tr_b16 v[150:151],v71 offset:12800
ds_read_b64_tr_b16 v[152:153],v71 offset:13312
v_mfma_f32_16x16x32_bf16 v[48:51],a[244:247],v[80:83],v[48:51]
v_mfma_f32_16x16x32_bf16 v[52:55],v[80:83],a[248:251],v[52:55]
v_mfma_f32_16x16x32_bf16 v[20:23],a[252:255],v[16:19],v[20:23]
s_add_u32 s96,s96,196608
s_add_u32 m0,s97,102400
s_nop 0
buffer_load_dwordx4 v67,s[36:39],s96 offen offset:0 lds
ds_read_b64_tr_b16 v[154:155],v71 offset:13824
ds_read_b64_tr_b16 v[156:157],v75 offset:14336
v_mfma_f32_16x16x32_bf16 v[24:27],v[128:131],v[16:19],v[24:27]
v_mfma_f32_16x16x32_bf16 v[32:35],v[16:19],v[132:135],v[32:35]
buffer_load_dwordx4 v67,s[40:43],s96 offen offset:1024 lds
ds_read_b64_tr_b16 v[158:159],v75 offset:14848
ds_read_b128 v[92:95],v58 offset:45056
v_mfma_f32_16x16x32_bf16 v[36:39],v[128:131],v[84:87],v[36:39]
v_mfma_f32_16x16x32_bf16 v[40:43],v[84:87],v[132:135],v[40:43]
buffer_load_dwordx4 v67,s[44:47],s96 offen offset:2048 lds
ds_read_b128 v[96:99],v58 offset:47104
ds_read_b128 v[100:103],v59 offset:45056
ds_read_b128 v[104:107],v59 offset:47104
v_mfma_f32_16x16x32_bf16 v[44:47],a[252:255],v[88:91],v[44:47]
v_mfma_f32_16x16x32_bf16 v[48:51],v[128:131],v[88:91],v[48:51]
v_mfma_f32_16x16x32_bf16 v[52:55],v[88:91],v[132:135],v[52:55]
s_add_u32 s96,s96,196608
s_waitcnt lgkmcnt(0)
s_waitcnt vmcnt(14)
s_barrier
s_add_u32 m0,s17,84096
s_nop 0
buffer_load_dwordx4 v57,s[12:15],s16 offen offset:1920 lds
ds_read_b64_tr_b16 v[160:161],v68 offset:0
v_mfma_f32_16x16x32_bf16 v[20:23],v[136:139],v[16:19],v[20:23]
s_add_u32 m0,s97,114688
s_nop 0
buffer_load_dwordx4 v67,s[36:39],s96 offen offset:0 lds
ds_read_b64_tr_b16 v[162:163],v68 offset:512
v_mfma_f32_16x16x32_bf16 v[24:27],v[140:143],v[16:19],v[24:27]
buffer_load_dwordx4 v67,s[40:43],s96 offen offset:1024 lds
ds_read_b64_tr_b16 v[164:165],v68 offset:1024
v_mfma_f32_16x16x32_bf16 v[32:35],v[16:19],v[144:147],v[32:35]
buffer_load_dwordx4 v67,s[44:47],s96 offen offset:2048 lds
ds_read_b64_tr_b16 v[166:167],v68 offset:1536
v_mfma_f32_16x16x32_bf16 v[36:39],v[140:143],v[92:95],v[36:39]
s_add_u32 s96,s96,196608
s_add_u32 m0,s97,126976
s_nop 0
buffer_load_dwordx4 v67,s[36:39],s96 offen offset:0 lds
ds_read_b64_tr_b16 v[168:169],v72 offset:2048
v_mfma_f32_16x16x32_bf16 v[40:43],v[92:95],v[144:147],v[40:43]
buffer_load_dwordx4 v67,s[40:43],s96 offen offset:1024 lds
ds_read_b64_tr_b16 v[170:171],v72 offset:2560
v_mfma_f32_16x16x32_bf16 v[44:47],v[136:139],v[96:99],v[44:47]
buffer_load_dwordx4 v67,s[44:47],s96 offen offset:2048 lds
ds_read_b64_tr_b16 v[172:173],v68 offset:12288
v_mfma_f32_16x16x32_bf16 v[48:51],v[140:143],v[96:99],v[48:51]
s_add_u32 s96,s96,196608
v_cmp_ne_u64 vcc,s[54:55],v[60:61]
s_cbranch_vccnz .L_rtqe
.L_bkqe:
buffer_load_dword v108,v6,s[32:35],s74 offen sc1
ds_read_b64_tr_b16 v[174:175],v68 offset:12800
v_mfma_f32_16x16x32_bf16 v[52:55],v[96:99],v[144:147],v[52:55]
s_add_u32 s74,s74,9216
buffer_load_dword v109,v6,s[32:35],s74 offen sc1
ds_read_b64_tr_b16 v[176:177],v68 offset:13312
v_mfma_f32_16x16x32_bf16 v[20:23],v[148:151],v[16:19],v[20:23]
s_add_u32 s74,s74,9216
buffer_load_dword v110,v6,s[32:35],s74 offen sc1
ds_read_b64_tr_b16 v[178:179],v68 offset:13824
v_mfma_f32_16x16x32_bf16 v[24:27],v[152:155],v[16:19],v[24:27]
s_add_u32 s74,s74,9216
buffer_load_dword v111,v6,s[32:35],s74 offen sc1
ds_read_b64_tr_b16 v[180:181],v72 offset:14336
v_mfma_f32_16x16x32_bf16 v[32:35],v[16:19],v[156:159],v[32:35]
s_sub_u32 s74,s66,286720
buffer_load_dword v112,v6,s[32:35],s74 offen sc1
ds_read_b64_tr_b16 v[182:183],v72 offset:14848
v_mfma_f32_16x16x32_bf16 v[36:39],v[152:155],v[100:103],v[36:39]
s_add_u32 s74,s74,9216
buffer_load_dword v113,v6,s[32:35],s74 offen sc1
ds_read_b128 v[76:79],v58 offset:49152
v_mfma_f32_16x16x32_bf16 v[40:43],v[100:103],v[156:159],v[40:43]
s_add_u32 s74,s74,9216
buffer_load_dword v114,v6,s[32:35],s74 offen sc1
ds_read_b128 v[80:83],v58 offset:51200
v_mfma_f32_16x16x32_bf16 v[44:47],v[148:151],v[104:107],v[44:47]
s_add_u32 s74,s74,9216
buffer_load_dword v115,v6,s[32:35],s74 offen sc1
ds_read_b128 v[84:87],v59 offset:49152
v_mfma_f32_16x16x32_bf16 v[48:51],v[152:155],v[104:107],v[48:51]
s_add_u32 s74,s74,9216
s_add_u32 s66,s66,1187840
s_add_u32 s63,s66,s71
s_add_u32 s66,s66,2375680
buffer_load_dwordx2 v[12:13],v0,s[32:35],s66 offen sc1
ds_read_b128 v[88:91],v59 offset:51200
v_mfma_f32_16x16x32_bf16 v[52:55],v[104:107],v[156:159],v[52:55]
s_waitcnt lgkmcnt(0)
s_waitcnt vmcnt(23)
s_barrier
s_add_u32 m0,s64,0
s_nop 0
buffer_load_dwordx4 v2,s[32:35],s63 offen offset:0 sc1 lds
ds_read_b64_tr_b16 v[184:185],v69 offset:0
v_mfma_f32_16x16x32_bf16 v[20:23],v[160:163],v[16:19],v[20:23]
buffer_load_dwordx4 v2,s[32:35],s63 offen offset:1024 sc1 lds
ds_read_b64_tr_b16 v[186:187],v69 offset:512
ds_read_b64_tr_b16 v[188:189],v69 offset:1024
v_mfma_f32_16x16x32_bf16 v[24:27],v[164:167],v[16:19],v[24:27]
v_mfma_f32_16x16x32_bf16 v[32:35],v[16:19],v[168:171],v[32:35]
buffer_load_dwordx4 v2,s[32:35],s63 offen offset:2048 sc1 lds
ds_read_b64_tr_b16 v[190:191],v69 offset:1536
ds_read_b64_tr_b16 v[192:193],v73 offset:2048
v_mfma_f32_16x16x32_bf16 v[36:39],v[164:167],v[76:79],v[36:39]
v_mfma_f32_16x16x32_bf16 v[40:43],v[76:79],v[168:171],v[40:43]
buffer_load_dwordx4 v2,s[32:35],s63 offen offset:3072 sc1 lds
ds_read_b64_tr_b16 v[194:195],v73 offset:2560
ds_read_b64_tr_b16 v[196:197],v69 offset:12288
v_mfma_f32_16x16x32_bf16 v[44:47],v[160:163],v[80:83],v[44:47]
v_mfma_f32_16x16x32_bf16 v[48:51],v[164:167],v[80:83],v[48:51]
s_add_u32 s63,s63,4096
s_add_u32 m0,s64,4096
s_nop 0
buffer_load_dwordx4 v2,s[32:35],s63 offen offset:0 sc1 lds
ds_read_b64_tr_b16 v[198:199],v69 offset:12800
v_mfma_f32_16x16x32_bf16 v[52:55],v[80:83],v[168:171],v[52:55]
buffer_load_dwordx4 v2,s[32:35],s63 offen offset:1024 sc1 lds
ds_read_b64_tr_b16 v[200:201],v69 offset:13312
ds_read_b64_tr_b16 v[202:203],v69 offset:13824
v_mfma_f32_16x16x32_bf16 v[20:23],v[172:175],v[16:19],v[20:23]
v_mfma_f32_16x16x32_bf16 v[24:27],v[176:179],v[16:19],v[24:27]
buffer_load_dwordx4 v2,s[32:35],s63 offen offset:2048 sc1 lds
ds_read_b64_tr_b16 v[204:205],v73 offset:14336
ds_read_b64_tr_b16 v[206:207],v73 offset:14848
v_mfma_f32_16x16x32_bf16 v[32:35],v[16:19],v[180:183],v[32:35]
v_mfma_f32_16x16x32_bf16 v[36:39],v[176:179],v[84:87],v[36:39]
buffer_load_dwordx4 v2,s[32:35],s63 offen offset:3072 sc1 lds
ds_read_b128 v[92:95],v58 offset:53248
ds_read_b128 v[96:99],v58 offset:55296
v_mfma_f32_16x16x32_bf16 v[40:43],v[84:87],v[180:183],v[40:43]
v_mfma_f32_16x16x32_bf16 v[44:47],v[172:175],v[88:91],v[44:47]
s_add_u32 s63,s63,4096
s_add_u32 m0,s64,8192
s_nop 0
buffer_load_dwordx4 v2,s[32:35],s63 offen offset:0 sc1 lds
ds_read_b128 v[100:103],v59 offset:53248
ds_read_b128 v[104:107],v59 offset:55296
v_mfma_f32_16x16x32_bf16 v[48:51],v[176:179],v[88:91],v[48:51]
v_mfma_f32_16x16x32_bf16 v[52:55],v[88:91],v[180:183],v[52:55]
s_waitcnt lgkmcnt(0)
s_waitcnt vmcnt(25)
s_barrier
s_add_u32 s63,s63,1179648
s_add_u32 m0,s64,36864
s_nop 0
buffer_load_dwordx4 v2,s[32:35],s63 offen offset:0 sc1 lds
ds_read_b64_tr_b16 v[208:209],v70 offset:0
ds_read_b64_tr_b16 v[210:211],v70 offset:512
ds_read_b64_tr_b16 v[212:213],v70 offset:1024
ds_read_b64_tr_b16 v[214:215],v70 offset:1536
v_mfma_f32_16x16x32_bf16 v[20:23],v[184:187],v[16:19],v[20:23]
v_mfma_f32_16x16x32_bf16 v[24:27],v[188:191],v[16:19],v[24:27]
v_mfma_f32_16x16x32_bf16 v[32:35],v[16:19],v[192:195],v[32:35]
v_mfma_f32_16x16x32_bf16 v[36:39],v[188:191],v[92:95],v[36:39]
buffer_load_dwordx4 v2,s[32:35],s63 offen offset:1024 sc1 lds
ds_read_b64_tr_b16 v[216:217],v74 offset:2048
ds_read_b64_tr_b16 v[218:219],v74 offset:2560
ds_read_b64_tr_b16 v[220:221],v70 offset:12288
ds_read_b64_tr_b16 v[222:223],v70 offset:12800
v_mfma_f32_16x16x32_bf16 v[40:43],v[92:95],v[192:195],v[40:43]
v_mfma_f32_16x16x32_bf16 v[44:47],v[184:187],v[96:99],v[44:47]
v_mfma_f32_16x16x32_bf16 v[48:51],v[188:191],v[96:99],v[48:51]
v_mfma_f32_16x16x32_bf16 v[52:55],v[96:99],v[192:195],v[52:55]
buffer_load_dwordx4 v2,s[32:35],s63 offen offset:2048 sc1 lds
ds_read_b64_tr_b16 v[224:225],v70 offset:13312
ds_read_b64_tr_b16 v[226:227],v70 offset:13824
ds_read_b64_tr_b16 v[228:229],v74 offset:14336
ds_read_b64_tr_b16 v[230:231],v74 offset:14848
v_mfma_f32_16x16x32_bf16 v[20:23],v[196:199],v[16:19],v[20:23]
v_mfma_f32_16x16x32_bf16 v[24:27],v[200:203],v[16:19],v[24:27]
v_mfma_f32_16x16x32_bf16 v[32:35],v[16:19],v[204:207],v[32:35]
v_mfma_f32_16x16x32_bf16 v[36:39],v[200:203],v[100:103],v[36:39]
buffer_load_dwordx4 v2,s[32:35],s63 offen offset:3072 sc1 lds
ds_read_b128 v[76:79],v58 offset:57344
ds_read_b128 v[80:83],v58 offset:59392
ds_read_b128 v[84:87],v59 offset:57344
ds_read_b128 v[88:91],v59 offset:59392
v_mfma_f32_16x16x32_bf16 v[40:43],v[100:103],v[204:207],v[40:43]
v_mfma_f32_16x16x32_bf16 v[44:47],v[196:199],v[104:107],v[44:47]
v_mfma_f32_16x16x32_bf16 v[48:51],v[200:203],v[104:107],v[48:51]
v_mfma_f32_16x16x32_bf16 v[52:55],v[104:107],v[204:207],v[52:55]
s_waitcnt lgkmcnt(0)
s_waitcnt vmcnt(22)
s_barrier
s_add_u32 s63,s63,4096
s_add_u32 m0,s64,40960
s_nop 0
buffer_load_dwordx4 v2,s[32:35],s63 offen offset:0 sc1 lds
ds_read_b64_tr_b16 v[232:233],v71 offset:0
ds_read_b64_tr_b16 v[234:235],v71 offset:512
ds_read_b64_tr_b16 v[236:237],v71 offset:1024
v_mfma_f32_16x16x32_bf16 v[20:23],v[208:211],v[16:19],v[20:23]
v_mfma_f32_16x16x32_bf16 v[24:27],v[212:215],v[16:19],v[24:27]
v_mfma_f32_16x16x32_bf16 v[32:35],v[16:19],v[216:219],v[32:35]
buffer_load_dwordx4 v2,s[32:35],s63 offen offset:1024 sc1 lds
ds_read_b64_tr_b16 v[238:239],v71 offset:1536
ds_read_b64_tr_b16 v[240:241],v75 offset:2048
ds_read_b64_tr_b16 v[242:243],v75 offset:2560
v_mfma_f32_16x16x32_bf16 v[36:39],v[212:215],v[76:79],v[36:39]
v_mfma_f32_16x16x32_bf16 v[40:43],v[76:79],v[216:219],v[40:43]
v_mfma_f32_16x16x32_bf16 v[44:47],v[208:211],v[80:83],v[44:47]
buffer_load_dwordx4 v2,s[32:35],s63 offen offset:2048 sc1 lds
ds_read_b64_tr_b16 v[244:245],v71 offset:12288
ds_read_b64_tr_b16 v[246:247],v71 offset:12800
ds_read_b64_tr_b16 v[248:249],v71 offset:13312
v_mfma_f32_16x16x32_bf16 v[48:51],v[212:215],v[80:83],v[48:51]
v_mfma_f32_16x16x32_bf16 v[52:55],v[80:83],v[216:219],v[52:55]
v_mfma_f32_16x16x32_bf16 v[20:23],v[220:223],v[16:19],v[20:23]
buffer_load_dwordx4 v2,s[32:35],s63 offen offset:3072 sc1 lds
ds_read_b64_tr_b16 v[250:251],v71 offset:13824
ds_read_b64_tr_b16 v[252:253],v75 offset:14336
ds_read_b64_tr_b16 v[254:255],v75 offset:14848
v_mfma_f32_16x16x32_bf16 v[24:27],v[224:227],v[16:19],v[24:27]
v_mfma_f32_16x16x32_bf16 v[32:35],v[16:19],v[228:231],v[32:35]
v_mfma_f32_16x16x32_bf16 v[36:39],v[224:227],v[84:87],v[36:39]
s_add_u32 s63,s63,4096
s_add_u32 m0,s64,45056
s_nop 0
buffer_load_dwordx4 v2,s[32:35],s63 offen offset:0 sc1 lds
ds_read_b128 v[92:95],v58 offset:61440
ds_read_b128 v[96:99],v58 offset:63488
ds_read_b128 v[100:103],v59 offset:61440
ds_read_b128 v[104:107],v59 offset:63488
v_mfma_f32_16x16x32_bf16 v[40:43],v[84:87],v[228:231],v[40:43]
v_mfma_f32_16x16x32_bf16 v[44:47],v[220:223],v[88:91],v[44:47]
v_mfma_f32_16x16x32_bf16 v[48:51],v[224:227],v[88:91],v[48:51]
v_mfma_f32_16x16x32_bf16 v[52:55],v[88:91],v[228:231],v[52:55]
s_xor_b32 s64,s64,s65
s_waitcnt lgkmcnt(0)
v_mfma_f32_16x16x32_bf16 v[20:23],v[232:235],v[16:19],v[20:23]
v_mfma_f32_16x16x32_bf16 v[24:27],v[236:239],v[16:19],v[24:27]
v_mfma_f32_16x16x32_bf16 v[32:35],v[16:19],v[240:243],v[32:35]
v_mfma_f32_16x16x32_bf16 v[36:39],v[236:239],v[92:95],v[36:39]
v_mfma_f32_16x16x32_bf16 v[40:43],v[92:95],v[240:243],v[40:43]
v_mfma_f32_16x16x32_bf16 v[44:47],v[232:235],v[96:99],v[44:47]
v_mfma_f32_16x16x32_bf16 v[48:51],v[236:239],v[96:99],v[48:51]
v_mfma_f32_16x16x32_bf16 v[52:55],v[96:99],v[240:243],v[52:55]
v_mfma_f32_16x16x32_bf16 v[20:23],v[244:247],v[16:19],v[20:23]
v_mfma_f32_16x16x32_bf16 v[24:27],v[248:251],v[16:19],v[24:27]
v_mfma_f32_16x16x32_bf16 v[32:35],v[16:19],v[252:255],v[32:35]
v_mfma_f32_16x16x32_bf16 v[36:39],v[248:251],v[100:103],v[36:39]
v_mfma_f32_16x16x32_bf16 v[40:43],v[100:103],v[252:255],v[40:43]
v_mfma_f32_16x16x32_bf16 v[44:47],v[244:247],v[104:107],v[44:47]
v_mfma_f32_16x16x32_bf16 v[48:51],v[248:251],v[104:107],v[48:51]
v_mfma_f32_16x16x32_bf16 v[52:55],v[104:107],v[252:255],v[52:55]
s_nop 2
v_mov_b32 v28,v32
s_waitcnt vmcnt(19)
v_add_f32 v108,v108,v109
v_add_f32 v110,v110,v111
v_add_f32 v108,v108,v110
s_nop 1
v_add_f32_dpp v108,v108,v108 row_ror:8 row_mask:0xf bank_mask:0xf
s_nop 1
v_add_f32_dpp v108,v108,v108 row_ror:4 row_mask:0xf bank_mask:0xf
s_nop 1
v_add_f32_dpp v108,v108,v108 row_ror:2 row_mask:0xf bank_mask:0xf
v_mul_f32 v108,0x3a800000,v108
v_mul_f32 v109,v108,v108
s_nop 1
v_sub_f32_dpp v109,v108,v109 quad_perm:[1,1,3,3] row_mask:0xf bank_mask:0xf
v_max_f32 v109,0,v109
v_cvt_pk_bf16_f32 v109,0,v109
v_add_f32 v109,0x3727c5ac,v109
v_cvt_pk_bf16_f32 v109,0,v109
v_sqrt_f32 v109,v109
v_cvt_pk_bf16_f32 v108,0,v108
v_cvt_pk_bf16_f32 v109,0,v109
v_rcp_f32 v109,v109
s_nop 0
ds_write2_b32 v8,v108,v109 offset0:0 offset1:16
v_add_f32 v112,v112,v113
v_add_f32 v114,v114,v115
v_add_f32 v112,v112,v114
s_nop 1
v_add_f32_dpp v112,v112,v112 row_ror:8 row_mask:0xf bank_mask:0xf
s_nop 1
v_add_f32_dpp v112,v112,v112 row_ror:4 row_mask:0xf bank_mask:0xf
s_nop 1
v_add_f32_dpp v112,v112,v112 row_ror:2 row_mask:0xf bank_mask:0xf
v_mul_f32 v112,0x3a800000,v112
v_mul_f32 v113,v112,v112
s_nop 1
v_sub_f32_dpp v113,v112,v113 quad_perm:[1,1,3,3] row_mask:0xf bank_mask:0xf
v_max_f32 v113,0,v113
v_cvt_pk_bf16_f32 v113,0,v113
v_add_f32 v113,0x3727c5ac,v113
v_cvt_pk_bf16_f32 v113,0,v113
v_sqrt_f32 v113,v113
v_cvt_pk_bf16_f32 v112,0,v112
v_cvt_pk_bf16_f32 v113,0,v113
v_rcp_f32 v113,v113
s_nop 0
ds_write2_b32 v8,v112,v113 offset0:32 offset1:48
s_waitcnt lgkmcnt(0)
s_barrier
ds_read2_b32 v[14:15],v9 offset0:0 offset1:16
ds_read_b128 v[92:95],v10 offset:0
ds_read_b128 v[96:99],v10 offset:64
s_add_u32 s68,s68,1187840
s_waitcnt lgkmcnt(0)
v_mul_f32 v29,0x3e8293ee,v15
v_fma_f32 v36,-v14,v24,v36
v_fma_f32 v37,-v14,v25,v37
v_fma_f32 v38,-v14,v26,v38
v_fma_f32 v39,-v14,v27,v39
v_fma_f32 v32,-v14,v20,v32
v_fma_f32 v33,-v14,v21,v33
v_fma_f32 v34,-v14,v22,v34
v_fma_f32 v35,-v14,v23,v35
v_mul_f32 v36,v36,v15
v_mul_f32 v37,v37,v15
v_mul_f32 v38,v38,v15
v_mul_f32 v39,v39,v15
v_mul_f32 v32,v32,v29
v_mul_f32 v33,v33,v29
v_mul_f32 v34,v34,v29
v_mul_f32 v35,v35,v29
v_cvt_pk_bf16_f32 v104,v36,v37
v_cvt_pk_bf16_f32 v105,v38,v39
v_cvt_pk_bf16_f32 v100,v32,v33
v_cvt_pk_bf16_f32 v101,v34,v35
ds_write2st64_b64 v11,v[100:101],v[104:105] offset0:0 offset1:1
v_fma_f32 v40,-v92,v28,v40
v_fma_f32 v41,-v93,v28,v41
v_fma_f32 v42,-v94,v28,v42
v_fma_f32 v43,-v95,v28,v43
v_mul_f32 v40,v40,v96
v_mul_f32 v41,v41,v97
v_mul_f32 v42,v42,v98
v_mul_f32 v43,v43,v99
s_waitcnt lgkmcnt(0)
s_barrier
ds_read_b64 v[102:103],v3
ds_read_b64 v[106:107],v3 offset:512
v_cvt_pk_bf16_f32 v112,v40,v41
v_cvt_pk_bf16_f32 v113,v42,v43
s_waitcnt lgkmcnt(0)
s_waitcnt vmcnt(9)
s_barrier
ds_read_b128 v[56:59],v4 offset:0
ds_read_b128 v[60:63],v4 offset:1152
ds_read_b128 v[64:67],v4 offset:2304
ds_read_b128 v[68:71],v4 offset:3456
ds_read_b128 v[72:75],v4 offset:4608
ds_read_b128 v[76:79],v4 offset:5760
.L_qgo:
.p2align 6
.L_ql:
s_bitcmp1_b64 s[58:59],s62
s_cbranch_scc1 .L_qs0
.L_qb0:
s_waitcnt lgkmcnt(2)
v_mfma_f32_16x16x32_bf16 v[32:35],a[0:3],v[56:59],0
ds_read_b128 v[80:83],v4 offset:6912
ds_read2_b32 v[14:15],v9 offset0:32 offset1:48
v_mfma_f32_16x16x32_bf16 v[32:35],a[12:15],v[60:63],v[32:35]
ds_read_b128 v[92:95],v10 offset:128
ds_read_b128 v[96:99],v10 offset:192
v_mfma_f32_16x16x32_bf16 v[36:39],a[16:19],v[60:63],0
s_add_u32 s68,s68,1187840
v_mfma_f32_16x16x32_bf16 v[36:39],a[4:7],v[56:59],v[36:39]
ds_read_b128 v[84:87],v4 offset:8064
v_mfma_f32_16x16x32_bf16 v[40:43],v[56:59],a[8:11],0
ds_read2st64_b32 v[88:89],v6 offset0:0 offset1:36
v_mfma_f32_16x16x32_bf16 v[40:43],v[60:63],a[20:23],v[40:43]
s_waitcnt vmcnt(4)
v_cmp_ne_u64 vcc,v[30:31],v[12:13]
v_mfma_f32_16x16x32_bf16 v[40:43],v[64:67],a[32:35],v[40:43]
ds_read_b128 v[56:59],v4 offset:9216
ds_read2st64_b32 v[90:91],v6 offset0:72 offset1:108
v_mfma_f32_16x16x32_bf16 v[40:43],v[68:71],a[44:47],v[40:43]
s_cbranch_vccnz .L_rtq0
.L_bkq0:
s_add_u32 s63,s66,s71
v_mfma_f32_16x16x32_bf16 v[36:39],a[40:43],v[68:71],v[36:39]
s_add_u32 s66,s66,s72
s_add_u32 m0,s64,0
v_mfma_f32_16x16x32_bf16 v[36:39],a[28:31],v[64:67],v[36:39]
ds_read_b128 v[60:63],v4 offset:10368
buffer_load_dwordx4 v2,s[32:35],s63 offen offset:0 sc1 lds
v_mfma_f32_16x16x32_bf16 v[32:35],a[24:27],v[64:67],v[32:35]
s_waitcnt lgkmcnt(5)
v_mul_f32 v29,0x3e8293ee,v15
v_mfma_f32_16x16x32_bf16 v[32:35],a[36:39],v[68:71],v[32:35]
v_fma_f32 v48,-v14,v24,v48
s_waitcnt lgkmcnt(4)
v_mfma_f32_16x16x32_bf16 v[32:35],a[48:51],v[72:75],v[32:35]
ds_read_b128 v[64:67],v4 offset:11520
v_fma_f32 v49,-v14,v25,v49
v_mfma_f32_16x16x32_bf16 v[32:35],a[60:63],v[76:79],v[32:35]
v_fma_f32 v50,-v14,v26,v50
v_fma_f32 v51,-v14,v27,v51
v_mfma_f32_16x16x32_bf16 v[36:39],a[64:67],v[76:79],v[36:39]
v_fma_f32 v44,-v14,v20,v44
v_fma_f32 v45,-v14,v21,v45
v_mfma_f32_16x16x32_bf16 v[36:39],a[52:55],v[72:75],v[36:39]
ds_read_b128 v[68:71],v4 offset:12672
v_fma_f32 v46,-v14,v22,v46
v_mfma_f32_16x16x32_bf16 v[40:43],v[72:75],a[56:59],v[40:43]
buffer_load_dwordx4 v2,s[32:35],s63 offen offset:1024 sc1 lds
v_fma_f32 v47,-v14,v23,v47
v_mfma_f32_16x16x32_bf16 v[40:43],v[76:79],a[68:71],v[40:43]
v_mul_f32 v48,v48,v15
v_mul_f32 v49,v49,v15
v_mfma_f32_16x16x32_bf16 v[40:43],v[80:83],a[80:83],v[40:43]
ds_read_b128 v[72:75],v4 offset:13824
v_mul_f32 v50,v50,v15
v_mfma_f32_16x16x32_bf16 v[40:43],v[84:87],a[92:95],v[40:43]
v_mul_f32 v51,v51,v15
v_mul_f32 v44,v44,v29
v_mfma_f32_16x16x32_bf16 v[36:39],a[88:91],v[84:87],v[36:39]
v_mul_f32 v45,v45,v29
v_mul_f32 v46,v46,v29
v_mfma_f32_16x16x32_bf16 v[36:39],a[76:79],v[80:83],v[36:39]
ds_read_b128 v[76:79],v4 offset:14976
v_mul_f32 v47,v47,v29
v_mfma_f32_16x16x32_bf16 v[32:35],a[72:75],v[80:83],v[32:35]
v_cvt_pk_bf16_f32 v108,v48,v49
v_cvt_pk_bf16_f32 v109,v50,v51
v_mfma_f32_16x16x32_bf16 v[32:35],a[84:87],v[84:87],v[32:35]
v_cvt_pk_bf16_f32 v100,v44,v45
s_waitcnt lgkmcnt(2)
v_mfma_f32_16x16x32_bf16 v[32:35],a[96:99],v[56:59],v[32:35]
ds_read_b128 v[80:83],v4 offset:16128
buffer_load_dwordx4 v2,s[32:35],s63 offen offset:2048 sc1 lds
v_mfma_f32_16x16x32_bf16 v[32:35],a[108:111],v[60:63],v[32:35]
v_cvt_pk_bf16_f32 v101,v46,v47
v_mfma_f32_16x16x32_bf16 v[36:39],a[112:115],v[60:63],v[36:39]
ds_write2st64_b64 v11,v[100:101],v[108:109] offset0:0 offset1:1
v_fma_f32 v52,-v92,v28,v52
v_mfma_f32_16x16x32_bf16 v[36:39],a[100:103],v[56:59],v[36:39]
ds_read_b128 v[84:87],v4 offset:17280
v_fma_f32 v53,-v93,v28,v53
v_mfma_f32_16x16x32_bf16 v[40:43],v[56:59],a[104:107],v[40:43]
v_fma_f32 v54,-v94,v28,v54
v_fma_f32 v55,-v95,v28,v55
v_mfma_f32_16x16x32_bf16 v[40:43],v[60:63],a[116:119],v[40:43]
v_mul_f32 v52,v52,v96
v_mul_f32 v53,v53,v97
v_mfma_f32_16x16x32_bf16 v[40:43],v[64:67],a[128:131],v[40:43]
ds_read_b128 v[56:59],v4 offset:18432
v_mul_f32 v54,v54,v98
v_mfma_f32_16x16x32_bf16 v[40:43],v[68:71],a[140:143],v[40:43]
v_mul_f32 v55,v55,v99
s_waitcnt lgkmcnt(2)
v_mfma_f32_16x16x32_bf16 v[36:39],a[136:139],v[68:71],v[36:39]
buffer_load_dwordx4 v2,s[32:35],s63 offen offset:3072 sc1 lds
s_waitcnt vmcnt(4)
v_mfma_f32_16x16x32_bf16 v[36:39],a[124:127],v[64:67],v[36:39]
ds_read_b128 v[60:63],v4 offset:19584
s_barrier
v_mfma_f32_16x16x32_bf16 v[32:35],a[120:123],v[64:67],v[32:35]
ds_read_b64 v[102:103],v3
ds_read_b64 v[110:111],v3 offset:512
v_mfma_f32_16x16x32_bf16 v[32:35],a[132:135],v[68:71],v[32:35]
buffer_store_dwordx2 v[30:31],v2,s[92:95],s67 idxen sc1
s_waitcnt lgkmcnt(4)
v_mfma_f32_16x16x32_bf16 v[32:35],a[144:147],v[72:75],v[32:35]
ds_read_b128 v[64:67],v4 offset:20736
v_cvt_pk_bf16_f32 v114,v52,v53
v_mfma_f32_16x16x32_bf16 v[32:35],a[156:159],v[76:79],v[32:35]
v_cvt_pk_bf16_f32 v115,v54,v55
v_add_f32 v88,v88,v89
v_mfma_f32_16x16x32_bf16 v[36:39],a[160:163],v[76:79],v[36:39]
v_add_f32 v90,v90,v91
v_mfma_f32_16x16x32_bf16 v[36:39],a[148:151],v[72:75],v[36:39]
ds_read_b128 v[68:71],v4 offset:21888
s_add_u32 s63,s63,4096
v_mfma_f32_16x16x32_bf16 v[40:43],v[72:75],a[152:155],v[40:43]
s_add_u32 m0,s64,4096
v_add_f32 v88,v88,v90
v_mfma_f32_16x16x32_bf16 v[40:43],v[76:79],a[164:167],v[40:43]
buffer_load_dwordx4 v2,s[32:35],s63 offen offset:0 sc1 lds
v_add_f32_dpp v88,v88,v88 row_ror:8 row_mask:0xf bank_mask:0xf
v_mfma_f32_16x16x32_bf16 v[40:43],v[80:83],a[176:179],v[40:43]
ds_read_b128 v[72:75],v4 offset:23040
s_waitcnt lgkmcnt(3)
)#"
R"#(
v_mfma_f32_16x16x32_bf16 v[48:51],v[104:107],v[100:103],v[120:123]
v_add_f32_dpp v88,v88,v88 row_ror:4 row_mask:0xf bank_mask:0xf
v_mfma_f32_16x16x32_bf16 v[44:47],v[108:111],v[100:103],v[116:119]
v_mfma_f32_16x16x32_bf16 v[40:43],v[84:87],a[188:191],v[40:43]
v_add_f32_dpp v88,v88,v88 row_ror:2 row_mask:0xf bank_mask:0xf
v_mfma_f32_16x16x32_bf16 v[36:39],a[184:187],v[84:87],v[36:39]
v_mul_f32 v88,0x3a800000,v88
v_mfma_f32_16x16x32_bf16 v[36:39],a[172:175],v[80:83],v[36:39]
ds_read_b128 v[76:79],v4 offset:24192
v_mul_f32 v89,v88,v88
v_mfma_f32_16x16x32_bf16 v[32:35],a[168:171],v[80:83],v[32:35]
v_max3_f32 v44,v44,v45,v48
v_max3_f32 v46,v46,v47,v50
v_mfma_f32_16x16x32_bf16 v[32:35],a[180:183],v[84:87],v[32:35]
v_max_f32 v44,v44,v49
s_waitcnt lgkmcnt(2)
v_mfma_f32_16x16x32_bf16 v[32:35],a[192:195],v[56:59],v[32:35]
ds_read_b128 v[80:83],v4 offset:25344
buffer_load_dwordx4 v2,s[32:35],s63 offen offset:1024 sc1 lds
v_mfma_f32_16x16x32_bf16 v[32:35],a[204:207],v[60:63],v[32:35]
v_max_f32 v46,v46,v51
v_sub_f32_dpp v89,v88,v89 quad_perm:[1,1,3,3] row_mask:0xf bank_mask:0xf
v_mfma_f32_16x16x32_bf16 v[36:39],a[208:211],v[60:63],v[36:39]
buffer_load_dwordx2 v[12:13],v0,s[32:35],s66 offen sc1
v_max_f32 v96,v44,v46
v_mfma_f32_16x16x32_bf16 v[36:39],a[196:199],v[56:59],v[36:39]
ds_read_b128 v[84:87],v4 offset:26496
v_max_f32 v97,v44,v46
v_mfma_f32_16x16x32_bf16 v[40:43],v[56:59],a[200:203],v[40:43]
v_max_f32 v89,0,v89
v_cvt_pk_bf16_f32 v88,0,v88
v_mfma_f32_16x16x32_bf16 v[40:43],v[60:63],a[212:215],v[40:43]
v_permlane16_swap_b32 v96,v97
v_mfma_f32_16x16x32_bf16 v[40:43],v[64:67],a[224:227],v[40:43]
ds_read_b128 v[56:59],v4 offset:27648
v_max_f32 v98,v96,v97
v_mfma_f32_16x16x32_bf16 v[40:43],v[68:71],a[236:239],v[40:43]
v_max_f32 v99,v96,v97
v_cvt_pk_bf16_f32 v89,0,v89
v_mfma_f32_16x16x32_bf16 v[36:39],a[232:235],v[68:71],v[36:39]
buffer_load_dwordx4 v2,s[32:35],s63 offen offset:2048 sc1 lds
v_add_f32 v89,0x3727c5ac,v89
v_mfma_f32_16x16x32_bf16 v[36:39],a[220:223],v[64:67],v[36:39]
ds_read_b128 v[60:63],v4 offset:28800
v_cvt_pk_bf16_f32 v89,0,v89
v_mfma_f32_16x16x32_bf16 v[32:35],a[216:219],v[64:67],v[32:35]
v_permlane32_swap_b32 v98,v99
v_mfma_f32_16x16x32_bf16 v[32:35],a[228:231],v[68:71],v[32:35]
v_max_f32 v96,v98,v99
s_waitcnt lgkmcnt(2)
v_mfma_f32_16x16x32_bf16 v[32:35],a[240:243],v[72:75],v[32:35]
ds_read_b128 v[64:67],v4 offset:29952
v_sub_f32 v44,v44,v96
v_mfma_f32_16x16x32_bf16 v[32:35],a[252:255],v[76:79],v[32:35]
v_exp_f32 v44,v44
v_mfma_f32_16x16x32_bf16 v[36:39],v[128:131],v[76:79],v[36:39]
v_sqrt_f32 v89,v89
v_mfma_f32_16x16x32_bf16 v[36:39],a[244:247],v[72:75],v[36:39]
ds_read_b128 v[68:71],v4 offset:31104
v_sub_f32 v46,v46,v96
v_mfma_f32_16x16x32_bf16 v[40:43],v[72:75],a[248:251],v[40:43]
buffer_load_dwordx4 v2,s[32:35],s63 offen offset:3072 sc1 lds
v_cvt_pk_bf16_f32 v44,v44,v44
v_mfma_f32_16x16x32_bf16 v[40:43],v[76:79],v[132:135],v[40:43]
v_exp_f32 v46,v46
v_mfma_f32_16x16x32_bf16 v[40:43],v[80:83],v[144:147],v[40:43]
ds_read_b128 v[72:75],v4 offset:32256
v_cvt_pk_bf16_f32 v46,v46,v46
v_mfma_f32_16x16x32_bf16 v[40:43],v[84:87],v[156:159],v[40:43]
v_and_b32 v94,v44,v124
v_and_b32 v92,v44,v126
v_mfma_f32_16x16x32_bf16 v[36:39],v[152:155],v[84:87],v[36:39]
v_and_b32 v95,v46,v125
v_and_b32 v93,v46,v127
v_mfma_f32_16x16x32_bf16 v[36:39],v[140:143],v[80:83],v[36:39]
ds_read_b128 v[76:79],v4 offset:33408
v_cvt_pk_bf16_f32 v89,0,v89
v_mfma_f32_16x16x32_bf16 v[48:51],v[16:19],v[92:95],0
v_rcp_f32 v89,v89
v_mfma_f32_16x16x32_bf16 v[52:55],v[112:115],v[92:95],0
ds_write2_b32 v8,v88,v89 offset0:0 offset1:16
v_mfma_f32_16x16x32_bf16 v[32:35],v[136:139],v[80:83],v[32:35]
v_mfma_f32_16x16x32_bf16 v[32:35],v[148:151],v[84:87],v[32:35]
s_add_u32 s63,s63,4096
s_waitcnt lgkmcnt(3)
v_mfma_f32_16x16x32_bf16 v[32:35],v[160:163],v[56:59],v[32:35]
ds_read_b128 v[80:83],v4 offset:34560
s_add_u32 m0,s64,8192
v_mfma_f32_16x16x32_bf16 v[32:35],v[172:175],v[60:63],v[32:35]
buffer_load_dwordx4 v2,s[32:35],s63 offen offset:0 sc1 lds
v_mfma_f32_16x16x32_bf16 v[36:39],v[176:179],v[60:63],v[36:39]
v_rcp_f32 v48,v48
v_mfma_f32_16x16x32_bf16 v[36:39],v[164:167],v[56:59],v[36:39]
ds_read_b128 v[84:87],v4 offset:35712
v_mul_f32 v52,v52,v48
v_mfma_f32_16x16x32_bf16 v[40:43],v[56:59],v[168:171],v[40:43]
v_xor_b32 v4,73728,v4
v_mul_f32 v53,v53,v48
v_mfma_f32_16x16x32_bf16 v[40:43],v[60:63],v[180:183],v[40:43]
s_waitcnt lgkmcnt(2)
s_barrier
v_mfma_f32_16x16x32_bf16 v[40:43],v[64:67],v[192:195],v[40:43]
ds_read_b128 v[56:59],v5 offset:0
v_mul_f32 v54,v54,v48
v_mfma_f32_16x16x32_bf16 v[40:43],v[68:71],v[204:207],v[40:43]
v_mul_f32 v55,v55,v48
v_cvt_pk_bf16_f32 v100,v52,v53
v_mfma_f32_16x16x32_bf16 v[36:39],v[200:203],v[68:71],v[36:39]
v_cvt_pk_bf16_f32 v101,v54,v55
v_mfma_f32_16x16x32_bf16 v[36:39],v[188:191],v[64:67],v[36:39]
ds_read_b128 v[60:63],v5 offset:1152
v_mfma_f32_16x16x32_bf16 v[32:35],v[184:187],v[64:67],v[32:35]
v_permlane32_swap_b32 v100,v102
v_mfma_f32_16x16x32_bf16 v[32:35],v[196:199],v[68:71],v[32:35]
s_waitcnt lgkmcnt(2)
v_mfma_f32_16x16x32_bf16 v[32:35],v[208:211],v[72:75],v[32:35]
ds_read_b128 v[64:67],v5 offset:2304
v_mfma_f32_16x16x32_bf16 v[32:35],v[220:223],v[76:79],v[32:35]
v_permlane32_swap_b32 v101,v103
v_mfma_f32_16x16x32_bf16 v[36:39],v[224:227],v[76:79],v[36:39]
buffer_store_dwordx4 v[100:103],v1,s[32:35],s68 offen sc1
s_mov_b32 s67,s68
v_mfma_f32_16x16x32_bf16 v[36:39],v[212:215],v[72:75],v[36:39]
ds_read_b128 v[68:71],v5 offset:3456
v_mfma_f32_16x16x32_bf16 v[40:43],v[72:75],v[216:219],v[40:43]
v_mfma_f32_16x16x32_bf16 v[40:43],v[76:79],v[228:231],v[40:43]
v_mfma_f32_16x16x32_bf16 v[40:43],v[80:83],v[240:243],v[40:43]
ds_read_b128 v[72:75],v5 offset:4608
v_mfma_f32_16x16x32_bf16 v[40:43],v[84:87],v[252:255],v[40:43]
v_mfma_f32_16x16x32_bf16 v[36:39],v[248:251],v[84:87],v[36:39]
v_mfma_f32_16x16x32_bf16 v[36:39],v[236:239],v[80:83],v[36:39]
ds_read_b128 v[76:79],v5 offset:5760
v_mfma_f32_16x16x32_bf16 v[32:35],v[232:235],v[80:83],v[32:35]
v_mfma_f32_16x16x32_bf16 v[32:35],v[244:247],v[84:87],v[32:35]
s_bitcmp1_b64 s[60:61],s62
s_cbranch_scc1 .L_qs1
.L_qb1:
s_waitcnt lgkmcnt(2)
v_mfma_f32_16x16x32_bf16 v[44:47],a[0:3],v[56:59],0
ds_read_b128 v[80:83],v5 offset:6912
ds_read2_b32 v[14:15],v9 offset0:0 offset1:16
v_mfma_f32_16x16x32_bf16 v[44:47],a[12:15],v[60:63],v[44:47]
ds_read_b128 v[92:95],v10 offset:0
ds_read_b128 v[96:99],v10 offset:64
v_mfma_f32_16x16x32_bf16 v[48:51],a[16:19],v[60:63],0
s_add_u32 s68,s68,1187840
v_mfma_f32_16x16x32_bf16 v[48:51],a[4:7],v[56:59],v[48:51]
ds_read_b128 v[84:87],v5 offset:8064
v_mfma_f32_16x16x32_bf16 v[52:55],v[56:59],a[8:11],0
ds_read2st64_b32 v[88:89],v6 offset0:144 offset1:180
v_mfma_f32_16x16x32_bf16 v[52:55],v[60:63],a[20:23],v[52:55]
s_waitcnt vmcnt(4)
v_cmp_ne_u64 vcc,v[30:31],v[12:13]
v_mfma_f32_16x16x32_bf16 v[52:55],v[64:67],a[32:35],v[52:55]
ds_read_b128 v[56:59],v5 offset:9216
ds_read2st64_b32 v[90:91],v6 offset0:216 offset1:252
v_mfma_f32_16x16x32_bf16 v[52:55],v[68:71],a[44:47],v[52:55]
s_cbranch_vccnz .L_rtq1
.L_bkq1:
s_add_u32 s63,s66,s71
v_mfma_f32_16x16x32_bf16 v[48:51],a[40:43],v[68:71],v[48:51]
s_add_u32 s66,s66,s72
s_add_u32 m0,s64,36864
v_mfma_f32_16x16x32_bf16 v[48:51],a[28:31],v[64:67],v[48:51]
ds_read_b128 v[60:63],v5 offset:10368
buffer_load_dwordx4 v2,s[32:35],s63 offen offset:0 sc1 lds
v_mfma_f32_16x16x32_bf16 v[44:47],a[24:27],v[64:67],v[44:47]
v_xor_b32 v6,v7,v6
s_waitcnt lgkmcnt(5)
v_mfma_f32_16x16x32_bf16 v[44:47],a[36:39],v[68:71],v[44:47]
v_mul_f32 v29,0x3e8293ee,v15
s_waitcnt lgkmcnt(4)
v_mfma_f32_16x16x32_bf16 v[44:47],a[48:51],v[72:75],v[44:47]
ds_read_b128 v[64:67],v5 offset:11520
v_fma_f32 v36,-v14,v24,v36
v_mfma_f32_16x16x32_bf16 v[44:47],a[60:63],v[76:79],v[44:47]
v_fma_f32 v37,-v14,v25,v37
v_fma_f32 v38,-v14,v26,v38
v_mfma_f32_16x16x32_bf16 v[48:51],a[64:67],v[76:79],v[48:51]
v_fma_f32 v39,-v14,v27,v39
v_fma_f32 v32,-v14,v20,v32
v_mfma_f32_16x16x32_bf16 v[48:51],a[52:55],v[72:75],v[48:51]
ds_read_b128 v[68:71],v5 offset:12672
v_fma_f32 v33,-v14,v21,v33
v_mfma_f32_16x16x32_bf16 v[52:55],v[72:75],a[56:59],v[52:55]
buffer_load_dwordx4 v2,s[32:35],s63 offen offset:1024 sc1 lds
v_fma_f32 v34,-v14,v22,v34
v_mfma_f32_16x16x32_bf16 v[52:55],v[76:79],a[68:71],v[52:55]
v_fma_f32 v35,-v14,v23,v35
v_mul_f32 v36,v36,v15
v_mfma_f32_16x16x32_bf16 v[52:55],v[80:83],a[80:83],v[52:55]
ds_read_b128 v[72:75],v5 offset:13824
v_mul_f32 v37,v37,v15
v_mfma_f32_16x16x32_bf16 v[52:55],v[84:87],a[92:95],v[52:55]
v_mul_f32 v38,v38,v15
v_mul_f32 v39,v39,v15
v_mfma_f32_16x16x32_bf16 v[48:51],a[88:91],v[84:87],v[48:51]
v_mul_f32 v32,v32,v29
v_mul_f32 v33,v33,v29
v_mfma_f32_16x16x32_bf16 v[48:51],a[76:79],v[80:83],v[48:51]
ds_read_b128 v[76:79],v5 offset:14976
v_mul_f32 v34,v34,v29
v_mfma_f32_16x16x32_bf16 v[44:47],a[72:75],v[80:83],v[44:47]
v_mul_f32 v35,v35,v29
v_cvt_pk_bf16_f32 v104,v36,v37
v_mfma_f32_16x16x32_bf16 v[44:47],a[84:87],v[84:87],v[44:47]
v_cvt_pk_bf16_f32 v105,v38,v39
s_waitcnt lgkmcnt(2)
v_mfma_f32_16x16x32_bf16 v[44:47],a[96:99],v[56:59],v[44:47]
ds_read_b128 v[80:83],v5 offset:16128
buffer_load_dwordx4 v2,s[32:35],s63 offen offset:2048 sc1 lds
v_mfma_f32_16x16x32_bf16 v[44:47],a[108:111],v[60:63],v[44:47]
v_cvt_pk_bf16_f32 v100,v32,v33
v_cvt_pk_bf16_f32 v101,v34,v35
v_mfma_f32_16x16x32_bf16 v[48:51],a[112:115],v[60:63],v[48:51]
ds_write2st64_b64 v11,v[100:101],v[104:105] offset0:0 offset1:1
v_fma_f32 v40,-v92,v28,v40
v_mfma_f32_16x16x32_bf16 v[48:51],a[100:103],v[56:59],v[48:51]
ds_read_b128 v[84:87],v5 offset:17280
v_fma_f32 v41,-v93,v28,v41
v_mfma_f32_16x16x32_bf16 v[52:55],v[56:59],a[104:107],v[52:55]
v_fma_f32 v42,-v94,v28,v42
v_fma_f32 v43,-v95,v28,v43
v_mfma_f32_16x16x32_bf16 v[52:55],v[60:63],a[116:119],v[52:55]
v_mul_f32 v40,v40,v96
v_mul_f32 v41,v41,v97
v_mfma_f32_16x16x32_bf16 v[52:55],v[64:67],a[128:131],v[52:55]
ds_read_b128 v[56:59],v5 offset:18432
v_mul_f32 v42,v42,v98
v_mfma_f32_16x16x32_bf16 v[52:55],v[68:71],a[140:143],v[52:55]
v_mul_f32 v43,v43,v99
s_waitcnt lgkmcnt(2)
v_mfma_f32_16x16x32_bf16 v[48:51],a[136:139],v[68:71],v[48:51]
buffer_load_dwordx4 v2,s[32:35],s63 offen offset:3072 sc1 lds
s_waitcnt vmcnt(4)
v_mfma_f32_16x16x32_bf16 v[48:51],a[124:127],v[64:67],v[48:51]
ds_read_b128 v[60:63],v5 offset:19584
s_barrier
v_mfma_f32_16x16x32_bf16 v[44:47],a[120:123],v[64:67],v[44:47]
ds_read_b64 v[102:103],v3
ds_read_b64 v[106:107],v3 offset:512
v_mfma_f32_16x16x32_bf16 v[44:47],a[132:135],v[68:71],v[44:47]
buffer_store_dwordx2 v[30:31],v2,s[92:95],s67 idxen sc1
s_waitcnt lgkmcnt(4)
v_mfma_f32_16x16x32_bf16 v[44:47],a[144:147],v[72:75],v[44:47]
ds_read_b128 v[64:67],v5 offset:20736
v_cvt_pk_bf16_f32 v112,v40,v41
v_mfma_f32_16x16x32_bf16 v[44:47],a[156:159],v[76:79],v[44:47]
v_cvt_pk_bf16_f32 v113,v42,v43
v_add_f32 v88,v88,v89
v_mfma_f32_16x16x32_bf16 v[48:51],a[160:163],v[76:79],v[48:51]
v_add_f32 v90,v90,v91
v_mfma_f32_16x16x32_bf16 v[48:51],a[148:151],v[72:75],v[48:51]
ds_read_b128 v[68:71],v5 offset:21888
s_add_u32 s63,s63,4096
v_mfma_f32_16x16x32_bf16 v[52:55],v[72:75],a[152:155],v[52:55]
s_add_u32 m0,s64,40960
v_add_f32 v88,v88,v90
v_mfma_f32_16x16x32_bf16 v[52:55],v[76:79],a[164:167],v[52:55]
buffer_load_dwordx4 v2,s[32:35],s63 offen offset:0 sc1 lds
v_add_f32_dpp v88,v88,v88 row_ror:8 row_mask:0xf bank_mask:0xf
v_mfma_f32_16x16x32_bf16 v[52:55],v[80:83],a[176:179],v[52:55]
ds_read_b128 v[72:75],v5 offset:23040
s_waitcnt lgkmcnt(3)
v_mfma_f32_16x16x32_bf16 v[36:39],v[108:111],v[100:103],v[120:123]
v_add_f32_dpp v88,v88,v88 row_ror:4 row_mask:0xf bank_mask:0xf
v_mfma_f32_16x16x32_bf16 v[32:35],v[104:107],v[100:103],v[116:119]
v_mfma_f32_16x16x32_bf16 v[52:55],v[84:87],a[188:191],v[52:55]
v_add_f32_dpp v88,v88,v88 row_ror:2 row_mask:0xf bank_mask:0xf
v_mfma_f32_16x16x32_bf16 v[48:51],a[184:187],v[84:87],v[48:51]
v_mul_f32 v88,0x3a800000,v88
v_mfma_f32_16x16x32_bf16 v[48:51],a[172:175],v[80:83],v[48:51]
ds_read_b128 v[76:79],v5 offset:24192
v_mul_f32 v89,v88,v88
v_mfma_f32_16x16x32_bf16 v[44:47],a[168:171],v[80:83],v[44:47]
v_max3_f32 v32,v32,v33,v36
v_max3_f32 v34,v34,v35,v38
v_mfma_f32_16x16x32_bf16 v[44:47],a[180:183],v[84:87],v[44:47]
v_max_f32 v32,v32,v37
s_waitcnt lgkmcnt(2)
v_mfma_f32_16x16x32_bf16 v[44:47],a[192:195],v[56:59],v[44:47]
ds_read_b128 v[80:83],v5 offset:25344
buffer_load_dwordx4 v2,s[32:35],s63 offen offset:1024 sc1 lds
v_mfma_f32_16x16x32_bf16 v[44:47],a[204:207],v[60:63],v[44:47]
v_max_f32 v34,v34,v39
v_sub_f32_dpp v89,v88,v89 quad_perm:[1,1,3,3] row_mask:0xf bank_mask:0xf
v_mfma_f32_16x16x32_bf16 v[48:51],a[208:211],v[60:63],v[48:51]
buffer_load_dwordx2 v[12:13],v0,s[32:35],s66 offen sc1
v_max_f32 v96,v32,v34
v_mfma_f32_16x16x32_bf16 v[48:51],a[196:199],v[56:59],v[48:51]
ds_read_b128 v[84:87],v5 offset:26496
v_max_f32 v97,v32,v34
v_mfma_f32_16x16x32_bf16 v[52:55],v[56:59],a[200:203],v[52:55]
v_max_f32 v89,0,v89
v_cvt_pk_bf16_f32 v88,0,v88
v_mfma_f32_16x16x32_bf16 v[52:55],v[60:63],a[212:215],v[52:55]
v_permlane16_swap_b32 v96,v97
v_mfma_f32_16x16x32_bf16 v[52:55],v[64:67],a[224:227],v[52:55]
ds_read_b128 v[56:59],v5 offset:27648
v_max_f32 v98,v96,v97
v_mfma_f32_16x16x32_bf16 v[52:55],v[68:71],a[236:239],v[52:55]
v_max_f32 v99,v96,v97
v_cvt_pk_bf16_f32 v89,0,v89
v_mfma_f32_16x16x32_bf16 v[48:51],a[232:235],v[68:71],v[48:51]
buffer_load_dwordx4 v2,s[32:35],s63 offen offset:2048 sc1 lds
v_add_f32 v89,0x3727c5ac,v89
v_mfma_f32_16x16x32_bf16 v[48:51],a[220:223],v[64:67],v[48:51]
ds_read_b128 v[60:63],v5 offset:28800
v_cvt_pk_bf16_f32 v89,0,v89
v_mfma_f32_16x16x32_bf16 v[44:47],a[216:219],v[64:67],v[44:47]
v_permlane32_swap_b32 v98,v99
v_mfma_f32_16x16x32_bf16 v[44:47],a[228:231],v[68:71],v[44:47]
v_max_f32 v96,v98,v99
s_waitcnt lgkmcnt(2)
v_mfma_f32_16x16x32_bf16 v[44:47],a[240:243],v[72:75],v[44:47]
ds_read_b128 v[64:67],v5 offset:29952
v_sub_f32 v32,v32,v96
v_mfma_f32_16x16x32_bf16 v[44:47],a[252:255],v[76:79],v[44:47]
v_exp_f32 v32,v32
v_mfma_f32_16x16x32_bf16 v[48:51],v[128:131],v[76:79],v[48:51]
v_sqrt_f32 v89,v89
v_mfma_f32_16x16x32_bf16 v[48:51],a[244:247],v[72:75],v[48:51]
ds_read_b128 v[68:71],v5 offset:31104
v_sub_f32 v34,v34,v96
v_mfma_f32_16x16x32_bf16 v[52:55],v[72:75],a[248:251],v[52:55]
buffer_load_dwordx4 v2,s[32:35],s63 offen offset:3072 sc1 lds
v_cvt_pk_bf16_f32 v32,v32,v32
v_mfma_f32_16x16x32_bf16 v[52:55],v[76:79],v[132:135],v[52:55]
v_exp_f32 v34,v34
v_mfma_f32_16x16x32_bf16 v[52:55],v[80:83],v[144:147],v[52:55]
ds_read_b128 v[72:75],v5 offset:32256
v_cvt_pk_bf16_f32 v34,v34,v34
v_mfma_f32_16x16x32_bf16 v[52:55],v[84:87],v[156:159],v[52:55]
v_and_b32 v92,v32,v124
v_and_b32 v94,v32,v126
v_mfma_f32_16x16x32_bf16 v[48:51],v[152:155],v[84:87],v[48:51]
v_and_b32 v93,v34,v125
v_and_b32 v95,v34,v127
v_mfma_f32_16x16x32_bf16 v[48:51],v[140:143],v[80:83],v[48:51]
ds_read_b128 v[76:79],v5 offset:33408
v_cvt_pk_bf16_f32 v89,0,v89
v_mfma_f32_16x16x32_bf16 v[36:39],v[16:19],v[92:95],0
v_rcp_f32 v89,v89
v_mfma_f32_16x16x32_bf16 v[40:43],v[112:115],v[92:95],0
ds_write2_b32 v8,v88,v89 offset0:32 offset1:48
v_mfma_f32_16x16x32_bf16 v[44:47],v[136:139],v[80:83],v[44:47]
v_mfma_f32_16x16x32_bf16 v[44:47],v[148:151],v[84:87],v[44:47]
s_add_u32 s63,s63,4096
s_waitcnt lgkmcnt(3)
v_mfma_f32_16x16x32_bf16 v[44:47],v[160:163],v[56:59],v[44:47]
ds_read_b128 v[80:83],v5 offset:34560
s_add_u32 m0,s64,45056
v_mfma_f32_16x16x32_bf16 v[44:47],v[172:175],v[60:63],v[44:47]
buffer_load_dwordx4 v2,s[32:35],s63 offen offset:0 sc1 lds
s_xor_b32 s64,s64,s65
v_mfma_f32_16x16x32_bf16 v[48:51],v[176:179],v[60:63],v[48:51]
v_rcp_f32 v36,v36
v_mfma_f32_16x16x32_bf16 v[48:51],v[164:167],v[56:59],v[48:51]
ds_read_b128 v[84:87],v5 offset:35712
v_mul_f32 v40,v40,v36
v_mfma_f32_16x16x32_bf16 v[52:55],v[56:59],v[168:171],v[52:55]
v_xor_b32 v5,73728,v5
v_mul_f32 v41,v41,v36
v_mfma_f32_16x16x32_bf16 v[52:55],v[60:63],v[180:183],v[52:55]
s_waitcnt lgkmcnt(2)
s_barrier
v_mfma_f32_16x16x32_bf16 v[52:55],v[64:67],v[192:195],v[52:55]
ds_read_b128 v[56:59],v4 offset:0
v_mul_f32 v42,v42,v36
v_mfma_f32_16x16x32_bf16 v[52:55],v[68:71],v[204:207],v[52:55]
v_mul_f32 v43,v43,v36
v_cvt_pk_bf16_f32 v100,v40,v41
v_mfma_f32_16x16x32_bf16 v[48:51],v[200:203],v[68:71],v[48:51]
v_cvt_pk_bf16_f32 v101,v42,v43
v_mfma_f32_16x16x32_bf16 v[48:51],v[188:191],v[64:67],v[48:51]
ds_read_b128 v[60:63],v4 offset:1152
v_mfma_f32_16x16x32_bf16 v[44:47],v[184:187],v[64:67],v[44:47]
v_permlane32_swap_b32 v100,v102
v_mfma_f32_16x16x32_bf16 v[44:47],v[196:199],v[68:71],v[44:47]
s_waitcnt lgkmcnt(2)
v_mfma_f32_16x16x32_bf16 v[44:47],v[208:211],v[72:75],v[44:47]
ds_read_b128 v[64:67],v4 offset:2304
v_mfma_f32_16x16x32_bf16 v[44:47],v[220:223],v[76:79],v[44:47]
v_permlane32_swap_b32 v101,v103
v_mfma_f32_16x16x32_bf16 v[48:51],v[224:227],v[76:79],v[48:51]
buffer_store_dwordx4 v[100:103],v1,s[32:35],s68 offen sc1
s_mov_b32 s67,s68
v_mfma_f32_16x16x32_bf16 v[48:51],v[212:215],v[72:75],v[48:51]
ds_read_b128 v[68:71],v4 offset:3456
v_mfma_f32_16x16x32_bf16 v[52:55],v[72:75],v[216:219],v[52:55]
v_mfma_f32_16x16x32_bf16 v[52:55],v[76:79],v[228:231],v[52:55]
v_mfma_f32_16x16x32_bf16 v[52:55],v[80:83],v[240:243],v[52:55]
ds_read_b128 v[72:75],v4 offset:4608
v_mfma_f32_16x16x32_bf16 v[52:55],v[84:87],v[252:255],v[52:55]
v_mfma_f32_16x16x32_bf16 v[48:51],v[248:251],v[84:87],v[48:51]
v_mfma_f32_16x16x32_bf16 v[48:51],v[236:239],v[80:83],v[48:51]
ds_read_b128 v[76:79],v4 offset:5760
v_mfma_f32_16x16x32_bf16 v[44:47],v[232:235],v[80:83],v[44:47]
s_sub_u32 s62,s62,1
v_mfma_f32_16x16x32_bf16 v[44:47],v[244:247],v[84:87],v[44:47]
s_cbranch_scc0 .L_ql
s_nop 1
ds_read2_b32 v[14:15],v9 offset0:32 offset1:48
ds_read_b128 v[92:95],v10 offset:128
ds_read_b128 v[96:99],v10 offset:192
s_add_u32 s68,s68,1187840
s_waitcnt lgkmcnt(0)
v_mul_f32 v29,0x3e8293ee,v15
v_fma_f32 v48,-v14,v24,v48
v_fma_f32 v49,-v14,v25,v49
v_fma_f32 v50,-v14,v26,v50
v_fma_f32 v51,-v14,v27,v51
v_fma_f32 v44,-v14,v20,v44
v_fma_f32 v45,-v14,v21,v45
v_fma_f32 v46,-v14,v22,v46
v_fma_f32 v47,-v14,v23,v47
v_mul_f32 v48,v48,v15
v_mul_f32 v49,v49,v15
v_mul_f32 v50,v50,v15
v_mul_f32 v51,v51,v15
v_mul_f32 v44,v44,v29
v_mul_f32 v45,v45,v29
v_mul_f32 v46,v46,v29
v_mul_f32 v47,v47,v29
v_cvt_pk_bf16_f32 v108,v48,v49
v_cvt_pk_bf16_f32 v109,v50,v51
v_cvt_pk_bf16_f32 v100,v44,v45
v_cvt_pk_bf16_f32 v101,v46,v47
ds_write2st64_b64 v11,v[100:101],v[108:109] offset0:0 offset1:1
v_fma_f32 v52,-v92,v28,v52
v_fma_f32 v53,-v93,v28,v53
v_fma_f32 v54,-v94,v28,v54
v_fma_f32 v55,-v95,v28,v55
v_mul_f32 v52,v52,v96
v_mul_f32 v53,v53,v97
v_mul_f32 v54,v54,v98
v_mul_f32 v55,v55,v99
s_waitcnt lgkmcnt(0)
s_waitcnt vmcnt(0)
s_barrier
ds_read_b64 v[102:103],v3
ds_read_b64 v[110:111],v3 offset:512
buffer_store_dwordx2 v[30:31],v2,s[92:95],s67 idxen sc1
v_cvt_pk_bf16_f32 v114,v52,v53
v_cvt_pk_bf16_f32 v115,v54,v55
s_waitcnt lgkmcnt(0)
v_mfma_f32_16x16x32_bf16 v[48:51],v[104:107],v[100:103],v[120:123]
v_mfma_f32_16x16x32_bf16 v[44:47],v[108:111],v[100:103],v[116:119]
s_nop 7
v_max3_f32 v44,v44,v45,v48
v_max3_f32 v46,v46,v47,v50
v_max_f32 v44,v44,v49
v_max_f32 v46,v46,v51
v_max_f32 v96,v44,v46
v_max_f32 v97,v44,v46
s_nop 1
v_permlane16_swap_b32 v96,v97
v_max_f32 v98,v96,v97
v_max_f32 v99,v96,v97
s_nop 1
v_permlane32_swap_b32 v98,v99
v_max_f32 v96,v98,v99
v_sub_f32 v44,v44,v96
v_sub_f32 v46,v46,v96
v_exp_f32 v44,v44
v_exp_f32 v46,v46
v_cvt_pk_bf16_f32 v44,v44,v44
v_cvt_pk_bf16_f32 v46,v46,v46
v_and_b32 v94,v44,v124
v_and_b32 v92,v44,v126
v_and_b32 v95,v46,v125
v_and_b32 v93,v46,v127
s_nop 1
v_mfma_f32_16x16x32_bf16 v[48:51],v[16:19],v[92:95],0
v_mfma_f32_16x16x32_bf16 v[52:55],v[112:115],v[92:95],0
s_nop 6
v_rcp_f32 v48,v48
s_nop 0
v_mul_f32 v52,v52,v48
v_mul_f32 v53,v53,v48
v_mul_f32 v54,v54,v48
v_mul_f32 v55,v55,v48
v_cvt_pk_bf16_f32 v100,v52,v53
v_cvt_pk_bf16_f32 v101,v54,v55
s_nop 0
v_permlane32_swap_b32 v100,v102
v_permlane32_swap_b32 v101,v103
buffer_store_dwordx4 v[100:103],v1,s[32:35],s68 offen sc1
s_mov_b32 s67,s68
s_waitcnt vmcnt(0)
s_barrier
buffer_store_dwordx2 v[30:31],v2,s[92:95],s67 idxen sc1
s_branch .L_end
.L_w:
s_mov_b32 s96,0
s_add_u32 s36,s12,0x0
s_addc_u32 s37,s13,0x0
s_and_b32 s37,s37,0xffff
s_mov_b32 s38,0x200000
s_mov_b32 s39,0x20000
)#"
R"#(
s_add_u32 s40,s12,0xfffffc80
s_addc_u32 s41,s13,0xffffffff
s_and_b32 s41,s41,0xffff
s_mov_b32 s42,0x200380
s_mov_b32 s43,0x20000
s_lshl_b32 s48,s27,8
s_add_u32 s96,s96,s48
v_and_b32 v8,3,v1
v_mul_u32_u24 v8,16,v8
s_and_b32 s48,s26,1
s_lshl_b32 s48,s48,6
s_lshr_b32 s49,s26,1
s_lshl_b32 s49,s49,10
s_add_u32 s48,s48,s49
v_add_u32 v8,s48,v8
v_lshrrev_b32 v4,3,v1
v_bfe_u32 v5,v1,4,1
s_and_b32 s48,s26,1
s_lshl_b32 s48,s48,1
v_or_b32 v5,s48,v5
v_lshlrev_b32 v5,1,v5
v_and_b32 v6,7,v1
v_xor_b32 v5,v5,v6
v_mul_u32_u24 v4,2048,v4
v_lshl_add_u32 v7,v5,4,v4
s_mul_i32 s48,s26,16384
s_add_u32 s96,s96,s48
s_mul_i32 s97,s26,2048
s_add_u32 m0,s97,0
s_nop 0
buffer_load_dwordx4 v7,s[36:39],s96 offen offset:0 lds
buffer_load_dwordx4 v7,s[40:43],s96 offen offset:1024 lds
s_add_u32 s96,s96,65536
s_add_u32 m0,s97,8192
s_nop 0
buffer_load_dwordx4 v7,s[36:39],s96 offen offset:0 lds
buffer_load_dwordx4 v7,s[40:43],s96 offen offset:1024 lds
s_add_u32 s96,s96,65536
s_add_u32 m0,s97,16384
s_nop 0
buffer_load_dwordx4 v7,s[36:39],s96 offen offset:0 lds
buffer_load_dwordx4 v7,s[40:43],s96 offen offset:1024 lds
s_add_u32 s96,s96,65536
s_add_u32 m0,s97,24576
s_nop 0
buffer_load_dwordx4 v7,s[36:39],s96 offen offset:0 lds
buffer_load_dwordx4 v7,s[40:43],s96 offen offset:1024 lds
s_add_u32 s96,s96,65536
s_add_u32 m0,s97,32768
s_nop 0
buffer_load_dwordx4 v7,s[36:39],s96 offen offset:0 lds
buffer_load_dwordx4 v7,s[40:43],s96 offen offset:1024 lds
s_add_u32 s96,s96,65536
s_add_u32 m0,s97,40960
s_nop 0
buffer_load_dwordx4 v7,s[36:39],s96 offen offset:0 lds
buffer_load_dwordx4 v7,s[40:43],s96 offen offset:1024 lds
s_add_u32 s96,s96,65536
s_add_u32 m0,s97,49152
s_nop 0
buffer_load_dwordx4 v7,s[36:39],s96 offen offset:0 lds
buffer_load_dwordx4 v7,s[40:43],s96 offen offset:1024 lds
s_add_u32 s96,s96,65536
s_add_u32 m0,s97,57344
s_nop 0
buffer_load_dwordx4 v7,s[36:39],s96 offen offset:0 lds
buffer_load_dwordx4 v7,s[40:43],s96 offen offset:1024 lds
s_add_u32 s96,s96,65536
s_add_u32 m0,s97,65536
s_nop 0
buffer_load_dwordx4 v7,s[36:39],s96 offen offset:0 lds
buffer_load_dwordx4 v7,s[40:43],s96 offen offset:1024 lds
s_add_u32 s96,s96,65536
s_add_u32 m0,s97,73728
s_nop 0
buffer_load_dwordx4 v7,s[36:39],s96 offen offset:0 lds
buffer_load_dwordx4 v7,s[40:43],s96 offen offset:1024 lds
s_add_u32 s96,s96,65536
s_add_u32 m0,s97,81920
s_nop 0
buffer_load_dwordx4 v7,s[36:39],s96 offen offset:0 lds
buffer_load_dwordx4 v7,s[40:43],s96 offen offset:1024 lds
s_add_u32 s96,s96,65536
s_add_u32 m0,s97,90112
s_nop 0
buffer_load_dwordx4 v7,s[36:39],s96 offen offset:0 lds
buffer_load_dwordx4 v7,s[40:43],s96 offen offset:1024 lds
s_add_u32 s96,s96,65536
s_add_u32 m0,s97,98304
s_nop 0
buffer_load_dwordx4 v7,s[36:39],s96 offen offset:0 lds
buffer_load_dwordx4 v7,s[40:43],s96 offen offset:1024 lds
s_add_u32 s96,s96,65536
s_add_u32 m0,s97,106496
s_nop 0
buffer_load_dwordx4 v7,s[36:39],s96 offen offset:0 lds
buffer_load_dwordx4 v7,s[40:43],s96 offen offset:1024 lds
s_add_u32 s96,s96,65536
v_lshrrev_b32 v5,4,v1
v_bfe_u32 v4,v1,3,1
v_and_b32 v6,1,v5
v_lshl_add_u32 v4,v6,1,v4
v_lshlrev_b32 v4,5,v4
v_mul_u32_u24 v5,2048,v5
v_bfe_u32 v6,v1,2,2
v_lshl_add_u32 v5,v6,7,v5
v_xor_b32 v8,v4,v8
v_add_u32 v8,v5,v8
v_add_u32 v9,65536,v8
s_waitcnt vmcnt(24)
s_barrier
s_add_u32 m0,s97,114688
s_nop 0
buffer_load_dwordx4 v7,s[36:39],s96 offen offset:0 lds
buffer_load_dwordx4 v7,s[40:43],s96 offen offset:1024 lds
s_add_u32 s96,s96,65536
s_add_u32 m0,s97,122880
s_nop 0
buffer_load_dwordx4 v7,s[36:39],s96 offen offset:0 lds
buffer_load_dwordx4 v7,s[40:43],s96 offen offset:1024 lds
s_add_u32 s96,s96,65536
ds_read_b64_tr_b16 a[0:1],v8 offset:0
ds_read_b64_tr_b16 a[2:3],v8 offset:512
ds_read_b64_tr_b16 a[4:5],v8 offset:8
ds_read_b64_tr_b16 a[6:7],v8 offset:520
ds_read_b64_tr_b16 a[8:9],v8 offset:8192
ds_read_b64_tr_b16 a[10:11],v8 offset:8704
ds_read_b64_tr_b16 a[12:13],v8 offset:8200
ds_read_b64_tr_b16 a[14:15],v8 offset:8712
s_waitcnt lgkmcnt(0)
s_waitcnt vmcnt(24)
s_barrier
s_add_u32 m0,s97,0
s_nop 0
buffer_load_dwordx4 v7,s[36:39],s96 offen offset:0 lds
buffer_load_dwordx4 v7,s[40:43],s96 offen offset:1024 lds
s_add_u32 s96,s96,65536
s_add_u32 m0,s97,8192
s_nop 0
buffer_load_dwordx4 v7,s[36:39],s96 offen offset:0 lds
buffer_load_dwordx4 v7,s[40:43],s96 offen offset:1024 lds
s_add_u32 s96,s96,65536
ds_read_b64_tr_b16 a[16:17],v8 offset:16384
ds_read_b64_tr_b16 a[18:19],v8 offset:16896
ds_read_b64_tr_b16 a[20:21],v8 offset:16392
ds_read_b64_tr_b16 a[22:23],v8 offset:16904
ds_read_b64_tr_b16 a[24:25],v8 offset:24576
ds_read_b64_tr_b16 a[26:27],v8 offset:25088
ds_read_b64_tr_b16 a[28:29],v8 offset:24584
ds_read_b64_tr_b16 a[30:31],v8 offset:25096
s_waitcnt lgkmcnt(0)
s_waitcnt vmcnt(24)
s_barrier
s_add_u32 m0,s97,16384
s_nop 0
buffer_load_dwordx4 v7,s[36:39],s96 offen offset:0 lds
buffer_load_dwordx4 v7,s[40:43],s96 offen offset:1024 lds
s_add_u32 s96,s96,65536
s_add_u32 m0,s97,24576
s_nop 0
buffer_load_dwordx4 v7,s[36:39],s96 offen offset:0 lds
buffer_load_dwordx4 v7,s[40:43],s96 offen offset:1024 lds
s_add_u32 s96,s96,65536
ds_read_b64_tr_b16 a[32:33],v8 offset:32768
ds_read_b64_tr_b16 a[34:35],v8 offset:33280
ds_read_b64_tr_b16 a[36:37],v8 offset:32776
ds_read_b64_tr_b16 a[38:39],v8 offset:33288
ds_read_b64_tr_b16 a[40:41],v8 offset:40960
ds_read_b64_tr_b16 a[42:43],v8 offset:41472
ds_read_b64_tr_b16 a[44:45],v8 offset:40968
ds_read_b64_tr_b16 a[46:47],v8 offset:41480
s_waitcnt lgkmcnt(0)
s_waitcnt vmcnt(24)
s_barrier
s_add_u32 m0,s97,32768
s_nop 0
buffer_load_dwordx4 v7,s[36:39],s96 offen offset:0 lds
buffer_load_dwordx4 v7,s[40:43],s96 offen offset:1024 lds
s_add_u32 s96,s96,65536
s_add_u32 m0,s97,40960
s_nop 0
buffer_load_dwordx4 v7,s[36:39],s96 offen offset:0 lds
buffer_load_dwordx4 v7,s[40:43],s96 offen offset:1024 lds
s_add_u32 s96,s96,65536
ds_read_b64_tr_b16 a[48:49],v8 offset:49152
ds_read_b64_tr_b16 a[50:51],v8 offset:49664
ds_read_b64_tr_b16 a[52:53],v8 offset:49160
ds_read_b64_tr_b16 a[54:55],v8 offset:49672
ds_read_b64_tr_b16 a[56:57],v8 offset:57344
ds_read_b64_tr_b16 a[58:59],v8 offset:57856
ds_read_b64_tr_b16 a[60:61],v8 offset:57352
ds_read_b64_tr_b16 a[62:63],v8 offset:57864
s_waitcnt lgkmcnt(0)
s_waitcnt vmcnt(24)
s_barrier
s_add_u32 m0,s97,49152
s_nop 0
buffer_load_dwordx4 v7,s[36:39],s96 offen offset:0 lds
buffer_load_dwordx4 v7,s[40:43],s96 offen offset:1024 lds
s_add_u32 s96,s96,65536
s_add_u32 m0,s97,57344
s_nop 0
buffer_load_dwordx4 v7,s[36:39],s96 offen offset:0 lds
buffer_load_dwordx4 v7,s[40:43],s96 offen offset:1024 lds
s_add_u32 s96,s96,65536
ds_read_b64_tr_b16 a[64:65],v9 offset:0
ds_read_b64_tr_b16 a[66:67],v9 offset:512
ds_read_b64_tr_b16 a[68:69],v9 offset:8
ds_read_b64_tr_b16 a[70:71],v9 offset:520
ds_read_b64_tr_b16 a[72:73],v9 offset:8192
ds_read_b64_tr_b16 a[74:75],v9 offset:8704
ds_read_b64_tr_b16 a[76:77],v9 offset:8200
ds_read_b64_tr_b16 a[78:79],v9 offset:8712
s_waitcnt lgkmcnt(0)
s_waitcnt vmcnt(24)
s_barrier
s_add_u32 m0,s97,65536
s_nop 0
buffer_load_dwordx4 v7,s[36:39],s96 offen offset:0 lds
buffer_load_dwordx4 v7,s[40:43],s96 offen offset:1024 lds
s_add_u32 s96,s96,65536
s_add_u32 m0,s97,73728
s_nop 0
buffer_load_dwordx4 v7,s[36:39],s96 offen offset:0 lds
buffer_load_dwordx4 v7,s[40:43],s96 offen offset:1024 lds
s_add_u32 s96,s96,65536
ds_read_b64_tr_b16 a[80:81],v9 offset:16384
ds_read_b64_tr_b16 a[82:83],v9 offset:16896
ds_read_b64_tr_b16 a[84:85],v9 offset:16392
ds_read_b64_tr_b16 a[86:87],v9 offset:16904
ds_read_b64_tr_b16 a[88:89],v9 offset:24576
ds_read_b64_tr_b16 a[90:91],v9 offset:25088
ds_read_b64_tr_b16 a[92:93],v9 offset:24584
ds_read_b64_tr_b16 a[94:95],v9 offset:25096
s_waitcnt lgkmcnt(0)
s_waitcnt vmcnt(24)
s_barrier
s_add_u32 m0,s97,81920
s_nop 0
buffer_load_dwordx4 v7,s[36:39],s96 offen offset:0 lds
buffer_load_dwordx4 v7,s[40:43],s96 offen offset:1024 lds
s_add_u32 s96,s96,65536
s_add_u32 m0,s97,90112
s_nop 0
buffer_load_dwordx4 v7,s[36:39],s96 offen offset:0 lds
buffer_load_dwordx4 v7,s[40:43],s96 offen offset:1024 lds
s_add_u32 s96,s96,65536
ds_read_b64_tr_b16 a[96:97],v9 offset:32768
ds_read_b64_tr_b16 a[98:99],v9 offset:33280
ds_read_b64_tr_b16 a[100:101],v9 offset:32776
ds_read_b64_tr_b16 a[102:103],v9 offset:33288
ds_read_b64_tr_b16 a[104:105],v9 offset:40960
ds_read_b64_tr_b16 a[106:107],v9 offset:41472
ds_read_b64_tr_b16 a[108:109],v9 offset:40968
ds_read_b64_tr_b16 a[110:111],v9 offset:41480
s_waitcnt lgkmcnt(0)
s_waitcnt vmcnt(24)
s_barrier
s_add_u32 m0,s97,98304
s_nop 0
buffer_load_dwordx4 v7,s[36:39],s96 offen offset:0 lds
buffer_load_dwordx4 v7,s[40:43],s96 offen offset:1024 lds
s_add_u32 s96,s96,65536
s_add_u32 m0,s97,106496
s_nop 0
buffer_load_dwordx4 v7,s[36:39],s96 offen offset:0 lds
buffer_load_dwordx4 v7,s[40:43],s96 offen offset:1024 lds
s_add_u32 s96,s96,65536
ds_read_b64_tr_b16 a[112:113],v9 offset:49152
ds_read_b64_tr_b16 a[114:115],v9 offset:49664
ds_read_b64_tr_b16 a[116:117],v9 offset:49160
ds_read_b64_tr_b16 a[118:119],v9 offset:49672
ds_read_b64_tr_b16 a[120:121],v9 offset:57344
ds_read_b64_tr_b16 a[122:123],v9 offset:57856
ds_read_b64_tr_b16 a[124:125],v9 offset:57352
ds_read_b64_tr_b16 a[126:127],v9 offset:57864
s_waitcnt lgkmcnt(0)
s_waitcnt vmcnt(24)
s_barrier
s_add_u32 m0,s97,114688
s_nop 0
buffer_load_dwordx4 v7,s[36:39],s96 offen offset:0 lds
buffer_load_dwordx4 v7,s[40:43],s96 offen offset:1024 lds
s_add_u32 s96,s96,65536
s_add_u32 m0,s97,122880
s_nop 0
buffer_load_dwordx4 v7,s[36:39],s96 offen offset:0 lds
buffer_load_dwordx4 v7,s[40:43],s96 offen offset:1024 lds
s_add_u32 s96,s96,65536
ds_read_b64_tr_b16 a[128:129],v8 offset:0
ds_read_b64_tr_b16 a[130:131],v8 offset:512
ds_read_b64_tr_b16 a[132:133],v8 offset:8
ds_read_b64_tr_b16 a[134:135],v8 offset:520
ds_read_b64_tr_b16 a[136:137],v8 offset:8192
ds_read_b64_tr_b16 a[138:139],v8 offset:8704
ds_read_b64_tr_b16 a[140:141],v8 offset:8200
ds_read_b64_tr_b16 a[142:143],v8 offset:8712
s_waitcnt lgkmcnt(0)
s_waitcnt vmcnt(24)
s_barrier
ds_read_b64_tr_b16 a[144:145],v8 offset:16384
ds_read_b64_tr_b16 a[146:147],v8 offset:16896
ds_read_b64_tr_b16 a[148:149],v8 offset:16392
ds_read_b64_tr_b16 a[150:151],v8 offset:16904
ds_read_b64_tr_b16 a[152:153],v8 offset:24576
ds_read_b64_tr_b16 a[154:155],v8 offset:25088
ds_read_b64_tr_b16 a[156:157],v8 offset:24584
ds_read_b64_tr_b16 a[158:159],v8 offset:25096
s_waitcnt lgkmcnt(0)
s_waitcnt vmcnt(20)
s_barrier
ds_read_b64_tr_b16 a[160:161],v8 offset:32768
ds_read_b64_tr_b16 a[162:163],v8 offset:33280
ds_read_b64_tr_b16 a[164:165],v8 offset:32776
ds_read_b64_tr_b16 a[166:167],v8 offset:33288
ds_read_b64_tr_b16 a[168:169],v8 offset:40960
ds_read_b64_tr_b16 a[170:171],v8 offset:41472
ds_read_b64_tr_b16 a[172:173],v8 offset:40968
ds_read_b64_tr_b16 a[174:175],v8 offset:41480
s_waitcnt lgkmcnt(0)
s_waitcnt vmcnt(16)
s_barrier
ds_read_b64_tr_b16 a[176:177],v8 offset:49152
ds_read_b64_tr_b16 a[178:179],v8 offset:49664
ds_read_b64_tr_b16 a[180:181],v8 offset:49160
ds_read_b64_tr_b16 a[182:183],v8 offset:49672
ds_read_b64_tr_b16 a[184:185],v8 offset:57344
ds_read_b64_tr_b16 a[186:187],v8 offset:57856
ds_read_b64_tr_b16 a[188:189],v8 offset:57352
ds_read_b64_tr_b16 a[190:191],v8 offset:57864
s_waitcnt lgkmcnt(0)
s_waitcnt vmcnt(12)
s_barrier
ds_read_b64_tr_b16 a[192:193],v9 offset:0
ds_read_b64_tr_b16 a[194:195],v9 offset:512
ds_read_b64_tr_b16 a[196:197],v9 offset:8
ds_read_b64_tr_b16 a[198:199],v9 offset:520
ds_read_b64_tr_b16 a[200:201],v9 offset:8192
ds_read_b64_tr_b16 a[202:203],v9 offset:8704
ds_read_b64_tr_b16 a[204:205],v9 offset:8200
ds_read_b64_tr_b16 a[206:207],v9 offset:8712
s_waitcnt lgkmcnt(0)
s_waitcnt vmcnt(8)
s_barrier
ds_read_b64_tr_b16 a[208:209],v9 offset:16384
ds_read_b64_tr_b16 a[210:211],v9 offset:16896
ds_read_b64_tr_b16 a[212:213],v9 offset:16392
ds_read_b64_tr_b16 a[214:215],v9 offset:16904
ds_read_b64_tr_b16 a[216:217],v9 offset:24576
ds_read_b64_tr_b16 a[218:219],v9 offset:25088
ds_read_b64_tr_b16 a[220:221],v9 offset:24584
ds_read_b64_tr_b16 a[222:223],v9 offset:25096
s_waitcnt lgkmcnt(0)
s_waitcnt vmcnt(4)
s_barrier
ds_read_b64_tr_b16 a[224:225],v9 offset:32768
ds_read_b64_tr_b16 a[226:227],v9 offset:33280
ds_read_b64_tr_b16 a[228:229],v9 offset:32776
ds_read_b64_tr_b16 a[230:231],v9 offset:33288
ds_read_b64_tr_b16 a[232:233],v9 offset:40960
ds_read_b64_tr_b16 a[234:235],v9 offset:41472
ds_read_b64_tr_b16 a[236:237],v9 offset:40968
ds_read_b64_tr_b16 a[238:239],v9 offset:41480
s_waitcnt lgkmcnt(0)
s_waitcnt vmcnt(0)
s_barrier
ds_read_b64_tr_b16 a[240:241],v9 offset:49152
ds_read_b64_tr_b16 a[242:243],v9 offset:49664
ds_read_b64_tr_b16 a[244:245],v9 offset:49160
ds_read_b64_tr_b16 a[246:247],v9 offset:49672
ds_read_b64_tr_b16 a[248:249],v9 offset:57344
ds_read_b64_tr_b16 a[250:251],v9 offset:57856
ds_read_b64_tr_b16 a[252:253],v9 offset:57352
ds_read_b64_tr_b16 a[254:255],v9 offset:57864
s_waitcnt lgkmcnt(0)
s_barrier
s_lshl2_add_u32 s50,s27,s26
s_and_b32 s51,s50,6
s_lshl_b32 s51,s51,1
v_add_u32 v3,s51,v1
v_bfi_b32 v3,15,v3,v1
v_lshlrev_b32 v3,2,v3
v_cmp_gt_u32 vcc,32,v1
v_mov_b32 v15,0x7fff0000
s_nop 0
v_cndmask_b32 v15,v15,v3,vcc
s_mul_i32 s48,s50,1152
s_add_u32 s68,s48,4681728
s_lshl2_add_u32 s49,s27,96
s_waitcnt lgkmcnt(0)
s_add_u32 s54,s52,0x7f4a7c15
s_addc_u32 s55,s53,0x9e3779b9
s_lshl_b32 s67,s49,5
s_add_u32 s67,s67,4898816
s_sub_u32 s51,s67,s68
s_add_u32 s92,s32,s51
s_addc_u32 s93,s33,0x10000
s_sub_u32 s92,s92,593920
s_subb_u32 s93,s93,0
s_cmp_eq_u32 s26,0
s_cselect_b32 s94,1,0
s_mov_b32 s95,0x20000
v_mov_b32 v16,s54
v_mov_b32 v17,s55
v_cmp_gt_u32 vcc,12,v1
v_mul_u32_u24 v3,296960,v1
v_mov_b32 v12,0x7fff0000
v_cndmask_b32 v3,v12,v3,vcc
s_add_u32 s51,s67,76021760
buffer_store_dwordx2 v[16:17],v3,s[32:35],s51 offen sc1
s_sub_u32 s67,s68,296960
s_mul_i32 s49,s28,296960
s_add_u32 s48,s48,s49
s_add_u32 s71,s48,4612096
s_add_u32 s68,s48,3493888
s_mov_b32 s62,63
s_add_u32 s63,s49,4648960
s_add_u32 s66,s49,4898816
s_mul_i32 s64,s26,8192
s_add_u32 s65,s64,65536
s_xor_b32 s65,s65,s64
s_add_u32 s63,s63,s64
v_mov_b32 v4,v2
v_add_u32 v6,4096,v2
v_and_b32 v14,15,v1
v_lshlrev_b32 v14,2,v14
s_mov_b32 s48,32
v_add_u32 v14,s48,v14
v_lshrrev_b32 v14,2,v14
v_lshlrev_b32 v14,7,v14
v_lshrrev_b32 v3,4,v1
v_mul_u32_u24 v3,593920,v3
v_add_u32 v14,v3,v14
v_cmp_gt_u32 vcc,32,v1
v_mov_b32 v3,0x7fff0000
s_nop 0
v_cndmask_b32 v14,v3,v14,vcc
v_cndmask_b32 v16,0,v16,vcc
v_cndmask_b32 v17,0,v17,vcc
s_mov_b64 s[8:9],s[62:63]
s_mov_b64 s[10:11],s[64:65]
s_mov_b64 s[12:13],s[66:67]
s_mov_b64 s[14:15],s[68:69]
s_mov_b64 s[16:17],s[70:71]
s_mov_b64 s[18:19],s[72:73]
s_mov_b64 s[20:21],s[74:75]
s_mov_b32 s62,0
s_mov_b64 vcc,0
s_mov_b64 exec,0
s_branch .L_wgo
.L_wbk:
s_mov_b64 exec,-1
s_mov_b64 s[62:63],s[8:9]
s_mov_b64 s[64:65],s[10:11]
s_mov_b64 s[66:67],s[12:13]
s_mov_b64 s[68:69],s[14:15]
s_mov_b64 s[70:71],s[16:17]
s_mov_b64 s[72:73],s[18:19]
s_mov_b64 s[74:75],s[20:21]
s_waitcnt vmcnt(0) lgkmcnt(0)
s_mov_b32 s48,20000
s_branch .L_rfwa
.L_rlwa:
s_sleep 8
.L_rfwa:
buffer_load_dwordx2 v[12:13],v14,s[32:35],s66 offen sc1
s_sub_u32 s48,s48,1
s_cbranch_scc1 .L_end
s_waitcnt vmcnt(0)
v_cmp_ne_u64 vcc,v[16:17],v[12:13]
s_cbranch_vccnz .L_rlwa
s_add_u32 s66,s66,1187840
s_add_u32 m0,s64,0
s_nop 0
buffer_load_dwordx4 v2,s[32:35],s63 offen offset:0 sc1 lds
buffer_load_dwordx4 v2,s[32:35],s63 offen offset:1024 sc1 lds
buffer_load_dwordx4 v2,s[32:35],s63 offen offset:2048 sc1 lds
buffer_load_dwordx4 v2,s[32:35],s63 offen offset:3072 sc1 lds
s_add_u32 m0,s64,4096
s_nop 0
buffer_load_dwordx4 v6,s[32:35],s63 offen offset:0 sc1 lds
buffer_load_dwordx4 v6,s[32:35],s63 offen offset:1024 sc1 lds
buffer_load_dwordx4 v6,s[32:35],s63 offen offset:2048 sc1 lds
buffer_load_dwordx4 v6,s[32:35],s63 offen offset:3072 sc1 lds
s_add_u32 s63,s63,593920
buffer_load_dwordx4 v[48:51],v2,s[32:35],s71 offen sc1
s_add_u32 s71,s71,593920
buffer_load_dwordx4 v[52:55],v2,s[32:35],s71 offen sc1
s_add_u32 s71,s71,593920
s_add_u32 m0,s64,32768
s_nop 0
buffer_load_dwordx4 v2,s[32:35],s63 offen offset:0 sc1 lds
buffer_load_dwordx4 v2,s[32:35],s63 offen offset:1024 sc1 lds
buffer_load_dwordx4 v2,s[32:35],s63 offen offset:2048 sc1 lds
buffer_load_dwordx4 v2,s[32:35],s63 offen offset:3072 sc1 lds
s_add_u32 m0,s64,36864
s_nop 0
buffer_load_dwordx4 v6,s[32:35],s63 offen offset:0 sc1 lds
buffer_load_dwordx4 v6,s[32:35],s63 offen offset:1024 sc1 lds
buffer_load_dwordx4 v6,s[32:35],s63 offen offset:2048 sc1 lds
)#"
R"#(
buffer_load_dwordx4 v6,s[32:35],s63 offen offset:3072 sc1 lds
s_add_u32 s63,s63,593920
s_xor_b32 s64,s64,s65
s_mov_b32 s48,20000
s_branch .L_rfwc
.L_rlwc:
s_sleep 8
.L_rfwc:
buffer_load_dwordx2 v[12:13],v14,s[32:35],s66 offen sc1
s_sub_u32 s48,s48,1
s_cbranch_scc1 .L_end
s_waitcnt vmcnt(0)
v_cmp_ne_u64 vcc,v[16:17],v[12:13]
s_cbranch_vccnz .L_rlwc
s_add_u32 s66,s66,1187840
s_add_u32 m0,s64,0
s_nop 0
buffer_load_dwordx4 v2,s[32:35],s63 offen offset:0 sc1 lds
buffer_load_dwordx4 v2,s[32:35],s63 offen offset:1024 sc1 lds
buffer_load_dwordx4 v2,s[32:35],s63 offen offset:2048 sc1 lds
buffer_load_dwordx4 v2,s[32:35],s63 offen offset:3072 sc1 lds
s_add_u32 m0,s64,4096
s_nop 0
buffer_load_dwordx4 v6,s[32:35],s63 offen offset:0 sc1 lds
s_barrier
ds_read_b128 v[128:131],v4 offset:0
ds_read_b128 v[132:135],v4 offset:1024
ds_read_b128 v[136:139],v4 offset:2048
ds_read_b128 v[140:143],v4 offset:3072
ds_read_b128 v[144:147],v4 offset:4096
ds_read_b128 v[148:151],v4 offset:5120
ds_read_b128 v[152:155],v4 offset:6144
ds_read_b128 v[156:159],v4 offset:7168
ds_read_b128 v[160:163],v4 offset:8192
ds_read_b128 v[164:167],v4 offset:9216
ds_read_b128 v[168:171],v4 offset:10240
ds_read_b128 v[172:175],v4 offset:11264
v_lshlrev_b32 v32,16,v48
v_and_b32 v33,0xffff0000,v48
v_lshlrev_b32 v34,16,v49
v_and_b32 v35,0xffff0000,v49
v_lshlrev_b32 v36,16,v50
v_and_b32 v37,0xffff0000,v50
v_lshlrev_b32 v38,16,v51
v_and_b32 v39,0xffff0000,v51
.L_wgo:
s_cmp_eq_u32 s26,1
s_cbranch_scc1 .L_wv1
s_cmp_eq_u32 s26,2
s_cbranch_scc1 .L_wv2
s_cmp_eq_u32 s26,3
s_cbranch_scc1 .L_wv3
.p2align 6
.L_wv0:
.L_wl_0:
s_waitcnt lgkmcnt(4)
v_mfma_f32_16x16x32_bf16 v[32:35],a[0:3],v[128:131],v[32:35]
ds_read_b128 v[176:179],v4 offset:12288
v_mfma_f32_16x16x32_bf16 v[32:35],a[8:11],v[132:135],v[32:35]
buffer_load_dwordx4 v6,s[32:35],s63 offen offset:1024 sc1 lds
v_mfma_f32_16x16x32_bf16 v[32:35],a[16:19],v[136:139],v[32:35]
ds_read_b128 v[180:183],v4 offset:13312
s_add_u32 s68,s68,593920
v_mfma_f32_16x16x32_bf16 v[32:35],a[24:27],v[140:143],v[32:35]
v_cvt_pk_bf16_f32 v56,v40,v41
v_cvt_pk_bf16_f32 v57,v42,v43
v_mfma_f32_16x16x32_bf16 v[36:39],a[28:31],v[140:143],v[36:39]
ds_read_b128 v[184:187],v4 offset:14336
v_cvt_pk_bf16_f32 v58,v44,v45
v_mfma_f32_16x16x32_bf16 v[36:39],a[20:23],v[136:139],v[36:39]
v_cvt_pk_bf16_f32 v59,v46,v47
v_add_f32 v60,v40,v41
v_mfma_f32_16x16x32_bf16 v[36:39],a[12:15],v[132:135],v[36:39]
ds_read_b128 v[188:191],v4 offset:15360
v_mul_f32 v61,v40,v40
v_mfma_f32_16x16x32_bf16 v[36:39],a[4:7],v[128:131],v[36:39]
v_fmac_f32 v61,v41,v41
v_add_f32 v60,v60,v42
v_mfma_f32_16x16x32_bf16 v[36:39],a[36:39],v[144:147],v[36:39]
ds_read_b128 v[128:131],v4 offset:16384
v_fmac_f32 v61,v42,v42
v_mfma_f32_16x16x32_bf16 v[36:39],a[44:47],v[148:151],v[36:39]
buffer_store_dwordx4 v[56:59],v2,s[32:35],s68 offen sc1
v_mfma_f32_16x16x32_bf16 v[36:39],a[52:55],v[152:155],v[36:39]
ds_read_b128 v[132:135],v4 offset:17408
v_add_f32 v60,v60,v43
v_mfma_f32_16x16x32_bf16 v[36:39],a[60:63],v[156:159],v[36:39]
v_fmac_f32 v61,v43,v43
v_add_f32 v60,v60,v44
v_mfma_f32_16x16x32_bf16 v[32:35],a[56:59],v[156:159],v[32:35]
ds_read_b128 v[136:139],v4 offset:18432
v_fmac_f32 v61,v44,v44
v_mfma_f32_16x16x32_bf16 v[32:35],a[48:51],v[152:155],v[32:35]
v_add_f32 v60,v60,v45
v_fmac_f32 v61,v45,v45
v_mfma_f32_16x16x32_bf16 v[32:35],a[40:43],v[148:151],v[32:35]
ds_read_b128 v[140:143],v4 offset:19456
v_add_f32 v60,v60,v46
v_mfma_f32_16x16x32_bf16 v[32:35],a[32:35],v[144:147],v[32:35]
v_fmac_f32 v61,v46,v46
s_waitcnt lgkmcnt(4)
v_mfma_f32_16x16x32_bf16 v[32:35],a[64:67],v[160:163],v[32:35]
ds_read_b128 v[144:147],v4 offset:20480
v_add_f32 v60,v60,v47
v_mfma_f32_16x16x32_bf16 v[32:35],a[72:75],v[164:167],v[32:35]
v_fmac_f32 v61,v47,v47
v_mfma_f32_16x16x32_bf16 v[32:35],a[80:83],v[168:171],v[32:35]
ds_read_b128 v[148:151],v4 offset:21504
v_mfma_f32_16x16x32_bf16 v[32:35],a[88:91],v[172:175],v[32:35]
v_permlane16_swap_b32 v60,v61
v_mfma_f32_16x16x32_bf16 v[36:39],a[92:95],v[172:175],v[36:39]
ds_read_b128 v[152:155],v4 offset:22528
v_add_f32 v60,v60,v61
v_mfma_f32_16x16x32_bf16 v[36:39],a[84:87],v[168:171],v[36:39]
buffer_load_dwordx4 v[48:51],v2,s[32:35],s71 offen sc1
v_mfma_f32_16x16x32_bf16 v[36:39],a[76:79],v[164:167],v[36:39]
ds_read_b128 v[156:159],v4 offset:23552
s_add_u32 s71,s71,593920
v_mfma_f32_16x16x32_bf16 v[36:39],a[68:71],v[160:163],v[36:39]
v_permlane32_swap_b32 v60,v61
v_mfma_f32_16x16x32_bf16 v[36:39],a[100:103],v[176:179],v[36:39]
ds_read_b128 v[160:163],v4 offset:24576
v_add_f32 v60,v60,v61
v_mfma_f32_16x16x32_bf16 v[36:39],a[108:111],v[180:183],v[36:39]
buffer_store_dword v60,v15,s[32:35],s68 offen offset:1024 sc1
v_mfma_f32_16x16x32_bf16 v[36:39],a[116:119],v[184:187],v[36:39]
ds_read_b128 v[164:167],v4 offset:25600
v_mfma_f32_16x16x32_bf16 v[36:39],a[124:127],v[188:191],v[36:39]
v_mfma_f32_16x16x32_bf16 v[32:35],a[120:123],v[188:191],v[32:35]
ds_read_b128 v[168:171],v4 offset:26624
v_mfma_f32_16x16x32_bf16 v[32:35],a[112:115],v[184:187],v[32:35]
buffer_load_dwordx4 v6,s[32:35],s63 offen offset:2048 sc1 lds
v_mfma_f32_16x16x32_bf16 v[32:35],a[104:107],v[180:183],v[32:35]
ds_read_b128 v[172:175],v4 offset:27648
v_mfma_f32_16x16x32_bf16 v[32:35],a[96:99],v[176:179],v[32:35]
s_waitcnt lgkmcnt(4)
v_mfma_f32_16x16x32_bf16 v[32:35],a[128:131],v[128:131],v[32:35]
ds_read_b128 v[176:179],v4 offset:28672
v_mfma_f32_16x16x32_bf16 v[32:35],a[136:139],v[132:135],v[32:35]
buffer_load_dwordx4 v6,s[32:35],s63 offen offset:3072 sc1 lds
v_mfma_f32_16x16x32_bf16 v[32:35],a[144:147],v[136:139],v[32:35]
ds_read_b128 v[180:183],v4 offset:29696
s_add_u32 s63,s63,593920
v_mfma_f32_16x16x32_bf16 v[32:35],a[152:155],v[140:143],v[32:35]
v_mfma_f32_16x16x32_bf16 v[36:39],a[156:159],v[140:143],v[36:39]
ds_read_b128 v[184:187],v4 offset:30720
v_mfma_f32_16x16x32_bf16 v[36:39],a[148:151],v[136:139],v[36:39]
v_mfma_f32_16x16x32_bf16 v[36:39],a[140:143],v[132:135],v[36:39]
ds_read_b128 v[188:191],v4 offset:31744
v_mfma_f32_16x16x32_bf16 v[36:39],a[132:135],v[128:131],v[36:39]
s_waitcnt vmcnt(12)
s_barrier
v_mfma_f32_16x16x32_bf16 v[36:39],a[164:167],v[144:147],v[36:39]
ds_read_b128 v[128:131],v4 offset:32768
v_lshlrev_b32 v40,16,v52
v_mfma_f32_16x16x32_bf16 v[36:39],a[172:175],v[148:151],v[36:39]
buffer_store_dwordx2 v[16:17],v2,s[92:95],s68 idxen sc1
v_mfma_f32_16x16x32_bf16 v[36:39],a[180:183],v[152:155],v[36:39]
ds_read_b128 v[132:135],v4 offset:33792
v_and_b32 v41,0xffff0000,v52
v_mfma_f32_16x16x32_bf16 v[36:39],a[188:191],v[156:159],v[36:39]
v_lshlrev_b32 v42,16,v53
v_and_b32 v43,0xffff0000,v53
v_mfma_f32_16x16x32_bf16 v[32:35],a[184:187],v[156:159],v[32:35]
ds_read_b128 v[136:139],v4 offset:34816
s_add_u32 m0,s64,32768
v_mfma_f32_16x16x32_bf16 v[32:35],a[176:179],v[152:155],v[32:35]
buffer_load_dwordx4 v2,s[32:35],s63 offen offset:0 sc1 lds
v_mfma_f32_16x16x32_bf16 v[32:35],a[168:171],v[148:151],v[32:35]
ds_read_b128 v[140:143],v4 offset:35840
v_lshlrev_b32 v44,16,v54
v_mfma_f32_16x16x32_bf16 v[32:35],a[160:163],v[144:147],v[32:35]
v_and_b32 v45,0xffff0000,v54
s_waitcnt lgkmcnt(4)
v_mfma_f32_16x16x32_bf16 v[32:35],a[192:195],v[160:163],v[32:35]
ds_read_b128 v[144:147],v4 offset:36864
v_lshlrev_b32 v46,16,v55
v_mfma_f32_16x16x32_bf16 v[32:35],a[200:203],v[164:167],v[32:35]
buffer_load_dwordx4 v2,s[32:35],s63 offen offset:1024 sc1 lds
v_mfma_f32_16x16x32_bf16 v[32:35],a[208:211],v[168:171],v[32:35]
ds_read_b128 v[148:151],v4 offset:37888
v_and_b32 v47,0xffff0000,v55
v_mfma_f32_16x16x32_bf16 v[32:35],a[216:219],v[172:175],v[32:35]
v_mfma_f32_16x16x32_bf16 v[36:39],a[220:223],v[172:175],v[36:39]
ds_read_b128 v[152:155],v4 offset:38912
v_mfma_f32_16x16x32_bf16 v[36:39],a[212:215],v[168:171],v[36:39]
buffer_load_dwordx4 v2,s[32:35],s63 offen offset:2048 sc1 lds
v_mfma_f32_16x16x32_bf16 v[36:39],a[204:207],v[164:167],v[36:39]
ds_read_b128 v[156:159],v4 offset:39936
v_mfma_f32_16x16x32_bf16 v[36:39],a[196:199],v[160:163],v[36:39]
v_mfma_f32_16x16x32_bf16 v[36:39],a[228:231],v[176:179],v[36:39]
ds_read_b128 v[160:163],v4 offset:40960
v_mfma_f32_16x16x32_bf16 v[36:39],a[236:239],v[180:183],v[36:39]
buffer_load_dwordx4 v2,s[32:35],s63 offen offset:3072 sc1 lds
v_mfma_f32_16x16x32_bf16 v[36:39],a[244:247],v[184:187],v[36:39]
ds_read_b128 v[164:167],v4 offset:41984
v_mfma_f32_16x16x32_bf16 v[36:39],a[252:255],v[188:191],v[36:39]
v_mfma_f32_16x16x32_bf16 v[32:35],a[248:251],v[188:191],v[32:35]
ds_read_b128 v[168:171],v4 offset:43008
s_add_u32 m0,s64,36864
v_mfma_f32_16x16x32_bf16 v[32:35],a[240:243],v[184:187],v[32:35]
buffer_load_dwordx4 v6,s[32:35],s63 offen offset:0 sc1 lds
v_mfma_f32_16x16x32_bf16 v[32:35],a[232:235],v[180:183],v[32:35]
ds_read_b128 v[172:175],v4 offset:44032
v_mfma_f32_16x16x32_bf16 v[32:35],a[224:227],v[176:179],v[32:35]
s_waitcnt lgkmcnt(4)
v_mfma_f32_16x16x32_bf16 v[40:43],a[0:3],v[128:131],v[40:43]
ds_read_b128 v[176:179],v4 offset:45056
v_mfma_f32_16x16x32_bf16 v[40:43],a[8:11],v[132:135],v[40:43]
buffer_load_dwordx4 v6,s[32:35],s63 offen offset:1024 sc1 lds
v_mfma_f32_16x16x32_bf16 v[40:43],a[16:19],v[136:139],v[40:43]
ds_read_b128 v[180:183],v4 offset:46080
s_add_u32 s68,s68,593920
v_mfma_f32_16x16x32_bf16 v[40:43],a[24:27],v[140:143],v[40:43]
v_cvt_pk_bf16_f32 v56,v32,v33
v_cvt_pk_bf16_f32 v57,v34,v35
v_mfma_f32_16x16x32_bf16 v[44:47],a[28:31],v[140:143],v[44:47]
ds_read_b128 v[184:187],v4 offset:47104
v_cvt_pk_bf16_f32 v58,v36,v37
v_mfma_f32_16x16x32_bf16 v[44:47],a[20:23],v[136:139],v[44:47]
v_cvt_pk_bf16_f32 v59,v38,v39
v_add_f32 v60,v32,v33
v_mfma_f32_16x16x32_bf16 v[44:47],a[12:15],v[132:135],v[44:47]
ds_read_b128 v[188:191],v4 offset:48128
v_mul_f32 v61,v32,v32
v_mfma_f32_16x16x32_bf16 v[44:47],a[4:7],v[128:131],v[44:47]
v_fmac_f32 v61,v33,v33
v_add_f32 v60,v60,v34
v_mfma_f32_16x16x32_bf16 v[44:47],a[36:39],v[144:147],v[44:47]
ds_read_b128 v[128:131],v4 offset:49152
v_fmac_f32 v61,v34,v34
v_mfma_f32_16x16x32_bf16 v[44:47],a[44:47],v[148:151],v[44:47]
buffer_store_dwordx4 v[56:59],v2,s[32:35],s68 offen sc1
v_mfma_f32_16x16x32_bf16 v[44:47],a[52:55],v[152:155],v[44:47]
ds_read_b128 v[132:135],v4 offset:50176
v_add_f32 v60,v60,v35
v_mfma_f32_16x16x32_bf16 v[44:47],a[60:63],v[156:159],v[44:47]
v_fmac_f32 v61,v35,v35
v_add_f32 v60,v60,v36
v_mfma_f32_16x16x32_bf16 v[40:43],a[56:59],v[156:159],v[40:43]
ds_read_b128 v[136:139],v4 offset:51200
v_fmac_f32 v61,v36,v36
v_mfma_f32_16x16x32_bf16 v[40:43],a[48:51],v[152:155],v[40:43]
v_add_f32 v60,v60,v37
v_fmac_f32 v61,v37,v37
v_mfma_f32_16x16x32_bf16 v[40:43],a[40:43],v[148:151],v[40:43]
ds_read_b128 v[140:143],v4 offset:52224
v_add_f32 v60,v60,v38
v_mfma_f32_16x16x32_bf16 v[40:43],a[32:35],v[144:147],v[40:43]
v_fmac_f32 v61,v38,v38
s_waitcnt lgkmcnt(4)
v_mfma_f32_16x16x32_bf16 v[40:43],a[64:67],v[160:163],v[40:43]
ds_read_b128 v[144:147],v4 offset:53248
v_add_f32 v60,v60,v39
v_mfma_f32_16x16x32_bf16 v[40:43],a[72:75],v[164:167],v[40:43]
v_fmac_f32 v61,v39,v39
v_mfma_f32_16x16x32_bf16 v[40:43],a[80:83],v[168:171],v[40:43]
ds_read_b128 v[148:151],v4 offset:54272
v_mfma_f32_16x16x32_bf16 v[40:43],a[88:91],v[172:175],v[40:43]
v_permlane16_swap_b32 v60,v61
v_mfma_f32_16x16x32_bf16 v[44:47],a[92:95],v[172:175],v[44:47]
ds_read_b128 v[152:155],v4 offset:55296
v_add_f32 v60,v60,v61
v_mfma_f32_16x16x32_bf16 v[44:47],a[84:87],v[168:171],v[44:47]
buffer_load_dwordx4 v[52:55],v2,s[32:35],s71 offen sc1
v_mfma_f32_16x16x32_bf16 v[44:47],a[76:79],v[164:167],v[44:47]
ds_read_b128 v[156:159],v4 offset:56320
s_add_u32 s71,s71,593920
v_mfma_f32_16x16x32_bf16 v[44:47],a[68:71],v[160:163],v[44:47]
v_permlane32_swap_b32 v60,v61
v_mfma_f32_16x16x32_bf16 v[44:47],a[100:103],v[176:179],v[44:47]
ds_read_b128 v[160:163],v4 offset:57344
v_add_f32 v60,v60,v61
v_mfma_f32_16x16x32_bf16 v[44:47],a[108:111],v[180:183],v[44:47]
buffer_store_dword v60,v15,s[32:35],s68 offen offset:1024 sc1
v_mfma_f32_16x16x32_bf16 v[44:47],a[116:119],v[184:187],v[44:47]
ds_read_b128 v[164:167],v4 offset:58368
v_mfma_f32_16x16x32_bf16 v[44:47],a[124:127],v[188:191],v[44:47]
v_mfma_f32_16x16x32_bf16 v[40:43],a[120:123],v[188:191],v[40:43]
ds_read_b128 v[168:171],v4 offset:59392
v_mfma_f32_16x16x32_bf16 v[40:43],a[112:115],v[184:187],v[40:43]
buffer_load_dwordx4 v6,s[32:35],s63 offen offset:2048 sc1 lds
v_mfma_f32_16x16x32_bf16 v[40:43],a[104:107],v[180:183],v[40:43]
ds_read_b128 v[172:175],v4 offset:60416
v_mfma_f32_16x16x32_bf16 v[40:43],a[96:99],v[176:179],v[40:43]
s_waitcnt lgkmcnt(4)
v_mfma_f32_16x16x32_bf16 v[40:43],a[128:131],v[128:131],v[40:43]
ds_read_b128 v[176:179],v4 offset:61440
v_mfma_f32_16x16x32_bf16 v[40:43],a[136:139],v[132:135],v[40:43]
buffer_load_dwordx4 v6,s[32:35],s63 offen offset:3072 sc1 lds
v_mfma_f32_16x16x32_bf16 v[40:43],a[144:147],v[136:139],v[40:43]
ds_read_b128 v[180:183],v4 offset:62464
s_add_u32 s63,s63,593920
v_mfma_f32_16x16x32_bf16 v[40:43],a[152:155],v[140:143],v[40:43]
s_xor_b32 s64,s64,s65
v_mfma_f32_16x16x32_bf16 v[44:47],a[156:159],v[140:143],v[44:47]
ds_read_b128 v[184:187],v4 offset:63488
v_mfma_f32_16x16x32_bf16 v[44:47],a[148:151],v[136:139],v[44:47]
v_mfma_f32_16x16x32_bf16 v[44:47],a[140:143],v[132:135],v[44:47]
ds_read_b128 v[188:191],v4 offset:64512
v_xor_b32 v4,65536,v4
v_mfma_f32_16x16x32_bf16 v[44:47],a[132:135],v[128:131],v[44:47]
s_waitcnt vmcnt(12)
s_barrier
v_mfma_f32_16x16x32_bf16 v[44:47],a[164:167],v[144:147],v[44:47]
ds_read_b128 v[128:131],v4 offset:0
v_lshlrev_b32 v32,16,v48
v_mfma_f32_16x16x32_bf16 v[44:47],a[172:175],v[148:151],v[44:47]
buffer_store_dwordx2 v[16:17],v2,s[92:95],s68 idxen sc1
v_mfma_f32_16x16x32_bf16 v[44:47],a[180:183],v[152:155],v[44:47]
ds_read_b128 v[132:135],v4 offset:1024
v_and_b32 v33,0xffff0000,v48
v_mfma_f32_16x16x32_bf16 v[44:47],a[188:191],v[156:159],v[44:47]
v_lshlrev_b32 v34,16,v49
v_and_b32 v35,0xffff0000,v49
v_mfma_f32_16x16x32_bf16 v[40:43],a[184:187],v[156:159],v[40:43]
ds_read_b128 v[136:139],v4 offset:2048
s_add_u32 m0,s64,0
v_mfma_f32_16x16x32_bf16 v[40:43],a[176:179],v[152:155],v[40:43]
buffer_load_dwordx4 v2,s[32:35],s63 offen offset:0 sc1 lds
v_mfma_f32_16x16x32_bf16 v[40:43],a[168:171],v[148:151],v[40:43]
ds_read_b128 v[140:143],v4 offset:3072
v_lshlrev_b32 v36,16,v50
v_mfma_f32_16x16x32_bf16 v[40:43],a[160:163],v[144:147],v[40:43]
v_and_b32 v37,0xffff0000,v50
s_waitcnt lgkmcnt(4)
v_mfma_f32_16x16x32_bf16 v[40:43],a[192:195],v[160:163],v[40:43]
ds_read_b128 v[144:147],v4 offset:4096
v_lshlrev_b32 v38,16,v51
v_mfma_f32_16x16x32_bf16 v[40:43],a[200:203],v[164:167],v[40:43]
buffer_load_dwordx4 v2,s[32:35],s63 offen offset:1024 sc1 lds
v_mfma_f32_16x16x32_bf16 v[40:43],a[208:211],v[168:171],v[40:43]
ds_read_b128 v[148:151],v4 offset:5120
v_and_b32 v39,0xffff0000,v51
v_mfma_f32_16x16x32_bf16 v[40:43],a[216:219],v[172:175],v[40:43]
v_mfma_f32_16x16x32_bf16 v[44:47],a[220:223],v[172:175],v[44:47]
ds_read_b128 v[152:155],v4 offset:6144
v_mfma_f32_16x16x32_bf16 v[44:47],a[212:215],v[168:171],v[44:47]
buffer_load_dwordx4 v2,s[32:35],s63 offen offset:2048 sc1 lds
v_mfma_f32_16x16x32_bf16 v[44:47],a[204:207],v[164:167],v[44:47]
ds_read_b128 v[156:159],v4 offset:7168
v_mfma_f32_16x16x32_bf16 v[44:47],a[196:199],v[160:163],v[44:47]
v_mfma_f32_16x16x32_bf16 v[44:47],a[228:231],v[176:179],v[44:47]
ds_read_b128 v[160:163],v4 offset:8192
v_mfma_f32_16x16x32_bf16 v[44:47],a[236:239],v[180:183],v[44:47]
buffer_load_dwordx4 v2,s[32:35],s63 offen offset:3072 sc1 lds
v_mfma_f32_16x16x32_bf16 v[44:47],a[244:247],v[184:187],v[44:47]
ds_read_b128 v[164:167],v4 offset:9216
v_mfma_f32_16x16x32_bf16 v[44:47],a[252:255],v[188:191],v[44:47]
v_mfma_f32_16x16x32_bf16 v[40:43],a[248:251],v[188:191],v[40:43]
ds_read_b128 v[168:171],v4 offset:10240
s_add_u32 m0,s64,4096
v_mfma_f32_16x16x32_bf16 v[40:43],a[240:243],v[184:187],v[40:43]
buffer_load_dwordx4 v6,s[32:35],s63 offen offset:0 sc1 lds
v_mfma_f32_16x16x32_bf16 v[40:43],a[232:235],v[180:183],v[40:43]
ds_read_b128 v[172:175],v4 offset:11264
s_sub_u32 s62,s62,1
v_mfma_f32_16x16x32_bf16 v[40:43],a[224:227],v[176:179],v[40:43]
s_cbranch_scc0 .L_wl_0
s_nop 1
s_add_u32 s68,s68,593920
s_nop 3
v_cvt_pk_bf16_f32 v56,v40,v41
v_cvt_pk_bf16_f32 v57,v42,v43
v_cvt_pk_bf16_f32 v58,v44,v45
v_cvt_pk_bf16_f32 v59,v46,v47
buffer_store_dwordx4 v[56:59],v2,s[32:35],s68 offen sc1
v_add_f32 v60,v40,v41
v_mul_f32 v61,v40,v40
v_fmac_f32 v61,v41,v41
v_add_f32 v60,v60,v42
v_fmac_f32 v61,v42,v42
v_add_f32 v60,v60,v43
v_fmac_f32 v61,v43,v43
v_add_f32 v60,v60,v44
v_fmac_f32 v61,v44,v44
v_add_f32 v60,v60,v45
v_fmac_f32 v61,v45,v45
v_add_f32 v60,v60,v46
v_fmac_f32 v61,v46,v46
v_add_f32 v60,v60,v47
v_fmac_f32 v61,v47,v47
s_nop 1
v_permlane16_swap_b32 v60,v61
v_add_f32 v60,v60,v61
s_nop 1
v_permlane32_swap_b32 v60,v61
v_add_f32 v60,v60,v61
buffer_store_dword v60,v15,s[32:35],s68 offen offset:1024 sc1
s_waitcnt vmcnt(10)
s_barrier
buffer_store_dwordx2 v[16:17],v2,s[92:95],s68 idxen sc1
s_add_u32 s68,s68,593920
s_waitcnt vmcnt(1)
s_barrier
buffer_store_dwordx2 v[16:17],v2,s[92:95],s68 idxen sc1
s_cbranch_execz .L_wbk
s_branch .L_end
.p2align 6
.L_wv1:
.L_wl_1:
s_waitcnt lgkmcnt(4)
v_mfma_f32_16x16x32_bf16 v[32:35],a[0:3],v[128:131],v[32:35]
ds_read_b128 v[176:179],v4 offset:12288
v_mfma_f32_16x16x32_bf16 v[32:35],a[8:11],v[132:135],v[32:35]
buffer_load_dwordx4 v6,s[32:35],s63 offen offset:1024 sc1 lds
v_mfma_f32_16x16x32_bf16 v[32:35],a[16:19],v[136:139],v[32:35]
ds_read_b128 v[180:183],v4 offset:13312
s_add_u32 s68,s68,593920
v_mfma_f32_16x16x32_bf16 v[32:35],a[24:27],v[140:143],v[32:35]
v_cvt_pk_bf16_f32 v56,v40,v41
v_cvt_pk_bf16_f32 v57,v42,v43
v_mfma_f32_16x16x32_bf16 v[36:39],a[28:31],v[140:143],v[36:39]
ds_read_b128 v[184:187],v4 offset:14336
v_cvt_pk_bf16_f32 v58,v44,v45
v_mfma_f32_16x16x32_bf16 v[36:39],a[20:23],v[136:139],v[36:39]
v_cvt_pk_bf16_f32 v59,v46,v47
v_add_f32 v60,v40,v41
v_mfma_f32_16x16x32_bf16 v[36:39],a[12:15],v[132:135],v[36:39]
ds_read_b128 v[188:191],v4 offset:15360
v_mul_f32 v61,v40,v40
v_mfma_f32_16x16x32_bf16 v[36:39],a[4:7],v[128:131],v[36:39]
v_fmac_f32 v61,v41,v41
v_add_f32 v60,v60,v42
v_mfma_f32_16x16x32_bf16 v[36:39],a[36:39],v[144:147],v[36:39]
ds_read_b128 v[128:131],v4 offset:16384
v_fmac_f32 v61,v42,v42
v_mfma_f32_16x16x32_bf16 v[36:39],a[44:47],v[148:151],v[36:39]
buffer_store_dwordx4 v[56:59],v2,s[32:35],s68 offen sc1
v_mfma_f32_16x16x32_bf16 v[36:39],a[52:55],v[152:155],v[36:39]
ds_read_b128 v[132:135],v4 offset:17408
v_add_f32 v60,v60,v43
v_mfma_f32_16x16x32_bf16 v[36:39],a[60:63],v[156:159],v[36:39]
v_fmac_f32 v61,v43,v43
v_add_f32 v60,v60,v44
v_mfma_f32_16x16x32_bf16 v[32:35],a[56:59],v[156:159],v[32:35]
ds_read_b128 v[136:139],v4 offset:18432
v_fmac_f32 v61,v44,v44
v_mfma_f32_16x16x32_bf16 v[32:35],a[48:51],v[152:155],v[32:35]
v_add_f32 v60,v60,v45
v_fmac_f32 v61,v45,v45
v_mfma_f32_16x16x32_bf16 v[32:35],a[40:43],v[148:151],v[32:35]
ds_read_b128 v[140:143],v4 offset:19456
v_add_f32 v60,v60,v46
v_mfma_f32_16x16x32_bf16 v[32:35],a[32:35],v[144:147],v[32:35]
v_fmac_f32 v61,v46,v46
s_waitcnt lgkmcnt(4)
v_mfma_f32_16x16x32_bf16 v[32:35],a[64:67],v[160:163],v[32:35]
ds_read_b128 v[144:147],v4 offset:20480
v_add_f32 v60,v60,v47
v_mfma_f32_16x16x32_bf16 v[32:35],a[72:75],v[164:167],v[32:35]
v_fmac_f32 v61,v47,v47
v_mfma_f32_16x16x32_bf16 v[32:35],a[80:83],v[168:171],v[32:35]
ds_read_b128 v[148:151],v4 offset:21504
v_mfma_f32_16x16x32_bf16 v[32:35],a[88:91],v[172:175],v[32:35]
v_permlane16_swap_b32 v60,v61
v_mfma_f32_16x16x32_bf16 v[36:39],a[92:95],v[172:175],v[36:39]
ds_read_b128 v[152:155],v4 offset:22528
v_add_f32 v60,v60,v61
v_mfma_f32_16x16x32_bf16 v[36:39],a[84:87],v[168:171],v[36:39]
buffer_load_dwordx4 v[48:51],v2,s[32:35],s71 offen sc1
v_mfma_f32_16x16x32_bf16 v[36:39],a[76:79],v[164:167],v[36:39]
ds_read_b128 v[156:159],v4 offset:23552
s_add_u32 s71,s71,593920
v_mfma_f32_16x16x32_bf16 v[36:39],a[68:71],v[160:163],v[36:39]
v_permlane32_swap_b32 v60,v61
v_mfma_f32_16x16x32_bf16 v[36:39],a[100:103],v[176:179],v[36:39]
ds_read_b128 v[160:163],v4 offset:24576
v_add_f32 v60,v60,v61
v_mfma_f32_16x16x32_bf16 v[36:39],a[108:111],v[180:183],v[36:39]
buffer_store_dword v60,v15,s[32:35],s68 offen offset:1024 sc1
v_mfma_f32_16x16x32_bf16 v[36:39],a[116:119],v[184:187],v[36:39]
ds_read_b128 v[164:167],v4 offset:25600
v_mfma_f32_16x16x32_bf16 v[36:39],a[124:127],v[188:191],v[36:39]
v_mfma_f32_16x16x32_bf16 v[32:35],a[120:123],v[188:191],v[32:35]
ds_read_b128 v[168:171],v4 offset:26624
v_mfma_f32_16x16x32_bf16 v[32:35],a[112:115],v[184:187],v[32:35]
buffer_load_dwordx4 v6,s[32:35],s63 offen offset:2048 sc1 lds
v_mfma_f32_16x16x32_bf16 v[32:35],a[104:107],v[180:183],v[32:35]
ds_read_b128 v[172:175],v4 offset:27648
v_mfma_f32_16x16x32_bf16 v[32:35],a[96:99],v[176:179],v[32:35]
s_waitcnt lgkmcnt(4)
v_mfma_f32_16x16x32_bf16 v[32:35],a[128:131],v[128:131],v[32:35]
ds_read_b128 v[176:179],v4 offset:28672
v_mfma_f32_16x16x32_bf16 v[32:35],a[136:139],v[132:135],v[32:35]
buffer_load_dwordx4 v6,s[32:35],s63 offen offset:3072 sc1 lds
v_mfma_f32_16x16x32_bf16 v[32:35],a[144:147],v[136:139],v[32:35]
ds_read_b128 v[180:183],v4 offset:29696
s_add_u32 s63,s63,593920
v_mfma_f32_16x16x32_bf16 v[32:35],a[152:155],v[140:143],v[32:35]
v_mfma_f32_16x16x32_bf16 v[36:39],a[156:159],v[140:143],v[36:39]
ds_read_b128 v[184:187],v4 offset:30720
v_mfma_f32_16x16x32_bf16 v[36:39],a[148:151],v[136:139],v[36:39]
buffer_load_dwordx2 v[12:13],v14,s[32:35],s66 offen sc1
v_mfma_f32_16x16x32_bf16 v[36:39],a[140:143],v[132:135],v[36:39]
s_waitcnt vmcnt(12)
s_barrier
v_mfma_f32_16x16x32_bf16 v[36:39],a[132:135],v[128:131],v[36:39]
ds_read_b128 v[188:191],v4 offset:31744
v_mfma_f32_16x16x32_bf16 v[36:39],a[164:167],v[144:147],v[36:39]
)#"
R"#(
ds_read_b128 v[128:131],v4 offset:32768
v_lshlrev_b32 v40,16,v52
v_mfma_f32_16x16x32_bf16 v[36:39],a[172:175],v[148:151],v[36:39]
v_and_b32 v41,0xffff0000,v52
v_lshlrev_b32 v42,16,v53
v_mfma_f32_16x16x32_bf16 v[36:39],a[180:183],v[152:155],v[36:39]
ds_read_b128 v[132:135],v4 offset:33792
v_and_b32 v43,0xffff0000,v53
v_mfma_f32_16x16x32_bf16 v[36:39],a[188:191],v[156:159],v[36:39]
v_lshlrev_b32 v44,16,v54
v_and_b32 v45,0xffff0000,v54
v_mfma_f32_16x16x32_bf16 v[32:35],a[184:187],v[156:159],v[32:35]
ds_read_b128 v[136:139],v4 offset:34816
s_add_u32 m0,s64,32768
v_mfma_f32_16x16x32_bf16 v[32:35],a[176:179],v[152:155],v[32:35]
buffer_load_dwordx4 v2,s[32:35],s63 offen offset:0 sc1 lds
v_mfma_f32_16x16x32_bf16 v[32:35],a[168:171],v[148:151],v[32:35]
ds_read_b128 v[140:143],v4 offset:35840
v_lshlrev_b32 v46,16,v55
v_mfma_f32_16x16x32_bf16 v[32:35],a[160:163],v[144:147],v[32:35]
v_and_b32 v47,0xffff0000,v55
s_waitcnt lgkmcnt(4)
v_mfma_f32_16x16x32_bf16 v[32:35],a[192:195],v[160:163],v[32:35]
ds_read_b128 v[144:147],v4 offset:36864
v_mfma_f32_16x16x32_bf16 v[32:35],a[200:203],v[164:167],v[32:35]
buffer_load_dwordx4 v2,s[32:35],s63 offen offset:1024 sc1 lds
v_mfma_f32_16x16x32_bf16 v[32:35],a[208:211],v[168:171],v[32:35]
ds_read_b128 v[148:151],v4 offset:37888
v_mfma_f32_16x16x32_bf16 v[32:35],a[216:219],v[172:175],v[32:35]
v_mfma_f32_16x16x32_bf16 v[36:39],a[220:223],v[172:175],v[36:39]
ds_read_b128 v[152:155],v4 offset:38912
v_mfma_f32_16x16x32_bf16 v[36:39],a[212:215],v[168:171],v[36:39]
buffer_load_dwordx4 v2,s[32:35],s63 offen offset:2048 sc1 lds
v_mfma_f32_16x16x32_bf16 v[36:39],a[204:207],v[164:167],v[36:39]
ds_read_b128 v[156:159],v4 offset:39936
v_mfma_f32_16x16x32_bf16 v[36:39],a[196:199],v[160:163],v[36:39]
v_mfma_f32_16x16x32_bf16 v[36:39],a[228:231],v[176:179],v[36:39]
ds_read_b128 v[160:163],v4 offset:40960
v_mfma_f32_16x16x32_bf16 v[36:39],a[236:239],v[180:183],v[36:39]
buffer_load_dwordx4 v2,s[32:35],s63 offen offset:3072 sc1 lds
v_mfma_f32_16x16x32_bf16 v[36:39],a[244:247],v[184:187],v[36:39]
ds_read_b128 v[164:167],v4 offset:41984
v_mfma_f32_16x16x32_bf16 v[36:39],a[252:255],v[188:191],v[36:39]
v_mfma_f32_16x16x32_bf16 v[32:35],a[248:251],v[188:191],v[32:35]
ds_read_b128 v[168:171],v4 offset:43008
s_add_u32 m0,s64,36864
v_mfma_f32_16x16x32_bf16 v[32:35],a[240:243],v[184:187],v[32:35]
buffer_load_dwordx4 v6,s[32:35],s63 offen offset:0 sc1 lds
v_mfma_f32_16x16x32_bf16 v[32:35],a[232:235],v[180:183],v[32:35]
ds_read_b128 v[172:175],v4 offset:44032
v_mfma_f32_16x16x32_bf16 v[32:35],a[224:227],v[176:179],v[32:35]
s_waitcnt lgkmcnt(4)
v_mfma_f32_16x16x32_bf16 v[40:43],a[0:3],v[128:131],v[40:43]
ds_read_b128 v[176:179],v4 offset:45056
v_mfma_f32_16x16x32_bf16 v[40:43],a[8:11],v[132:135],v[40:43]
buffer_load_dwordx4 v6,s[32:35],s63 offen offset:1024 sc1 lds
v_mfma_f32_16x16x32_bf16 v[40:43],a[16:19],v[136:139],v[40:43]
ds_read_b128 v[180:183],v4 offset:46080
s_add_u32 s68,s68,593920
v_mfma_f32_16x16x32_bf16 v[40:43],a[24:27],v[140:143],v[40:43]
v_cvt_pk_bf16_f32 v56,v32,v33
v_cvt_pk_bf16_f32 v57,v34,v35
v_mfma_f32_16x16x32_bf16 v[44:47],a[28:31],v[140:143],v[44:47]
ds_read_b128 v[184:187],v4 offset:47104
v_cvt_pk_bf16_f32 v58,v36,v37
v_mfma_f32_16x16x32_bf16 v[44:47],a[20:23],v[136:139],v[44:47]
v_cvt_pk_bf16_f32 v59,v38,v39
v_add_f32 v60,v32,v33
v_mfma_f32_16x16x32_bf16 v[44:47],a[12:15],v[132:135],v[44:47]
ds_read_b128 v[188:191],v4 offset:48128
v_mul_f32 v61,v32,v32
v_mfma_f32_16x16x32_bf16 v[44:47],a[4:7],v[128:131],v[44:47]
v_fmac_f32 v61,v33,v33
v_add_f32 v60,v60,v34
v_mfma_f32_16x16x32_bf16 v[44:47],a[36:39],v[144:147],v[44:47]
ds_read_b128 v[128:131],v4 offset:49152
v_fmac_f32 v61,v34,v34
v_mfma_f32_16x16x32_bf16 v[44:47],a[44:47],v[148:151],v[44:47]
buffer_store_dwordx4 v[56:59],v2,s[32:35],s68 offen sc1
v_mfma_f32_16x16x32_bf16 v[44:47],a[52:55],v[152:155],v[44:47]
ds_read_b128 v[132:135],v4 offset:50176
v_add_f32 v60,v60,v35
v_mfma_f32_16x16x32_bf16 v[44:47],a[60:63],v[156:159],v[44:47]
v_fmac_f32 v61,v35,v35
v_add_f32 v60,v60,v36
v_mfma_f32_16x16x32_bf16 v[40:43],a[56:59],v[156:159],v[40:43]
ds_read_b128 v[136:139],v4 offset:51200
v_fmac_f32 v61,v36,v36
v_mfma_f32_16x16x32_bf16 v[40:43],a[48:51],v[152:155],v[40:43]
v_add_f32 v60,v60,v37
v_fmac_f32 v61,v37,v37
v_mfma_f32_16x16x32_bf16 v[40:43],a[40:43],v[148:151],v[40:43]
ds_read_b128 v[140:143],v4 offset:52224
v_add_f32 v60,v60,v38
v_mfma_f32_16x16x32_bf16 v[40:43],a[32:35],v[144:147],v[40:43]
v_fmac_f32 v61,v38,v38
s_waitcnt lgkmcnt(4)
v_mfma_f32_16x16x32_bf16 v[40:43],a[64:67],v[160:163],v[40:43]
ds_read_b128 v[144:147],v4 offset:53248
v_add_f32 v60,v60,v39
v_mfma_f32_16x16x32_bf16 v[40:43],a[72:75],v[164:167],v[40:43]
v_fmac_f32 v61,v39,v39
v_mfma_f32_16x16x32_bf16 v[40:43],a[80:83],v[168:171],v[40:43]
ds_read_b128 v[148:151],v4 offset:54272
v_mfma_f32_16x16x32_bf16 v[40:43],a[88:91],v[172:175],v[40:43]
v_permlane16_swap_b32 v60,v61
v_mfma_f32_16x16x32_bf16 v[44:47],a[92:95],v[172:175],v[44:47]
ds_read_b128 v[152:155],v4 offset:55296
v_add_f32 v60,v60,v61
v_mfma_f32_16x16x32_bf16 v[44:47],a[84:87],v[168:171],v[44:47]
buffer_load_dwordx4 v[52:55],v2,s[32:35],s71 offen sc1
v_mfma_f32_16x16x32_bf16 v[44:47],a[76:79],v[164:167],v[44:47]
ds_read_b128 v[156:159],v4 offset:56320
s_add_u32 s71,s71,593920
v_mfma_f32_16x16x32_bf16 v[44:47],a[68:71],v[160:163],v[44:47]
v_permlane32_swap_b32 v60,v61
v_mfma_f32_16x16x32_bf16 v[44:47],a[100:103],v[176:179],v[44:47]
ds_read_b128 v[160:163],v4 offset:57344
v_add_f32 v60,v60,v61
v_mfma_f32_16x16x32_bf16 v[44:47],a[108:111],v[180:183],v[44:47]
buffer_store_dword v60,v15,s[32:35],s68 offen offset:1024 sc1
v_mfma_f32_16x16x32_bf16 v[44:47],a[116:119],v[184:187],v[44:47]
ds_read_b128 v[164:167],v4 offset:58368
v_mfma_f32_16x16x32_bf16 v[44:47],a[124:127],v[188:191],v[44:47]
v_mfma_f32_16x16x32_bf16 v[40:43],a[120:123],v[188:191],v[40:43]
ds_read_b128 v[168:171],v4 offset:59392
v_mfma_f32_16x16x32_bf16 v[40:43],a[112:115],v[184:187],v[40:43]
buffer_load_dwordx4 v6,s[32:35],s63 offen offset:2048 sc1 lds
v_mfma_f32_16x16x32_bf16 v[40:43],a[104:107],v[180:183],v[40:43]
ds_read_b128 v[172:175],v4 offset:60416
v_mfma_f32_16x16x32_bf16 v[40:43],a[96:99],v[176:179],v[40:43]
s_waitcnt lgkmcnt(4)
v_mfma_f32_16x16x32_bf16 v[40:43],a[128:131],v[128:131],v[40:43]
ds_read_b128 v[176:179],v4 offset:61440
v_mfma_f32_16x16x32_bf16 v[40:43],a[136:139],v[132:135],v[40:43]
buffer_load_dwordx4 v6,s[32:35],s63 offen offset:3072 sc1 lds
v_mfma_f32_16x16x32_bf16 v[40:43],a[144:147],v[136:139],v[40:43]
ds_read_b128 v[180:183],v4 offset:62464
s_waitcnt vmcnt(11)
v_mfma_f32_16x16x32_bf16 v[40:43],a[152:155],v[140:143],v[40:43]
v_cmp_ne_u64 vcc,v[16:17],v[12:13]
s_add_u32 s63,s63,593920
v_mfma_f32_16x16x32_bf16 v[44:47],a[156:159],v[140:143],v[44:47]
ds_read_b128 v[184:187],v4 offset:63488
s_cbranch_vccnz .L_rtw1_1
.L_bkw1_1:
v_mfma_f32_16x16x32_bf16 v[44:47],a[148:151],v[136:139],v[44:47]
s_add_u32 s66,s66,1187840
s_xor_b32 s64,s64,s65
v_mfma_f32_16x16x32_bf16 v[44:47],a[140:143],v[132:135],v[44:47]
ds_read_b128 v[188:191],v4 offset:64512
s_barrier
v_mfma_f32_16x16x32_bf16 v[44:47],a[132:135],v[128:131],v[44:47]
v_xor_b32 v4,65536,v4
v_mfma_f32_16x16x32_bf16 v[44:47],a[164:167],v[144:147],v[44:47]
ds_read_b128 v[128:131],v4 offset:0
v_lshlrev_b32 v32,16,v48
v_mfma_f32_16x16x32_bf16 v[44:47],a[172:175],v[148:151],v[44:47]
v_and_b32 v33,0xffff0000,v48
v_lshlrev_b32 v34,16,v49
v_mfma_f32_16x16x32_bf16 v[44:47],a[180:183],v[152:155],v[44:47]
ds_read_b128 v[132:135],v4 offset:1024
v_and_b32 v35,0xffff0000,v49
v_mfma_f32_16x16x32_bf16 v[44:47],a[188:191],v[156:159],v[44:47]
v_lshlrev_b32 v36,16,v50
v_and_b32 v37,0xffff0000,v50
v_mfma_f32_16x16x32_bf16 v[40:43],a[184:187],v[156:159],v[40:43]
ds_read_b128 v[136:139],v4 offset:2048
s_add_u32 m0,s64,0
v_mfma_f32_16x16x32_bf16 v[40:43],a[176:179],v[152:155],v[40:43]
buffer_load_dwordx4 v2,s[32:35],s63 offen offset:0 sc1 lds
v_mfma_f32_16x16x32_bf16 v[40:43],a[168:171],v[148:151],v[40:43]
ds_read_b128 v[140:143],v4 offset:3072
v_lshlrev_b32 v38,16,v51
v_mfma_f32_16x16x32_bf16 v[40:43],a[160:163],v[144:147],v[40:43]
v_and_b32 v39,0xffff0000,v51
s_waitcnt lgkmcnt(4)
v_mfma_f32_16x16x32_bf16 v[40:43],a[192:195],v[160:163],v[40:43]
ds_read_b128 v[144:147],v4 offset:4096
v_mfma_f32_16x16x32_bf16 v[40:43],a[200:203],v[164:167],v[40:43]
buffer_load_dwordx4 v2,s[32:35],s63 offen offset:1024 sc1 lds
v_mfma_f32_16x16x32_bf16 v[40:43],a[208:211],v[168:171],v[40:43]
ds_read_b128 v[148:151],v4 offset:5120
v_mfma_f32_16x16x32_bf16 v[40:43],a[216:219],v[172:175],v[40:43]
v_mfma_f32_16x16x32_bf16 v[44:47],a[220:223],v[172:175],v[44:47]
ds_read_b128 v[152:155],v4 offset:6144
v_mfma_f32_16x16x32_bf16 v[44:47],a[212:215],v[168:171],v[44:47]
buffer_load_dwordx4 v2,s[32:35],s63 offen offset:2048 sc1 lds
v_mfma_f32_16x16x32_bf16 v[44:47],a[204:207],v[164:167],v[44:47]
ds_read_b128 v[156:159],v4 offset:7168
v_mfma_f32_16x16x32_bf16 v[44:47],a[196:199],v[160:163],v[44:47]
v_mfma_f32_16x16x32_bf16 v[44:47],a[228:231],v[176:179],v[44:47]
ds_read_b128 v[160:163],v4 offset:8192
v_mfma_f32_16x16x32_bf16 v[44:47],a[236:239],v[180:183],v[44:47]
buffer_load_dwordx4 v2,s[32:35],s63 offen offset:3072 sc1 lds
v_mfma_f32_16x16x32_bf16 v[44:47],a[244:247],v[184:187],v[44:47]
ds_read_b128 v[164:167],v4 offset:9216
v_mfma_f32_16x16x32_bf16 v[44:47],a[252:255],v[188:191],v[44:47]
v_mfma_f32_16x16x32_bf16 v[40:43],a[248:251],v[188:191],v[40:43]
ds_read_b128 v[168:171],v4 offset:10240
s_add_u32 m0,s64,4096
v_mfma_f32_16x16x32_bf16 v[40:43],a[240:243],v[184:187],v[40:43]
buffer_load_dwordx4 v6,s[32:35],s63 offen offset:0 sc1 lds
v_mfma_f32_16x16x32_bf16 v[40:43],a[232:235],v[180:183],v[40:43]
ds_read_b128 v[172:175],v4 offset:11264
s_sub_u32 s62,s62,1
v_mfma_f32_16x16x32_bf16 v[40:43],a[224:227],v[176:179],v[40:43]
s_cbranch_scc0 .L_wl_1
s_nop 1
s_add_u32 s68,s68,593920
s_nop 3
v_cvt_pk_bf16_f32 v56,v40,v41
v_cvt_pk_bf16_f32 v57,v42,v43
v_cvt_pk_bf16_f32 v58,v44,v45
v_cvt_pk_bf16_f32 v59,v46,v47
buffer_store_dwordx4 v[56:59],v2,s[32:35],s68 offen sc1
v_add_f32 v60,v40,v41
v_mul_f32 v61,v40,v40
v_fmac_f32 v61,v41,v41
v_add_f32 v60,v60,v42
v_fmac_f32 v61,v42,v42
v_add_f32 v60,v60,v43
v_fmac_f32 v61,v43,v43
v_add_f32 v60,v60,v44
v_fmac_f32 v61,v44,v44
v_add_f32 v60,v60,v45
v_fmac_f32 v61,v45,v45
v_add_f32 v60,v60,v46
v_fmac_f32 v61,v46,v46
v_add_f32 v60,v60,v47
v_fmac_f32 v61,v47,v47
s_nop 1
v_permlane16_swap_b32 v60,v61
v_add_f32 v60,v60,v61
s_nop 1
v_permlane32_swap_b32 v60,v61
v_add_f32 v60,v60,v61
buffer_store_dword v60,v15,s[32:35],s68 offen offset:1024 sc1
s_waitcnt vmcnt(9)
s_barrier
buffer_store_dwordx2 v[16:17],v2,s[92:95],s68 idxen sc1
s_add_u32 s68,s68,593920
s_waitcnt vmcnt(1)
s_barrier
buffer_store_dwordx2 v[16:17],v2,s[92:95],s68 idxen sc1
s_cbranch_execz .L_wbk
s_branch .L_end
.p2align 6
.L_wv2:
.L_wl_2:
s_waitcnt lgkmcnt(4)
v_mfma_f32_16x16x32_bf16 v[32:35],a[0:3],v[128:131],v[32:35]
ds_read_b128 v[176:179],v4 offset:12288
v_mfma_f32_16x16x32_bf16 v[32:35],a[8:11],v[132:135],v[32:35]
buffer_load_dwordx4 v6,s[32:35],s63 offen offset:1024 sc1 lds
v_mfma_f32_16x16x32_bf16 v[32:35],a[16:19],v[136:139],v[32:35]
ds_read_b128 v[180:183],v4 offset:13312
s_add_u32 s68,s68,593920
v_mfma_f32_16x16x32_bf16 v[32:35],a[24:27],v[140:143],v[32:35]
v_cvt_pk_bf16_f32 v56,v40,v41
v_cvt_pk_bf16_f32 v57,v42,v43
v_mfma_f32_16x16x32_bf16 v[36:39],a[28:31],v[140:143],v[36:39]
ds_read_b128 v[184:187],v4 offset:14336
v_cvt_pk_bf16_f32 v58,v44,v45
v_mfma_f32_16x16x32_bf16 v[36:39],a[20:23],v[136:139],v[36:39]
v_cvt_pk_bf16_f32 v59,v46,v47
v_add_f32 v60,v40,v41
v_mfma_f32_16x16x32_bf16 v[36:39],a[12:15],v[132:135],v[36:39]
ds_read_b128 v[188:191],v4 offset:15360
v_mul_f32 v61,v40,v40
v_mfma_f32_16x16x32_bf16 v[36:39],a[4:7],v[128:131],v[36:39]
v_fmac_f32 v61,v41,v41
v_add_f32 v60,v60,v42
v_mfma_f32_16x16x32_bf16 v[36:39],a[36:39],v[144:147],v[36:39]
ds_read_b128 v[128:131],v4 offset:16384
v_fmac_f32 v61,v42,v42
v_mfma_f32_16x16x32_bf16 v[36:39],a[44:47],v[148:151],v[36:39]
buffer_store_dwordx4 v[56:59],v2,s[32:35],s68 offen sc1
v_mfma_f32_16x16x32_bf16 v[36:39],a[52:55],v[152:155],v[36:39]
ds_read_b128 v[132:135],v4 offset:17408
v_add_f32 v60,v60,v43
v_mfma_f32_16x16x32_bf16 v[36:39],a[60:63],v[156:159],v[36:39]
v_fmac_f32 v61,v43,v43
v_add_f32 v60,v60,v44
v_mfma_f32_16x16x32_bf16 v[32:35],a[56:59],v[156:159],v[32:35]
ds_read_b128 v[136:139],v4 offset:18432
v_fmac_f32 v61,v44,v44
v_mfma_f32_16x16x32_bf16 v[32:35],a[48:51],v[152:155],v[32:35]
v_add_f32 v60,v60,v45
v_fmac_f32 v61,v45,v45
v_mfma_f32_16x16x32_bf16 v[32:35],a[40:43],v[148:151],v[32:35]
ds_read_b128 v[140:143],v4 offset:19456
v_add_f32 v60,v60,v46
v_mfma_f32_16x16x32_bf16 v[32:35],a[32:35],v[144:147],v[32:35]
v_fmac_f32 v61,v46,v46
s_waitcnt lgkmcnt(4)
v_mfma_f32_16x16x32_bf16 v[32:35],a[64:67],v[160:163],v[32:35]
ds_read_b128 v[144:147],v4 offset:20480
v_add_f32 v60,v60,v47
v_mfma_f32_16x16x32_bf16 v[32:35],a[72:75],v[164:167],v[32:35]
v_fmac_f32 v61,v47,v47
v_mfma_f32_16x16x32_bf16 v[32:35],a[80:83],v[168:171],v[32:35]
ds_read_b128 v[148:151],v4 offset:21504
v_mfma_f32_16x16x32_bf16 v[32:35],a[88:91],v[172:175],v[32:35]
v_permlane16_swap_b32 v60,v61
v_mfma_f32_16x16x32_bf16 v[36:39],a[92:95],v[172:175],v[36:39]
ds_read_b128 v[152:155],v4 offset:22528
v_add_f32 v60,v60,v61
v_mfma_f32_16x16x32_bf16 v[36:39],a[84:87],v[168:171],v[36:39]
buffer_load_dwordx4 v[48:51],v2,s[32:35],s71 offen sc1
v_mfma_f32_16x16x32_bf16 v[36:39],a[76:79],v[164:167],v[36:39]
ds_read_b128 v[156:159],v4 offset:23552
s_add_u32 s71,s71,593920
v_mfma_f32_16x16x32_bf16 v[36:39],a[68:71],v[160:163],v[36:39]
v_permlane32_swap_b32 v60,v61
v_mfma_f32_16x16x32_bf16 v[36:39],a[100:103],v[176:179],v[36:39]
ds_read_b128 v[160:163],v4 offset:24576
v_add_f32 v60,v60,v61
v_mfma_f32_16x16x32_bf16 v[36:39],a[108:111],v[180:183],v[36:39]
buffer_store_dword v60,v15,s[32:35],s68 offen offset:1024 sc1
v_mfma_f32_16x16x32_bf16 v[36:39],a[116:119],v[184:187],v[36:39]
ds_read_b128 v[164:167],v4 offset:25600
v_mfma_f32_16x16x32_bf16 v[36:39],a[124:127],v[188:191],v[36:39]
v_mfma_f32_16x16x32_bf16 v[32:35],a[120:123],v[188:191],v[32:35]
ds_read_b128 v[168:171],v4 offset:26624
v_mfma_f32_16x16x32_bf16 v[32:35],a[112:115],v[184:187],v[32:35]
buffer_load_dwordx4 v6,s[32:35],s63 offen offset:2048 sc1 lds
v_mfma_f32_16x16x32_bf16 v[32:35],a[104:107],v[180:183],v[32:35]
ds_read_b128 v[172:175],v4 offset:27648
v_mfma_f32_16x16x32_bf16 v[32:35],a[96:99],v[176:179],v[32:35]
s_waitcnt lgkmcnt(4)
v_mfma_f32_16x16x32_bf16 v[32:35],a[128:131],v[128:131],v[32:35]
ds_read_b128 v[176:179],v4 offset:28672
v_mfma_f32_16x16x32_bf16 v[32:35],a[136:139],v[132:135],v[32:35]
buffer_load_dwordx4 v6,s[32:35],s63 offen offset:3072 sc1 lds
v_mfma_f32_16x16x32_bf16 v[32:35],a[144:147],v[136:139],v[32:35]
ds_read_b128 v[180:183],v4 offset:29696
s_add_u32 s63,s63,593920
v_mfma_f32_16x16x32_bf16 v[32:35],a[152:155],v[140:143],v[32:35]
v_mfma_f32_16x16x32_bf16 v[36:39],a[156:159],v[140:143],v[36:39]
ds_read_b128 v[184:187],v4 offset:30720
v_mfma_f32_16x16x32_bf16 v[36:39],a[148:151],v[136:139],v[36:39]
s_waitcnt vmcnt(11)
s_barrier
v_mfma_f32_16x16x32_bf16 v[36:39],a[140:143],v[132:135],v[36:39]
ds_read_b128 v[188:191],v4 offset:31744
v_mfma_f32_16x16x32_bf16 v[36:39],a[132:135],v[128:131],v[36:39]
v_mfma_f32_16x16x32_bf16 v[36:39],a[164:167],v[144:147],v[36:39]
ds_read_b128 v[128:131],v4 offset:32768
v_lshlrev_b32 v40,16,v52
v_mfma_f32_16x16x32_bf16 v[36:39],a[172:175],v[148:151],v[36:39]
v_and_b32 v41,0xffff0000,v52
v_lshlrev_b32 v42,16,v53
v_mfma_f32_16x16x32_bf16 v[36:39],a[180:183],v[152:155],v[36:39]
ds_read_b128 v[132:135],v4 offset:33792
v_and_b32 v43,0xffff0000,v53
v_mfma_f32_16x16x32_bf16 v[36:39],a[188:191],v[156:159],v[36:39]
v_lshlrev_b32 v44,16,v54
v_and_b32 v45,0xffff0000,v54
v_mfma_f32_16x16x32_bf16 v[32:35],a[184:187],v[156:159],v[32:35]
ds_read_b128 v[136:139],v4 offset:34816
s_add_u32 m0,s64,32768
v_mfma_f32_16x16x32_bf16 v[32:35],a[176:179],v[152:155],v[32:35]
buffer_load_dwordx4 v2,s[32:35],s63 offen offset:0 sc1 lds
v_mfma_f32_16x16x32_bf16 v[32:35],a[168:171],v[148:151],v[32:35]
ds_read_b128 v[140:143],v4 offset:35840
v_lshlrev_b32 v46,16,v55
v_mfma_f32_16x16x32_bf16 v[32:35],a[160:163],v[144:147],v[32:35]
v_and_b32 v47,0xffff0000,v55
s_waitcnt lgkmcnt(4)
v_mfma_f32_16x16x32_bf16 v[32:35],a[192:195],v[160:163],v[32:35]
ds_read_b128 v[144:147],v4 offset:36864
v_mfma_f32_16x16x32_bf16 v[32:35],a[200:203],v[164:167],v[32:35]
buffer_load_dwordx4 v2,s[32:35],s63 offen offset:1024 sc1 lds
v_mfma_f32_16x16x32_bf16 v[32:35],a[208:211],v[168:171],v[32:35]
ds_read_b128 v[148:151],v4 offset:37888
v_mfma_f32_16x16x32_bf16 v[32:35],a[216:219],v[172:175],v[32:35]
v_mfma_f32_16x16x32_bf16 v[36:39],a[220:223],v[172:175],v[36:39]
ds_read_b128 v[152:155],v4 offset:38912
v_mfma_f32_16x16x32_bf16 v[36:39],a[212:215],v[168:171],v[36:39]
buffer_load_dwordx4 v2,s[32:35],s63 offen offset:2048 sc1 lds
v_mfma_f32_16x16x32_bf16 v[36:39],a[204:207],v[164:167],v[36:39]
ds_read_b128 v[156:159],v4 offset:39936
v_mfma_f32_16x16x32_bf16 v[36:39],a[196:199],v[160:163],v[36:39]
v_mfma_f32_16x16x32_bf16 v[36:39],a[228:231],v[176:179],v[36:39]
ds_read_b128 v[160:163],v4 offset:40960
v_mfma_f32_16x16x32_bf16 v[36:39],a[236:239],v[180:183],v[36:39]
buffer_load_dwordx4 v2,s[32:35],s63 offen offset:3072 sc1 lds
v_mfma_f32_16x16x32_bf16 v[36:39],a[244:247],v[184:187],v[36:39]
ds_read_b128 v[164:167],v4 offset:41984
v_mfma_f32_16x16x32_bf16 v[36:39],a[252:255],v[188:191],v[36:39]
v_mfma_f32_16x16x32_bf16 v[32:35],a[248:251],v[188:191],v[32:35]
ds_read_b128 v[168:171],v4 offset:43008
s_add_u32 m0,s64,36864
v_mfma_f32_16x16x32_bf16 v[32:35],a[240:243],v[184:187],v[32:35]
buffer_load_dwordx4 v6,s[32:35],s63 offen offset:0 sc1 lds
v_mfma_f32_16x16x32_bf16 v[32:35],a[232:235],v[180:183],v[32:35]
ds_read_b128 v[172:175],v4 offset:44032
v_mfma_f32_16x16x32_bf16 v[32:35],a[224:227],v[176:179],v[32:35]
s_waitcnt lgkmcnt(4)
v_mfma_f32_16x16x32_bf16 v[40:43],a[0:3],v[128:131],v[40:43]
ds_read_b128 v[176:179],v4 offset:45056
v_mfma_f32_16x16x32_bf16 v[40:43],a[8:11],v[132:135],v[40:43]
buffer_load_dwordx4 v6,s[32:35],s63 offen offset:1024 sc1 lds
v_mfma_f32_16x16x32_bf16 v[40:43],a[16:19],v[136:139],v[40:43]
ds_read_b128 v[180:183],v4 offset:46080
s_add_u32 s68,s68,593920
v_mfma_f32_16x16x32_bf16 v[40:43],a[24:27],v[140:143],v[40:43]
v_cvt_pk_bf16_f32 v56,v32,v33
v_cvt_pk_bf16_f32 v57,v34,v35
v_mfma_f32_16x16x32_bf16 v[44:47],a[28:31],v[140:143],v[44:47]
ds_read_b128 v[184:187],v4 offset:47104
v_cvt_pk_bf16_f32 v58,v36,v37
v_mfma_f32_16x16x32_bf16 v[44:47],a[20:23],v[136:139],v[44:47]
v_cvt_pk_bf16_f32 v59,v38,v39
v_add_f32 v60,v32,v33
v_mfma_f32_16x16x32_bf16 v[44:47],a[12:15],v[132:135],v[44:47]
ds_read_b128 v[188:191],v4 offset:48128
v_mul_f32 v61,v32,v32
v_mfma_f32_16x16x32_bf16 v[44:47],a[4:7],v[128:131],v[44:47]
v_fmac_f32 v61,v33,v33
v_add_f32 v60,v60,v34
v_mfma_f32_16x16x32_bf16 v[44:47],a[36:39],v[144:147],v[44:47]
ds_read_b128 v[128:131],v4 offset:49152
v_fmac_f32 v61,v34,v34
v_mfma_f32_16x16x32_bf16 v[44:47],a[44:47],v[148:151],v[44:47]
buffer_store_dwordx4 v[56:59],v2,s[32:35],s68 offen sc1
v_mfma_f32_16x16x32_bf16 v[44:47],a[52:55],v[152:155],v[44:47]
ds_read_b128 v[132:135],v4 offset:50176
v_add_f32 v60,v60,v35
v_mfma_f32_16x16x32_bf16 v[44:47],a[60:63],v[156:159],v[44:47]
v_fmac_f32 v61,v35,v35
v_add_f32 v60,v60,v36
v_mfma_f32_16x16x32_bf16 v[40:43],a[56:59],v[156:159],v[40:43]
ds_read_b128 v[136:139],v4 offset:51200
v_fmac_f32 v61,v36,v36
v_mfma_f32_16x16x32_bf16 v[40:43],a[48:51],v[152:155],v[40:43]
v_add_f32 v60,v60,v37
v_fmac_f32 v61,v37,v37
v_mfma_f32_16x16x32_bf16 v[40:43],a[40:43],v[148:151],v[40:43]
ds_read_b128 v[140:143],v4 offset:52224
v_add_f32 v60,v60,v38
v_mfma_f32_16x16x32_bf16 v[40:43],a[32:35],v[144:147],v[40:43]
v_fmac_f32 v61,v38,v38
s_waitcnt lgkmcnt(4)
v_mfma_f32_16x16x32_bf16 v[40:43],a[64:67],v[160:163],v[40:43]
ds_read_b128 v[144:147],v4 offset:53248
v_add_f32 v60,v60,v39
v_mfma_f32_16x16x32_bf16 v[40:43],a[72:75],v[164:167],v[40:43]
v_fmac_f32 v61,v39,v39
v_mfma_f32_16x16x32_bf16 v[40:43],a[80:83],v[168:171],v[40:43]
ds_read_b128 v[148:151],v4 offset:54272
v_mfma_f32_16x16x32_bf16 v[40:43],a[88:91],v[172:175],v[40:43]
v_permlane16_swap_b32 v60,v61
v_mfma_f32_16x16x32_bf16 v[44:47],a[92:95],v[172:175],v[44:47]
ds_read_b128 v[152:155],v4 offset:55296
v_add_f32 v60,v60,v61
v_mfma_f32_16x16x32_bf16 v[44:47],a[84:87],v[168:171],v[44:47]
buffer_load_dwordx4 v[52:55],v2,s[32:35],s71 offen sc1
v_mfma_f32_16x16x32_bf16 v[44:47],a[76:79],v[164:167],v[44:47]
ds_read_b128 v[156:159],v4 offset:56320
s_add_u32 s71,s71,593920
v_mfma_f32_16x16x32_bf16 v[44:47],a[68:71],v[160:163],v[44:47]
v_permlane32_swap_b32 v60,v61
v_mfma_f32_16x16x32_bf16 v[44:47],a[100:103],v[176:179],v[44:47]
ds_read_b128 v[160:163],v4 offset:57344
v_add_f32 v60,v60,v61
v_mfma_f32_16x16x32_bf16 v[44:47],a[108:111],v[180:183],v[44:47]
buffer_store_dword v60,v15,s[32:35],s68 offen offset:1024 sc1
v_mfma_f32_16x16x32_bf16 v[44:47],a[116:119],v[184:187],v[44:47]
ds_read_b128 v[164:167],v4 offset:58368
v_mfma_f32_16x16x32_bf16 v[44:47],a[124:127],v[188:191],v[44:47]
v_mfma_f32_16x16x32_bf16 v[40:43],a[120:123],v[188:191],v[40:43]
ds_read_b128 v[168:171],v4 offset:59392
v_mfma_f32_16x16x32_bf16 v[40:43],a[112:115],v[184:187],v[40:43]
buffer_load_dwordx4 v6,s[32:35],s63 offen offset:2048 sc1 lds
v_mfma_f32_16x16x32_bf16 v[40:43],a[104:107],v[180:183],v[40:43]
ds_read_b128 v[172:175],v4 offset:60416
v_mfma_f32_16x16x32_bf16 v[40:43],a[96:99],v[176:179],v[40:43]
s_waitcnt lgkmcnt(4)
v_mfma_f32_16x16x32_bf16 v[40:43],a[128:131],v[128:131],v[40:43]
ds_read_b128 v[176:179],v4 offset:61440
v_mfma_f32_16x16x32_bf16 v[40:43],a[136:139],v[132:135],v[40:43]
buffer_load_dwordx4 v6,s[32:35],s63 offen offset:3072 sc1 lds
v_mfma_f32_16x16x32_bf16 v[40:43],a[144:147],v[136:139],v[40:43]
ds_read_b128 v[180:183],v4 offset:62464
s_add_u32 s63,s63,593920
v_mfma_f32_16x16x32_bf16 v[40:43],a[152:155],v[140:143],v[40:43]
s_xor_b32 s64,s64,s65
v_mfma_f32_16x16x32_bf16 v[44:47],a[156:159],v[140:143],v[44:47]
ds_read_b128 v[184:187],v4 offset:63488
v_mfma_f32_16x16x32_bf16 v[44:47],a[148:151],v[136:139],v[44:47]
s_waitcnt vmcnt(11)
s_barrier
v_mfma_f32_16x16x32_bf16 v[44:47],a[140:143],v[132:135],v[44:47]
ds_read_b128 v[188:191],v4 offset:64512
v_xor_b32 v4,65536,v4
v_mfma_f32_16x16x32_bf16 v[44:47],a[132:135],v[128:131],v[44:47]
v_mfma_f32_16x16x32_bf16 v[44:47],a[164:167],v[144:147],v[44:47]
ds_read_b128 v[128:131],v4 offset:0
)#"
R"#(
v_lshlrev_b32 v32,16,v48
v_mfma_f32_16x16x32_bf16 v[44:47],a[172:175],v[148:151],v[44:47]
v_and_b32 v33,0xffff0000,v48
v_lshlrev_b32 v34,16,v49
v_mfma_f32_16x16x32_bf16 v[44:47],a[180:183],v[152:155],v[44:47]
ds_read_b128 v[132:135],v4 offset:1024
v_and_b32 v35,0xffff0000,v49
v_mfma_f32_16x16x32_bf16 v[44:47],a[188:191],v[156:159],v[44:47]
v_lshlrev_b32 v36,16,v50
v_and_b32 v37,0xffff0000,v50
v_mfma_f32_16x16x32_bf16 v[40:43],a[184:187],v[156:159],v[40:43]
ds_read_b128 v[136:139],v4 offset:2048
s_add_u32 m0,s64,0
v_mfma_f32_16x16x32_bf16 v[40:43],a[176:179],v[152:155],v[40:43]
buffer_load_dwordx4 v2,s[32:35],s63 offen offset:0 sc1 lds
v_mfma_f32_16x16x32_bf16 v[40:43],a[168:171],v[148:151],v[40:43]
ds_read_b128 v[140:143],v4 offset:3072
v_lshlrev_b32 v38,16,v51
v_mfma_f32_16x16x32_bf16 v[40:43],a[160:163],v[144:147],v[40:43]
v_and_b32 v39,0xffff0000,v51
s_waitcnt lgkmcnt(4)
v_mfma_f32_16x16x32_bf16 v[40:43],a[192:195],v[160:163],v[40:43]
ds_read_b128 v[144:147],v4 offset:4096
v_mfma_f32_16x16x32_bf16 v[40:43],a[200:203],v[164:167],v[40:43]
buffer_load_dwordx4 v2,s[32:35],s63 offen offset:1024 sc1 lds
v_mfma_f32_16x16x32_bf16 v[40:43],a[208:211],v[168:171],v[40:43]
ds_read_b128 v[148:151],v4 offset:5120
v_mfma_f32_16x16x32_bf16 v[40:43],a[216:219],v[172:175],v[40:43]
v_mfma_f32_16x16x32_bf16 v[44:47],a[220:223],v[172:175],v[44:47]
ds_read_b128 v[152:155],v4 offset:6144
v_mfma_f32_16x16x32_bf16 v[44:47],a[212:215],v[168:171],v[44:47]
buffer_load_dwordx4 v2,s[32:35],s63 offen offset:2048 sc1 lds
v_mfma_f32_16x16x32_bf16 v[44:47],a[204:207],v[164:167],v[44:47]
ds_read_b128 v[156:159],v4 offset:7168
v_mfma_f32_16x16x32_bf16 v[44:47],a[196:199],v[160:163],v[44:47]
v_mfma_f32_16x16x32_bf16 v[44:47],a[228:231],v[176:179],v[44:47]
ds_read_b128 v[160:163],v4 offset:8192
v_mfma_f32_16x16x32_bf16 v[44:47],a[236:239],v[180:183],v[44:47]
buffer_load_dwordx4 v2,s[32:35],s63 offen offset:3072 sc1 lds
v_mfma_f32_16x16x32_bf16 v[44:47],a[244:247],v[184:187],v[44:47]
ds_read_b128 v[164:167],v4 offset:9216
v_mfma_f32_16x16x32_bf16 v[44:47],a[252:255],v[188:191],v[44:47]
v_mfma_f32_16x16x32_bf16 v[40:43],a[248:251],v[188:191],v[40:43]
ds_read_b128 v[168:171],v4 offset:10240
s_add_u32 m0,s64,4096
v_mfma_f32_16x16x32_bf16 v[40:43],a[240:243],v[184:187],v[40:43]
buffer_load_dwordx4 v6,s[32:35],s63 offen offset:0 sc1 lds
v_mfma_f32_16x16x32_bf16 v[40:43],a[232:235],v[180:183],v[40:43]
ds_read_b128 v[172:175],v4 offset:11264
s_sub_u32 s62,s62,1
v_mfma_f32_16x16x32_bf16 v[40:43],a[224:227],v[176:179],v[40:43]
s_cbranch_scc0 .L_wl_2
s_nop 1
s_add_u32 s68,s68,593920
s_nop 3
v_cvt_pk_bf16_f32 v56,v40,v41
v_cvt_pk_bf16_f32 v57,v42,v43
v_cvt_pk_bf16_f32 v58,v44,v45
v_cvt_pk_bf16_f32 v59,v46,v47
buffer_store_dwordx4 v[56:59],v2,s[32:35],s68 offen sc1
v_add_f32 v60,v40,v41
v_mul_f32 v61,v40,v40
v_fmac_f32 v61,v41,v41
v_add_f32 v60,v60,v42
v_fmac_f32 v61,v42,v42
v_add_f32 v60,v60,v43
v_fmac_f32 v61,v43,v43
v_add_f32 v60,v60,v44
v_fmac_f32 v61,v44,v44
v_add_f32 v60,v60,v45
v_fmac_f32 v61,v45,v45
v_add_f32 v60,v60,v46
v_fmac_f32 v61,v46,v46
v_add_f32 v60,v60,v47
v_fmac_f32 v61,v47,v47
s_nop 1
v_permlane16_swap_b32 v60,v61
v_add_f32 v60,v60,v61
s_nop 1
v_permlane32_swap_b32 v60,v61
v_add_f32 v60,v60,v61
buffer_store_dword v60,v15,s[32:35],s68 offen offset:1024 sc1
s_waitcnt vmcnt(9)
s_barrier
buffer_store_dwordx2 v[16:17],v2,s[92:95],s68 idxen sc1
s_add_u32 s68,s68,593920
s_waitcnt vmcnt(1)
s_barrier
buffer_store_dwordx2 v[16:17],v2,s[92:95],s68 idxen sc1
s_cbranch_execz .L_wbk
s_branch .L_end
.p2align 6
.L_wv3:
.L_wl_3:
s_waitcnt lgkmcnt(4)
v_mfma_f32_16x16x32_bf16 v[32:35],a[0:3],v[128:131],v[32:35]
ds_read_b128 v[176:179],v4 offset:12288
v_mfma_f32_16x16x32_bf16 v[32:35],a[8:11],v[132:135],v[32:35]
buffer_load_dwordx4 v6,s[32:35],s63 offen offset:1024 sc1 lds
v_mfma_f32_16x16x32_bf16 v[32:35],a[16:19],v[136:139],v[32:35]
ds_read_b128 v[180:183],v4 offset:13312
s_add_u32 s68,s68,593920
v_mfma_f32_16x16x32_bf16 v[32:35],a[24:27],v[140:143],v[32:35]
v_cvt_pk_bf16_f32 v56,v40,v41
v_cvt_pk_bf16_f32 v57,v42,v43
v_mfma_f32_16x16x32_bf16 v[36:39],a[28:31],v[140:143],v[36:39]
ds_read_b128 v[184:187],v4 offset:14336
v_cvt_pk_bf16_f32 v58,v44,v45
v_mfma_f32_16x16x32_bf16 v[36:39],a[20:23],v[136:139],v[36:39]
v_cvt_pk_bf16_f32 v59,v46,v47
v_add_f32 v60,v40,v41
v_mfma_f32_16x16x32_bf16 v[36:39],a[12:15],v[132:135],v[36:39]
ds_read_b128 v[188:191],v4 offset:15360
v_mul_f32 v61,v40,v40
v_mfma_f32_16x16x32_bf16 v[36:39],a[4:7],v[128:131],v[36:39]
v_fmac_f32 v61,v41,v41
v_add_f32 v60,v60,v42
v_mfma_f32_16x16x32_bf16 v[36:39],a[36:39],v[144:147],v[36:39]
ds_read_b128 v[128:131],v4 offset:16384
v_fmac_f32 v61,v42,v42
v_mfma_f32_16x16x32_bf16 v[36:39],a[44:47],v[148:151],v[36:39]
buffer_store_dwordx4 v[56:59],v2,s[32:35],s68 offen sc1
v_mfma_f32_16x16x32_bf16 v[36:39],a[52:55],v[152:155],v[36:39]
ds_read_b128 v[132:135],v4 offset:17408
v_add_f32 v60,v60,v43
v_mfma_f32_16x16x32_bf16 v[36:39],a[60:63],v[156:159],v[36:39]
v_fmac_f32 v61,v43,v43
v_add_f32 v60,v60,v44
v_mfma_f32_16x16x32_bf16 v[32:35],a[56:59],v[156:159],v[32:35]
ds_read_b128 v[136:139],v4 offset:18432
v_fmac_f32 v61,v44,v44
v_mfma_f32_16x16x32_bf16 v[32:35],a[48:51],v[152:155],v[32:35]
v_add_f32 v60,v60,v45
v_fmac_f32 v61,v45,v45
v_mfma_f32_16x16x32_bf16 v[32:35],a[40:43],v[148:151],v[32:35]
ds_read_b128 v[140:143],v4 offset:19456
v_add_f32 v60,v60,v46
v_mfma_f32_16x16x32_bf16 v[32:35],a[32:35],v[144:147],v[32:35]
v_fmac_f32 v61,v46,v46
s_waitcnt lgkmcnt(4)
v_mfma_f32_16x16x32_bf16 v[32:35],a[64:67],v[160:163],v[32:35]
ds_read_b128 v[144:147],v4 offset:20480
v_add_f32 v60,v60,v47
v_mfma_f32_16x16x32_bf16 v[32:35],a[72:75],v[164:167],v[32:35]
v_fmac_f32 v61,v47,v47
v_mfma_f32_16x16x32_bf16 v[32:35],a[80:83],v[168:171],v[32:35]
ds_read_b128 v[148:151],v4 offset:21504
v_mfma_f32_16x16x32_bf16 v[32:35],a[88:91],v[172:175],v[32:35]
v_permlane16_swap_b32 v60,v61
v_mfma_f32_16x16x32_bf16 v[36:39],a[92:95],v[172:175],v[36:39]
ds_read_b128 v[152:155],v4 offset:22528
v_add_f32 v60,v60,v61
v_mfma_f32_16x16x32_bf16 v[36:39],a[84:87],v[168:171],v[36:39]
buffer_load_dwordx4 v[48:51],v2,s[32:35],s71 offen sc1
v_mfma_f32_16x16x32_bf16 v[36:39],a[76:79],v[164:167],v[36:39]
ds_read_b128 v[156:159],v4 offset:23552
s_add_u32 s71,s71,593920
v_mfma_f32_16x16x32_bf16 v[36:39],a[68:71],v[160:163],v[36:39]
v_permlane32_swap_b32 v60,v61
v_mfma_f32_16x16x32_bf16 v[36:39],a[100:103],v[176:179],v[36:39]
ds_read_b128 v[160:163],v4 offset:24576
v_add_f32 v60,v60,v61
v_mfma_f32_16x16x32_bf16 v[36:39],a[108:111],v[180:183],v[36:39]
buffer_store_dword v60,v15,s[32:35],s68 offen offset:1024 sc1
v_mfma_f32_16x16x32_bf16 v[36:39],a[116:119],v[184:187],v[36:39]
ds_read_b128 v[164:167],v4 offset:25600
v_mfma_f32_16x16x32_bf16 v[36:39],a[124:127],v[188:191],v[36:39]
v_mfma_f32_16x16x32_bf16 v[32:35],a[120:123],v[188:191],v[32:35]
ds_read_b128 v[168:171],v4 offset:26624
v_mfma_f32_16x16x32_bf16 v[32:35],a[112:115],v[184:187],v[32:35]
buffer_load_dwordx4 v6,s[32:35],s63 offen offset:2048 sc1 lds
v_mfma_f32_16x16x32_bf16 v[32:35],a[104:107],v[180:183],v[32:35]
ds_read_b128 v[172:175],v4 offset:27648
v_mfma_f32_16x16x32_bf16 v[32:35],a[96:99],v[176:179],v[32:35]
s_waitcnt lgkmcnt(4)
v_mfma_f32_16x16x32_bf16 v[32:35],a[128:131],v[128:131],v[32:35]
ds_read_b128 v[176:179],v4 offset:28672
v_mfma_f32_16x16x32_bf16 v[32:35],a[136:139],v[132:135],v[32:35]
buffer_load_dwordx4 v6,s[32:35],s63 offen offset:3072 sc1 lds
v_mfma_f32_16x16x32_bf16 v[32:35],a[144:147],v[136:139],v[32:35]
ds_read_b128 v[180:183],v4 offset:29696
s_add_u32 s63,s63,593920
v_mfma_f32_16x16x32_bf16 v[32:35],a[152:155],v[140:143],v[32:35]
v_mfma_f32_16x16x32_bf16 v[36:39],a[156:159],v[140:143],v[36:39]
s_waitcnt vmcnt(11)
s_barrier
v_mfma_f32_16x16x32_bf16 v[36:39],a[148:151],v[136:139],v[36:39]
ds_read_b128 v[184:187],v4 offset:30720
v_mfma_f32_16x16x32_bf16 v[36:39],a[140:143],v[132:135],v[36:39]
ds_read_b128 v[188:191],v4 offset:31744
v_mfma_f32_16x16x32_bf16 v[36:39],a[132:135],v[128:131],v[36:39]
v_mfma_f32_16x16x32_bf16 v[36:39],a[164:167],v[144:147],v[36:39]
ds_read_b128 v[128:131],v4 offset:32768
v_lshlrev_b32 v40,16,v52
v_mfma_f32_16x16x32_bf16 v[36:39],a[172:175],v[148:151],v[36:39]
v_and_b32 v41,0xffff0000,v52
v_lshlrev_b32 v42,16,v53
v_mfma_f32_16x16x32_bf16 v[36:39],a[180:183],v[152:155],v[36:39]
ds_read_b128 v[132:135],v4 offset:33792
v_and_b32 v43,0xffff0000,v53
v_mfma_f32_16x16x32_bf16 v[36:39],a[188:191],v[156:159],v[36:39]
v_lshlrev_b32 v44,16,v54
v_and_b32 v45,0xffff0000,v54
v_mfma_f32_16x16x32_bf16 v[32:35],a[184:187],v[156:159],v[32:35]
ds_read_b128 v[136:139],v4 offset:34816
s_add_u32 m0,s64,32768
v_mfma_f32_16x16x32_bf16 v[32:35],a[176:179],v[152:155],v[32:35]
buffer_load_dwordx4 v2,s[32:35],s63 offen offset:0 sc1 lds
v_mfma_f32_16x16x32_bf16 v[32:35],a[168:171],v[148:151],v[32:35]
ds_read_b128 v[140:143],v4 offset:35840
v_lshlrev_b32 v46,16,v55
v_mfma_f32_16x16x32_bf16 v[32:35],a[160:163],v[144:147],v[32:35]
v_and_b32 v47,0xffff0000,v55
s_waitcnt lgkmcnt(4)
v_mfma_f32_16x16x32_bf16 v[32:35],a[192:195],v[160:163],v[32:35]
ds_read_b128 v[144:147],v4 offset:36864
v_mfma_f32_16x16x32_bf16 v[32:35],a[200:203],v[164:167],v[32:35]
buffer_load_dwordx4 v2,s[32:35],s63 offen offset:1024 sc1 lds
v_mfma_f32_16x16x32_bf16 v[32:35],a[208:211],v[168:171],v[32:35]
ds_read_b128 v[148:151],v4 offset:37888
v_mfma_f32_16x16x32_bf16 v[32:35],a[216:219],v[172:175],v[32:35]
v_mfma_f32_16x16x32_bf16 v[36:39],a[220:223],v[172:175],v[36:39]
ds_read_b128 v[152:155],v4 offset:38912
v_mfma_f32_16x16x32_bf16 v[36:39],a[212:215],v[168:171],v[36:39]
buffer_load_dwordx4 v2,s[32:35],s63 offen offset:2048 sc1 lds
v_mfma_f32_16x16x32_bf16 v[36:39],a[204:207],v[164:167],v[36:39]
ds_read_b128 v[156:159],v4 offset:39936
v_mfma_f32_16x16x32_bf16 v[36:39],a[196:199],v[160:163],v[36:39]
v_mfma_f32_16x16x32_bf16 v[36:39],a[228:231],v[176:179],v[36:39]
ds_read_b128 v[160:163],v4 offset:40960
v_mfma_f32_16x16x32_bf16 v[36:39],a[236:239],v[180:183],v[36:39]
buffer_load_dwordx4 v2,s[32:35],s63 offen offset:3072 sc1 lds
v_mfma_f32_16x16x32_bf16 v[36:39],a[244:247],v[184:187],v[36:39]
ds_read_b128 v[164:167],v4 offset:41984
v_mfma_f32_16x16x32_bf16 v[36:39],a[252:255],v[188:191],v[36:39]
v_mfma_f32_16x16x32_bf16 v[32:35],a[248:251],v[188:191],v[32:35]
ds_read_b128 v[168:171],v4 offset:43008
s_add_u32 m0,s64,36864
v_mfma_f32_16x16x32_bf16 v[32:35],a[240:243],v[184:187],v[32:35]
buffer_load_dwordx4 v6,s[32:35],s63 offen offset:0 sc1 lds
v_mfma_f32_16x16x32_bf16 v[32:35],a[232:235],v[180:183],v[32:35]
ds_read_b128 v[172:175],v4 offset:44032
v_mfma_f32_16x16x32_bf16 v[32:35],a[224:227],v[176:179],v[32:35]
s_waitcnt lgkmcnt(4)
v_mfma_f32_16x16x32_bf16 v[40:43],a[0:3],v[128:131],v[40:43]
ds_read_b128 v[176:179],v4 offset:45056
v_mfma_f32_16x16x32_bf16 v[40:43],a[8:11],v[132:135],v[40:43]
buffer_load_dwordx4 v6,s[32:35],s63 offen offset:1024 sc1 lds
v_mfma_f32_16x16x32_bf16 v[40:43],a[16:19],v[136:139],v[40:43]
ds_read_b128 v[180:183],v4 offset:46080
s_add_u32 s68,s68,593920
v_mfma_f32_16x16x32_bf16 v[40:43],a[24:27],v[140:143],v[40:43]
v_cvt_pk_bf16_f32 v56,v32,v33
v_cvt_pk_bf16_f32 v57,v34,v35
v_mfma_f32_16x16x32_bf16 v[44:47],a[28:31],v[140:143],v[44:47]
ds_read_b128 v[184:187],v4 offset:47104
v_cvt_pk_bf16_f32 v58,v36,v37
v_mfma_f32_16x16x32_bf16 v[44:47],a[20:23],v[136:139],v[44:47]
v_cvt_pk_bf16_f32 v59,v38,v39
v_add_f32 v60,v32,v33
v_mfma_f32_16x16x32_bf16 v[44:47],a[12:15],v[132:135],v[44:47]
ds_read_b128 v[188:191],v4 offset:48128
v_mul_f32 v61,v32,v32
v_mfma_f32_16x16x32_bf16 v[44:47],a[4:7],v[128:131],v[44:47]
v_fmac_f32 v61,v33,v33
v_add_f32 v60,v60,v34
v_mfma_f32_16x16x32_bf16 v[44:47],a[36:39],v[144:147],v[44:47]
ds_read_b128 v[128:131],v4 offset:49152
v_fmac_f32 v61,v34,v34
v_mfma_f32_16x16x32_bf16 v[44:47],a[44:47],v[148:151],v[44:47]
buffer_store_dwordx4 v[56:59],v2,s[32:35],s68 offen sc1
v_mfma_f32_16x16x32_bf16 v[44:47],a[52:55],v[152:155],v[44:47]
ds_read_b128 v[132:135],v4 offset:50176
v_add_f32 v60,v60,v35
v_mfma_f32_16x16x32_bf16 v[44:47],a[60:63],v[156:159],v[44:47]
v_fmac_f32 v61,v35,v35
v_add_f32 v60,v60,v36
v_mfma_f32_16x16x32_bf16 v[40:43],a[56:59],v[156:159],v[40:43]
ds_read_b128 v[136:139],v4 offset:51200
v_fmac_f32 v61,v36,v36
v_mfma_f32_16x16x32_bf16 v[40:43],a[48:51],v[152:155],v[40:43]
v_add_f32 v60,v60,v37
v_fmac_f32 v61,v37,v37
v_mfma_f32_16x16x32_bf16 v[40:43],a[40:43],v[148:151],v[40:43]
ds_read_b128 v[140:143],v4 offset:52224
v_add_f32 v60,v60,v38
v_mfma_f32_16x16x32_bf16 v[40:43],a[32:35],v[144:147],v[40:43]
v_fmac_f32 v61,v38,v38
s_waitcnt lgkmcnt(4)
v_mfma_f32_16x16x32_bf16 v[40:43],a[64:67],v[160:163],v[40:43]
ds_read_b128 v[144:147],v4 offset:53248
v_add_f32 v60,v60,v39
v_mfma_f32_16x16x32_bf16 v[40:43],a[72:75],v[164:167],v[40:43]
v_fmac_f32 v61,v39,v39
v_mfma_f32_16x16x32_bf16 v[40:43],a[80:83],v[168:171],v[40:43]
ds_read_b128 v[148:151],v4 offset:54272
v_mfma_f32_16x16x32_bf16 v[40:43],a[88:91],v[172:175],v[40:43]
v_permlane16_swap_b32 v60,v61
v_mfma_f32_16x16x32_bf16 v[44:47],a[92:95],v[172:175],v[44:47]
ds_read_b128 v[152:155],v4 offset:55296
v_add_f32 v60,v60,v61
v_mfma_f32_16x16x32_bf16 v[44:47],a[84:87],v[168:171],v[44:47]
buffer_load_dwordx4 v[52:55],v2,s[32:35],s71 offen sc1
v_mfma_f32_16x16x32_bf16 v[44:47],a[76:79],v[164:167],v[44:47]
ds_read_b128 v[156:159],v4 offset:56320
s_add_u32 s71,s71,593920
v_mfma_f32_16x16x32_bf16 v[44:47],a[68:71],v[160:163],v[44:47]
v_permlane32_swap_b32 v60,v61
v_mfma_f32_16x16x32_bf16 v[44:47],a[100:103],v[176:179],v[44:47]
ds_read_b128 v[160:163],v4 offset:57344
v_add_f32 v60,v60,v61
v_mfma_f32_16x16x32_bf16 v[44:47],a[108:111],v[180:183],v[44:47]
buffer_store_dword v60,v15,s[32:35],s68 offen offset:1024 sc1
v_mfma_f32_16x16x32_bf16 v[44:47],a[116:119],v[184:187],v[44:47]
ds_read_b128 v[164:167],v4 offset:58368
v_mfma_f32_16x16x32_bf16 v[44:47],a[124:127],v[188:191],v[44:47]
v_mfma_f32_16x16x32_bf16 v[40:43],a[120:123],v[188:191],v[40:43]
ds_read_b128 v[168:171],v4 offset:59392
v_mfma_f32_16x16x32_bf16 v[40:43],a[112:115],v[184:187],v[40:43]
buffer_load_dwordx4 v6,s[32:35],s63 offen offset:2048 sc1 lds
v_mfma_f32_16x16x32_bf16 v[40:43],a[104:107],v[180:183],v[40:43]
ds_read_b128 v[172:175],v4 offset:60416
v_mfma_f32_16x16x32_bf16 v[40:43],a[96:99],v[176:179],v[40:43]
s_waitcnt lgkmcnt(4)
v_mfma_f32_16x16x32_bf16 v[40:43],a[128:131],v[128:131],v[40:43]
ds_read_b128 v[176:179],v4 offset:61440
v_mfma_f32_16x16x32_bf16 v[40:43],a[136:139],v[132:135],v[40:43]
buffer_load_dwordx4 v6,s[32:35],s63 offen offset:3072 sc1 lds
v_mfma_f32_16x16x32_bf16 v[40:43],a[144:147],v[136:139],v[40:43]
ds_read_b128 v[180:183],v4 offset:62464
s_add_u32 s63,s63,593920
v_mfma_f32_16x16x32_bf16 v[40:43],a[152:155],v[140:143],v[40:43]
s_xor_b32 s64,s64,s65
v_mfma_f32_16x16x32_bf16 v[44:47],a[156:159],v[140:143],v[44:47]
s_waitcnt vmcnt(11)
s_barrier
v_mfma_f32_16x16x32_bf16 v[44:47],a[148:151],v[136:139],v[44:47]
ds_read_b128 v[184:187],v4 offset:63488
v_mfma_f32_16x16x32_bf16 v[44:47],a[140:143],v[132:135],v[44:47]
ds_read_b128 v[188:191],v4 offset:64512
v_xor_b32 v4,65536,v4
v_mfma_f32_16x16x32_bf16 v[44:47],a[132:135],v[128:131],v[44:47]
v_mfma_f32_16x16x32_bf16 v[44:47],a[164:167],v[144:147],v[44:47]
ds_read_b128 v[128:131],v4 offset:0
v_lshlrev_b32 v32,16,v48
v_mfma_f32_16x16x32_bf16 v[44:47],a[172:175],v[148:151],v[44:47]
v_and_b32 v33,0xffff0000,v48
v_lshlrev_b32 v34,16,v49
v_mfma_f32_16x16x32_bf16 v[44:47],a[180:183],v[152:155],v[44:47]
ds_read_b128 v[132:135],v4 offset:1024
v_and_b32 v35,0xffff0000,v49
v_mfma_f32_16x16x32_bf16 v[44:47],a[188:191],v[156:159],v[44:47]
v_lshlrev_b32 v36,16,v50
v_and_b32 v37,0xffff0000,v50
v_mfma_f32_16x16x32_bf16 v[40:43],a[184:187],v[156:159],v[40:43]
ds_read_b128 v[136:139],v4 offset:2048
s_add_u32 m0,s64,0
v_mfma_f32_16x16x32_bf16 v[40:43],a[176:179],v[152:155],v[40:43]
buffer_load_dwordx4 v2,s[32:35],s63 offen offset:0 sc1 lds
v_mfma_f32_16x16x32_bf16 v[40:43],a[168:171],v[148:151],v[40:43]
ds_read_b128 v[140:143],v4 offset:3072
v_lshlrev_b32 v38,16,v51
v_mfma_f32_16x16x32_bf16 v[40:43],a[160:163],v[144:147],v[40:43]
v_and_b32 v39,0xffff0000,v51
s_waitcnt lgkmcnt(4)
v_mfma_f32_16x16x32_bf16 v[40:43],a[192:195],v[160:163],v[40:43]
ds_read_b128 v[144:147],v4 offset:4096
v_mfma_f32_16x16x32_bf16 v[40:43],a[200:203],v[164:167],v[40:43]
buffer_load_dwordx4 v2,s[32:35],s63 offen offset:1024 sc1 lds
v_mfma_f32_16x16x32_bf16 v[40:43],a[208:211],v[168:171],v[40:43]
ds_read_b128 v[148:151],v4 offset:5120
v_mfma_f32_16x16x32_bf16 v[40:43],a[216:219],v[172:175],v[40:43]
v_mfma_f32_16x16x32_bf16 v[44:47],a[220:223],v[172:175],v[44:47]
ds_read_b128 v[152:155],v4 offset:6144
v_mfma_f32_16x16x32_bf16 v[44:47],a[212:215],v[168:171],v[44:47]
buffer_load_dwordx4 v2,s[32:35],s63 offen offset:2048 sc1 lds
v_mfma_f32_16x16x32_bf16 v[44:47],a[204:207],v[164:167],v[44:47]
ds_read_b128 v[156:159],v4 offset:7168
v_mfma_f32_16x16x32_bf16 v[44:47],a[196:199],v[160:163],v[44:47]
v_mfma_f32_16x16x32_bf16 v[44:47],a[228:231],v[176:179],v[44:47]
ds_read_b128 v[160:163],v4 offset:8192
v_mfma_f32_16x16x32_bf16 v[44:47],a[236:239],v[180:183],v[44:47]
buffer_load_dwordx4 v2,s[32:35],s63 offen offset:3072 sc1 lds
v_mfma_f32_16x16x32_bf16 v[44:47],a[244:247],v[184:187],v[44:47]
ds_read_b128 v[164:167],v4 offset:9216
v_mfma_f32_16x16x32_bf16 v[44:47],a[252:255],v[188:191],v[44:47]
v_mfma_f32_16x16x32_bf16 v[40:43],a[248:251],v[188:191],v[40:43]
ds_read_b128 v[168:171],v4 offset:10240
s_add_u32 m0,s64,4096
v_mfma_f32_16x16x32_bf16 v[40:43],a[240:243],v[184:187],v[40:43]
buffer_load_dwordx4 v6,s[32:35],s63 offen offset:0 sc1 lds
v_mfma_f32_16x16x32_bf16 v[40:43],a[232:235],v[180:183],v[40:43]
ds_read_b128 v[172:175],v4 offset:11264
s_sub_u32 s62,s62,1
v_mfma_f32_16x16x32_bf16 v[40:43],a[224:227],v[176:179],v[40:43]
s_cbranch_scc0 .L_wl_3
s_nop 1
s_add_u32 s68,s68,593920
s_nop 3
v_cvt_pk_bf16_f32 v56,v40,v41
v_cvt_pk_bf16_f32 v57,v42,v43
v_cvt_pk_bf16_f32 v58,v44,v45
v_cvt_pk_bf16_f32 v59,v46,v47
buffer_store_dwordx4 v[56:59],v2,s[32:35],s68 offen sc1
v_add_f32 v60,v40,v41
v_mul_f32 v61,v40,v40
v_fmac_f32 v61,v41,v41
v_add_f32 v60,v60,v42
v_fmac_f32 v61,v42,v42
v_add_f32 v60,v60,v43
v_fmac_f32 v61,v43,v43
v_add_f32 v60,v60,v44
v_fmac_f32 v61,v44,v44
v_add_f32 v60,v60,v45
v_fmac_f32 v61,v45,v45
v_add_f32 v60,v60,v46
v_fmac_f32 v61,v46,v46
v_add_f32 v60,v60,v47
v_fmac_f32 v61,v47,v47
s_nop 1
v_permlane16_swap_b32 v60,v61
v_add_f32 v60,v60,v61
s_nop 1
v_permlane32_swap_b32 v60,v61
v_add_f32 v60,v60,v61
buffer_store_dword v60,v15,s[32:35],s68 offen offset:1024 sc1
s_waitcnt vmcnt(9)
s_barrier
buffer_store_dwordx2 v[16:17],v2,s[92:95],s68 idxen sc1
s_add_u32 s68,s68,593920
s_waitcnt vmcnt(1)
s_barrier
buffer_store_dwordx2 v[16:17],v2,s[92:95],s68 idxen sc1
s_cbranch_execz .L_wbk
s_branch .L_end
.L_h:
v_mov_b32 v16,0x3f803f80
v_mov_b32 v17,0x3f803f80
v_mov_b32 v18,0x3f803f80
v_mov_b32 v19,0x3f803f80
s_add_u32 s36,s14,0x0
s_addc_u32 s37,s15,0x0
s_and_b32 s37,s37,0xffff
s_mov_b32 s38,0x700000
s_mov_b32 s39,0x20000
s_add_u32 s40,s16,0xfffffc00
s_addc_u32 s41,s17,0xffffffff
s_and_b32 s41,s41,0xffff
s_mov_b32 s42,0x700400
s_mov_b32 s43,0x20000
s_lshl_b32 s96,s27,7
s_add_u32 s96,s96,6912
v_and_b32 v8,1,v1
v_bfe_u32 v3,v1,1,1
v_lshl_add_u32 v8,v8,1,v3
v_mul_u32_u24 v8,8,v8
s_lshl_b32 s48,s26,5
v_add_u32 v8,s48,v8
v_bfe_u32 v3,v1,4,1
v_lshl_add_u32 v8,v3,10,v8
v_lshrrev_b32 v4,3,v1
v_bfe_u32 v5,v1,4,1
s_and_b32 s48,s26,1
s_lshl_b32 s48,s48,1
v_or_b32 v5,s48,v5
v_lshlrev_b32 v5,1,v5
v_and_b32 v6,7,v1
v_xor_b32 v5,v5,v6
v_mul_u32_u24 v4,7168,v4
v_lshl_add_u32 v7,v5,4,v4
s_mul_i32 s48,s26,57344
s_add_u32 s96,s96,s48
s_mul_i32 s97,s26,2048
s_add_u32 m0,s97,0
s_nop 0
buffer_load_dwordx4 v7,s[36:39],s96 offen offset:0 lds
buffer_load_dwordx4 v7,s[40:43],s96 offen offset:1024 lds
s_add_u32 s96,s96,229376
s_add_u32 m0,s97,8192
s_nop 0
buffer_load_dwordx4 v7,s[36:39],s96 offen offset:0 lds
buffer_load_dwordx4 v7,s[40:43],s96 offen offset:1024 lds
s_add_u32 s96,s96,229376
s_add_u32 m0,s97,16384
s_nop 0
buffer_load_dwordx4 v7,s[36:39],s96 offen offset:0 lds
buffer_load_dwordx4 v7,s[40:43],s96 offen offset:1024 lds
s_add_u32 s96,s96,229376
s_add_u32 m0,s97,24576
s_nop 0
buffer_load_dwordx4 v7,s[36:39],s96 offen offset:0 lds
buffer_load_dwordx4 v7,s[40:43],s96 offen offset:1024 lds
s_add_u32 s96,s96,229376
s_add_u32 m0,s97,32768
s_nop 0
buffer_load_dwordx4 v7,s[36:39],s96 offen offset:0 lds
buffer_load_dwordx4 v7,s[40:43],s96 offen offset:1024 lds
s_add_u32 s96,s96,229376
s_add_u32 m0,s97,40960
s_nop 0
)#"
R"#(
buffer_load_dwordx4 v7,s[36:39],s96 offen offset:0 lds
buffer_load_dwordx4 v7,s[40:43],s96 offen offset:1024 lds
s_add_u32 s96,s96,229376
s_add_u32 m0,s97,49152
s_nop 0
buffer_load_dwordx4 v7,s[36:39],s96 offen offset:0 lds
buffer_load_dwordx4 v7,s[40:43],s96 offen offset:1024 lds
s_add_u32 s96,s96,229376
s_add_u32 m0,s97,57344
s_nop 0
buffer_load_dwordx4 v7,s[36:39],s96 offen offset:0 lds
buffer_load_dwordx4 v7,s[40:43],s96 offen offset:1024 lds
s_add_u32 s96,s96,229376
s_add_u32 m0,s97,65536
s_nop 0
buffer_load_dwordx4 v7,s[36:39],s96 offen offset:0 lds
buffer_load_dwordx4 v7,s[40:43],s96 offen offset:1024 lds
s_add_u32 s96,s96,229376
s_add_u32 m0,s97,73728
s_nop 0
buffer_load_dwordx4 v7,s[36:39],s96 offen offset:0 lds
buffer_load_dwordx4 v7,s[40:43],s96 offen offset:1024 lds
s_add_u32 s96,s96,229376
s_add_u32 m0,s97,81920
s_nop 0
buffer_load_dwordx4 v7,s[36:39],s96 offen offset:0 lds
buffer_load_dwordx4 v7,s[40:43],s96 offen offset:1024 lds
s_add_u32 s96,s96,229376
s_add_u32 m0,s97,90112
s_nop 0
buffer_load_dwordx4 v7,s[36:39],s96 offen offset:0 lds
buffer_load_dwordx4 v7,s[40:43],s96 offen offset:1024 lds
s_add_u32 s96,s96,229376
s_add_u32 m0,s97,98304
s_nop 0
buffer_load_dwordx4 v7,s[36:39],s96 offen offset:0 lds
buffer_load_dwordx4 v7,s[40:43],s96 offen offset:1024 lds
s_add_u32 s96,s96,229376
s_add_u32 m0,s97,106496
s_nop 0
buffer_load_dwordx4 v7,s[36:39],s96 offen offset:0 lds
buffer_load_dwordx4 v7,s[40:43],s96 offen offset:1024 lds
s_add_u32 s96,s96,229376
v_lshrrev_b32 v5,5,v1
v_bfe_u32 v4,v1,3,1
v_and_b32 v6,1,v5
v_lshl_add_u32 v4,v6,1,v4
v_lshlrev_b32 v4,5,v4
v_mul_u32_u24 v5,2048,v5
v_bfe_u32 v6,v1,2,2
v_lshl_add_u32 v5,v6,7,v5
v_xor_b32 v8,v4,v8
v_add_u32 v8,v5,v8
v_add_u32 v9,65536,v8
s_waitcnt vmcnt(24)
s_barrier
s_add_u32 m0,s97,114688
s_nop 0
buffer_load_dwordx4 v7,s[36:39],s96 offen offset:0 lds
buffer_load_dwordx4 v7,s[40:43],s96 offen offset:1024 lds
s_add_u32 s96,s96,229376
s_add_u32 m0,s97,122880
s_nop 0
buffer_load_dwordx4 v7,s[36:39],s96 offen offset:0 lds
buffer_load_dwordx4 v7,s[40:43],s96 offen offset:1024 lds
s_add_u32 s96,s96,229376
ds_read_b64_tr_b16 a[0:1],v8 offset:0
ds_read_b64_tr_b16 a[2:3],v8 offset:512
ds_read_b64_tr_b16 a[4:5],v8 offset:4096
ds_read_b64_tr_b16 a[6:7],v8 offset:4608
ds_read_b64_tr_b16 a[32:33],v8 offset:8192
ds_read_b64_tr_b16 a[34:35],v8 offset:8704
ds_read_b64_tr_b16 a[36:37],v8 offset:12288
ds_read_b64_tr_b16 a[38:39],v8 offset:12800
s_waitcnt lgkmcnt(0)
v_mfma_f32_32x32x16_bf16 v[112:127],a[0:3],v[16:19],0
v_mfma_f32_32x32x16_bf16 v[112:127],a[4:7],v[16:19],v[112:127]
v_mfma_f32_32x32x16_bf16 v[112:127],a[32:35],v[16:19],v[112:127]
v_mfma_f32_32x32x16_bf16 v[112:127],a[36:39],v[16:19],v[112:127]
s_waitcnt vmcnt(24)
s_barrier
s_add_u32 m0,s97,0
s_nop 0
buffer_load_dwordx4 v7,s[36:39],s96 offen offset:0 lds
buffer_load_dwordx4 v7,s[40:43],s96 offen offset:1024 lds
s_add_u32 s96,s96,229376
s_add_u32 m0,s97,8192
s_nop 0
buffer_load_dwordx4 v7,s[36:39],s96 offen offset:0 lds
buffer_load_dwordx4 v7,s[40:43],s96 offen offset:1024 lds
s_add_u32 s96,s96,229376
ds_read_b64_tr_b16 a[64:65],v8 offset:16384
ds_read_b64_tr_b16 a[66:67],v8 offset:16896
ds_read_b64_tr_b16 a[68:69],v8 offset:20480
ds_read_b64_tr_b16 a[70:71],v8 offset:20992
ds_read_b64_tr_b16 a[96:97],v8 offset:24576
ds_read_b64_tr_b16 a[98:99],v8 offset:25088
ds_read_b64_tr_b16 a[100:101],v8 offset:28672
ds_read_b64_tr_b16 a[102:103],v8 offset:29184
s_waitcnt lgkmcnt(0)
v_mfma_f32_32x32x16_bf16 v[112:127],a[64:67],v[16:19],v[112:127]
v_mfma_f32_32x32x16_bf16 v[112:127],a[68:71],v[16:19],v[112:127]
v_mfma_f32_32x32x16_bf16 v[112:127],a[96:99],v[16:19],v[112:127]
v_mfma_f32_32x32x16_bf16 v[112:127],a[100:103],v[16:19],v[112:127]
s_waitcnt vmcnt(24)
s_barrier
s_add_u32 m0,s97,16384
s_nop 0
buffer_load_dwordx4 v7,s[36:39],s96 offen offset:0 lds
buffer_load_dwordx4 v7,s[40:43],s96 offen offset:1024 lds
s_add_u32 s96,s96,229376
s_add_u32 m0,s97,24576
s_nop 0
buffer_load_dwordx4 v7,s[36:39],s96 offen offset:0 lds
buffer_load_dwordx4 v7,s[40:43],s96 offen offset:1024 lds
s_add_u32 s96,s96,229376
ds_read_b64_tr_b16 a[128:129],v8 offset:32768
ds_read_b64_tr_b16 a[130:131],v8 offset:33280
ds_read_b64_tr_b16 a[132:133],v8 offset:36864
ds_read_b64_tr_b16 a[134:135],v8 offset:37376
ds_read_b64_tr_b16 a[160:161],v8 offset:40960
ds_read_b64_tr_b16 a[162:163],v8 offset:41472
ds_read_b64_tr_b16 a[164:165],v8 offset:45056
ds_read_b64_tr_b16 a[166:167],v8 offset:45568
s_waitcnt lgkmcnt(0)
v_mfma_f32_32x32x16_bf16 v[112:127],a[128:131],v[16:19],v[112:127]
v_mfma_f32_32x32x16_bf16 v[112:127],a[132:135],v[16:19],v[112:127]
v_mfma_f32_32x32x16_bf16 v[112:127],a[160:163],v[16:19],v[112:127]
v_mfma_f32_32x32x16_bf16 v[112:127],a[164:167],v[16:19],v[112:127]
s_waitcnt vmcnt(24)
s_barrier
s_add_u32 m0,s97,32768
s_nop 0
buffer_load_dwordx4 v7,s[36:39],s96 offen offset:0 lds
buffer_load_dwordx4 v7,s[40:43],s96 offen offset:1024 lds
s_add_u32 s96,s96,229376
s_add_u32 m0,s97,40960
s_nop 0
buffer_load_dwordx4 v7,s[36:39],s96 offen offset:0 lds
buffer_load_dwordx4 v7,s[40:43],s96 offen offset:1024 lds
s_add_u32 s96,s96,229376
ds_read_b64_tr_b16 a[192:193],v8 offset:49152
ds_read_b64_tr_b16 a[194:195],v8 offset:49664
ds_read_b64_tr_b16 a[196:197],v8 offset:53248
ds_read_b64_tr_b16 a[198:199],v8 offset:53760
ds_read_b64_tr_b16 a[224:225],v8 offset:57344
ds_read_b64_tr_b16 a[226:227],v8 offset:57856
ds_read_b64_tr_b16 a[228:229],v8 offset:61440
ds_read_b64_tr_b16 a[230:231],v8 offset:61952
s_waitcnt lgkmcnt(0)
v_mfma_f32_32x32x16_bf16 v[112:127],a[192:195],v[16:19],v[112:127]
v_mfma_f32_32x32x16_bf16 v[112:127],a[196:199],v[16:19],v[112:127]
v_mfma_f32_32x32x16_bf16 v[112:127],a[224:227],v[16:19],v[112:127]
v_mfma_f32_32x32x16_bf16 v[112:127],a[228:231],v[16:19],v[112:127]
s_waitcnt vmcnt(24)
s_barrier
s_add_u32 m0,s97,49152
s_nop 0
buffer_load_dwordx4 v7,s[36:39],s96 offen offset:0 lds
buffer_load_dwordx4 v7,s[40:43],s96 offen offset:1024 lds
s_add_u32 s96,s96,229376
s_add_u32 m0,s97,57344
s_nop 0
buffer_load_dwordx4 v7,s[36:39],s96 offen offset:0 lds
buffer_load_dwordx4 v7,s[40:43],s96 offen offset:1024 lds
s_add_u32 s96,s96,229376
ds_read_b64_tr_b16 a[8:9],v9 offset:0
ds_read_b64_tr_b16 a[10:11],v9 offset:512
ds_read_b64_tr_b16 a[12:13],v9 offset:4096
ds_read_b64_tr_b16 a[14:15],v9 offset:4608
ds_read_b64_tr_b16 a[40:41],v9 offset:8192
ds_read_b64_tr_b16 a[42:43],v9 offset:8704
ds_read_b64_tr_b16 a[44:45],v9 offset:12288
ds_read_b64_tr_b16 a[46:47],v9 offset:12800
s_waitcnt lgkmcnt(0)
v_mfma_f32_32x32x16_bf16 v[112:127],a[8:11],v[16:19],v[112:127]
v_mfma_f32_32x32x16_bf16 v[112:127],a[12:15],v[16:19],v[112:127]
v_mfma_f32_32x32x16_bf16 v[112:127],a[40:43],v[16:19],v[112:127]
v_mfma_f32_32x32x16_bf16 v[112:127],a[44:47],v[16:19],v[112:127]
s_waitcnt vmcnt(24)
s_barrier
s_add_u32 m0,s97,65536
s_nop 0
buffer_load_dwordx4 v7,s[36:39],s96 offen offset:0 lds
buffer_load_dwordx4 v7,s[40:43],s96 offen offset:1024 lds
s_add_u32 s96,s96,229376
s_add_u32 m0,s97,73728
s_nop 0
buffer_load_dwordx4 v7,s[36:39],s96 offen offset:0 lds
buffer_load_dwordx4 v7,s[40:43],s96 offen offset:1024 lds
s_add_u32 s96,s96,229376
ds_read_b64_tr_b16 a[72:73],v9 offset:16384
ds_read_b64_tr_b16 a[74:75],v9 offset:16896
ds_read_b64_tr_b16 a[76:77],v9 offset:20480
ds_read_b64_tr_b16 a[78:79],v9 offset:20992
ds_read_b64_tr_b16 a[104:105],v9 offset:24576
ds_read_b64_tr_b16 a[106:107],v9 offset:25088
ds_read_b64_tr_b16 a[108:109],v9 offset:28672
ds_read_b64_tr_b16 a[110:111],v9 offset:29184
s_waitcnt lgkmcnt(0)
v_mfma_f32_32x32x16_bf16 v[112:127],a[72:75],v[16:19],v[112:127]
v_mfma_f32_32x32x16_bf16 v[112:127],a[76:79],v[16:19],v[112:127]
v_mfma_f32_32x32x16_bf16 v[112:127],a[104:107],v[16:19],v[112:127]
v_mfma_f32_32x32x16_bf16 v[112:127],a[108:111],v[16:19],v[112:127]
s_waitcnt vmcnt(24)
s_barrier
s_add_u32 m0,s97,81920
s_nop 0
buffer_load_dwordx4 v7,s[36:39],s96 offen offset:0 lds
buffer_load_dwordx4 v7,s[40:43],s96 offen offset:1024 lds
s_add_u32 s96,s96,229376
s_add_u32 m0,s97,90112
s_nop 0
buffer_load_dwordx4 v7,s[36:39],s96 offen offset:0 lds
buffer_load_dwordx4 v7,s[40:43],s96 offen offset:1024 lds
s_add_u32 s96,s96,229376
ds_read_b64_tr_b16 a[136:137],v9 offset:32768
ds_read_b64_tr_b16 a[138:139],v9 offset:33280
ds_read_b64_tr_b16 a[140:141],v9 offset:36864
ds_read_b64_tr_b16 a[142:143],v9 offset:37376
ds_read_b64_tr_b16 a[168:169],v9 offset:40960
ds_read_b64_tr_b16 a[170:171],v9 offset:41472
ds_read_b64_tr_b16 a[172:173],v9 offset:45056
ds_read_b64_tr_b16 a[174:175],v9 offset:45568
s_waitcnt lgkmcnt(0)
v_mfma_f32_32x32x16_bf16 v[112:127],a[136:139],v[16:19],v[112:127]
v_mfma_f32_32x32x16_bf16 v[112:127],a[140:143],v[16:19],v[112:127]
v_mfma_f32_32x32x16_bf16 v[112:127],a[168:171],v[16:19],v[112:127]
v_mfma_f32_32x32x16_bf16 v[112:127],a[172:175],v[16:19],v[112:127]
s_waitcnt vmcnt(24)
s_barrier
s_add_u32 m0,s97,98304
s_nop 0
buffer_load_dwordx4 v7,s[36:39],s96 offen offset:0 lds
buffer_load_dwordx4 v7,s[40:43],s96 offen offset:1024 lds
s_add_u32 s96,s96,229376
s_add_u32 m0,s97,106496
s_nop 0
buffer_load_dwordx4 v7,s[36:39],s96 offen offset:0 lds
buffer_load_dwordx4 v7,s[40:43],s96 offen offset:1024 lds
s_add_u32 s96,s96,229376
ds_read_b64_tr_b16 a[200:201],v9 offset:49152
ds_read_b64_tr_b16 a[202:203],v9 offset:49664
ds_read_b64_tr_b16 a[204:205],v9 offset:53248
ds_read_b64_tr_b16 a[206:207],v9 offset:53760
ds_read_b64_tr_b16 a[232:233],v9 offset:57344
ds_read_b64_tr_b16 a[234:235],v9 offset:57856
ds_read_b64_tr_b16 a[236:237],v9 offset:61440
ds_read_b64_tr_b16 a[238:239],v9 offset:61952
s_waitcnt lgkmcnt(0)
v_mfma_f32_32x32x16_bf16 v[112:127],a[200:203],v[16:19],v[112:127]
v_mfma_f32_32x32x16_bf16 v[112:127],a[204:207],v[16:19],v[112:127]
v_mfma_f32_32x32x16_bf16 v[112:127],a[232:235],v[16:19],v[112:127]
v_mfma_f32_32x32x16_bf16 v[112:127],a[236:239],v[16:19],v[112:127]
s_waitcnt vmcnt(24)
s_barrier
s_add_u32 m0,s97,114688
s_nop 0
buffer_load_dwordx4 v7,s[36:39],s96 offen offset:0 lds
buffer_load_dwordx4 v7,s[40:43],s96 offen offset:1024 lds
s_add_u32 s96,s96,229376
s_add_u32 m0,s97,122880
s_nop 0
buffer_load_dwordx4 v7,s[36:39],s96 offen offset:0 lds
buffer_load_dwordx4 v7,s[40:43],s96 offen offset:1024 lds
s_add_u32 s96,s96,229376
ds_read_b64_tr_b16 a[16:17],v8 offset:0
ds_read_b64_tr_b16 a[18:19],v8 offset:512
ds_read_b64_tr_b16 a[20:21],v8 offset:4096
ds_read_b64_tr_b16 a[22:23],v8 offset:4608
ds_read_b64_tr_b16 a[48:49],v8 offset:8192
ds_read_b64_tr_b16 a[50:51],v8 offset:8704
ds_read_b64_tr_b16 a[52:53],v8 offset:12288
ds_read_b64_tr_b16 a[54:55],v8 offset:12800
s_waitcnt lgkmcnt(0)
v_mfma_f32_32x32x16_bf16 v[112:127],a[16:19],v[16:19],v[112:127]
v_mfma_f32_32x32x16_bf16 v[112:127],a[20:23],v[16:19],v[112:127]
v_mfma_f32_32x32x16_bf16 v[112:127],a[48:51],v[16:19],v[112:127]
v_mfma_f32_32x32x16_bf16 v[112:127],a[52:55],v[16:19],v[112:127]
s_waitcnt vmcnt(24)
s_barrier
ds_read_b64_tr_b16 a[80:81],v8 offset:16384
ds_read_b64_tr_b16 a[82:83],v8 offset:16896
ds_read_b64_tr_b16 a[84:85],v8 offset:20480
ds_read_b64_tr_b16 a[86:87],v8 offset:20992
ds_read_b64_tr_b16 a[112:113],v8 offset:24576
ds_read_b64_tr_b16 a[114:115],v8 offset:25088
ds_read_b64_tr_b16 a[116:117],v8 offset:28672
ds_read_b64_tr_b16 a[118:119],v8 offset:29184
s_waitcnt lgkmcnt(0)
v_mfma_f32_32x32x16_bf16 v[112:127],a[80:83],v[16:19],v[112:127]
v_mfma_f32_32x32x16_bf16 v[112:127],a[84:87],v[16:19],v[112:127]
v_mfma_f32_32x32x16_bf16 v[112:127],a[112:115],v[16:19],v[112:127]
v_mfma_f32_32x32x16_bf16 v[112:127],a[116:119],v[16:19],v[112:127]
s_waitcnt vmcnt(20)
s_barrier
ds_read_b64_tr_b16 a[144:145],v8 offset:32768
ds_read_b64_tr_b16 a[146:147],v8 offset:33280
ds_read_b64_tr_b16 a[148:149],v8 offset:36864
ds_read_b64_tr_b16 a[150:151],v8 offset:37376
ds_read_b64_tr_b16 a[176:177],v8 offset:40960
ds_read_b64_tr_b16 a[178:179],v8 offset:41472
ds_read_b64_tr_b16 a[180:181],v8 offset:45056
ds_read_b64_tr_b16 a[182:183],v8 offset:45568
s_waitcnt lgkmcnt(0)
v_mfma_f32_32x32x16_bf16 v[112:127],a[144:147],v[16:19],v[112:127]
v_mfma_f32_32x32x16_bf16 v[112:127],a[148:151],v[16:19],v[112:127]
v_mfma_f32_32x32x16_bf16 v[112:127],a[176:179],v[16:19],v[112:127]
v_mfma_f32_32x32x16_bf16 v[112:127],a[180:183],v[16:19],v[112:127]
s_waitcnt vmcnt(16)
s_barrier
ds_read_b64_tr_b16 a[208:209],v8 offset:49152
ds_read_b64_tr_b16 a[210:211],v8 offset:49664
ds_read_b64_tr_b16 a[212:213],v8 offset:53248
ds_read_b64_tr_b16 a[214:215],v8 offset:53760
ds_read_b64_tr_b16 a[240:241],v8 offset:57344
ds_read_b64_tr_b16 a[242:243],v8 offset:57856
ds_read_b64_tr_b16 a[244:245],v8 offset:61440
ds_read_b64_tr_b16 a[246:247],v8 offset:61952
s_waitcnt lgkmcnt(0)
v_mfma_f32_32x32x16_bf16 v[112:127],a[208:211],v[16:19],v[112:127]
v_mfma_f32_32x32x16_bf16 v[112:127],a[212:215],v[16:19],v[112:127]
v_mfma_f32_32x32x16_bf16 v[112:127],a[240:243],v[16:19],v[112:127]
v_mfma_f32_32x32x16_bf16 v[112:127],a[244:247],v[16:19],v[112:127]
s_waitcnt vmcnt(12)
s_barrier
ds_read_b64_tr_b16 a[24:25],v9 offset:0
ds_read_b64_tr_b16 a[26:27],v9 offset:512
ds_read_b64_tr_b16 a[28:29],v9 offset:4096
ds_read_b64_tr_b16 a[30:31],v9 offset:4608
ds_read_b64_tr_b16 a[56:57],v9 offset:8192
ds_read_b64_tr_b16 a[58:59],v9 offset:8704
ds_read_b64_tr_b16 a[60:61],v9 offset:12288
ds_read_b64_tr_b16 a[62:63],v9 offset:12800
s_waitcnt lgkmcnt(0)
v_mfma_f32_32x32x16_bf16 v[112:127],a[24:27],v[16:19],v[112:127]
v_mfma_f32_32x32x16_bf16 v[112:127],a[28:31],v[16:19],v[112:127]
v_mfma_f32_32x32x16_bf16 v[112:127],a[56:59],v[16:19],v[112:127]
v_mfma_f32_32x32x16_bf16 v[112:127],a[60:63],v[16:19],v[112:127]
s_waitcnt vmcnt(8)
s_barrier
ds_read_b64_tr_b16 a[88:89],v9 offset:16384
ds_read_b64_tr_b16 a[90:91],v9 offset:16896
ds_read_b64_tr_b16 a[92:93],v9 offset:20480
ds_read_b64_tr_b16 a[94:95],v9 offset:20992
ds_read_b64_tr_b16 a[120:121],v9 offset:24576
ds_read_b64_tr_b16 a[122:123],v9 offset:25088
ds_read_b64_tr_b16 a[124:125],v9 offset:28672
ds_read_b64_tr_b16 a[126:127],v9 offset:29184
s_waitcnt lgkmcnt(0)
v_mfma_f32_32x32x16_bf16 v[112:127],a[88:91],v[16:19],v[112:127]
v_mfma_f32_32x32x16_bf16 v[112:127],a[92:95],v[16:19],v[112:127]
v_mfma_f32_32x32x16_bf16 v[112:127],a[120:123],v[16:19],v[112:127]
v_mfma_f32_32x32x16_bf16 v[112:127],a[124:127],v[16:19],v[112:127]
s_waitcnt vmcnt(4)
s_barrier
ds_read_b64_tr_b16 a[152:153],v9 offset:32768
ds_read_b64_tr_b16 a[154:155],v9 offset:33280
ds_read_b64_tr_b16 a[156:157],v9 offset:36864
ds_read_b64_tr_b16 a[158:159],v9 offset:37376
ds_read_b64_tr_b16 a[184:185],v9 offset:40960
ds_read_b64_tr_b16 a[186:187],v9 offset:41472
ds_read_b64_tr_b16 a[188:189],v9 offset:45056
ds_read_b64_tr_b16 a[190:191],v9 offset:45568
s_waitcnt lgkmcnt(0)
v_mfma_f32_32x32x16_bf16 v[112:127],a[152:155],v[16:19],v[112:127]
v_mfma_f32_32x32x16_bf16 v[112:127],a[156:159],v[16:19],v[112:127]
v_mfma_f32_32x32x16_bf16 v[112:127],a[184:187],v[16:19],v[112:127]
v_mfma_f32_32x32x16_bf16 v[112:127],a[188:191],v[16:19],v[112:127]
s_waitcnt vmcnt(0)
s_barrier
ds_read_b64_tr_b16 a[216:217],v9 offset:49152
ds_read_b64_tr_b16 a[218:219],v9 offset:49664
ds_read_b64_tr_b16 a[220:221],v9 offset:53248
ds_read_b64_tr_b16 a[222:223],v9 offset:53760
ds_read_b64_tr_b16 a[248:249],v9 offset:57344
ds_read_b64_tr_b16 a[250:251],v9 offset:57856
ds_read_b64_tr_b16 a[252:253],v9 offset:61440
ds_read_b64_tr_b16 a[254:255],v9 offset:61952
s_waitcnt lgkmcnt(0)
v_mfma_f32_32x32x16_bf16 v[112:127],a[216:219],v[16:19],v[112:127]
v_mfma_f32_32x32x16_bf16 v[112:127],a[220:223],v[16:19],v[112:127]
v_mfma_f32_32x32x16_bf16 v[112:127],a[248:251],v[16:19],v[112:127]
v_mfma_f32_32x32x16_bf16 v[112:127],a[252:255],v[16:19],v[112:127]
s_barrier
s_lshl2_add_u32 s49,s27,272
s_lshl2_add_u32 s68,s27,s26
s_lshl_b32 s68,s68,9
s_add_u32 s68,s68,4829184
s_mov_b32 s63,4681728
s_waitcnt lgkmcnt(0)
s_add_u32 s54,s52,0x7f4a7c15
s_addc_u32 s55,s53,0x9e3779b9
s_lshl_b32 s67,s49,5
s_add_u32 s67,s67,4898816
s_sub_u32 s51,s67,s68
s_add_u32 s92,s32,s51
s_addc_u32 s93,s33,0x10000
s_cmp_eq_u32 s26,0
s_cselect_b32 s94,1,0
s_mov_b32 s95,0x20000
v_mov_b32 v16,s54
v_mov_b32 v17,s55
v_cmp_gt_u32 vcc,12,v1
v_mul_u32_u24 v3,296960,v1
v_mov_b32 v12,0x7fff0000
v_cndmask_b32 v3,v12,v3,vcc
s_add_u32 s51,s67,76021760
buffer_store_dwordx2 v[16:17],v3,s[32:35],s51 offen sc1
s_sub_u32 s67,s68,296960
s_sub_u32 s51,s51,76021760
s_sub_u32 s51,s51,s68
v_mul_u32_u24 v3,296960,v1
v_add_u32 v3,s51,v3
v_subrev_u32 v18,0,v0
v_cmp_gt_u32 vcc,2,v18
v_mov_b32 v18,0x7fff0000
s_nop 0
v_cndmask_b32 v18,v18,v3,vcc
s_mul_i32 s48,s28,593920
s_add_u32 s68,s68,s48
s_sub_u32 s68,s68,2375680
s_add_u32 s63,s63,s48
s_add_u32 s66,s48,4898816
s_mul_i32 s64,s26,9216
s_add_u32 s63,s63,s64
s_mov_b32 s62,31
v_and_b32 v4,0xf0,v2
v_lshrrev_b32 v3,5,v1
v_lshl_add_u32 v4,v3,8,v4
v_bfe_u32 v3,v1,4,1
v_mul_u32_u24 v15,296960,v3
v_add_u32 v15,v15,v4
v_add_u32 v15,0,v15
v_mul_u32_u24 v3,36864,v3
v_add_u32 v4,v3,v4
v_add_u32 v5,73728,v4
v_add_u32 v20,0,v2
v_add_u32 v21,296960,v2
v_add_u32 v22,4096,v2
v_add_u32 v23,301056,v2
v_add_u32 v24,5120,v2
v_add_u32 v25,302080,v2
v_and_b32 v14,7,v1
s_mul_i32 s48,s26,8
s_add_u32 s48,s48,96
v_add_u32 v14,s48,v14
v_lshrrev_b32 v14,2,v14
v_lshlrev_b32 v14,7,v14
v_lshrrev_b32 v3,4,v1
v_mul_u32_u24 v3,296960,v3
v_add_u32 v14,v3,v14
v_cmp_gt_u32 vcc,32,v1
v_mov_b32 v3,0x7fff0000
s_nop 0
v_cndmask_b32 v14,v3,v14,vcc
v_cndmask_b32 v16,0,v16,vcc
v_cndmask_b32 v17,0,v17,vcc
v_bfe_u32 v3,v1,1,3
v_mul_u32_u24 v8,1152,v3
v_lshrrev_b32 v3,1,v3
v_add_u32 v3,s26,v3
v_lshrrev_b32 v9,4,v1
v_lshl_add_u32 v3,v3,2,v9
v_and_b32 v3,15,v3
v_lshl_add_u32 v8,v3,2,v8
v_and_b32 v3,1,v1
v_lshl_add_u32 v8,v3,6,v8
v_add_u32 v8,1024,v8
s_lshl_b32 s48,s26,2
v_add_u32 v10,s48,v9
v_and_b32 v11,15,v1
v_add_u32 v3,64,v1
v_cmp_eq_u32 vcc,0,v11
v_add_u32 v9,73728,v8
v_xor_b32 v9,v9,v8
v_cndmask_b32 v10,v3,v10,vcc
v_lshlrev_b32 v10,2,v10
v_add_u32 v10,147456,v10
v_lshlrev_b32 v11,2,v11
v_add_u32 v11,147456,v11
v_and_b32 v3,15,v1
v_cmp_eq_u32 vcc,8,v3
v_lshrrev_b32 v3,4,v1
s_lshl2_add_u32 s48,s26,36880
v_add_lshl_u32 v3,v3,s48,2
v_cndmask_b32 v10,v10,v3,vcc
v_and_b32 v3,16,v1
v_lshl_add_u32 v11,v3,2,v11
s_mov_b64 s[8:9],s[62:63]
s_mov_b64 s[10:11],s[64:65]
s_mov_b64 s[12:13],s[66:67]
s_mov_b64 s[14:15],s[68:69]
s_mov_b64 s[16:17],s[70:71]
s_mov_b64 s[18:19],s[72:73]
s_mov_b64 s[20:21],s[74:75]
s_mov_b32 s62,0
s_mov_b64 vcc,0
s_mov_b64 exec,0
s_branch .L_hgo
)#"
R"#(
.L_hbk:
s_mov_b64 exec,-1
s_mov_b64 s[62:63],s[8:9]
s_mov_b64 s[64:65],s[10:11]
s_mov_b64 s[66:67],s[12:13]
s_mov_b64 s[68:69],s[14:15]
s_mov_b64 s[70:71],s[16:17]
s_mov_b64 s[72:73],s[18:19]
s_mov_b64 s[74:75],s[20:21]
s_waitcnt vmcnt(0) lgkmcnt(0)
s_mov_b32 s48,20000
s_branch .L_rfha
.L_rlha:
s_sleep 8
.L_rfha:
buffer_load_dwordx2 v[12:13],v14,s[32:35],s66 offen sc1
s_sub_u32 s48,s48,1
s_cbranch_scc1 .L_end
s_waitcnt vmcnt(0)
v_cmp_ne_u64 vcc,v[16:17],v[12:13]
s_cbranch_vccnz .L_rlha
s_add_u32 s66,s66,1187840
s_add_u32 m0,s64,0
s_nop 0
buffer_load_dwordx4 v20,s[32:35],s63 offen offset:0 sc1 lds
buffer_load_dwordx4 v20,s[32:35],s63 offen offset:1024 sc1 lds
buffer_load_dwordx4 v20,s[32:35],s63 offen offset:2048 sc1 lds
buffer_load_dwordx4 v20,s[32:35],s63 offen offset:3072 sc1 lds
s_add_u32 m0,s64,36864
s_nop 0
buffer_load_dwordx4 v21,s[32:35],s63 offen offset:0 sc1 lds
buffer_load_dwordx4 v21,s[32:35],s63 offen offset:1024 sc1 lds
buffer_load_dwordx4 v21,s[32:35],s63 offen offset:2048 sc1 lds
buffer_load_dwordx4 v21,s[32:35],s63 offen offset:3072 sc1 lds
s_add_u32 m0,s64,4096
s_nop 0
buffer_load_dwordx4 v22,s[32:35],s63 offen offset:0 sc1 lds
s_add_u32 m0,s64,40960
s_nop 0
buffer_load_dwordx4 v23,s[32:35],s63 offen offset:0 sc1 lds
s_add_u32 m0,s64,5120
s_nop 0
buffer_load_dwordx4 v24,s[32:35],s63 offen offset:0 sc1 lds
buffer_load_dwordx4 v24,s[32:35],s63 offen offset:1024 sc1 lds
buffer_load_dwordx4 v24,s[32:35],s63 offen offset:2048 sc1 lds
buffer_load_dwordx4 v24,s[32:35],s63 offen offset:3072 sc1 lds
s_add_u32 m0,s64,41984
s_nop 0
buffer_load_dwordx4 v25,s[32:35],s63 offen offset:0 sc1 lds
buffer_load_dwordx4 v25,s[32:35],s63 offen offset:1024 sc1 lds
buffer_load_dwordx4 v25,s[32:35],s63 offen offset:2048 sc1 lds
buffer_load_dwordx4 v25,s[32:35],s63 offen offset:3072 sc1 lds
s_add_u32 s63,s63,1187840
s_waitcnt vmcnt(8)
s_barrier
ds_read_b128 v[128:131],v4 offset:0
ds_read_b128 v[132:135],v4 offset:512
ds_read_b128 v[136:139],v4 offset:9216
ds_read_b128 v[140:143],v4 offset:9728
ds_read_b128 v[144:147],v4 offset:18432
ds_read_b128 v[148:151],v4 offset:18944
ds_read_b128 v[152:155],v4 offset:27648
ds_read_b128 v[156:159],v4 offset:28160
s_mov_b32 s48,20000
s_branch .L_rfhb
.L_rlhb:
s_sleep 8
.L_rfhb:
buffer_load_dwordx2 v[12:13],v14,s[32:35],s66 offen sc1
s_sub_u32 s48,s48,1
s_cbranch_scc1 .L_end
s_waitcnt vmcnt(0)
v_cmp_ne_u64 vcc,v[16:17],v[12:13]
s_cbranch_vccnz .L_rlhb
.L_hgo:
s_cmp_eq_u32 s26,1
s_cbranch_scc1 .L_hv1
s_cmp_eq_u32 s26,2
s_cbranch_scc1 .L_hv2
s_cmp_eq_u32 s26,3
s_cbranch_scc1 .L_hv3
.p2align 6
.L_hv0:
.L_hl_0:
s_waitcnt lgkmcnt(4)
v_mfma_f32_32x32x16_bf16 v[32:47],a[0:3],v[128:131],0
ds_read_b128 v[160:163],v4 offset:1152
s_waitcnt vmcnt(6)
v_cmp_ne_u64 vcc,v[16:17],v[12:13]
s_add_u32 s68,s68,1187840
v_mfma_f32_32x32x16_bf16 v[32:47],a[4:7],v[132:135],v[32:47]
ds_read_b128 v[164:167],v4 offset:1664
s_cbranch_vccnz .L_rth0_0
.L_bkh0_0:
s_add_u32 s66,s66,1187840
v_mfma_f32_32x32x16_bf16 v[32:47],a[8:11],v[136:139],v[32:47]
ds_read_b128 v[168:171],v4 offset:10368
s_add_u32 m0,s64,73728
v_fma_f32 v48,-v72,v112,v48
buffer_load_dwordx4 v20,s[32:35],s63 offen offset:0 sc1 lds
v_mfma_f32_32x32x16_bf16 v[32:47],a[12:15],v[140:143],v[32:47]
ds_read_b128 v[172:175],v4 offset:10880
v_fma_f32 v49,-v72,v113,v49
v_fma_f32 v50,-v72,v114,v50
v_fma_f32 v51,-v72,v115,v51
v_fma_f32 v52,-v72,v116,v52
s_waitcnt lgkmcnt(4)
v_mfma_f32_32x32x16_bf16 v[32:47],a[16:19],v[144:147],v[32:47]
buffer_load_dwordx4 v20,s[32:35],s63 offen offset:1024 sc1 lds
ds_read_b128 v[176:179],v4 offset:19584
v_fma_f32 v53,-v72,v117,v53
v_fma_f32 v54,-v72,v118,v54
v_mfma_f32_32x32x16_bf16 v[32:47],a[20:23],v[148:151],v[32:47]
ds_read_b128 v[180:183],v4 offset:20096
v_fma_f32 v55,-v72,v119,v55
v_fma_f32 v56,-v72,v120,v56
v_fma_f32 v57,-v72,v121,v57
v_fma_f32 v58,-v72,v122,v58
v_fma_f32 v59,-v72,v123,v59
v_mfma_f32_32x32x16_bf16 v[32:47],a[24:27],v[152:155],v[32:47]
buffer_load_dwordx4 v20,s[32:35],s63 offen offset:2048 sc1 lds
ds_read_b128 v[184:187],v4 offset:28800
v_fma_f32 v60,-v72,v124,v60
v_fma_f32 v61,-v72,v125,v61
v_mfma_f32_32x32x16_bf16 v[32:47],a[28:31],v[156:159],v[32:47]
ds_read_b128 v[188:191],v4 offset:29312
v_fma_f32 v62,-v72,v126,v62
v_fma_f32 v63,-v72,v127,v63
v_mul_f32 v76,v48,v73
v_mul_f32 v77,v49,v73
s_waitcnt lgkmcnt(4)
v_mfma_f32_32x32x16_bf16 v[32:47],a[32:35],v[160:163],v[32:47]
buffer_load_dwordx4 v20,s[32:35],s63 offen offset:3072 sc1 lds
ds_read_b128 v[128:131],v4 offset:2304
v_exp_f32 v76,v76
v_mfma_f32_32x32x16_bf16 v[32:47],a[36:39],v[164:167],v[32:47]
ds_read_b128 v[132:135],v4 offset:2816
v_exp_f32 v77,v77
v_mul_f32 v78,v50,v73
v_mul_f32 v79,v51,v73
v_mul_f32 v80,v52,v73
v_mfma_f32_32x32x16_bf16 v[32:47],a[40:43],v[168:171],v[32:47]
ds_read_b128 v[136:139],v4 offset:11520
s_add_u32 m0,s64,110592
v_exp_f32 v78,v78
v_exp_f32 v79,v79
v_mfma_f32_32x32x16_bf16 v[32:47],a[44:47],v[172:175],v[32:47]
ds_read_b128 v[140:143],v4 offset:12032
v_exp_f32 v80,v80
v_mul_f32 v81,v53,v73
v_mul_f32 v82,v54,v73
s_waitcnt lgkmcnt(4)
v_mfma_f32_32x32x16_bf16 v[32:47],a[48:51],v[176:179],v[32:47]
buffer_load_dwordx4 v21,s[32:35],s63 offen offset:0 sc1 lds
ds_read_b128 v[144:147],v4 offset:20736
v_exp_f32 v81,v81
v_mfma_f32_32x32x16_bf16 v[32:47],a[52:55],v[180:183],v[32:47]
ds_read_b128 v[148:151],v4 offset:21248
v_exp_f32 v82,v82
v_mul_f32 v83,v55,v73
v_mul_f32 v48,v48,v56
v_mul_f32 v49,v49,v57
v_mfma_f32_32x32x16_bf16 v[32:47],a[56:59],v[184:187],v[32:47]
buffer_load_dwordx4 v21,s[32:35],s63 offen offset:1024 sc1 lds
ds_read_b128 v[152:155],v4 offset:29952
v_exp_f32 v83,v83
v_mfma_f32_32x32x16_bf16 v[32:47],a[60:63],v[188:191],v[32:47]
ds_read_b128 v[156:159],v4 offset:30464
v_mul_f32 v50,v50,v58
v_mul_f32 v51,v51,v59
v_mul_f32 v52,v52,v60
v_mul_f32 v53,v53,v61
s_waitcnt lgkmcnt(4)
v_mfma_f32_32x32x16_bf16 v[32:47],a[64:67],v[128:131],v[32:47]
buffer_load_dwordx4 v21,s[32:35],s63 offen offset:2048 sc1 lds
ds_read_b128 v[160:163],v4 offset:3456
v_mul_f32 v54,v54,v62
v_mul_f32 v55,v55,v63
v_mfma_f32_32x32x16_bf16 v[32:47],a[68:71],v[132:135],v[32:47]
ds_read_b128 v[164:167],v4 offset:3968
v_fma_f32 v76,v76,v74,v74
v_fma_f32 v77,v77,v74,v74
v_rcp_f32 v76,v76
v_fma_f32 v78,v78,v74,v74
v_mfma_f32_32x32x16_bf16 v[32:47],a[72:75],v[136:139],v[32:47]
buffer_load_dwordx4 v21,s[32:35],s63 offen offset:3072 sc1 lds
ds_read_b128 v[168:171],v4 offset:12672
s_add_u32 m0,s64,77824
v_fma_f32 v79,v79,v74,v74
v_mfma_f32_32x32x16_bf16 v[32:47],a[76:79],v[140:143],v[32:47]
ds_read_b128 v[172:175],v4 offset:13184
v_rcp_f32 v77,v77
v_rcp_f32 v78,v78
s_waitcnt lgkmcnt(4)
v_mfma_f32_32x32x16_bf16 v[32:47],a[80:83],v[144:147],v[32:47]
buffer_load_dwordx4 v22,s[32:35],s63 offen offset:0 sc1 lds
ds_read_b128 v[176:179],v4 offset:21888
s_add_u32 m0,s64,114688
v_fma_f32 v80,v80,v74,v74
v_mfma_f32_32x32x16_bf16 v[32:47],a[84:87],v[148:151],v[32:47]
ds_read_b128 v[180:183],v4 offset:22400
v_rcp_f32 v79,v79
v_rcp_f32 v80,v80
v_fma_f32 v81,v81,v74,v74
v_mfma_f32_32x32x16_bf16 v[32:47],a[88:91],v[152:155],v[32:47]
buffer_load_dwordx4 v23,s[32:35],s63 offen offset:0 sc1 lds
ds_read_b128 v[184:187],v4 offset:31104
v_rcp_f32 v81,v81
v_mfma_f32_32x32x16_bf16 v[32:47],a[92:95],v[156:159],v[32:47]
ds_read_b128 v[188:191],v4 offset:31616
v_fma_f32 v82,v82,v74,v74
v_fma_f32 v83,v83,v74,v74
v_rcp_f32 v82,v82
s_waitcnt lgkmcnt(4)
v_mfma_f32_32x32x16_bf16 v[32:47],a[96:99],v[160:163],v[32:47]
s_waitcnt vmcnt(11)
s_barrier
ds_read_b128 v[128:131],v4 offset:4608
v_rcp_f32 v83,v83
v_mul_f32 v48,v48,v76
v_mfma_f32_32x32x16_bf16 v[32:47],a[100:103],v[164:167],v[32:47]
ds_read_b128 v[132:135],v4 offset:5120
v_mul_f32 v49,v49,v77
v_mul_f32 v50,v50,v78
v_mul_f32 v51,v51,v79
v_mul_f32 v52,v52,v80
v_mul_f32 v53,v53,v81
v_mfma_f32_32x32x16_bf16 v[32:47],a[104:107],v[168:171],v[32:47]
ds_read_b128 v[136:139],v4 offset:13824
s_add_u32 m0,s64,78848
v_mul_f32 v54,v54,v82
buffer_load_dwordx4 v24,s[32:35],s63 offen offset:0 sc1 lds
v_mfma_f32_32x32x16_bf16 v[32:47],a[108:111],v[172:175],v[32:47]
ds_read_b128 v[140:143],v4 offset:14336
v_mul_f32 v55,v55,v83
v_cvt_pk_bf16_f32 v84,v48,v49
v_cvt_pk_bf16_f32 v85,v50,v51
v_cvt_pk_bf16_f32 v86,v52,v53
s_waitcnt lgkmcnt(4)
v_mfma_f32_32x32x16_bf16 v[32:47],a[112:115],v[176:179],v[32:47]
ds_read_b128 v[144:147],v4 offset:23040
v_cvt_pk_bf16_f32 v87,v54,v55
ds_read2st64_b32 v[64:65],v8 offset0:0 offset1:36
ds_read2st64_b32 v[66:67],v8 offset0:72 offset1:108
ds_read2st64_b32 v[68:69],v8 offset0:144 offset1:180
ds_read2st64_b32 v[70:71],v8 offset0:216 offset1:252
v_mfma_f32_32x32x16_bf16 v[32:47],a[116:119],v[180:183],v[32:47]
ds_read_b128 v[148:151],v4 offset:23552
v_xor_b32 v8,v9,v8
v_mfma_f32_32x32x16_bf16 v[32:47],a[120:123],v[184:187],v[32:47]
buffer_store_dwordx4 v[84:87],v15,s[32:35],s68 offen sc1
ds_read_b128 v[152:155],v4 offset:32256
v_mfma_f32_32x32x16_bf16 v[32:47],a[124:127],v[188:191],v[32:47]
ds_read_b128 v[156:159],v4 offset:32768
s_waitcnt lgkmcnt(8)
v_mfma_f32_32x32x16_bf16 v[32:47],a[128:131],v[128:131],v[32:47]
buffer_load_dwordx4 v24,s[32:35],s63 offen offset:1024 sc1 lds
ds_read_b128 v[160:163],v4 offset:5760
v_mfma_f32_32x32x16_bf16 v[32:47],a[132:135],v[132:135],v[32:47]
ds_read_b128 v[164:167],v4 offset:6272
s_waitcnt lgkmcnt(5)
v_add_f32 v64,v64,v65
v_add_f32 v66,v66,v67
v_add_f32 v68,v68,v69
v_add_f32 v70,v70,v71
v_mfma_f32_32x32x16_bf16 v[32:47],a[136:139],v[136:139],v[32:47]
buffer_load_dwordx4 v24,s[32:35],s63 offen offset:2048 sc1 lds
ds_read_b128 v[168:171],v4 offset:14976
v_add_f32 v66,v64,v66
v_add_f32 v68,v68,v70
v_mfma_f32_32x32x16_bf16 v[32:47],a[140:143],v[140:143],v[32:47]
ds_read_b128 v[172:175],v4 offset:15488
v_add_f32_dpp v64,v66,v66 row_ror:8 row_mask:0xf bank_mask:0x3
s_waitcnt lgkmcnt(4)
v_mfma_f32_32x32x16_bf16 v[32:47],a[144:147],v[144:147],v[32:47]
buffer_load_dwordx2 v[12:13],v14,s[32:35],s66 offen sc1
ds_read_b128 v[176:179],v4 offset:24192
v_add_f32_dpp v64,v68,v68 row_ror:8 row_mask:0xf bank_mask:0xc
v_mfma_f32_32x32x16_bf16 v[32:47],a[148:151],v[148:151],v[32:47]
ds_read_b128 v[180:183],v4 offset:24704
v_add_f32_dpp v64,v64,v64 row_ror:12 row_mask:0xf bank_mask:0xf
v_mfma_f32_32x32x16_bf16 v[32:47],a[152:155],v[152:155],v[32:47]
buffer_load_dwordx4 v24,s[32:35],s63 offen offset:3072 sc1 lds
ds_read_b128 v[184:187],v4 offset:33408
v_add_f32_dpp v64,v64,v64 quad_perm:[2,3,0,1] row_mask:0xf bank_mask:0xf
v_mfma_f32_32x32x16_bf16 v[32:47],a[156:159],v[156:159],v[32:47]
ds_read_b128 v[188:191],v4 offset:33920
s_add_u32 m0,s64,115712
v_mul_f32 v64,0x3a800000,v64
s_waitcnt lgkmcnt(4)
v_mfma_f32_32x32x16_bf16 v[32:47],a[160:163],v[160:163],v[32:47]
buffer_load_dwordx4 v25,s[32:35],s63 offen offset:0 sc1 lds
ds_read_b128 v[128:131],v4 offset:6912
v_mul_f32 v65,v64,v64
v_mfma_f32_32x32x16_bf16 v[32:47],a[164:167],v[164:167],v[32:47]
ds_read_b128 v[132:135],v4 offset:7424
v_sub_f32_dpp v65,v64,v65 quad_perm:[1,1,3,3] row_mask:0xf bank_mask:0xf
v_mfma_f32_32x32x16_bf16 v[32:47],a[168:171],v[168:171],v[32:47]
ds_read_b128 v[136:139],v4 offset:16128
v_max_f32 v65,0,v65
v_mfma_f32_32x32x16_bf16 v[32:47],a[172:175],v[172:175],v[32:47]
ds_read_b128 v[140:143],v4 offset:16640
v_cvt_pk_bf16_f32 v65,0,v65
s_waitcnt lgkmcnt(4)
v_mfma_f32_32x32x16_bf16 v[32:47],a[176:179],v[176:179],v[32:47]
buffer_load_dwordx4 v25,s[32:35],s63 offen offset:1024 sc1 lds
ds_read_b128 v[144:147],v4 offset:25344
v_add_f32 v65,0x3727c5ac,v65
v_mfma_f32_32x32x16_bf16 v[32:47],a[180:183],v[180:183],v[32:47]
ds_read_b128 v[148:151],v4 offset:25856
v_cvt_pk_bf16_f32 v65,0,v65
v_mfma_f32_32x32x16_bf16 v[32:47],a[184:187],v[184:187],v[32:47]
buffer_load_dwordx4 v25,s[32:35],s63 offen offset:2048 sc1 lds
ds_read_b128 v[152:155],v4 offset:34560
v_sqrt_f32 v65,v65
v_mfma_f32_32x32x16_bf16 v[32:47],a[188:191],v[188:191],v[32:47]
ds_read_b128 v[156:159],v4 offset:35072
v_cvt_pk_bf16_f32 v65,v64,v65
s_waitcnt lgkmcnt(4)
v_mfma_f32_32x32x16_bf16 v[32:47],a[192:195],v[128:131],v[32:47]
ds_read_b128 v[160:163],v4 offset:8064
ds_write_b32 v10,v65 offset:0
v_mfma_f32_32x32x16_bf16 v[32:47],a[196:199],v[132:135],v[32:47]
ds_read_b128 v[164:167],v4 offset:8576
v_mfma_f32_32x32x16_bf16 v[32:47],a[200:203],v[136:139],v[32:47]
buffer_load_dwordx4 v25,s[32:35],s63 offen offset:3072 sc1 lds
ds_read_b128 v[168:171],v4 offset:17280
s_add_u32 s63,s63,1187840
v_mfma_f32_32x32x16_bf16 v[32:47],a[204:207],v[140:143],v[32:47]
ds_read_b128 v[172:175],v4 offset:17792
s_waitcnt lgkmcnt(5)
v_mfma_f32_32x32x16_bf16 v[32:47],a[208:211],v[144:147],v[32:47]
ds_read_b128 v[176:179],v4 offset:26496
v_mfma_f32_32x32x16_bf16 v[32:47],a[212:215],v[148:151],v[32:47]
ds_read_b128 v[180:183],v4 offset:27008
v_mfma_f32_32x32x16_bf16 v[32:47],a[216:219],v[152:155],v[32:47]
ds_read_b128 v[184:187],v4 offset:35712
v_mfma_f32_32x32x16_bf16 v[32:47],a[220:223],v[156:159],v[32:47]
ds_read_b128 v[188:191],v4 offset:36224
s_waitcnt lgkmcnt(4)
v_mfma_f32_32x32x16_bf16 v[32:47],a[224:227],v[160:163],v[32:47]
s_waitcnt vmcnt(8)
s_barrier
buffer_store_dwordx2 v[16:17],v18,s[32:35],s68 offen sc1
ds_read_b128 v[128:131],v5 offset:0
v_mfma_f32_32x32x16_bf16 v[32:47],a[228:231],v[164:167],v[32:47]
ds_read_b128 v[132:135],v5 offset:512
ds_read_b32 v74,v11 offset:0
v_mfma_f32_32x32x16_bf16 v[32:47],a[232:235],v[168:171],v[32:47]
ds_read_b128 v[136:139],v5 offset:9216
v_mfma_f32_32x32x16_bf16 v[32:47],a[236:239],v[172:175],v[32:47]
ds_read_b128 v[140:143],v5 offset:9728
s_waitcnt lgkmcnt(5)
v_mfma_f32_32x32x16_bf16 v[32:47],a[240:243],v[176:179],v[32:47]
ds_read_b128 v[144:147],v5 offset:18432
s_waitcnt lgkmcnt(3)
v_lshlrev_b32 v72,16,v74
v_and_b32 v74,0xffff0000,v74
v_mfma_f32_32x32x16_bf16 v[32:47],a[244:247],v[180:183],v[32:47]
ds_read_b128 v[148:151],v5 offset:18944
v_rcp_f32 v73,v74
v_mul_f32 v74,v74,v74
v_mfma_f32_32x32x16_bf16 v[32:47],a[248:251],v[184:187],v[32:47]
ds_read_b128 v[152:155],v5 offset:27648
v_mul_f32 v73,0xbfb8aa3b,v73
v_mfma_f32_32x32x16_bf16 v[32:47],a[252:255],v[188:191],v[32:47]
ds_read_b128 v[156:159],v5 offset:28160
s_waitcnt lgkmcnt(4)
v_mfma_f32_32x32x16_bf16 v[48:63],a[0:3],v[128:131],0
ds_read_b128 v[160:163],v5 offset:1152
s_waitcnt vmcnt(6)
v_cmp_ne_u64 vcc,v[16:17],v[12:13]
s_add_u32 s68,s68,1187840
v_mfma_f32_32x32x16_bf16 v[48:63],a[4:7],v[132:135],v[48:63]
ds_read_b128 v[164:167],v5 offset:1664
s_cbranch_vccnz .L_rth1_0
.L_bkh1_0:
s_add_u32 s66,s66,1187840
v_mfma_f32_32x32x16_bf16 v[48:63],a[8:11],v[136:139],v[48:63]
ds_read_b128 v[168:171],v5 offset:10368
s_add_u32 m0,s64,0
v_fma_f32 v32,-v72,v112,v32
buffer_load_dwordx4 v20,s[32:35],s63 offen offset:0 sc1 lds
v_mfma_f32_32x32x16_bf16 v[48:63],a[12:15],v[140:143],v[48:63]
ds_read_b128 v[172:175],v5 offset:10880
v_fma_f32 v33,-v72,v113,v33
v_fma_f32 v34,-v72,v114,v34
v_fma_f32 v35,-v72,v115,v35
v_fma_f32 v36,-v72,v116,v36
s_waitcnt lgkmcnt(4)
v_mfma_f32_32x32x16_bf16 v[48:63],a[16:19],v[144:147],v[48:63]
buffer_load_dwordx4 v20,s[32:35],s63 offen offset:1024 sc1 lds
ds_read_b128 v[176:179],v5 offset:19584
v_fma_f32 v37,-v72,v117,v37
v_fma_f32 v38,-v72,v118,v38
v_mfma_f32_32x32x16_bf16 v[48:63],a[20:23],v[148:151],v[48:63]
ds_read_b128 v[180:183],v5 offset:20096
v_fma_f32 v39,-v72,v119,v39
v_fma_f32 v40,-v72,v120,v40
v_fma_f32 v41,-v72,v121,v41
v_fma_f32 v42,-v72,v122,v42
v_fma_f32 v43,-v72,v123,v43
v_mfma_f32_32x32x16_bf16 v[48:63],a[24:27],v[152:155],v[48:63]
buffer_load_dwordx4 v20,s[32:35],s63 offen offset:2048 sc1 lds
ds_read_b128 v[184:187],v5 offset:28800
v_fma_f32 v44,-v72,v124,v44
v_fma_f32 v45,-v72,v125,v45
v_mfma_f32_32x32x16_bf16 v[48:63],a[28:31],v[156:159],v[48:63]
ds_read_b128 v[188:191],v5 offset:29312
v_fma_f32 v46,-v72,v126,v46
v_fma_f32 v47,-v72,v127,v47
v_mul_f32 v76,v32,v73
v_mul_f32 v77,v33,v73
s_waitcnt lgkmcnt(4)
v_mfma_f32_32x32x16_bf16 v[48:63],a[32:35],v[160:163],v[48:63]
buffer_load_dwordx4 v20,s[32:35],s63 offen offset:3072 sc1 lds
ds_read_b128 v[128:131],v5 offset:2304
v_exp_f32 v76,v76
v_mfma_f32_32x32x16_bf16 v[48:63],a[36:39],v[164:167],v[48:63]
ds_read_b128 v[132:135],v5 offset:2816
v_exp_f32 v77,v77
v_mul_f32 v78,v34,v73
v_mul_f32 v79,v35,v73
v_mul_f32 v80,v36,v73
v_mfma_f32_32x32x16_bf16 v[48:63],a[40:43],v[168:171],v[48:63]
ds_read_b128 v[136:139],v5 offset:11520
s_add_u32 m0,s64,36864
v_exp_f32 v78,v78
v_exp_f32 v79,v79
v_mfma_f32_32x32x16_bf16 v[48:63],a[44:47],v[172:175],v[48:63]
ds_read_b128 v[140:143],v5 offset:12032
v_exp_f32 v80,v80
v_mul_f32 v81,v37,v73
v_mul_f32 v82,v38,v73
s_waitcnt lgkmcnt(4)
v_mfma_f32_32x32x16_bf16 v[48:63],a[48:51],v[176:179],v[48:63]
buffer_load_dwordx4 v21,s[32:35],s63 offen offset:0 sc1 lds
ds_read_b128 v[144:147],v5 offset:20736
v_exp_f32 v81,v81
v_mfma_f32_32x32x16_bf16 v[48:63],a[52:55],v[180:183],v[48:63]
ds_read_b128 v[148:151],v5 offset:21248
v_exp_f32 v82,v82
v_mul_f32 v83,v39,v73
v_mul_f32 v32,v32,v40
v_mul_f32 v33,v33,v41
v_mfma_f32_32x32x16_bf16 v[48:63],a[56:59],v[184:187],v[48:63]
buffer_load_dwordx4 v21,s[32:35],s63 offen offset:1024 sc1 lds
ds_read_b128 v[152:155],v5 offset:29952
v_exp_f32 v83,v83
v_mfma_f32_32x32x16_bf16 v[48:63],a[60:63],v[188:191],v[48:63]
ds_read_b128 v[156:159],v5 offset:30464
v_mul_f32 v34,v34,v42
v_mul_f32 v35,v35,v43
v_mul_f32 v36,v36,v44
v_mul_f32 v37,v37,v45
s_waitcnt lgkmcnt(4)
v_mfma_f32_32x32x16_bf16 v[48:63],a[64:67],v[128:131],v[48:63]
buffer_load_dwordx4 v21,s[32:35],s63 offen offset:2048 sc1 lds
ds_read_b128 v[160:163],v5 offset:3456
v_mul_f32 v38,v38,v46
v_mul_f32 v39,v39,v47
v_mfma_f32_32x32x16_bf16 v[48:63],a[68:71],v[132:135],v[48:63]
ds_read_b128 v[164:167],v5 offset:3968
v_fma_f32 v76,v76,v74,v74
v_fma_f32 v77,v77,v74,v74
v_rcp_f32 v76,v76
v_fma_f32 v78,v78,v74,v74
v_mfma_f32_32x32x16_bf16 v[48:63],a[72:75],v[136:139],v[48:63]
buffer_load_dwordx4 v21,s[32:35],s63 offen offset:3072 sc1 lds
ds_read_b128 v[168:171],v5 offset:12672
s_add_u32 m0,s64,4096
v_fma_f32 v79,v79,v74,v74
v_mfma_f32_32x32x16_bf16 v[48:63],a[76:79],v[140:143],v[48:63]
ds_read_b128 v[172:175],v5 offset:13184
v_rcp_f32 v77,v77
v_rcp_f32 v78,v78
s_waitcnt lgkmcnt(4)
v_mfma_f32_32x32x16_bf16 v[48:63],a[80:83],v[144:147],v[48:63]
buffer_load_dwordx4 v22,s[32:35],s63 offen offset:0 sc1 lds
ds_read_b128 v[176:179],v5 offset:21888
s_add_u32 m0,s64,40960
v_fma_f32 v80,v80,v74,v74
v_mfma_f32_32x32x16_bf16 v[48:63],a[84:87],v[148:151],v[48:63]
ds_read_b128 v[180:183],v5 offset:22400
v_rcp_f32 v79,v79
v_rcp_f32 v80,v80
v_fma_f32 v81,v81,v74,v74
v_mfma_f32_32x32x16_bf16 v[48:63],a[88:91],v[152:155],v[48:63]
buffer_load_dwordx4 v23,s[32:35],s63 offen offset:0 sc1 lds
ds_read_b128 v[184:187],v5 offset:31104
v_rcp_f32 v81,v81
v_mfma_f32_32x32x16_bf16 v[48:63],a[92:95],v[156:159],v[48:63]
ds_read_b128 v[188:191],v5 offset:31616
v_fma_f32 v82,v82,v74,v74
v_fma_f32 v83,v83,v74,v74
v_rcp_f32 v82,v82
s_waitcnt lgkmcnt(4)
v_mfma_f32_32x32x16_bf16 v[48:63],a[96:99],v[160:163],v[48:63]
s_waitcnt vmcnt(11)
s_barrier
)#"
R"#(
ds_read_b128 v[128:131],v5 offset:4608
v_rcp_f32 v83,v83
v_mul_f32 v32,v32,v76
v_mfma_f32_32x32x16_bf16 v[48:63],a[100:103],v[164:167],v[48:63]
ds_read_b128 v[132:135],v5 offset:5120
v_mul_f32 v33,v33,v77
v_mul_f32 v34,v34,v78
v_mul_f32 v35,v35,v79
v_mul_f32 v36,v36,v80
v_mul_f32 v37,v37,v81
v_mfma_f32_32x32x16_bf16 v[48:63],a[104:107],v[168:171],v[48:63]
ds_read_b128 v[136:139],v5 offset:13824
s_add_u32 m0,s64,5120
v_mul_f32 v38,v38,v82
buffer_load_dwordx4 v24,s[32:35],s63 offen offset:0 sc1 lds
v_mfma_f32_32x32x16_bf16 v[48:63],a[108:111],v[172:175],v[48:63]
ds_read_b128 v[140:143],v5 offset:14336
v_mul_f32 v39,v39,v83
v_cvt_pk_bf16_f32 v84,v32,v33
v_cvt_pk_bf16_f32 v85,v34,v35
v_cvt_pk_bf16_f32 v86,v36,v37
s_waitcnt lgkmcnt(4)
v_mfma_f32_32x32x16_bf16 v[48:63],a[112:115],v[176:179],v[48:63]
ds_read_b128 v[144:147],v5 offset:23040
v_cvt_pk_bf16_f32 v87,v38,v39
ds_read2st64_b32 v[64:65],v8 offset0:0 offset1:36
ds_read2st64_b32 v[66:67],v8 offset0:72 offset1:108
ds_read2st64_b32 v[68:69],v8 offset0:144 offset1:180
ds_read2st64_b32 v[70:71],v8 offset0:216 offset1:252
v_mfma_f32_32x32x16_bf16 v[48:63],a[116:119],v[180:183],v[48:63]
ds_read_b128 v[148:151],v5 offset:23552
v_xor_b32 v8,v9,v8
v_mfma_f32_32x32x16_bf16 v[48:63],a[120:123],v[184:187],v[48:63]
buffer_store_dwordx4 v[84:87],v15,s[32:35],s68 offen sc1
ds_read_b128 v[152:155],v5 offset:32256
v_mfma_f32_32x32x16_bf16 v[48:63],a[124:127],v[188:191],v[48:63]
ds_read_b128 v[156:159],v5 offset:32768
s_waitcnt lgkmcnt(8)
v_mfma_f32_32x32x16_bf16 v[48:63],a[128:131],v[128:131],v[48:63]
buffer_load_dwordx4 v24,s[32:35],s63 offen offset:1024 sc1 lds
ds_read_b128 v[160:163],v5 offset:5760
v_mfma_f32_32x32x16_bf16 v[48:63],a[132:135],v[132:135],v[48:63]
ds_read_b128 v[164:167],v5 offset:6272
s_waitcnt lgkmcnt(5)
v_add_f32 v64,v64,v65
v_add_f32 v66,v66,v67
v_add_f32 v68,v68,v69
v_add_f32 v70,v70,v71
v_mfma_f32_32x32x16_bf16 v[48:63],a[136:139],v[136:139],v[48:63]
buffer_load_dwordx4 v24,s[32:35],s63 offen offset:2048 sc1 lds
ds_read_b128 v[168:171],v5 offset:14976
v_add_f32 v66,v64,v66
v_add_f32 v68,v68,v70
v_mfma_f32_32x32x16_bf16 v[48:63],a[140:143],v[140:143],v[48:63]
ds_read_b128 v[172:175],v5 offset:15488
v_add_f32_dpp v64,v66,v66 row_ror:8 row_mask:0xf bank_mask:0x3
s_waitcnt lgkmcnt(4)
v_mfma_f32_32x32x16_bf16 v[48:63],a[144:147],v[144:147],v[48:63]
buffer_load_dwordx2 v[12:13],v14,s[32:35],s66 offen sc1
ds_read_b128 v[176:179],v5 offset:24192
v_add_f32_dpp v64,v68,v68 row_ror:8 row_mask:0xf bank_mask:0xc
v_mfma_f32_32x32x16_bf16 v[48:63],a[148:151],v[148:151],v[48:63]
ds_read_b128 v[180:183],v5 offset:24704
v_add_f32_dpp v64,v64,v64 row_ror:12 row_mask:0xf bank_mask:0xf
v_mfma_f32_32x32x16_bf16 v[48:63],a[152:155],v[152:155],v[48:63]
buffer_load_dwordx4 v24,s[32:35],s63 offen offset:3072 sc1 lds
ds_read_b128 v[184:187],v5 offset:33408
v_add_f32_dpp v64,v64,v64 quad_perm:[2,3,0,1] row_mask:0xf bank_mask:0xf
v_mfma_f32_32x32x16_bf16 v[48:63],a[156:159],v[156:159],v[48:63]
ds_read_b128 v[188:191],v5 offset:33920
s_add_u32 m0,s64,41984
v_mul_f32 v64,0x3a800000,v64
s_waitcnt lgkmcnt(4)
v_mfma_f32_32x32x16_bf16 v[48:63],a[160:163],v[160:163],v[48:63]
buffer_load_dwordx4 v25,s[32:35],s63 offen offset:0 sc1 lds
ds_read_b128 v[128:131],v5 offset:6912
v_mul_f32 v65,v64,v64
v_mfma_f32_32x32x16_bf16 v[48:63],a[164:167],v[164:167],v[48:63]
ds_read_b128 v[132:135],v5 offset:7424
v_sub_f32_dpp v65,v64,v65 quad_perm:[1,1,3,3] row_mask:0xf bank_mask:0xf
v_mfma_f32_32x32x16_bf16 v[48:63],a[168:171],v[168:171],v[48:63]
ds_read_b128 v[136:139],v5 offset:16128
v_max_f32 v65,0,v65
v_mfma_f32_32x32x16_bf16 v[48:63],a[172:175],v[172:175],v[48:63]
ds_read_b128 v[140:143],v5 offset:16640
v_cvt_pk_bf16_f32 v65,0,v65
s_waitcnt lgkmcnt(4)
v_mfma_f32_32x32x16_bf16 v[48:63],a[176:179],v[176:179],v[48:63]
buffer_load_dwordx4 v25,s[32:35],s63 offen offset:1024 sc1 lds
ds_read_b128 v[144:147],v5 offset:25344
v_add_f32 v65,0x3727c5ac,v65
v_mfma_f32_32x32x16_bf16 v[48:63],a[180:183],v[180:183],v[48:63]
ds_read_b128 v[148:151],v5 offset:25856
v_cvt_pk_bf16_f32 v65,0,v65
v_mfma_f32_32x32x16_bf16 v[48:63],a[184:187],v[184:187],v[48:63]
buffer_load_dwordx4 v25,s[32:35],s63 offen offset:2048 sc1 lds
ds_read_b128 v[152:155],v5 offset:34560
v_sqrt_f32 v65,v65
v_mfma_f32_32x32x16_bf16 v[48:63],a[188:191],v[188:191],v[48:63]
ds_read_b128 v[156:159],v5 offset:35072
v_cvt_pk_bf16_f32 v65,v64,v65
s_waitcnt lgkmcnt(4)
v_mfma_f32_32x32x16_bf16 v[48:63],a[192:195],v[128:131],v[48:63]
ds_read_b128 v[160:163],v5 offset:8064
ds_write_b32 v10,v65 offset:128
v_mfma_f32_32x32x16_bf16 v[48:63],a[196:199],v[132:135],v[48:63]
ds_read_b128 v[164:167],v5 offset:8576
v_mfma_f32_32x32x16_bf16 v[48:63],a[200:203],v[136:139],v[48:63]
buffer_load_dwordx4 v25,s[32:35],s63 offen offset:3072 sc1 lds
ds_read_b128 v[168:171],v5 offset:17280
s_add_u32 s63,s63,1187840
v_mfma_f32_32x32x16_bf16 v[48:63],a[204:207],v[140:143],v[48:63]
ds_read_b128 v[172:175],v5 offset:17792
s_waitcnt lgkmcnt(5)
v_mfma_f32_32x32x16_bf16 v[48:63],a[208:211],v[144:147],v[48:63]
ds_read_b128 v[176:179],v5 offset:26496
v_mfma_f32_32x32x16_bf16 v[48:63],a[212:215],v[148:151],v[48:63]
ds_read_b128 v[180:183],v5 offset:27008
v_mfma_f32_32x32x16_bf16 v[48:63],a[216:219],v[152:155],v[48:63]
ds_read_b128 v[184:187],v5 offset:35712
v_mfma_f32_32x32x16_bf16 v[48:63],a[220:223],v[156:159],v[48:63]
ds_read_b128 v[188:191],v5 offset:36224
s_waitcnt lgkmcnt(4)
v_mfma_f32_32x32x16_bf16 v[48:63],a[224:227],v[160:163],v[48:63]
s_waitcnt vmcnt(8)
s_barrier
buffer_store_dwordx2 v[16:17],v18,s[32:35],s68 offen sc1
ds_read_b128 v[128:131],v4 offset:0
v_mfma_f32_32x32x16_bf16 v[48:63],a[228:231],v[164:167],v[48:63]
ds_read_b128 v[132:135],v4 offset:512
ds_read_b32 v74,v11 offset:128
v_mfma_f32_32x32x16_bf16 v[48:63],a[232:235],v[168:171],v[48:63]
ds_read_b128 v[136:139],v4 offset:9216
v_mfma_f32_32x32x16_bf16 v[48:63],a[236:239],v[172:175],v[48:63]
ds_read_b128 v[140:143],v4 offset:9728
s_waitcnt lgkmcnt(5)
v_mfma_f32_32x32x16_bf16 v[48:63],a[240:243],v[176:179],v[48:63]
ds_read_b128 v[144:147],v4 offset:18432
s_waitcnt lgkmcnt(3)
v_lshlrev_b32 v72,16,v74
v_and_b32 v74,0xffff0000,v74
v_mfma_f32_32x32x16_bf16 v[48:63],a[244:247],v[180:183],v[48:63]
ds_read_b128 v[148:151],v4 offset:18944
v_rcp_f32 v73,v74
v_mul_f32 v74,v74,v74
v_mfma_f32_32x32x16_bf16 v[48:63],a[248:251],v[184:187],v[48:63]
ds_read_b128 v[152:155],v4 offset:27648
v_mul_f32 v73,0xbfb8aa3b,v73
s_sub_u32 s62,s62,1
v_mfma_f32_32x32x16_bf16 v[48:63],a[252:255],v[188:191],v[48:63]
ds_read_b128 v[156:159],v4 offset:28160
s_cbranch_scc0 .L_hl_0
s_nop 1
s_add_u32 s68,s68,1187840
s_nop 6
v_fma_f32 v48,-v72,v112,v48
v_fma_f32 v49,-v72,v113,v49
v_fma_f32 v50,-v72,v114,v50
v_fma_f32 v51,-v72,v115,v51
v_fma_f32 v52,-v72,v116,v52
v_fma_f32 v53,-v72,v117,v53
v_fma_f32 v54,-v72,v118,v54
v_fma_f32 v55,-v72,v119,v55
v_fma_f32 v56,-v72,v120,v56
v_fma_f32 v57,-v72,v121,v57
v_fma_f32 v58,-v72,v122,v58
v_fma_f32 v59,-v72,v123,v59
v_fma_f32 v60,-v72,v124,v60
v_fma_f32 v61,-v72,v125,v61
v_fma_f32 v62,-v72,v126,v62
v_fma_f32 v63,-v72,v127,v63
v_mul_f32 v76,v48,v73
v_mul_f32 v77,v49,v73
v_mul_f32 v78,v50,v73
v_mul_f32 v79,v51,v73
v_mul_f32 v80,v52,v73
v_mul_f32 v81,v53,v73
v_mul_f32 v82,v54,v73
v_mul_f32 v83,v55,v73
v_exp_f32 v76,v76
v_exp_f32 v77,v77
v_mul_f32 v48,v48,v56
v_mul_f32 v49,v49,v57
v_exp_f32 v78,v78
v_exp_f32 v79,v79
v_mul_f32 v50,v50,v58
v_mul_f32 v51,v51,v59
v_exp_f32 v80,v80
v_exp_f32 v81,v81
v_mul_f32 v52,v52,v60
v_mul_f32 v53,v53,v61
v_exp_f32 v82,v82
v_exp_f32 v83,v83
v_mul_f32 v54,v54,v62
v_mul_f32 v55,v55,v63
v_fma_f32 v76,v76,v74,v74
v_fma_f32 v77,v77,v74,v74
v_rcp_f32 v76,v76
v_rcp_f32 v77,v77
v_fma_f32 v78,v78,v74,v74
v_fma_f32 v79,v79,v74,v74
v_rcp_f32 v78,v78
v_rcp_f32 v79,v79
v_fma_f32 v80,v80,v74,v74
v_fma_f32 v81,v81,v74,v74
v_rcp_f32 v80,v80
v_rcp_f32 v81,v81
v_fma_f32 v82,v82,v74,v74
v_fma_f32 v83,v83,v74,v74
v_rcp_f32 v82,v82
v_rcp_f32 v83,v83
v_mul_f32 v48,v48,v76
v_mul_f32 v49,v49,v77
v_mul_f32 v50,v50,v78
v_mul_f32 v51,v51,v79
v_mul_f32 v52,v52,v80
v_mul_f32 v53,v53,v81
v_mul_f32 v54,v54,v82
v_mul_f32 v55,v55,v83
v_cvt_pk_bf16_f32 v84,v48,v49
v_cvt_pk_bf16_f32 v85,v50,v51
v_cvt_pk_bf16_f32 v86,v52,v53
v_cvt_pk_bf16_f32 v87,v54,v55
buffer_store_dwordx4 v[84:87],v15,s[32:35],s68 offen sc1
s_waitcnt vmcnt(0)
s_barrier
buffer_store_dwordx2 v[16:17],v18,s[32:35],s68 offen sc1
s_cbranch_execz .L_hbk
s_branch .L_end
.p2align 6
.L_hv1:
.L_hl_1:
s_waitcnt lgkmcnt(4)
v_mfma_f32_32x32x16_bf16 v[32:47],a[0:3],v[128:131],0
ds_read_b128 v[160:163],v4 offset:1152
s_waitcnt vmcnt(5)
v_cmp_ne_u64 vcc,v[16:17],v[12:13]
s_add_u32 s68,s68,1187840
v_mfma_f32_32x32x16_bf16 v[32:47],a[4:7],v[132:135],v[32:47]
ds_read_b128 v[164:167],v4 offset:1664
s_cbranch_vccnz .L_rth0_1
.L_bkh0_1:
s_add_u32 s66,s66,1187840
v_mfma_f32_32x32x16_bf16 v[32:47],a[8:11],v[136:139],v[32:47]
ds_read_b128 v[168:171],v4 offset:10368
s_add_u32 m0,s64,73728
v_fma_f32 v48,-v72,v112,v48
buffer_load_dwordx4 v20,s[32:35],s63 offen offset:0 sc1 lds
v_mfma_f32_32x32x16_bf16 v[32:47],a[12:15],v[140:143],v[32:47]
ds_read_b128 v[172:175],v4 offset:10880
v_fma_f32 v49,-v72,v113,v49
v_fma_f32 v50,-v72,v114,v50
v_fma_f32 v51,-v72,v115,v51
v_fma_f32 v52,-v72,v116,v52
s_waitcnt lgkmcnt(4)
v_mfma_f32_32x32x16_bf16 v[32:47],a[16:19],v[144:147],v[32:47]
buffer_load_dwordx4 v20,s[32:35],s63 offen offset:1024 sc1 lds
ds_read_b128 v[176:179],v4 offset:19584
v_fma_f32 v53,-v72,v117,v53
v_fma_f32 v54,-v72,v118,v54
v_mfma_f32_32x32x16_bf16 v[32:47],a[20:23],v[148:151],v[32:47]
ds_read_b128 v[180:183],v4 offset:20096
v_fma_f32 v55,-v72,v119,v55
v_fma_f32 v56,-v72,v120,v56
v_fma_f32 v57,-v72,v121,v57
v_fma_f32 v58,-v72,v122,v58
v_fma_f32 v59,-v72,v123,v59
v_mfma_f32_32x32x16_bf16 v[32:47],a[24:27],v[152:155],v[32:47]
buffer_load_dwordx4 v20,s[32:35],s63 offen offset:2048 sc1 lds
ds_read_b128 v[184:187],v4 offset:28800
v_fma_f32 v60,-v72,v124,v60
v_fma_f32 v61,-v72,v125,v61
v_mfma_f32_32x32x16_bf16 v[32:47],a[28:31],v[156:159],v[32:47]
ds_read_b128 v[188:191],v4 offset:29312
v_fma_f32 v62,-v72,v126,v62
v_fma_f32 v63,-v72,v127,v63
v_mul_f32 v76,v48,v73
v_mul_f32 v77,v49,v73
s_waitcnt lgkmcnt(4)
v_mfma_f32_32x32x16_bf16 v[32:47],a[32:35],v[160:163],v[32:47]
buffer_load_dwordx4 v20,s[32:35],s63 offen offset:3072 sc1 lds
ds_read_b128 v[128:131],v4 offset:2304
v_exp_f32 v76,v76
v_mfma_f32_32x32x16_bf16 v[32:47],a[36:39],v[164:167],v[32:47]
ds_read_b128 v[132:135],v4 offset:2816
v_exp_f32 v77,v77
v_mul_f32 v78,v50,v73
v_mul_f32 v79,v51,v73
v_mul_f32 v80,v52,v73
v_mfma_f32_32x32x16_bf16 v[32:47],a[40:43],v[168:171],v[32:47]
ds_read_b128 v[136:139],v4 offset:11520
s_add_u32 m0,s64,110592
v_exp_f32 v78,v78
v_exp_f32 v79,v79
v_mfma_f32_32x32x16_bf16 v[32:47],a[44:47],v[172:175],v[32:47]
ds_read_b128 v[140:143],v4 offset:12032
v_exp_f32 v80,v80
v_mul_f32 v81,v53,v73
v_mul_f32 v82,v54,v73
s_waitcnt lgkmcnt(4)
v_mfma_f32_32x32x16_bf16 v[32:47],a[48:51],v[176:179],v[32:47]
buffer_load_dwordx4 v21,s[32:35],s63 offen offset:0 sc1 lds
ds_read_b128 v[144:147],v4 offset:20736
v_exp_f32 v81,v81
v_mfma_f32_32x32x16_bf16 v[32:47],a[52:55],v[180:183],v[32:47]
ds_read_b128 v[148:151],v4 offset:21248
v_exp_f32 v82,v82
v_mul_f32 v83,v55,v73
v_mul_f32 v48,v48,v56
v_mul_f32 v49,v49,v57
v_mfma_f32_32x32x16_bf16 v[32:47],a[56:59],v[184:187],v[32:47]
buffer_load_dwordx4 v21,s[32:35],s63 offen offset:1024 sc1 lds
ds_read_b128 v[152:155],v4 offset:29952
v_exp_f32 v83,v83
v_mfma_f32_32x32x16_bf16 v[32:47],a[60:63],v[188:191],v[32:47]
ds_read_b128 v[156:159],v4 offset:30464
v_mul_f32 v50,v50,v58
v_mul_f32 v51,v51,v59
v_mul_f32 v52,v52,v60
v_mul_f32 v53,v53,v61
s_waitcnt lgkmcnt(4)
v_mfma_f32_32x32x16_bf16 v[32:47],a[64:67],v[128:131],v[32:47]
buffer_load_dwordx4 v21,s[32:35],s63 offen offset:2048 sc1 lds
ds_read_b128 v[160:163],v4 offset:3456
v_mul_f32 v54,v54,v62
v_mul_f32 v55,v55,v63
v_mfma_f32_32x32x16_bf16 v[32:47],a[68:71],v[132:135],v[32:47]
ds_read_b128 v[164:167],v4 offset:3968
v_fma_f32 v76,v76,v74,v74
v_fma_f32 v77,v77,v74,v74
v_rcp_f32 v76,v76
v_fma_f32 v78,v78,v74,v74
v_mfma_f32_32x32x16_bf16 v[32:47],a[72:75],v[136:139],v[32:47]
buffer_load_dwordx4 v21,s[32:35],s63 offen offset:3072 sc1 lds
ds_read_b128 v[168:171],v4 offset:12672
s_add_u32 m0,s64,77824
v_fma_f32 v79,v79,v74,v74
v_mfma_f32_32x32x16_bf16 v[32:47],a[76:79],v[140:143],v[32:47]
ds_read_b128 v[172:175],v4 offset:13184
v_rcp_f32 v77,v77
v_rcp_f32 v78,v78
s_waitcnt lgkmcnt(4)
v_mfma_f32_32x32x16_bf16 v[32:47],a[80:83],v[144:147],v[32:47]
buffer_load_dwordx4 v22,s[32:35],s63 offen offset:0 sc1 lds
ds_read_b128 v[176:179],v4 offset:21888
s_add_u32 m0,s64,114688
v_fma_f32 v80,v80,v74,v74
v_mfma_f32_32x32x16_bf16 v[32:47],a[84:87],v[148:151],v[32:47]
ds_read_b128 v[180:183],v4 offset:22400
v_rcp_f32 v79,v79
v_rcp_f32 v80,v80
v_fma_f32 v81,v81,v74,v74
v_mfma_f32_32x32x16_bf16 v[32:47],a[88:91],v[152:155],v[32:47]
buffer_load_dwordx4 v23,s[32:35],s63 offen offset:0 sc1 lds
ds_read_b128 v[184:187],v4 offset:31104
v_rcp_f32 v81,v81
v_mfma_f32_32x32x16_bf16 v[32:47],a[92:95],v[156:159],v[32:47]
ds_read_b128 v[188:191],v4 offset:31616
v_fma_f32 v82,v82,v74,v74
v_fma_f32 v83,v83,v74,v74
v_rcp_f32 v82,v82
s_waitcnt lgkmcnt(4)
s_waitcnt vmcnt(10)
s_barrier
v_mfma_f32_32x32x16_bf16 v[32:47],a[96:99],v[160:163],v[32:47]
ds_read_b128 v[128:131],v4 offset:4608
v_rcp_f32 v83,v83
v_mul_f32 v48,v48,v76
v_mfma_f32_32x32x16_bf16 v[32:47],a[100:103],v[164:167],v[32:47]
ds_read_b128 v[132:135],v4 offset:5120
v_mul_f32 v49,v49,v77
v_mul_f32 v50,v50,v78
v_mul_f32 v51,v51,v79
v_mul_f32 v52,v52,v80
v_mul_f32 v53,v53,v81
v_mfma_f32_32x32x16_bf16 v[32:47],a[104:107],v[168:171],v[32:47]
ds_read_b128 v[136:139],v4 offset:13824
s_add_u32 m0,s64,78848
v_mul_f32 v54,v54,v82
buffer_load_dwordx4 v24,s[32:35],s63 offen offset:0 sc1 lds
v_mfma_f32_32x32x16_bf16 v[32:47],a[108:111],v[172:175],v[32:47]
ds_read_b128 v[140:143],v4 offset:14336
v_mul_f32 v55,v55,v83
v_cvt_pk_bf16_f32 v84,v48,v49
v_cvt_pk_bf16_f32 v85,v50,v51
v_cvt_pk_bf16_f32 v86,v52,v53
s_waitcnt lgkmcnt(4)
v_mfma_f32_32x32x16_bf16 v[32:47],a[112:115],v[176:179],v[32:47]
ds_read_b128 v[144:147],v4 offset:23040
v_cvt_pk_bf16_f32 v87,v54,v55
ds_read2st64_b32 v[64:65],v8 offset0:0 offset1:36
ds_read2st64_b32 v[66:67],v8 offset0:72 offset1:108
ds_read2st64_b32 v[68:69],v8 offset0:144 offset1:180
ds_read2st64_b32 v[70:71],v8 offset0:216 offset1:252
v_mfma_f32_32x32x16_bf16 v[32:47],a[116:119],v[180:183],v[32:47]
ds_read_b128 v[148:151],v4 offset:23552
v_xor_b32 v8,v9,v8
v_mfma_f32_32x32x16_bf16 v[32:47],a[120:123],v[184:187],v[32:47]
buffer_store_dwordx4 v[84:87],v15,s[32:35],s68 offen sc1
ds_read_b128 v[152:155],v4 offset:32256
v_mfma_f32_32x32x16_bf16 v[32:47],a[124:127],v[188:191],v[32:47]
ds_read_b128 v[156:159],v4 offset:32768
s_waitcnt lgkmcnt(8)
v_mfma_f32_32x32x16_bf16 v[32:47],a[128:131],v[128:131],v[32:47]
buffer_load_dwordx4 v24,s[32:35],s63 offen offset:1024 sc1 lds
ds_read_b128 v[160:163],v4 offset:5760
v_mfma_f32_32x32x16_bf16 v[32:47],a[132:135],v[132:135],v[32:47]
ds_read_b128 v[164:167],v4 offset:6272
s_waitcnt lgkmcnt(5)
v_add_f32 v64,v64,v65
v_add_f32 v66,v66,v67
v_add_f32 v68,v68,v69
v_add_f32 v70,v70,v71
v_mfma_f32_32x32x16_bf16 v[32:47],a[136:139],v[136:139],v[32:47]
buffer_load_dwordx4 v24,s[32:35],s63 offen offset:2048 sc1 lds
ds_read_b128 v[168:171],v4 offset:14976
v_add_f32 v66,v64,v66
v_add_f32 v68,v68,v70
v_mfma_f32_32x32x16_bf16 v[32:47],a[140:143],v[140:143],v[32:47]
ds_read_b128 v[172:175],v4 offset:15488
v_add_f32_dpp v64,v66,v66 row_ror:8 row_mask:0xf bank_mask:0x3
s_waitcnt lgkmcnt(4)
v_mfma_f32_32x32x16_bf16 v[32:47],a[144:147],v[144:147],v[32:47]
buffer_load_dwordx2 v[12:13],v14,s[32:35],s66 offen sc1
ds_read_b128 v[176:179],v4 offset:24192
v_add_f32_dpp v64,v68,v68 row_ror:8 row_mask:0xf bank_mask:0xc
v_mfma_f32_32x32x16_bf16 v[32:47],a[148:151],v[148:151],v[32:47]
ds_read_b128 v[180:183],v4 offset:24704
v_add_f32_dpp v64,v64,v64 row_ror:12 row_mask:0xf bank_mask:0xf
v_mfma_f32_32x32x16_bf16 v[32:47],a[152:155],v[152:155],v[32:47]
buffer_load_dwordx4 v24,s[32:35],s63 offen offset:3072 sc1 lds
ds_read_b128 v[184:187],v4 offset:33408
v_add_f32_dpp v64,v64,v64 quad_perm:[2,3,0,1] row_mask:0xf bank_mask:0xf
v_mfma_f32_32x32x16_bf16 v[32:47],a[156:159],v[156:159],v[32:47]
ds_read_b128 v[188:191],v4 offset:33920
s_add_u32 m0,s64,115712
v_mul_f32 v64,0x3a800000,v64
s_waitcnt lgkmcnt(4)
v_mfma_f32_32x32x16_bf16 v[32:47],a[160:163],v[160:163],v[32:47]
buffer_load_dwordx4 v25,s[32:35],s63 offen offset:0 sc1 lds
ds_read_b128 v[128:131],v4 offset:6912
v_mul_f32 v65,v64,v64
v_mfma_f32_32x32x16_bf16 v[32:47],a[164:167],v[164:167],v[32:47]
ds_read_b128 v[132:135],v4 offset:7424
v_sub_f32_dpp v65,v64,v65 quad_perm:[1,1,3,3] row_mask:0xf bank_mask:0xf
v_mfma_f32_32x32x16_bf16 v[32:47],a[168:171],v[168:171],v[32:47]
ds_read_b128 v[136:139],v4 offset:16128
v_max_f32 v65,0,v65
v_mfma_f32_32x32x16_bf16 v[32:47],a[172:175],v[172:175],v[32:47]
ds_read_b128 v[140:143],v4 offset:16640
v_cvt_pk_bf16_f32 v65,0,v65
s_waitcnt lgkmcnt(4)
v_mfma_f32_32x32x16_bf16 v[32:47],a[176:179],v[176:179],v[32:47]
buffer_load_dwordx4 v25,s[32:35],s63 offen offset:1024 sc1 lds
ds_read_b128 v[144:147],v4 offset:25344
v_add_f32 v65,0x3727c5ac,v65
v_mfma_f32_32x32x16_bf16 v[32:47],a[180:183],v[180:183],v[32:47]
ds_read_b128 v[148:151],v4 offset:25856
v_cvt_pk_bf16_f32 v65,0,v65
v_mfma_f32_32x32x16_bf16 v[32:47],a[184:187],v[184:187],v[32:47]
buffer_load_dwordx4 v25,s[32:35],s63 offen offset:2048 sc1 lds
ds_read_b128 v[152:155],v4 offset:34560
v_sqrt_f32 v65,v65
v_mfma_f32_32x32x16_bf16 v[32:47],a[188:191],v[188:191],v[32:47]
ds_read_b128 v[156:159],v4 offset:35072
v_cvt_pk_bf16_f32 v65,v64,v65
s_waitcnt lgkmcnt(4)
v_mfma_f32_32x32x16_bf16 v[32:47],a[192:195],v[128:131],v[32:47]
ds_read_b128 v[160:163],v4 offset:8064
ds_write_b32 v10,v65 offset:0
v_mfma_f32_32x32x16_bf16 v[32:47],a[196:199],v[132:135],v[32:47]
ds_read_b128 v[164:167],v4 offset:8576
v_mfma_f32_32x32x16_bf16 v[32:47],a[200:203],v[136:139],v[32:47]
buffer_load_dwordx4 v25,s[32:35],s63 offen offset:3072 sc1 lds
ds_read_b128 v[168:171],v4 offset:17280
s_add_u32 s63,s63,1187840
v_mfma_f32_32x32x16_bf16 v[32:47],a[204:207],v[140:143],v[32:47]
ds_read_b128 v[172:175],v4 offset:17792
s_waitcnt lgkmcnt(5)
v_mfma_f32_32x32x16_bf16 v[32:47],a[208:211],v[144:147],v[32:47]
ds_read_b128 v[176:179],v4 offset:26496
v_mfma_f32_32x32x16_bf16 v[32:47],a[212:215],v[148:151],v[32:47]
ds_read_b128 v[180:183],v4 offset:27008
v_mfma_f32_32x32x16_bf16 v[32:47],a[216:219],v[152:155],v[32:47]
ds_read_b128 v[184:187],v4 offset:35712
v_mfma_f32_32x32x16_bf16 v[32:47],a[220:223],v[156:159],v[32:47]
ds_read_b128 v[188:191],v4 offset:36224
s_waitcnt lgkmcnt(4)
s_waitcnt vmcnt(8)
s_barrier
v_mfma_f32_32x32x16_bf16 v[32:47],a[224:227],v[160:163],v[32:47]
ds_read_b128 v[128:131],v5 offset:0
v_mfma_f32_32x32x16_bf16 v[32:47],a[228:231],v[164:167],v[32:47]
ds_read_b128 v[132:135],v5 offset:512
ds_read_b32 v74,v11 offset:0
v_mfma_f32_32x32x16_bf16 v[32:47],a[232:235],v[168:171],v[32:47]
ds_read_b128 v[136:139],v5 offset:9216
v_mfma_f32_32x32x16_bf16 v[32:47],a[236:239],v[172:175],v[32:47]
ds_read_b128 v[140:143],v5 offset:9728
s_waitcnt lgkmcnt(5)
)#"
R"#(
v_mfma_f32_32x32x16_bf16 v[32:47],a[240:243],v[176:179],v[32:47]
ds_read_b128 v[144:147],v5 offset:18432
s_waitcnt lgkmcnt(3)
v_lshlrev_b32 v72,16,v74
v_and_b32 v74,0xffff0000,v74
v_mfma_f32_32x32x16_bf16 v[32:47],a[244:247],v[180:183],v[32:47]
ds_read_b128 v[148:151],v5 offset:18944
v_rcp_f32 v73,v74
v_mul_f32 v74,v74,v74
v_mfma_f32_32x32x16_bf16 v[32:47],a[248:251],v[184:187],v[32:47]
ds_read_b128 v[152:155],v5 offset:27648
v_mul_f32 v73,0xbfb8aa3b,v73
v_mfma_f32_32x32x16_bf16 v[32:47],a[252:255],v[188:191],v[32:47]
ds_read_b128 v[156:159],v5 offset:28160
s_waitcnt lgkmcnt(4)
v_mfma_f32_32x32x16_bf16 v[48:63],a[0:3],v[128:131],0
ds_read_b128 v[160:163],v5 offset:1152
s_waitcnt vmcnt(5)
v_cmp_ne_u64 vcc,v[16:17],v[12:13]
s_add_u32 s68,s68,1187840
v_mfma_f32_32x32x16_bf16 v[48:63],a[4:7],v[132:135],v[48:63]
ds_read_b128 v[164:167],v5 offset:1664
s_cbranch_vccnz .L_rth1_1
.L_bkh1_1:
s_add_u32 s66,s66,1187840
v_mfma_f32_32x32x16_bf16 v[48:63],a[8:11],v[136:139],v[48:63]
ds_read_b128 v[168:171],v5 offset:10368
s_add_u32 m0,s64,0
v_fma_f32 v32,-v72,v112,v32
buffer_load_dwordx4 v20,s[32:35],s63 offen offset:0 sc1 lds
v_mfma_f32_32x32x16_bf16 v[48:63],a[12:15],v[140:143],v[48:63]
ds_read_b128 v[172:175],v5 offset:10880
v_fma_f32 v33,-v72,v113,v33
v_fma_f32 v34,-v72,v114,v34
v_fma_f32 v35,-v72,v115,v35
v_fma_f32 v36,-v72,v116,v36
s_waitcnt lgkmcnt(4)
v_mfma_f32_32x32x16_bf16 v[48:63],a[16:19],v[144:147],v[48:63]
buffer_load_dwordx4 v20,s[32:35],s63 offen offset:1024 sc1 lds
ds_read_b128 v[176:179],v5 offset:19584
v_fma_f32 v37,-v72,v117,v37
v_fma_f32 v38,-v72,v118,v38
v_mfma_f32_32x32x16_bf16 v[48:63],a[20:23],v[148:151],v[48:63]
ds_read_b128 v[180:183],v5 offset:20096
v_fma_f32 v39,-v72,v119,v39
v_fma_f32 v40,-v72,v120,v40
v_fma_f32 v41,-v72,v121,v41
v_fma_f32 v42,-v72,v122,v42
v_fma_f32 v43,-v72,v123,v43
v_mfma_f32_32x32x16_bf16 v[48:63],a[24:27],v[152:155],v[48:63]
buffer_load_dwordx4 v20,s[32:35],s63 offen offset:2048 sc1 lds
ds_read_b128 v[184:187],v5 offset:28800
v_fma_f32 v44,-v72,v124,v44
v_fma_f32 v45,-v72,v125,v45
v_mfma_f32_32x32x16_bf16 v[48:63],a[28:31],v[156:159],v[48:63]
ds_read_b128 v[188:191],v5 offset:29312
v_fma_f32 v46,-v72,v126,v46
v_fma_f32 v47,-v72,v127,v47
v_mul_f32 v76,v32,v73
v_mul_f32 v77,v33,v73
s_waitcnt lgkmcnt(4)
v_mfma_f32_32x32x16_bf16 v[48:63],a[32:35],v[160:163],v[48:63]
buffer_load_dwordx4 v20,s[32:35],s63 offen offset:3072 sc1 lds
ds_read_b128 v[128:131],v5 offset:2304
v_exp_f32 v76,v76
v_mfma_f32_32x32x16_bf16 v[48:63],a[36:39],v[164:167],v[48:63]
ds_read_b128 v[132:135],v5 offset:2816
v_exp_f32 v77,v77
v_mul_f32 v78,v34,v73
v_mul_f32 v79,v35,v73
v_mul_f32 v80,v36,v73
v_mfma_f32_32x32x16_bf16 v[48:63],a[40:43],v[168:171],v[48:63]
ds_read_b128 v[136:139],v5 offset:11520
s_add_u32 m0,s64,36864
v_exp_f32 v78,v78
v_exp_f32 v79,v79
v_mfma_f32_32x32x16_bf16 v[48:63],a[44:47],v[172:175],v[48:63]
ds_read_b128 v[140:143],v5 offset:12032
v_exp_f32 v80,v80
v_mul_f32 v81,v37,v73
v_mul_f32 v82,v38,v73
s_waitcnt lgkmcnt(4)
v_mfma_f32_32x32x16_bf16 v[48:63],a[48:51],v[176:179],v[48:63]
buffer_load_dwordx4 v21,s[32:35],s63 offen offset:0 sc1 lds
ds_read_b128 v[144:147],v5 offset:20736
v_exp_f32 v81,v81
v_mfma_f32_32x32x16_bf16 v[48:63],a[52:55],v[180:183],v[48:63]
ds_read_b128 v[148:151],v5 offset:21248
v_exp_f32 v82,v82
v_mul_f32 v83,v39,v73
v_mul_f32 v32,v32,v40
v_mul_f32 v33,v33,v41
v_mfma_f32_32x32x16_bf16 v[48:63],a[56:59],v[184:187],v[48:63]
buffer_load_dwordx4 v21,s[32:35],s63 offen offset:1024 sc1 lds
ds_read_b128 v[152:155],v5 offset:29952
v_exp_f32 v83,v83
v_mfma_f32_32x32x16_bf16 v[48:63],a[60:63],v[188:191],v[48:63]
ds_read_b128 v[156:159],v5 offset:30464
v_mul_f32 v34,v34,v42
v_mul_f32 v35,v35,v43
v_mul_f32 v36,v36,v44
v_mul_f32 v37,v37,v45
s_waitcnt lgkmcnt(4)
v_mfma_f32_32x32x16_bf16 v[48:63],a[64:67],v[128:131],v[48:63]
buffer_load_dwordx4 v21,s[32:35],s63 offen offset:2048 sc1 lds
ds_read_b128 v[160:163],v5 offset:3456
v_mul_f32 v38,v38,v46
v_mul_f32 v39,v39,v47
v_mfma_f32_32x32x16_bf16 v[48:63],a[68:71],v[132:135],v[48:63]
ds_read_b128 v[164:167],v5 offset:3968
v_fma_f32 v76,v76,v74,v74
v_fma_f32 v77,v77,v74,v74
v_rcp_f32 v76,v76
v_fma_f32 v78,v78,v74,v74
v_mfma_f32_32x32x16_bf16 v[48:63],a[72:75],v[136:139],v[48:63]
buffer_load_dwordx4 v21,s[32:35],s63 offen offset:3072 sc1 lds
ds_read_b128 v[168:171],v5 offset:12672
s_add_u32 m0,s64,4096
v_fma_f32 v79,v79,v74,v74
v_mfma_f32_32x32x16_bf16 v[48:63],a[76:79],v[140:143],v[48:63]
ds_read_b128 v[172:175],v5 offset:13184
v_rcp_f32 v77,v77
v_rcp_f32 v78,v78
s_waitcnt lgkmcnt(4)
v_mfma_f32_32x32x16_bf16 v[48:63],a[80:83],v[144:147],v[48:63]
buffer_load_dwordx4 v22,s[32:35],s63 offen offset:0 sc1 lds
ds_read_b128 v[176:179],v5 offset:21888
s_add_u32 m0,s64,40960
v_fma_f32 v80,v80,v74,v74
v_mfma_f32_32x32x16_bf16 v[48:63],a[84:87],v[148:151],v[48:63]
ds_read_b128 v[180:183],v5 offset:22400
v_rcp_f32 v79,v79
v_rcp_f32 v80,v80
v_fma_f32 v81,v81,v74,v74
v_mfma_f32_32x32x16_bf16 v[48:63],a[88:91],v[152:155],v[48:63]
buffer_load_dwordx4 v23,s[32:35],s63 offen offset:0 sc1 lds
ds_read_b128 v[184:187],v5 offset:31104
v_rcp_f32 v81,v81
v_mfma_f32_32x32x16_bf16 v[48:63],a[92:95],v[156:159],v[48:63]
ds_read_b128 v[188:191],v5 offset:31616
v_fma_f32 v82,v82,v74,v74
v_fma_f32 v83,v83,v74,v74
v_rcp_f32 v82,v82
s_waitcnt lgkmcnt(4)
s_waitcnt vmcnt(10)
s_barrier
v_mfma_f32_32x32x16_bf16 v[48:63],a[96:99],v[160:163],v[48:63]
ds_read_b128 v[128:131],v5 offset:4608
v_rcp_f32 v83,v83
v_mul_f32 v32,v32,v76
v_mfma_f32_32x32x16_bf16 v[48:63],a[100:103],v[164:167],v[48:63]
ds_read_b128 v[132:135],v5 offset:5120
v_mul_f32 v33,v33,v77
v_mul_f32 v34,v34,v78
v_mul_f32 v35,v35,v79
v_mul_f32 v36,v36,v80
v_mul_f32 v37,v37,v81
v_mfma_f32_32x32x16_bf16 v[48:63],a[104:107],v[168:171],v[48:63]
ds_read_b128 v[136:139],v5 offset:13824
s_add_u32 m0,s64,5120
v_mul_f32 v38,v38,v82
buffer_load_dwordx4 v24,s[32:35],s63 offen offset:0 sc1 lds
v_mfma_f32_32x32x16_bf16 v[48:63],a[108:111],v[172:175],v[48:63]
ds_read_b128 v[140:143],v5 offset:14336
v_mul_f32 v39,v39,v83
v_cvt_pk_bf16_f32 v84,v32,v33
v_cvt_pk_bf16_f32 v85,v34,v35
v_cvt_pk_bf16_f32 v86,v36,v37
s_waitcnt lgkmcnt(4)
v_mfma_f32_32x32x16_bf16 v[48:63],a[112:115],v[176:179],v[48:63]
ds_read_b128 v[144:147],v5 offset:23040
v_cvt_pk_bf16_f32 v87,v38,v39
ds_read2st64_b32 v[64:65],v8 offset0:0 offset1:36
ds_read2st64_b32 v[66:67],v8 offset0:72 offset1:108
ds_read2st64_b32 v[68:69],v8 offset0:144 offset1:180
ds_read2st64_b32 v[70:71],v8 offset0:216 offset1:252
v_mfma_f32_32x32x16_bf16 v[48:63],a[116:119],v[180:183],v[48:63]
ds_read_b128 v[148:151],v5 offset:23552
v_xor_b32 v8,v9,v8
v_mfma_f32_32x32x16_bf16 v[48:63],a[120:123],v[184:187],v[48:63]
buffer_store_dwordx4 v[84:87],v15,s[32:35],s68 offen sc1
ds_read_b128 v[152:155],v5 offset:32256
v_mfma_f32_32x32x16_bf16 v[48:63],a[124:127],v[188:191],v[48:63]
ds_read_b128 v[156:159],v5 offset:32768
s_waitcnt lgkmcnt(8)
v_mfma_f32_32x32x16_bf16 v[48:63],a[128:131],v[128:131],v[48:63]
buffer_load_dwordx4 v24,s[32:35],s63 offen offset:1024 sc1 lds
ds_read_b128 v[160:163],v5 offset:5760
v_mfma_f32_32x32x16_bf16 v[48:63],a[132:135],v[132:135],v[48:63]
ds_read_b128 v[164:167],v5 offset:6272
s_waitcnt lgkmcnt(5)
v_add_f32 v64,v64,v65
v_add_f32 v66,v66,v67
v_add_f32 v68,v68,v69
v_add_f32 v70,v70,v71
v_mfma_f32_32x32x16_bf16 v[48:63],a[136:139],v[136:139],v[48:63]
buffer_load_dwordx4 v24,s[32:35],s63 offen offset:2048 sc1 lds
ds_read_b128 v[168:171],v5 offset:14976
v_add_f32 v66,v64,v66
v_add_f32 v68,v68,v70
v_mfma_f32_32x32x16_bf16 v[48:63],a[140:143],v[140:143],v[48:63]
ds_read_b128 v[172:175],v5 offset:15488
v_add_f32_dpp v64,v66,v66 row_ror:8 row_mask:0xf bank_mask:0x3
s_waitcnt lgkmcnt(4)
v_mfma_f32_32x32x16_bf16 v[48:63],a[144:147],v[144:147],v[48:63]
buffer_load_dwordx2 v[12:13],v14,s[32:35],s66 offen sc1
ds_read_b128 v[176:179],v5 offset:24192
v_add_f32_dpp v64,v68,v68 row_ror:8 row_mask:0xf bank_mask:0xc
v_mfma_f32_32x32x16_bf16 v[48:63],a[148:151],v[148:151],v[48:63]
ds_read_b128 v[180:183],v5 offset:24704
v_add_f32_dpp v64,v64,v64 row_ror:12 row_mask:0xf bank_mask:0xf
v_mfma_f32_32x32x16_bf16 v[48:63],a[152:155],v[152:155],v[48:63]
buffer_load_dwordx4 v24,s[32:35],s63 offen offset:3072 sc1 lds
ds_read_b128 v[184:187],v5 offset:33408
v_add_f32_dpp v64,v64,v64 quad_perm:[2,3,0,1] row_mask:0xf bank_mask:0xf
v_mfma_f32_32x32x16_bf16 v[48:63],a[156:159],v[156:159],v[48:63]
ds_read_b128 v[188:191],v5 offset:33920
s_add_u32 m0,s64,41984
v_mul_f32 v64,0x3a800000,v64
s_waitcnt lgkmcnt(4)
v_mfma_f32_32x32x16_bf16 v[48:63],a[160:163],v[160:163],v[48:63]
buffer_load_dwordx4 v25,s[32:35],s63 offen offset:0 sc1 lds
ds_read_b128 v[128:131],v5 offset:6912
v_mul_f32 v65,v64,v64
v_mfma_f32_32x32x16_bf16 v[48:63],a[164:167],v[164:167],v[48:63]
ds_read_b128 v[132:135],v5 offset:7424
v_sub_f32_dpp v65,v64,v65 quad_perm:[1,1,3,3] row_mask:0xf bank_mask:0xf
v_mfma_f32_32x32x16_bf16 v[48:63],a[168:171],v[168:171],v[48:63]
ds_read_b128 v[136:139],v5 offset:16128
v_max_f32 v65,0,v65
v_mfma_f32_32x32x16_bf16 v[48:63],a[172:175],v[172:175],v[48:63]
ds_read_b128 v[140:143],v5 offset:16640
v_cvt_pk_bf16_f32 v65,0,v65
s_waitcnt lgkmcnt(4)
v_mfma_f32_32x32x16_bf16 v[48:63],a[176:179],v[176:179],v[48:63]
buffer_load_dwordx4 v25,s[32:35],s63 offen offset:1024 sc1 lds
ds_read_b128 v[144:147],v5 offset:25344
v_add_f32 v65,0x3727c5ac,v65
v_mfma_f32_32x32x16_bf16 v[48:63],a[180:183],v[180:183],v[48:63]
ds_read_b128 v[148:151],v5 offset:25856
v_cvt_pk_bf16_f32 v65,0,v65
v_mfma_f32_32x32x16_bf16 v[48:63],a[184:187],v[184:187],v[48:63]
buffer_load_dwordx4 v25,s[32:35],s63 offen offset:2048 sc1 lds
ds_read_b128 v[152:155],v5 offset:34560
v_sqrt_f32 v65,v65
v_mfma_f32_32x32x16_bf16 v[48:63],a[188:191],v[188:191],v[48:63]
ds_read_b128 v[156:159],v5 offset:35072
v_cvt_pk_bf16_f32 v65,v64,v65
s_waitcnt lgkmcnt(4)
v_mfma_f32_32x32x16_bf16 v[48:63],a[192:195],v[128:131],v[48:63]
ds_read_b128 v[160:163],v5 offset:8064
ds_write_b32 v10,v65 offset:128
v_mfma_f32_32x32x16_bf16 v[48:63],a[196:199],v[132:135],v[48:63]
ds_read_b128 v[164:167],v5 offset:8576
v_mfma_f32_32x32x16_bf16 v[48:63],a[200:203],v[136:139],v[48:63]
buffer_load_dwordx4 v25,s[32:35],s63 offen offset:3072 sc1 lds
ds_read_b128 v[168:171],v5 offset:17280
s_add_u32 s63,s63,1187840
v_mfma_f32_32x32x16_bf16 v[48:63],a[204:207],v[140:143],v[48:63]
ds_read_b128 v[172:175],v5 offset:17792
s_waitcnt lgkmcnt(5)
v_mfma_f32_32x32x16_bf16 v[48:63],a[208:211],v[144:147],v[48:63]
ds_read_b128 v[176:179],v5 offset:26496
v_mfma_f32_32x32x16_bf16 v[48:63],a[212:215],v[148:151],v[48:63]
ds_read_b128 v[180:183],v5 offset:27008
v_mfma_f32_32x32x16_bf16 v[48:63],a[216:219],v[152:155],v[48:63]
ds_read_b128 v[184:187],v5 offset:35712
v_mfma_f32_32x32x16_bf16 v[48:63],a[220:223],v[156:159],v[48:63]
ds_read_b128 v[188:191],v5 offset:36224
s_waitcnt lgkmcnt(4)
s_waitcnt vmcnt(8)
s_barrier
v_mfma_f32_32x32x16_bf16 v[48:63],a[224:227],v[160:163],v[48:63]
ds_read_b128 v[128:131],v4 offset:0
v_mfma_f32_32x32x16_bf16 v[48:63],a[228:231],v[164:167],v[48:63]
ds_read_b128 v[132:135],v4 offset:512
ds_read_b32 v74,v11 offset:128
v_mfma_f32_32x32x16_bf16 v[48:63],a[232:235],v[168:171],v[48:63]
ds_read_b128 v[136:139],v4 offset:9216
v_mfma_f32_32x32x16_bf16 v[48:63],a[236:239],v[172:175],v[48:63]
ds_read_b128 v[140:143],v4 offset:9728
s_waitcnt lgkmcnt(5)
v_mfma_f32_32x32x16_bf16 v[48:63],a[240:243],v[176:179],v[48:63]
ds_read_b128 v[144:147],v4 offset:18432
s_waitcnt lgkmcnt(3)
v_lshlrev_b32 v72,16,v74
v_and_b32 v74,0xffff0000,v74
v_mfma_f32_32x32x16_bf16 v[48:63],a[244:247],v[180:183],v[48:63]
ds_read_b128 v[148:151],v4 offset:18944
v_rcp_f32 v73,v74
v_mul_f32 v74,v74,v74
v_mfma_f32_32x32x16_bf16 v[48:63],a[248:251],v[184:187],v[48:63]
ds_read_b128 v[152:155],v4 offset:27648
v_mul_f32 v73,0xbfb8aa3b,v73
s_sub_u32 s62,s62,1
v_mfma_f32_32x32x16_bf16 v[48:63],a[252:255],v[188:191],v[48:63]
ds_read_b128 v[156:159],v4 offset:28160
s_cbranch_scc0 .L_hl_1
s_nop 1
s_add_u32 s68,s68,1187840
s_nop 6
v_fma_f32 v48,-v72,v112,v48
v_fma_f32 v49,-v72,v113,v49
v_fma_f32 v50,-v72,v114,v50
v_fma_f32 v51,-v72,v115,v51
v_fma_f32 v52,-v72,v116,v52
v_fma_f32 v53,-v72,v117,v53
v_fma_f32 v54,-v72,v118,v54
v_fma_f32 v55,-v72,v119,v55
v_fma_f32 v56,-v72,v120,v56
v_fma_f32 v57,-v72,v121,v57
v_fma_f32 v58,-v72,v122,v58
v_fma_f32 v59,-v72,v123,v59
v_fma_f32 v60,-v72,v124,v60
v_fma_f32 v61,-v72,v125,v61
v_fma_f32 v62,-v72,v126,v62
v_fma_f32 v63,-v72,v127,v63
v_mul_f32 v76,v48,v73
v_mul_f32 v77,v49,v73
v_mul_f32 v78,v50,v73
v_mul_f32 v79,v51,v73
v_mul_f32 v80,v52,v73
v_mul_f32 v81,v53,v73
v_mul_f32 v82,v54,v73
v_mul_f32 v83,v55,v73
v_exp_f32 v76,v76
v_exp_f32 v77,v77
v_mul_f32 v48,v48,v56
v_mul_f32 v49,v49,v57
v_exp_f32 v78,v78
v_exp_f32 v79,v79
v_mul_f32 v50,v50,v58
v_mul_f32 v51,v51,v59
v_exp_f32 v80,v80
v_exp_f32 v81,v81
v_mul_f32 v52,v52,v60
v_mul_f32 v53,v53,v61
v_exp_f32 v82,v82
v_exp_f32 v83,v83
v_mul_f32 v54,v54,v62
v_mul_f32 v55,v55,v63
v_fma_f32 v76,v76,v74,v74
v_fma_f32 v77,v77,v74,v74
v_rcp_f32 v76,v76
v_rcp_f32 v77,v77
v_fma_f32 v78,v78,v74,v74
v_fma_f32 v79,v79,v74,v74
v_rcp_f32 v78,v78
v_rcp_f32 v79,v79
v_fma_f32 v80,v80,v74,v74
v_fma_f32 v81,v81,v74,v74
v_rcp_f32 v80,v80
v_rcp_f32 v81,v81
v_fma_f32 v82,v82,v74,v74
v_fma_f32 v83,v83,v74,v74
v_rcp_f32 v82,v82
v_rcp_f32 v83,v83
v_mul_f32 v48,v48,v76
v_mul_f32 v49,v49,v77
v_mul_f32 v50,v50,v78
v_mul_f32 v51,v51,v79
v_mul_f32 v52,v52,v80
v_mul_f32 v53,v53,v81
v_mul_f32 v54,v54,v82
v_mul_f32 v55,v55,v83
v_cvt_pk_bf16_f32 v84,v48,v49
v_cvt_pk_bf16_f32 v85,v50,v51
v_cvt_pk_bf16_f32 v86,v52,v53
v_cvt_pk_bf16_f32 v87,v54,v55
buffer_store_dwordx4 v[84:87],v15,s[32:35],s68 offen sc1
s_waitcnt vmcnt(0)
s_barrier
buffer_store_dwordx2 v[16:17],v18,s[32:35],s68 offen sc1
s_cbranch_execz .L_hbk
s_branch .L_end
.p2align 6
.L_hv2:
.L_hl_2:
s_waitcnt lgkmcnt(4)
v_mfma_f32_32x32x16_bf16 v[32:47],a[0:3],v[128:131],0
ds_read_b128 v[160:163],v4 offset:1152
s_waitcnt vmcnt(5)
v_cmp_ne_u64 vcc,v[16:17],v[12:13]
s_add_u32 s68,s68,1187840
v_mfma_f32_32x32x16_bf16 v[32:47],a[4:7],v[132:135],v[32:47]
ds_read_b128 v[164:167],v4 offset:1664
s_cbranch_vccnz .L_rth0_2
.L_bkh0_2:
s_add_u32 s66,s66,1187840
v_mfma_f32_32x32x16_bf16 v[32:47],a[8:11],v[136:139],v[32:47]
ds_read_b128 v[168:171],v4 offset:10368
s_add_u32 m0,s64,73728
v_fma_f32 v48,-v72,v112,v48
buffer_load_dwordx4 v20,s[32:35],s63 offen offset:0 sc1 lds
v_mfma_f32_32x32x16_bf16 v[32:47],a[12:15],v[140:143],v[32:47]
ds_read_b128 v[172:175],v4 offset:10880
v_fma_f32 v49,-v72,v113,v49
v_fma_f32 v50,-v72,v114,v50
v_fma_f32 v51,-v72,v115,v51
v_fma_f32 v52,-v72,v116,v52
s_waitcnt lgkmcnt(4)
v_mfma_f32_32x32x16_bf16 v[32:47],a[16:19],v[144:147],v[32:47]
buffer_load_dwordx4 v20,s[32:35],s63 offen offset:1024 sc1 lds
ds_read_b128 v[176:179],v4 offset:19584
v_fma_f32 v53,-v72,v117,v53
v_fma_f32 v54,-v72,v118,v54
v_mfma_f32_32x32x16_bf16 v[32:47],a[20:23],v[148:151],v[32:47]
ds_read_b128 v[180:183],v4 offset:20096
v_fma_f32 v55,-v72,v119,v55
v_fma_f32 v56,-v72,v120,v56
v_fma_f32 v57,-v72,v121,v57
v_fma_f32 v58,-v72,v122,v58
v_fma_f32 v59,-v72,v123,v59
v_mfma_f32_32x32x16_bf16 v[32:47],a[24:27],v[152:155],v[32:47]
buffer_load_dwordx4 v20,s[32:35],s63 offen offset:2048 sc1 lds
ds_read_b128 v[184:187],v4 offset:28800
v_fma_f32 v60,-v72,v124,v60
v_fma_f32 v61,-v72,v125,v61
v_mfma_f32_32x32x16_bf16 v[32:47],a[28:31],v[156:159],v[32:47]
ds_read_b128 v[188:191],v4 offset:29312
v_fma_f32 v62,-v72,v126,v62
v_fma_f32 v63,-v72,v127,v63
v_mul_f32 v76,v48,v73
v_mul_f32 v77,v49,v73
s_waitcnt lgkmcnt(4)
v_mfma_f32_32x32x16_bf16 v[32:47],a[32:35],v[160:163],v[32:47]
buffer_load_dwordx4 v20,s[32:35],s63 offen offset:3072 sc1 lds
ds_read_b128 v[128:131],v4 offset:2304
v_exp_f32 v76,v76
v_mfma_f32_32x32x16_bf16 v[32:47],a[36:39],v[164:167],v[32:47]
ds_read_b128 v[132:135],v4 offset:2816
v_exp_f32 v77,v77
v_mul_f32 v78,v50,v73
v_mul_f32 v79,v51,v73
v_mul_f32 v80,v52,v73
v_mfma_f32_32x32x16_bf16 v[32:47],a[40:43],v[168:171],v[32:47]
ds_read_b128 v[136:139],v4 offset:11520
s_add_u32 m0,s64,110592
v_exp_f32 v78,v78
v_exp_f32 v79,v79
v_mfma_f32_32x32x16_bf16 v[32:47],a[44:47],v[172:175],v[32:47]
ds_read_b128 v[140:143],v4 offset:12032
v_exp_f32 v80,v80
v_mul_f32 v81,v53,v73
v_mul_f32 v82,v54,v73
s_waitcnt lgkmcnt(4)
v_mfma_f32_32x32x16_bf16 v[32:47],a[48:51],v[176:179],v[32:47]
buffer_load_dwordx4 v21,s[32:35],s63 offen offset:0 sc1 lds
ds_read_b128 v[144:147],v4 offset:20736
v_exp_f32 v81,v81
v_mfma_f32_32x32x16_bf16 v[32:47],a[52:55],v[180:183],v[32:47]
ds_read_b128 v[148:151],v4 offset:21248
v_exp_f32 v82,v82
v_mul_f32 v83,v55,v73
v_mul_f32 v48,v48,v56
v_mul_f32 v49,v49,v57
v_mfma_f32_32x32x16_bf16 v[32:47],a[56:59],v[184:187],v[32:47]
buffer_load_dwordx4 v21,s[32:35],s63 offen offset:1024 sc1 lds
ds_read_b128 v[152:155],v4 offset:29952
v_exp_f32 v83,v83
v_mfma_f32_32x32x16_bf16 v[32:47],a[60:63],v[188:191],v[32:47]
ds_read_b128 v[156:159],v4 offset:30464
v_mul_f32 v50,v50,v58
v_mul_f32 v51,v51,v59
v_mul_f32 v52,v52,v60
v_mul_f32 v53,v53,v61
s_waitcnt lgkmcnt(4)
v_mfma_f32_32x32x16_bf16 v[32:47],a[64:67],v[128:131],v[32:47]
buffer_load_dwordx4 v21,s[32:35],s63 offen offset:2048 sc1 lds
ds_read_b128 v[160:163],v4 offset:3456
v_mul_f32 v54,v54,v62
v_mul_f32 v55,v55,v63
v_mfma_f32_32x32x16_bf16 v[32:47],a[68:71],v[132:135],v[32:47]
ds_read_b128 v[164:167],v4 offset:3968
v_fma_f32 v76,v76,v74,v74
v_fma_f32 v77,v77,v74,v74
v_rcp_f32 v76,v76
v_fma_f32 v78,v78,v74,v74
v_mfma_f32_32x32x16_bf16 v[32:47],a[72:75],v[136:139],v[32:47]
buffer_load_dwordx4 v21,s[32:35],s63 offen offset:3072 sc1 lds
ds_read_b128 v[168:171],v4 offset:12672
s_add_u32 m0,s64,77824
v_fma_f32 v79,v79,v74,v74
v_mfma_f32_32x32x16_bf16 v[32:47],a[76:79],v[140:143],v[32:47]
ds_read_b128 v[172:175],v4 offset:13184
v_rcp_f32 v77,v77
v_rcp_f32 v78,v78
s_waitcnt lgkmcnt(4)
v_mfma_f32_32x32x16_bf16 v[32:47],a[80:83],v[144:147],v[32:47]
buffer_load_dwordx4 v22,s[32:35],s63 offen offset:0 sc1 lds
ds_read_b128 v[176:179],v4 offset:21888
s_add_u32 m0,s64,114688
v_fma_f32 v80,v80,v74,v74
v_mfma_f32_32x32x16_bf16 v[32:47],a[84:87],v[148:151],v[32:47]
ds_read_b128 v[180:183],v4 offset:22400
v_rcp_f32 v79,v79
v_rcp_f32 v80,v80
v_fma_f32 v81,v81,v74,v74
v_mfma_f32_32x32x16_bf16 v[32:47],a[88:91],v[152:155],v[32:47]
buffer_load_dwordx4 v23,s[32:35],s63 offen offset:0 sc1 lds
)#"
R"#(
ds_read_b128 v[184:187],v4 offset:31104
v_rcp_f32 v81,v81
s_waitcnt vmcnt(10)
s_barrier
v_mfma_f32_32x32x16_bf16 v[32:47],a[92:95],v[156:159],v[32:47]
ds_read_b128 v[188:191],v4 offset:31616
v_fma_f32 v82,v82,v74,v74
v_fma_f32 v83,v83,v74,v74
v_rcp_f32 v82,v82
s_waitcnt lgkmcnt(4)
v_mfma_f32_32x32x16_bf16 v[32:47],a[96:99],v[160:163],v[32:47]
ds_read_b128 v[128:131],v4 offset:4608
v_rcp_f32 v83,v83
v_mul_f32 v48,v48,v76
v_mfma_f32_32x32x16_bf16 v[32:47],a[100:103],v[164:167],v[32:47]
ds_read_b128 v[132:135],v4 offset:5120
v_mul_f32 v49,v49,v77
v_mul_f32 v50,v50,v78
v_mul_f32 v51,v51,v79
v_mul_f32 v52,v52,v80
v_mul_f32 v53,v53,v81
v_mfma_f32_32x32x16_bf16 v[32:47],a[104:107],v[168:171],v[32:47]
ds_read_b128 v[136:139],v4 offset:13824
s_add_u32 m0,s64,78848
v_mul_f32 v54,v54,v82
buffer_load_dwordx4 v24,s[32:35],s63 offen offset:0 sc1 lds
v_mfma_f32_32x32x16_bf16 v[32:47],a[108:111],v[172:175],v[32:47]
ds_read_b128 v[140:143],v4 offset:14336
v_mul_f32 v55,v55,v83
v_cvt_pk_bf16_f32 v84,v48,v49
v_cvt_pk_bf16_f32 v85,v50,v51
v_cvt_pk_bf16_f32 v86,v52,v53
s_waitcnt lgkmcnt(4)
v_mfma_f32_32x32x16_bf16 v[32:47],a[112:115],v[176:179],v[32:47]
ds_read_b128 v[144:147],v4 offset:23040
v_cvt_pk_bf16_f32 v87,v54,v55
ds_read2st64_b32 v[64:65],v8 offset0:0 offset1:36
ds_read2st64_b32 v[66:67],v8 offset0:72 offset1:108
ds_read2st64_b32 v[68:69],v8 offset0:144 offset1:180
ds_read2st64_b32 v[70:71],v8 offset0:216 offset1:252
v_mfma_f32_32x32x16_bf16 v[32:47],a[116:119],v[180:183],v[32:47]
ds_read_b128 v[148:151],v4 offset:23552
v_xor_b32 v8,v9,v8
v_mfma_f32_32x32x16_bf16 v[32:47],a[120:123],v[184:187],v[32:47]
buffer_store_dwordx4 v[84:87],v15,s[32:35],s68 offen sc1
ds_read_b128 v[152:155],v4 offset:32256
v_mfma_f32_32x32x16_bf16 v[32:47],a[124:127],v[188:191],v[32:47]
ds_read_b128 v[156:159],v4 offset:32768
s_waitcnt lgkmcnt(8)
v_mfma_f32_32x32x16_bf16 v[32:47],a[128:131],v[128:131],v[32:47]
buffer_load_dwordx4 v24,s[32:35],s63 offen offset:1024 sc1 lds
ds_read_b128 v[160:163],v4 offset:5760
v_mfma_f32_32x32x16_bf16 v[32:47],a[132:135],v[132:135],v[32:47]
ds_read_b128 v[164:167],v4 offset:6272
s_waitcnt lgkmcnt(5)
v_add_f32 v64,v64,v65
v_add_f32 v66,v66,v67
v_add_f32 v68,v68,v69
v_add_f32 v70,v70,v71
v_mfma_f32_32x32x16_bf16 v[32:47],a[136:139],v[136:139],v[32:47]
buffer_load_dwordx4 v24,s[32:35],s63 offen offset:2048 sc1 lds
ds_read_b128 v[168:171],v4 offset:14976
v_add_f32 v66,v64,v66
v_add_f32 v68,v68,v70
v_mfma_f32_32x32x16_bf16 v[32:47],a[140:143],v[140:143],v[32:47]
ds_read_b128 v[172:175],v4 offset:15488
v_add_f32_dpp v64,v66,v66 row_ror:8 row_mask:0xf bank_mask:0x3
s_waitcnt lgkmcnt(4)
v_mfma_f32_32x32x16_bf16 v[32:47],a[144:147],v[144:147],v[32:47]
buffer_load_dwordx2 v[12:13],v14,s[32:35],s66 offen sc1
ds_read_b128 v[176:179],v4 offset:24192
v_add_f32_dpp v64,v68,v68 row_ror:8 row_mask:0xf bank_mask:0xc
v_mfma_f32_32x32x16_bf16 v[32:47],a[148:151],v[148:151],v[32:47]
ds_read_b128 v[180:183],v4 offset:24704
v_add_f32_dpp v64,v64,v64 row_ror:12 row_mask:0xf bank_mask:0xf
v_mfma_f32_32x32x16_bf16 v[32:47],a[152:155],v[152:155],v[32:47]
buffer_load_dwordx4 v24,s[32:35],s63 offen offset:3072 sc1 lds
ds_read_b128 v[184:187],v4 offset:33408
v_add_f32_dpp v64,v64,v64 quad_perm:[2,3,0,1] row_mask:0xf bank_mask:0xf
v_mfma_f32_32x32x16_bf16 v[32:47],a[156:159],v[156:159],v[32:47]
ds_read_b128 v[188:191],v4 offset:33920
s_add_u32 m0,s64,115712
v_mul_f32 v64,0x3a800000,v64
s_waitcnt lgkmcnt(4)
v_mfma_f32_32x32x16_bf16 v[32:47],a[160:163],v[160:163],v[32:47]
buffer_load_dwordx4 v25,s[32:35],s63 offen offset:0 sc1 lds
ds_read_b128 v[128:131],v4 offset:6912
v_mul_f32 v65,v64,v64
v_mfma_f32_32x32x16_bf16 v[32:47],a[164:167],v[164:167],v[32:47]
ds_read_b128 v[132:135],v4 offset:7424
v_sub_f32_dpp v65,v64,v65 quad_perm:[1,1,3,3] row_mask:0xf bank_mask:0xf
v_mfma_f32_32x32x16_bf16 v[32:47],a[168:171],v[168:171],v[32:47]
ds_read_b128 v[136:139],v4 offset:16128
v_max_f32 v65,0,v65
v_mfma_f32_32x32x16_bf16 v[32:47],a[172:175],v[172:175],v[32:47]
ds_read_b128 v[140:143],v4 offset:16640
v_cvt_pk_bf16_f32 v65,0,v65
s_waitcnt lgkmcnt(4)
v_mfma_f32_32x32x16_bf16 v[32:47],a[176:179],v[176:179],v[32:47]
buffer_load_dwordx4 v25,s[32:35],s63 offen offset:1024 sc1 lds
ds_read_b128 v[144:147],v4 offset:25344
v_add_f32 v65,0x3727c5ac,v65
v_mfma_f32_32x32x16_bf16 v[32:47],a[180:183],v[180:183],v[32:47]
ds_read_b128 v[148:151],v4 offset:25856
v_cvt_pk_bf16_f32 v65,0,v65
v_mfma_f32_32x32x16_bf16 v[32:47],a[184:187],v[184:187],v[32:47]
buffer_load_dwordx4 v25,s[32:35],s63 offen offset:2048 sc1 lds
ds_read_b128 v[152:155],v4 offset:34560
v_sqrt_f32 v65,v65
v_mfma_f32_32x32x16_bf16 v[32:47],a[188:191],v[188:191],v[32:47]
ds_read_b128 v[156:159],v4 offset:35072
v_cvt_pk_bf16_f32 v65,v64,v65
s_waitcnt lgkmcnt(4)
v_mfma_f32_32x32x16_bf16 v[32:47],a[192:195],v[128:131],v[32:47]
ds_read_b128 v[160:163],v4 offset:8064
ds_write_b32 v10,v65 offset:0
v_mfma_f32_32x32x16_bf16 v[32:47],a[196:199],v[132:135],v[32:47]
ds_read_b128 v[164:167],v4 offset:8576
v_mfma_f32_32x32x16_bf16 v[32:47],a[200:203],v[136:139],v[32:47]
buffer_load_dwordx4 v25,s[32:35],s63 offen offset:3072 sc1 lds
ds_read_b128 v[168:171],v4 offset:17280
s_add_u32 s63,s63,1187840
v_mfma_f32_32x32x16_bf16 v[32:47],a[204:207],v[140:143],v[32:47]
ds_read_b128 v[172:175],v4 offset:17792
s_waitcnt lgkmcnt(5)
v_mfma_f32_32x32x16_bf16 v[32:47],a[208:211],v[144:147],v[32:47]
ds_read_b128 v[176:179],v4 offset:26496
v_mfma_f32_32x32x16_bf16 v[32:47],a[212:215],v[148:151],v[32:47]
ds_read_b128 v[180:183],v4 offset:27008
v_mfma_f32_32x32x16_bf16 v[32:47],a[216:219],v[152:155],v[32:47]
ds_read_b128 v[184:187],v4 offset:35712
s_waitcnt vmcnt(8)
s_waitcnt lgkmcnt(6)
s_barrier
v_mfma_f32_32x32x16_bf16 v[32:47],a[220:223],v[156:159],v[32:47]
ds_read_b128 v[188:191],v4 offset:36224
s_waitcnt lgkmcnt(4)
v_mfma_f32_32x32x16_bf16 v[32:47],a[224:227],v[160:163],v[32:47]
ds_read_b128 v[128:131],v5 offset:0
v_mfma_f32_32x32x16_bf16 v[32:47],a[228:231],v[164:167],v[32:47]
ds_read_b128 v[132:135],v5 offset:512
ds_read_b32 v74,v11 offset:0
v_mfma_f32_32x32x16_bf16 v[32:47],a[232:235],v[168:171],v[32:47]
ds_read_b128 v[136:139],v5 offset:9216
v_mfma_f32_32x32x16_bf16 v[32:47],a[236:239],v[172:175],v[32:47]
ds_read_b128 v[140:143],v5 offset:9728
s_waitcnt lgkmcnt(5)
v_mfma_f32_32x32x16_bf16 v[32:47],a[240:243],v[176:179],v[32:47]
ds_read_b128 v[144:147],v5 offset:18432
s_waitcnt lgkmcnt(3)
v_lshlrev_b32 v72,16,v74
v_and_b32 v74,0xffff0000,v74
v_mfma_f32_32x32x16_bf16 v[32:47],a[244:247],v[180:183],v[32:47]
ds_read_b128 v[148:151],v5 offset:18944
v_rcp_f32 v73,v74
v_mul_f32 v74,v74,v74
v_mfma_f32_32x32x16_bf16 v[32:47],a[248:251],v[184:187],v[32:47]
ds_read_b128 v[152:155],v5 offset:27648
v_mul_f32 v73,0xbfb8aa3b,v73
v_mfma_f32_32x32x16_bf16 v[32:47],a[252:255],v[188:191],v[32:47]
ds_read_b128 v[156:159],v5 offset:28160
s_waitcnt lgkmcnt(4)
v_mfma_f32_32x32x16_bf16 v[48:63],a[0:3],v[128:131],0
ds_read_b128 v[160:163],v5 offset:1152
s_waitcnt vmcnt(5)
v_cmp_ne_u64 vcc,v[16:17],v[12:13]
s_add_u32 s68,s68,1187840
v_mfma_f32_32x32x16_bf16 v[48:63],a[4:7],v[132:135],v[48:63]
ds_read_b128 v[164:167],v5 offset:1664
s_cbranch_vccnz .L_rth1_2
.L_bkh1_2:
s_add_u32 s66,s66,1187840
v_mfma_f32_32x32x16_bf16 v[48:63],a[8:11],v[136:139],v[48:63]
ds_read_b128 v[168:171],v5 offset:10368
s_add_u32 m0,s64,0
v_fma_f32 v32,-v72,v112,v32
buffer_load_dwordx4 v20,s[32:35],s63 offen offset:0 sc1 lds
v_mfma_f32_32x32x16_bf16 v[48:63],a[12:15],v[140:143],v[48:63]
ds_read_b128 v[172:175],v5 offset:10880
v_fma_f32 v33,-v72,v113,v33
v_fma_f32 v34,-v72,v114,v34
v_fma_f32 v35,-v72,v115,v35
v_fma_f32 v36,-v72,v116,v36
s_waitcnt lgkmcnt(4)
v_mfma_f32_32x32x16_bf16 v[48:63],a[16:19],v[144:147],v[48:63]
buffer_load_dwordx4 v20,s[32:35],s63 offen offset:1024 sc1 lds
ds_read_b128 v[176:179],v5 offset:19584
v_fma_f32 v37,-v72,v117,v37
v_fma_f32 v38,-v72,v118,v38
v_mfma_f32_32x32x16_bf16 v[48:63],a[20:23],v[148:151],v[48:63]
ds_read_b128 v[180:183],v5 offset:20096
v_fma_f32 v39,-v72,v119,v39
v_fma_f32 v40,-v72,v120,v40
v_fma_f32 v41,-v72,v121,v41
v_fma_f32 v42,-v72,v122,v42
v_fma_f32 v43,-v72,v123,v43
v_mfma_f32_32x32x16_bf16 v[48:63],a[24:27],v[152:155],v[48:63]
buffer_load_dwordx4 v20,s[32:35],s63 offen offset:2048 sc1 lds
ds_read_b128 v[184:187],v5 offset:28800
v_fma_f32 v44,-v72,v124,v44
v_fma_f32 v45,-v72,v125,v45
v_mfma_f32_32x32x16_bf16 v[48:63],a[28:31],v[156:159],v[48:63]
ds_read_b128 v[188:191],v5 offset:29312
v_fma_f32 v46,-v72,v126,v46
v_fma_f32 v47,-v72,v127,v47
v_mul_f32 v76,v32,v73
v_mul_f32 v77,v33,v73
s_waitcnt lgkmcnt(4)
v_mfma_f32_32x32x16_bf16 v[48:63],a[32:35],v[160:163],v[48:63]
buffer_load_dwordx4 v20,s[32:35],s63 offen offset:3072 sc1 lds
ds_read_b128 v[128:131],v5 offset:2304
v_exp_f32 v76,v76
v_mfma_f32_32x32x16_bf16 v[48:63],a[36:39],v[164:167],v[48:63]
ds_read_b128 v[132:135],v5 offset:2816
v_exp_f32 v77,v77
v_mul_f32 v78,v34,v73
v_mul_f32 v79,v35,v73
v_mul_f32 v80,v36,v73
v_mfma_f32_32x32x16_bf16 v[48:63],a[40:43],v[168:171],v[48:63]
ds_read_b128 v[136:139],v5 offset:11520
s_add_u32 m0,s64,36864
v_exp_f32 v78,v78
v_exp_f32 v79,v79
v_mfma_f32_32x32x16_bf16 v[48:63],a[44:47],v[172:175],v[48:63]
ds_read_b128 v[140:143],v5 offset:12032
v_exp_f32 v80,v80
v_mul_f32 v81,v37,v73
v_mul_f32 v82,v38,v73
s_waitcnt lgkmcnt(4)
v_mfma_f32_32x32x16_bf16 v[48:63],a[48:51],v[176:179],v[48:63]
buffer_load_dwordx4 v21,s[32:35],s63 offen offset:0 sc1 lds
ds_read_b128 v[144:147],v5 offset:20736
v_exp_f32 v81,v81
v_mfma_f32_32x32x16_bf16 v[48:63],a[52:55],v[180:183],v[48:63]
ds_read_b128 v[148:151],v5 offset:21248
v_exp_f32 v82,v82
v_mul_f32 v83,v39,v73
v_mul_f32 v32,v32,v40
v_mul_f32 v33,v33,v41
v_mfma_f32_32x32x16_bf16 v[48:63],a[56:59],v[184:187],v[48:63]
buffer_load_dwordx4 v21,s[32:35],s63 offen offset:1024 sc1 lds
ds_read_b128 v[152:155],v5 offset:29952
v_exp_f32 v83,v83
v_mfma_f32_32x32x16_bf16 v[48:63],a[60:63],v[188:191],v[48:63]
ds_read_b128 v[156:159],v5 offset:30464
v_mul_f32 v34,v34,v42
v_mul_f32 v35,v35,v43
v_mul_f32 v36,v36,v44
v_mul_f32 v37,v37,v45
s_waitcnt lgkmcnt(4)
v_mfma_f32_32x32x16_bf16 v[48:63],a[64:67],v[128:131],v[48:63]
buffer_load_dwordx4 v21,s[32:35],s63 offen offset:2048 sc1 lds
ds_read_b128 v[160:163],v5 offset:3456
v_mul_f32 v38,v38,v46
v_mul_f32 v39,v39,v47
v_mfma_f32_32x32x16_bf16 v[48:63],a[68:71],v[132:135],v[48:63]
ds_read_b128 v[164:167],v5 offset:3968
v_fma_f32 v76,v76,v74,v74
v_fma_f32 v77,v77,v74,v74
v_rcp_f32 v76,v76
v_fma_f32 v78,v78,v74,v74
v_mfma_f32_32x32x16_bf16 v[48:63],a[72:75],v[136:139],v[48:63]
buffer_load_dwordx4 v21,s[32:35],s63 offen offset:3072 sc1 lds
ds_read_b128 v[168:171],v5 offset:12672
s_add_u32 m0,s64,4096
v_fma_f32 v79,v79,v74,v74
v_mfma_f32_32x32x16_bf16 v[48:63],a[76:79],v[140:143],v[48:63]
ds_read_b128 v[172:175],v5 offset:13184
v_rcp_f32 v77,v77
v_rcp_f32 v78,v78
s_waitcnt lgkmcnt(4)
v_mfma_f32_32x32x16_bf16 v[48:63],a[80:83],v[144:147],v[48:63]
buffer_load_dwordx4 v22,s[32:35],s63 offen offset:0 sc1 lds
ds_read_b128 v[176:179],v5 offset:21888
s_add_u32 m0,s64,40960
v_fma_f32 v80,v80,v74,v74
v_mfma_f32_32x32x16_bf16 v[48:63],a[84:87],v[148:151],v[48:63]
ds_read_b128 v[180:183],v5 offset:22400
v_rcp_f32 v79,v79
v_rcp_f32 v80,v80
v_fma_f32 v81,v81,v74,v74
v_mfma_f32_32x32x16_bf16 v[48:63],a[88:91],v[152:155],v[48:63]
buffer_load_dwordx4 v23,s[32:35],s63 offen offset:0 sc1 lds
ds_read_b128 v[184:187],v5 offset:31104
v_rcp_f32 v81,v81
s_waitcnt vmcnt(10)
s_barrier
v_mfma_f32_32x32x16_bf16 v[48:63],a[92:95],v[156:159],v[48:63]
ds_read_b128 v[188:191],v5 offset:31616
v_fma_f32 v82,v82,v74,v74
v_fma_f32 v83,v83,v74,v74
v_rcp_f32 v82,v82
s_waitcnt lgkmcnt(4)
v_mfma_f32_32x32x16_bf16 v[48:63],a[96:99],v[160:163],v[48:63]
ds_read_b128 v[128:131],v5 offset:4608
v_rcp_f32 v83,v83
v_mul_f32 v32,v32,v76
v_mfma_f32_32x32x16_bf16 v[48:63],a[100:103],v[164:167],v[48:63]
ds_read_b128 v[132:135],v5 offset:5120
v_mul_f32 v33,v33,v77
v_mul_f32 v34,v34,v78
v_mul_f32 v35,v35,v79
v_mul_f32 v36,v36,v80
v_mul_f32 v37,v37,v81
v_mfma_f32_32x32x16_bf16 v[48:63],a[104:107],v[168:171],v[48:63]
ds_read_b128 v[136:139],v5 offset:13824
s_add_u32 m0,s64,5120
v_mul_f32 v38,v38,v82
buffer_load_dwordx4 v24,s[32:35],s63 offen offset:0 sc1 lds
v_mfma_f32_32x32x16_bf16 v[48:63],a[108:111],v[172:175],v[48:63]
ds_read_b128 v[140:143],v5 offset:14336
v_mul_f32 v39,v39,v83
v_cvt_pk_bf16_f32 v84,v32,v33
v_cvt_pk_bf16_f32 v85,v34,v35
v_cvt_pk_bf16_f32 v86,v36,v37
s_waitcnt lgkmcnt(4)
v_mfma_f32_32x32x16_bf16 v[48:63],a[112:115],v[176:179],v[48:63]
ds_read_b128 v[144:147],v5 offset:23040
v_cvt_pk_bf16_f32 v87,v38,v39
ds_read2st64_b32 v[64:65],v8 offset0:0 offset1:36
ds_read2st64_b32 v[66:67],v8 offset0:72 offset1:108
ds_read2st64_b32 v[68:69],v8 offset0:144 offset1:180
ds_read2st64_b32 v[70:71],v8 offset0:216 offset1:252
v_mfma_f32_32x32x16_bf16 v[48:63],a[116:119],v[180:183],v[48:63]
ds_read_b128 v[148:151],v5 offset:23552
v_xor_b32 v8,v9,v8
v_mfma_f32_32x32x16_bf16 v[48:63],a[120:123],v[184:187],v[48:63]
buffer_store_dwordx4 v[84:87],v15,s[32:35],s68 offen sc1
ds_read_b128 v[152:155],v5 offset:32256
v_mfma_f32_32x32x16_bf16 v[48:63],a[124:127],v[188:191],v[48:63]
ds_read_b128 v[156:159],v5 offset:32768
s_waitcnt lgkmcnt(8)
v_mfma_f32_32x32x16_bf16 v[48:63],a[128:131],v[128:131],v[48:63]
buffer_load_dwordx4 v24,s[32:35],s63 offen offset:1024 sc1 lds
ds_read_b128 v[160:163],v5 offset:5760
v_mfma_f32_32x32x16_bf16 v[48:63],a[132:135],v[132:135],v[48:63]
ds_read_b128 v[164:167],v5 offset:6272
s_waitcnt lgkmcnt(5)
v_add_f32 v64,v64,v65
v_add_f32 v66,v66,v67
v_add_f32 v68,v68,v69
v_add_f32 v70,v70,v71
v_mfma_f32_32x32x16_bf16 v[48:63],a[136:139],v[136:139],v[48:63]
buffer_load_dwordx4 v24,s[32:35],s63 offen offset:2048 sc1 lds
ds_read_b128 v[168:171],v5 offset:14976
v_add_f32 v66,v64,v66
v_add_f32 v68,v68,v70
v_mfma_f32_32x32x16_bf16 v[48:63],a[140:143],v[140:143],v[48:63]
ds_read_b128 v[172:175],v5 offset:15488
v_add_f32_dpp v64,v66,v66 row_ror:8 row_mask:0xf bank_mask:0x3
s_waitcnt lgkmcnt(4)
v_mfma_f32_32x32x16_bf16 v[48:63],a[144:147],v[144:147],v[48:63]
buffer_load_dwordx2 v[12:13],v14,s[32:35],s66 offen sc1
ds_read_b128 v[176:179],v5 offset:24192
v_add_f32_dpp v64,v68,v68 row_ror:8 row_mask:0xf bank_mask:0xc
v_mfma_f32_32x32x16_bf16 v[48:63],a[148:151],v[148:151],v[48:63]
ds_read_b128 v[180:183],v5 offset:24704
v_add_f32_dpp v64,v64,v64 row_ror:12 row_mask:0xf bank_mask:0xf
v_mfma_f32_32x32x16_bf16 v[48:63],a[152:155],v[152:155],v[48:63]
buffer_load_dwordx4 v24,s[32:35],s63 offen offset:3072 sc1 lds
ds_read_b128 v[184:187],v5 offset:33408
v_add_f32_dpp v64,v64,v64 quad_perm:[2,3,0,1] row_mask:0xf bank_mask:0xf
v_mfma_f32_32x32x16_bf16 v[48:63],a[156:159],v[156:159],v[48:63]
ds_read_b128 v[188:191],v5 offset:33920
s_add_u32 m0,s64,41984
v_mul_f32 v64,0x3a800000,v64
s_waitcnt lgkmcnt(4)
v_mfma_f32_32x32x16_bf16 v[48:63],a[160:163],v[160:163],v[48:63]
buffer_load_dwordx4 v25,s[32:35],s63 offen offset:0 sc1 lds
ds_read_b128 v[128:131],v5 offset:6912
v_mul_f32 v65,v64,v64
v_mfma_f32_32x32x16_bf16 v[48:63],a[164:167],v[164:167],v[48:63]
ds_read_b128 v[132:135],v5 offset:7424
v_sub_f32_dpp v65,v64,v65 quad_perm:[1,1,3,3] row_mask:0xf bank_mask:0xf
v_mfma_f32_32x32x16_bf16 v[48:63],a[168:171],v[168:171],v[48:63]
ds_read_b128 v[136:139],v5 offset:16128
v_max_f32 v65,0,v65
v_mfma_f32_32x32x16_bf16 v[48:63],a[172:175],v[172:175],v[48:63]
ds_read_b128 v[140:143],v5 offset:16640
v_cvt_pk_bf16_f32 v65,0,v65
s_waitcnt lgkmcnt(4)
v_mfma_f32_32x32x16_bf16 v[48:63],a[176:179],v[176:179],v[48:63]
buffer_load_dwordx4 v25,s[32:35],s63 offen offset:1024 sc1 lds
ds_read_b128 v[144:147],v5 offset:25344
v_add_f32 v65,0x3727c5ac,v65
v_mfma_f32_32x32x16_bf16 v[48:63],a[180:183],v[180:183],v[48:63]
ds_read_b128 v[148:151],v5 offset:25856
v_cvt_pk_bf16_f32 v65,0,v65
v_mfma_f32_32x32x16_bf16 v[48:63],a[184:187],v[184:187],v[48:63]
buffer_load_dwordx4 v25,s[32:35],s63 offen offset:2048 sc1 lds
ds_read_b128 v[152:155],v5 offset:34560
v_sqrt_f32 v65,v65
v_mfma_f32_32x32x16_bf16 v[48:63],a[188:191],v[188:191],v[48:63]
ds_read_b128 v[156:159],v5 offset:35072
v_cvt_pk_bf16_f32 v65,v64,v65
s_waitcnt lgkmcnt(4)
v_mfma_f32_32x32x16_bf16 v[48:63],a[192:195],v[128:131],v[48:63]
ds_read_b128 v[160:163],v5 offset:8064
ds_write_b32 v10,v65 offset:128
v_mfma_f32_32x32x16_bf16 v[48:63],a[196:199],v[132:135],v[48:63]
ds_read_b128 v[164:167],v5 offset:8576
v_mfma_f32_32x32x16_bf16 v[48:63],a[200:203],v[136:139],v[48:63]
buffer_load_dwordx4 v25,s[32:35],s63 offen offset:3072 sc1 lds
ds_read_b128 v[168:171],v5 offset:17280
s_add_u32 s63,s63,1187840
v_mfma_f32_32x32x16_bf16 v[48:63],a[204:207],v[140:143],v[48:63]
ds_read_b128 v[172:175],v5 offset:17792
s_waitcnt lgkmcnt(5)
v_mfma_f32_32x32x16_bf16 v[48:63],a[208:211],v[144:147],v[48:63]
ds_read_b128 v[176:179],v5 offset:26496
v_mfma_f32_32x32x16_bf16 v[48:63],a[212:215],v[148:151],v[48:63]
ds_read_b128 v[180:183],v5 offset:27008
v_mfma_f32_32x32x16_bf16 v[48:63],a[216:219],v[152:155],v[48:63]
ds_read_b128 v[184:187],v5 offset:35712
s_waitcnt vmcnt(8)
s_waitcnt lgkmcnt(6)
s_barrier
v_mfma_f32_32x32x16_bf16 v[48:63],a[220:223],v[156:159],v[48:63]
ds_read_b128 v[188:191],v5 offset:36224
s_waitcnt lgkmcnt(4)
v_mfma_f32_32x32x16_bf16 v[48:63],a[224:227],v[160:163],v[48:63]
ds_read_b128 v[128:131],v4 offset:0
v_mfma_f32_32x32x16_bf16 v[48:63],a[228:231],v[164:167],v[48:63]
ds_read_b128 v[132:135],v4 offset:512
ds_read_b32 v74,v11 offset:128
v_mfma_f32_32x32x16_bf16 v[48:63],a[232:235],v[168:171],v[48:63]
ds_read_b128 v[136:139],v4 offset:9216
v_mfma_f32_32x32x16_bf16 v[48:63],a[236:239],v[172:175],v[48:63]
ds_read_b128 v[140:143],v4 offset:9728
s_waitcnt lgkmcnt(5)
v_mfma_f32_32x32x16_bf16 v[48:63],a[240:243],v[176:179],v[48:63]
ds_read_b128 v[144:147],v4 offset:18432
s_waitcnt lgkmcnt(3)
v_lshlrev_b32 v72,16,v74
v_and_b32 v74,0xffff0000,v74
v_mfma_f32_32x32x16_bf16 v[48:63],a[244:247],v[180:183],v[48:63]
ds_read_b128 v[148:151],v4 offset:18944
v_rcp_f32 v73,v74
v_mul_f32 v74,v74,v74
v_mfma_f32_32x32x16_bf16 v[48:63],a[248:251],v[184:187],v[48:63]
ds_read_b128 v[152:155],v4 offset:27648
v_mul_f32 v73,0xbfb8aa3b,v73
s_sub_u32 s62,s62,1
v_mfma_f32_32x32x16_bf16 v[48:63],a[252:255],v[188:191],v[48:63]
ds_read_b128 v[156:159],v4 offset:28160
s_cbranch_scc0 .L_hl_2
s_nop 1
s_add_u32 s68,s68,1187840
s_nop 6
v_fma_f32 v48,-v72,v112,v48
v_fma_f32 v49,-v72,v113,v49
v_fma_f32 v50,-v72,v114,v50
v_fma_f32 v51,-v72,v115,v51
v_fma_f32 v52,-v72,v116,v52
v_fma_f32 v53,-v72,v117,v53
v_fma_f32 v54,-v72,v118,v54
v_fma_f32 v55,-v72,v119,v55
v_fma_f32 v56,-v72,v120,v56
v_fma_f32 v57,-v72,v121,v57
v_fma_f32 v58,-v72,v122,v58
v_fma_f32 v59,-v72,v123,v59
v_fma_f32 v60,-v72,v124,v60
v_fma_f32 v61,-v72,v125,v61
v_fma_f32 v62,-v72,v126,v62
v_fma_f32 v63,-v72,v127,v63
v_mul_f32 v76,v48,v73
v_mul_f32 v77,v49,v73
v_mul_f32 v78,v50,v73
v_mul_f32 v79,v51,v73
v_mul_f32 v80,v52,v73
v_mul_f32 v81,v53,v73
v_mul_f32 v82,v54,v73
v_mul_f32 v83,v55,v73
v_exp_f32 v76,v76
v_exp_f32 v77,v77
v_mul_f32 v48,v48,v56
v_mul_f32 v49,v49,v57
v_exp_f32 v78,v78
v_exp_f32 v79,v79
v_mul_f32 v50,v50,v58
v_mul_f32 v51,v51,v59
v_exp_f32 v80,v80
v_exp_f32 v81,v81
v_mul_f32 v52,v52,v60
v_mul_f32 v53,v53,v61
v_exp_f32 v82,v82
v_exp_f32 v83,v83
v_mul_f32 v54,v54,v62
v_mul_f32 v55,v55,v63
v_fma_f32 v76,v76,v74,v74
v_fma_f32 v77,v77,v74,v74
v_rcp_f32 v76,v76
v_rcp_f32 v77,v77
v_fma_f32 v78,v78,v74,v74
v_fma_f32 v79,v79,v74,v74
v_rcp_f32 v78,v78
v_rcp_f32 v79,v79
v_fma_f32 v80,v80,v74,v74
v_fma_f32 v81,v81,v74,v74
v_rcp_f32 v80,v80
)#"
R"#(
v_rcp_f32 v81,v81
v_fma_f32 v82,v82,v74,v74
v_fma_f32 v83,v83,v74,v74
v_rcp_f32 v82,v82
v_rcp_f32 v83,v83
v_mul_f32 v48,v48,v76
v_mul_f32 v49,v49,v77
v_mul_f32 v50,v50,v78
v_mul_f32 v51,v51,v79
v_mul_f32 v52,v52,v80
v_mul_f32 v53,v53,v81
v_mul_f32 v54,v54,v82
v_mul_f32 v55,v55,v83
v_cvt_pk_bf16_f32 v84,v48,v49
v_cvt_pk_bf16_f32 v85,v50,v51
v_cvt_pk_bf16_f32 v86,v52,v53
v_cvt_pk_bf16_f32 v87,v54,v55
buffer_store_dwordx4 v[84:87],v15,s[32:35],s68 offen sc1
s_waitcnt vmcnt(0)
s_barrier
buffer_store_dwordx2 v[16:17],v18,s[32:35],s68 offen sc1
s_cbranch_execz .L_hbk
s_branch .L_end
.p2align 6
.L_hv3:
.L_hl_3:
s_waitcnt lgkmcnt(4)
v_mfma_f32_32x32x16_bf16 v[32:47],a[0:3],v[128:131],0
ds_read_b128 v[160:163],v4 offset:1152
s_waitcnt vmcnt(5)
v_cmp_ne_u64 vcc,v[16:17],v[12:13]
s_add_u32 s68,s68,1187840
v_mfma_f32_32x32x16_bf16 v[32:47],a[4:7],v[132:135],v[32:47]
ds_read_b128 v[164:167],v4 offset:1664
s_cbranch_vccnz .L_rth0_3
.L_bkh0_3:
s_add_u32 s66,s66,1187840
v_mfma_f32_32x32x16_bf16 v[32:47],a[8:11],v[136:139],v[32:47]
ds_read_b128 v[168:171],v4 offset:10368
s_add_u32 m0,s64,73728
v_fma_f32 v48,-v72,v112,v48
buffer_load_dwordx4 v20,s[32:35],s63 offen offset:0 sc1 lds
v_mfma_f32_32x32x16_bf16 v[32:47],a[12:15],v[140:143],v[32:47]
ds_read_b128 v[172:175],v4 offset:10880
v_fma_f32 v49,-v72,v113,v49
v_fma_f32 v50,-v72,v114,v50
v_fma_f32 v51,-v72,v115,v51
v_fma_f32 v52,-v72,v116,v52
s_waitcnt lgkmcnt(4)
v_mfma_f32_32x32x16_bf16 v[32:47],a[16:19],v[144:147],v[32:47]
buffer_load_dwordx4 v20,s[32:35],s63 offen offset:1024 sc1 lds
ds_read_b128 v[176:179],v4 offset:19584
v_fma_f32 v53,-v72,v117,v53
v_fma_f32 v54,-v72,v118,v54
v_mfma_f32_32x32x16_bf16 v[32:47],a[20:23],v[148:151],v[32:47]
ds_read_b128 v[180:183],v4 offset:20096
v_fma_f32 v55,-v72,v119,v55
v_fma_f32 v56,-v72,v120,v56
v_fma_f32 v57,-v72,v121,v57
v_fma_f32 v58,-v72,v122,v58
v_fma_f32 v59,-v72,v123,v59
v_mfma_f32_32x32x16_bf16 v[32:47],a[24:27],v[152:155],v[32:47]
buffer_load_dwordx4 v20,s[32:35],s63 offen offset:2048 sc1 lds
ds_read_b128 v[184:187],v4 offset:28800
v_fma_f32 v60,-v72,v124,v60
v_fma_f32 v61,-v72,v125,v61
v_mfma_f32_32x32x16_bf16 v[32:47],a[28:31],v[156:159],v[32:47]
ds_read_b128 v[188:191],v4 offset:29312
v_fma_f32 v62,-v72,v126,v62
v_fma_f32 v63,-v72,v127,v63
v_mul_f32 v76,v48,v73
v_mul_f32 v77,v49,v73
s_waitcnt lgkmcnt(4)
v_mfma_f32_32x32x16_bf16 v[32:47],a[32:35],v[160:163],v[32:47]
buffer_load_dwordx4 v20,s[32:35],s63 offen offset:3072 sc1 lds
ds_read_b128 v[128:131],v4 offset:2304
v_exp_f32 v76,v76
v_mfma_f32_32x32x16_bf16 v[32:47],a[36:39],v[164:167],v[32:47]
ds_read_b128 v[132:135],v4 offset:2816
v_exp_f32 v77,v77
v_mul_f32 v78,v50,v73
v_mul_f32 v79,v51,v73
v_mul_f32 v80,v52,v73
v_mfma_f32_32x32x16_bf16 v[32:47],a[40:43],v[168:171],v[32:47]
ds_read_b128 v[136:139],v4 offset:11520
s_add_u32 m0,s64,110592
v_exp_f32 v78,v78
v_exp_f32 v79,v79
v_mfma_f32_32x32x16_bf16 v[32:47],a[44:47],v[172:175],v[32:47]
ds_read_b128 v[140:143],v4 offset:12032
v_exp_f32 v80,v80
v_mul_f32 v81,v53,v73
v_mul_f32 v82,v54,v73
s_waitcnt lgkmcnt(4)
v_mfma_f32_32x32x16_bf16 v[32:47],a[48:51],v[176:179],v[32:47]
buffer_load_dwordx4 v21,s[32:35],s63 offen offset:0 sc1 lds
ds_read_b128 v[144:147],v4 offset:20736
v_exp_f32 v81,v81
v_mfma_f32_32x32x16_bf16 v[32:47],a[52:55],v[180:183],v[32:47]
ds_read_b128 v[148:151],v4 offset:21248
v_exp_f32 v82,v82
v_mul_f32 v83,v55,v73
v_mul_f32 v48,v48,v56
v_mul_f32 v49,v49,v57
v_mfma_f32_32x32x16_bf16 v[32:47],a[56:59],v[184:187],v[32:47]
buffer_load_dwordx4 v21,s[32:35],s63 offen offset:1024 sc1 lds
ds_read_b128 v[152:155],v4 offset:29952
v_exp_f32 v83,v83
v_mfma_f32_32x32x16_bf16 v[32:47],a[60:63],v[188:191],v[32:47]
ds_read_b128 v[156:159],v4 offset:30464
v_mul_f32 v50,v50,v58
v_mul_f32 v51,v51,v59
v_mul_f32 v52,v52,v60
v_mul_f32 v53,v53,v61
s_waitcnt lgkmcnt(4)
v_mfma_f32_32x32x16_bf16 v[32:47],a[64:67],v[128:131],v[32:47]
buffer_load_dwordx4 v21,s[32:35],s63 offen offset:2048 sc1 lds
ds_read_b128 v[160:163],v4 offset:3456
v_mul_f32 v54,v54,v62
v_mul_f32 v55,v55,v63
v_mfma_f32_32x32x16_bf16 v[32:47],a[68:71],v[132:135],v[32:47]
ds_read_b128 v[164:167],v4 offset:3968
v_fma_f32 v76,v76,v74,v74
v_fma_f32 v77,v77,v74,v74
v_rcp_f32 v76,v76
v_fma_f32 v78,v78,v74,v74
v_mfma_f32_32x32x16_bf16 v[32:47],a[72:75],v[136:139],v[32:47]
buffer_load_dwordx4 v21,s[32:35],s63 offen offset:3072 sc1 lds
ds_read_b128 v[168:171],v4 offset:12672
s_add_u32 m0,s64,77824
v_fma_f32 v79,v79,v74,v74
v_mfma_f32_32x32x16_bf16 v[32:47],a[76:79],v[140:143],v[32:47]
ds_read_b128 v[172:175],v4 offset:13184
v_rcp_f32 v77,v77
v_rcp_f32 v78,v78
s_waitcnt lgkmcnt(4)
v_mfma_f32_32x32x16_bf16 v[32:47],a[80:83],v[144:147],v[32:47]
buffer_load_dwordx4 v22,s[32:35],s63 offen offset:0 sc1 lds
ds_read_b128 v[176:179],v4 offset:21888
s_add_u32 m0,s64,114688
v_fma_f32 v80,v80,v74,v74
v_mfma_f32_32x32x16_bf16 v[32:47],a[84:87],v[148:151],v[32:47]
ds_read_b128 v[180:183],v4 offset:22400
v_rcp_f32 v79,v79
v_rcp_f32 v80,v80
v_fma_f32 v81,v81,v74,v74
s_waitcnt vmcnt(9)
s_barrier
v_mfma_f32_32x32x16_bf16 v[32:47],a[88:91],v[152:155],v[32:47]
buffer_load_dwordx4 v23,s[32:35],s63 offen offset:0 sc1 lds
ds_read_b128 v[184:187],v4 offset:31104
v_rcp_f32 v81,v81
v_mfma_f32_32x32x16_bf16 v[32:47],a[92:95],v[156:159],v[32:47]
ds_read_b128 v[188:191],v4 offset:31616
v_fma_f32 v82,v82,v74,v74
v_fma_f32 v83,v83,v74,v74
v_rcp_f32 v82,v82
s_waitcnt lgkmcnt(4)
v_mfma_f32_32x32x16_bf16 v[32:47],a[96:99],v[160:163],v[32:47]
ds_read_b128 v[128:131],v4 offset:4608
v_rcp_f32 v83,v83
v_mul_f32 v48,v48,v76
v_mfma_f32_32x32x16_bf16 v[32:47],a[100:103],v[164:167],v[32:47]
ds_read_b128 v[132:135],v4 offset:5120
v_mul_f32 v49,v49,v77
v_mul_f32 v50,v50,v78
v_mul_f32 v51,v51,v79
v_mul_f32 v52,v52,v80
v_mul_f32 v53,v53,v81
v_mfma_f32_32x32x16_bf16 v[32:47],a[104:107],v[168:171],v[32:47]
ds_read_b128 v[136:139],v4 offset:13824
s_add_u32 m0,s64,78848
v_mul_f32 v54,v54,v82
buffer_load_dwordx4 v24,s[32:35],s63 offen offset:0 sc1 lds
v_mfma_f32_32x32x16_bf16 v[32:47],a[108:111],v[172:175],v[32:47]
ds_read_b128 v[140:143],v4 offset:14336
v_mul_f32 v55,v55,v83
v_cvt_pk_bf16_f32 v84,v48,v49
v_cvt_pk_bf16_f32 v85,v50,v51
v_cvt_pk_bf16_f32 v86,v52,v53
s_waitcnt lgkmcnt(4)
v_mfma_f32_32x32x16_bf16 v[32:47],a[112:115],v[176:179],v[32:47]
ds_read_b128 v[144:147],v4 offset:23040
v_cvt_pk_bf16_f32 v87,v54,v55
ds_read2st64_b32 v[64:65],v8 offset0:0 offset1:36
ds_read2st64_b32 v[66:67],v8 offset0:72 offset1:108
ds_read2st64_b32 v[68:69],v8 offset0:144 offset1:180
ds_read2st64_b32 v[70:71],v8 offset0:216 offset1:252
v_mfma_f32_32x32x16_bf16 v[32:47],a[116:119],v[180:183],v[32:47]
ds_read_b128 v[148:151],v4 offset:23552
v_xor_b32 v8,v9,v8
v_mfma_f32_32x32x16_bf16 v[32:47],a[120:123],v[184:187],v[32:47]
buffer_store_dwordx4 v[84:87],v15,s[32:35],s68 offen sc1
ds_read_b128 v[152:155],v4 offset:32256
v_mfma_f32_32x32x16_bf16 v[32:47],a[124:127],v[188:191],v[32:47]
ds_read_b128 v[156:159],v4 offset:32768
s_waitcnt lgkmcnt(8)
v_mfma_f32_32x32x16_bf16 v[32:47],a[128:131],v[128:131],v[32:47]
buffer_load_dwordx4 v24,s[32:35],s63 offen offset:1024 sc1 lds
ds_read_b128 v[160:163],v4 offset:5760
v_mfma_f32_32x32x16_bf16 v[32:47],a[132:135],v[132:135],v[32:47]
ds_read_b128 v[164:167],v4 offset:6272
s_waitcnt lgkmcnt(5)
v_add_f32 v64,v64,v65
v_add_f32 v66,v66,v67
v_add_f32 v68,v68,v69
v_add_f32 v70,v70,v71
v_mfma_f32_32x32x16_bf16 v[32:47],a[136:139],v[136:139],v[32:47]
buffer_load_dwordx4 v24,s[32:35],s63 offen offset:2048 sc1 lds
ds_read_b128 v[168:171],v4 offset:14976
v_add_f32 v66,v64,v66
v_add_f32 v68,v68,v70
v_mfma_f32_32x32x16_bf16 v[32:47],a[140:143],v[140:143],v[32:47]
ds_read_b128 v[172:175],v4 offset:15488
v_add_f32_dpp v64,v66,v66 row_ror:8 row_mask:0xf bank_mask:0x3
s_waitcnt lgkmcnt(4)
v_mfma_f32_32x32x16_bf16 v[32:47],a[144:147],v[144:147],v[32:47]
buffer_load_dwordx2 v[12:13],v14,s[32:35],s66 offen sc1
ds_read_b128 v[176:179],v4 offset:24192
v_add_f32_dpp v64,v68,v68 row_ror:8 row_mask:0xf bank_mask:0xc
v_mfma_f32_32x32x16_bf16 v[32:47],a[148:151],v[148:151],v[32:47]
ds_read_b128 v[180:183],v4 offset:24704
v_add_f32_dpp v64,v64,v64 row_ror:12 row_mask:0xf bank_mask:0xf
v_mfma_f32_32x32x16_bf16 v[32:47],a[152:155],v[152:155],v[32:47]
buffer_load_dwordx4 v24,s[32:35],s63 offen offset:3072 sc1 lds
ds_read_b128 v[184:187],v4 offset:33408
v_add_f32_dpp v64,v64,v64 quad_perm:[2,3,0,1] row_mask:0xf bank_mask:0xf
v_mfma_f32_32x32x16_bf16 v[32:47],a[156:159],v[156:159],v[32:47]
ds_read_b128 v[188:191],v4 offset:33920
s_add_u32 m0,s64,115712
v_mul_f32 v64,0x3a800000,v64
s_waitcnt lgkmcnt(4)
v_mfma_f32_32x32x16_bf16 v[32:47],a[160:163],v[160:163],v[32:47]
buffer_load_dwordx4 v25,s[32:35],s63 offen offset:0 sc1 lds
ds_read_b128 v[128:131],v4 offset:6912
v_mul_f32 v65,v64,v64
v_mfma_f32_32x32x16_bf16 v[32:47],a[164:167],v[164:167],v[32:47]
ds_read_b128 v[132:135],v4 offset:7424
v_sub_f32_dpp v65,v64,v65 quad_perm:[1,1,3,3] row_mask:0xf bank_mask:0xf
v_mfma_f32_32x32x16_bf16 v[32:47],a[168:171],v[168:171],v[32:47]
ds_read_b128 v[136:139],v4 offset:16128
v_max_f32 v65,0,v65
v_mfma_f32_32x32x16_bf16 v[32:47],a[172:175],v[172:175],v[32:47]
ds_read_b128 v[140:143],v4 offset:16640
v_cvt_pk_bf16_f32 v65,0,v65
s_waitcnt lgkmcnt(4)
v_mfma_f32_32x32x16_bf16 v[32:47],a[176:179],v[176:179],v[32:47]
buffer_load_dwordx4 v25,s[32:35],s63 offen offset:1024 sc1 lds
ds_read_b128 v[144:147],v4 offset:25344
v_add_f32 v65,0x3727c5ac,v65
v_mfma_f32_32x32x16_bf16 v[32:47],a[180:183],v[180:183],v[32:47]
ds_read_b128 v[148:151],v4 offset:25856
v_cvt_pk_bf16_f32 v65,0,v65
v_mfma_f32_32x32x16_bf16 v[32:47],a[184:187],v[184:187],v[32:47]
buffer_load_dwordx4 v25,s[32:35],s63 offen offset:2048 sc1 lds
ds_read_b128 v[152:155],v4 offset:34560
v_sqrt_f32 v65,v65
v_mfma_f32_32x32x16_bf16 v[32:47],a[188:191],v[188:191],v[32:47]
ds_read_b128 v[156:159],v4 offset:35072
v_cvt_pk_bf16_f32 v65,v64,v65
s_waitcnt lgkmcnt(4)
v_mfma_f32_32x32x16_bf16 v[32:47],a[192:195],v[128:131],v[32:47]
ds_read_b128 v[160:163],v4 offset:8064
ds_write_b32 v10,v65 offset:0
v_mfma_f32_32x32x16_bf16 v[32:47],a[196:199],v[132:135],v[32:47]
ds_read_b128 v[164:167],v4 offset:8576
v_mfma_f32_32x32x16_bf16 v[32:47],a[200:203],v[136:139],v[32:47]
buffer_load_dwordx4 v25,s[32:35],s63 offen offset:3072 sc1 lds
ds_read_b128 v[168:171],v4 offset:17280
s_add_u32 s63,s63,1187840
v_mfma_f32_32x32x16_bf16 v[32:47],a[204:207],v[140:143],v[32:47]
ds_read_b128 v[172:175],v4 offset:17792
s_waitcnt lgkmcnt(5)
v_mfma_f32_32x32x16_bf16 v[32:47],a[208:211],v[144:147],v[32:47]
ds_read_b128 v[176:179],v4 offset:26496
v_mfma_f32_32x32x16_bf16 v[32:47],a[212:215],v[148:151],v[32:47]
ds_read_b128 v[180:183],v4 offset:27008
s_waitcnt vmcnt(8)
s_waitcnt lgkmcnt(5)
s_barrier
v_mfma_f32_32x32x16_bf16 v[32:47],a[216:219],v[152:155],v[32:47]
ds_read_b128 v[184:187],v4 offset:35712
v_mfma_f32_32x32x16_bf16 v[32:47],a[220:223],v[156:159],v[32:47]
ds_read_b128 v[188:191],v4 offset:36224
s_waitcnt lgkmcnt(4)
v_mfma_f32_32x32x16_bf16 v[32:47],a[224:227],v[160:163],v[32:47]
ds_read_b128 v[128:131],v5 offset:0
v_mfma_f32_32x32x16_bf16 v[32:47],a[228:231],v[164:167],v[32:47]
ds_read_b128 v[132:135],v5 offset:512
ds_read_b32 v74,v11 offset:0
v_mfma_f32_32x32x16_bf16 v[32:47],a[232:235],v[168:171],v[32:47]
ds_read_b128 v[136:139],v5 offset:9216
v_mfma_f32_32x32x16_bf16 v[32:47],a[236:239],v[172:175],v[32:47]
ds_read_b128 v[140:143],v5 offset:9728
s_waitcnt lgkmcnt(5)
v_mfma_f32_32x32x16_bf16 v[32:47],a[240:243],v[176:179],v[32:47]
ds_read_b128 v[144:147],v5 offset:18432
s_waitcnt lgkmcnt(3)
v_lshlrev_b32 v72,16,v74
v_and_b32 v74,0xffff0000,v74
v_mfma_f32_32x32x16_bf16 v[32:47],a[244:247],v[180:183],v[32:47]
ds_read_b128 v[148:151],v5 offset:18944
v_rcp_f32 v73,v74
v_mul_f32 v74,v74,v74
v_mfma_f32_32x32x16_bf16 v[32:47],a[248:251],v[184:187],v[32:47]
ds_read_b128 v[152:155],v5 offset:27648
v_mul_f32 v73,0xbfb8aa3b,v73
v_mfma_f32_32x32x16_bf16 v[32:47],a[252:255],v[188:191],v[32:47]
ds_read_b128 v[156:159],v5 offset:28160
s_waitcnt lgkmcnt(4)
v_mfma_f32_32x32x16_bf16 v[48:63],a[0:3],v[128:131],0
ds_read_b128 v[160:163],v5 offset:1152
s_waitcnt vmcnt(5)
v_cmp_ne_u64 vcc,v[16:17],v[12:13]
s_add_u32 s68,s68,1187840
v_mfma_f32_32x32x16_bf16 v[48:63],a[4:7],v[132:135],v[48:63]
ds_read_b128 v[164:167],v5 offset:1664
s_cbranch_vccnz .L_rth1_3
.L_bkh1_3:
s_add_u32 s66,s66,1187840
v_mfma_f32_32x32x16_bf16 v[48:63],a[8:11],v[136:139],v[48:63]
ds_read_b128 v[168:171],v5 offset:10368
s_add_u32 m0,s64,0
v_fma_f32 v32,-v72,v112,v32
buffer_load_dwordx4 v20,s[32:35],s63 offen offset:0 sc1 lds
v_mfma_f32_32x32x16_bf16 v[48:63],a[12:15],v[140:143],v[48:63]
ds_read_b128 v[172:175],v5 offset:10880
v_fma_f32 v33,-v72,v113,v33
v_fma_f32 v34,-v72,v114,v34
v_fma_f32 v35,-v72,v115,v35
v_fma_f32 v36,-v72,v116,v36
s_waitcnt lgkmcnt(4)
v_mfma_f32_32x32x16_bf16 v[48:63],a[16:19],v[144:147],v[48:63]
buffer_load_dwordx4 v20,s[32:35],s63 offen offset:1024 sc1 lds
ds_read_b128 v[176:179],v5 offset:19584
v_fma_f32 v37,-v72,v117,v37
v_fma_f32 v38,-v72,v118,v38
v_mfma_f32_32x32x16_bf16 v[48:63],a[20:23],v[148:151],v[48:63]
ds_read_b128 v[180:183],v5 offset:20096
v_fma_f32 v39,-v72,v119,v39
v_fma_f32 v40,-v72,v120,v40
v_fma_f32 v41,-v72,v121,v41
v_fma_f32 v42,-v72,v122,v42
v_fma_f32 v43,-v72,v123,v43
v_mfma_f32_32x32x16_bf16 v[48:63],a[24:27],v[152:155],v[48:63]
buffer_load_dwordx4 v20,s[32:35],s63 offen offset:2048 sc1 lds
ds_read_b128 v[184:187],v5 offset:28800
v_fma_f32 v44,-v72,v124,v44
v_fma_f32 v45,-v72,v125,v45
v_mfma_f32_32x32x16_bf16 v[48:63],a[28:31],v[156:159],v[48:63]
ds_read_b128 v[188:191],v5 offset:29312
v_fma_f32 v46,-v72,v126,v46
v_fma_f32 v47,-v72,v127,v47
v_mul_f32 v76,v32,v73
v_mul_f32 v77,v33,v73
s_waitcnt lgkmcnt(4)
v_mfma_f32_32x32x16_bf16 v[48:63],a[32:35],v[160:163],v[48:63]
buffer_load_dwordx4 v20,s[32:35],s63 offen offset:3072 sc1 lds
ds_read_b128 v[128:131],v5 offset:2304
v_exp_f32 v76,v76
v_mfma_f32_32x32x16_bf16 v[48:63],a[36:39],v[164:167],v[48:63]
ds_read_b128 v[132:135],v5 offset:2816
v_exp_f32 v77,v77
v_mul_f32 v78,v34,v73
v_mul_f32 v79,v35,v73
v_mul_f32 v80,v36,v73
v_mfma_f32_32x32x16_bf16 v[48:63],a[40:43],v[168:171],v[48:63]
ds_read_b128 v[136:139],v5 offset:11520
s_add_u32 m0,s64,36864
v_exp_f32 v78,v78
v_exp_f32 v79,v79
v_mfma_f32_32x32x16_bf16 v[48:63],a[44:47],v[172:175],v[48:63]
ds_read_b128 v[140:143],v5 offset:12032
v_exp_f32 v80,v80
v_mul_f32 v81,v37,v73
v_mul_f32 v82,v38,v73
s_waitcnt lgkmcnt(4)
v_mfma_f32_32x32x16_bf16 v[48:63],a[48:51],v[176:179],v[48:63]
buffer_load_dwordx4 v21,s[32:35],s63 offen offset:0 sc1 lds
ds_read_b128 v[144:147],v5 offset:20736
v_exp_f32 v81,v81
v_mfma_f32_32x32x16_bf16 v[48:63],a[52:55],v[180:183],v[48:63]
ds_read_b128 v[148:151],v5 offset:21248
v_exp_f32 v82,v82
v_mul_f32 v83,v39,v73
v_mul_f32 v32,v32,v40
v_mul_f32 v33,v33,v41
v_mfma_f32_32x32x16_bf16 v[48:63],a[56:59],v[184:187],v[48:63]
buffer_load_dwordx4 v21,s[32:35],s63 offen offset:1024 sc1 lds
ds_read_b128 v[152:155],v5 offset:29952
v_exp_f32 v83,v83
v_mfma_f32_32x32x16_bf16 v[48:63],a[60:63],v[188:191],v[48:63]
ds_read_b128 v[156:159],v5 offset:30464
v_mul_f32 v34,v34,v42
v_mul_f32 v35,v35,v43
v_mul_f32 v36,v36,v44
v_mul_f32 v37,v37,v45
s_waitcnt lgkmcnt(4)
v_mfma_f32_32x32x16_bf16 v[48:63],a[64:67],v[128:131],v[48:63]
buffer_load_dwordx4 v21,s[32:35],s63 offen offset:2048 sc1 lds
ds_read_b128 v[160:163],v5 offset:3456
v_mul_f32 v38,v38,v46
v_mul_f32 v39,v39,v47
v_mfma_f32_32x32x16_bf16 v[48:63],a[68:71],v[132:135],v[48:63]
ds_read_b128 v[164:167],v5 offset:3968
v_fma_f32 v76,v76,v74,v74
v_fma_f32 v77,v77,v74,v74
v_rcp_f32 v76,v76
v_fma_f32 v78,v78,v74,v74
v_mfma_f32_32x32x16_bf16 v[48:63],a[72:75],v[136:139],v[48:63]
buffer_load_dwordx4 v21,s[32:35],s63 offen offset:3072 sc1 lds
ds_read_b128 v[168:171],v5 offset:12672
s_add_u32 m0,s64,4096
v_fma_f32 v79,v79,v74,v74
v_mfma_f32_32x32x16_bf16 v[48:63],a[76:79],v[140:143],v[48:63]
ds_read_b128 v[172:175],v5 offset:13184
v_rcp_f32 v77,v77
v_rcp_f32 v78,v78
s_waitcnt lgkmcnt(4)
v_mfma_f32_32x32x16_bf16 v[48:63],a[80:83],v[144:147],v[48:63]
buffer_load_dwordx4 v22,s[32:35],s63 offen offset:0 sc1 lds
ds_read_b128 v[176:179],v5 offset:21888
s_add_u32 m0,s64,40960
v_fma_f32 v80,v80,v74,v74
v_mfma_f32_32x32x16_bf16 v[48:63],a[84:87],v[148:151],v[48:63]
ds_read_b128 v[180:183],v5 offset:22400
v_rcp_f32 v79,v79
v_rcp_f32 v80,v80
v_fma_f32 v81,v81,v74,v74
s_waitcnt vmcnt(9)
s_barrier
v_mfma_f32_32x32x16_bf16 v[48:63],a[88:91],v[152:155],v[48:63]
buffer_load_dwordx4 v23,s[32:35],s63 offen offset:0 sc1 lds
ds_read_b128 v[184:187],v5 offset:31104
v_rcp_f32 v81,v81
v_mfma_f32_32x32x16_bf16 v[48:63],a[92:95],v[156:159],v[48:63]
ds_read_b128 v[188:191],v5 offset:31616
v_fma_f32 v82,v82,v74,v74
v_fma_f32 v83,v83,v74,v74
v_rcp_f32 v82,v82
s_waitcnt lgkmcnt(4)
v_mfma_f32_32x32x16_bf16 v[48:63],a[96:99],v[160:163],v[48:63]
ds_read_b128 v[128:131],v5 offset:4608
v_rcp_f32 v83,v83
v_mul_f32 v32,v32,v76
v_mfma_f32_32x32x16_bf16 v[48:63],a[100:103],v[164:167],v[48:63]
ds_read_b128 v[132:135],v5 offset:5120
v_mul_f32 v33,v33,v77
v_mul_f32 v34,v34,v78
v_mul_f32 v35,v35,v79
v_mul_f32 v36,v36,v80
v_mul_f32 v37,v37,v81
v_mfma_f32_32x32x16_bf16 v[48:63],a[104:107],v[168:171],v[48:63]
ds_read_b128 v[136:139],v5 offset:13824
s_add_u32 m0,s64,5120
v_mul_f32 v38,v38,v82
buffer_load_dwordx4 v24,s[32:35],s63 offen offset:0 sc1 lds
v_mfma_f32_32x32x16_bf16 v[48:63],a[108:111],v[172:175],v[48:63]
ds_read_b128 v[140:143],v5 offset:14336
v_mul_f32 v39,v39,v83
v_cvt_pk_bf16_f32 v84,v32,v33
v_cvt_pk_bf16_f32 v85,v34,v35
v_cvt_pk_bf16_f32 v86,v36,v37
s_waitcnt lgkmcnt(4)
v_mfma_f32_32x32x16_bf16 v[48:63],a[112:115],v[176:179],v[48:63]
ds_read_b128 v[144:147],v5 offset:23040
v_cvt_pk_bf16_f32 v87,v38,v39
ds_read2st64_b32 v[64:65],v8 offset0:0 offset1:36
ds_read2st64_b32 v[66:67],v8 offset0:72 offset1:108
ds_read2st64_b32 v[68:69],v8 offset0:144 offset1:180
ds_read2st64_b32 v[70:71],v8 offset0:216 offset1:252
v_mfma_f32_32x32x16_bf16 v[48:63],a[116:119],v[180:183],v[48:63]
ds_read_b128 v[148:151],v5 offset:23552
v_xor_b32 v8,v9,v8
v_mfma_f32_32x32x16_bf16 v[48:63],a[120:123],v[184:187],v[48:63]
buffer_store_dwordx4 v[84:87],v15,s[32:35],s68 offen sc1
ds_read_b128 v[152:155],v5 offset:32256
v_mfma_f32_32x32x16_bf16 v[48:63],a[124:127],v[188:191],v[48:63]
ds_read_b128 v[156:159],v5 offset:32768
s_waitcnt lgkmcnt(8)
v_mfma_f32_32x32x16_bf16 v[48:63],a[128:131],v[128:131],v[48:63]
buffer_load_dwordx4 v24,s[32:35],s63 offen offset:1024 sc1 lds
ds_read_b128 v[160:163],v5 offset:5760
v_mfma_f32_32x32x16_bf16 v[48:63],a[132:135],v[132:135],v[48:63]
ds_read_b128 v[164:167],v5 offset:6272
s_waitcnt lgkmcnt(5)
v_add_f32 v64,v64,v65
v_add_f32 v66,v66,v67
v_add_f32 v68,v68,v69
v_add_f32 v70,v70,v71
v_mfma_f32_32x32x16_bf16 v[48:63],a[136:139],v[136:139],v[48:63]
buffer_load_dwordx4 v24,s[32:35],s63 offen offset:2048 sc1 lds
ds_read_b128 v[168:171],v5 offset:14976
v_add_f32 v66,v64,v66
v_add_f32 v68,v68,v70
v_mfma_f32_32x32x16_bf16 v[48:63],a[140:143],v[140:143],v[48:63]
ds_read_b128 v[172:175],v5 offset:15488
v_add_f32_dpp v64,v66,v66 row_ror:8 row_mask:0xf bank_mask:0x3
s_waitcnt lgkmcnt(4)
v_mfma_f32_32x32x16_bf16 v[48:63],a[144:147],v[144:147],v[48:63]
)#"
R"#(
buffer_load_dwordx2 v[12:13],v14,s[32:35],s66 offen sc1
ds_read_b128 v[176:179],v5 offset:24192
v_add_f32_dpp v64,v68,v68 row_ror:8 row_mask:0xf bank_mask:0xc
v_mfma_f32_32x32x16_bf16 v[48:63],a[148:151],v[148:151],v[48:63]
ds_read_b128 v[180:183],v5 offset:24704
v_add_f32_dpp v64,v64,v64 row_ror:12 row_mask:0xf bank_mask:0xf
v_mfma_f32_32x32x16_bf16 v[48:63],a[152:155],v[152:155],v[48:63]
buffer_load_dwordx4 v24,s[32:35],s63 offen offset:3072 sc1 lds
ds_read_b128 v[184:187],v5 offset:33408
v_add_f32_dpp v64,v64,v64 quad_perm:[2,3,0,1] row_mask:0xf bank_mask:0xf
v_mfma_f32_32x32x16_bf16 v[48:63],a[156:159],v[156:159],v[48:63]
ds_read_b128 v[188:191],v5 offset:33920
s_add_u32 m0,s64,41984
v_mul_f32 v64,0x3a800000,v64
s_waitcnt lgkmcnt(4)
v_mfma_f32_32x32x16_bf16 v[48:63],a[160:163],v[160:163],v[48:63]
buffer_load_dwordx4 v25,s[32:35],s63 offen offset:0 sc1 lds
ds_read_b128 v[128:131],v5 offset:6912
v_mul_f32 v65,v64,v64
v_mfma_f32_32x32x16_bf16 v[48:63],a[164:167],v[164:167],v[48:63]
ds_read_b128 v[132:135],v5 offset:7424
v_sub_f32_dpp v65,v64,v65 quad_perm:[1,1,3,3] row_mask:0xf bank_mask:0xf
v_mfma_f32_32x32x16_bf16 v[48:63],a[168:171],v[168:171],v[48:63]
ds_read_b128 v[136:139],v5 offset:16128
v_max_f32 v65,0,v65
v_mfma_f32_32x32x16_bf16 v[48:63],a[172:175],v[172:175],v[48:63]
ds_read_b128 v[140:143],v5 offset:16640
v_cvt_pk_bf16_f32 v65,0,v65
s_waitcnt lgkmcnt(4)
v_mfma_f32_32x32x16_bf16 v[48:63],a[176:179],v[176:179],v[48:63]
buffer_load_dwordx4 v25,s[32:35],s63 offen offset:1024 sc1 lds
ds_read_b128 v[144:147],v5 offset:25344
v_add_f32 v65,0x3727c5ac,v65
v_mfma_f32_32x32x16_bf16 v[48:63],a[180:183],v[180:183],v[48:63]
ds_read_b128 v[148:151],v5 offset:25856
v_cvt_pk_bf16_f32 v65,0,v65
v_mfma_f32_32x32x16_bf16 v[48:63],a[184:187],v[184:187],v[48:63]
buffer_load_dwordx4 v25,s[32:35],s63 offen offset:2048 sc1 lds
ds_read_b128 v[152:155],v5 offset:34560
v_sqrt_f32 v65,v65
v_mfma_f32_32x32x16_bf16 v[48:63],a[188:191],v[188:191],v[48:63]
ds_read_b128 v[156:159],v5 offset:35072
v_cvt_pk_bf16_f32 v65,v64,v65
s_waitcnt lgkmcnt(4)
v_mfma_f32_32x32x16_bf16 v[48:63],a[192:195],v[128:131],v[48:63]
ds_read_b128 v[160:163],v5 offset:8064
ds_write_b32 v10,v65 offset:128
v_mfma_f32_32x32x16_bf16 v[48:63],a[196:199],v[132:135],v[48:63]
ds_read_b128 v[164:167],v5 offset:8576
v_mfma_f32_32x32x16_bf16 v[48:63],a[200:203],v[136:139],v[48:63]
buffer_load_dwordx4 v25,s[32:35],s63 offen offset:3072 sc1 lds
ds_read_b128 v[168:171],v5 offset:17280
s_add_u32 s63,s63,1187840
v_mfma_f32_32x32x16_bf16 v[48:63],a[204:207],v[140:143],v[48:63]
ds_read_b128 v[172:175],v5 offset:17792
s_waitcnt lgkmcnt(5)
v_mfma_f32_32x32x16_bf16 v[48:63],a[208:211],v[144:147],v[48:63]
ds_read_b128 v[176:179],v5 offset:26496
v_mfma_f32_32x32x16_bf16 v[48:63],a[212:215],v[148:151],v[48:63]
ds_read_b128 v[180:183],v5 offset:27008
s_waitcnt vmcnt(8)
s_waitcnt lgkmcnt(5)
s_barrier
v_mfma_f32_32x32x16_bf16 v[48:63],a[216:219],v[152:155],v[48:63]
ds_read_b128 v[184:187],v5 offset:35712
v_mfma_f32_32x32x16_bf16 v[48:63],a[220:223],v[156:159],v[48:63]
ds_read_b128 v[188:191],v5 offset:36224
s_waitcnt lgkmcnt(4)
v_mfma_f32_32x32x16_bf16 v[48:63],a[224:227],v[160:163],v[48:63]
ds_read_b128 v[128:131],v4 offset:0
v_mfma_f32_32x32x16_bf16 v[48:63],a[228:231],v[164:167],v[48:63]
ds_read_b128 v[132:135],v4 offset:512
ds_read_b32 v74,v11 offset:128
v_mfma_f32_32x32x16_bf16 v[48:63],a[232:235],v[168:171],v[48:63]
ds_read_b128 v[136:139],v4 offset:9216
v_mfma_f32_32x32x16_bf16 v[48:63],a[236:239],v[172:175],v[48:63]
ds_read_b128 v[140:143],v4 offset:9728
s_waitcnt lgkmcnt(5)
v_mfma_f32_32x32x16_bf16 v[48:63],a[240:243],v[176:179],v[48:63]
ds_read_b128 v[144:147],v4 offset:18432
s_waitcnt lgkmcnt(3)
v_lshlrev_b32 v72,16,v74
v_and_b32 v74,0xffff0000,v74
v_mfma_f32_32x32x16_bf16 v[48:63],a[244:247],v[180:183],v[48:63]
ds_read_b128 v[148:151],v4 offset:18944
v_rcp_f32 v73,v74
v_mul_f32 v74,v74,v74
v_mfma_f32_32x32x16_bf16 v[48:63],a[248:251],v[184:187],v[48:63]
ds_read_b128 v[152:155],v4 offset:27648
v_mul_f32 v73,0xbfb8aa3b,v73
s_sub_u32 s62,s62,1
v_mfma_f32_32x32x16_bf16 v[48:63],a[252:255],v[188:191],v[48:63]
ds_read_b128 v[156:159],v4 offset:28160
s_cbranch_scc0 .L_hl_3
s_nop 1
s_add_u32 s68,s68,1187840
s_nop 6
v_fma_f32 v48,-v72,v112,v48
v_fma_f32 v49,-v72,v113,v49
v_fma_f32 v50,-v72,v114,v50
v_fma_f32 v51,-v72,v115,v51
v_fma_f32 v52,-v72,v116,v52
v_fma_f32 v53,-v72,v117,v53
v_fma_f32 v54,-v72,v118,v54
v_fma_f32 v55,-v72,v119,v55
v_fma_f32 v56,-v72,v120,v56
v_fma_f32 v57,-v72,v121,v57
v_fma_f32 v58,-v72,v122,v58
v_fma_f32 v59,-v72,v123,v59
v_fma_f32 v60,-v72,v124,v60
v_fma_f32 v61,-v72,v125,v61
v_fma_f32 v62,-v72,v126,v62
v_fma_f32 v63,-v72,v127,v63
v_mul_f32 v76,v48,v73
v_mul_f32 v77,v49,v73
v_mul_f32 v78,v50,v73
v_mul_f32 v79,v51,v73
v_mul_f32 v80,v52,v73
v_mul_f32 v81,v53,v73
v_mul_f32 v82,v54,v73
v_mul_f32 v83,v55,v73
v_exp_f32 v76,v76
v_exp_f32 v77,v77
v_mul_f32 v48,v48,v56
v_mul_f32 v49,v49,v57
v_exp_f32 v78,v78
v_exp_f32 v79,v79
v_mul_f32 v50,v50,v58
v_mul_f32 v51,v51,v59
v_exp_f32 v80,v80
v_exp_f32 v81,v81
v_mul_f32 v52,v52,v60
v_mul_f32 v53,v53,v61
v_exp_f32 v82,v82
v_exp_f32 v83,v83
v_mul_f32 v54,v54,v62
v_mul_f32 v55,v55,v63
v_fma_f32 v76,v76,v74,v74
v_fma_f32 v77,v77,v74,v74
v_rcp_f32 v76,v76
v_rcp_f32 v77,v77
v_fma_f32 v78,v78,v74,v74
v_fma_f32 v79,v79,v74,v74
v_rcp_f32 v78,v78
v_rcp_f32 v79,v79
v_fma_f32 v80,v80,v74,v74
v_fma_f32 v81,v81,v74,v74
v_rcp_f32 v80,v80
v_rcp_f32 v81,v81
v_fma_f32 v82,v82,v74,v74
v_fma_f32 v83,v83,v74,v74
v_rcp_f32 v82,v82
v_rcp_f32 v83,v83
v_mul_f32 v48,v48,v76
v_mul_f32 v49,v49,v77
v_mul_f32 v50,v50,v78
v_mul_f32 v51,v51,v79
v_mul_f32 v52,v52,v80
v_mul_f32 v53,v53,v81
v_mul_f32 v54,v54,v82
v_mul_f32 v55,v55,v83
v_cvt_pk_bf16_f32 v84,v48,v49
v_cvt_pk_bf16_f32 v85,v50,v51
v_cvt_pk_bf16_f32 v86,v52,v53
v_cvt_pk_bf16_f32 v87,v54,v55
buffer_store_dwordx4 v[84:87],v15,s[32:35],s68 offen sc1
s_waitcnt vmcnt(0)
s_barrier
buffer_store_dwordx2 v[16:17],v18,s[32:35],s68 offen sc1
s_cbranch_execz .L_hbk
s_branch .L_end
.L_rtg0_1:
s_mov_b32 s48,20000
.L_rlg0_1:
s_sleep 8
.L_rfg0_1:
buffer_load_dwordx2 v[12:13],v14,s[32:35],s66 offen sc1
s_sub_u32 s48,s48,1
s_cbranch_scc1 .L_end
s_waitcnt vmcnt(0)
v_cmp_ne_u64 vcc,v[16:17],v[12:13]
s_cbranch_vccnz .L_rlg0_1
s_branch .L_bkg0_1
.L_rtg1_2:
s_mov_b32 s48,20000
.L_rlg1_2:
s_sleep 8
.L_rfg1_2:
buffer_load_dwordx2 v[12:13],v14,s[32:35],s66 offen sc1
s_sub_u32 s48,s48,1
s_cbranch_scc1 .L_end
s_waitcnt vmcnt(0)
v_cmp_ne_u64 vcc,v[16:17],v[12:13]
s_cbranch_vccnz .L_rlg1_2
s_branch .L_bkg1_2
.L_rtqe:
s_mov_b32 s48,20000
s_branch .L_rfqe
.L_rlqe:
s_sleep 8
.L_rfqe:
buffer_load_dwordx2 v[60:61],v56,s[32:35],s66 offen sc1
s_sub_u32 s48,s48,1
s_cbranch_scc1 .L_end
s_waitcnt vmcnt(0)
v_cmp_ne_u64 vcc,s[54:55],v[60:61]
s_cbranch_vccnz .L_rlqe
s_branch .L_bkqe
.L_rtq0:
s_mov_b32 s48,20000
.L_rlq0:
s_sleep 8
.L_rfq0:
buffer_load_dwordx2 v[12:13],v0,s[32:35],s66 offen sc1
s_sub_u32 s48,s48,1
s_cbranch_scc1 .L_end
s_waitcnt vmcnt(0)
v_cmp_ne_u64 vcc,v[30:31],v[12:13]
s_cbranch_vccnz .L_rlq0
s_branch .L_bkq0
.L_rtq1:
s_mov_b32 s48,20000
.L_rlq1:
s_sleep 8
.L_rfq1:
buffer_load_dwordx2 v[12:13],v0,s[32:35],s66 offen sc1
s_sub_u32 s48,s48,1
s_cbranch_scc1 .L_end
s_waitcnt vmcnt(0)
v_cmp_ne_u64 vcc,v[30:31],v[12:13]
s_cbranch_vccnz .L_rlq1
s_branch .L_bkq1
.L_qs0:
s_cmp_lg_u32 s62,30
s_cbranch_scc1 .L_qn4
v_mov_b32 v120,0xff61b1e6
v_mov_b32 v121,0xff61b1e6
v_mov_b32 v122,0xff61b1e6
v_mov_b32 v123,0xff61b1e6
v_cmp_ne_u32 vcc,0xffff,v126
s_nop 1
v_cndmask_b32 v120,0,v120,vcc
v_cmp_ne_u32 vcc,0xffff0000,v126
s_nop 1
v_cndmask_b32 v121,0,v121,vcc
v_cmp_ne_u32 vcc,0xffff,v127
s_nop 1
v_cndmask_b32 v122,0,v122,vcc
v_cmp_ne_u32 vcc,0xffff0000,v127
s_nop 1
v_cndmask_b32 v123,0,v123,vcc
s_nop 1
s_branch .L_qb0
.L_qn4:
s_cmp_lg_u32 s62,17
s_cbranch_scc1 .L_qn30
s_mov_b32 s72,s69
s_nop 1
s_branch .L_qb0
.L_qn30:
s_cmp_lg_u32 s62,16
s_cbranch_scc1 .L_qn32
s_mov_b32 s72,1187840
s_nop 1
s_branch .L_qb0
.L_qn32:
s_cmp_lg_u32 s62,15
s_cbranch_scc1 .L_qn34
s_sub_u32 s68,s68,39198720
s_nop 1
s_branch .L_qb0
.L_qn34:
s_cmp_lg_u32 s62,14
s_cbranch_scc1 .L_qn36
v_mov_b32 v120,0xff61b1e6
v_mov_b32 v121,0xff61b1e6
v_mov_b32 v122,0xff61b1e6
v_mov_b32 v123,0xff61b1e6
v_cmp_ne_u32 vcc,0xffff,v126
s_nop 1
v_cndmask_b32 v120,0,v120,vcc
v_cmp_ne_u32 vcc,0xffff0000,v126
s_nop 1
v_cndmask_b32 v121,0,v121,vcc
v_cmp_ne_u32 vcc,0xffff,v127
s_nop 1
v_cndmask_b32 v122,0,v122,vcc
v_cmp_ne_u32 vcc,0xffff0000,v127
s_nop 1
v_cndmask_b32 v123,0,v123,vcc
s_nop 1
s_branch .L_qb0
.L_qn36:
s_branch .L_qb0
.L_qs1:
s_cmp_lg_u32 s62,31
s_cbranch_scc1 .L_qn3
s_cmp_lg_u32 s28,0
s_cbranch_scc1 .L_qb1
v_mov_b32 v120,0xff61b1e6
v_mov_b32 v121,0xff61b1e6
v_mov_b32 v122,0xff61b1e6
v_mov_b32 v123,0xff61b1e6
s_nop 1
s_branch .L_qb1
.L_qn3:
s_cmp_lg_u32 s62,17
s_cbranch_scc1 .L_qn31
s_mov_b32 s72,s70
s_nop 1
s_branch .L_qb1
.L_qn31:
s_cmp_lg_u32 s62,15
s_cbranch_scc1 .L_qn35
s_add_u32 s68,s68,38010880
s_cmp_lg_u32 s28,0
s_cbranch_scc1 .L_qb1
v_mov_b32 v120,0xff61b1e6
v_mov_b32 v121,0xff61b1e6
v_mov_b32 v122,0xff61b1e6
v_mov_b32 v123,0xff61b1e6
s_nop 1
s_branch .L_qb1
.L_qn35:
s_branch .L_qb1
.L_rtw1_1:
s_mov_b32 s48,20000
.L_rlw1_1:
s_sleep 8
.L_rfw1_1:
buffer_load_dwordx2 v[12:13],v14,s[32:35],s66 offen sc1
s_sub_u32 s48,s48,1
s_cbranch_scc1 .L_end
s_waitcnt vmcnt(0)
v_cmp_ne_u64 vcc,v[16:17],v[12:13]
s_cbranch_vccnz .L_rlw1_1
s_branch .L_bkw1_1
.L_rth0_0:
s_mov_b32 s48,20000
.L_rlh0_0:
s_sleep 8
.L_rfh0_0:
buffer_load_dwordx2 v[12:13],v14,s[32:35],s66 offen sc1
s_sub_u32 s48,s48,1
s_cbranch_scc1 .L_end
s_waitcnt vmcnt(0)
v_cmp_ne_u64 vcc,v[16:17],v[12:13]
s_cbranch_vccnz .L_rlh0_0
s_branch .L_bkh0_0
.L_rth1_0:
s_mov_b32 s48,20000
.L_rlh1_0:
s_sleep 8
.L_rfh1_0:
buffer_load_dwordx2 v[12:13],v14,s[32:35],s66 offen sc1
s_sub_u32 s48,s48,1
s_cbranch_scc1 .L_end
s_waitcnt vmcnt(0)
v_cmp_ne_u64 vcc,v[16:17],v[12:13]
s_cbranch_vccnz .L_rlh1_0
s_branch .L_bkh1_0
.L_rth0_1:
s_mov_b32 s48,20000
.L_rlh0_1:
s_sleep 8
.L_rfh0_1:
buffer_load_dwordx2 v[12:13],v14,s[32:35],s66 offen sc1
s_sub_u32 s48,s48,1
s_cbranch_scc1 .L_end
s_waitcnt vmcnt(0)
v_cmp_ne_u64 vcc,v[16:17],v[12:13]
s_cbranch_vccnz .L_rlh0_1
s_branch .L_bkh0_1
.L_rth1_1:
s_mov_b32 s48,20000
.L_rlh1_1:
s_sleep 8
.L_rfh1_1:
buffer_load_dwordx2 v[12:13],v14,s[32:35],s66 offen sc1
s_sub_u32 s48,s48,1
s_cbranch_scc1 .L_end
s_waitcnt vmcnt(0)
v_cmp_ne_u64 vcc,v[16:17],v[12:13]
s_cbranch_vccnz .L_rlh1_1
s_branch .L_bkh1_1
.L_rth0_2:
s_mov_b32 s48,20000
.L_rlh0_2:
s_sleep 8
.L_rfh0_2:
buffer_load_dwordx2 v[12:13],v14,s[32:35],s66 offen sc1
s_sub_u32 s48,s48,1
s_cbranch_scc1 .L_end
s_waitcnt vmcnt(0)
v_cmp_ne_u64 vcc,v[16:17],v[12:13]
s_cbranch_vccnz .L_rlh0_2
s_branch .L_bkh0_2
.L_rth1_2:
s_mov_b32 s48,20000
.L_rlh1_2:
s_sleep 8
.L_rfh1_2:
buffer_load_dwordx2 v[12:13],v14,s[32:35],s66 offen sc1
s_sub_u32 s48,s48,1
s_cbranch_scc1 .L_end
s_waitcnt vmcnt(0)
v_cmp_ne_u64 vcc,v[16:17],v[12:13]
s_cbranch_vccnz .L_rlh1_2
s_branch .L_bkh1_2
.L_rth0_3:
s_mov_b32 s48,20000
.L_rlh0_3:
s_sleep 8
.L_rfh0_3:
buffer_load_dwordx2 v[12:13],v14,s[32:35],s66 offen sc1
s_sub_u32 s48,s48,1
s_cbranch_scc1 .L_end
s_waitcnt vmcnt(0)
v_cmp_ne_u64 vcc,v[16:17],v[12:13]
s_cbranch_vccnz .L_rlh0_3
s_branch .L_bkh0_3
.L_rth1_3:
s_mov_b32 s48,20000
.L_rlh1_3:
s_sleep 8
.L_rfh1_3:
buffer_load_dwordx2 v[12:13],v14,s[32:35],s66 offen sc1
s_sub_u32 s48,s48,1
s_cbranch_scc1 .L_end
s_waitcnt vmcnt(0)
v_cmp_ne_u64 vcc,v[16:17],v[12:13]
s_cbranch_vccnz .L_rlh1_3
s_branch .L_bkh1_3
.L_rtl0_1:
s_mov_b32 s48,20000
.L_rll0_1:
s_sleep 8
.L_rfl0_1:
buffer_load_dwordx2 v[6:7],v0,s[32:35],s66 offen sc1
s_sub_u32 s48,s48,1
s_cbranch_scc1 .L_end
s_waitcnt vmcnt(0)
v_cmp_ne_u64 vcc,v[8:9],v[6:7]
s_cbranch_vccnz .L_rll0_1
s_branch .L_bkl0_1
.L_rtl1_1:
s_mov_b32 s48,20000
.L_rll1_1:
s_sleep 8
.L_rfl1_1:
buffer_load_dwordx2 v[6:7],v0,s[32:35],s66 offen sc1
s_sub_u32 s48,s48,1
s_cbranch_scc1 .L_end
s_waitcnt vmcnt(0)
v_cmp_ne_u64 vcc,v[8:9],v[6:7]
s_cbranch_vccnz .L_rll1_1
s_branch .L_bkl1_1
.L_rtm1_1:
s_mov_b32 s48,20000
.L_rlm1_1:
s_sleep 8
.L_rfm1_1:
buffer_load_dwordx2 v[6:7],v0,s[32:35],s66 offen sc1
s_sub_u32 s48,s48,1
s_cbranch_scc1 .L_end
s_waitcnt vmcnt(0)
v_cmp_ne_u64 vcc,v[8:9],v[6:7]
s_cbranch_vccnz .L_rlm1_1
s_branch .L_bkm1_1
.L_rtm0_1:
s_mov_b32 s48,20000
.L_rlm0_1:
s_sleep 8
.L_rfm0_1:
buffer_load_dwordx2 v[6:7],v0,s[32:35],s66 offen sc1
s_sub_u32 s48,s48,1
s_cbranch_scc1 .L_end
s_waitcnt vmcnt(0)
v_cmp_ne_u64 vcc,v[8:9],v[6:7]
s_cbranch_vccnz .L_rlm0_1
s_branch .L_bkm0_1
.L_rta0_1:
s_mov_b32 s48,20000
.L_rla0_1:
s_sleep 8
.L_rfa0_1:
buffer_load_dwordx2 v[6:7],v0,s[32:35],s66 offen sc1
s_sub_u32 s48,s48,1
s_cbranch_scc1 .L_end
s_waitcnt vmcnt(0)
v_cmp_ne_u64 vcc,v[8:9],v[6:7]
s_cbranch_vccnz .L_rla0_1
s_branch .L_bka0_1
.L_rta1_1:
s_mov_b32 s48,20000
.L_rla1_1:
s_sleep 8
.L_rfa1_1:
buffer_load_dwordx2 v[6:7],v0,s[32:35],s66 offen sc1
s_sub_u32 s48,s48,1
)#"
R"#(
s_cbranch_scc1 .L_end
s_waitcnt vmcnt(0)
v_cmp_ne_u64 vcc,v[8:9],v[6:7]
s_cbranch_vccnz .L_rla1_1
s_branch .L_bka1_1
.L_rtb1_1:
s_mov_b32 s48,20000
.L_rlb1_1:
s_sleep 8
.L_rfb1_1:
buffer_load_dwordx2 v[6:7],v0,s[32:35],s66 offen sc1
s_sub_u32 s48,s48,1
s_cbranch_scc1 .L_end
s_waitcnt vmcnt(0)
v_cmp_ne_u64 vcc,v[8:9],v[6:7]
s_cbranch_vccnz .L_rlb1_1
s_branch .L_bkb1_1
.L_rtb0_1:
s_mov_b32 s48,20000
.L_rlb0_1:
s_sleep 8
.L_rfb0_1:
buffer_load_dwordx2 v[6:7],v0,s[32:35],s66 offen sc1
s_sub_u32 s48,s48,1
s_cbranch_scc1 .L_end
s_waitcnt vmcnt(0)
v_cmp_ne_u64 vcc,v[8:9],v[6:7]
s_cbranch_vccnz .L_rlb0_1
s_branch .L_bkb0_1
.L_end:
s_waitcnt vmcnt(0) lgkmcnt(0)
s_endpgm
.L_d:
s_lshl_b32 s65,s50,11
s_mul_i32 s48,s50,1152
s_add_u32 s48,s48,4612096
s_add_u32 s65,s65,4833280
s_cmp_eq_u32 s29,0
s_cselect_b32 s48,s65,s48
s_cselect_b32 s49,280,0
s_lshl2_add_u32 s49,s27,s49
s_waitcnt lgkmcnt(0)
s_add_u32 s54,s52,0x7f4a7c15
s_addc_u32 s55,s53,0x9e3779b9
s_lshl_b32 s67,s49,5
s_add_u32 s67,s67,4898816
s_sub_u32 s51,s67,s48
s_add_u32 s92,s32,s51
s_addc_u32 s93,s33,0x10000
s_cmp_eq_u32 s26,0
s_cselect_b32 s94,1,0
s_mov_b32 s95,0x20000
v_mov_b32 v8,s54
v_mov_b32 v9,s55
v_cmp_gt_u32 vcc,12,v1
v_mul_u32_u24 v3,296960,v1
v_mov_b32 v6,0x7fff0000
v_cndmask_b32 v3,v6,v3,vcc
s_add_u32 s51,s67,76021760
buffer_store_dwordx2 v[8:9],v3,s[32:35],s51 offen sc1
s_sub_u32 s67,s48,296960
s_mul_i32 s48,s28,296960
s_add_u32 s65,s65,s48
s_mul_i32 s96,s29,3670016
s_add_u32 s36,s18,0x0
s_addc_u32 s37,s19,0x0
s_and_b32 s37,s37,0xffff
s_mov_b32 s38,0x700000
s_mov_b32 s39,0x20000
s_add_u32 s40,s18,0xfffffc80
s_addc_u32 s41,s19,0xffffffff
s_and_b32 s41,s41,0xffff
s_mov_b32 s42,0x700380
s_mov_b32 s43,0x20000
s_lshl_b32 s48,s27,8
s_add_u32 s96,s96,s48
v_and_b32 v8,3,v1
v_mul_u32_u24 v8,16,v8
s_and_b32 s48,s26,1
s_lshl_b32 s48,s48,6
s_lshr_b32 s49,s26,1
s_lshl_b32 s49,s49,10
s_add_u32 s48,s48,s49
v_add_u32 v8,s48,v8
v_lshrrev_b32 v4,3,v1
v_bfe_u32 v5,v1,4,1
s_and_b32 s48,s26,1
s_lshl_b32 s48,s48,1
v_or_b32 v5,s48,v5
v_lshlrev_b32 v5,1,v5
v_and_b32 v6,7,v1
v_xor_b32 v5,v5,v6
v_mul_u32_u24 v4,2048,v4
v_lshl_add_u32 v7,v5,4,v4
s_mul_i32 s48,s26,16384
s_add_u32 s96,s96,s48
s_mul_i32 s97,s26,2048
s_add_u32 m0,s97,0
s_nop 0
buffer_load_dwordx4 v7,s[36:39],s96 offen offset:0 lds
buffer_load_dwordx4 v7,s[40:43],s96 offen offset:1024 lds
s_add_u32 s96,s96,65536
s_add_u32 m0,s97,8192
s_nop 0
buffer_load_dwordx4 v7,s[36:39],s96 offen offset:0 lds
buffer_load_dwordx4 v7,s[40:43],s96 offen offset:1024 lds
s_add_u32 s96,s96,65536
s_add_u32 m0,s97,16384
s_nop 0
buffer_load_dwordx4 v7,s[36:39],s96 offen offset:0 lds
buffer_load_dwordx4 v7,s[40:43],s96 offen offset:1024 lds
s_add_u32 s96,s96,65536
s_add_u32 m0,s97,24576
s_nop 0
buffer_load_dwordx4 v7,s[36:39],s96 offen offset:0 lds
buffer_load_dwordx4 v7,s[40:43],s96 offen offset:1024 lds
s_add_u32 s96,s96,65536
s_add_u32 m0,s97,32768
s_nop 0
buffer_load_dwordx4 v7,s[36:39],s96 offen offset:0 lds
buffer_load_dwordx4 v7,s[40:43],s96 offen offset:1024 lds
s_add_u32 s96,s96,65536
s_add_u32 m0,s97,40960
s_nop 0
buffer_load_dwordx4 v7,s[36:39],s96 offen offset:0 lds
buffer_load_dwordx4 v7,s[40:43],s96 offen offset:1024 lds
s_add_u32 s96,s96,65536
s_add_u32 m0,s97,49152
s_nop 0
buffer_load_dwordx4 v7,s[36:39],s96 offen offset:0 lds
buffer_load_dwordx4 v7,s[40:43],s96 offen offset:1024 lds
s_add_u32 s96,s96,65536
s_add_u32 m0,s97,57344
s_nop 0
buffer_load_dwordx4 v7,s[36:39],s96 offen offset:0 lds
buffer_load_dwordx4 v7,s[40:43],s96 offen offset:1024 lds
s_add_u32 s96,s96,65536
s_add_u32 m0,s97,65536
s_nop 0
buffer_load_dwordx4 v7,s[36:39],s96 offen offset:0 lds
buffer_load_dwordx4 v7,s[40:43],s96 offen offset:1024 lds
s_add_u32 s96,s96,65536
s_add_u32 m0,s97,73728
s_nop 0
buffer_load_dwordx4 v7,s[36:39],s96 offen offset:0 lds
buffer_load_dwordx4 v7,s[40:43],s96 offen offset:1024 lds
s_add_u32 s96,s96,65536
s_add_u32 m0,s97,81920
s_nop 0
buffer_load_dwordx4 v7,s[36:39],s96 offen offset:0 lds
buffer_load_dwordx4 v7,s[40:43],s96 offen offset:1024 lds
s_add_u32 s96,s96,65536
s_add_u32 m0,s97,90112
s_nop 0
buffer_load_dwordx4 v7,s[36:39],s96 offen offset:0 lds
buffer_load_dwordx4 v7,s[40:43],s96 offen offset:1024 lds
s_add_u32 s96,s96,65536
s_add_u32 m0,s97,98304
s_nop 0
buffer_load_dwordx4 v7,s[36:39],s96 offen offset:0 lds
buffer_load_dwordx4 v7,s[40:43],s96 offen offset:1024 lds
s_add_u32 s96,s96,65536
s_add_u32 m0,s97,106496
s_nop 0
buffer_load_dwordx4 v7,s[36:39],s96 offen offset:0 lds
buffer_load_dwordx4 v7,s[40:43],s96 offen offset:1024 lds
s_add_u32 s96,s96,65536
v_lshrrev_b32 v5,4,v1
v_bfe_u32 v4,v1,3,1
v_and_b32 v6,1,v5
v_lshl_add_u32 v4,v6,1,v4
v_lshlrev_b32 v4,5,v4
v_mul_u32_u24 v5,2048,v5
v_bfe_u32 v6,v1,2,2
v_lshl_add_u32 v5,v6,7,v5
v_xor_b32 v8,v4,v8
v_add_u32 v8,v5,v8
v_add_u32 v9,65536,v8
s_waitcnt vmcnt(24)
s_barrier
s_add_u32 m0,s97,114688
s_nop 0
buffer_load_dwordx4 v7,s[36:39],s96 offen offset:0 lds
buffer_load_dwordx4 v7,s[40:43],s96 offen offset:1024 lds
s_add_u32 s96,s96,65536
s_add_u32 m0,s97,122880
s_nop 0
buffer_load_dwordx4 v7,s[36:39],s96 offen offset:0 lds
buffer_load_dwordx4 v7,s[40:43],s96 offen offset:1024 lds
s_add_u32 s96,s96,65536
ds_read_b64_tr_b16 a[0:1],v8 offset:0
ds_read_b64_tr_b16 a[2:3],v8 offset:512
ds_read_b64_tr_b16 a[4:5],v8 offset:8
ds_read_b64_tr_b16 a[6:7],v8 offset:520
ds_read_b64_tr_b16 a[8:9],v8 offset:8192
ds_read_b64_tr_b16 a[10:11],v8 offset:8704
ds_read_b64_tr_b16 a[12:13],v8 offset:8200
ds_read_b64_tr_b16 a[14:15],v8 offset:8712
s_waitcnt lgkmcnt(0)
s_waitcnt vmcnt(24)
s_barrier
s_add_u32 m0,s97,0
s_nop 0
buffer_load_dwordx4 v7,s[36:39],s96 offen offset:0 lds
buffer_load_dwordx4 v7,s[40:43],s96 offen offset:1024 lds
s_add_u32 s96,s96,65536
s_add_u32 m0,s97,8192
s_nop 0
buffer_load_dwordx4 v7,s[36:39],s96 offen offset:0 lds
buffer_load_dwordx4 v7,s[40:43],s96 offen offset:1024 lds
s_add_u32 s96,s96,65536
ds_read_b64_tr_b16 a[16:17],v8 offset:16384
ds_read_b64_tr_b16 a[18:19],v8 offset:16896
ds_read_b64_tr_b16 a[20:21],v8 offset:16392
ds_read_b64_tr_b16 a[22:23],v8 offset:16904
ds_read_b64_tr_b16 a[24:25],v8 offset:24576
ds_read_b64_tr_b16 a[26:27],v8 offset:25088
ds_read_b64_tr_b16 a[28:29],v8 offset:24584
ds_read_b64_tr_b16 a[30:31],v8 offset:25096
s_waitcnt lgkmcnt(0)
s_waitcnt vmcnt(24)
s_barrier
s_add_u32 m0,s97,16384
s_nop 0
buffer_load_dwordx4 v7,s[36:39],s96 offen offset:0 lds
buffer_load_dwordx4 v7,s[40:43],s96 offen offset:1024 lds
s_add_u32 s96,s96,65536
s_add_u32 m0,s97,24576
s_nop 0
buffer_load_dwordx4 v7,s[36:39],s96 offen offset:0 lds
buffer_load_dwordx4 v7,s[40:43],s96 offen offset:1024 lds
s_add_u32 s96,s96,65536
ds_read_b64_tr_b16 a[32:33],v8 offset:32768
ds_read_b64_tr_b16 a[34:35],v8 offset:33280
ds_read_b64_tr_b16 a[36:37],v8 offset:32776
ds_read_b64_tr_b16 a[38:39],v8 offset:33288
ds_read_b64_tr_b16 a[40:41],v8 offset:40960
ds_read_b64_tr_b16 a[42:43],v8 offset:41472
ds_read_b64_tr_b16 a[44:45],v8 offset:40968
ds_read_b64_tr_b16 a[46:47],v8 offset:41480
s_waitcnt lgkmcnt(0)
s_waitcnt vmcnt(24)
s_barrier
s_add_u32 m0,s97,32768
s_nop 0
buffer_load_dwordx4 v7,s[36:39],s96 offen offset:0 lds
buffer_load_dwordx4 v7,s[40:43],s96 offen offset:1024 lds
s_add_u32 s96,s96,65536
s_add_u32 m0,s97,40960
s_nop 0
buffer_load_dwordx4 v7,s[36:39],s96 offen offset:0 lds
buffer_load_dwordx4 v7,s[40:43],s96 offen offset:1024 lds
s_add_u32 s96,s96,65536
ds_read_b64_tr_b16 a[48:49],v8 offset:49152
ds_read_b64_tr_b16 a[50:51],v8 offset:49664
ds_read_b64_tr_b16 a[52:53],v8 offset:49160
ds_read_b64_tr_b16 a[54:55],v8 offset:49672
ds_read_b64_tr_b16 a[56:57],v8 offset:57344
ds_read_b64_tr_b16 a[58:59],v8 offset:57856
ds_read_b64_tr_b16 a[60:61],v8 offset:57352
ds_read_b64_tr_b16 a[62:63],v8 offset:57864
s_waitcnt lgkmcnt(0)
s_waitcnt vmcnt(24)
s_barrier
s_add_u32 m0,s97,49152
s_nop 0
buffer_load_dwordx4 v7,s[36:39],s96 offen offset:0 lds
buffer_load_dwordx4 v7,s[40:43],s96 offen offset:1024 lds
s_add_u32 s96,s96,65536
s_add_u32 m0,s97,57344
s_nop 0
buffer_load_dwordx4 v7,s[36:39],s96 offen offset:0 lds
buffer_load_dwordx4 v7,s[40:43],s96 offen offset:1024 lds
s_add_u32 s96,s96,65536
ds_read_b64_tr_b16 a[64:65],v9 offset:0
ds_read_b64_tr_b16 a[66:67],v9 offset:512
ds_read_b64_tr_b16 a[68:69],v9 offset:8
ds_read_b64_tr_b16 a[70:71],v9 offset:520
ds_read_b64_tr_b16 a[72:73],v9 offset:8192
ds_read_b64_tr_b16 a[74:75],v9 offset:8704
ds_read_b64_tr_b16 a[76:77],v9 offset:8200
ds_read_b64_tr_b16 a[78:79],v9 offset:8712
s_waitcnt lgkmcnt(0)
s_waitcnt vmcnt(24)
s_barrier
s_add_u32 m0,s97,65536
s_nop 0
buffer_load_dwordx4 v7,s[36:39],s96 offen offset:0 lds
buffer_load_dwordx4 v7,s[40:43],s96 offen offset:1024 lds
s_add_u32 s96,s96,65536
s_add_u32 m0,s97,73728
s_nop 0
buffer_load_dwordx4 v7,s[36:39],s96 offen offset:0 lds
buffer_load_dwordx4 v7,s[40:43],s96 offen offset:1024 lds
s_add_u32 s96,s96,65536
ds_read_b64_tr_b16 a[80:81],v9 offset:16384
ds_read_b64_tr_b16 a[82:83],v9 offset:16896
ds_read_b64_tr_b16 a[84:85],v9 offset:16392
ds_read_b64_tr_b16 a[86:87],v9 offset:16904
ds_read_b64_tr_b16 a[88:89],v9 offset:24576
ds_read_b64_tr_b16 a[90:91],v9 offset:25088
ds_read_b64_tr_b16 a[92:93],v9 offset:24584
ds_read_b64_tr_b16 a[94:95],v9 offset:25096
s_waitcnt lgkmcnt(0)
s_waitcnt vmcnt(24)
s_barrier
s_add_u32 m0,s97,81920
s_nop 0
buffer_load_dwordx4 v7,s[36:39],s96 offen offset:0 lds
buffer_load_dwordx4 v7,s[40:43],s96 offen offset:1024 lds
s_add_u32 s96,s96,65536
s_add_u32 m0,s97,90112
s_nop 0
buffer_load_dwordx4 v7,s[36:39],s96 offen offset:0 lds
buffer_load_dwordx4 v7,s[40:43],s96 offen offset:1024 lds
s_add_u32 s96,s96,65536
ds_read_b64_tr_b16 a[96:97],v9 offset:32768
ds_read_b64_tr_b16 a[98:99],v9 offset:33280
ds_read_b64_tr_b16 a[100:101],v9 offset:32776
ds_read_b64_tr_b16 a[102:103],v9 offset:33288
ds_read_b64_tr_b16 a[104:105],v9 offset:40960
ds_read_b64_tr_b16 a[106:107],v9 offset:41472
ds_read_b64_tr_b16 a[108:109],v9 offset:40968
ds_read_b64_tr_b16 a[110:111],v9 offset:41480
s_waitcnt lgkmcnt(0)
s_waitcnt vmcnt(24)
s_barrier
s_add_u32 m0,s97,98304
s_nop 0
buffer_load_dwordx4 v7,s[36:39],s96 offen offset:0 lds
buffer_load_dwordx4 v7,s[40:43],s96 offen offset:1024 lds
s_add_u32 s96,s96,65536
s_add_u32 m0,s97,106496
s_nop 0
buffer_load_dwordx4 v7,s[36:39],s96 offen offset:0 lds
buffer_load_dwordx4 v7,s[40:43],s96 offen offset:1024 lds
s_add_u32 s96,s96,65536
ds_read_b64_tr_b16 a[112:113],v9 offset:49152
ds_read_b64_tr_b16 a[114:115],v9 offset:49664
ds_read_b64_tr_b16 a[116:117],v9 offset:49160
ds_read_b64_tr_b16 a[118:119],v9 offset:49672
ds_read_b64_tr_b16 a[120:121],v9 offset:57344
ds_read_b64_tr_b16 a[122:123],v9 offset:57856
ds_read_b64_tr_b16 a[124:125],v9 offset:57352
ds_read_b64_tr_b16 a[126:127],v9 offset:57864
s_waitcnt lgkmcnt(0)
s_waitcnt vmcnt(24)
s_barrier
s_add_u32 m0,s97,114688
s_nop 0
buffer_load_dwordx4 v7,s[36:39],s96 offen offset:0 lds
buffer_load_dwordx4 v7,s[40:43],s96 offen offset:1024 lds
s_add_u32 s96,s96,65536
s_add_u32 m0,s97,122880
s_nop 0
buffer_load_dwordx4 v7,s[36:39],s96 offen offset:0 lds
buffer_load_dwordx4 v7,s[40:43],s96 offen offset:1024 lds
s_add_u32 s96,s96,65536
ds_read_b64_tr_b16 a[128:129],v8 offset:0
ds_read_b64_tr_b16 a[130:131],v8 offset:512
ds_read_b64_tr_b16 a[132:133],v8 offset:8
ds_read_b64_tr_b16 a[134:135],v8 offset:520
ds_read_b64_tr_b16 a[136:137],v8 offset:8192
ds_read_b64_tr_b16 a[138:139],v8 offset:8704
ds_read_b64_tr_b16 a[140:141],v8 offset:8200
ds_read_b64_tr_b16 a[142:143],v8 offset:8712
s_waitcnt lgkmcnt(0)
s_waitcnt vmcnt(24)
s_barrier
s_add_u32 m0,s97,0
s_nop 0
buffer_load_dwordx4 v7,s[36:39],s96 offen offset:0 lds
buffer_load_dwordx4 v7,s[40:43],s96 offen offset:1024 lds
s_add_u32 s96,s96,65536
s_add_u32 m0,s97,8192
s_nop 0
buffer_load_dwordx4 v7,s[36:39],s96 offen offset:0 lds
buffer_load_dwordx4 v7,s[40:43],s96 offen offset:1024 lds
s_add_u32 s96,s96,65536
ds_read_b64_tr_b16 a[144:145],v8 offset:16384
ds_read_b64_tr_b16 a[146:147],v8 offset:16896
ds_read_b64_tr_b16 a[148:149],v8 offset:16392
ds_read_b64_tr_b16 a[150:151],v8 offset:16904
ds_read_b64_tr_b16 a[152:153],v8 offset:24576
ds_read_b64_tr_b16 a[154:155],v8 offset:25088
ds_read_b64_tr_b16 a[156:157],v8 offset:24584
ds_read_b64_tr_b16 a[158:159],v8 offset:25096
s_waitcnt lgkmcnt(0)
s_waitcnt vmcnt(24)
s_barrier
s_add_u32 m0,s97,16384
s_nop 0
buffer_load_dwordx4 v7,s[36:39],s96 offen offset:0 lds
buffer_load_dwordx4 v7,s[40:43],s96 offen offset:1024 lds
s_add_u32 s96,s96,65536
s_add_u32 m0,s97,24576
s_nop 0
buffer_load_dwordx4 v7,s[36:39],s96 offen offset:0 lds
buffer_load_dwordx4 v7,s[40:43],s96 offen offset:1024 lds
s_add_u32 s96,s96,65536
ds_read_b64_tr_b16 a[160:161],v8 offset:32768
ds_read_b64_tr_b16 a[162:163],v8 offset:33280
ds_read_b64_tr_b16 a[164:165],v8 offset:32776
ds_read_b64_tr_b16 a[166:167],v8 offset:33288
ds_read_b64_tr_b16 a[168:169],v8 offset:40960
ds_read_b64_tr_b16 a[170:171],v8 offset:41472
ds_read_b64_tr_b16 a[172:173],v8 offset:40968
ds_read_b64_tr_b16 a[174:175],v8 offset:41480
s_waitcnt lgkmcnt(0)
s_waitcnt vmcnt(24)
s_barrier
s_add_u32 m0,s97,32768
s_nop 0
buffer_load_dwordx4 v7,s[36:39],s96 offen offset:0 lds
buffer_load_dwordx4 v7,s[40:43],s96 offen offset:1024 lds
s_add_u32 s96,s96,65536
s_add_u32 m0,s97,40960
s_nop 0
buffer_load_dwordx4 v7,s[36:39],s96 offen offset:0 lds
buffer_load_dwordx4 v7,s[40:43],s96 offen offset:1024 lds
s_add_u32 s96,s96,65536
ds_read_b64_tr_b16 a[176:177],v8 offset:49152
ds_read_b64_tr_b16 a[178:179],v8 offset:49664
ds_read_b64_tr_b16 a[180:181],v8 offset:49160
ds_read_b64_tr_b16 a[182:183],v8 offset:49672
ds_read_b64_tr_b16 a[184:185],v8 offset:57344
ds_read_b64_tr_b16 a[186:187],v8 offset:57856
ds_read_b64_tr_b16 a[188:189],v8 offset:57352
ds_read_b64_tr_b16 a[190:191],v8 offset:57864
s_waitcnt lgkmcnt(0)
s_waitcnt vmcnt(24)
s_barrier
s_add_u32 m0,s97,49152
s_nop 0
buffer_load_dwordx4 v7,s[36:39],s96 offen offset:0 lds
buffer_load_dwordx4 v7,s[40:43],s96 offen offset:1024 lds
s_add_u32 s96,s96,65536
s_add_u32 m0,s97,57344
s_nop 0
buffer_load_dwordx4 v7,s[36:39],s96 offen offset:0 lds
buffer_load_dwordx4 v7,s[40:43],s96 offen offset:1024 lds
s_add_u32 s96,s96,65536
ds_read_b64_tr_b16 a[192:193],v9 offset:0
ds_read_b64_tr_b16 a[194:195],v9 offset:512
ds_read_b64_tr_b16 a[196:197],v9 offset:8
ds_read_b64_tr_b16 a[198:199],v9 offset:520
ds_read_b64_tr_b16 a[200:201],v9 offset:8192
ds_read_b64_tr_b16 a[202:203],v9 offset:8704
ds_read_b64_tr_b16 a[204:205],v9 offset:8200
ds_read_b64_tr_b16 a[206:207],v9 offset:8712
s_waitcnt lgkmcnt(0)
s_waitcnt vmcnt(24)
s_barrier
s_add_u32 m0,s97,65536
s_nop 0
buffer_load_dwordx4 v7,s[36:39],s96 offen offset:0 lds
buffer_load_dwordx4 v7,s[40:43],s96 offen offset:1024 lds
s_add_u32 s96,s96,65536
s_add_u32 m0,s97,73728
s_nop 0
buffer_load_dwordx4 v7,s[36:39],s96 offen offset:0 lds
buffer_load_dwordx4 v7,s[40:43],s96 offen offset:1024 lds
s_add_u32 s96,s96,65536
ds_read_b64_tr_b16 a[208:209],v9 offset:16384
ds_read_b64_tr_b16 a[210:211],v9 offset:16896
ds_read_b64_tr_b16 a[212:213],v9 offset:16392
ds_read_b64_tr_b16 a[214:215],v9 offset:16904
ds_read_b64_tr_b16 a[216:217],v9 offset:24576
ds_read_b64_tr_b16 a[218:219],v9 offset:25088
ds_read_b64_tr_b16 a[220:221],v9 offset:24584
ds_read_b64_tr_b16 a[222:223],v9 offset:25096
s_waitcnt lgkmcnt(0)
s_waitcnt vmcnt(24)
s_barrier
s_add_u32 m0,s97,81920
s_nop 0
buffer_load_dwordx4 v7,s[36:39],s96 offen offset:0 lds
buffer_load_dwordx4 v7,s[40:43],s96 offen offset:1024 lds
s_add_u32 s96,s96,65536
s_add_u32 m0,s97,90112
s_nop 0
buffer_load_dwordx4 v7,s[36:39],s96 offen offset:0 lds
buffer_load_dwordx4 v7,s[40:43],s96 offen offset:1024 lds
s_add_u32 s96,s96,65536
ds_read_b64_tr_b16 a[224:225],v9 offset:32768
ds_read_b64_tr_b16 a[226:227],v9 offset:33280
ds_read_b64_tr_b16 a[228:229],v9 offset:32776
ds_read_b64_tr_b16 a[230:231],v9 offset:33288
ds_read_b64_tr_b16 a[232:233],v9 offset:40960
ds_read_b64_tr_b16 a[234:235],v9 offset:41472
ds_read_b64_tr_b16 a[236:237],v9 offset:40968
ds_read_b64_tr_b16 a[238:239],v9 offset:41480
s_waitcnt lgkmcnt(0)
s_waitcnt vmcnt(24)
s_barrier
s_add_u32 m0,s97,98304
s_nop 0
buffer_load_dwordx4 v7,s[36:39],s96 offen offset:0 lds
buffer_load_dwordx4 v7,s[40:43],s96 offen offset:1024 lds
s_add_u32 s96,s96,65536
)#"
R"#(
s_add_u32 m0,s97,106496
s_nop 0
buffer_load_dwordx4 v7,s[36:39],s96 offen offset:0 lds
buffer_load_dwordx4 v7,s[40:43],s96 offen offset:1024 lds
s_add_u32 s96,s96,65536
ds_read_b64_tr_b16 a[240:241],v9 offset:49152
ds_read_b64_tr_b16 a[242:243],v9 offset:49664
ds_read_b64_tr_b16 a[244:245],v9 offset:49160
ds_read_b64_tr_b16 a[246:247],v9 offset:49672
ds_read_b64_tr_b16 a[248:249],v9 offset:57344
ds_read_b64_tr_b16 a[250:251],v9 offset:57856
ds_read_b64_tr_b16 a[252:253],v9 offset:57352
ds_read_b64_tr_b16 a[254:255],v9 offset:57864
s_waitcnt lgkmcnt(0)
s_waitcnt vmcnt(24)
s_barrier
s_add_u32 m0,s97,114688
s_nop 0
buffer_load_dwordx4 v7,s[36:39],s96 offen offset:0 lds
buffer_load_dwordx4 v7,s[40:43],s96 offen offset:1024 lds
s_add_u32 s96,s96,65536
s_add_u32 m0,s97,122880
s_nop 0
buffer_load_dwordx4 v7,s[36:39],s96 offen offset:0 lds
buffer_load_dwordx4 v7,s[40:43],s96 offen offset:1024 lds
s_add_u32 s96,s96,65536
ds_read_b64_tr_b16 v[64:65],v8 offset:0
ds_read_b64_tr_b16 v[66:67],v8 offset:512
ds_read_b64_tr_b16 v[68:69],v8 offset:8
ds_read_b64_tr_b16 v[70:71],v8 offset:520
ds_read_b64_tr_b16 v[72:73],v8 offset:8192
ds_read_b64_tr_b16 v[74:75],v8 offset:8704
ds_read_b64_tr_b16 v[76:77],v8 offset:8200
ds_read_b64_tr_b16 v[78:79],v8 offset:8712
s_waitcnt lgkmcnt(0)
s_waitcnt vmcnt(24)
s_barrier
s_add_u32 m0,s97,0
s_nop 0
buffer_load_dwordx4 v7,s[36:39],s96 offen offset:0 lds
buffer_load_dwordx4 v7,s[40:43],s96 offen offset:1024 lds
s_add_u32 s96,s96,65536
s_add_u32 m0,s97,8192
s_nop 0
buffer_load_dwordx4 v7,s[36:39],s96 offen offset:0 lds
buffer_load_dwordx4 v7,s[40:43],s96 offen offset:1024 lds
s_add_u32 s96,s96,65536
ds_read_b64_tr_b16 v[80:81],v8 offset:16384
ds_read_b64_tr_b16 v[82:83],v8 offset:16896
ds_read_b64_tr_b16 v[84:85],v8 offset:16392
ds_read_b64_tr_b16 v[86:87],v8 offset:16904
ds_read_b64_tr_b16 v[88:89],v8 offset:24576
ds_read_b64_tr_b16 v[90:91],v8 offset:25088
ds_read_b64_tr_b16 v[92:93],v8 offset:24584
ds_read_b64_tr_b16 v[94:95],v8 offset:25096
s_waitcnt lgkmcnt(0)
s_waitcnt vmcnt(24)
s_barrier
s_add_u32 m0,s97,16384
s_nop 0
buffer_load_dwordx4 v7,s[36:39],s96 offen offset:0 lds
buffer_load_dwordx4 v7,s[40:43],s96 offen offset:1024 lds
s_add_u32 s96,s96,65536
s_add_u32 m0,s97,24576
s_nop 0
buffer_load_dwordx4 v7,s[36:39],s96 offen offset:0 lds
buffer_load_dwordx4 v7,s[40:43],s96 offen offset:1024 lds
s_add_u32 s96,s96,65536
ds_read_b64_tr_b16 v[96:97],v8 offset:32768
ds_read_b64_tr_b16 v[98:99],v8 offset:33280
ds_read_b64_tr_b16 v[100:101],v8 offset:32776
ds_read_b64_tr_b16 v[102:103],v8 offset:33288
ds_read_b64_tr_b16 v[104:105],v8 offset:40960
ds_read_b64_tr_b16 v[106:107],v8 offset:41472
ds_read_b64_tr_b16 v[108:109],v8 offset:40968
ds_read_b64_tr_b16 v[110:111],v8 offset:41480
s_waitcnt lgkmcnt(0)
s_waitcnt vmcnt(24)
s_barrier
s_add_u32 m0,s97,32768
s_nop 0
buffer_load_dwordx4 v7,s[36:39],s96 offen offset:0 lds
buffer_load_dwordx4 v7,s[40:43],s96 offen offset:1024 lds
s_add_u32 s96,s96,65536
s_add_u32 m0,s97,40960
s_nop 0
buffer_load_dwordx4 v7,s[36:39],s96 offen offset:0 lds
buffer_load_dwordx4 v7,s[40:43],s96 offen offset:1024 lds
s_add_u32 s96,s96,65536
ds_read_b64_tr_b16 v[112:113],v8 offset:49152
ds_read_b64_tr_b16 v[114:115],v8 offset:49664
ds_read_b64_tr_b16 v[116:117],v8 offset:49160
ds_read_b64_tr_b16 v[118:119],v8 offset:49672
ds_read_b64_tr_b16 v[120:121],v8 offset:57344
ds_read_b64_tr_b16 v[122:123],v8 offset:57856
ds_read_b64_tr_b16 v[124:125],v8 offset:57352
ds_read_b64_tr_b16 v[126:127],v8 offset:57864
s_waitcnt lgkmcnt(0)
s_waitcnt vmcnt(24)
s_barrier
s_add_u32 m0,s97,49152
s_nop 0
buffer_load_dwordx4 v7,s[36:39],s96 offen offset:0 lds
buffer_load_dwordx4 v7,s[40:43],s96 offen offset:1024 lds
s_add_u32 s96,s96,65536
s_add_u32 m0,s97,57344
s_nop 0
buffer_load_dwordx4 v7,s[36:39],s96 offen offset:0 lds
buffer_load_dwordx4 v7,s[40:43],s96 offen offset:1024 lds
s_add_u32 s96,s96,65536
ds_read_b64_tr_b16 v[128:129],v9 offset:0
ds_read_b64_tr_b16 v[130:131],v9 offset:512
ds_read_b64_tr_b16 v[132:133],v9 offset:8
ds_read_b64_tr_b16 v[134:135],v9 offset:520
ds_read_b64_tr_b16 v[136:137],v9 offset:8192
ds_read_b64_tr_b16 v[138:139],v9 offset:8704
ds_read_b64_tr_b16 v[140:141],v9 offset:8200
ds_read_b64_tr_b16 v[142:143],v9 offset:8712
s_waitcnt lgkmcnt(0)
s_waitcnt vmcnt(24)
s_barrier
ds_read_b64_tr_b16 v[144:145],v9 offset:16384
ds_read_b64_tr_b16 v[146:147],v9 offset:16896
ds_read_b64_tr_b16 v[148:149],v9 offset:16392
ds_read_b64_tr_b16 v[150:151],v9 offset:16904
ds_read_b64_tr_b16 v[152:153],v9 offset:24576
ds_read_b64_tr_b16 v[154:155],v9 offset:25088
ds_read_b64_tr_b16 v[156:157],v9 offset:24584
ds_read_b64_tr_b16 v[158:159],v9 offset:25096
s_waitcnt lgkmcnt(0)
s_waitcnt vmcnt(20)
s_barrier
ds_read_b64_tr_b16 v[160:161],v9 offset:32768
ds_read_b64_tr_b16 v[162:163],v9 offset:33280
ds_read_b64_tr_b16 v[164:165],v9 offset:32776
ds_read_b64_tr_b16 v[166:167],v9 offset:33288
ds_read_b64_tr_b16 v[168:169],v9 offset:40960
ds_read_b64_tr_b16 v[170:171],v9 offset:41472
ds_read_b64_tr_b16 v[172:173],v9 offset:40968
ds_read_b64_tr_b16 v[174:175],v9 offset:41480
s_waitcnt lgkmcnt(0)
s_waitcnt vmcnt(16)
s_barrier
ds_read_b64_tr_b16 v[176:177],v9 offset:49152
ds_read_b64_tr_b16 v[178:179],v9 offset:49664
ds_read_b64_tr_b16 v[180:181],v9 offset:49160
ds_read_b64_tr_b16 v[182:183],v9 offset:49672
ds_read_b64_tr_b16 v[184:185],v9 offset:57344
ds_read_b64_tr_b16 v[186:187],v9 offset:57856
ds_read_b64_tr_b16 v[188:189],v9 offset:57352
ds_read_b64_tr_b16 v[190:191],v9 offset:57864
s_waitcnt lgkmcnt(0)
s_waitcnt vmcnt(12)
s_barrier
ds_read_b64_tr_b16 v[192:193],v8 offset:0
ds_read_b64_tr_b16 v[194:195],v8 offset:512
ds_read_b64_tr_b16 v[196:197],v8 offset:8
ds_read_b64_tr_b16 v[198:199],v8 offset:520
ds_read_b64_tr_b16 v[200:201],v8 offset:8192
ds_read_b64_tr_b16 v[202:203],v8 offset:8704
ds_read_b64_tr_b16 v[204:205],v8 offset:8200
ds_read_b64_tr_b16 v[206:207],v8 offset:8712
s_waitcnt lgkmcnt(0)
s_waitcnt vmcnt(8)
s_barrier
ds_read_b64_tr_b16 v[208:209],v8 offset:16384
ds_read_b64_tr_b16 v[210:211],v8 offset:16896
ds_read_b64_tr_b16 v[212:213],v8 offset:16392
ds_read_b64_tr_b16 v[214:215],v8 offset:16904
ds_read_b64_tr_b16 v[216:217],v8 offset:24576
ds_read_b64_tr_b16 v[218:219],v8 offset:25088
ds_read_b64_tr_b16 v[220:221],v8 offset:24584
ds_read_b64_tr_b16 v[222:223],v8 offset:25096
s_waitcnt lgkmcnt(0)
s_waitcnt vmcnt(4)
s_barrier
ds_read_b64_tr_b16 v[224:225],v8 offset:32768
ds_read_b64_tr_b16 v[226:227],v8 offset:33280
ds_read_b64_tr_b16 v[228:229],v8 offset:32776
ds_read_b64_tr_b16 v[230:231],v8 offset:33288
ds_read_b64_tr_b16 v[232:233],v8 offset:40960
ds_read_b64_tr_b16 v[234:235],v8 offset:41472
ds_read_b64_tr_b16 v[236:237],v8 offset:40968
ds_read_b64_tr_b16 v[238:239],v8 offset:41480
s_waitcnt lgkmcnt(0)
s_waitcnt vmcnt(0)
s_barrier
ds_read_b64_tr_b16 v[240:241],v8 offset:49152
ds_read_b64_tr_b16 v[242:243],v8 offset:49664
ds_read_b64_tr_b16 v[244:245],v8 offset:49160
ds_read_b64_tr_b16 v[246:247],v8 offset:49672
ds_read_b64_tr_b16 v[248:249],v8 offset:57344
ds_read_b64_tr_b16 v[250:251],v8 offset:57856
ds_read_b64_tr_b16 v[252:253],v8 offset:57352
ds_read_b64_tr_b16 v[254:255],v8 offset:57864
s_waitcnt lgkmcnt(0)
s_barrier
v_mov_b32 v8,s54
v_mov_b32 v9,s55
s_lshl2_add_u32 s48,s29,s26
s_mul_i32 s63,s48,14336
s_add_u32 s48,s29,18
s_mul_i32 s49,s29,72
s_add_u32 s49,s49,128
v_lshl_add_u32 v0,v1,2,s49
s_add_u32 s49,s50,280
v_writelane_b32 v0,s49,63
v_lshrrev_b32 v0,2,v0
v_lshlrev_b32 v0,7,v0
v_mov_b32 v3,2375680
v_writelane_b32 v3,0,63
v_add_u32 v0,v3,v0
v_cmp_ge_u32 vcc,s48,v1
s_lshl_b32 s49,s29,31
s_nop 4
s_or_b32 vcc_hi,vcc_hi,s49
s_nop 4
v_mov_b32 v3,0x7fff0000
v_cndmask_b32 v0,v3,v0,vcc
v_cndmask_b32 v8,0,v8,vcc
v_cndmask_b32 v9,0,v9,vcc
v_mov_b32 v3,v21
s_mul_i32 s48,s28,296960
s_add_u32 s63,s63,s48
s_add_u32 s63,s63,4718592
s_add_u32 s66,s48,2523136
s_mul_i32 s64,s26,14336
s_lshl_b32 s74,s26,11
s_add_u32 s74,s74,114688
v_add_u32 v5,s74,v2
v_add_u32 v4,57344,v2
s_add_u32 s44,s20,0x0
s_addc_u32 s45,s21,0x0
s_and_b32 s45,s45,0xffff
s_mov_b32 s46,0x400000
s_mov_b32 s47,0x20000
s_mov_b32 s62,16
s_cmp_lg_u32 s29,0
s_cbranch_scc1 .L_dh
s_lshl_b32 s48,s27,7
s_add_u32 s48,s48,s66
s_add_u32 s48,s48,8960
v_add_u32 v10,34,v1
v_mul_u32_u24 v10,1187840,v10
v_writelane_b32 v10,0,32
s_cmp_eq_u32 s26,0
s_cselect_b32 s49,33,0
v_cmp_gt_u32 vcc,s49,v1
v_mov_b32 v11,0x7fff0000
v_mov_b32 v12,s54
v_mov_b32 v13,s55
v_cndmask_b32 v10,v11,v10,vcc
buffer_store_dwordx2 v[12:13],v10,s[32:35],s48 offen sc1
v_lshrrev_b32 v10,4,v1
v_and_b32 v1,15,v1
s_lshl_b32 s48,s50,6
v_lshl_add_u32 v1,v1,2,s48
v_lshl_add_u32 v1,v10,12,v1
s_add_u32 s71,s70,4681728
s_sub_u32 s68,s65,2375680
s_lshl_b32 s65,s28,20
s_mov_b32 s48,20000
s_branch .L_rfdl
.L_rldl:
s_sleep 8
.L_rfdl:
buffer_load_dwordx2 v[6:7],v0,s[32:35],s66 offen sc1
s_sub_u32 s48,s48,1
s_cbranch_scc1 .L_end
s_waitcnt vmcnt(0)
v_cmp_ne_u64 vcc,v[8:9],v[6:7]
s_cbranch_vccnz .L_rldl
s_add_u32 s66,s66,1187840
s_add_u32 m0,s64,0
s_nop 0
buffer_load_dwordx4 v2,s[32:35],s63 offen offset:0 sc1 lds
buffer_load_dwordx4 v2,s[32:35],s63 offen offset:1024 sc1 lds
buffer_load_dwordx4 v2,s[32:35],s63 offen offset:2048 sc1 lds
buffer_load_dwordx4 v2,s[32:35],s63 offen offset:3072 sc1 lds
s_add_u32 s63,s63,4096
s_add_u32 m0,s64,4096
s_nop 0
buffer_load_dwordx4 v2,s[32:35],s63 offen offset:0 sc1 lds
buffer_load_dwordx4 v2,s[32:35],s63 offen offset:1024 sc1 lds
buffer_load_dwordx4 v2,s[32:35],s63 offen offset:2048 sc1 lds
buffer_load_dwordx4 v2,s[32:35],s63 offen offset:3072 sc1 lds
s_add_u32 s63,s63,4096
s_add_u32 m0,s64,8192
s_nop 0
buffer_load_dwordx4 v2,s[32:35],s63 offen offset:0 sc1 lds
buffer_load_dwordx4 v2,s[32:35],s63 offen offset:1024 sc1 lds
buffer_load_dwordx4 v2,s[32:35],s63 offen offset:2048 sc1 lds
buffer_load_dwordx4 v2,s[32:35],s63 offen offset:3072 sc1 lds
s_add_u32 s63,s63,4096
s_add_u32 m0,s64,12288
s_nop 0
buffer_load_dwordx4 v2,s[32:35],s63 offen offset:0 sc1 lds
buffer_load_dwordx4 v2,s[32:35],s63 offen offset:1024 sc1 lds
s_add_u32 s63,s63,1175552
buffer_load_dwordx4 v[12:15],v2,s[32:35],s71 offen sc1
s_add_u32 s71,s71,1187840
buffer_load_dwordx2 v[6:7],v0,s[32:35],s66 offen sc1
s_waitcnt vmcnt(0)
s_barrier
v_lshlrev_b32 v16,16,v12
v_and_b32 v17,0xffff0000,v12
v_lshlrev_b32 v18,16,v13
v_and_b32 v19,0xffff0000,v13
v_lshlrev_b32 v20,16,v14
v_and_b32 v21,0xffff0000,v14
v_lshlrev_b32 v22,16,v15
v_and_b32 v23,0xffff0000,v15
ds_read_b128 v[32:35],v2 offset:0
ds_read_b128 v[36:39],v2 offset:14336
ds_read_b128 v[40:43],v2 offset:28672
ds_read_b128 v[44:47],v2 offset:43008
ds_read_b128 v[48:51],v2 offset:1024
ds_read_b128 v[52:55],v2 offset:15360
.L_dlgo:
s_cmp_eq_u32 s26,1
s_cbranch_scc1 .L_dlv1
s_cmp_eq_u32 s26,2
s_cbranch_scc1 .L_dlv2
s_cmp_eq_u32 s26,3
s_cbranch_scc1 .L_dlv3
.p2align 6
.L_dlv0:
.L_dll_0:
s_waitcnt lgkmcnt(4)
v_mfma_f32_16x16x32_bf16 v[16:19],a[0:3],v[32:35],v[16:19]
ds_read_b128 v[56:59],v2 offset:29696
v_mfma_f32_16x16x32_bf16 v[16:19],a[112:115],v[36:39],v[16:19]
v_mfma_f32_16x16x32_bf16 v[20:23],a[116:119],v[36:39],v[20:23]
ds_read_b128 v[60:63],v2 offset:44032
v_mfma_f32_16x16x32_bf16 v[20:23],a[4:7],v[32:35],v[20:23]
s_add_u32 s68,s68,1187840
s_waitcnt lgkmcnt(4)
v_mfma_f32_16x16x32_bf16 v[20:23],a[228:231],v[40:43],v[20:23]
ds_read_b128 v[32:35],v2 offset:2048
v_mfma_f32_16x16x32_bf16 v[20:23],v[148:151],v[44:47],v[20:23]
buffer_store_dwordx4 v[28:31],v2,s[32:35],s68 offen offset:1024 sc1
v_mfma_f32_16x16x32_bf16 v[16:19],v[144:147],v[44:47],v[16:19]
ds_read_b128 v[36:39],v2 offset:16384
v_mfma_f32_16x16x32_bf16 v[16:19],a[224:227],v[40:43],v[16:19]
s_waitcnt lgkmcnt(4)
v_mfma_f32_16x16x32_bf16 v[16:19],a[8:11],v[48:51],v[16:19]
ds_read_b128 v[40:43],v2 offset:30720
v_mfma_f32_16x16x32_bf16 v[16:19],a[120:123],v[52:55],v[16:19]
buffer_store_dwordx4 v[24:27],v2,s[32:35],s68 offen sc1
s_mov_b32 s67,s68
v_mfma_f32_16x16x32_bf16 v[20:23],a[124:127],v[52:55],v[20:23]
ds_read_b128 v[44:47],v2 offset:45056
v_mfma_f32_16x16x32_bf16 v[20:23],a[12:15],v[48:51],v[20:23]
s_waitcnt lgkmcnt(4)
v_mfma_f32_16x16x32_bf16 v[20:23],a[236:239],v[56:59],v[20:23]
ds_read_b128 v[48:51],v2 offset:3072
v_mfma_f32_16x16x32_bf16 v[20:23],v[156:159],v[60:63],v[20:23]
v_mfma_f32_16x16x32_bf16 v[16:19],v[152:155],v[60:63],v[16:19]
ds_read_b128 v[52:55],v2 offset:17408
v_mfma_f32_16x16x32_bf16 v[16:19],a[232:235],v[56:59],v[16:19]
s_waitcnt lgkmcnt(4)
v_mfma_f32_16x16x32_bf16 v[16:19],a[16:19],v[32:35],v[16:19]
ds_read_b128 v[56:59],v2 offset:31744
v_mfma_f32_16x16x32_bf16 v[16:19],a[128:131],v[36:39],v[16:19]
v_mfma_f32_16x16x32_bf16 v[20:23],a[132:135],v[36:39],v[20:23]
ds_read_b128 v[60:63],v2 offset:46080
v_mfma_f32_16x16x32_bf16 v[20:23],a[20:23],v[32:35],v[20:23]
s_waitcnt lgkmcnt(4)
s_waitcnt vmcnt(3)
s_barrier
v_mfma_f32_16x16x32_bf16 v[20:23],a[244:247],v[40:43],v[20:23]
ds_read_b128 v[32:35],v2 offset:4096
s_add_u32 m0,s64,57344
v_mfma_f32_16x16x32_bf16 v[20:23],v[164:167],v[44:47],v[20:23]
buffer_load_dwordx4 v2,s[32:35],s63 offen offset:0 sc1 lds
v_mfma_f32_16x16x32_bf16 v[16:19],v[160:163],v[44:47],v[16:19]
ds_read_b128 v[36:39],v2 offset:18432
v_mfma_f32_16x16x32_bf16 v[16:19],a[240:243],v[40:43],v[16:19]
s_waitcnt lgkmcnt(4)
v_mfma_f32_16x16x32_bf16 v[16:19],a[24:27],v[48:51],v[16:19]
ds_read_b128 v[40:43],v2 offset:32768
v_mfma_f32_16x16x32_bf16 v[16:19],a[136:139],v[52:55],v[16:19]
v_mfma_f32_16x16x32_bf16 v[20:23],a[140:143],v[52:55],v[20:23]
ds_read_b128 v[44:47],v2 offset:47104
v_mfma_f32_16x16x32_bf16 v[20:23],a[28:31],v[48:51],v[20:23]
s_waitcnt lgkmcnt(4)
v_mfma_f32_16x16x32_bf16 v[20:23],a[252:255],v[56:59],v[20:23]
ds_read_b128 v[48:51],v2 offset:5120
v_mfma_f32_16x16x32_bf16 v[20:23],v[172:175],v[60:63],v[20:23]
buffer_load_dwordx4 v2,s[32:35],s63 offen offset:1024 sc1 lds
v_mfma_f32_16x16x32_bf16 v[16:19],v[168:171],v[60:63],v[16:19]
ds_read_b128 v[52:55],v2 offset:19456
v_mfma_f32_16x16x32_bf16 v[16:19],a[248:251],v[56:59],v[16:19]
s_waitcnt lgkmcnt(4)
v_mfma_f32_16x16x32_bf16 v[16:19],a[32:35],v[32:35],v[16:19]
ds_read_b128 v[56:59],v2 offset:33792
v_mfma_f32_16x16x32_bf16 v[16:19],a[144:147],v[36:39],v[16:19]
buffer_load_dwordx4 v2,s[32:35],s63 offen offset:2048 sc1 lds
v_mfma_f32_16x16x32_bf16 v[20:23],a[148:151],v[36:39],v[20:23]
ds_read_b128 v[60:63],v2 offset:48128
v_mfma_f32_16x16x32_bf16 v[20:23],a[36:39],v[32:35],v[20:23]
s_waitcnt lgkmcnt(4)
v_mfma_f32_16x16x32_bf16 v[20:23],v[68:71],v[40:43],v[20:23]
ds_read_b128 v[32:35],v2 offset:6144
v_mfma_f32_16x16x32_bf16 v[20:23],v[180:183],v[44:47],v[20:23]
buffer_load_dwordx4 v2,s[32:35],s63 offen offset:3072 sc1 lds
v_mfma_f32_16x16x32_bf16 v[16:19],v[176:179],v[44:47],v[16:19]
ds_read_b128 v[36:39],v2 offset:20480
v_mfma_f32_16x16x32_bf16 v[16:19],v[64:67],v[40:43],v[16:19]
s_waitcnt lgkmcnt(4)
v_mfma_f32_16x16x32_bf16 v[16:19],a[40:43],v[48:51],v[16:19]
ds_read_b128 v[40:43],v2 offset:34816
v_mfma_f32_16x16x32_bf16 v[16:19],a[152:155],v[52:55],v[16:19]
buffer_load_dwordx4 v[12:15],v2,s[32:35],s71 offen sc1
s_add_u32 s71,s71,1187840
v_mfma_f32_16x16x32_bf16 v[20:23],a[156:159],v[52:55],v[20:23]
ds_read_b128 v[44:47],v2 offset:49152
s_add_u32 s63,s63,4096
v_mfma_f32_16x16x32_bf16 v[20:23],a[44:47],v[48:51],v[20:23]
s_add_u32 m0,s64,61440
s_waitcnt lgkmcnt(4)
v_mfma_f32_16x16x32_bf16 v[20:23],v[76:79],v[56:59],v[20:23]
ds_read_b128 v[48:51],v2 offset:7168
v_mfma_f32_16x16x32_bf16 v[20:23],v[188:191],v[60:63],v[20:23]
buffer_load_dwordx4 v2,s[32:35],s63 offen offset:0 sc1 lds
v_mfma_f32_16x16x32_bf16 v[16:19],v[184:187],v[60:63],v[16:19]
ds_read_b128 v[52:55],v2 offset:21504
v_mfma_f32_16x16x32_bf16 v[16:19],v[72:75],v[56:59],v[16:19]
s_waitcnt lgkmcnt(4)
v_mfma_f32_16x16x32_bf16 v[16:19],a[48:51],v[32:35],v[16:19]
ds_read_b128 v[56:59],v2 offset:35840
v_mfma_f32_16x16x32_bf16 v[16:19],a[160:163],v[36:39],v[16:19]
buffer_load_dwordx4 v2,s[32:35],s63 offen offset:1024 sc1 lds
v_mfma_f32_16x16x32_bf16 v[20:23],a[164:167],v[36:39],v[20:23]
ds_read_b128 v[60:63],v2 offset:50176
v_mfma_f32_16x16x32_bf16 v[20:23],a[52:55],v[32:35],v[20:23]
s_waitcnt lgkmcnt(4)
v_mfma_f32_16x16x32_bf16 v[20:23],v[84:87],v[40:43],v[20:23]
ds_read_b128 v[32:35],v2 offset:8192
v_mfma_f32_16x16x32_bf16 v[20:23],v[196:199],v[44:47],v[20:23]
buffer_load_dwordx4 v2,s[32:35],s63 offen offset:2048 sc1 lds
v_mfma_f32_16x16x32_bf16 v[16:19],v[192:195],v[44:47],v[16:19]
ds_read_b128 v[36:39],v2 offset:22528
v_mfma_f32_16x16x32_bf16 v[16:19],v[80:83],v[40:43],v[16:19]
s_waitcnt lgkmcnt(4)
v_mfma_f32_16x16x32_bf16 v[16:19],a[56:59],v[48:51],v[16:19]
ds_read_b128 v[40:43],v2 offset:36864
v_mfma_f32_16x16x32_bf16 v[16:19],a[168:171],v[52:55],v[16:19]
buffer_load_dwordx4 v2,s[32:35],s63 offen offset:3072 sc1 lds
v_mfma_f32_16x16x32_bf16 v[20:23],a[172:175],v[52:55],v[20:23]
ds_read_b128 v[44:47],v2 offset:51200
v_mfma_f32_16x16x32_bf16 v[20:23],a[60:63],v[48:51],v[20:23]
s_waitcnt lgkmcnt(4)
v_mfma_f32_16x16x32_bf16 v[20:23],v[92:95],v[56:59],v[20:23]
ds_read_b128 v[48:51],v2 offset:9216
v_mfma_f32_16x16x32_bf16 v[20:23],v[204:207],v[60:63],v[20:23]
s_add_u32 s63,s63,4096
s_add_u32 m0,s64,65536
v_mfma_f32_16x16x32_bf16 v[16:19],v[200:203],v[60:63],v[16:19]
ds_read_b128 v[52:55],v2 offset:23552
v_mfma_f32_16x16x32_bf16 v[16:19],v[88:91],v[56:59],v[16:19]
s_waitcnt lgkmcnt(4)
v_mfma_f32_16x16x32_bf16 v[16:19],a[64:67],v[32:35],v[16:19]
ds_read_b128 v[56:59],v2 offset:37888
v_mfma_f32_16x16x32_bf16 v[16:19],a[176:179],v[36:39],v[16:19]
buffer_load_dwordx4 v2,s[32:35],s63 offen offset:0 sc1 lds
v_mfma_f32_16x16x32_bf16 v[20:23],a[180:183],v[36:39],v[20:23]
ds_read_b128 v[60:63],v2 offset:52224
v_mfma_f32_16x16x32_bf16 v[20:23],a[68:71],v[32:35],v[20:23]
s_waitcnt lgkmcnt(4)
v_mfma_f32_16x16x32_bf16 v[20:23],v[100:103],v[40:43],v[20:23]
ds_read_b128 v[32:35],v2 offset:10240
v_mfma_f32_16x16x32_bf16 v[20:23],v[212:215],v[44:47],v[20:23]
buffer_load_dwordx4 v2,s[32:35],s63 offen offset:1024 sc1 lds
v_mfma_f32_16x16x32_bf16 v[16:19],v[208:211],v[44:47],v[16:19]
ds_read_b128 v[36:39],v2 offset:24576
v_mfma_f32_16x16x32_bf16 v[16:19],v[96:99],v[40:43],v[16:19]
s_waitcnt lgkmcnt(4)
v_mfma_f32_16x16x32_bf16 v[16:19],a[72:75],v[48:51],v[16:19]
ds_read_b128 v[40:43],v2 offset:38912
v_mfma_f32_16x16x32_bf16 v[16:19],a[184:187],v[52:55],v[16:19]
buffer_load_dwordx4 v2,s[32:35],s63 offen offset:2048 sc1 lds
v_mfma_f32_16x16x32_bf16 v[20:23],a[188:191],v[52:55],v[20:23]
ds_read_b128 v[44:47],v2 offset:53248
v_mfma_f32_16x16x32_bf16 v[20:23],a[76:79],v[48:51],v[20:23]
s_waitcnt lgkmcnt(4)
v_mfma_f32_16x16x32_bf16 v[20:23],v[108:111],v[56:59],v[20:23]
ds_read_b128 v[48:51],v2 offset:11264
v_mfma_f32_16x16x32_bf16 v[20:23],v[220:223],v[60:63],v[20:23]
v_mfma_f32_16x16x32_bf16 v[16:19],v[216:219],v[60:63],v[16:19]
ds_read_b128 v[52:55],v2 offset:25600
v_mfma_f32_16x16x32_bf16 v[16:19],v[104:107],v[56:59],v[16:19]
s_waitcnt lgkmcnt(4)
v_mfma_f32_16x16x32_bf16 v[16:19],a[80:83],v[32:35],v[16:19]
ds_read_b128 v[56:59],v2 offset:39936
v_mfma_f32_16x16x32_bf16 v[16:19],a[192:195],v[36:39],v[16:19]
buffer_load_dwordx4 v2,s[32:35],s63 offen offset:3072 sc1 lds
s_add_u32 s63,s63,4096
v_mfma_f32_16x16x32_bf16 v[20:23],a[196:199],v[36:39],v[20:23]
ds_read_b128 v[60:63],v2 offset:54272
)#"
R"#(
s_add_u32 m0,s64,69632
v_mfma_f32_16x16x32_bf16 v[20:23],a[84:87],v[32:35],v[20:23]
s_waitcnt lgkmcnt(4)
v_mfma_f32_16x16x32_bf16 v[20:23],v[116:119],v[40:43],v[20:23]
ds_read_b128 v[32:35],v2 offset:12288
v_mfma_f32_16x16x32_bf16 v[20:23],v[228:231],v[44:47],v[20:23]
buffer_load_dwordx4 v2,s[32:35],s63 offen offset:0 sc1 lds
v_mfma_f32_16x16x32_bf16 v[16:19],v[224:227],v[44:47],v[16:19]
ds_read_b128 v[36:39],v2 offset:26624
v_mfma_f32_16x16x32_bf16 v[16:19],v[112:115],v[40:43],v[16:19]
s_waitcnt lgkmcnt(4)
v_mfma_f32_16x16x32_bf16 v[16:19],a[88:91],v[48:51],v[16:19]
ds_read_b128 v[40:43],v2 offset:40960
v_mfma_f32_16x16x32_bf16 v[16:19],a[200:203],v[52:55],v[16:19]
buffer_load_dwordx4 v2,s[32:35],s63 offen offset:1024 sc1 lds
s_add_u32 s63,s63,1175552
v_mfma_f32_16x16x32_bf16 v[20:23],a[204:207],v[52:55],v[20:23]
ds_read_b128 v[44:47],v2 offset:55296
v_mfma_f32_16x16x32_bf16 v[20:23],a[92:95],v[48:51],v[20:23]
s_waitcnt lgkmcnt(4)
v_mfma_f32_16x16x32_bf16 v[20:23],v[124:127],v[56:59],v[20:23]
ds_read_b128 v[48:51],v2 offset:13312
s_waitcnt vmcnt(10)
v_mfma_f32_16x16x32_bf16 v[20:23],v[236:239],v[60:63],v[20:23]
v_lshlrev_b32 v24,16,v12
v_and_b32 v25,0xffff0000,v12
v_mfma_f32_16x16x32_bf16 v[16:19],v[232:235],v[60:63],v[16:19]
ds_read_b128 v[52:55],v2 offset:27648
v_lshlrev_b32 v26,16,v13
v_mfma_f32_16x16x32_bf16 v[16:19],v[120:123],v[56:59],v[16:19]
v_and_b32 v27,0xffff0000,v13
s_waitcnt lgkmcnt(4)
v_mfma_f32_16x16x32_bf16 v[16:19],a[96:99],v[32:35],v[16:19]
ds_read_b128 v[56:59],v2 offset:41984
v_lshlrev_b32 v28,16,v14
v_mfma_f32_16x16x32_bf16 v[16:19],a[208:211],v[36:39],v[16:19]
v_and_b32 v29,0xffff0000,v14
v_lshlrev_b32 v30,16,v15
v_mfma_f32_16x16x32_bf16 v[20:23],a[212:215],v[36:39],v[20:23]
ds_read_b128 v[60:63],v2 offset:56320
v_and_b32 v31,0xffff0000,v15
v_mfma_f32_16x16x32_bf16 v[20:23],a[100:103],v[32:35],v[20:23]
s_waitcnt lgkmcnt(4)
s_barrier
v_mfma_f32_16x16x32_bf16 v[20:23],v[132:135],v[40:43],v[20:23]
ds_read_b128 v[32:35],v4 offset:0
v_mfma_f32_16x16x32_bf16 v[20:23],v[244:247],v[44:47],v[20:23]
v_mfma_f32_16x16x32_bf16 v[16:19],v[240:243],v[44:47],v[16:19]
ds_read_b128 v[36:39],v4 offset:14336
v_mfma_f32_16x16x32_bf16 v[16:19],v[128:131],v[40:43],v[16:19]
s_waitcnt lgkmcnt(4)
v_mfma_f32_16x16x32_bf16 v[16:19],a[104:107],v[48:51],v[16:19]
ds_read_b128 v[40:43],v4 offset:28672
v_mfma_f32_16x16x32_bf16 v[16:19],a[216:219],v[52:55],v[16:19]
buffer_store_dwordx2 v[8:9],v2,s[92:95],s67 idxen sc1
v_mfma_f32_16x16x32_bf16 v[20:23],a[220:223],v[52:55],v[20:23]
ds_read_b128 v[44:47],v4 offset:43008
v_mfma_f32_16x16x32_bf16 v[20:23],a[108:111],v[48:51],v[20:23]
s_waitcnt lgkmcnt(4)
v_mfma_f32_16x16x32_bf16 v[20:23],v[140:143],v[56:59],v[20:23]
ds_read_b128 v[48:51],v4 offset:1024
v_mfma_f32_16x16x32_bf16 v[20:23],v[252:255],v[60:63],v[20:23]
v_mfma_f32_16x16x32_bf16 v[16:19],v[248:251],v[60:63],v[16:19]
ds_read_b128 v[52:55],v4 offset:15360
s_sub_u32 s62,s62,1
v_mfma_f32_16x16x32_bf16 v[16:19],v[136:139],v[56:59],v[16:19]
s_cbranch_scc1 .L_dlb_0
s_waitcnt lgkmcnt(4)
v_mfma_f32_16x16x32_bf16 v[24:27],a[0:3],v[32:35],v[24:27]
ds_read_b128 v[56:59],v4 offset:29696
v_mfma_f32_16x16x32_bf16 v[24:27],a[112:115],v[36:39],v[24:27]
v_mfma_f32_16x16x32_bf16 v[28:31],a[116:119],v[36:39],v[28:31]
ds_read_b128 v[60:63],v4 offset:44032
v_mfma_f32_16x16x32_bf16 v[28:31],a[4:7],v[32:35],v[28:31]
s_add_u32 s68,s68,1187840
s_waitcnt lgkmcnt(4)
v_mfma_f32_16x16x32_bf16 v[28:31],a[228:231],v[40:43],v[28:31]
ds_read_b128 v[32:35],v4 offset:2048
v_mfma_f32_16x16x32_bf16 v[28:31],v[148:151],v[44:47],v[28:31]
buffer_store_dwordx4 v[20:23],v2,s[32:35],s68 offen offset:1024 sc1
v_mfma_f32_16x16x32_bf16 v[24:27],v[144:147],v[44:47],v[24:27]
ds_read_b128 v[36:39],v4 offset:16384
v_mfma_f32_16x16x32_bf16 v[24:27],a[224:227],v[40:43],v[24:27]
s_waitcnt lgkmcnt(4)
v_mfma_f32_16x16x32_bf16 v[24:27],a[8:11],v[48:51],v[24:27]
ds_read_b128 v[40:43],v4 offset:30720
v_mfma_f32_16x16x32_bf16 v[24:27],a[120:123],v[52:55],v[24:27]
buffer_store_dwordx4 v[16:19],v2,s[32:35],s68 offen sc1
s_mov_b32 s67,s68
v_mfma_f32_16x16x32_bf16 v[28:31],a[124:127],v[52:55],v[28:31]
ds_read_b128 v[44:47],v4 offset:45056
v_mfma_f32_16x16x32_bf16 v[28:31],a[12:15],v[48:51],v[28:31]
s_waitcnt lgkmcnt(4)
v_mfma_f32_16x16x32_bf16 v[28:31],a[236:239],v[56:59],v[28:31]
ds_read_b128 v[48:51],v4 offset:3072
v_mfma_f32_16x16x32_bf16 v[28:31],v[156:159],v[60:63],v[28:31]
v_mfma_f32_16x16x32_bf16 v[24:27],v[152:155],v[60:63],v[24:27]
ds_read_b128 v[52:55],v4 offset:17408
v_mfma_f32_16x16x32_bf16 v[24:27],a[232:235],v[56:59],v[24:27]
s_waitcnt lgkmcnt(4)
v_mfma_f32_16x16x32_bf16 v[24:27],a[16:19],v[32:35],v[24:27]
ds_read_b128 v[56:59],v4 offset:31744
v_mfma_f32_16x16x32_bf16 v[24:27],a[128:131],v[36:39],v[24:27]
v_mfma_f32_16x16x32_bf16 v[28:31],a[132:135],v[36:39],v[28:31]
ds_read_b128 v[60:63],v4 offset:46080
v_mfma_f32_16x16x32_bf16 v[28:31],a[20:23],v[32:35],v[28:31]
s_waitcnt lgkmcnt(4)
s_waitcnt vmcnt(3)
s_barrier
v_mfma_f32_16x16x32_bf16 v[28:31],a[244:247],v[40:43],v[28:31]
ds_read_b128 v[32:35],v4 offset:4096
s_add_u32 m0,s64,0
v_mfma_f32_16x16x32_bf16 v[28:31],v[164:167],v[44:47],v[28:31]
buffer_load_dwordx4 v2,s[32:35],s63 offen offset:0 sc1 lds
v_mfma_f32_16x16x32_bf16 v[24:27],v[160:163],v[44:47],v[24:27]
ds_read_b128 v[36:39],v4 offset:18432
v_mfma_f32_16x16x32_bf16 v[24:27],a[240:243],v[40:43],v[24:27]
s_waitcnt lgkmcnt(4)
v_mfma_f32_16x16x32_bf16 v[24:27],a[24:27],v[48:51],v[24:27]
ds_read_b128 v[40:43],v4 offset:32768
v_mfma_f32_16x16x32_bf16 v[24:27],a[136:139],v[52:55],v[24:27]
v_mfma_f32_16x16x32_bf16 v[28:31],a[140:143],v[52:55],v[28:31]
ds_read_b128 v[44:47],v4 offset:47104
v_mfma_f32_16x16x32_bf16 v[28:31],a[28:31],v[48:51],v[28:31]
s_waitcnt lgkmcnt(4)
v_mfma_f32_16x16x32_bf16 v[28:31],a[252:255],v[56:59],v[28:31]
ds_read_b128 v[48:51],v4 offset:5120
v_mfma_f32_16x16x32_bf16 v[28:31],v[172:175],v[60:63],v[28:31]
buffer_load_dwordx4 v2,s[32:35],s63 offen offset:1024 sc1 lds
v_mfma_f32_16x16x32_bf16 v[24:27],v[168:171],v[60:63],v[24:27]
ds_read_b128 v[52:55],v4 offset:19456
v_mfma_f32_16x16x32_bf16 v[24:27],a[248:251],v[56:59],v[24:27]
s_waitcnt lgkmcnt(4)
v_mfma_f32_16x16x32_bf16 v[24:27],a[32:35],v[32:35],v[24:27]
ds_read_b128 v[56:59],v4 offset:33792
v_mfma_f32_16x16x32_bf16 v[24:27],a[144:147],v[36:39],v[24:27]
buffer_load_dwordx4 v2,s[32:35],s63 offen offset:2048 sc1 lds
v_mfma_f32_16x16x32_bf16 v[28:31],a[148:151],v[36:39],v[28:31]
ds_read_b128 v[60:63],v4 offset:48128
v_mfma_f32_16x16x32_bf16 v[28:31],a[36:39],v[32:35],v[28:31]
s_waitcnt lgkmcnt(4)
v_mfma_f32_16x16x32_bf16 v[28:31],v[68:71],v[40:43],v[28:31]
ds_read_b128 v[32:35],v4 offset:6144
v_mfma_f32_16x16x32_bf16 v[28:31],v[180:183],v[44:47],v[28:31]
buffer_load_dwordx4 v2,s[32:35],s63 offen offset:3072 sc1 lds
v_mfma_f32_16x16x32_bf16 v[24:27],v[176:179],v[44:47],v[24:27]
ds_read_b128 v[36:39],v4 offset:20480
v_mfma_f32_16x16x32_bf16 v[24:27],v[64:67],v[40:43],v[24:27]
s_waitcnt lgkmcnt(4)
v_mfma_f32_16x16x32_bf16 v[24:27],a[40:43],v[48:51],v[24:27]
ds_read_b128 v[40:43],v4 offset:34816
v_mfma_f32_16x16x32_bf16 v[24:27],a[152:155],v[52:55],v[24:27]
buffer_load_dwordx4 v[12:15],v2,s[32:35],s71 offen sc1
s_add_u32 s71,s71,1187840
v_mfma_f32_16x16x32_bf16 v[28:31],a[156:159],v[52:55],v[28:31]
ds_read_b128 v[44:47],v4 offset:49152
s_add_u32 s63,s63,4096
v_mfma_f32_16x16x32_bf16 v[28:31],a[44:47],v[48:51],v[28:31]
s_add_u32 m0,s64,4096
s_waitcnt lgkmcnt(4)
v_mfma_f32_16x16x32_bf16 v[28:31],v[76:79],v[56:59],v[28:31]
ds_read_b128 v[48:51],v4 offset:7168
v_mfma_f32_16x16x32_bf16 v[28:31],v[188:191],v[60:63],v[28:31]
buffer_load_dwordx4 v2,s[32:35],s63 offen offset:0 sc1 lds
v_mfma_f32_16x16x32_bf16 v[24:27],v[184:187],v[60:63],v[24:27]
ds_read_b128 v[52:55],v4 offset:21504
v_mfma_f32_16x16x32_bf16 v[24:27],v[72:75],v[56:59],v[24:27]
s_waitcnt lgkmcnt(4)
v_mfma_f32_16x16x32_bf16 v[24:27],a[48:51],v[32:35],v[24:27]
ds_read_b128 v[56:59],v4 offset:35840
v_mfma_f32_16x16x32_bf16 v[24:27],a[160:163],v[36:39],v[24:27]
buffer_load_dwordx4 v2,s[32:35],s63 offen offset:1024 sc1 lds
v_mfma_f32_16x16x32_bf16 v[28:31],a[164:167],v[36:39],v[28:31]
ds_read_b128 v[60:63],v4 offset:50176
v_mfma_f32_16x16x32_bf16 v[28:31],a[52:55],v[32:35],v[28:31]
s_waitcnt lgkmcnt(4)
v_mfma_f32_16x16x32_bf16 v[28:31],v[84:87],v[40:43],v[28:31]
ds_read_b128 v[32:35],v4 offset:8192
v_mfma_f32_16x16x32_bf16 v[28:31],v[196:199],v[44:47],v[28:31]
buffer_load_dwordx4 v2,s[32:35],s63 offen offset:2048 sc1 lds
v_mfma_f32_16x16x32_bf16 v[24:27],v[192:195],v[44:47],v[24:27]
ds_read_b128 v[36:39],v4 offset:22528
v_mfma_f32_16x16x32_bf16 v[24:27],v[80:83],v[40:43],v[24:27]
s_waitcnt lgkmcnt(4)
v_mfma_f32_16x16x32_bf16 v[24:27],a[56:59],v[48:51],v[24:27]
ds_read_b128 v[40:43],v4 offset:36864
v_mfma_f32_16x16x32_bf16 v[24:27],a[168:171],v[52:55],v[24:27]
buffer_load_dwordx4 v2,s[32:35],s63 offen offset:3072 sc1 lds
v_mfma_f32_16x16x32_bf16 v[28:31],a[172:175],v[52:55],v[28:31]
ds_read_b128 v[44:47],v4 offset:51200
v_mfma_f32_16x16x32_bf16 v[28:31],a[60:63],v[48:51],v[28:31]
s_waitcnt lgkmcnt(4)
v_mfma_f32_16x16x32_bf16 v[28:31],v[92:95],v[56:59],v[28:31]
ds_read_b128 v[48:51],v4 offset:9216
v_mfma_f32_16x16x32_bf16 v[28:31],v[204:207],v[60:63],v[28:31]
s_add_u32 s63,s63,4096
s_add_u32 m0,s64,8192
v_mfma_f32_16x16x32_bf16 v[24:27],v[200:203],v[60:63],v[24:27]
ds_read_b128 v[52:55],v4 offset:23552
v_mfma_f32_16x16x32_bf16 v[24:27],v[88:91],v[56:59],v[24:27]
s_waitcnt lgkmcnt(4)
v_mfma_f32_16x16x32_bf16 v[24:27],a[64:67],v[32:35],v[24:27]
ds_read_b128 v[56:59],v4 offset:37888
v_mfma_f32_16x16x32_bf16 v[24:27],a[176:179],v[36:39],v[24:27]
buffer_load_dwordx4 v2,s[32:35],s63 offen offset:0 sc1 lds
v_mfma_f32_16x16x32_bf16 v[28:31],a[180:183],v[36:39],v[28:31]
ds_read_b128 v[60:63],v4 offset:52224
v_mfma_f32_16x16x32_bf16 v[28:31],a[68:71],v[32:35],v[28:31]
s_waitcnt lgkmcnt(4)
v_mfma_f32_16x16x32_bf16 v[28:31],v[100:103],v[40:43],v[28:31]
ds_read_b128 v[32:35],v4 offset:10240
v_mfma_f32_16x16x32_bf16 v[28:31],v[212:215],v[44:47],v[28:31]
buffer_load_dwordx4 v2,s[32:35],s63 offen offset:1024 sc1 lds
v_mfma_f32_16x16x32_bf16 v[24:27],v[208:211],v[44:47],v[24:27]
ds_read_b128 v[36:39],v4 offset:24576
v_mfma_f32_16x16x32_bf16 v[24:27],v[96:99],v[40:43],v[24:27]
s_waitcnt lgkmcnt(4)
v_mfma_f32_16x16x32_bf16 v[24:27],a[72:75],v[48:51],v[24:27]
ds_read_b128 v[40:43],v4 offset:38912
v_mfma_f32_16x16x32_bf16 v[24:27],a[184:187],v[52:55],v[24:27]
buffer_load_dwordx4 v2,s[32:35],s63 offen offset:2048 sc1 lds
v_mfma_f32_16x16x32_bf16 v[28:31],a[188:191],v[52:55],v[28:31]
ds_read_b128 v[44:47],v4 offset:53248
v_mfma_f32_16x16x32_bf16 v[28:31],a[76:79],v[48:51],v[28:31]
s_waitcnt lgkmcnt(4)
v_mfma_f32_16x16x32_bf16 v[28:31],v[108:111],v[56:59],v[28:31]
ds_read_b128 v[48:51],v4 offset:11264
v_mfma_f32_16x16x32_bf16 v[28:31],v[220:223],v[60:63],v[28:31]
v_mfma_f32_16x16x32_bf16 v[24:27],v[216:219],v[60:63],v[24:27]
ds_read_b128 v[52:55],v4 offset:25600
v_mfma_f32_16x16x32_bf16 v[24:27],v[104:107],v[56:59],v[24:27]
s_waitcnt lgkmcnt(4)
v_mfma_f32_16x16x32_bf16 v[24:27],a[80:83],v[32:35],v[24:27]
ds_read_b128 v[56:59],v4 offset:39936
v_mfma_f32_16x16x32_bf16 v[24:27],a[192:195],v[36:39],v[24:27]
buffer_load_dwordx4 v2,s[32:35],s63 offen offset:3072 sc1 lds
s_add_u32 s63,s63,4096
v_mfma_f32_16x16x32_bf16 v[28:31],a[196:199],v[36:39],v[28:31]
ds_read_b128 v[60:63],v4 offset:54272
s_add_u32 m0,s64,12288
v_mfma_f32_16x16x32_bf16 v[28:31],a[84:87],v[32:35],v[28:31]
s_waitcnt lgkmcnt(4)
v_mfma_f32_16x16x32_bf16 v[28:31],v[116:119],v[40:43],v[28:31]
ds_read_b128 v[32:35],v4 offset:12288
v_mfma_f32_16x16x32_bf16 v[28:31],v[228:231],v[44:47],v[28:31]
buffer_load_dwordx4 v2,s[32:35],s63 offen offset:0 sc1 lds
v_mfma_f32_16x16x32_bf16 v[24:27],v[224:227],v[44:47],v[24:27]
ds_read_b128 v[36:39],v4 offset:26624
v_mfma_f32_16x16x32_bf16 v[24:27],v[112:115],v[40:43],v[24:27]
s_waitcnt lgkmcnt(4)
v_mfma_f32_16x16x32_bf16 v[24:27],a[88:91],v[48:51],v[24:27]
ds_read_b128 v[40:43],v4 offset:40960
v_mfma_f32_16x16x32_bf16 v[24:27],a[200:203],v[52:55],v[24:27]
buffer_load_dwordx4 v2,s[32:35],s63 offen offset:1024 sc1 lds
s_add_u32 s63,s63,1175552
v_mfma_f32_16x16x32_bf16 v[28:31],a[204:207],v[52:55],v[28:31]
ds_read_b128 v[44:47],v4 offset:55296
v_mfma_f32_16x16x32_bf16 v[28:31],a[92:95],v[48:51],v[28:31]
s_waitcnt lgkmcnt(4)
v_mfma_f32_16x16x32_bf16 v[28:31],v[124:127],v[56:59],v[28:31]
ds_read_b128 v[48:51],v4 offset:13312
s_waitcnt vmcnt(10)
v_mfma_f32_16x16x32_bf16 v[28:31],v[236:239],v[60:63],v[28:31]
v_lshlrev_b32 v16,16,v12
v_and_b32 v17,0xffff0000,v12
v_mfma_f32_16x16x32_bf16 v[24:27],v[232:235],v[60:63],v[24:27]
ds_read_b128 v[52:55],v4 offset:27648
v_lshlrev_b32 v18,16,v13
v_mfma_f32_16x16x32_bf16 v[24:27],v[120:123],v[56:59],v[24:27]
v_and_b32 v19,0xffff0000,v13
s_waitcnt lgkmcnt(4)
v_mfma_f32_16x16x32_bf16 v[24:27],a[96:99],v[32:35],v[24:27]
ds_read_b128 v[56:59],v4 offset:41984
v_lshlrev_b32 v20,16,v14
v_mfma_f32_16x16x32_bf16 v[24:27],a[208:211],v[36:39],v[24:27]
v_and_b32 v21,0xffff0000,v14
v_lshlrev_b32 v22,16,v15
v_mfma_f32_16x16x32_bf16 v[28:31],a[212:215],v[36:39],v[28:31]
ds_read_b128 v[60:63],v4 offset:56320
v_and_b32 v23,0xffff0000,v15
v_mfma_f32_16x16x32_bf16 v[28:31],a[100:103],v[32:35],v[28:31]
s_waitcnt lgkmcnt(4)
s_barrier
v_mfma_f32_16x16x32_bf16 v[28:31],v[132:135],v[40:43],v[28:31]
ds_read_b128 v[32:35],v2 offset:0
v_mfma_f32_16x16x32_bf16 v[28:31],v[244:247],v[44:47],v[28:31]
v_mfma_f32_16x16x32_bf16 v[24:27],v[240:243],v[44:47],v[24:27]
ds_read_b128 v[36:39],v2 offset:14336
v_mfma_f32_16x16x32_bf16 v[24:27],v[128:131],v[40:43],v[24:27]
s_waitcnt lgkmcnt(4)
v_mfma_f32_16x16x32_bf16 v[24:27],a[104:107],v[48:51],v[24:27]
ds_read_b128 v[40:43],v2 offset:28672
v_mfma_f32_16x16x32_bf16 v[24:27],a[216:219],v[52:55],v[24:27]
buffer_store_dwordx2 v[8:9],v2,s[92:95],s67 idxen sc1
v_mfma_f32_16x16x32_bf16 v[28:31],a[220:223],v[52:55],v[28:31]
ds_read_b128 v[44:47],v2 offset:43008
v_mfma_f32_16x16x32_bf16 v[28:31],a[108:111],v[48:51],v[28:31]
s_waitcnt lgkmcnt(4)
v_mfma_f32_16x16x32_bf16 v[28:31],v[140:143],v[56:59],v[28:31]
ds_read_b128 v[48:51],v2 offset:1024
v_mfma_f32_16x16x32_bf16 v[28:31],v[252:255],v[60:63],v[28:31]
v_mfma_f32_16x16x32_bf16 v[24:27],v[248:251],v[60:63],v[24:27]
ds_read_b128 v[52:55],v2 offset:15360
v_mfma_f32_16x16x32_bf16 v[24:27],v[136:139],v[56:59],v[24:27]
s_branch .L_dll_0
.L_dlb_0:
s_mov_b32 s62,15
s_add_u32 s74,s74,8192
v_lshrrev_b32 v5,8,v2
v_lshl_add_u32 v5,v5,4,s74
v_and_b32 v3,128,v2
v_lshl_add_u32 v5,v3,2,v5
v_and_b32 v3,16,v2
v_lshl_add_u32 v5,v3,4,v5
v_and_b32 v3,96,v2
v_lshl_add_u32 v5,v3,1,v5
v_lshrrev_b32 v3,2,v2
v_add_u32 v3,s74,v3
.p2align 6
.L_dml_0:
s_waitcnt lgkmcnt(4)
v_mfma_f32_16x16x32_bf16 v[24:27],a[0:3],v[32:35],v[24:27]
ds_read_b128 v[56:59],v4 offset:29696
v_mfma_f32_16x16x32_bf16 v[24:27],a[112:115],v[36:39],v[24:27]
v_mfma_f32_16x16x32_bf16 v[28:31],a[116:119],v[36:39],v[28:31]
ds_read_b128 v[60:63],v4 offset:44032
v_mfma_f32_16x16x32_bf16 v[28:31],a[4:7],v[32:35],v[28:31]
v_cvt_pk_bf16_f32 v15,v22,v23
s_waitcnt lgkmcnt(4)
v_mfma_f32_16x16x32_bf16 v[28:31],a[228:231],v[40:43],v[28:31]
ds_read_b128 v[32:35],v4 offset:2048
v_cvt_pk_bf16_f32 v14,v20,v21
v_mfma_f32_16x16x32_bf16 v[28:31],v[148:151],v[44:47],v[28:31]
v_cvt_pk_bf16_f32 v13,v18,v19
v_cvt_pk_bf16_f32 v12,v16,v17
v_mfma_f32_16x16x32_bf16 v[24:27],v[144:147],v[44:47],v[24:27]
ds_read_b128 v[36:39],v4 offset:16384
ds_write_b128 v5,v[12:15]
v_mfma_f32_16x16x32_bf16 v[24:27],a[224:227],v[40:43],v[24:27]
ds_read2st64_b32 v[12:13],v3 offset0:0 offset1:1
s_waitcnt lgkmcnt(6)
v_mfma_f32_16x16x32_bf16 v[24:27],a[8:11],v[48:51],v[24:27]
ds_read_b128 v[40:43],v4 offset:30720
ds_read2st64_b32 v[14:15],v3 offset0:2 offset1:3
v_mfma_f32_16x16x32_bf16 v[24:27],a[120:123],v[52:55],v[24:27]
v_mfma_f32_16x16x32_bf16 v[28:31],a[124:127],v[52:55],v[28:31]
ds_read_b128 v[44:47],v4 offset:45056
v_mfma_f32_16x16x32_bf16 v[28:31],a[12:15],v[48:51],v[28:31]
s_waitcnt lgkmcnt(7)
v_mfma_f32_16x16x32_bf16 v[28:31],a[236:239],v[56:59],v[28:31]
ds_read_b128 v[48:51],v4 offset:3072
v_mfma_f32_16x16x32_bf16 v[28:31],v[156:159],v[60:63],v[28:31]
s_waitcnt lgkmcnt(2)
buffer_atomic_pk_add_bf16 v12,v1,s[44:47],s65 offen offset:0 sc1
v_mfma_f32_16x16x32_bf16 v[24:27],v[152:155],v[60:63],v[24:27]
ds_read_b128 v[52:55],v4 offset:17408
v_mfma_f32_16x16x32_bf16 v[24:27],a[232:235],v[56:59],v[24:27]
buffer_atomic_pk_add_bf16 v13,v1,s[44:47],s65 offen offset:2048 sc1
v_mfma_f32_16x16x32_bf16 v[24:27],a[16:19],v[32:35],v[24:27]
ds_read_b128 v[56:59],v4 offset:31744
s_add_u32 s65,s65,16384
v_mfma_f32_16x16x32_bf16 v[24:27],a[128:131],v[36:39],v[24:27]
buffer_atomic_pk_add_bf16 v14,v1,s[44:47],s65 offen offset:0 sc1
v_mfma_f32_16x16x32_bf16 v[28:31],a[132:135],v[36:39],v[28:31]
ds_read_b128 v[60:63],v4 offset:46080
v_mfma_f32_16x16x32_bf16 v[28:31],a[20:23],v[32:35],v[28:31]
s_waitcnt lgkmcnt(4)
s_waitcnt vmcnt(3)
s_barrier
v_mfma_f32_16x16x32_bf16 v[28:31],a[244:247],v[40:43],v[28:31]
ds_read_b128 v[32:35],v4 offset:4096
s_add_u32 m0,s64,0
v_mfma_f32_16x16x32_bf16 v[28:31],v[164:167],v[44:47],v[28:31]
buffer_atomic_pk_add_bf16 v15,v1,s[44:47],s65 offen offset:2048 sc1
s_add_u32 s65,s65,16384
v_mfma_f32_16x16x32_bf16 v[24:27],v[160:163],v[44:47],v[24:27]
ds_read_b128 v[36:39],v4 offset:18432
v_mfma_f32_16x16x32_bf16 v[24:27],a[240:243],v[40:43],v[24:27]
s_waitcnt lgkmcnt(4)
v_mfma_f32_16x16x32_bf16 v[24:27],a[24:27],v[48:51],v[24:27]
ds_read_b128 v[40:43],v4 offset:32768
v_mfma_f32_16x16x32_bf16 v[24:27],a[136:139],v[52:55],v[24:27]
buffer_load_dwordx4 v2,s[32:35],s63 offen offset:0 sc1 lds
v_mfma_f32_16x16x32_bf16 v[28:31],a[140:143],v[52:55],v[28:31]
ds_read_b128 v[44:47],v4 offset:47104
v_mfma_f32_16x16x32_bf16 v[28:31],a[28:31],v[48:51],v[28:31]
s_waitcnt lgkmcnt(4)
v_mfma_f32_16x16x32_bf16 v[28:31],a[252:255],v[56:59],v[28:31]
ds_read_b128 v[48:51],v4 offset:5120
v_mfma_f32_16x16x32_bf16 v[28:31],v[172:175],v[60:63],v[28:31]
buffer_load_dwordx4 v2,s[32:35],s63 offen offset:1024 sc1 lds
v_mfma_f32_16x16x32_bf16 v[24:27],v[168:171],v[60:63],v[24:27]
ds_read_b128 v[52:55],v4 offset:19456
v_mfma_f32_16x16x32_bf16 v[24:27],a[248:251],v[56:59],v[24:27]
s_waitcnt lgkmcnt(4)
v_mfma_f32_16x16x32_bf16 v[24:27],a[32:35],v[32:35],v[24:27]
ds_read_b128 v[56:59],v4 offset:33792
v_mfma_f32_16x16x32_bf16 v[24:27],a[144:147],v[36:39],v[24:27]
buffer_load_dwordx4 v2,s[32:35],s63 offen offset:2048 sc1 lds
v_mfma_f32_16x16x32_bf16 v[28:31],a[148:151],v[36:39],v[28:31]
ds_read_b128 v[60:63],v4 offset:48128
v_mfma_f32_16x16x32_bf16 v[28:31],a[36:39],v[32:35],v[28:31]
s_waitcnt lgkmcnt(4)
v_mfma_f32_16x16x32_bf16 v[28:31],v[68:71],v[40:43],v[28:31]
ds_read_b128 v[32:35],v4 offset:6144
v_mfma_f32_16x16x32_bf16 v[28:31],v[180:183],v[44:47],v[28:31]
buffer_load_dwordx4 v2,s[32:35],s63 offen offset:3072 sc1 lds
v_mfma_f32_16x16x32_bf16 v[24:27],v[176:179],v[44:47],v[24:27]
ds_read_b128 v[36:39],v4 offset:20480
v_mfma_f32_16x16x32_bf16 v[24:27],v[64:67],v[40:43],v[24:27]
s_waitcnt lgkmcnt(4)
v_mfma_f32_16x16x32_bf16 v[24:27],a[40:43],v[48:51],v[24:27]
ds_read_b128 v[40:43],v4 offset:34816
v_mfma_f32_16x16x32_bf16 v[24:27],a[152:155],v[52:55],v[24:27]
buffer_load_dwordx4 v[12:15],v2,s[32:35],s71 offen sc1
s_add_u32 s71,s71,1187840
v_mfma_f32_16x16x32_bf16 v[28:31],a[156:159],v[52:55],v[28:31]
ds_read_b128 v[44:47],v4 offset:49152
s_add_u32 s63,s63,4096
v_mfma_f32_16x16x32_bf16 v[28:31],a[44:47],v[48:51],v[28:31]
s_add_u32 m0,s64,4096
s_waitcnt lgkmcnt(4)
v_mfma_f32_16x16x32_bf16 v[28:31],v[76:79],v[56:59],v[28:31]
ds_read_b128 v[48:51],v4 offset:7168
v_mfma_f32_16x16x32_bf16 v[28:31],v[188:191],v[60:63],v[28:31]
buffer_load_dwordx4 v2,s[32:35],s63 offen offset:0 sc1 lds
v_mfma_f32_16x16x32_bf16 v[24:27],v[184:187],v[60:63],v[24:27]
ds_read_b128 v[52:55],v4 offset:21504
v_mfma_f32_16x16x32_bf16 v[24:27],v[72:75],v[56:59],v[24:27]
s_waitcnt lgkmcnt(4)
v_mfma_f32_16x16x32_bf16 v[24:27],a[48:51],v[32:35],v[24:27]
ds_read_b128 v[56:59],v4 offset:35840
v_mfma_f32_16x16x32_bf16 v[24:27],a[160:163],v[36:39],v[24:27]
buffer_load_dwordx4 v2,s[32:35],s63 offen offset:1024 sc1 lds
v_mfma_f32_16x16x32_bf16 v[28:31],a[164:167],v[36:39],v[28:31]
ds_read_b128 v[60:63],v4 offset:50176
v_mfma_f32_16x16x32_bf16 v[28:31],a[52:55],v[32:35],v[28:31]
s_waitcnt lgkmcnt(4)
v_mfma_f32_16x16x32_bf16 v[28:31],v[84:87],v[40:43],v[28:31]
ds_read_b128 v[32:35],v4 offset:8192
v_mfma_f32_16x16x32_bf16 v[28:31],v[196:199],v[44:47],v[28:31]
buffer_load_dwordx4 v2,s[32:35],s63 offen offset:2048 sc1 lds
v_mfma_f32_16x16x32_bf16 v[24:27],v[192:195],v[44:47],v[24:27]
ds_read_b128 v[36:39],v4 offset:22528
v_mfma_f32_16x16x32_bf16 v[24:27],v[80:83],v[40:43],v[24:27]
s_waitcnt lgkmcnt(4)
v_mfma_f32_16x16x32_bf16 v[24:27],a[56:59],v[48:51],v[24:27]
ds_read_b128 v[40:43],v4 offset:36864
v_mfma_f32_16x16x32_bf16 v[24:27],a[168:171],v[52:55],v[24:27]
buffer_load_dwordx4 v2,s[32:35],s63 offen offset:3072 sc1 lds
v_mfma_f32_16x16x32_bf16 v[28:31],a[172:175],v[52:55],v[28:31]
ds_read_b128 v[44:47],v4 offset:51200
v_mfma_f32_16x16x32_bf16 v[28:31],a[60:63],v[48:51],v[28:31]
s_waitcnt lgkmcnt(4)
v_mfma_f32_16x16x32_bf16 v[28:31],v[92:95],v[56:59],v[28:31]
ds_read_b128 v[48:51],v4 offset:9216
v_mfma_f32_16x16x32_bf16 v[28:31],v[204:207],v[60:63],v[28:31]
s_add_u32 s63,s63,4096
s_add_u32 m0,s64,8192
v_mfma_f32_16x16x32_bf16 v[24:27],v[200:203],v[60:63],v[24:27]
ds_read_b128 v[52:55],v4 offset:23552
v_mfma_f32_16x16x32_bf16 v[24:27],v[88:91],v[56:59],v[24:27]
s_waitcnt lgkmcnt(4)
v_mfma_f32_16x16x32_bf16 v[24:27],a[64:67],v[32:35],v[24:27]
ds_read_b128 v[56:59],v4 offset:37888
v_mfma_f32_16x16x32_bf16 v[24:27],a[176:179],v[36:39],v[24:27]
buffer_load_dwordx4 v2,s[32:35],s63 offen offset:0 sc1 lds
v_mfma_f32_16x16x32_bf16 v[28:31],a[180:183],v[36:39],v[28:31]
ds_read_b128 v[60:63],v4 offset:52224
v_mfma_f32_16x16x32_bf16 v[28:31],a[68:71],v[32:35],v[28:31]
s_waitcnt lgkmcnt(4)
v_mfma_f32_16x16x32_bf16 v[28:31],v[100:103],v[40:43],v[28:31]
ds_read_b128 v[32:35],v4 offset:10240
v_mfma_f32_16x16x32_bf16 v[28:31],v[212:215],v[44:47],v[28:31]
buffer_load_dwordx4 v2,s[32:35],s63 offen offset:1024 sc1 lds
v_mfma_f32_16x16x32_bf16 v[24:27],v[208:211],v[44:47],v[24:27]
ds_read_b128 v[36:39],v4 offset:24576
v_mfma_f32_16x16x32_bf16 v[24:27],v[96:99],v[40:43],v[24:27]
s_waitcnt lgkmcnt(4)
v_mfma_f32_16x16x32_bf16 v[24:27],a[72:75],v[48:51],v[24:27]
ds_read_b128 v[40:43],v4 offset:38912
v_mfma_f32_16x16x32_bf16 v[24:27],a[184:187],v[52:55],v[24:27]
buffer_load_dwordx4 v2,s[32:35],s63 offen offset:2048 sc1 lds
v_mfma_f32_16x16x32_bf16 v[28:31],a[188:191],v[52:55],v[28:31]
ds_read_b128 v[44:47],v4 offset:53248
v_mfma_f32_16x16x32_bf16 v[28:31],a[76:79],v[48:51],v[28:31]
s_waitcnt lgkmcnt(4)
v_mfma_f32_16x16x32_bf16 v[28:31],v[108:111],v[56:59],v[28:31]
ds_read_b128 v[48:51],v4 offset:11264
v_mfma_f32_16x16x32_bf16 v[28:31],v[220:223],v[60:63],v[28:31]
v_mfma_f32_16x16x32_bf16 v[24:27],v[216:219],v[60:63],v[24:27]
ds_read_b128 v[52:55],v4 offset:25600
v_mfma_f32_16x16x32_bf16 v[24:27],v[104:107],v[56:59],v[24:27]
s_waitcnt lgkmcnt(4)
v_mfma_f32_16x16x32_bf16 v[24:27],a[80:83],v[32:35],v[24:27]
ds_read_b128 v[56:59],v4 offset:39936
v_mfma_f32_16x16x32_bf16 v[24:27],a[192:195],v[36:39],v[24:27]
buffer_load_dwordx4 v2,s[32:35],s63 offen offset:3072 sc1 lds
s_add_u32 s63,s63,4096
v_mfma_f32_16x16x32_bf16 v[28:31],a[196:199],v[36:39],v[28:31]
)#"
R"#(
ds_read_b128 v[60:63],v4 offset:54272
s_add_u32 m0,s64,12288
v_mfma_f32_16x16x32_bf16 v[28:31],a[84:87],v[32:35],v[28:31]
s_waitcnt lgkmcnt(4)
v_mfma_f32_16x16x32_bf16 v[28:31],v[116:119],v[40:43],v[28:31]
ds_read_b128 v[32:35],v4 offset:12288
v_mfma_f32_16x16x32_bf16 v[28:31],v[228:231],v[44:47],v[28:31]
buffer_load_dwordx4 v2,s[32:35],s63 offen offset:0 sc1 lds
v_mfma_f32_16x16x32_bf16 v[24:27],v[224:227],v[44:47],v[24:27]
ds_read_b128 v[36:39],v4 offset:26624
v_mfma_f32_16x16x32_bf16 v[24:27],v[112:115],v[40:43],v[24:27]
s_waitcnt lgkmcnt(4)
v_mfma_f32_16x16x32_bf16 v[24:27],a[88:91],v[48:51],v[24:27]
ds_read_b128 v[40:43],v4 offset:40960
v_mfma_f32_16x16x32_bf16 v[24:27],a[200:203],v[52:55],v[24:27]
buffer_load_dwordx4 v2,s[32:35],s63 offen offset:1024 sc1 lds
s_add_u32 s63,s63,1175552
v_mfma_f32_16x16x32_bf16 v[28:31],a[204:207],v[52:55],v[28:31]
ds_read_b128 v[44:47],v4 offset:55296
v_mfma_f32_16x16x32_bf16 v[28:31],a[92:95],v[48:51],v[28:31]
s_waitcnt lgkmcnt(4)
v_mfma_f32_16x16x32_bf16 v[28:31],v[124:127],v[56:59],v[28:31]
ds_read_b128 v[48:51],v4 offset:13312
s_waitcnt vmcnt(10)
v_mfma_f32_16x16x32_bf16 v[28:31],v[236:239],v[60:63],v[28:31]
v_lshlrev_b32 v16,16,v12
v_and_b32 v17,0xffff0000,v12
v_mfma_f32_16x16x32_bf16 v[24:27],v[232:235],v[60:63],v[24:27]
ds_read_b128 v[52:55],v4 offset:27648
v_lshlrev_b32 v18,16,v13
v_mfma_f32_16x16x32_bf16 v[24:27],v[120:123],v[56:59],v[24:27]
v_and_b32 v19,0xffff0000,v13
s_waitcnt lgkmcnt(4)
v_mfma_f32_16x16x32_bf16 v[24:27],a[96:99],v[32:35],v[24:27]
ds_read_b128 v[56:59],v4 offset:41984
v_lshlrev_b32 v20,16,v14
v_mfma_f32_16x16x32_bf16 v[24:27],a[208:211],v[36:39],v[24:27]
v_and_b32 v21,0xffff0000,v14
v_lshlrev_b32 v22,16,v15
v_mfma_f32_16x16x32_bf16 v[28:31],a[212:215],v[36:39],v[28:31]
ds_read_b128 v[60:63],v4 offset:56320
v_and_b32 v23,0xffff0000,v15
v_mfma_f32_16x16x32_bf16 v[28:31],a[100:103],v[32:35],v[28:31]
s_waitcnt lgkmcnt(4)
s_barrier
v_mfma_f32_16x16x32_bf16 v[28:31],v[132:135],v[40:43],v[28:31]
ds_read_b128 v[32:35],v2 offset:0
v_mfma_f32_16x16x32_bf16 v[28:31],v[244:247],v[44:47],v[28:31]
v_mfma_f32_16x16x32_bf16 v[24:27],v[240:243],v[44:47],v[24:27]
ds_read_b128 v[36:39],v2 offset:14336
v_mfma_f32_16x16x32_bf16 v[24:27],v[128:131],v[40:43],v[24:27]
s_waitcnt lgkmcnt(4)
v_mfma_f32_16x16x32_bf16 v[24:27],a[104:107],v[48:51],v[24:27]
ds_read_b128 v[40:43],v2 offset:28672
v_mfma_f32_16x16x32_bf16 v[24:27],a[216:219],v[52:55],v[24:27]
v_mfma_f32_16x16x32_bf16 v[28:31],a[220:223],v[52:55],v[28:31]
ds_read_b128 v[44:47],v2 offset:43008
v_mfma_f32_16x16x32_bf16 v[28:31],a[108:111],v[48:51],v[28:31]
s_waitcnt lgkmcnt(4)
v_mfma_f32_16x16x32_bf16 v[28:31],v[140:143],v[56:59],v[28:31]
ds_read_b128 v[48:51],v2 offset:1024
v_mfma_f32_16x16x32_bf16 v[28:31],v[252:255],v[60:63],v[28:31]
v_mfma_f32_16x16x32_bf16 v[24:27],v[248:251],v[60:63],v[24:27]
ds_read_b128 v[52:55],v2 offset:15360
s_sub_u32 s62,s62,1
v_mfma_f32_16x16x32_bf16 v[24:27],v[136:139],v[56:59],v[24:27]
s_cbranch_scc1 .L_dmt_0
s_waitcnt lgkmcnt(4)
v_mfma_f32_16x16x32_bf16 v[16:19],a[0:3],v[32:35],v[16:19]
ds_read_b128 v[56:59],v2 offset:29696
v_mfma_f32_16x16x32_bf16 v[16:19],a[112:115],v[36:39],v[16:19]
v_mfma_f32_16x16x32_bf16 v[20:23],a[116:119],v[36:39],v[20:23]
ds_read_b128 v[60:63],v2 offset:44032
v_mfma_f32_16x16x32_bf16 v[20:23],a[4:7],v[32:35],v[20:23]
v_cvt_pk_bf16_f32 v15,v30,v31
s_waitcnt lgkmcnt(4)
v_mfma_f32_16x16x32_bf16 v[20:23],a[228:231],v[40:43],v[20:23]
ds_read_b128 v[32:35],v2 offset:2048
v_cvt_pk_bf16_f32 v14,v28,v29
v_mfma_f32_16x16x32_bf16 v[20:23],v[148:151],v[44:47],v[20:23]
v_cvt_pk_bf16_f32 v13,v26,v27
v_cvt_pk_bf16_f32 v12,v24,v25
v_mfma_f32_16x16x32_bf16 v[16:19],v[144:147],v[44:47],v[16:19]
ds_read_b128 v[36:39],v2 offset:16384
ds_write_b128 v5,v[12:15]
v_mfma_f32_16x16x32_bf16 v[16:19],a[224:227],v[40:43],v[16:19]
ds_read2st64_b32 v[12:13],v3 offset0:0 offset1:1
s_waitcnt lgkmcnt(6)
v_mfma_f32_16x16x32_bf16 v[16:19],a[8:11],v[48:51],v[16:19]
ds_read_b128 v[40:43],v2 offset:30720
ds_read2st64_b32 v[14:15],v3 offset0:2 offset1:3
v_mfma_f32_16x16x32_bf16 v[16:19],a[120:123],v[52:55],v[16:19]
v_mfma_f32_16x16x32_bf16 v[20:23],a[124:127],v[52:55],v[20:23]
ds_read_b128 v[44:47],v2 offset:45056
v_mfma_f32_16x16x32_bf16 v[20:23],a[12:15],v[48:51],v[20:23]
s_waitcnt lgkmcnt(7)
v_mfma_f32_16x16x32_bf16 v[20:23],a[236:239],v[56:59],v[20:23]
ds_read_b128 v[48:51],v2 offset:3072
v_mfma_f32_16x16x32_bf16 v[20:23],v[156:159],v[60:63],v[20:23]
s_waitcnt lgkmcnt(2)
buffer_atomic_pk_add_bf16 v12,v1,s[44:47],s65 offen offset:0 sc1
v_mfma_f32_16x16x32_bf16 v[16:19],v[152:155],v[60:63],v[16:19]
ds_read_b128 v[52:55],v2 offset:17408
v_mfma_f32_16x16x32_bf16 v[16:19],a[232:235],v[56:59],v[16:19]
buffer_atomic_pk_add_bf16 v13,v1,s[44:47],s65 offen offset:2048 sc1
v_mfma_f32_16x16x32_bf16 v[16:19],a[16:19],v[32:35],v[16:19]
ds_read_b128 v[56:59],v2 offset:31744
s_add_u32 s65,s65,16384
v_mfma_f32_16x16x32_bf16 v[16:19],a[128:131],v[36:39],v[16:19]
buffer_atomic_pk_add_bf16 v14,v1,s[44:47],s65 offen offset:0 sc1
v_mfma_f32_16x16x32_bf16 v[20:23],a[132:135],v[36:39],v[20:23]
ds_read_b128 v[60:63],v2 offset:46080
v_mfma_f32_16x16x32_bf16 v[20:23],a[20:23],v[32:35],v[20:23]
s_waitcnt lgkmcnt(4)
s_waitcnt vmcnt(3)
s_barrier
v_mfma_f32_16x16x32_bf16 v[20:23],a[244:247],v[40:43],v[20:23]
ds_read_b128 v[32:35],v2 offset:4096
s_add_u32 m0,s64,57344
v_mfma_f32_16x16x32_bf16 v[20:23],v[164:167],v[44:47],v[20:23]
buffer_atomic_pk_add_bf16 v15,v1,s[44:47],s65 offen offset:2048 sc1
s_add_u32 s65,s65,16384
v_mfma_f32_16x16x32_bf16 v[16:19],v[160:163],v[44:47],v[16:19]
ds_read_b128 v[36:39],v2 offset:18432
v_mfma_f32_16x16x32_bf16 v[16:19],a[240:243],v[40:43],v[16:19]
s_waitcnt lgkmcnt(4)
v_mfma_f32_16x16x32_bf16 v[16:19],a[24:27],v[48:51],v[16:19]
ds_read_b128 v[40:43],v2 offset:32768
v_mfma_f32_16x16x32_bf16 v[16:19],a[136:139],v[52:55],v[16:19]
buffer_load_dwordx4 v2,s[32:35],s63 offen offset:0 sc1 lds
v_mfma_f32_16x16x32_bf16 v[20:23],a[140:143],v[52:55],v[20:23]
ds_read_b128 v[44:47],v2 offset:47104
v_mfma_f32_16x16x32_bf16 v[20:23],a[28:31],v[48:51],v[20:23]
s_waitcnt lgkmcnt(4)
v_mfma_f32_16x16x32_bf16 v[20:23],a[252:255],v[56:59],v[20:23]
ds_read_b128 v[48:51],v2 offset:5120
v_mfma_f32_16x16x32_bf16 v[20:23],v[172:175],v[60:63],v[20:23]
buffer_load_dwordx4 v2,s[32:35],s63 offen offset:1024 sc1 lds
v_mfma_f32_16x16x32_bf16 v[16:19],v[168:171],v[60:63],v[16:19]
ds_read_b128 v[52:55],v2 offset:19456
v_mfma_f32_16x16x32_bf16 v[16:19],a[248:251],v[56:59],v[16:19]
s_waitcnt lgkmcnt(4)
v_mfma_f32_16x16x32_bf16 v[16:19],a[32:35],v[32:35],v[16:19]
ds_read_b128 v[56:59],v2 offset:33792
v_mfma_f32_16x16x32_bf16 v[16:19],a[144:147],v[36:39],v[16:19]
buffer_load_dwordx4 v2,s[32:35],s63 offen offset:2048 sc1 lds
v_mfma_f32_16x16x32_bf16 v[20:23],a[148:151],v[36:39],v[20:23]
ds_read_b128 v[60:63],v2 offset:48128
v_mfma_f32_16x16x32_bf16 v[20:23],a[36:39],v[32:35],v[20:23]
s_waitcnt lgkmcnt(4)
v_mfma_f32_16x16x32_bf16 v[20:23],v[68:71],v[40:43],v[20:23]
ds_read_b128 v[32:35],v2 offset:6144
v_mfma_f32_16x16x32_bf16 v[20:23],v[180:183],v[44:47],v[20:23]
buffer_load_dwordx4 v2,s[32:35],s63 offen offset:3072 sc1 lds
v_mfma_f32_16x16x32_bf16 v[16:19],v[176:179],v[44:47],v[16:19]
ds_read_b128 v[36:39],v2 offset:20480
v_mfma_f32_16x16x32_bf16 v[16:19],v[64:67],v[40:43],v[16:19]
s_waitcnt lgkmcnt(4)
v_mfma_f32_16x16x32_bf16 v[16:19],a[40:43],v[48:51],v[16:19]
ds_read_b128 v[40:43],v2 offset:34816
v_mfma_f32_16x16x32_bf16 v[16:19],a[152:155],v[52:55],v[16:19]
buffer_load_dwordx4 v[12:15],v2,s[32:35],s71 offen sc1
s_add_u32 s71,s71,1187840
v_mfma_f32_16x16x32_bf16 v[20:23],a[156:159],v[52:55],v[20:23]
ds_read_b128 v[44:47],v2 offset:49152
s_add_u32 s63,s63,4096
v_mfma_f32_16x16x32_bf16 v[20:23],a[44:47],v[48:51],v[20:23]
s_add_u32 m0,s64,61440
s_waitcnt lgkmcnt(4)
v_mfma_f32_16x16x32_bf16 v[20:23],v[76:79],v[56:59],v[20:23]
ds_read_b128 v[48:51],v2 offset:7168
v_mfma_f32_16x16x32_bf16 v[20:23],v[188:191],v[60:63],v[20:23]
buffer_load_dwordx4 v2,s[32:35],s63 offen offset:0 sc1 lds
v_mfma_f32_16x16x32_bf16 v[16:19],v[184:187],v[60:63],v[16:19]
ds_read_b128 v[52:55],v2 offset:21504
v_mfma_f32_16x16x32_bf16 v[16:19],v[72:75],v[56:59],v[16:19]
s_waitcnt lgkmcnt(4)
v_mfma_f32_16x16x32_bf16 v[16:19],a[48:51],v[32:35],v[16:19]
ds_read_b128 v[56:59],v2 offset:35840
v_mfma_f32_16x16x32_bf16 v[16:19],a[160:163],v[36:39],v[16:19]
buffer_load_dwordx4 v2,s[32:35],s63 offen offset:1024 sc1 lds
v_mfma_f32_16x16x32_bf16 v[20:23],a[164:167],v[36:39],v[20:23]
ds_read_b128 v[60:63],v2 offset:50176
v_mfma_f32_16x16x32_bf16 v[20:23],a[52:55],v[32:35],v[20:23]
s_waitcnt lgkmcnt(4)
v_mfma_f32_16x16x32_bf16 v[20:23],v[84:87],v[40:43],v[20:23]
ds_read_b128 v[32:35],v2 offset:8192
v_mfma_f32_16x16x32_bf16 v[20:23],v[196:199],v[44:47],v[20:23]
buffer_load_dwordx4 v2,s[32:35],s63 offen offset:2048 sc1 lds
v_mfma_f32_16x16x32_bf16 v[16:19],v[192:195],v[44:47],v[16:19]
ds_read_b128 v[36:39],v2 offset:22528
v_mfma_f32_16x16x32_bf16 v[16:19],v[80:83],v[40:43],v[16:19]
s_waitcnt lgkmcnt(4)
v_mfma_f32_16x16x32_bf16 v[16:19],a[56:59],v[48:51],v[16:19]
ds_read_b128 v[40:43],v2 offset:36864
v_mfma_f32_16x16x32_bf16 v[16:19],a[168:171],v[52:55],v[16:19]
buffer_load_dwordx4 v2,s[32:35],s63 offen offset:3072 sc1 lds
v_mfma_f32_16x16x32_bf16 v[20:23],a[172:175],v[52:55],v[20:23]
ds_read_b128 v[44:47],v2 offset:51200
v_mfma_f32_16x16x32_bf16 v[20:23],a[60:63],v[48:51],v[20:23]
s_waitcnt lgkmcnt(4)
v_mfma_f32_16x16x32_bf16 v[20:23],v[92:95],v[56:59],v[20:23]
ds_read_b128 v[48:51],v2 offset:9216
v_mfma_f32_16x16x32_bf16 v[20:23],v[204:207],v[60:63],v[20:23]
s_add_u32 s63,s63,4096
s_add_u32 m0,s64,65536
v_mfma_f32_16x16x32_bf16 v[16:19],v[200:203],v[60:63],v[16:19]
ds_read_b128 v[52:55],v2 offset:23552
v_mfma_f32_16x16x32_bf16 v[16:19],v[88:91],v[56:59],v[16:19]
s_waitcnt lgkmcnt(4)
v_mfma_f32_16x16x32_bf16 v[16:19],a[64:67],v[32:35],v[16:19]
ds_read_b128 v[56:59],v2 offset:37888
v_mfma_f32_16x16x32_bf16 v[16:19],a[176:179],v[36:39],v[16:19]
buffer_load_dwordx4 v2,s[32:35],s63 offen offset:0 sc1 lds
v_mfma_f32_16x16x32_bf16 v[20:23],a[180:183],v[36:39],v[20:23]
ds_read_b128 v[60:63],v2 offset:52224
v_mfma_f32_16x16x32_bf16 v[20:23],a[68:71],v[32:35],v[20:23]
s_waitcnt lgkmcnt(4)
v_mfma_f32_16x16x32_bf16 v[20:23],v[100:103],v[40:43],v[20:23]
ds_read_b128 v[32:35],v2 offset:10240
v_mfma_f32_16x16x32_bf16 v[20:23],v[212:215],v[44:47],v[20:23]
buffer_load_dwordx4 v2,s[32:35],s63 offen offset:1024 sc1 lds
v_mfma_f32_16x16x32_bf16 v[16:19],v[208:211],v[44:47],v[16:19]
ds_read_b128 v[36:39],v2 offset:24576
v_mfma_f32_16x16x32_bf16 v[16:19],v[96:99],v[40:43],v[16:19]
s_waitcnt lgkmcnt(4)
v_mfma_f32_16x16x32_bf16 v[16:19],a[72:75],v[48:51],v[16:19]
ds_read_b128 v[40:43],v2 offset:38912
v_mfma_f32_16x16x32_bf16 v[16:19],a[184:187],v[52:55],v[16:19]
buffer_load_dwordx4 v2,s[32:35],s63 offen offset:2048 sc1 lds
v_mfma_f32_16x16x32_bf16 v[20:23],a[188:191],v[52:55],v[20:23]
ds_read_b128 v[44:47],v2 offset:53248
v_mfma_f32_16x16x32_bf16 v[20:23],a[76:79],v[48:51],v[20:23]
s_waitcnt lgkmcnt(4)
v_mfma_f32_16x16x32_bf16 v[20:23],v[108:111],v[56:59],v[20:23]
ds_read_b128 v[48:51],v2 offset:11264
v_mfma_f32_16x16x32_bf16 v[20:23],v[220:223],v[60:63],v[20:23]
v_mfma_f32_16x16x32_bf16 v[16:19],v[216:219],v[60:63],v[16:19]
ds_read_b128 v[52:55],v2 offset:25600
v_mfma_f32_16x16x32_bf16 v[16:19],v[104:107],v[56:59],v[16:19]
s_waitcnt lgkmcnt(4)
v_mfma_f32_16x16x32_bf16 v[16:19],a[80:83],v[32:35],v[16:19]
ds_read_b128 v[56:59],v2 offset:39936
v_mfma_f32_16x16x32_bf16 v[16:19],a[192:195],v[36:39],v[16:19]
buffer_load_dwordx4 v2,s[32:35],s63 offen offset:3072 sc1 lds
s_add_u32 s63,s63,4096
v_mfma_f32_16x16x32_bf16 v[20:23],a[196:199],v[36:39],v[20:23]
ds_read_b128 v[60:63],v2 offset:54272
s_add_u32 m0,s64,69632
v_mfma_f32_16x16x32_bf16 v[20:23],a[84:87],v[32:35],v[20:23]
s_waitcnt lgkmcnt(4)
v_mfma_f32_16x16x32_bf16 v[20:23],v[116:119],v[40:43],v[20:23]
ds_read_b128 v[32:35],v2 offset:12288
v_mfma_f32_16x16x32_bf16 v[20:23],v[228:231],v[44:47],v[20:23]
buffer_load_dwordx4 v2,s[32:35],s63 offen offset:0 sc1 lds
v_mfma_f32_16x16x32_bf16 v[16:19],v[224:227],v[44:47],v[16:19]
ds_read_b128 v[36:39],v2 offset:26624
v_mfma_f32_16x16x32_bf16 v[16:19],v[112:115],v[40:43],v[16:19]
s_waitcnt lgkmcnt(4)
v_mfma_f32_16x16x32_bf16 v[16:19],a[88:91],v[48:51],v[16:19]
ds_read_b128 v[40:43],v2 offset:40960
v_mfma_f32_16x16x32_bf16 v[16:19],a[200:203],v[52:55],v[16:19]
buffer_load_dwordx4 v2,s[32:35],s63 offen offset:1024 sc1 lds
s_add_u32 s63,s63,1175552
v_mfma_f32_16x16x32_bf16 v[20:23],a[204:207],v[52:55],v[20:23]
ds_read_b128 v[44:47],v2 offset:55296
v_mfma_f32_16x16x32_bf16 v[20:23],a[92:95],v[48:51],v[20:23]
s_waitcnt lgkmcnt(4)
v_mfma_f32_16x16x32_bf16 v[20:23],v[124:127],v[56:59],v[20:23]
ds_read_b128 v[48:51],v2 offset:13312
s_waitcnt vmcnt(10)
v_mfma_f32_16x16x32_bf16 v[20:23],v[236:239],v[60:63],v[20:23]
v_lshlrev_b32 v24,16,v12
v_and_b32 v25,0xffff0000,v12
v_mfma_f32_16x16x32_bf16 v[16:19],v[232:235],v[60:63],v[16:19]
ds_read_b128 v[52:55],v2 offset:27648
v_lshlrev_b32 v26,16,v13
v_mfma_f32_16x16x32_bf16 v[16:19],v[120:123],v[56:59],v[16:19]
v_and_b32 v27,0xffff0000,v13
s_waitcnt lgkmcnt(4)
v_mfma_f32_16x16x32_bf16 v[16:19],a[96:99],v[32:35],v[16:19]
ds_read_b128 v[56:59],v2 offset:41984
v_lshlrev_b32 v28,16,v14
v_mfma_f32_16x16x32_bf16 v[16:19],a[208:211],v[36:39],v[16:19]
v_and_b32 v29,0xffff0000,v14
v_lshlrev_b32 v30,16,v15
v_mfma_f32_16x16x32_bf16 v[20:23],a[212:215],v[36:39],v[20:23]
ds_read_b128 v[60:63],v2 offset:56320
v_and_b32 v31,0xffff0000,v15
v_mfma_f32_16x16x32_bf16 v[20:23],a[100:103],v[32:35],v[20:23]
s_waitcnt lgkmcnt(4)
s_barrier
v_mfma_f32_16x16x32_bf16 v[20:23],v[132:135],v[40:43],v[20:23]
ds_read_b128 v[32:35],v4 offset:0
v_mfma_f32_16x16x32_bf16 v[20:23],v[244:247],v[44:47],v[20:23]
v_mfma_f32_16x16x32_bf16 v[16:19],v[240:243],v[44:47],v[16:19]
ds_read_b128 v[36:39],v4 offset:14336
v_mfma_f32_16x16x32_bf16 v[16:19],v[128:131],v[40:43],v[16:19]
s_waitcnt lgkmcnt(4)
v_mfma_f32_16x16x32_bf16 v[16:19],a[104:107],v[48:51],v[16:19]
ds_read_b128 v[40:43],v4 offset:28672
v_mfma_f32_16x16x32_bf16 v[16:19],a[216:219],v[52:55],v[16:19]
v_mfma_f32_16x16x32_bf16 v[20:23],a[220:223],v[52:55],v[20:23]
ds_read_b128 v[44:47],v4 offset:43008
v_mfma_f32_16x16x32_bf16 v[20:23],a[108:111],v[48:51],v[20:23]
s_waitcnt lgkmcnt(4)
v_mfma_f32_16x16x32_bf16 v[20:23],v[140:143],v[56:59],v[20:23]
ds_read_b128 v[48:51],v4 offset:1024
v_mfma_f32_16x16x32_bf16 v[20:23],v[252:255],v[60:63],v[20:23]
v_mfma_f32_16x16x32_bf16 v[16:19],v[248:251],v[60:63],v[16:19]
ds_read_b128 v[52:55],v4 offset:15360
v_mfma_f32_16x16x32_bf16 v[16:19],v[136:139],v[56:59],v[16:19]
s_branch .L_dml_0
.L_dmt_0:
s_nop 1
s_nop 0
v_cvt_pk_bf16_f32 v15,v30,v31
v_cvt_pk_bf16_f32 v14,v28,v29
s_nop 1
v_cvt_pk_bf16_f32 v13,v26,v27
v_cvt_pk_bf16_f32 v12,v24,v25
ds_write_b128 v5,v[12:15]
ds_read2st64_b32 v[12:13],v3 offset0:0 offset1:1
ds_read2st64_b32 v[14:15],v3 offset0:2 offset1:3
s_waitcnt lgkmcnt(0)
buffer_atomic_pk_add_bf16 v12,v1,s[44:47],s65 offen offset:0 sc1
buffer_atomic_pk_add_bf16 v13,v1,s[44:47],s65 offen offset:2048 sc1
s_add_u32 s65,s65,16384
buffer_atomic_pk_add_bf16 v14,v1,s[44:47],s65 offen offset:0 sc1
buffer_atomic_pk_add_bf16 v15,v1,s[44:47],s65 offen offset:2048 sc1
s_add_u32 s65,s65,16384
s_branch .L_end
.p2align 6
.L_dlv1:
.L_dll_1:
s_waitcnt lgkmcnt(4)
v_mfma_f32_16x16x32_bf16 v[16:19],a[0:3],v[32:35],v[16:19]
ds_read_b128 v[56:59],v2 offset:29696
v_mfma_f32_16x16x32_bf16 v[16:19],a[112:115],v[36:39],v[16:19]
v_mfma_f32_16x16x32_bf16 v[20:23],a[116:119],v[36:39],v[20:23]
ds_read_b128 v[60:63],v2 offset:44032
v_mfma_f32_16x16x32_bf16 v[20:23],a[4:7],v[32:35],v[20:23]
s_add_u32 s68,s68,1187840
s_waitcnt lgkmcnt(4)
v_mfma_f32_16x16x32_bf16 v[20:23],a[228:231],v[40:43],v[20:23]
ds_read_b128 v[32:35],v2 offset:2048
v_mfma_f32_16x16x32_bf16 v[20:23],v[148:151],v[44:47],v[20:23]
buffer_store_dwordx4 v[28:31],v2,s[32:35],s68 offen offset:1024 sc1
v_mfma_f32_16x16x32_bf16 v[16:19],v[144:147],v[44:47],v[16:19]
ds_read_b128 v[36:39],v2 offset:16384
v_mfma_f32_16x16x32_bf16 v[16:19],a[224:227],v[40:43],v[16:19]
s_waitcnt lgkmcnt(4)
v_mfma_f32_16x16x32_bf16 v[16:19],a[8:11],v[48:51],v[16:19]
ds_read_b128 v[40:43],v2 offset:30720
v_mfma_f32_16x16x32_bf16 v[16:19],a[120:123],v[52:55],v[16:19]
buffer_store_dwordx4 v[24:27],v2,s[32:35],s68 offen sc1
s_mov_b32 s67,s68
v_mfma_f32_16x16x32_bf16 v[20:23],a[124:127],v[52:55],v[20:23]
ds_read_b128 v[44:47],v2 offset:45056
v_mfma_f32_16x16x32_bf16 v[20:23],a[12:15],v[48:51],v[20:23]
s_waitcnt lgkmcnt(4)
v_mfma_f32_16x16x32_bf16 v[20:23],a[236:239],v[56:59],v[20:23]
ds_read_b128 v[48:51],v2 offset:3072
v_mfma_f32_16x16x32_bf16 v[20:23],v[156:159],v[60:63],v[20:23]
v_mfma_f32_16x16x32_bf16 v[16:19],v[152:155],v[60:63],v[16:19]
ds_read_b128 v[52:55],v2 offset:17408
s_waitcnt vmcnt(5)
v_mfma_f32_16x16x32_bf16 v[16:19],a[232:235],v[56:59],v[16:19]
v_cmp_ne_u64 vcc,v[8:9],v[6:7]
s_waitcnt lgkmcnt(4)
v_mfma_f32_16x16x32_bf16 v[16:19],a[16:19],v[32:35],v[16:19]
ds_read_b128 v[56:59],v2 offset:31744
s_cbranch_vccnz .L_rtl0_1
.L_bkl0_1:
v_mfma_f32_16x16x32_bf16 v[16:19],a[128:131],v[36:39],v[16:19]
s_add_u32 s66,s66,1187840
v_mfma_f32_16x16x32_bf16 v[20:23],a[132:135],v[36:39],v[20:23]
s_waitcnt vmcnt(2)
s_barrier
v_mfma_f32_16x16x32_bf16 v[20:23],a[20:23],v[32:35],v[20:23]
ds_read_b128 v[60:63],v2 offset:46080
s_waitcnt lgkmcnt(4)
v_mfma_f32_16x16x32_bf16 v[20:23],a[244:247],v[40:43],v[20:23]
ds_read_b128 v[32:35],v2 offset:4096
s_add_u32 m0,s64,57344
v_mfma_f32_16x16x32_bf16 v[20:23],v[164:167],v[44:47],v[20:23]
buffer_load_dwordx4 v2,s[32:35],s63 offen offset:0 sc1 lds
v_mfma_f32_16x16x32_bf16 v[16:19],v[160:163],v[44:47],v[16:19]
ds_read_b128 v[36:39],v2 offset:18432
v_mfma_f32_16x16x32_bf16 v[16:19],a[240:243],v[40:43],v[16:19]
s_waitcnt lgkmcnt(4)
v_mfma_f32_16x16x32_bf16 v[16:19],a[24:27],v[48:51],v[16:19]
ds_read_b128 v[40:43],v2 offset:32768
v_mfma_f32_16x16x32_bf16 v[16:19],a[136:139],v[52:55],v[16:19]
v_mfma_f32_16x16x32_bf16 v[20:23],a[140:143],v[52:55],v[20:23]
ds_read_b128 v[44:47],v2 offset:47104
v_mfma_f32_16x16x32_bf16 v[20:23],a[28:31],v[48:51],v[20:23]
s_waitcnt lgkmcnt(4)
v_mfma_f32_16x16x32_bf16 v[20:23],a[252:255],v[56:59],v[20:23]
ds_read_b128 v[48:51],v2 offset:5120
v_mfma_f32_16x16x32_bf16 v[20:23],v[172:175],v[60:63],v[20:23]
buffer_load_dwordx4 v2,s[32:35],s63 offen offset:1024 sc1 lds
v_mfma_f32_16x16x32_bf16 v[16:19],v[168:171],v[60:63],v[16:19]
ds_read_b128 v[52:55],v2 offset:19456
v_mfma_f32_16x16x32_bf16 v[16:19],a[248:251],v[56:59],v[16:19]
s_waitcnt lgkmcnt(4)
v_mfma_f32_16x16x32_bf16 v[16:19],a[32:35],v[32:35],v[16:19]
ds_read_b128 v[56:59],v2 offset:33792
v_mfma_f32_16x16x32_bf16 v[16:19],a[144:147],v[36:39],v[16:19]
buffer_load_dwordx4 v2,s[32:35],s63 offen offset:2048 sc1 lds
v_mfma_f32_16x16x32_bf16 v[20:23],a[148:151],v[36:39],v[20:23]
ds_read_b128 v[60:63],v2 offset:48128
v_mfma_f32_16x16x32_bf16 v[20:23],a[36:39],v[32:35],v[20:23]
s_waitcnt lgkmcnt(4)
v_mfma_f32_16x16x32_bf16 v[20:23],v[68:71],v[40:43],v[20:23]
ds_read_b128 v[32:35],v2 offset:6144
v_mfma_f32_16x16x32_bf16 v[20:23],v[180:183],v[44:47],v[20:23]
buffer_load_dwordx4 v2,s[32:35],s63 offen offset:3072 sc1 lds
v_mfma_f32_16x16x32_bf16 v[16:19],v[176:179],v[44:47],v[16:19]
ds_read_b128 v[36:39],v2 offset:20480
v_mfma_f32_16x16x32_bf16 v[16:19],v[64:67],v[40:43],v[16:19]
s_waitcnt lgkmcnt(4)
v_mfma_f32_16x16x32_bf16 v[16:19],a[40:43],v[48:51],v[16:19]
ds_read_b128 v[40:43],v2 offset:34816
v_mfma_f32_16x16x32_bf16 v[16:19],a[152:155],v[52:55],v[16:19]
buffer_load_dwordx4 v[12:15],v2,s[32:35],s71 offen sc1
s_add_u32 s71,s71,1187840
v_mfma_f32_16x16x32_bf16 v[20:23],a[156:159],v[52:55],v[20:23]
ds_read_b128 v[44:47],v2 offset:49152
s_add_u32 s63,s63,4096
v_mfma_f32_16x16x32_bf16 v[20:23],a[44:47],v[48:51],v[20:23]
s_add_u32 m0,s64,61440
s_waitcnt lgkmcnt(4)
v_mfma_f32_16x16x32_bf16 v[20:23],v[76:79],v[56:59],v[20:23]
ds_read_b128 v[48:51],v2 offset:7168
v_mfma_f32_16x16x32_bf16 v[20:23],v[188:191],v[60:63],v[20:23]
buffer_load_dwordx4 v2,s[32:35],s63 offen offset:0 sc1 lds
v_mfma_f32_16x16x32_bf16 v[16:19],v[184:187],v[60:63],v[16:19]
ds_read_b128 v[52:55],v2 offset:21504
v_mfma_f32_16x16x32_bf16 v[16:19],v[72:75],v[56:59],v[16:19]
s_waitcnt lgkmcnt(4)
v_mfma_f32_16x16x32_bf16 v[16:19],a[48:51],v[32:35],v[16:19]
ds_read_b128 v[56:59],v2 offset:35840
v_mfma_f32_16x16x32_bf16 v[16:19],a[160:163],v[36:39],v[16:19]
buffer_load_dwordx4 v2,s[32:35],s63 offen offset:1024 sc1 lds
v_mfma_f32_16x16x32_bf16 v[20:23],a[164:167],v[36:39],v[20:23]
ds_read_b128 v[60:63],v2 offset:50176
v_mfma_f32_16x16x32_bf16 v[20:23],a[52:55],v[32:35],v[20:23]
s_waitcnt lgkmcnt(4)
v_mfma_f32_16x16x32_bf16 v[20:23],v[84:87],v[40:43],v[20:23]
ds_read_b128 v[32:35],v2 offset:8192
v_mfma_f32_16x16x32_bf16 v[20:23],v[196:199],v[44:47],v[20:23]
buffer_load_dwordx4 v2,s[32:35],s63 offen offset:2048 sc1 lds
v_mfma_f32_16x16x32_bf16 v[16:19],v[192:195],v[44:47],v[16:19]
ds_read_b128 v[36:39],v2 offset:22528
v_mfma_f32_16x16x32_bf16 v[16:19],v[80:83],v[40:43],v[16:19]
s_waitcnt lgkmcnt(4)
v_mfma_f32_16x16x32_bf16 v[16:19],a[56:59],v[48:51],v[16:19]
ds_read_b128 v[40:43],v2 offset:36864
v_mfma_f32_16x16x32_bf16 v[16:19],a[168:171],v[52:55],v[16:19]
buffer_load_dwordx4 v2,s[32:35],s63 offen offset:3072 sc1 lds
v_mfma_f32_16x16x32_bf16 v[20:23],a[172:175],v[52:55],v[20:23]
ds_read_b128 v[44:47],v2 offset:51200
v_mfma_f32_16x16x32_bf16 v[20:23],a[60:63],v[48:51],v[20:23]
s_waitcnt lgkmcnt(4)
v_mfma_f32_16x16x32_bf16 v[20:23],v[92:95],v[56:59],v[20:23]
ds_read_b128 v[48:51],v2 offset:9216
v_mfma_f32_16x16x32_bf16 v[20:23],v[204:207],v[60:63],v[20:23]
s_add_u32 s63,s63,4096
s_add_u32 m0,s64,65536
v_mfma_f32_16x16x32_bf16 v[16:19],v[200:203],v[60:63],v[16:19]
ds_read_b128 v[52:55],v2 offset:23552
v_mfma_f32_16x16x32_bf16 v[16:19],v[88:91],v[56:59],v[16:19]
s_waitcnt lgkmcnt(4)
v_mfma_f32_16x16x32_bf16 v[16:19],a[64:67],v[32:35],v[16:19]
ds_read_b128 v[56:59],v2 offset:37888
v_mfma_f32_16x16x32_bf16 v[16:19],a[176:179],v[36:39],v[16:19]
buffer_load_dwordx4 v2,s[32:35],s63 offen offset:0 sc1 lds
v_mfma_f32_16x16x32_bf16 v[20:23],a[180:183],v[36:39],v[20:23]
ds_read_b128 v[60:63],v2 offset:52224
v_mfma_f32_16x16x32_bf16 v[20:23],a[68:71],v[32:35],v[20:23]
s_waitcnt lgkmcnt(4)
v_mfma_f32_16x16x32_bf16 v[20:23],v[100:103],v[40:43],v[20:23]
ds_read_b128 v[32:35],v2 offset:10240
v_mfma_f32_16x16x32_bf16 v[20:23],v[212:215],v[44:47],v[20:23]
buffer_load_dwordx4 v2,s[32:35],s63 offen offset:1024 sc1 lds
v_mfma_f32_16x16x32_bf16 v[16:19],v[208:211],v[44:47],v[16:19]
ds_read_b128 v[36:39],v2 offset:24576
v_mfma_f32_16x16x32_bf16 v[16:19],v[96:99],v[40:43],v[16:19]
s_waitcnt lgkmcnt(4)
v_mfma_f32_16x16x32_bf16 v[16:19],a[72:75],v[48:51],v[16:19]
ds_read_b128 v[40:43],v2 offset:38912
v_mfma_f32_16x16x32_bf16 v[16:19],a[184:187],v[52:55],v[16:19]
buffer_load_dwordx4 v2,s[32:35],s63 offen offset:2048 sc1 lds
v_mfma_f32_16x16x32_bf16 v[20:23],a[188:191],v[52:55],v[20:23]
ds_read_b128 v[44:47],v2 offset:53248
v_mfma_f32_16x16x32_bf16 v[20:23],a[76:79],v[48:51],v[20:23]
s_waitcnt lgkmcnt(4)
v_mfma_f32_16x16x32_bf16 v[20:23],v[108:111],v[56:59],v[20:23]
ds_read_b128 v[48:51],v2 offset:11264
)#"
R"#(
v_mfma_f32_16x16x32_bf16 v[20:23],v[220:223],v[60:63],v[20:23]
buffer_load_dwordx2 v[6:7],v0,s[32:35],s66 offen sc1
v_mfma_f32_16x16x32_bf16 v[16:19],v[216:219],v[60:63],v[16:19]
ds_read_b128 v[52:55],v2 offset:25600
v_mfma_f32_16x16x32_bf16 v[16:19],v[104:107],v[56:59],v[16:19]
s_waitcnt lgkmcnt(4)
v_mfma_f32_16x16x32_bf16 v[16:19],a[80:83],v[32:35],v[16:19]
ds_read_b128 v[56:59],v2 offset:39936
v_mfma_f32_16x16x32_bf16 v[16:19],a[192:195],v[36:39],v[16:19]
buffer_load_dwordx4 v2,s[32:35],s63 offen offset:3072 sc1 lds
s_add_u32 s63,s63,4096
v_mfma_f32_16x16x32_bf16 v[20:23],a[196:199],v[36:39],v[20:23]
ds_read_b128 v[60:63],v2 offset:54272
s_add_u32 m0,s64,69632
v_mfma_f32_16x16x32_bf16 v[20:23],a[84:87],v[32:35],v[20:23]
s_waitcnt lgkmcnt(4)
v_mfma_f32_16x16x32_bf16 v[20:23],v[116:119],v[40:43],v[20:23]
ds_read_b128 v[32:35],v2 offset:12288
v_mfma_f32_16x16x32_bf16 v[20:23],v[228:231],v[44:47],v[20:23]
buffer_load_dwordx4 v2,s[32:35],s63 offen offset:0 sc1 lds
v_mfma_f32_16x16x32_bf16 v[16:19],v[224:227],v[44:47],v[16:19]
ds_read_b128 v[36:39],v2 offset:26624
v_mfma_f32_16x16x32_bf16 v[16:19],v[112:115],v[40:43],v[16:19]
s_waitcnt lgkmcnt(4)
v_mfma_f32_16x16x32_bf16 v[16:19],a[88:91],v[48:51],v[16:19]
ds_read_b128 v[40:43],v2 offset:40960
v_mfma_f32_16x16x32_bf16 v[16:19],a[200:203],v[52:55],v[16:19]
buffer_load_dwordx4 v2,s[32:35],s63 offen offset:1024 sc1 lds
s_add_u32 s63,s63,1175552
v_mfma_f32_16x16x32_bf16 v[20:23],a[204:207],v[52:55],v[20:23]
ds_read_b128 v[44:47],v2 offset:55296
v_mfma_f32_16x16x32_bf16 v[20:23],a[92:95],v[48:51],v[20:23]
s_waitcnt lgkmcnt(4)
v_mfma_f32_16x16x32_bf16 v[20:23],v[124:127],v[56:59],v[20:23]
ds_read_b128 v[48:51],v2 offset:13312
s_waitcnt vmcnt(11)
v_mfma_f32_16x16x32_bf16 v[20:23],v[236:239],v[60:63],v[20:23]
v_lshlrev_b32 v24,16,v12
v_and_b32 v25,0xffff0000,v12
v_mfma_f32_16x16x32_bf16 v[16:19],v[232:235],v[60:63],v[16:19]
ds_read_b128 v[52:55],v2 offset:27648
v_lshlrev_b32 v26,16,v13
v_mfma_f32_16x16x32_bf16 v[16:19],v[120:123],v[56:59],v[16:19]
v_and_b32 v27,0xffff0000,v13
s_waitcnt lgkmcnt(4)
v_mfma_f32_16x16x32_bf16 v[16:19],a[96:99],v[32:35],v[16:19]
ds_read_b128 v[56:59],v2 offset:41984
v_lshlrev_b32 v28,16,v14
v_mfma_f32_16x16x32_bf16 v[16:19],a[208:211],v[36:39],v[16:19]
v_and_b32 v29,0xffff0000,v14
v_lshlrev_b32 v30,16,v15
v_mfma_f32_16x16x32_bf16 v[20:23],a[212:215],v[36:39],v[20:23]
s_barrier
v_mfma_f32_16x16x32_bf16 v[20:23],a[100:103],v[32:35],v[20:23]
ds_read_b128 v[60:63],v2 offset:56320
s_waitcnt lgkmcnt(4)
v_mfma_f32_16x16x32_bf16 v[20:23],v[132:135],v[40:43],v[20:23]
ds_read_b128 v[32:35],v4 offset:0
v_and_b32 v31,0xffff0000,v15
v_mfma_f32_16x16x32_bf16 v[20:23],v[244:247],v[44:47],v[20:23]
v_mfma_f32_16x16x32_bf16 v[16:19],v[240:243],v[44:47],v[16:19]
ds_read_b128 v[36:39],v4 offset:14336
v_mfma_f32_16x16x32_bf16 v[16:19],v[128:131],v[40:43],v[16:19]
s_waitcnt lgkmcnt(4)
v_mfma_f32_16x16x32_bf16 v[16:19],a[104:107],v[48:51],v[16:19]
ds_read_b128 v[40:43],v4 offset:28672
v_mfma_f32_16x16x32_bf16 v[16:19],a[216:219],v[52:55],v[16:19]
v_mfma_f32_16x16x32_bf16 v[20:23],a[220:223],v[52:55],v[20:23]
ds_read_b128 v[44:47],v4 offset:43008
v_mfma_f32_16x16x32_bf16 v[20:23],a[108:111],v[48:51],v[20:23]
s_waitcnt lgkmcnt(4)
v_mfma_f32_16x16x32_bf16 v[20:23],v[140:143],v[56:59],v[20:23]
ds_read_b128 v[48:51],v4 offset:1024
v_mfma_f32_16x16x32_bf16 v[20:23],v[252:255],v[60:63],v[20:23]
v_mfma_f32_16x16x32_bf16 v[16:19],v[248:251],v[60:63],v[16:19]
ds_read_b128 v[52:55],v4 offset:15360
s_sub_u32 s62,s62,1
v_mfma_f32_16x16x32_bf16 v[16:19],v[136:139],v[56:59],v[16:19]
s_cbranch_scc1 .L_dlb_1
s_waitcnt lgkmcnt(4)
v_mfma_f32_16x16x32_bf16 v[24:27],a[0:3],v[32:35],v[24:27]
ds_read_b128 v[56:59],v4 offset:29696
v_mfma_f32_16x16x32_bf16 v[24:27],a[112:115],v[36:39],v[24:27]
v_mfma_f32_16x16x32_bf16 v[28:31],a[116:119],v[36:39],v[28:31]
ds_read_b128 v[60:63],v4 offset:44032
v_mfma_f32_16x16x32_bf16 v[28:31],a[4:7],v[32:35],v[28:31]
s_add_u32 s68,s68,1187840
s_waitcnt lgkmcnt(4)
v_mfma_f32_16x16x32_bf16 v[28:31],a[228:231],v[40:43],v[28:31]
ds_read_b128 v[32:35],v4 offset:2048
v_mfma_f32_16x16x32_bf16 v[28:31],v[148:151],v[44:47],v[28:31]
buffer_store_dwordx4 v[20:23],v2,s[32:35],s68 offen offset:1024 sc1
v_mfma_f32_16x16x32_bf16 v[24:27],v[144:147],v[44:47],v[24:27]
ds_read_b128 v[36:39],v4 offset:16384
v_mfma_f32_16x16x32_bf16 v[24:27],a[224:227],v[40:43],v[24:27]
s_waitcnt lgkmcnt(4)
v_mfma_f32_16x16x32_bf16 v[24:27],a[8:11],v[48:51],v[24:27]
ds_read_b128 v[40:43],v4 offset:30720
v_mfma_f32_16x16x32_bf16 v[24:27],a[120:123],v[52:55],v[24:27]
buffer_store_dwordx4 v[16:19],v2,s[32:35],s68 offen sc1
s_mov_b32 s67,s68
v_mfma_f32_16x16x32_bf16 v[28:31],a[124:127],v[52:55],v[28:31]
ds_read_b128 v[44:47],v4 offset:45056
v_mfma_f32_16x16x32_bf16 v[28:31],a[12:15],v[48:51],v[28:31]
s_waitcnt lgkmcnt(4)
v_mfma_f32_16x16x32_bf16 v[28:31],a[236:239],v[56:59],v[28:31]
ds_read_b128 v[48:51],v4 offset:3072
v_mfma_f32_16x16x32_bf16 v[28:31],v[156:159],v[60:63],v[28:31]
v_mfma_f32_16x16x32_bf16 v[24:27],v[152:155],v[60:63],v[24:27]
ds_read_b128 v[52:55],v4 offset:17408
s_waitcnt vmcnt(5)
v_mfma_f32_16x16x32_bf16 v[24:27],a[232:235],v[56:59],v[24:27]
v_cmp_ne_u64 vcc,v[8:9],v[6:7]
s_waitcnt lgkmcnt(4)
v_mfma_f32_16x16x32_bf16 v[24:27],a[16:19],v[32:35],v[24:27]
ds_read_b128 v[56:59],v4 offset:31744
s_cbranch_vccnz .L_rtl1_1
.L_bkl1_1:
v_mfma_f32_16x16x32_bf16 v[24:27],a[128:131],v[36:39],v[24:27]
s_add_u32 s66,s66,1187840
v_mfma_f32_16x16x32_bf16 v[28:31],a[132:135],v[36:39],v[28:31]
s_waitcnt vmcnt(2)
s_barrier
v_mfma_f32_16x16x32_bf16 v[28:31],a[20:23],v[32:35],v[28:31]
ds_read_b128 v[60:63],v4 offset:46080
s_waitcnt lgkmcnt(4)
v_mfma_f32_16x16x32_bf16 v[28:31],a[244:247],v[40:43],v[28:31]
ds_read_b128 v[32:35],v4 offset:4096
s_add_u32 m0,s64,0
v_mfma_f32_16x16x32_bf16 v[28:31],v[164:167],v[44:47],v[28:31]
buffer_load_dwordx4 v2,s[32:35],s63 offen offset:0 sc1 lds
v_mfma_f32_16x16x32_bf16 v[24:27],v[160:163],v[44:47],v[24:27]
ds_read_b128 v[36:39],v4 offset:18432
v_mfma_f32_16x16x32_bf16 v[24:27],a[240:243],v[40:43],v[24:27]
s_waitcnt lgkmcnt(4)
v_mfma_f32_16x16x32_bf16 v[24:27],a[24:27],v[48:51],v[24:27]
ds_read_b128 v[40:43],v4 offset:32768
v_mfma_f32_16x16x32_bf16 v[24:27],a[136:139],v[52:55],v[24:27]
v_mfma_f32_16x16x32_bf16 v[28:31],a[140:143],v[52:55],v[28:31]
ds_read_b128 v[44:47],v4 offset:47104
v_mfma_f32_16x16x32_bf16 v[28:31],a[28:31],v[48:51],v[28:31]
s_waitcnt lgkmcnt(4)
v_mfma_f32_16x16x32_bf16 v[28:31],a[252:255],v[56:59],v[28:31]
ds_read_b128 v[48:51],v4 offset:5120
v_mfma_f32_16x16x32_bf16 v[28:31],v[172:175],v[60:63],v[28:31]
buffer_load_dwordx4 v2,s[32:35],s63 offen offset:1024 sc1 lds
v_mfma_f32_16x16x32_bf16 v[24:27],v[168:171],v[60:63],v[24:27]
ds_read_b128 v[52:55],v4 offset:19456
v_mfma_f32_16x16x32_bf16 v[24:27],a[248:251],v[56:59],v[24:27]
s_waitcnt lgkmcnt(4)
v_mfma_f32_16x16x32_bf16 v[24:27],a[32:35],v[32:35],v[24:27]
ds_read_b128 v[56:59],v4 offset:33792
v_mfma_f32_16x16x32_bf16 v[24:27],a[144:147],v[36:39],v[24:27]
buffer_load_dwordx4 v2,s[32:35],s63 offen offset:2048 sc1 lds
v_mfma_f32_16x16x32_bf16 v[28:31],a[148:151],v[36:39],v[28:31]
ds_read_b128 v[60:63],v4 offset:48128
v_mfma_f32_16x16x32_bf16 v[28:31],a[36:39],v[32:35],v[28:31]
s_waitcnt lgkmcnt(4)
v_mfma_f32_16x16x32_bf16 v[28:31],v[68:71],v[40:43],v[28:31]
ds_read_b128 v[32:35],v4 offset:6144
v_mfma_f32_16x16x32_bf16 v[28:31],v[180:183],v[44:47],v[28:31]
buffer_load_dwordx4 v2,s[32:35],s63 offen offset:3072 sc1 lds
v_mfma_f32_16x16x32_bf16 v[24:27],v[176:179],v[44:47],v[24:27]
ds_read_b128 v[36:39],v4 offset:20480
v_mfma_f32_16x16x32_bf16 v[24:27],v[64:67],v[40:43],v[24:27]
s_waitcnt lgkmcnt(4)
v_mfma_f32_16x16x32_bf16 v[24:27],a[40:43],v[48:51],v[24:27]
ds_read_b128 v[40:43],v4 offset:34816
v_mfma_f32_16x16x32_bf16 v[24:27],a[152:155],v[52:55],v[24:27]
buffer_load_dwordx4 v[12:15],v2,s[32:35],s71 offen sc1
s_add_u32 s71,s71,1187840
v_mfma_f32_16x16x32_bf16 v[28:31],a[156:159],v[52:55],v[28:31]
ds_read_b128 v[44:47],v4 offset:49152
s_add_u32 s63,s63,4096
v_mfma_f32_16x16x32_bf16 v[28:31],a[44:47],v[48:51],v[28:31]
s_add_u32 m0,s64,4096
s_waitcnt lgkmcnt(4)
v_mfma_f32_16x16x32_bf16 v[28:31],v[76:79],v[56:59],v[28:31]
ds_read_b128 v[48:51],v4 offset:7168
v_mfma_f32_16x16x32_bf16 v[28:31],v[188:191],v[60:63],v[28:31]
buffer_load_dwordx4 v2,s[32:35],s63 offen offset:0 sc1 lds
v_mfma_f32_16x16x32_bf16 v[24:27],v[184:187],v[60:63],v[24:27]
ds_read_b128 v[52:55],v4 offset:21504
v_mfma_f32_16x16x32_bf16 v[24:27],v[72:75],v[56:59],v[24:27]
s_waitcnt lgkmcnt(4)
v_mfma_f32_16x16x32_bf16 v[24:27],a[48:51],v[32:35],v[24:27]
ds_read_b128 v[56:59],v4 offset:35840
v_mfma_f32_16x16x32_bf16 v[24:27],a[160:163],v[36:39],v[24:27]
buffer_load_dwordx4 v2,s[32:35],s63 offen offset:1024 sc1 lds
v_mfma_f32_16x16x32_bf16 v[28:31],a[164:167],v[36:39],v[28:31]
ds_read_b128 v[60:63],v4 offset:50176
v_mfma_f32_16x16x32_bf16 v[28:31],a[52:55],v[32:35],v[28:31]
s_waitcnt lgkmcnt(4)
v_mfma_f32_16x16x32_bf16 v[28:31],v[84:87],v[40:43],v[28:31]
ds_read_b128 v[32:35],v4 offset:8192
v_mfma_f32_16x16x32_bf16 v[28:31],v[196:199],v[44:47],v[28:31]
buffer_load_dwordx4 v2,s[32:35],s63 offen offset:2048 sc1 lds
v_mfma_f32_16x16x32_bf16 v[24:27],v[192:195],v[44:47],v[24:27]
ds_read_b128 v[36:39],v4 offset:22528
v_mfma_f32_16x16x32_bf16 v[24:27],v[80:83],v[40:43],v[24:27]
s_waitcnt lgkmcnt(4)
v_mfma_f32_16x16x32_bf16 v[24:27],a[56:59],v[48:51],v[24:27]
ds_read_b128 v[40:43],v4 offset:36864
v_mfma_f32_16x16x32_bf16 v[24:27],a[168:171],v[52:55],v[24:27]
buffer_load_dwordx4 v2,s[32:35],s63 offen offset:3072 sc1 lds
v_mfma_f32_16x16x32_bf16 v[28:31],a[172:175],v[52:55],v[28:31]
ds_read_b128 v[44:47],v4 offset:51200
v_mfma_f32_16x16x32_bf16 v[28:31],a[60:63],v[48:51],v[28:31]
s_waitcnt lgkmcnt(4)
v_mfma_f32_16x16x32_bf16 v[28:31],v[92:95],v[56:59],v[28:31]
ds_read_b128 v[48:51],v4 offset:9216
v_mfma_f32_16x16x32_bf16 v[28:31],v[204:207],v[60:63],v[28:31]
s_add_u32 s63,s63,4096
s_add_u32 m0,s64,8192
v_mfma_f32_16x16x32_bf16 v[24:27],v[200:203],v[60:63],v[24:27]
ds_read_b128 v[52:55],v4 offset:23552
v_mfma_f32_16x16x32_bf16 v[24:27],v[88:91],v[56:59],v[24:27]
s_waitcnt lgkmcnt(4)
v_mfma_f32_16x16x32_bf16 v[24:27],a[64:67],v[32:35],v[24:27]
ds_read_b128 v[56:59],v4 offset:37888
v_mfma_f32_16x16x32_bf16 v[24:27],a[176:179],v[36:39],v[24:27]
buffer_load_dwordx4 v2,s[32:35],s63 offen offset:0 sc1 lds
v_mfma_f32_16x16x32_bf16 v[28:31],a[180:183],v[36:39],v[28:31]
ds_read_b128 v[60:63],v4 offset:52224
v_mfma_f32_16x16x32_bf16 v[28:31],a[68:71],v[32:35],v[28:31]
s_waitcnt lgkmcnt(4)
v_mfma_f32_16x16x32_bf16 v[28:31],v[100:103],v[40:43],v[28:31]
ds_read_b128 v[32:35],v4 offset:10240
v_mfma_f32_16x16x32_bf16 v[28:31],v[212:215],v[44:47],v[28:31]
buffer_load_dwordx4 v2,s[32:35],s63 offen offset:1024 sc1 lds
v_mfma_f32_16x16x32_bf16 v[24:27],v[208:211],v[44:47],v[24:27]
ds_read_b128 v[36:39],v4 offset:24576
v_mfma_f32_16x16x32_bf16 v[24:27],v[96:99],v[40:43],v[24:27]
s_waitcnt lgkmcnt(4)
v_mfma_f32_16x16x32_bf16 v[24:27],a[72:75],v[48:51],v[24:27]
ds_read_b128 v[40:43],v4 offset:38912
v_mfma_f32_16x16x32_bf16 v[24:27],a[184:187],v[52:55],v[24:27]
buffer_load_dwordx4 v2,s[32:35],s63 offen offset:2048 sc1 lds
v_mfma_f32_16x16x32_bf16 v[28:31],a[188:191],v[52:55],v[28:31]
ds_read_b128 v[44:47],v4 offset:53248
v_mfma_f32_16x16x32_bf16 v[28:31],a[76:79],v[48:51],v[28:31]
s_waitcnt lgkmcnt(4)
v_mfma_f32_16x16x32_bf16 v[28:31],v[108:111],v[56:59],v[28:31]
ds_read_b128 v[48:51],v4 offset:11264
v_mfma_f32_16x16x32_bf16 v[28:31],v[220:223],v[60:63],v[28:31]
buffer_load_dwordx2 v[6:7],v0,s[32:35],s66 offen sc1
v_mfma_f32_16x16x32_bf16 v[24:27],v[216:219],v[60:63],v[24:27]
ds_read_b128 v[52:55],v4 offset:25600
v_mfma_f32_16x16x32_bf16 v[24:27],v[104:107],v[56:59],v[24:27]
s_waitcnt lgkmcnt(4)
v_mfma_f32_16x16x32_bf16 v[24:27],a[80:83],v[32:35],v[24:27]
ds_read_b128 v[56:59],v4 offset:39936
v_mfma_f32_16x16x32_bf16 v[24:27],a[192:195],v[36:39],v[24:27]
buffer_load_dwordx4 v2,s[32:35],s63 offen offset:3072 sc1 lds
s_add_u32 s63,s63,4096
v_mfma_f32_16x16x32_bf16 v[28:31],a[196:199],v[36:39],v[28:31]
ds_read_b128 v[60:63],v4 offset:54272
s_add_u32 m0,s64,12288
v_mfma_f32_16x16x32_bf16 v[28:31],a[84:87],v[32:35],v[28:31]
s_waitcnt lgkmcnt(4)
v_mfma_f32_16x16x32_bf16 v[28:31],v[116:119],v[40:43],v[28:31]
ds_read_b128 v[32:35],v4 offset:12288
v_mfma_f32_16x16x32_bf16 v[28:31],v[228:231],v[44:47],v[28:31]
buffer_load_dwordx4 v2,s[32:35],s63 offen offset:0 sc1 lds
v_mfma_f32_16x16x32_bf16 v[24:27],v[224:227],v[44:47],v[24:27]
ds_read_b128 v[36:39],v4 offset:26624
v_mfma_f32_16x16x32_bf16 v[24:27],v[112:115],v[40:43],v[24:27]
s_waitcnt lgkmcnt(4)
v_mfma_f32_16x16x32_bf16 v[24:27],a[88:91],v[48:51],v[24:27]
ds_read_b128 v[40:43],v4 offset:40960
v_mfma_f32_16x16x32_bf16 v[24:27],a[200:203],v[52:55],v[24:27]
buffer_load_dwordx4 v2,s[32:35],s63 offen offset:1024 sc1 lds
s_add_u32 s63,s63,1175552
v_mfma_f32_16x16x32_bf16 v[28:31],a[204:207],v[52:55],v[28:31]
ds_read_b128 v[44:47],v4 offset:55296
v_mfma_f32_16x16x32_bf16 v[28:31],a[92:95],v[48:51],v[28:31]
s_waitcnt lgkmcnt(4)
v_mfma_f32_16x16x32_bf16 v[28:31],v[124:127],v[56:59],v[28:31]
ds_read_b128 v[48:51],v4 offset:13312
s_waitcnt vmcnt(11)
v_mfma_f32_16x16x32_bf16 v[28:31],v[236:239],v[60:63],v[28:31]
v_lshlrev_b32 v16,16,v12
v_and_b32 v17,0xffff0000,v12
v_mfma_f32_16x16x32_bf16 v[24:27],v[232:235],v[60:63],v[24:27]
ds_read_b128 v[52:55],v4 offset:27648
v_lshlrev_b32 v18,16,v13
v_mfma_f32_16x16x32_bf16 v[24:27],v[120:123],v[56:59],v[24:27]
v_and_b32 v19,0xffff0000,v13
s_waitcnt lgkmcnt(4)
v_mfma_f32_16x16x32_bf16 v[24:27],a[96:99],v[32:35],v[24:27]
ds_read_b128 v[56:59],v4 offset:41984
v_lshlrev_b32 v20,16,v14
v_mfma_f32_16x16x32_bf16 v[24:27],a[208:211],v[36:39],v[24:27]
v_and_b32 v21,0xffff0000,v14
v_lshlrev_b32 v22,16,v15
v_mfma_f32_16x16x32_bf16 v[28:31],a[212:215],v[36:39],v[28:31]
s_barrier
v_mfma_f32_16x16x32_bf16 v[28:31],a[100:103],v[32:35],v[28:31]
ds_read_b128 v[60:63],v4 offset:56320
s_waitcnt lgkmcnt(4)
v_mfma_f32_16x16x32_bf16 v[28:31],v[132:135],v[40:43],v[28:31]
ds_read_b128 v[32:35],v2 offset:0
v_and_b32 v23,0xffff0000,v15
v_mfma_f32_16x16x32_bf16 v[28:31],v[244:247],v[44:47],v[28:31]
v_mfma_f32_16x16x32_bf16 v[24:27],v[240:243],v[44:47],v[24:27]
ds_read_b128 v[36:39],v2 offset:14336
v_mfma_f32_16x16x32_bf16 v[24:27],v[128:131],v[40:43],v[24:27]
s_waitcnt lgkmcnt(4)
v_mfma_f32_16x16x32_bf16 v[24:27],a[104:107],v[48:51],v[24:27]
ds_read_b128 v[40:43],v2 offset:28672
v_mfma_f32_16x16x32_bf16 v[24:27],a[216:219],v[52:55],v[24:27]
v_mfma_f32_16x16x32_bf16 v[28:31],a[220:223],v[52:55],v[28:31]
ds_read_b128 v[44:47],v2 offset:43008
v_mfma_f32_16x16x32_bf16 v[28:31],a[108:111],v[48:51],v[28:31]
s_waitcnt lgkmcnt(4)
v_mfma_f32_16x16x32_bf16 v[28:31],v[140:143],v[56:59],v[28:31]
ds_read_b128 v[48:51],v2 offset:1024
v_mfma_f32_16x16x32_bf16 v[28:31],v[252:255],v[60:63],v[28:31]
v_mfma_f32_16x16x32_bf16 v[24:27],v[248:251],v[60:63],v[24:27]
ds_read_b128 v[52:55],v2 offset:15360
v_mfma_f32_16x16x32_bf16 v[24:27],v[136:139],v[56:59],v[24:27]
s_branch .L_dll_1
.L_dlb_1:
s_mov_b32 s62,15
s_add_u32 s74,s74,8192
v_lshrrev_b32 v5,8,v2
v_lshl_add_u32 v5,v5,4,s74
v_and_b32 v3,128,v2
v_lshl_add_u32 v5,v3,2,v5
v_and_b32 v3,16,v2
v_lshl_add_u32 v5,v3,4,v5
v_and_b32 v3,96,v2
v_lshl_add_u32 v5,v3,1,v5
v_lshrrev_b32 v3,2,v2
v_add_u32 v3,s74,v3
.p2align 6
.L_dml_1:
s_waitcnt lgkmcnt(4)
v_mfma_f32_16x16x32_bf16 v[24:27],a[0:3],v[32:35],v[24:27]
ds_read_b128 v[56:59],v4 offset:29696
v_mfma_f32_16x16x32_bf16 v[24:27],a[112:115],v[36:39],v[24:27]
v_mfma_f32_16x16x32_bf16 v[28:31],a[116:119],v[36:39],v[28:31]
ds_read_b128 v[60:63],v4 offset:44032
v_mfma_f32_16x16x32_bf16 v[28:31],a[4:7],v[32:35],v[28:31]
v_cvt_pk_bf16_f32 v15,v22,v23
s_waitcnt lgkmcnt(4)
v_mfma_f32_16x16x32_bf16 v[28:31],a[228:231],v[40:43],v[28:31]
ds_read_b128 v[32:35],v4 offset:2048
v_cvt_pk_bf16_f32 v14,v20,v21
v_mfma_f32_16x16x32_bf16 v[28:31],v[148:151],v[44:47],v[28:31]
v_cvt_pk_bf16_f32 v13,v18,v19
v_cvt_pk_bf16_f32 v12,v16,v17
v_mfma_f32_16x16x32_bf16 v[24:27],v[144:147],v[44:47],v[24:27]
ds_read_b128 v[36:39],v4 offset:16384
ds_write_b128 v5,v[12:15]
v_mfma_f32_16x16x32_bf16 v[24:27],a[224:227],v[40:43],v[24:27]
ds_read2st64_b32 v[12:13],v3 offset0:0 offset1:1
s_waitcnt lgkmcnt(6)
v_mfma_f32_16x16x32_bf16 v[24:27],a[8:11],v[48:51],v[24:27]
ds_read_b128 v[40:43],v4 offset:30720
ds_read2st64_b32 v[14:15],v3 offset0:2 offset1:3
v_mfma_f32_16x16x32_bf16 v[24:27],a[120:123],v[52:55],v[24:27]
v_mfma_f32_16x16x32_bf16 v[28:31],a[124:127],v[52:55],v[28:31]
ds_read_b128 v[44:47],v4 offset:45056
v_mfma_f32_16x16x32_bf16 v[28:31],a[12:15],v[48:51],v[28:31]
s_waitcnt lgkmcnt(7)
v_mfma_f32_16x16x32_bf16 v[28:31],a[236:239],v[56:59],v[28:31]
ds_read_b128 v[48:51],v4 offset:3072
v_mfma_f32_16x16x32_bf16 v[28:31],v[156:159],v[60:63],v[28:31]
s_waitcnt lgkmcnt(2)
buffer_atomic_pk_add_bf16 v12,v1,s[44:47],s65 offen offset:0 sc1
v_mfma_f32_16x16x32_bf16 v[24:27],v[152:155],v[60:63],v[24:27]
ds_read_b128 v[52:55],v4 offset:17408
s_waitcnt vmcnt(4)
v_mfma_f32_16x16x32_bf16 v[24:27],a[232:235],v[56:59],v[24:27]
buffer_atomic_pk_add_bf16 v13,v1,s[44:47],s65 offen offset:2048 sc1
v_mfma_f32_16x16x32_bf16 v[24:27],a[16:19],v[32:35],v[24:27]
ds_read_b128 v[56:59],v4 offset:31744
v_cmp_ne_u64 vcc,v[8:9],v[6:7]
v_mfma_f32_16x16x32_bf16 v[24:27],a[128:131],v[36:39],v[24:27]
s_cbranch_vccnz .L_rtm1_1
.L_bkm1_1:
s_add_u32 s66,s66,1187840
v_mfma_f32_16x16x32_bf16 v[28:31],a[132:135],v[36:39],v[28:31]
s_waitcnt vmcnt(2)
s_barrier
v_mfma_f32_16x16x32_bf16 v[28:31],a[20:23],v[32:35],v[28:31]
ds_read_b128 v[60:63],v4 offset:46080
s_waitcnt lgkmcnt(4)
v_mfma_f32_16x16x32_bf16 v[28:31],a[244:247],v[40:43],v[28:31]
ds_read_b128 v[32:35],v4 offset:4096
s_add_u32 m0,s64,0
v_mfma_f32_16x16x32_bf16 v[28:31],v[164:167],v[44:47],v[28:31]
buffer_load_dwordx4 v2,s[32:35],s63 offen offset:0 sc1 lds
s_add_u32 s65,s65,16384
v_mfma_f32_16x16x32_bf16 v[24:27],v[160:163],v[44:47],v[24:27]
ds_read_b128 v[36:39],v4 offset:18432
v_mfma_f32_16x16x32_bf16 v[24:27],a[240:243],v[40:43],v[24:27]
buffer_atomic_pk_add_bf16 v14,v1,s[44:47],s65 offen offset:0 sc1
s_waitcnt lgkmcnt(4)
v_mfma_f32_16x16x32_bf16 v[24:27],a[24:27],v[48:51],v[24:27]
ds_read_b128 v[40:43],v4 offset:32768
v_mfma_f32_16x16x32_bf16 v[24:27],a[136:139],v[52:55],v[24:27]
buffer_atomic_pk_add_bf16 v15,v1,s[44:47],s65 offen offset:2048 sc1
s_add_u32 s65,s65,16384
v_mfma_f32_16x16x32_bf16 v[28:31],a[140:143],v[52:55],v[28:31]
ds_read_b128 v[44:47],v4 offset:47104
v_mfma_f32_16x16x32_bf16 v[28:31],a[28:31],v[48:51],v[28:31]
s_waitcnt lgkmcnt(4)
v_mfma_f32_16x16x32_bf16 v[28:31],a[252:255],v[56:59],v[28:31]
ds_read_b128 v[48:51],v4 offset:5120
v_mfma_f32_16x16x32_bf16 v[28:31],v[172:175],v[60:63],v[28:31]
buffer_load_dwordx4 v2,s[32:35],s63 offen offset:1024 sc1 lds
v_mfma_f32_16x16x32_bf16 v[24:27],v[168:171],v[60:63],v[24:27]
ds_read_b128 v[52:55],v4 offset:19456
v_mfma_f32_16x16x32_bf16 v[24:27],a[248:251],v[56:59],v[24:27]
s_waitcnt lgkmcnt(4)
v_mfma_f32_16x16x32_bf16 v[24:27],a[32:35],v[32:35],v[24:27]
ds_read_b128 v[56:59],v4 offset:33792
v_mfma_f32_16x16x32_bf16 v[24:27],a[144:147],v[36:39],v[24:27]
buffer_load_dwordx4 v2,s[32:35],s63 offen offset:2048 sc1 lds
v_mfma_f32_16x16x32_bf16 v[28:31],a[148:151],v[36:39],v[28:31]
ds_read_b128 v[60:63],v4 offset:48128
v_mfma_f32_16x16x32_bf16 v[28:31],a[36:39],v[32:35],v[28:31]
s_waitcnt lgkmcnt(4)
v_mfma_f32_16x16x32_bf16 v[28:31],v[68:71],v[40:43],v[28:31]
ds_read_b128 v[32:35],v4 offset:6144
v_mfma_f32_16x16x32_bf16 v[28:31],v[180:183],v[44:47],v[28:31]
buffer_load_dwordx4 v2,s[32:35],s63 offen offset:3072 sc1 lds
v_mfma_f32_16x16x32_bf16 v[24:27],v[176:179],v[44:47],v[24:27]
ds_read_b128 v[36:39],v4 offset:20480
v_mfma_f32_16x16x32_bf16 v[24:27],v[64:67],v[40:43],v[24:27]
s_waitcnt lgkmcnt(4)
v_mfma_f32_16x16x32_bf16 v[24:27],a[40:43],v[48:51],v[24:27]
ds_read_b128 v[40:43],v4 offset:34816
v_mfma_f32_16x16x32_bf16 v[24:27],a[152:155],v[52:55],v[24:27]
buffer_load_dwordx4 v[12:15],v2,s[32:35],s71 offen sc1
s_add_u32 s71,s71,1187840
v_mfma_f32_16x16x32_bf16 v[28:31],a[156:159],v[52:55],v[28:31]
ds_read_b128 v[44:47],v4 offset:49152
s_add_u32 s63,s63,4096
v_mfma_f32_16x16x32_bf16 v[28:31],a[44:47],v[48:51],v[28:31]
s_add_u32 m0,s64,4096
s_waitcnt lgkmcnt(4)
v_mfma_f32_16x16x32_bf16 v[28:31],v[76:79],v[56:59],v[28:31]
ds_read_b128 v[48:51],v4 offset:7168
v_mfma_f32_16x16x32_bf16 v[28:31],v[188:191],v[60:63],v[28:31]
buffer_load_dwordx4 v2,s[32:35],s63 offen offset:0 sc1 lds
v_mfma_f32_16x16x32_bf16 v[24:27],v[184:187],v[60:63],v[24:27]
ds_read_b128 v[52:55],v4 offset:21504
v_mfma_f32_16x16x32_bf16 v[24:27],v[72:75],v[56:59],v[24:27]
s_waitcnt lgkmcnt(4)
v_mfma_f32_16x16x32_bf16 v[24:27],a[48:51],v[32:35],v[24:27]
ds_read_b128 v[56:59],v4 offset:35840
v_mfma_f32_16x16x32_bf16 v[24:27],a[160:163],v[36:39],v[24:27]
buffer_load_dwordx4 v2,s[32:35],s63 offen offset:1024 sc1 lds
v_mfma_f32_16x16x32_bf16 v[28:31],a[164:167],v[36:39],v[28:31]
ds_read_b128 v[60:63],v4 offset:50176
v_mfma_f32_16x16x32_bf16 v[28:31],a[52:55],v[32:35],v[28:31]
s_waitcnt lgkmcnt(4)
v_mfma_f32_16x16x32_bf16 v[28:31],v[84:87],v[40:43],v[28:31]
ds_read_b128 v[32:35],v4 offset:8192
v_mfma_f32_16x16x32_bf16 v[28:31],v[196:199],v[44:47],v[28:31]
buffer_load_dwordx4 v2,s[32:35],s63 offen offset:2048 sc1 lds
v_mfma_f32_16x16x32_bf16 v[24:27],v[192:195],v[44:47],v[24:27]
ds_read_b128 v[36:39],v4 offset:22528
v_mfma_f32_16x16x32_bf16 v[24:27],v[80:83],v[40:43],v[24:27]
s_waitcnt lgkmcnt(4)
v_mfma_f32_16x16x32_bf16 v[24:27],a[56:59],v[48:51],v[24:27]
ds_read_b128 v[40:43],v4 offset:36864
v_mfma_f32_16x16x32_bf16 v[24:27],a[168:171],v[52:55],v[24:27]
buffer_load_dwordx4 v2,s[32:35],s63 offen offset:3072 sc1 lds
v_mfma_f32_16x16x32_bf16 v[28:31],a[172:175],v[52:55],v[28:31]
ds_read_b128 v[44:47],v4 offset:51200
v_mfma_f32_16x16x32_bf16 v[28:31],a[60:63],v[48:51],v[28:31]
s_waitcnt lgkmcnt(4)
v_mfma_f32_16x16x32_bf16 v[28:31],v[92:95],v[56:59],v[28:31]
ds_read_b128 v[48:51],v4 offset:9216
v_mfma_f32_16x16x32_bf16 v[28:31],v[204:207],v[60:63],v[28:31]
s_add_u32 s63,s63,4096
s_add_u32 m0,s64,8192
v_mfma_f32_16x16x32_bf16 v[24:27],v[200:203],v[60:63],v[24:27]
ds_read_b128 v[52:55],v4 offset:23552
v_mfma_f32_16x16x32_bf16 v[24:27],v[88:91],v[56:59],v[24:27]
s_waitcnt lgkmcnt(4)
v_mfma_f32_16x16x32_bf16 v[24:27],a[64:67],v[32:35],v[24:27]
ds_read_b128 v[56:59],v4 offset:37888
v_mfma_f32_16x16x32_bf16 v[24:27],a[176:179],v[36:39],v[24:27]
buffer_load_dwordx4 v2,s[32:35],s63 offen offset:0 sc1 lds
v_mfma_f32_16x16x32_bf16 v[28:31],a[180:183],v[36:39],v[28:31]
ds_read_b128 v[60:63],v4 offset:52224
v_mfma_f32_16x16x32_bf16 v[28:31],a[68:71],v[32:35],v[28:31]
s_waitcnt lgkmcnt(4)
v_mfma_f32_16x16x32_bf16 v[28:31],v[100:103],v[40:43],v[28:31]
ds_read_b128 v[32:35],v4 offset:10240
v_mfma_f32_16x16x32_bf16 v[28:31],v[212:215],v[44:47],v[28:31]
buffer_load_dwordx4 v2,s[32:35],s63 offen offset:1024 sc1 lds
v_mfma_f32_16x16x32_bf16 v[24:27],v[208:211],v[44:47],v[24:27]
ds_read_b128 v[36:39],v4 offset:24576
v_mfma_f32_16x16x32_bf16 v[24:27],v[96:99],v[40:43],v[24:27]
)#"
R"#(
s_waitcnt lgkmcnt(4)
v_mfma_f32_16x16x32_bf16 v[24:27],a[72:75],v[48:51],v[24:27]
ds_read_b128 v[40:43],v4 offset:38912
v_mfma_f32_16x16x32_bf16 v[24:27],a[184:187],v[52:55],v[24:27]
buffer_load_dwordx4 v2,s[32:35],s63 offen offset:2048 sc1 lds
v_mfma_f32_16x16x32_bf16 v[28:31],a[188:191],v[52:55],v[28:31]
ds_read_b128 v[44:47],v4 offset:53248
v_mfma_f32_16x16x32_bf16 v[28:31],a[76:79],v[48:51],v[28:31]
s_waitcnt lgkmcnt(4)
v_mfma_f32_16x16x32_bf16 v[28:31],v[108:111],v[56:59],v[28:31]
ds_read_b128 v[48:51],v4 offset:11264
v_mfma_f32_16x16x32_bf16 v[28:31],v[220:223],v[60:63],v[28:31]
buffer_load_dwordx2 v[6:7],v0,s[32:35],s66 offen sc1
v_mfma_f32_16x16x32_bf16 v[24:27],v[216:219],v[60:63],v[24:27]
ds_read_b128 v[52:55],v4 offset:25600
v_mfma_f32_16x16x32_bf16 v[24:27],v[104:107],v[56:59],v[24:27]
s_waitcnt lgkmcnt(4)
v_mfma_f32_16x16x32_bf16 v[24:27],a[80:83],v[32:35],v[24:27]
ds_read_b128 v[56:59],v4 offset:39936
v_mfma_f32_16x16x32_bf16 v[24:27],a[192:195],v[36:39],v[24:27]
buffer_load_dwordx4 v2,s[32:35],s63 offen offset:3072 sc1 lds
s_add_u32 s63,s63,4096
v_mfma_f32_16x16x32_bf16 v[28:31],a[196:199],v[36:39],v[28:31]
ds_read_b128 v[60:63],v4 offset:54272
s_add_u32 m0,s64,12288
v_mfma_f32_16x16x32_bf16 v[28:31],a[84:87],v[32:35],v[28:31]
s_waitcnt lgkmcnt(4)
v_mfma_f32_16x16x32_bf16 v[28:31],v[116:119],v[40:43],v[28:31]
ds_read_b128 v[32:35],v4 offset:12288
v_mfma_f32_16x16x32_bf16 v[28:31],v[228:231],v[44:47],v[28:31]
buffer_load_dwordx4 v2,s[32:35],s63 offen offset:0 sc1 lds
v_mfma_f32_16x16x32_bf16 v[24:27],v[224:227],v[44:47],v[24:27]
ds_read_b128 v[36:39],v4 offset:26624
v_mfma_f32_16x16x32_bf16 v[24:27],v[112:115],v[40:43],v[24:27]
s_waitcnt lgkmcnt(4)
v_mfma_f32_16x16x32_bf16 v[24:27],a[88:91],v[48:51],v[24:27]
ds_read_b128 v[40:43],v4 offset:40960
v_mfma_f32_16x16x32_bf16 v[24:27],a[200:203],v[52:55],v[24:27]
buffer_load_dwordx4 v2,s[32:35],s63 offen offset:1024 sc1 lds
s_add_u32 s63,s63,1175552
v_mfma_f32_16x16x32_bf16 v[28:31],a[204:207],v[52:55],v[28:31]
ds_read_b128 v[44:47],v4 offset:55296
v_mfma_f32_16x16x32_bf16 v[28:31],a[92:95],v[48:51],v[28:31]
s_waitcnt lgkmcnt(4)
v_mfma_f32_16x16x32_bf16 v[28:31],v[124:127],v[56:59],v[28:31]
ds_read_b128 v[48:51],v4 offset:13312
s_waitcnt vmcnt(11)
v_mfma_f32_16x16x32_bf16 v[28:31],v[236:239],v[60:63],v[28:31]
v_lshlrev_b32 v16,16,v12
v_and_b32 v17,0xffff0000,v12
v_mfma_f32_16x16x32_bf16 v[24:27],v[232:235],v[60:63],v[24:27]
ds_read_b128 v[52:55],v4 offset:27648
v_lshlrev_b32 v18,16,v13
v_mfma_f32_16x16x32_bf16 v[24:27],v[120:123],v[56:59],v[24:27]
v_and_b32 v19,0xffff0000,v13
s_waitcnt lgkmcnt(4)
v_mfma_f32_16x16x32_bf16 v[24:27],a[96:99],v[32:35],v[24:27]
ds_read_b128 v[56:59],v4 offset:41984
v_lshlrev_b32 v20,16,v14
v_mfma_f32_16x16x32_bf16 v[24:27],a[208:211],v[36:39],v[24:27]
v_and_b32 v21,0xffff0000,v14
v_lshlrev_b32 v22,16,v15
v_mfma_f32_16x16x32_bf16 v[28:31],a[212:215],v[36:39],v[28:31]
s_barrier
v_mfma_f32_16x16x32_bf16 v[28:31],a[100:103],v[32:35],v[28:31]
ds_read_b128 v[60:63],v4 offset:56320
s_waitcnt lgkmcnt(4)
v_mfma_f32_16x16x32_bf16 v[28:31],v[132:135],v[40:43],v[28:31]
ds_read_b128 v[32:35],v2 offset:0
v_and_b32 v23,0xffff0000,v15
v_mfma_f32_16x16x32_bf16 v[28:31],v[244:247],v[44:47],v[28:31]
v_mfma_f32_16x16x32_bf16 v[24:27],v[240:243],v[44:47],v[24:27]
ds_read_b128 v[36:39],v2 offset:14336
v_mfma_f32_16x16x32_bf16 v[24:27],v[128:131],v[40:43],v[24:27]
s_waitcnt lgkmcnt(4)
v_mfma_f32_16x16x32_bf16 v[24:27],a[104:107],v[48:51],v[24:27]
ds_read_b128 v[40:43],v2 offset:28672
v_mfma_f32_16x16x32_bf16 v[24:27],a[216:219],v[52:55],v[24:27]
v_mfma_f32_16x16x32_bf16 v[28:31],a[220:223],v[52:55],v[28:31]
ds_read_b128 v[44:47],v2 offset:43008
v_mfma_f32_16x16x32_bf16 v[28:31],a[108:111],v[48:51],v[28:31]
s_waitcnt lgkmcnt(4)
v_mfma_f32_16x16x32_bf16 v[28:31],v[140:143],v[56:59],v[28:31]
ds_read_b128 v[48:51],v2 offset:1024
v_mfma_f32_16x16x32_bf16 v[28:31],v[252:255],v[60:63],v[28:31]
v_mfma_f32_16x16x32_bf16 v[24:27],v[248:251],v[60:63],v[24:27]
ds_read_b128 v[52:55],v2 offset:15360
s_sub_u32 s62,s62,1
v_mfma_f32_16x16x32_bf16 v[24:27],v[136:139],v[56:59],v[24:27]
s_cbranch_scc1 .L_dmt_1
s_waitcnt lgkmcnt(4)
v_mfma_f32_16x16x32_bf16 v[16:19],a[0:3],v[32:35],v[16:19]
ds_read_b128 v[56:59],v2 offset:29696
v_mfma_f32_16x16x32_bf16 v[16:19],a[112:115],v[36:39],v[16:19]
v_mfma_f32_16x16x32_bf16 v[20:23],a[116:119],v[36:39],v[20:23]
ds_read_b128 v[60:63],v2 offset:44032
v_mfma_f32_16x16x32_bf16 v[20:23],a[4:7],v[32:35],v[20:23]
v_cvt_pk_bf16_f32 v15,v30,v31
s_waitcnt lgkmcnt(4)
v_mfma_f32_16x16x32_bf16 v[20:23],a[228:231],v[40:43],v[20:23]
ds_read_b128 v[32:35],v2 offset:2048
v_cvt_pk_bf16_f32 v14,v28,v29
v_mfma_f32_16x16x32_bf16 v[20:23],v[148:151],v[44:47],v[20:23]
v_cvt_pk_bf16_f32 v13,v26,v27
v_cvt_pk_bf16_f32 v12,v24,v25
v_mfma_f32_16x16x32_bf16 v[16:19],v[144:147],v[44:47],v[16:19]
ds_read_b128 v[36:39],v2 offset:16384
ds_write_b128 v5,v[12:15]
v_mfma_f32_16x16x32_bf16 v[16:19],a[224:227],v[40:43],v[16:19]
ds_read2st64_b32 v[12:13],v3 offset0:0 offset1:1
s_waitcnt lgkmcnt(6)
v_mfma_f32_16x16x32_bf16 v[16:19],a[8:11],v[48:51],v[16:19]
ds_read_b128 v[40:43],v2 offset:30720
ds_read2st64_b32 v[14:15],v3 offset0:2 offset1:3
v_mfma_f32_16x16x32_bf16 v[16:19],a[120:123],v[52:55],v[16:19]
v_mfma_f32_16x16x32_bf16 v[20:23],a[124:127],v[52:55],v[20:23]
ds_read_b128 v[44:47],v2 offset:45056
v_mfma_f32_16x16x32_bf16 v[20:23],a[12:15],v[48:51],v[20:23]
s_waitcnt lgkmcnt(7)
v_mfma_f32_16x16x32_bf16 v[20:23],a[236:239],v[56:59],v[20:23]
ds_read_b128 v[48:51],v2 offset:3072
v_mfma_f32_16x16x32_bf16 v[20:23],v[156:159],v[60:63],v[20:23]
s_waitcnt lgkmcnt(2)
buffer_atomic_pk_add_bf16 v12,v1,s[44:47],s65 offen offset:0 sc1
v_mfma_f32_16x16x32_bf16 v[16:19],v[152:155],v[60:63],v[16:19]
ds_read_b128 v[52:55],v2 offset:17408
s_waitcnt vmcnt(4)
v_mfma_f32_16x16x32_bf16 v[16:19],a[232:235],v[56:59],v[16:19]
buffer_atomic_pk_add_bf16 v13,v1,s[44:47],s65 offen offset:2048 sc1
v_mfma_f32_16x16x32_bf16 v[16:19],a[16:19],v[32:35],v[16:19]
ds_read_b128 v[56:59],v2 offset:31744
v_cmp_ne_u64 vcc,v[8:9],v[6:7]
v_mfma_f32_16x16x32_bf16 v[16:19],a[128:131],v[36:39],v[16:19]
s_cbranch_vccnz .L_rtm0_1
.L_bkm0_1:
s_add_u32 s66,s66,1187840
v_mfma_f32_16x16x32_bf16 v[20:23],a[132:135],v[36:39],v[20:23]
s_waitcnt vmcnt(2)
s_barrier
v_mfma_f32_16x16x32_bf16 v[20:23],a[20:23],v[32:35],v[20:23]
ds_read_b128 v[60:63],v2 offset:46080
s_waitcnt lgkmcnt(4)
v_mfma_f32_16x16x32_bf16 v[20:23],a[244:247],v[40:43],v[20:23]
ds_read_b128 v[32:35],v2 offset:4096
s_add_u32 m0,s64,57344
v_mfma_f32_16x16x32_bf16 v[20:23],v[164:167],v[44:47],v[20:23]
buffer_load_dwordx4 v2,s[32:35],s63 offen offset:0 sc1 lds
s_add_u32 s65,s65,16384
v_mfma_f32_16x16x32_bf16 v[16:19],v[160:163],v[44:47],v[16:19]
ds_read_b128 v[36:39],v2 offset:18432
v_mfma_f32_16x16x32_bf16 v[16:19],a[240:243],v[40:43],v[16:19]
buffer_atomic_pk_add_bf16 v14,v1,s[44:47],s65 offen offset:0 sc1
s_waitcnt lgkmcnt(4)
v_mfma_f32_16x16x32_bf16 v[16:19],a[24:27],v[48:51],v[16:19]
ds_read_b128 v[40:43],v2 offset:32768
v_mfma_f32_16x16x32_bf16 v[16:19],a[136:139],v[52:55],v[16:19]
buffer_atomic_pk_add_bf16 v15,v1,s[44:47],s65 offen offset:2048 sc1
s_add_u32 s65,s65,16384
v_mfma_f32_16x16x32_bf16 v[20:23],a[140:143],v[52:55],v[20:23]
ds_read_b128 v[44:47],v2 offset:47104
v_mfma_f32_16x16x32_bf16 v[20:23],a[28:31],v[48:51],v[20:23]
s_waitcnt lgkmcnt(4)
v_mfma_f32_16x16x32_bf16 v[20:23],a[252:255],v[56:59],v[20:23]
ds_read_b128 v[48:51],v2 offset:5120
v_mfma_f32_16x16x32_bf16 v[20:23],v[172:175],v[60:63],v[20:23]
buffer_load_dwordx4 v2,s[32:35],s63 offen offset:1024 sc1 lds
v_mfma_f32_16x16x32_bf16 v[16:19],v[168:171],v[60:63],v[16:19]
ds_read_b128 v[52:55],v2 offset:19456
v_mfma_f32_16x16x32_bf16 v[16:19],a[248:251],v[56:59],v[16:19]
s_waitcnt lgkmcnt(4)
v_mfma_f32_16x16x32_bf16 v[16:19],a[32:35],v[32:35],v[16:19]
ds_read_b128 v[56:59],v2 offset:33792
v_mfma_f32_16x16x32_bf16 v[16:19],a[144:147],v[36:39],v[16:19]
buffer_load_dwordx4 v2,s[32:35],s63 offen offset:2048 sc1 lds
v_mfma_f32_16x16x32_bf16 v[20:23],a[148:151],v[36:39],v[20:23]
ds_read_b128 v[60:63],v2 offset:48128
v_mfma_f32_16x16x32_bf16 v[20:23],a[36:39],v[32:35],v[20:23]
s_waitcnt lgkmcnt(4)
v_mfma_f32_16x16x32_bf16 v[20:23],v[68:71],v[40:43],v[20:23]
ds_read_b128 v[32:35],v2 offset:6144
v_mfma_f32_16x16x32_bf16 v[20:23],v[180:183],v[44:47],v[20:23]
buffer_load_dwordx4 v2,s[32:35],s63 offen offset:3072 sc1 lds
v_mfma_f32_16x16x32_bf16 v[16:19],v[176:179],v[44:47],v[16:19]
ds_read_b128 v[36:39],v2 offset:20480
v_mfma_f32_16x16x32_bf16 v[16:19],v[64:67],v[40:43],v[16:19]
s_waitcnt lgkmcnt(4)
v_mfma_f32_16x16x32_bf16 v[16:19],a[40:43],v[48:51],v[16:19]
ds_read_b128 v[40:43],v2 offset:34816
v_mfma_f32_16x16x32_bf16 v[16:19],a[152:155],v[52:55],v[16:19]
buffer_load_dwordx4 v[12:15],v2,s[32:35],s71 offen sc1
s_add_u32 s71,s71,1187840
v_mfma_f32_16x16x32_bf16 v[20:23],a[156:159],v[52:55],v[20:23]
ds_read_b128 v[44:47],v2 offset:49152
s_add_u32 s63,s63,4096
v_mfma_f32_16x16x32_bf16 v[20:23],a[44:47],v[48:51],v[20:23]
s_add_u32 m0,s64,61440
s_waitcnt lgkmcnt(4)
v_mfma_f32_16x16x32_bf16 v[20:23],v[76:79],v[56:59],v[20:23]
ds_read_b128 v[48:51],v2 offset:7168
v_mfma_f32_16x16x32_bf16 v[20:23],v[188:191],v[60:63],v[20:23]
buffer_load_dwordx4 v2,s[32:35],s63 offen offset:0 sc1 lds
v_mfma_f32_16x16x32_bf16 v[16:19],v[184:187],v[60:63],v[16:19]
ds_read_b128 v[52:55],v2 offset:21504
v_mfma_f32_16x16x32_bf16 v[16:19],v[72:75],v[56:59],v[16:19]
s_waitcnt lgkmcnt(4)
v_mfma_f32_16x16x32_bf16 v[16:19],a[48:51],v[32:35],v[16:19]
ds_read_b128 v[56:59],v2 offset:35840
v_mfma_f32_16x16x32_bf16 v[16:19],a[160:163],v[36:39],v[16:19]
buffer_load_dwordx4 v2,s[32:35],s63 offen offset:1024 sc1 lds
v_mfma_f32_16x16x32_bf16 v[20:23],a[164:167],v[36:39],v[20:23]
ds_read_b128 v[60:63],v2 offset:50176
v_mfma_f32_16x16x32_bf16 v[20:23],a[52:55],v[32:35],v[20:23]
s_waitcnt lgkmcnt(4)
v_mfma_f32_16x16x32_bf16 v[20:23],v[84:87],v[40:43],v[20:23]
ds_read_b128 v[32:35],v2 offset:8192
v_mfma_f32_16x16x32_bf16 v[20:23],v[196:199],v[44:47],v[20:23]
buffer_load_dwordx4 v2,s[32:35],s63 offen offset:2048 sc1 lds
v_mfma_f32_16x16x32_bf16 v[16:19],v[192:195],v[44:47],v[16:19]
ds_read_b128 v[36:39],v2 offset:22528
v_mfma_f32_16x16x32_bf16 v[16:19],v[80:83],v[40:43],v[16:19]
s_waitcnt lgkmcnt(4)
v_mfma_f32_16x16x32_bf16 v[16:19],a[56:59],v[48:51],v[16:19]
ds_read_b128 v[40:43],v2 offset:36864
v_mfma_f32_16x16x32_bf16 v[16:19],a[168:171],v[52:55],v[16:19]
buffer_load_dwordx4 v2,s[32:35],s63 offen offset:3072 sc1 lds
v_mfma_f32_16x16x32_bf16 v[20:23],a[172:175],v[52:55],v[20:23]
ds_read_b128 v[44:47],v2 offset:51200
v_mfma_f32_16x16x32_bf16 v[20:23],a[60:63],v[48:51],v[20:23]
s_waitcnt lgkmcnt(4)
v_mfma_f32_16x16x32_bf16 v[20:23],v[92:95],v[56:59],v[20:23]
ds_read_b128 v[48:51],v2 offset:9216
v_mfma_f32_16x16x32_bf16 v[20:23],v[204:207],v[60:63],v[20:23]
s_add_u32 s63,s63,4096
s_add_u32 m0,s64,65536
v_mfma_f32_16x16x32_bf16 v[16:19],v[200:203],v[60:63],v[16:19]
ds_read_b128 v[52:55],v2 offset:23552
v_mfma_f32_16x16x32_bf16 v[16:19],v[88:91],v[56:59],v[16:19]
s_waitcnt lgkmcnt(4)
v_mfma_f32_16x16x32_bf16 v[16:19],a[64:67],v[32:35],v[16:19]
ds_read_b128 v[56:59],v2 offset:37888
v_mfma_f32_16x16x32_bf16 v[16:19],a[176:179],v[36:39],v[16:19]
buffer_load_dwordx4 v2,s[32:35],s63 offen offset:0 sc1 lds
v_mfma_f32_16x16x32_bf16 v[20:23],a[180:183],v[36:39],v[20:23]
ds_read_b128 v[60:63],v2 offset:52224
v_mfma_f32_16x16x32_bf16 v[20:23],a[68:71],v[32:35],v[20:23]
s_waitcnt lgkmcnt(4)
v_mfma_f32_16x16x32_bf16 v[20:23],v[100:103],v[40:43],v[20:23]
ds_read_b128 v[32:35],v2 offset:10240
v_mfma_f32_16x16x32_bf16 v[20:23],v[212:215],v[44:47],v[20:23]
buffer_load_dwordx4 v2,s[32:35],s63 offen offset:1024 sc1 lds
v_mfma_f32_16x16x32_bf16 v[16:19],v[208:211],v[44:47],v[16:19]
ds_read_b128 v[36:39],v2 offset:24576
v_mfma_f32_16x16x32_bf16 v[16:19],v[96:99],v[40:43],v[16:19]
s_waitcnt lgkmcnt(4)
v_mfma_f32_16x16x32_bf16 v[16:19],a[72:75],v[48:51],v[16:19]
ds_read_b128 v[40:43],v2 offset:38912
v_mfma_f32_16x16x32_bf16 v[16:19],a[184:187],v[52:55],v[16:19]
buffer_load_dwordx4 v2,s[32:35],s63 offen offset:2048 sc1 lds
v_mfma_f32_16x16x32_bf16 v[20:23],a[188:191],v[52:55],v[20:23]
ds_read_b128 v[44:47],v2 offset:53248
v_mfma_f32_16x16x32_bf16 v[20:23],a[76:79],v[48:51],v[20:23]
s_waitcnt lgkmcnt(4)
v_mfma_f32_16x16x32_bf16 v[20:23],v[108:111],v[56:59],v[20:23]
ds_read_b128 v[48:51],v2 offset:11264
v_mfma_f32_16x16x32_bf16 v[20:23],v[220:223],v[60:63],v[20:23]
buffer_load_dwordx2 v[6:7],v0,s[32:35],s66 offen sc1
v_mfma_f32_16x16x32_bf16 v[16:19],v[216:219],v[60:63],v[16:19]
ds_read_b128 v[52:55],v2 offset:25600
v_mfma_f32_16x16x32_bf16 v[16:19],v[104:107],v[56:59],v[16:19]
s_waitcnt lgkmcnt(4)
v_mfma_f32_16x16x32_bf16 v[16:19],a[80:83],v[32:35],v[16:19]
ds_read_b128 v[56:59],v2 offset:39936
v_mfma_f32_16x16x32_bf16 v[16:19],a[192:195],v[36:39],v[16:19]
buffer_load_dwordx4 v2,s[32:35],s63 offen offset:3072 sc1 lds
s_add_u32 s63,s63,4096
v_mfma_f32_16x16x32_bf16 v[20:23],a[196:199],v[36:39],v[20:23]
ds_read_b128 v[60:63],v2 offset:54272
s_add_u32 m0,s64,69632
v_mfma_f32_16x16x32_bf16 v[20:23],a[84:87],v[32:35],v[20:23]
s_waitcnt lgkmcnt(4)
v_mfma_f32_16x16x32_bf16 v[20:23],v[116:119],v[40:43],v[20:23]
ds_read_b128 v[32:35],v2 offset:12288
v_mfma_f32_16x16x32_bf16 v[20:23],v[228:231],v[44:47],v[20:23]
buffer_load_dwordx4 v2,s[32:35],s63 offen offset:0 sc1 lds
v_mfma_f32_16x16x32_bf16 v[16:19],v[224:227],v[44:47],v[16:19]
ds_read_b128 v[36:39],v2 offset:26624
v_mfma_f32_16x16x32_bf16 v[16:19],v[112:115],v[40:43],v[16:19]
s_waitcnt lgkmcnt(4)
v_mfma_f32_16x16x32_bf16 v[16:19],a[88:91],v[48:51],v[16:19]
ds_read_b128 v[40:43],v2 offset:40960
v_mfma_f32_16x16x32_bf16 v[16:19],a[200:203],v[52:55],v[16:19]
buffer_load_dwordx4 v2,s[32:35],s63 offen offset:1024 sc1 lds
s_add_u32 s63,s63,1175552
v_mfma_f32_16x16x32_bf16 v[20:23],a[204:207],v[52:55],v[20:23]
ds_read_b128 v[44:47],v2 offset:55296
v_mfma_f32_16x16x32_bf16 v[20:23],a[92:95],v[48:51],v[20:23]
s_waitcnt lgkmcnt(4)
v_mfma_f32_16x16x32_bf16 v[20:23],v[124:127],v[56:59],v[20:23]
ds_read_b128 v[48:51],v2 offset:13312
s_waitcnt vmcnt(11)
v_mfma_f32_16x16x32_bf16 v[20:23],v[236:239],v[60:63],v[20:23]
v_lshlrev_b32 v24,16,v12
v_and_b32 v25,0xffff0000,v12
v_mfma_f32_16x16x32_bf16 v[16:19],v[232:235],v[60:63],v[16:19]
ds_read_b128 v[52:55],v2 offset:27648
v_lshlrev_b32 v26,16,v13
v_mfma_f32_16x16x32_bf16 v[16:19],v[120:123],v[56:59],v[16:19]
v_and_b32 v27,0xffff0000,v13
s_waitcnt lgkmcnt(4)
v_mfma_f32_16x16x32_bf16 v[16:19],a[96:99],v[32:35],v[16:19]
ds_read_b128 v[56:59],v2 offset:41984
v_lshlrev_b32 v28,16,v14
v_mfma_f32_16x16x32_bf16 v[16:19],a[208:211],v[36:39],v[16:19]
v_and_b32 v29,0xffff0000,v14
v_lshlrev_b32 v30,16,v15
v_mfma_f32_16x16x32_bf16 v[20:23],a[212:215],v[36:39],v[20:23]
s_barrier
v_mfma_f32_16x16x32_bf16 v[20:23],a[100:103],v[32:35],v[20:23]
ds_read_b128 v[60:63],v2 offset:56320
s_waitcnt lgkmcnt(4)
v_mfma_f32_16x16x32_bf16 v[20:23],v[132:135],v[40:43],v[20:23]
ds_read_b128 v[32:35],v4 offset:0
v_and_b32 v31,0xffff0000,v15
v_mfma_f32_16x16x32_bf16 v[20:23],v[244:247],v[44:47],v[20:23]
v_mfma_f32_16x16x32_bf16 v[16:19],v[240:243],v[44:47],v[16:19]
ds_read_b128 v[36:39],v4 offset:14336
v_mfma_f32_16x16x32_bf16 v[16:19],v[128:131],v[40:43],v[16:19]
s_waitcnt lgkmcnt(4)
v_mfma_f32_16x16x32_bf16 v[16:19],a[104:107],v[48:51],v[16:19]
ds_read_b128 v[40:43],v4 offset:28672
v_mfma_f32_16x16x32_bf16 v[16:19],a[216:219],v[52:55],v[16:19]
v_mfma_f32_16x16x32_bf16 v[20:23],a[220:223],v[52:55],v[20:23]
ds_read_b128 v[44:47],v4 offset:43008
v_mfma_f32_16x16x32_bf16 v[20:23],a[108:111],v[48:51],v[20:23]
s_waitcnt lgkmcnt(4)
v_mfma_f32_16x16x32_bf16 v[20:23],v[140:143],v[56:59],v[20:23]
ds_read_b128 v[48:51],v4 offset:1024
v_mfma_f32_16x16x32_bf16 v[20:23],v[252:255],v[60:63],v[20:23]
v_mfma_f32_16x16x32_bf16 v[16:19],v[248:251],v[60:63],v[16:19]
ds_read_b128 v[52:55],v4 offset:15360
v_mfma_f32_16x16x32_bf16 v[16:19],v[136:139],v[56:59],v[16:19]
s_branch .L_dml_1
.L_dmt_1:
s_nop 1
s_nop 0
v_cvt_pk_bf16_f32 v15,v30,v31
v_cvt_pk_bf16_f32 v14,v28,v29
s_nop 1
v_cvt_pk_bf16_f32 v13,v26,v27
v_cvt_pk_bf16_f32 v12,v24,v25
ds_write_b128 v5,v[12:15]
ds_read2st64_b32 v[12:13],v3 offset0:0 offset1:1
ds_read2st64_b32 v[14:15],v3 offset0:2 offset1:3
s_waitcnt lgkmcnt(0)
buffer_atomic_pk_add_bf16 v12,v1,s[44:47],s65 offen offset:0 sc1
buffer_atomic_pk_add_bf16 v13,v1,s[44:47],s65 offen offset:2048 sc1
s_add_u32 s65,s65,16384
buffer_atomic_pk_add_bf16 v14,v1,s[44:47],s65 offen offset:0 sc1
buffer_atomic_pk_add_bf16 v15,v1,s[44:47],s65 offen offset:2048 sc1
s_add_u32 s65,s65,16384
s_branch .L_end
.p2align 6
.L_dlv2:
.L_dll_2:
s_waitcnt lgkmcnt(4)
v_mfma_f32_16x16x32_bf16 v[16:19],a[0:3],v[32:35],v[16:19]
ds_read_b128 v[56:59],v2 offset:29696
v_mfma_f32_16x16x32_bf16 v[16:19],a[112:115],v[36:39],v[16:19]
v_mfma_f32_16x16x32_bf16 v[20:23],a[116:119],v[36:39],v[20:23]
ds_read_b128 v[60:63],v2 offset:44032
v_mfma_f32_16x16x32_bf16 v[20:23],a[4:7],v[32:35],v[20:23]
s_add_u32 s68,s68,1187840
s_waitcnt lgkmcnt(4)
v_mfma_f32_16x16x32_bf16 v[20:23],a[228:231],v[40:43],v[20:23]
ds_read_b128 v[32:35],v2 offset:2048
v_mfma_f32_16x16x32_bf16 v[20:23],v[148:151],v[44:47],v[20:23]
buffer_store_dwordx4 v[28:31],v2,s[32:35],s68 offen offset:1024 sc1
v_mfma_f32_16x16x32_bf16 v[16:19],v[144:147],v[44:47],v[16:19]
ds_read_b128 v[36:39],v2 offset:16384
v_mfma_f32_16x16x32_bf16 v[16:19],a[224:227],v[40:43],v[16:19]
s_waitcnt lgkmcnt(4)
v_mfma_f32_16x16x32_bf16 v[16:19],a[8:11],v[48:51],v[16:19]
ds_read_b128 v[40:43],v2 offset:30720
v_mfma_f32_16x16x32_bf16 v[16:19],a[120:123],v[52:55],v[16:19]
buffer_store_dwordx4 v[24:27],v2,s[32:35],s68 offen sc1
s_mov_b32 s67,s68
v_mfma_f32_16x16x32_bf16 v[20:23],a[124:127],v[52:55],v[20:23]
ds_read_b128 v[44:47],v2 offset:45056
v_mfma_f32_16x16x32_bf16 v[20:23],a[12:15],v[48:51],v[20:23]
s_waitcnt lgkmcnt(4)
v_mfma_f32_16x16x32_bf16 v[20:23],a[236:239],v[56:59],v[20:23]
ds_read_b128 v[48:51],v2 offset:3072
v_mfma_f32_16x16x32_bf16 v[20:23],v[156:159],v[60:63],v[20:23]
v_mfma_f32_16x16x32_bf16 v[16:19],v[152:155],v[60:63],v[16:19]
ds_read_b128 v[52:55],v2 offset:17408
v_mfma_f32_16x16x32_bf16 v[16:19],a[232:235],v[56:59],v[16:19]
s_waitcnt lgkmcnt(4)
v_mfma_f32_16x16x32_bf16 v[16:19],a[16:19],v[32:35],v[16:19]
ds_read_b128 v[56:59],v2 offset:31744
v_mfma_f32_16x16x32_bf16 v[16:19],a[128:131],v[36:39],v[16:19]
s_waitcnt vmcnt(2)
s_barrier
v_mfma_f32_16x16x32_bf16 v[20:23],a[132:135],v[36:39],v[20:23]
ds_read_b128 v[60:63],v2 offset:46080
v_mfma_f32_16x16x32_bf16 v[20:23],a[20:23],v[32:35],v[20:23]
s_waitcnt lgkmcnt(4)
v_mfma_f32_16x16x32_bf16 v[20:23],a[244:247],v[40:43],v[20:23]
ds_read_b128 v[32:35],v2 offset:4096
s_add_u32 m0,s64,57344
v_mfma_f32_16x16x32_bf16 v[20:23],v[164:167],v[44:47],v[20:23]
buffer_load_dwordx4 v2,s[32:35],s63 offen offset:0 sc1 lds
v_mfma_f32_16x16x32_bf16 v[16:19],v[160:163],v[44:47],v[16:19]
ds_read_b128 v[36:39],v2 offset:18432
v_mfma_f32_16x16x32_bf16 v[16:19],a[240:243],v[40:43],v[16:19]
s_waitcnt lgkmcnt(4)
v_mfma_f32_16x16x32_bf16 v[16:19],a[24:27],v[48:51],v[16:19]
ds_read_b128 v[40:43],v2 offset:32768
v_mfma_f32_16x16x32_bf16 v[16:19],a[136:139],v[52:55],v[16:19]
v_mfma_f32_16x16x32_bf16 v[20:23],a[140:143],v[52:55],v[20:23]
ds_read_b128 v[44:47],v2 offset:47104
v_mfma_f32_16x16x32_bf16 v[20:23],a[28:31],v[48:51],v[20:23]
s_waitcnt lgkmcnt(4)
v_mfma_f32_16x16x32_bf16 v[20:23],a[252:255],v[56:59],v[20:23]
ds_read_b128 v[48:51],v2 offset:5120
v_mfma_f32_16x16x32_bf16 v[20:23],v[172:175],v[60:63],v[20:23]
buffer_load_dwordx4 v2,s[32:35],s63 offen offset:1024 sc1 lds
v_mfma_f32_16x16x32_bf16 v[16:19],v[168:171],v[60:63],v[16:19]
ds_read_b128 v[52:55],v2 offset:19456
v_mfma_f32_16x16x32_bf16 v[16:19],a[248:251],v[56:59],v[16:19]
s_waitcnt lgkmcnt(4)
v_mfma_f32_16x16x32_bf16 v[16:19],a[32:35],v[32:35],v[16:19]
ds_read_b128 v[56:59],v2 offset:33792
v_mfma_f32_16x16x32_bf16 v[16:19],a[144:147],v[36:39],v[16:19]
buffer_load_dwordx4 v2,s[32:35],s63 offen offset:2048 sc1 lds
v_mfma_f32_16x16x32_bf16 v[20:23],a[148:151],v[36:39],v[20:23]
ds_read_b128 v[60:63],v2 offset:48128
v_mfma_f32_16x16x32_bf16 v[20:23],a[36:39],v[32:35],v[20:23]
s_waitcnt lgkmcnt(4)
v_mfma_f32_16x16x32_bf16 v[20:23],v[68:71],v[40:43],v[20:23]
ds_read_b128 v[32:35],v2 offset:6144
v_mfma_f32_16x16x32_bf16 v[20:23],v[180:183],v[44:47],v[20:23]
buffer_load_dwordx4 v2,s[32:35],s63 offen offset:3072 sc1 lds
v_mfma_f32_16x16x32_bf16 v[16:19],v[176:179],v[44:47],v[16:19]
ds_read_b128 v[36:39],v2 offset:20480
v_mfma_f32_16x16x32_bf16 v[16:19],v[64:67],v[40:43],v[16:19]
s_waitcnt lgkmcnt(4)
v_mfma_f32_16x16x32_bf16 v[16:19],a[40:43],v[48:51],v[16:19]
ds_read_b128 v[40:43],v2 offset:34816
v_mfma_f32_16x16x32_bf16 v[16:19],a[152:155],v[52:55],v[16:19]
buffer_load_dwordx4 v[12:15],v2,s[32:35],s71 offen sc1
s_add_u32 s71,s71,1187840
v_mfma_f32_16x16x32_bf16 v[20:23],a[156:159],v[52:55],v[20:23]
ds_read_b128 v[44:47],v2 offset:49152
s_add_u32 s63,s63,4096
v_mfma_f32_16x16x32_bf16 v[20:23],a[44:47],v[48:51],v[20:23]
s_add_u32 m0,s64,61440
s_waitcnt lgkmcnt(4)
v_mfma_f32_16x16x32_bf16 v[20:23],v[76:79],v[56:59],v[20:23]
ds_read_b128 v[48:51],v2 offset:7168
v_mfma_f32_16x16x32_bf16 v[20:23],v[188:191],v[60:63],v[20:23]
buffer_load_dwordx4 v2,s[32:35],s63 offen offset:0 sc1 lds
v_mfma_f32_16x16x32_bf16 v[16:19],v[184:187],v[60:63],v[16:19]
ds_read_b128 v[52:55],v2 offset:21504
v_mfma_f32_16x16x32_bf16 v[16:19],v[72:75],v[56:59],v[16:19]
s_waitcnt lgkmcnt(4)
v_mfma_f32_16x16x32_bf16 v[16:19],a[48:51],v[32:35],v[16:19]
ds_read_b128 v[56:59],v2 offset:35840
v_mfma_f32_16x16x32_bf16 v[16:19],a[160:163],v[36:39],v[16:19]
buffer_load_dwordx4 v2,s[32:35],s63 offen offset:1024 sc1 lds
v_mfma_f32_16x16x32_bf16 v[20:23],a[164:167],v[36:39],v[20:23]
ds_read_b128 v[60:63],v2 offset:50176
v_mfma_f32_16x16x32_bf16 v[20:23],a[52:55],v[32:35],v[20:23]
s_waitcnt lgkmcnt(4)
v_mfma_f32_16x16x32_bf16 v[20:23],v[84:87],v[40:43],v[20:23]
ds_read_b128 v[32:35],v2 offset:8192
v_mfma_f32_16x16x32_bf16 v[20:23],v[196:199],v[44:47],v[20:23]
buffer_load_dwordx4 v2,s[32:35],s63 offen offset:2048 sc1 lds
v_mfma_f32_16x16x32_bf16 v[16:19],v[192:195],v[44:47],v[16:19]
ds_read_b128 v[36:39],v2 offset:22528
v_mfma_f32_16x16x32_bf16 v[16:19],v[80:83],v[40:43],v[16:19]
s_waitcnt lgkmcnt(4)
v_mfma_f32_16x16x32_bf16 v[16:19],a[56:59],v[48:51],v[16:19]
ds_read_b128 v[40:43],v2 offset:36864
v_mfma_f32_16x16x32_bf16 v[16:19],a[168:171],v[52:55],v[16:19]
buffer_load_dwordx4 v2,s[32:35],s63 offen offset:3072 sc1 lds
v_mfma_f32_16x16x32_bf16 v[20:23],a[172:175],v[52:55],v[20:23]
ds_read_b128 v[44:47],v2 offset:51200
v_mfma_f32_16x16x32_bf16 v[20:23],a[60:63],v[48:51],v[20:23]
s_waitcnt lgkmcnt(4)
v_mfma_f32_16x16x32_bf16 v[20:23],v[92:95],v[56:59],v[20:23]
ds_read_b128 v[48:51],v2 offset:9216
v_mfma_f32_16x16x32_bf16 v[20:23],v[204:207],v[60:63],v[20:23]
s_add_u32 s63,s63,4096
s_add_u32 m0,s64,65536
v_mfma_f32_16x16x32_bf16 v[16:19],v[200:203],v[60:63],v[16:19]
ds_read_b128 v[52:55],v2 offset:23552
v_mfma_f32_16x16x32_bf16 v[16:19],v[88:91],v[56:59],v[16:19]
s_waitcnt lgkmcnt(4)
v_mfma_f32_16x16x32_bf16 v[16:19],a[64:67],v[32:35],v[16:19]
ds_read_b128 v[56:59],v2 offset:37888
)#"
R"#(
v_mfma_f32_16x16x32_bf16 v[16:19],a[176:179],v[36:39],v[16:19]
buffer_load_dwordx4 v2,s[32:35],s63 offen offset:0 sc1 lds
v_mfma_f32_16x16x32_bf16 v[20:23],a[180:183],v[36:39],v[20:23]
ds_read_b128 v[60:63],v2 offset:52224
v_mfma_f32_16x16x32_bf16 v[20:23],a[68:71],v[32:35],v[20:23]
s_waitcnt lgkmcnt(4)
v_mfma_f32_16x16x32_bf16 v[20:23],v[100:103],v[40:43],v[20:23]
ds_read_b128 v[32:35],v2 offset:10240
v_mfma_f32_16x16x32_bf16 v[20:23],v[212:215],v[44:47],v[20:23]
buffer_load_dwordx4 v2,s[32:35],s63 offen offset:1024 sc1 lds
v_mfma_f32_16x16x32_bf16 v[16:19],v[208:211],v[44:47],v[16:19]
ds_read_b128 v[36:39],v2 offset:24576
v_mfma_f32_16x16x32_bf16 v[16:19],v[96:99],v[40:43],v[16:19]
s_waitcnt lgkmcnt(4)
v_mfma_f32_16x16x32_bf16 v[16:19],a[72:75],v[48:51],v[16:19]
ds_read_b128 v[40:43],v2 offset:38912
v_mfma_f32_16x16x32_bf16 v[16:19],a[184:187],v[52:55],v[16:19]
buffer_load_dwordx4 v2,s[32:35],s63 offen offset:2048 sc1 lds
v_mfma_f32_16x16x32_bf16 v[20:23],a[188:191],v[52:55],v[20:23]
ds_read_b128 v[44:47],v2 offset:53248
v_mfma_f32_16x16x32_bf16 v[20:23],a[76:79],v[48:51],v[20:23]
s_waitcnt lgkmcnt(4)
v_mfma_f32_16x16x32_bf16 v[20:23],v[108:111],v[56:59],v[20:23]
ds_read_b128 v[48:51],v2 offset:11264
v_mfma_f32_16x16x32_bf16 v[20:23],v[220:223],v[60:63],v[20:23]
v_mfma_f32_16x16x32_bf16 v[16:19],v[216:219],v[60:63],v[16:19]
ds_read_b128 v[52:55],v2 offset:25600
v_mfma_f32_16x16x32_bf16 v[16:19],v[104:107],v[56:59],v[16:19]
s_waitcnt lgkmcnt(4)
v_mfma_f32_16x16x32_bf16 v[16:19],a[80:83],v[32:35],v[16:19]
ds_read_b128 v[56:59],v2 offset:39936
v_mfma_f32_16x16x32_bf16 v[16:19],a[192:195],v[36:39],v[16:19]
buffer_load_dwordx4 v2,s[32:35],s63 offen offset:3072 sc1 lds
s_add_u32 s63,s63,4096
v_mfma_f32_16x16x32_bf16 v[20:23],a[196:199],v[36:39],v[20:23]
ds_read_b128 v[60:63],v2 offset:54272
s_add_u32 m0,s64,69632
v_mfma_f32_16x16x32_bf16 v[20:23],a[84:87],v[32:35],v[20:23]
s_waitcnt lgkmcnt(4)
v_mfma_f32_16x16x32_bf16 v[20:23],v[116:119],v[40:43],v[20:23]
ds_read_b128 v[32:35],v2 offset:12288
v_mfma_f32_16x16x32_bf16 v[20:23],v[228:231],v[44:47],v[20:23]
buffer_load_dwordx4 v2,s[32:35],s63 offen offset:0 sc1 lds
v_mfma_f32_16x16x32_bf16 v[16:19],v[224:227],v[44:47],v[16:19]
ds_read_b128 v[36:39],v2 offset:26624
v_mfma_f32_16x16x32_bf16 v[16:19],v[112:115],v[40:43],v[16:19]
s_waitcnt lgkmcnt(4)
v_mfma_f32_16x16x32_bf16 v[16:19],a[88:91],v[48:51],v[16:19]
ds_read_b128 v[40:43],v2 offset:40960
v_mfma_f32_16x16x32_bf16 v[16:19],a[200:203],v[52:55],v[16:19]
buffer_load_dwordx4 v2,s[32:35],s63 offen offset:1024 sc1 lds
s_add_u32 s63,s63,1175552
v_mfma_f32_16x16x32_bf16 v[20:23],a[204:207],v[52:55],v[20:23]
ds_read_b128 v[44:47],v2 offset:55296
v_mfma_f32_16x16x32_bf16 v[20:23],a[92:95],v[48:51],v[20:23]
s_waitcnt lgkmcnt(4)
v_mfma_f32_16x16x32_bf16 v[20:23],v[124:127],v[56:59],v[20:23]
ds_read_b128 v[48:51],v2 offset:13312
s_waitcnt vmcnt(10)
v_mfma_f32_16x16x32_bf16 v[20:23],v[236:239],v[60:63],v[20:23]
v_lshlrev_b32 v24,16,v12
v_and_b32 v25,0xffff0000,v12
v_mfma_f32_16x16x32_bf16 v[16:19],v[232:235],v[60:63],v[16:19]
ds_read_b128 v[52:55],v2 offset:27648
v_lshlrev_b32 v26,16,v13
v_mfma_f32_16x16x32_bf16 v[16:19],v[120:123],v[56:59],v[16:19]
v_and_b32 v27,0xffff0000,v13
s_waitcnt lgkmcnt(4)
v_mfma_f32_16x16x32_bf16 v[16:19],a[96:99],v[32:35],v[16:19]
ds_read_b128 v[56:59],v2 offset:41984
v_lshlrev_b32 v28,16,v14
v_mfma_f32_16x16x32_bf16 v[16:19],a[208:211],v[36:39],v[16:19]
s_barrier
v_mfma_f32_16x16x32_bf16 v[20:23],a[212:215],v[36:39],v[20:23]
ds_read_b128 v[60:63],v2 offset:56320
v_and_b32 v29,0xffff0000,v14
v_mfma_f32_16x16x32_bf16 v[20:23],a[100:103],v[32:35],v[20:23]
v_lshlrev_b32 v30,16,v15
s_waitcnt lgkmcnt(4)
v_mfma_f32_16x16x32_bf16 v[20:23],v[132:135],v[40:43],v[20:23]
ds_read_b128 v[32:35],v4 offset:0
v_and_b32 v31,0xffff0000,v15
v_mfma_f32_16x16x32_bf16 v[20:23],v[244:247],v[44:47],v[20:23]
v_mfma_f32_16x16x32_bf16 v[16:19],v[240:243],v[44:47],v[16:19]
ds_read_b128 v[36:39],v4 offset:14336
v_mfma_f32_16x16x32_bf16 v[16:19],v[128:131],v[40:43],v[16:19]
s_waitcnt lgkmcnt(4)
v_mfma_f32_16x16x32_bf16 v[16:19],a[104:107],v[48:51],v[16:19]
ds_read_b128 v[40:43],v4 offset:28672
v_mfma_f32_16x16x32_bf16 v[16:19],a[216:219],v[52:55],v[16:19]
v_mfma_f32_16x16x32_bf16 v[20:23],a[220:223],v[52:55],v[20:23]
ds_read_b128 v[44:47],v4 offset:43008
v_mfma_f32_16x16x32_bf16 v[20:23],a[108:111],v[48:51],v[20:23]
s_waitcnt lgkmcnt(4)
v_mfma_f32_16x16x32_bf16 v[20:23],v[140:143],v[56:59],v[20:23]
ds_read_b128 v[48:51],v4 offset:1024
v_mfma_f32_16x16x32_bf16 v[20:23],v[252:255],v[60:63],v[20:23]
v_mfma_f32_16x16x32_bf16 v[16:19],v[248:251],v[60:63],v[16:19]
ds_read_b128 v[52:55],v4 offset:15360
s_sub_u32 s62,s62,1
v_mfma_f32_16x16x32_bf16 v[16:19],v[136:139],v[56:59],v[16:19]
s_cbranch_scc1 .L_dlb_2
s_waitcnt lgkmcnt(4)
v_mfma_f32_16x16x32_bf16 v[24:27],a[0:3],v[32:35],v[24:27]
ds_read_b128 v[56:59],v4 offset:29696
v_mfma_f32_16x16x32_bf16 v[24:27],a[112:115],v[36:39],v[24:27]
v_mfma_f32_16x16x32_bf16 v[28:31],a[116:119],v[36:39],v[28:31]
ds_read_b128 v[60:63],v4 offset:44032
v_mfma_f32_16x16x32_bf16 v[28:31],a[4:7],v[32:35],v[28:31]
s_add_u32 s68,s68,1187840
s_waitcnt lgkmcnt(4)
v_mfma_f32_16x16x32_bf16 v[28:31],a[228:231],v[40:43],v[28:31]
ds_read_b128 v[32:35],v4 offset:2048
v_mfma_f32_16x16x32_bf16 v[28:31],v[148:151],v[44:47],v[28:31]
buffer_store_dwordx4 v[20:23],v2,s[32:35],s68 offen offset:1024 sc1
v_mfma_f32_16x16x32_bf16 v[24:27],v[144:147],v[44:47],v[24:27]
ds_read_b128 v[36:39],v4 offset:16384
v_mfma_f32_16x16x32_bf16 v[24:27],a[224:227],v[40:43],v[24:27]
s_waitcnt lgkmcnt(4)
v_mfma_f32_16x16x32_bf16 v[24:27],a[8:11],v[48:51],v[24:27]
ds_read_b128 v[40:43],v4 offset:30720
v_mfma_f32_16x16x32_bf16 v[24:27],a[120:123],v[52:55],v[24:27]
buffer_store_dwordx4 v[16:19],v2,s[32:35],s68 offen sc1
s_mov_b32 s67,s68
v_mfma_f32_16x16x32_bf16 v[28:31],a[124:127],v[52:55],v[28:31]
ds_read_b128 v[44:47],v4 offset:45056
v_mfma_f32_16x16x32_bf16 v[28:31],a[12:15],v[48:51],v[28:31]
s_waitcnt lgkmcnt(4)
v_mfma_f32_16x16x32_bf16 v[28:31],a[236:239],v[56:59],v[28:31]
ds_read_b128 v[48:51],v4 offset:3072
v_mfma_f32_16x16x32_bf16 v[28:31],v[156:159],v[60:63],v[28:31]
v_mfma_f32_16x16x32_bf16 v[24:27],v[152:155],v[60:63],v[24:27]
ds_read_b128 v[52:55],v4 offset:17408
v_mfma_f32_16x16x32_bf16 v[24:27],a[232:235],v[56:59],v[24:27]
s_waitcnt lgkmcnt(4)
v_mfma_f32_16x16x32_bf16 v[24:27],a[16:19],v[32:35],v[24:27]
ds_read_b128 v[56:59],v4 offset:31744
v_mfma_f32_16x16x32_bf16 v[24:27],a[128:131],v[36:39],v[24:27]
s_waitcnt vmcnt(2)
s_barrier
v_mfma_f32_16x16x32_bf16 v[28:31],a[132:135],v[36:39],v[28:31]
ds_read_b128 v[60:63],v4 offset:46080
v_mfma_f32_16x16x32_bf16 v[28:31],a[20:23],v[32:35],v[28:31]
s_waitcnt lgkmcnt(4)
v_mfma_f32_16x16x32_bf16 v[28:31],a[244:247],v[40:43],v[28:31]
ds_read_b128 v[32:35],v4 offset:4096
s_add_u32 m0,s64,0
v_mfma_f32_16x16x32_bf16 v[28:31],v[164:167],v[44:47],v[28:31]
buffer_load_dwordx4 v2,s[32:35],s63 offen offset:0 sc1 lds
v_mfma_f32_16x16x32_bf16 v[24:27],v[160:163],v[44:47],v[24:27]
ds_read_b128 v[36:39],v4 offset:18432
v_mfma_f32_16x16x32_bf16 v[24:27],a[240:243],v[40:43],v[24:27]
s_waitcnt lgkmcnt(4)
v_mfma_f32_16x16x32_bf16 v[24:27],a[24:27],v[48:51],v[24:27]
ds_read_b128 v[40:43],v4 offset:32768
v_mfma_f32_16x16x32_bf16 v[24:27],a[136:139],v[52:55],v[24:27]
v_mfma_f32_16x16x32_bf16 v[28:31],a[140:143],v[52:55],v[28:31]
ds_read_b128 v[44:47],v4 offset:47104
v_mfma_f32_16x16x32_bf16 v[28:31],a[28:31],v[48:51],v[28:31]
s_waitcnt lgkmcnt(4)
v_mfma_f32_16x16x32_bf16 v[28:31],a[252:255],v[56:59],v[28:31]
ds_read_b128 v[48:51],v4 offset:5120
v_mfma_f32_16x16x32_bf16 v[28:31],v[172:175],v[60:63],v[28:31]
buffer_load_dwordx4 v2,s[32:35],s63 offen offset:1024 sc1 lds
v_mfma_f32_16x16x32_bf16 v[24:27],v[168:171],v[60:63],v[24:27]
ds_read_b128 v[52:55],v4 offset:19456
v_mfma_f32_16x16x32_bf16 v[24:27],a[248:251],v[56:59],v[24:27]
s_waitcnt lgkmcnt(4)
v_mfma_f32_16x16x32_bf16 v[24:27],a[32:35],v[32:35],v[24:27]
ds_read_b128 v[56:59],v4 offset:33792
v_mfma_f32_16x16x32_bf16 v[24:27],a[144:147],v[36:39],v[24:27]
buffer_load_dwordx4 v2,s[32:35],s63 offen offset:2048 sc1 lds
v_mfma_f32_16x16x32_bf16 v[28:31],a[148:151],v[36:39],v[28:31]
ds_read_b128 v[60:63],v4 offset:48128
v_mfma_f32_16x16x32_bf16 v[28:31],a[36:39],v[32:35],v[28:31]
s_waitcnt lgkmcnt(4)
v_mfma_f32_16x16x32_bf16 v[28:31],v[68:71],v[40:43],v[28:31]
ds_read_b128 v[32:35],v4 offset:6144
v_mfma_f32_16x16x32_bf16 v[28:31],v[180:183],v[44:47],v[28:31]
buffer_load_dwordx4 v2,s[32:35],s63 offen offset:3072 sc1 lds
v_mfma_f32_16x16x32_bf16 v[24:27],v[176:179],v[44:47],v[24:27]
ds_read_b128 v[36:39],v4 offset:20480
v_mfma_f32_16x16x32_bf16 v[24:27],v[64:67],v[40:43],v[24:27]
s_waitcnt lgkmcnt(4)
v_mfma_f32_16x16x32_bf16 v[24:27],a[40:43],v[48:51],v[24:27]
ds_read_b128 v[40:43],v4 offset:34816
v_mfma_f32_16x16x32_bf16 v[24:27],a[152:155],v[52:55],v[24:27]
buffer_load_dwordx4 v[12:15],v2,s[32:35],s71 offen sc1
s_add_u32 s71,s71,1187840
v_mfma_f32_16x16x32_bf16 v[28:31],a[156:159],v[52:55],v[28:31]
ds_read_b128 v[44:47],v4 offset:49152
s_add_u32 s63,s63,4096
v_mfma_f32_16x16x32_bf16 v[28:31],a[44:47],v[48:51],v[28:31]
s_add_u32 m0,s64,4096
s_waitcnt lgkmcnt(4)
v_mfma_f32_16x16x32_bf16 v[28:31],v[76:79],v[56:59],v[28:31]
ds_read_b128 v[48:51],v4 offset:7168
v_mfma_f32_16x16x32_bf16 v[28:31],v[188:191],v[60:63],v[28:31]
buffer_load_dwordx4 v2,s[32:35],s63 offen offset:0 sc1 lds
v_mfma_f32_16x16x32_bf16 v[24:27],v[184:187],v[60:63],v[24:27]
ds_read_b128 v[52:55],v4 offset:21504
v_mfma_f32_16x16x32_bf16 v[24:27],v[72:75],v[56:59],v[24:27]
s_waitcnt lgkmcnt(4)
v_mfma_f32_16x16x32_bf16 v[24:27],a[48:51],v[32:35],v[24:27]
ds_read_b128 v[56:59],v4 offset:35840
v_mfma_f32_16x16x32_bf16 v[24:27],a[160:163],v[36:39],v[24:27]
buffer_load_dwordx4 v2,s[32:35],s63 offen offset:1024 sc1 lds
v_mfma_f32_16x16x32_bf16 v[28:31],a[164:167],v[36:39],v[28:31]
ds_read_b128 v[60:63],v4 offset:50176
v_mfma_f32_16x16x32_bf16 v[28:31],a[52:55],v[32:35],v[28:31]
s_waitcnt lgkmcnt(4)
v_mfma_f32_16x16x32_bf16 v[28:31],v[84:87],v[40:43],v[28:31]
ds_read_b128 v[32:35],v4 offset:8192
v_mfma_f32_16x16x32_bf16 v[28:31],v[196:199],v[44:47],v[28:31]
buffer_load_dwordx4 v2,s[32:35],s63 offen offset:2048 sc1 lds
v_mfma_f32_16x16x32_bf16 v[24:27],v[192:195],v[44:47],v[24:27]
ds_read_b128 v[36:39],v4 offset:22528
v_mfma_f32_16x16x32_bf16 v[24:27],v[80:83],v[40:43],v[24:27]
s_waitcnt lgkmcnt(4)
v_mfma_f32_16x16x32_bf16 v[24:27],a[56:59],v[48:51],v[24:27]
ds_read_b128 v[40:43],v4 offset:36864
v_mfma_f32_16x16x32_bf16 v[24:27],a[168:171],v[52:55],v[24:27]
buffer_load_dwordx4 v2,s[32:35],s63 offen offset:3072 sc1 lds
v_mfma_f32_16x16x32_bf16 v[28:31],a[172:175],v[52:55],v[28:31]
ds_read_b128 v[44:47],v4 offset:51200
v_mfma_f32_16x16x32_bf16 v[28:31],a[60:63],v[48:51],v[28:31]
s_waitcnt lgkmcnt(4)
v_mfma_f32_16x16x32_bf16 v[28:31],v[92:95],v[56:59],v[28:31]
ds_read_b128 v[48:51],v4 offset:9216
v_mfma_f32_16x16x32_bf16 v[28:31],v[204:207],v[60:63],v[28:31]
s_add_u32 s63,s63,4096
s_add_u32 m0,s64,8192
v_mfma_f32_16x16x32_bf16 v[24:27],v[200:203],v[60:63],v[24:27]
ds_read_b128 v[52:55],v4 offset:23552
v_mfma_f32_16x16x32_bf16 v[24:27],v[88:91],v[56:59],v[24:27]
s_waitcnt lgkmcnt(4)
v_mfma_f32_16x16x32_bf16 v[24:27],a[64:67],v[32:35],v[24:27]
ds_read_b128 v[56:59],v4 offset:37888
v_mfma_f32_16x16x32_bf16 v[24:27],a[176:179],v[36:39],v[24:27]
buffer_load_dwordx4 v2,s[32:35],s63 offen offset:0 sc1 lds
v_mfma_f32_16x16x32_bf16 v[28:31],a[180:183],v[36:39],v[28:31]
ds_read_b128 v[60:63],v4 offset:52224
v_mfma_f32_16x16x32_bf16 v[28:31],a[68:71],v[32:35],v[28:31]
s_waitcnt lgkmcnt(4)
v_mfma_f32_16x16x32_bf16 v[28:31],v[100:103],v[40:43],v[28:31]
ds_read_b128 v[32:35],v4 offset:10240
v_mfma_f32_16x16x32_bf16 v[28:31],v[212:215],v[44:47],v[28:31]
buffer_load_dwordx4 v2,s[32:35],s63 offen offset:1024 sc1 lds
v_mfma_f32_16x16x32_bf16 v[24:27],v[208:211],v[44:47],v[24:27]
ds_read_b128 v[36:39],v4 offset:24576
v_mfma_f32_16x16x32_bf16 v[24:27],v[96:99],v[40:43],v[24:27]
s_waitcnt lgkmcnt(4)
v_mfma_f32_16x16x32_bf16 v[24:27],a[72:75],v[48:51],v[24:27]
ds_read_b128 v[40:43],v4 offset:38912
v_mfma_f32_16x16x32_bf16 v[24:27],a[184:187],v[52:55],v[24:27]
buffer_load_dwordx4 v2,s[32:35],s63 offen offset:2048 sc1 lds
v_mfma_f32_16x16x32_bf16 v[28:31],a[188:191],v[52:55],v[28:31]
ds_read_b128 v[44:47],v4 offset:53248
v_mfma_f32_16x16x32_bf16 v[28:31],a[76:79],v[48:51],v[28:31]
s_waitcnt lgkmcnt(4)
v_mfma_f32_16x16x32_bf16 v[28:31],v[108:111],v[56:59],v[28:31]
ds_read_b128 v[48:51],v4 offset:11264
v_mfma_f32_16x16x32_bf16 v[28:31],v[220:223],v[60:63],v[28:31]
v_mfma_f32_16x16x32_bf16 v[24:27],v[216:219],v[60:63],v[24:27]
ds_read_b128 v[52:55],v4 offset:25600
v_mfma_f32_16x16x32_bf16 v[24:27],v[104:107],v[56:59],v[24:27]
s_waitcnt lgkmcnt(4)
v_mfma_f32_16x16x32_bf16 v[24:27],a[80:83],v[32:35],v[24:27]
ds_read_b128 v[56:59],v4 offset:39936
v_mfma_f32_16x16x32_bf16 v[24:27],a[192:195],v[36:39],v[24:27]
buffer_load_dwordx4 v2,s[32:35],s63 offen offset:3072 sc1 lds
s_add_u32 s63,s63,4096
v_mfma_f32_16x16x32_bf16 v[28:31],a[196:199],v[36:39],v[28:31]
ds_read_b128 v[60:63],v4 offset:54272
s_add_u32 m0,s64,12288
v_mfma_f32_16x16x32_bf16 v[28:31],a[84:87],v[32:35],v[28:31]
s_waitcnt lgkmcnt(4)
v_mfma_f32_16x16x32_bf16 v[28:31],v[116:119],v[40:43],v[28:31]
ds_read_b128 v[32:35],v4 offset:12288
v_mfma_f32_16x16x32_bf16 v[28:31],v[228:231],v[44:47],v[28:31]
buffer_load_dwordx4 v2,s[32:35],s63 offen offset:0 sc1 lds
v_mfma_f32_16x16x32_bf16 v[24:27],v[224:227],v[44:47],v[24:27]
ds_read_b128 v[36:39],v4 offset:26624
v_mfma_f32_16x16x32_bf16 v[24:27],v[112:115],v[40:43],v[24:27]
s_waitcnt lgkmcnt(4)
v_mfma_f32_16x16x32_bf16 v[24:27],a[88:91],v[48:51],v[24:27]
ds_read_b128 v[40:43],v4 offset:40960
v_mfma_f32_16x16x32_bf16 v[24:27],a[200:203],v[52:55],v[24:27]
buffer_load_dwordx4 v2,s[32:35],s63 offen offset:1024 sc1 lds
s_add_u32 s63,s63,1175552
v_mfma_f32_16x16x32_bf16 v[28:31],a[204:207],v[52:55],v[28:31]
ds_read_b128 v[44:47],v4 offset:55296
v_mfma_f32_16x16x32_bf16 v[28:31],a[92:95],v[48:51],v[28:31]
s_waitcnt lgkmcnt(4)
v_mfma_f32_16x16x32_bf16 v[28:31],v[124:127],v[56:59],v[28:31]
ds_read_b128 v[48:51],v4 offset:13312
s_waitcnt vmcnt(10)
v_mfma_f32_16x16x32_bf16 v[28:31],v[236:239],v[60:63],v[28:31]
v_lshlrev_b32 v16,16,v12
v_and_b32 v17,0xffff0000,v12
v_mfma_f32_16x16x32_bf16 v[24:27],v[232:235],v[60:63],v[24:27]
ds_read_b128 v[52:55],v4 offset:27648
v_lshlrev_b32 v18,16,v13
v_mfma_f32_16x16x32_bf16 v[24:27],v[120:123],v[56:59],v[24:27]
v_and_b32 v19,0xffff0000,v13
s_waitcnt lgkmcnt(4)
v_mfma_f32_16x16x32_bf16 v[24:27],a[96:99],v[32:35],v[24:27]
ds_read_b128 v[56:59],v4 offset:41984
v_lshlrev_b32 v20,16,v14
v_mfma_f32_16x16x32_bf16 v[24:27],a[208:211],v[36:39],v[24:27]
s_barrier
v_mfma_f32_16x16x32_bf16 v[28:31],a[212:215],v[36:39],v[28:31]
ds_read_b128 v[60:63],v4 offset:56320
v_and_b32 v21,0xffff0000,v14
v_mfma_f32_16x16x32_bf16 v[28:31],a[100:103],v[32:35],v[28:31]
v_lshlrev_b32 v22,16,v15
s_waitcnt lgkmcnt(4)
v_mfma_f32_16x16x32_bf16 v[28:31],v[132:135],v[40:43],v[28:31]
ds_read_b128 v[32:35],v2 offset:0
v_and_b32 v23,0xffff0000,v15
v_mfma_f32_16x16x32_bf16 v[28:31],v[244:247],v[44:47],v[28:31]
v_mfma_f32_16x16x32_bf16 v[24:27],v[240:243],v[44:47],v[24:27]
ds_read_b128 v[36:39],v2 offset:14336
v_mfma_f32_16x16x32_bf16 v[24:27],v[128:131],v[40:43],v[24:27]
s_waitcnt lgkmcnt(4)
v_mfma_f32_16x16x32_bf16 v[24:27],a[104:107],v[48:51],v[24:27]
ds_read_b128 v[40:43],v2 offset:28672
v_mfma_f32_16x16x32_bf16 v[24:27],a[216:219],v[52:55],v[24:27]
v_mfma_f32_16x16x32_bf16 v[28:31],a[220:223],v[52:55],v[28:31]
ds_read_b128 v[44:47],v2 offset:43008
v_mfma_f32_16x16x32_bf16 v[28:31],a[108:111],v[48:51],v[28:31]
s_waitcnt lgkmcnt(4)
v_mfma_f32_16x16x32_bf16 v[28:31],v[140:143],v[56:59],v[28:31]
ds_read_b128 v[48:51],v2 offset:1024
v_mfma_f32_16x16x32_bf16 v[28:31],v[252:255],v[60:63],v[28:31]
v_mfma_f32_16x16x32_bf16 v[24:27],v[248:251],v[60:63],v[24:27]
ds_read_b128 v[52:55],v2 offset:15360
v_mfma_f32_16x16x32_bf16 v[24:27],v[136:139],v[56:59],v[24:27]
s_branch .L_dll_2
.L_dlb_2:
s_mov_b32 s62,15
s_add_u32 s74,s74,8192
v_lshrrev_b32 v5,8,v2
v_lshl_add_u32 v5,v5,4,s74
v_and_b32 v3,128,v2
v_lshl_add_u32 v5,v3,2,v5
v_and_b32 v3,16,v2
v_lshl_add_u32 v5,v3,4,v5
v_and_b32 v3,96,v2
v_lshl_add_u32 v5,v3,1,v5
v_lshrrev_b32 v3,2,v2
v_add_u32 v3,s74,v3
.p2align 6
.L_dml_2:
s_waitcnt lgkmcnt(4)
v_mfma_f32_16x16x32_bf16 v[24:27],a[0:3],v[32:35],v[24:27]
ds_read_b128 v[56:59],v4 offset:29696
v_mfma_f32_16x16x32_bf16 v[24:27],a[112:115],v[36:39],v[24:27]
v_mfma_f32_16x16x32_bf16 v[28:31],a[116:119],v[36:39],v[28:31]
ds_read_b128 v[60:63],v4 offset:44032
v_mfma_f32_16x16x32_bf16 v[28:31],a[4:7],v[32:35],v[28:31]
v_cvt_pk_bf16_f32 v15,v22,v23
s_waitcnt lgkmcnt(4)
v_mfma_f32_16x16x32_bf16 v[28:31],a[228:231],v[40:43],v[28:31]
ds_read_b128 v[32:35],v4 offset:2048
v_cvt_pk_bf16_f32 v14,v20,v21
v_mfma_f32_16x16x32_bf16 v[28:31],v[148:151],v[44:47],v[28:31]
v_cvt_pk_bf16_f32 v13,v18,v19
v_cvt_pk_bf16_f32 v12,v16,v17
v_mfma_f32_16x16x32_bf16 v[24:27],v[144:147],v[44:47],v[24:27]
ds_read_b128 v[36:39],v4 offset:16384
ds_write_b128 v5,v[12:15]
v_mfma_f32_16x16x32_bf16 v[24:27],a[224:227],v[40:43],v[24:27]
ds_read2st64_b32 v[12:13],v3 offset0:0 offset1:1
s_waitcnt lgkmcnt(6)
v_mfma_f32_16x16x32_bf16 v[24:27],a[8:11],v[48:51],v[24:27]
ds_read_b128 v[40:43],v4 offset:30720
ds_read2st64_b32 v[14:15],v3 offset0:2 offset1:3
v_mfma_f32_16x16x32_bf16 v[24:27],a[120:123],v[52:55],v[24:27]
v_mfma_f32_16x16x32_bf16 v[28:31],a[124:127],v[52:55],v[28:31]
ds_read_b128 v[44:47],v4 offset:45056
v_mfma_f32_16x16x32_bf16 v[28:31],a[12:15],v[48:51],v[28:31]
s_waitcnt lgkmcnt(7)
v_mfma_f32_16x16x32_bf16 v[28:31],a[236:239],v[56:59],v[28:31]
ds_read_b128 v[48:51],v4 offset:3072
v_mfma_f32_16x16x32_bf16 v[28:31],v[156:159],v[60:63],v[28:31]
s_waitcnt lgkmcnt(2)
buffer_atomic_pk_add_bf16 v12,v1,s[44:47],s65 offen offset:0 sc1
v_mfma_f32_16x16x32_bf16 v[24:27],v[152:155],v[60:63],v[24:27]
ds_read_b128 v[52:55],v4 offset:17408
v_mfma_f32_16x16x32_bf16 v[24:27],a[232:235],v[56:59],v[24:27]
buffer_atomic_pk_add_bf16 v13,v1,s[44:47],s65 offen offset:2048 sc1
v_mfma_f32_16x16x32_bf16 v[24:27],a[16:19],v[32:35],v[24:27]
ds_read_b128 v[56:59],v4 offset:31744
s_add_u32 s65,s65,16384
v_mfma_f32_16x16x32_bf16 v[24:27],a[128:131],v[36:39],v[24:27]
s_waitcnt vmcnt(2)
s_barrier
v_mfma_f32_16x16x32_bf16 v[28:31],a[132:135],v[36:39],v[28:31]
ds_read_b128 v[60:63],v4 offset:46080
v_mfma_f32_16x16x32_bf16 v[28:31],a[20:23],v[32:35],v[28:31]
buffer_atomic_pk_add_bf16 v14,v1,s[44:47],s65 offen offset:0 sc1
s_waitcnt lgkmcnt(4)
v_mfma_f32_16x16x32_bf16 v[28:31],a[244:247],v[40:43],v[28:31]
ds_read_b128 v[32:35],v4 offset:4096
s_add_u32 m0,s64,0
v_mfma_f32_16x16x32_bf16 v[28:31],v[164:167],v[44:47],v[28:31]
buffer_atomic_pk_add_bf16 v15,v1,s[44:47],s65 offen offset:2048 sc1
s_add_u32 s65,s65,16384
v_mfma_f32_16x16x32_bf16 v[24:27],v[160:163],v[44:47],v[24:27]
ds_read_b128 v[36:39],v4 offset:18432
v_mfma_f32_16x16x32_bf16 v[24:27],a[240:243],v[40:43],v[24:27]
s_waitcnt lgkmcnt(4)
v_mfma_f32_16x16x32_bf16 v[24:27],a[24:27],v[48:51],v[24:27]
ds_read_b128 v[40:43],v4 offset:32768
v_mfma_f32_16x16x32_bf16 v[24:27],a[136:139],v[52:55],v[24:27]
buffer_load_dwordx4 v2,s[32:35],s63 offen offset:0 sc1 lds
v_mfma_f32_16x16x32_bf16 v[28:31],a[140:143],v[52:55],v[28:31]
ds_read_b128 v[44:47],v4 offset:47104
v_mfma_f32_16x16x32_bf16 v[28:31],a[28:31],v[48:51],v[28:31]
s_waitcnt lgkmcnt(4)
v_mfma_f32_16x16x32_bf16 v[28:31],a[252:255],v[56:59],v[28:31]
ds_read_b128 v[48:51],v4 offset:5120
v_mfma_f32_16x16x32_bf16 v[28:31],v[172:175],v[60:63],v[28:31]
buffer_load_dwordx4 v2,s[32:35],s63 offen offset:1024 sc1 lds
v_mfma_f32_16x16x32_bf16 v[24:27],v[168:171],v[60:63],v[24:27]
ds_read_b128 v[52:55],v4 offset:19456
v_mfma_f32_16x16x32_bf16 v[24:27],a[248:251],v[56:59],v[24:27]
s_waitcnt lgkmcnt(4)
v_mfma_f32_16x16x32_bf16 v[24:27],a[32:35],v[32:35],v[24:27]
ds_read_b128 v[56:59],v4 offset:33792
v_mfma_f32_16x16x32_bf16 v[24:27],a[144:147],v[36:39],v[24:27]
buffer_load_dwordx4 v2,s[32:35],s63 offen offset:2048 sc1 lds
v_mfma_f32_16x16x32_bf16 v[28:31],a[148:151],v[36:39],v[28:31]
ds_read_b128 v[60:63],v4 offset:48128
v_mfma_f32_16x16x32_bf16 v[28:31],a[36:39],v[32:35],v[28:31]
s_waitcnt lgkmcnt(4)
v_mfma_f32_16x16x32_bf16 v[28:31],v[68:71],v[40:43],v[28:31]
ds_read_b128 v[32:35],v4 offset:6144
v_mfma_f32_16x16x32_bf16 v[28:31],v[180:183],v[44:47],v[28:31]
buffer_load_dwordx4 v2,s[32:35],s63 offen offset:3072 sc1 lds
v_mfma_f32_16x16x32_bf16 v[24:27],v[176:179],v[44:47],v[24:27]
ds_read_b128 v[36:39],v4 offset:20480
v_mfma_f32_16x16x32_bf16 v[24:27],v[64:67],v[40:43],v[24:27]
s_waitcnt lgkmcnt(4)
v_mfma_f32_16x16x32_bf16 v[24:27],a[40:43],v[48:51],v[24:27]
ds_read_b128 v[40:43],v4 offset:34816
v_mfma_f32_16x16x32_bf16 v[24:27],a[152:155],v[52:55],v[24:27]
buffer_load_dwordx4 v[12:15],v2,s[32:35],s71 offen sc1
s_add_u32 s71,s71,1187840
v_mfma_f32_16x16x32_bf16 v[28:31],a[156:159],v[52:55],v[28:31]
ds_read_b128 v[44:47],v4 offset:49152
s_add_u32 s63,s63,4096
v_mfma_f32_16x16x32_bf16 v[28:31],a[44:47],v[48:51],v[28:31]
s_add_u32 m0,s64,4096
s_waitcnt lgkmcnt(4)
v_mfma_f32_16x16x32_bf16 v[28:31],v[76:79],v[56:59],v[28:31]
ds_read_b128 v[48:51],v4 offset:7168
v_mfma_f32_16x16x32_bf16 v[28:31],v[188:191],v[60:63],v[28:31]
buffer_load_dwordx4 v2,s[32:35],s63 offen offset:0 sc1 lds
v_mfma_f32_16x16x32_bf16 v[24:27],v[184:187],v[60:63],v[24:27]
ds_read_b128 v[52:55],v4 offset:21504
v_mfma_f32_16x16x32_bf16 v[24:27],v[72:75],v[56:59],v[24:27]
s_waitcnt lgkmcnt(4)
v_mfma_f32_16x16x32_bf16 v[24:27],a[48:51],v[32:35],v[24:27]
ds_read_b128 v[56:59],v4 offset:35840
v_mfma_f32_16x16x32_bf16 v[24:27],a[160:163],v[36:39],v[24:27]
buffer_load_dwordx4 v2,s[32:35],s63 offen offset:1024 sc1 lds
v_mfma_f32_16x16x32_bf16 v[28:31],a[164:167],v[36:39],v[28:31]
ds_read_b128 v[60:63],v4 offset:50176
v_mfma_f32_16x16x32_bf16 v[28:31],a[52:55],v[32:35],v[28:31]
s_waitcnt lgkmcnt(4)
v_mfma_f32_16x16x32_bf16 v[28:31],v[84:87],v[40:43],v[28:31]
ds_read_b128 v[32:35],v4 offset:8192
v_mfma_f32_16x16x32_bf16 v[28:31],v[196:199],v[44:47],v[28:31]
buffer_load_dwordx4 v2,s[32:35],s63 offen offset:2048 sc1 lds
v_mfma_f32_16x16x32_bf16 v[24:27],v[192:195],v[44:47],v[24:27]
ds_read_b128 v[36:39],v4 offset:22528
v_mfma_f32_16x16x32_bf16 v[24:27],v[80:83],v[40:43],v[24:27]
s_waitcnt lgkmcnt(4)
v_mfma_f32_16x16x32_bf16 v[24:27],a[56:59],v[48:51],v[24:27]
ds_read_b128 v[40:43],v4 offset:36864
v_mfma_f32_16x16x32_bf16 v[24:27],a[168:171],v[52:55],v[24:27]
buffer_load_dwordx4 v2,s[32:35],s63 offen offset:3072 sc1 lds
v_mfma_f32_16x16x32_bf16 v[28:31],a[172:175],v[52:55],v[28:31]
ds_read_b128 v[44:47],v4 offset:51200
v_mfma_f32_16x16x32_bf16 v[28:31],a[60:63],v[48:51],v[28:31]
s_waitcnt lgkmcnt(4)
v_mfma_f32_16x16x32_bf16 v[28:31],v[92:95],v[56:59],v[28:31]
ds_read_b128 v[48:51],v4 offset:9216
v_mfma_f32_16x16x32_bf16 v[28:31],v[204:207],v[60:63],v[28:31]
s_add_u32 s63,s63,4096
s_add_u32 m0,s64,8192
v_mfma_f32_16x16x32_bf16 v[24:27],v[200:203],v[60:63],v[24:27]
ds_read_b128 v[52:55],v4 offset:23552
v_mfma_f32_16x16x32_bf16 v[24:27],v[88:91],v[56:59],v[24:27]
s_waitcnt lgkmcnt(4)
v_mfma_f32_16x16x32_bf16 v[24:27],a[64:67],v[32:35],v[24:27]
ds_read_b128 v[56:59],v4 offset:37888
v_mfma_f32_16x16x32_bf16 v[24:27],a[176:179],v[36:39],v[24:27]
)#"
R"#(
buffer_load_dwordx4 v2,s[32:35],s63 offen offset:0 sc1 lds
v_mfma_f32_16x16x32_bf16 v[28:31],a[180:183],v[36:39],v[28:31]
ds_read_b128 v[60:63],v4 offset:52224
v_mfma_f32_16x16x32_bf16 v[28:31],a[68:71],v[32:35],v[28:31]
s_waitcnt lgkmcnt(4)
v_mfma_f32_16x16x32_bf16 v[28:31],v[100:103],v[40:43],v[28:31]
ds_read_b128 v[32:35],v4 offset:10240
v_mfma_f32_16x16x32_bf16 v[28:31],v[212:215],v[44:47],v[28:31]
buffer_load_dwordx4 v2,s[32:35],s63 offen offset:1024 sc1 lds
v_mfma_f32_16x16x32_bf16 v[24:27],v[208:211],v[44:47],v[24:27]
ds_read_b128 v[36:39],v4 offset:24576
v_mfma_f32_16x16x32_bf16 v[24:27],v[96:99],v[40:43],v[24:27]
s_waitcnt lgkmcnt(4)
v_mfma_f32_16x16x32_bf16 v[24:27],a[72:75],v[48:51],v[24:27]
ds_read_b128 v[40:43],v4 offset:38912
v_mfma_f32_16x16x32_bf16 v[24:27],a[184:187],v[52:55],v[24:27]
buffer_load_dwordx4 v2,s[32:35],s63 offen offset:2048 sc1 lds
v_mfma_f32_16x16x32_bf16 v[28:31],a[188:191],v[52:55],v[28:31]
ds_read_b128 v[44:47],v4 offset:53248
v_mfma_f32_16x16x32_bf16 v[28:31],a[76:79],v[48:51],v[28:31]
s_waitcnt lgkmcnt(4)
v_mfma_f32_16x16x32_bf16 v[28:31],v[108:111],v[56:59],v[28:31]
ds_read_b128 v[48:51],v4 offset:11264
v_mfma_f32_16x16x32_bf16 v[28:31],v[220:223],v[60:63],v[28:31]
v_mfma_f32_16x16x32_bf16 v[24:27],v[216:219],v[60:63],v[24:27]
ds_read_b128 v[52:55],v4 offset:25600
v_mfma_f32_16x16x32_bf16 v[24:27],v[104:107],v[56:59],v[24:27]
s_waitcnt lgkmcnt(4)
v_mfma_f32_16x16x32_bf16 v[24:27],a[80:83],v[32:35],v[24:27]
ds_read_b128 v[56:59],v4 offset:39936
v_mfma_f32_16x16x32_bf16 v[24:27],a[192:195],v[36:39],v[24:27]
buffer_load_dwordx4 v2,s[32:35],s63 offen offset:3072 sc1 lds
s_add_u32 s63,s63,4096
v_mfma_f32_16x16x32_bf16 v[28:31],a[196:199],v[36:39],v[28:31]
ds_read_b128 v[60:63],v4 offset:54272
s_add_u32 m0,s64,12288
v_mfma_f32_16x16x32_bf16 v[28:31],a[84:87],v[32:35],v[28:31]
s_waitcnt lgkmcnt(4)
v_mfma_f32_16x16x32_bf16 v[28:31],v[116:119],v[40:43],v[28:31]
ds_read_b128 v[32:35],v4 offset:12288
v_mfma_f32_16x16x32_bf16 v[28:31],v[228:231],v[44:47],v[28:31]
buffer_load_dwordx4 v2,s[32:35],s63 offen offset:0 sc1 lds
v_mfma_f32_16x16x32_bf16 v[24:27],v[224:227],v[44:47],v[24:27]
ds_read_b128 v[36:39],v4 offset:26624
v_mfma_f32_16x16x32_bf16 v[24:27],v[112:115],v[40:43],v[24:27]
s_waitcnt lgkmcnt(4)
v_mfma_f32_16x16x32_bf16 v[24:27],a[88:91],v[48:51],v[24:27]
ds_read_b128 v[40:43],v4 offset:40960
v_mfma_f32_16x16x32_bf16 v[24:27],a[200:203],v[52:55],v[24:27]
buffer_load_dwordx4 v2,s[32:35],s63 offen offset:1024 sc1 lds
s_add_u32 s63,s63,1175552
v_mfma_f32_16x16x32_bf16 v[28:31],a[204:207],v[52:55],v[28:31]
ds_read_b128 v[44:47],v4 offset:55296
v_mfma_f32_16x16x32_bf16 v[28:31],a[92:95],v[48:51],v[28:31]
s_waitcnt lgkmcnt(4)
v_mfma_f32_16x16x32_bf16 v[28:31],v[124:127],v[56:59],v[28:31]
ds_read_b128 v[48:51],v4 offset:13312
s_waitcnt vmcnt(10)
v_mfma_f32_16x16x32_bf16 v[28:31],v[236:239],v[60:63],v[28:31]
v_lshlrev_b32 v16,16,v12
v_and_b32 v17,0xffff0000,v12
v_mfma_f32_16x16x32_bf16 v[24:27],v[232:235],v[60:63],v[24:27]
ds_read_b128 v[52:55],v4 offset:27648
v_lshlrev_b32 v18,16,v13
v_mfma_f32_16x16x32_bf16 v[24:27],v[120:123],v[56:59],v[24:27]
v_and_b32 v19,0xffff0000,v13
s_waitcnt lgkmcnt(4)
v_mfma_f32_16x16x32_bf16 v[24:27],a[96:99],v[32:35],v[24:27]
ds_read_b128 v[56:59],v4 offset:41984
v_lshlrev_b32 v20,16,v14
v_mfma_f32_16x16x32_bf16 v[24:27],a[208:211],v[36:39],v[24:27]
s_barrier
v_mfma_f32_16x16x32_bf16 v[28:31],a[212:215],v[36:39],v[28:31]
ds_read_b128 v[60:63],v4 offset:56320
v_and_b32 v21,0xffff0000,v14
v_mfma_f32_16x16x32_bf16 v[28:31],a[100:103],v[32:35],v[28:31]
v_lshlrev_b32 v22,16,v15
s_waitcnt lgkmcnt(4)
v_mfma_f32_16x16x32_bf16 v[28:31],v[132:135],v[40:43],v[28:31]
ds_read_b128 v[32:35],v2 offset:0
v_and_b32 v23,0xffff0000,v15
v_mfma_f32_16x16x32_bf16 v[28:31],v[244:247],v[44:47],v[28:31]
v_mfma_f32_16x16x32_bf16 v[24:27],v[240:243],v[44:47],v[24:27]
ds_read_b128 v[36:39],v2 offset:14336
v_mfma_f32_16x16x32_bf16 v[24:27],v[128:131],v[40:43],v[24:27]
s_waitcnt lgkmcnt(4)
v_mfma_f32_16x16x32_bf16 v[24:27],a[104:107],v[48:51],v[24:27]
ds_read_b128 v[40:43],v2 offset:28672
v_mfma_f32_16x16x32_bf16 v[24:27],a[216:219],v[52:55],v[24:27]
v_mfma_f32_16x16x32_bf16 v[28:31],a[220:223],v[52:55],v[28:31]
ds_read_b128 v[44:47],v2 offset:43008
v_mfma_f32_16x16x32_bf16 v[28:31],a[108:111],v[48:51],v[28:31]
s_waitcnt lgkmcnt(4)
v_mfma_f32_16x16x32_bf16 v[28:31],v[140:143],v[56:59],v[28:31]
ds_read_b128 v[48:51],v2 offset:1024
v_mfma_f32_16x16x32_bf16 v[28:31],v[252:255],v[60:63],v[28:31]
v_mfma_f32_16x16x32_bf16 v[24:27],v[248:251],v[60:63],v[24:27]
ds_read_b128 v[52:55],v2 offset:15360
s_sub_u32 s62,s62,1
v_mfma_f32_16x16x32_bf16 v[24:27],v[136:139],v[56:59],v[24:27]
s_cbranch_scc1 .L_dmt_2
s_waitcnt lgkmcnt(4)
v_mfma_f32_16x16x32_bf16 v[16:19],a[0:3],v[32:35],v[16:19]
ds_read_b128 v[56:59],v2 offset:29696
v_mfma_f32_16x16x32_bf16 v[16:19],a[112:115],v[36:39],v[16:19]
v_mfma_f32_16x16x32_bf16 v[20:23],a[116:119],v[36:39],v[20:23]
ds_read_b128 v[60:63],v2 offset:44032
v_mfma_f32_16x16x32_bf16 v[20:23],a[4:7],v[32:35],v[20:23]
v_cvt_pk_bf16_f32 v15,v30,v31
s_waitcnt lgkmcnt(4)
v_mfma_f32_16x16x32_bf16 v[20:23],a[228:231],v[40:43],v[20:23]
ds_read_b128 v[32:35],v2 offset:2048
v_cvt_pk_bf16_f32 v14,v28,v29
v_mfma_f32_16x16x32_bf16 v[20:23],v[148:151],v[44:47],v[20:23]
v_cvt_pk_bf16_f32 v13,v26,v27
v_cvt_pk_bf16_f32 v12,v24,v25
v_mfma_f32_16x16x32_bf16 v[16:19],v[144:147],v[44:47],v[16:19]
ds_read_b128 v[36:39],v2 offset:16384
ds_write_b128 v5,v[12:15]
v_mfma_f32_16x16x32_bf16 v[16:19],a[224:227],v[40:43],v[16:19]
ds_read2st64_b32 v[12:13],v3 offset0:0 offset1:1
s_waitcnt lgkmcnt(6)
v_mfma_f32_16x16x32_bf16 v[16:19],a[8:11],v[48:51],v[16:19]
ds_read_b128 v[40:43],v2 offset:30720
ds_read2st64_b32 v[14:15],v3 offset0:2 offset1:3
v_mfma_f32_16x16x32_bf16 v[16:19],a[120:123],v[52:55],v[16:19]
v_mfma_f32_16x16x32_bf16 v[20:23],a[124:127],v[52:55],v[20:23]
ds_read_b128 v[44:47],v2 offset:45056
v_mfma_f32_16x16x32_bf16 v[20:23],a[12:15],v[48:51],v[20:23]
s_waitcnt lgkmcnt(7)
v_mfma_f32_16x16x32_bf16 v[20:23],a[236:239],v[56:59],v[20:23]
ds_read_b128 v[48:51],v2 offset:3072
v_mfma_f32_16x16x32_bf16 v[20:23],v[156:159],v[60:63],v[20:23]
s_waitcnt lgkmcnt(2)
buffer_atomic_pk_add_bf16 v12,v1,s[44:47],s65 offen offset:0 sc1
v_mfma_f32_16x16x32_bf16 v[16:19],v[152:155],v[60:63],v[16:19]
ds_read_b128 v[52:55],v2 offset:17408
v_mfma_f32_16x16x32_bf16 v[16:19],a[232:235],v[56:59],v[16:19]
buffer_atomic_pk_add_bf16 v13,v1,s[44:47],s65 offen offset:2048 sc1
v_mfma_f32_16x16x32_bf16 v[16:19],a[16:19],v[32:35],v[16:19]
ds_read_b128 v[56:59],v2 offset:31744
s_add_u32 s65,s65,16384
v_mfma_f32_16x16x32_bf16 v[16:19],a[128:131],v[36:39],v[16:19]
s_waitcnt vmcnt(2)
s_barrier
v_mfma_f32_16x16x32_bf16 v[20:23],a[132:135],v[36:39],v[20:23]
ds_read_b128 v[60:63],v2 offset:46080
v_mfma_f32_16x16x32_bf16 v[20:23],a[20:23],v[32:35],v[20:23]
buffer_atomic_pk_add_bf16 v14,v1,s[44:47],s65 offen offset:0 sc1
s_waitcnt lgkmcnt(4)
v_mfma_f32_16x16x32_bf16 v[20:23],a[244:247],v[40:43],v[20:23]
ds_read_b128 v[32:35],v2 offset:4096
s_add_u32 m0,s64,57344
v_mfma_f32_16x16x32_bf16 v[20:23],v[164:167],v[44:47],v[20:23]
buffer_atomic_pk_add_bf16 v15,v1,s[44:47],s65 offen offset:2048 sc1
s_add_u32 s65,s65,16384
v_mfma_f32_16x16x32_bf16 v[16:19],v[160:163],v[44:47],v[16:19]
ds_read_b128 v[36:39],v2 offset:18432
v_mfma_f32_16x16x32_bf16 v[16:19],a[240:243],v[40:43],v[16:19]
s_waitcnt lgkmcnt(4)
v_mfma_f32_16x16x32_bf16 v[16:19],a[24:27],v[48:51],v[16:19]
ds_read_b128 v[40:43],v2 offset:32768
v_mfma_f32_16x16x32_bf16 v[16:19],a[136:139],v[52:55],v[16:19]
buffer_load_dwordx4 v2,s[32:35],s63 offen offset:0 sc1 lds
v_mfma_f32_16x16x32_bf16 v[20:23],a[140:143],v[52:55],v[20:23]
ds_read_b128 v[44:47],v2 offset:47104
v_mfma_f32_16x16x32_bf16 v[20:23],a[28:31],v[48:51],v[20:23]
s_waitcnt lgkmcnt(4)
v_mfma_f32_16x16x32_bf16 v[20:23],a[252:255],v[56:59],v[20:23]
ds_read_b128 v[48:51],v2 offset:5120
v_mfma_f32_16x16x32_bf16 v[20:23],v[172:175],v[60:63],v[20:23]
buffer_load_dwordx4 v2,s[32:35],s63 offen offset:1024 sc1 lds
v_mfma_f32_16x16x32_bf16 v[16:19],v[168:171],v[60:63],v[16:19]
ds_read_b128 v[52:55],v2 offset:19456
v_mfma_f32_16x16x32_bf16 v[16:19],a[248:251],v[56:59],v[16:19]
s_waitcnt lgkmcnt(4)
v_mfma_f32_16x16x32_bf16 v[16:19],a[32:35],v[32:35],v[16:19]
ds_read_b128 v[56:59],v2 offset:33792
v_mfma_f32_16x16x32_bf16 v[16:19],a[144:147],v[36:39],v[16:19]
buffer_load_dwordx4 v2,s[32:35],s63 offen offset:2048 sc1 lds
v_mfma_f32_16x16x32_bf16 v[20:23],a[148:151],v[36:39],v[20:23]
ds_read_b128 v[60:63],v2 offset:48128
v_mfma_f32_16x16x32_bf16 v[20:23],a[36:39],v[32:35],v[20:23]
s_waitcnt lgkmcnt(4)
v_mfma_f32_16x16x32_bf16 v[20:23],v[68:71],v[40:43],v[20:23]
ds_read_b128 v[32:35],v2 offset:6144
v_mfma_f32_16x16x32_bf16 v[20:23],v[180:183],v[44:47],v[20:23]
buffer_load_dwordx4 v2,s[32:35],s63 offen offset:3072 sc1 lds
v_mfma_f32_16x16x32_bf16 v[16:19],v[176:179],v[44:47],v[16:19]
ds_read_b128 v[36:39],v2 offset:20480
v_mfma_f32_16x16x32_bf16 v[16:19],v[64:67],v[40:43],v[16:19]
s_waitcnt lgkmcnt(4)
v_mfma_f32_16x16x32_bf16 v[16:19],a[40:43],v[48:51],v[16:19]
ds_read_b128 v[40:43],v2 offset:34816
v_mfma_f32_16x16x32_bf16 v[16:19],a[152:155],v[52:55],v[16:19]
buffer_load_dwordx4 v[12:15],v2,s[32:35],s71 offen sc1
s_add_u32 s71,s71,1187840
v_mfma_f32_16x16x32_bf16 v[20:23],a[156:159],v[52:55],v[20:23]
ds_read_b128 v[44:47],v2 offset:49152
s_add_u32 s63,s63,4096
v_mfma_f32_16x16x32_bf16 v[20:23],a[44:47],v[48:51],v[20:23]
s_add_u32 m0,s64,61440
s_waitcnt lgkmcnt(4)
v_mfma_f32_16x16x32_bf16 v[20:23],v[76:79],v[56:59],v[20:23]
ds_read_b128 v[48:51],v2 offset:7168
v_mfma_f32_16x16x32_bf16 v[20:23],v[188:191],v[60:63],v[20:23]
buffer_load_dwordx4 v2,s[32:35],s63 offen offset:0 sc1 lds
v_mfma_f32_16x16x32_bf16 v[16:19],v[184:187],v[60:63],v[16:19]
ds_read_b128 v[52:55],v2 offset:21504
v_mfma_f32_16x16x32_bf16 v[16:19],v[72:75],v[56:59],v[16:19]
s_waitcnt lgkmcnt(4)
v_mfma_f32_16x16x32_bf16 v[16:19],a[48:51],v[32:35],v[16:19]
ds_read_b128 v[56:59],v2 offset:35840
v_mfma_f32_16x16x32_bf16 v[16:19],a[160:163],v[36:39],v[16:19]
buffer_load_dwordx4 v2,s[32:35],s63 offen offset:1024 sc1 lds
v_mfma_f32_16x16x32_bf16 v[20:23],a[164:167],v[36:39],v[20:23]
ds_read_b128 v[60:63],v2 offset:50176
v_mfma_f32_16x16x32_bf16 v[20:23],a[52:55],v[32:35],v[20:23]
s_waitcnt lgkmcnt(4)
v_mfma_f32_16x16x32_bf16 v[20:23],v[84:87],v[40:43],v[20:23]
ds_read_b128 v[32:35],v2 offset:8192
v_mfma_f32_16x16x32_bf16 v[20:23],v[196:199],v[44:47],v[20:23]
buffer_load_dwordx4 v2,s[32:35],s63 offen offset:2048 sc1 lds
v_mfma_f32_16x16x32_bf16 v[16:19],v[192:195],v[44:47],v[16:19]
ds_read_b128 v[36:39],v2 offset:22528
v_mfma_f32_16x16x32_bf16 v[16:19],v[80:83],v[40:43],v[16:19]
s_waitcnt lgkmcnt(4)
v_mfma_f32_16x16x32_bf16 v[16:19],a[56:59],v[48:51],v[16:19]
ds_read_b128 v[40:43],v2 offset:36864
v_mfma_f32_16x16x32_bf16 v[16:19],a[168:171],v[52:55],v[16:19]
buffer_load_dwordx4 v2,s[32:35],s63 offen offset:3072 sc1 lds
v_mfma_f32_16x16x32_bf16 v[20:23],a[172:175],v[52:55],v[20:23]
ds_read_b128 v[44:47],v2 offset:51200
v_mfma_f32_16x16x32_bf16 v[20:23],a[60:63],v[48:51],v[20:23]
s_waitcnt lgkmcnt(4)
v_mfma_f32_16x16x32_bf16 v[20:23],v[92:95],v[56:59],v[20:23]
ds_read_b128 v[48:51],v2 offset:9216
v_mfma_f32_16x16x32_bf16 v[20:23],v[204:207],v[60:63],v[20:23]
s_add_u32 s63,s63,4096
s_add_u32 m0,s64,65536
v_mfma_f32_16x16x32_bf16 v[16:19],v[200:203],v[60:63],v[16:19]
ds_read_b128 v[52:55],v2 offset:23552
v_mfma_f32_16x16x32_bf16 v[16:19],v[88:91],v[56:59],v[16:19]
s_waitcnt lgkmcnt(4)
v_mfma_f32_16x16x32_bf16 v[16:19],a[64:67],v[32:35],v[16:19]
ds_read_b128 v[56:59],v2 offset:37888
v_mfma_f32_16x16x32_bf16 v[16:19],a[176:179],v[36:39],v[16:19]
buffer_load_dwordx4 v2,s[32:35],s63 offen offset:0 sc1 lds
v_mfma_f32_16x16x32_bf16 v[20:23],a[180:183],v[36:39],v[20:23]
ds_read_b128 v[60:63],v2 offset:52224
v_mfma_f32_16x16x32_bf16 v[20:23],a[68:71],v[32:35],v[20:23]
s_waitcnt lgkmcnt(4)
v_mfma_f32_16x16x32_bf16 v[20:23],v[100:103],v[40:43],v[20:23]
ds_read_b128 v[32:35],v2 offset:10240
v_mfma_f32_16x16x32_bf16 v[20:23],v[212:215],v[44:47],v[20:23]
buffer_load_dwordx4 v2,s[32:35],s63 offen offset:1024 sc1 lds
v_mfma_f32_16x16x32_bf16 v[16:19],v[208:211],v[44:47],v[16:19]
ds_read_b128 v[36:39],v2 offset:24576
v_mfma_f32_16x16x32_bf16 v[16:19],v[96:99],v[40:43],v[16:19]
s_waitcnt lgkmcnt(4)
v_mfma_f32_16x16x32_bf16 v[16:19],a[72:75],v[48:51],v[16:19]
ds_read_b128 v[40:43],v2 offset:38912
v_mfma_f32_16x16x32_bf16 v[16:19],a[184:187],v[52:55],v[16:19]
buffer_load_dwordx4 v2,s[32:35],s63 offen offset:2048 sc1 lds
v_mfma_f32_16x16x32_bf16 v[20:23],a[188:191],v[52:55],v[20:23]
ds_read_b128 v[44:47],v2 offset:53248
v_mfma_f32_16x16x32_bf16 v[20:23],a[76:79],v[48:51],v[20:23]
s_waitcnt lgkmcnt(4)
v_mfma_f32_16x16x32_bf16 v[20:23],v[108:111],v[56:59],v[20:23]
ds_read_b128 v[48:51],v2 offset:11264
v_mfma_f32_16x16x32_bf16 v[20:23],v[220:223],v[60:63],v[20:23]
v_mfma_f32_16x16x32_bf16 v[16:19],v[216:219],v[60:63],v[16:19]
ds_read_b128 v[52:55],v2 offset:25600
v_mfma_f32_16x16x32_bf16 v[16:19],v[104:107],v[56:59],v[16:19]
s_waitcnt lgkmcnt(4)
v_mfma_f32_16x16x32_bf16 v[16:19],a[80:83],v[32:35],v[16:19]
ds_read_b128 v[56:59],v2 offset:39936
v_mfma_f32_16x16x32_bf16 v[16:19],a[192:195],v[36:39],v[16:19]
buffer_load_dwordx4 v2,s[32:35],s63 offen offset:3072 sc1 lds
s_add_u32 s63,s63,4096
v_mfma_f32_16x16x32_bf16 v[20:23],a[196:199],v[36:39],v[20:23]
ds_read_b128 v[60:63],v2 offset:54272
s_add_u32 m0,s64,69632
v_mfma_f32_16x16x32_bf16 v[20:23],a[84:87],v[32:35],v[20:23]
s_waitcnt lgkmcnt(4)
v_mfma_f32_16x16x32_bf16 v[20:23],v[116:119],v[40:43],v[20:23]
ds_read_b128 v[32:35],v2 offset:12288
v_mfma_f32_16x16x32_bf16 v[20:23],v[228:231],v[44:47],v[20:23]
buffer_load_dwordx4 v2,s[32:35],s63 offen offset:0 sc1 lds
v_mfma_f32_16x16x32_bf16 v[16:19],v[224:227],v[44:47],v[16:19]
ds_read_b128 v[36:39],v2 offset:26624
v_mfma_f32_16x16x32_bf16 v[16:19],v[112:115],v[40:43],v[16:19]
s_waitcnt lgkmcnt(4)
v_mfma_f32_16x16x32_bf16 v[16:19],a[88:91],v[48:51],v[16:19]
ds_read_b128 v[40:43],v2 offset:40960
v_mfma_f32_16x16x32_bf16 v[16:19],a[200:203],v[52:55],v[16:19]
buffer_load_dwordx4 v2,s[32:35],s63 offen offset:1024 sc1 lds
s_add_u32 s63,s63,1175552
v_mfma_f32_16x16x32_bf16 v[20:23],a[204:207],v[52:55],v[20:23]
ds_read_b128 v[44:47],v2 offset:55296
v_mfma_f32_16x16x32_bf16 v[20:23],a[92:95],v[48:51],v[20:23]
s_waitcnt lgkmcnt(4)
v_mfma_f32_16x16x32_bf16 v[20:23],v[124:127],v[56:59],v[20:23]
ds_read_b128 v[48:51],v2 offset:13312
s_waitcnt vmcnt(10)
v_mfma_f32_16x16x32_bf16 v[20:23],v[236:239],v[60:63],v[20:23]
v_lshlrev_b32 v24,16,v12
v_and_b32 v25,0xffff0000,v12
v_mfma_f32_16x16x32_bf16 v[16:19],v[232:235],v[60:63],v[16:19]
ds_read_b128 v[52:55],v2 offset:27648
v_lshlrev_b32 v26,16,v13
v_mfma_f32_16x16x32_bf16 v[16:19],v[120:123],v[56:59],v[16:19]
v_and_b32 v27,0xffff0000,v13
s_waitcnt lgkmcnt(4)
v_mfma_f32_16x16x32_bf16 v[16:19],a[96:99],v[32:35],v[16:19]
ds_read_b128 v[56:59],v2 offset:41984
v_lshlrev_b32 v28,16,v14
v_mfma_f32_16x16x32_bf16 v[16:19],a[208:211],v[36:39],v[16:19]
s_barrier
v_mfma_f32_16x16x32_bf16 v[20:23],a[212:215],v[36:39],v[20:23]
ds_read_b128 v[60:63],v2 offset:56320
v_and_b32 v29,0xffff0000,v14
v_mfma_f32_16x16x32_bf16 v[20:23],a[100:103],v[32:35],v[20:23]
v_lshlrev_b32 v30,16,v15
s_waitcnt lgkmcnt(4)
v_mfma_f32_16x16x32_bf16 v[20:23],v[132:135],v[40:43],v[20:23]
ds_read_b128 v[32:35],v4 offset:0
v_and_b32 v31,0xffff0000,v15
v_mfma_f32_16x16x32_bf16 v[20:23],v[244:247],v[44:47],v[20:23]
v_mfma_f32_16x16x32_bf16 v[16:19],v[240:243],v[44:47],v[16:19]
ds_read_b128 v[36:39],v4 offset:14336
v_mfma_f32_16x16x32_bf16 v[16:19],v[128:131],v[40:43],v[16:19]
s_waitcnt lgkmcnt(4)
v_mfma_f32_16x16x32_bf16 v[16:19],a[104:107],v[48:51],v[16:19]
ds_read_b128 v[40:43],v4 offset:28672
v_mfma_f32_16x16x32_bf16 v[16:19],a[216:219],v[52:55],v[16:19]
v_mfma_f32_16x16x32_bf16 v[20:23],a[220:223],v[52:55],v[20:23]
ds_read_b128 v[44:47],v4 offset:43008
v_mfma_f32_16x16x32_bf16 v[20:23],a[108:111],v[48:51],v[20:23]
s_waitcnt lgkmcnt(4)
v_mfma_f32_16x16x32_bf16 v[20:23],v[140:143],v[56:59],v[20:23]
ds_read_b128 v[48:51],v4 offset:1024
v_mfma_f32_16x16x32_bf16 v[20:23],v[252:255],v[60:63],v[20:23]
v_mfma_f32_16x16x32_bf16 v[16:19],v[248:251],v[60:63],v[16:19]
ds_read_b128 v[52:55],v4 offset:15360
v_mfma_f32_16x16x32_bf16 v[16:19],v[136:139],v[56:59],v[16:19]
s_branch .L_dml_2
.L_dmt_2:
s_nop 1
s_nop 0
v_cvt_pk_bf16_f32 v15,v30,v31
v_cvt_pk_bf16_f32 v14,v28,v29
s_nop 1
v_cvt_pk_bf16_f32 v13,v26,v27
v_cvt_pk_bf16_f32 v12,v24,v25
ds_write_b128 v5,v[12:15]
ds_read2st64_b32 v[12:13],v3 offset0:0 offset1:1
ds_read2st64_b32 v[14:15],v3 offset0:2 offset1:3
s_waitcnt lgkmcnt(0)
buffer_atomic_pk_add_bf16 v12,v1,s[44:47],s65 offen offset:0 sc1
buffer_atomic_pk_add_bf16 v13,v1,s[44:47],s65 offen offset:2048 sc1
s_add_u32 s65,s65,16384
buffer_atomic_pk_add_bf16 v14,v1,s[44:47],s65 offen offset:0 sc1
buffer_atomic_pk_add_bf16 v15,v1,s[44:47],s65 offen offset:2048 sc1
s_add_u32 s65,s65,16384
s_branch .L_end
.p2align 6
.L_dlv3:
.L_dll_3:
s_waitcnt lgkmcnt(4)
v_mfma_f32_16x16x32_bf16 v[16:19],a[0:3],v[32:35],v[16:19]
ds_read_b128 v[56:59],v2 offset:29696
v_mfma_f32_16x16x32_bf16 v[16:19],a[112:115],v[36:39],v[16:19]
v_mfma_f32_16x16x32_bf16 v[20:23],a[116:119],v[36:39],v[20:23]
ds_read_b128 v[60:63],v2 offset:44032
v_mfma_f32_16x16x32_bf16 v[20:23],a[4:7],v[32:35],v[20:23]
s_add_u32 s68,s68,1187840
s_waitcnt lgkmcnt(4)
v_mfma_f32_16x16x32_bf16 v[20:23],a[228:231],v[40:43],v[20:23]
ds_read_b128 v[32:35],v2 offset:2048
v_mfma_f32_16x16x32_bf16 v[20:23],v[148:151],v[44:47],v[20:23]
buffer_store_dwordx4 v[28:31],v2,s[32:35],s68 offen offset:1024 sc1
v_mfma_f32_16x16x32_bf16 v[16:19],v[144:147],v[44:47],v[16:19]
ds_read_b128 v[36:39],v2 offset:16384
v_mfma_f32_16x16x32_bf16 v[16:19],a[224:227],v[40:43],v[16:19]
s_waitcnt lgkmcnt(4)
v_mfma_f32_16x16x32_bf16 v[16:19],a[8:11],v[48:51],v[16:19]
ds_read_b128 v[40:43],v2 offset:30720
v_mfma_f32_16x16x32_bf16 v[16:19],a[120:123],v[52:55],v[16:19]
buffer_store_dwordx4 v[24:27],v2,s[32:35],s68 offen sc1
s_mov_b32 s67,s68
v_mfma_f32_16x16x32_bf16 v[20:23],a[124:127],v[52:55],v[20:23]
ds_read_b128 v[44:47],v2 offset:45056
v_mfma_f32_16x16x32_bf16 v[20:23],a[12:15],v[48:51],v[20:23]
s_waitcnt lgkmcnt(4)
v_mfma_f32_16x16x32_bf16 v[20:23],a[236:239],v[56:59],v[20:23]
ds_read_b128 v[48:51],v2 offset:3072
v_mfma_f32_16x16x32_bf16 v[20:23],v[156:159],v[60:63],v[20:23]
v_mfma_f32_16x16x32_bf16 v[16:19],v[152:155],v[60:63],v[16:19]
ds_read_b128 v[52:55],v2 offset:17408
v_mfma_f32_16x16x32_bf16 v[16:19],a[232:235],v[56:59],v[16:19]
s_waitcnt lgkmcnt(4)
v_mfma_f32_16x16x32_bf16 v[16:19],a[16:19],v[32:35],v[16:19]
s_waitcnt vmcnt(2)
s_barrier
v_mfma_f32_16x16x32_bf16 v[16:19],a[128:131],v[36:39],v[16:19]
ds_read_b128 v[56:59],v2 offset:31744
v_mfma_f32_16x16x32_bf16 v[20:23],a[132:135],v[36:39],v[20:23]
ds_read_b128 v[60:63],v2 offset:46080
v_mfma_f32_16x16x32_bf16 v[20:23],a[20:23],v[32:35],v[20:23]
s_waitcnt lgkmcnt(4)
v_mfma_f32_16x16x32_bf16 v[20:23],a[244:247],v[40:43],v[20:23]
ds_read_b128 v[32:35],v2 offset:4096
s_add_u32 m0,s64,57344
v_mfma_f32_16x16x32_bf16 v[20:23],v[164:167],v[44:47],v[20:23]
buffer_load_dwordx4 v2,s[32:35],s63 offen offset:0 sc1 lds
v_mfma_f32_16x16x32_bf16 v[16:19],v[160:163],v[44:47],v[16:19]
ds_read_b128 v[36:39],v2 offset:18432
v_mfma_f32_16x16x32_bf16 v[16:19],a[240:243],v[40:43],v[16:19]
s_waitcnt lgkmcnt(4)
v_mfma_f32_16x16x32_bf16 v[16:19],a[24:27],v[48:51],v[16:19]
ds_read_b128 v[40:43],v2 offset:32768
v_mfma_f32_16x16x32_bf16 v[16:19],a[136:139],v[52:55],v[16:19]
v_mfma_f32_16x16x32_bf16 v[20:23],a[140:143],v[52:55],v[20:23]
ds_read_b128 v[44:47],v2 offset:47104
v_mfma_f32_16x16x32_bf16 v[20:23],a[28:31],v[48:51],v[20:23]
s_waitcnt lgkmcnt(4)
v_mfma_f32_16x16x32_bf16 v[20:23],a[252:255],v[56:59],v[20:23]
ds_read_b128 v[48:51],v2 offset:5120
v_mfma_f32_16x16x32_bf16 v[20:23],v[172:175],v[60:63],v[20:23]
buffer_load_dwordx4 v2,s[32:35],s63 offen offset:1024 sc1 lds
v_mfma_f32_16x16x32_bf16 v[16:19],v[168:171],v[60:63],v[16:19]
ds_read_b128 v[52:55],v2 offset:19456
v_mfma_f32_16x16x32_bf16 v[16:19],a[248:251],v[56:59],v[16:19]
s_waitcnt lgkmcnt(4)
v_mfma_f32_16x16x32_bf16 v[16:19],a[32:35],v[32:35],v[16:19]
ds_read_b128 v[56:59],v2 offset:33792
v_mfma_f32_16x16x32_bf16 v[16:19],a[144:147],v[36:39],v[16:19]
buffer_load_dwordx4 v2,s[32:35],s63 offen offset:2048 sc1 lds
v_mfma_f32_16x16x32_bf16 v[20:23],a[148:151],v[36:39],v[20:23]
ds_read_b128 v[60:63],v2 offset:48128
v_mfma_f32_16x16x32_bf16 v[20:23],a[36:39],v[32:35],v[20:23]
s_waitcnt lgkmcnt(4)
v_mfma_f32_16x16x32_bf16 v[20:23],v[68:71],v[40:43],v[20:23]
ds_read_b128 v[32:35],v2 offset:6144
v_mfma_f32_16x16x32_bf16 v[20:23],v[180:183],v[44:47],v[20:23]
buffer_load_dwordx4 v2,s[32:35],s63 offen offset:3072 sc1 lds
v_mfma_f32_16x16x32_bf16 v[16:19],v[176:179],v[44:47],v[16:19]
ds_read_b128 v[36:39],v2 offset:20480
v_mfma_f32_16x16x32_bf16 v[16:19],v[64:67],v[40:43],v[16:19]
s_waitcnt lgkmcnt(4)
v_mfma_f32_16x16x32_bf16 v[16:19],a[40:43],v[48:51],v[16:19]
ds_read_b128 v[40:43],v2 offset:34816
v_mfma_f32_16x16x32_bf16 v[16:19],a[152:155],v[52:55],v[16:19]
buffer_load_dwordx4 v[12:15],v2,s[32:35],s71 offen sc1
s_add_u32 s71,s71,1187840
v_mfma_f32_16x16x32_bf16 v[20:23],a[156:159],v[52:55],v[20:23]
ds_read_b128 v[44:47],v2 offset:49152
s_add_u32 s63,s63,4096
v_mfma_f32_16x16x32_bf16 v[20:23],a[44:47],v[48:51],v[20:23]
s_add_u32 m0,s64,61440
s_waitcnt lgkmcnt(4)
v_mfma_f32_16x16x32_bf16 v[20:23],v[76:79],v[56:59],v[20:23]
ds_read_b128 v[48:51],v2 offset:7168
v_mfma_f32_16x16x32_bf16 v[20:23],v[188:191],v[60:63],v[20:23]
buffer_load_dwordx4 v2,s[32:35],s63 offen offset:0 sc1 lds
v_mfma_f32_16x16x32_bf16 v[16:19],v[184:187],v[60:63],v[16:19]
ds_read_b128 v[52:55],v2 offset:21504
v_mfma_f32_16x16x32_bf16 v[16:19],v[72:75],v[56:59],v[16:19]
s_waitcnt lgkmcnt(4)
v_mfma_f32_16x16x32_bf16 v[16:19],a[48:51],v[32:35],v[16:19]
ds_read_b128 v[56:59],v2 offset:35840
v_mfma_f32_16x16x32_bf16 v[16:19],a[160:163],v[36:39],v[16:19]
buffer_load_dwordx4 v2,s[32:35],s63 offen offset:1024 sc1 lds
v_mfma_f32_16x16x32_bf16 v[20:23],a[164:167],v[36:39],v[20:23]
ds_read_b128 v[60:63],v2 offset:50176
v_mfma_f32_16x16x32_bf16 v[20:23],a[52:55],v[32:35],v[20:23]
s_waitcnt lgkmcnt(4)
v_mfma_f32_16x16x32_bf16 v[20:23],v[84:87],v[40:43],v[20:23]
ds_read_b128 v[32:35],v2 offset:8192
v_mfma_f32_16x16x32_bf16 v[20:23],v[196:199],v[44:47],v[20:23]
buffer_load_dwordx4 v2,s[32:35],s63 offen offset:2048 sc1 lds
v_mfma_f32_16x16x32_bf16 v[16:19],v[192:195],v[44:47],v[16:19]
ds_read_b128 v[36:39],v2 offset:22528
v_mfma_f32_16x16x32_bf16 v[16:19],v[80:83],v[40:43],v[16:19]
s_waitcnt lgkmcnt(4)
v_mfma_f32_16x16x32_bf16 v[16:19],a[56:59],v[48:51],v[16:19]
ds_read_b128 v[40:43],v2 offset:36864
v_mfma_f32_16x16x32_bf16 v[16:19],a[168:171],v[52:55],v[16:19]
buffer_load_dwordx4 v2,s[32:35],s63 offen offset:3072 sc1 lds
v_mfma_f32_16x16x32_bf16 v[20:23],a[172:175],v[52:55],v[20:23]
ds_read_b128 v[44:47],v2 offset:51200
v_mfma_f32_16x16x32_bf16 v[20:23],a[60:63],v[48:51],v[20:23]
s_waitcnt lgkmcnt(4)
v_mfma_f32_16x16x32_bf16 v[20:23],v[92:95],v[56:59],v[20:23]
ds_read_b128 v[48:51],v2 offset:9216
v_mfma_f32_16x16x32_bf16 v[20:23],v[204:207],v[60:63],v[20:23]
s_add_u32 s63,s63,4096
s_add_u32 m0,s64,65536
v_mfma_f32_16x16x32_bf16 v[16:19],v[200:203],v[60:63],v[16:19]
)#"
R"#(
ds_read_b128 v[52:55],v2 offset:23552
v_mfma_f32_16x16x32_bf16 v[16:19],v[88:91],v[56:59],v[16:19]
s_waitcnt lgkmcnt(4)
v_mfma_f32_16x16x32_bf16 v[16:19],a[64:67],v[32:35],v[16:19]
ds_read_b128 v[56:59],v2 offset:37888
v_mfma_f32_16x16x32_bf16 v[16:19],a[176:179],v[36:39],v[16:19]
buffer_load_dwordx4 v2,s[32:35],s63 offen offset:0 sc1 lds
v_mfma_f32_16x16x32_bf16 v[20:23],a[180:183],v[36:39],v[20:23]
ds_read_b128 v[60:63],v2 offset:52224
v_mfma_f32_16x16x32_bf16 v[20:23],a[68:71],v[32:35],v[20:23]
s_waitcnt lgkmcnt(4)
v_mfma_f32_16x16x32_bf16 v[20:23],v[100:103],v[40:43],v[20:23]
ds_read_b128 v[32:35],v2 offset:10240
v_mfma_f32_16x16x32_bf16 v[20:23],v[212:215],v[44:47],v[20:23]
buffer_load_dwordx4 v2,s[32:35],s63 offen offset:1024 sc1 lds
v_mfma_f32_16x16x32_bf16 v[16:19],v[208:211],v[44:47],v[16:19]
ds_read_b128 v[36:39],v2 offset:24576
v_mfma_f32_16x16x32_bf16 v[16:19],v[96:99],v[40:43],v[16:19]
s_waitcnt lgkmcnt(4)
v_mfma_f32_16x16x32_bf16 v[16:19],a[72:75],v[48:51],v[16:19]
ds_read_b128 v[40:43],v2 offset:38912
v_mfma_f32_16x16x32_bf16 v[16:19],a[184:187],v[52:55],v[16:19]
buffer_load_dwordx4 v2,s[32:35],s63 offen offset:2048 sc1 lds
v_mfma_f32_16x16x32_bf16 v[20:23],a[188:191],v[52:55],v[20:23]
ds_read_b128 v[44:47],v2 offset:53248
v_mfma_f32_16x16x32_bf16 v[20:23],a[76:79],v[48:51],v[20:23]
s_waitcnt lgkmcnt(4)
v_mfma_f32_16x16x32_bf16 v[20:23],v[108:111],v[56:59],v[20:23]
ds_read_b128 v[48:51],v2 offset:11264
v_mfma_f32_16x16x32_bf16 v[20:23],v[220:223],v[60:63],v[20:23]
v_mfma_f32_16x16x32_bf16 v[16:19],v[216:219],v[60:63],v[16:19]
ds_read_b128 v[52:55],v2 offset:25600
v_mfma_f32_16x16x32_bf16 v[16:19],v[104:107],v[56:59],v[16:19]
s_waitcnt lgkmcnt(4)
v_mfma_f32_16x16x32_bf16 v[16:19],a[80:83],v[32:35],v[16:19]
ds_read_b128 v[56:59],v2 offset:39936
v_mfma_f32_16x16x32_bf16 v[16:19],a[192:195],v[36:39],v[16:19]
buffer_load_dwordx4 v2,s[32:35],s63 offen offset:3072 sc1 lds
s_add_u32 s63,s63,4096
v_mfma_f32_16x16x32_bf16 v[20:23],a[196:199],v[36:39],v[20:23]
ds_read_b128 v[60:63],v2 offset:54272
s_add_u32 m0,s64,69632
v_mfma_f32_16x16x32_bf16 v[20:23],a[84:87],v[32:35],v[20:23]
s_waitcnt lgkmcnt(4)
v_mfma_f32_16x16x32_bf16 v[20:23],v[116:119],v[40:43],v[20:23]
ds_read_b128 v[32:35],v2 offset:12288
v_mfma_f32_16x16x32_bf16 v[20:23],v[228:231],v[44:47],v[20:23]
buffer_load_dwordx4 v2,s[32:35],s63 offen offset:0 sc1 lds
v_mfma_f32_16x16x32_bf16 v[16:19],v[224:227],v[44:47],v[16:19]
ds_read_b128 v[36:39],v2 offset:26624
v_mfma_f32_16x16x32_bf16 v[16:19],v[112:115],v[40:43],v[16:19]
s_waitcnt lgkmcnt(4)
v_mfma_f32_16x16x32_bf16 v[16:19],a[88:91],v[48:51],v[16:19]
ds_read_b128 v[40:43],v2 offset:40960
v_mfma_f32_16x16x32_bf16 v[16:19],a[200:203],v[52:55],v[16:19]
buffer_load_dwordx4 v2,s[32:35],s63 offen offset:1024 sc1 lds
s_add_u32 s63,s63,1175552
v_mfma_f32_16x16x32_bf16 v[20:23],a[204:207],v[52:55],v[20:23]
ds_read_b128 v[44:47],v2 offset:55296
v_mfma_f32_16x16x32_bf16 v[20:23],a[92:95],v[48:51],v[20:23]
s_waitcnt lgkmcnt(4)
v_mfma_f32_16x16x32_bf16 v[20:23],v[124:127],v[56:59],v[20:23]
ds_read_b128 v[48:51],v2 offset:13312
s_waitcnt vmcnt(10)
v_mfma_f32_16x16x32_bf16 v[20:23],v[236:239],v[60:63],v[20:23]
v_lshlrev_b32 v24,16,v12
v_and_b32 v25,0xffff0000,v12
v_mfma_f32_16x16x32_bf16 v[16:19],v[232:235],v[60:63],v[16:19]
ds_read_b128 v[52:55],v2 offset:27648
v_lshlrev_b32 v26,16,v13
v_mfma_f32_16x16x32_bf16 v[16:19],v[120:123],v[56:59],v[16:19]
v_and_b32 v27,0xffff0000,v13
s_waitcnt lgkmcnt(4)
v_mfma_f32_16x16x32_bf16 v[16:19],a[96:99],v[32:35],v[16:19]
s_barrier
v_mfma_f32_16x16x32_bf16 v[16:19],a[208:211],v[36:39],v[16:19]
ds_read_b128 v[56:59],v2 offset:41984
v_lshlrev_b32 v28,16,v14
v_mfma_f32_16x16x32_bf16 v[20:23],a[212:215],v[36:39],v[20:23]
ds_read_b128 v[60:63],v2 offset:56320
v_and_b32 v29,0xffff0000,v14
v_mfma_f32_16x16x32_bf16 v[20:23],a[100:103],v[32:35],v[20:23]
v_lshlrev_b32 v30,16,v15
s_waitcnt lgkmcnt(4)
v_mfma_f32_16x16x32_bf16 v[20:23],v[132:135],v[40:43],v[20:23]
ds_read_b128 v[32:35],v4 offset:0
v_and_b32 v31,0xffff0000,v15
v_mfma_f32_16x16x32_bf16 v[20:23],v[244:247],v[44:47],v[20:23]
v_mfma_f32_16x16x32_bf16 v[16:19],v[240:243],v[44:47],v[16:19]
ds_read_b128 v[36:39],v4 offset:14336
v_mfma_f32_16x16x32_bf16 v[16:19],v[128:131],v[40:43],v[16:19]
s_waitcnt lgkmcnt(4)
v_mfma_f32_16x16x32_bf16 v[16:19],a[104:107],v[48:51],v[16:19]
ds_read_b128 v[40:43],v4 offset:28672
v_mfma_f32_16x16x32_bf16 v[16:19],a[216:219],v[52:55],v[16:19]
v_mfma_f32_16x16x32_bf16 v[20:23],a[220:223],v[52:55],v[20:23]
ds_read_b128 v[44:47],v4 offset:43008
v_mfma_f32_16x16x32_bf16 v[20:23],a[108:111],v[48:51],v[20:23]
s_waitcnt lgkmcnt(4)
v_mfma_f32_16x16x32_bf16 v[20:23],v[140:143],v[56:59],v[20:23]
ds_read_b128 v[48:51],v4 offset:1024
v_mfma_f32_16x16x32_bf16 v[20:23],v[252:255],v[60:63],v[20:23]
v_mfma_f32_16x16x32_bf16 v[16:19],v[248:251],v[60:63],v[16:19]
ds_read_b128 v[52:55],v4 offset:15360
s_sub_u32 s62,s62,1
v_mfma_f32_16x16x32_bf16 v[16:19],v[136:139],v[56:59],v[16:19]
s_cbranch_scc1 .L_dlb_3
s_waitcnt lgkmcnt(4)
v_mfma_f32_16x16x32_bf16 v[24:27],a[0:3],v[32:35],v[24:27]
ds_read_b128 v[56:59],v4 offset:29696
v_mfma_f32_16x16x32_bf16 v[24:27],a[112:115],v[36:39],v[24:27]
v_mfma_f32_16x16x32_bf16 v[28:31],a[116:119],v[36:39],v[28:31]
ds_read_b128 v[60:63],v4 offset:44032
v_mfma_f32_16x16x32_bf16 v[28:31],a[4:7],v[32:35],v[28:31]
s_add_u32 s68,s68,1187840
s_waitcnt lgkmcnt(4)
v_mfma_f32_16x16x32_bf16 v[28:31],a[228:231],v[40:43],v[28:31]
ds_read_b128 v[32:35],v4 offset:2048
v_mfma_f32_16x16x32_bf16 v[28:31],v[148:151],v[44:47],v[28:31]
buffer_store_dwordx4 v[20:23],v2,s[32:35],s68 offen offset:1024 sc1
v_mfma_f32_16x16x32_bf16 v[24:27],v[144:147],v[44:47],v[24:27]
ds_read_b128 v[36:39],v4 offset:16384
v_mfma_f32_16x16x32_bf16 v[24:27],a[224:227],v[40:43],v[24:27]
s_waitcnt lgkmcnt(4)
v_mfma_f32_16x16x32_bf16 v[24:27],a[8:11],v[48:51],v[24:27]
ds_read_b128 v[40:43],v4 offset:30720
v_mfma_f32_16x16x32_bf16 v[24:27],a[120:123],v[52:55],v[24:27]
buffer_store_dwordx4 v[16:19],v2,s[32:35],s68 offen sc1
s_mov_b32 s67,s68
v_mfma_f32_16x16x32_bf16 v[28:31],a[124:127],v[52:55],v[28:31]
ds_read_b128 v[44:47],v4 offset:45056
v_mfma_f32_16x16x32_bf16 v[28:31],a[12:15],v[48:51],v[28:31]
s_waitcnt lgkmcnt(4)
v_mfma_f32_16x16x32_bf16 v[28:31],a[236:239],v[56:59],v[28:31]
ds_read_b128 v[48:51],v4 offset:3072
v_mfma_f32_16x16x32_bf16 v[28:31],v[156:159],v[60:63],v[28:31]
v_mfma_f32_16x16x32_bf16 v[24:27],v[152:155],v[60:63],v[24:27]
ds_read_b128 v[52:55],v4 offset:17408
v_mfma_f32_16x16x32_bf16 v[24:27],a[232:235],v[56:59],v[24:27]
s_waitcnt lgkmcnt(4)
v_mfma_f32_16x16x32_bf16 v[24:27],a[16:19],v[32:35],v[24:27]
s_waitcnt vmcnt(2)
s_barrier
v_mfma_f32_16x16x32_bf16 v[24:27],a[128:131],v[36:39],v[24:27]
ds_read_b128 v[56:59],v4 offset:31744
v_mfma_f32_16x16x32_bf16 v[28:31],a[132:135],v[36:39],v[28:31]
ds_read_b128 v[60:63],v4 offset:46080
v_mfma_f32_16x16x32_bf16 v[28:31],a[20:23],v[32:35],v[28:31]
s_waitcnt lgkmcnt(4)
v_mfma_f32_16x16x32_bf16 v[28:31],a[244:247],v[40:43],v[28:31]
ds_read_b128 v[32:35],v4 offset:4096
s_add_u32 m0,s64,0
v_mfma_f32_16x16x32_bf16 v[28:31],v[164:167],v[44:47],v[28:31]
buffer_load_dwordx4 v2,s[32:35],s63 offen offset:0 sc1 lds
v_mfma_f32_16x16x32_bf16 v[24:27],v[160:163],v[44:47],v[24:27]
ds_read_b128 v[36:39],v4 offset:18432
v_mfma_f32_16x16x32_bf16 v[24:27],a[240:243],v[40:43],v[24:27]
s_waitcnt lgkmcnt(4)
v_mfma_f32_16x16x32_bf16 v[24:27],a[24:27],v[48:51],v[24:27]
ds_read_b128 v[40:43],v4 offset:32768
v_mfma_f32_16x16x32_bf16 v[24:27],a[136:139],v[52:55],v[24:27]
v_mfma_f32_16x16x32_bf16 v[28:31],a[140:143],v[52:55],v[28:31]
ds_read_b128 v[44:47],v4 offset:47104
v_mfma_f32_16x16x32_bf16 v[28:31],a[28:31],v[48:51],v[28:31]
s_waitcnt lgkmcnt(4)
v_mfma_f32_16x16x32_bf16 v[28:31],a[252:255],v[56:59],v[28:31]
ds_read_b128 v[48:51],v4 offset:5120
v_mfma_f32_16x16x32_bf16 v[28:31],v[172:175],v[60:63],v[28:31]
buffer_load_dwordx4 v2,s[32:35],s63 offen offset:1024 sc1 lds
v_mfma_f32_16x16x32_bf16 v[24:27],v[168:171],v[60:63],v[24:27]
ds_read_b128 v[52:55],v4 offset:19456
v_mfma_f32_16x16x32_bf16 v[24:27],a[248:251],v[56:59],v[24:27]
s_waitcnt lgkmcnt(4)
v_mfma_f32_16x16x32_bf16 v[24:27],a[32:35],v[32:35],v[24:27]
ds_read_b128 v[56:59],v4 offset:33792
v_mfma_f32_16x16x32_bf16 v[24:27],a[144:147],v[36:39],v[24:27]
buffer_load_dwordx4 v2,s[32:35],s63 offen offset:2048 sc1 lds
v_mfma_f32_16x16x32_bf16 v[28:31],a[148:151],v[36:39],v[28:31]
ds_read_b128 v[60:63],v4 offset:48128
v_mfma_f32_16x16x32_bf16 v[28:31],a[36:39],v[32:35],v[28:31]
s_waitcnt lgkmcnt(4)
v_mfma_f32_16x16x32_bf16 v[28:31],v[68:71],v[40:43],v[28:31]
ds_read_b128 v[32:35],v4 offset:6144
v_mfma_f32_16x16x32_bf16 v[28:31],v[180:183],v[44:47],v[28:31]
buffer_load_dwordx4 v2,s[32:35],s63 offen offset:3072 sc1 lds
v_mfma_f32_16x16x32_bf16 v[24:27],v[176:179],v[44:47],v[24:27]
ds_read_b128 v[36:39],v4 offset:20480
v_mfma_f32_16x16x32_bf16 v[24:27],v[64:67],v[40:43],v[24:27]
s_waitcnt lgkmcnt(4)
v_mfma_f32_16x16x32_bf16 v[24:27],a[40:43],v[48:51],v[24:27]
ds_read_b128 v[40:43],v4 offset:34816
v_mfma_f32_16x16x32_bf16 v[24:27],a[152:155],v[52:55],v[24:27]
buffer_load_dwordx4 v[12:15],v2,s[32:35],s71 offen sc1
s_add_u32 s71,s71,1187840
v_mfma_f32_16x16x32_bf16 v[28:31],a[156:159],v[52:55],v[28:31]
ds_read_b128 v[44:47],v4 offset:49152
s_add_u32 s63,s63,4096
v_mfma_f32_16x16x32_bf16 v[28:31],a[44:47],v[48:51],v[28:31]
s_add_u32 m0,s64,4096
s_waitcnt lgkmcnt(4)
v_mfma_f32_16x16x32_bf16 v[28:31],v[76:79],v[56:59],v[28:31]
ds_read_b128 v[48:51],v4 offset:7168
v_mfma_f32_16x16x32_bf16 v[28:31],v[188:191],v[60:63],v[28:31]
buffer_load_dwordx4 v2,s[32:35],s63 offen offset:0 sc1 lds
v_mfma_f32_16x16x32_bf16 v[24:27],v[184:187],v[60:63],v[24:27]
ds_read_b128 v[52:55],v4 offset:21504
v_mfma_f32_16x16x32_bf16 v[24:27],v[72:75],v[56:59],v[24:27]
s_waitcnt lgkmcnt(4)
v_mfma_f32_16x16x32_bf16 v[24:27],a[48:51],v[32:35],v[24:27]
ds_read_b128 v[56:59],v4 offset:35840
v_mfma_f32_16x16x32_bf16 v[24:27],a[160:163],v[36:39],v[24:27]
buffer_load_dwordx4 v2,s[32:35],s63 offen offset:1024 sc1 lds
v_mfma_f32_16x16x32_bf16 v[28:31],a[164:167],v[36:39],v[28:31]
ds_read_b128 v[60:63],v4 offset:50176
v_mfma_f32_16x16x32_bf16 v[28:31],a[52:55],v[32:35],v[28:31]
s_waitcnt lgkmcnt(4)
v_mfma_f32_16x16x32_bf16 v[28:31],v[84:87],v[40:43],v[28:31]
ds_read_b128 v[32:35],v4 offset:8192
v_mfma_f32_16x16x32_bf16 v[28:31],v[196:199],v[44:47],v[28:31]
buffer_load_dwordx4 v2,s[32:35],s63 offen offset:2048 sc1 lds
v_mfma_f32_16x16x32_bf16 v[24:27],v[192:195],v[44:47],v[24:27]
ds_read_b128 v[36:39],v4 offset:22528
v_mfma_f32_16x16x32_bf16 v[24:27],v[80:83],v[40:43],v[24:27]
s_waitcnt lgkmcnt(4)
v_mfma_f32_16x16x32_bf16 v[24:27],a[56:59],v[48:51],v[24:27]
ds_read_b128 v[40:43],v4 offset:36864
v_mfma_f32_16x16x32_bf16 v[24:27],a[168:171],v[52:55],v[24:27]
buffer_load_dwordx4 v2,s[32:35],s63 offen offset:3072 sc1 lds
v_mfma_f32_16x16x32_bf16 v[28:31],a[172:175],v[52:55],v[28:31]
ds_read_b128 v[44:47],v4 offset:51200
v_mfma_f32_16x16x32_bf16 v[28:31],a[60:63],v[48:51],v[28:31]
s_waitcnt lgkmcnt(4)
v_mfma_f32_16x16x32_bf16 v[28:31],v[92:95],v[56:59],v[28:31]
ds_read_b128 v[48:51],v4 offset:9216
v_mfma_f32_16x16x32_bf16 v[28:31],v[204:207],v[60:63],v[28:31]
s_add_u32 s63,s63,4096
s_add_u32 m0,s64,8192
v_mfma_f32_16x16x32_bf16 v[24:27],v[200:203],v[60:63],v[24:27]
ds_read_b128 v[52:55],v4 offset:23552
v_mfma_f32_16x16x32_bf16 v[24:27],v[88:91],v[56:59],v[24:27]
s_waitcnt lgkmcnt(4)
v_mfma_f32_16x16x32_bf16 v[24:27],a[64:67],v[32:35],v[24:27]
ds_read_b128 v[56:59],v4 offset:37888
v_mfma_f32_16x16x32_bf16 v[24:27],a[176:179],v[36:39],v[24:27]
buffer_load_dwordx4 v2,s[32:35],s63 offen offset:0 sc1 lds
v_mfma_f32_16x16x32_bf16 v[28:31],a[180:183],v[36:39],v[28:31]
ds_read_b128 v[60:63],v4 offset:52224
v_mfma_f32_16x16x32_bf16 v[28:31],a[68:71],v[32:35],v[28:31]
s_waitcnt lgkmcnt(4)
v_mfma_f32_16x16x32_bf16 v[28:31],v[100:103],v[40:43],v[28:31]
ds_read_b128 v[32:35],v4 offset:10240
v_mfma_f32_16x16x32_bf16 v[28:31],v[212:215],v[44:47],v[28:31]
buffer_load_dwordx4 v2,s[32:35],s63 offen offset:1024 sc1 lds
v_mfma_f32_16x16x32_bf16 v[24:27],v[208:211],v[44:47],v[24:27]
ds_read_b128 v[36:39],v4 offset:24576
v_mfma_f32_16x16x32_bf16 v[24:27],v[96:99],v[40:43],v[24:27]
s_waitcnt lgkmcnt(4)
v_mfma_f32_16x16x32_bf16 v[24:27],a[72:75],v[48:51],v[24:27]
ds_read_b128 v[40:43],v4 offset:38912
v_mfma_f32_16x16x32_bf16 v[24:27],a[184:187],v[52:55],v[24:27]
buffer_load_dwordx4 v2,s[32:35],s63 offen offset:2048 sc1 lds
v_mfma_f32_16x16x32_bf16 v[28:31],a[188:191],v[52:55],v[28:31]
ds_read_b128 v[44:47],v4 offset:53248
v_mfma_f32_16x16x32_bf16 v[28:31],a[76:79],v[48:51],v[28:31]
s_waitcnt lgkmcnt(4)
v_mfma_f32_16x16x32_bf16 v[28:31],v[108:111],v[56:59],v[28:31]
ds_read_b128 v[48:51],v4 offset:11264
v_mfma_f32_16x16x32_bf16 v[28:31],v[220:223],v[60:63],v[28:31]
v_mfma_f32_16x16x32_bf16 v[24:27],v[216:219],v[60:63],v[24:27]
ds_read_b128 v[52:55],v4 offset:25600
v_mfma_f32_16x16x32_bf16 v[24:27],v[104:107],v[56:59],v[24:27]
s_waitcnt lgkmcnt(4)
v_mfma_f32_16x16x32_bf16 v[24:27],a[80:83],v[32:35],v[24:27]
ds_read_b128 v[56:59],v4 offset:39936
v_mfma_f32_16x16x32_bf16 v[24:27],a[192:195],v[36:39],v[24:27]
buffer_load_dwordx4 v2,s[32:35],s63 offen offset:3072 sc1 lds
s_add_u32 s63,s63,4096
v_mfma_f32_16x16x32_bf16 v[28:31],a[196:199],v[36:39],v[28:31]
ds_read_b128 v[60:63],v4 offset:54272
s_add_u32 m0,s64,12288
v_mfma_f32_16x16x32_bf16 v[28:31],a[84:87],v[32:35],v[28:31]
s_waitcnt lgkmcnt(4)
v_mfma_f32_16x16x32_bf16 v[28:31],v[116:119],v[40:43],v[28:31]
ds_read_b128 v[32:35],v4 offset:12288
v_mfma_f32_16x16x32_bf16 v[28:31],v[228:231],v[44:47],v[28:31]
buffer_load_dwordx4 v2,s[32:35],s63 offen offset:0 sc1 lds
v_mfma_f32_16x16x32_bf16 v[24:27],v[224:227],v[44:47],v[24:27]
ds_read_b128 v[36:39],v4 offset:26624
v_mfma_f32_16x16x32_bf16 v[24:27],v[112:115],v[40:43],v[24:27]
s_waitcnt lgkmcnt(4)
v_mfma_f32_16x16x32_bf16 v[24:27],a[88:91],v[48:51],v[24:27]
ds_read_b128 v[40:43],v4 offset:40960
v_mfma_f32_16x16x32_bf16 v[24:27],a[200:203],v[52:55],v[24:27]
buffer_load_dwordx4 v2,s[32:35],s63 offen offset:1024 sc1 lds
s_add_u32 s63,s63,1175552
v_mfma_f32_16x16x32_bf16 v[28:31],a[204:207],v[52:55],v[28:31]
ds_read_b128 v[44:47],v4 offset:55296
v_mfma_f32_16x16x32_bf16 v[28:31],a[92:95],v[48:51],v[28:31]
s_waitcnt lgkmcnt(4)
v_mfma_f32_16x16x32_bf16 v[28:31],v[124:127],v[56:59],v[28:31]
ds_read_b128 v[48:51],v4 offset:13312
s_waitcnt vmcnt(10)
v_mfma_f32_16x16x32_bf16 v[28:31],v[236:239],v[60:63],v[28:31]
v_lshlrev_b32 v16,16,v12
v_and_b32 v17,0xffff0000,v12
v_mfma_f32_16x16x32_bf16 v[24:27],v[232:235],v[60:63],v[24:27]
ds_read_b128 v[52:55],v4 offset:27648
v_lshlrev_b32 v18,16,v13
v_mfma_f32_16x16x32_bf16 v[24:27],v[120:123],v[56:59],v[24:27]
v_and_b32 v19,0xffff0000,v13
s_waitcnt lgkmcnt(4)
v_mfma_f32_16x16x32_bf16 v[24:27],a[96:99],v[32:35],v[24:27]
s_barrier
v_mfma_f32_16x16x32_bf16 v[24:27],a[208:211],v[36:39],v[24:27]
ds_read_b128 v[56:59],v4 offset:41984
v_lshlrev_b32 v20,16,v14
v_mfma_f32_16x16x32_bf16 v[28:31],a[212:215],v[36:39],v[28:31]
ds_read_b128 v[60:63],v4 offset:56320
v_and_b32 v21,0xffff0000,v14
v_mfma_f32_16x16x32_bf16 v[28:31],a[100:103],v[32:35],v[28:31]
v_lshlrev_b32 v22,16,v15
s_waitcnt lgkmcnt(4)
v_mfma_f32_16x16x32_bf16 v[28:31],v[132:135],v[40:43],v[28:31]
ds_read_b128 v[32:35],v2 offset:0
v_and_b32 v23,0xffff0000,v15
v_mfma_f32_16x16x32_bf16 v[28:31],v[244:247],v[44:47],v[28:31]
v_mfma_f32_16x16x32_bf16 v[24:27],v[240:243],v[44:47],v[24:27]
ds_read_b128 v[36:39],v2 offset:14336
v_mfma_f32_16x16x32_bf16 v[24:27],v[128:131],v[40:43],v[24:27]
s_waitcnt lgkmcnt(4)
v_mfma_f32_16x16x32_bf16 v[24:27],a[104:107],v[48:51],v[24:27]
ds_read_b128 v[40:43],v2 offset:28672
v_mfma_f32_16x16x32_bf16 v[24:27],a[216:219],v[52:55],v[24:27]
v_mfma_f32_16x16x32_bf16 v[28:31],a[220:223],v[52:55],v[28:31]
ds_read_b128 v[44:47],v2 offset:43008
v_mfma_f32_16x16x32_bf16 v[28:31],a[108:111],v[48:51],v[28:31]
s_waitcnt lgkmcnt(4)
v_mfma_f32_16x16x32_bf16 v[28:31],v[140:143],v[56:59],v[28:31]
ds_read_b128 v[48:51],v2 offset:1024
v_mfma_f32_16x16x32_bf16 v[28:31],v[252:255],v[60:63],v[28:31]
v_mfma_f32_16x16x32_bf16 v[24:27],v[248:251],v[60:63],v[24:27]
ds_read_b128 v[52:55],v2 offset:15360
v_mfma_f32_16x16x32_bf16 v[24:27],v[136:139],v[56:59],v[24:27]
s_branch .L_dll_3
.L_dlb_3:
s_mov_b32 s62,15
s_add_u32 s74,s74,8192
v_lshrrev_b32 v5,8,v2
v_lshl_add_u32 v5,v5,4,s74
v_and_b32 v3,128,v2
v_lshl_add_u32 v5,v3,2,v5
v_and_b32 v3,16,v2
v_lshl_add_u32 v5,v3,4,v5
v_and_b32 v3,96,v2
v_lshl_add_u32 v5,v3,1,v5
v_lshrrev_b32 v3,2,v2
v_add_u32 v3,s74,v3
.p2align 6
.L_dml_3:
s_waitcnt lgkmcnt(4)
v_mfma_f32_16x16x32_bf16 v[24:27],a[0:3],v[32:35],v[24:27]
ds_read_b128 v[56:59],v4 offset:29696
v_mfma_f32_16x16x32_bf16 v[24:27],a[112:115],v[36:39],v[24:27]
v_mfma_f32_16x16x32_bf16 v[28:31],a[116:119],v[36:39],v[28:31]
ds_read_b128 v[60:63],v4 offset:44032
v_mfma_f32_16x16x32_bf16 v[28:31],a[4:7],v[32:35],v[28:31]
v_cvt_pk_bf16_f32 v15,v22,v23
s_waitcnt lgkmcnt(4)
v_mfma_f32_16x16x32_bf16 v[28:31],a[228:231],v[40:43],v[28:31]
ds_read_b128 v[32:35],v4 offset:2048
v_cvt_pk_bf16_f32 v14,v20,v21
v_mfma_f32_16x16x32_bf16 v[28:31],v[148:151],v[44:47],v[28:31]
v_cvt_pk_bf16_f32 v13,v18,v19
v_cvt_pk_bf16_f32 v12,v16,v17
v_mfma_f32_16x16x32_bf16 v[24:27],v[144:147],v[44:47],v[24:27]
ds_read_b128 v[36:39],v4 offset:16384
ds_write_b128 v5,v[12:15]
v_mfma_f32_16x16x32_bf16 v[24:27],a[224:227],v[40:43],v[24:27]
ds_read2st64_b32 v[12:13],v3 offset0:0 offset1:1
s_waitcnt lgkmcnt(6)
v_mfma_f32_16x16x32_bf16 v[24:27],a[8:11],v[48:51],v[24:27]
ds_read_b128 v[40:43],v4 offset:30720
ds_read2st64_b32 v[14:15],v3 offset0:2 offset1:3
v_mfma_f32_16x16x32_bf16 v[24:27],a[120:123],v[52:55],v[24:27]
v_mfma_f32_16x16x32_bf16 v[28:31],a[124:127],v[52:55],v[28:31]
ds_read_b128 v[44:47],v4 offset:45056
v_mfma_f32_16x16x32_bf16 v[28:31],a[12:15],v[48:51],v[28:31]
s_waitcnt lgkmcnt(7)
v_mfma_f32_16x16x32_bf16 v[28:31],a[236:239],v[56:59],v[28:31]
ds_read_b128 v[48:51],v4 offset:3072
v_mfma_f32_16x16x32_bf16 v[28:31],v[156:159],v[60:63],v[28:31]
s_waitcnt lgkmcnt(2)
buffer_atomic_pk_add_bf16 v12,v1,s[44:47],s65 offen offset:0 sc1
v_mfma_f32_16x16x32_bf16 v[24:27],v[152:155],v[60:63],v[24:27]
ds_read_b128 v[52:55],v4 offset:17408
v_mfma_f32_16x16x32_bf16 v[24:27],a[232:235],v[56:59],v[24:27]
buffer_atomic_pk_add_bf16 v13,v1,s[44:47],s65 offen offset:2048 sc1
v_mfma_f32_16x16x32_bf16 v[24:27],a[16:19],v[32:35],v[24:27]
s_waitcnt vmcnt(2)
s_barrier
v_mfma_f32_16x16x32_bf16 v[24:27],a[128:131],v[36:39],v[24:27]
ds_read_b128 v[56:59],v4 offset:31744
s_add_u32 s65,s65,16384
v_mfma_f32_16x16x32_bf16 v[28:31],a[132:135],v[36:39],v[28:31]
ds_read_b128 v[60:63],v4 offset:46080
v_mfma_f32_16x16x32_bf16 v[28:31],a[20:23],v[32:35],v[28:31]
buffer_atomic_pk_add_bf16 v14,v1,s[44:47],s65 offen offset:0 sc1
s_waitcnt lgkmcnt(4)
v_mfma_f32_16x16x32_bf16 v[28:31],a[244:247],v[40:43],v[28:31]
ds_read_b128 v[32:35],v4 offset:4096
s_add_u32 m0,s64,0
v_mfma_f32_16x16x32_bf16 v[28:31],v[164:167],v[44:47],v[28:31]
buffer_atomic_pk_add_bf16 v15,v1,s[44:47],s65 offen offset:2048 sc1
s_add_u32 s65,s65,16384
v_mfma_f32_16x16x32_bf16 v[24:27],v[160:163],v[44:47],v[24:27]
ds_read_b128 v[36:39],v4 offset:18432
v_mfma_f32_16x16x32_bf16 v[24:27],a[240:243],v[40:43],v[24:27]
s_waitcnt lgkmcnt(4)
v_mfma_f32_16x16x32_bf16 v[24:27],a[24:27],v[48:51],v[24:27]
ds_read_b128 v[40:43],v4 offset:32768
v_mfma_f32_16x16x32_bf16 v[24:27],a[136:139],v[52:55],v[24:27]
buffer_load_dwordx4 v2,s[32:35],s63 offen offset:0 sc1 lds
v_mfma_f32_16x16x32_bf16 v[28:31],a[140:143],v[52:55],v[28:31]
ds_read_b128 v[44:47],v4 offset:47104
v_mfma_f32_16x16x32_bf16 v[28:31],a[28:31],v[48:51],v[28:31]
s_waitcnt lgkmcnt(4)
v_mfma_f32_16x16x32_bf16 v[28:31],a[252:255],v[56:59],v[28:31]
ds_read_b128 v[48:51],v4 offset:5120
v_mfma_f32_16x16x32_bf16 v[28:31],v[172:175],v[60:63],v[28:31]
buffer_load_dwordx4 v2,s[32:35],s63 offen offset:1024 sc1 lds
v_mfma_f32_16x16x32_bf16 v[24:27],v[168:171],v[60:63],v[24:27]
ds_read_b128 v[52:55],v4 offset:19456
v_mfma_f32_16x16x32_bf16 v[24:27],a[248:251],v[56:59],v[24:27]
s_waitcnt lgkmcnt(4)
v_mfma_f32_16x16x32_bf16 v[24:27],a[32:35],v[32:35],v[24:27]
ds_read_b128 v[56:59],v4 offset:33792
v_mfma_f32_16x16x32_bf16 v[24:27],a[144:147],v[36:39],v[24:27]
buffer_load_dwordx4 v2,s[32:35],s63 offen offset:2048 sc1 lds
v_mfma_f32_16x16x32_bf16 v[28:31],a[148:151],v[36:39],v[28:31]
ds_read_b128 v[60:63],v4 offset:48128
v_mfma_f32_16x16x32_bf16 v[28:31],a[36:39],v[32:35],v[28:31]
s_waitcnt lgkmcnt(4)
v_mfma_f32_16x16x32_bf16 v[28:31],v[68:71],v[40:43],v[28:31]
ds_read_b128 v[32:35],v4 offset:6144
v_mfma_f32_16x16x32_bf16 v[28:31],v[180:183],v[44:47],v[28:31]
buffer_load_dwordx4 v2,s[32:35],s63 offen offset:3072 sc1 lds
v_mfma_f32_16x16x32_bf16 v[24:27],v[176:179],v[44:47],v[24:27]
ds_read_b128 v[36:39],v4 offset:20480
v_mfma_f32_16x16x32_bf16 v[24:27],v[64:67],v[40:43],v[24:27]
s_waitcnt lgkmcnt(4)
v_mfma_f32_16x16x32_bf16 v[24:27],a[40:43],v[48:51],v[24:27]
ds_read_b128 v[40:43],v4 offset:34816
v_mfma_f32_16x16x32_bf16 v[24:27],a[152:155],v[52:55],v[24:27]
buffer_load_dwordx4 v[12:15],v2,s[32:35],s71 offen sc1
s_add_u32 s71,s71,1187840
v_mfma_f32_16x16x32_bf16 v[28:31],a[156:159],v[52:55],v[28:31]
ds_read_b128 v[44:47],v4 offset:49152
s_add_u32 s63,s63,4096
v_mfma_f32_16x16x32_bf16 v[28:31],a[44:47],v[48:51],v[28:31]
s_add_u32 m0,s64,4096
s_waitcnt lgkmcnt(4)
v_mfma_f32_16x16x32_bf16 v[28:31],v[76:79],v[56:59],v[28:31]
ds_read_b128 v[48:51],v4 offset:7168
v_mfma_f32_16x16x32_bf16 v[28:31],v[188:191],v[60:63],v[28:31]
buffer_load_dwordx4 v2,s[32:35],s63 offen offset:0 sc1 lds
v_mfma_f32_16x16x32_bf16 v[24:27],v[184:187],v[60:63],v[24:27]
ds_read_b128 v[52:55],v4 offset:21504
v_mfma_f32_16x16x32_bf16 v[24:27],v[72:75],v[56:59],v[24:27]
s_waitcnt lgkmcnt(4)
v_mfma_f32_16x16x32_bf16 v[24:27],a[48:51],v[32:35],v[24:27]
ds_read_b128 v[56:59],v4 offset:35840
v_mfma_f32_16x16x32_bf16 v[24:27],a[160:163],v[36:39],v[24:27]
buffer_load_dwordx4 v2,s[32:35],s63 offen offset:1024 sc1 lds
v_mfma_f32_16x16x32_bf16 v[28:31],a[164:167],v[36:39],v[28:31]
ds_read_b128 v[60:63],v4 offset:50176
v_mfma_f32_16x16x32_bf16 v[28:31],a[52:55],v[32:35],v[28:31]
s_waitcnt lgkmcnt(4)
v_mfma_f32_16x16x32_bf16 v[28:31],v[84:87],v[40:43],v[28:31]
ds_read_b128 v[32:35],v4 offset:8192
v_mfma_f32_16x16x32_bf16 v[28:31],v[196:199],v[44:47],v[28:31]
buffer_load_dwordx4 v2,s[32:35],s63 offen offset:2048 sc1 lds
v_mfma_f32_16x16x32_bf16 v[24:27],v[192:195],v[44:47],v[24:27]
ds_read_b128 v[36:39],v4 offset:22528
v_mfma_f32_16x16x32_bf16 v[24:27],v[80:83],v[40:43],v[24:27]
s_waitcnt lgkmcnt(4)
v_mfma_f32_16x16x32_bf16 v[24:27],a[56:59],v[48:51],v[24:27]
ds_read_b128 v[40:43],v4 offset:36864
v_mfma_f32_16x16x32_bf16 v[24:27],a[168:171],v[52:55],v[24:27]
buffer_load_dwordx4 v2,s[32:35],s63 offen offset:3072 sc1 lds
v_mfma_f32_16x16x32_bf16 v[28:31],a[172:175],v[52:55],v[28:31]
ds_read_b128 v[44:47],v4 offset:51200
v_mfma_f32_16x16x32_bf16 v[28:31],a[60:63],v[48:51],v[28:31]
s_waitcnt lgkmcnt(4)
v_mfma_f32_16x16x32_bf16 v[28:31],v[92:95],v[56:59],v[28:31]
ds_read_b128 v[48:51],v4 offset:9216
v_mfma_f32_16x16x32_bf16 v[28:31],v[204:207],v[60:63],v[28:31]
s_add_u32 s63,s63,4096
s_add_u32 m0,s64,8192
v_mfma_f32_16x16x32_bf16 v[24:27],v[200:203],v[60:63],v[24:27]
ds_read_b128 v[52:55],v4 offset:23552
)#"
R"#(
v_mfma_f32_16x16x32_bf16 v[24:27],v[88:91],v[56:59],v[24:27]
s_waitcnt lgkmcnt(4)
v_mfma_f32_16x16x32_bf16 v[24:27],a[64:67],v[32:35],v[24:27]
ds_read_b128 v[56:59],v4 offset:37888
v_mfma_f32_16x16x32_bf16 v[24:27],a[176:179],v[36:39],v[24:27]
buffer_load_dwordx4 v2,s[32:35],s63 offen offset:0 sc1 lds
v_mfma_f32_16x16x32_bf16 v[28:31],a[180:183],v[36:39],v[28:31]
ds_read_b128 v[60:63],v4 offset:52224
v_mfma_f32_16x16x32_bf16 v[28:31],a[68:71],v[32:35],v[28:31]
s_waitcnt lgkmcnt(4)
v_mfma_f32_16x16x32_bf16 v[28:31],v[100:103],v[40:43],v[28:31]
ds_read_b128 v[32:35],v4 offset:10240
v_mfma_f32_16x16x32_bf16 v[28:31],v[212:215],v[44:47],v[28:31]
buffer_load_dwordx4 v2,s[32:35],s63 offen offset:1024 sc1 lds
v_mfma_f32_16x16x32_bf16 v[24:27],v[208:211],v[44:47],v[24:27]
ds_read_b128 v[36:39],v4 offset:24576
v_mfma_f32_16x16x32_bf16 v[24:27],v[96:99],v[40:43],v[24:27]
s_waitcnt lgkmcnt(4)
v_mfma_f32_16x16x32_bf16 v[24:27],a[72:75],v[48:51],v[24:27]
ds_read_b128 v[40:43],v4 offset:38912
v_mfma_f32_16x16x32_bf16 v[24:27],a[184:187],v[52:55],v[24:27]
buffer_load_dwordx4 v2,s[32:35],s63 offen offset:2048 sc1 lds
v_mfma_f32_16x16x32_bf16 v[28:31],a[188:191],v[52:55],v[28:31]
ds_read_b128 v[44:47],v4 offset:53248
v_mfma_f32_16x16x32_bf16 v[28:31],a[76:79],v[48:51],v[28:31]
s_waitcnt lgkmcnt(4)
v_mfma_f32_16x16x32_bf16 v[28:31],v[108:111],v[56:59],v[28:31]
ds_read_b128 v[48:51],v4 offset:11264
v_mfma_f32_16x16x32_bf16 v[28:31],v[220:223],v[60:63],v[28:31]
v_mfma_f32_16x16x32_bf16 v[24:27],v[216:219],v[60:63],v[24:27]
ds_read_b128 v[52:55],v4 offset:25600
v_mfma_f32_16x16x32_bf16 v[24:27],v[104:107],v[56:59],v[24:27]
s_waitcnt lgkmcnt(4)
v_mfma_f32_16x16x32_bf16 v[24:27],a[80:83],v[32:35],v[24:27]
ds_read_b128 v[56:59],v4 offset:39936
v_mfma_f32_16x16x32_bf16 v[24:27],a[192:195],v[36:39],v[24:27]
buffer_load_dwordx4 v2,s[32:35],s63 offen offset:3072 sc1 lds
s_add_u32 s63,s63,4096
v_mfma_f32_16x16x32_bf16 v[28:31],a[196:199],v[36:39],v[28:31]
ds_read_b128 v[60:63],v4 offset:54272
s_add_u32 m0,s64,12288
v_mfma_f32_16x16x32_bf16 v[28:31],a[84:87],v[32:35],v[28:31]
s_waitcnt lgkmcnt(4)
v_mfma_f32_16x16x32_bf16 v[28:31],v[116:119],v[40:43],v[28:31]
ds_read_b128 v[32:35],v4 offset:12288
v_mfma_f32_16x16x32_bf16 v[28:31],v[228:231],v[44:47],v[28:31]
buffer_load_dwordx4 v2,s[32:35],s63 offen offset:0 sc1 lds
v_mfma_f32_16x16x32_bf16 v[24:27],v[224:227],v[44:47],v[24:27]
ds_read_b128 v[36:39],v4 offset:26624
v_mfma_f32_16x16x32_bf16 v[24:27],v[112:115],v[40:43],v[24:27]
s_waitcnt lgkmcnt(4)
v_mfma_f32_16x16x32_bf16 v[24:27],a[88:91],v[48:51],v[24:27]
ds_read_b128 v[40:43],v4 offset:40960
v_mfma_f32_16x16x32_bf16 v[24:27],a[200:203],v[52:55],v[24:27]
buffer_load_dwordx4 v2,s[32:35],s63 offen offset:1024 sc1 lds
s_add_u32 s63,s63,1175552
v_mfma_f32_16x16x32_bf16 v[28:31],a[204:207],v[52:55],v[28:31]
ds_read_b128 v[44:47],v4 offset:55296
v_mfma_f32_16x16x32_bf16 v[28:31],a[92:95],v[48:51],v[28:31]
s_waitcnt lgkmcnt(4)
v_mfma_f32_16x16x32_bf16 v[28:31],v[124:127],v[56:59],v[28:31]
ds_read_b128 v[48:51],v4 offset:13312
s_waitcnt vmcnt(10)
v_mfma_f32_16x16x32_bf16 v[28:31],v[236:239],v[60:63],v[28:31]
v_lshlrev_b32 v16,16,v12
v_and_b32 v17,0xffff0000,v12
v_mfma_f32_16x16x32_bf16 v[24:27],v[232:235],v[60:63],v[24:27]
ds_read_b128 v[52:55],v4 offset:27648
v_lshlrev_b32 v18,16,v13
v_mfma_f32_16x16x32_bf16 v[24:27],v[120:123],v[56:59],v[24:27]
v_and_b32 v19,0xffff0000,v13
s_waitcnt lgkmcnt(4)
v_mfma_f32_16x16x32_bf16 v[24:27],a[96:99],v[32:35],v[24:27]
s_barrier
v_mfma_f32_16x16x32_bf16 v[24:27],a[208:211],v[36:39],v[24:27]
ds_read_b128 v[56:59],v4 offset:41984
v_lshlrev_b32 v20,16,v14
v_mfma_f32_16x16x32_bf16 v[28:31],a[212:215],v[36:39],v[28:31]
ds_read_b128 v[60:63],v4 offset:56320
v_and_b32 v21,0xffff0000,v14
v_mfma_f32_16x16x32_bf16 v[28:31],a[100:103],v[32:35],v[28:31]
v_lshlrev_b32 v22,16,v15
s_waitcnt lgkmcnt(4)
v_mfma_f32_16x16x32_bf16 v[28:31],v[132:135],v[40:43],v[28:31]
ds_read_b128 v[32:35],v2 offset:0
v_and_b32 v23,0xffff0000,v15
v_mfma_f32_16x16x32_bf16 v[28:31],v[244:247],v[44:47],v[28:31]
v_mfma_f32_16x16x32_bf16 v[24:27],v[240:243],v[44:47],v[24:27]
ds_read_b128 v[36:39],v2 offset:14336
v_mfma_f32_16x16x32_bf16 v[24:27],v[128:131],v[40:43],v[24:27]
s_waitcnt lgkmcnt(4)
v_mfma_f32_16x16x32_bf16 v[24:27],a[104:107],v[48:51],v[24:27]
ds_read_b128 v[40:43],v2 offset:28672
v_mfma_f32_16x16x32_bf16 v[24:27],a[216:219],v[52:55],v[24:27]
v_mfma_f32_16x16x32_bf16 v[28:31],a[220:223],v[52:55],v[28:31]
ds_read_b128 v[44:47],v2 offset:43008
v_mfma_f32_16x16x32_bf16 v[28:31],a[108:111],v[48:51],v[28:31]
s_waitcnt lgkmcnt(4)
v_mfma_f32_16x16x32_bf16 v[28:31],v[140:143],v[56:59],v[28:31]
ds_read_b128 v[48:51],v2 offset:1024
v_mfma_f32_16x16x32_bf16 v[28:31],v[252:255],v[60:63],v[28:31]
v_mfma_f32_16x16x32_bf16 v[24:27],v[248:251],v[60:63],v[24:27]
ds_read_b128 v[52:55],v2 offset:15360
s_sub_u32 s62,s62,1
v_mfma_f32_16x16x32_bf16 v[24:27],v[136:139],v[56:59],v[24:27]
s_cbranch_scc1 .L_dmt_3
s_waitcnt lgkmcnt(4)
v_mfma_f32_16x16x32_bf16 v[16:19],a[0:3],v[32:35],v[16:19]
ds_read_b128 v[56:59],v2 offset:29696
v_mfma_f32_16x16x32_bf16 v[16:19],a[112:115],v[36:39],v[16:19]
v_mfma_f32_16x16x32_bf16 v[20:23],a[116:119],v[36:39],v[20:23]
ds_read_b128 v[60:63],v2 offset:44032
v_mfma_f32_16x16x32_bf16 v[20:23],a[4:7],v[32:35],v[20:23]
v_cvt_pk_bf16_f32 v15,v30,v31
s_waitcnt lgkmcnt(4)
v_mfma_f32_16x16x32_bf16 v[20:23],a[228:231],v[40:43],v[20:23]
ds_read_b128 v[32:35],v2 offset:2048
v_cvt_pk_bf16_f32 v14,v28,v29
v_mfma_f32_16x16x32_bf16 v[20:23],v[148:151],v[44:47],v[20:23]
v_cvt_pk_bf16_f32 v13,v26,v27
v_cvt_pk_bf16_f32 v12,v24,v25
v_mfma_f32_16x16x32_bf16 v[16:19],v[144:147],v[44:47],v[16:19]
ds_read_b128 v[36:39],v2 offset:16384
ds_write_b128 v5,v[12:15]
v_mfma_f32_16x16x32_bf16 v[16:19],a[224:227],v[40:43],v[16:19]
ds_read2st64_b32 v[12:13],v3 offset0:0 offset1:1
s_waitcnt lgkmcnt(6)
v_mfma_f32_16x16x32_bf16 v[16:19],a[8:11],v[48:51],v[16:19]
ds_read_b128 v[40:43],v2 offset:30720
ds_read2st64_b32 v[14:15],v3 offset0:2 offset1:3
v_mfma_f32_16x16x32_bf16 v[16:19],a[120:123],v[52:55],v[16:19]
v_mfma_f32_16x16x32_bf16 v[20:23],a[124:127],v[52:55],v[20:23]
ds_read_b128 v[44:47],v2 offset:45056
v_mfma_f32_16x16x32_bf16 v[20:23],a[12:15],v[48:51],v[20:23]
s_waitcnt lgkmcnt(7)
v_mfma_f32_16x16x32_bf16 v[20:23],a[236:239],v[56:59],v[20:23]
ds_read_b128 v[48:51],v2 offset:3072
v_mfma_f32_16x16x32_bf16 v[20:23],v[156:159],v[60:63],v[20:23]
s_waitcnt lgkmcnt(2)
buffer_atomic_pk_add_bf16 v12,v1,s[44:47],s65 offen offset:0 sc1
v_mfma_f32_16x16x32_bf16 v[16:19],v[152:155],v[60:63],v[16:19]
ds_read_b128 v[52:55],v2 offset:17408
v_mfma_f32_16x16x32_bf16 v[16:19],a[232:235],v[56:59],v[16:19]
buffer_atomic_pk_add_bf16 v13,v1,s[44:47],s65 offen offset:2048 sc1
v_mfma_f32_16x16x32_bf16 v[16:19],a[16:19],v[32:35],v[16:19]
s_waitcnt vmcnt(2)
s_barrier
v_mfma_f32_16x16x32_bf16 v[16:19],a[128:131],v[36:39],v[16:19]
ds_read_b128 v[56:59],v2 offset:31744
s_add_u32 s65,s65,16384
v_mfma_f32_16x16x32_bf16 v[20:23],a[132:135],v[36:39],v[20:23]
ds_read_b128 v[60:63],v2 offset:46080
v_mfma_f32_16x16x32_bf16 v[20:23],a[20:23],v[32:35],v[20:23]
buffer_atomic_pk_add_bf16 v14,v1,s[44:47],s65 offen offset:0 sc1
s_waitcnt lgkmcnt(4)
v_mfma_f32_16x16x32_bf16 v[20:23],a[244:247],v[40:43],v[20:23]
ds_read_b128 v[32:35],v2 offset:4096
s_add_u32 m0,s64,57344
v_mfma_f32_16x16x32_bf16 v[20:23],v[164:167],v[44:47],v[20:23]
buffer_atomic_pk_add_bf16 v15,v1,s[44:47],s65 offen offset:2048 sc1
s_add_u32 s65,s65,16384
v_mfma_f32_16x16x32_bf16 v[16:19],v[160:163],v[44:47],v[16:19]
ds_read_b128 v[36:39],v2 offset:18432
v_mfma_f32_16x16x32_bf16 v[16:19],a[240:243],v[40:43],v[16:19]
s_waitcnt lgkmcnt(4)
v_mfma_f32_16x16x32_bf16 v[16:19],a[24:27],v[48:51],v[16:19]
ds_read_b128 v[40:43],v2 offset:32768
v_mfma_f32_16x16x32_bf16 v[16:19],a[136:139],v[52:55],v[16:19]
buffer_load_dwordx4 v2,s[32:35],s63 offen offset:0 sc1 lds
v_mfma_f32_16x16x32_bf16 v[20:23],a[140:143],v[52:55],v[20:23]
ds_read_b128 v[44:47],v2 offset:47104
v_mfma_f32_16x16x32_bf16 v[20:23],a[28:31],v[48:51],v[20:23]
s_waitcnt lgkmcnt(4)
v_mfma_f32_16x16x32_bf16 v[20:23],a[252:255],v[56:59],v[20:23]
ds_read_b128 v[48:51],v2 offset:5120
v_mfma_f32_16x16x32_bf16 v[20:23],v[172:175],v[60:63],v[20:23]
buffer_load_dwordx4 v2,s[32:35],s63 offen offset:1024 sc1 lds
v_mfma_f32_16x16x32_bf16 v[16:19],v[168:171],v[60:63],v[16:19]
ds_read_b128 v[52:55],v2 offset:19456
v_mfma_f32_16x16x32_bf16 v[16:19],a[248:251],v[56:59],v[16:19]
s_waitcnt lgkmcnt(4)
v_mfma_f32_16x16x32_bf16 v[16:19],a[32:35],v[32:35],v[16:19]
ds_read_b128 v[56:59],v2 offset:33792
v_mfma_f32_16x16x32_bf16 v[16:19],a[144:147],v[36:39],v[16:19]
buffer_load_dwordx4 v2,s[32:35],s63 offen offset:2048 sc1 lds
v_mfma_f32_16x16x32_bf16 v[20:23],a[148:151],v[36:39],v[20:23]
ds_read_b128 v[60:63],v2 offset:48128
v_mfma_f32_16x16x32_bf16 v[20:23],a[36:39],v[32:35],v[20:23]
s_waitcnt lgkmcnt(4)
v_mfma_f32_16x16x32_bf16 v[20:23],v[68:71],v[40:43],v[20:23]
ds_read_b128 v[32:35],v2 offset:6144
v_mfma_f32_16x16x32_bf16 v[20:23],v[180:183],v[44:47],v[20:23]
buffer_load_dwordx4 v2,s[32:35],s63 offen offset:3072 sc1 lds
v_mfma_f32_16x16x32_bf16 v[16:19],v[176:179],v[44:47],v[16:19]
ds_read_b128 v[36:39],v2 offset:20480
v_mfma_f32_16x16x32_bf16 v[16:19],v[64:67],v[40:43],v[16:19]
s_waitcnt lgkmcnt(4)
v_mfma_f32_16x16x32_bf16 v[16:19],a[40:43],v[48:51],v[16:19]
ds_read_b128 v[40:43],v2 offset:34816
v_mfma_f32_16x16x32_bf16 v[16:19],a[152:155],v[52:55],v[16:19]
buffer_load_dwordx4 v[12:15],v2,s[32:35],s71 offen sc1
s_add_u32 s71,s71,1187840
v_mfma_f32_16x16x32_bf16 v[20:23],a[156:159],v[52:55],v[20:23]
ds_read_b128 v[44:47],v2 offset:49152
s_add_u32 s63,s63,4096
v_mfma_f32_16x16x32_bf16 v[20:23],a[44:47],v[48:51],v[20:23]
s_add_u32 m0,s64,61440
s_waitcnt lgkmcnt(4)
v_mfma_f32_16x16x32_bf16 v[20:23],v[76:79],v[56:59],v[20:23]
ds_read_b128 v[48:51],v2 offset:7168
v_mfma_f32_16x16x32_bf16 v[20:23],v[188:191],v[60:63],v[20:23]
buffer_load_dwordx4 v2,s[32:35],s63 offen offset:0 sc1 lds
v_mfma_f32_16x16x32_bf16 v[16:19],v[184:187],v[60:63],v[16:19]
ds_read_b128 v[52:55],v2 offset:21504
v_mfma_f32_16x16x32_bf16 v[16:19],v[72:75],v[56:59],v[16:19]
s_waitcnt lgkmcnt(4)
v_mfma_f32_16x16x32_bf16 v[16:19],a[48:51],v[32:35],v[16:19]
ds_read_b128 v[56:59],v2 offset:35840
v_mfma_f32_16x16x32_bf16 v[16:19],a[160:163],v[36:39],v[16:19]
buffer_load_dwordx4 v2,s[32:35],s63 offen offset:1024 sc1 lds
v_mfma_f32_16x16x32_bf16 v[20:23],a[164:167],v[36:39],v[20:23]
ds_read_b128 v[60:63],v2 offset:50176
v_mfma_f32_16x16x32_bf16 v[20:23],a[52:55],v[32:35],v[20:23]
s_waitcnt lgkmcnt(4)
v_mfma_f32_16x16x32_bf16 v[20:23],v[84:87],v[40:43],v[20:23]
ds_read_b128 v[32:35],v2 offset:8192
v_mfma_f32_16x16x32_bf16 v[20:23],v[196:199],v[44:47],v[20:23]
buffer_load_dwordx4 v2,s[32:35],s63 offen offset:2048 sc1 lds
v_mfma_f32_16x16x32_bf16 v[16:19],v[192:195],v[44:47],v[16:19]
ds_read_b128 v[36:39],v2 offset:22528
v_mfma_f32_16x16x32_bf16 v[16:19],v[80:83],v[40:43],v[16:19]
s_waitcnt lgkmcnt(4)
v_mfma_f32_16x16x32_bf16 v[16:19],a[56:59],v[48:51],v[16:19]
ds_read_b128 v[40:43],v2 offset:36864
v_mfma_f32_16x16x32_bf16 v[16:19],a[168:171],v[52:55],v[16:19]
buffer_load_dwordx4 v2,s[32:35],s63 offen offset:3072 sc1 lds
v_mfma_f32_16x16x32_bf16 v[20:23],a[172:175],v[52:55],v[20:23]
ds_read_b128 v[44:47],v2 offset:51200
v_mfma_f32_16x16x32_bf16 v[20:23],a[60:63],v[48:51],v[20:23]
s_waitcnt lgkmcnt(4)
v_mfma_f32_16x16x32_bf16 v[20:23],v[92:95],v[56:59],v[20:23]
ds_read_b128 v[48:51],v2 offset:9216
v_mfma_f32_16x16x32_bf16 v[20:23],v[204:207],v[60:63],v[20:23]
s_add_u32 s63,s63,4096
s_add_u32 m0,s64,65536
v_mfma_f32_16x16x32_bf16 v[16:19],v[200:203],v[60:63],v[16:19]
ds_read_b128 v[52:55],v2 offset:23552
v_mfma_f32_16x16x32_bf16 v[16:19],v[88:91],v[56:59],v[16:19]
s_waitcnt lgkmcnt(4)
v_mfma_f32_16x16x32_bf16 v[16:19],a[64:67],v[32:35],v[16:19]
ds_read_b128 v[56:59],v2 offset:37888
v_mfma_f32_16x16x32_bf16 v[16:19],a[176:179],v[36:39],v[16:19]
buffer_load_dwordx4 v2,s[32:35],s63 offen offset:0 sc1 lds
v_mfma_f32_16x16x32_bf16 v[20:23],a[180:183],v[36:39],v[20:23]
ds_read_b128 v[60:63],v2 offset:52224
v_mfma_f32_16x16x32_bf16 v[20:23],a[68:71],v[32:35],v[20:23]
s_waitcnt lgkmcnt(4)
v_mfma_f32_16x16x32_bf16 v[20:23],v[100:103],v[40:43],v[20:23]
ds_read_b128 v[32:35],v2 offset:10240
v_mfma_f32_16x16x32_bf16 v[20:23],v[212:215],v[44:47],v[20:23]
buffer_load_dwordx4 v2,s[32:35],s63 offen offset:1024 sc1 lds
v_mfma_f32_16x16x32_bf16 v[16:19],v[208:211],v[44:47],v[16:19]
ds_read_b128 v[36:39],v2 offset:24576
v_mfma_f32_16x16x32_bf16 v[16:19],v[96:99],v[40:43],v[16:19]
s_waitcnt lgkmcnt(4)
v_mfma_f32_16x16x32_bf16 v[16:19],a[72:75],v[48:51],v[16:19]
ds_read_b128 v[40:43],v2 offset:38912
v_mfma_f32_16x16x32_bf16 v[16:19],a[184:187],v[52:55],v[16:19]
buffer_load_dwordx4 v2,s[32:35],s63 offen offset:2048 sc1 lds
v_mfma_f32_16x16x32_bf16 v[20:23],a[188:191],v[52:55],v[20:23]
ds_read_b128 v[44:47],v2 offset:53248
v_mfma_f32_16x16x32_bf16 v[20:23],a[76:79],v[48:51],v[20:23]
s_waitcnt lgkmcnt(4)
v_mfma_f32_16x16x32_bf16 v[20:23],v[108:111],v[56:59],v[20:23]
ds_read_b128 v[48:51],v2 offset:11264
v_mfma_f32_16x16x32_bf16 v[20:23],v[220:223],v[60:63],v[20:23]
v_mfma_f32_16x16x32_bf16 v[16:19],v[216:219],v[60:63],v[16:19]
ds_read_b128 v[52:55],v2 offset:25600
v_mfma_f32_16x16x32_bf16 v[16:19],v[104:107],v[56:59],v[16:19]
s_waitcnt lgkmcnt(4)
v_mfma_f32_16x16x32_bf16 v[16:19],a[80:83],v[32:35],v[16:19]
ds_read_b128 v[56:59],v2 offset:39936
v_mfma_f32_16x16x32_bf16 v[16:19],a[192:195],v[36:39],v[16:19]
buffer_load_dwordx4 v2,s[32:35],s63 offen offset:3072 sc1 lds
s_add_u32 s63,s63,4096
v_mfma_f32_16x16x32_bf16 v[20:23],a[196:199],v[36:39],v[20:23]
ds_read_b128 v[60:63],v2 offset:54272
s_add_u32 m0,s64,69632
v_mfma_f32_16x16x32_bf16 v[20:23],a[84:87],v[32:35],v[20:23]
s_waitcnt lgkmcnt(4)
v_mfma_f32_16x16x32_bf16 v[20:23],v[116:119],v[40:43],v[20:23]
ds_read_b128 v[32:35],v2 offset:12288
v_mfma_f32_16x16x32_bf16 v[20:23],v[228:231],v[44:47],v[20:23]
buffer_load_dwordx4 v2,s[32:35],s63 offen offset:0 sc1 lds
v_mfma_f32_16x16x32_bf16 v[16:19],v[224:227],v[44:47],v[16:19]
ds_read_b128 v[36:39],v2 offset:26624
v_mfma_f32_16x16x32_bf16 v[16:19],v[112:115],v[40:43],v[16:19]
s_waitcnt lgkmcnt(4)
v_mfma_f32_16x16x32_bf16 v[16:19],a[88:91],v[48:51],v[16:19]
ds_read_b128 v[40:43],v2 offset:40960
v_mfma_f32_16x16x32_bf16 v[16:19],a[200:203],v[52:55],v[16:19]
buffer_load_dwordx4 v2,s[32:35],s63 offen offset:1024 sc1 lds
s_add_u32 s63,s63,1175552
v_mfma_f32_16x16x32_bf16 v[20:23],a[204:207],v[52:55],v[20:23]
ds_read_b128 v[44:47],v2 offset:55296
v_mfma_f32_16x16x32_bf16 v[20:23],a[92:95],v[48:51],v[20:23]
s_waitcnt lgkmcnt(4)
v_mfma_f32_16x16x32_bf16 v[20:23],v[124:127],v[56:59],v[20:23]
ds_read_b128 v[48:51],v2 offset:13312
s_waitcnt vmcnt(10)
v_mfma_f32_16x16x32_bf16 v[20:23],v[236:239],v[60:63],v[20:23]
v_lshlrev_b32 v24,16,v12
v_and_b32 v25,0xffff0000,v12
v_mfma_f32_16x16x32_bf16 v[16:19],v[232:235],v[60:63],v[16:19]
ds_read_b128 v[52:55],v2 offset:27648
v_lshlrev_b32 v26,16,v13
v_mfma_f32_16x16x32_bf16 v[16:19],v[120:123],v[56:59],v[16:19]
v_and_b32 v27,0xffff0000,v13
s_waitcnt lgkmcnt(4)
v_mfma_f32_16x16x32_bf16 v[16:19],a[96:99],v[32:35],v[16:19]
s_barrier
v_mfma_f32_16x16x32_bf16 v[16:19],a[208:211],v[36:39],v[16:19]
ds_read_b128 v[56:59],v2 offset:41984
v_lshlrev_b32 v28,16,v14
v_mfma_f32_16x16x32_bf16 v[20:23],a[212:215],v[36:39],v[20:23]
ds_read_b128 v[60:63],v2 offset:56320
v_and_b32 v29,0xffff0000,v14
v_mfma_f32_16x16x32_bf16 v[20:23],a[100:103],v[32:35],v[20:23]
v_lshlrev_b32 v30,16,v15
s_waitcnt lgkmcnt(4)
v_mfma_f32_16x16x32_bf16 v[20:23],v[132:135],v[40:43],v[20:23]
ds_read_b128 v[32:35],v4 offset:0
v_and_b32 v31,0xffff0000,v15
v_mfma_f32_16x16x32_bf16 v[20:23],v[244:247],v[44:47],v[20:23]
v_mfma_f32_16x16x32_bf16 v[16:19],v[240:243],v[44:47],v[16:19]
ds_read_b128 v[36:39],v4 offset:14336
v_mfma_f32_16x16x32_bf16 v[16:19],v[128:131],v[40:43],v[16:19]
s_waitcnt lgkmcnt(4)
v_mfma_f32_16x16x32_bf16 v[16:19],a[104:107],v[48:51],v[16:19]
ds_read_b128 v[40:43],v4 offset:28672
v_mfma_f32_16x16x32_bf16 v[16:19],a[216:219],v[52:55],v[16:19]
v_mfma_f32_16x16x32_bf16 v[20:23],a[220:223],v[52:55],v[20:23]
ds_read_b128 v[44:47],v4 offset:43008
v_mfma_f32_16x16x32_bf16 v[20:23],a[108:111],v[48:51],v[20:23]
s_waitcnt lgkmcnt(4)
v_mfma_f32_16x16x32_bf16 v[20:23],v[140:143],v[56:59],v[20:23]
ds_read_b128 v[48:51],v4 offset:1024
v_mfma_f32_16x16x32_bf16 v[20:23],v[252:255],v[60:63],v[20:23]
v_mfma_f32_16x16x32_bf16 v[16:19],v[248:251],v[60:63],v[16:19]
ds_read_b128 v[52:55],v4 offset:15360
v_mfma_f32_16x16x32_bf16 v[16:19],v[136:139],v[56:59],v[16:19]
s_branch .L_dml_3
.L_dmt_3:
s_nop 1
s_nop 0
v_cvt_pk_bf16_f32 v15,v30,v31
v_cvt_pk_bf16_f32 v14,v28,v29
s_nop 1
v_cvt_pk_bf16_f32 v13,v26,v27
v_cvt_pk_bf16_f32 v12,v24,v25
ds_write_b128 v5,v[12:15]
ds_read2st64_b32 v[12:13],v3 offset0:0 offset1:1
ds_read2st64_b32 v[14:15],v3 offset0:2 offset1:3
s_waitcnt lgkmcnt(0)
buffer_atomic_pk_add_bf16 v12,v1,s[44:47],s65 offen offset:0 sc1
buffer_atomic_pk_add_bf16 v13,v1,s[44:47],s65 offen offset:2048 sc1
s_add_u32 s65,s65,16384
buffer_atomic_pk_add_bf16 v14,v1,s[44:47],s65 offen offset:0 sc1
buffer_atomic_pk_add_bf16 v15,v1,s[44:47],s65 offen offset:2048 sc1
s_add_u32 s65,s65,16384
s_branch .L_end
.L_dh:
v_mov_b32 v12,0
v_mov_b32 v13,0
v_mov_b32 v14,0
v_mov_b32 v15,0
v_lshrrev_b32 v10,4,v1
v_and_b32 v11,0xf0,v2
v_lshl_add_u32 v10,v10,11,v11
s_lshl2_add_u32 s48,s28,s26
s_lshl_b32 s48,s48,10
s_add_u32 s48,s48,s27
s_lshl_b32 s48,s48,8
s_mov_b32 s49,31
.L_dz:
buffer_store_dwordx4 v[12:15],v10,s[44:47],s48 offen sc1
s_add_u32 s48,s48,8192
s_sub_u32 s49,s49,1
s_cbranch_scc0 .L_dz
v_lshrrev_b32 v10,4,v1
v_and_b32 v1,15,v1
s_lshl_b32 s48,s50,6
v_lshl_add_u32 v1,v1,2,s48
v_lshl_add_u32 v1,v10,12,v1
s_sub_u32 s71,s65,1187840
s_add_u32 s69,s69,38010880
s_add_u32 s68,s70,2236416
s_mov_b32 s67,s68
s_mov_b32 s72,0x3f803f80
s_add_u32 s70,s70,42622976
s_lshl_b32 s65,s28,20
s_mov_b32 s48,20000
s_branch .L_rfda
.L_rlda:
s_sleep 8
.L_rfda:
buffer_load_dwordx2 v[6:7],v0,s[32:35],s66 offen sc1
s_sub_u32 s48,s48,1
s_cbranch_scc1 .L_end
s_waitcnt vmcnt(0)
v_cmp_ne_u64 vcc,v[8:9],v[6:7]
s_cbranch_vccnz .L_rlda
s_add_u32 s66,s66,1187840
s_add_u32 m0,s64,0
s_nop 0
buffer_load_dwordx4 v2,s[32:35],s63 offen offset:0 sc1 lds
buffer_load_dwordx4 v2,s[32:35],s63 offen offset:1024 sc1 lds
buffer_load_dwordx4 v2,s[32:35],s63 offen offset:2048 sc1 lds
buffer_load_dwordx4 v2,s[32:35],s63 offen offset:3072 sc1 lds
s_add_u32 s63,s63,4096
s_add_u32 m0,s64,4096
s_nop 0
buffer_load_dwordx4 v2,s[32:35],s63 offen offset:0 sc1 lds
buffer_load_dwordx4 v2,s[32:35],s63 offen offset:1024 sc1 lds
buffer_load_dwordx4 v2,s[32:35],s63 offen offset:2048 sc1 lds
buffer_load_dwordx4 v2,s[32:35],s63 offen offset:3072 sc1 lds
s_add_u32 s63,s63,4096
s_add_u32 m0,s64,8192
s_nop 0
buffer_load_dwordx4 v2,s[32:35],s63 offen offset:0 sc1 lds
buffer_load_dwordx4 v2,s[32:35],s63 offen offset:1024 sc1 lds
buffer_load_dwordx4 v2,s[32:35],s63 offen offset:2048 sc1 lds
buffer_load_dwordx4 v2,s[32:35],s63 offen offset:3072 sc1 lds
s_add_u32 s63,s63,4096
s_add_u32 m0,s64,12288
s_nop 0
buffer_load_dwordx4 v2,s[32:35],s63 offen offset:0 sc1 lds
buffer_load_dwordx4 v2,s[32:35],s63 offen offset:1024 sc1 lds
s_add_u32 s63,s63,1175552
buffer_load_dwordx2 v[6:7],v0,s[32:35],s66 offen sc1
s_waitcnt vmcnt(0)
s_barrier
ds_read_b128 v[32:35],v2 offset:0
ds_read_b128 v[36:39],v2 offset:14336
ds_read_b128 v[40:43],v2 offset:28672
ds_read_b128 v[44:47],v2 offset:43008
ds_read_b128 v[48:51],v2 offset:1024
ds_read_b128 v[52:55],v2 offset:15360
.L_dhgo:
s_cmp_eq_u32 s26,1
s_cbranch_scc1 .L_dhv1
s_cmp_eq_u32 s26,2
s_cbranch_scc1 .L_dhv2
s_cmp_eq_u32 s26,3
s_cbranch_scc1 .L_dhv3
.p2align 6
.L_dhv0:
.L_dal_0:
s_waitcnt lgkmcnt(4)
v_mfma_f32_16x16x32_bf16 v[16:19],a[0:3],v[32:35],0
ds_read_b128 v[56:59],v2 offset:29696
v_dot2_f32_bf16 v10,v12,s72,0
v_mfma_f32_16x16x32_bf16 v[16:19],a[112:115],v[36:39],v[16:19]
v_dot2_f32_bf16 v11,v12,v12,0
v_dot2_f32_bf16 v10,v13,s72,v10
v_mfma_f32_16x16x32_bf16 v[20:23],a[116:119],v[36:39],0
ds_read_b128 v[60:63],v2 offset:44032
v_dot2_f32_bf16 v11,v13,v13,v11
v_mfma_f32_16x16x32_bf16 v[20:23],a[4:7],v[32:35],v[20:23]
v_dot2_f32_bf16 v10,v14,s72,v10
s_waitcnt lgkmcnt(4)
v_mfma_f32_16x16x32_bf16 v[20:23],a[228:231],v[40:43],v[20:23]
ds_read_b128 v[32:35],v2 offset:2048
v_dot2_f32_bf16 v11,v14,v14,v11
v_mfma_f32_16x16x32_bf16 v[20:23],v[148:151],v[44:47],v[20:23]
v_dot2_f32_bf16 v10,v15,s72,v10
v_dot2_f32_bf16 v11,v15,v15,v11
v_mfma_f32_16x16x32_bf16 v[16:19],v[144:147],v[44:47],v[16:19]
ds_read_b128 v[36:39],v2 offset:16384
v_mfma_f32_16x16x32_bf16 v[16:19],a[224:227],v[40:43],v[16:19]
v_permlane16_swap_b32 v10,v11
s_waitcnt lgkmcnt(4)
v_mfma_f32_16x16x32_bf16 v[16:19],a[8:11],v[48:51],v[16:19]
ds_read_b128 v[40:43],v2 offset:30720
v_add_f32 v10,v10,v11
v_mfma_f32_16x16x32_bf16 v[16:19],a[120:123],v[52:55],v[16:19]
v_mfma_f32_16x16x32_bf16 v[20:23],a[124:127],v[52:55],v[20:23]
ds_read_b128 v[44:47],v2 offset:45056
v_permlane32_swap_b32 v10,v11
v_mfma_f32_16x16x32_bf16 v[20:23],a[12:15],v[48:51],v[20:23]
v_add_f32 v10,v10,v11
s_waitcnt lgkmcnt(4)
v_mfma_f32_16x16x32_bf16 v[20:23],a[236:239],v[56:59],v[20:23]
ds_read_b128 v[48:51],v2 offset:3072
v_mfma_f32_16x16x32_bf16 v[20:23],v[156:159],v[60:63],v[20:23]
buffer_store_dword v10,v3,s[32:35],s67 offen offset:1024 sc1
v_mfma_f32_16x16x32_bf16 v[16:19],v[152:155],v[60:63],v[16:19]
ds_read_b128 v[52:55],v2 offset:17408
v_mfma_f32_16x16x32_bf16 v[16:19],a[232:235],v[56:59],v[16:19]
s_waitcnt lgkmcnt(4)
)#"
R"#(
v_mfma_f32_16x16x32_bf16 v[16:19],a[16:19],v[32:35],v[16:19]
ds_read_b128 v[56:59],v2 offset:31744
v_mfma_f32_16x16x32_bf16 v[16:19],a[128:131],v[36:39],v[16:19]
v_mfma_f32_16x16x32_bf16 v[20:23],a[132:135],v[36:39],v[20:23]
ds_read_b128 v[60:63],v2 offset:46080
v_mfma_f32_16x16x32_bf16 v[20:23],a[20:23],v[32:35],v[20:23]
s_waitcnt lgkmcnt(4)
s_waitcnt vmcnt(3)
s_barrier
v_mfma_f32_16x16x32_bf16 v[20:23],a[244:247],v[40:43],v[20:23]
ds_read_b128 v[32:35],v2 offset:4096
s_mov_b32 m0,s74
v_mfma_f32_16x16x32_bf16 v[20:23],v[164:167],v[44:47],v[20:23]
buffer_load_dwordx4 v2,s[32:35],s71 offen offset:0 sc1 lds
v_mfma_f32_16x16x32_bf16 v[16:19],v[160:163],v[44:47],v[16:19]
ds_read_b128 v[36:39],v2 offset:18432
v_mfma_f32_16x16x32_bf16 v[16:19],a[240:243],v[40:43],v[16:19]
s_waitcnt lgkmcnt(4)
v_mfma_f32_16x16x32_bf16 v[16:19],a[24:27],v[48:51],v[16:19]
ds_read_b128 v[40:43],v2 offset:32768
v_mfma_f32_16x16x32_bf16 v[16:19],a[136:139],v[52:55],v[16:19]
buffer_load_dwordx4 v2,s[32:35],s71 offen offset:1024 sc1 lds
s_add_u32 s71,s71,1187840
v_mfma_f32_16x16x32_bf16 v[20:23],a[140:143],v[52:55],v[20:23]
ds_read_b128 v[44:47],v2 offset:47104
s_add_u32 m0,s64,57344
v_mfma_f32_16x16x32_bf16 v[20:23],a[28:31],v[48:51],v[20:23]
s_waitcnt lgkmcnt(4)
v_mfma_f32_16x16x32_bf16 v[20:23],a[252:255],v[56:59],v[20:23]
ds_read_b128 v[48:51],v2 offset:5120
v_mfma_f32_16x16x32_bf16 v[20:23],v[172:175],v[60:63],v[20:23]
buffer_load_dwordx4 v2,s[32:35],s63 offen offset:0 sc1 lds
v_mfma_f32_16x16x32_bf16 v[16:19],v[168:171],v[60:63],v[16:19]
ds_read_b128 v[52:55],v2 offset:19456
v_mfma_f32_16x16x32_bf16 v[16:19],a[248:251],v[56:59],v[16:19]
s_waitcnt lgkmcnt(4)
v_mfma_f32_16x16x32_bf16 v[16:19],a[32:35],v[32:35],v[16:19]
ds_read_b128 v[56:59],v2 offset:33792
v_mfma_f32_16x16x32_bf16 v[16:19],a[144:147],v[36:39],v[16:19]
buffer_load_dwordx4 v2,s[32:35],s63 offen offset:1024 sc1 lds
v_mfma_f32_16x16x32_bf16 v[20:23],a[148:151],v[36:39],v[20:23]
ds_read_b128 v[60:63],v2 offset:48128
v_mfma_f32_16x16x32_bf16 v[20:23],a[36:39],v[32:35],v[20:23]
s_waitcnt lgkmcnt(4)
v_mfma_f32_16x16x32_bf16 v[20:23],v[68:71],v[40:43],v[20:23]
ds_read_b128 v[32:35],v2 offset:6144
v_mfma_f32_16x16x32_bf16 v[20:23],v[180:183],v[44:47],v[20:23]
buffer_load_dwordx4 v2,s[32:35],s63 offen offset:2048 sc1 lds
v_mfma_f32_16x16x32_bf16 v[16:19],v[176:179],v[44:47],v[16:19]
ds_read_b128 v[36:39],v2 offset:20480
v_mfma_f32_16x16x32_bf16 v[16:19],v[64:67],v[40:43],v[16:19]
s_waitcnt lgkmcnt(4)
v_mfma_f32_16x16x32_bf16 v[16:19],a[40:43],v[48:51],v[16:19]
ds_read_b128 v[40:43],v2 offset:34816
v_mfma_f32_16x16x32_bf16 v[16:19],a[152:155],v[52:55],v[16:19]
buffer_load_dwordx4 v2,s[32:35],s63 offen offset:3072 sc1 lds
s_add_u32 s63,s63,4096
v_mfma_f32_16x16x32_bf16 v[20:23],a[156:159],v[52:55],v[20:23]
ds_read_b128 v[44:47],v2 offset:49152
s_add_u32 m0,s64,61440
v_mfma_f32_16x16x32_bf16 v[20:23],a[44:47],v[48:51],v[20:23]
s_waitcnt lgkmcnt(4)
v_mfma_f32_16x16x32_bf16 v[20:23],v[76:79],v[56:59],v[20:23]
ds_read_b128 v[48:51],v2 offset:7168
v_mfma_f32_16x16x32_bf16 v[20:23],v[188:191],v[60:63],v[20:23]
buffer_load_dwordx4 v2,s[32:35],s63 offen offset:0 sc1 lds
v_mfma_f32_16x16x32_bf16 v[16:19],v[184:187],v[60:63],v[16:19]
ds_read_b128 v[52:55],v2 offset:21504
v_mfma_f32_16x16x32_bf16 v[16:19],v[72:75],v[56:59],v[16:19]
s_waitcnt lgkmcnt(4)
v_mfma_f32_16x16x32_bf16 v[16:19],a[48:51],v[32:35],v[16:19]
ds_read_b128 v[56:59],v2 offset:35840
v_mfma_f32_16x16x32_bf16 v[16:19],a[160:163],v[36:39],v[16:19]
buffer_load_dwordx4 v2,s[32:35],s63 offen offset:1024 sc1 lds
v_mfma_f32_16x16x32_bf16 v[20:23],a[164:167],v[36:39],v[20:23]
ds_read_b128 v[60:63],v2 offset:50176
v_mfma_f32_16x16x32_bf16 v[20:23],a[52:55],v[32:35],v[20:23]
s_waitcnt lgkmcnt(4)
v_mfma_f32_16x16x32_bf16 v[20:23],v[84:87],v[40:43],v[20:23]
ds_read_b128 v[32:35],v2 offset:8192
v_mfma_f32_16x16x32_bf16 v[20:23],v[196:199],v[44:47],v[20:23]
buffer_load_dwordx4 v2,s[32:35],s63 offen offset:2048 sc1 lds
v_mfma_f32_16x16x32_bf16 v[16:19],v[192:195],v[44:47],v[16:19]
ds_read_b128 v[36:39],v2 offset:22528
v_mfma_f32_16x16x32_bf16 v[16:19],v[80:83],v[40:43],v[16:19]
s_waitcnt lgkmcnt(4)
v_mfma_f32_16x16x32_bf16 v[16:19],a[56:59],v[48:51],v[16:19]
ds_read_b128 v[40:43],v2 offset:36864
v_mfma_f32_16x16x32_bf16 v[16:19],a[168:171],v[52:55],v[16:19]
buffer_load_dwordx4 v2,s[32:35],s63 offen offset:3072 sc1 lds
v_mfma_f32_16x16x32_bf16 v[20:23],a[172:175],v[52:55],v[20:23]
ds_read_b128 v[44:47],v2 offset:51200
v_mfma_f32_16x16x32_bf16 v[20:23],a[60:63],v[48:51],v[20:23]
s_waitcnt lgkmcnt(4)
v_mfma_f32_16x16x32_bf16 v[20:23],v[92:95],v[56:59],v[20:23]
ds_read_b128 v[48:51],v2 offset:9216
v_mfma_f32_16x16x32_bf16 v[20:23],v[204:207],v[60:63],v[20:23]
s_add_u32 s63,s63,4096
s_add_u32 m0,s64,65536
v_mfma_f32_16x16x32_bf16 v[16:19],v[200:203],v[60:63],v[16:19]
ds_read_b128 v[52:55],v2 offset:23552
v_mfma_f32_16x16x32_bf16 v[16:19],v[88:91],v[56:59],v[16:19]
s_waitcnt lgkmcnt(4)
v_mfma_f32_16x16x32_bf16 v[16:19],a[64:67],v[32:35],v[16:19]
ds_read_b128 v[56:59],v2 offset:37888
v_mfma_f32_16x16x32_bf16 v[16:19],a[176:179],v[36:39],v[16:19]
buffer_load_dwordx4 v2,s[32:35],s63 offen offset:0 sc1 lds
v_mfma_f32_16x16x32_bf16 v[20:23],a[180:183],v[36:39],v[20:23]
ds_read_b128 v[60:63],v2 offset:52224
v_mfma_f32_16x16x32_bf16 v[20:23],a[68:71],v[32:35],v[20:23]
s_waitcnt lgkmcnt(4)
v_mfma_f32_16x16x32_bf16 v[20:23],v[100:103],v[40:43],v[20:23]
ds_read_b128 v[32:35],v2 offset:10240
v_mfma_f32_16x16x32_bf16 v[20:23],v[212:215],v[44:47],v[20:23]
buffer_load_dwordx4 v2,s[32:35],s63 offen offset:1024 sc1 lds
v_mfma_f32_16x16x32_bf16 v[16:19],v[208:211],v[44:47],v[16:19]
ds_read_b128 v[36:39],v2 offset:24576
v_mfma_f32_16x16x32_bf16 v[16:19],v[96:99],v[40:43],v[16:19]
s_waitcnt lgkmcnt(4)
v_mfma_f32_16x16x32_bf16 v[16:19],a[72:75],v[48:51],v[16:19]
ds_read_b128 v[40:43],v2 offset:38912
s_waitcnt vmcnt(10)
v_mfma_f32_16x16x32_bf16 v[16:19],a[184:187],v[52:55],v[16:19]
buffer_load_dwordx4 v2,s[32:35],s63 offen offset:2048 sc1 lds
ds_read_b128 v[12:15],v5 offset:0
v_mfma_f32_16x16x32_bf16 v[20:23],a[188:191],v[52:55],v[20:23]
ds_read_b128 v[44:47],v2 offset:53248
v_mfma_f32_16x16x32_bf16 v[20:23],a[76:79],v[48:51],v[20:23]
s_waitcnt lgkmcnt(5)
v_mfma_f32_16x16x32_bf16 v[20:23],v[108:111],v[56:59],v[20:23]
ds_read_b128 v[48:51],v2 offset:11264
v_mfma_f32_16x16x32_bf16 v[20:23],v[220:223],v[60:63],v[20:23]
v_mfma_f32_16x16x32_bf16 v[16:19],v[216:219],v[60:63],v[16:19]
ds_read_b128 v[52:55],v2 offset:25600
s_waitcnt lgkmcnt(3)
v_mfma_f32_16x16x32_bf16 v[16:19],v[104:107],v[56:59],v[16:19]
v_add_f32 v24,v24,v12
v_mfma_f32_16x16x32_bf16 v[16:19],a[80:83],v[32:35],v[16:19]
ds_read_b128 v[56:59],v2 offset:39936
v_add_f32 v25,v25,v13
v_mfma_f32_16x16x32_bf16 v[16:19],a[192:195],v[36:39],v[16:19]
buffer_load_dwordx4 v2,s[32:35],s63 offen offset:3072 sc1 lds
s_add_u32 s63,s63,4096
v_mfma_f32_16x16x32_bf16 v[20:23],a[196:199],v[36:39],v[20:23]
ds_read_b128 v[60:63],v2 offset:54272
s_add_u32 m0,s64,69632
v_mfma_f32_16x16x32_bf16 v[20:23],a[84:87],v[32:35],v[20:23]
v_add_f32 v26,v26,v14
s_waitcnt lgkmcnt(4)
v_mfma_f32_16x16x32_bf16 v[20:23],v[116:119],v[40:43],v[20:23]
ds_read_b128 v[32:35],v2 offset:12288
v_add_f32 v27,v27,v15
v_mfma_f32_16x16x32_bf16 v[20:23],v[228:231],v[44:47],v[20:23]
buffer_load_dwordx4 v2,s[32:35],s63 offen offset:0 sc1 lds
ds_read_b128 v[12:15],v5 offset:1024
v_mfma_f32_16x16x32_bf16 v[16:19],v[224:227],v[44:47],v[16:19]
ds_read_b128 v[36:39],v2 offset:26624
v_mfma_f32_16x16x32_bf16 v[16:19],v[112:115],v[40:43],v[16:19]
s_waitcnt lgkmcnt(5)
v_mfma_f32_16x16x32_bf16 v[16:19],a[88:91],v[48:51],v[16:19]
ds_read_b128 v[40:43],v2 offset:40960
v_mfma_f32_16x16x32_bf16 v[16:19],a[200:203],v[52:55],v[16:19]
buffer_load_dwordx4 v2,s[32:35],s63 offen offset:1024 sc1 lds
s_add_u32 s63,s63,1175552
v_mfma_f32_16x16x32_bf16 v[20:23],a[204:207],v[52:55],v[20:23]
ds_read_b128 v[44:47],v2 offset:55296
s_waitcnt lgkmcnt(3)
v_mfma_f32_16x16x32_bf16 v[20:23],a[92:95],v[48:51],v[20:23]
v_add_f32 v28,v28,v12
v_mfma_f32_16x16x32_bf16 v[20:23],v[124:127],v[56:59],v[20:23]
ds_read_b128 v[48:51],v2 offset:13312
v_add_f32 v29,v29,v13
v_mfma_f32_16x16x32_bf16 v[20:23],v[236:239],v[60:63],v[20:23]
v_add_f32 v30,v30,v14
v_add_f32 v31,v31,v15
v_mfma_f32_16x16x32_bf16 v[16:19],v[232:235],v[60:63],v[16:19]
ds_read_b128 v[52:55],v2 offset:27648
v_cvt_pk_bf16_f32 v12,v24,v25
v_mfma_f32_16x16x32_bf16 v[16:19],v[120:123],v[56:59],v[16:19]
v_cvt_pk_bf16_f32 v13,v26,v27
s_waitcnt lgkmcnt(4)
v_mfma_f32_16x16x32_bf16 v[16:19],a[96:99],v[32:35],v[16:19]
ds_read_b128 v[56:59],v2 offset:41984
v_cvt_pk_bf16_f32 v14,v28,v29
v_mfma_f32_16x16x32_bf16 v[16:19],a[208:211],v[36:39],v[16:19]
v_cvt_pk_bf16_f32 v15,v30,v31
v_mfma_f32_16x16x32_bf16 v[20:23],a[212:215],v[36:39],v[20:23]
ds_read_b128 v[60:63],v2 offset:56320
v_mfma_f32_16x16x32_bf16 v[20:23],a[100:103],v[32:35],v[20:23]
s_waitcnt lgkmcnt(4)
s_waitcnt vmcnt(10)
s_barrier
v_mfma_f32_16x16x32_bf16 v[20:23],v[132:135],v[40:43],v[20:23]
ds_read_b128 v[32:35],v4 offset:0
v_mfma_f32_16x16x32_bf16 v[20:23],v[244:247],v[44:47],v[20:23]
buffer_store_dwordx4 v[12:15],v2,s[32:35],s68 offen sc1
v_mfma_f32_16x16x32_bf16 v[16:19],v[240:243],v[44:47],v[16:19]
ds_read_b128 v[36:39],v4 offset:14336
v_mfma_f32_16x16x32_bf16 v[16:19],v[128:131],v[40:43],v[16:19]
s_waitcnt lgkmcnt(4)
v_mfma_f32_16x16x32_bf16 v[16:19],a[104:107],v[48:51],v[16:19]
ds_read_b128 v[40:43],v4 offset:28672
v_mfma_f32_16x16x32_bf16 v[16:19],a[216:219],v[52:55],v[16:19]
buffer_store_dwordx2 v[8:9],v2,s[92:95],s67 idxen sc1
v_mfma_f32_16x16x32_bf16 v[20:23],a[220:223],v[52:55],v[20:23]
ds_read_b128 v[44:47],v4 offset:43008
s_mov_b32 s67,s68
v_mfma_f32_16x16x32_bf16 v[20:23],a[108:111],v[48:51],v[20:23]
s_mov_b32 s68,s70
s_waitcnt lgkmcnt(4)
v_mfma_f32_16x16x32_bf16 v[20:23],v[140:143],v[56:59],v[20:23]
ds_read_b128 v[48:51],v4 offset:1024
s_add_u32 s70,s70,1187840
v_mfma_f32_16x16x32_bf16 v[20:23],v[252:255],v[60:63],v[20:23]
v_mfma_f32_16x16x32_bf16 v[16:19],v[248:251],v[60:63],v[16:19]
ds_read_b128 v[52:55],v4 offset:15360
s_sub_u32 s62,s62,1
v_mfma_f32_16x16x32_bf16 v[16:19],v[136:139],v[56:59],v[16:19]
s_cbranch_scc1 .L_dab_0
s_waitcnt lgkmcnt(4)
v_mfma_f32_16x16x32_bf16 v[24:27],a[0:3],v[32:35],0
ds_read_b128 v[56:59],v4 offset:29696
v_dot2_f32_bf16 v10,v12,s72,0
v_mfma_f32_16x16x32_bf16 v[24:27],a[112:115],v[36:39],v[24:27]
v_dot2_f32_bf16 v11,v12,v12,0
v_dot2_f32_bf16 v10,v13,s72,v10
v_mfma_f32_16x16x32_bf16 v[28:31],a[116:119],v[36:39],0
ds_read_b128 v[60:63],v4 offset:44032
v_dot2_f32_bf16 v11,v13,v13,v11
v_mfma_f32_16x16x32_bf16 v[28:31],a[4:7],v[32:35],v[28:31]
v_dot2_f32_bf16 v10,v14,s72,v10
s_waitcnt lgkmcnt(4)
v_mfma_f32_16x16x32_bf16 v[28:31],a[228:231],v[40:43],v[28:31]
ds_read_b128 v[32:35],v4 offset:2048
v_dot2_f32_bf16 v11,v14,v14,v11
v_mfma_f32_16x16x32_bf16 v[28:31],v[148:151],v[44:47],v[28:31]
v_dot2_f32_bf16 v10,v15,s72,v10
v_dot2_f32_bf16 v11,v15,v15,v11
v_mfma_f32_16x16x32_bf16 v[24:27],v[144:147],v[44:47],v[24:27]
ds_read_b128 v[36:39],v4 offset:16384
v_mfma_f32_16x16x32_bf16 v[24:27],a[224:227],v[40:43],v[24:27]
v_permlane16_swap_b32 v10,v11
s_waitcnt lgkmcnt(4)
v_mfma_f32_16x16x32_bf16 v[24:27],a[8:11],v[48:51],v[24:27]
ds_read_b128 v[40:43],v4 offset:30720
v_add_f32 v10,v10,v11
v_mfma_f32_16x16x32_bf16 v[24:27],a[120:123],v[52:55],v[24:27]
v_mfma_f32_16x16x32_bf16 v[28:31],a[124:127],v[52:55],v[28:31]
ds_read_b128 v[44:47],v4 offset:45056
v_permlane32_swap_b32 v10,v11
v_mfma_f32_16x16x32_bf16 v[28:31],a[12:15],v[48:51],v[28:31]
v_add_f32 v10,v10,v11
s_waitcnt lgkmcnt(4)
v_mfma_f32_16x16x32_bf16 v[28:31],a[236:239],v[56:59],v[28:31]
ds_read_b128 v[48:51],v4 offset:3072
v_mfma_f32_16x16x32_bf16 v[28:31],v[156:159],v[60:63],v[28:31]
buffer_store_dword v10,v3,s[32:35],s67 offen offset:1024 sc1
v_mfma_f32_16x16x32_bf16 v[24:27],v[152:155],v[60:63],v[24:27]
ds_read_b128 v[52:55],v4 offset:17408
v_mfma_f32_16x16x32_bf16 v[24:27],a[232:235],v[56:59],v[24:27]
s_waitcnt lgkmcnt(4)
v_mfma_f32_16x16x32_bf16 v[24:27],a[16:19],v[32:35],v[24:27]
ds_read_b128 v[56:59],v4 offset:31744
v_mfma_f32_16x16x32_bf16 v[24:27],a[128:131],v[36:39],v[24:27]
v_mfma_f32_16x16x32_bf16 v[28:31],a[132:135],v[36:39],v[28:31]
ds_read_b128 v[60:63],v4 offset:46080
v_mfma_f32_16x16x32_bf16 v[28:31],a[20:23],v[32:35],v[28:31]
s_waitcnt lgkmcnt(4)
s_waitcnt vmcnt(3)
s_barrier
v_mfma_f32_16x16x32_bf16 v[28:31],a[244:247],v[40:43],v[28:31]
ds_read_b128 v[32:35],v4 offset:4096
s_mov_b32 m0,s74
v_mfma_f32_16x16x32_bf16 v[28:31],v[164:167],v[44:47],v[28:31]
buffer_load_dwordx4 v2,s[32:35],s71 offen offset:0 sc1 lds
v_mfma_f32_16x16x32_bf16 v[24:27],v[160:163],v[44:47],v[24:27]
ds_read_b128 v[36:39],v4 offset:18432
v_mfma_f32_16x16x32_bf16 v[24:27],a[240:243],v[40:43],v[24:27]
s_waitcnt lgkmcnt(4)
v_mfma_f32_16x16x32_bf16 v[24:27],a[24:27],v[48:51],v[24:27]
ds_read_b128 v[40:43],v4 offset:32768
v_mfma_f32_16x16x32_bf16 v[24:27],a[136:139],v[52:55],v[24:27]
buffer_load_dwordx4 v2,s[32:35],s71 offen offset:1024 sc1 lds
s_add_u32 s71,s71,1187840
v_mfma_f32_16x16x32_bf16 v[28:31],a[140:143],v[52:55],v[28:31]
ds_read_b128 v[44:47],v4 offset:47104
s_add_u32 m0,s64,0
v_mfma_f32_16x16x32_bf16 v[28:31],a[28:31],v[48:51],v[28:31]
s_waitcnt lgkmcnt(4)
v_mfma_f32_16x16x32_bf16 v[28:31],a[252:255],v[56:59],v[28:31]
ds_read_b128 v[48:51],v4 offset:5120
v_mfma_f32_16x16x32_bf16 v[28:31],v[172:175],v[60:63],v[28:31]
buffer_load_dwordx4 v2,s[32:35],s63 offen offset:0 sc1 lds
v_mfma_f32_16x16x32_bf16 v[24:27],v[168:171],v[60:63],v[24:27]
ds_read_b128 v[52:55],v4 offset:19456
v_mfma_f32_16x16x32_bf16 v[24:27],a[248:251],v[56:59],v[24:27]
s_waitcnt lgkmcnt(4)
v_mfma_f32_16x16x32_bf16 v[24:27],a[32:35],v[32:35],v[24:27]
ds_read_b128 v[56:59],v4 offset:33792
v_mfma_f32_16x16x32_bf16 v[24:27],a[144:147],v[36:39],v[24:27]
buffer_load_dwordx4 v2,s[32:35],s63 offen offset:1024 sc1 lds
v_mfma_f32_16x16x32_bf16 v[28:31],a[148:151],v[36:39],v[28:31]
ds_read_b128 v[60:63],v4 offset:48128
v_mfma_f32_16x16x32_bf16 v[28:31],a[36:39],v[32:35],v[28:31]
s_waitcnt lgkmcnt(4)
v_mfma_f32_16x16x32_bf16 v[28:31],v[68:71],v[40:43],v[28:31]
ds_read_b128 v[32:35],v4 offset:6144
v_mfma_f32_16x16x32_bf16 v[28:31],v[180:183],v[44:47],v[28:31]
buffer_load_dwordx4 v2,s[32:35],s63 offen offset:2048 sc1 lds
v_mfma_f32_16x16x32_bf16 v[24:27],v[176:179],v[44:47],v[24:27]
ds_read_b128 v[36:39],v4 offset:20480
v_mfma_f32_16x16x32_bf16 v[24:27],v[64:67],v[40:43],v[24:27]
s_waitcnt lgkmcnt(4)
v_mfma_f32_16x16x32_bf16 v[24:27],a[40:43],v[48:51],v[24:27]
ds_read_b128 v[40:43],v4 offset:34816
v_mfma_f32_16x16x32_bf16 v[24:27],a[152:155],v[52:55],v[24:27]
buffer_load_dwordx4 v2,s[32:35],s63 offen offset:3072 sc1 lds
s_add_u32 s63,s63,4096
v_mfma_f32_16x16x32_bf16 v[28:31],a[156:159],v[52:55],v[28:31]
ds_read_b128 v[44:47],v4 offset:49152
s_add_u32 m0,s64,4096
v_mfma_f32_16x16x32_bf16 v[28:31],a[44:47],v[48:51],v[28:31]
s_waitcnt lgkmcnt(4)
v_mfma_f32_16x16x32_bf16 v[28:31],v[76:79],v[56:59],v[28:31]
ds_read_b128 v[48:51],v4 offset:7168
v_mfma_f32_16x16x32_bf16 v[28:31],v[188:191],v[60:63],v[28:31]
buffer_load_dwordx4 v2,s[32:35],s63 offen offset:0 sc1 lds
v_mfma_f32_16x16x32_bf16 v[24:27],v[184:187],v[60:63],v[24:27]
ds_read_b128 v[52:55],v4 offset:21504
v_mfma_f32_16x16x32_bf16 v[24:27],v[72:75],v[56:59],v[24:27]
s_waitcnt lgkmcnt(4)
v_mfma_f32_16x16x32_bf16 v[24:27],a[48:51],v[32:35],v[24:27]
ds_read_b128 v[56:59],v4 offset:35840
v_mfma_f32_16x16x32_bf16 v[24:27],a[160:163],v[36:39],v[24:27]
buffer_load_dwordx4 v2,s[32:35],s63 offen offset:1024 sc1 lds
v_mfma_f32_16x16x32_bf16 v[28:31],a[164:167],v[36:39],v[28:31]
ds_read_b128 v[60:63],v4 offset:50176
v_mfma_f32_16x16x32_bf16 v[28:31],a[52:55],v[32:35],v[28:31]
s_waitcnt lgkmcnt(4)
v_mfma_f32_16x16x32_bf16 v[28:31],v[84:87],v[40:43],v[28:31]
ds_read_b128 v[32:35],v4 offset:8192
v_mfma_f32_16x16x32_bf16 v[28:31],v[196:199],v[44:47],v[28:31]
buffer_load_dwordx4 v2,s[32:35],s63 offen offset:2048 sc1 lds
v_mfma_f32_16x16x32_bf16 v[24:27],v[192:195],v[44:47],v[24:27]
ds_read_b128 v[36:39],v4 offset:22528
v_mfma_f32_16x16x32_bf16 v[24:27],v[80:83],v[40:43],v[24:27]
s_waitcnt lgkmcnt(4)
v_mfma_f32_16x16x32_bf16 v[24:27],a[56:59],v[48:51],v[24:27]
ds_read_b128 v[40:43],v4 offset:36864
v_mfma_f32_16x16x32_bf16 v[24:27],a[168:171],v[52:55],v[24:27]
buffer_load_dwordx4 v2,s[32:35],s63 offen offset:3072 sc1 lds
v_mfma_f32_16x16x32_bf16 v[28:31],a[172:175],v[52:55],v[28:31]
ds_read_b128 v[44:47],v4 offset:51200
v_mfma_f32_16x16x32_bf16 v[28:31],a[60:63],v[48:51],v[28:31]
s_waitcnt lgkmcnt(4)
v_mfma_f32_16x16x32_bf16 v[28:31],v[92:95],v[56:59],v[28:31]
ds_read_b128 v[48:51],v4 offset:9216
v_mfma_f32_16x16x32_bf16 v[28:31],v[204:207],v[60:63],v[28:31]
s_add_u32 s63,s63,4096
s_add_u32 m0,s64,8192
v_mfma_f32_16x16x32_bf16 v[24:27],v[200:203],v[60:63],v[24:27]
ds_read_b128 v[52:55],v4 offset:23552
v_mfma_f32_16x16x32_bf16 v[24:27],v[88:91],v[56:59],v[24:27]
s_waitcnt lgkmcnt(4)
v_mfma_f32_16x16x32_bf16 v[24:27],a[64:67],v[32:35],v[24:27]
ds_read_b128 v[56:59],v4 offset:37888
v_mfma_f32_16x16x32_bf16 v[24:27],a[176:179],v[36:39],v[24:27]
buffer_load_dwordx4 v2,s[32:35],s63 offen offset:0 sc1 lds
v_mfma_f32_16x16x32_bf16 v[28:31],a[180:183],v[36:39],v[28:31]
ds_read_b128 v[60:63],v4 offset:52224
v_mfma_f32_16x16x32_bf16 v[28:31],a[68:71],v[32:35],v[28:31]
s_waitcnt lgkmcnt(4)
v_mfma_f32_16x16x32_bf16 v[28:31],v[100:103],v[40:43],v[28:31]
ds_read_b128 v[32:35],v4 offset:10240
v_mfma_f32_16x16x32_bf16 v[28:31],v[212:215],v[44:47],v[28:31]
buffer_load_dwordx4 v2,s[32:35],s63 offen offset:1024 sc1 lds
v_mfma_f32_16x16x32_bf16 v[24:27],v[208:211],v[44:47],v[24:27]
ds_read_b128 v[36:39],v4 offset:24576
v_mfma_f32_16x16x32_bf16 v[24:27],v[96:99],v[40:43],v[24:27]
s_waitcnt lgkmcnt(4)
v_mfma_f32_16x16x32_bf16 v[24:27],a[72:75],v[48:51],v[24:27]
ds_read_b128 v[40:43],v4 offset:38912
s_waitcnt vmcnt(10)
v_mfma_f32_16x16x32_bf16 v[24:27],a[184:187],v[52:55],v[24:27]
buffer_load_dwordx4 v2,s[32:35],s63 offen offset:2048 sc1 lds
ds_read_b128 v[12:15],v5 offset:0
v_mfma_f32_16x16x32_bf16 v[28:31],a[188:191],v[52:55],v[28:31]
ds_read_b128 v[44:47],v4 offset:53248
v_mfma_f32_16x16x32_bf16 v[28:31],a[76:79],v[48:51],v[28:31]
s_waitcnt lgkmcnt(5)
v_mfma_f32_16x16x32_bf16 v[28:31],v[108:111],v[56:59],v[28:31]
ds_read_b128 v[48:51],v4 offset:11264
v_mfma_f32_16x16x32_bf16 v[28:31],v[220:223],v[60:63],v[28:31]
v_mfma_f32_16x16x32_bf16 v[24:27],v[216:219],v[60:63],v[24:27]
ds_read_b128 v[52:55],v4 offset:25600
s_waitcnt lgkmcnt(3)
v_mfma_f32_16x16x32_bf16 v[24:27],v[104:107],v[56:59],v[24:27]
v_add_f32 v16,v16,v12
v_mfma_f32_16x16x32_bf16 v[24:27],a[80:83],v[32:35],v[24:27]
ds_read_b128 v[56:59],v4 offset:39936
v_add_f32 v17,v17,v13
v_mfma_f32_16x16x32_bf16 v[24:27],a[192:195],v[36:39],v[24:27]
buffer_load_dwordx4 v2,s[32:35],s63 offen offset:3072 sc1 lds
s_add_u32 s63,s63,4096
v_mfma_f32_16x16x32_bf16 v[28:31],a[196:199],v[36:39],v[28:31]
ds_read_b128 v[60:63],v4 offset:54272
s_add_u32 m0,s64,12288
v_mfma_f32_16x16x32_bf16 v[28:31],a[84:87],v[32:35],v[28:31]
v_add_f32 v18,v18,v14
s_waitcnt lgkmcnt(4)
v_mfma_f32_16x16x32_bf16 v[28:31],v[116:119],v[40:43],v[28:31]
ds_read_b128 v[32:35],v4 offset:12288
v_add_f32 v19,v19,v15
v_mfma_f32_16x16x32_bf16 v[28:31],v[228:231],v[44:47],v[28:31]
buffer_load_dwordx4 v2,s[32:35],s63 offen offset:0 sc1 lds
ds_read_b128 v[12:15],v5 offset:1024
v_mfma_f32_16x16x32_bf16 v[24:27],v[224:227],v[44:47],v[24:27]
ds_read_b128 v[36:39],v4 offset:26624
v_mfma_f32_16x16x32_bf16 v[24:27],v[112:115],v[40:43],v[24:27]
s_waitcnt lgkmcnt(5)
v_mfma_f32_16x16x32_bf16 v[24:27],a[88:91],v[48:51],v[24:27]
ds_read_b128 v[40:43],v4 offset:40960
v_mfma_f32_16x16x32_bf16 v[24:27],a[200:203],v[52:55],v[24:27]
buffer_load_dwordx4 v2,s[32:35],s63 offen offset:1024 sc1 lds
s_add_u32 s63,s63,1175552
v_mfma_f32_16x16x32_bf16 v[28:31],a[204:207],v[52:55],v[28:31]
ds_read_b128 v[44:47],v4 offset:55296
s_waitcnt lgkmcnt(3)
v_mfma_f32_16x16x32_bf16 v[28:31],a[92:95],v[48:51],v[28:31]
v_add_f32 v20,v20,v12
v_mfma_f32_16x16x32_bf16 v[28:31],v[124:127],v[56:59],v[28:31]
ds_read_b128 v[48:51],v4 offset:13312
v_add_f32 v21,v21,v13
v_mfma_f32_16x16x32_bf16 v[28:31],v[236:239],v[60:63],v[28:31]
v_add_f32 v22,v22,v14
v_add_f32 v23,v23,v15
v_mfma_f32_16x16x32_bf16 v[24:27],v[232:235],v[60:63],v[24:27]
ds_read_b128 v[52:55],v4 offset:27648
v_cvt_pk_bf16_f32 v12,v16,v17
v_mfma_f32_16x16x32_bf16 v[24:27],v[120:123],v[56:59],v[24:27]
v_cvt_pk_bf16_f32 v13,v18,v19
s_waitcnt lgkmcnt(4)
v_mfma_f32_16x16x32_bf16 v[24:27],a[96:99],v[32:35],v[24:27]
ds_read_b128 v[56:59],v4 offset:41984
v_cvt_pk_bf16_f32 v14,v20,v21
v_mfma_f32_16x16x32_bf16 v[24:27],a[208:211],v[36:39],v[24:27]
v_cvt_pk_bf16_f32 v15,v22,v23
v_mfma_f32_16x16x32_bf16 v[28:31],a[212:215],v[36:39],v[28:31]
ds_read_b128 v[60:63],v4 offset:56320
v_mfma_f32_16x16x32_bf16 v[28:31],a[100:103],v[32:35],v[28:31]
s_waitcnt lgkmcnt(4)
s_waitcnt vmcnt(10)
s_barrier
v_mfma_f32_16x16x32_bf16 v[28:31],v[132:135],v[40:43],v[28:31]
ds_read_b128 v[32:35],v2 offset:0
v_mfma_f32_16x16x32_bf16 v[28:31],v[244:247],v[44:47],v[28:31]
buffer_store_dwordx4 v[12:15],v2,s[32:35],s69 offen sc1
v_mfma_f32_16x16x32_bf16 v[24:27],v[240:243],v[44:47],v[24:27]
ds_read_b128 v[36:39],v2 offset:14336
v_mfma_f32_16x16x32_bf16 v[24:27],v[128:131],v[40:43],v[24:27]
s_waitcnt lgkmcnt(4)
v_mfma_f32_16x16x32_bf16 v[24:27],a[104:107],v[48:51],v[24:27]
ds_read_b128 v[40:43],v2 offset:28672
v_mfma_f32_16x16x32_bf16 v[24:27],a[216:219],v[52:55],v[24:27]
buffer_store_dwordx2 v[8:9],v2,s[92:95],s67 idxen sc1
v_mfma_f32_16x16x32_bf16 v[28:31],a[220:223],v[52:55],v[28:31]
ds_read_b128 v[44:47],v2 offset:43008
s_mov_b32 s67,s69
v_mfma_f32_16x16x32_bf16 v[28:31],a[108:111],v[48:51],v[28:31]
s_mov_b32 s69,s70
s_waitcnt lgkmcnt(4)
v_mfma_f32_16x16x32_bf16 v[28:31],v[140:143],v[56:59],v[28:31]
ds_read_b128 v[48:51],v2 offset:1024
s_add_u32 s70,s70,1187840
v_mfma_f32_16x16x32_bf16 v[28:31],v[252:255],v[60:63],v[28:31]
v_mfma_f32_16x16x32_bf16 v[24:27],v[248:251],v[60:63],v[24:27]
ds_read_b128 v[52:55],v2 offset:15360
v_mfma_f32_16x16x32_bf16 v[24:27],v[136:139],v[56:59],v[24:27]
s_branch .L_dal_0
.L_dab_0:
v_dot2_f32_bf16 v10,v12,s72,0
v_dot2_f32_bf16 v11,v12,v12,0
v_dot2_f32_bf16 v10,v13,s72,v10
v_dot2_f32_bf16 v11,v13,v13,v11
v_dot2_f32_bf16 v10,v14,s72,v10
v_dot2_f32_bf16 v11,v14,v14,v11
v_dot2_f32_bf16 v10,v15,s72,v10
v_dot2_f32_bf16 v11,v15,v15,v11
s_nop 2
v_permlane16_swap_b32 v10,v11
v_add_f32 v10,v10,v11
s_nop 1
v_permlane32_swap_b32 v10,v11
v_add_f32 v10,v10,v11
buffer_store_dword v10,v3,s[32:35],s67 offen offset:1024 sc1
s_mov_b32 s62,15
s_add_u32 s74,s74,8192
v_lshrrev_b32 v5,8,v2
v_lshl_add_u32 v5,v5,4,s74
v_and_b32 v3,128,v2
)#"
R"#(
v_lshl_add_u32 v5,v3,2,v5
v_and_b32 v3,16,v2
v_lshl_add_u32 v5,v3,4,v5
v_and_b32 v3,96,v2
v_lshl_add_u32 v5,v3,1,v5
v_lshrrev_b32 v3,2,v2
v_add_u32 v3,s74,v3
.p2align 6
.L_dbl_0:
s_waitcnt lgkmcnt(4)
v_mfma_f32_16x16x32_bf16 v[24:27],a[0:3],v[32:35],0
ds_read_b128 v[56:59],v4 offset:29696
v_mfma_f32_16x16x32_bf16 v[24:27],a[112:115],v[36:39],v[24:27]
v_mfma_f32_16x16x32_bf16 v[28:31],a[116:119],v[36:39],0
ds_read_b128 v[60:63],v4 offset:44032
v_mfma_f32_16x16x32_bf16 v[28:31],a[4:7],v[32:35],v[28:31]
v_cvt_pk_bf16_f32 v15,v22,v23
s_waitcnt lgkmcnt(4)
v_mfma_f32_16x16x32_bf16 v[28:31],a[228:231],v[40:43],v[28:31]
ds_read_b128 v[32:35],v4 offset:2048
v_cvt_pk_bf16_f32 v14,v20,v21
v_mfma_f32_16x16x32_bf16 v[28:31],v[148:151],v[44:47],v[28:31]
v_cvt_pk_bf16_f32 v13,v18,v19
v_cvt_pk_bf16_f32 v12,v16,v17
v_mfma_f32_16x16x32_bf16 v[24:27],v[144:147],v[44:47],v[24:27]
ds_read_b128 v[36:39],v4 offset:16384
ds_write_b128 v5,v[12:15]
v_mfma_f32_16x16x32_bf16 v[24:27],a[224:227],v[40:43],v[24:27]
ds_read2st64_b32 v[12:13],v3 offset0:0 offset1:1
s_waitcnt lgkmcnt(6)
v_mfma_f32_16x16x32_bf16 v[24:27],a[8:11],v[48:51],v[24:27]
ds_read_b128 v[40:43],v4 offset:30720
ds_read2st64_b32 v[14:15],v3 offset0:2 offset1:3
v_mfma_f32_16x16x32_bf16 v[24:27],a[120:123],v[52:55],v[24:27]
v_mfma_f32_16x16x32_bf16 v[28:31],a[124:127],v[52:55],v[28:31]
ds_read_b128 v[44:47],v4 offset:45056
v_mfma_f32_16x16x32_bf16 v[28:31],a[12:15],v[48:51],v[28:31]
s_waitcnt lgkmcnt(7)
v_mfma_f32_16x16x32_bf16 v[28:31],a[236:239],v[56:59],v[28:31]
ds_read_b128 v[48:51],v4 offset:3072
v_mfma_f32_16x16x32_bf16 v[28:31],v[156:159],v[60:63],v[28:31]
s_waitcnt lgkmcnt(2)
buffer_atomic_pk_add_bf16 v12,v1,s[44:47],s65 offen offset:0 sc1
v_mfma_f32_16x16x32_bf16 v[24:27],v[152:155],v[60:63],v[24:27]
ds_read_b128 v[52:55],v4 offset:17408
v_mfma_f32_16x16x32_bf16 v[24:27],a[232:235],v[56:59],v[24:27]
buffer_atomic_pk_add_bf16 v13,v1,s[44:47],s65 offen offset:2048 sc1
v_mfma_f32_16x16x32_bf16 v[24:27],a[16:19],v[32:35],v[24:27]
ds_read_b128 v[56:59],v4 offset:31744
s_add_u32 s65,s65,16384
v_mfma_f32_16x16x32_bf16 v[24:27],a[128:131],v[36:39],v[24:27]
buffer_atomic_pk_add_bf16 v14,v1,s[44:47],s65 offen offset:0 sc1
v_mfma_f32_16x16x32_bf16 v[28:31],a[132:135],v[36:39],v[28:31]
ds_read_b128 v[60:63],v4 offset:46080
v_mfma_f32_16x16x32_bf16 v[28:31],a[20:23],v[32:35],v[28:31]
s_waitcnt lgkmcnt(4)
s_waitcnt vmcnt(3)
s_barrier
v_mfma_f32_16x16x32_bf16 v[28:31],a[244:247],v[40:43],v[28:31]
ds_read_b128 v[32:35],v4 offset:4096
s_add_u32 m0,s64,0
v_mfma_f32_16x16x32_bf16 v[28:31],v[164:167],v[44:47],v[28:31]
buffer_atomic_pk_add_bf16 v15,v1,s[44:47],s65 offen offset:2048 sc1
s_add_u32 s65,s65,16384
v_mfma_f32_16x16x32_bf16 v[24:27],v[160:163],v[44:47],v[24:27]
ds_read_b128 v[36:39],v4 offset:18432
v_mfma_f32_16x16x32_bf16 v[24:27],a[240:243],v[40:43],v[24:27]
s_waitcnt lgkmcnt(4)
v_mfma_f32_16x16x32_bf16 v[24:27],a[24:27],v[48:51],v[24:27]
ds_read_b128 v[40:43],v4 offset:32768
v_mfma_f32_16x16x32_bf16 v[24:27],a[136:139],v[52:55],v[24:27]
buffer_load_dwordx4 v2,s[32:35],s63 offen offset:0 sc1 lds
v_mfma_f32_16x16x32_bf16 v[28:31],a[140:143],v[52:55],v[28:31]
ds_read_b128 v[44:47],v4 offset:47104
v_mfma_f32_16x16x32_bf16 v[28:31],a[28:31],v[48:51],v[28:31]
s_waitcnt lgkmcnt(4)
v_mfma_f32_16x16x32_bf16 v[28:31],a[252:255],v[56:59],v[28:31]
ds_read_b128 v[48:51],v4 offset:5120
v_mfma_f32_16x16x32_bf16 v[28:31],v[172:175],v[60:63],v[28:31]
buffer_load_dwordx4 v2,s[32:35],s63 offen offset:1024 sc1 lds
v_mfma_f32_16x16x32_bf16 v[24:27],v[168:171],v[60:63],v[24:27]
ds_read_b128 v[52:55],v4 offset:19456
v_mfma_f32_16x16x32_bf16 v[24:27],a[248:251],v[56:59],v[24:27]
s_waitcnt lgkmcnt(4)
v_mfma_f32_16x16x32_bf16 v[24:27],a[32:35],v[32:35],v[24:27]
ds_read_b128 v[56:59],v4 offset:33792
v_mfma_f32_16x16x32_bf16 v[24:27],a[144:147],v[36:39],v[24:27]
buffer_load_dwordx4 v2,s[32:35],s63 offen offset:2048 sc1 lds
v_mfma_f32_16x16x32_bf16 v[28:31],a[148:151],v[36:39],v[28:31]
ds_read_b128 v[60:63],v4 offset:48128
v_mfma_f32_16x16x32_bf16 v[28:31],a[36:39],v[32:35],v[28:31]
s_waitcnt lgkmcnt(4)
v_mfma_f32_16x16x32_bf16 v[28:31],v[68:71],v[40:43],v[28:31]
ds_read_b128 v[32:35],v4 offset:6144
v_mfma_f32_16x16x32_bf16 v[28:31],v[180:183],v[44:47],v[28:31]
buffer_load_dwordx4 v2,s[32:35],s63 offen offset:3072 sc1 lds
v_mfma_f32_16x16x32_bf16 v[24:27],v[176:179],v[44:47],v[24:27]
ds_read_b128 v[36:39],v4 offset:20480
v_mfma_f32_16x16x32_bf16 v[24:27],v[64:67],v[40:43],v[24:27]
s_waitcnt lgkmcnt(4)
v_mfma_f32_16x16x32_bf16 v[24:27],a[40:43],v[48:51],v[24:27]
ds_read_b128 v[40:43],v4 offset:34816
s_add_u32 s63,s63,4096
v_mfma_f32_16x16x32_bf16 v[24:27],a[152:155],v[52:55],v[24:27]
s_add_u32 m0,s64,4096
v_mfma_f32_16x16x32_bf16 v[28:31],a[156:159],v[52:55],v[28:31]
ds_read_b128 v[44:47],v4 offset:49152
v_mfma_f32_16x16x32_bf16 v[28:31],a[44:47],v[48:51],v[28:31]
s_waitcnt lgkmcnt(4)
v_mfma_f32_16x16x32_bf16 v[28:31],v[76:79],v[56:59],v[28:31]
ds_read_b128 v[48:51],v4 offset:7168
v_mfma_f32_16x16x32_bf16 v[28:31],v[188:191],v[60:63],v[28:31]
buffer_load_dwordx4 v2,s[32:35],s63 offen offset:0 sc1 lds
v_mfma_f32_16x16x32_bf16 v[24:27],v[184:187],v[60:63],v[24:27]
ds_read_b128 v[52:55],v4 offset:21504
v_mfma_f32_16x16x32_bf16 v[24:27],v[72:75],v[56:59],v[24:27]
s_waitcnt lgkmcnt(4)
v_mfma_f32_16x16x32_bf16 v[24:27],a[48:51],v[32:35],v[24:27]
ds_read_b128 v[56:59],v4 offset:35840
v_mfma_f32_16x16x32_bf16 v[24:27],a[160:163],v[36:39],v[24:27]
buffer_load_dwordx4 v2,s[32:35],s63 offen offset:1024 sc1 lds
v_mfma_f32_16x16x32_bf16 v[28:31],a[164:167],v[36:39],v[28:31]
ds_read_b128 v[60:63],v4 offset:50176
v_mfma_f32_16x16x32_bf16 v[28:31],a[52:55],v[32:35],v[28:31]
s_waitcnt lgkmcnt(4)
v_mfma_f32_16x16x32_bf16 v[28:31],v[84:87],v[40:43],v[28:31]
ds_read_b128 v[32:35],v4 offset:8192
v_mfma_f32_16x16x32_bf16 v[28:31],v[196:199],v[44:47],v[28:31]
buffer_load_dwordx4 v2,s[32:35],s63 offen offset:2048 sc1 lds
v_mfma_f32_16x16x32_bf16 v[24:27],v[192:195],v[44:47],v[24:27]
ds_read_b128 v[36:39],v4 offset:22528
v_mfma_f32_16x16x32_bf16 v[24:27],v[80:83],v[40:43],v[24:27]
s_waitcnt lgkmcnt(4)
v_mfma_f32_16x16x32_bf16 v[24:27],a[56:59],v[48:51],v[24:27]
ds_read_b128 v[40:43],v4 offset:36864
v_mfma_f32_16x16x32_bf16 v[24:27],a[168:171],v[52:55],v[24:27]
buffer_load_dwordx4 v2,s[32:35],s63 offen offset:3072 sc1 lds
v_mfma_f32_16x16x32_bf16 v[28:31],a[172:175],v[52:55],v[28:31]
ds_read_b128 v[44:47],v4 offset:51200
v_mfma_f32_16x16x32_bf16 v[28:31],a[60:63],v[48:51],v[28:31]
s_waitcnt lgkmcnt(4)
v_mfma_f32_16x16x32_bf16 v[28:31],v[92:95],v[56:59],v[28:31]
ds_read_b128 v[48:51],v4 offset:9216
v_mfma_f32_16x16x32_bf16 v[28:31],v[204:207],v[60:63],v[28:31]
s_add_u32 s63,s63,4096
s_add_u32 m0,s64,8192
v_mfma_f32_16x16x32_bf16 v[24:27],v[200:203],v[60:63],v[24:27]
ds_read_b128 v[52:55],v4 offset:23552
v_mfma_f32_16x16x32_bf16 v[24:27],v[88:91],v[56:59],v[24:27]
s_waitcnt lgkmcnt(4)
v_mfma_f32_16x16x32_bf16 v[24:27],a[64:67],v[32:35],v[24:27]
ds_read_b128 v[56:59],v4 offset:37888
v_mfma_f32_16x16x32_bf16 v[24:27],a[176:179],v[36:39],v[24:27]
buffer_load_dwordx4 v2,s[32:35],s63 offen offset:0 sc1 lds
v_mfma_f32_16x16x32_bf16 v[28:31],a[180:183],v[36:39],v[28:31]
ds_read_b128 v[60:63],v4 offset:52224
v_mfma_f32_16x16x32_bf16 v[28:31],a[68:71],v[32:35],v[28:31]
s_waitcnt lgkmcnt(4)
v_mfma_f32_16x16x32_bf16 v[28:31],v[100:103],v[40:43],v[28:31]
ds_read_b128 v[32:35],v4 offset:10240
v_mfma_f32_16x16x32_bf16 v[28:31],v[212:215],v[44:47],v[28:31]
buffer_load_dwordx4 v2,s[32:35],s63 offen offset:1024 sc1 lds
v_mfma_f32_16x16x32_bf16 v[24:27],v[208:211],v[44:47],v[24:27]
ds_read_b128 v[36:39],v4 offset:24576
v_mfma_f32_16x16x32_bf16 v[24:27],v[96:99],v[40:43],v[24:27]
s_waitcnt lgkmcnt(4)
v_mfma_f32_16x16x32_bf16 v[24:27],a[72:75],v[48:51],v[24:27]
ds_read_b128 v[40:43],v4 offset:38912
v_mfma_f32_16x16x32_bf16 v[24:27],a[184:187],v[52:55],v[24:27]
buffer_load_dwordx4 v2,s[32:35],s63 offen offset:2048 sc1 lds
v_mfma_f32_16x16x32_bf16 v[28:31],a[188:191],v[52:55],v[28:31]
ds_read_b128 v[44:47],v4 offset:53248
v_mfma_f32_16x16x32_bf16 v[28:31],a[76:79],v[48:51],v[28:31]
s_waitcnt lgkmcnt(4)
v_mfma_f32_16x16x32_bf16 v[28:31],v[108:111],v[56:59],v[28:31]
ds_read_b128 v[48:51],v4 offset:11264
v_mfma_f32_16x16x32_bf16 v[28:31],v[220:223],v[60:63],v[28:31]
v_mfma_f32_16x16x32_bf16 v[24:27],v[216:219],v[60:63],v[24:27]
ds_read_b128 v[52:55],v4 offset:25600
v_mfma_f32_16x16x32_bf16 v[24:27],v[104:107],v[56:59],v[24:27]
s_waitcnt lgkmcnt(4)
v_mfma_f32_16x16x32_bf16 v[24:27],a[80:83],v[32:35],v[24:27]
ds_read_b128 v[56:59],v4 offset:39936
v_mfma_f32_16x16x32_bf16 v[24:27],a[192:195],v[36:39],v[24:27]
buffer_load_dwordx4 v2,s[32:35],s63 offen offset:3072 sc1 lds
s_add_u32 s63,s63,4096
v_mfma_f32_16x16x32_bf16 v[28:31],a[196:199],v[36:39],v[28:31]
ds_read_b128 v[60:63],v4 offset:54272
s_add_u32 m0,s64,12288
v_mfma_f32_16x16x32_bf16 v[28:31],a[84:87],v[32:35],v[28:31]
s_waitcnt lgkmcnt(4)
v_mfma_f32_16x16x32_bf16 v[28:31],v[116:119],v[40:43],v[28:31]
ds_read_b128 v[32:35],v4 offset:12288
v_mfma_f32_16x16x32_bf16 v[28:31],v[228:231],v[44:47],v[28:31]
buffer_load_dwordx4 v2,s[32:35],s63 offen offset:0 sc1 lds
v_mfma_f32_16x16x32_bf16 v[24:27],v[224:227],v[44:47],v[24:27]
ds_read_b128 v[36:39],v4 offset:26624
v_mfma_f32_16x16x32_bf16 v[24:27],v[112:115],v[40:43],v[24:27]
s_waitcnt lgkmcnt(4)
v_mfma_f32_16x16x32_bf16 v[24:27],a[88:91],v[48:51],v[24:27]
ds_read_b128 v[40:43],v4 offset:40960
v_mfma_f32_16x16x32_bf16 v[24:27],a[200:203],v[52:55],v[24:27]
buffer_load_dwordx4 v2,s[32:35],s63 offen offset:1024 sc1 lds
s_add_u32 s63,s63,1175552
v_mfma_f32_16x16x32_bf16 v[28:31],a[204:207],v[52:55],v[28:31]
ds_read_b128 v[44:47],v4 offset:55296
v_mfma_f32_16x16x32_bf16 v[28:31],a[92:95],v[48:51],v[28:31]
s_waitcnt lgkmcnt(4)
v_mfma_f32_16x16x32_bf16 v[28:31],v[124:127],v[56:59],v[28:31]
ds_read_b128 v[48:51],v4 offset:13312
v_mfma_f32_16x16x32_bf16 v[28:31],v[236:239],v[60:63],v[28:31]
v_mfma_f32_16x16x32_bf16 v[24:27],v[232:235],v[60:63],v[24:27]
ds_read_b128 v[52:55],v4 offset:27648
v_mfma_f32_16x16x32_bf16 v[24:27],v[120:123],v[56:59],v[24:27]
s_waitcnt lgkmcnt(4)
v_mfma_f32_16x16x32_bf16 v[24:27],a[96:99],v[32:35],v[24:27]
ds_read_b128 v[56:59],v4 offset:41984
v_mfma_f32_16x16x32_bf16 v[24:27],a[208:211],v[36:39],v[24:27]
v_mfma_f32_16x16x32_bf16 v[28:31],a[212:215],v[36:39],v[28:31]
ds_read_b128 v[60:63],v4 offset:56320
v_mfma_f32_16x16x32_bf16 v[28:31],a[100:103],v[32:35],v[28:31]
s_waitcnt lgkmcnt(4)
s_waitcnt vmcnt(10)
s_barrier
v_mfma_f32_16x16x32_bf16 v[28:31],v[132:135],v[40:43],v[28:31]
ds_read_b128 v[32:35],v2 offset:0
v_mfma_f32_16x16x32_bf16 v[28:31],v[244:247],v[44:47],v[28:31]
v_mfma_f32_16x16x32_bf16 v[24:27],v[240:243],v[44:47],v[24:27]
ds_read_b128 v[36:39],v2 offset:14336
v_mfma_f32_16x16x32_bf16 v[24:27],v[128:131],v[40:43],v[24:27]
s_waitcnt lgkmcnt(4)
v_mfma_f32_16x16x32_bf16 v[24:27],a[104:107],v[48:51],v[24:27]
ds_read_b128 v[40:43],v2 offset:28672
v_mfma_f32_16x16x32_bf16 v[24:27],a[216:219],v[52:55],v[24:27]
buffer_store_dwordx2 v[8:9],v2,s[92:95],s67 idxen sc1
v_mfma_f32_16x16x32_bf16 v[28:31],a[220:223],v[52:55],v[28:31]
ds_read_b128 v[44:47],v2 offset:43008
v_mfma_f32_16x16x32_bf16 v[28:31],a[108:111],v[48:51],v[28:31]
s_waitcnt lgkmcnt(4)
v_mfma_f32_16x16x32_bf16 v[28:31],v[140:143],v[56:59],v[28:31]
ds_read_b128 v[48:51],v2 offset:1024
v_mfma_f32_16x16x32_bf16 v[28:31],v[252:255],v[60:63],v[28:31]
v_mfma_f32_16x16x32_bf16 v[24:27],v[248:251],v[60:63],v[24:27]
ds_read_b128 v[52:55],v2 offset:15360
s_sub_u32 s62,s62,1
v_mfma_f32_16x16x32_bf16 v[24:27],v[136:139],v[56:59],v[24:27]
s_cbranch_scc1 .L_dbt_0
s_waitcnt lgkmcnt(4)
v_mfma_f32_16x16x32_bf16 v[16:19],a[0:3],v[32:35],0
ds_read_b128 v[56:59],v2 offset:29696
v_mfma_f32_16x16x32_bf16 v[16:19],a[112:115],v[36:39],v[16:19]
v_mfma_f32_16x16x32_bf16 v[20:23],a[116:119],v[36:39],0
ds_read_b128 v[60:63],v2 offset:44032
v_mfma_f32_16x16x32_bf16 v[20:23],a[4:7],v[32:35],v[20:23]
v_cvt_pk_bf16_f32 v15,v30,v31
s_waitcnt lgkmcnt(4)
v_mfma_f32_16x16x32_bf16 v[20:23],a[228:231],v[40:43],v[20:23]
ds_read_b128 v[32:35],v2 offset:2048
v_cvt_pk_bf16_f32 v14,v28,v29
v_mfma_f32_16x16x32_bf16 v[20:23],v[148:151],v[44:47],v[20:23]
v_cvt_pk_bf16_f32 v13,v26,v27
v_cvt_pk_bf16_f32 v12,v24,v25
v_mfma_f32_16x16x32_bf16 v[16:19],v[144:147],v[44:47],v[16:19]
ds_read_b128 v[36:39],v2 offset:16384
ds_write_b128 v5,v[12:15]
v_mfma_f32_16x16x32_bf16 v[16:19],a[224:227],v[40:43],v[16:19]
ds_read2st64_b32 v[12:13],v3 offset0:0 offset1:1
s_waitcnt lgkmcnt(6)
v_mfma_f32_16x16x32_bf16 v[16:19],a[8:11],v[48:51],v[16:19]
ds_read_b128 v[40:43],v2 offset:30720
ds_read2st64_b32 v[14:15],v3 offset0:2 offset1:3
v_mfma_f32_16x16x32_bf16 v[16:19],a[120:123],v[52:55],v[16:19]
v_mfma_f32_16x16x32_bf16 v[20:23],a[124:127],v[52:55],v[20:23]
ds_read_b128 v[44:47],v2 offset:45056
v_mfma_f32_16x16x32_bf16 v[20:23],a[12:15],v[48:51],v[20:23]
s_waitcnt lgkmcnt(7)
v_mfma_f32_16x16x32_bf16 v[20:23],a[236:239],v[56:59],v[20:23]
ds_read_b128 v[48:51],v2 offset:3072
v_mfma_f32_16x16x32_bf16 v[20:23],v[156:159],v[60:63],v[20:23]
s_waitcnt lgkmcnt(2)
buffer_atomic_pk_add_bf16 v12,v1,s[44:47],s65 offen offset:0 sc1
v_mfma_f32_16x16x32_bf16 v[16:19],v[152:155],v[60:63],v[16:19]
ds_read_b128 v[52:55],v2 offset:17408
v_mfma_f32_16x16x32_bf16 v[16:19],a[232:235],v[56:59],v[16:19]
buffer_atomic_pk_add_bf16 v13,v1,s[44:47],s65 offen offset:2048 sc1
v_mfma_f32_16x16x32_bf16 v[16:19],a[16:19],v[32:35],v[16:19]
ds_read_b128 v[56:59],v2 offset:31744
s_add_u32 s65,s65,16384
v_mfma_f32_16x16x32_bf16 v[16:19],a[128:131],v[36:39],v[16:19]
buffer_atomic_pk_add_bf16 v14,v1,s[44:47],s65 offen offset:0 sc1
v_mfma_f32_16x16x32_bf16 v[20:23],a[132:135],v[36:39],v[20:23]
ds_read_b128 v[60:63],v2 offset:46080
v_mfma_f32_16x16x32_bf16 v[20:23],a[20:23],v[32:35],v[20:23]
s_waitcnt lgkmcnt(4)
s_waitcnt vmcnt(4)
s_barrier
v_mfma_f32_16x16x32_bf16 v[20:23],a[244:247],v[40:43],v[20:23]
ds_read_b128 v[32:35],v2 offset:4096
s_add_u32 m0,s64,57344
v_mfma_f32_16x16x32_bf16 v[20:23],v[164:167],v[44:47],v[20:23]
buffer_atomic_pk_add_bf16 v15,v1,s[44:47],s65 offen offset:2048 sc1
s_add_u32 s65,s65,16384
v_mfma_f32_16x16x32_bf16 v[16:19],v[160:163],v[44:47],v[16:19]
ds_read_b128 v[36:39],v2 offset:18432
v_mfma_f32_16x16x32_bf16 v[16:19],a[240:243],v[40:43],v[16:19]
s_waitcnt lgkmcnt(4)
v_mfma_f32_16x16x32_bf16 v[16:19],a[24:27],v[48:51],v[16:19]
ds_read_b128 v[40:43],v2 offset:32768
v_mfma_f32_16x16x32_bf16 v[16:19],a[136:139],v[52:55],v[16:19]
buffer_load_dwordx4 v2,s[32:35],s63 offen offset:0 sc1 lds
v_mfma_f32_16x16x32_bf16 v[20:23],a[140:143],v[52:55],v[20:23]
ds_read_b128 v[44:47],v2 offset:47104
v_mfma_f32_16x16x32_bf16 v[20:23],a[28:31],v[48:51],v[20:23]
s_waitcnt lgkmcnt(4)
v_mfma_f32_16x16x32_bf16 v[20:23],a[252:255],v[56:59],v[20:23]
ds_read_b128 v[48:51],v2 offset:5120
v_mfma_f32_16x16x32_bf16 v[20:23],v[172:175],v[60:63],v[20:23]
buffer_load_dwordx4 v2,s[32:35],s63 offen offset:1024 sc1 lds
v_mfma_f32_16x16x32_bf16 v[16:19],v[168:171],v[60:63],v[16:19]
ds_read_b128 v[52:55],v2 offset:19456
v_mfma_f32_16x16x32_bf16 v[16:19],a[248:251],v[56:59],v[16:19]
s_waitcnt lgkmcnt(4)
v_mfma_f32_16x16x32_bf16 v[16:19],a[32:35],v[32:35],v[16:19]
ds_read_b128 v[56:59],v2 offset:33792
v_mfma_f32_16x16x32_bf16 v[16:19],a[144:147],v[36:39],v[16:19]
buffer_load_dwordx4 v2,s[32:35],s63 offen offset:2048 sc1 lds
v_mfma_f32_16x16x32_bf16 v[20:23],a[148:151],v[36:39],v[20:23]
ds_read_b128 v[60:63],v2 offset:48128
v_mfma_f32_16x16x32_bf16 v[20:23],a[36:39],v[32:35],v[20:23]
s_waitcnt lgkmcnt(4)
v_mfma_f32_16x16x32_bf16 v[20:23],v[68:71],v[40:43],v[20:23]
ds_read_b128 v[32:35],v2 offset:6144
v_mfma_f32_16x16x32_bf16 v[20:23],v[180:183],v[44:47],v[20:23]
buffer_load_dwordx4 v2,s[32:35],s63 offen offset:3072 sc1 lds
v_mfma_f32_16x16x32_bf16 v[16:19],v[176:179],v[44:47],v[16:19]
ds_read_b128 v[36:39],v2 offset:20480
v_mfma_f32_16x16x32_bf16 v[16:19],v[64:67],v[40:43],v[16:19]
s_waitcnt lgkmcnt(4)
v_mfma_f32_16x16x32_bf16 v[16:19],a[40:43],v[48:51],v[16:19]
ds_read_b128 v[40:43],v2 offset:34816
s_add_u32 s63,s63,4096
v_mfma_f32_16x16x32_bf16 v[16:19],a[152:155],v[52:55],v[16:19]
s_add_u32 m0,s64,61440
v_mfma_f32_16x16x32_bf16 v[20:23],a[156:159],v[52:55],v[20:23]
ds_read_b128 v[44:47],v2 offset:49152
v_mfma_f32_16x16x32_bf16 v[20:23],a[44:47],v[48:51],v[20:23]
s_waitcnt lgkmcnt(4)
v_mfma_f32_16x16x32_bf16 v[20:23],v[76:79],v[56:59],v[20:23]
ds_read_b128 v[48:51],v2 offset:7168
v_mfma_f32_16x16x32_bf16 v[20:23],v[188:191],v[60:63],v[20:23]
buffer_load_dwordx4 v2,s[32:35],s63 offen offset:0 sc1 lds
v_mfma_f32_16x16x32_bf16 v[16:19],v[184:187],v[60:63],v[16:19]
ds_read_b128 v[52:55],v2 offset:21504
v_mfma_f32_16x16x32_bf16 v[16:19],v[72:75],v[56:59],v[16:19]
s_waitcnt lgkmcnt(4)
v_mfma_f32_16x16x32_bf16 v[16:19],a[48:51],v[32:35],v[16:19]
ds_read_b128 v[56:59],v2 offset:35840
v_mfma_f32_16x16x32_bf16 v[16:19],a[160:163],v[36:39],v[16:19]
buffer_load_dwordx4 v2,s[32:35],s63 offen offset:1024 sc1 lds
v_mfma_f32_16x16x32_bf16 v[20:23],a[164:167],v[36:39],v[20:23]
ds_read_b128 v[60:63],v2 offset:50176
v_mfma_f32_16x16x32_bf16 v[20:23],a[52:55],v[32:35],v[20:23]
s_waitcnt lgkmcnt(4)
v_mfma_f32_16x16x32_bf16 v[20:23],v[84:87],v[40:43],v[20:23]
ds_read_b128 v[32:35],v2 offset:8192
v_mfma_f32_16x16x32_bf16 v[20:23],v[196:199],v[44:47],v[20:23]
buffer_load_dwordx4 v2,s[32:35],s63 offen offset:2048 sc1 lds
v_mfma_f32_16x16x32_bf16 v[16:19],v[192:195],v[44:47],v[16:19]
ds_read_b128 v[36:39],v2 offset:22528
v_mfma_f32_16x16x32_bf16 v[16:19],v[80:83],v[40:43],v[16:19]
s_waitcnt lgkmcnt(4)
v_mfma_f32_16x16x32_bf16 v[16:19],a[56:59],v[48:51],v[16:19]
ds_read_b128 v[40:43],v2 offset:36864
v_mfma_f32_16x16x32_bf16 v[16:19],a[168:171],v[52:55],v[16:19]
buffer_load_dwordx4 v2,s[32:35],s63 offen offset:3072 sc1 lds
v_mfma_f32_16x16x32_bf16 v[20:23],a[172:175],v[52:55],v[20:23]
ds_read_b128 v[44:47],v2 offset:51200
v_mfma_f32_16x16x32_bf16 v[20:23],a[60:63],v[48:51],v[20:23]
s_waitcnt lgkmcnt(4)
v_mfma_f32_16x16x32_bf16 v[20:23],v[92:95],v[56:59],v[20:23]
ds_read_b128 v[48:51],v2 offset:9216
v_mfma_f32_16x16x32_bf16 v[20:23],v[204:207],v[60:63],v[20:23]
s_add_u32 s63,s63,4096
s_add_u32 m0,s64,65536
v_mfma_f32_16x16x32_bf16 v[16:19],v[200:203],v[60:63],v[16:19]
ds_read_b128 v[52:55],v2 offset:23552
v_mfma_f32_16x16x32_bf16 v[16:19],v[88:91],v[56:59],v[16:19]
s_waitcnt lgkmcnt(4)
v_mfma_f32_16x16x32_bf16 v[16:19],a[64:67],v[32:35],v[16:19]
ds_read_b128 v[56:59],v2 offset:37888
v_mfma_f32_16x16x32_bf16 v[16:19],a[176:179],v[36:39],v[16:19]
buffer_load_dwordx4 v2,s[32:35],s63 offen offset:0 sc1 lds
v_mfma_f32_16x16x32_bf16 v[20:23],a[180:183],v[36:39],v[20:23]
ds_read_b128 v[60:63],v2 offset:52224
v_mfma_f32_16x16x32_bf16 v[20:23],a[68:71],v[32:35],v[20:23]
s_waitcnt lgkmcnt(4)
v_mfma_f32_16x16x32_bf16 v[20:23],v[100:103],v[40:43],v[20:23]
ds_read_b128 v[32:35],v2 offset:10240
v_mfma_f32_16x16x32_bf16 v[20:23],v[212:215],v[44:47],v[20:23]
buffer_load_dwordx4 v2,s[32:35],s63 offen offset:1024 sc1 lds
v_mfma_f32_16x16x32_bf16 v[16:19],v[208:211],v[44:47],v[16:19]
ds_read_b128 v[36:39],v2 offset:24576
v_mfma_f32_16x16x32_bf16 v[16:19],v[96:99],v[40:43],v[16:19]
s_waitcnt lgkmcnt(4)
v_mfma_f32_16x16x32_bf16 v[16:19],a[72:75],v[48:51],v[16:19]
ds_read_b128 v[40:43],v2 offset:38912
v_mfma_f32_16x16x32_bf16 v[16:19],a[184:187],v[52:55],v[16:19]
buffer_load_dwordx4 v2,s[32:35],s63 offen offset:2048 sc1 lds
v_mfma_f32_16x16x32_bf16 v[20:23],a[188:191],v[52:55],v[20:23]
ds_read_b128 v[44:47],v2 offset:53248
v_mfma_f32_16x16x32_bf16 v[20:23],a[76:79],v[48:51],v[20:23]
s_waitcnt lgkmcnt(4)
v_mfma_f32_16x16x32_bf16 v[20:23],v[108:111],v[56:59],v[20:23]
ds_read_b128 v[48:51],v2 offset:11264
v_mfma_f32_16x16x32_bf16 v[20:23],v[220:223],v[60:63],v[20:23]
v_mfma_f32_16x16x32_bf16 v[16:19],v[216:219],v[60:63],v[16:19]
ds_read_b128 v[52:55],v2 offset:25600
v_mfma_f32_16x16x32_bf16 v[16:19],v[104:107],v[56:59],v[16:19]
s_waitcnt lgkmcnt(4)
v_mfma_f32_16x16x32_bf16 v[16:19],a[80:83],v[32:35],v[16:19]
ds_read_b128 v[56:59],v2 offset:39936
v_mfma_f32_16x16x32_bf16 v[16:19],a[192:195],v[36:39],v[16:19]
buffer_load_dwordx4 v2,s[32:35],s63 offen offset:3072 sc1 lds
s_add_u32 s63,s63,4096
v_mfma_f32_16x16x32_bf16 v[20:23],a[196:199],v[36:39],v[20:23]
ds_read_b128 v[60:63],v2 offset:54272
s_add_u32 m0,s64,69632
v_mfma_f32_16x16x32_bf16 v[20:23],a[84:87],v[32:35],v[20:23]
s_waitcnt lgkmcnt(4)
v_mfma_f32_16x16x32_bf16 v[20:23],v[116:119],v[40:43],v[20:23]
ds_read_b128 v[32:35],v2 offset:12288
v_mfma_f32_16x16x32_bf16 v[20:23],v[228:231],v[44:47],v[20:23]
buffer_load_dwordx4 v2,s[32:35],s63 offen offset:0 sc1 lds
v_mfma_f32_16x16x32_bf16 v[16:19],v[224:227],v[44:47],v[16:19]
ds_read_b128 v[36:39],v2 offset:26624
v_mfma_f32_16x16x32_bf16 v[16:19],v[112:115],v[40:43],v[16:19]
s_waitcnt lgkmcnt(4)
v_mfma_f32_16x16x32_bf16 v[16:19],a[88:91],v[48:51],v[16:19]
ds_read_b128 v[40:43],v2 offset:40960
v_mfma_f32_16x16x32_bf16 v[16:19],a[200:203],v[52:55],v[16:19]
buffer_load_dwordx4 v2,s[32:35],s63 offen offset:1024 sc1 lds
s_add_u32 s63,s63,1175552
v_mfma_f32_16x16x32_bf16 v[20:23],a[204:207],v[52:55],v[20:23]
ds_read_b128 v[44:47],v2 offset:55296
v_mfma_f32_16x16x32_bf16 v[20:23],a[92:95],v[48:51],v[20:23]
s_waitcnt lgkmcnt(4)
v_mfma_f32_16x16x32_bf16 v[20:23],v[124:127],v[56:59],v[20:23]
ds_read_b128 v[48:51],v2 offset:13312
v_mfma_f32_16x16x32_bf16 v[20:23],v[236:239],v[60:63],v[20:23]
v_mfma_f32_16x16x32_bf16 v[16:19],v[232:235],v[60:63],v[16:19]
ds_read_b128 v[52:55],v2 offset:27648
v_mfma_f32_16x16x32_bf16 v[16:19],v[120:123],v[56:59],v[16:19]
s_waitcnt lgkmcnt(4)
v_mfma_f32_16x16x32_bf16 v[16:19],a[96:99],v[32:35],v[16:19]
ds_read_b128 v[56:59],v2 offset:41984
v_mfma_f32_16x16x32_bf16 v[16:19],a[208:211],v[36:39],v[16:19]
v_mfma_f32_16x16x32_bf16 v[20:23],a[212:215],v[36:39],v[20:23]
ds_read_b128 v[60:63],v2 offset:56320
v_mfma_f32_16x16x32_bf16 v[20:23],a[100:103],v[32:35],v[20:23]
s_waitcnt lgkmcnt(4)
s_waitcnt vmcnt(10)
s_barrier
v_mfma_f32_16x16x32_bf16 v[20:23],v[132:135],v[40:43],v[20:23]
ds_read_b128 v[32:35],v4 offset:0
v_mfma_f32_16x16x32_bf16 v[20:23],v[244:247],v[44:47],v[20:23]
v_mfma_f32_16x16x32_bf16 v[16:19],v[240:243],v[44:47],v[16:19]
ds_read_b128 v[36:39],v4 offset:14336
v_mfma_f32_16x16x32_bf16 v[16:19],v[128:131],v[40:43],v[16:19]
s_waitcnt lgkmcnt(4)
v_mfma_f32_16x16x32_bf16 v[16:19],a[104:107],v[48:51],v[16:19]
ds_read_b128 v[40:43],v4 offset:28672
v_mfma_f32_16x16x32_bf16 v[16:19],a[216:219],v[52:55],v[16:19]
v_mfma_f32_16x16x32_bf16 v[20:23],a[220:223],v[52:55],v[20:23]
ds_read_b128 v[44:47],v4 offset:43008
v_mfma_f32_16x16x32_bf16 v[20:23],a[108:111],v[48:51],v[20:23]
s_waitcnt lgkmcnt(4)
v_mfma_f32_16x16x32_bf16 v[20:23],v[140:143],v[56:59],v[20:23]
ds_read_b128 v[48:51],v4 offset:1024
v_mfma_f32_16x16x32_bf16 v[20:23],v[252:255],v[60:63],v[20:23]
v_mfma_f32_16x16x32_bf16 v[16:19],v[248:251],v[60:63],v[16:19]
ds_read_b128 v[52:55],v4 offset:15360
v_mfma_f32_16x16x32_bf16 v[16:19],v[136:139],v[56:59],v[16:19]
s_branch .L_dbl_0
.L_dbt_0:
s_nop 1
s_nop 0
v_cvt_pk_bf16_f32 v15,v30,v31
v_cvt_pk_bf16_f32 v14,v28,v29
s_nop 1
v_cvt_pk_bf16_f32 v13,v26,v27
v_cvt_pk_bf16_f32 v12,v24,v25
ds_write_b128 v5,v[12:15]
ds_read2st64_b32 v[12:13],v3 offset0:0 offset1:1
ds_read2st64_b32 v[14:15],v3 offset0:2 offset1:3
s_waitcnt lgkmcnt(0)
buffer_atomic_pk_add_bf16 v12,v1,s[44:47],s65 offen offset:0 sc1
buffer_atomic_pk_add_bf16 v13,v1,s[44:47],s65 offen offset:2048 sc1
s_add_u32 s65,s65,16384
buffer_atomic_pk_add_bf16 v14,v1,s[44:47],s65 offen offset:0 sc1
buffer_atomic_pk_add_bf16 v15,v1,s[44:47],s65 offen offset:2048 sc1
)#"
R"#(
s_add_u32 s65,s65,16384
s_or_b32 s48,s28,s27
s_or_b32 s48,s48,s26
s_cbranch_scc1 .L_end
v_mov_b32 v8,s54
v_mov_b32 v9,s55
buffer_store_dwordx2 v[8:9],off,s[32:35],0 sc1
s_branch .L_end
.p2align 6
.L_dhv1:
.L_dal_1:
s_waitcnt lgkmcnt(4)
v_mfma_f32_16x16x32_bf16 v[16:19],a[0:3],v[32:35],0
ds_read_b128 v[56:59],v2 offset:29696
v_dot2_f32_bf16 v10,v12,s72,0
v_mfma_f32_16x16x32_bf16 v[16:19],a[112:115],v[36:39],v[16:19]
v_dot2_f32_bf16 v11,v12,v12,0
v_dot2_f32_bf16 v10,v13,s72,v10
v_mfma_f32_16x16x32_bf16 v[20:23],a[116:119],v[36:39],0
ds_read_b128 v[60:63],v2 offset:44032
v_dot2_f32_bf16 v11,v13,v13,v11
v_mfma_f32_16x16x32_bf16 v[20:23],a[4:7],v[32:35],v[20:23]
v_dot2_f32_bf16 v10,v14,s72,v10
s_waitcnt lgkmcnt(4)
v_mfma_f32_16x16x32_bf16 v[20:23],a[228:231],v[40:43],v[20:23]
ds_read_b128 v[32:35],v2 offset:2048
v_dot2_f32_bf16 v11,v14,v14,v11
v_mfma_f32_16x16x32_bf16 v[20:23],v[148:151],v[44:47],v[20:23]
v_dot2_f32_bf16 v10,v15,s72,v10
v_dot2_f32_bf16 v11,v15,v15,v11
v_mfma_f32_16x16x32_bf16 v[16:19],v[144:147],v[44:47],v[16:19]
ds_read_b128 v[36:39],v2 offset:16384
v_mfma_f32_16x16x32_bf16 v[16:19],a[224:227],v[40:43],v[16:19]
v_permlane16_swap_b32 v10,v11
s_waitcnt lgkmcnt(4)
v_mfma_f32_16x16x32_bf16 v[16:19],a[8:11],v[48:51],v[16:19]
ds_read_b128 v[40:43],v2 offset:30720
v_add_f32 v10,v10,v11
v_mfma_f32_16x16x32_bf16 v[16:19],a[120:123],v[52:55],v[16:19]
v_mfma_f32_16x16x32_bf16 v[20:23],a[124:127],v[52:55],v[20:23]
ds_read_b128 v[44:47],v2 offset:45056
v_permlane32_swap_b32 v10,v11
v_mfma_f32_16x16x32_bf16 v[20:23],a[12:15],v[48:51],v[20:23]
v_add_f32 v10,v10,v11
s_waitcnt lgkmcnt(4)
v_mfma_f32_16x16x32_bf16 v[20:23],a[236:239],v[56:59],v[20:23]
ds_read_b128 v[48:51],v2 offset:3072
v_mfma_f32_16x16x32_bf16 v[20:23],v[156:159],v[60:63],v[20:23]
buffer_store_dword v10,v3,s[32:35],s67 offen offset:1024 sc1
v_mfma_f32_16x16x32_bf16 v[16:19],v[152:155],v[60:63],v[16:19]
ds_read_b128 v[52:55],v2 offset:17408
s_waitcnt vmcnt(5)
v_mfma_f32_16x16x32_bf16 v[16:19],a[232:235],v[56:59],v[16:19]
v_cmp_ne_u64 vcc,v[8:9],v[6:7]
s_waitcnt lgkmcnt(4)
v_mfma_f32_16x16x32_bf16 v[16:19],a[16:19],v[32:35],v[16:19]
ds_read_b128 v[56:59],v2 offset:31744
s_cbranch_vccnz .L_rta0_1
.L_bka0_1:
v_mfma_f32_16x16x32_bf16 v[16:19],a[128:131],v[36:39],v[16:19]
s_add_u32 s66,s66,1187840
v_mfma_f32_16x16x32_bf16 v[20:23],a[132:135],v[36:39],v[20:23]
s_waitcnt vmcnt(2)
s_barrier
v_mfma_f32_16x16x32_bf16 v[20:23],a[20:23],v[32:35],v[20:23]
ds_read_b128 v[60:63],v2 offset:46080
s_waitcnt lgkmcnt(4)
v_mfma_f32_16x16x32_bf16 v[20:23],a[244:247],v[40:43],v[20:23]
ds_read_b128 v[32:35],v2 offset:4096
s_mov_b32 m0,s74
v_mfma_f32_16x16x32_bf16 v[20:23],v[164:167],v[44:47],v[20:23]
buffer_load_dwordx4 v2,s[32:35],s71 offen offset:0 sc1 lds
v_mfma_f32_16x16x32_bf16 v[16:19],v[160:163],v[44:47],v[16:19]
ds_read_b128 v[36:39],v2 offset:18432
v_mfma_f32_16x16x32_bf16 v[16:19],a[240:243],v[40:43],v[16:19]
s_waitcnt lgkmcnt(4)
v_mfma_f32_16x16x32_bf16 v[16:19],a[24:27],v[48:51],v[16:19]
ds_read_b128 v[40:43],v2 offset:32768
v_mfma_f32_16x16x32_bf16 v[16:19],a[136:139],v[52:55],v[16:19]
buffer_load_dwordx4 v2,s[32:35],s71 offen offset:1024 sc1 lds
s_add_u32 s71,s71,1187840
v_mfma_f32_16x16x32_bf16 v[20:23],a[140:143],v[52:55],v[20:23]
ds_read_b128 v[44:47],v2 offset:47104
s_add_u32 m0,s64,57344
v_mfma_f32_16x16x32_bf16 v[20:23],a[28:31],v[48:51],v[20:23]
s_waitcnt lgkmcnt(4)
v_mfma_f32_16x16x32_bf16 v[20:23],a[252:255],v[56:59],v[20:23]
ds_read_b128 v[48:51],v2 offset:5120
v_mfma_f32_16x16x32_bf16 v[20:23],v[172:175],v[60:63],v[20:23]
buffer_load_dwordx4 v2,s[32:35],s63 offen offset:0 sc1 lds
v_mfma_f32_16x16x32_bf16 v[16:19],v[168:171],v[60:63],v[16:19]
ds_read_b128 v[52:55],v2 offset:19456
v_mfma_f32_16x16x32_bf16 v[16:19],a[248:251],v[56:59],v[16:19]
s_waitcnt lgkmcnt(4)
v_mfma_f32_16x16x32_bf16 v[16:19],a[32:35],v[32:35],v[16:19]
ds_read_b128 v[56:59],v2 offset:33792
v_mfma_f32_16x16x32_bf16 v[16:19],a[144:147],v[36:39],v[16:19]
buffer_load_dwordx4 v2,s[32:35],s63 offen offset:1024 sc1 lds
v_mfma_f32_16x16x32_bf16 v[20:23],a[148:151],v[36:39],v[20:23]
ds_read_b128 v[60:63],v2 offset:48128
v_mfma_f32_16x16x32_bf16 v[20:23],a[36:39],v[32:35],v[20:23]
s_waitcnt lgkmcnt(4)
v_mfma_f32_16x16x32_bf16 v[20:23],v[68:71],v[40:43],v[20:23]
ds_read_b128 v[32:35],v2 offset:6144
v_mfma_f32_16x16x32_bf16 v[20:23],v[180:183],v[44:47],v[20:23]
buffer_load_dwordx4 v2,s[32:35],s63 offen offset:2048 sc1 lds
v_mfma_f32_16x16x32_bf16 v[16:19],v[176:179],v[44:47],v[16:19]
ds_read_b128 v[36:39],v2 offset:20480
v_mfma_f32_16x16x32_bf16 v[16:19],v[64:67],v[40:43],v[16:19]
s_waitcnt lgkmcnt(4)
v_mfma_f32_16x16x32_bf16 v[16:19],a[40:43],v[48:51],v[16:19]
ds_read_b128 v[40:43],v2 offset:34816
v_mfma_f32_16x16x32_bf16 v[16:19],a[152:155],v[52:55],v[16:19]
buffer_load_dwordx4 v2,s[32:35],s63 offen offset:3072 sc1 lds
s_add_u32 s63,s63,4096
v_mfma_f32_16x16x32_bf16 v[20:23],a[156:159],v[52:55],v[20:23]
ds_read_b128 v[44:47],v2 offset:49152
s_add_u32 m0,s64,61440
v_mfma_f32_16x16x32_bf16 v[20:23],a[44:47],v[48:51],v[20:23]
s_waitcnt lgkmcnt(4)
v_mfma_f32_16x16x32_bf16 v[20:23],v[76:79],v[56:59],v[20:23]
ds_read_b128 v[48:51],v2 offset:7168
v_mfma_f32_16x16x32_bf16 v[20:23],v[188:191],v[60:63],v[20:23]
buffer_load_dwordx4 v2,s[32:35],s63 offen offset:0 sc1 lds
v_mfma_f32_16x16x32_bf16 v[16:19],v[184:187],v[60:63],v[16:19]
ds_read_b128 v[52:55],v2 offset:21504
v_mfma_f32_16x16x32_bf16 v[16:19],v[72:75],v[56:59],v[16:19]
s_waitcnt lgkmcnt(4)
v_mfma_f32_16x16x32_bf16 v[16:19],a[48:51],v[32:35],v[16:19]
ds_read_b128 v[56:59],v2 offset:35840
v_mfma_f32_16x16x32_bf16 v[16:19],a[160:163],v[36:39],v[16:19]
buffer_load_dwordx4 v2,s[32:35],s63 offen offset:1024 sc1 lds
v_mfma_f32_16x16x32_bf16 v[20:23],a[164:167],v[36:39],v[20:23]
ds_read_b128 v[60:63],v2 offset:50176
v_mfma_f32_16x16x32_bf16 v[20:23],a[52:55],v[32:35],v[20:23]
s_waitcnt lgkmcnt(4)
v_mfma_f32_16x16x32_bf16 v[20:23],v[84:87],v[40:43],v[20:23]
ds_read_b128 v[32:35],v2 offset:8192
v_mfma_f32_16x16x32_bf16 v[20:23],v[196:199],v[44:47],v[20:23]
buffer_load_dwordx4 v2,s[32:35],s63 offen offset:2048 sc1 lds
v_mfma_f32_16x16x32_bf16 v[16:19],v[192:195],v[44:47],v[16:19]
ds_read_b128 v[36:39],v2 offset:22528
v_mfma_f32_16x16x32_bf16 v[16:19],v[80:83],v[40:43],v[16:19]
s_waitcnt lgkmcnt(4)
v_mfma_f32_16x16x32_bf16 v[16:19],a[56:59],v[48:51],v[16:19]
ds_read_b128 v[40:43],v2 offset:36864
v_mfma_f32_16x16x32_bf16 v[16:19],a[168:171],v[52:55],v[16:19]
buffer_load_dwordx4 v2,s[32:35],s63 offen offset:3072 sc1 lds
v_mfma_f32_16x16x32_bf16 v[20:23],a[172:175],v[52:55],v[20:23]
ds_read_b128 v[44:47],v2 offset:51200
v_mfma_f32_16x16x32_bf16 v[20:23],a[60:63],v[48:51],v[20:23]
s_waitcnt lgkmcnt(4)
v_mfma_f32_16x16x32_bf16 v[20:23],v[92:95],v[56:59],v[20:23]
ds_read_b128 v[48:51],v2 offset:9216
v_mfma_f32_16x16x32_bf16 v[20:23],v[204:207],v[60:63],v[20:23]
s_add_u32 s63,s63,4096
s_add_u32 m0,s64,65536
v_mfma_f32_16x16x32_bf16 v[16:19],v[200:203],v[60:63],v[16:19]
ds_read_b128 v[52:55],v2 offset:23552
v_mfma_f32_16x16x32_bf16 v[16:19],v[88:91],v[56:59],v[16:19]
s_waitcnt lgkmcnt(4)
v_mfma_f32_16x16x32_bf16 v[16:19],a[64:67],v[32:35],v[16:19]
ds_read_b128 v[56:59],v2 offset:37888
v_mfma_f32_16x16x32_bf16 v[16:19],a[176:179],v[36:39],v[16:19]
buffer_load_dwordx4 v2,s[32:35],s63 offen offset:0 sc1 lds
v_mfma_f32_16x16x32_bf16 v[20:23],a[180:183],v[36:39],v[20:23]
ds_read_b128 v[60:63],v2 offset:52224
v_mfma_f32_16x16x32_bf16 v[20:23],a[68:71],v[32:35],v[20:23]
s_waitcnt lgkmcnt(4)
v_mfma_f32_16x16x32_bf16 v[20:23],v[100:103],v[40:43],v[20:23]
ds_read_b128 v[32:35],v2 offset:10240
v_mfma_f32_16x16x32_bf16 v[20:23],v[212:215],v[44:47],v[20:23]
buffer_load_dwordx4 v2,s[32:35],s63 offen offset:1024 sc1 lds
v_mfma_f32_16x16x32_bf16 v[16:19],v[208:211],v[44:47],v[16:19]
ds_read_b128 v[36:39],v2 offset:24576
v_mfma_f32_16x16x32_bf16 v[16:19],v[96:99],v[40:43],v[16:19]
s_waitcnt lgkmcnt(4)
v_mfma_f32_16x16x32_bf16 v[16:19],a[72:75],v[48:51],v[16:19]
ds_read_b128 v[40:43],v2 offset:38912
s_waitcnt vmcnt(10)
v_mfma_f32_16x16x32_bf16 v[16:19],a[184:187],v[52:55],v[16:19]
buffer_load_dwordx4 v2,s[32:35],s63 offen offset:2048 sc1 lds
ds_read_b128 v[12:15],v5 offset:0
v_mfma_f32_16x16x32_bf16 v[20:23],a[188:191],v[52:55],v[20:23]
ds_read_b128 v[44:47],v2 offset:53248
v_mfma_f32_16x16x32_bf16 v[20:23],a[76:79],v[48:51],v[20:23]
s_waitcnt lgkmcnt(5)
v_mfma_f32_16x16x32_bf16 v[20:23],v[108:111],v[56:59],v[20:23]
ds_read_b128 v[48:51],v2 offset:11264
v_mfma_f32_16x16x32_bf16 v[20:23],v[220:223],v[60:63],v[20:23]
buffer_load_dwordx2 v[6:7],v0,s[32:35],s66 offen sc1
v_mfma_f32_16x16x32_bf16 v[16:19],v[216:219],v[60:63],v[16:19]
ds_read_b128 v[52:55],v2 offset:25600
s_waitcnt lgkmcnt(3)
v_mfma_f32_16x16x32_bf16 v[16:19],v[104:107],v[56:59],v[16:19]
v_add_f32 v24,v24,v12
v_mfma_f32_16x16x32_bf16 v[16:19],a[80:83],v[32:35],v[16:19]
ds_read_b128 v[56:59],v2 offset:39936
v_add_f32 v25,v25,v13
v_mfma_f32_16x16x32_bf16 v[16:19],a[192:195],v[36:39],v[16:19]
buffer_load_dwordx4 v2,s[32:35],s63 offen offset:3072 sc1 lds
s_add_u32 s63,s63,4096
v_mfma_f32_16x16x32_bf16 v[20:23],a[196:199],v[36:39],v[20:23]
ds_read_b128 v[60:63],v2 offset:54272
s_add_u32 m0,s64,69632
v_mfma_f32_16x16x32_bf16 v[20:23],a[84:87],v[32:35],v[20:23]
v_add_f32 v26,v26,v14
s_waitcnt lgkmcnt(4)
v_mfma_f32_16x16x32_bf16 v[20:23],v[116:119],v[40:43],v[20:23]
ds_read_b128 v[32:35],v2 offset:12288
v_add_f32 v27,v27,v15
v_mfma_f32_16x16x32_bf16 v[20:23],v[228:231],v[44:47],v[20:23]
buffer_load_dwordx4 v2,s[32:35],s63 offen offset:0 sc1 lds
ds_read_b128 v[12:15],v5 offset:1024
v_mfma_f32_16x16x32_bf16 v[16:19],v[224:227],v[44:47],v[16:19]
ds_read_b128 v[36:39],v2 offset:26624
v_mfma_f32_16x16x32_bf16 v[16:19],v[112:115],v[40:43],v[16:19]
s_waitcnt lgkmcnt(5)
v_mfma_f32_16x16x32_bf16 v[16:19],a[88:91],v[48:51],v[16:19]
ds_read_b128 v[40:43],v2 offset:40960
v_mfma_f32_16x16x32_bf16 v[16:19],a[200:203],v[52:55],v[16:19]
buffer_load_dwordx4 v2,s[32:35],s63 offen offset:1024 sc1 lds
s_add_u32 s63,s63,1175552
v_mfma_f32_16x16x32_bf16 v[20:23],a[204:207],v[52:55],v[20:23]
ds_read_b128 v[44:47],v2 offset:55296
s_waitcnt lgkmcnt(3)
v_mfma_f32_16x16x32_bf16 v[20:23],a[92:95],v[48:51],v[20:23]
v_add_f32 v28,v28,v12
v_mfma_f32_16x16x32_bf16 v[20:23],v[124:127],v[56:59],v[20:23]
ds_read_b128 v[48:51],v2 offset:13312
v_add_f32 v29,v29,v13
v_mfma_f32_16x16x32_bf16 v[20:23],v[236:239],v[60:63],v[20:23]
v_add_f32 v30,v30,v14
v_add_f32 v31,v31,v15
v_mfma_f32_16x16x32_bf16 v[16:19],v[232:235],v[60:63],v[16:19]
ds_read_b128 v[52:55],v2 offset:27648
v_cvt_pk_bf16_f32 v12,v24,v25
v_mfma_f32_16x16x32_bf16 v[16:19],v[120:123],v[56:59],v[16:19]
v_cvt_pk_bf16_f32 v13,v26,v27
s_waitcnt lgkmcnt(4)
v_mfma_f32_16x16x32_bf16 v[16:19],a[96:99],v[32:35],v[16:19]
ds_read_b128 v[56:59],v2 offset:41984
v_cvt_pk_bf16_f32 v14,v28,v29
v_mfma_f32_16x16x32_bf16 v[16:19],a[208:211],v[36:39],v[16:19]
v_cvt_pk_bf16_f32 v15,v30,v31
v_mfma_f32_16x16x32_bf16 v[20:23],a[212:215],v[36:39],v[20:23]
s_waitcnt vmcnt(11)
s_barrier
v_mfma_f32_16x16x32_bf16 v[20:23],a[100:103],v[32:35],v[20:23]
ds_read_b128 v[60:63],v2 offset:56320
s_waitcnt lgkmcnt(4)
v_mfma_f32_16x16x32_bf16 v[20:23],v[132:135],v[40:43],v[20:23]
ds_read_b128 v[32:35],v4 offset:0
v_mfma_f32_16x16x32_bf16 v[20:23],v[244:247],v[44:47],v[20:23]
buffer_store_dwordx4 v[12:15],v2,s[32:35],s68 offen sc1
v_mfma_f32_16x16x32_bf16 v[16:19],v[240:243],v[44:47],v[16:19]
ds_read_b128 v[36:39],v4 offset:14336
v_mfma_f32_16x16x32_bf16 v[16:19],v[128:131],v[40:43],v[16:19]
s_waitcnt lgkmcnt(4)
v_mfma_f32_16x16x32_bf16 v[16:19],a[104:107],v[48:51],v[16:19]
ds_read_b128 v[40:43],v4 offset:28672
v_mfma_f32_16x16x32_bf16 v[16:19],a[216:219],v[52:55],v[16:19]
v_mfma_f32_16x16x32_bf16 v[20:23],a[220:223],v[52:55],v[20:23]
ds_read_b128 v[44:47],v4 offset:43008
s_mov_b32 s67,s68
v_mfma_f32_16x16x32_bf16 v[20:23],a[108:111],v[48:51],v[20:23]
s_mov_b32 s68,s70
s_waitcnt lgkmcnt(4)
v_mfma_f32_16x16x32_bf16 v[20:23],v[140:143],v[56:59],v[20:23]
ds_read_b128 v[48:51],v4 offset:1024
s_add_u32 s70,s70,1187840
v_mfma_f32_16x16x32_bf16 v[20:23],v[252:255],v[60:63],v[20:23]
v_mfma_f32_16x16x32_bf16 v[16:19],v[248:251],v[60:63],v[16:19]
ds_read_b128 v[52:55],v4 offset:15360
s_sub_u32 s62,s62,1
v_mfma_f32_16x16x32_bf16 v[16:19],v[136:139],v[56:59],v[16:19]
s_cbranch_scc1 .L_dab_1
s_waitcnt lgkmcnt(4)
v_mfma_f32_16x16x32_bf16 v[24:27],a[0:3],v[32:35],0
ds_read_b128 v[56:59],v4 offset:29696
v_dot2_f32_bf16 v10,v12,s72,0
v_mfma_f32_16x16x32_bf16 v[24:27],a[112:115],v[36:39],v[24:27]
v_dot2_f32_bf16 v11,v12,v12,0
v_dot2_f32_bf16 v10,v13,s72,v10
v_mfma_f32_16x16x32_bf16 v[28:31],a[116:119],v[36:39],0
ds_read_b128 v[60:63],v4 offset:44032
v_dot2_f32_bf16 v11,v13,v13,v11
v_mfma_f32_16x16x32_bf16 v[28:31],a[4:7],v[32:35],v[28:31]
v_dot2_f32_bf16 v10,v14,s72,v10
s_waitcnt lgkmcnt(4)
v_mfma_f32_16x16x32_bf16 v[28:31],a[228:231],v[40:43],v[28:31]
ds_read_b128 v[32:35],v4 offset:2048
v_dot2_f32_bf16 v11,v14,v14,v11
v_mfma_f32_16x16x32_bf16 v[28:31],v[148:151],v[44:47],v[28:31]
v_dot2_f32_bf16 v10,v15,s72,v10
v_dot2_f32_bf16 v11,v15,v15,v11
v_mfma_f32_16x16x32_bf16 v[24:27],v[144:147],v[44:47],v[24:27]
ds_read_b128 v[36:39],v4 offset:16384
v_mfma_f32_16x16x32_bf16 v[24:27],a[224:227],v[40:43],v[24:27]
v_permlane16_swap_b32 v10,v11
s_waitcnt lgkmcnt(4)
v_mfma_f32_16x16x32_bf16 v[24:27],a[8:11],v[48:51],v[24:27]
ds_read_b128 v[40:43],v4 offset:30720
v_add_f32 v10,v10,v11
v_mfma_f32_16x16x32_bf16 v[24:27],a[120:123],v[52:55],v[24:27]
v_mfma_f32_16x16x32_bf16 v[28:31],a[124:127],v[52:55],v[28:31]
ds_read_b128 v[44:47],v4 offset:45056
v_permlane32_swap_b32 v10,v11
v_mfma_f32_16x16x32_bf16 v[28:31],a[12:15],v[48:51],v[28:31]
v_add_f32 v10,v10,v11
s_waitcnt lgkmcnt(4)
v_mfma_f32_16x16x32_bf16 v[28:31],a[236:239],v[56:59],v[28:31]
ds_read_b128 v[48:51],v4 offset:3072
v_mfma_f32_16x16x32_bf16 v[28:31],v[156:159],v[60:63],v[28:31]
buffer_store_dword v10,v3,s[32:35],s67 offen offset:1024 sc1
v_mfma_f32_16x16x32_bf16 v[24:27],v[152:155],v[60:63],v[24:27]
ds_read_b128 v[52:55],v4 offset:17408
s_waitcnt vmcnt(5)
v_mfma_f32_16x16x32_bf16 v[24:27],a[232:235],v[56:59],v[24:27]
v_cmp_ne_u64 vcc,v[8:9],v[6:7]
s_waitcnt lgkmcnt(4)
v_mfma_f32_16x16x32_bf16 v[24:27],a[16:19],v[32:35],v[24:27]
ds_read_b128 v[56:59],v4 offset:31744
s_cbranch_vccnz .L_rta1_1
.L_bka1_1:
v_mfma_f32_16x16x32_bf16 v[24:27],a[128:131],v[36:39],v[24:27]
s_add_u32 s66,s66,1187840
v_mfma_f32_16x16x32_bf16 v[28:31],a[132:135],v[36:39],v[28:31]
s_waitcnt vmcnt(2)
s_barrier
v_mfma_f32_16x16x32_bf16 v[28:31],a[20:23],v[32:35],v[28:31]
ds_read_b128 v[60:63],v4 offset:46080
s_waitcnt lgkmcnt(4)
v_mfma_f32_16x16x32_bf16 v[28:31],a[244:247],v[40:43],v[28:31]
ds_read_b128 v[32:35],v4 offset:4096
s_mov_b32 m0,s74
v_mfma_f32_16x16x32_bf16 v[28:31],v[164:167],v[44:47],v[28:31]
buffer_load_dwordx4 v2,s[32:35],s71 offen offset:0 sc1 lds
v_mfma_f32_16x16x32_bf16 v[24:27],v[160:163],v[44:47],v[24:27]
ds_read_b128 v[36:39],v4 offset:18432
v_mfma_f32_16x16x32_bf16 v[24:27],a[240:243],v[40:43],v[24:27]
s_waitcnt lgkmcnt(4)
v_mfma_f32_16x16x32_bf16 v[24:27],a[24:27],v[48:51],v[24:27]
ds_read_b128 v[40:43],v4 offset:32768
v_mfma_f32_16x16x32_bf16 v[24:27],a[136:139],v[52:55],v[24:27]
buffer_load_dwordx4 v2,s[32:35],s71 offen offset:1024 sc1 lds
s_add_u32 s71,s71,1187840
v_mfma_f32_16x16x32_bf16 v[28:31],a[140:143],v[52:55],v[28:31]
ds_read_b128 v[44:47],v4 offset:47104
s_add_u32 m0,s64,0
v_mfma_f32_16x16x32_bf16 v[28:31],a[28:31],v[48:51],v[28:31]
s_waitcnt lgkmcnt(4)
v_mfma_f32_16x16x32_bf16 v[28:31],a[252:255],v[56:59],v[28:31]
ds_read_b128 v[48:51],v4 offset:5120
v_mfma_f32_16x16x32_bf16 v[28:31],v[172:175],v[60:63],v[28:31]
buffer_load_dwordx4 v2,s[32:35],s63 offen offset:0 sc1 lds
v_mfma_f32_16x16x32_bf16 v[24:27],v[168:171],v[60:63],v[24:27]
ds_read_b128 v[52:55],v4 offset:19456
v_mfma_f32_16x16x32_bf16 v[24:27],a[248:251],v[56:59],v[24:27]
s_waitcnt lgkmcnt(4)
v_mfma_f32_16x16x32_bf16 v[24:27],a[32:35],v[32:35],v[24:27]
ds_read_b128 v[56:59],v4 offset:33792
v_mfma_f32_16x16x32_bf16 v[24:27],a[144:147],v[36:39],v[24:27]
buffer_load_dwordx4 v2,s[32:35],s63 offen offset:1024 sc1 lds
v_mfma_f32_16x16x32_bf16 v[28:31],a[148:151],v[36:39],v[28:31]
ds_read_b128 v[60:63],v4 offset:48128
v_mfma_f32_16x16x32_bf16 v[28:31],a[36:39],v[32:35],v[28:31]
s_waitcnt lgkmcnt(4)
v_mfma_f32_16x16x32_bf16 v[28:31],v[68:71],v[40:43],v[28:31]
ds_read_b128 v[32:35],v4 offset:6144
v_mfma_f32_16x16x32_bf16 v[28:31],v[180:183],v[44:47],v[28:31]
buffer_load_dwordx4 v2,s[32:35],s63 offen offset:2048 sc1 lds
v_mfma_f32_16x16x32_bf16 v[24:27],v[176:179],v[44:47],v[24:27]
ds_read_b128 v[36:39],v4 offset:20480
v_mfma_f32_16x16x32_bf16 v[24:27],v[64:67],v[40:43],v[24:27]
s_waitcnt lgkmcnt(4)
v_mfma_f32_16x16x32_bf16 v[24:27],a[40:43],v[48:51],v[24:27]
ds_read_b128 v[40:43],v4 offset:34816
v_mfma_f32_16x16x32_bf16 v[24:27],a[152:155],v[52:55],v[24:27]
buffer_load_dwordx4 v2,s[32:35],s63 offen offset:3072 sc1 lds
s_add_u32 s63,s63,4096
v_mfma_f32_16x16x32_bf16 v[28:31],a[156:159],v[52:55],v[28:31]
ds_read_b128 v[44:47],v4 offset:49152
s_add_u32 m0,s64,4096
v_mfma_f32_16x16x32_bf16 v[28:31],a[44:47],v[48:51],v[28:31]
s_waitcnt lgkmcnt(4)
v_mfma_f32_16x16x32_bf16 v[28:31],v[76:79],v[56:59],v[28:31]
ds_read_b128 v[48:51],v4 offset:7168
v_mfma_f32_16x16x32_bf16 v[28:31],v[188:191],v[60:63],v[28:31]
buffer_load_dwordx4 v2,s[32:35],s63 offen offset:0 sc1 lds
v_mfma_f32_16x16x32_bf16 v[24:27],v[184:187],v[60:63],v[24:27]
ds_read_b128 v[52:55],v4 offset:21504
v_mfma_f32_16x16x32_bf16 v[24:27],v[72:75],v[56:59],v[24:27]
s_waitcnt lgkmcnt(4)
v_mfma_f32_16x16x32_bf16 v[24:27],a[48:51],v[32:35],v[24:27]
ds_read_b128 v[56:59],v4 offset:35840
v_mfma_f32_16x16x32_bf16 v[24:27],a[160:163],v[36:39],v[24:27]
buffer_load_dwordx4 v2,s[32:35],s63 offen offset:1024 sc1 lds
v_mfma_f32_16x16x32_bf16 v[28:31],a[164:167],v[36:39],v[28:31]
ds_read_b128 v[60:63],v4 offset:50176
v_mfma_f32_16x16x32_bf16 v[28:31],a[52:55],v[32:35],v[28:31]
s_waitcnt lgkmcnt(4)
v_mfma_f32_16x16x32_bf16 v[28:31],v[84:87],v[40:43],v[28:31]
ds_read_b128 v[32:35],v4 offset:8192
v_mfma_f32_16x16x32_bf16 v[28:31],v[196:199],v[44:47],v[28:31]
buffer_load_dwordx4 v2,s[32:35],s63 offen offset:2048 sc1 lds
v_mfma_f32_16x16x32_bf16 v[24:27],v[192:195],v[44:47],v[24:27]
ds_read_b128 v[36:39],v4 offset:22528
v_mfma_f32_16x16x32_bf16 v[24:27],v[80:83],v[40:43],v[24:27]
s_waitcnt lgkmcnt(4)
v_mfma_f32_16x16x32_bf16 v[24:27],a[56:59],v[48:51],v[24:27]
ds_read_b128 v[40:43],v4 offset:36864
v_mfma_f32_16x16x32_bf16 v[24:27],a[168:171],v[52:55],v[24:27]
buffer_load_dwordx4 v2,s[32:35],s63 offen offset:3072 sc1 lds
v_mfma_f32_16x16x32_bf16 v[28:31],a[172:175],v[52:55],v[28:31]
ds_read_b128 v[44:47],v4 offset:51200
v_mfma_f32_16x16x32_bf16 v[28:31],a[60:63],v[48:51],v[28:31]
s_waitcnt lgkmcnt(4)
v_mfma_f32_16x16x32_bf16 v[28:31],v[92:95],v[56:59],v[28:31]
ds_read_b128 v[48:51],v4 offset:9216
v_mfma_f32_16x16x32_bf16 v[28:31],v[204:207],v[60:63],v[28:31]
s_add_u32 s63,s63,4096
s_add_u32 m0,s64,8192
v_mfma_f32_16x16x32_bf16 v[24:27],v[200:203],v[60:63],v[24:27]
ds_read_b128 v[52:55],v4 offset:23552
v_mfma_f32_16x16x32_bf16 v[24:27],v[88:91],v[56:59],v[24:27]
s_waitcnt lgkmcnt(4)
v_mfma_f32_16x16x32_bf16 v[24:27],a[64:67],v[32:35],v[24:27]
ds_read_b128 v[56:59],v4 offset:37888
v_mfma_f32_16x16x32_bf16 v[24:27],a[176:179],v[36:39],v[24:27]
buffer_load_dwordx4 v2,s[32:35],s63 offen offset:0 sc1 lds
v_mfma_f32_16x16x32_bf16 v[28:31],a[180:183],v[36:39],v[28:31]
ds_read_b128 v[60:63],v4 offset:52224
v_mfma_f32_16x16x32_bf16 v[28:31],a[68:71],v[32:35],v[28:31]
s_waitcnt lgkmcnt(4)
v_mfma_f32_16x16x32_bf16 v[28:31],v[100:103],v[40:43],v[28:31]
ds_read_b128 v[32:35],v4 offset:10240
v_mfma_f32_16x16x32_bf16 v[28:31],v[212:215],v[44:47],v[28:31]
buffer_load_dwordx4 v2,s[32:35],s63 offen offset:1024 sc1 lds
v_mfma_f32_16x16x32_bf16 v[24:27],v[208:211],v[44:47],v[24:27]
ds_read_b128 v[36:39],v4 offset:24576
v_mfma_f32_16x16x32_bf16 v[24:27],v[96:99],v[40:43],v[24:27]
s_waitcnt lgkmcnt(4)
v_mfma_f32_16x16x32_bf16 v[24:27],a[72:75],v[48:51],v[24:27]
ds_read_b128 v[40:43],v4 offset:38912
s_waitcnt vmcnt(10)
v_mfma_f32_16x16x32_bf16 v[24:27],a[184:187],v[52:55],v[24:27]
buffer_load_dwordx4 v2,s[32:35],s63 offen offset:2048 sc1 lds
ds_read_b128 v[12:15],v5 offset:0
v_mfma_f32_16x16x32_bf16 v[28:31],a[188:191],v[52:55],v[28:31]
ds_read_b128 v[44:47],v4 offset:53248
v_mfma_f32_16x16x32_bf16 v[28:31],a[76:79],v[48:51],v[28:31]
s_waitcnt lgkmcnt(5)
v_mfma_f32_16x16x32_bf16 v[28:31],v[108:111],v[56:59],v[28:31]
ds_read_b128 v[48:51],v4 offset:11264
v_mfma_f32_16x16x32_bf16 v[28:31],v[220:223],v[60:63],v[28:31]
buffer_load_dwordx2 v[6:7],v0,s[32:35],s66 offen sc1
v_mfma_f32_16x16x32_bf16 v[24:27],v[216:219],v[60:63],v[24:27]
ds_read_b128 v[52:55],v4 offset:25600
s_waitcnt lgkmcnt(3)
v_mfma_f32_16x16x32_bf16 v[24:27],v[104:107],v[56:59],v[24:27]
v_add_f32 v16,v16,v12
v_mfma_f32_16x16x32_bf16 v[24:27],a[80:83],v[32:35],v[24:27]
ds_read_b128 v[56:59],v4 offset:39936
v_add_f32 v17,v17,v13
v_mfma_f32_16x16x32_bf16 v[24:27],a[192:195],v[36:39],v[24:27]
buffer_load_dwordx4 v2,s[32:35],s63 offen offset:3072 sc1 lds
s_add_u32 s63,s63,4096
v_mfma_f32_16x16x32_bf16 v[28:31],a[196:199],v[36:39],v[28:31]
ds_read_b128 v[60:63],v4 offset:54272
s_add_u32 m0,s64,12288
v_mfma_f32_16x16x32_bf16 v[28:31],a[84:87],v[32:35],v[28:31]
v_add_f32 v18,v18,v14
s_waitcnt lgkmcnt(4)
v_mfma_f32_16x16x32_bf16 v[28:31],v[116:119],v[40:43],v[28:31]
ds_read_b128 v[32:35],v4 offset:12288
v_add_f32 v19,v19,v15
v_mfma_f32_16x16x32_bf16 v[28:31],v[228:231],v[44:47],v[28:31]
buffer_load_dwordx4 v2,s[32:35],s63 offen offset:0 sc1 lds
ds_read_b128 v[12:15],v5 offset:1024
v_mfma_f32_16x16x32_bf16 v[24:27],v[224:227],v[44:47],v[24:27]
ds_read_b128 v[36:39],v4 offset:26624
v_mfma_f32_16x16x32_bf16 v[24:27],v[112:115],v[40:43],v[24:27]
s_waitcnt lgkmcnt(5)
v_mfma_f32_16x16x32_bf16 v[24:27],a[88:91],v[48:51],v[24:27]
ds_read_b128 v[40:43],v4 offset:40960
v_mfma_f32_16x16x32_bf16 v[24:27],a[200:203],v[52:55],v[24:27]
buffer_load_dwordx4 v2,s[32:35],s63 offen offset:1024 sc1 lds
s_add_u32 s63,s63,1175552
v_mfma_f32_16x16x32_bf16 v[28:31],a[204:207],v[52:55],v[28:31]
ds_read_b128 v[44:47],v4 offset:55296
s_waitcnt lgkmcnt(3)
v_mfma_f32_16x16x32_bf16 v[28:31],a[92:95],v[48:51],v[28:31]
v_add_f32 v20,v20,v12
v_mfma_f32_16x16x32_bf16 v[28:31],v[124:127],v[56:59],v[28:31]
ds_read_b128 v[48:51],v4 offset:13312
v_add_f32 v21,v21,v13
v_mfma_f32_16x16x32_bf16 v[28:31],v[236:239],v[60:63],v[28:31]
v_add_f32 v22,v22,v14
v_add_f32 v23,v23,v15
)#"
R"#(
v_mfma_f32_16x16x32_bf16 v[24:27],v[232:235],v[60:63],v[24:27]
ds_read_b128 v[52:55],v4 offset:27648
v_cvt_pk_bf16_f32 v12,v16,v17
v_mfma_f32_16x16x32_bf16 v[24:27],v[120:123],v[56:59],v[24:27]
v_cvt_pk_bf16_f32 v13,v18,v19
s_waitcnt lgkmcnt(4)
v_mfma_f32_16x16x32_bf16 v[24:27],a[96:99],v[32:35],v[24:27]
ds_read_b128 v[56:59],v4 offset:41984
v_cvt_pk_bf16_f32 v14,v20,v21
v_mfma_f32_16x16x32_bf16 v[24:27],a[208:211],v[36:39],v[24:27]
v_cvt_pk_bf16_f32 v15,v22,v23
v_mfma_f32_16x16x32_bf16 v[28:31],a[212:215],v[36:39],v[28:31]
s_waitcnt vmcnt(11)
s_barrier
v_mfma_f32_16x16x32_bf16 v[28:31],a[100:103],v[32:35],v[28:31]
ds_read_b128 v[60:63],v4 offset:56320
s_waitcnt lgkmcnt(4)
v_mfma_f32_16x16x32_bf16 v[28:31],v[132:135],v[40:43],v[28:31]
ds_read_b128 v[32:35],v2 offset:0
v_mfma_f32_16x16x32_bf16 v[28:31],v[244:247],v[44:47],v[28:31]
buffer_store_dwordx4 v[12:15],v2,s[32:35],s69 offen sc1
v_mfma_f32_16x16x32_bf16 v[24:27],v[240:243],v[44:47],v[24:27]
ds_read_b128 v[36:39],v2 offset:14336
v_mfma_f32_16x16x32_bf16 v[24:27],v[128:131],v[40:43],v[24:27]
s_waitcnt lgkmcnt(4)
v_mfma_f32_16x16x32_bf16 v[24:27],a[104:107],v[48:51],v[24:27]
ds_read_b128 v[40:43],v2 offset:28672
v_mfma_f32_16x16x32_bf16 v[24:27],a[216:219],v[52:55],v[24:27]
v_mfma_f32_16x16x32_bf16 v[28:31],a[220:223],v[52:55],v[28:31]
ds_read_b128 v[44:47],v2 offset:43008
s_mov_b32 s67,s69
v_mfma_f32_16x16x32_bf16 v[28:31],a[108:111],v[48:51],v[28:31]
s_mov_b32 s69,s70
s_waitcnt lgkmcnt(4)
v_mfma_f32_16x16x32_bf16 v[28:31],v[140:143],v[56:59],v[28:31]
ds_read_b128 v[48:51],v2 offset:1024
s_add_u32 s70,s70,1187840
v_mfma_f32_16x16x32_bf16 v[28:31],v[252:255],v[60:63],v[28:31]
v_mfma_f32_16x16x32_bf16 v[24:27],v[248:251],v[60:63],v[24:27]
ds_read_b128 v[52:55],v2 offset:15360
v_mfma_f32_16x16x32_bf16 v[24:27],v[136:139],v[56:59],v[24:27]
s_branch .L_dal_1
.L_dab_1:
v_dot2_f32_bf16 v10,v12,s72,0
v_dot2_f32_bf16 v11,v12,v12,0
v_dot2_f32_bf16 v10,v13,s72,v10
v_dot2_f32_bf16 v11,v13,v13,v11
v_dot2_f32_bf16 v10,v14,s72,v10
v_dot2_f32_bf16 v11,v14,v14,v11
v_dot2_f32_bf16 v10,v15,s72,v10
v_dot2_f32_bf16 v11,v15,v15,v11
s_nop 2
v_permlane16_swap_b32 v10,v11
v_add_f32 v10,v10,v11
s_nop 1
v_permlane32_swap_b32 v10,v11
v_add_f32 v10,v10,v11
buffer_store_dword v10,v3,s[32:35],s67 offen offset:1024 sc1
s_mov_b32 s62,15
s_add_u32 s74,s74,8192
v_lshrrev_b32 v5,8,v2
v_lshl_add_u32 v5,v5,4,s74
v_and_b32 v3,128,v2
v_lshl_add_u32 v5,v3,2,v5
v_and_b32 v3,16,v2
v_lshl_add_u32 v5,v3,4,v5
v_and_b32 v3,96,v2
v_lshl_add_u32 v5,v3,1,v5
v_lshrrev_b32 v3,2,v2
v_add_u32 v3,s74,v3
.p2align 6
.L_dbl_1:
s_waitcnt lgkmcnt(4)
v_mfma_f32_16x16x32_bf16 v[24:27],a[0:3],v[32:35],0
ds_read_b128 v[56:59],v4 offset:29696
v_mfma_f32_16x16x32_bf16 v[24:27],a[112:115],v[36:39],v[24:27]
v_mfma_f32_16x16x32_bf16 v[28:31],a[116:119],v[36:39],0
ds_read_b128 v[60:63],v4 offset:44032
v_mfma_f32_16x16x32_bf16 v[28:31],a[4:7],v[32:35],v[28:31]
v_cvt_pk_bf16_f32 v15,v22,v23
s_waitcnt lgkmcnt(4)
v_mfma_f32_16x16x32_bf16 v[28:31],a[228:231],v[40:43],v[28:31]
ds_read_b128 v[32:35],v4 offset:2048
v_cvt_pk_bf16_f32 v14,v20,v21
v_mfma_f32_16x16x32_bf16 v[28:31],v[148:151],v[44:47],v[28:31]
v_cvt_pk_bf16_f32 v13,v18,v19
v_cvt_pk_bf16_f32 v12,v16,v17
v_mfma_f32_16x16x32_bf16 v[24:27],v[144:147],v[44:47],v[24:27]
ds_read_b128 v[36:39],v4 offset:16384
ds_write_b128 v5,v[12:15]
v_mfma_f32_16x16x32_bf16 v[24:27],a[224:227],v[40:43],v[24:27]
ds_read2st64_b32 v[12:13],v3 offset0:0 offset1:1
s_waitcnt lgkmcnt(6)
v_mfma_f32_16x16x32_bf16 v[24:27],a[8:11],v[48:51],v[24:27]
ds_read_b128 v[40:43],v4 offset:30720
ds_read2st64_b32 v[14:15],v3 offset0:2 offset1:3
v_mfma_f32_16x16x32_bf16 v[24:27],a[120:123],v[52:55],v[24:27]
v_mfma_f32_16x16x32_bf16 v[28:31],a[124:127],v[52:55],v[28:31]
ds_read_b128 v[44:47],v4 offset:45056
v_mfma_f32_16x16x32_bf16 v[28:31],a[12:15],v[48:51],v[28:31]
s_waitcnt lgkmcnt(7)
v_mfma_f32_16x16x32_bf16 v[28:31],a[236:239],v[56:59],v[28:31]
ds_read_b128 v[48:51],v4 offset:3072
v_mfma_f32_16x16x32_bf16 v[28:31],v[156:159],v[60:63],v[28:31]
s_waitcnt lgkmcnt(2)
buffer_atomic_pk_add_bf16 v12,v1,s[44:47],s65 offen offset:0 sc1
v_mfma_f32_16x16x32_bf16 v[24:27],v[152:155],v[60:63],v[24:27]
ds_read_b128 v[52:55],v4 offset:17408
s_waitcnt vmcnt(4)
v_mfma_f32_16x16x32_bf16 v[24:27],a[232:235],v[56:59],v[24:27]
buffer_atomic_pk_add_bf16 v13,v1,s[44:47],s65 offen offset:2048 sc1
v_mfma_f32_16x16x32_bf16 v[24:27],a[16:19],v[32:35],v[24:27]
ds_read_b128 v[56:59],v4 offset:31744
v_cmp_ne_u64 vcc,v[8:9],v[6:7]
v_mfma_f32_16x16x32_bf16 v[24:27],a[128:131],v[36:39],v[24:27]
s_cbranch_vccnz .L_rtb1_1
.L_bkb1_1:
s_add_u32 s66,s66,1187840
v_mfma_f32_16x16x32_bf16 v[28:31],a[132:135],v[36:39],v[28:31]
s_waitcnt vmcnt(2)
s_barrier
v_mfma_f32_16x16x32_bf16 v[28:31],a[20:23],v[32:35],v[28:31]
ds_read_b128 v[60:63],v4 offset:46080
s_waitcnt lgkmcnt(4)
v_mfma_f32_16x16x32_bf16 v[28:31],a[244:247],v[40:43],v[28:31]
ds_read_b128 v[32:35],v4 offset:4096
s_add_u32 m0,s64,0
v_mfma_f32_16x16x32_bf16 v[28:31],v[164:167],v[44:47],v[28:31]
buffer_load_dwordx4 v2,s[32:35],s63 offen offset:0 sc1 lds
s_add_u32 s65,s65,16384
v_mfma_f32_16x16x32_bf16 v[24:27],v[160:163],v[44:47],v[24:27]
ds_read_b128 v[36:39],v4 offset:18432
v_mfma_f32_16x16x32_bf16 v[24:27],a[240:243],v[40:43],v[24:27]
buffer_atomic_pk_add_bf16 v14,v1,s[44:47],s65 offen offset:0 sc1
s_waitcnt lgkmcnt(4)
v_mfma_f32_16x16x32_bf16 v[24:27],a[24:27],v[48:51],v[24:27]
ds_read_b128 v[40:43],v4 offset:32768
v_mfma_f32_16x16x32_bf16 v[24:27],a[136:139],v[52:55],v[24:27]
buffer_atomic_pk_add_bf16 v15,v1,s[44:47],s65 offen offset:2048 sc1
s_add_u32 s65,s65,16384
v_mfma_f32_16x16x32_bf16 v[28:31],a[140:143],v[52:55],v[28:31]
ds_read_b128 v[44:47],v4 offset:47104
v_mfma_f32_16x16x32_bf16 v[28:31],a[28:31],v[48:51],v[28:31]
s_waitcnt lgkmcnt(4)
v_mfma_f32_16x16x32_bf16 v[28:31],a[252:255],v[56:59],v[28:31]
ds_read_b128 v[48:51],v4 offset:5120
v_mfma_f32_16x16x32_bf16 v[28:31],v[172:175],v[60:63],v[28:31]
buffer_load_dwordx4 v2,s[32:35],s63 offen offset:1024 sc1 lds
v_mfma_f32_16x16x32_bf16 v[24:27],v[168:171],v[60:63],v[24:27]
ds_read_b128 v[52:55],v4 offset:19456
v_mfma_f32_16x16x32_bf16 v[24:27],a[248:251],v[56:59],v[24:27]
s_waitcnt lgkmcnt(4)
v_mfma_f32_16x16x32_bf16 v[24:27],a[32:35],v[32:35],v[24:27]
ds_read_b128 v[56:59],v4 offset:33792
v_mfma_f32_16x16x32_bf16 v[24:27],a[144:147],v[36:39],v[24:27]
buffer_load_dwordx4 v2,s[32:35],s63 offen offset:2048 sc1 lds
v_mfma_f32_16x16x32_bf16 v[28:31],a[148:151],v[36:39],v[28:31]
ds_read_b128 v[60:63],v4 offset:48128
v_mfma_f32_16x16x32_bf16 v[28:31],a[36:39],v[32:35],v[28:31]
s_waitcnt lgkmcnt(4)
v_mfma_f32_16x16x32_bf16 v[28:31],v[68:71],v[40:43],v[28:31]
ds_read_b128 v[32:35],v4 offset:6144
v_mfma_f32_16x16x32_bf16 v[28:31],v[180:183],v[44:47],v[28:31]
buffer_load_dwordx4 v2,s[32:35],s63 offen offset:3072 sc1 lds
v_mfma_f32_16x16x32_bf16 v[24:27],v[176:179],v[44:47],v[24:27]
ds_read_b128 v[36:39],v4 offset:20480
v_mfma_f32_16x16x32_bf16 v[24:27],v[64:67],v[40:43],v[24:27]
s_waitcnt lgkmcnt(4)
v_mfma_f32_16x16x32_bf16 v[24:27],a[40:43],v[48:51],v[24:27]
ds_read_b128 v[40:43],v4 offset:34816
s_add_u32 s63,s63,4096
v_mfma_f32_16x16x32_bf16 v[24:27],a[152:155],v[52:55],v[24:27]
s_add_u32 m0,s64,4096
v_mfma_f32_16x16x32_bf16 v[28:31],a[156:159],v[52:55],v[28:31]
ds_read_b128 v[44:47],v4 offset:49152
v_mfma_f32_16x16x32_bf16 v[28:31],a[44:47],v[48:51],v[28:31]
s_waitcnt lgkmcnt(4)
v_mfma_f32_16x16x32_bf16 v[28:31],v[76:79],v[56:59],v[28:31]
ds_read_b128 v[48:51],v4 offset:7168
v_mfma_f32_16x16x32_bf16 v[28:31],v[188:191],v[60:63],v[28:31]
buffer_load_dwordx4 v2,s[32:35],s63 offen offset:0 sc1 lds
v_mfma_f32_16x16x32_bf16 v[24:27],v[184:187],v[60:63],v[24:27]
ds_read_b128 v[52:55],v4 offset:21504
v_mfma_f32_16x16x32_bf16 v[24:27],v[72:75],v[56:59],v[24:27]
s_waitcnt lgkmcnt(4)
v_mfma_f32_16x16x32_bf16 v[24:27],a[48:51],v[32:35],v[24:27]
ds_read_b128 v[56:59],v4 offset:35840
v_mfma_f32_16x16x32_bf16 v[24:27],a[160:163],v[36:39],v[24:27]
buffer_load_dwordx4 v2,s[32:35],s63 offen offset:1024 sc1 lds
v_mfma_f32_16x16x32_bf16 v[28:31],a[164:167],v[36:39],v[28:31]
ds_read_b128 v[60:63],v4 offset:50176
v_mfma_f32_16x16x32_bf16 v[28:31],a[52:55],v[32:35],v[28:31]
s_waitcnt lgkmcnt(4)
v_mfma_f32_16x16x32_bf16 v[28:31],v[84:87],v[40:43],v[28:31]
ds_read_b128 v[32:35],v4 offset:8192
v_mfma_f32_16x16x32_bf16 v[28:31],v[196:199],v[44:47],v[28:31]
buffer_load_dwordx4 v2,s[32:35],s63 offen offset:2048 sc1 lds
v_mfma_f32_16x16x32_bf16 v[24:27],v[192:195],v[44:47],v[24:27]
ds_read_b128 v[36:39],v4 offset:22528
v_mfma_f32_16x16x32_bf16 v[24:27],v[80:83],v[40:43],v[24:27]
s_waitcnt lgkmcnt(4)
v_mfma_f32_16x16x32_bf16 v[24:27],a[56:59],v[48:51],v[24:27]
ds_read_b128 v[40:43],v4 offset:36864
v_mfma_f32_16x16x32_bf16 v[24:27],a[168:171],v[52:55],v[24:27]
buffer_load_dwordx4 v2,s[32:35],s63 offen offset:3072 sc1 lds
v_mfma_f32_16x16x32_bf16 v[28:31],a[172:175],v[52:55],v[28:31]
ds_read_b128 v[44:47],v4 offset:51200
v_mfma_f32_16x16x32_bf16 v[28:31],a[60:63],v[48:51],v[28:31]
s_waitcnt lgkmcnt(4)
v_mfma_f32_16x16x32_bf16 v[28:31],v[92:95],v[56:59],v[28:31]
ds_read_b128 v[48:51],v4 offset:9216
v_mfma_f32_16x16x32_bf16 v[28:31],v[204:207],v[60:63],v[28:31]
s_add_u32 s63,s63,4096
s_add_u32 m0,s64,8192
v_mfma_f32_16x16x32_bf16 v[24:27],v[200:203],v[60:63],v[24:27]
ds_read_b128 v[52:55],v4 offset:23552
v_mfma_f32_16x16x32_bf16 v[24:27],v[88:91],v[56:59],v[24:27]
s_waitcnt lgkmcnt(4)
v_mfma_f32_16x16x32_bf16 v[24:27],a[64:67],v[32:35],v[24:27]
ds_read_b128 v[56:59],v4 offset:37888
v_mfma_f32_16x16x32_bf16 v[24:27],a[176:179],v[36:39],v[24:27]
buffer_load_dwordx4 v2,s[32:35],s63 offen offset:0 sc1 lds
v_mfma_f32_16x16x32_bf16 v[28:31],a[180:183],v[36:39],v[28:31]
ds_read_b128 v[60:63],v4 offset:52224
v_mfma_f32_16x16x32_bf16 v[28:31],a[68:71],v[32:35],v[28:31]
s_waitcnt lgkmcnt(4)
v_mfma_f32_16x16x32_bf16 v[28:31],v[100:103],v[40:43],v[28:31]
ds_read_b128 v[32:35],v4 offset:10240
v_mfma_f32_16x16x32_bf16 v[28:31],v[212:215],v[44:47],v[28:31]
buffer_load_dwordx4 v2,s[32:35],s63 offen offset:1024 sc1 lds
v_mfma_f32_16x16x32_bf16 v[24:27],v[208:211],v[44:47],v[24:27]
ds_read_b128 v[36:39],v4 offset:24576
v_mfma_f32_16x16x32_bf16 v[24:27],v[96:99],v[40:43],v[24:27]
s_waitcnt lgkmcnt(4)
v_mfma_f32_16x16x32_bf16 v[24:27],a[72:75],v[48:51],v[24:27]
ds_read_b128 v[40:43],v4 offset:38912
v_mfma_f32_16x16x32_bf16 v[24:27],a[184:187],v[52:55],v[24:27]
buffer_load_dwordx4 v2,s[32:35],s63 offen offset:2048 sc1 lds
v_mfma_f32_16x16x32_bf16 v[28:31],a[188:191],v[52:55],v[28:31]
ds_read_b128 v[44:47],v4 offset:53248
v_mfma_f32_16x16x32_bf16 v[28:31],a[76:79],v[48:51],v[28:31]
s_waitcnt lgkmcnt(4)
v_mfma_f32_16x16x32_bf16 v[28:31],v[108:111],v[56:59],v[28:31]
ds_read_b128 v[48:51],v4 offset:11264
v_mfma_f32_16x16x32_bf16 v[28:31],v[220:223],v[60:63],v[28:31]
buffer_load_dwordx2 v[6:7],v0,s[32:35],s66 offen sc1
v_mfma_f32_16x16x32_bf16 v[24:27],v[216:219],v[60:63],v[24:27]
ds_read_b128 v[52:55],v4 offset:25600
v_mfma_f32_16x16x32_bf16 v[24:27],v[104:107],v[56:59],v[24:27]
s_waitcnt lgkmcnt(4)
v_mfma_f32_16x16x32_bf16 v[24:27],a[80:83],v[32:35],v[24:27]
ds_read_b128 v[56:59],v4 offset:39936
v_mfma_f32_16x16x32_bf16 v[24:27],a[192:195],v[36:39],v[24:27]
buffer_load_dwordx4 v2,s[32:35],s63 offen offset:3072 sc1 lds
s_add_u32 s63,s63,4096
v_mfma_f32_16x16x32_bf16 v[28:31],a[196:199],v[36:39],v[28:31]
ds_read_b128 v[60:63],v4 offset:54272
s_add_u32 m0,s64,12288
v_mfma_f32_16x16x32_bf16 v[28:31],a[84:87],v[32:35],v[28:31]
s_waitcnt lgkmcnt(4)
v_mfma_f32_16x16x32_bf16 v[28:31],v[116:119],v[40:43],v[28:31]
ds_read_b128 v[32:35],v4 offset:12288
v_mfma_f32_16x16x32_bf16 v[28:31],v[228:231],v[44:47],v[28:31]
buffer_load_dwordx4 v2,s[32:35],s63 offen offset:0 sc1 lds
v_mfma_f32_16x16x32_bf16 v[24:27],v[224:227],v[44:47],v[24:27]
ds_read_b128 v[36:39],v4 offset:26624
v_mfma_f32_16x16x32_bf16 v[24:27],v[112:115],v[40:43],v[24:27]
s_waitcnt lgkmcnt(4)
v_mfma_f32_16x16x32_bf16 v[24:27],a[88:91],v[48:51],v[24:27]
ds_read_b128 v[40:43],v4 offset:40960
v_mfma_f32_16x16x32_bf16 v[24:27],a[200:203],v[52:55],v[24:27]
buffer_load_dwordx4 v2,s[32:35],s63 offen offset:1024 sc1 lds
s_add_u32 s63,s63,1175552
v_mfma_f32_16x16x32_bf16 v[28:31],a[204:207],v[52:55],v[28:31]
ds_read_b128 v[44:47],v4 offset:55296
v_mfma_f32_16x16x32_bf16 v[28:31],a[92:95],v[48:51],v[28:31]
s_waitcnt lgkmcnt(4)
v_mfma_f32_16x16x32_bf16 v[28:31],v[124:127],v[56:59],v[28:31]
ds_read_b128 v[48:51],v4 offset:13312
v_mfma_f32_16x16x32_bf16 v[28:31],v[236:239],v[60:63],v[28:31]
v_mfma_f32_16x16x32_bf16 v[24:27],v[232:235],v[60:63],v[24:27]
ds_read_b128 v[52:55],v4 offset:27648
v_mfma_f32_16x16x32_bf16 v[24:27],v[120:123],v[56:59],v[24:27]
s_waitcnt lgkmcnt(4)
v_mfma_f32_16x16x32_bf16 v[24:27],a[96:99],v[32:35],v[24:27]
ds_read_b128 v[56:59],v4 offset:41984
v_mfma_f32_16x16x32_bf16 v[24:27],a[208:211],v[36:39],v[24:27]
v_mfma_f32_16x16x32_bf16 v[28:31],a[212:215],v[36:39],v[28:31]
s_waitcnt vmcnt(11)
s_barrier
v_mfma_f32_16x16x32_bf16 v[28:31],a[100:103],v[32:35],v[28:31]
ds_read_b128 v[60:63],v4 offset:56320
s_waitcnt lgkmcnt(4)
v_mfma_f32_16x16x32_bf16 v[28:31],v[132:135],v[40:43],v[28:31]
ds_read_b128 v[32:35],v2 offset:0
v_mfma_f32_16x16x32_bf16 v[28:31],v[244:247],v[44:47],v[28:31]
v_mfma_f32_16x16x32_bf16 v[24:27],v[240:243],v[44:47],v[24:27]
ds_read_b128 v[36:39],v2 offset:14336
v_mfma_f32_16x16x32_bf16 v[24:27],v[128:131],v[40:43],v[24:27]
s_waitcnt lgkmcnt(4)
v_mfma_f32_16x16x32_bf16 v[24:27],a[104:107],v[48:51],v[24:27]
ds_read_b128 v[40:43],v2 offset:28672
v_mfma_f32_16x16x32_bf16 v[24:27],a[216:219],v[52:55],v[24:27]
v_mfma_f32_16x16x32_bf16 v[28:31],a[220:223],v[52:55],v[28:31]
ds_read_b128 v[44:47],v2 offset:43008
v_mfma_f32_16x16x32_bf16 v[28:31],a[108:111],v[48:51],v[28:31]
s_waitcnt lgkmcnt(4)
v_mfma_f32_16x16x32_bf16 v[28:31],v[140:143],v[56:59],v[28:31]
ds_read_b128 v[48:51],v2 offset:1024
v_mfma_f32_16x16x32_bf16 v[28:31],v[252:255],v[60:63],v[28:31]
v_mfma_f32_16x16x32_bf16 v[24:27],v[248:251],v[60:63],v[24:27]
ds_read_b128 v[52:55],v2 offset:15360
s_sub_u32 s62,s62,1
v_mfma_f32_16x16x32_bf16 v[24:27],v[136:139],v[56:59],v[24:27]
s_cbranch_scc1 .L_dbt_1
s_waitcnt lgkmcnt(4)
v_mfma_f32_16x16x32_bf16 v[16:19],a[0:3],v[32:35],0
ds_read_b128 v[56:59],v2 offset:29696
v_mfma_f32_16x16x32_bf16 v[16:19],a[112:115],v[36:39],v[16:19]
v_mfma_f32_16x16x32_bf16 v[20:23],a[116:119],v[36:39],0
ds_read_b128 v[60:63],v2 offset:44032
v_mfma_f32_16x16x32_bf16 v[20:23],a[4:7],v[32:35],v[20:23]
v_cvt_pk_bf16_f32 v15,v30,v31
s_waitcnt lgkmcnt(4)
v_mfma_f32_16x16x32_bf16 v[20:23],a[228:231],v[40:43],v[20:23]
ds_read_b128 v[32:35],v2 offset:2048
v_cvt_pk_bf16_f32 v14,v28,v29
v_mfma_f32_16x16x32_bf16 v[20:23],v[148:151],v[44:47],v[20:23]
v_cvt_pk_bf16_f32 v13,v26,v27
v_cvt_pk_bf16_f32 v12,v24,v25
v_mfma_f32_16x16x32_bf16 v[16:19],v[144:147],v[44:47],v[16:19]
ds_read_b128 v[36:39],v2 offset:16384
ds_write_b128 v5,v[12:15]
v_mfma_f32_16x16x32_bf16 v[16:19],a[224:227],v[40:43],v[16:19]
ds_read2st64_b32 v[12:13],v3 offset0:0 offset1:1
s_waitcnt lgkmcnt(6)
v_mfma_f32_16x16x32_bf16 v[16:19],a[8:11],v[48:51],v[16:19]
ds_read_b128 v[40:43],v2 offset:30720
ds_read2st64_b32 v[14:15],v3 offset0:2 offset1:3
v_mfma_f32_16x16x32_bf16 v[16:19],a[120:123],v[52:55],v[16:19]
v_mfma_f32_16x16x32_bf16 v[20:23],a[124:127],v[52:55],v[20:23]
ds_read_b128 v[44:47],v2 offset:45056
v_mfma_f32_16x16x32_bf16 v[20:23],a[12:15],v[48:51],v[20:23]
s_waitcnt lgkmcnt(7)
v_mfma_f32_16x16x32_bf16 v[20:23],a[236:239],v[56:59],v[20:23]
ds_read_b128 v[48:51],v2 offset:3072
v_mfma_f32_16x16x32_bf16 v[20:23],v[156:159],v[60:63],v[20:23]
s_waitcnt lgkmcnt(2)
buffer_atomic_pk_add_bf16 v12,v1,s[44:47],s65 offen offset:0 sc1
v_mfma_f32_16x16x32_bf16 v[16:19],v[152:155],v[60:63],v[16:19]
ds_read_b128 v[52:55],v2 offset:17408
s_waitcnt vmcnt(4)
v_mfma_f32_16x16x32_bf16 v[16:19],a[232:235],v[56:59],v[16:19]
buffer_atomic_pk_add_bf16 v13,v1,s[44:47],s65 offen offset:2048 sc1
v_mfma_f32_16x16x32_bf16 v[16:19],a[16:19],v[32:35],v[16:19]
ds_read_b128 v[56:59],v2 offset:31744
v_cmp_ne_u64 vcc,v[8:9],v[6:7]
v_mfma_f32_16x16x32_bf16 v[16:19],a[128:131],v[36:39],v[16:19]
s_cbranch_vccnz .L_rtb0_1
.L_bkb0_1:
s_add_u32 s66,s66,1187840
v_mfma_f32_16x16x32_bf16 v[20:23],a[132:135],v[36:39],v[20:23]
s_waitcnt vmcnt(2)
s_barrier
v_mfma_f32_16x16x32_bf16 v[20:23],a[20:23],v[32:35],v[20:23]
ds_read_b128 v[60:63],v2 offset:46080
s_waitcnt lgkmcnt(4)
v_mfma_f32_16x16x32_bf16 v[20:23],a[244:247],v[40:43],v[20:23]
ds_read_b128 v[32:35],v2 offset:4096
s_add_u32 m0,s64,57344
v_mfma_f32_16x16x32_bf16 v[20:23],v[164:167],v[44:47],v[20:23]
buffer_load_dwordx4 v2,s[32:35],s63 offen offset:0 sc1 lds
s_add_u32 s65,s65,16384
v_mfma_f32_16x16x32_bf16 v[16:19],v[160:163],v[44:47],v[16:19]
ds_read_b128 v[36:39],v2 offset:18432
v_mfma_f32_16x16x32_bf16 v[16:19],a[240:243],v[40:43],v[16:19]
buffer_atomic_pk_add_bf16 v14,v1,s[44:47],s65 offen offset:0 sc1
s_waitcnt lgkmcnt(4)
v_mfma_f32_16x16x32_bf16 v[16:19],a[24:27],v[48:51],v[16:19]
ds_read_b128 v[40:43],v2 offset:32768
v_mfma_f32_16x16x32_bf16 v[16:19],a[136:139],v[52:55],v[16:19]
buffer_atomic_pk_add_bf16 v15,v1,s[44:47],s65 offen offset:2048 sc1
s_add_u32 s65,s65,16384
v_mfma_f32_16x16x32_bf16 v[20:23],a[140:143],v[52:55],v[20:23]
ds_read_b128 v[44:47],v2 offset:47104
v_mfma_f32_16x16x32_bf16 v[20:23],a[28:31],v[48:51],v[20:23]
s_waitcnt lgkmcnt(4)
v_mfma_f32_16x16x32_bf16 v[20:23],a[252:255],v[56:59],v[20:23]
ds_read_b128 v[48:51],v2 offset:5120
v_mfma_f32_16x16x32_bf16 v[20:23],v[172:175],v[60:63],v[20:23]
buffer_load_dwordx4 v2,s[32:35],s63 offen offset:1024 sc1 lds
v_mfma_f32_16x16x32_bf16 v[16:19],v[168:171],v[60:63],v[16:19]
ds_read_b128 v[52:55],v2 offset:19456
v_mfma_f32_16x16x32_bf16 v[16:19],a[248:251],v[56:59],v[16:19]
s_waitcnt lgkmcnt(4)
v_mfma_f32_16x16x32_bf16 v[16:19],a[32:35],v[32:35],v[16:19]
ds_read_b128 v[56:59],v2 offset:33792
v_mfma_f32_16x16x32_bf16 v[16:19],a[144:147],v[36:39],v[16:19]
buffer_load_dwordx4 v2,s[32:35],s63 offen offset:2048 sc1 lds
v_mfma_f32_16x16x32_bf16 v[20:23],a[148:151],v[36:39],v[20:23]
ds_read_b128 v[60:63],v2 offset:48128
v_mfma_f32_16x16x32_bf16 v[20:23],a[36:39],v[32:35],v[20:23]
s_waitcnt lgkmcnt(4)
v_mfma_f32_16x16x32_bf16 v[20:23],v[68:71],v[40:43],v[20:23]
ds_read_b128 v[32:35],v2 offset:6144
v_mfma_f32_16x16x32_bf16 v[20:23],v[180:183],v[44:47],v[20:23]
buffer_load_dwordx4 v2,s[32:35],s63 offen offset:3072 sc1 lds
v_mfma_f32_16x16x32_bf16 v[16:19],v[176:179],v[44:47],v[16:19]
ds_read_b128 v[36:39],v2 offset:20480
v_mfma_f32_16x16x32_bf16 v[16:19],v[64:67],v[40:43],v[16:19]
s_waitcnt lgkmcnt(4)
v_mfma_f32_16x16x32_bf16 v[16:19],a[40:43],v[48:51],v[16:19]
ds_read_b128 v[40:43],v2 offset:34816
s_add_u32 s63,s63,4096
v_mfma_f32_16x16x32_bf16 v[16:19],a[152:155],v[52:55],v[16:19]
s_add_u32 m0,s64,61440
v_mfma_f32_16x16x32_bf16 v[20:23],a[156:159],v[52:55],v[20:23]
ds_read_b128 v[44:47],v2 offset:49152
v_mfma_f32_16x16x32_bf16 v[20:23],a[44:47],v[48:51],v[20:23]
s_waitcnt lgkmcnt(4)
v_mfma_f32_16x16x32_bf16 v[20:23],v[76:79],v[56:59],v[20:23]
ds_read_b128 v[48:51],v2 offset:7168
v_mfma_f32_16x16x32_bf16 v[20:23],v[188:191],v[60:63],v[20:23]
buffer_load_dwordx4 v2,s[32:35],s63 offen offset:0 sc1 lds
v_mfma_f32_16x16x32_bf16 v[16:19],v[184:187],v[60:63],v[16:19]
ds_read_b128 v[52:55],v2 offset:21504
v_mfma_f32_16x16x32_bf16 v[16:19],v[72:75],v[56:59],v[16:19]
s_waitcnt lgkmcnt(4)
v_mfma_f32_16x16x32_bf16 v[16:19],a[48:51],v[32:35],v[16:19]
ds_read_b128 v[56:59],v2 offset:35840
v_mfma_f32_16x16x32_bf16 v[16:19],a[160:163],v[36:39],v[16:19]
buffer_load_dwordx4 v2,s[32:35],s63 offen offset:1024 sc1 lds
v_mfma_f32_16x16x32_bf16 v[20:23],a[164:167],v[36:39],v[20:23]
ds_read_b128 v[60:63],v2 offset:50176
v_mfma_f32_16x16x32_bf16 v[20:23],a[52:55],v[32:35],v[20:23]
s_waitcnt lgkmcnt(4)
v_mfma_f32_16x16x32_bf16 v[20:23],v[84:87],v[40:43],v[20:23]
ds_read_b128 v[32:35],v2 offset:8192
v_mfma_f32_16x16x32_bf16 v[20:23],v[196:199],v[44:47],v[20:23]
buffer_load_dwordx4 v2,s[32:35],s63 offen offset:2048 sc1 lds
v_mfma_f32_16x16x32_bf16 v[16:19],v[192:195],v[44:47],v[16:19]
ds_read_b128 v[36:39],v2 offset:22528
v_mfma_f32_16x16x32_bf16 v[16:19],v[80:83],v[40:43],v[16:19]
s_waitcnt lgkmcnt(4)
v_mfma_f32_16x16x32_bf16 v[16:19],a[56:59],v[48:51],v[16:19]
ds_read_b128 v[40:43],v2 offset:36864
v_mfma_f32_16x16x32_bf16 v[16:19],a[168:171],v[52:55],v[16:19]
buffer_load_dwordx4 v2,s[32:35],s63 offen offset:3072 sc1 lds
v_mfma_f32_16x16x32_bf16 v[20:23],a[172:175],v[52:55],v[20:23]
ds_read_b128 v[44:47],v2 offset:51200
v_mfma_f32_16x16x32_bf16 v[20:23],a[60:63],v[48:51],v[20:23]
s_waitcnt lgkmcnt(4)
v_mfma_f32_16x16x32_bf16 v[20:23],v[92:95],v[56:59],v[20:23]
ds_read_b128 v[48:51],v2 offset:9216
v_mfma_f32_16x16x32_bf16 v[20:23],v[204:207],v[60:63],v[20:23]
s_add_u32 s63,s63,4096
s_add_u32 m0,s64,65536
v_mfma_f32_16x16x32_bf16 v[16:19],v[200:203],v[60:63],v[16:19]
ds_read_b128 v[52:55],v2 offset:23552
v_mfma_f32_16x16x32_bf16 v[16:19],v[88:91],v[56:59],v[16:19]
s_waitcnt lgkmcnt(4)
v_mfma_f32_16x16x32_bf16 v[16:19],a[64:67],v[32:35],v[16:19]
ds_read_b128 v[56:59],v2 offset:37888
v_mfma_f32_16x16x32_bf16 v[16:19],a[176:179],v[36:39],v[16:19]
buffer_load_dwordx4 v2,s[32:35],s63 offen offset:0 sc1 lds
v_mfma_f32_16x16x32_bf16 v[20:23],a[180:183],v[36:39],v[20:23]
ds_read_b128 v[60:63],v2 offset:52224
v_mfma_f32_16x16x32_bf16 v[20:23],a[68:71],v[32:35],v[20:23]
s_waitcnt lgkmcnt(4)
v_mfma_f32_16x16x32_bf16 v[20:23],v[100:103],v[40:43],v[20:23]
ds_read_b128 v[32:35],v2 offset:10240
v_mfma_f32_16x16x32_bf16 v[20:23],v[212:215],v[44:47],v[20:23]
buffer_load_dwordx4 v2,s[32:35],s63 offen offset:1024 sc1 lds
v_mfma_f32_16x16x32_bf16 v[16:19],v[208:211],v[44:47],v[16:19]
ds_read_b128 v[36:39],v2 offset:24576
v_mfma_f32_16x16x32_bf16 v[16:19],v[96:99],v[40:43],v[16:19]
s_waitcnt lgkmcnt(4)
v_mfma_f32_16x16x32_bf16 v[16:19],a[72:75],v[48:51],v[16:19]
ds_read_b128 v[40:43],v2 offset:38912
v_mfma_f32_16x16x32_bf16 v[16:19],a[184:187],v[52:55],v[16:19]
buffer_load_dwordx4 v2,s[32:35],s63 offen offset:2048 sc1 lds
v_mfma_f32_16x16x32_bf16 v[20:23],a[188:191],v[52:55],v[20:23]
ds_read_b128 v[44:47],v2 offset:53248
v_mfma_f32_16x16x32_bf16 v[20:23],a[76:79],v[48:51],v[20:23]
s_waitcnt lgkmcnt(4)
v_mfma_f32_16x16x32_bf16 v[20:23],v[108:111],v[56:59],v[20:23]
ds_read_b128 v[48:51],v2 offset:11264
v_mfma_f32_16x16x32_bf16 v[20:23],v[220:223],v[60:63],v[20:23]
buffer_load_dwordx2 v[6:7],v0,s[32:35],s66 offen sc1
v_mfma_f32_16x16x32_bf16 v[16:19],v[216:219],v[60:63],v[16:19]
ds_read_b128 v[52:55],v2 offset:25600
v_mfma_f32_16x16x32_bf16 v[16:19],v[104:107],v[56:59],v[16:19]
s_waitcnt lgkmcnt(4)
v_mfma_f32_16x16x32_bf16 v[16:19],a[80:83],v[32:35],v[16:19]
ds_read_b128 v[56:59],v2 offset:39936
v_mfma_f32_16x16x32_bf16 v[16:19],a[192:195],v[36:39],v[16:19]
buffer_load_dwordx4 v2,s[32:35],s63 offen offset:3072 sc1 lds
s_add_u32 s63,s63,4096
v_mfma_f32_16x16x32_bf16 v[20:23],a[196:199],v[36:39],v[20:23]
ds_read_b128 v[60:63],v2 offset:54272
)#"
R"#(
s_add_u32 m0,s64,69632
v_mfma_f32_16x16x32_bf16 v[20:23],a[84:87],v[32:35],v[20:23]
s_waitcnt lgkmcnt(4)
v_mfma_f32_16x16x32_bf16 v[20:23],v[116:119],v[40:43],v[20:23]
ds_read_b128 v[32:35],v2 offset:12288
v_mfma_f32_16x16x32_bf16 v[20:23],v[228:231],v[44:47],v[20:23]
buffer_load_dwordx4 v2,s[32:35],s63 offen offset:0 sc1 lds
v_mfma_f32_16x16x32_bf16 v[16:19],v[224:227],v[44:47],v[16:19]
ds_read_b128 v[36:39],v2 offset:26624
v_mfma_f32_16x16x32_bf16 v[16:19],v[112:115],v[40:43],v[16:19]
s_waitcnt lgkmcnt(4)
v_mfma_f32_16x16x32_bf16 v[16:19],a[88:91],v[48:51],v[16:19]
ds_read_b128 v[40:43],v2 offset:40960
v_mfma_f32_16x16x32_bf16 v[16:19],a[200:203],v[52:55],v[16:19]
buffer_load_dwordx4 v2,s[32:35],s63 offen offset:1024 sc1 lds
s_add_u32 s63,s63,1175552
v_mfma_f32_16x16x32_bf16 v[20:23],a[204:207],v[52:55],v[20:23]
ds_read_b128 v[44:47],v2 offset:55296
v_mfma_f32_16x16x32_bf16 v[20:23],a[92:95],v[48:51],v[20:23]
s_waitcnt lgkmcnt(4)
v_mfma_f32_16x16x32_bf16 v[20:23],v[124:127],v[56:59],v[20:23]
ds_read_b128 v[48:51],v2 offset:13312
v_mfma_f32_16x16x32_bf16 v[20:23],v[236:239],v[60:63],v[20:23]
v_mfma_f32_16x16x32_bf16 v[16:19],v[232:235],v[60:63],v[16:19]
ds_read_b128 v[52:55],v2 offset:27648
v_mfma_f32_16x16x32_bf16 v[16:19],v[120:123],v[56:59],v[16:19]
s_waitcnt lgkmcnt(4)
v_mfma_f32_16x16x32_bf16 v[16:19],a[96:99],v[32:35],v[16:19]
ds_read_b128 v[56:59],v2 offset:41984
v_mfma_f32_16x16x32_bf16 v[16:19],a[208:211],v[36:39],v[16:19]
v_mfma_f32_16x16x32_bf16 v[20:23],a[212:215],v[36:39],v[20:23]
s_waitcnt vmcnt(11)
s_barrier
v_mfma_f32_16x16x32_bf16 v[20:23],a[100:103],v[32:35],v[20:23]
ds_read_b128 v[60:63],v2 offset:56320
s_waitcnt lgkmcnt(4)
v_mfma_f32_16x16x32_bf16 v[20:23],v[132:135],v[40:43],v[20:23]
ds_read_b128 v[32:35],v4 offset:0
v_mfma_f32_16x16x32_bf16 v[20:23],v[244:247],v[44:47],v[20:23]
v_mfma_f32_16x16x32_bf16 v[16:19],v[240:243],v[44:47],v[16:19]
ds_read_b128 v[36:39],v4 offset:14336
v_mfma_f32_16x16x32_bf16 v[16:19],v[128:131],v[40:43],v[16:19]
s_waitcnt lgkmcnt(4)
v_mfma_f32_16x16x32_bf16 v[16:19],a[104:107],v[48:51],v[16:19]
ds_read_b128 v[40:43],v4 offset:28672
v_mfma_f32_16x16x32_bf16 v[16:19],a[216:219],v[52:55],v[16:19]
v_mfma_f32_16x16x32_bf16 v[20:23],a[220:223],v[52:55],v[20:23]
ds_read_b128 v[44:47],v4 offset:43008
v_mfma_f32_16x16x32_bf16 v[20:23],a[108:111],v[48:51],v[20:23]
s_waitcnt lgkmcnt(4)
v_mfma_f32_16x16x32_bf16 v[20:23],v[140:143],v[56:59],v[20:23]
ds_read_b128 v[48:51],v4 offset:1024
v_mfma_f32_16x16x32_bf16 v[20:23],v[252:255],v[60:63],v[20:23]
v_mfma_f32_16x16x32_bf16 v[16:19],v[248:251],v[60:63],v[16:19]
ds_read_b128 v[52:55],v4 offset:15360
v_mfma_f32_16x16x32_bf16 v[16:19],v[136:139],v[56:59],v[16:19]
s_branch .L_dbl_1
.L_dbt_1:
s_nop 1
s_nop 0
v_cvt_pk_bf16_f32 v15,v30,v31
v_cvt_pk_bf16_f32 v14,v28,v29
s_nop 1
v_cvt_pk_bf16_f32 v13,v26,v27
v_cvt_pk_bf16_f32 v12,v24,v25
ds_write_b128 v5,v[12:15]
ds_read2st64_b32 v[12:13],v3 offset0:0 offset1:1
ds_read2st64_b32 v[14:15],v3 offset0:2 offset1:3
s_waitcnt lgkmcnt(0)
buffer_atomic_pk_add_bf16 v12,v1,s[44:47],s65 offen offset:0 sc1
buffer_atomic_pk_add_bf16 v13,v1,s[44:47],s65 offen offset:2048 sc1
s_add_u32 s65,s65,16384
buffer_atomic_pk_add_bf16 v14,v1,s[44:47],s65 offen offset:0 sc1
buffer_atomic_pk_add_bf16 v15,v1,s[44:47],s65 offen offset:2048 sc1
s_add_u32 s65,s65,16384
s_or_b32 s48,s28,s27
s_or_b32 s48,s48,s26
s_cbranch_scc1 .L_end
v_mov_b32 v8,s54
v_mov_b32 v9,s55
buffer_store_dwordx2 v[8:9],off,s[32:35],0 sc1
s_branch .L_end
.p2align 6
.L_dhv2:
.L_dal_2:
s_waitcnt lgkmcnt(4)
v_mfma_f32_16x16x32_bf16 v[16:19],a[0:3],v[32:35],0
ds_read_b128 v[56:59],v2 offset:29696
v_dot2_f32_bf16 v10,v12,s72,0
v_mfma_f32_16x16x32_bf16 v[16:19],a[112:115],v[36:39],v[16:19]
v_dot2_f32_bf16 v11,v12,v12,0
v_dot2_f32_bf16 v10,v13,s72,v10
v_mfma_f32_16x16x32_bf16 v[20:23],a[116:119],v[36:39],0
ds_read_b128 v[60:63],v2 offset:44032
v_dot2_f32_bf16 v11,v13,v13,v11
v_mfma_f32_16x16x32_bf16 v[20:23],a[4:7],v[32:35],v[20:23]
v_dot2_f32_bf16 v10,v14,s72,v10
s_waitcnt lgkmcnt(4)
v_mfma_f32_16x16x32_bf16 v[20:23],a[228:231],v[40:43],v[20:23]
ds_read_b128 v[32:35],v2 offset:2048
v_dot2_f32_bf16 v11,v14,v14,v11
v_mfma_f32_16x16x32_bf16 v[20:23],v[148:151],v[44:47],v[20:23]
v_dot2_f32_bf16 v10,v15,s72,v10
v_dot2_f32_bf16 v11,v15,v15,v11
v_mfma_f32_16x16x32_bf16 v[16:19],v[144:147],v[44:47],v[16:19]
ds_read_b128 v[36:39],v2 offset:16384
v_mfma_f32_16x16x32_bf16 v[16:19],a[224:227],v[40:43],v[16:19]
v_permlane16_swap_b32 v10,v11
s_waitcnt lgkmcnt(4)
v_mfma_f32_16x16x32_bf16 v[16:19],a[8:11],v[48:51],v[16:19]
ds_read_b128 v[40:43],v2 offset:30720
v_add_f32 v10,v10,v11
v_mfma_f32_16x16x32_bf16 v[16:19],a[120:123],v[52:55],v[16:19]
v_mfma_f32_16x16x32_bf16 v[20:23],a[124:127],v[52:55],v[20:23]
ds_read_b128 v[44:47],v2 offset:45056
v_permlane32_swap_b32 v10,v11
v_mfma_f32_16x16x32_bf16 v[20:23],a[12:15],v[48:51],v[20:23]
v_add_f32 v10,v10,v11
s_waitcnt lgkmcnt(4)
v_mfma_f32_16x16x32_bf16 v[20:23],a[236:239],v[56:59],v[20:23]
ds_read_b128 v[48:51],v2 offset:3072
v_mfma_f32_16x16x32_bf16 v[20:23],v[156:159],v[60:63],v[20:23]
buffer_store_dword v10,v3,s[32:35],s67 offen offset:1024 sc1
v_mfma_f32_16x16x32_bf16 v[16:19],v[152:155],v[60:63],v[16:19]
ds_read_b128 v[52:55],v2 offset:17408
v_mfma_f32_16x16x32_bf16 v[16:19],a[232:235],v[56:59],v[16:19]
s_waitcnt lgkmcnt(4)
v_mfma_f32_16x16x32_bf16 v[16:19],a[16:19],v[32:35],v[16:19]
ds_read_b128 v[56:59],v2 offset:31744
v_mfma_f32_16x16x32_bf16 v[16:19],a[128:131],v[36:39],v[16:19]
s_waitcnt vmcnt(2)
s_barrier
v_mfma_f32_16x16x32_bf16 v[20:23],a[132:135],v[36:39],v[20:23]
ds_read_b128 v[60:63],v2 offset:46080
v_mfma_f32_16x16x32_bf16 v[20:23],a[20:23],v[32:35],v[20:23]
s_waitcnt lgkmcnt(4)
v_mfma_f32_16x16x32_bf16 v[20:23],a[244:247],v[40:43],v[20:23]
ds_read_b128 v[32:35],v2 offset:4096
s_mov_b32 m0,s74
v_mfma_f32_16x16x32_bf16 v[20:23],v[164:167],v[44:47],v[20:23]
buffer_load_dwordx4 v2,s[32:35],s71 offen offset:0 sc1 lds
v_mfma_f32_16x16x32_bf16 v[16:19],v[160:163],v[44:47],v[16:19]
ds_read_b128 v[36:39],v2 offset:18432
v_mfma_f32_16x16x32_bf16 v[16:19],a[240:243],v[40:43],v[16:19]
s_waitcnt lgkmcnt(4)
v_mfma_f32_16x16x32_bf16 v[16:19],a[24:27],v[48:51],v[16:19]
ds_read_b128 v[40:43],v2 offset:32768
v_mfma_f32_16x16x32_bf16 v[16:19],a[136:139],v[52:55],v[16:19]
buffer_load_dwordx4 v2,s[32:35],s71 offen offset:1024 sc1 lds
s_add_u32 s71,s71,1187840
v_mfma_f32_16x16x32_bf16 v[20:23],a[140:143],v[52:55],v[20:23]
ds_read_b128 v[44:47],v2 offset:47104
s_add_u32 m0,s64,57344
v_mfma_f32_16x16x32_bf16 v[20:23],a[28:31],v[48:51],v[20:23]
s_waitcnt lgkmcnt(4)
v_mfma_f32_16x16x32_bf16 v[20:23],a[252:255],v[56:59],v[20:23]
ds_read_b128 v[48:51],v2 offset:5120
v_mfma_f32_16x16x32_bf16 v[20:23],v[172:175],v[60:63],v[20:23]
buffer_load_dwordx4 v2,s[32:35],s63 offen offset:0 sc1 lds
v_mfma_f32_16x16x32_bf16 v[16:19],v[168:171],v[60:63],v[16:19]
ds_read_b128 v[52:55],v2 offset:19456
v_mfma_f32_16x16x32_bf16 v[16:19],a[248:251],v[56:59],v[16:19]
s_waitcnt lgkmcnt(4)
v_mfma_f32_16x16x32_bf16 v[16:19],a[32:35],v[32:35],v[16:19]
ds_read_b128 v[56:59],v2 offset:33792
v_mfma_f32_16x16x32_bf16 v[16:19],a[144:147],v[36:39],v[16:19]
buffer_load_dwordx4 v2,s[32:35],s63 offen offset:1024 sc1 lds
v_mfma_f32_16x16x32_bf16 v[20:23],a[148:151],v[36:39],v[20:23]
ds_read_b128 v[60:63],v2 offset:48128
v_mfma_f32_16x16x32_bf16 v[20:23],a[36:39],v[32:35],v[20:23]
s_waitcnt lgkmcnt(4)
v_mfma_f32_16x16x32_bf16 v[20:23],v[68:71],v[40:43],v[20:23]
ds_read_b128 v[32:35],v2 offset:6144
v_mfma_f32_16x16x32_bf16 v[20:23],v[180:183],v[44:47],v[20:23]
buffer_load_dwordx4 v2,s[32:35],s63 offen offset:2048 sc1 lds
v_mfma_f32_16x16x32_bf16 v[16:19],v[176:179],v[44:47],v[16:19]
ds_read_b128 v[36:39],v2 offset:20480
v_mfma_f32_16x16x32_bf16 v[16:19],v[64:67],v[40:43],v[16:19]
s_waitcnt lgkmcnt(4)
v_mfma_f32_16x16x32_bf16 v[16:19],a[40:43],v[48:51],v[16:19]
ds_read_b128 v[40:43],v2 offset:34816
v_mfma_f32_16x16x32_bf16 v[16:19],a[152:155],v[52:55],v[16:19]
buffer_load_dwordx4 v2,s[32:35],s63 offen offset:3072 sc1 lds
s_add_u32 s63,s63,4096
v_mfma_f32_16x16x32_bf16 v[20:23],a[156:159],v[52:55],v[20:23]
ds_read_b128 v[44:47],v2 offset:49152
s_add_u32 m0,s64,61440
v_mfma_f32_16x16x32_bf16 v[20:23],a[44:47],v[48:51],v[20:23]
s_waitcnt lgkmcnt(4)
v_mfma_f32_16x16x32_bf16 v[20:23],v[76:79],v[56:59],v[20:23]
ds_read_b128 v[48:51],v2 offset:7168
v_mfma_f32_16x16x32_bf16 v[20:23],v[188:191],v[60:63],v[20:23]
buffer_load_dwordx4 v2,s[32:35],s63 offen offset:0 sc1 lds
v_mfma_f32_16x16x32_bf16 v[16:19],v[184:187],v[60:63],v[16:19]
ds_read_b128 v[52:55],v2 offset:21504
v_mfma_f32_16x16x32_bf16 v[16:19],v[72:75],v[56:59],v[16:19]
s_waitcnt lgkmcnt(4)
v_mfma_f32_16x16x32_bf16 v[16:19],a[48:51],v[32:35],v[16:19]
ds_read_b128 v[56:59],v2 offset:35840
v_mfma_f32_16x16x32_bf16 v[16:19],a[160:163],v[36:39],v[16:19]
buffer_load_dwordx4 v2,s[32:35],s63 offen offset:1024 sc1 lds
v_mfma_f32_16x16x32_bf16 v[20:23],a[164:167],v[36:39],v[20:23]
ds_read_b128 v[60:63],v2 offset:50176
v_mfma_f32_16x16x32_bf16 v[20:23],a[52:55],v[32:35],v[20:23]
s_waitcnt lgkmcnt(4)
v_mfma_f32_16x16x32_bf16 v[20:23],v[84:87],v[40:43],v[20:23]
ds_read_b128 v[32:35],v2 offset:8192
v_mfma_f32_16x16x32_bf16 v[20:23],v[196:199],v[44:47],v[20:23]
buffer_load_dwordx4 v2,s[32:35],s63 offen offset:2048 sc1 lds
v_mfma_f32_16x16x32_bf16 v[16:19],v[192:195],v[44:47],v[16:19]
ds_read_b128 v[36:39],v2 offset:22528
v_mfma_f32_16x16x32_bf16 v[16:19],v[80:83],v[40:43],v[16:19]
s_waitcnt lgkmcnt(4)
v_mfma_f32_16x16x32_bf16 v[16:19],a[56:59],v[48:51],v[16:19]
ds_read_b128 v[40:43],v2 offset:36864
v_mfma_f32_16x16x32_bf16 v[16:19],a[168:171],v[52:55],v[16:19]
buffer_load_dwordx4 v2,s[32:35],s63 offen offset:3072 sc1 lds
v_mfma_f32_16x16x32_bf16 v[20:23],a[172:175],v[52:55],v[20:23]
ds_read_b128 v[44:47],v2 offset:51200
v_mfma_f32_16x16x32_bf16 v[20:23],a[60:63],v[48:51],v[20:23]
s_waitcnt lgkmcnt(4)
v_mfma_f32_16x16x32_bf16 v[20:23],v[92:95],v[56:59],v[20:23]
ds_read_b128 v[48:51],v2 offset:9216
v_mfma_f32_16x16x32_bf16 v[20:23],v[204:207],v[60:63],v[20:23]
s_add_u32 s63,s63,4096
s_add_u32 m0,s64,65536
v_mfma_f32_16x16x32_bf16 v[16:19],v[200:203],v[60:63],v[16:19]
ds_read_b128 v[52:55],v2 offset:23552
v_mfma_f32_16x16x32_bf16 v[16:19],v[88:91],v[56:59],v[16:19]
s_waitcnt lgkmcnt(4)
v_mfma_f32_16x16x32_bf16 v[16:19],a[64:67],v[32:35],v[16:19]
ds_read_b128 v[56:59],v2 offset:37888
v_mfma_f32_16x16x32_bf16 v[16:19],a[176:179],v[36:39],v[16:19]
buffer_load_dwordx4 v2,s[32:35],s63 offen offset:0 sc1 lds
v_mfma_f32_16x16x32_bf16 v[20:23],a[180:183],v[36:39],v[20:23]
ds_read_b128 v[60:63],v2 offset:52224
v_mfma_f32_16x16x32_bf16 v[20:23],a[68:71],v[32:35],v[20:23]
s_waitcnt lgkmcnt(4)
v_mfma_f32_16x16x32_bf16 v[20:23],v[100:103],v[40:43],v[20:23]
ds_read_b128 v[32:35],v2 offset:10240
v_mfma_f32_16x16x32_bf16 v[20:23],v[212:215],v[44:47],v[20:23]
buffer_load_dwordx4 v2,s[32:35],s63 offen offset:1024 sc1 lds
v_mfma_f32_16x16x32_bf16 v[16:19],v[208:211],v[44:47],v[16:19]
ds_read_b128 v[36:39],v2 offset:24576
v_mfma_f32_16x16x32_bf16 v[16:19],v[96:99],v[40:43],v[16:19]
s_waitcnt lgkmcnt(4)
v_mfma_f32_16x16x32_bf16 v[16:19],a[72:75],v[48:51],v[16:19]
ds_read_b128 v[40:43],v2 offset:38912
s_waitcnt vmcnt(10)
v_mfma_f32_16x16x32_bf16 v[16:19],a[184:187],v[52:55],v[16:19]
buffer_load_dwordx4 v2,s[32:35],s63 offen offset:2048 sc1 lds
ds_read_b128 v[12:15],v5 offset:0
v_mfma_f32_16x16x32_bf16 v[20:23],a[188:191],v[52:55],v[20:23]
ds_read_b128 v[44:47],v2 offset:53248
v_mfma_f32_16x16x32_bf16 v[20:23],a[76:79],v[48:51],v[20:23]
s_waitcnt lgkmcnt(5)
v_mfma_f32_16x16x32_bf16 v[20:23],v[108:111],v[56:59],v[20:23]
ds_read_b128 v[48:51],v2 offset:11264
v_mfma_f32_16x16x32_bf16 v[20:23],v[220:223],v[60:63],v[20:23]
v_mfma_f32_16x16x32_bf16 v[16:19],v[216:219],v[60:63],v[16:19]
ds_read_b128 v[52:55],v2 offset:25600
s_waitcnt lgkmcnt(3)
v_mfma_f32_16x16x32_bf16 v[16:19],v[104:107],v[56:59],v[16:19]
v_add_f32 v24,v24,v12
v_mfma_f32_16x16x32_bf16 v[16:19],a[80:83],v[32:35],v[16:19]
ds_read_b128 v[56:59],v2 offset:39936
v_add_f32 v25,v25,v13
v_mfma_f32_16x16x32_bf16 v[16:19],a[192:195],v[36:39],v[16:19]
buffer_load_dwordx4 v2,s[32:35],s63 offen offset:3072 sc1 lds
s_add_u32 s63,s63,4096
v_mfma_f32_16x16x32_bf16 v[20:23],a[196:199],v[36:39],v[20:23]
ds_read_b128 v[60:63],v2 offset:54272
s_add_u32 m0,s64,69632
v_mfma_f32_16x16x32_bf16 v[20:23],a[84:87],v[32:35],v[20:23]
v_add_f32 v26,v26,v14
s_waitcnt lgkmcnt(4)
v_mfma_f32_16x16x32_bf16 v[20:23],v[116:119],v[40:43],v[20:23]
ds_read_b128 v[32:35],v2 offset:12288
v_add_f32 v27,v27,v15
v_mfma_f32_16x16x32_bf16 v[20:23],v[228:231],v[44:47],v[20:23]
buffer_load_dwordx4 v2,s[32:35],s63 offen offset:0 sc1 lds
ds_read_b128 v[12:15],v5 offset:1024
v_mfma_f32_16x16x32_bf16 v[16:19],v[224:227],v[44:47],v[16:19]
ds_read_b128 v[36:39],v2 offset:26624
v_mfma_f32_16x16x32_bf16 v[16:19],v[112:115],v[40:43],v[16:19]
s_waitcnt lgkmcnt(5)
v_mfma_f32_16x16x32_bf16 v[16:19],a[88:91],v[48:51],v[16:19]
ds_read_b128 v[40:43],v2 offset:40960
v_mfma_f32_16x16x32_bf16 v[16:19],a[200:203],v[52:55],v[16:19]
buffer_load_dwordx4 v2,s[32:35],s63 offen offset:1024 sc1 lds
s_add_u32 s63,s63,1175552
v_mfma_f32_16x16x32_bf16 v[20:23],a[204:207],v[52:55],v[20:23]
ds_read_b128 v[44:47],v2 offset:55296
s_waitcnt lgkmcnt(3)
v_mfma_f32_16x16x32_bf16 v[20:23],a[92:95],v[48:51],v[20:23]
v_add_f32 v28,v28,v12
v_mfma_f32_16x16x32_bf16 v[20:23],v[124:127],v[56:59],v[20:23]
ds_read_b128 v[48:51],v2 offset:13312
v_add_f32 v29,v29,v13
v_mfma_f32_16x16x32_bf16 v[20:23],v[236:239],v[60:63],v[20:23]
v_add_f32 v30,v30,v14
v_add_f32 v31,v31,v15
v_mfma_f32_16x16x32_bf16 v[16:19],v[232:235],v[60:63],v[16:19]
ds_read_b128 v[52:55],v2 offset:27648
v_cvt_pk_bf16_f32 v12,v24,v25
v_mfma_f32_16x16x32_bf16 v[16:19],v[120:123],v[56:59],v[16:19]
v_cvt_pk_bf16_f32 v13,v26,v27
s_waitcnt lgkmcnt(4)
v_mfma_f32_16x16x32_bf16 v[16:19],a[96:99],v[32:35],v[16:19]
ds_read_b128 v[56:59],v2 offset:41984
v_cvt_pk_bf16_f32 v14,v28,v29
v_mfma_f32_16x16x32_bf16 v[16:19],a[208:211],v[36:39],v[16:19]
s_waitcnt vmcnt(10)
s_barrier
v_mfma_f32_16x16x32_bf16 v[20:23],a[212:215],v[36:39],v[20:23]
ds_read_b128 v[60:63],v2 offset:56320
v_cvt_pk_bf16_f32 v15,v30,v31
v_mfma_f32_16x16x32_bf16 v[20:23],a[100:103],v[32:35],v[20:23]
s_waitcnt lgkmcnt(4)
v_mfma_f32_16x16x32_bf16 v[20:23],v[132:135],v[40:43],v[20:23]
ds_read_b128 v[32:35],v4 offset:0
v_mfma_f32_16x16x32_bf16 v[20:23],v[244:247],v[44:47],v[20:23]
buffer_store_dwordx4 v[12:15],v2,s[32:35],s68 offen sc1
v_mfma_f32_16x16x32_bf16 v[16:19],v[240:243],v[44:47],v[16:19]
ds_read_b128 v[36:39],v4 offset:14336
v_mfma_f32_16x16x32_bf16 v[16:19],v[128:131],v[40:43],v[16:19]
s_waitcnt lgkmcnt(4)
v_mfma_f32_16x16x32_bf16 v[16:19],a[104:107],v[48:51],v[16:19]
ds_read_b128 v[40:43],v4 offset:28672
v_mfma_f32_16x16x32_bf16 v[16:19],a[216:219],v[52:55],v[16:19]
v_mfma_f32_16x16x32_bf16 v[20:23],a[220:223],v[52:55],v[20:23]
ds_read_b128 v[44:47],v4 offset:43008
s_mov_b32 s67,s68
v_mfma_f32_16x16x32_bf16 v[20:23],a[108:111],v[48:51],v[20:23]
s_mov_b32 s68,s70
s_waitcnt lgkmcnt(4)
v_mfma_f32_16x16x32_bf16 v[20:23],v[140:143],v[56:59],v[20:23]
ds_read_b128 v[48:51],v4 offset:1024
s_add_u32 s70,s70,1187840
v_mfma_f32_16x16x32_bf16 v[20:23],v[252:255],v[60:63],v[20:23]
v_mfma_f32_16x16x32_bf16 v[16:19],v[248:251],v[60:63],v[16:19]
ds_read_b128 v[52:55],v4 offset:15360
s_sub_u32 s62,s62,1
v_mfma_f32_16x16x32_bf16 v[16:19],v[136:139],v[56:59],v[16:19]
s_cbranch_scc1 .L_dab_2
s_waitcnt lgkmcnt(4)
v_mfma_f32_16x16x32_bf16 v[24:27],a[0:3],v[32:35],0
ds_read_b128 v[56:59],v4 offset:29696
v_dot2_f32_bf16 v10,v12,s72,0
v_mfma_f32_16x16x32_bf16 v[24:27],a[112:115],v[36:39],v[24:27]
v_dot2_f32_bf16 v11,v12,v12,0
v_dot2_f32_bf16 v10,v13,s72,v10
v_mfma_f32_16x16x32_bf16 v[28:31],a[116:119],v[36:39],0
ds_read_b128 v[60:63],v4 offset:44032
v_dot2_f32_bf16 v11,v13,v13,v11
v_mfma_f32_16x16x32_bf16 v[28:31],a[4:7],v[32:35],v[28:31]
v_dot2_f32_bf16 v10,v14,s72,v10
s_waitcnt lgkmcnt(4)
v_mfma_f32_16x16x32_bf16 v[28:31],a[228:231],v[40:43],v[28:31]
ds_read_b128 v[32:35],v4 offset:2048
v_dot2_f32_bf16 v11,v14,v14,v11
v_mfma_f32_16x16x32_bf16 v[28:31],v[148:151],v[44:47],v[28:31]
v_dot2_f32_bf16 v10,v15,s72,v10
v_dot2_f32_bf16 v11,v15,v15,v11
v_mfma_f32_16x16x32_bf16 v[24:27],v[144:147],v[44:47],v[24:27]
ds_read_b128 v[36:39],v4 offset:16384
v_mfma_f32_16x16x32_bf16 v[24:27],a[224:227],v[40:43],v[24:27]
v_permlane16_swap_b32 v10,v11
s_waitcnt lgkmcnt(4)
v_mfma_f32_16x16x32_bf16 v[24:27],a[8:11],v[48:51],v[24:27]
ds_read_b128 v[40:43],v4 offset:30720
v_add_f32 v10,v10,v11
v_mfma_f32_16x16x32_bf16 v[24:27],a[120:123],v[52:55],v[24:27]
v_mfma_f32_16x16x32_bf16 v[28:31],a[124:127],v[52:55],v[28:31]
ds_read_b128 v[44:47],v4 offset:45056
v_permlane32_swap_b32 v10,v11
v_mfma_f32_16x16x32_bf16 v[28:31],a[12:15],v[48:51],v[28:31]
v_add_f32 v10,v10,v11
s_waitcnt lgkmcnt(4)
v_mfma_f32_16x16x32_bf16 v[28:31],a[236:239],v[56:59],v[28:31]
ds_read_b128 v[48:51],v4 offset:3072
v_mfma_f32_16x16x32_bf16 v[28:31],v[156:159],v[60:63],v[28:31]
buffer_store_dword v10,v3,s[32:35],s67 offen offset:1024 sc1
v_mfma_f32_16x16x32_bf16 v[24:27],v[152:155],v[60:63],v[24:27]
ds_read_b128 v[52:55],v4 offset:17408
v_mfma_f32_16x16x32_bf16 v[24:27],a[232:235],v[56:59],v[24:27]
s_waitcnt lgkmcnt(4)
v_mfma_f32_16x16x32_bf16 v[24:27],a[16:19],v[32:35],v[24:27]
ds_read_b128 v[56:59],v4 offset:31744
v_mfma_f32_16x16x32_bf16 v[24:27],a[128:131],v[36:39],v[24:27]
s_waitcnt vmcnt(2)
s_barrier
v_mfma_f32_16x16x32_bf16 v[28:31],a[132:135],v[36:39],v[28:31]
ds_read_b128 v[60:63],v4 offset:46080
v_mfma_f32_16x16x32_bf16 v[28:31],a[20:23],v[32:35],v[28:31]
s_waitcnt lgkmcnt(4)
v_mfma_f32_16x16x32_bf16 v[28:31],a[244:247],v[40:43],v[28:31]
ds_read_b128 v[32:35],v4 offset:4096
s_mov_b32 m0,s74
v_mfma_f32_16x16x32_bf16 v[28:31],v[164:167],v[44:47],v[28:31]
buffer_load_dwordx4 v2,s[32:35],s71 offen offset:0 sc1 lds
v_mfma_f32_16x16x32_bf16 v[24:27],v[160:163],v[44:47],v[24:27]
ds_read_b128 v[36:39],v4 offset:18432
v_mfma_f32_16x16x32_bf16 v[24:27],a[240:243],v[40:43],v[24:27]
s_waitcnt lgkmcnt(4)
v_mfma_f32_16x16x32_bf16 v[24:27],a[24:27],v[48:51],v[24:27]
ds_read_b128 v[40:43],v4 offset:32768
v_mfma_f32_16x16x32_bf16 v[24:27],a[136:139],v[52:55],v[24:27]
buffer_load_dwordx4 v2,s[32:35],s71 offen offset:1024 sc1 lds
s_add_u32 s71,s71,1187840
v_mfma_f32_16x16x32_bf16 v[28:31],a[140:143],v[52:55],v[28:31]
ds_read_b128 v[44:47],v4 offset:47104
s_add_u32 m0,s64,0
v_mfma_f32_16x16x32_bf16 v[28:31],a[28:31],v[48:51],v[28:31]
s_waitcnt lgkmcnt(4)
v_mfma_f32_16x16x32_bf16 v[28:31],a[252:255],v[56:59],v[28:31]
ds_read_b128 v[48:51],v4 offset:5120
v_mfma_f32_16x16x32_bf16 v[28:31],v[172:175],v[60:63],v[28:31]
buffer_load_dwordx4 v2,s[32:35],s63 offen offset:0 sc1 lds
v_mfma_f32_16x16x32_bf16 v[24:27],v[168:171],v[60:63],v[24:27]
ds_read_b128 v[52:55],v4 offset:19456
v_mfma_f32_16x16x32_bf16 v[24:27],a[248:251],v[56:59],v[24:27]
s_waitcnt lgkmcnt(4)
v_mfma_f32_16x16x32_bf16 v[24:27],a[32:35],v[32:35],v[24:27]
ds_read_b128 v[56:59],v4 offset:33792
v_mfma_f32_16x16x32_bf16 v[24:27],a[144:147],v[36:39],v[24:27]
buffer_load_dwordx4 v2,s[32:35],s63 offen offset:1024 sc1 lds
v_mfma_f32_16x16x32_bf16 v[28:31],a[148:151],v[36:39],v[28:31]
ds_read_b128 v[60:63],v4 offset:48128
v_mfma_f32_16x16x32_bf16 v[28:31],a[36:39],v[32:35],v[28:31]
s_waitcnt lgkmcnt(4)
v_mfma_f32_16x16x32_bf16 v[28:31],v[68:71],v[40:43],v[28:31]
ds_read_b128 v[32:35],v4 offset:6144
v_mfma_f32_16x16x32_bf16 v[28:31],v[180:183],v[44:47],v[28:31]
buffer_load_dwordx4 v2,s[32:35],s63 offen offset:2048 sc1 lds
v_mfma_f32_16x16x32_bf16 v[24:27],v[176:179],v[44:47],v[24:27]
ds_read_b128 v[36:39],v4 offset:20480
v_mfma_f32_16x16x32_bf16 v[24:27],v[64:67],v[40:43],v[24:27]
s_waitcnt lgkmcnt(4)
v_mfma_f32_16x16x32_bf16 v[24:27],a[40:43],v[48:51],v[24:27]
ds_read_b128 v[40:43],v4 offset:34816
v_mfma_f32_16x16x32_bf16 v[24:27],a[152:155],v[52:55],v[24:27]
buffer_load_dwordx4 v2,s[32:35],s63 offen offset:3072 sc1 lds
s_add_u32 s63,s63,4096
v_mfma_f32_16x16x32_bf16 v[28:31],a[156:159],v[52:55],v[28:31]
ds_read_b128 v[44:47],v4 offset:49152
s_add_u32 m0,s64,4096
v_mfma_f32_16x16x32_bf16 v[28:31],a[44:47],v[48:51],v[28:31]
s_waitcnt lgkmcnt(4)
v_mfma_f32_16x16x32_bf16 v[28:31],v[76:79],v[56:59],v[28:31]
ds_read_b128 v[48:51],v4 offset:7168
v_mfma_f32_16x16x32_bf16 v[28:31],v[188:191],v[60:63],v[28:31]
buffer_load_dwordx4 v2,s[32:35],s63 offen offset:0 sc1 lds
v_mfma_f32_16x16x32_bf16 v[24:27],v[184:187],v[60:63],v[24:27]
ds_read_b128 v[52:55],v4 offset:21504
v_mfma_f32_16x16x32_bf16 v[24:27],v[72:75],v[56:59],v[24:27]
s_waitcnt lgkmcnt(4)
v_mfma_f32_16x16x32_bf16 v[24:27],a[48:51],v[32:35],v[24:27]
ds_read_b128 v[56:59],v4 offset:35840
v_mfma_f32_16x16x32_bf16 v[24:27],a[160:163],v[36:39],v[24:27]
buffer_load_dwordx4 v2,s[32:35],s63 offen offset:1024 sc1 lds
v_mfma_f32_16x16x32_bf16 v[28:31],a[164:167],v[36:39],v[28:31]
ds_read_b128 v[60:63],v4 offset:50176
v_mfma_f32_16x16x32_bf16 v[28:31],a[52:55],v[32:35],v[28:31]
s_waitcnt lgkmcnt(4)
v_mfma_f32_16x16x32_bf16 v[28:31],v[84:87],v[40:43],v[28:31]
ds_read_b128 v[32:35],v4 offset:8192
v_mfma_f32_16x16x32_bf16 v[28:31],v[196:199],v[44:47],v[28:31]
buffer_load_dwordx4 v2,s[32:35],s63 offen offset:2048 sc1 lds
v_mfma_f32_16x16x32_bf16 v[24:27],v[192:195],v[44:47],v[24:27]
ds_read_b128 v[36:39],v4 offset:22528
v_mfma_f32_16x16x32_bf16 v[24:27],v[80:83],v[40:43],v[24:27]
s_waitcnt lgkmcnt(4)
v_mfma_f32_16x16x32_bf16 v[24:27],a[56:59],v[48:51],v[24:27]
ds_read_b128 v[40:43],v4 offset:36864
v_mfma_f32_16x16x32_bf16 v[24:27],a[168:171],v[52:55],v[24:27]
buffer_load_dwordx4 v2,s[32:35],s63 offen offset:3072 sc1 lds
v_mfma_f32_16x16x32_bf16 v[28:31],a[172:175],v[52:55],v[28:31]
ds_read_b128 v[44:47],v4 offset:51200
v_mfma_f32_16x16x32_bf16 v[28:31],a[60:63],v[48:51],v[28:31]
s_waitcnt lgkmcnt(4)
v_mfma_f32_16x16x32_bf16 v[28:31],v[92:95],v[56:59],v[28:31]
ds_read_b128 v[48:51],v4 offset:9216
v_mfma_f32_16x16x32_bf16 v[28:31],v[204:207],v[60:63],v[28:31]
s_add_u32 s63,s63,4096
s_add_u32 m0,s64,8192
v_mfma_f32_16x16x32_bf16 v[24:27],v[200:203],v[60:63],v[24:27]
ds_read_b128 v[52:55],v4 offset:23552
v_mfma_f32_16x16x32_bf16 v[24:27],v[88:91],v[56:59],v[24:27]
s_waitcnt lgkmcnt(4)
v_mfma_f32_16x16x32_bf16 v[24:27],a[64:67],v[32:35],v[24:27]
ds_read_b128 v[56:59],v4 offset:37888
v_mfma_f32_16x16x32_bf16 v[24:27],a[176:179],v[36:39],v[24:27]
buffer_load_dwordx4 v2,s[32:35],s63 offen offset:0 sc1 lds
v_mfma_f32_16x16x32_bf16 v[28:31],a[180:183],v[36:39],v[28:31]
ds_read_b128 v[60:63],v4 offset:52224
v_mfma_f32_16x16x32_bf16 v[28:31],a[68:71],v[32:35],v[28:31]
s_waitcnt lgkmcnt(4)
v_mfma_f32_16x16x32_bf16 v[28:31],v[100:103],v[40:43],v[28:31]
ds_read_b128 v[32:35],v4 offset:10240
)#"
R"#(
v_mfma_f32_16x16x32_bf16 v[28:31],v[212:215],v[44:47],v[28:31]
buffer_load_dwordx4 v2,s[32:35],s63 offen offset:1024 sc1 lds
v_mfma_f32_16x16x32_bf16 v[24:27],v[208:211],v[44:47],v[24:27]
ds_read_b128 v[36:39],v4 offset:24576
v_mfma_f32_16x16x32_bf16 v[24:27],v[96:99],v[40:43],v[24:27]
s_waitcnt lgkmcnt(4)
v_mfma_f32_16x16x32_bf16 v[24:27],a[72:75],v[48:51],v[24:27]
ds_read_b128 v[40:43],v4 offset:38912
s_waitcnt vmcnt(10)
v_mfma_f32_16x16x32_bf16 v[24:27],a[184:187],v[52:55],v[24:27]
buffer_load_dwordx4 v2,s[32:35],s63 offen offset:2048 sc1 lds
ds_read_b128 v[12:15],v5 offset:0
v_mfma_f32_16x16x32_bf16 v[28:31],a[188:191],v[52:55],v[28:31]
ds_read_b128 v[44:47],v4 offset:53248
v_mfma_f32_16x16x32_bf16 v[28:31],a[76:79],v[48:51],v[28:31]
s_waitcnt lgkmcnt(5)
v_mfma_f32_16x16x32_bf16 v[28:31],v[108:111],v[56:59],v[28:31]
ds_read_b128 v[48:51],v4 offset:11264
v_mfma_f32_16x16x32_bf16 v[28:31],v[220:223],v[60:63],v[28:31]
v_mfma_f32_16x16x32_bf16 v[24:27],v[216:219],v[60:63],v[24:27]
ds_read_b128 v[52:55],v4 offset:25600
s_waitcnt lgkmcnt(3)
v_mfma_f32_16x16x32_bf16 v[24:27],v[104:107],v[56:59],v[24:27]
v_add_f32 v16,v16,v12
v_mfma_f32_16x16x32_bf16 v[24:27],a[80:83],v[32:35],v[24:27]
ds_read_b128 v[56:59],v4 offset:39936
v_add_f32 v17,v17,v13
v_mfma_f32_16x16x32_bf16 v[24:27],a[192:195],v[36:39],v[24:27]
buffer_load_dwordx4 v2,s[32:35],s63 offen offset:3072 sc1 lds
s_add_u32 s63,s63,4096
v_mfma_f32_16x16x32_bf16 v[28:31],a[196:199],v[36:39],v[28:31]
ds_read_b128 v[60:63],v4 offset:54272
s_add_u32 m0,s64,12288
v_mfma_f32_16x16x32_bf16 v[28:31],a[84:87],v[32:35],v[28:31]
v_add_f32 v18,v18,v14
s_waitcnt lgkmcnt(4)
v_mfma_f32_16x16x32_bf16 v[28:31],v[116:119],v[40:43],v[28:31]
ds_read_b128 v[32:35],v4 offset:12288
v_add_f32 v19,v19,v15
v_mfma_f32_16x16x32_bf16 v[28:31],v[228:231],v[44:47],v[28:31]
buffer_load_dwordx4 v2,s[32:35],s63 offen offset:0 sc1 lds
ds_read_b128 v[12:15],v5 offset:1024
v_mfma_f32_16x16x32_bf16 v[24:27],v[224:227],v[44:47],v[24:27]
ds_read_b128 v[36:39],v4 offset:26624
v_mfma_f32_16x16x32_bf16 v[24:27],v[112:115],v[40:43],v[24:27]
s_waitcnt lgkmcnt(5)
v_mfma_f32_16x16x32_bf16 v[24:27],a[88:91],v[48:51],v[24:27]
ds_read_b128 v[40:43],v4 offset:40960
v_mfma_f32_16x16x32_bf16 v[24:27],a[200:203],v[52:55],v[24:27]
buffer_load_dwordx4 v2,s[32:35],s63 offen offset:1024 sc1 lds
s_add_u32 s63,s63,1175552
v_mfma_f32_16x16x32_bf16 v[28:31],a[204:207],v[52:55],v[28:31]
ds_read_b128 v[44:47],v4 offset:55296
s_waitcnt lgkmcnt(3)
v_mfma_f32_16x16x32_bf16 v[28:31],a[92:95],v[48:51],v[28:31]
v_add_f32 v20,v20,v12
v_mfma_f32_16x16x32_bf16 v[28:31],v[124:127],v[56:59],v[28:31]
ds_read_b128 v[48:51],v4 offset:13312
v_add_f32 v21,v21,v13
v_mfma_f32_16x16x32_bf16 v[28:31],v[236:239],v[60:63],v[28:31]
v_add_f32 v22,v22,v14
v_add_f32 v23,v23,v15
v_mfma_f32_16x16x32_bf16 v[24:27],v[232:235],v[60:63],v[24:27]
ds_read_b128 v[52:55],v4 offset:27648
v_cvt_pk_bf16_f32 v12,v16,v17
v_mfma_f32_16x16x32_bf16 v[24:27],v[120:123],v[56:59],v[24:27]
v_cvt_pk_bf16_f32 v13,v18,v19
s_waitcnt lgkmcnt(4)
v_mfma_f32_16x16x32_bf16 v[24:27],a[96:99],v[32:35],v[24:27]
ds_read_b128 v[56:59],v4 offset:41984
v_cvt_pk_bf16_f32 v14,v20,v21
v_mfma_f32_16x16x32_bf16 v[24:27],a[208:211],v[36:39],v[24:27]
s_waitcnt vmcnt(10)
s_barrier
v_mfma_f32_16x16x32_bf16 v[28:31],a[212:215],v[36:39],v[28:31]
ds_read_b128 v[60:63],v4 offset:56320
v_cvt_pk_bf16_f32 v15,v22,v23
v_mfma_f32_16x16x32_bf16 v[28:31],a[100:103],v[32:35],v[28:31]
s_waitcnt lgkmcnt(4)
v_mfma_f32_16x16x32_bf16 v[28:31],v[132:135],v[40:43],v[28:31]
ds_read_b128 v[32:35],v2 offset:0
v_mfma_f32_16x16x32_bf16 v[28:31],v[244:247],v[44:47],v[28:31]
buffer_store_dwordx4 v[12:15],v2,s[32:35],s69 offen sc1
v_mfma_f32_16x16x32_bf16 v[24:27],v[240:243],v[44:47],v[24:27]
ds_read_b128 v[36:39],v2 offset:14336
v_mfma_f32_16x16x32_bf16 v[24:27],v[128:131],v[40:43],v[24:27]
s_waitcnt lgkmcnt(4)
v_mfma_f32_16x16x32_bf16 v[24:27],a[104:107],v[48:51],v[24:27]
ds_read_b128 v[40:43],v2 offset:28672
v_mfma_f32_16x16x32_bf16 v[24:27],a[216:219],v[52:55],v[24:27]
v_mfma_f32_16x16x32_bf16 v[28:31],a[220:223],v[52:55],v[28:31]
ds_read_b128 v[44:47],v2 offset:43008
s_mov_b32 s67,s69
v_mfma_f32_16x16x32_bf16 v[28:31],a[108:111],v[48:51],v[28:31]
s_mov_b32 s69,s70
s_waitcnt lgkmcnt(4)
v_mfma_f32_16x16x32_bf16 v[28:31],v[140:143],v[56:59],v[28:31]
ds_read_b128 v[48:51],v2 offset:1024
s_add_u32 s70,s70,1187840
v_mfma_f32_16x16x32_bf16 v[28:31],v[252:255],v[60:63],v[28:31]
v_mfma_f32_16x16x32_bf16 v[24:27],v[248:251],v[60:63],v[24:27]
ds_read_b128 v[52:55],v2 offset:15360
v_mfma_f32_16x16x32_bf16 v[24:27],v[136:139],v[56:59],v[24:27]
s_branch .L_dal_2
.L_dab_2:
v_dot2_f32_bf16 v10,v12,s72,0
v_dot2_f32_bf16 v11,v12,v12,0
v_dot2_f32_bf16 v10,v13,s72,v10
v_dot2_f32_bf16 v11,v13,v13,v11
v_dot2_f32_bf16 v10,v14,s72,v10
v_dot2_f32_bf16 v11,v14,v14,v11
v_dot2_f32_bf16 v10,v15,s72,v10
v_dot2_f32_bf16 v11,v15,v15,v11
s_nop 2
v_permlane16_swap_b32 v10,v11
v_add_f32 v10,v10,v11
s_nop 1
v_permlane32_swap_b32 v10,v11
v_add_f32 v10,v10,v11
buffer_store_dword v10,v3,s[32:35],s67 offen offset:1024 sc1
s_mov_b32 s62,15
s_add_u32 s74,s74,8192
v_lshrrev_b32 v5,8,v2
v_lshl_add_u32 v5,v5,4,s74
v_and_b32 v3,128,v2
v_lshl_add_u32 v5,v3,2,v5
v_and_b32 v3,16,v2
v_lshl_add_u32 v5,v3,4,v5
v_and_b32 v3,96,v2
v_lshl_add_u32 v5,v3,1,v5
v_lshrrev_b32 v3,2,v2
v_add_u32 v3,s74,v3
.p2align 6
.L_dbl_2:
s_waitcnt lgkmcnt(4)
v_mfma_f32_16x16x32_bf16 v[24:27],a[0:3],v[32:35],0
ds_read_b128 v[56:59],v4 offset:29696
v_mfma_f32_16x16x32_bf16 v[24:27],a[112:115],v[36:39],v[24:27]
v_mfma_f32_16x16x32_bf16 v[28:31],a[116:119],v[36:39],0
ds_read_b128 v[60:63],v4 offset:44032
v_mfma_f32_16x16x32_bf16 v[28:31],a[4:7],v[32:35],v[28:31]
v_cvt_pk_bf16_f32 v15,v22,v23
s_waitcnt lgkmcnt(4)
v_mfma_f32_16x16x32_bf16 v[28:31],a[228:231],v[40:43],v[28:31]
ds_read_b128 v[32:35],v4 offset:2048
v_cvt_pk_bf16_f32 v14,v20,v21
v_mfma_f32_16x16x32_bf16 v[28:31],v[148:151],v[44:47],v[28:31]
v_cvt_pk_bf16_f32 v13,v18,v19
v_cvt_pk_bf16_f32 v12,v16,v17
v_mfma_f32_16x16x32_bf16 v[24:27],v[144:147],v[44:47],v[24:27]
ds_read_b128 v[36:39],v4 offset:16384
ds_write_b128 v5,v[12:15]
v_mfma_f32_16x16x32_bf16 v[24:27],a[224:227],v[40:43],v[24:27]
ds_read2st64_b32 v[12:13],v3 offset0:0 offset1:1
s_waitcnt lgkmcnt(6)
v_mfma_f32_16x16x32_bf16 v[24:27],a[8:11],v[48:51],v[24:27]
ds_read_b128 v[40:43],v4 offset:30720
ds_read2st64_b32 v[14:15],v3 offset0:2 offset1:3
v_mfma_f32_16x16x32_bf16 v[24:27],a[120:123],v[52:55],v[24:27]
v_mfma_f32_16x16x32_bf16 v[28:31],a[124:127],v[52:55],v[28:31]
ds_read_b128 v[44:47],v4 offset:45056
v_mfma_f32_16x16x32_bf16 v[28:31],a[12:15],v[48:51],v[28:31]
s_waitcnt lgkmcnt(7)
v_mfma_f32_16x16x32_bf16 v[28:31],a[236:239],v[56:59],v[28:31]
ds_read_b128 v[48:51],v4 offset:3072
v_mfma_f32_16x16x32_bf16 v[28:31],v[156:159],v[60:63],v[28:31]
s_waitcnt lgkmcnt(2)
buffer_atomic_pk_add_bf16 v12,v1,s[44:47],s65 offen offset:0 sc1
v_mfma_f32_16x16x32_bf16 v[24:27],v[152:155],v[60:63],v[24:27]
ds_read_b128 v[52:55],v4 offset:17408
v_mfma_f32_16x16x32_bf16 v[24:27],a[232:235],v[56:59],v[24:27]
buffer_atomic_pk_add_bf16 v13,v1,s[44:47],s65 offen offset:2048 sc1
v_mfma_f32_16x16x32_bf16 v[24:27],a[16:19],v[32:35],v[24:27]
ds_read_b128 v[56:59],v4 offset:31744
s_add_u32 s65,s65,16384
v_mfma_f32_16x16x32_bf16 v[24:27],a[128:131],v[36:39],v[24:27]
s_waitcnt vmcnt(2)
s_barrier
v_mfma_f32_16x16x32_bf16 v[28:31],a[132:135],v[36:39],v[28:31]
ds_read_b128 v[60:63],v4 offset:46080
v_mfma_f32_16x16x32_bf16 v[28:31],a[20:23],v[32:35],v[28:31]
buffer_atomic_pk_add_bf16 v14,v1,s[44:47],s65 offen offset:0 sc1
s_waitcnt lgkmcnt(4)
v_mfma_f32_16x16x32_bf16 v[28:31],a[244:247],v[40:43],v[28:31]
ds_read_b128 v[32:35],v4 offset:4096
s_add_u32 m0,s64,0
v_mfma_f32_16x16x32_bf16 v[28:31],v[164:167],v[44:47],v[28:31]
buffer_atomic_pk_add_bf16 v15,v1,s[44:47],s65 offen offset:2048 sc1
s_add_u32 s65,s65,16384
v_mfma_f32_16x16x32_bf16 v[24:27],v[160:163],v[44:47],v[24:27]
ds_read_b128 v[36:39],v4 offset:18432
v_mfma_f32_16x16x32_bf16 v[24:27],a[240:243],v[40:43],v[24:27]
s_waitcnt lgkmcnt(4)
v_mfma_f32_16x16x32_bf16 v[24:27],a[24:27],v[48:51],v[24:27]
ds_read_b128 v[40:43],v4 offset:32768
v_mfma_f32_16x16x32_bf16 v[24:27],a[136:139],v[52:55],v[24:27]
buffer_load_dwordx4 v2,s[32:35],s63 offen offset:0 sc1 lds
v_mfma_f32_16x16x32_bf16 v[28:31],a[140:143],v[52:55],v[28:31]
ds_read_b128 v[44:47],v4 offset:47104
v_mfma_f32_16x16x32_bf16 v[28:31],a[28:31],v[48:51],v[28:31]
s_waitcnt lgkmcnt(4)
v_mfma_f32_16x16x32_bf16 v[28:31],a[252:255],v[56:59],v[28:31]
ds_read_b128 v[48:51],v4 offset:5120
v_mfma_f32_16x16x32_bf16 v[28:31],v[172:175],v[60:63],v[28:31]
buffer_load_dwordx4 v2,s[32:35],s63 offen offset:1024 sc1 lds
v_mfma_f32_16x16x32_bf16 v[24:27],v[168:171],v[60:63],v[24:27]
ds_read_b128 v[52:55],v4 offset:19456
v_mfma_f32_16x16x32_bf16 v[24:27],a[248:251],v[56:59],v[24:27]
s_waitcnt lgkmcnt(4)
v_mfma_f32_16x16x32_bf16 v[24:27],a[32:35],v[32:35],v[24:27]
ds_read_b128 v[56:59],v4 offset:33792
v_mfma_f32_16x16x32_bf16 v[24:27],a[144:147],v[36:39],v[24:27]
buffer_load_dwordx4 v2,s[32:35],s63 offen offset:2048 sc1 lds
v_mfma_f32_16x16x32_bf16 v[28:31],a[148:151],v[36:39],v[28:31]
ds_read_b128 v[60:63],v4 offset:48128
v_mfma_f32_16x16x32_bf16 v[28:31],a[36:39],v[32:35],v[28:31]
s_waitcnt lgkmcnt(4)
v_mfma_f32_16x16x32_bf16 v[28:31],v[68:71],v[40:43],v[28:31]
ds_read_b128 v[32:35],v4 offset:6144
v_mfma_f32_16x16x32_bf16 v[28:31],v[180:183],v[44:47],v[28:31]
buffer_load_dwordx4 v2,s[32:35],s63 offen offset:3072 sc1 lds
v_mfma_f32_16x16x32_bf16 v[24:27],v[176:179],v[44:47],v[24:27]
ds_read_b128 v[36:39],v4 offset:20480
v_mfma_f32_16x16x32_bf16 v[24:27],v[64:67],v[40:43],v[24:27]
s_waitcnt lgkmcnt(4)
v_mfma_f32_16x16x32_bf16 v[24:27],a[40:43],v[48:51],v[24:27]
ds_read_b128 v[40:43],v4 offset:34816
s_add_u32 s63,s63,4096
v_mfma_f32_16x16x32_bf16 v[24:27],a[152:155],v[52:55],v[24:27]
s_add_u32 m0,s64,4096
v_mfma_f32_16x16x32_bf16 v[28:31],a[156:159],v[52:55],v[28:31]
ds_read_b128 v[44:47],v4 offset:49152
v_mfma_f32_16x16x32_bf16 v[28:31],a[44:47],v[48:51],v[28:31]
s_waitcnt lgkmcnt(4)
v_mfma_f32_16x16x32_bf16 v[28:31],v[76:79],v[56:59],v[28:31]
ds_read_b128 v[48:51],v4 offset:7168
v_mfma_f32_16x16x32_bf16 v[28:31],v[188:191],v[60:63],v[28:31]
buffer_load_dwordx4 v2,s[32:35],s63 offen offset:0 sc1 lds
v_mfma_f32_16x16x32_bf16 v[24:27],v[184:187],v[60:63],v[24:27]
ds_read_b128 v[52:55],v4 offset:21504
v_mfma_f32_16x16x32_bf16 v[24:27],v[72:75],v[56:59],v[24:27]
s_waitcnt lgkmcnt(4)
v_mfma_f32_16x16x32_bf16 v[24:27],a[48:51],v[32:35],v[24:27]
ds_read_b128 v[56:59],v4 offset:35840
v_mfma_f32_16x16x32_bf16 v[24:27],a[160:163],v[36:39],v[24:27]
buffer_load_dwordx4 v2,s[32:35],s63 offen offset:1024 sc1 lds
v_mfma_f32_16x16x32_bf16 v[28:31],a[164:167],v[36:39],v[28:31]
ds_read_b128 v[60:63],v4 offset:50176
v_mfma_f32_16x16x32_bf16 v[28:31],a[52:55],v[32:35],v[28:31]
s_waitcnt lgkmcnt(4)
v_mfma_f32_16x16x32_bf16 v[28:31],v[84:87],v[40:43],v[28:31]
ds_read_b128 v[32:35],v4 offset:8192
v_mfma_f32_16x16x32_bf16 v[28:31],v[196:199],v[44:47],v[28:31]
buffer_load_dwordx4 v2,s[32:35],s63 offen offset:2048 sc1 lds
v_mfma_f32_16x16x32_bf16 v[24:27],v[192:195],v[44:47],v[24:27]
ds_read_b128 v[36:39],v4 offset:22528
v_mfma_f32_16x16x32_bf16 v[24:27],v[80:83],v[40:43],v[24:27]
s_waitcnt lgkmcnt(4)
v_mfma_f32_16x16x32_bf16 v[24:27],a[56:59],v[48:51],v[24:27]
ds_read_b128 v[40:43],v4 offset:36864
v_mfma_f32_16x16x32_bf16 v[24:27],a[168:171],v[52:55],v[24:27]
buffer_load_dwordx4 v2,s[32:35],s63 offen offset:3072 sc1 lds
v_mfma_f32_16x16x32_bf16 v[28:31],a[172:175],v[52:55],v[28:31]
ds_read_b128 v[44:47],v4 offset:51200
v_mfma_f32_16x16x32_bf16 v[28:31],a[60:63],v[48:51],v[28:31]
s_waitcnt lgkmcnt(4)
v_mfma_f32_16x16x32_bf16 v[28:31],v[92:95],v[56:59],v[28:31]
ds_read_b128 v[48:51],v4 offset:9216
v_mfma_f32_16x16x32_bf16 v[28:31],v[204:207],v[60:63],v[28:31]
s_add_u32 s63,s63,4096
s_add_u32 m0,s64,8192
v_mfma_f32_16x16x32_bf16 v[24:27],v[200:203],v[60:63],v[24:27]
ds_read_b128 v[52:55],v4 offset:23552
v_mfma_f32_16x16x32_bf16 v[24:27],v[88:91],v[56:59],v[24:27]
s_waitcnt lgkmcnt(4)
v_mfma_f32_16x16x32_bf16 v[24:27],a[64:67],v[32:35],v[24:27]
ds_read_b128 v[56:59],v4 offset:37888
v_mfma_f32_16x16x32_bf16 v[24:27],a[176:179],v[36:39],v[24:27]
buffer_load_dwordx4 v2,s[32:35],s63 offen offset:0 sc1 lds
v_mfma_f32_16x16x32_bf16 v[28:31],a[180:183],v[36:39],v[28:31]
ds_read_b128 v[60:63],v4 offset:52224
v_mfma_f32_16x16x32_bf16 v[28:31],a[68:71],v[32:35],v[28:31]
s_waitcnt lgkmcnt(4)
v_mfma_f32_16x16x32_bf16 v[28:31],v[100:103],v[40:43],v[28:31]
ds_read_b128 v[32:35],v4 offset:10240
v_mfma_f32_16x16x32_bf16 v[28:31],v[212:215],v[44:47],v[28:31]
buffer_load_dwordx4 v2,s[32:35],s63 offen offset:1024 sc1 lds
v_mfma_f32_16x16x32_bf16 v[24:27],v[208:211],v[44:47],v[24:27]
ds_read_b128 v[36:39],v4 offset:24576
v_mfma_f32_16x16x32_bf16 v[24:27],v[96:99],v[40:43],v[24:27]
s_waitcnt lgkmcnt(4)
v_mfma_f32_16x16x32_bf16 v[24:27],a[72:75],v[48:51],v[24:27]
ds_read_b128 v[40:43],v4 offset:38912
v_mfma_f32_16x16x32_bf16 v[24:27],a[184:187],v[52:55],v[24:27]
buffer_load_dwordx4 v2,s[32:35],s63 offen offset:2048 sc1 lds
v_mfma_f32_16x16x32_bf16 v[28:31],a[188:191],v[52:55],v[28:31]
ds_read_b128 v[44:47],v4 offset:53248
v_mfma_f32_16x16x32_bf16 v[28:31],a[76:79],v[48:51],v[28:31]
s_waitcnt lgkmcnt(4)
v_mfma_f32_16x16x32_bf16 v[28:31],v[108:111],v[56:59],v[28:31]
ds_read_b128 v[48:51],v4 offset:11264
v_mfma_f32_16x16x32_bf16 v[28:31],v[220:223],v[60:63],v[28:31]
v_mfma_f32_16x16x32_bf16 v[24:27],v[216:219],v[60:63],v[24:27]
ds_read_b128 v[52:55],v4 offset:25600
v_mfma_f32_16x16x32_bf16 v[24:27],v[104:107],v[56:59],v[24:27]
s_waitcnt lgkmcnt(4)
v_mfma_f32_16x16x32_bf16 v[24:27],a[80:83],v[32:35],v[24:27]
ds_read_b128 v[56:59],v4 offset:39936
v_mfma_f32_16x16x32_bf16 v[24:27],a[192:195],v[36:39],v[24:27]
buffer_load_dwordx4 v2,s[32:35],s63 offen offset:3072 sc1 lds
s_add_u32 s63,s63,4096
v_mfma_f32_16x16x32_bf16 v[28:31],a[196:199],v[36:39],v[28:31]
ds_read_b128 v[60:63],v4 offset:54272
s_add_u32 m0,s64,12288
v_mfma_f32_16x16x32_bf16 v[28:31],a[84:87],v[32:35],v[28:31]
s_waitcnt lgkmcnt(4)
v_mfma_f32_16x16x32_bf16 v[28:31],v[116:119],v[40:43],v[28:31]
ds_read_b128 v[32:35],v4 offset:12288
v_mfma_f32_16x16x32_bf16 v[28:31],v[228:231],v[44:47],v[28:31]
buffer_load_dwordx4 v2,s[32:35],s63 offen offset:0 sc1 lds
v_mfma_f32_16x16x32_bf16 v[24:27],v[224:227],v[44:47],v[24:27]
ds_read_b128 v[36:39],v4 offset:26624
v_mfma_f32_16x16x32_bf16 v[24:27],v[112:115],v[40:43],v[24:27]
s_waitcnt lgkmcnt(4)
v_mfma_f32_16x16x32_bf16 v[24:27],a[88:91],v[48:51],v[24:27]
ds_read_b128 v[40:43],v4 offset:40960
v_mfma_f32_16x16x32_bf16 v[24:27],a[200:203],v[52:55],v[24:27]
buffer_load_dwordx4 v2,s[32:35],s63 offen offset:1024 sc1 lds
s_add_u32 s63,s63,1175552
v_mfma_f32_16x16x32_bf16 v[28:31],a[204:207],v[52:55],v[28:31]
ds_read_b128 v[44:47],v4 offset:55296
v_mfma_f32_16x16x32_bf16 v[28:31],a[92:95],v[48:51],v[28:31]
s_waitcnt lgkmcnt(4)
v_mfma_f32_16x16x32_bf16 v[28:31],v[124:127],v[56:59],v[28:31]
ds_read_b128 v[48:51],v4 offset:13312
v_mfma_f32_16x16x32_bf16 v[28:31],v[236:239],v[60:63],v[28:31]
v_mfma_f32_16x16x32_bf16 v[24:27],v[232:235],v[60:63],v[24:27]
ds_read_b128 v[52:55],v4 offset:27648
v_mfma_f32_16x16x32_bf16 v[24:27],v[120:123],v[56:59],v[24:27]
s_waitcnt lgkmcnt(4)
v_mfma_f32_16x16x32_bf16 v[24:27],a[96:99],v[32:35],v[24:27]
ds_read_b128 v[56:59],v4 offset:41984
v_mfma_f32_16x16x32_bf16 v[24:27],a[208:211],v[36:39],v[24:27]
s_waitcnt vmcnt(10)
s_barrier
v_mfma_f32_16x16x32_bf16 v[28:31],a[212:215],v[36:39],v[28:31]
ds_read_b128 v[60:63],v4 offset:56320
v_mfma_f32_16x16x32_bf16 v[28:31],a[100:103],v[32:35],v[28:31]
s_waitcnt lgkmcnt(4)
v_mfma_f32_16x16x32_bf16 v[28:31],v[132:135],v[40:43],v[28:31]
ds_read_b128 v[32:35],v2 offset:0
v_mfma_f32_16x16x32_bf16 v[28:31],v[244:247],v[44:47],v[28:31]
v_mfma_f32_16x16x32_bf16 v[24:27],v[240:243],v[44:47],v[24:27]
ds_read_b128 v[36:39],v2 offset:14336
v_mfma_f32_16x16x32_bf16 v[24:27],v[128:131],v[40:43],v[24:27]
s_waitcnt lgkmcnt(4)
v_mfma_f32_16x16x32_bf16 v[24:27],a[104:107],v[48:51],v[24:27]
ds_read_b128 v[40:43],v2 offset:28672
v_mfma_f32_16x16x32_bf16 v[24:27],a[216:219],v[52:55],v[24:27]
v_mfma_f32_16x16x32_bf16 v[28:31],a[220:223],v[52:55],v[28:31]
ds_read_b128 v[44:47],v2 offset:43008
v_mfma_f32_16x16x32_bf16 v[28:31],a[108:111],v[48:51],v[28:31]
s_waitcnt lgkmcnt(4)
v_mfma_f32_16x16x32_bf16 v[28:31],v[140:143],v[56:59],v[28:31]
ds_read_b128 v[48:51],v2 offset:1024
v_mfma_f32_16x16x32_bf16 v[28:31],v[252:255],v[60:63],v[28:31]
v_mfma_f32_16x16x32_bf16 v[24:27],v[248:251],v[60:63],v[24:27]
ds_read_b128 v[52:55],v2 offset:15360
s_sub_u32 s62,s62,1
v_mfma_f32_16x16x32_bf16 v[24:27],v[136:139],v[56:59],v[24:27]
s_cbranch_scc1 .L_dbt_2
s_waitcnt lgkmcnt(4)
v_mfma_f32_16x16x32_bf16 v[16:19],a[0:3],v[32:35],0
ds_read_b128 v[56:59],v2 offset:29696
v_mfma_f32_16x16x32_bf16 v[16:19],a[112:115],v[36:39],v[16:19]
v_mfma_f32_16x16x32_bf16 v[20:23],a[116:119],v[36:39],0
ds_read_b128 v[60:63],v2 offset:44032
v_mfma_f32_16x16x32_bf16 v[20:23],a[4:7],v[32:35],v[20:23]
v_cvt_pk_bf16_f32 v15,v30,v31
s_waitcnt lgkmcnt(4)
v_mfma_f32_16x16x32_bf16 v[20:23],a[228:231],v[40:43],v[20:23]
ds_read_b128 v[32:35],v2 offset:2048
v_cvt_pk_bf16_f32 v14,v28,v29
v_mfma_f32_16x16x32_bf16 v[20:23],v[148:151],v[44:47],v[20:23]
v_cvt_pk_bf16_f32 v13,v26,v27
v_cvt_pk_bf16_f32 v12,v24,v25
v_mfma_f32_16x16x32_bf16 v[16:19],v[144:147],v[44:47],v[16:19]
ds_read_b128 v[36:39],v2 offset:16384
ds_write_b128 v5,v[12:15]
v_mfma_f32_16x16x32_bf16 v[16:19],a[224:227],v[40:43],v[16:19]
ds_read2st64_b32 v[12:13],v3 offset0:0 offset1:1
s_waitcnt lgkmcnt(6)
v_mfma_f32_16x16x32_bf16 v[16:19],a[8:11],v[48:51],v[16:19]
ds_read_b128 v[40:43],v2 offset:30720
ds_read2st64_b32 v[14:15],v3 offset0:2 offset1:3
v_mfma_f32_16x16x32_bf16 v[16:19],a[120:123],v[52:55],v[16:19]
v_mfma_f32_16x16x32_bf16 v[20:23],a[124:127],v[52:55],v[20:23]
ds_read_b128 v[44:47],v2 offset:45056
v_mfma_f32_16x16x32_bf16 v[20:23],a[12:15],v[48:51],v[20:23]
s_waitcnt lgkmcnt(7)
v_mfma_f32_16x16x32_bf16 v[20:23],a[236:239],v[56:59],v[20:23]
ds_read_b128 v[48:51],v2 offset:3072
v_mfma_f32_16x16x32_bf16 v[20:23],v[156:159],v[60:63],v[20:23]
s_waitcnt lgkmcnt(2)
buffer_atomic_pk_add_bf16 v12,v1,s[44:47],s65 offen offset:0 sc1
v_mfma_f32_16x16x32_bf16 v[16:19],v[152:155],v[60:63],v[16:19]
ds_read_b128 v[52:55],v2 offset:17408
v_mfma_f32_16x16x32_bf16 v[16:19],a[232:235],v[56:59],v[16:19]
buffer_atomic_pk_add_bf16 v13,v1,s[44:47],s65 offen offset:2048 sc1
v_mfma_f32_16x16x32_bf16 v[16:19],a[16:19],v[32:35],v[16:19]
ds_read_b128 v[56:59],v2 offset:31744
s_add_u32 s65,s65,16384
v_mfma_f32_16x16x32_bf16 v[16:19],a[128:131],v[36:39],v[16:19]
s_waitcnt vmcnt(2)
s_barrier
v_mfma_f32_16x16x32_bf16 v[20:23],a[132:135],v[36:39],v[20:23]
ds_read_b128 v[60:63],v2 offset:46080
v_mfma_f32_16x16x32_bf16 v[20:23],a[20:23],v[32:35],v[20:23]
buffer_atomic_pk_add_bf16 v14,v1,s[44:47],s65 offen offset:0 sc1
s_waitcnt lgkmcnt(4)
v_mfma_f32_16x16x32_bf16 v[20:23],a[244:247],v[40:43],v[20:23]
ds_read_b128 v[32:35],v2 offset:4096
s_add_u32 m0,s64,57344
v_mfma_f32_16x16x32_bf16 v[20:23],v[164:167],v[44:47],v[20:23]
buffer_atomic_pk_add_bf16 v15,v1,s[44:47],s65 offen offset:2048 sc1
s_add_u32 s65,s65,16384
v_mfma_f32_16x16x32_bf16 v[16:19],v[160:163],v[44:47],v[16:19]
ds_read_b128 v[36:39],v2 offset:18432
v_mfma_f32_16x16x32_bf16 v[16:19],a[240:243],v[40:43],v[16:19]
s_waitcnt lgkmcnt(4)
v_mfma_f32_16x16x32_bf16 v[16:19],a[24:27],v[48:51],v[16:19]
ds_read_b128 v[40:43],v2 offset:32768
v_mfma_f32_16x16x32_bf16 v[16:19],a[136:139],v[52:55],v[16:19]
buffer_load_dwordx4 v2,s[32:35],s63 offen offset:0 sc1 lds
v_mfma_f32_16x16x32_bf16 v[20:23],a[140:143],v[52:55],v[20:23]
ds_read_b128 v[44:47],v2 offset:47104
v_mfma_f32_16x16x32_bf16 v[20:23],a[28:31],v[48:51],v[20:23]
s_waitcnt lgkmcnt(4)
v_mfma_f32_16x16x32_bf16 v[20:23],a[252:255],v[56:59],v[20:23]
ds_read_b128 v[48:51],v2 offset:5120
v_mfma_f32_16x16x32_bf16 v[20:23],v[172:175],v[60:63],v[20:23]
buffer_load_dwordx4 v2,s[32:35],s63 offen offset:1024 sc1 lds
v_mfma_f32_16x16x32_bf16 v[16:19],v[168:171],v[60:63],v[16:19]
ds_read_b128 v[52:55],v2 offset:19456
v_mfma_f32_16x16x32_bf16 v[16:19],a[248:251],v[56:59],v[16:19]
s_waitcnt lgkmcnt(4)
v_mfma_f32_16x16x32_bf16 v[16:19],a[32:35],v[32:35],v[16:19]
ds_read_b128 v[56:59],v2 offset:33792
v_mfma_f32_16x16x32_bf16 v[16:19],a[144:147],v[36:39],v[16:19]
buffer_load_dwordx4 v2,s[32:35],s63 offen offset:2048 sc1 lds
v_mfma_f32_16x16x32_bf16 v[20:23],a[148:151],v[36:39],v[20:23]
ds_read_b128 v[60:63],v2 offset:48128
v_mfma_f32_16x16x32_bf16 v[20:23],a[36:39],v[32:35],v[20:23]
s_waitcnt lgkmcnt(4)
v_mfma_f32_16x16x32_bf16 v[20:23],v[68:71],v[40:43],v[20:23]
ds_read_b128 v[32:35],v2 offset:6144
v_mfma_f32_16x16x32_bf16 v[20:23],v[180:183],v[44:47],v[20:23]
buffer_load_dwordx4 v2,s[32:35],s63 offen offset:3072 sc1 lds
v_mfma_f32_16x16x32_bf16 v[16:19],v[176:179],v[44:47],v[16:19]
ds_read_b128 v[36:39],v2 offset:20480
v_mfma_f32_16x16x32_bf16 v[16:19],v[64:67],v[40:43],v[16:19]
s_waitcnt lgkmcnt(4)
v_mfma_f32_16x16x32_bf16 v[16:19],a[40:43],v[48:51],v[16:19]
ds_read_b128 v[40:43],v2 offset:34816
s_add_u32 s63,s63,4096
v_mfma_f32_16x16x32_bf16 v[16:19],a[152:155],v[52:55],v[16:19]
s_add_u32 m0,s64,61440
v_mfma_f32_16x16x32_bf16 v[20:23],a[156:159],v[52:55],v[20:23]
ds_read_b128 v[44:47],v2 offset:49152
v_mfma_f32_16x16x32_bf16 v[20:23],a[44:47],v[48:51],v[20:23]
s_waitcnt lgkmcnt(4)
v_mfma_f32_16x16x32_bf16 v[20:23],v[76:79],v[56:59],v[20:23]
ds_read_b128 v[48:51],v2 offset:7168
v_mfma_f32_16x16x32_bf16 v[20:23],v[188:191],v[60:63],v[20:23]
buffer_load_dwordx4 v2,s[32:35],s63 offen offset:0 sc1 lds
v_mfma_f32_16x16x32_bf16 v[16:19],v[184:187],v[60:63],v[16:19]
ds_read_b128 v[52:55],v2 offset:21504
v_mfma_f32_16x16x32_bf16 v[16:19],v[72:75],v[56:59],v[16:19]
s_waitcnt lgkmcnt(4)
v_mfma_f32_16x16x32_bf16 v[16:19],a[48:51],v[32:35],v[16:19]
ds_read_b128 v[56:59],v2 offset:35840
v_mfma_f32_16x16x32_bf16 v[16:19],a[160:163],v[36:39],v[16:19]
buffer_load_dwordx4 v2,s[32:35],s63 offen offset:1024 sc1 lds
v_mfma_f32_16x16x32_bf16 v[20:23],a[164:167],v[36:39],v[20:23]
ds_read_b128 v[60:63],v2 offset:50176
v_mfma_f32_16x16x32_bf16 v[20:23],a[52:55],v[32:35],v[20:23]
s_waitcnt lgkmcnt(4)
v_mfma_f32_16x16x32_bf16 v[20:23],v[84:87],v[40:43],v[20:23]
ds_read_b128 v[32:35],v2 offset:8192
v_mfma_f32_16x16x32_bf16 v[20:23],v[196:199],v[44:47],v[20:23]
buffer_load_dwordx4 v2,s[32:35],s63 offen offset:2048 sc1 lds
v_mfma_f32_16x16x32_bf16 v[16:19],v[192:195],v[44:47],v[16:19]
ds_read_b128 v[36:39],v2 offset:22528
v_mfma_f32_16x16x32_bf16 v[16:19],v[80:83],v[40:43],v[16:19]
s_waitcnt lgkmcnt(4)
v_mfma_f32_16x16x32_bf16 v[16:19],a[56:59],v[48:51],v[16:19]
ds_read_b128 v[40:43],v2 offset:36864
v_mfma_f32_16x16x32_bf16 v[16:19],a[168:171],v[52:55],v[16:19]
buffer_load_dwordx4 v2,s[32:35],s63 offen offset:3072 sc1 lds
v_mfma_f32_16x16x32_bf16 v[20:23],a[172:175],v[52:55],v[20:23]
)#"
R"#(
ds_read_b128 v[44:47],v2 offset:51200
v_mfma_f32_16x16x32_bf16 v[20:23],a[60:63],v[48:51],v[20:23]
s_waitcnt lgkmcnt(4)
v_mfma_f32_16x16x32_bf16 v[20:23],v[92:95],v[56:59],v[20:23]
ds_read_b128 v[48:51],v2 offset:9216
v_mfma_f32_16x16x32_bf16 v[20:23],v[204:207],v[60:63],v[20:23]
s_add_u32 s63,s63,4096
s_add_u32 m0,s64,65536
v_mfma_f32_16x16x32_bf16 v[16:19],v[200:203],v[60:63],v[16:19]
ds_read_b128 v[52:55],v2 offset:23552
v_mfma_f32_16x16x32_bf16 v[16:19],v[88:91],v[56:59],v[16:19]
s_waitcnt lgkmcnt(4)
v_mfma_f32_16x16x32_bf16 v[16:19],a[64:67],v[32:35],v[16:19]
ds_read_b128 v[56:59],v2 offset:37888
v_mfma_f32_16x16x32_bf16 v[16:19],a[176:179],v[36:39],v[16:19]
buffer_load_dwordx4 v2,s[32:35],s63 offen offset:0 sc1 lds
v_mfma_f32_16x16x32_bf16 v[20:23],a[180:183],v[36:39],v[20:23]
ds_read_b128 v[60:63],v2 offset:52224
v_mfma_f32_16x16x32_bf16 v[20:23],a[68:71],v[32:35],v[20:23]
s_waitcnt lgkmcnt(4)
v_mfma_f32_16x16x32_bf16 v[20:23],v[100:103],v[40:43],v[20:23]
ds_read_b128 v[32:35],v2 offset:10240
v_mfma_f32_16x16x32_bf16 v[20:23],v[212:215],v[44:47],v[20:23]
buffer_load_dwordx4 v2,s[32:35],s63 offen offset:1024 sc1 lds
v_mfma_f32_16x16x32_bf16 v[16:19],v[208:211],v[44:47],v[16:19]
ds_read_b128 v[36:39],v2 offset:24576
v_mfma_f32_16x16x32_bf16 v[16:19],v[96:99],v[40:43],v[16:19]
s_waitcnt lgkmcnt(4)
v_mfma_f32_16x16x32_bf16 v[16:19],a[72:75],v[48:51],v[16:19]
ds_read_b128 v[40:43],v2 offset:38912
v_mfma_f32_16x16x32_bf16 v[16:19],a[184:187],v[52:55],v[16:19]
buffer_load_dwordx4 v2,s[32:35],s63 offen offset:2048 sc1 lds
v_mfma_f32_16x16x32_bf16 v[20:23],a[188:191],v[52:55],v[20:23]
ds_read_b128 v[44:47],v2 offset:53248
v_mfma_f32_16x16x32_bf16 v[20:23],a[76:79],v[48:51],v[20:23]
s_waitcnt lgkmcnt(4)
v_mfma_f32_16x16x32_bf16 v[20:23],v[108:111],v[56:59],v[20:23]
ds_read_b128 v[48:51],v2 offset:11264
v_mfma_f32_16x16x32_bf16 v[20:23],v[220:223],v[60:63],v[20:23]
v_mfma_f32_16x16x32_bf16 v[16:19],v[216:219],v[60:63],v[16:19]
ds_read_b128 v[52:55],v2 offset:25600
v_mfma_f32_16x16x32_bf16 v[16:19],v[104:107],v[56:59],v[16:19]
s_waitcnt lgkmcnt(4)
v_mfma_f32_16x16x32_bf16 v[16:19],a[80:83],v[32:35],v[16:19]
ds_read_b128 v[56:59],v2 offset:39936
v_mfma_f32_16x16x32_bf16 v[16:19],a[192:195],v[36:39],v[16:19]
buffer_load_dwordx4 v2,s[32:35],s63 offen offset:3072 sc1 lds
s_add_u32 s63,s63,4096
v_mfma_f32_16x16x32_bf16 v[20:23],a[196:199],v[36:39],v[20:23]
ds_read_b128 v[60:63],v2 offset:54272
s_add_u32 m0,s64,69632
v_mfma_f32_16x16x32_bf16 v[20:23],a[84:87],v[32:35],v[20:23]
s_waitcnt lgkmcnt(4)
v_mfma_f32_16x16x32_bf16 v[20:23],v[116:119],v[40:43],v[20:23]
ds_read_b128 v[32:35],v2 offset:12288
v_mfma_f32_16x16x32_bf16 v[20:23],v[228:231],v[44:47],v[20:23]
buffer_load_dwordx4 v2,s[32:35],s63 offen offset:0 sc1 lds
v_mfma_f32_16x16x32_bf16 v[16:19],v[224:227],v[44:47],v[16:19]
ds_read_b128 v[36:39],v2 offset:26624
v_mfma_f32_16x16x32_bf16 v[16:19],v[112:115],v[40:43],v[16:19]
s_waitcnt lgkmcnt(4)
v_mfma_f32_16x16x32_bf16 v[16:19],a[88:91],v[48:51],v[16:19]
ds_read_b128 v[40:43],v2 offset:40960
v_mfma_f32_16x16x32_bf16 v[16:19],a[200:203],v[52:55],v[16:19]
buffer_load_dwordx4 v2,s[32:35],s63 offen offset:1024 sc1 lds
s_add_u32 s63,s63,1175552
v_mfma_f32_16x16x32_bf16 v[20:23],a[204:207],v[52:55],v[20:23]
ds_read_b128 v[44:47],v2 offset:55296
v_mfma_f32_16x16x32_bf16 v[20:23],a[92:95],v[48:51],v[20:23]
s_waitcnt lgkmcnt(4)
v_mfma_f32_16x16x32_bf16 v[20:23],v[124:127],v[56:59],v[20:23]
ds_read_b128 v[48:51],v2 offset:13312
v_mfma_f32_16x16x32_bf16 v[20:23],v[236:239],v[60:63],v[20:23]
v_mfma_f32_16x16x32_bf16 v[16:19],v[232:235],v[60:63],v[16:19]
ds_read_b128 v[52:55],v2 offset:27648
v_mfma_f32_16x16x32_bf16 v[16:19],v[120:123],v[56:59],v[16:19]
s_waitcnt lgkmcnt(4)
v_mfma_f32_16x16x32_bf16 v[16:19],a[96:99],v[32:35],v[16:19]
ds_read_b128 v[56:59],v2 offset:41984
v_mfma_f32_16x16x32_bf16 v[16:19],a[208:211],v[36:39],v[16:19]
s_waitcnt vmcnt(10)
s_barrier
v_mfma_f32_16x16x32_bf16 v[20:23],a[212:215],v[36:39],v[20:23]
ds_read_b128 v[60:63],v2 offset:56320
v_mfma_f32_16x16x32_bf16 v[20:23],a[100:103],v[32:35],v[20:23]
s_waitcnt lgkmcnt(4)
v_mfma_f32_16x16x32_bf16 v[20:23],v[132:135],v[40:43],v[20:23]
ds_read_b128 v[32:35],v4 offset:0
v_mfma_f32_16x16x32_bf16 v[20:23],v[244:247],v[44:47],v[20:23]
v_mfma_f32_16x16x32_bf16 v[16:19],v[240:243],v[44:47],v[16:19]
ds_read_b128 v[36:39],v4 offset:14336
v_mfma_f32_16x16x32_bf16 v[16:19],v[128:131],v[40:43],v[16:19]
s_waitcnt lgkmcnt(4)
v_mfma_f32_16x16x32_bf16 v[16:19],a[104:107],v[48:51],v[16:19]
ds_read_b128 v[40:43],v4 offset:28672
v_mfma_f32_16x16x32_bf16 v[16:19],a[216:219],v[52:55],v[16:19]
v_mfma_f32_16x16x32_bf16 v[20:23],a[220:223],v[52:55],v[20:23]
ds_read_b128 v[44:47],v4 offset:43008
v_mfma_f32_16x16x32_bf16 v[20:23],a[108:111],v[48:51],v[20:23]
s_waitcnt lgkmcnt(4)
v_mfma_f32_16x16x32_bf16 v[20:23],v[140:143],v[56:59],v[20:23]
ds_read_b128 v[48:51],v4 offset:1024
v_mfma_f32_16x16x32_bf16 v[20:23],v[252:255],v[60:63],v[20:23]
v_mfma_f32_16x16x32_bf16 v[16:19],v[248:251],v[60:63],v[16:19]
ds_read_b128 v[52:55],v4 offset:15360
v_mfma_f32_16x16x32_bf16 v[16:19],v[136:139],v[56:59],v[16:19]
s_branch .L_dbl_2
.L_dbt_2:
s_nop 1
s_nop 0
v_cvt_pk_bf16_f32 v15,v30,v31
v_cvt_pk_bf16_f32 v14,v28,v29
s_nop 1
v_cvt_pk_bf16_f32 v13,v26,v27
v_cvt_pk_bf16_f32 v12,v24,v25
ds_write_b128 v5,v[12:15]
ds_read2st64_b32 v[12:13],v3 offset0:0 offset1:1
ds_read2st64_b32 v[14:15],v3 offset0:2 offset1:3
s_waitcnt lgkmcnt(0)
buffer_atomic_pk_add_bf16 v12,v1,s[44:47],s65 offen offset:0 sc1
buffer_atomic_pk_add_bf16 v13,v1,s[44:47],s65 offen offset:2048 sc1
s_add_u32 s65,s65,16384
buffer_atomic_pk_add_bf16 v14,v1,s[44:47],s65 offen offset:0 sc1
buffer_atomic_pk_add_bf16 v15,v1,s[44:47],s65 offen offset:2048 sc1
s_add_u32 s65,s65,16384
s_or_b32 s48,s28,s27
s_or_b32 s48,s48,s26
s_cbranch_scc1 .L_end
v_mov_b32 v8,s54
v_mov_b32 v9,s55
buffer_store_dwordx2 v[8:9],off,s[32:35],0 sc1
s_branch .L_end
.p2align 6
.L_dhv3:
.L_dal_3:
s_waitcnt lgkmcnt(4)
v_mfma_f32_16x16x32_bf16 v[16:19],a[0:3],v[32:35],0
ds_read_b128 v[56:59],v2 offset:29696
v_dot2_f32_bf16 v10,v12,s72,0
v_mfma_f32_16x16x32_bf16 v[16:19],a[112:115],v[36:39],v[16:19]
v_dot2_f32_bf16 v11,v12,v12,0
v_dot2_f32_bf16 v10,v13,s72,v10
v_mfma_f32_16x16x32_bf16 v[20:23],a[116:119],v[36:39],0
ds_read_b128 v[60:63],v2 offset:44032
v_dot2_f32_bf16 v11,v13,v13,v11
v_mfma_f32_16x16x32_bf16 v[20:23],a[4:7],v[32:35],v[20:23]
v_dot2_f32_bf16 v10,v14,s72,v10
s_waitcnt lgkmcnt(4)
v_mfma_f32_16x16x32_bf16 v[20:23],a[228:231],v[40:43],v[20:23]
ds_read_b128 v[32:35],v2 offset:2048
v_dot2_f32_bf16 v11,v14,v14,v11
v_mfma_f32_16x16x32_bf16 v[20:23],v[148:151],v[44:47],v[20:23]
v_dot2_f32_bf16 v10,v15,s72,v10
v_dot2_f32_bf16 v11,v15,v15,v11
v_mfma_f32_16x16x32_bf16 v[16:19],v[144:147],v[44:47],v[16:19]
ds_read_b128 v[36:39],v2 offset:16384
v_mfma_f32_16x16x32_bf16 v[16:19],a[224:227],v[40:43],v[16:19]
v_permlane16_swap_b32 v10,v11
s_waitcnt lgkmcnt(4)
v_mfma_f32_16x16x32_bf16 v[16:19],a[8:11],v[48:51],v[16:19]
ds_read_b128 v[40:43],v2 offset:30720
v_add_f32 v10,v10,v11
v_mfma_f32_16x16x32_bf16 v[16:19],a[120:123],v[52:55],v[16:19]
v_mfma_f32_16x16x32_bf16 v[20:23],a[124:127],v[52:55],v[20:23]
ds_read_b128 v[44:47],v2 offset:45056
v_permlane32_swap_b32 v10,v11
v_mfma_f32_16x16x32_bf16 v[20:23],a[12:15],v[48:51],v[20:23]
v_add_f32 v10,v10,v11
s_waitcnt lgkmcnt(4)
v_mfma_f32_16x16x32_bf16 v[20:23],a[236:239],v[56:59],v[20:23]
ds_read_b128 v[48:51],v2 offset:3072
v_mfma_f32_16x16x32_bf16 v[20:23],v[156:159],v[60:63],v[20:23]
buffer_store_dword v10,v3,s[32:35],s67 offen offset:1024 sc1
v_mfma_f32_16x16x32_bf16 v[16:19],v[152:155],v[60:63],v[16:19]
ds_read_b128 v[52:55],v2 offset:17408
v_mfma_f32_16x16x32_bf16 v[16:19],a[232:235],v[56:59],v[16:19]
s_waitcnt lgkmcnt(4)
v_mfma_f32_16x16x32_bf16 v[16:19],a[16:19],v[32:35],v[16:19]
s_waitcnt vmcnt(2)
s_barrier
v_mfma_f32_16x16x32_bf16 v[16:19],a[128:131],v[36:39],v[16:19]
ds_read_b128 v[56:59],v2 offset:31744
v_mfma_f32_16x16x32_bf16 v[20:23],a[132:135],v[36:39],v[20:23]
ds_read_b128 v[60:63],v2 offset:46080
v_mfma_f32_16x16x32_bf16 v[20:23],a[20:23],v[32:35],v[20:23]
s_waitcnt lgkmcnt(4)
v_mfma_f32_16x16x32_bf16 v[20:23],a[244:247],v[40:43],v[20:23]
ds_read_b128 v[32:35],v2 offset:4096
s_mov_b32 m0,s74
v_mfma_f32_16x16x32_bf16 v[20:23],v[164:167],v[44:47],v[20:23]
buffer_load_dwordx4 v2,s[32:35],s71 offen offset:0 sc1 lds
v_mfma_f32_16x16x32_bf16 v[16:19],v[160:163],v[44:47],v[16:19]
ds_read_b128 v[36:39],v2 offset:18432
v_mfma_f32_16x16x32_bf16 v[16:19],a[240:243],v[40:43],v[16:19]
s_waitcnt lgkmcnt(4)
v_mfma_f32_16x16x32_bf16 v[16:19],a[24:27],v[48:51],v[16:19]
ds_read_b128 v[40:43],v2 offset:32768
v_mfma_f32_16x16x32_bf16 v[16:19],a[136:139],v[52:55],v[16:19]
buffer_load_dwordx4 v2,s[32:35],s71 offen offset:1024 sc1 lds
s_add_u32 s71,s71,1187840
v_mfma_f32_16x16x32_bf16 v[20:23],a[140:143],v[52:55],v[20:23]
ds_read_b128 v[44:47],v2 offset:47104
s_add_u32 m0,s64,57344
v_mfma_f32_16x16x32_bf16 v[20:23],a[28:31],v[48:51],v[20:23]
s_waitcnt lgkmcnt(4)
v_mfma_f32_16x16x32_bf16 v[20:23],a[252:255],v[56:59],v[20:23]
ds_read_b128 v[48:51],v2 offset:5120
v_mfma_f32_16x16x32_bf16 v[20:23],v[172:175],v[60:63],v[20:23]
buffer_load_dwordx4 v2,s[32:35],s63 offen offset:0 sc1 lds
v_mfma_f32_16x16x32_bf16 v[16:19],v[168:171],v[60:63],v[16:19]
ds_read_b128 v[52:55],v2 offset:19456
v_mfma_f32_16x16x32_bf16 v[16:19],a[248:251],v[56:59],v[16:19]
s_waitcnt lgkmcnt(4)
v_mfma_f32_16x16x32_bf16 v[16:19],a[32:35],v[32:35],v[16:19]
ds_read_b128 v[56:59],v2 offset:33792
v_mfma_f32_16x16x32_bf16 v[16:19],a[144:147],v[36:39],v[16:19]
buffer_load_dwordx4 v2,s[32:35],s63 offen offset:1024 sc1 lds
v_mfma_f32_16x16x32_bf16 v[20:23],a[148:151],v[36:39],v[20:23]
ds_read_b128 v[60:63],v2 offset:48128
v_mfma_f32_16x16x32_bf16 v[20:23],a[36:39],v[32:35],v[20:23]
s_waitcnt lgkmcnt(4)
v_mfma_f32_16x16x32_bf16 v[20:23],v[68:71],v[40:43],v[20:23]
ds_read_b128 v[32:35],v2 offset:6144
v_mfma_f32_16x16x32_bf16 v[20:23],v[180:183],v[44:47],v[20:23]
buffer_load_dwordx4 v2,s[32:35],s63 offen offset:2048 sc1 lds
v_mfma_f32_16x16x32_bf16 v[16:19],v[176:179],v[44:47],v[16:19]
ds_read_b128 v[36:39],v2 offset:20480
v_mfma_f32_16x16x32_bf16 v[16:19],v[64:67],v[40:43],v[16:19]
s_waitcnt lgkmcnt(4)
v_mfma_f32_16x16x32_bf16 v[16:19],a[40:43],v[48:51],v[16:19]
ds_read_b128 v[40:43],v2 offset:34816
v_mfma_f32_16x16x32_bf16 v[16:19],a[152:155],v[52:55],v[16:19]
buffer_load_dwordx4 v2,s[32:35],s63 offen offset:3072 sc1 lds
s_add_u32 s63,s63,4096
v_mfma_f32_16x16x32_bf16 v[20:23],a[156:159],v[52:55],v[20:23]
ds_read_b128 v[44:47],v2 offset:49152
s_add_u32 m0,s64,61440
v_mfma_f32_16x16x32_bf16 v[20:23],a[44:47],v[48:51],v[20:23]
s_waitcnt lgkmcnt(4)
v_mfma_f32_16x16x32_bf16 v[20:23],v[76:79],v[56:59],v[20:23]
ds_read_b128 v[48:51],v2 offset:7168
v_mfma_f32_16x16x32_bf16 v[20:23],v[188:191],v[60:63],v[20:23]
buffer_load_dwordx4 v2,s[32:35],s63 offen offset:0 sc1 lds
v_mfma_f32_16x16x32_bf16 v[16:19],v[184:187],v[60:63],v[16:19]
ds_read_b128 v[52:55],v2 offset:21504
v_mfma_f32_16x16x32_bf16 v[16:19],v[72:75],v[56:59],v[16:19]
s_waitcnt lgkmcnt(4)
v_mfma_f32_16x16x32_bf16 v[16:19],a[48:51],v[32:35],v[16:19]
ds_read_b128 v[56:59],v2 offset:35840
v_mfma_f32_16x16x32_bf16 v[16:19],a[160:163],v[36:39],v[16:19]
buffer_load_dwordx4 v2,s[32:35],s63 offen offset:1024 sc1 lds
v_mfma_f32_16x16x32_bf16 v[20:23],a[164:167],v[36:39],v[20:23]
ds_read_b128 v[60:63],v2 offset:50176
v_mfma_f32_16x16x32_bf16 v[20:23],a[52:55],v[32:35],v[20:23]
s_waitcnt lgkmcnt(4)
v_mfma_f32_16x16x32_bf16 v[20:23],v[84:87],v[40:43],v[20:23]
ds_read_b128 v[32:35],v2 offset:8192
v_mfma_f32_16x16x32_bf16 v[20:23],v[196:199],v[44:47],v[20:23]
buffer_load_dwordx4 v2,s[32:35],s63 offen offset:2048 sc1 lds
v_mfma_f32_16x16x32_bf16 v[16:19],v[192:195],v[44:47],v[16:19]
ds_read_b128 v[36:39],v2 offset:22528
v_mfma_f32_16x16x32_bf16 v[16:19],v[80:83],v[40:43],v[16:19]
s_waitcnt lgkmcnt(4)
v_mfma_f32_16x16x32_bf16 v[16:19],a[56:59],v[48:51],v[16:19]
ds_read_b128 v[40:43],v2 offset:36864
v_mfma_f32_16x16x32_bf16 v[16:19],a[168:171],v[52:55],v[16:19]
buffer_load_dwordx4 v2,s[32:35],s63 offen offset:3072 sc1 lds
v_mfma_f32_16x16x32_bf16 v[20:23],a[172:175],v[52:55],v[20:23]
ds_read_b128 v[44:47],v2 offset:51200
v_mfma_f32_16x16x32_bf16 v[20:23],a[60:63],v[48:51],v[20:23]
s_waitcnt lgkmcnt(4)
v_mfma_f32_16x16x32_bf16 v[20:23],v[92:95],v[56:59],v[20:23]
ds_read_b128 v[48:51],v2 offset:9216
v_mfma_f32_16x16x32_bf16 v[20:23],v[204:207],v[60:63],v[20:23]
s_add_u32 s63,s63,4096
s_add_u32 m0,s64,65536
v_mfma_f32_16x16x32_bf16 v[16:19],v[200:203],v[60:63],v[16:19]
ds_read_b128 v[52:55],v2 offset:23552
v_mfma_f32_16x16x32_bf16 v[16:19],v[88:91],v[56:59],v[16:19]
s_waitcnt lgkmcnt(4)
v_mfma_f32_16x16x32_bf16 v[16:19],a[64:67],v[32:35],v[16:19]
ds_read_b128 v[56:59],v2 offset:37888
v_mfma_f32_16x16x32_bf16 v[16:19],a[176:179],v[36:39],v[16:19]
buffer_load_dwordx4 v2,s[32:35],s63 offen offset:0 sc1 lds
v_mfma_f32_16x16x32_bf16 v[20:23],a[180:183],v[36:39],v[20:23]
ds_read_b128 v[60:63],v2 offset:52224
v_mfma_f32_16x16x32_bf16 v[20:23],a[68:71],v[32:35],v[20:23]
s_waitcnt lgkmcnt(4)
v_mfma_f32_16x16x32_bf16 v[20:23],v[100:103],v[40:43],v[20:23]
ds_read_b128 v[32:35],v2 offset:10240
v_mfma_f32_16x16x32_bf16 v[20:23],v[212:215],v[44:47],v[20:23]
buffer_load_dwordx4 v2,s[32:35],s63 offen offset:1024 sc1 lds
v_mfma_f32_16x16x32_bf16 v[16:19],v[208:211],v[44:47],v[16:19]
ds_read_b128 v[36:39],v2 offset:24576
v_mfma_f32_16x16x32_bf16 v[16:19],v[96:99],v[40:43],v[16:19]
s_waitcnt lgkmcnt(4)
v_mfma_f32_16x16x32_bf16 v[16:19],a[72:75],v[48:51],v[16:19]
ds_read_b128 v[40:43],v2 offset:38912
s_waitcnt vmcnt(10)
v_mfma_f32_16x16x32_bf16 v[16:19],a[184:187],v[52:55],v[16:19]
buffer_load_dwordx4 v2,s[32:35],s63 offen offset:2048 sc1 lds
ds_read_b128 v[12:15],v5 offset:0
v_mfma_f32_16x16x32_bf16 v[20:23],a[188:191],v[52:55],v[20:23]
ds_read_b128 v[44:47],v2 offset:53248
v_mfma_f32_16x16x32_bf16 v[20:23],a[76:79],v[48:51],v[20:23]
s_waitcnt lgkmcnt(5)
v_mfma_f32_16x16x32_bf16 v[20:23],v[108:111],v[56:59],v[20:23]
ds_read_b128 v[48:51],v2 offset:11264
v_mfma_f32_16x16x32_bf16 v[20:23],v[220:223],v[60:63],v[20:23]
v_mfma_f32_16x16x32_bf16 v[16:19],v[216:219],v[60:63],v[16:19]
ds_read_b128 v[52:55],v2 offset:25600
s_waitcnt lgkmcnt(3)
v_mfma_f32_16x16x32_bf16 v[16:19],v[104:107],v[56:59],v[16:19]
v_add_f32 v24,v24,v12
v_mfma_f32_16x16x32_bf16 v[16:19],a[80:83],v[32:35],v[16:19]
ds_read_b128 v[56:59],v2 offset:39936
v_add_f32 v25,v25,v13
v_mfma_f32_16x16x32_bf16 v[16:19],a[192:195],v[36:39],v[16:19]
buffer_load_dwordx4 v2,s[32:35],s63 offen offset:3072 sc1 lds
s_add_u32 s63,s63,4096
v_mfma_f32_16x16x32_bf16 v[20:23],a[196:199],v[36:39],v[20:23]
ds_read_b128 v[60:63],v2 offset:54272
s_add_u32 m0,s64,69632
v_mfma_f32_16x16x32_bf16 v[20:23],a[84:87],v[32:35],v[20:23]
v_add_f32 v26,v26,v14
s_waitcnt lgkmcnt(4)
v_mfma_f32_16x16x32_bf16 v[20:23],v[116:119],v[40:43],v[20:23]
ds_read_b128 v[32:35],v2 offset:12288
v_add_f32 v27,v27,v15
v_mfma_f32_16x16x32_bf16 v[20:23],v[228:231],v[44:47],v[20:23]
buffer_load_dwordx4 v2,s[32:35],s63 offen offset:0 sc1 lds
ds_read_b128 v[12:15],v5 offset:1024
v_mfma_f32_16x16x32_bf16 v[16:19],v[224:227],v[44:47],v[16:19]
ds_read_b128 v[36:39],v2 offset:26624
v_mfma_f32_16x16x32_bf16 v[16:19],v[112:115],v[40:43],v[16:19]
s_waitcnt lgkmcnt(5)
v_mfma_f32_16x16x32_bf16 v[16:19],a[88:91],v[48:51],v[16:19]
ds_read_b128 v[40:43],v2 offset:40960
v_mfma_f32_16x16x32_bf16 v[16:19],a[200:203],v[52:55],v[16:19]
buffer_load_dwordx4 v2,s[32:35],s63 offen offset:1024 sc1 lds
s_add_u32 s63,s63,1175552
v_mfma_f32_16x16x32_bf16 v[20:23],a[204:207],v[52:55],v[20:23]
ds_read_b128 v[44:47],v2 offset:55296
s_waitcnt lgkmcnt(3)
v_mfma_f32_16x16x32_bf16 v[20:23],a[92:95],v[48:51],v[20:23]
v_add_f32 v28,v28,v12
v_mfma_f32_16x16x32_bf16 v[20:23],v[124:127],v[56:59],v[20:23]
ds_read_b128 v[48:51],v2 offset:13312
v_add_f32 v29,v29,v13
v_mfma_f32_16x16x32_bf16 v[20:23],v[236:239],v[60:63],v[20:23]
v_add_f32 v30,v30,v14
v_add_f32 v31,v31,v15
v_mfma_f32_16x16x32_bf16 v[16:19],v[232:235],v[60:63],v[16:19]
ds_read_b128 v[52:55],v2 offset:27648
v_cvt_pk_bf16_f32 v12,v24,v25
v_mfma_f32_16x16x32_bf16 v[16:19],v[120:123],v[56:59],v[16:19]
v_cvt_pk_bf16_f32 v13,v26,v27
s_waitcnt lgkmcnt(4)
v_mfma_f32_16x16x32_bf16 v[16:19],a[96:99],v[32:35],v[16:19]
s_waitcnt vmcnt(10)
s_barrier
v_mfma_f32_16x16x32_bf16 v[16:19],a[208:211],v[36:39],v[16:19]
ds_read_b128 v[56:59],v2 offset:41984
v_cvt_pk_bf16_f32 v14,v28,v29
v_mfma_f32_16x16x32_bf16 v[20:23],a[212:215],v[36:39],v[20:23]
ds_read_b128 v[60:63],v2 offset:56320
v_cvt_pk_bf16_f32 v15,v30,v31
v_mfma_f32_16x16x32_bf16 v[20:23],a[100:103],v[32:35],v[20:23]
s_waitcnt lgkmcnt(4)
v_mfma_f32_16x16x32_bf16 v[20:23],v[132:135],v[40:43],v[20:23]
ds_read_b128 v[32:35],v4 offset:0
v_mfma_f32_16x16x32_bf16 v[20:23],v[244:247],v[44:47],v[20:23]
buffer_store_dwordx4 v[12:15],v2,s[32:35],s68 offen sc1
v_mfma_f32_16x16x32_bf16 v[16:19],v[240:243],v[44:47],v[16:19]
ds_read_b128 v[36:39],v4 offset:14336
v_mfma_f32_16x16x32_bf16 v[16:19],v[128:131],v[40:43],v[16:19]
s_waitcnt lgkmcnt(4)
v_mfma_f32_16x16x32_bf16 v[16:19],a[104:107],v[48:51],v[16:19]
ds_read_b128 v[40:43],v4 offset:28672
v_mfma_f32_16x16x32_bf16 v[16:19],a[216:219],v[52:55],v[16:19]
v_mfma_f32_16x16x32_bf16 v[20:23],a[220:223],v[52:55],v[20:23]
ds_read_b128 v[44:47],v4 offset:43008
s_mov_b32 s67,s68
v_mfma_f32_16x16x32_bf16 v[20:23],a[108:111],v[48:51],v[20:23]
s_mov_b32 s68,s70
s_waitcnt lgkmcnt(4)
v_mfma_f32_16x16x32_bf16 v[20:23],v[140:143],v[56:59],v[20:23]
ds_read_b128 v[48:51],v4 offset:1024
s_add_u32 s70,s70,1187840
v_mfma_f32_16x16x32_bf16 v[20:23],v[252:255],v[60:63],v[20:23]
v_mfma_f32_16x16x32_bf16 v[16:19],v[248:251],v[60:63],v[16:19]
ds_read_b128 v[52:55],v4 offset:15360
s_sub_u32 s62,s62,1
v_mfma_f32_16x16x32_bf16 v[16:19],v[136:139],v[56:59],v[16:19]
s_cbranch_scc1 .L_dab_3
s_waitcnt lgkmcnt(4)
v_mfma_f32_16x16x32_bf16 v[24:27],a[0:3],v[32:35],0
ds_read_b128 v[56:59],v4 offset:29696
v_dot2_f32_bf16 v10,v12,s72,0
v_mfma_f32_16x16x32_bf16 v[24:27],a[112:115],v[36:39],v[24:27]
v_dot2_f32_bf16 v11,v12,v12,0
v_dot2_f32_bf16 v10,v13,s72,v10
v_mfma_f32_16x16x32_bf16 v[28:31],a[116:119],v[36:39],0
ds_read_b128 v[60:63],v4 offset:44032
v_dot2_f32_bf16 v11,v13,v13,v11
v_mfma_f32_16x16x32_bf16 v[28:31],a[4:7],v[32:35],v[28:31]
v_dot2_f32_bf16 v10,v14,s72,v10
s_waitcnt lgkmcnt(4)
v_mfma_f32_16x16x32_bf16 v[28:31],a[228:231],v[40:43],v[28:31]
ds_read_b128 v[32:35],v4 offset:2048
v_dot2_f32_bf16 v11,v14,v14,v11
v_mfma_f32_16x16x32_bf16 v[28:31],v[148:151],v[44:47],v[28:31]
v_dot2_f32_bf16 v10,v15,s72,v10
v_dot2_f32_bf16 v11,v15,v15,v11
v_mfma_f32_16x16x32_bf16 v[24:27],v[144:147],v[44:47],v[24:27]
ds_read_b128 v[36:39],v4 offset:16384
v_mfma_f32_16x16x32_bf16 v[24:27],a[224:227],v[40:43],v[24:27]
v_permlane16_swap_b32 v10,v11
s_waitcnt lgkmcnt(4)
v_mfma_f32_16x16x32_bf16 v[24:27],a[8:11],v[48:51],v[24:27]
ds_read_b128 v[40:43],v4 offset:30720
v_add_f32 v10,v10,v11
v_mfma_f32_16x16x32_bf16 v[24:27],a[120:123],v[52:55],v[24:27]
v_mfma_f32_16x16x32_bf16 v[28:31],a[124:127],v[52:55],v[28:31]
ds_read_b128 v[44:47],v4 offset:45056
v_permlane32_swap_b32 v10,v11
v_mfma_f32_16x16x32_bf16 v[28:31],a[12:15],v[48:51],v[28:31]
v_add_f32 v10,v10,v11
s_waitcnt lgkmcnt(4)
v_mfma_f32_16x16x32_bf16 v[28:31],a[236:239],v[56:59],v[28:31]
ds_read_b128 v[48:51],v4 offset:3072
v_mfma_f32_16x16x32_bf16 v[28:31],v[156:159],v[60:63],v[28:31]
buffer_store_dword v10,v3,s[32:35],s67 offen offset:1024 sc1
v_mfma_f32_16x16x32_bf16 v[24:27],v[152:155],v[60:63],v[24:27]
ds_read_b128 v[52:55],v4 offset:17408
v_mfma_f32_16x16x32_bf16 v[24:27],a[232:235],v[56:59],v[24:27]
s_waitcnt lgkmcnt(4)
v_mfma_f32_16x16x32_bf16 v[24:27],a[16:19],v[32:35],v[24:27]
s_waitcnt vmcnt(2)
s_barrier
v_mfma_f32_16x16x32_bf16 v[24:27],a[128:131],v[36:39],v[24:27]
ds_read_b128 v[56:59],v4 offset:31744
v_mfma_f32_16x16x32_bf16 v[28:31],a[132:135],v[36:39],v[28:31]
ds_read_b128 v[60:63],v4 offset:46080
v_mfma_f32_16x16x32_bf16 v[28:31],a[20:23],v[32:35],v[28:31]
s_waitcnt lgkmcnt(4)
v_mfma_f32_16x16x32_bf16 v[28:31],a[244:247],v[40:43],v[28:31]
ds_read_b128 v[32:35],v4 offset:4096
s_mov_b32 m0,s74
v_mfma_f32_16x16x32_bf16 v[28:31],v[164:167],v[44:47],v[28:31]
buffer_load_dwordx4 v2,s[32:35],s71 offen offset:0 sc1 lds
v_mfma_f32_16x16x32_bf16 v[24:27],v[160:163],v[44:47],v[24:27]
ds_read_b128 v[36:39],v4 offset:18432
v_mfma_f32_16x16x32_bf16 v[24:27],a[240:243],v[40:43],v[24:27]
s_waitcnt lgkmcnt(4)
v_mfma_f32_16x16x32_bf16 v[24:27],a[24:27],v[48:51],v[24:27]
ds_read_b128 v[40:43],v4 offset:32768
v_mfma_f32_16x16x32_bf16 v[24:27],a[136:139],v[52:55],v[24:27]
buffer_load_dwordx4 v2,s[32:35],s71 offen offset:1024 sc1 lds
s_add_u32 s71,s71,1187840
v_mfma_f32_16x16x32_bf16 v[28:31],a[140:143],v[52:55],v[28:31]
ds_read_b128 v[44:47],v4 offset:47104
s_add_u32 m0,s64,0
v_mfma_f32_16x16x32_bf16 v[28:31],a[28:31],v[48:51],v[28:31]
s_waitcnt lgkmcnt(4)
v_mfma_f32_16x16x32_bf16 v[28:31],a[252:255],v[56:59],v[28:31]
ds_read_b128 v[48:51],v4 offset:5120
v_mfma_f32_16x16x32_bf16 v[28:31],v[172:175],v[60:63],v[28:31]
buffer_load_dwordx4 v2,s[32:35],s63 offen offset:0 sc1 lds
v_mfma_f32_16x16x32_bf16 v[24:27],v[168:171],v[60:63],v[24:27]
ds_read_b128 v[52:55],v4 offset:19456
v_mfma_f32_16x16x32_bf16 v[24:27],a[248:251],v[56:59],v[24:27]
s_waitcnt lgkmcnt(4)
v_mfma_f32_16x16x32_bf16 v[24:27],a[32:35],v[32:35],v[24:27]
ds_read_b128 v[56:59],v4 offset:33792
v_mfma_f32_16x16x32_bf16 v[24:27],a[144:147],v[36:39],v[24:27]
buffer_load_dwordx4 v2,s[32:35],s63 offen offset:1024 sc1 lds
v_mfma_f32_16x16x32_bf16 v[28:31],a[148:151],v[36:39],v[28:31]
ds_read_b128 v[60:63],v4 offset:48128
v_mfma_f32_16x16x32_bf16 v[28:31],a[36:39],v[32:35],v[28:31]
s_waitcnt lgkmcnt(4)
v_mfma_f32_16x16x32_bf16 v[28:31],v[68:71],v[40:43],v[28:31]
ds_read_b128 v[32:35],v4 offset:6144
v_mfma_f32_16x16x32_bf16 v[28:31],v[180:183],v[44:47],v[28:31]
buffer_load_dwordx4 v2,s[32:35],s63 offen offset:2048 sc1 lds
v_mfma_f32_16x16x32_bf16 v[24:27],v[176:179],v[44:47],v[24:27]
ds_read_b128 v[36:39],v4 offset:20480
v_mfma_f32_16x16x32_bf16 v[24:27],v[64:67],v[40:43],v[24:27]
s_waitcnt lgkmcnt(4)
v_mfma_f32_16x16x32_bf16 v[24:27],a[40:43],v[48:51],v[24:27]
ds_read_b128 v[40:43],v4 offset:34816
v_mfma_f32_16x16x32_bf16 v[24:27],a[152:155],v[52:55],v[24:27]
buffer_load_dwordx4 v2,s[32:35],s63 offen offset:3072 sc1 lds
s_add_u32 s63,s63,4096
v_mfma_f32_16x16x32_bf16 v[28:31],a[156:159],v[52:55],v[28:31]
ds_read_b128 v[44:47],v4 offset:49152
s_add_u32 m0,s64,4096
v_mfma_f32_16x16x32_bf16 v[28:31],a[44:47],v[48:51],v[28:31]
s_waitcnt lgkmcnt(4)
v_mfma_f32_16x16x32_bf16 v[28:31],v[76:79],v[56:59],v[28:31]
)#"
R"#(
ds_read_b128 v[48:51],v4 offset:7168
v_mfma_f32_16x16x32_bf16 v[28:31],v[188:191],v[60:63],v[28:31]
buffer_load_dwordx4 v2,s[32:35],s63 offen offset:0 sc1 lds
v_mfma_f32_16x16x32_bf16 v[24:27],v[184:187],v[60:63],v[24:27]
ds_read_b128 v[52:55],v4 offset:21504
v_mfma_f32_16x16x32_bf16 v[24:27],v[72:75],v[56:59],v[24:27]
s_waitcnt lgkmcnt(4)
v_mfma_f32_16x16x32_bf16 v[24:27],a[48:51],v[32:35],v[24:27]
ds_read_b128 v[56:59],v4 offset:35840
v_mfma_f32_16x16x32_bf16 v[24:27],a[160:163],v[36:39],v[24:27]
buffer_load_dwordx4 v2,s[32:35],s63 offen offset:1024 sc1 lds
v_mfma_f32_16x16x32_bf16 v[28:31],a[164:167],v[36:39],v[28:31]
ds_read_b128 v[60:63],v4 offset:50176
v_mfma_f32_16x16x32_bf16 v[28:31],a[52:55],v[32:35],v[28:31]
s_waitcnt lgkmcnt(4)
v_mfma_f32_16x16x32_bf16 v[28:31],v[84:87],v[40:43],v[28:31]
ds_read_b128 v[32:35],v4 offset:8192
v_mfma_f32_16x16x32_bf16 v[28:31],v[196:199],v[44:47],v[28:31]
buffer_load_dwordx4 v2,s[32:35],s63 offen offset:2048 sc1 lds
v_mfma_f32_16x16x32_bf16 v[24:27],v[192:195],v[44:47],v[24:27]
ds_read_b128 v[36:39],v4 offset:22528
v_mfma_f32_16x16x32_bf16 v[24:27],v[80:83],v[40:43],v[24:27]
s_waitcnt lgkmcnt(4)
v_mfma_f32_16x16x32_bf16 v[24:27],a[56:59],v[48:51],v[24:27]
ds_read_b128 v[40:43],v4 offset:36864
v_mfma_f32_16x16x32_bf16 v[24:27],a[168:171],v[52:55],v[24:27]
buffer_load_dwordx4 v2,s[32:35],s63 offen offset:3072 sc1 lds
v_mfma_f32_16x16x32_bf16 v[28:31],a[172:175],v[52:55],v[28:31]
ds_read_b128 v[44:47],v4 offset:51200
v_mfma_f32_16x16x32_bf16 v[28:31],a[60:63],v[48:51],v[28:31]
s_waitcnt lgkmcnt(4)
v_mfma_f32_16x16x32_bf16 v[28:31],v[92:95],v[56:59],v[28:31]
ds_read_b128 v[48:51],v4 offset:9216
v_mfma_f32_16x16x32_bf16 v[28:31],v[204:207],v[60:63],v[28:31]
s_add_u32 s63,s63,4096
s_add_u32 m0,s64,8192
v_mfma_f32_16x16x32_bf16 v[24:27],v[200:203],v[60:63],v[24:27]
ds_read_b128 v[52:55],v4 offset:23552
v_mfma_f32_16x16x32_bf16 v[24:27],v[88:91],v[56:59],v[24:27]
s_waitcnt lgkmcnt(4)
v_mfma_f32_16x16x32_bf16 v[24:27],a[64:67],v[32:35],v[24:27]
ds_read_b128 v[56:59],v4 offset:37888
v_mfma_f32_16x16x32_bf16 v[24:27],a[176:179],v[36:39],v[24:27]
buffer_load_dwordx4 v2,s[32:35],s63 offen offset:0 sc1 lds
v_mfma_f32_16x16x32_bf16 v[28:31],a[180:183],v[36:39],v[28:31]
ds_read_b128 v[60:63],v4 offset:52224
v_mfma_f32_16x16x32_bf16 v[28:31],a[68:71],v[32:35],v[28:31]
s_waitcnt lgkmcnt(4)
v_mfma_f32_16x16x32_bf16 v[28:31],v[100:103],v[40:43],v[28:31]
ds_read_b128 v[32:35],v4 offset:10240
v_mfma_f32_16x16x32_bf16 v[28:31],v[212:215],v[44:47],v[28:31]
buffer_load_dwordx4 v2,s[32:35],s63 offen offset:1024 sc1 lds
v_mfma_f32_16x16x32_bf16 v[24:27],v[208:211],v[44:47],v[24:27]
ds_read_b128 v[36:39],v4 offset:24576
v_mfma_f32_16x16x32_bf16 v[24:27],v[96:99],v[40:43],v[24:27]
s_waitcnt lgkmcnt(4)
v_mfma_f32_16x16x32_bf16 v[24:27],a[72:75],v[48:51],v[24:27]
ds_read_b128 v[40:43],v4 offset:38912
s_waitcnt vmcnt(10)
v_mfma_f32_16x16x32_bf16 v[24:27],a[184:187],v[52:55],v[24:27]
buffer_load_dwordx4 v2,s[32:35],s63 offen offset:2048 sc1 lds
ds_read_b128 v[12:15],v5 offset:0
v_mfma_f32_16x16x32_bf16 v[28:31],a[188:191],v[52:55],v[28:31]
ds_read_b128 v[44:47],v4 offset:53248
v_mfma_f32_16x16x32_bf16 v[28:31],a[76:79],v[48:51],v[28:31]
s_waitcnt lgkmcnt(5)
v_mfma_f32_16x16x32_bf16 v[28:31],v[108:111],v[56:59],v[28:31]
ds_read_b128 v[48:51],v4 offset:11264
v_mfma_f32_16x16x32_bf16 v[28:31],v[220:223],v[60:63],v[28:31]
v_mfma_f32_16x16x32_bf16 v[24:27],v[216:219],v[60:63],v[24:27]
ds_read_b128 v[52:55],v4 offset:25600
s_waitcnt lgkmcnt(3)
v_mfma_f32_16x16x32_bf16 v[24:27],v[104:107],v[56:59],v[24:27]
v_add_f32 v16,v16,v12
v_mfma_f32_16x16x32_bf16 v[24:27],a[80:83],v[32:35],v[24:27]
ds_read_b128 v[56:59],v4 offset:39936
v_add_f32 v17,v17,v13
v_mfma_f32_16x16x32_bf16 v[24:27],a[192:195],v[36:39],v[24:27]
buffer_load_dwordx4 v2,s[32:35],s63 offen offset:3072 sc1 lds
s_add_u32 s63,s63,4096
v_mfma_f32_16x16x32_bf16 v[28:31],a[196:199],v[36:39],v[28:31]
ds_read_b128 v[60:63],v4 offset:54272
s_add_u32 m0,s64,12288
v_mfma_f32_16x16x32_bf16 v[28:31],a[84:87],v[32:35],v[28:31]
v_add_f32 v18,v18,v14
s_waitcnt lgkmcnt(4)
v_mfma_f32_16x16x32_bf16 v[28:31],v[116:119],v[40:43],v[28:31]
ds_read_b128 v[32:35],v4 offset:12288
v_add_f32 v19,v19,v15
v_mfma_f32_16x16x32_bf16 v[28:31],v[228:231],v[44:47],v[28:31]
buffer_load_dwordx4 v2,s[32:35],s63 offen offset:0 sc1 lds
ds_read_b128 v[12:15],v5 offset:1024
v_mfma_f32_16x16x32_bf16 v[24:27],v[224:227],v[44:47],v[24:27]
ds_read_b128 v[36:39],v4 offset:26624
v_mfma_f32_16x16x32_bf16 v[24:27],v[112:115],v[40:43],v[24:27]
s_waitcnt lgkmcnt(5)
v_mfma_f32_16x16x32_bf16 v[24:27],a[88:91],v[48:51],v[24:27]
ds_read_b128 v[40:43],v4 offset:40960
v_mfma_f32_16x16x32_bf16 v[24:27],a[200:203],v[52:55],v[24:27]
buffer_load_dwordx4 v2,s[32:35],s63 offen offset:1024 sc1 lds
s_add_u32 s63,s63,1175552
v_mfma_f32_16x16x32_bf16 v[28:31],a[204:207],v[52:55],v[28:31]
ds_read_b128 v[44:47],v4 offset:55296
s_waitcnt lgkmcnt(3)
v_mfma_f32_16x16x32_bf16 v[28:31],a[92:95],v[48:51],v[28:31]
v_add_f32 v20,v20,v12
v_mfma_f32_16x16x32_bf16 v[28:31],v[124:127],v[56:59],v[28:31]
ds_read_b128 v[48:51],v4 offset:13312
v_add_f32 v21,v21,v13
v_mfma_f32_16x16x32_bf16 v[28:31],v[236:239],v[60:63],v[28:31]
v_add_f32 v22,v22,v14
v_add_f32 v23,v23,v15
v_mfma_f32_16x16x32_bf16 v[24:27],v[232:235],v[60:63],v[24:27]
ds_read_b128 v[52:55],v4 offset:27648
v_cvt_pk_bf16_f32 v12,v16,v17
v_mfma_f32_16x16x32_bf16 v[24:27],v[120:123],v[56:59],v[24:27]
v_cvt_pk_bf16_f32 v13,v18,v19
s_waitcnt lgkmcnt(4)
v_mfma_f32_16x16x32_bf16 v[24:27],a[96:99],v[32:35],v[24:27]
s_waitcnt vmcnt(10)
s_barrier
v_mfma_f32_16x16x32_bf16 v[24:27],a[208:211],v[36:39],v[24:27]
ds_read_b128 v[56:59],v4 offset:41984
v_cvt_pk_bf16_f32 v14,v20,v21
v_mfma_f32_16x16x32_bf16 v[28:31],a[212:215],v[36:39],v[28:31]
ds_read_b128 v[60:63],v4 offset:56320
v_cvt_pk_bf16_f32 v15,v22,v23
v_mfma_f32_16x16x32_bf16 v[28:31],a[100:103],v[32:35],v[28:31]
s_waitcnt lgkmcnt(4)
v_mfma_f32_16x16x32_bf16 v[28:31],v[132:135],v[40:43],v[28:31]
ds_read_b128 v[32:35],v2 offset:0
v_mfma_f32_16x16x32_bf16 v[28:31],v[244:247],v[44:47],v[28:31]
buffer_store_dwordx4 v[12:15],v2,s[32:35],s69 offen sc1
v_mfma_f32_16x16x32_bf16 v[24:27],v[240:243],v[44:47],v[24:27]
ds_read_b128 v[36:39],v2 offset:14336
v_mfma_f32_16x16x32_bf16 v[24:27],v[128:131],v[40:43],v[24:27]
s_waitcnt lgkmcnt(4)
v_mfma_f32_16x16x32_bf16 v[24:27],a[104:107],v[48:51],v[24:27]
ds_read_b128 v[40:43],v2 offset:28672
v_mfma_f32_16x16x32_bf16 v[24:27],a[216:219],v[52:55],v[24:27]
v_mfma_f32_16x16x32_bf16 v[28:31],a[220:223],v[52:55],v[28:31]
ds_read_b128 v[44:47],v2 offset:43008
s_mov_b32 s67,s69
v_mfma_f32_16x16x32_bf16 v[28:31],a[108:111],v[48:51],v[28:31]
s_mov_b32 s69,s70
s_waitcnt lgkmcnt(4)
v_mfma_f32_16x16x32_bf16 v[28:31],v[140:143],v[56:59],v[28:31]
ds_read_b128 v[48:51],v2 offset:1024
s_add_u32 s70,s70,1187840
v_mfma_f32_16x16x32_bf16 v[28:31],v[252:255],v[60:63],v[28:31]
v_mfma_f32_16x16x32_bf16 v[24:27],v[248:251],v[60:63],v[24:27]
ds_read_b128 v[52:55],v2 offset:15360
v_mfma_f32_16x16x32_bf16 v[24:27],v[136:139],v[56:59],v[24:27]
s_branch .L_dal_3
.L_dab_3:
v_dot2_f32_bf16 v10,v12,s72,0
v_dot2_f32_bf16 v11,v12,v12,0
v_dot2_f32_bf16 v10,v13,s72,v10
v_dot2_f32_bf16 v11,v13,v13,v11
v_dot2_f32_bf16 v10,v14,s72,v10
v_dot2_f32_bf16 v11,v14,v14,v11
v_dot2_f32_bf16 v10,v15,s72,v10
v_dot2_f32_bf16 v11,v15,v15,v11
s_nop 2
v_permlane16_swap_b32 v10,v11
v_add_f32 v10,v10,v11
s_nop 1
v_permlane32_swap_b32 v10,v11
v_add_f32 v10,v10,v11
buffer_store_dword v10,v3,s[32:35],s67 offen offset:1024 sc1
s_mov_b32 s62,15
s_add_u32 s74,s74,8192
v_lshrrev_b32 v5,8,v2
v_lshl_add_u32 v5,v5,4,s74
v_and_b32 v3,128,v2
v_lshl_add_u32 v5,v3,2,v5
v_and_b32 v3,16,v2
v_lshl_add_u32 v5,v3,4,v5
v_and_b32 v3,96,v2
v_lshl_add_u32 v5,v3,1,v5
v_lshrrev_b32 v3,2,v2
v_add_u32 v3,s74,v3
.p2align 6
.L_dbl_3:
s_waitcnt lgkmcnt(4)
v_mfma_f32_16x16x32_bf16 v[24:27],a[0:3],v[32:35],0
ds_read_b128 v[56:59],v4 offset:29696
v_mfma_f32_16x16x32_bf16 v[24:27],a[112:115],v[36:39],v[24:27]
v_mfma_f32_16x16x32_bf16 v[28:31],a[116:119],v[36:39],0
ds_read_b128 v[60:63],v4 offset:44032
v_mfma_f32_16x16x32_bf16 v[28:31],a[4:7],v[32:35],v[28:31]
v_cvt_pk_bf16_f32 v15,v22,v23
s_waitcnt lgkmcnt(4)
v_mfma_f32_16x16x32_bf16 v[28:31],a[228:231],v[40:43],v[28:31]
ds_read_b128 v[32:35],v4 offset:2048
v_cvt_pk_bf16_f32 v14,v20,v21
v_mfma_f32_16x16x32_bf16 v[28:31],v[148:151],v[44:47],v[28:31]
v_cvt_pk_bf16_f32 v13,v18,v19
v_cvt_pk_bf16_f32 v12,v16,v17
v_mfma_f32_16x16x32_bf16 v[24:27],v[144:147],v[44:47],v[24:27]
ds_read_b128 v[36:39],v4 offset:16384
ds_write_b128 v5,v[12:15]
v_mfma_f32_16x16x32_bf16 v[24:27],a[224:227],v[40:43],v[24:27]
ds_read2st64_b32 v[12:13],v3 offset0:0 offset1:1
s_waitcnt lgkmcnt(6)
v_mfma_f32_16x16x32_bf16 v[24:27],a[8:11],v[48:51],v[24:27]
ds_read_b128 v[40:43],v4 offset:30720
ds_read2st64_b32 v[14:15],v3 offset0:2 offset1:3
v_mfma_f32_16x16x32_bf16 v[24:27],a[120:123],v[52:55],v[24:27]
v_mfma_f32_16x16x32_bf16 v[28:31],a[124:127],v[52:55],v[28:31]
ds_read_b128 v[44:47],v4 offset:45056
v_mfma_f32_16x16x32_bf16 v[28:31],a[12:15],v[48:51],v[28:31]
s_waitcnt lgkmcnt(7)
v_mfma_f32_16x16x32_bf16 v[28:31],a[236:239],v[56:59],v[28:31]
ds_read_b128 v[48:51],v4 offset:3072
v_mfma_f32_16x16x32_bf16 v[28:31],v[156:159],v[60:63],v[28:31]
s_waitcnt lgkmcnt(2)
buffer_atomic_pk_add_bf16 v12,v1,s[44:47],s65 offen offset:0 sc1
v_mfma_f32_16x16x32_bf16 v[24:27],v[152:155],v[60:63],v[24:27]
ds_read_b128 v[52:55],v4 offset:17408
v_mfma_f32_16x16x32_bf16 v[24:27],a[232:235],v[56:59],v[24:27]
buffer_atomic_pk_add_bf16 v13,v1,s[44:47],s65 offen offset:2048 sc1
v_mfma_f32_16x16x32_bf16 v[24:27],a[16:19],v[32:35],v[24:27]
s_waitcnt vmcnt(2)
s_barrier
v_mfma_f32_16x16x32_bf16 v[24:27],a[128:131],v[36:39],v[24:27]
ds_read_b128 v[56:59],v4 offset:31744
s_add_u32 s65,s65,16384
v_mfma_f32_16x16x32_bf16 v[28:31],a[132:135],v[36:39],v[28:31]
ds_read_b128 v[60:63],v4 offset:46080
v_mfma_f32_16x16x32_bf16 v[28:31],a[20:23],v[32:35],v[28:31]
buffer_atomic_pk_add_bf16 v14,v1,s[44:47],s65 offen offset:0 sc1
s_waitcnt lgkmcnt(4)
v_mfma_f32_16x16x32_bf16 v[28:31],a[244:247],v[40:43],v[28:31]
ds_read_b128 v[32:35],v4 offset:4096
s_add_u32 m0,s64,0
v_mfma_f32_16x16x32_bf16 v[28:31],v[164:167],v[44:47],v[28:31]
buffer_atomic_pk_add_bf16 v15,v1,s[44:47],s65 offen offset:2048 sc1
s_add_u32 s65,s65,16384
v_mfma_f32_16x16x32_bf16 v[24:27],v[160:163],v[44:47],v[24:27]
ds_read_b128 v[36:39],v4 offset:18432
v_mfma_f32_16x16x32_bf16 v[24:27],a[240:243],v[40:43],v[24:27]
s_waitcnt lgkmcnt(4)
v_mfma_f32_16x16x32_bf16 v[24:27],a[24:27],v[48:51],v[24:27]
ds_read_b128 v[40:43],v4 offset:32768
v_mfma_f32_16x16x32_bf16 v[24:27],a[136:139],v[52:55],v[24:27]
buffer_load_dwordx4 v2,s[32:35],s63 offen offset:0 sc1 lds
v_mfma_f32_16x16x32_bf16 v[28:31],a[140:143],v[52:55],v[28:31]
ds_read_b128 v[44:47],v4 offset:47104
v_mfma_f32_16x16x32_bf16 v[28:31],a[28:31],v[48:51],v[28:31]
s_waitcnt lgkmcnt(4)
v_mfma_f32_16x16x32_bf16 v[28:31],a[252:255],v[56:59],v[28:31]
ds_read_b128 v[48:51],v4 offset:5120
v_mfma_f32_16x16x32_bf16 v[28:31],v[172:175],v[60:63],v[28:31]
buffer_load_dwordx4 v2,s[32:35],s63 offen offset:1024 sc1 lds
v_mfma_f32_16x16x32_bf16 v[24:27],v[168:171],v[60:63],v[24:27]
ds_read_b128 v[52:55],v4 offset:19456
v_mfma_f32_16x16x32_bf16 v[24:27],a[248:251],v[56:59],v[24:27]
s_waitcnt lgkmcnt(4)
v_mfma_f32_16x16x32_bf16 v[24:27],a[32:35],v[32:35],v[24:27]
ds_read_b128 v[56:59],v4 offset:33792
v_mfma_f32_16x16x32_bf16 v[24:27],a[144:147],v[36:39],v[24:27]
buffer_load_dwordx4 v2,s[32:35],s63 offen offset:2048 sc1 lds
v_mfma_f32_16x16x32_bf16 v[28:31],a[148:151],v[36:39],v[28:31]
ds_read_b128 v[60:63],v4 offset:48128
v_mfma_f32_16x16x32_bf16 v[28:31],a[36:39],v[32:35],v[28:31]
s_waitcnt lgkmcnt(4)
v_mfma_f32_16x16x32_bf16 v[28:31],v[68:71],v[40:43],v[28:31]
ds_read_b128 v[32:35],v4 offset:6144
v_mfma_f32_16x16x32_bf16 v[28:31],v[180:183],v[44:47],v[28:31]
buffer_load_dwordx4 v2,s[32:35],s63 offen offset:3072 sc1 lds
v_mfma_f32_16x16x32_bf16 v[24:27],v[176:179],v[44:47],v[24:27]
ds_read_b128 v[36:39],v4 offset:20480
v_mfma_f32_16x16x32_bf16 v[24:27],v[64:67],v[40:43],v[24:27]
s_waitcnt lgkmcnt(4)
v_mfma_f32_16x16x32_bf16 v[24:27],a[40:43],v[48:51],v[24:27]
ds_read_b128 v[40:43],v4 offset:34816
s_add_u32 s63,s63,4096
v_mfma_f32_16x16x32_bf16 v[24:27],a[152:155],v[52:55],v[24:27]
s_add_u32 m0,s64,4096
v_mfma_f32_16x16x32_bf16 v[28:31],a[156:159],v[52:55],v[28:31]
ds_read_b128 v[44:47],v4 offset:49152
v_mfma_f32_16x16x32_bf16 v[28:31],a[44:47],v[48:51],v[28:31]
s_waitcnt lgkmcnt(4)
v_mfma_f32_16x16x32_bf16 v[28:31],v[76:79],v[56:59],v[28:31]
ds_read_b128 v[48:51],v4 offset:7168
v_mfma_f32_16x16x32_bf16 v[28:31],v[188:191],v[60:63],v[28:31]
buffer_load_dwordx4 v2,s[32:35],s63 offen offset:0 sc1 lds
v_mfma_f32_16x16x32_bf16 v[24:27],v[184:187],v[60:63],v[24:27]
ds_read_b128 v[52:55],v4 offset:21504
v_mfma_f32_16x16x32_bf16 v[24:27],v[72:75],v[56:59],v[24:27]
s_waitcnt lgkmcnt(4)
v_mfma_f32_16x16x32_bf16 v[24:27],a[48:51],v[32:35],v[24:27]
ds_read_b128 v[56:59],v4 offset:35840
v_mfma_f32_16x16x32_bf16 v[24:27],a[160:163],v[36:39],v[24:27]
buffer_load_dwordx4 v2,s[32:35],s63 offen offset:1024 sc1 lds
v_mfma_f32_16x16x32_bf16 v[28:31],a[164:167],v[36:39],v[28:31]
ds_read_b128 v[60:63],v4 offset:50176
v_mfma_f32_16x16x32_bf16 v[28:31],a[52:55],v[32:35],v[28:31]
s_waitcnt lgkmcnt(4)
v_mfma_f32_16x16x32_bf16 v[28:31],v[84:87],v[40:43],v[28:31]
ds_read_b128 v[32:35],v4 offset:8192
v_mfma_f32_16x16x32_bf16 v[28:31],v[196:199],v[44:47],v[28:31]
buffer_load_dwordx4 v2,s[32:35],s63 offen offset:2048 sc1 lds
v_mfma_f32_16x16x32_bf16 v[24:27],v[192:195],v[44:47],v[24:27]
ds_read_b128 v[36:39],v4 offset:22528
v_mfma_f32_16x16x32_bf16 v[24:27],v[80:83],v[40:43],v[24:27]
s_waitcnt lgkmcnt(4)
v_mfma_f32_16x16x32_bf16 v[24:27],a[56:59],v[48:51],v[24:27]
ds_read_b128 v[40:43],v4 offset:36864
v_mfma_f32_16x16x32_bf16 v[24:27],a[168:171],v[52:55],v[24:27]
buffer_load_dwordx4 v2,s[32:35],s63 offen offset:3072 sc1 lds
v_mfma_f32_16x16x32_bf16 v[28:31],a[172:175],v[52:55],v[28:31]
ds_read_b128 v[44:47],v4 offset:51200
v_mfma_f32_16x16x32_bf16 v[28:31],a[60:63],v[48:51],v[28:31]
s_waitcnt lgkmcnt(4)
v_mfma_f32_16x16x32_bf16 v[28:31],v[92:95],v[56:59],v[28:31]
ds_read_b128 v[48:51],v4 offset:9216
v_mfma_f32_16x16x32_bf16 v[28:31],v[204:207],v[60:63],v[28:31]
s_add_u32 s63,s63,4096
s_add_u32 m0,s64,8192
v_mfma_f32_16x16x32_bf16 v[24:27],v[200:203],v[60:63],v[24:27]
ds_read_b128 v[52:55],v4 offset:23552
v_mfma_f32_16x16x32_bf16 v[24:27],v[88:91],v[56:59],v[24:27]
s_waitcnt lgkmcnt(4)
v_mfma_f32_16x16x32_bf16 v[24:27],a[64:67],v[32:35],v[24:27]
ds_read_b128 v[56:59],v4 offset:37888
v_mfma_f32_16x16x32_bf16 v[24:27],a[176:179],v[36:39],v[24:27]
buffer_load_dwordx4 v2,s[32:35],s63 offen offset:0 sc1 lds
v_mfma_f32_16x16x32_bf16 v[28:31],a[180:183],v[36:39],v[28:31]
ds_read_b128 v[60:63],v4 offset:52224
v_mfma_f32_16x16x32_bf16 v[28:31],a[68:71],v[32:35],v[28:31]
s_waitcnt lgkmcnt(4)
v_mfma_f32_16x16x32_bf16 v[28:31],v[100:103],v[40:43],v[28:31]
ds_read_b128 v[32:35],v4 offset:10240
v_mfma_f32_16x16x32_bf16 v[28:31],v[212:215],v[44:47],v[28:31]
buffer_load_dwordx4 v2,s[32:35],s63 offen offset:1024 sc1 lds
v_mfma_f32_16x16x32_bf16 v[24:27],v[208:211],v[44:47],v[24:27]
ds_read_b128 v[36:39],v4 offset:24576
v_mfma_f32_16x16x32_bf16 v[24:27],v[96:99],v[40:43],v[24:27]
s_waitcnt lgkmcnt(4)
v_mfma_f32_16x16x32_bf16 v[24:27],a[72:75],v[48:51],v[24:27]
ds_read_b128 v[40:43],v4 offset:38912
v_mfma_f32_16x16x32_bf16 v[24:27],a[184:187],v[52:55],v[24:27]
buffer_load_dwordx4 v2,s[32:35],s63 offen offset:2048 sc1 lds
v_mfma_f32_16x16x32_bf16 v[28:31],a[188:191],v[52:55],v[28:31]
ds_read_b128 v[44:47],v4 offset:53248
v_mfma_f32_16x16x32_bf16 v[28:31],a[76:79],v[48:51],v[28:31]
s_waitcnt lgkmcnt(4)
v_mfma_f32_16x16x32_bf16 v[28:31],v[108:111],v[56:59],v[28:31]
ds_read_b128 v[48:51],v4 offset:11264
v_mfma_f32_16x16x32_bf16 v[28:31],v[220:223],v[60:63],v[28:31]
v_mfma_f32_16x16x32_bf16 v[24:27],v[216:219],v[60:63],v[24:27]
ds_read_b128 v[52:55],v4 offset:25600
v_mfma_f32_16x16x32_bf16 v[24:27],v[104:107],v[56:59],v[24:27]
s_waitcnt lgkmcnt(4)
v_mfma_f32_16x16x32_bf16 v[24:27],a[80:83],v[32:35],v[24:27]
ds_read_b128 v[56:59],v4 offset:39936
v_mfma_f32_16x16x32_bf16 v[24:27],a[192:195],v[36:39],v[24:27]
buffer_load_dwordx4 v2,s[32:35],s63 offen offset:3072 sc1 lds
s_add_u32 s63,s63,4096
v_mfma_f32_16x16x32_bf16 v[28:31],a[196:199],v[36:39],v[28:31]
ds_read_b128 v[60:63],v4 offset:54272
s_add_u32 m0,s64,12288
v_mfma_f32_16x16x32_bf16 v[28:31],a[84:87],v[32:35],v[28:31]
s_waitcnt lgkmcnt(4)
v_mfma_f32_16x16x32_bf16 v[28:31],v[116:119],v[40:43],v[28:31]
ds_read_b128 v[32:35],v4 offset:12288
v_mfma_f32_16x16x32_bf16 v[28:31],v[228:231],v[44:47],v[28:31]
buffer_load_dwordx4 v2,s[32:35],s63 offen offset:0 sc1 lds
v_mfma_f32_16x16x32_bf16 v[24:27],v[224:227],v[44:47],v[24:27]
ds_read_b128 v[36:39],v4 offset:26624
v_mfma_f32_16x16x32_bf16 v[24:27],v[112:115],v[40:43],v[24:27]
s_waitcnt lgkmcnt(4)
v_mfma_f32_16x16x32_bf16 v[24:27],a[88:91],v[48:51],v[24:27]
ds_read_b128 v[40:43],v4 offset:40960
v_mfma_f32_16x16x32_bf16 v[24:27],a[200:203],v[52:55],v[24:27]
buffer_load_dwordx4 v2,s[32:35],s63 offen offset:1024 sc1 lds
s_add_u32 s63,s63,1175552
v_mfma_f32_16x16x32_bf16 v[28:31],a[204:207],v[52:55],v[28:31]
ds_read_b128 v[44:47],v4 offset:55296
v_mfma_f32_16x16x32_bf16 v[28:31],a[92:95],v[48:51],v[28:31]
s_waitcnt lgkmcnt(4)
v_mfma_f32_16x16x32_bf16 v[28:31],v[124:127],v[56:59],v[28:31]
ds_read_b128 v[48:51],v4 offset:13312
v_mfma_f32_16x16x32_bf16 v[28:31],v[236:239],v[60:63],v[28:31]
v_mfma_f32_16x16x32_bf16 v[24:27],v[232:235],v[60:63],v[24:27]
ds_read_b128 v[52:55],v4 offset:27648
v_mfma_f32_16x16x32_bf16 v[24:27],v[120:123],v[56:59],v[24:27]
s_waitcnt lgkmcnt(4)
v_mfma_f32_16x16x32_bf16 v[24:27],a[96:99],v[32:35],v[24:27]
s_waitcnt vmcnt(10)
s_barrier
v_mfma_f32_16x16x32_bf16 v[24:27],a[208:211],v[36:39],v[24:27]
ds_read_b128 v[56:59],v4 offset:41984
v_mfma_f32_16x16x32_bf16 v[28:31],a[212:215],v[36:39],v[28:31]
ds_read_b128 v[60:63],v4 offset:56320
v_mfma_f32_16x16x32_bf16 v[28:31],a[100:103],v[32:35],v[28:31]
s_waitcnt lgkmcnt(4)
v_mfma_f32_16x16x32_bf16 v[28:31],v[132:135],v[40:43],v[28:31]
ds_read_b128 v[32:35],v2 offset:0
v_mfma_f32_16x16x32_bf16 v[28:31],v[244:247],v[44:47],v[28:31]
v_mfma_f32_16x16x32_bf16 v[24:27],v[240:243],v[44:47],v[24:27]
ds_read_b128 v[36:39],v2 offset:14336
v_mfma_f32_16x16x32_bf16 v[24:27],v[128:131],v[40:43],v[24:27]
s_waitcnt lgkmcnt(4)
v_mfma_f32_16x16x32_bf16 v[24:27],a[104:107],v[48:51],v[24:27]
ds_read_b128 v[40:43],v2 offset:28672
v_mfma_f32_16x16x32_bf16 v[24:27],a[216:219],v[52:55],v[24:27]
v_mfma_f32_16x16x32_bf16 v[28:31],a[220:223],v[52:55],v[28:31]
ds_read_b128 v[44:47],v2 offset:43008
v_mfma_f32_16x16x32_bf16 v[28:31],a[108:111],v[48:51],v[28:31]
s_waitcnt lgkmcnt(4)
v_mfma_f32_16x16x32_bf16 v[28:31],v[140:143],v[56:59],v[28:31]
ds_read_b128 v[48:51],v2 offset:1024
v_mfma_f32_16x16x32_bf16 v[28:31],v[252:255],v[60:63],v[28:31]
v_mfma_f32_16x16x32_bf16 v[24:27],v[248:251],v[60:63],v[24:27]
ds_read_b128 v[52:55],v2 offset:15360
s_sub_u32 s62,s62,1
v_mfma_f32_16x16x32_bf16 v[24:27],v[136:139],v[56:59],v[24:27]
s_cbranch_scc1 .L_dbt_3
s_waitcnt lgkmcnt(4)
v_mfma_f32_16x16x32_bf16 v[16:19],a[0:3],v[32:35],0
ds_read_b128 v[56:59],v2 offset:29696
v_mfma_f32_16x16x32_bf16 v[16:19],a[112:115],v[36:39],v[16:19]
v_mfma_f32_16x16x32_bf16 v[20:23],a[116:119],v[36:39],0
ds_read_b128 v[60:63],v2 offset:44032
v_mfma_f32_16x16x32_bf16 v[20:23],a[4:7],v[32:35],v[20:23]
v_cvt_pk_bf16_f32 v15,v30,v31
s_waitcnt lgkmcnt(4)
v_mfma_f32_16x16x32_bf16 v[20:23],a[228:231],v[40:43],v[20:23]
ds_read_b128 v[32:35],v2 offset:2048
v_cvt_pk_bf16_f32 v14,v28,v29
v_mfma_f32_16x16x32_bf16 v[20:23],v[148:151],v[44:47],v[20:23]
v_cvt_pk_bf16_f32 v13,v26,v27
v_cvt_pk_bf16_f32 v12,v24,v25
v_mfma_f32_16x16x32_bf16 v[16:19],v[144:147],v[44:47],v[16:19]
ds_read_b128 v[36:39],v2 offset:16384
ds_write_b128 v5,v[12:15]
v_mfma_f32_16x16x32_bf16 v[16:19],a[224:227],v[40:43],v[16:19]
ds_read2st64_b32 v[12:13],v3 offset0:0 offset1:1
s_waitcnt lgkmcnt(6)
v_mfma_f32_16x16x32_bf16 v[16:19],a[8:11],v[48:51],v[16:19]
ds_read_b128 v[40:43],v2 offset:30720
ds_read2st64_b32 v[14:15],v3 offset0:2 offset1:3
v_mfma_f32_16x16x32_bf16 v[16:19],a[120:123],v[52:55],v[16:19]
v_mfma_f32_16x16x32_bf16 v[20:23],a[124:127],v[52:55],v[20:23]
ds_read_b128 v[44:47],v2 offset:45056
v_mfma_f32_16x16x32_bf16 v[20:23],a[12:15],v[48:51],v[20:23]
s_waitcnt lgkmcnt(7)
v_mfma_f32_16x16x32_bf16 v[20:23],a[236:239],v[56:59],v[20:23]
ds_read_b128 v[48:51],v2 offset:3072
v_mfma_f32_16x16x32_bf16 v[20:23],v[156:159],v[60:63],v[20:23]
s_waitcnt lgkmcnt(2)
buffer_atomic_pk_add_bf16 v12,v1,s[44:47],s65 offen offset:0 sc1
v_mfma_f32_16x16x32_bf16 v[16:19],v[152:155],v[60:63],v[16:19]
ds_read_b128 v[52:55],v2 offset:17408
v_mfma_f32_16x16x32_bf16 v[16:19],a[232:235],v[56:59],v[16:19]
buffer_atomic_pk_add_bf16 v13,v1,s[44:47],s65 offen offset:2048 sc1
v_mfma_f32_16x16x32_bf16 v[16:19],a[16:19],v[32:35],v[16:19]
s_waitcnt vmcnt(2)
s_barrier
v_mfma_f32_16x16x32_bf16 v[16:19],a[128:131],v[36:39],v[16:19]
ds_read_b128 v[56:59],v2 offset:31744
s_add_u32 s65,s65,16384
v_mfma_f32_16x16x32_bf16 v[20:23],a[132:135],v[36:39],v[20:23]
ds_read_b128 v[60:63],v2 offset:46080
v_mfma_f32_16x16x32_bf16 v[20:23],a[20:23],v[32:35],v[20:23]
buffer_atomic_pk_add_bf16 v14,v1,s[44:47],s65 offen offset:0 sc1
s_waitcnt lgkmcnt(4)
v_mfma_f32_16x16x32_bf16 v[20:23],a[244:247],v[40:43],v[20:23]
ds_read_b128 v[32:35],v2 offset:4096
s_add_u32 m0,s64,57344
v_mfma_f32_16x16x32_bf16 v[20:23],v[164:167],v[44:47],v[20:23]
buffer_atomic_pk_add_bf16 v15,v1,s[44:47],s65 offen offset:2048 sc1
s_add_u32 s65,s65,16384
v_mfma_f32_16x16x32_bf16 v[16:19],v[160:163],v[44:47],v[16:19]
ds_read_b128 v[36:39],v2 offset:18432
v_mfma_f32_16x16x32_bf16 v[16:19],a[240:243],v[40:43],v[16:19]
s_waitcnt lgkmcnt(4)
v_mfma_f32_16x16x32_bf16 v[16:19],a[24:27],v[48:51],v[16:19]
ds_read_b128 v[40:43],v2 offset:32768
v_mfma_f32_16x16x32_bf16 v[16:19],a[136:139],v[52:55],v[16:19]
buffer_load_dwordx4 v2,s[32:35],s63 offen offset:0 sc1 lds
v_mfma_f32_16x16x32_bf16 v[20:23],a[140:143],v[52:55],v[20:23]
ds_read_b128 v[44:47],v2 offset:47104
v_mfma_f32_16x16x32_bf16 v[20:23],a[28:31],v[48:51],v[20:23]
s_waitcnt lgkmcnt(4)
v_mfma_f32_16x16x32_bf16 v[20:23],a[252:255],v[56:59],v[20:23]
ds_read_b128 v[48:51],v2 offset:5120
v_mfma_f32_16x16x32_bf16 v[20:23],v[172:175],v[60:63],v[20:23]
buffer_load_dwordx4 v2,s[32:35],s63 offen offset:1024 sc1 lds
v_mfma_f32_16x16x32_bf16 v[16:19],v[168:171],v[60:63],v[16:19]
ds_read_b128 v[52:55],v2 offset:19456
v_mfma_f32_16x16x32_bf16 v[16:19],a[248:251],v[56:59],v[16:19]
s_waitcnt lgkmcnt(4)
v_mfma_f32_16x16x32_bf16 v[16:19],a[32:35],v[32:35],v[16:19]
ds_read_b128 v[56:59],v2 offset:33792
v_mfma_f32_16x16x32_bf16 v[16:19],a[144:147],v[36:39],v[16:19]
buffer_load_dwordx4 v2,s[32:35],s63 offen offset:2048 sc1 lds
)#"
R"#(
v_mfma_f32_16x16x32_bf16 v[20:23],a[148:151],v[36:39],v[20:23]
ds_read_b128 v[60:63],v2 offset:48128
v_mfma_f32_16x16x32_bf16 v[20:23],a[36:39],v[32:35],v[20:23]
s_waitcnt lgkmcnt(4)
v_mfma_f32_16x16x32_bf16 v[20:23],v[68:71],v[40:43],v[20:23]
ds_read_b128 v[32:35],v2 offset:6144
v_mfma_f32_16x16x32_bf16 v[20:23],v[180:183],v[44:47],v[20:23]
buffer_load_dwordx4 v2,s[32:35],s63 offen offset:3072 sc1 lds
v_mfma_f32_16x16x32_bf16 v[16:19],v[176:179],v[44:47],v[16:19]
ds_read_b128 v[36:39],v2 offset:20480
v_mfma_f32_16x16x32_bf16 v[16:19],v[64:67],v[40:43],v[16:19]
s_waitcnt lgkmcnt(4)
v_mfma_f32_16x16x32_bf16 v[16:19],a[40:43],v[48:51],v[16:19]
ds_read_b128 v[40:43],v2 offset:34816
s_add_u32 s63,s63,4096
v_mfma_f32_16x16x32_bf16 v[16:19],a[152:155],v[52:55],v[16:19]
s_add_u32 m0,s64,61440
v_mfma_f32_16x16x32_bf16 v[20:23],a[156:159],v[52:55],v[20:23]
ds_read_b128 v[44:47],v2 offset:49152
v_mfma_f32_16x16x32_bf16 v[20:23],a[44:47],v[48:51],v[20:23]
s_waitcnt lgkmcnt(4)
v_mfma_f32_16x16x32_bf16 v[20:23],v[76:79],v[56:59],v[20:23]
ds_read_b128 v[48:51],v2 offset:7168
v_mfma_f32_16x16x32_bf16 v[20:23],v[188:191],v[60:63],v[20:23]
buffer_load_dwordx4 v2,s[32:35],s63 offen offset:0 sc1 lds
v_mfma_f32_16x16x32_bf16 v[16:19],v[184:187],v[60:63],v[16:19]
ds_read_b128 v[52:55],v2 offset:21504
v_mfma_f32_16x16x32_bf16 v[16:19],v[72:75],v[56:59],v[16:19]
s_waitcnt lgkmcnt(4)
v_mfma_f32_16x16x32_bf16 v[16:19],a[48:51],v[32:35],v[16:19]
ds_read_b128 v[56:59],v2 offset:35840
v_mfma_f32_16x16x32_bf16 v[16:19],a[160:163],v[36:39],v[16:19]
buffer_load_dwordx4 v2,s[32:35],s63 offen offset:1024 sc1 lds
v_mfma_f32_16x16x32_bf16 v[20:23],a[164:167],v[36:39],v[20:23]
ds_read_b128 v[60:63],v2 offset:50176
v_mfma_f32_16x16x32_bf16 v[20:23],a[52:55],v[32:35],v[20:23]
s_waitcnt lgkmcnt(4)
v_mfma_f32_16x16x32_bf16 v[20:23],v[84:87],v[40:43],v[20:23]
ds_read_b128 v[32:35],v2 offset:8192
v_mfma_f32_16x16x32_bf16 v[20:23],v[196:199],v[44:47],v[20:23]
buffer_load_dwordx4 v2,s[32:35],s63 offen offset:2048 sc1 lds
v_mfma_f32_16x16x32_bf16 v[16:19],v[192:195],v[44:47],v[16:19]
ds_read_b128 v[36:39],v2 offset:22528
v_mfma_f32_16x16x32_bf16 v[16:19],v[80:83],v[40:43],v[16:19]
s_waitcnt lgkmcnt(4)
v_mfma_f32_16x16x32_bf16 v[16:19],a[56:59],v[48:51],v[16:19]
ds_read_b128 v[40:43],v2 offset:36864
v_mfma_f32_16x16x32_bf16 v[16:19],a[168:171],v[52:55],v[16:19]
buffer_load_dwordx4 v2,s[32:35],s63 offen offset:3072 sc1 lds
v_mfma_f32_16x16x32_bf16 v[20:23],a[172:175],v[52:55],v[20:23]
ds_read_b128 v[44:47],v2 offset:51200
v_mfma_f32_16x16x32_bf16 v[20:23],a[60:63],v[48:51],v[20:23]
s_waitcnt lgkmcnt(4)
v_mfma_f32_16x16x32_bf16 v[20:23],v[92:95],v[56:59],v[20:23]
ds_read_b128 v[48:51],v2 offset:9216
v_mfma_f32_16x16x32_bf16 v[20:23],v[204:207],v[60:63],v[20:23]
s_add_u32 s63,s63,4096
s_add_u32 m0,s64,65536
v_mfma_f32_16x16x32_bf16 v[16:19],v[200:203],v[60:63],v[16:19]
ds_read_b128 v[52:55],v2 offset:23552
v_mfma_f32_16x16x32_bf16 v[16:19],v[88:91],v[56:59],v[16:19]
s_waitcnt lgkmcnt(4)
v_mfma_f32_16x16x32_bf16 v[16:19],a[64:67],v[32:35],v[16:19]
ds_read_b128 v[56:59],v2 offset:37888
v_mfma_f32_16x16x32_bf16 v[16:19],a[176:179],v[36:39],v[16:19]
buffer_load_dwordx4 v2,s[32:35],s63 offen offset:0 sc1 lds
v_mfma_f32_16x16x32_bf16 v[20:23],a[180:183],v[36:39],v[20:23]
ds_read_b128 v[60:63],v2 offset:52224
v_mfma_f32_16x16x32_bf16 v[20:23],a[68:71],v[32:35],v[20:23]
s_waitcnt lgkmcnt(4)
v_mfma_f32_16x16x32_bf16 v[20:23],v[100:103],v[40:43],v[20:23]
ds_read_b128 v[32:35],v2 offset:10240
v_mfma_f32_16x16x32_bf16 v[20:23],v[212:215],v[44:47],v[20:23]
buffer_load_dwordx4 v2,s[32:35],s63 offen offset:1024 sc1 lds
v_mfma_f32_16x16x32_bf16 v[16:19],v[208:211],v[44:47],v[16:19]
ds_read_b128 v[36:39],v2 offset:24576
v_mfma_f32_16x16x32_bf16 v[16:19],v[96:99],v[40:43],v[16:19]
s_waitcnt lgkmcnt(4)
v_mfma_f32_16x16x32_bf16 v[16:19],a[72:75],v[48:51],v[16:19]
ds_read_b128 v[40:43],v2 offset:38912
v_mfma_f32_16x16x32_bf16 v[16:19],a[184:187],v[52:55],v[16:19]
buffer_load_dwordx4 v2,s[32:35],s63 offen offset:2048 sc1 lds
v_mfma_f32_16x16x32_bf16 v[20:23],a[188:191],v[52:55],v[20:23]
ds_read_b128 v[44:47],v2 offset:53248
v_mfma_f32_16x16x32_bf16 v[20:23],a[76:79],v[48:51],v[20:23]
s_waitcnt lgkmcnt(4)
v_mfma_f32_16x16x32_bf16 v[20:23],v[108:111],v[56:59],v[20:23]
ds_read_b128 v[48:51],v2 offset:11264
v_mfma_f32_16x16x32_bf16 v[20:23],v[220:223],v[60:63],v[20:23]
v_mfma_f32_16x16x32_bf16 v[16:19],v[216:219],v[60:63],v[16:19]
ds_read_b128 v[52:55],v2 offset:25600
v_mfma_f32_16x16x32_bf16 v[16:19],v[104:107],v[56:59],v[16:19]
s_waitcnt lgkmcnt(4)
v_mfma_f32_16x16x32_bf16 v[16:19],a[80:83],v[32:35],v[16:19]
ds_read_b128 v[56:59],v2 offset:39936
v_mfma_f32_16x16x32_bf16 v[16:19],a[192:195],v[36:39],v[16:19]
buffer_load_dwordx4 v2,s[32:35],s63 offen offset:3072 sc1 lds
s_add_u32 s63,s63,4096
v_mfma_f32_16x16x32_bf16 v[20:23],a[196:199],v[36:39],v[20:23]
ds_read_b128 v[60:63],v2 offset:54272
s_add_u32 m0,s64,69632
v_mfma_f32_16x16x32_bf16 v[20:23],a[84:87],v[32:35],v[20:23]
s_waitcnt lgkmcnt(4)
v_mfma_f32_16x16x32_bf16 v[20:23],v[116:119],v[40:43],v[20:23]
ds_read_b128 v[32:35],v2 offset:12288
v_mfma_f32_16x16x32_bf16 v[20:23],v[228:231],v[44:47],v[20:23]
buffer_load_dwordx4 v2,s[32:35],s63 offen offset:0 sc1 lds
v_mfma_f32_16x16x32_bf16 v[16:19],v[224:227],v[44:47],v[16:19]
ds_read_b128 v[36:39],v2 offset:26624
v_mfma_f32_16x16x32_bf16 v[16:19],v[112:115],v[40:43],v[16:19]
s_waitcnt lgkmcnt(4)
v_mfma_f32_16x16x32_bf16 v[16:19],a[88:91],v[48:51],v[16:19]
ds_read_b128 v[40:43],v2 offset:40960
v_mfma_f32_16x16x32_bf16 v[16:19],a[200:203],v[52:55],v[16:19]
buffer_load_dwordx4 v2,s[32:35],s63 offen offset:1024 sc1 lds
s_add_u32 s63,s63,1175552
v_mfma_f32_16x16x32_bf16 v[20:23],a[204:207],v[52:55],v[20:23]
ds_read_b128 v[44:47],v2 offset:55296
v_mfma_f32_16x16x32_bf16 v[20:23],a[92:95],v[48:51],v[20:23]
s_waitcnt lgkmcnt(4)
v_mfma_f32_16x16x32_bf16 v[20:23],v[124:127],v[56:59],v[20:23]
ds_read_b128 v[48:51],v2 offset:13312
v_mfma_f32_16x16x32_bf16 v[20:23],v[236:239],v[60:63],v[20:23]
v_mfma_f32_16x16x32_bf16 v[16:19],v[232:235],v[60:63],v[16:19]
ds_read_b128 v[52:55],v2 offset:27648
v_mfma_f32_16x16x32_bf16 v[16:19],v[120:123],v[56:59],v[16:19]
s_waitcnt lgkmcnt(4)
v_mfma_f32_16x16x32_bf16 v[16:19],a[96:99],v[32:35],v[16:19]
s_waitcnt vmcnt(10)
s_barrier
v_mfma_f32_16x16x32_bf16 v[16:19],a[208:211],v[36:39],v[16:19]
ds_read_b128 v[56:59],v2 offset:41984
v_mfma_f32_16x16x32_bf16 v[20:23],a[212:215],v[36:39],v[20:23]
ds_read_b128 v[60:63],v2 offset:56320
v_mfma_f32_16x16x32_bf16 v[20:23],a[100:103],v[32:35],v[20:23]
s_waitcnt lgkmcnt(4)
v_mfma_f32_16x16x32_bf16 v[20:23],v[132:135],v[40:43],v[20:23]
ds_read_b128 v[32:35],v4 offset:0
v_mfma_f32_16x16x32_bf16 v[20:23],v[244:247],v[44:47],v[20:23]
v_mfma_f32_16x16x32_bf16 v[16:19],v[240:243],v[44:47],v[16:19]
ds_read_b128 v[36:39],v4 offset:14336
v_mfma_f32_16x16x32_bf16 v[16:19],v[128:131],v[40:43],v[16:19]
s_waitcnt lgkmcnt(4)
v_mfma_f32_16x16x32_bf16 v[16:19],a[104:107],v[48:51],v[16:19]
ds_read_b128 v[40:43],v4 offset:28672
v_mfma_f32_16x16x32_bf16 v[16:19],a[216:219],v[52:55],v[16:19]
v_mfma_f32_16x16x32_bf16 v[20:23],a[220:223],v[52:55],v[20:23]
ds_read_b128 v[44:47],v4 offset:43008
v_mfma_f32_16x16x32_bf16 v[20:23],a[108:111],v[48:51],v[20:23]
s_waitcnt lgkmcnt(4)
v_mfma_f32_16x16x32_bf16 v[20:23],v[140:143],v[56:59],v[20:23]
ds_read_b128 v[48:51],v4 offset:1024
v_mfma_f32_16x16x32_bf16 v[20:23],v[252:255],v[60:63],v[20:23]
v_mfma_f32_16x16x32_bf16 v[16:19],v[248:251],v[60:63],v[16:19]
ds_read_b128 v[52:55],v4 offset:15360
v_mfma_f32_16x16x32_bf16 v[16:19],v[136:139],v[56:59],v[16:19]
s_branch .L_dbl_3
.L_dbt_3:
s_nop 1
s_nop 0
v_cvt_pk_bf16_f32 v15,v30,v31
v_cvt_pk_bf16_f32 v14,v28,v29
s_nop 1
v_cvt_pk_bf16_f32 v13,v26,v27
v_cvt_pk_bf16_f32 v12,v24,v25
ds_write_b128 v5,v[12:15]
ds_read2st64_b32 v[12:13],v3 offset0:0 offset1:1
ds_read2st64_b32 v[14:15],v3 offset0:2 offset1:3
s_waitcnt lgkmcnt(0)
buffer_atomic_pk_add_bf16 v12,v1,s[44:47],s65 offen offset:0 sc1
buffer_atomic_pk_add_bf16 v13,v1,s[44:47],s65 offen offset:2048 sc1
s_add_u32 s65,s65,16384
buffer_atomic_pk_add_bf16 v14,v1,s[44:47],s65 offen offset:0 sc1
buffer_atomic_pk_add_bf16 v15,v1,s[44:47],s65 offen offset:2048 sc1
s_add_u32 s65,s65,16384
s_or_b32 s48,s28,s27
s_or_b32 s48,s48,s26
s_cbranch_scc1 .L_end
v_mov_b32 v8,s54
v_mov_b32 v9,s55
buffer_store_dwordx2 v[8:9],off,s[32:35],0 sc1
s_branch .L_end
.L_last:
)#"
    : "+{v0}"(tid)
    : "{s[8:9]}"(x), "{s[10:11]}"(wq), "{s[12:13]}"(wo), "{s[14:15]}"(wg), "{s[16:17]}"(wu), "{s[18:19]}"(wd),
      "{s[20:21]}"(out), "{s[22:23]}"(ws), "{s24}"(blockIdx.x)
    : "v1", "v2", "v3", "v4", "v5", "v6", "v7", "v8", "v9", "v10", "v11", "v12", "v13", "v14", "v15", "v16", "v17", "v18", "v19", "v20", "v21", "v22", "v23", "v24", "v25", "v26", "v27", "v28", "v29", "v30", "v31", "v32", "v33", "v34", "v35", "v36", "v37", "v38", "v39", "v40", "v41", "v42", "v43", "v44", "v45", "v46", "v47", "v48", "v49", "v50", "v51", "v52", "v53", "v54", "v55", "v56", "v57", "v58", "v59", "v60", "v61", "v62", "v63", "v64", "v65", "v66", "v67", "v68", "v69", "v70", "v71", "v72", "v73", "v74", "v75", "v76", "v77", "v78", "v79", "v80", "v81", "v82", "v83", "v84", "v85", "v86", "v87", "v88", "v89", "v90", "v91", "v92", "v93", "v94", "v95", "v96", "v97", "v98", "v99", "v100", "v101", "v102", "v103", "v104", "v105", "v106", "v107", "v108", "v109", "v110", "v111", "v112", "v113", "v114", "v115", "v116", "v117", "v118", "v119", "v120", "v121", "v122", "v123", "v124", "v125", "v126", "v127", "v128", "v129", "v130", "v131", "v132", "v133", "v134", "v135", "v136", "v137", "v138", "v139", "v140", "v141", "v142", "v143", "v144", "v145", "v146", "v147", "v148", "v149", "v150", "v151", "v152", "v153", "v154", "v155", "v156", "v157", "v158", "v159", "v160", "v161", "v162", "v163", "v164", "v165", "v166", "v167", "v168", "v169", "v170", "v171", "v172", "v173", "v174", "v175", "v176", "v177", "v178", "v179", "v180", "v181", "v182", "v183", "v184", "v185", "v186", "v187", "v188", "v189", "v190", "v191", "v192", "v193", "v194", "v195", "v196", "v197", "v198", "v199", "v200", "v201", "v202", "v203", "v204", "v205", "v206", "v207", "v208", "v209", "v210", "v211", "v212", "v213", "v214", "v215", "v216", "v217", "v218", "v219", "v220", "v221", "v222", "v223", "v224", "v225", "v226", "v227", "v228", "v229", "v230", "v231", "v232", "v233", "v234", "v235", "v236", "v237", "v238", "v239", "v240", "v241", "v242", "v243", "v244", "v245", "v246", "v247", "v248", "v249", "v250", "v251", "v252", "v253", "v254", "v255", "a0", "a1", "a2", "a3", "a4", "a5", "a6", "a7", "a8", "a9", "a10", "a11", "a12", "a13", "a14", "a15", "a16", "a17", "a18", "a19", "a20", "a21", "a22", "a23", "a24", "a25", "a26", "a27", "a28", "a29", "a30", "a31", "a32", "a33", "a34", "a35", "a36", "a37", "a38", "a39", "a40", "a41", "a42", "a43", "a44", "a45", "a46", "a47", "a48", "a49", "a50", "a51", "a52", "a53", "a54", "a55", "a56", "a57", "a58", "a59", "a60", "a61", "a62", "a63", "a64", "a65", "a66", "a67", "a68", "a69", "a70", "a71", "a72", "a73", "a74", "a75", "a76", "a77", "a78", "a79", "a80", "a81", "a82", "a83", "a84", "a85", "a86", "a87", "a88", "a89", "a90", "a91", "a92", "a93", "a94", "a95", "a96", "a97", "a98", "a99", "a100", "a101", "a102", "a103", "a104", "a105", "a106", "a107", "a108", "a109", "a110", "a111", "a112", "a113", "a114", "a115", "a116", "a117", "a118", "a119", "a120", "a121", "a122", "a123", "a124", "a125", "a126", "a127", "a128", "a129", "a130", "a131", "a132", "a133", "a134", "a135", "a136", "a137", "a138", "a139", "a140", "a141", "a142", "a143", "a144", "a145", "a146", "a147", "a148", "a149", "a150", "a151", "a152", "a153", "a154", "a155", "a156", "a157", "a158", "a159", "a160", "a161", "a162", "a163", "a164", "a165", "a166", "a167", "a168", "a169", "a170", "a171", "a172", "a173", "a174", "a175", "a176", "a177", "a178", "a179", "a180", "a181", "a182", "a183", "a184", "a185", "a186", "a187", "a188", "a189", "a190", "a191", "a192", "a193", "a194", "a195", "a196", "a197", "a198", "a199", "a200", "a201", "a202", "a203", "a204", "a205", "a206", "a207", "a208", "a209", "a210", "a211", "a212", "a213", "a214", "a215", "a216", "a217", "a218", "a219", "a220", "a221", "a222", "a223", "a224", "a225", "a226", "a227", "a228", "a229", "a230", "a231", "a232", "a233", "a234", "a235", "a236", "a237", "a238", "a239", "a240", "a241", "a242", "a243", "a244", "a245", "a246", "a247", "a248", "a249", "a250", "a251", "a252", "a253", "a254", "a255", "s25", "s26", "s27", "s28", "s29", "s30", "s31", "s32", "s33", "s34", "s35", "s36", "s37", "s38", "s39", "s40", "s41", "s42", "s43", "s44", "s45", "s46", "s47", "s48", "s49", "s50", "s51", "s52", "s53", "s54", "s55", "s56", "s57", "s58", "s59", "s60", "s61", "s62", "s63", "s64", "s65", "s66", "s67", "s68", "s69", "s70", "s71", "s72", "s73", "s74", "s75", "s76", "s77", "s78", "s79", "s80", "s81", "s82", "s83", "s84", "s85", "s86", "s87", "s88", "s89", "s90", "s91", "s92", "s93", "s94", "s95", "s96", "s97", "s98", "s99", "vcc", "scc", "m0", "memory");
}

extern "C" void kernel_launch(void* const* d_in, const int* in_sizes, int n_in, void* d_out, int out_size,
                              void* d_ws, size_t ws_size, hipStream_t stream) {
    constexpr int smem = 163840;
    (void)hipFuncSetAttribute((const void*)body_k, hipFuncAttributeMaxDynamicSharedMemorySize, smem);
    body_k<<<256, 256, smem, stream>>>(d_in[0], d_in[1], d_in[2], d_in[4], d_in[5], d_in[6], d_out, d_ws);
}
```

```cpp
#include <hip/hip_runtime.h>
#pragma clang diagnostic ignored "-Winline-asm"
#define W_XI 4612096
#define W_O 4648960
#define W_XA 4681728
#define W_H 4718592
#define W_P 4833280
#define W_EB 296960
#define W_HK 0
#define W_SIDE 276
#define W_HL 3
#define W_MAP 1
extern "C" __global__ __launch_bounds__(256, 1) void body_k(const void* x, const void* wq, const void* wo, const void* wg, const void* wu,
                                                            const void* wd, void* out, void* ws) {
    unsigned tid = threadIdx.x;
    asm volatile(
R"#(
s_load_dwordx2 s[52:53],s[22:23],0x0
v_lshrrev_b32 v3,6,v0
v_and_b32 v1,63,v0
v_readfirstlane_b32 s26,v3
v_lshlrev_b32 v2,4,v1
s_add_u32 s32,s22,0x0
s_addc_u32 s33,s23,0x0
s_and_b32 s33,s33,0xffff
s_mov_b32 s34,0x4ce6000
s_mov_b32 s35,0x20000
s_mov_b64 s[0:1],s[32:33]
s_mov_b32 s2,84197376
s_mov_b32 s3,0x20000
s_mov_b32 s73,0
s_mov_b32 s25,0
s_lshr_b32 s49,s24,3
s_cmp_lt_u32 s49,16
s_cbranch_scc1 .L_nc
s_bfe_u32 s28,s24,0x20001
s_and_b32 s27,s49,7
s_bfe_u32 s29,s49,0x10003
s_mov_b32 s62,0
s_mov_b32 s74,1
s_bitcmp1_b32 s24,0
s_cbranch_scc0 .L_nt
s_getpc_b64 s[40:41]
.L_ct16:
s_and_b32 s41,s41,0xffff
s_mov_b32 s42,.L_last-.L_ct16
s_mov_b32 s43,0x20000
s_sub_u32 s48,s49,16
s_lshl_b32 s48,s48,8
v_add_lshl_u32 v5,v0,s48,7
buffer_load_dword v10,v5,s[40:43],0 offen
s_add_u32 s36,s10,0x0
s_addc_u32 s37,s11,0x0
s_and_b32 s37,s37,0xffff
s_mov_b32 s38,0x600000
s_mov_b32 s39,0x20000
s_lshl_b32 s48,s49,8
s_sub_u32 s48,s48,4096
v_add_u32 v4,s48,v0
s_lshl_b32 s48,s28,9
s_add_u32 s44,s8,0x0
s_addc_u32 s45,s9,0x0
s_and_b32 s45,s45,0xffff
s_mov_b32 s46,0x400000
s_mov_b32 s47,0x20000
v_lshrrev_b32 v5,10,v4
v_bfe_u32 v6,v4,1,9
v_lshl_add_u32 v5,v5,13,v6
v_subrev_u32 v5,512,v5
v_and_b32 v5,0x7fff,v5
v_lshlrev_b32 v5,7,v5
buffer_load_dword v10,v5,s[44:47],0 offen
v_mul_u32_u24 v5,43691,v4
v_lshrrev_b32 v5,19,v5
v_mul_u32_u24 v6,12,v5
v_sub_u32 v6,v4,v6
v_mul_u32_u24 v5,48,v5
v_lshrrev_b32 v7,2,v6
v_mul_u32_u24 v7,12,v7
v_add3_u32 v5,v5,v6,v7
v_lshlrev_b32 v5,7,v5
v_add_u32 v4,4096,v4
buffer_load_dword v10,v5,s[36:39],s48 offen
v_mul_u32_u24 v5,43691,v4
v_lshrrev_b32 v5,19,v5
v_mul_u32_u24 v6,12,v5
v_sub_u32 v6,v4,v6
v_mul_u32_u24 v5,48,v5
v_lshrrev_b32 v7,2,v6
v_mul_u32_u24 v7,12,v7
v_add3_u32 v5,v5,v6,v7
v_lshlrev_b32 v5,7,v5
v_add_u32 v4,4096,v4
buffer_load_dword v10,v5,s[36:39],s48 offen
v_mul_u32_u24 v5,43691,v4
v_lshrrev_b32 v5,19,v5
v_mul_u32_u24 v6,12,v5
v_sub_u32 v6,v4,v6
v_mul_u32_u24 v5,48,v5
v_lshrrev_b32 v7,2,v6
v_mul_u32_u24 v7,12,v7
v_add3_u32 v5,v5,v6,v7
v_lshlrev_b32 v5,7,v5
v_add_u32 v4,4096,v4
buffer_load_dword v10,v5,s[36:39],s48 offen
s_mov_b32 s62,1
s_mov_b32 s74,4
.L_nt:
s_lshl2_add_u32 s50,s27,s26
s_mul_i32 s70,s50,1152
v_and_b32 v20,15,v1
v_lshlrev_b32 v20,11,v20
v_and_b32 v21,48,v1
s_lshl_b32 s48,s50,6
v_add3_u32 v20,v20,v21,s48
s_add_u32 s36,s8,0x0
s_addc_u32 s37,s9,0x0
s_and_b32 s37,s37,0xffff
s_mov_b32 s38,0x400000
s_mov_b32 s39,0x20000
s_lshl2_add_u32 s48,s28,s62
s_lshl_b32 s48,s48,3
s_sub_u32 s48,s48,s29
s_add_u32 s48,s48,127
s_lshl_b32 s66,s27,7
s_add_u32 s66,s66,286720
s_sub_u32 s66,s66,s70
s_add_u32 s49,s28,3
s_and_b32 s49,s49,3
s_mul_i32 s49,s49,296960
s_add_u32 s69,s70,s49
s_add_u32 s69,s69,41435136
s_mul_i32 s49,s28,296960
s_add_u32 s70,s70,s49
s_and_b32 s51,s50,6
s_lshl_b32 s51,s51,1
v_add_u32 v3,s51,v1
v_bfi_b32 v3,15,v3,v1
v_lshlrev_b32 v3,2,v3
v_cmp_gt_u32 vcc,32,v1
v_mov_b32 v21,0x7fff0000
s_nop 0
v_cndmask_b32 v21,v21,v3,vcc
s_mul_i32 s49,s29,1187840
s_sub_u32 s71,s70,s49
s_add_u32 s71,s71,4612096
v_mul_u32_u24 v24,2375680,v1
v_add_u32 v24,s71,v24
s_cmp_eq_u32 s29,0
s_cselect_b32 s68,s71,s69
v_writelane_b32 v24,s68,0
s_mul_i32 s49,s62,9502720
s_add_u32 s71,s71,s49
s_cmp_eq_u32 s62,0
s_cselect_b32 s68,s68,s71
s_add_u32 s71,s71,2375680
v_lshrrev_b32 v22,2,v0
v_mov_b32 v23,0x3f803f80
.L_dc:
s_and_b32 s49,s48,127
s_lshl_b32 s49,s49,15
s_add_u32 s48,s48,2
buffer_load_dwordx4 v[32:35],v20,s[36:39],s49 offen
s_and_b32 s49,s48,127
s_lshl_b32 s49,s49,15
s_add_u32 s48,s48,2
buffer_load_dwordx4 v[36:39],v20,s[36:39],s49 offen
s_and_b32 s49,s48,127
s_lshl_b32 s49,s49,15
s_add_u32 s48,s48,2
buffer_load_dwordx4 v[40:43],v20,s[36:39],s49 offen
s_and_b32 s49,s48,127
s_lshl_b32 s49,s49,15
s_add_u32 s48,s48,2
buffer_load_dwordx4 v[44:47],v20,s[36:39],s49 offen
v_cmp_eq_u32 vcc,s62,v22
v_mov_b32 v25,0x7fff0000
s_add_u32 s62,s62,1
v_cndmask_b32 v25,v25,v24,vcc
s_waitcnt lgkmcnt(0)
s_add_u32 s54,s52,0x7f4a7c15
s_addc_u32 s55,s53,0x9e3779b9
v_mov_b32 v8,s54
v_mov_b32 v9,s55
s_waitcnt vmcnt(0)
v_dot2_f32_bf16 v48,v32,v23,0
v_dot2_f32_bf16 v49,v32,v32,0
v_dot2_f32_bf16 v50,v36,v23,0
v_dot2_f32_bf16 v51,v36,v36,0
v_dot2_f32_bf16 v52,v40,v23,0
v_dot2_f32_bf16 v53,v40,v40,0
v_dot2_f32_bf16 v54,v44,v23,0
v_dot2_f32_bf16 v55,v44,v44,0
v_dot2_f32_bf16 v48,v33,v23,v48
v_dot2_f32_bf16 v49,v33,v33,v49
v_dot2_f32_bf16 v50,v37,v23,v50
v_dot2_f32_bf16 v51,v37,v37,v51
v_dot2_f32_bf16 v52,v41,v23,v52
v_dot2_f32_bf16 v53,v41,v41,v53
v_dot2_f32_bf16 v54,v45,v23,v54
v_dot2_f32_bf16 v55,v45,v45,v55
v_dot2_f32_bf16 v48,v34,v23,v48
v_dot2_f32_bf16 v49,v34,v34,v49
v_dot2_f32_bf16 v50,v38,v23,v50
v_dot2_f32_bf16 v51,v38,v38,v51
v_dot2_f32_bf16 v52,v42,v23,v52
v_dot2_f32_bf16 v53,v42,v42,v53
v_dot2_f32_bf16 v54,v46,v23,v54
v_dot2_f32_bf16 v55,v46,v46,v55
v_dot2_f32_bf16 v48,v35,v23,v48
v_dot2_f32_bf16 v49,v35,v35,v49
v_dot2_f32_bf16 v50,v39,v23,v50
v_dot2_f32_bf16 v51,v39,v39,v51
v_dot2_f32_bf16 v52,v43,v23,v52
v_dot2_f32_bf16 v53,v43,v43,v53
v_dot2_f32_bf16 v54,v47,v23,v54
v_dot2_f32_bf16 v55,v47,v47,v55
v_permlane16_swap_b32 v48,v49
v_permlane16_swap_b32 v50,v51
v_permlane16_swap_b32 v52,v53
v_permlane16_swap_b32 v54,v55
v_add_f32 v48,v48,v49
v_add_f32 v50,v50,v51
v_add_f32 v52,v52,v53
v_add_f32 v54,v54,v55
v_permlane32_swap_b32 v48,v49
v_permlane32_swap_b32 v50,v51
v_permlane32_swap_b32 v52,v53
v_permlane32_swap_b32 v54,v55
v_add_f32 v48,v48,v49
v_add_f32 v50,v50,v51
v_add_f32 v52,v52,v53
v_add_f32 v54,v54,v55
buffer_store_dwordx4 v[32:35],v2,s[32:35],s68 offen sc1
buffer_store_dword v48,v21,s[32:35],s68 offen offset:1024 sc1
s_mov_b32 s68,s71
s_add_u32 s71,s71,2375680
buffer_store_dwordx4 v[36:39],v2,s[32:35],s68 offen sc1
buffer_store_dword v50,v21,s[32:35],s68 offen offset:1024 sc1
s_mov_b32 s68,s71
s_add_u32 s71,s71,2375680
buffer_store_dwordx4 v[40:43],v2,s[32:35],s68 offen sc1
buffer_store_dword v52,v21,s[32:35],s68 offen offset:1024 sc1
s_mov_b32 s68,s71
s_add_u32 s71,s71,2375680
buffer_store_dwordx4 v[44:47],v2,s[32:35],s68 offen sc1
buffer_store_dword v54,v21,s[32:35],s68 offen offset:1024 sc1
s_mov_b32 s68,s71
s_add_u32 s71,s71,2375680
s_waitcnt vmcnt(0)
s_barrier
buffer_store_dwordx2 v[8:9],v25,s[32:35],s66 offen sc1
s_cmp_lt_u32 s62,s74
s_cbranch_scc1 .L_dc
s_bitcmp1_b32 s24,0
s_cbranch_scc1 .L_d
s_lshr_b32 s49,s24,3
.L_nc:
s_bfe_u32 s50,s24,0x20001
s_and_b32 s27,s49,3
s_lshl2_add_u32 s27,s50,s27
s_lshr_b32 s28,s49,2
s_bitcmp1_b32 s24,0
s_cbranch_scc1 .L_q
s_and_b32 s27,s49,1
s_lshl1_add_u32 s27,s50,s27
s_lshr_b32 s28,s49,1
s_cmp_lt_u32 s49,4
s_cbranch_scc1 .L_w
s_cmp_ge_u32 s49,16
s_cbranch_scc1 .L_ns
s_getpc_b64 s[40:41]
.L_ct5:
s_and_b32 s41,s41,0xffff
s_mov_b32 s42,.L_last-.L_ct5
s_mov_b32 s43,0x20000
s_sub_u32 s48,s49,5
s_lshl_b32 s48,s48,8
v_add_lshl_u32 v5,v0,s48,7
buffer_load_dword v120,v5,s[40:43],0 offen
s_lshl2_add_u32 s48,s49,s50
s_lshl_b32 s48,s48,8
s_sub_u32 s48,s48,4096
v_add_u32 v4,s48,v0
v_mul_u32_u24 v5,52429,v4
v_lshrrev_b32 v5,22,v5
v_mul_u32_u24 v5,2240,v5
v_add_u32 v5,v5,v4
v_lshlrev_b32 v5,7,v5
v_add_u32 v5,4898816,v5
v_add_u32 v4,12288,v4
buffer_load_dword v120,v5,s[0:3],0 offen sc1
v_mul_u32_u24 v5,52429,v4
v_lshrrev_b32 v5,22,v5
v_mul_u32_u24 v5,2240,v5
v_add_u32 v5,v5,v4
v_lshlrev_b32 v5,7,v5
v_add_u32 v5,4898816,v5
v_add_u32 v4,12288,v4
buffer_load_dword v120,v5,s[0:3],0 offen sc1
s_sleep 30
.L_ns:
s_lshr_b32 s27,s50,1
s_and_b32 s28,s50,1
s_cmp_eq_u32 s49,4
s_cbranch_scc1 .L_h
s_sub_u32 s27,s49,14
s_lshr_b32 s27,s27,1
s_and_b32 s28,s49,1
s_add_u32 s28,s28,1
s_sub_u32 s48,s49,5
s_cmp_lt_u32 s49,14
s_cselect_b32 s27,s48,s27
s_cselect_b32 s28,0,s28
s_mul_i32 s50,s50,9
s_add_u32 s27,s27,s50
.L_g:
v_mov_b32 v16,0x3f803f80
v_mov_b32 v17,0x3f803f80
v_mov_b32 v18,0x3f803f80
v_mov_b32 v19,0x3f803f80
s_add_u32 s36,s14,0x0
s_addc_u32 s37,s15,0x0
s_and_b32 s37,s37,0xffff
s_mov_b32 s38,0x700000
s_mov_b32 s39,0x20000
s_add_u32 s40,s16,0x0
s_addc_u32 s41,s17,0x0
s_and_b32 s41,s41,0xffff
s_mov_b32 s42,0x700000
s_mov_b32 s43,0x20000
v_add_u32 v8,0,v1
v_mul_u32_u24 v9,5462,v8
v_lshrrev_b32 v9,16,v9
v_mul_u32_u24 v10,12,v9
v_sub_u32 v10,v8,v10
v_mul_u32_u24 v9,7168,v9
v_lshl_add_u32 v9,v10,4,v9
v_subrev_u32 v4,0,v9
v_add_u32 v8,64,v1
v_mul_u32_u24 v9,5462,v8
v_lshrrev_b32 v9,16,v9
v_mul_u32_u24 v10,12,v9
v_sub_u32 v10,v8,v10
v_mul_u32_u24 v9,7168,v9
v_lshl_add_u32 v9,v10,4,v9
v_subrev_u32 v5,1024,v9
v_add_u32 v8,128,v1
v_mul_u32_u24 v9,5462,v8
v_lshrrev_b32 v9,16,v9
v_mul_u32_u24 v10,12,v9
v_sub_u32 v10,v8,v10
v_mul_u32_u24 v9,7168,v9
v_lshl_add_u32 v9,v10,4,v9
v_subrev_u32 v6,2048,v9
s_mul_i32 s48,s26,114688
s_mul_i32 s49,s27,192
s_min_u32 s49,s49,6976
s_add_u32 s63,s48,s49
s_mul_i32 s64,s26,3072
s_mul_i32 s48,s28,11
s_lshr_b32 s48,s48,1
s_mul_i32 s49,s48,458752
s_add_u32 s63,s63,s49
s_and_b32 s49,s48,3
s_mul_i32 s49,s49,24576
s_add_u32 s49,s49,s64
s_add_u32 s48,s48,1
s_add_u32 m0,s49,0
s_nop 0
buffer_load_dwordx4 v4,s[36:39],s63 offen offset:0 lds
buffer_load_dwordx4 v5,s[36:39],s63 offen offset:1024 lds
buffer_load_dwordx4 v6,s[36:39],s63 offen offset:2048 lds
s_add_u32 m0,s49,12288
s_nop 0
buffer_load_dwordx4 v4,s[40:43],s63 offen offset:0 lds
buffer_load_dwordx4 v5,s[40:43],s63 offen offset:1024 lds
buffer_load_dwordx4 v6,s[40:43],s63 offen offset:2048 lds
s_add_u32 s63,s63,458752
s_and_b32 s49,s48,3
s_mul_i32 s49,s49,24576
s_add_u32 s49,s49,s64
s_add_u32 s48,s48,1
s_add_u32 m0,s49,0
s_nop 0
buffer_load_dwordx4 v4,s[36:39],s63 offen offset:0 lds
buffer_load_dwordx4 v5,s[36:39],s63 offen offset:1024 lds
buffer_load_dwordx4 v6,s[36:39],s63 offen offset:2048 lds
s_add_u32 m0,s49,12288
s_nop 0
buffer_load_dwordx4 v4,s[40:43],s63 offen offset:0 lds
buffer_load_dwordx4 v5,s[40:43],s63 offen offset:1024 lds
buffer_load_dwordx4 v6,s[40:43],s63 offen offset:2048 lds
s_add_u32 s63,s63,458752
s_and_b32 s49,s48,3
s_mul_i32 s49,s49,24576
s_add_u32 s49,s49,s64
s_add_u32 s48,s48,1
s_add_u32 m0,s49,0
s_nop 0
buffer_load_dwordx4 v4,s[36:39],s63 offen offset:0 lds
buffer_load_dwordx4 v5,s[36:39],s63 offen offset:1024 lds
buffer_load_dwordx4 v6,s[36:39],s63 offen offset:2048 lds
s_add_u32 m0,s49,12288
s_nop 0
buffer_load_dwordx4 v4,s[40:43],s63 offen offset:0 lds
buffer_load_dwordx4 v5,s[40:43],s63 offen offset:1024 lds
buffer_load_dwordx4 v6,s[40:43],s63 offen offset:2048 lds
s_add_u32 s63,s63,458752
v_mov_b32 v20,0
v_mov_b32 v21,0
v_mov_b32 v22,0
v_mov_b32 v23,0
v_mov_b32 v24,0
v_mov_b32 v25,0
v_mov_b32 v26,0
v_mov_b32 v27,0
v_mov_b32 v28,0
v_mov_b32 v29,0
v_mov_b32 v30,0
v_mov_b32 v31,0
v_and_b32 v12,15,v1
v_lshrrev_b32 v13,4,v1
v_lshrrev_b32 v14,2,v12
v_and_b32 v15,3,v12
v_lshl_add_u32 v13,v13,3,v14
v_mul_u32_u24 v13,192,v13
s_mul_i32 s48,s26,48
v_add_u32 v13,s48,v13
v_and_b32 v14,1,v15
v_lshrrev_b32 v15,1,v15
v_lshl_add_u32 v13,v14,3,v13
v_lshl_add_u32 v8,v15,5,v13
v_mul_u32_u24 v15,12288,v15
v_add3_u32 v9,v13,v15,16
v_add_u32 v10,49152,v8
v_add_u32 v11,49152,v9
v_add_u32 v12,98304,v8
v_add_u32 v13,98304,v9
s_cmp_eq_u32 s28,1
s_cbranch_scc1 .L_gw5
s_cmp_eq_u32 s28,2
s_cbranch_scc1 .L_gw11
.L_gw0:
s_waitcnt vmcnt(12)
s_barrier
ds_read_b64_tr_b16 a[0:1],v8 offset:0
ds_read_b64_tr_b16 a[2:3],v8 offset:768
ds_read_b64_tr_b16 a[4:5],v8 offset:12288
ds_read_b64_tr_b16 a[6:7],v8 offset:13056
ds_read_b64_tr_b16 a[8:9],v9 offset:0
ds_read_b64_tr_b16 a[10:11],v9 offset:768
ds_read_b64_tr_b16 a[12:13],v8 offset:6144
ds_read_b64_tr_b16 a[14:15],v8 offset:6912
ds_read_b64_tr_b16 a[16:17],v8 offset:18432
ds_read_b64_tr_b16 a[18:19],v8 offset:19200
ds_read_b64_tr_b16 a[20:21],v9 offset:6144
ds_read_b64_tr_b16 a[22:23],v9 offset:6912
s_add_u32 m0,s64,73728
s_nop 0
buffer_load_dwordx4 v4,s[36:39],s63 offen offset:0 lds
buffer_load_dwordx4 v5,s[36:39],s63 offen offset:1024 lds
buffer_load_dwordx4 v6,s[36:39],s63 offen offset:2048 lds
s_add_u32 m0,s64,86016
s_nop 0
buffer_load_dwordx4 v4,s[40:43],s63 offen offset:0 lds
buffer_load_dwordx4 v5,s[40:43],s63 offen offset:1024 lds
buffer_load_dwordx4 v6,s[40:43],s63 offen offset:2048 lds
s_add_u32 s63,s63,458752
.L_gy0:
s_waitcnt lgkmcnt(0)
v_mfma_f32_16x16x32_bf16 v[20:23],a[0:3],v[16:19],v[20:23]
v_mfma_f32_16x16x32_bf16 v[24:27],a[4:7],v[16:19],v[24:27]
v_mfma_f32_16x16x32_bf16 v[28:31],a[8:11],v[16:19],v[28:31]
v_mfma_f32_16x16x32_bf16 v[20:23],a[12:15],v[16:19],v[20:23]
v_mfma_f32_16x16x32_bf16 v[24:27],a[16:19],v[16:19],v[24:27]
v_mfma_f32_16x16x32_bf16 v[28:31],a[20:23],v[16:19],v[28:31]
.L_gw1:
s_waitcnt vmcnt(12)
s_barrier
ds_read_b64_tr_b16 a[24:25],v8 offset:24576
ds_read_b64_tr_b16 a[26:27],v8 offset:25344
ds_read_b64_tr_b16 a[28:29],v8 offset:36864
ds_read_b64_tr_b16 a[30:31],v8 offset:37632
ds_read_b64_tr_b16 a[32:33],v9 offset:24576
ds_read_b64_tr_b16 a[34:35],v9 offset:25344
ds_read_b64_tr_b16 a[36:37],v8 offset:30720
ds_read_b64_tr_b16 a[38:39],v8 offset:31488
ds_read_b64_tr_b16 a[40:41],v8 offset:43008
ds_read_b64_tr_b16 a[42:43],v8 offset:43776
ds_read_b64_tr_b16 a[44:45],v9 offset:30720
ds_read_b64_tr_b16 a[46:47],v9 offset:31488
s_add_u32 m0,s64,0
s_nop 0
buffer_load_dwordx4 v4,s[36:39],s63 offen offset:0 lds
buffer_load_dwordx4 v5,s[36:39],s63 offen offset:1024 lds
buffer_load_dwordx4 v6,s[36:39],s63 offen offset:2048 lds
s_add_u32 m0,s64,12288
s_nop 0
buffer_load_dwordx4 v4,s[40:43],s63 offen offset:0 lds
buffer_load_dwordx4 v5,s[40:43],s63 offen offset:1024 lds
buffer_load_dwordx4 v6,s[40:43],s63 offen offset:2048 lds
s_add_u32 s63,s63,458752
.L_gy1:
s_waitcnt lgkmcnt(0)
v_mfma_f32_16x16x32_bf16 v[20:23],a[24:27],v[16:19],v[20:23]
v_mfma_f32_16x16x32_bf16 v[24:27],a[28:31],v[16:19],v[24:27]
v_mfma_f32_16x16x32_bf16 v[28:31],a[32:35],v[16:19],v[28:31]
v_mfma_f32_16x16x32_bf16 v[20:23],a[36:39],v[16:19],v[20:23]
v_mfma_f32_16x16x32_bf16 v[24:27],a[40:43],v[16:19],v[24:27]
v_mfma_f32_16x16x32_bf16 v[28:31],a[44:47],v[16:19],v[28:31]
.L_gw2:
s_waitcnt vmcnt(12)
s_cmp_eq_u32 s28,1
s_barrier
ds_read_b64_tr_b16 a[48:49],v10 offset:0
ds_read_b64_tr_b16 a[50:51],v10 offset:768
)#"
R"#(
ds_read_b64_tr_b16 a[52:53],v10 offset:12288
ds_read_b64_tr_b16 a[54:55],v10 offset:13056
ds_read_b64_tr_b16 a[56:57],v11 offset:0
ds_read_b64_tr_b16 a[58:59],v11 offset:768
ds_read_b64_tr_b16 a[60:61],v10 offset:6144
ds_read_b64_tr_b16 a[62:63],v10 offset:6912
ds_read_b64_tr_b16 a[64:65],v10 offset:18432
ds_read_b64_tr_b16 a[66:67],v10 offset:19200
ds_read_b64_tr_b16 a[68:69],v11 offset:6144
ds_read_b64_tr_b16 a[70:71],v11 offset:6912
s_cbranch_scc1 .L_gy2
s_add_u32 m0,s64,24576
s_nop 0
buffer_load_dwordx4 v4,s[36:39],s63 offen offset:0 lds
buffer_load_dwordx4 v5,s[36:39],s63 offen offset:1024 lds
buffer_load_dwordx4 v6,s[36:39],s63 offen offset:2048 lds
s_add_u32 m0,s64,36864
s_nop 0
buffer_load_dwordx4 v4,s[40:43],s63 offen offset:0 lds
buffer_load_dwordx4 v5,s[40:43],s63 offen offset:1024 lds
buffer_load_dwordx4 v6,s[40:43],s63 offen offset:2048 lds
s_add_u32 s63,s63,458752
.L_gy2:
s_waitcnt lgkmcnt(0)
v_mfma_f32_16x16x32_bf16 v[20:23],a[48:51],v[16:19],v[20:23]
v_mfma_f32_16x16x32_bf16 v[24:27],a[52:55],v[16:19],v[24:27]
v_mfma_f32_16x16x32_bf16 v[28:31],a[56:59],v[16:19],v[28:31]
v_mfma_f32_16x16x32_bf16 v[20:23],a[60:63],v[16:19],v[20:23]
v_mfma_f32_16x16x32_bf16 v[24:27],a[64:67],v[16:19],v[24:27]
v_mfma_f32_16x16x32_bf16 v[28:31],a[68:71],v[16:19],v[28:31]
.L_gw3:
s_waitcnt vmcnt(12)
s_cmp_eq_u32 s28,1
s_cbranch_scc0 .L_gx3
s_waitcnt vmcnt(6)
.L_gx3:
s_barrier
ds_read_b64_tr_b16 a[72:73],v10 offset:24576
ds_read_b64_tr_b16 a[74:75],v10 offset:25344
ds_read_b64_tr_b16 a[76:77],v10 offset:36864
ds_read_b64_tr_b16 a[78:79],v10 offset:37632
ds_read_b64_tr_b16 a[80:81],v11 offset:24576
ds_read_b64_tr_b16 a[82:83],v11 offset:25344
ds_read_b64_tr_b16 a[84:85],v10 offset:30720
ds_read_b64_tr_b16 a[86:87],v10 offset:31488
ds_read_b64_tr_b16 a[88:89],v10 offset:43008
ds_read_b64_tr_b16 a[90:91],v10 offset:43776
ds_read_b64_tr_b16 a[92:93],v11 offset:30720
ds_read_b64_tr_b16 a[94:95],v11 offset:31488
s_cbranch_scc1 .L_gy3
s_add_u32 m0,s64,49152
s_nop 0
buffer_load_dwordx4 v4,s[36:39],s63 offen offset:0 lds
buffer_load_dwordx4 v5,s[36:39],s63 offen offset:1024 lds
buffer_load_dwordx4 v6,s[36:39],s63 offen offset:2048 lds
s_add_u32 m0,s64,61440
s_nop 0
buffer_load_dwordx4 v4,s[40:43],s63 offen offset:0 lds
buffer_load_dwordx4 v5,s[40:43],s63 offen offset:1024 lds
buffer_load_dwordx4 v6,s[40:43],s63 offen offset:2048 lds
s_add_u32 s63,s63,458752
.L_gy3:
s_waitcnt lgkmcnt(0)
v_mfma_f32_16x16x32_bf16 v[20:23],a[72:75],v[16:19],v[20:23]
v_mfma_f32_16x16x32_bf16 v[24:27],a[76:79],v[16:19],v[24:27]
v_mfma_f32_16x16x32_bf16 v[28:31],a[80:83],v[16:19],v[28:31]
v_mfma_f32_16x16x32_bf16 v[20:23],a[84:87],v[16:19],v[20:23]
v_mfma_f32_16x16x32_bf16 v[24:27],a[88:91],v[16:19],v[24:27]
v_mfma_f32_16x16x32_bf16 v[28:31],a[92:95],v[16:19],v[28:31]
.L_gw4:
s_waitcnt vmcnt(12)
s_cmp_eq_u32 s28,1
s_cbranch_scc0 .L_gx4
s_waitcnt vmcnt(0)
.L_gx4:
s_barrier
ds_read_b64_tr_b16 a[96:97],v8 offset:0
ds_read_b64_tr_b16 a[98:99],v8 offset:768
ds_read_b64_tr_b16 a[100:101],v8 offset:12288
ds_read_b64_tr_b16 a[102:103],v8 offset:13056
ds_read_b64_tr_b16 a[104:105],v9 offset:0
ds_read_b64_tr_b16 a[106:107],v9 offset:768
ds_read_b64_tr_b16 a[108:109],v8 offset:6144
ds_read_b64_tr_b16 a[110:111],v8 offset:6912
ds_read_b64_tr_b16 a[112:113],v8 offset:18432
ds_read_b64_tr_b16 a[114:115],v8 offset:19200
ds_read_b64_tr_b16 a[116:117],v9 offset:6144
ds_read_b64_tr_b16 a[118:119],v9 offset:6912
s_cbranch_scc1 .L_gy4
s_add_u32 m0,s64,73728
s_nop 0
buffer_load_dwordx4 v4,s[36:39],s63 offen offset:0 lds
buffer_load_dwordx4 v5,s[36:39],s63 offen offset:1024 lds
buffer_load_dwordx4 v6,s[36:39],s63 offen offset:2048 lds
s_add_u32 m0,s64,86016
s_nop 0
buffer_load_dwordx4 v4,s[40:43],s63 offen offset:0 lds
buffer_load_dwordx4 v5,s[40:43],s63 offen offset:1024 lds
buffer_load_dwordx4 v6,s[40:43],s63 offen offset:2048 lds
s_add_u32 s63,s63,458752
.L_gy4:
s_waitcnt lgkmcnt(0)
v_mfma_f32_16x16x32_bf16 v[20:23],a[96:99],v[16:19],v[20:23]
v_mfma_f32_16x16x32_bf16 v[24:27],a[100:103],v[16:19],v[24:27]
v_mfma_f32_16x16x32_bf16 v[28:31],a[104:107],v[16:19],v[28:31]
v_mfma_f32_16x16x32_bf16 v[20:23],a[108:111],v[16:19],v[20:23]
v_mfma_f32_16x16x32_bf16 v[24:27],a[112:115],v[16:19],v[24:27]
v_mfma_f32_16x16x32_bf16 v[28:31],a[116:119],v[16:19],v[28:31]
s_cmp_eq_u32 s28,1
s_cbranch_scc1 .L_gz
.L_gw5:
s_waitcnt vmcnt(12)
s_barrier
ds_read_b64_tr_b16 a[120:121],v8 offset:24576
ds_read_b64_tr_b16 a[122:123],v8 offset:25344
ds_read_b64_tr_b16 a[124:125],v8 offset:36864
ds_read_b64_tr_b16 a[126:127],v8 offset:37632
ds_read_b64_tr_b16 a[128:129],v9 offset:24576
ds_read_b64_tr_b16 a[130:131],v9 offset:25344
ds_read_b64_tr_b16 a[132:133],v8 offset:30720
ds_read_b64_tr_b16 a[134:135],v8 offset:31488
ds_read_b64_tr_b16 a[136:137],v8 offset:43008
ds_read_b64_tr_b16 a[138:139],v8 offset:43776
ds_read_b64_tr_b16 a[140:141],v9 offset:30720
ds_read_b64_tr_b16 a[142:143],v9 offset:31488
s_add_u32 m0,s64,0
s_nop 0
buffer_load_dwordx4 v4,s[36:39],s63 offen offset:0 lds
buffer_load_dwordx4 v5,s[36:39],s63 offen offset:1024 lds
buffer_load_dwordx4 v6,s[36:39],s63 offen offset:2048 lds
s_add_u32 m0,s64,12288
s_nop 0
buffer_load_dwordx4 v4,s[40:43],s63 offen offset:0 lds
buffer_load_dwordx4 v5,s[40:43],s63 offen offset:1024 lds
buffer_load_dwordx4 v6,s[40:43],s63 offen offset:2048 lds
s_add_u32 s63,s63,458752
.L_gy5:
s_waitcnt lgkmcnt(0)
v_mfma_f32_16x16x32_bf16 v[20:23],a[120:123],v[16:19],v[20:23]
v_mfma_f32_16x16x32_bf16 v[24:27],a[124:127],v[16:19],v[24:27]
v_mfma_f32_16x16x32_bf16 v[28:31],a[128:131],v[16:19],v[28:31]
v_mfma_f32_16x16x32_bf16 v[20:23],a[132:135],v[16:19],v[20:23]
v_mfma_f32_16x16x32_bf16 v[24:27],a[136:139],v[16:19],v[24:27]
v_mfma_f32_16x16x32_bf16 v[28:31],a[140:143],v[16:19],v[28:31]
.L_gw6:
s_waitcnt vmcnt(12)
s_barrier
ds_read_b64_tr_b16 a[144:145],v10 offset:0
ds_read_b64_tr_b16 a[146:147],v10 offset:768
ds_read_b64_tr_b16 a[148:149],v10 offset:12288
ds_read_b64_tr_b16 a[150:151],v10 offset:13056
ds_read_b64_tr_b16 a[152:153],v11 offset:0
ds_read_b64_tr_b16 a[154:155],v11 offset:768
ds_read_b64_tr_b16 a[156:157],v10 offset:6144
ds_read_b64_tr_b16 a[158:159],v10 offset:6912
ds_read_b64_tr_b16 a[160:161],v10 offset:18432
ds_read_b64_tr_b16 a[162:163],v10 offset:19200
ds_read_b64_tr_b16 a[164:165],v11 offset:6144
ds_read_b64_tr_b16 a[166:167],v11 offset:6912
s_add_u32 m0,s64,24576
s_nop 0
buffer_load_dwordx4 v4,s[36:39],s63 offen offset:0 lds
buffer_load_dwordx4 v5,s[36:39],s63 offen offset:1024 lds
buffer_load_dwordx4 v6,s[36:39],s63 offen offset:2048 lds
s_add_u32 m0,s64,36864
s_nop 0
buffer_load_dwordx4 v4,s[40:43],s63 offen offset:0 lds
buffer_load_dwordx4 v5,s[40:43],s63 offen offset:1024 lds
buffer_load_dwordx4 v6,s[40:43],s63 offen offset:2048 lds
s_add_u32 s63,s63,458752
.L_gy6:
s_waitcnt lgkmcnt(0)
v_mfma_f32_16x16x32_bf16 v[20:23],a[144:147],v[16:19],v[20:23]
v_mfma_f32_16x16x32_bf16 v[24:27],a[148:151],v[16:19],v[24:27]
v_mfma_f32_16x16x32_bf16 v[28:31],a[152:155],v[16:19],v[28:31]
v_mfma_f32_16x16x32_bf16 v[20:23],a[156:159],v[16:19],v[20:23]
v_mfma_f32_16x16x32_bf16 v[24:27],a[160:163],v[16:19],v[24:27]
v_mfma_f32_16x16x32_bf16 v[28:31],a[164:167],v[16:19],v[28:31]
.L_gw7:
s_waitcnt vmcnt(12)
s_barrier
ds_read_b64_tr_b16 a[168:169],v10 offset:24576
ds_read_b64_tr_b16 a[170:171],v10 offset:25344
ds_read_b64_tr_b16 a[172:173],v10 offset:36864
ds_read_b64_tr_b16 a[174:175],v10 offset:37632
ds_read_b64_tr_b16 a[176:177],v11 offset:24576
ds_read_b64_tr_b16 a[178:179],v11 offset:25344
ds_read_b64_tr_b16 a[180:181],v10 offset:30720
ds_read_b64_tr_b16 a[182:183],v10 offset:31488
ds_read_b64_tr_b16 a[184:185],v10 offset:43008
ds_read_b64_tr_b16 a[186:187],v10 offset:43776
ds_read_b64_tr_b16 a[188:189],v11 offset:30720
ds_read_b64_tr_b16 a[190:191],v11 offset:31488
s_add_u32 m0,s64,49152
s_nop 0
buffer_load_dwordx4 v4,s[36:39],s63 offen offset:0 lds
buffer_load_dwordx4 v5,s[36:39],s63 offen offset:1024 lds
buffer_load_dwordx4 v6,s[36:39],s63 offen offset:2048 lds
s_add_u32 m0,s64,61440
s_nop 0
buffer_load_dwordx4 v4,s[40:43],s63 offen offset:0 lds
buffer_load_dwordx4 v5,s[40:43],s63 offen offset:1024 lds
buffer_load_dwordx4 v6,s[40:43],s63 offen offset:2048 lds
s_add_u32 s63,s63,458752
.L_gy7:
s_waitcnt lgkmcnt(0)
v_mfma_f32_16x16x32_bf16 v[20:23],a[168:171],v[16:19],v[20:23]
v_mfma_f32_16x16x32_bf16 v[24:27],a[172:175],v[16:19],v[24:27]
v_mfma_f32_16x16x32_bf16 v[28:31],a[176:179],v[16:19],v[28:31]
v_mfma_f32_16x16x32_bf16 v[20:23],a[180:183],v[16:19],v[20:23]
v_mfma_f32_16x16x32_bf16 v[24:27],a[184:187],v[16:19],v[24:27]
v_mfma_f32_16x16x32_bf16 v[28:31],a[188:191],v[16:19],v[28:31]
.L_gw8:
s_waitcnt vmcnt(12)
s_cmp_eq_u32 s28,2
s_barrier
ds_read_b64_tr_b16 a[192:193],v8 offset:0
ds_read_b64_tr_b16 a[194:195],v8 offset:768
ds_read_b64_tr_b16 a[196:197],v8 offset:12288
ds_read_b64_tr_b16 a[198:199],v8 offset:13056
ds_read_b64_tr_b16 a[200:201],v9 offset:0
ds_read_b64_tr_b16 a[202:203],v9 offset:768
ds_read_b64_tr_b16 a[204:205],v8 offset:6144
ds_read_b64_tr_b16 a[206:207],v8 offset:6912
ds_read_b64_tr_b16 a[208:209],v8 offset:18432
ds_read_b64_tr_b16 a[210:211],v8 offset:19200
ds_read_b64_tr_b16 a[212:213],v9 offset:6144
ds_read_b64_tr_b16 a[214:215],v9 offset:6912
s_cbranch_scc1 .L_gy8
s_add_u32 m0,s64,73728
s_nop 0
buffer_load_dwordx4 v4,s[36:39],s63 offen offset:0 lds
buffer_load_dwordx4 v5,s[36:39],s63 offen offset:1024 lds
buffer_load_dwordx4 v6,s[36:39],s63 offen offset:2048 lds
s_add_u32 m0,s64,86016
s_nop 0
buffer_load_dwordx4 v4,s[40:43],s63 offen offset:0 lds
buffer_load_dwordx4 v5,s[40:43],s63 offen offset:1024 lds
buffer_load_dwordx4 v6,s[40:43],s63 offen offset:2048 lds
s_add_u32 s63,s63,458752
.L_gy8:
s_waitcnt lgkmcnt(0)
v_mfma_f32_16x16x32_bf16 v[20:23],a[192:195],v[16:19],v[20:23]
v_mfma_f32_16x16x32_bf16 v[24:27],a[196:199],v[16:19],v[24:27]
v_mfma_f32_16x16x32_bf16 v[28:31],a[200:203],v[16:19],v[28:31]
v_mfma_f32_16x16x32_bf16 v[20:23],a[204:207],v[16:19],v[20:23]
v_mfma_f32_16x16x32_bf16 v[24:27],a[208:211],v[16:19],v[24:27]
v_mfma_f32_16x16x32_bf16 v[28:31],a[212:215],v[16:19],v[28:31]
.L_gw9:
s_waitcnt vmcnt(12)
s_cmp_eq_u32 s28,2
s_cbranch_scc0 .L_gx9
s_waitcnt vmcnt(6)
.L_gx9:
s_barrier
ds_read_b64_tr_b16 a[216:217],v8 offset:24576
ds_read_b64_tr_b16 a[218:219],v8 offset:25344
ds_read_b64_tr_b16 a[220:221],v8 offset:36864
ds_read_b64_tr_b16 a[222:223],v8 offset:37632
ds_read_b64_tr_b16 a[224:225],v9 offset:24576
ds_read_b64_tr_b16 a[226:227],v9 offset:25344
ds_read_b64_tr_b16 a[228:229],v8 offset:30720
ds_read_b64_tr_b16 a[230:231],v8 offset:31488
ds_read_b64_tr_b16 a[232:233],v8 offset:43008
ds_read_b64_tr_b16 a[234:235],v8 offset:43776
ds_read_b64_tr_b16 a[236:237],v9 offset:30720
ds_read_b64_tr_b16 a[238:239],v9 offset:31488
s_cbranch_scc1 .L_gy9
s_add_u32 m0,s64,0
s_nop 0
buffer_load_dwordx4 v4,s[36:39],s63 offen offset:0 lds
buffer_load_dwordx4 v5,s[36:39],s63 offen offset:1024 lds
buffer_load_dwordx4 v6,s[36:39],s63 offen offset:2048 lds
s_add_u32 m0,s64,12288
s_nop 0
buffer_load_dwordx4 v4,s[40:43],s63 offen offset:0 lds
buffer_load_dwordx4 v5,s[40:43],s63 offen offset:1024 lds
buffer_load_dwordx4 v6,s[40:43],s63 offen offset:2048 lds
s_add_u32 s63,s63,458752
.L_gy9:
s_waitcnt lgkmcnt(0)
v_mfma_f32_16x16x32_bf16 v[20:23],a[216:219],v[16:19],v[20:23]
v_mfma_f32_16x16x32_bf16 v[24:27],a[220:223],v[16:19],v[24:27]
v_mfma_f32_16x16x32_bf16 v[28:31],a[224:227],v[16:19],v[28:31]
v_mfma_f32_16x16x32_bf16 v[20:23],a[228:231],v[16:19],v[20:23]
v_mfma_f32_16x16x32_bf16 v[24:27],a[232:235],v[16:19],v[24:27]
v_mfma_f32_16x16x32_bf16 v[28:31],a[236:239],v[16:19],v[28:31]
.L_gw10:
s_waitcnt vmcnt(12)
s_cmp_eq_u32 s28,2
s_cbranch_scc0 .L_gx10
s_waitcnt vmcnt(0)
.L_gx10:
s_barrier
ds_read_b64_tr_b16 a[240:241],v10 offset:0
ds_read_b64_tr_b16 a[242:243],v10 offset:768
ds_read_b64_tr_b16 a[244:245],v10 offset:12288
ds_read_b64_tr_b16 a[246:247],v10 offset:13056
ds_read_b64_tr_b16 a[248:249],v11 offset:0
ds_read_b64_tr_b16 a[250:251],v11 offset:768
ds_read_b64_tr_b16 a[252:253],v10 offset:6144
ds_read_b64_tr_b16 a[254:255],v10 offset:6912
ds_read_b64_tr_b16 v[128:129],v10 offset:18432
ds_read_b64_tr_b16 v[130:131],v10 offset:19200
ds_read_b64_tr_b16 v[132:133],v11 offset:6144
ds_read_b64_tr_b16 v[134:135],v11 offset:6912
s_cbranch_scc1 .L_gy10
s_add_u32 m0,s64,24576
s_nop 0
buffer_load_dwordx4 v4,s[36:39],s63 offen offset:0 lds
buffer_load_dwordx4 v5,s[36:39],s63 offen offset:1024 lds
buffer_load_dwordx4 v6,s[36:39],s63 offen offset:2048 lds
s_add_u32 m0,s64,36864
s_nop 0
buffer_load_dwordx4 v4,s[40:43],s63 offen offset:0 lds
buffer_load_dwordx4 v5,s[40:43],s63 offen offset:1024 lds
buffer_load_dwordx4 v6,s[40:43],s63 offen offset:2048 lds
s_add_u32 s63,s63,458752
.L_gy10:
s_waitcnt lgkmcnt(0)
v_mfma_f32_16x16x32_bf16 v[20:23],a[240:243],v[16:19],v[20:23]
v_mfma_f32_16x16x32_bf16 v[24:27],a[244:247],v[16:19],v[24:27]
v_mfma_f32_16x16x32_bf16 v[28:31],a[248:251],v[16:19],v[28:31]
v_mfma_f32_16x16x32_bf16 v[20:23],a[252:255],v[16:19],v[20:23]
v_mfma_f32_16x16x32_bf16 v[24:27],v[128:131],v[16:19],v[24:27]
v_mfma_f32_16x16x32_bf16 v[28:31],v[132:135],v[16:19],v[28:31]
s_cmp_eq_u32 s28,2
s_cbranch_scc1 .L_gz
.L_gw11:
s_waitcnt vmcnt(12)
s_barrier
ds_read_b64_tr_b16 v[136:137],v10 offset:24576
ds_read_b64_tr_b16 v[138:139],v10 offset:25344
ds_read_b64_tr_b16 v[140:141],v10 offset:36864
ds_read_b64_tr_b16 v[142:143],v10 offset:37632
ds_read_b64_tr_b16 v[144:145],v11 offset:24576
ds_read_b64_tr_b16 v[146:147],v11 offset:25344
ds_read_b64_tr_b16 v[148:149],v10 offset:30720
ds_read_b64_tr_b16 v[150:151],v10 offset:31488
ds_read_b64_tr_b16 v[152:153],v10 offset:43008
ds_read_b64_tr_b16 v[154:155],v10 offset:43776
ds_read_b64_tr_b16 v[156:157],v11 offset:30720
ds_read_b64_tr_b16 v[158:159],v11 offset:31488
s_add_u32 m0,s64,49152
s_nop 0
buffer_load_dwordx4 v4,s[36:39],s63 offen offset:0 lds
buffer_load_dwordx4 v5,s[36:39],s63 offen offset:1024 lds
buffer_load_dwordx4 v6,s[36:39],s63 offen offset:2048 lds
s_add_u32 m0,s64,61440
s_nop 0
buffer_load_dwordx4 v4,s[40:43],s63 offen offset:0 lds
buffer_load_dwordx4 v5,s[40:43],s63 offen offset:1024 lds
buffer_load_dwordx4 v6,s[40:43],s63 offen offset:2048 lds
s_add_u32 s63,s63,458752
.L_gy11:
s_waitcnt lgkmcnt(0)
v_mfma_f32_16x16x32_bf16 v[20:23],v[136:139],v[16:19],v[20:23]
v_mfma_f32_16x16x32_bf16 v[24:27],v[140:143],v[16:19],v[24:27]
v_mfma_f32_16x16x32_bf16 v[28:31],v[144:147],v[16:19],v[28:31]
v_mfma_f32_16x16x32_bf16 v[20:23],v[148:151],v[16:19],v[20:23]
v_mfma_f32_16x16x32_bf16 v[24:27],v[152:155],v[16:19],v[24:27]
v_mfma_f32_16x16x32_bf16 v[28:31],v[156:159],v[16:19],v[28:31]
.L_gw12:
s_waitcnt vmcnt(12)
s_barrier
ds_read_b64_tr_b16 v[160:161],v8 offset:0
ds_read_b64_tr_b16 v[162:163],v8 offset:768
ds_read_b64_tr_b16 v[164:165],v8 offset:12288
ds_read_b64_tr_b16 v[166:167],v8 offset:13056
ds_read_b64_tr_b16 v[168:169],v9 offset:0
ds_read_b64_tr_b16 v[170:171],v9 offset:768
ds_read_b64_tr_b16 v[172:173],v8 offset:6144
ds_read_b64_tr_b16 v[174:175],v8 offset:6912
ds_read_b64_tr_b16 v[176:177],v8 offset:18432
ds_read_b64_tr_b16 v[178:179],v8 offset:19200
ds_read_b64_tr_b16 v[180:181],v9 offset:6144
ds_read_b64_tr_b16 v[182:183],v9 offset:6912
s_add_u32 m0,s64,73728
s_nop 0
buffer_load_dwordx4 v4,s[36:39],s63 offen offset:0 lds
buffer_load_dwordx4 v5,s[36:39],s63 offen offset:1024 lds
buffer_load_dwordx4 v6,s[36:39],s63 offen offset:2048 lds
s_add_u32 m0,s64,86016
s_nop 0
buffer_load_dwordx4 v4,s[40:43],s63 offen offset:0 lds
buffer_load_dwordx4 v5,s[40:43],s63 offen offset:1024 lds
buffer_load_dwordx4 v6,s[40:43],s63 offen offset:2048 lds
s_sub_u32 s63,s63,6881280
.L_gy12:
s_waitcnt lgkmcnt(0)
v_mfma_f32_16x16x32_bf16 v[20:23],v[160:163],v[16:19],v[20:23]
v_mfma_f32_16x16x32_bf16 v[24:27],v[164:167],v[16:19],v[24:27]
v_mfma_f32_16x16x32_bf16 v[28:31],v[168:171],v[16:19],v[28:31]
v_mfma_f32_16x16x32_bf16 v[20:23],v[172:175],v[16:19],v[20:23]
v_mfma_f32_16x16x32_bf16 v[24:27],v[176:179],v[16:19],v[24:27]
v_mfma_f32_16x16x32_bf16 v[28:31],v[180:183],v[16:19],v[28:31]
.L_gw13:
s_waitcnt vmcnt(12)
s_cmp_eq_u32 s28,0
s_barrier
ds_read_b64_tr_b16 v[184:185],v8 offset:24576
ds_read_b64_tr_b16 v[186:187],v8 offset:25344
ds_read_b64_tr_b16 v[188:189],v8 offset:36864
ds_read_b64_tr_b16 v[190:191],v8 offset:37632
ds_read_b64_tr_b16 v[192:193],v9 offset:24576
ds_read_b64_tr_b16 v[194:195],v9 offset:25344
ds_read_b64_tr_b16 v[196:197],v8 offset:30720
ds_read_b64_tr_b16 v[198:199],v8 offset:31488
ds_read_b64_tr_b16 v[200:201],v8 offset:43008
ds_read_b64_tr_b16 v[202:203],v8 offset:43776
ds_read_b64_tr_b16 v[204:205],v9 offset:30720
ds_read_b64_tr_b16 v[206:207],v9 offset:31488
s_cbranch_scc1 .L_gy13
s_add_u32 m0,s64,0
s_nop 0
buffer_load_dwordx4 v4,s[36:39],s63 offen offset:0 lds
buffer_load_dwordx4 v5,s[36:39],s63 offen offset:1024 lds
buffer_load_dwordx4 v6,s[36:39],s63 offen offset:2048 lds
s_add_u32 m0,s64,12288
s_nop 0
buffer_load_dwordx4 v4,s[40:43],s63 offen offset:0 lds
buffer_load_dwordx4 v5,s[40:43],s63 offen offset:1024 lds
buffer_load_dwordx4 v6,s[40:43],s63 offen offset:2048 lds
s_add_u32 s63,s63,458752
.L_gy13:
s_waitcnt lgkmcnt(0)
v_mfma_f32_16x16x32_bf16 v[20:23],v[184:187],v[16:19],v[20:23]
v_mfma_f32_16x16x32_bf16 v[24:27],v[188:191],v[16:19],v[24:27]
v_mfma_f32_16x16x32_bf16 v[28:31],v[192:195],v[16:19],v[28:31]
v_mfma_f32_16x16x32_bf16 v[20:23],v[196:199],v[16:19],v[20:23]
v_mfma_f32_16x16x32_bf16 v[24:27],v[200:203],v[16:19],v[24:27]
v_mfma_f32_16x16x32_bf16 v[28:31],v[204:207],v[16:19],v[28:31]
.L_gw14:
s_waitcnt vmcnt(12)
s_cmp_eq_u32 s28,0
s_cbranch_scc0 .L_gx14
s_waitcnt vmcnt(6)
.L_gx14:
s_barrier
ds_read_b64_tr_b16 v[208:209],v10 offset:0
ds_read_b64_tr_b16 v[210:211],v10 offset:768
ds_read_b64_tr_b16 v[212:213],v10 offset:12288
ds_read_b64_tr_b16 v[214:215],v10 offset:13056
ds_read_b64_tr_b16 v[216:217],v11 offset:0
ds_read_b64_tr_b16 v[218:219],v11 offset:768
ds_read_b64_tr_b16 v[220:221],v10 offset:6144
ds_read_b64_tr_b16 v[222:223],v10 offset:6912
ds_read_b64_tr_b16 v[224:225],v10 offset:18432
ds_read_b64_tr_b16 v[226:227],v10 offset:19200
ds_read_b64_tr_b16 v[228:229],v11 offset:6144
ds_read_b64_tr_b16 v[230:231],v11 offset:6912
s_cbranch_scc1 .L_gy14
s_add_u32 m0,s64,24576
s_nop 0
buffer_load_dwordx4 v4,s[36:39],s63 offen offset:0 lds
buffer_load_dwordx4 v5,s[36:39],s63 offen offset:1024 lds
buffer_load_dwordx4 v6,s[36:39],s63 offen offset:2048 lds
s_add_u32 m0,s64,36864
s_nop 0
buffer_load_dwordx4 v4,s[40:43],s63 offen offset:0 lds
buffer_load_dwordx4 v5,s[40:43],s63 offen offset:1024 lds
buffer_load_dwordx4 v6,s[40:43],s63 offen offset:2048 lds
s_add_u32 s63,s63,458752
.L_gy14:
s_waitcnt lgkmcnt(0)
v_mfma_f32_16x16x32_bf16 v[20:23],v[208:211],v[16:19],v[20:23]
v_mfma_f32_16x16x32_bf16 v[24:27],v[212:215],v[16:19],v[24:27]
v_mfma_f32_16x16x32_bf16 v[28:31],v[216:219],v[16:19],v[28:31]
v_mfma_f32_16x16x32_bf16 v[20:23],v[220:223],v[16:19],v[20:23]
v_mfma_f32_16x16x32_bf16 v[24:27],v[224:227],v[16:19],v[24:27]
v_mfma_f32_16x16x32_bf16 v[28:31],v[228:231],v[16:19],v[28:31]
.L_gw15:
s_waitcnt vmcnt(12)
s_cmp_eq_u32 s28,0
s_cbranch_scc0 .L_gx15
s_waitcnt vmcnt(0)
.L_gx15:
s_barrier
ds_read_b64_tr_b16 v[232:233],v10 offset:24576
ds_read_b64_tr_b16 v[234:235],v10 offset:25344
ds_read_b64_tr_b16 v[236:237],v10 offset:36864
ds_read_b64_tr_b16 v[238:239],v10 offset:37632
ds_read_b64_tr_b16 v[240:241],v11 offset:24576
ds_read_b64_tr_b16 v[242:243],v11 offset:25344
ds_read_b64_tr_b16 v[244:245],v10 offset:30720
ds_read_b64_tr_b16 v[246:247],v10 offset:31488
ds_read_b64_tr_b16 v[248:249],v10 offset:43008
ds_read_b64_tr_b16 v[250:251],v10 offset:43776
ds_read_b64_tr_b16 v[252:253],v11 offset:30720
ds_read_b64_tr_b16 v[254:255],v11 offset:31488
s_cbranch_scc1 .L_gy15
s_add_u32 m0,s64,49152
s_nop 0
buffer_load_dwordx4 v4,s[36:39],s63 offen offset:0 lds
buffer_load_dwordx4 v5,s[36:39],s63 offen offset:1024 lds
buffer_load_dwordx4 v6,s[36:39],s63 offen offset:2048 lds
s_add_u32 m0,s64,61440
s_nop 0
buffer_load_dwordx4 v4,s[40:43],s63 offen offset:0 lds
buffer_load_dwordx4 v5,s[40:43],s63 offen offset:1024 lds
)#"
R"#(
buffer_load_dwordx4 v6,s[40:43],s63 offen offset:2048 lds
s_add_u32 s63,s63,458752
.L_gy15:
s_waitcnt lgkmcnt(0)
v_mfma_f32_16x16x32_bf16 v[20:23],v[232:235],v[16:19],v[20:23]
v_mfma_f32_16x16x32_bf16 v[24:27],v[236:239],v[16:19],v[24:27]
v_mfma_f32_16x16x32_bf16 v[28:31],v[240:243],v[16:19],v[28:31]
v_mfma_f32_16x16x32_bf16 v[20:23],v[244:247],v[16:19],v[20:23]
v_mfma_f32_16x16x32_bf16 v[24:27],v[248:251],v[16:19],v[24:27]
v_mfma_f32_16x16x32_bf16 v[28:31],v[252:255],v[16:19],v[28:31]
s_cmp_eq_u32 s28,0
s_cbranch_scc0 .L_gw0
.L_gz:
s_barrier
s_lshl2_add_u32 s49,s27,128
s_mul_i32 s68,s27,3072
s_min_u32 s68,s68,111616
s_mul_i32 s48,s26,768
s_cmp_le_u32 s28,0
s_addc_u32 s62,84,0
s_mov_b32 s30,890880
s_mul_i32 s57,s28,296960
s_mov_b32 s56,76021760
s_cmp_lg_u32 s25,0
s_cmov_b32 s30,296960
s_cmov_b32 s57,81960960
s_cmov_b32 s56,81960960
s_lshl_b32 s31,s30,1
s_cmp_lg_u32 s25,0
s_cmov_b32 s62,-1
v_cmp_gt_u32 vcc,48,v1
v_mov_b32 v15,0x7fff0000
s_nop 0
v_cndmask_b32 v15,v15,v2,vcc
s_add_u32 s68,s68,s48
s_add_u32 s68,s68,4718592
s_waitcnt lgkmcnt(0)
s_add_u32 s54,s52,0x7f4a7c15
s_addc_u32 s55,s53,0x9e3779b9
s_lshl_b32 s67,s49,5
s_add_u32 s67,s67,4898816
s_sub_u32 s51,s67,s68
s_add_u32 s92,s32,s51
s_addc_u32 s93,s33,0x10000
s_sub_u32 s92,s92,s30
s_subb_u32 s93,s93,0
s_cmp_eq_u32 s26,3
s_cselect_b32 s94,1,0
s_mov_b32 s95,0x20000
v_mov_b32 v16,s54
v_mov_b32 v17,s55
v_cmp_gt_u32 vcc,12,v1
v_mul_u32_u24 v3,296960,v1
v_mov_b32 v12,0x7fff0000
v_cndmask_b32 v3,v12,v3,vcc
s_add_u32 s51,s67,s56
buffer_store_dwordx2 v[16:17],v3,s[0:3],s51 offen sc1
s_sub_u32 s67,s68,296960
s_add_u32 s68,s68,s57
s_sub_u32 s68,s68,s31
s_add_u32 s63,s57,4681728
s_add_u32 s66,s57,4898816
s_mul_i32 s64,s26,9216
s_add_u32 s65,s64,73728
s_xor_b32 s65,s65,s64
s_add_u32 s63,s63,s64
v_mov_b32 v4,v2
v_add_u32 v5,36864,v2
v_add_u32 v6,4096,v2
v_add_u32 v7,8192,v2
v_bfe_u32 v3,v1,1,3
v_mul_u32_u24 v8,1152,v3
v_lshrrev_b32 v3,1,v3
v_add_u32 v3,s26,v3
v_lshrrev_b32 v9,4,v1
v_lshl_add_u32 v3,v3,2,v9
v_and_b32 v3,15,v3
v_lshl_add_u32 v8,v3,2,v8
v_and_b32 v3,1,v1
v_lshl_add_u32 v8,v3,6,v8
v_add_u32 v8,1024,v8
s_lshl_b32 s48,s26,2
v_add_u32 v10,s48,v9
v_and_b32 v11,15,v1
v_add_u32 v3,64,v1
v_cmp_eq_u32 vcc,0,v11
v_add_u32 v9,73728,v8
v_xor_b32 v9,v9,v8
v_cndmask_b32 v10,v3,v10,vcc
v_lshlrev_b32 v10,2,v10
v_add_u32 v10,147456,v10
v_lshlrev_b32 v11,2,v11
v_add_u32 v11,147456,v11
v_lshlrev_b32 v14,2,v1
v_add_u32 v14,96,v14
v_lshrrev_b32 v14,2,v14
v_lshlrev_b32 v14,7,v14
v_cmp_gt_u32 vcc,8,v1
v_mov_b32 v3,0x7fff0000
s_nop 0
v_cndmask_b32 v14,v3,v14,vcc
v_cndmask_b32 v16,0,v16,vcc
v_cndmask_b32 v17,0,v17,vcc
s_cmp_lg_u32 s26,1
s_cbranch_scc1 .L_skga
s_mov_b32 s48,20000
s_branch .L_rfga
.L_rlga:
s_sleep 2
.L_rfga:
buffer_load_dwordx2 v[12:13],v14,s[0:3],s66 offen sc1
s_sub_u32 s48,s48,1
s_cbranch_scc1 .L_end
s_waitcnt vmcnt(0)
v_cmp_ne_u64 vcc,v[16:17],v[12:13]
s_cbranch_vccnz .L_rlga
.L_skga:
s_waitcnt vmcnt(0)
s_barrier
s_add_u32 s66,s66,s30
s_add_u32 m0,s64,0
s_nop 0
buffer_load_dwordx4 v2,s[32:35],s63 offen offset:0 sc1 lds
buffer_load_dwordx4 v2,s[32:35],s63 offen offset:1024 sc1 lds
buffer_load_dwordx4 v2,s[32:35],s63 offen offset:2048 sc1 lds
buffer_load_dwordx4 v2,s[32:35],s63 offen offset:3072 sc1 lds
s_add_u32 m0,s64,4096
s_nop 0
buffer_load_dwordx4 v6,s[32:35],s63 offen offset:0 sc1 lds
buffer_load_dwordx4 v6,s[32:35],s63 offen offset:1024 sc1 lds
buffer_load_dwordx4 v6,s[32:35],s63 offen offset:2048 sc1 lds
buffer_load_dwordx4 v6,s[32:35],s63 offen offset:3072 sc1 lds
s_add_u32 m0,s64,8192
s_nop 0
buffer_load_dwordx4 v7,s[32:35],s63 offen offset:0 sc1 lds
s_add_u32 s63,s63,s30
s_cmp_lg_u32 s26,2
s_cbranch_scc1 .L_skgb
s_mov_b32 s48,20000
s_branch .L_rfgb
.L_rlgb:
s_sleep 2
.L_rfgb:
buffer_load_dwordx2 v[12:13],v14,s[0:3],s66 offen sc1
s_sub_u32 s48,s48,1
s_cbranch_scc1 .L_end
s_waitcnt vmcnt(0)
v_cmp_ne_u64 vcc,v[16:17],v[12:13]
s_cbranch_vccnz .L_rlgb
.L_skgb:
s_waitcnt vmcnt(0)
s_add_u32 s66,s66,s30
s_barrier
s_add_u32 m0,s64,36864
s_nop 0
buffer_load_dwordx4 v2,s[32:35],s63 offen offset:0 sc1 lds
buffer_load_dwordx4 v2,s[32:35],s63 offen offset:1024 sc1 lds
buffer_load_dwordx4 v2,s[32:35],s63 offen offset:2048 sc1 lds
buffer_load_dwordx4 v2,s[32:35],s63 offen offset:3072 sc1 lds
s_add_u32 m0,s64,40960
s_nop 0
buffer_load_dwordx4 v6,s[32:35],s63 offen offset:0 sc1 lds
buffer_load_dwordx4 v6,s[32:35],s63 offen offset:1024 sc1 lds
ds_read2st64_b32 v[56:57],v8 offset0:0 offset1:36
ds_read2st64_b32 v[58:59],v8 offset0:72 offset1:108
s_waitcnt lgkmcnt(0)
v_add_f32 v56,v56,v57
v_add_f32 v58,v58,v59
v_add_f32 v56,v56,v58
s_nop 1
v_add_f32_dpp v56,v56,v56 row_ror:8 row_mask:0xf bank_mask:0xf
s_nop 1
v_add_f32_dpp v56,v56,v56 row_ror:4 row_mask:0xf bank_mask:0xf
ds_read_b128 v[76:79],v4 offset:0
ds_read_b128 v[80:83],v4 offset:1152
ds_read_b128 v[84:87],v4 offset:2304
ds_read_b128 v[88:91],v4 offset:3456
ds_read_b128 v[92:95],v4 offset:4608
ds_read_b128 v[96:99],v4 offset:5760
ds_read_b128 v[100:103],v4 offset:6912
ds_read_b128 v[104:107],v4 offset:8064
s_waitcnt lgkmcnt(0)
s_cmp_eq_u32 s26,2
s_cselect_b32 s48,s30,0
s_add_u32 s66,s66,s48
.L_ggo:
s_cmp_eq_u32 s26,1
s_cbranch_scc1 .L_gv1
s_cmp_eq_u32 s26,2
s_cbranch_scc1 .L_gv2
s_cmp_eq_u32 s26,3
s_cbranch_scc1 .L_gv3
.p2align 6
.L_gv0:
.L_gl_0:
s_waitcnt lgkmcnt(4)
v_mfma_f32_16x16x32_bf16 v[32:35],a[0:3],v[76:79],0
ds_read_b128 v[108:111],v4 offset:9216
s_add_u32 s68,s68,s30
v_mfma_f32_16x16x32_bf16 v[32:35],a[12:15],v[80:83],v[32:35]
buffer_load_dwordx4 v6,s[32:35],s63 offen offset:2048 sc1 lds
v_fma_f32 v52,-v60,v28,v52
v_mfma_f32_16x16x32_bf16 v[32:35],a[24:27],v[84:87],v[32:35]
v_fma_f32 v53,-v60,v29,v53
v_fma_f32 v54,-v60,v30,v54
v_mfma_f32_16x16x32_bf16 v[32:35],a[36:39],v[88:91],v[32:35]
ds_read_b128 v[112:115],v4 offset:10368
v_fma_f32 v55,-v60,v31,v55
v_mfma_f32_16x16x32_bf16 v[36:39],a[40:43],v[88:91],0
v_permlane32_swap_b32 v52,v54
v_mfma_f32_16x16x32_bf16 v[36:39],a[28:31],v[84:87],v[36:39]
buffer_load_dwordx4 v6,s[32:35],s63 offen offset:3072 sc1 lds
v_fma_f32 v48,-v60,v24,v48
v_mfma_f32_16x16x32_bf16 v[36:39],a[16:19],v[80:83],v[36:39]
ds_read_b128 v[116:119],v4 offset:11520
v_fma_f32 v49,-v60,v25,v49
v_mfma_f32_16x16x32_bf16 v[36:39],a[4:7],v[76:79],v[36:39]
v_permlane32_swap_b32 v53,v55
v_mfma_f32_16x16x32_bf16 v[40:43],a[8:11],v[76:79],0
v_fma_f32 v50,-v60,v26,v50
v_fma_f32 v51,-v60,v27,v51
v_mfma_f32_16x16x32_bf16 v[40:43],a[20:23],v[80:83],v[40:43]
ds_read_b128 v[120:123],v4 offset:12672
v_fma_f32 v44,-v60,v20,v44
v_mfma_f32_16x16x32_bf16 v[40:43],a[32:35],v[84:87],v[40:43]
v_fma_f32 v45,-v60,v21,v45
v_fma_f32 v46,-v60,v22,v46
v_mfma_f32_16x16x32_bf16 v[40:43],a[44:47],v[88:91],v[40:43]
s_waitcnt lgkmcnt(4)
s_waitcnt vmcnt(8)
s_barrier
v_mfma_f32_16x16x32_bf16 v[40:43],a[56:59],v[92:95],v[40:43]
ds_read_b128 v[124:127],v4 offset:13824
s_add_u32 m0,s64,45056
v_mfma_f32_16x16x32_bf16 v[40:43],a[68:71],v[96:99],v[40:43]
buffer_load_dwordx4 v7,s[32:35],s63 offen offset:0 sc1 lds
s_add_u32 s63,s63,s30
v_mfma_f32_16x16x32_bf16 v[40:43],a[80:83],v[100:103],v[40:43]
s_xor_b32 s64,s64,s65
v_fma_f32 v47,-v60,v23,v47
v_mfma_f32_16x16x32_bf16 v[40:43],a[92:95],v[104:107],v[40:43]
ds_read_b128 v[76:79],v4 offset:14976
v_mul_f32 v64,v44,v61
v_mfma_f32_16x16x32_bf16 v[36:39],a[88:91],v[104:107],v[36:39]
v_exp_f32 v64,v64
v_mfma_f32_16x16x32_bf16 v[36:39],a[76:79],v[100:103],v[36:39]
v_mul_f32 v65,v45,v61
v_mul_f32 v66,v46,v61
v_mfma_f32_16x16x32_bf16 v[36:39],a[64:67],v[96:99],v[36:39]
ds_read_b128 v[80:83],v4 offset:16128
v_mul_f32 v67,v47,v61
v_mfma_f32_16x16x32_bf16 v[36:39],a[52:55],v[92:95],v[36:39]
v_exp_f32 v65,v65
v_mfma_f32_16x16x32_bf16 v[32:35],a[48:51],v[92:95],v[32:35]
v_exp_f32 v66,v66
v_mfma_f32_16x16x32_bf16 v[32:35],a[60:63],v[96:99],v[32:35]
ds_read_b128 v[84:87],v4 offset:17280
v_mul_f32 v68,v52,v61
v_mfma_f32_16x16x32_bf16 v[32:35],a[72:75],v[100:103],v[32:35]
v_exp_f32 v67,v67
v_mfma_f32_16x16x32_bf16 v[32:35],a[84:87],v[104:107],v[32:35]
v_mul_f32 v69,v53,v61
s_waitcnt lgkmcnt(4)
v_mfma_f32_16x16x32_bf16 v[32:35],a[96:99],v[108:111],v[32:35]
ds_read_b128 v[88:91],v4 offset:18432
v_mul_f32 v44,v44,v48
v_mfma_f32_16x16x32_bf16 v[32:35],a[108:111],v[112:115],v[32:35]
v_exp_f32 v68,v68
v_mfma_f32_16x16x32_bf16 v[32:35],a[120:123],v[116:119],v[32:35]
v_exp_f32 v69,v69
v_mfma_f32_16x16x32_bf16 v[32:35],a[132:135],v[120:123],v[32:35]
ds_read_b128 v[92:95],v4 offset:19584
v_mul_f32 v45,v45,v49
v_mfma_f32_16x16x32_bf16 v[36:39],a[136:139],v[120:123],v[36:39]
v_mul_f32 v46,v46,v50
v_mul_f32 v47,v47,v51
v_mfma_f32_16x16x32_bf16 v[36:39],a[124:127],v[116:119],v[36:39]
v_mul_f32 v52,v52,v54
v_mul_f32 v53,v53,v55
v_mfma_f32_16x16x32_bf16 v[36:39],a[112:115],v[112:115],v[36:39]
ds_read_b128 v[96:99],v4 offset:20736
v_fma_f32 v64,v64,v62,v62
v_mfma_f32_16x16x32_bf16 v[36:39],a[100:103],v[108:111],v[36:39]
v_rcp_f32 v64,v64
v_mfma_f32_16x16x32_bf16 v[40:43],a[104:107],v[108:111],v[40:43]
v_fma_f32 v65,v65,v62,v62
v_fma_f32 v66,v66,v62,v62
v_mfma_f32_16x16x32_bf16 v[40:43],a[116:119],v[112:115],v[40:43]
ds_read_b128 v[100:103],v4 offset:21888
v_fma_f32 v67,v67,v62,v62
v_mfma_f32_16x16x32_bf16 v[40:43],a[128:131],v[116:119],v[40:43]
v_rcp_f32 v65,v65
v_mfma_f32_16x16x32_bf16 v[40:43],a[140:143],v[120:123],v[40:43]
v_fma_f32 v68,v68,v62,v62
s_waitcnt lgkmcnt(4)
v_mfma_f32_16x16x32_bf16 v[40:43],a[152:155],v[124:127],v[40:43]
ds_read_b128 v[104:107],v4 offset:23040
v_fma_f32 v69,v69,v62,v62
v_mfma_f32_16x16x32_bf16 v[40:43],a[164:167],v[76:79],v[40:43]
v_rcp_f32 v66,v66
v_mfma_f32_16x16x32_bf16 v[40:43],a[176:179],v[80:83],v[40:43]
v_rcp_f32 v67,v67
v_mfma_f32_16x16x32_bf16 v[40:43],a[188:191],v[84:87],v[40:43]
ds_read_b128 v[108:111],v4 offset:24192
v_mul_f32 v44,v44,v64
v_mfma_f32_16x16x32_bf16 v[36:39],a[184:187],v[84:87],v[36:39]
v_rcp_f32 v68,v68
v_mfma_f32_16x16x32_bf16 v[36:39],a[172:175],v[80:83],v[36:39]
v_rcp_f32 v69,v69
v_mfma_f32_16x16x32_bf16 v[36:39],a[160:163],v[76:79],v[36:39]
ds_read_b128 v[112:115],v4 offset:25344
v_mul_f32 v45,v45,v65
v_mfma_f32_16x16x32_bf16 v[36:39],a[148:151],v[124:127],v[36:39]
v_mul_f32 v46,v46,v66
v_mul_f32 v47,v47,v67
v_mfma_f32_16x16x32_bf16 v[32:35],a[144:147],v[124:127],v[32:35]
v_mul_f32 v52,v52,v68
v_mul_f32 v53,v53,v69
v_mfma_f32_16x16x32_bf16 v[32:35],a[156:159],v[76:79],v[32:35]
ds_read_b128 v[116:119],v4 offset:26496
v_cvt_pk_bf16_f32 v74,v52,v53
v_mfma_f32_16x16x32_bf16 v[32:35],a[168:171],v[80:83],v[32:35]
v_cvt_pk_bf16_f32 v72,v44,v45
v_cvt_pk_bf16_f32 v73,v46,v47
v_mfma_f32_16x16x32_bf16 v[32:35],a[180:183],v[84:87],v[32:35]
v_add_f32_dpp v56,v56,v56 row_ror:2 row_mask:0xf bank_mask:0xf
s_waitcnt lgkmcnt(4)
v_mfma_f32_16x16x32_bf16 v[32:35],a[192:195],v[88:91],v[32:35]
ds_read_b128 v[120:123],v4 offset:27648
v_mul_f32 v56,0x3a800000,v56
v_mfma_f32_16x16x32_bf16 v[32:35],a[204:207],v[92:95],v[32:35]
v_permlane32_swap_b32 v74,v75
v_mfma_f32_16x16x32_bf16 v[32:35],a[216:219],v[96:99],v[32:35]
v_mul_f32 v57,v56,v56
v_mfma_f32_16x16x32_bf16 v[32:35],a[228:231],v[100:103],v[32:35]
ds_read_b128 v[124:127],v4 offset:28800
v_sub_f32_dpp v57,v56,v57 quad_perm:[1,1,3,3] row_mask:0xf bank_mask:0xf
v_mfma_f32_16x16x32_bf16 v[36:39],a[232:235],v[100:103],v[36:39]
v_permlane16_swap_b32 v72,v74
v_mfma_f32_16x16x32_bf16 v[36:39],a[220:223],v[96:99],v[36:39]
v_permlane16_swap_b32 v73,v75
v_mfma_f32_16x16x32_bf16 v[36:39],a[208:211],v[92:95],v[36:39]
buffer_store_dwordx4 v[72:75],v15,s[32:35],s68 offen sc1
ds_read_b128 v[76:79],v4 offset:29952
v_mfma_f32_16x16x32_bf16 v[36:39],a[196:199],v[88:91],v[36:39]
v_max_f32 v57,0,v57
v_mfma_f32_16x16x32_bf16 v[40:43],a[200:203],v[88:91],v[40:43]
v_cvt_pk_bf16_f32 v57,0,v57
v_mfma_f32_16x16x32_bf16 v[40:43],a[212:215],v[92:95],v[40:43]
ds_read_b128 v[80:83],v4 offset:31104
v_add_f32 v57,0x3727c5ac,v57
v_mfma_f32_16x16x32_bf16 v[40:43],a[224:227],v[96:99],v[40:43]
v_cvt_pk_bf16_f32 v57,0,v57
v_mfma_f32_16x16x32_bf16 v[40:43],a[236:239],v[100:103],v[40:43]
s_waitcnt lgkmcnt(4)
v_mfma_f32_16x16x32_bf16 v[40:43],a[248:251],v[104:107],v[40:43]
ds_read_b128 v[84:87],v4 offset:32256
v_mfma_f32_16x16x32_bf16 v[40:43],v[132:135],v[108:111],v[40:43]
v_sqrt_f32 v57,v57
v_mfma_f32_16x16x32_bf16 v[40:43],v[144:147],v[112:115],v[40:43]
v_cvt_pk_bf16_f32 v57,v56,v57
v_mfma_f32_16x16x32_bf16 v[40:43],v[156:159],v[116:119],v[40:43]
ds_read_b128 v[88:91],v4 offset:33408
ds_write_b32 v10,v57 offset:0
v_mfma_f32_16x16x32_bf16 v[36:39],v[152:155],v[116:119],v[36:39]
v_mfma_f32_16x16x32_bf16 v[36:39],v[140:143],v[112:115],v[36:39]
v_mfma_f32_16x16x32_bf16 v[36:39],v[128:131],v[108:111],v[36:39]
ds_read_b128 v[92:95],v4 offset:34560
v_mfma_f32_16x16x32_bf16 v[36:39],a[244:247],v[104:107],v[36:39]
v_mfma_f32_16x16x32_bf16 v[32:35],a[240:243],v[104:107],v[32:35]
v_mfma_f32_16x16x32_bf16 v[32:35],a[252:255],v[108:111],v[32:35]
ds_read_b128 v[96:99],v4 offset:35712
v_mfma_f32_16x16x32_bf16 v[32:35],v[136:139],v[112:115],v[32:35]
v_xor_b32 v4,73728,v4
v_mfma_f32_16x16x32_bf16 v[32:35],v[148:151],v[116:119],v[32:35]
s_waitcnt lgkmcnt(5)
s_waitcnt vmcnt(1)
s_waitcnt lgkmcnt(2)
s_barrier
v_mfma_f32_16x16x32_bf16 v[32:35],v[160:163],v[120:123],v[32:35]
ds_read_b128 v[100:103],v5 offset:0
s_add_u32 m0,s64,0
v_mfma_f32_16x16x32_bf16 v[32:35],v[172:175],v[124:127],v[32:35]
buffer_load_dwordx4 v2,s[32:35],s63 offen offset:0 sc1 lds
v_mfma_f32_16x16x32_bf16 v[32:35],v[184:187],v[76:79],v[32:35]
ds_read_b32 v62,v11 offset:0
v_mfma_f32_16x16x32_bf16 v[32:35],v[196:199],v[80:83],v[32:35]
ds_read_b128 v[104:107],v5 offset:1152
v_mfma_f32_16x16x32_bf16 v[36:39],v[200:203],v[80:83],v[36:39]
v_mfma_f32_16x16x32_bf16 v[36:39],v[188:191],v[76:79],v[36:39]
buffer_load_dwordx4 v2,s[32:35],s63 offen offset:1024 sc1 lds
v_mfma_f32_16x16x32_bf16 v[36:39],v[176:179],v[124:127],v[36:39]
ds_read_b128 v[108:111],v5 offset:2304
ds_read2st64_b32 v[56:57],v8 offset0:144 offset1:180
v_mfma_f32_16x16x32_bf16 v[36:39],v[164:167],v[120:123],v[36:39]
ds_read2st64_b32 v[58:59],v8 offset0:216 offset1:252
v_xor_b32 v8,v9,v8
v_mfma_f32_16x16x32_bf16 v[40:43],v[168:171],v[120:123],v[40:43]
s_waitcnt lgkmcnt(4)
v_lshlrev_b32 v60,16,v62
v_mfma_f32_16x16x32_bf16 v[40:43],v[180:183],v[124:127],v[40:43]
buffer_load_dwordx4 v2,s[32:35],s63 offen offset:2048 sc1 lds
ds_read_b128 v[112:115],v5 offset:3456
v_mfma_f32_16x16x32_bf16 v[40:43],v[192:195],v[76:79],v[40:43]
v_and_b32 v62,0xffff0000,v62
v_mfma_f32_16x16x32_bf16 v[40:43],v[204:207],v[80:83],v[40:43]
v_mfma_f32_16x16x32_bf16 v[40:43],v[216:219],v[84:87],v[40:43]
ds_read_b128 v[116:119],v5 offset:4608
v_mfma_f32_16x16x32_bf16 v[40:43],v[228:231],v[88:91],v[40:43]
buffer_load_dwordx4 v2,s[32:35],s63 offen offset:3072 sc1 lds
v_mfma_f32_16x16x32_bf16 v[40:43],v[240:243],v[92:95],v[40:43]
v_rcp_f32 v61,v62
v_mfma_f32_16x16x32_bf16 v[40:43],v[252:255],v[96:99],v[40:43]
ds_read_b128 v[120:123],v5 offset:5760
v_mul_f32 v62,v62,v62
v_mfma_f32_16x16x32_bf16 v[36:39],v[248:251],v[96:99],v[36:39]
s_add_u32 m0,s64,4096
s_waitcnt lgkmcnt(3)
v_add_f32 v56,v56,v57
v_mfma_f32_16x16x32_bf16 v[36:39],v[236:239],v[92:95],v[36:39]
buffer_load_dwordx4 v6,s[32:35],s63 offen offset:0 sc1 lds
v_add_f32 v58,v58,v59
v_mfma_f32_16x16x32_bf16 v[36:39],v[224:227],v[88:91],v[36:39]
ds_read_b128 v[124:127],v5 offset:6912
v_add_f32 v56,v56,v58
v_mfma_f32_16x16x32_bf16 v[36:39],v[212:215],v[84:87],v[36:39]
v_mul_f32 v61,0xbfb8aa3b,v61
v_add_f32_dpp v56,v56,v56 row_ror:8 row_mask:0xf bank_mask:0xf
v_mfma_f32_16x16x32_bf16 v[32:35],v[208:211],v[84:87],v[32:35]
v_mfma_f32_16x16x32_bf16 v[32:35],v[220:223],v[88:91],v[32:35]
buffer_load_dwordx4 v6,s[32:35],s63 offen offset:1024 sc1 lds
ds_read_b128 v[76:79],v5 offset:8064
v_mfma_f32_16x16x32_bf16 v[32:35],v[232:235],v[92:95],v[32:35]
v_add_f32_dpp v56,v56,v56 row_ror:4 row_mask:0xf bank_mask:0xf
s_sub_u32 s62,s62,1
v_mfma_f32_16x16x32_bf16 v[32:35],v[244:247],v[96:99],v[32:35]
s_cbranch_scc1 .L_gt0_0
s_waitcnt lgkmcnt(4)
v_mfma_f32_16x16x32_bf16 v[44:47],a[0:3],v[100:103],0
ds_read_b128 v[80:83],v5 offset:9216
s_add_u32 s68,s68,s30
v_mfma_f32_16x16x32_bf16 v[44:47],a[12:15],v[104:107],v[44:47]
buffer_load_dwordx4 v6,s[32:35],s63 offen offset:2048 sc1 lds
v_fma_f32 v40,-v60,v28,v40
v_mfma_f32_16x16x32_bf16 v[44:47],a[24:27],v[108:111],v[44:47]
v_fma_f32 v41,-v60,v29,v41
v_fma_f32 v42,-v60,v30,v42
v_mfma_f32_16x16x32_bf16 v[44:47],a[36:39],v[112:115],v[44:47]
ds_read_b128 v[84:87],v5 offset:10368
v_fma_f32 v43,-v60,v31,v43
v_mfma_f32_16x16x32_bf16 v[48:51],a[40:43],v[112:115],0
v_permlane32_swap_b32 v40,v42
v_mfma_f32_16x16x32_bf16 v[48:51],a[28:31],v[108:111],v[48:51]
buffer_load_dwordx4 v6,s[32:35],s63 offen offset:3072 sc1 lds
v_fma_f32 v36,-v60,v24,v36
v_mfma_f32_16x16x32_bf16 v[48:51],a[16:19],v[104:107],v[48:51]
ds_read_b128 v[88:91],v5 offset:11520
v_fma_f32 v37,-v60,v25,v37
v_mfma_f32_16x16x32_bf16 v[48:51],a[4:7],v[100:103],v[48:51]
v_permlane32_swap_b32 v41,v43
v_mfma_f32_16x16x32_bf16 v[52:55],a[8:11],v[100:103],0
v_fma_f32 v38,-v60,v26,v38
v_fma_f32 v39,-v60,v27,v39
v_mfma_f32_16x16x32_bf16 v[52:55],a[20:23],v[104:107],v[52:55]
ds_read_b128 v[92:95],v5 offset:12672
v_fma_f32 v32,-v60,v20,v32
v_mfma_f32_16x16x32_bf16 v[52:55],a[32:35],v[108:111],v[52:55]
v_fma_f32 v33,-v60,v21,v33
v_fma_f32 v34,-v60,v22,v34
v_mfma_f32_16x16x32_bf16 v[52:55],a[44:47],v[112:115],v[52:55]
s_waitcnt lgkmcnt(4)
s_waitcnt vmcnt(8)
s_barrier
v_mfma_f32_16x16x32_bf16 v[52:55],a[56:59],v[116:119],v[52:55]
ds_read_b128 v[96:99],v5 offset:13824
s_add_u32 m0,s64,8192
v_mfma_f32_16x16x32_bf16 v[52:55],a[68:71],v[120:123],v[52:55]
buffer_load_dwordx4 v7,s[32:35],s63 offen offset:0 sc1 lds
s_add_u32 s63,s63,s30
v_mfma_f32_16x16x32_bf16 v[52:55],a[80:83],v[124:127],v[52:55]
v_fma_f32 v35,-v60,v23,v35
v_mul_f32 v64,v32,v61
v_mfma_f32_16x16x32_bf16 v[52:55],a[92:95],v[76:79],v[52:55]
ds_read_b128 v[100:103],v5 offset:14976
v_mul_f32 v65,v33,v61
v_mfma_f32_16x16x32_bf16 v[48:51],a[88:91],v[76:79],v[48:51]
v_exp_f32 v64,v64
v_mfma_f32_16x16x32_bf16 v[48:51],a[76:79],v[124:127],v[48:51]
v_exp_f32 v65,v65
v_mfma_f32_16x16x32_bf16 v[48:51],a[64:67],v[120:123],v[48:51]
ds_read_b128 v[104:107],v5 offset:16128
v_mul_f32 v66,v34,v61
v_mfma_f32_16x16x32_bf16 v[48:51],a[52:55],v[116:119],v[48:51]
v_exp_f32 v66,v66
v_mfma_f32_16x16x32_bf16 v[44:47],a[48:51],v[116:119],v[44:47]
v_mul_f32 v67,v35,v61
v_mul_f32 v68,v40,v61
v_mfma_f32_16x16x32_bf16 v[44:47],a[60:63],v[120:123],v[44:47]
ds_read_b128 v[108:111],v5 offset:17280
v_mul_f32 v69,v41,v61
)#"
R"#(
v_mfma_f32_16x16x32_bf16 v[44:47],a[72:75],v[124:127],v[44:47]
v_exp_f32 v67,v67
v_mfma_f32_16x16x32_bf16 v[44:47],a[84:87],v[76:79],v[44:47]
v_mul_f32 v32,v32,v36
s_waitcnt lgkmcnt(4)
v_mfma_f32_16x16x32_bf16 v[44:47],a[96:99],v[80:83],v[44:47]
ds_read_b128 v[112:115],v5 offset:18432
v_mul_f32 v33,v33,v37
v_mfma_f32_16x16x32_bf16 v[44:47],a[108:111],v[84:87],v[44:47]
v_exp_f32 v68,v68
v_mfma_f32_16x16x32_bf16 v[44:47],a[120:123],v[88:91],v[44:47]
v_exp_f32 v69,v69
v_mfma_f32_16x16x32_bf16 v[44:47],a[132:135],v[92:95],v[44:47]
ds_read_b128 v[116:119],v5 offset:19584
v_mul_f32 v34,v34,v38
v_mfma_f32_16x16x32_bf16 v[48:51],a[136:139],v[92:95],v[48:51]
v_mul_f32 v35,v35,v39
v_mul_f32 v40,v40,v42
v_mfma_f32_16x16x32_bf16 v[48:51],a[124:127],v[88:91],v[48:51]
v_mul_f32 v41,v41,v43
v_fma_f32 v64,v64,v62,v62
v_mfma_f32_16x16x32_bf16 v[48:51],a[112:115],v[84:87],v[48:51]
ds_read_b128 v[120:123],v5 offset:20736
v_fma_f32 v65,v65,v62,v62
v_mfma_f32_16x16x32_bf16 v[48:51],a[100:103],v[80:83],v[48:51]
v_rcp_f32 v64,v64
v_mfma_f32_16x16x32_bf16 v[52:55],a[104:107],v[80:83],v[52:55]
v_rcp_f32 v65,v65
v_mfma_f32_16x16x32_bf16 v[52:55],a[116:119],v[84:87],v[52:55]
ds_read_b128 v[124:127],v5 offset:21888
v_fma_f32 v66,v66,v62,v62
v_mfma_f32_16x16x32_bf16 v[52:55],a[128:131],v[88:91],v[52:55]
v_rcp_f32 v66,v66
v_mfma_f32_16x16x32_bf16 v[52:55],a[140:143],v[92:95],v[52:55]
v_fma_f32 v67,v67,v62,v62
s_waitcnt lgkmcnt(4)
v_mfma_f32_16x16x32_bf16 v[52:55],a[152:155],v[96:99],v[52:55]
ds_read_b128 v[76:79],v5 offset:23040
v_fma_f32 v68,v68,v62,v62
v_mfma_f32_16x16x32_bf16 v[52:55],a[164:167],v[100:103],v[52:55]
v_rcp_f32 v67,v67
v_mfma_f32_16x16x32_bf16 v[52:55],a[176:179],v[104:107],v[52:55]
v_rcp_f32 v68,v68
v_mfma_f32_16x16x32_bf16 v[52:55],a[188:191],v[108:111],v[52:55]
ds_read_b128 v[80:83],v5 offset:24192
v_fma_f32 v69,v69,v62,v62
v_mfma_f32_16x16x32_bf16 v[48:51],a[184:187],v[108:111],v[48:51]
v_rcp_f32 v69,v69
v_mfma_f32_16x16x32_bf16 v[48:51],a[172:175],v[104:107],v[48:51]
v_mul_f32 v32,v32,v64
v_mul_f32 v33,v33,v65
v_mfma_f32_16x16x32_bf16 v[48:51],a[160:163],v[100:103],v[48:51]
ds_read_b128 v[84:87],v5 offset:25344
v_mul_f32 v34,v34,v66
v_mfma_f32_16x16x32_bf16 v[48:51],a[148:151],v[96:99],v[48:51]
v_mul_f32 v35,v35,v67
v_mul_f32 v40,v40,v68
v_mfma_f32_16x16x32_bf16 v[44:47],a[144:147],v[96:99],v[44:47]
v_mul_f32 v41,v41,v69
v_cvt_pk_bf16_f32 v72,v32,v33
v_mfma_f32_16x16x32_bf16 v[44:47],a[156:159],v[100:103],v[44:47]
ds_read_b128 v[88:91],v5 offset:26496
v_cvt_pk_bf16_f32 v74,v40,v41
v_mfma_f32_16x16x32_bf16 v[44:47],a[168:171],v[104:107],v[44:47]
v_cvt_pk_bf16_f32 v73,v34,v35
v_add_f32_dpp v56,v56,v56 row_ror:2 row_mask:0xf bank_mask:0xf
v_mfma_f32_16x16x32_bf16 v[44:47],a[180:183],v[108:111],v[44:47]
v_mul_f32 v56,0x3a800000,v56
s_waitcnt lgkmcnt(4)
v_mfma_f32_16x16x32_bf16 v[44:47],a[192:195],v[112:115],v[44:47]
ds_read_b128 v[92:95],v5 offset:27648
v_mul_f32 v57,v56,v56
v_mfma_f32_16x16x32_bf16 v[44:47],a[204:207],v[116:119],v[44:47]
v_permlane32_swap_b32 v74,v75
v_mfma_f32_16x16x32_bf16 v[44:47],a[216:219],v[120:123],v[44:47]
v_sub_f32_dpp v57,v56,v57 quad_perm:[1,1,3,3] row_mask:0xf bank_mask:0xf
v_mfma_f32_16x16x32_bf16 v[44:47],a[228:231],v[124:127],v[44:47]
ds_read_b128 v[96:99],v5 offset:28800
v_max_f32 v57,0,v57
v_mfma_f32_16x16x32_bf16 v[48:51],a[232:235],v[124:127],v[48:51]
v_permlane16_swap_b32 v72,v74
v_mfma_f32_16x16x32_bf16 v[48:51],a[220:223],v[120:123],v[48:51]
v_permlane16_swap_b32 v73,v75
v_mfma_f32_16x16x32_bf16 v[48:51],a[208:211],v[116:119],v[48:51]
buffer_store_dwordx4 v[72:75],v15,s[32:35],s68 offen sc1
ds_read_b128 v[100:103],v5 offset:29952
v_mfma_f32_16x16x32_bf16 v[48:51],a[196:199],v[112:115],v[48:51]
v_cvt_pk_bf16_f32 v57,0,v57
v_mfma_f32_16x16x32_bf16 v[52:55],a[200:203],v[112:115],v[52:55]
v_add_f32 v57,0x3727c5ac,v57
v_mfma_f32_16x16x32_bf16 v[52:55],a[212:215],v[116:119],v[52:55]
ds_read_b128 v[104:107],v5 offset:31104
v_cvt_pk_bf16_f32 v57,0,v57
v_mfma_f32_16x16x32_bf16 v[52:55],a[224:227],v[120:123],v[52:55]
v_sqrt_f32 v57,v57
v_mfma_f32_16x16x32_bf16 v[52:55],a[236:239],v[124:127],v[52:55]
v_cvt_pk_bf16_f32 v57,v56,v57
s_waitcnt lgkmcnt(4)
v_mfma_f32_16x16x32_bf16 v[52:55],a[248:251],v[76:79],v[52:55]
ds_read_b128 v[108:111],v5 offset:32256
ds_write_b32 v10,v57 offset:128
v_mfma_f32_16x16x32_bf16 v[52:55],v[132:135],v[80:83],v[52:55]
v_mfma_f32_16x16x32_bf16 v[52:55],v[144:147],v[84:87],v[52:55]
v_mfma_f32_16x16x32_bf16 v[52:55],v[156:159],v[88:91],v[52:55]
ds_read_b128 v[112:115],v5 offset:33408
v_mfma_f32_16x16x32_bf16 v[48:51],v[152:155],v[88:91],v[48:51]
v_mfma_f32_16x16x32_bf16 v[48:51],v[140:143],v[84:87],v[48:51]
v_mfma_f32_16x16x32_bf16 v[48:51],v[128:131],v[80:83],v[48:51]
ds_read_b128 v[116:119],v5 offset:34560
v_mfma_f32_16x16x32_bf16 v[48:51],a[244:247],v[76:79],v[48:51]
v_mfma_f32_16x16x32_bf16 v[44:47],a[240:243],v[76:79],v[44:47]
v_mfma_f32_16x16x32_bf16 v[44:47],a[252:255],v[80:83],v[44:47]
ds_read_b128 v[120:123],v5 offset:35712
v_mfma_f32_16x16x32_bf16 v[44:47],v[136:139],v[84:87],v[44:47]
v_xor_b32 v5,73728,v5
v_mfma_f32_16x16x32_bf16 v[44:47],v[148:151],v[88:91],v[44:47]
s_waitcnt lgkmcnt(5)
s_waitcnt vmcnt(1)
s_waitcnt lgkmcnt(3)
s_barrier
v_mfma_f32_16x16x32_bf16 v[44:47],v[160:163],v[92:95],v[44:47]
ds_read_b128 v[76:79],v4 offset:0
s_add_u32 m0,s64,36864
v_mfma_f32_16x16x32_bf16 v[44:47],v[172:175],v[96:99],v[44:47]
buffer_load_dwordx4 v2,s[32:35],s63 offen offset:0 sc1 lds
v_mfma_f32_16x16x32_bf16 v[44:47],v[184:187],v[100:103],v[44:47]
ds_read_b32 v62,v11 offset:128
v_mfma_f32_16x16x32_bf16 v[44:47],v[196:199],v[104:107],v[44:47]
ds_read_b128 v[80:83],v4 offset:1152
v_mfma_f32_16x16x32_bf16 v[48:51],v[200:203],v[104:107],v[48:51]
v_mfma_f32_16x16x32_bf16 v[48:51],v[188:191],v[100:103],v[48:51]
buffer_load_dwordx4 v2,s[32:35],s63 offen offset:1024 sc1 lds
v_mfma_f32_16x16x32_bf16 v[48:51],v[176:179],v[96:99],v[48:51]
ds_read_b128 v[84:87],v4 offset:2304
ds_read2st64_b32 v[56:57],v8 offset0:0 offset1:36
v_mfma_f32_16x16x32_bf16 v[48:51],v[164:167],v[92:95],v[48:51]
ds_read2st64_b32 v[58:59],v8 offset0:72 offset1:108
v_mfma_f32_16x16x32_bf16 v[52:55],v[168:171],v[92:95],v[52:55]
s_waitcnt lgkmcnt(4)
v_lshlrev_b32 v60,16,v62
v_mfma_f32_16x16x32_bf16 v[52:55],v[180:183],v[96:99],v[52:55]
buffer_load_dwordx4 v2,s[32:35],s63 offen offset:2048 sc1 lds
ds_read_b128 v[88:91],v4 offset:3456
v_mfma_f32_16x16x32_bf16 v[52:55],v[192:195],v[100:103],v[52:55]
v_and_b32 v62,0xffff0000,v62
v_mfma_f32_16x16x32_bf16 v[52:55],v[204:207],v[104:107],v[52:55]
v_mfma_f32_16x16x32_bf16 v[52:55],v[216:219],v[108:111],v[52:55]
ds_read_b128 v[92:95],v4 offset:4608
v_mfma_f32_16x16x32_bf16 v[52:55],v[228:231],v[112:115],v[52:55]
buffer_load_dwordx4 v2,s[32:35],s63 offen offset:3072 sc1 lds
v_mfma_f32_16x16x32_bf16 v[52:55],v[240:243],v[116:119],v[52:55]
v_rcp_f32 v61,v62
v_mfma_f32_16x16x32_bf16 v[52:55],v[252:255],v[120:123],v[52:55]
ds_read_b128 v[96:99],v4 offset:5760
v_mul_f32 v62,v62,v62
v_mfma_f32_16x16x32_bf16 v[48:51],v[248:251],v[120:123],v[48:51]
s_add_u32 m0,s64,40960
s_waitcnt lgkmcnt(3)
v_add_f32 v56,v56,v57
v_mfma_f32_16x16x32_bf16 v[48:51],v[236:239],v[116:119],v[48:51]
buffer_load_dwordx4 v6,s[32:35],s63 offen offset:0 sc1 lds
v_add_f32 v58,v58,v59
v_mfma_f32_16x16x32_bf16 v[48:51],v[224:227],v[112:115],v[48:51]
ds_read_b128 v[100:103],v4 offset:6912
v_add_f32 v56,v56,v58
v_mfma_f32_16x16x32_bf16 v[48:51],v[212:215],v[108:111],v[48:51]
v_mul_f32 v61,0xbfb8aa3b,v61
v_add_f32_dpp v56,v56,v56 row_ror:8 row_mask:0xf bank_mask:0xf
v_mfma_f32_16x16x32_bf16 v[44:47],v[208:211],v[108:111],v[44:47]
v_mfma_f32_16x16x32_bf16 v[44:47],v[220:223],v[112:115],v[44:47]
buffer_load_dwordx4 v6,s[32:35],s63 offen offset:1024 sc1 lds
ds_read_b128 v[104:107],v4 offset:8064
v_mfma_f32_16x16x32_bf16 v[44:47],v[232:235],v[116:119],v[44:47]
v_add_f32_dpp v56,v56,v56 row_ror:4 row_mask:0xf bank_mask:0xf
s_sub_u32 s62,s62,1
v_mfma_f32_16x16x32_bf16 v[44:47],v[244:247],v[120:123],v[44:47]
s_cbranch_scc0 .L_gl_0
s_nop 1
s_add_u32 s68,s68,s30
s_waitcnt vmcnt(6)
s_barrier
buffer_store_dwordx2 v[16:17],v2,s[92:95],s68 idxen sc1
v_fma_f32 v52,-v60,v28,v52
v_fma_f32 v53,-v60,v29,v53
v_fma_f32 v54,-v60,v30,v54
v_fma_f32 v55,-v60,v31,v55
v_fma_f32 v48,-v60,v24,v48
v_fma_f32 v49,-v60,v25,v49
v_fma_f32 v50,-v60,v26,v50
v_fma_f32 v51,-v60,v27,v51
v_fma_f32 v44,-v60,v20,v44
v_fma_f32 v45,-v60,v21,v45
v_fma_f32 v46,-v60,v22,v46
v_fma_f32 v47,-v60,v23,v47
v_permlane32_swap_b32 v52,v54
v_permlane32_swap_b32 v53,v55
v_mul_f32 v64,v44,v61
v_mul_f32 v65,v45,v61
v_mul_f32 v66,v46,v61
v_mul_f32 v67,v47,v61
v_mul_f32 v68,v52,v61
v_mul_f32 v69,v53,v61
v_exp_f32 v64,v64
v_exp_f32 v65,v65
v_mul_f32 v44,v44,v48
v_mul_f32 v45,v45,v49
v_exp_f32 v66,v66
v_exp_f32 v67,v67
v_mul_f32 v46,v46,v50
v_mul_f32 v47,v47,v51
v_exp_f32 v68,v68
v_exp_f32 v69,v69
v_mul_f32 v52,v52,v54
v_mul_f32 v53,v53,v55
v_fma_f32 v64,v64,v62,v62
v_fma_f32 v65,v65,v62,v62
v_rcp_f32 v64,v64
v_rcp_f32 v65,v65
v_fma_f32 v66,v66,v62,v62
v_fma_f32 v67,v67,v62,v62
v_rcp_f32 v66,v66
v_rcp_f32 v67,v67
v_fma_f32 v68,v68,v62,v62
v_fma_f32 v69,v69,v62,v62
v_rcp_f32 v68,v68
v_rcp_f32 v69,v69
v_mul_f32 v44,v44,v64
v_mul_f32 v45,v45,v65
v_mul_f32 v46,v46,v66
v_mul_f32 v47,v47,v67
v_mul_f32 v52,v52,v68
v_mul_f32 v53,v53,v69
v_cvt_pk_bf16_f32 v74,v52,v53
v_cvt_pk_bf16_f32 v72,v44,v45
v_cvt_pk_bf16_f32 v73,v46,v47
v_permlane32_swap_b32 v74,v75
s_nop 1
v_permlane16_swap_b32 v72,v74
v_permlane16_swap_b32 v73,v75
buffer_store_dwordx4 v[72:75],v15,s[32:35],s68 offen sc1
s_add_u32 s68,s68,s30
s_waitcnt vmcnt(0)
s_barrier
buffer_store_dwordx2 v[16:17],v2,s[92:95],s68 idxen sc1
s_branch .L_end
.L_gt0_0:
s_nop 1
s_add_u32 s68,s68,s30
s_waitcnt vmcnt(6)
s_barrier
buffer_store_dwordx2 v[16:17],v2,s[92:95],s68 idxen sc1
v_fma_f32 v40,-v60,v28,v40
v_fma_f32 v41,-v60,v29,v41
v_fma_f32 v42,-v60,v30,v42
v_fma_f32 v43,-v60,v31,v43
v_fma_f32 v36,-v60,v24,v36
v_fma_f32 v37,-v60,v25,v37
v_fma_f32 v38,-v60,v26,v38
v_fma_f32 v39,-v60,v27,v39
v_fma_f32 v32,-v60,v20,v32
v_fma_f32 v33,-v60,v21,v33
v_fma_f32 v34,-v60,v22,v34
v_fma_f32 v35,-v60,v23,v35
v_permlane32_swap_b32 v40,v42
v_permlane32_swap_b32 v41,v43
v_mul_f32 v64,v32,v61
v_mul_f32 v65,v33,v61
v_mul_f32 v66,v34,v61
v_mul_f32 v67,v35,v61
v_mul_f32 v68,v40,v61
v_mul_f32 v69,v41,v61
v_exp_f32 v64,v64
v_exp_f32 v65,v65
v_mul_f32 v32,v32,v36
v_mul_f32 v33,v33,v37
v_exp_f32 v66,v66
v_exp_f32 v67,v67
v_mul_f32 v34,v34,v38
v_mul_f32 v35,v35,v39
v_exp_f32 v68,v68
v_exp_f32 v69,v69
v_mul_f32 v40,v40,v42
v_mul_f32 v41,v41,v43
v_fma_f32 v64,v64,v62,v62
v_fma_f32 v65,v65,v62,v62
v_rcp_f32 v64,v64
v_rcp_f32 v65,v65
v_fma_f32 v66,v66,v62,v62
v_fma_f32 v67,v67,v62,v62
v_rcp_f32 v66,v66
v_rcp_f32 v67,v67
v_fma_f32 v68,v68,v62,v62
v_fma_f32 v69,v69,v62,v62
v_rcp_f32 v68,v68
v_rcp_f32 v69,v69
v_mul_f32 v32,v32,v64
v_mul_f32 v33,v33,v65
v_mul_f32 v34,v34,v66
v_mul_f32 v35,v35,v67
v_mul_f32 v40,v40,v68
v_mul_f32 v41,v41,v69
v_cvt_pk_bf16_f32 v74,v40,v41
v_cvt_pk_bf16_f32 v72,v32,v33
v_cvt_pk_bf16_f32 v73,v34,v35
v_permlane32_swap_b32 v74,v75
s_nop 1
v_permlane16_swap_b32 v72,v74
v_permlane16_swap_b32 v73,v75
buffer_store_dwordx4 v[72:75],v15,s[32:35],s68 offen sc1
s_add_u32 s68,s68,s30
s_waitcnt vmcnt(0)
s_barrier
buffer_store_dwordx2 v[16:17],v2,s[92:95],s68 idxen sc1
s_branch .L_end
.p2align 6
.L_gv1:
.L_gl_1:
s_waitcnt lgkmcnt(4)
v_mfma_f32_16x16x32_bf16 v[32:35],a[0:3],v[76:79],0
ds_read_b128 v[108:111],v4 offset:9216
s_add_u32 s68,s68,s30
v_mfma_f32_16x16x32_bf16 v[32:35],a[12:15],v[80:83],v[32:35]
buffer_load_dwordx4 v6,s[32:35],s63 offen offset:2048 sc1 lds
v_fma_f32 v52,-v60,v28,v52
v_mfma_f32_16x16x32_bf16 v[32:35],a[24:27],v[84:87],v[32:35]
v_fma_f32 v53,-v60,v29,v53
v_fma_f32 v54,-v60,v30,v54
v_mfma_f32_16x16x32_bf16 v[32:35],a[36:39],v[88:91],v[32:35]
ds_read_b128 v[112:115],v4 offset:10368
v_fma_f32 v55,-v60,v31,v55
v_mfma_f32_16x16x32_bf16 v[36:39],a[40:43],v[88:91],0
v_permlane32_swap_b32 v52,v54
v_mfma_f32_16x16x32_bf16 v[36:39],a[28:31],v[84:87],v[36:39]
buffer_load_dwordx4 v6,s[32:35],s63 offen offset:3072 sc1 lds
v_fma_f32 v48,-v60,v24,v48
v_mfma_f32_16x16x32_bf16 v[36:39],a[16:19],v[80:83],v[36:39]
ds_read_b128 v[116:119],v4 offset:11520
v_fma_f32 v49,-v60,v25,v49
v_mfma_f32_16x16x32_bf16 v[36:39],a[4:7],v[76:79],v[36:39]
v_permlane32_swap_b32 v53,v55
v_mfma_f32_16x16x32_bf16 v[40:43],a[8:11],v[76:79],0
v_fma_f32 v50,-v60,v26,v50
v_fma_f32 v51,-v60,v27,v51
v_mfma_f32_16x16x32_bf16 v[40:43],a[20:23],v[80:83],v[40:43]
ds_read_b128 v[120:123],v4 offset:12672
v_fma_f32 v44,-v60,v20,v44
v_mfma_f32_16x16x32_bf16 v[40:43],a[32:35],v[84:87],v[40:43]
s_waitcnt vmcnt(8)
s_barrier
v_mfma_f32_16x16x32_bf16 v[40:43],a[44:47],v[88:91],v[40:43]
v_fma_f32 v45,-v60,v21,v45
s_waitcnt lgkmcnt(4)
v_mfma_f32_16x16x32_bf16 v[40:43],a[56:59],v[92:95],v[40:43]
ds_read_b128 v[124:127],v4 offset:13824
s_add_u32 m0,s64,45056
v_mfma_f32_16x16x32_bf16 v[40:43],a[68:71],v[96:99],v[40:43]
buffer_load_dwordx4 v7,s[32:35],s63 offen offset:0 sc1 lds
s_add_u32 s63,s63,s30
v_mfma_f32_16x16x32_bf16 v[40:43],a[80:83],v[100:103],v[40:43]
s_xor_b32 s64,s64,s65
v_fma_f32 v46,-v60,v22,v46
v_mfma_f32_16x16x32_bf16 v[40:43],a[92:95],v[104:107],v[40:43]
ds_read_b128 v[76:79],v4 offset:14976
v_fma_f32 v47,-v60,v23,v47
v_mfma_f32_16x16x32_bf16 v[36:39],a[88:91],v[104:107],v[36:39]
v_mul_f32 v64,v44,v61
v_mul_f32 v65,v45,v61
v_mfma_f32_16x16x32_bf16 v[36:39],a[76:79],v[100:103],v[36:39]
buffer_load_dwordx2 v[12:13],v14,s[0:3],s66 offen sc1
v_mul_f32 v66,v46,v61
v_mfma_f32_16x16x32_bf16 v[36:39],a[64:67],v[96:99],v[36:39]
ds_read_b128 v[80:83],v4 offset:16128
v_mul_f32 v67,v47,v61
v_mfma_f32_16x16x32_bf16 v[36:39],a[52:55],v[92:95],v[36:39]
v_exp_f32 v64,v64
v_mfma_f32_16x16x32_bf16 v[32:35],a[48:51],v[92:95],v[32:35]
v_exp_f32 v65,v65
v_mfma_f32_16x16x32_bf16 v[32:35],a[60:63],v[96:99],v[32:35]
ds_read_b128 v[84:87],v4 offset:17280
v_mul_f32 v68,v52,v61
v_mfma_f32_16x16x32_bf16 v[32:35],a[72:75],v[100:103],v[32:35]
v_exp_f32 v66,v66
v_mfma_f32_16x16x32_bf16 v[32:35],a[84:87],v[104:107],v[32:35]
v_mul_f32 v69,v53,v61
s_waitcnt lgkmcnt(4)
v_mfma_f32_16x16x32_bf16 v[32:35],a[96:99],v[108:111],v[32:35]
ds_read_b128 v[88:91],v4 offset:18432
v_mul_f32 v44,v44,v48
v_mfma_f32_16x16x32_bf16 v[32:35],a[108:111],v[112:115],v[32:35]
v_exp_f32 v67,v67
v_mfma_f32_16x16x32_bf16 v[32:35],a[120:123],v[116:119],v[32:35]
v_exp_f32 v68,v68
v_mfma_f32_16x16x32_bf16 v[32:35],a[132:135],v[120:123],v[32:35]
ds_read_b128 v[92:95],v4 offset:19584
v_mul_f32 v45,v45,v49
v_mfma_f32_16x16x32_bf16 v[36:39],a[136:139],v[120:123],v[36:39]
v_exp_f32 v69,v69
v_mfma_f32_16x16x32_bf16 v[36:39],a[124:127],v[116:119],v[36:39]
v_mul_f32 v46,v46,v50
v_mul_f32 v47,v47,v51
v_mfma_f32_16x16x32_bf16 v[36:39],a[112:115],v[112:115],v[36:39]
ds_read_b128 v[96:99],v4 offset:20736
v_mul_f32 v52,v52,v54
v_mfma_f32_16x16x32_bf16 v[36:39],a[100:103],v[108:111],v[36:39]
v_mul_f32 v53,v53,v55
v_fma_f32 v64,v64,v62,v62
v_mfma_f32_16x16x32_bf16 v[40:43],a[104:107],v[108:111],v[40:43]
v_rcp_f32 v64,v64
v_mfma_f32_16x16x32_bf16 v[40:43],a[116:119],v[112:115],v[40:43]
ds_read_b128 v[100:103],v4 offset:21888
v_fma_f32 v65,v65,v62,v62
v_mfma_f32_16x16x32_bf16 v[40:43],a[128:131],v[116:119],v[40:43]
v_rcp_f32 v65,v65
v_mfma_f32_16x16x32_bf16 v[40:43],a[140:143],v[120:123],v[40:43]
v_fma_f32 v66,v66,v62,v62
s_waitcnt lgkmcnt(4)
v_mfma_f32_16x16x32_bf16 v[40:43],a[152:155],v[124:127],v[40:43]
ds_read_b128 v[104:107],v4 offset:23040
v_fma_f32 v67,v67,v62,v62
v_mfma_f32_16x16x32_bf16 v[40:43],a[164:167],v[76:79],v[40:43]
v_rcp_f32 v66,v66
v_mfma_f32_16x16x32_bf16 v[40:43],a[176:179],v[80:83],v[40:43]
v_rcp_f32 v67,v67
v_mfma_f32_16x16x32_bf16 v[40:43],a[188:191],v[84:87],v[40:43]
ds_read_b128 v[108:111],v4 offset:24192
v_fma_f32 v68,v68,v62,v62
v_mfma_f32_16x16x32_bf16 v[36:39],a[184:187],v[84:87],v[36:39]
v_rcp_f32 v68,v68
v_mfma_f32_16x16x32_bf16 v[36:39],a[172:175],v[80:83],v[36:39]
v_fma_f32 v69,v69,v62,v62
v_mul_f32 v44,v44,v64
v_mfma_f32_16x16x32_bf16 v[36:39],a[160:163],v[76:79],v[36:39]
ds_read_b128 v[112:115],v4 offset:25344
v_mul_f32 v45,v45,v65
v_mfma_f32_16x16x32_bf16 v[36:39],a[148:151],v[124:127],v[36:39]
v_rcp_f32 v69,v69
v_mfma_f32_16x16x32_bf16 v[32:35],a[144:147],v[124:127],v[32:35]
v_mul_f32 v46,v46,v66
v_mul_f32 v47,v47,v67
v_mfma_f32_16x16x32_bf16 v[32:35],a[156:159],v[76:79],v[32:35]
ds_read_b128 v[116:119],v4 offset:26496
v_mul_f32 v52,v52,v68
v_mfma_f32_16x16x32_bf16 v[32:35],a[168:171],v[80:83],v[32:35]
v_mul_f32 v53,v53,v69
v_cvt_pk_bf16_f32 v72,v44,v45
v_mfma_f32_16x16x32_bf16 v[32:35],a[180:183],v[84:87],v[32:35]
v_cvt_pk_bf16_f32 v74,v52,v53
s_waitcnt lgkmcnt(4)
v_mfma_f32_16x16x32_bf16 v[32:35],a[192:195],v[88:91],v[32:35]
ds_read_b128 v[120:123],v4 offset:27648
v_cvt_pk_bf16_f32 v73,v46,v47
v_mfma_f32_16x16x32_bf16 v[32:35],a[204:207],v[92:95],v[32:35]
v_permlane32_swap_b32 v74,v75
v_mfma_f32_16x16x32_bf16 v[32:35],a[216:219],v[96:99],v[32:35]
v_add_f32_dpp v56,v56,v56 row_ror:2 row_mask:0xf bank_mask:0xf
v_mfma_f32_16x16x32_bf16 v[32:35],a[228:231],v[100:103],v[32:35]
ds_read_b128 v[124:127],v4 offset:28800
v_mul_f32 v56,0x3a800000,v56
v_mfma_f32_16x16x32_bf16 v[36:39],a[232:235],v[100:103],v[36:39]
v_permlane16_swap_b32 v72,v74
v_mfma_f32_16x16x32_bf16 v[36:39],a[220:223],v[96:99],v[36:39]
v_permlane16_swap_b32 v73,v75
v_mfma_f32_16x16x32_bf16 v[36:39],a[208:211],v[92:95],v[36:39]
buffer_store_dwordx4 v[72:75],v15,s[32:35],s68 offen sc1
ds_read_b128 v[76:79],v4 offset:29952
v_mfma_f32_16x16x32_bf16 v[36:39],a[196:199],v[88:91],v[36:39]
v_mul_f32 v57,v56,v56
v_mfma_f32_16x16x32_bf16 v[40:43],a[200:203],v[88:91],v[40:43]
v_mfma_f32_16x16x32_bf16 v[40:43],a[212:215],v[92:95],v[40:43]
ds_read_b128 v[80:83],v4 offset:31104
v_sub_f32_dpp v57,v56,v57 quad_perm:[1,1,3,3] row_mask:0xf bank_mask:0xf
v_mfma_f32_16x16x32_bf16 v[40:43],a[224:227],v[96:99],v[40:43]
v_max_f32 v57,0,v57
v_mfma_f32_16x16x32_bf16 v[40:43],a[236:239],v[100:103],v[40:43]
v_cvt_pk_bf16_f32 v57,0,v57
s_waitcnt lgkmcnt(4)
v_mfma_f32_16x16x32_bf16 v[40:43],a[248:251],v[104:107],v[40:43]
ds_read_b128 v[84:87],v4 offset:32256
v_add_f32 v57,0x3727c5ac,v57
v_mfma_f32_16x16x32_bf16 v[40:43],v[132:135],v[108:111],v[40:43]
v_cvt_pk_bf16_f32 v57,0,v57
v_mfma_f32_16x16x32_bf16 v[40:43],v[144:147],v[112:115],v[40:43]
v_sqrt_f32 v57,v57
v_mfma_f32_16x16x32_bf16 v[40:43],v[156:159],v[116:119],v[40:43]
ds_read_b128 v[88:91],v4 offset:33408
v_cvt_pk_bf16_f32 v57,v56,v57
v_mfma_f32_16x16x32_bf16 v[36:39],v[152:155],v[116:119],v[36:39]
ds_write_b32 v10,v57 offset:0
v_mfma_f32_16x16x32_bf16 v[36:39],v[140:143],v[112:115],v[36:39]
v_mfma_f32_16x16x32_bf16 v[36:39],v[128:131],v[108:111],v[36:39]
ds_read_b128 v[92:95],v4 offset:34560
s_waitcnt vmcnt(1)
v_mfma_f32_16x16x32_bf16 v[36:39],a[244:247],v[104:107],v[36:39]
v_cmp_ne_u64 vcc,v[16:17],v[12:13]
v_mfma_f32_16x16x32_bf16 v[32:35],a[240:243],v[104:107],v[32:35]
s_cbranch_vccnz .L_rtg0_1
.L_bkg0_1:
s_add_u32 s66,s66,s31
v_mfma_f32_16x16x32_bf16 v[32:35],a[252:255],v[108:111],v[32:35]
ds_read_b128 v[96:99],v4 offset:35712
)#"
R"#(
v_mfma_f32_16x16x32_bf16 v[32:35],v[136:139],v[112:115],v[32:35]
v_xor_b32 v4,73728,v4
s_waitcnt lgkmcnt(2)
s_barrier
v_mfma_f32_16x16x32_bf16 v[32:35],v[148:151],v[116:119],v[32:35]
v_mfma_f32_16x16x32_bf16 v[32:35],v[160:163],v[120:123],v[32:35]
ds_read_b128 v[100:103],v5 offset:0
s_add_u32 m0,s64,0
v_mfma_f32_16x16x32_bf16 v[32:35],v[172:175],v[124:127],v[32:35]
buffer_load_dwordx4 v2,s[32:35],s63 offen offset:0 sc1 lds
v_mfma_f32_16x16x32_bf16 v[32:35],v[184:187],v[76:79],v[32:35]
ds_read_b32 v62,v11 offset:0
v_mfma_f32_16x16x32_bf16 v[32:35],v[196:199],v[80:83],v[32:35]
ds_read_b128 v[104:107],v5 offset:1152
v_mfma_f32_16x16x32_bf16 v[36:39],v[200:203],v[80:83],v[36:39]
v_mfma_f32_16x16x32_bf16 v[36:39],v[188:191],v[76:79],v[36:39]
buffer_load_dwordx4 v2,s[32:35],s63 offen offset:1024 sc1 lds
v_mfma_f32_16x16x32_bf16 v[36:39],v[176:179],v[124:127],v[36:39]
ds_read_b128 v[108:111],v5 offset:2304
ds_read2st64_b32 v[56:57],v8 offset0:144 offset1:180
v_mfma_f32_16x16x32_bf16 v[36:39],v[164:167],v[120:123],v[36:39]
ds_read2st64_b32 v[58:59],v8 offset0:216 offset1:252
v_xor_b32 v8,v9,v8
v_mfma_f32_16x16x32_bf16 v[40:43],v[168:171],v[120:123],v[40:43]
s_waitcnt lgkmcnt(4)
v_lshlrev_b32 v60,16,v62
v_mfma_f32_16x16x32_bf16 v[40:43],v[180:183],v[124:127],v[40:43]
buffer_load_dwordx4 v2,s[32:35],s63 offen offset:2048 sc1 lds
ds_read_b128 v[112:115],v5 offset:3456
v_mfma_f32_16x16x32_bf16 v[40:43],v[192:195],v[76:79],v[40:43]
v_and_b32 v62,0xffff0000,v62
v_mfma_f32_16x16x32_bf16 v[40:43],v[204:207],v[80:83],v[40:43]
v_mfma_f32_16x16x32_bf16 v[40:43],v[216:219],v[84:87],v[40:43]
ds_read_b128 v[116:119],v5 offset:4608
v_mfma_f32_16x16x32_bf16 v[40:43],v[228:231],v[88:91],v[40:43]
buffer_load_dwordx4 v2,s[32:35],s63 offen offset:3072 sc1 lds
v_mfma_f32_16x16x32_bf16 v[40:43],v[240:243],v[92:95],v[40:43]
v_rcp_f32 v61,v62
v_mfma_f32_16x16x32_bf16 v[40:43],v[252:255],v[96:99],v[40:43]
ds_read_b128 v[120:123],v5 offset:5760
v_mul_f32 v62,v62,v62
v_mfma_f32_16x16x32_bf16 v[36:39],v[248:251],v[96:99],v[36:39]
s_add_u32 m0,s64,4096
s_waitcnt lgkmcnt(3)
v_add_f32 v56,v56,v57
v_mfma_f32_16x16x32_bf16 v[36:39],v[236:239],v[92:95],v[36:39]
buffer_load_dwordx4 v6,s[32:35],s63 offen offset:0 sc1 lds
v_add_f32 v58,v58,v59
v_mfma_f32_16x16x32_bf16 v[36:39],v[224:227],v[88:91],v[36:39]
ds_read_b128 v[124:127],v5 offset:6912
v_add_f32 v56,v56,v58
v_mfma_f32_16x16x32_bf16 v[36:39],v[212:215],v[84:87],v[36:39]
v_mul_f32 v61,0xbfb8aa3b,v61
v_add_f32_dpp v56,v56,v56 row_ror:8 row_mask:0xf bank_mask:0xf
v_mfma_f32_16x16x32_bf16 v[32:35],v[208:211],v[84:87],v[32:35]
v_mfma_f32_16x16x32_bf16 v[32:35],v[220:223],v[88:91],v[32:35]
buffer_load_dwordx4 v6,s[32:35],s63 offen offset:1024 sc1 lds
ds_read_b128 v[76:79],v5 offset:8064
v_mfma_f32_16x16x32_bf16 v[32:35],v[232:235],v[92:95],v[32:35]
v_add_f32_dpp v56,v56,v56 row_ror:4 row_mask:0xf bank_mask:0xf
s_sub_u32 s62,s62,1
v_mfma_f32_16x16x32_bf16 v[32:35],v[244:247],v[96:99],v[32:35]
s_cbranch_scc1 .L_gt0_1
s_waitcnt lgkmcnt(4)
v_mfma_f32_16x16x32_bf16 v[44:47],a[0:3],v[100:103],0
ds_read_b128 v[80:83],v5 offset:9216
s_add_u32 s68,s68,s30
v_mfma_f32_16x16x32_bf16 v[44:47],a[12:15],v[104:107],v[44:47]
buffer_load_dwordx4 v6,s[32:35],s63 offen offset:2048 sc1 lds
v_fma_f32 v40,-v60,v28,v40
v_mfma_f32_16x16x32_bf16 v[44:47],a[24:27],v[108:111],v[44:47]
v_fma_f32 v41,-v60,v29,v41
v_fma_f32 v42,-v60,v30,v42
v_mfma_f32_16x16x32_bf16 v[44:47],a[36:39],v[112:115],v[44:47]
ds_read_b128 v[84:87],v5 offset:10368
v_fma_f32 v43,-v60,v31,v43
v_mfma_f32_16x16x32_bf16 v[48:51],a[40:43],v[112:115],0
v_permlane32_swap_b32 v40,v42
v_mfma_f32_16x16x32_bf16 v[48:51],a[28:31],v[108:111],v[48:51]
buffer_load_dwordx4 v6,s[32:35],s63 offen offset:3072 sc1 lds
v_fma_f32 v36,-v60,v24,v36
v_mfma_f32_16x16x32_bf16 v[48:51],a[16:19],v[104:107],v[48:51]
ds_read_b128 v[88:91],v5 offset:11520
v_fma_f32 v37,-v60,v25,v37
v_mfma_f32_16x16x32_bf16 v[48:51],a[4:7],v[100:103],v[48:51]
v_permlane32_swap_b32 v41,v43
v_mfma_f32_16x16x32_bf16 v[52:55],a[8:11],v[100:103],0
v_fma_f32 v38,-v60,v26,v38
v_fma_f32 v39,-v60,v27,v39
v_mfma_f32_16x16x32_bf16 v[52:55],a[20:23],v[104:107],v[52:55]
ds_read_b128 v[92:95],v5 offset:12672
v_fma_f32 v32,-v60,v20,v32
v_mfma_f32_16x16x32_bf16 v[52:55],a[32:35],v[108:111],v[52:55]
s_waitcnt vmcnt(8)
s_barrier
v_mfma_f32_16x16x32_bf16 v[52:55],a[44:47],v[112:115],v[52:55]
v_fma_f32 v33,-v60,v21,v33
s_waitcnt lgkmcnt(4)
v_mfma_f32_16x16x32_bf16 v[52:55],a[56:59],v[116:119],v[52:55]
ds_read_b128 v[96:99],v5 offset:13824
s_add_u32 m0,s64,8192
v_mfma_f32_16x16x32_bf16 v[52:55],a[68:71],v[120:123],v[52:55]
buffer_load_dwordx4 v7,s[32:35],s63 offen offset:0 sc1 lds
s_add_u32 s63,s63,s30
v_mfma_f32_16x16x32_bf16 v[52:55],a[80:83],v[124:127],v[52:55]
v_fma_f32 v34,-v60,v22,v34
v_fma_f32 v35,-v60,v23,v35
v_mfma_f32_16x16x32_bf16 v[52:55],a[92:95],v[76:79],v[52:55]
ds_read_b128 v[100:103],v5 offset:14976
v_mul_f32 v64,v32,v61
v_mfma_f32_16x16x32_bf16 v[48:51],a[88:91],v[76:79],v[48:51]
v_exp_f32 v64,v64
v_mfma_f32_16x16x32_bf16 v[48:51],a[76:79],v[124:127],v[48:51]
v_mul_f32 v65,v33,v61
v_mul_f32 v66,v34,v61
v_mfma_f32_16x16x32_bf16 v[48:51],a[64:67],v[120:123],v[48:51]
ds_read_b128 v[104:107],v5 offset:16128
v_mul_f32 v67,v35,v61
v_mfma_f32_16x16x32_bf16 v[48:51],a[52:55],v[116:119],v[48:51]
v_exp_f32 v65,v65
v_mfma_f32_16x16x32_bf16 v[44:47],a[48:51],v[116:119],v[44:47]
v_exp_f32 v66,v66
v_mfma_f32_16x16x32_bf16 v[44:47],a[60:63],v[120:123],v[44:47]
ds_read_b128 v[108:111],v5 offset:17280
v_mul_f32 v68,v40,v61
v_mfma_f32_16x16x32_bf16 v[44:47],a[72:75],v[124:127],v[44:47]
v_exp_f32 v67,v67
v_mfma_f32_16x16x32_bf16 v[44:47],a[84:87],v[76:79],v[44:47]
v_mul_f32 v69,v41,v61
s_waitcnt lgkmcnt(4)
v_mfma_f32_16x16x32_bf16 v[44:47],a[96:99],v[80:83],v[44:47]
ds_read_b128 v[112:115],v5 offset:18432
v_mul_f32 v32,v32,v36
v_mfma_f32_16x16x32_bf16 v[44:47],a[108:111],v[84:87],v[44:47]
v_exp_f32 v68,v68
v_mfma_f32_16x16x32_bf16 v[44:47],a[120:123],v[88:91],v[44:47]
v_exp_f32 v69,v69
v_mfma_f32_16x16x32_bf16 v[44:47],a[132:135],v[92:95],v[44:47]
ds_read_b128 v[116:119],v5 offset:19584
v_mul_f32 v33,v33,v37
v_mfma_f32_16x16x32_bf16 v[48:51],a[136:139],v[92:95],v[48:51]
v_mul_f32 v34,v34,v38
v_mul_f32 v35,v35,v39
v_mfma_f32_16x16x32_bf16 v[48:51],a[124:127],v[88:91],v[48:51]
v_mul_f32 v40,v40,v42
v_mul_f32 v41,v41,v43
v_mfma_f32_16x16x32_bf16 v[48:51],a[112:115],v[84:87],v[48:51]
ds_read_b128 v[120:123],v5 offset:20736
v_fma_f32 v64,v64,v62,v62
v_mfma_f32_16x16x32_bf16 v[48:51],a[100:103],v[80:83],v[48:51]
v_rcp_f32 v64,v64
v_mfma_f32_16x16x32_bf16 v[52:55],a[104:107],v[80:83],v[52:55]
v_fma_f32 v65,v65,v62,v62
v_fma_f32 v66,v66,v62,v62
v_mfma_f32_16x16x32_bf16 v[52:55],a[116:119],v[84:87],v[52:55]
ds_read_b128 v[124:127],v5 offset:21888
v_fma_f32 v67,v67,v62,v62
v_mfma_f32_16x16x32_bf16 v[52:55],a[128:131],v[88:91],v[52:55]
v_rcp_f32 v65,v65
v_mfma_f32_16x16x32_bf16 v[52:55],a[140:143],v[92:95],v[52:55]
v_fma_f32 v68,v68,v62,v62
s_waitcnt lgkmcnt(4)
v_mfma_f32_16x16x32_bf16 v[52:55],a[152:155],v[96:99],v[52:55]
ds_read_b128 v[76:79],v5 offset:23040
v_fma_f32 v69,v69,v62,v62
v_mfma_f32_16x16x32_bf16 v[52:55],a[164:167],v[100:103],v[52:55]
v_rcp_f32 v66,v66
v_mfma_f32_16x16x32_bf16 v[52:55],a[176:179],v[104:107],v[52:55]
v_rcp_f32 v67,v67
v_mfma_f32_16x16x32_bf16 v[52:55],a[188:191],v[108:111],v[52:55]
ds_read_b128 v[80:83],v5 offset:24192
v_mul_f32 v32,v32,v64
v_mfma_f32_16x16x32_bf16 v[48:51],a[184:187],v[108:111],v[48:51]
v_rcp_f32 v68,v68
v_mfma_f32_16x16x32_bf16 v[48:51],a[172:175],v[104:107],v[48:51]
v_rcp_f32 v69,v69
v_mfma_f32_16x16x32_bf16 v[48:51],a[160:163],v[100:103],v[48:51]
ds_read_b128 v[84:87],v5 offset:25344
v_mul_f32 v33,v33,v65
v_mfma_f32_16x16x32_bf16 v[48:51],a[148:151],v[96:99],v[48:51]
v_mul_f32 v34,v34,v66
v_mul_f32 v35,v35,v67
v_mfma_f32_16x16x32_bf16 v[44:47],a[144:147],v[96:99],v[44:47]
v_mul_f32 v40,v40,v68
v_mul_f32 v41,v41,v69
v_mfma_f32_16x16x32_bf16 v[44:47],a[156:159],v[100:103],v[44:47]
ds_read_b128 v[88:91],v5 offset:26496
v_cvt_pk_bf16_f32 v74,v40,v41
v_mfma_f32_16x16x32_bf16 v[44:47],a[168:171],v[104:107],v[44:47]
v_cvt_pk_bf16_f32 v72,v32,v33
v_cvt_pk_bf16_f32 v73,v34,v35
v_mfma_f32_16x16x32_bf16 v[44:47],a[180:183],v[108:111],v[44:47]
v_add_f32_dpp v56,v56,v56 row_ror:2 row_mask:0xf bank_mask:0xf
s_waitcnt lgkmcnt(4)
v_mfma_f32_16x16x32_bf16 v[44:47],a[192:195],v[112:115],v[44:47]
ds_read_b128 v[92:95],v5 offset:27648
v_mul_f32 v56,0x3a800000,v56
v_mfma_f32_16x16x32_bf16 v[44:47],a[204:207],v[116:119],v[44:47]
v_permlane32_swap_b32 v74,v75
v_mfma_f32_16x16x32_bf16 v[44:47],a[216:219],v[120:123],v[44:47]
v_mul_f32 v57,v56,v56
v_mfma_f32_16x16x32_bf16 v[44:47],a[228:231],v[124:127],v[44:47]
ds_read_b128 v[96:99],v5 offset:28800
v_sub_f32_dpp v57,v56,v57 quad_perm:[1,1,3,3] row_mask:0xf bank_mask:0xf
v_mfma_f32_16x16x32_bf16 v[48:51],a[232:235],v[124:127],v[48:51]
v_permlane16_swap_b32 v72,v74
v_mfma_f32_16x16x32_bf16 v[48:51],a[220:223],v[120:123],v[48:51]
v_permlane16_swap_b32 v73,v75
v_mfma_f32_16x16x32_bf16 v[48:51],a[208:211],v[116:119],v[48:51]
buffer_store_dwordx4 v[72:75],v15,s[32:35],s68 offen sc1
ds_read_b128 v[100:103],v5 offset:29952
v_mfma_f32_16x16x32_bf16 v[48:51],a[196:199],v[112:115],v[48:51]
v_max_f32 v57,0,v57
v_mfma_f32_16x16x32_bf16 v[52:55],a[200:203],v[112:115],v[52:55]
v_cvt_pk_bf16_f32 v57,0,v57
v_mfma_f32_16x16x32_bf16 v[52:55],a[212:215],v[116:119],v[52:55]
ds_read_b128 v[104:107],v5 offset:31104
v_add_f32 v57,0x3727c5ac,v57
v_mfma_f32_16x16x32_bf16 v[52:55],a[224:227],v[120:123],v[52:55]
v_cvt_pk_bf16_f32 v57,0,v57
v_mfma_f32_16x16x32_bf16 v[52:55],a[236:239],v[124:127],v[52:55]
s_waitcnt lgkmcnt(4)
v_mfma_f32_16x16x32_bf16 v[52:55],a[248:251],v[76:79],v[52:55]
ds_read_b128 v[108:111],v5 offset:32256
v_mfma_f32_16x16x32_bf16 v[52:55],v[132:135],v[80:83],v[52:55]
v_sqrt_f32 v57,v57
v_mfma_f32_16x16x32_bf16 v[52:55],v[144:147],v[84:87],v[52:55]
v_cvt_pk_bf16_f32 v57,v56,v57
v_mfma_f32_16x16x32_bf16 v[52:55],v[156:159],v[88:91],v[52:55]
ds_read_b128 v[112:115],v5 offset:33408
ds_write_b32 v10,v57 offset:128
v_mfma_f32_16x16x32_bf16 v[48:51],v[152:155],v[88:91],v[48:51]
v_mfma_f32_16x16x32_bf16 v[48:51],v[140:143],v[84:87],v[48:51]
v_mfma_f32_16x16x32_bf16 v[48:51],v[128:131],v[80:83],v[48:51]
ds_read_b128 v[116:119],v5 offset:34560
v_mfma_f32_16x16x32_bf16 v[48:51],a[244:247],v[76:79],v[48:51]
v_mfma_f32_16x16x32_bf16 v[44:47],a[240:243],v[76:79],v[44:47]
v_mfma_f32_16x16x32_bf16 v[44:47],a[252:255],v[80:83],v[44:47]
ds_read_b128 v[120:123],v5 offset:35712
v_mfma_f32_16x16x32_bf16 v[44:47],v[136:139],v[84:87],v[44:47]
s_waitcnt vmcnt(1)
s_waitcnt lgkmcnt(2)
s_barrier
v_mfma_f32_16x16x32_bf16 v[44:47],v[148:151],v[88:91],v[44:47]
v_xor_b32 v5,73728,v5
v_mfma_f32_16x16x32_bf16 v[44:47],v[160:163],v[92:95],v[44:47]
ds_read_b128 v[76:79],v4 offset:0
s_add_u32 m0,s64,36864
v_mfma_f32_16x16x32_bf16 v[44:47],v[172:175],v[96:99],v[44:47]
buffer_load_dwordx4 v2,s[32:35],s63 offen offset:0 sc1 lds
v_mfma_f32_16x16x32_bf16 v[44:47],v[184:187],v[100:103],v[44:47]
ds_read_b32 v62,v11 offset:128
v_mfma_f32_16x16x32_bf16 v[44:47],v[196:199],v[104:107],v[44:47]
ds_read_b128 v[80:83],v4 offset:1152
v_mfma_f32_16x16x32_bf16 v[48:51],v[200:203],v[104:107],v[48:51]
v_mfma_f32_16x16x32_bf16 v[48:51],v[188:191],v[100:103],v[48:51]
buffer_load_dwordx4 v2,s[32:35],s63 offen offset:1024 sc1 lds
v_mfma_f32_16x16x32_bf16 v[48:51],v[176:179],v[96:99],v[48:51]
ds_read_b128 v[84:87],v4 offset:2304
ds_read2st64_b32 v[56:57],v8 offset0:0 offset1:36
v_mfma_f32_16x16x32_bf16 v[48:51],v[164:167],v[92:95],v[48:51]
ds_read2st64_b32 v[58:59],v8 offset0:72 offset1:108
v_mfma_f32_16x16x32_bf16 v[52:55],v[168:171],v[92:95],v[52:55]
s_waitcnt lgkmcnt(4)
v_lshlrev_b32 v60,16,v62
v_mfma_f32_16x16x32_bf16 v[52:55],v[180:183],v[96:99],v[52:55]
buffer_load_dwordx4 v2,s[32:35],s63 offen offset:2048 sc1 lds
ds_read_b128 v[88:91],v4 offset:3456
v_mfma_f32_16x16x32_bf16 v[52:55],v[192:195],v[100:103],v[52:55]
v_and_b32 v62,0xffff0000,v62
v_mfma_f32_16x16x32_bf16 v[52:55],v[204:207],v[104:107],v[52:55]
v_mfma_f32_16x16x32_bf16 v[52:55],v[216:219],v[108:111],v[52:55]
ds_read_b128 v[92:95],v4 offset:4608
v_mfma_f32_16x16x32_bf16 v[52:55],v[228:231],v[112:115],v[52:55]
buffer_load_dwordx4 v2,s[32:35],s63 offen offset:3072 sc1 lds
v_mfma_f32_16x16x32_bf16 v[52:55],v[240:243],v[116:119],v[52:55]
v_rcp_f32 v61,v62
v_mfma_f32_16x16x32_bf16 v[52:55],v[252:255],v[120:123],v[52:55]
ds_read_b128 v[96:99],v4 offset:5760
v_mul_f32 v62,v62,v62
v_mfma_f32_16x16x32_bf16 v[48:51],v[248:251],v[120:123],v[48:51]
s_add_u32 m0,s64,40960
s_waitcnt lgkmcnt(3)
v_add_f32 v56,v56,v57
v_mfma_f32_16x16x32_bf16 v[48:51],v[236:239],v[116:119],v[48:51]
buffer_load_dwordx4 v6,s[32:35],s63 offen offset:0 sc1 lds
v_add_f32 v58,v58,v59
v_mfma_f32_16x16x32_bf16 v[48:51],v[224:227],v[112:115],v[48:51]
ds_read_b128 v[100:103],v4 offset:6912
v_add_f32 v56,v56,v58
v_mfma_f32_16x16x32_bf16 v[48:51],v[212:215],v[108:111],v[48:51]
v_mul_f32 v61,0xbfb8aa3b,v61
v_add_f32_dpp v56,v56,v56 row_ror:8 row_mask:0xf bank_mask:0xf
v_mfma_f32_16x16x32_bf16 v[44:47],v[208:211],v[108:111],v[44:47]
v_mfma_f32_16x16x32_bf16 v[44:47],v[220:223],v[112:115],v[44:47]
buffer_load_dwordx4 v6,s[32:35],s63 offen offset:1024 sc1 lds
ds_read_b128 v[104:107],v4 offset:8064
v_mfma_f32_16x16x32_bf16 v[44:47],v[232:235],v[116:119],v[44:47]
v_add_f32_dpp v56,v56,v56 row_ror:4 row_mask:0xf bank_mask:0xf
s_sub_u32 s62,s62,1
v_mfma_f32_16x16x32_bf16 v[44:47],v[244:247],v[120:123],v[44:47]
s_cbranch_scc0 .L_gl_1
s_nop 1
s_add_u32 s68,s68,s30
s_waitcnt vmcnt(6)
s_barrier
buffer_store_dwordx2 v[16:17],v2,s[92:95],s68 idxen sc1
v_fma_f32 v52,-v60,v28,v52
v_fma_f32 v53,-v60,v29,v53
v_fma_f32 v54,-v60,v30,v54
v_fma_f32 v55,-v60,v31,v55
v_fma_f32 v48,-v60,v24,v48
v_fma_f32 v49,-v60,v25,v49
v_fma_f32 v50,-v60,v26,v50
v_fma_f32 v51,-v60,v27,v51
v_fma_f32 v44,-v60,v20,v44
v_fma_f32 v45,-v60,v21,v45
v_fma_f32 v46,-v60,v22,v46
v_fma_f32 v47,-v60,v23,v47
v_permlane32_swap_b32 v52,v54
v_permlane32_swap_b32 v53,v55
v_mul_f32 v64,v44,v61
v_mul_f32 v65,v45,v61
v_mul_f32 v66,v46,v61
v_mul_f32 v67,v47,v61
v_mul_f32 v68,v52,v61
v_mul_f32 v69,v53,v61
v_exp_f32 v64,v64
v_exp_f32 v65,v65
v_mul_f32 v44,v44,v48
v_mul_f32 v45,v45,v49
v_exp_f32 v66,v66
v_exp_f32 v67,v67
v_mul_f32 v46,v46,v50
v_mul_f32 v47,v47,v51
v_exp_f32 v68,v68
v_exp_f32 v69,v69
v_mul_f32 v52,v52,v54
v_mul_f32 v53,v53,v55
v_fma_f32 v64,v64,v62,v62
v_fma_f32 v65,v65,v62,v62
v_rcp_f32 v64,v64
v_rcp_f32 v65,v65
v_fma_f32 v66,v66,v62,v62
v_fma_f32 v67,v67,v62,v62
v_rcp_f32 v66,v66
v_rcp_f32 v67,v67
v_fma_f32 v68,v68,v62,v62
v_fma_f32 v69,v69,v62,v62
v_rcp_f32 v68,v68
v_rcp_f32 v69,v69
v_mul_f32 v44,v44,v64
v_mul_f32 v45,v45,v65
v_mul_f32 v46,v46,v66
v_mul_f32 v47,v47,v67
v_mul_f32 v52,v52,v68
v_mul_f32 v53,v53,v69
v_cvt_pk_bf16_f32 v74,v52,v53
v_cvt_pk_bf16_f32 v72,v44,v45
v_cvt_pk_bf16_f32 v73,v46,v47
v_permlane32_swap_b32 v74,v75
s_nop 1
v_permlane16_swap_b32 v72,v74
v_permlane16_swap_b32 v73,v75
buffer_store_dwordx4 v[72:75],v15,s[32:35],s68 offen sc1
s_add_u32 s68,s68,s30
s_waitcnt vmcnt(0)
s_barrier
buffer_store_dwordx2 v[16:17],v2,s[92:95],s68 idxen sc1
s_branch .L_end
.L_gt0_1:
s_nop 1
s_add_u32 s68,s68,s30
s_waitcnt vmcnt(6)
s_barrier
buffer_store_dwordx2 v[16:17],v2,s[92:95],s68 idxen sc1
v_fma_f32 v40,-v60,v28,v40
v_fma_f32 v41,-v60,v29,v41
v_fma_f32 v42,-v60,v30,v42
v_fma_f32 v43,-v60,v31,v43
v_fma_f32 v36,-v60,v24,v36
v_fma_f32 v37,-v60,v25,v37
v_fma_f32 v38,-v60,v26,v38
v_fma_f32 v39,-v60,v27,v39
v_fma_f32 v32,-v60,v20,v32
v_fma_f32 v33,-v60,v21,v33
v_fma_f32 v34,-v60,v22,v34
v_fma_f32 v35,-v60,v23,v35
v_permlane32_swap_b32 v40,v42
v_permlane32_swap_b32 v41,v43
v_mul_f32 v64,v32,v61
v_mul_f32 v65,v33,v61
v_mul_f32 v66,v34,v61
v_mul_f32 v67,v35,v61
v_mul_f32 v68,v40,v61
v_mul_f32 v69,v41,v61
v_exp_f32 v64,v64
v_exp_f32 v65,v65
v_mul_f32 v32,v32,v36
v_mul_f32 v33,v33,v37
v_exp_f32 v66,v66
v_exp_f32 v67,v67
v_mul_f32 v34,v34,v38
v_mul_f32 v35,v35,v39
v_exp_f32 v68,v68
v_exp_f32 v69,v69
v_mul_f32 v40,v40,v42
v_mul_f32 v41,v41,v43
v_fma_f32 v64,v64,v62,v62
v_fma_f32 v65,v65,v62,v62
v_rcp_f32 v64,v64
v_rcp_f32 v65,v65
v_fma_f32 v66,v66,v62,v62
v_fma_f32 v67,v67,v62,v62
v_rcp_f32 v66,v66
v_rcp_f32 v67,v67
v_fma_f32 v68,v68,v62,v62
v_fma_f32 v69,v69,v62,v62
v_rcp_f32 v68,v68
v_rcp_f32 v69,v69
v_mul_f32 v32,v32,v64
v_mul_f32 v33,v33,v65
v_mul_f32 v34,v34,v66
v_mul_f32 v35,v35,v67
v_mul_f32 v40,v40,v68
v_mul_f32 v41,v41,v69
v_cvt_pk_bf16_f32 v74,v40,v41
v_cvt_pk_bf16_f32 v72,v32,v33
v_cvt_pk_bf16_f32 v73,v34,v35
v_permlane32_swap_b32 v74,v75
s_nop 1
v_permlane16_swap_b32 v72,v74
v_permlane16_swap_b32 v73,v75
buffer_store_dwordx4 v[72:75],v15,s[32:35],s68 offen sc1
s_add_u32 s68,s68,s30
s_waitcnt vmcnt(0)
s_barrier
buffer_store_dwordx2 v[16:17],v2,s[92:95],s68 idxen sc1
s_branch .L_end
.p2align 6
.L_gv2:
.L_gl_2:
s_waitcnt lgkmcnt(4)
v_mfma_f32_16x16x32_bf16 v[32:35],a[0:3],v[76:79],0
ds_read_b128 v[108:111],v4 offset:9216
s_add_u32 s68,s68,s30
v_mfma_f32_16x16x32_bf16 v[32:35],a[12:15],v[80:83],v[32:35]
buffer_load_dwordx4 v6,s[32:35],s63 offen offset:2048 sc1 lds
v_fma_f32 v52,-v60,v28,v52
v_mfma_f32_16x16x32_bf16 v[32:35],a[24:27],v[84:87],v[32:35]
v_fma_f32 v53,-v60,v29,v53
v_fma_f32 v54,-v60,v30,v54
v_mfma_f32_16x16x32_bf16 v[32:35],a[36:39],v[88:91],v[32:35]
ds_read_b128 v[112:115],v4 offset:10368
v_fma_f32 v55,-v60,v31,v55
v_mfma_f32_16x16x32_bf16 v[36:39],a[40:43],v[88:91],0
v_permlane32_swap_b32 v52,v54
v_mfma_f32_16x16x32_bf16 v[36:39],a[28:31],v[84:87],v[36:39]
buffer_load_dwordx4 v6,s[32:35],s63 offen offset:3072 sc1 lds
v_fma_f32 v48,-v60,v24,v48
v_mfma_f32_16x16x32_bf16 v[36:39],a[16:19],v[80:83],v[36:39]
ds_read_b128 v[116:119],v4 offset:11520
v_fma_f32 v49,-v60,v25,v49
v_mfma_f32_16x16x32_bf16 v[36:39],a[4:7],v[76:79],v[36:39]
v_permlane32_swap_b32 v53,v55
v_mfma_f32_16x16x32_bf16 v[40:43],a[8:11],v[76:79],0
v_fma_f32 v50,-v60,v26,v50
v_fma_f32 v51,-v60,v27,v51
v_mfma_f32_16x16x32_bf16 v[40:43],a[20:23],v[80:83],v[40:43]
s_waitcnt vmcnt(8)
s_barrier
v_mfma_f32_16x16x32_bf16 v[40:43],a[32:35],v[84:87],v[40:43]
ds_read_b128 v[120:123],v4 offset:12672
v_fma_f32 v44,-v60,v20,v44
v_mfma_f32_16x16x32_bf16 v[40:43],a[44:47],v[88:91],v[40:43]
v_fma_f32 v45,-v60,v21,v45
s_waitcnt lgkmcnt(4)
v_mfma_f32_16x16x32_bf16 v[40:43],a[56:59],v[92:95],v[40:43]
ds_read_b128 v[124:127],v4 offset:13824
s_add_u32 m0,s64,45056
v_mfma_f32_16x16x32_bf16 v[40:43],a[68:71],v[96:99],v[40:43]
buffer_load_dwordx4 v7,s[32:35],s63 offen offset:0 sc1 lds
s_add_u32 s63,s63,s30
v_mfma_f32_16x16x32_bf16 v[40:43],a[80:83],v[100:103],v[40:43]
s_xor_b32 s64,s64,s65
v_fma_f32 v46,-v60,v22,v46
v_mfma_f32_16x16x32_bf16 v[40:43],a[92:95],v[104:107],v[40:43]
ds_read_b128 v[76:79],v4 offset:14976
v_fma_f32 v47,-v60,v23,v47
v_mfma_f32_16x16x32_bf16 v[36:39],a[88:91],v[104:107],v[36:39]
v_mul_f32 v64,v44,v61
v_mul_f32 v65,v45,v61
v_mfma_f32_16x16x32_bf16 v[36:39],a[76:79],v[100:103],v[36:39]
v_exp_f32 v64,v64
v_mfma_f32_16x16x32_bf16 v[36:39],a[64:67],v[96:99],v[36:39]
ds_read_b128 v[80:83],v4 offset:16128
v_mul_f32 v66,v46,v61
v_mfma_f32_16x16x32_bf16 v[36:39],a[52:55],v[92:95],v[36:39]
v_exp_f32 v65,v65
v_mfma_f32_16x16x32_bf16 v[32:35],a[48:51],v[92:95],v[32:35]
)#"
R"#(
v_exp_f32 v66,v66
v_mfma_f32_16x16x32_bf16 v[32:35],a[60:63],v[96:99],v[32:35]
ds_read_b128 v[84:87],v4 offset:17280
v_mul_f32 v67,v47,v61
v_mfma_f32_16x16x32_bf16 v[32:35],a[72:75],v[100:103],v[32:35]
v_exp_f32 v67,v67
v_mfma_f32_16x16x32_bf16 v[32:35],a[84:87],v[104:107],v[32:35]
v_mul_f32 v68,v52,v61
s_waitcnt lgkmcnt(4)
v_mfma_f32_16x16x32_bf16 v[32:35],a[96:99],v[108:111],v[32:35]
ds_read_b128 v[88:91],v4 offset:18432
v_mul_f32 v69,v53,v61
v_mfma_f32_16x16x32_bf16 v[32:35],a[108:111],v[112:115],v[32:35]
v_exp_f32 v68,v68
v_mfma_f32_16x16x32_bf16 v[32:35],a[120:123],v[116:119],v[32:35]
v_exp_f32 v69,v69
v_mfma_f32_16x16x32_bf16 v[32:35],a[132:135],v[120:123],v[32:35]
ds_read_b128 v[92:95],v4 offset:19584
v_mul_f32 v44,v44,v48
v_mfma_f32_16x16x32_bf16 v[36:39],a[136:139],v[120:123],v[36:39]
v_mul_f32 v45,v45,v49
v_mul_f32 v46,v46,v50
v_mfma_f32_16x16x32_bf16 v[36:39],a[124:127],v[116:119],v[36:39]
v_mul_f32 v47,v47,v51
v_mul_f32 v52,v52,v54
v_mfma_f32_16x16x32_bf16 v[36:39],a[112:115],v[112:115],v[36:39]
ds_read_b128 v[96:99],v4 offset:20736
v_mul_f32 v53,v53,v55
v_mfma_f32_16x16x32_bf16 v[36:39],a[100:103],v[108:111],v[36:39]
v_fma_f32 v64,v64,v62,v62
v_fma_f32 v65,v65,v62,v62
v_mfma_f32_16x16x32_bf16 v[40:43],a[104:107],v[108:111],v[40:43]
v_rcp_f32 v64,v64
v_mfma_f32_16x16x32_bf16 v[40:43],a[116:119],v[112:115],v[40:43]
ds_read_b128 v[100:103],v4 offset:21888
v_fma_f32 v66,v66,v62,v62
v_mfma_f32_16x16x32_bf16 v[40:43],a[128:131],v[116:119],v[40:43]
v_rcp_f32 v65,v65
v_mfma_f32_16x16x32_bf16 v[40:43],a[140:143],v[120:123],v[40:43]
v_fma_f32 v67,v67,v62,v62
s_waitcnt lgkmcnt(4)
v_mfma_f32_16x16x32_bf16 v[40:43],a[152:155],v[124:127],v[40:43]
ds_read_b128 v[104:107],v4 offset:23040
v_fma_f32 v68,v68,v62,v62
v_mfma_f32_16x16x32_bf16 v[40:43],a[164:167],v[76:79],v[40:43]
v_rcp_f32 v66,v66
v_mfma_f32_16x16x32_bf16 v[40:43],a[176:179],v[80:83],v[40:43]
v_rcp_f32 v67,v67
v_mfma_f32_16x16x32_bf16 v[40:43],a[188:191],v[84:87],v[40:43]
ds_read_b128 v[108:111],v4 offset:24192
v_fma_f32 v69,v69,v62,v62
v_mfma_f32_16x16x32_bf16 v[36:39],a[184:187],v[84:87],v[36:39]
v_rcp_f32 v68,v68
v_mfma_f32_16x16x32_bf16 v[36:39],a[172:175],v[80:83],v[36:39]
v_rcp_f32 v69,v69
v_mfma_f32_16x16x32_bf16 v[36:39],a[160:163],v[76:79],v[36:39]
ds_read_b128 v[112:115],v4 offset:25344
v_mul_f32 v44,v44,v64
v_mfma_f32_16x16x32_bf16 v[36:39],a[148:151],v[124:127],v[36:39]
v_mul_f32 v45,v45,v65
v_mul_f32 v46,v46,v66
v_mfma_f32_16x16x32_bf16 v[32:35],a[144:147],v[124:127],v[32:35]
v_mul_f32 v47,v47,v67
v_mul_f32 v52,v52,v68
v_mfma_f32_16x16x32_bf16 v[32:35],a[156:159],v[76:79],v[32:35]
ds_read_b128 v[116:119],v4 offset:26496
v_mul_f32 v53,v53,v69
v_mfma_f32_16x16x32_bf16 v[32:35],a[168:171],v[80:83],v[32:35]
v_cvt_pk_bf16_f32 v74,v52,v53
v_cvt_pk_bf16_f32 v72,v44,v45
v_mfma_f32_16x16x32_bf16 v[32:35],a[180:183],v[84:87],v[32:35]
v_cvt_pk_bf16_f32 v73,v46,v47
s_waitcnt lgkmcnt(4)
v_mfma_f32_16x16x32_bf16 v[32:35],a[192:195],v[88:91],v[32:35]
ds_read_b128 v[120:123],v4 offset:27648
v_add_f32_dpp v56,v56,v56 row_ror:2 row_mask:0xf bank_mask:0xf
v_mfma_f32_16x16x32_bf16 v[32:35],a[204:207],v[92:95],v[32:35]
v_permlane32_swap_b32 v74,v75
v_mfma_f32_16x16x32_bf16 v[32:35],a[216:219],v[96:99],v[32:35]
v_mul_f32 v56,0x3a800000,v56
v_mfma_f32_16x16x32_bf16 v[32:35],a[228:231],v[100:103],v[32:35]
ds_read_b128 v[124:127],v4 offset:28800
v_mul_f32 v57,v56,v56
v_mfma_f32_16x16x32_bf16 v[36:39],a[232:235],v[100:103],v[36:39]
v_permlane16_swap_b32 v72,v74
v_mfma_f32_16x16x32_bf16 v[36:39],a[220:223],v[96:99],v[36:39]
v_permlane16_swap_b32 v73,v75
v_mfma_f32_16x16x32_bf16 v[36:39],a[208:211],v[92:95],v[36:39]
buffer_store_dwordx4 v[72:75],v15,s[32:35],s68 offen sc1
ds_read_b128 v[76:79],v4 offset:29952
v_mfma_f32_16x16x32_bf16 v[36:39],a[196:199],v[88:91],v[36:39]
v_sub_f32_dpp v57,v56,v57 quad_perm:[1,1,3,3] row_mask:0xf bank_mask:0xf
v_mfma_f32_16x16x32_bf16 v[40:43],a[200:203],v[88:91],v[40:43]
v_max_f32 v57,0,v57
v_mfma_f32_16x16x32_bf16 v[40:43],a[212:215],v[92:95],v[40:43]
ds_read_b128 v[80:83],v4 offset:31104
v_cvt_pk_bf16_f32 v57,0,v57
v_mfma_f32_16x16x32_bf16 v[40:43],a[224:227],v[96:99],v[40:43]
v_add_f32 v57,0x3727c5ac,v57
v_mfma_f32_16x16x32_bf16 v[40:43],a[236:239],v[100:103],v[40:43]
v_cvt_pk_bf16_f32 v57,0,v57
s_waitcnt lgkmcnt(4)
v_mfma_f32_16x16x32_bf16 v[40:43],a[248:251],v[104:107],v[40:43]
ds_read_b128 v[84:87],v4 offset:32256
v_mfma_f32_16x16x32_bf16 v[40:43],v[132:135],v[108:111],v[40:43]
v_sqrt_f32 v57,v57
v_mfma_f32_16x16x32_bf16 v[40:43],v[144:147],v[112:115],v[40:43]
v_cvt_pk_bf16_f32 v57,v56,v57
v_mfma_f32_16x16x32_bf16 v[40:43],v[156:159],v[116:119],v[40:43]
ds_read_b128 v[88:91],v4 offset:33408
ds_write_b32 v10,v57 offset:0
v_mfma_f32_16x16x32_bf16 v[36:39],v[152:155],v[116:119],v[36:39]
v_mfma_f32_16x16x32_bf16 v[36:39],v[140:143],v[112:115],v[36:39]
v_mfma_f32_16x16x32_bf16 v[36:39],v[128:131],v[108:111],v[36:39]
ds_read_b128 v[92:95],v4 offset:34560
v_mfma_f32_16x16x32_bf16 v[36:39],a[244:247],v[104:107],v[36:39]
v_mfma_f32_16x16x32_bf16 v[32:35],a[240:243],v[104:107],v[32:35]
v_mfma_f32_16x16x32_bf16 v[32:35],a[252:255],v[108:111],v[32:35]
s_waitcnt vmcnt(1)
s_waitcnt lgkmcnt(1)
s_barrier
v_mfma_f32_16x16x32_bf16 v[32:35],v[136:139],v[112:115],v[32:35]
ds_read_b128 v[96:99],v4 offset:35712
v_xor_b32 v4,73728,v4
v_mfma_f32_16x16x32_bf16 v[32:35],v[148:151],v[116:119],v[32:35]
v_mfma_f32_16x16x32_bf16 v[32:35],v[160:163],v[120:123],v[32:35]
ds_read_b128 v[100:103],v5 offset:0
s_add_u32 m0,s64,0
v_mfma_f32_16x16x32_bf16 v[32:35],v[172:175],v[124:127],v[32:35]
buffer_load_dwordx4 v2,s[32:35],s63 offen offset:0 sc1 lds
v_mfma_f32_16x16x32_bf16 v[32:35],v[184:187],v[76:79],v[32:35]
ds_read_b32 v62,v11 offset:0
v_mfma_f32_16x16x32_bf16 v[32:35],v[196:199],v[80:83],v[32:35]
ds_read_b128 v[104:107],v5 offset:1152
v_mfma_f32_16x16x32_bf16 v[36:39],v[200:203],v[80:83],v[36:39]
v_mfma_f32_16x16x32_bf16 v[36:39],v[188:191],v[76:79],v[36:39]
buffer_load_dwordx4 v2,s[32:35],s63 offen offset:1024 sc1 lds
v_mfma_f32_16x16x32_bf16 v[36:39],v[176:179],v[124:127],v[36:39]
ds_read_b128 v[108:111],v5 offset:2304
ds_read2st64_b32 v[56:57],v8 offset0:144 offset1:180
v_mfma_f32_16x16x32_bf16 v[36:39],v[164:167],v[120:123],v[36:39]
ds_read2st64_b32 v[58:59],v8 offset0:216 offset1:252
v_xor_b32 v8,v9,v8
v_mfma_f32_16x16x32_bf16 v[40:43],v[168:171],v[120:123],v[40:43]
s_waitcnt lgkmcnt(4)
v_lshlrev_b32 v60,16,v62
v_mfma_f32_16x16x32_bf16 v[40:43],v[180:183],v[124:127],v[40:43]
buffer_load_dwordx4 v2,s[32:35],s63 offen offset:2048 sc1 lds
ds_read_b128 v[112:115],v5 offset:3456
v_mfma_f32_16x16x32_bf16 v[40:43],v[192:195],v[76:79],v[40:43]
v_and_b32 v62,0xffff0000,v62
v_mfma_f32_16x16x32_bf16 v[40:43],v[204:207],v[80:83],v[40:43]
v_mfma_f32_16x16x32_bf16 v[40:43],v[216:219],v[84:87],v[40:43]
ds_read_b128 v[116:119],v5 offset:4608
v_mfma_f32_16x16x32_bf16 v[40:43],v[228:231],v[88:91],v[40:43]
buffer_load_dwordx4 v2,s[32:35],s63 offen offset:3072 sc1 lds
v_mfma_f32_16x16x32_bf16 v[40:43],v[240:243],v[92:95],v[40:43]
v_rcp_f32 v61,v62
v_mfma_f32_16x16x32_bf16 v[40:43],v[252:255],v[96:99],v[40:43]
ds_read_b128 v[120:123],v5 offset:5760
v_mul_f32 v62,v62,v62
v_mfma_f32_16x16x32_bf16 v[36:39],v[248:251],v[96:99],v[36:39]
s_add_u32 m0,s64,4096
s_waitcnt lgkmcnt(3)
v_add_f32 v56,v56,v57
v_mfma_f32_16x16x32_bf16 v[36:39],v[236:239],v[92:95],v[36:39]
buffer_load_dwordx4 v6,s[32:35],s63 offen offset:0 sc1 lds
v_add_f32 v58,v58,v59
v_mfma_f32_16x16x32_bf16 v[36:39],v[224:227],v[88:91],v[36:39]
ds_read_b128 v[124:127],v5 offset:6912
v_add_f32 v56,v56,v58
v_mfma_f32_16x16x32_bf16 v[36:39],v[212:215],v[84:87],v[36:39]
v_mul_f32 v61,0xbfb8aa3b,v61
v_add_f32_dpp v56,v56,v56 row_ror:8 row_mask:0xf bank_mask:0xf
v_mfma_f32_16x16x32_bf16 v[32:35],v[208:211],v[84:87],v[32:35]
v_mfma_f32_16x16x32_bf16 v[32:35],v[220:223],v[88:91],v[32:35]
buffer_load_dwordx4 v6,s[32:35],s63 offen offset:1024 sc1 lds
ds_read_b128 v[76:79],v5 offset:8064
v_mfma_f32_16x16x32_bf16 v[32:35],v[232:235],v[92:95],v[32:35]
v_add_f32_dpp v56,v56,v56 row_ror:4 row_mask:0xf bank_mask:0xf
s_sub_u32 s62,s62,1
v_mfma_f32_16x16x32_bf16 v[32:35],v[244:247],v[96:99],v[32:35]
s_cbranch_scc1 .L_gt0_2
s_waitcnt lgkmcnt(4)
v_mfma_f32_16x16x32_bf16 v[44:47],a[0:3],v[100:103],0
ds_read_b128 v[80:83],v5 offset:9216
s_add_u32 s68,s68,s30
v_mfma_f32_16x16x32_bf16 v[44:47],a[12:15],v[104:107],v[44:47]
buffer_load_dwordx4 v6,s[32:35],s63 offen offset:2048 sc1 lds
v_fma_f32 v40,-v60,v28,v40
v_mfma_f32_16x16x32_bf16 v[44:47],a[24:27],v[108:111],v[44:47]
v_fma_f32 v41,-v60,v29,v41
v_fma_f32 v42,-v60,v30,v42
v_mfma_f32_16x16x32_bf16 v[44:47],a[36:39],v[112:115],v[44:47]
ds_read_b128 v[84:87],v5 offset:10368
v_fma_f32 v43,-v60,v31,v43
v_mfma_f32_16x16x32_bf16 v[48:51],a[40:43],v[112:115],0
v_permlane32_swap_b32 v40,v42
v_mfma_f32_16x16x32_bf16 v[48:51],a[28:31],v[108:111],v[48:51]
buffer_load_dwordx4 v6,s[32:35],s63 offen offset:3072 sc1 lds
v_fma_f32 v36,-v60,v24,v36
v_mfma_f32_16x16x32_bf16 v[48:51],a[16:19],v[104:107],v[48:51]
ds_read_b128 v[88:91],v5 offset:11520
v_fma_f32 v37,-v60,v25,v37
v_mfma_f32_16x16x32_bf16 v[48:51],a[4:7],v[100:103],v[48:51]
v_permlane32_swap_b32 v41,v43
v_mfma_f32_16x16x32_bf16 v[52:55],a[8:11],v[100:103],0
v_fma_f32 v38,-v60,v26,v38
v_fma_f32 v39,-v60,v27,v39
v_mfma_f32_16x16x32_bf16 v[52:55],a[20:23],v[104:107],v[52:55]
s_waitcnt vmcnt(8)
s_barrier
v_mfma_f32_16x16x32_bf16 v[52:55],a[32:35],v[108:111],v[52:55]
ds_read_b128 v[92:95],v5 offset:12672
v_fma_f32 v32,-v60,v20,v32
v_mfma_f32_16x16x32_bf16 v[52:55],a[44:47],v[112:115],v[52:55]
v_fma_f32 v33,-v60,v21,v33
s_waitcnt lgkmcnt(4)
v_mfma_f32_16x16x32_bf16 v[52:55],a[56:59],v[116:119],v[52:55]
ds_read_b128 v[96:99],v5 offset:13824
s_add_u32 m0,s64,8192
v_mfma_f32_16x16x32_bf16 v[52:55],a[68:71],v[120:123],v[52:55]
buffer_load_dwordx4 v7,s[32:35],s63 offen offset:0 sc1 lds
s_add_u32 s63,s63,s30
v_mfma_f32_16x16x32_bf16 v[52:55],a[80:83],v[124:127],v[52:55]
v_fma_f32 v34,-v60,v22,v34
v_fma_f32 v35,-v60,v23,v35
v_mfma_f32_16x16x32_bf16 v[52:55],a[92:95],v[76:79],v[52:55]
ds_read_b128 v[100:103],v5 offset:14976
v_mul_f32 v64,v32,v61
v_mfma_f32_16x16x32_bf16 v[48:51],a[88:91],v[76:79],v[48:51]
v_exp_f32 v64,v64
v_mfma_f32_16x16x32_bf16 v[48:51],a[76:79],v[124:127],v[48:51]
buffer_load_dwordx2 v[12:13],v14,s[0:3],s66 offen sc1
v_mul_f32 v65,v33,v61
v_mfma_f32_16x16x32_bf16 v[48:51],a[64:67],v[120:123],v[48:51]
ds_read_b128 v[104:107],v5 offset:16128
v_mul_f32 v66,v34,v61
v_mfma_f32_16x16x32_bf16 v[48:51],a[52:55],v[116:119],v[48:51]
v_exp_f32 v65,v65
v_mfma_f32_16x16x32_bf16 v[44:47],a[48:51],v[116:119],v[44:47]
v_exp_f32 v66,v66
v_mfma_f32_16x16x32_bf16 v[44:47],a[60:63],v[120:123],v[44:47]
ds_read_b128 v[108:111],v5 offset:17280
v_mul_f32 v67,v35,v61
v_mfma_f32_16x16x32_bf16 v[44:47],a[72:75],v[124:127],v[44:47]
v_exp_f32 v67,v67
v_mfma_f32_16x16x32_bf16 v[44:47],a[84:87],v[76:79],v[44:47]
v_mul_f32 v68,v40,v61
s_waitcnt lgkmcnt(4)
v_mfma_f32_16x16x32_bf16 v[44:47],a[96:99],v[80:83],v[44:47]
ds_read_b128 v[112:115],v5 offset:18432
v_mul_f32 v69,v41,v61
v_mfma_f32_16x16x32_bf16 v[44:47],a[108:111],v[84:87],v[44:47]
v_exp_f32 v68,v68
v_mfma_f32_16x16x32_bf16 v[44:47],a[120:123],v[88:91],v[44:47]
v_exp_f32 v69,v69
v_mfma_f32_16x16x32_bf16 v[44:47],a[132:135],v[92:95],v[44:47]
ds_read_b128 v[116:119],v5 offset:19584
v_mul_f32 v32,v32,v36
v_mfma_f32_16x16x32_bf16 v[48:51],a[136:139],v[92:95],v[48:51]
v_mul_f32 v33,v33,v37
v_mul_f32 v34,v34,v38
v_mfma_f32_16x16x32_bf16 v[48:51],a[124:127],v[88:91],v[48:51]
v_mul_f32 v35,v35,v39
v_mul_f32 v40,v40,v42
v_mfma_f32_16x16x32_bf16 v[48:51],a[112:115],v[84:87],v[48:51]
ds_read_b128 v[120:123],v5 offset:20736
v_mul_f32 v41,v41,v43
v_mfma_f32_16x16x32_bf16 v[48:51],a[100:103],v[80:83],v[48:51]
v_fma_f32 v64,v64,v62,v62
v_fma_f32 v65,v65,v62,v62
v_mfma_f32_16x16x32_bf16 v[52:55],a[104:107],v[80:83],v[52:55]
v_rcp_f32 v64,v64
v_mfma_f32_16x16x32_bf16 v[52:55],a[116:119],v[84:87],v[52:55]
ds_read_b128 v[124:127],v5 offset:21888
v_fma_f32 v66,v66,v62,v62
v_mfma_f32_16x16x32_bf16 v[52:55],a[128:131],v[88:91],v[52:55]
v_rcp_f32 v65,v65
v_mfma_f32_16x16x32_bf16 v[52:55],a[140:143],v[92:95],v[52:55]
v_fma_f32 v67,v67,v62,v62
s_waitcnt lgkmcnt(4)
v_mfma_f32_16x16x32_bf16 v[52:55],a[152:155],v[96:99],v[52:55]
ds_read_b128 v[76:79],v5 offset:23040
v_fma_f32 v68,v68,v62,v62
v_mfma_f32_16x16x32_bf16 v[52:55],a[164:167],v[100:103],v[52:55]
v_rcp_f32 v66,v66
v_mfma_f32_16x16x32_bf16 v[52:55],a[176:179],v[104:107],v[52:55]
v_rcp_f32 v67,v67
v_mfma_f32_16x16x32_bf16 v[52:55],a[188:191],v[108:111],v[52:55]
ds_read_b128 v[80:83],v5 offset:24192
v_fma_f32 v69,v69,v62,v62
v_mfma_f32_16x16x32_bf16 v[48:51],a[184:187],v[108:111],v[48:51]
v_rcp_f32 v68,v68
v_mfma_f32_16x16x32_bf16 v[48:51],a[172:175],v[104:107],v[48:51]
v_rcp_f32 v69,v69
v_mfma_f32_16x16x32_bf16 v[48:51],a[160:163],v[100:103],v[48:51]
ds_read_b128 v[84:87],v5 offset:25344
v_mul_f32 v32,v32,v64
v_mfma_f32_16x16x32_bf16 v[48:51],a[148:151],v[96:99],v[48:51]
v_mul_f32 v33,v33,v65
v_mul_f32 v34,v34,v66
v_mfma_f32_16x16x32_bf16 v[44:47],a[144:147],v[96:99],v[44:47]
v_mul_f32 v35,v35,v67
v_mul_f32 v40,v40,v68
v_mfma_f32_16x16x32_bf16 v[44:47],a[156:159],v[100:103],v[44:47]
ds_read_b128 v[88:91],v5 offset:26496
v_mul_f32 v41,v41,v69
v_mfma_f32_16x16x32_bf16 v[44:47],a[168:171],v[104:107],v[44:47]
v_cvt_pk_bf16_f32 v74,v40,v41
v_cvt_pk_bf16_f32 v72,v32,v33
v_mfma_f32_16x16x32_bf16 v[44:47],a[180:183],v[108:111],v[44:47]
v_cvt_pk_bf16_f32 v73,v34,v35
s_waitcnt lgkmcnt(4)
v_mfma_f32_16x16x32_bf16 v[44:47],a[192:195],v[112:115],v[44:47]
ds_read_b128 v[92:95],v5 offset:27648
v_add_f32_dpp v56,v56,v56 row_ror:2 row_mask:0xf bank_mask:0xf
v_mfma_f32_16x16x32_bf16 v[44:47],a[204:207],v[116:119],v[44:47]
v_permlane32_swap_b32 v74,v75
v_mfma_f32_16x16x32_bf16 v[44:47],a[216:219],v[120:123],v[44:47]
v_mul_f32 v56,0x3a800000,v56
v_mfma_f32_16x16x32_bf16 v[44:47],a[228:231],v[124:127],v[44:47]
ds_read_b128 v[96:99],v5 offset:28800
v_mul_f32 v57,v56,v56
v_mfma_f32_16x16x32_bf16 v[48:51],a[232:235],v[124:127],v[48:51]
v_permlane16_swap_b32 v72,v74
v_mfma_f32_16x16x32_bf16 v[48:51],a[220:223],v[120:123],v[48:51]
v_permlane16_swap_b32 v73,v75
v_mfma_f32_16x16x32_bf16 v[48:51],a[208:211],v[116:119],v[48:51]
buffer_store_dwordx4 v[72:75],v15,s[32:35],s68 offen sc1
ds_read_b128 v[100:103],v5 offset:29952
v_mfma_f32_16x16x32_bf16 v[48:51],a[196:199],v[112:115],v[48:51]
v_sub_f32_dpp v57,v56,v57 quad_perm:[1,1,3,3] row_mask:0xf bank_mask:0xf
v_mfma_f32_16x16x32_bf16 v[52:55],a[200:203],v[112:115],v[52:55]
v_max_f32 v57,0,v57
v_mfma_f32_16x16x32_bf16 v[52:55],a[212:215],v[116:119],v[52:55]
ds_read_b128 v[104:107],v5 offset:31104
v_cvt_pk_bf16_f32 v57,0,v57
v_mfma_f32_16x16x32_bf16 v[52:55],a[224:227],v[120:123],v[52:55]
v_add_f32 v57,0x3727c5ac,v57
v_mfma_f32_16x16x32_bf16 v[52:55],a[236:239],v[124:127],v[52:55]
v_cvt_pk_bf16_f32 v57,0,v57
s_waitcnt lgkmcnt(4)
v_mfma_f32_16x16x32_bf16 v[52:55],a[248:251],v[76:79],v[52:55]
ds_read_b128 v[108:111],v5 offset:32256
v_mfma_f32_16x16x32_bf16 v[52:55],v[132:135],v[80:83],v[52:55]
v_sqrt_f32 v57,v57
v_mfma_f32_16x16x32_bf16 v[52:55],v[144:147],v[84:87],v[52:55]
v_cvt_pk_bf16_f32 v57,v56,v57
v_mfma_f32_16x16x32_bf16 v[52:55],v[156:159],v[88:91],v[52:55]
ds_read_b128 v[112:115],v5 offset:33408
ds_write_b32 v10,v57 offset:128
v_mfma_f32_16x16x32_bf16 v[48:51],v[152:155],v[88:91],v[48:51]
v_mfma_f32_16x16x32_bf16 v[48:51],v[140:143],v[84:87],v[48:51]
s_waitcnt vmcnt(1)
v_cmp_ne_u64 vcc,v[16:17],v[12:13]
v_mfma_f32_16x16x32_bf16 v[48:51],v[128:131],v[80:83],v[48:51]
ds_read_b128 v[116:119],v5 offset:34560
v_mfma_f32_16x16x32_bf16 v[48:51],a[244:247],v[76:79],v[48:51]
s_cbranch_vccnz .L_rtg1_2
.L_bkg1_2:
s_add_u32 s66,s66,s31
v_mfma_f32_16x16x32_bf16 v[44:47],a[240:243],v[76:79],v[44:47]
v_mfma_f32_16x16x32_bf16 v[44:47],a[252:255],v[80:83],v[44:47]
ds_read_b128 v[120:123],v5 offset:35712
s_waitcnt lgkmcnt(2)
s_barrier
v_mfma_f32_16x16x32_bf16 v[44:47],v[136:139],v[84:87],v[44:47]
v_xor_b32 v5,73728,v5
v_mfma_f32_16x16x32_bf16 v[44:47],v[148:151],v[88:91],v[44:47]
v_mfma_f32_16x16x32_bf16 v[44:47],v[160:163],v[92:95],v[44:47]
ds_read_b128 v[76:79],v4 offset:0
s_add_u32 m0,s64,36864
v_mfma_f32_16x16x32_bf16 v[44:47],v[172:175],v[96:99],v[44:47]
buffer_load_dwordx4 v2,s[32:35],s63 offen offset:0 sc1 lds
v_mfma_f32_16x16x32_bf16 v[44:47],v[184:187],v[100:103],v[44:47]
ds_read_b32 v62,v11 offset:128
v_mfma_f32_16x16x32_bf16 v[44:47],v[196:199],v[104:107],v[44:47]
ds_read_b128 v[80:83],v4 offset:1152
v_mfma_f32_16x16x32_bf16 v[48:51],v[200:203],v[104:107],v[48:51]
v_mfma_f32_16x16x32_bf16 v[48:51],v[188:191],v[100:103],v[48:51]
buffer_load_dwordx4 v2,s[32:35],s63 offen offset:1024 sc1 lds
v_mfma_f32_16x16x32_bf16 v[48:51],v[176:179],v[96:99],v[48:51]
ds_read_b128 v[84:87],v4 offset:2304
ds_read2st64_b32 v[56:57],v8 offset0:0 offset1:36
v_mfma_f32_16x16x32_bf16 v[48:51],v[164:167],v[92:95],v[48:51]
ds_read2st64_b32 v[58:59],v8 offset0:72 offset1:108
v_mfma_f32_16x16x32_bf16 v[52:55],v[168:171],v[92:95],v[52:55]
s_waitcnt lgkmcnt(4)
v_lshlrev_b32 v60,16,v62
v_mfma_f32_16x16x32_bf16 v[52:55],v[180:183],v[96:99],v[52:55]
buffer_load_dwordx4 v2,s[32:35],s63 offen offset:2048 sc1 lds
ds_read_b128 v[88:91],v4 offset:3456
v_mfma_f32_16x16x32_bf16 v[52:55],v[192:195],v[100:103],v[52:55]
v_and_b32 v62,0xffff0000,v62
v_mfma_f32_16x16x32_bf16 v[52:55],v[204:207],v[104:107],v[52:55]
v_mfma_f32_16x16x32_bf16 v[52:55],v[216:219],v[108:111],v[52:55]
ds_read_b128 v[92:95],v4 offset:4608
v_mfma_f32_16x16x32_bf16 v[52:55],v[228:231],v[112:115],v[52:55]
buffer_load_dwordx4 v2,s[32:35],s63 offen offset:3072 sc1 lds
v_mfma_f32_16x16x32_bf16 v[52:55],v[240:243],v[116:119],v[52:55]
v_rcp_f32 v61,v62
v_mfma_f32_16x16x32_bf16 v[52:55],v[252:255],v[120:123],v[52:55]
ds_read_b128 v[96:99],v4 offset:5760
v_mul_f32 v62,v62,v62
v_mfma_f32_16x16x32_bf16 v[48:51],v[248:251],v[120:123],v[48:51]
s_add_u32 m0,s64,40960
s_waitcnt lgkmcnt(3)
v_add_f32 v56,v56,v57
v_mfma_f32_16x16x32_bf16 v[48:51],v[236:239],v[116:119],v[48:51]
buffer_load_dwordx4 v6,s[32:35],s63 offen offset:0 sc1 lds
v_add_f32 v58,v58,v59
v_mfma_f32_16x16x32_bf16 v[48:51],v[224:227],v[112:115],v[48:51]
ds_read_b128 v[100:103],v4 offset:6912
v_add_f32 v56,v56,v58
v_mfma_f32_16x16x32_bf16 v[48:51],v[212:215],v[108:111],v[48:51]
v_mul_f32 v61,0xbfb8aa3b,v61
v_add_f32_dpp v56,v56,v56 row_ror:8 row_mask:0xf bank_mask:0xf
v_mfma_f32_16x16x32_bf16 v[44:47],v[208:211],v[108:111],v[44:47]
v_mfma_f32_16x16x32_bf16 v[44:47],v[220:223],v[112:115],v[44:47]
buffer_load_dwordx4 v6,s[32:35],s63 offen offset:1024 sc1 lds
ds_read_b128 v[104:107],v4 offset:8064
v_mfma_f32_16x16x32_bf16 v[44:47],v[232:235],v[116:119],v[44:47]
v_add_f32_dpp v56,v56,v56 row_ror:4 row_mask:0xf bank_mask:0xf
s_sub_u32 s62,s62,1
v_mfma_f32_16x16x32_bf16 v[44:47],v[244:247],v[120:123],v[44:47]
s_cbranch_scc0 .L_gl_2
s_nop 1
s_add_u32 s68,s68,s30
s_waitcnt vmcnt(6)
s_barrier
buffer_store_dwordx2 v[16:17],v2,s[92:95],s68 idxen sc1
v_fma_f32 v52,-v60,v28,v52
v_fma_f32 v53,-v60,v29,v53
v_fma_f32 v54,-v60,v30,v54
v_fma_f32 v55,-v60,v31,v55
v_fma_f32 v48,-v60,v24,v48
v_fma_f32 v49,-v60,v25,v49
v_fma_f32 v50,-v60,v26,v50
v_fma_f32 v51,-v60,v27,v51
v_fma_f32 v44,-v60,v20,v44
v_fma_f32 v45,-v60,v21,v45
v_fma_f32 v46,-v60,v22,v46
v_fma_f32 v47,-v60,v23,v47
v_permlane32_swap_b32 v52,v54
v_permlane32_swap_b32 v53,v55
v_mul_f32 v64,v44,v61
v_mul_f32 v65,v45,v61
v_mul_f32 v66,v46,v61
v_mul_f32 v67,v47,v61
v_mul_f32 v68,v52,v61
v_mul_f32 v69,v53,v61
v_exp_f32 v64,v64
v_exp_f32 v65,v65
v_mul_f32 v44,v44,v48
v_mul_f32 v45,v45,v49
v_exp_f32 v66,v66
v_exp_f32 v67,v67
v_mul_f32 v46,v46,v50
v_mul_f32 v47,v47,v51
v_exp_f32 v68,v68
v_exp_f32 v69,v69
v_mul_f32 v52,v52,v54
v_mul_f32 v53,v53,v55
v_fma_f32 v64,v64,v62,v62
v_fma_f32 v65,v65,v62,v62
v_rcp_f32 v64,v64
v_rcp_f32 v65,v65
v_fma_f32 v66,v66,v62,v62
v_fma_f32 v67,v67,v62,v62
v_rcp_f32 v66,v66
v_rcp_f32 v67,v67
v_fma_f32 v68,v68,v62,v62
v_fma_f32 v69,v69,v62,v62
v_rcp_f32 v68,v68
v_rcp_f32 v69,v69
v_mul_f32 v44,v44,v64
v_mul_f32 v45,v45,v65
v_mul_f32 v46,v46,v66
v_mul_f32 v47,v47,v67
v_mul_f32 v52,v52,v68
v_mul_f32 v53,v53,v69
v_cvt_pk_bf16_f32 v74,v52,v53
v_cvt_pk_bf16_f32 v72,v44,v45
v_cvt_pk_bf16_f32 v73,v46,v47
v_permlane32_swap_b32 v74,v75
s_nop 1
v_permlane16_swap_b32 v72,v74
v_permlane16_swap_b32 v73,v75
buffer_store_dwordx4 v[72:75],v15,s[32:35],s68 offen sc1
s_add_u32 s68,s68,s30
s_waitcnt vmcnt(0)
s_barrier
buffer_store_dwordx2 v[16:17],v2,s[92:95],s68 idxen sc1
s_branch .L_end
.L_gt0_2:
s_nop 1
s_add_u32 s68,s68,s30
s_waitcnt vmcnt(6)
s_barrier
)#"
R"#(
buffer_store_dwordx2 v[16:17],v2,s[92:95],s68 idxen sc1
v_fma_f32 v40,-v60,v28,v40
v_fma_f32 v41,-v60,v29,v41
v_fma_f32 v42,-v60,v30,v42
v_fma_f32 v43,-v60,v31,v43
v_fma_f32 v36,-v60,v24,v36
v_fma_f32 v37,-v60,v25,v37
v_fma_f32 v38,-v60,v26,v38
v_fma_f32 v39,-v60,v27,v39
v_fma_f32 v32,-v60,v20,v32
v_fma_f32 v33,-v60,v21,v33
v_fma_f32 v34,-v60,v22,v34
v_fma_f32 v35,-v60,v23,v35
v_permlane32_swap_b32 v40,v42
v_permlane32_swap_b32 v41,v43
v_mul_f32 v64,v32,v61
v_mul_f32 v65,v33,v61
v_mul_f32 v66,v34,v61
v_mul_f32 v67,v35,v61
v_mul_f32 v68,v40,v61
v_mul_f32 v69,v41,v61
v_exp_f32 v64,v64
v_exp_f32 v65,v65
v_mul_f32 v32,v32,v36
v_mul_f32 v33,v33,v37
v_exp_f32 v66,v66
v_exp_f32 v67,v67
v_mul_f32 v34,v34,v38
v_mul_f32 v35,v35,v39
v_exp_f32 v68,v68
v_exp_f32 v69,v69
v_mul_f32 v40,v40,v42
v_mul_f32 v41,v41,v43
v_fma_f32 v64,v64,v62,v62
v_fma_f32 v65,v65,v62,v62
v_rcp_f32 v64,v64
v_rcp_f32 v65,v65
v_fma_f32 v66,v66,v62,v62
v_fma_f32 v67,v67,v62,v62
v_rcp_f32 v66,v66
v_rcp_f32 v67,v67
v_fma_f32 v68,v68,v62,v62
v_fma_f32 v69,v69,v62,v62
v_rcp_f32 v68,v68
v_rcp_f32 v69,v69
v_mul_f32 v32,v32,v64
v_mul_f32 v33,v33,v65
v_mul_f32 v34,v34,v66
v_mul_f32 v35,v35,v67
v_mul_f32 v40,v40,v68
v_mul_f32 v41,v41,v69
v_cvt_pk_bf16_f32 v74,v40,v41
v_cvt_pk_bf16_f32 v72,v32,v33
v_cvt_pk_bf16_f32 v73,v34,v35
v_permlane32_swap_b32 v74,v75
s_nop 1
v_permlane16_swap_b32 v72,v74
v_permlane16_swap_b32 v73,v75
buffer_store_dwordx4 v[72:75],v15,s[32:35],s68 offen sc1
s_add_u32 s68,s68,s30
s_waitcnt vmcnt(0)
s_barrier
buffer_store_dwordx2 v[16:17],v2,s[92:95],s68 idxen sc1
s_branch .L_end
.p2align 6
.L_gv3:
.L_gl_3:
s_waitcnt lgkmcnt(4)
v_mfma_f32_16x16x32_bf16 v[32:35],a[0:3],v[76:79],0
ds_read_b128 v[108:111],v4 offset:9216
s_add_u32 s68,s68,s30
v_mfma_f32_16x16x32_bf16 v[32:35],a[12:15],v[80:83],v[32:35]
buffer_load_dwordx4 v6,s[32:35],s63 offen offset:2048 sc1 lds
v_fma_f32 v52,-v60,v28,v52
v_mfma_f32_16x16x32_bf16 v[32:35],a[24:27],v[84:87],v[32:35]
v_fma_f32 v53,-v60,v29,v53
v_fma_f32 v54,-v60,v30,v54
v_mfma_f32_16x16x32_bf16 v[32:35],a[36:39],v[88:91],v[32:35]
ds_read_b128 v[112:115],v4 offset:10368
v_fma_f32 v55,-v60,v31,v55
v_mfma_f32_16x16x32_bf16 v[36:39],a[40:43],v[88:91],0
v_permlane32_swap_b32 v52,v54
v_mfma_f32_16x16x32_bf16 v[36:39],a[28:31],v[84:87],v[36:39]
buffer_load_dwordx4 v6,s[32:35],s63 offen offset:3072 sc1 lds
v_fma_f32 v48,-v60,v24,v48
v_mfma_f32_16x16x32_bf16 v[36:39],a[16:19],v[80:83],v[36:39]
ds_read_b128 v[116:119],v4 offset:11520
v_fma_f32 v49,-v60,v25,v49
v_mfma_f32_16x16x32_bf16 v[36:39],a[4:7],v[76:79],v[36:39]
v_permlane32_swap_b32 v53,v55
v_mfma_f32_16x16x32_bf16 v[40:43],a[8:11],v[76:79],0
s_waitcnt vmcnt(8)
s_barrier
v_mfma_f32_16x16x32_bf16 v[40:43],a[20:23],v[80:83],v[40:43]
buffer_store_dwordx2 v[16:17],v2,s[92:95],s68 idxen sc1
ds_read_b128 v[120:123],v4 offset:12672
v_mfma_f32_16x16x32_bf16 v[40:43],a[32:35],v[84:87],v[40:43]
v_fma_f32 v50,-v60,v26,v50
v_fma_f32 v51,-v60,v27,v51
v_mfma_f32_16x16x32_bf16 v[40:43],a[44:47],v[88:91],v[40:43]
v_fma_f32 v44,-v60,v20,v44
s_waitcnt lgkmcnt(4)
v_mfma_f32_16x16x32_bf16 v[40:43],a[56:59],v[92:95],v[40:43]
ds_read_b128 v[124:127],v4 offset:13824
s_add_u32 m0,s64,45056
v_mfma_f32_16x16x32_bf16 v[40:43],a[68:71],v[96:99],v[40:43]
buffer_load_dwordx4 v7,s[32:35],s63 offen offset:0 sc1 lds
s_add_u32 s63,s63,s30
v_mfma_f32_16x16x32_bf16 v[40:43],a[80:83],v[100:103],v[40:43]
s_xor_b32 s64,s64,s65
v_fma_f32 v45,-v60,v21,v45
v_mfma_f32_16x16x32_bf16 v[40:43],a[92:95],v[104:107],v[40:43]
ds_read_b128 v[76:79],v4 offset:14976
v_fma_f32 v46,-v60,v22,v46
v_mfma_f32_16x16x32_bf16 v[36:39],a[88:91],v[104:107],v[36:39]
v_fma_f32 v47,-v60,v23,v47
v_mul_f32 v64,v44,v61
v_mfma_f32_16x16x32_bf16 v[36:39],a[76:79],v[100:103],v[36:39]
v_exp_f32 v64,v64
v_mfma_f32_16x16x32_bf16 v[36:39],a[64:67],v[96:99],v[36:39]
ds_read_b128 v[80:83],v4 offset:16128
v_mul_f32 v65,v45,v61
v_mfma_f32_16x16x32_bf16 v[36:39],a[52:55],v[92:95],v[36:39]
v_exp_f32 v65,v65
v_mfma_f32_16x16x32_bf16 v[32:35],a[48:51],v[92:95],v[32:35]
v_mul_f32 v66,v46,v61
v_mul_f32 v67,v47,v61
v_mfma_f32_16x16x32_bf16 v[32:35],a[60:63],v[96:99],v[32:35]
ds_read_b128 v[84:87],v4 offset:17280
v_mul_f32 v68,v52,v61
v_mfma_f32_16x16x32_bf16 v[32:35],a[72:75],v[100:103],v[32:35]
v_exp_f32 v66,v66
v_mfma_f32_16x16x32_bf16 v[32:35],a[84:87],v[104:107],v[32:35]
v_mul_f32 v69,v53,v61
s_waitcnt lgkmcnt(4)
v_mfma_f32_16x16x32_bf16 v[32:35],a[96:99],v[108:111],v[32:35]
ds_read_b128 v[88:91],v4 offset:18432
v_mul_f32 v44,v44,v48
v_mfma_f32_16x16x32_bf16 v[32:35],a[108:111],v[112:115],v[32:35]
v_exp_f32 v67,v67
v_mfma_f32_16x16x32_bf16 v[32:35],a[120:123],v[116:119],v[32:35]
v_exp_f32 v68,v68
v_mfma_f32_16x16x32_bf16 v[32:35],a[132:135],v[120:123],v[32:35]
ds_read_b128 v[92:95],v4 offset:19584
v_mul_f32 v45,v45,v49
v_mfma_f32_16x16x32_bf16 v[36:39],a[136:139],v[120:123],v[36:39]
v_exp_f32 v69,v69
v_mfma_f32_16x16x32_bf16 v[36:39],a[124:127],v[116:119],v[36:39]
v_mul_f32 v46,v46,v50
v_mul_f32 v47,v47,v51
v_mfma_f32_16x16x32_bf16 v[36:39],a[112:115],v[112:115],v[36:39]
ds_read_b128 v[96:99],v4 offset:20736
v_mul_f32 v52,v52,v54
v_mfma_f32_16x16x32_bf16 v[36:39],a[100:103],v[108:111],v[36:39]
v_mul_f32 v53,v53,v55
v_fma_f32 v64,v64,v62,v62
v_mfma_f32_16x16x32_bf16 v[40:43],a[104:107],v[108:111],v[40:43]
v_rcp_f32 v64,v64
v_mfma_f32_16x16x32_bf16 v[40:43],a[116:119],v[112:115],v[40:43]
ds_read_b128 v[100:103],v4 offset:21888
v_fma_f32 v65,v65,v62,v62
v_mfma_f32_16x16x32_bf16 v[40:43],a[128:131],v[116:119],v[40:43]
v_rcp_f32 v65,v65
v_mfma_f32_16x16x32_bf16 v[40:43],a[140:143],v[120:123],v[40:43]
v_fma_f32 v66,v66,v62,v62
s_waitcnt lgkmcnt(4)
v_mfma_f32_16x16x32_bf16 v[40:43],a[152:155],v[124:127],v[40:43]
ds_read_b128 v[104:107],v4 offset:23040
v_fma_f32 v67,v67,v62,v62
v_mfma_f32_16x16x32_bf16 v[40:43],a[164:167],v[76:79],v[40:43]
v_rcp_f32 v66,v66
v_mfma_f32_16x16x32_bf16 v[40:43],a[176:179],v[80:83],v[40:43]
v_rcp_f32 v67,v67
v_mfma_f32_16x16x32_bf16 v[40:43],a[188:191],v[84:87],v[40:43]
ds_read_b128 v[108:111],v4 offset:24192
v_fma_f32 v68,v68,v62,v62
v_mfma_f32_16x16x32_bf16 v[36:39],a[184:187],v[84:87],v[36:39]
v_rcp_f32 v68,v68
v_mfma_f32_16x16x32_bf16 v[36:39],a[172:175],v[80:83],v[36:39]
v_fma_f32 v69,v69,v62,v62
v_mul_f32 v44,v44,v64
v_mfma_f32_16x16x32_bf16 v[36:39],a[160:163],v[76:79],v[36:39]
ds_read_b128 v[112:115],v4 offset:25344
v_mul_f32 v45,v45,v65
v_mfma_f32_16x16x32_bf16 v[36:39],a[148:151],v[124:127],v[36:39]
v_rcp_f32 v69,v69
v_mfma_f32_16x16x32_bf16 v[32:35],a[144:147],v[124:127],v[32:35]
v_mul_f32 v46,v46,v66
v_mul_f32 v47,v47,v67
v_mfma_f32_16x16x32_bf16 v[32:35],a[156:159],v[76:79],v[32:35]
ds_read_b128 v[116:119],v4 offset:26496
v_mul_f32 v52,v52,v68
v_mfma_f32_16x16x32_bf16 v[32:35],a[168:171],v[80:83],v[32:35]
v_mul_f32 v53,v53,v69
v_cvt_pk_bf16_f32 v72,v44,v45
v_mfma_f32_16x16x32_bf16 v[32:35],a[180:183],v[84:87],v[32:35]
v_cvt_pk_bf16_f32 v74,v52,v53
s_waitcnt lgkmcnt(4)
v_mfma_f32_16x16x32_bf16 v[32:35],a[192:195],v[88:91],v[32:35]
ds_read_b128 v[120:123],v4 offset:27648
v_cvt_pk_bf16_f32 v73,v46,v47
v_mfma_f32_16x16x32_bf16 v[32:35],a[204:207],v[92:95],v[32:35]
v_permlane32_swap_b32 v74,v75
v_mfma_f32_16x16x32_bf16 v[32:35],a[216:219],v[96:99],v[32:35]
v_add_f32_dpp v56,v56,v56 row_ror:2 row_mask:0xf bank_mask:0xf
v_mfma_f32_16x16x32_bf16 v[32:35],a[228:231],v[100:103],v[32:35]
ds_read_b128 v[124:127],v4 offset:28800
v_mul_f32 v56,0x3a800000,v56
v_mfma_f32_16x16x32_bf16 v[36:39],a[232:235],v[100:103],v[36:39]
v_permlane16_swap_b32 v72,v74
v_mfma_f32_16x16x32_bf16 v[36:39],a[220:223],v[96:99],v[36:39]
v_permlane16_swap_b32 v73,v75
v_mfma_f32_16x16x32_bf16 v[36:39],a[208:211],v[92:95],v[36:39]
buffer_store_dwordx4 v[72:75],v15,s[32:35],s68 offen sc1
ds_read_b128 v[76:79],v4 offset:29952
v_mfma_f32_16x16x32_bf16 v[36:39],a[196:199],v[88:91],v[36:39]
v_mul_f32 v57,v56,v56
v_mfma_f32_16x16x32_bf16 v[40:43],a[200:203],v[88:91],v[40:43]
v_mfma_f32_16x16x32_bf16 v[40:43],a[212:215],v[92:95],v[40:43]
ds_read_b128 v[80:83],v4 offset:31104
v_sub_f32_dpp v57,v56,v57 quad_perm:[1,1,3,3] row_mask:0xf bank_mask:0xf
v_mfma_f32_16x16x32_bf16 v[40:43],a[224:227],v[96:99],v[40:43]
v_max_f32 v57,0,v57
v_mfma_f32_16x16x32_bf16 v[40:43],a[236:239],v[100:103],v[40:43]
v_cvt_pk_bf16_f32 v57,0,v57
s_waitcnt lgkmcnt(4)
v_mfma_f32_16x16x32_bf16 v[40:43],a[248:251],v[104:107],v[40:43]
ds_read_b128 v[84:87],v4 offset:32256
v_add_f32 v57,0x3727c5ac,v57
v_mfma_f32_16x16x32_bf16 v[40:43],v[132:135],v[108:111],v[40:43]
v_cvt_pk_bf16_f32 v57,0,v57
v_mfma_f32_16x16x32_bf16 v[40:43],v[144:147],v[112:115],v[40:43]
v_sqrt_f32 v57,v57
v_mfma_f32_16x16x32_bf16 v[40:43],v[156:159],v[116:119],v[40:43]
ds_read_b128 v[88:91],v4 offset:33408
v_cvt_pk_bf16_f32 v57,v56,v57
v_mfma_f32_16x16x32_bf16 v[36:39],v[152:155],v[116:119],v[36:39]
ds_write_b32 v10,v57 offset:0
v_mfma_f32_16x16x32_bf16 v[36:39],v[140:143],v[112:115],v[36:39]
v_mfma_f32_16x16x32_bf16 v[36:39],v[128:131],v[108:111],v[36:39]
ds_read_b128 v[92:95],v4 offset:34560
v_mfma_f32_16x16x32_bf16 v[36:39],a[244:247],v[104:107],v[36:39]
v_mfma_f32_16x16x32_bf16 v[32:35],a[240:243],v[104:107],v[32:35]
s_waitcnt vmcnt(1)
s_waitcnt lgkmcnt(1)
s_barrier
v_mfma_f32_16x16x32_bf16 v[32:35],a[252:255],v[108:111],v[32:35]
ds_read_b128 v[96:99],v4 offset:35712
v_mfma_f32_16x16x32_bf16 v[32:35],v[136:139],v[112:115],v[32:35]
v_xor_b32 v4,73728,v4
v_mfma_f32_16x16x32_bf16 v[32:35],v[148:151],v[116:119],v[32:35]
v_mfma_f32_16x16x32_bf16 v[32:35],v[160:163],v[120:123],v[32:35]
ds_read_b128 v[100:103],v5 offset:0
s_add_u32 m0,s64,0
v_mfma_f32_16x16x32_bf16 v[32:35],v[172:175],v[124:127],v[32:35]
buffer_load_dwordx4 v2,s[32:35],s63 offen offset:0 sc1 lds
v_mfma_f32_16x16x32_bf16 v[32:35],v[184:187],v[76:79],v[32:35]
ds_read_b32 v62,v11 offset:0
v_mfma_f32_16x16x32_bf16 v[32:35],v[196:199],v[80:83],v[32:35]
ds_read_b128 v[104:107],v5 offset:1152
v_mfma_f32_16x16x32_bf16 v[36:39],v[200:203],v[80:83],v[36:39]
v_mfma_f32_16x16x32_bf16 v[36:39],v[188:191],v[76:79],v[36:39]
buffer_load_dwordx4 v2,s[32:35],s63 offen offset:1024 sc1 lds
v_mfma_f32_16x16x32_bf16 v[36:39],v[176:179],v[124:127],v[36:39]
ds_read_b128 v[108:111],v5 offset:2304
ds_read2st64_b32 v[56:57],v8 offset0:144 offset1:180
v_mfma_f32_16x16x32_bf16 v[36:39],v[164:167],v[120:123],v[36:39]
ds_read2st64_b32 v[58:59],v8 offset0:216 offset1:252
v_xor_b32 v8,v9,v8
v_mfma_f32_16x16x32_bf16 v[40:43],v[168:171],v[120:123],v[40:43]
s_waitcnt lgkmcnt(4)
v_lshlrev_b32 v60,16,v62
v_mfma_f32_16x16x32_bf16 v[40:43],v[180:183],v[124:127],v[40:43]
buffer_load_dwordx4 v2,s[32:35],s63 offen offset:2048 sc1 lds
ds_read_b128 v[112:115],v5 offset:3456
v_mfma_f32_16x16x32_bf16 v[40:43],v[192:195],v[76:79],v[40:43]
v_and_b32 v62,0xffff0000,v62
v_mfma_f32_16x16x32_bf16 v[40:43],v[204:207],v[80:83],v[40:43]
v_mfma_f32_16x16x32_bf16 v[40:43],v[216:219],v[84:87],v[40:43]
ds_read_b128 v[116:119],v5 offset:4608
v_mfma_f32_16x16x32_bf16 v[40:43],v[228:231],v[88:91],v[40:43]
buffer_load_dwordx4 v2,s[32:35],s63 offen offset:3072 sc1 lds
v_mfma_f32_16x16x32_bf16 v[40:43],v[240:243],v[92:95],v[40:43]
v_rcp_f32 v61,v62
v_mfma_f32_16x16x32_bf16 v[40:43],v[252:255],v[96:99],v[40:43]
ds_read_b128 v[120:123],v5 offset:5760
v_mul_f32 v62,v62,v62
v_mfma_f32_16x16x32_bf16 v[36:39],v[248:251],v[96:99],v[36:39]
s_add_u32 m0,s64,4096
s_waitcnt lgkmcnt(3)
v_add_f32 v56,v56,v57
v_mfma_f32_16x16x32_bf16 v[36:39],v[236:239],v[92:95],v[36:39]
buffer_load_dwordx4 v6,s[32:35],s63 offen offset:0 sc1 lds
v_add_f32 v58,v58,v59
v_mfma_f32_16x16x32_bf16 v[36:39],v[224:227],v[88:91],v[36:39]
ds_read_b128 v[124:127],v5 offset:6912
v_add_f32 v56,v56,v58
v_mfma_f32_16x16x32_bf16 v[36:39],v[212:215],v[84:87],v[36:39]
v_mul_f32 v61,0xbfb8aa3b,v61
v_add_f32_dpp v56,v56,v56 row_ror:8 row_mask:0xf bank_mask:0xf
v_mfma_f32_16x16x32_bf16 v[32:35],v[208:211],v[84:87],v[32:35]
v_mfma_f32_16x16x32_bf16 v[32:35],v[220:223],v[88:91],v[32:35]
buffer_load_dwordx4 v6,s[32:35],s63 offen offset:1024 sc1 lds
ds_read_b128 v[76:79],v5 offset:8064
v_mfma_f32_16x16x32_bf16 v[32:35],v[232:235],v[92:95],v[32:35]
v_add_f32_dpp v56,v56,v56 row_ror:4 row_mask:0xf bank_mask:0xf
s_sub_u32 s62,s62,1
v_mfma_f32_16x16x32_bf16 v[32:35],v[244:247],v[96:99],v[32:35]
s_cbranch_scc1 .L_gt0_3
s_waitcnt lgkmcnt(4)
v_mfma_f32_16x16x32_bf16 v[44:47],a[0:3],v[100:103],0
ds_read_b128 v[80:83],v5 offset:9216
s_add_u32 s68,s68,s30
v_mfma_f32_16x16x32_bf16 v[44:47],a[12:15],v[104:107],v[44:47]
buffer_load_dwordx4 v6,s[32:35],s63 offen offset:2048 sc1 lds
v_fma_f32 v40,-v60,v28,v40
v_mfma_f32_16x16x32_bf16 v[44:47],a[24:27],v[108:111],v[44:47]
v_fma_f32 v41,-v60,v29,v41
v_fma_f32 v42,-v60,v30,v42
v_mfma_f32_16x16x32_bf16 v[44:47],a[36:39],v[112:115],v[44:47]
ds_read_b128 v[84:87],v5 offset:10368
v_fma_f32 v43,-v60,v31,v43
v_mfma_f32_16x16x32_bf16 v[48:51],a[40:43],v[112:115],0
v_permlane32_swap_b32 v40,v42
v_mfma_f32_16x16x32_bf16 v[48:51],a[28:31],v[108:111],v[48:51]
buffer_load_dwordx4 v6,s[32:35],s63 offen offset:3072 sc1 lds
v_fma_f32 v36,-v60,v24,v36
v_mfma_f32_16x16x32_bf16 v[48:51],a[16:19],v[104:107],v[48:51]
ds_read_b128 v[88:91],v5 offset:11520
v_fma_f32 v37,-v60,v25,v37
v_mfma_f32_16x16x32_bf16 v[48:51],a[4:7],v[100:103],v[48:51]
v_permlane32_swap_b32 v41,v43
v_mfma_f32_16x16x32_bf16 v[52:55],a[8:11],v[100:103],0
s_waitcnt vmcnt(8)
s_barrier
v_mfma_f32_16x16x32_bf16 v[52:55],a[20:23],v[104:107],v[52:55]
buffer_store_dwordx2 v[16:17],v2,s[92:95],s68 idxen sc1
ds_read_b128 v[92:95],v5 offset:12672
v_mfma_f32_16x16x32_bf16 v[52:55],a[32:35],v[108:111],v[52:55]
v_fma_f32 v38,-v60,v26,v38
v_fma_f32 v39,-v60,v27,v39
v_mfma_f32_16x16x32_bf16 v[52:55],a[44:47],v[112:115],v[52:55]
v_fma_f32 v32,-v60,v20,v32
s_waitcnt lgkmcnt(4)
v_mfma_f32_16x16x32_bf16 v[52:55],a[56:59],v[116:119],v[52:55]
ds_read_b128 v[96:99],v5 offset:13824
s_add_u32 m0,s64,8192
v_mfma_f32_16x16x32_bf16 v[52:55],a[68:71],v[120:123],v[52:55]
buffer_load_dwordx4 v7,s[32:35],s63 offen offset:0 sc1 lds
s_add_u32 s63,s63,s30
v_mfma_f32_16x16x32_bf16 v[52:55],a[80:83],v[124:127],v[52:55]
v_fma_f32 v33,-v60,v21,v33
v_fma_f32 v34,-v60,v22,v34
v_mfma_f32_16x16x32_bf16 v[52:55],a[92:95],v[76:79],v[52:55]
ds_read_b128 v[100:103],v5 offset:14976
v_fma_f32 v35,-v60,v23,v35
v_mfma_f32_16x16x32_bf16 v[48:51],a[88:91],v[76:79],v[48:51]
v_mul_f32 v64,v32,v61
v_mul_f32 v65,v33,v61
v_mfma_f32_16x16x32_bf16 v[48:51],a[76:79],v[124:127],v[48:51]
v_exp_f32 v64,v64
v_mfma_f32_16x16x32_bf16 v[48:51],a[64:67],v[120:123],v[48:51]
ds_read_b128 v[104:107],v5 offset:16128
v_mul_f32 v66,v34,v61
v_mfma_f32_16x16x32_bf16 v[48:51],a[52:55],v[116:119],v[48:51]
v_exp_f32 v65,v65
v_mfma_f32_16x16x32_bf16 v[44:47],a[48:51],v[116:119],v[44:47]
v_exp_f32 v66,v66
v_mfma_f32_16x16x32_bf16 v[44:47],a[60:63],v[120:123],v[44:47]
ds_read_b128 v[108:111],v5 offset:17280
v_mul_f32 v67,v35,v61
v_mfma_f32_16x16x32_bf16 v[44:47],a[72:75],v[124:127],v[44:47]
v_exp_f32 v67,v67
v_mfma_f32_16x16x32_bf16 v[44:47],a[84:87],v[76:79],v[44:47]
v_mul_f32 v68,v40,v61
s_waitcnt lgkmcnt(4)
v_mfma_f32_16x16x32_bf16 v[44:47],a[96:99],v[80:83],v[44:47]
ds_read_b128 v[112:115],v5 offset:18432
v_mul_f32 v69,v41,v61
v_mfma_f32_16x16x32_bf16 v[44:47],a[108:111],v[84:87],v[44:47]
v_exp_f32 v68,v68
v_mfma_f32_16x16x32_bf16 v[44:47],a[120:123],v[88:91],v[44:47]
v_exp_f32 v69,v69
v_mfma_f32_16x16x32_bf16 v[44:47],a[132:135],v[92:95],v[44:47]
ds_read_b128 v[116:119],v5 offset:19584
v_mul_f32 v32,v32,v36
v_mfma_f32_16x16x32_bf16 v[48:51],a[136:139],v[92:95],v[48:51]
v_mul_f32 v33,v33,v37
v_mul_f32 v34,v34,v38
v_mfma_f32_16x16x32_bf16 v[48:51],a[124:127],v[88:91],v[48:51]
v_mul_f32 v35,v35,v39
v_mul_f32 v40,v40,v42
v_mfma_f32_16x16x32_bf16 v[48:51],a[112:115],v[84:87],v[48:51]
ds_read_b128 v[120:123],v5 offset:20736
v_mul_f32 v41,v41,v43
v_mfma_f32_16x16x32_bf16 v[48:51],a[100:103],v[80:83],v[48:51]
v_fma_f32 v64,v64,v62,v62
v_fma_f32 v65,v65,v62,v62
v_mfma_f32_16x16x32_bf16 v[52:55],a[104:107],v[80:83],v[52:55]
v_rcp_f32 v64,v64
v_mfma_f32_16x16x32_bf16 v[52:55],a[116:119],v[84:87],v[52:55]
ds_read_b128 v[124:127],v5 offset:21888
v_fma_f32 v66,v66,v62,v62
v_mfma_f32_16x16x32_bf16 v[52:55],a[128:131],v[88:91],v[52:55]
v_rcp_f32 v65,v65
v_mfma_f32_16x16x32_bf16 v[52:55],a[140:143],v[92:95],v[52:55]
v_fma_f32 v67,v67,v62,v62
s_waitcnt lgkmcnt(4)
v_mfma_f32_16x16x32_bf16 v[52:55],a[152:155],v[96:99],v[52:55]
ds_read_b128 v[76:79],v5 offset:23040
v_fma_f32 v68,v68,v62,v62
v_mfma_f32_16x16x32_bf16 v[52:55],a[164:167],v[100:103],v[52:55]
v_rcp_f32 v66,v66
v_mfma_f32_16x16x32_bf16 v[52:55],a[176:179],v[104:107],v[52:55]
v_rcp_f32 v67,v67
v_mfma_f32_16x16x32_bf16 v[52:55],a[188:191],v[108:111],v[52:55]
ds_read_b128 v[80:83],v5 offset:24192
v_fma_f32 v69,v69,v62,v62
v_mfma_f32_16x16x32_bf16 v[48:51],a[184:187],v[108:111],v[48:51]
v_rcp_f32 v68,v68
v_mfma_f32_16x16x32_bf16 v[48:51],a[172:175],v[104:107],v[48:51]
v_rcp_f32 v69,v69
v_mfma_f32_16x16x32_bf16 v[48:51],a[160:163],v[100:103],v[48:51]
ds_read_b128 v[84:87],v5 offset:25344
v_mul_f32 v32,v32,v64
v_mfma_f32_16x16x32_bf16 v[48:51],a[148:151],v[96:99],v[48:51]
v_mul_f32 v33,v33,v65
v_mul_f32 v34,v34,v66
v_mfma_f32_16x16x32_bf16 v[44:47],a[144:147],v[96:99],v[44:47]
v_mul_f32 v35,v35,v67
v_mul_f32 v40,v40,v68
v_mfma_f32_16x16x32_bf16 v[44:47],a[156:159],v[100:103],v[44:47]
ds_read_b128 v[88:91],v5 offset:26496
v_mul_f32 v41,v41,v69
v_mfma_f32_16x16x32_bf16 v[44:47],a[168:171],v[104:107],v[44:47]
v_cvt_pk_bf16_f32 v74,v40,v41
v_cvt_pk_bf16_f32 v72,v32,v33
v_mfma_f32_16x16x32_bf16 v[44:47],a[180:183],v[108:111],v[44:47]
v_cvt_pk_bf16_f32 v73,v34,v35
s_waitcnt lgkmcnt(4)
v_mfma_f32_16x16x32_bf16 v[44:47],a[192:195],v[112:115],v[44:47]
ds_read_b128 v[92:95],v5 offset:27648
v_add_f32_dpp v56,v56,v56 row_ror:2 row_mask:0xf bank_mask:0xf
v_mfma_f32_16x16x32_bf16 v[44:47],a[204:207],v[116:119],v[44:47]
v_permlane32_swap_b32 v74,v75
v_mfma_f32_16x16x32_bf16 v[44:47],a[216:219],v[120:123],v[44:47]
v_mul_f32 v56,0x3a800000,v56
v_mfma_f32_16x16x32_bf16 v[44:47],a[228:231],v[124:127],v[44:47]
ds_read_b128 v[96:99],v5 offset:28800
v_mul_f32 v57,v56,v56
v_mfma_f32_16x16x32_bf16 v[48:51],a[232:235],v[124:127],v[48:51]
v_permlane16_swap_b32 v72,v74
v_mfma_f32_16x16x32_bf16 v[48:51],a[220:223],v[120:123],v[48:51]
v_permlane16_swap_b32 v73,v75
v_mfma_f32_16x16x32_bf16 v[48:51],a[208:211],v[116:119],v[48:51]
buffer_store_dwordx4 v[72:75],v15,s[32:35],s68 offen sc1
ds_read_b128 v[100:103],v5 offset:29952
v_mfma_f32_16x16x32_bf16 v[48:51],a[196:199],v[112:115],v[48:51]
v_sub_f32_dpp v57,v56,v57 quad_perm:[1,1,3,3] row_mask:0xf bank_mask:0xf
v_mfma_f32_16x16x32_bf16 v[52:55],a[200:203],v[112:115],v[52:55]
v_max_f32 v57,0,v57
v_mfma_f32_16x16x32_bf16 v[52:55],a[212:215],v[116:119],v[52:55]
ds_read_b128 v[104:107],v5 offset:31104
v_cvt_pk_bf16_f32 v57,0,v57
v_mfma_f32_16x16x32_bf16 v[52:55],a[224:227],v[120:123],v[52:55]
v_add_f32 v57,0x3727c5ac,v57
v_mfma_f32_16x16x32_bf16 v[52:55],a[236:239],v[124:127],v[52:55]
v_cvt_pk_bf16_f32 v57,0,v57
s_waitcnt lgkmcnt(4)
v_mfma_f32_16x16x32_bf16 v[52:55],a[248:251],v[76:79],v[52:55]
ds_read_b128 v[108:111],v5 offset:32256
v_mfma_f32_16x16x32_bf16 v[52:55],v[132:135],v[80:83],v[52:55]
v_sqrt_f32 v57,v57
v_mfma_f32_16x16x32_bf16 v[52:55],v[144:147],v[84:87],v[52:55]
v_cvt_pk_bf16_f32 v57,v56,v57
v_mfma_f32_16x16x32_bf16 v[52:55],v[156:159],v[88:91],v[52:55]
ds_read_b128 v[112:115],v5 offset:33408
ds_write_b32 v10,v57 offset:128
v_mfma_f32_16x16x32_bf16 v[48:51],v[152:155],v[88:91],v[48:51]
v_mfma_f32_16x16x32_bf16 v[48:51],v[140:143],v[84:87],v[48:51]
v_mfma_f32_16x16x32_bf16 v[48:51],v[128:131],v[80:83],v[48:51]
ds_read_b128 v[116:119],v5 offset:34560
v_mfma_f32_16x16x32_bf16 v[48:51],a[244:247],v[76:79],v[48:51]
v_mfma_f32_16x16x32_bf16 v[44:47],a[240:243],v[76:79],v[44:47]
s_waitcnt vmcnt(1)
s_waitcnt lgkmcnt(1)
s_barrier
v_mfma_f32_16x16x32_bf16 v[44:47],a[252:255],v[80:83],v[44:47]
ds_read_b128 v[120:123],v5 offset:35712
v_mfma_f32_16x16x32_bf16 v[44:47],v[136:139],v[84:87],v[44:47]
v_xor_b32 v5,73728,v5
v_mfma_f32_16x16x32_bf16 v[44:47],v[148:151],v[88:91],v[44:47]
v_mfma_f32_16x16x32_bf16 v[44:47],v[160:163],v[92:95],v[44:47]
ds_read_b128 v[76:79],v4 offset:0
s_add_u32 m0,s64,36864
v_mfma_f32_16x16x32_bf16 v[44:47],v[172:175],v[96:99],v[44:47]
buffer_load_dwordx4 v2,s[32:35],s63 offen offset:0 sc1 lds
v_mfma_f32_16x16x32_bf16 v[44:47],v[184:187],v[100:103],v[44:47]
ds_read_b32 v62,v11 offset:128
v_mfma_f32_16x16x32_bf16 v[44:47],v[196:199],v[104:107],v[44:47]
)#"
R"#(
ds_read_b128 v[80:83],v4 offset:1152
v_mfma_f32_16x16x32_bf16 v[48:51],v[200:203],v[104:107],v[48:51]
v_mfma_f32_16x16x32_bf16 v[48:51],v[188:191],v[100:103],v[48:51]
buffer_load_dwordx4 v2,s[32:35],s63 offen offset:1024 sc1 lds
v_mfma_f32_16x16x32_bf16 v[48:51],v[176:179],v[96:99],v[48:51]
ds_read_b128 v[84:87],v4 offset:2304
ds_read2st64_b32 v[56:57],v8 offset0:0 offset1:36
v_mfma_f32_16x16x32_bf16 v[48:51],v[164:167],v[92:95],v[48:51]
ds_read2st64_b32 v[58:59],v8 offset0:72 offset1:108
v_mfma_f32_16x16x32_bf16 v[52:55],v[168:171],v[92:95],v[52:55]
s_waitcnt lgkmcnt(4)
v_lshlrev_b32 v60,16,v62
v_mfma_f32_16x16x32_bf16 v[52:55],v[180:183],v[96:99],v[52:55]
buffer_load_dwordx4 v2,s[32:35],s63 offen offset:2048 sc1 lds
ds_read_b128 v[88:91],v4 offset:3456
v_mfma_f32_16x16x32_bf16 v[52:55],v[192:195],v[100:103],v[52:55]
v_and_b32 v62,0xffff0000,v62
v_mfma_f32_16x16x32_bf16 v[52:55],v[204:207],v[104:107],v[52:55]
v_mfma_f32_16x16x32_bf16 v[52:55],v[216:219],v[108:111],v[52:55]
ds_read_b128 v[92:95],v4 offset:4608
v_mfma_f32_16x16x32_bf16 v[52:55],v[228:231],v[112:115],v[52:55]
buffer_load_dwordx4 v2,s[32:35],s63 offen offset:3072 sc1 lds
v_mfma_f32_16x16x32_bf16 v[52:55],v[240:243],v[116:119],v[52:55]
v_rcp_f32 v61,v62
v_mfma_f32_16x16x32_bf16 v[52:55],v[252:255],v[120:123],v[52:55]
ds_read_b128 v[96:99],v4 offset:5760
v_mul_f32 v62,v62,v62
v_mfma_f32_16x16x32_bf16 v[48:51],v[248:251],v[120:123],v[48:51]
s_add_u32 m0,s64,40960
s_waitcnt lgkmcnt(3)
v_add_f32 v56,v56,v57
v_mfma_f32_16x16x32_bf16 v[48:51],v[236:239],v[116:119],v[48:51]
buffer_load_dwordx4 v6,s[32:35],s63 offen offset:0 sc1 lds
v_add_f32 v58,v58,v59
v_mfma_f32_16x16x32_bf16 v[48:51],v[224:227],v[112:115],v[48:51]
ds_read_b128 v[100:103],v4 offset:6912
v_add_f32 v56,v56,v58
v_mfma_f32_16x16x32_bf16 v[48:51],v[212:215],v[108:111],v[48:51]
v_mul_f32 v61,0xbfb8aa3b,v61
v_add_f32_dpp v56,v56,v56 row_ror:8 row_mask:0xf bank_mask:0xf
v_mfma_f32_16x16x32_bf16 v[44:47],v[208:211],v[108:111],v[44:47]
v_mfma_f32_16x16x32_bf16 v[44:47],v[220:223],v[112:115],v[44:47]
buffer_load_dwordx4 v6,s[32:35],s63 offen offset:1024 sc1 lds
ds_read_b128 v[104:107],v4 offset:8064
v_mfma_f32_16x16x32_bf16 v[44:47],v[232:235],v[116:119],v[44:47]
v_add_f32_dpp v56,v56,v56 row_ror:4 row_mask:0xf bank_mask:0xf
s_sub_u32 s62,s62,1
v_mfma_f32_16x16x32_bf16 v[44:47],v[244:247],v[120:123],v[44:47]
s_cbranch_scc0 .L_gl_3
s_nop 1
s_add_u32 s68,s68,s30
s_waitcnt vmcnt(6)
s_barrier
buffer_store_dwordx2 v[16:17],v2,s[92:95],s68 idxen sc1
v_fma_f32 v52,-v60,v28,v52
v_fma_f32 v53,-v60,v29,v53
v_fma_f32 v54,-v60,v30,v54
v_fma_f32 v55,-v60,v31,v55
v_fma_f32 v48,-v60,v24,v48
v_fma_f32 v49,-v60,v25,v49
v_fma_f32 v50,-v60,v26,v50
v_fma_f32 v51,-v60,v27,v51
v_fma_f32 v44,-v60,v20,v44
v_fma_f32 v45,-v60,v21,v45
v_fma_f32 v46,-v60,v22,v46
v_fma_f32 v47,-v60,v23,v47
v_permlane32_swap_b32 v52,v54
v_permlane32_swap_b32 v53,v55
v_mul_f32 v64,v44,v61
v_mul_f32 v65,v45,v61
v_mul_f32 v66,v46,v61
v_mul_f32 v67,v47,v61
v_mul_f32 v68,v52,v61
v_mul_f32 v69,v53,v61
v_exp_f32 v64,v64
v_exp_f32 v65,v65
v_mul_f32 v44,v44,v48
v_mul_f32 v45,v45,v49
v_exp_f32 v66,v66
v_exp_f32 v67,v67
v_mul_f32 v46,v46,v50
v_mul_f32 v47,v47,v51
v_exp_f32 v68,v68
v_exp_f32 v69,v69
v_mul_f32 v52,v52,v54
v_mul_f32 v53,v53,v55
v_fma_f32 v64,v64,v62,v62
v_fma_f32 v65,v65,v62,v62
v_rcp_f32 v64,v64
v_rcp_f32 v65,v65
v_fma_f32 v66,v66,v62,v62
v_fma_f32 v67,v67,v62,v62
v_rcp_f32 v66,v66
v_rcp_f32 v67,v67
v_fma_f32 v68,v68,v62,v62
v_fma_f32 v69,v69,v62,v62
v_rcp_f32 v68,v68
v_rcp_f32 v69,v69
v_mul_f32 v44,v44,v64
v_mul_f32 v45,v45,v65
v_mul_f32 v46,v46,v66
v_mul_f32 v47,v47,v67
v_mul_f32 v52,v52,v68
v_mul_f32 v53,v53,v69
v_cvt_pk_bf16_f32 v74,v52,v53
v_cvt_pk_bf16_f32 v72,v44,v45
v_cvt_pk_bf16_f32 v73,v46,v47
v_permlane32_swap_b32 v74,v75
s_nop 1
v_permlane16_swap_b32 v72,v74
v_permlane16_swap_b32 v73,v75
buffer_store_dwordx4 v[72:75],v15,s[32:35],s68 offen sc1
s_add_u32 s68,s68,s30
s_waitcnt vmcnt(0)
s_barrier
buffer_store_dwordx2 v[16:17],v2,s[92:95],s68 idxen sc1
s_branch .L_end
.L_gt0_3:
s_nop 1
s_add_u32 s68,s68,s30
s_waitcnt vmcnt(6)
s_barrier
buffer_store_dwordx2 v[16:17],v2,s[92:95],s68 idxen sc1
v_fma_f32 v40,-v60,v28,v40
v_fma_f32 v41,-v60,v29,v41
v_fma_f32 v42,-v60,v30,v42
v_fma_f32 v43,-v60,v31,v43
v_fma_f32 v36,-v60,v24,v36
v_fma_f32 v37,-v60,v25,v37
v_fma_f32 v38,-v60,v26,v38
v_fma_f32 v39,-v60,v27,v39
v_fma_f32 v32,-v60,v20,v32
v_fma_f32 v33,-v60,v21,v33
v_fma_f32 v34,-v60,v22,v34
v_fma_f32 v35,-v60,v23,v35
v_permlane32_swap_b32 v40,v42
v_permlane32_swap_b32 v41,v43
v_mul_f32 v64,v32,v61
v_mul_f32 v65,v33,v61
v_mul_f32 v66,v34,v61
v_mul_f32 v67,v35,v61
v_mul_f32 v68,v40,v61
v_mul_f32 v69,v41,v61
v_exp_f32 v64,v64
v_exp_f32 v65,v65
v_mul_f32 v32,v32,v36
v_mul_f32 v33,v33,v37
v_exp_f32 v66,v66
v_exp_f32 v67,v67
v_mul_f32 v34,v34,v38
v_mul_f32 v35,v35,v39
v_exp_f32 v68,v68
v_exp_f32 v69,v69
v_mul_f32 v40,v40,v42
v_mul_f32 v41,v41,v43
v_fma_f32 v64,v64,v62,v62
v_fma_f32 v65,v65,v62,v62
v_rcp_f32 v64,v64
v_rcp_f32 v65,v65
v_fma_f32 v66,v66,v62,v62
v_fma_f32 v67,v67,v62,v62
v_rcp_f32 v66,v66
v_rcp_f32 v67,v67
v_fma_f32 v68,v68,v62,v62
v_fma_f32 v69,v69,v62,v62
v_rcp_f32 v68,v68
v_rcp_f32 v69,v69
v_mul_f32 v32,v32,v64
v_mul_f32 v33,v33,v65
v_mul_f32 v34,v34,v66
v_mul_f32 v35,v35,v67
v_mul_f32 v40,v40,v68
v_mul_f32 v41,v41,v69
v_cvt_pk_bf16_f32 v74,v40,v41
v_cvt_pk_bf16_f32 v72,v32,v33
v_cvt_pk_bf16_f32 v73,v34,v35
v_permlane32_swap_b32 v74,v75
s_nop 1
v_permlane16_swap_b32 v72,v74
v_permlane16_swap_b32 v73,v75
buffer_store_dwordx4 v[72:75],v15,s[32:35],s68 offen sc1
s_add_u32 s68,s68,s30
s_waitcnt vmcnt(0)
s_barrier
buffer_store_dwordx2 v[16:17],v2,s[92:95],s68 idxen sc1
s_branch .L_end
.L_q:
v_mov_b32 v16,0x3f803f80
v_mov_b32 v17,0x3f803f80
v_mov_b32 v18,0x3f803f80
v_mov_b32 v19,0x3f803f80
s_add_u32 s36,s10,0x0
s_addc_u32 s37,s11,0x0
s_and_b32 s37,s37,0xffff
s_mov_b32 s38,0x600000
s_mov_b32 s39,0x20000
s_add_u32 s40,s10,0x400
s_addc_u32 s41,s11,0x0
s_and_b32 s41,s41,0xffff
s_mov_b32 s42,0x5ffc00
s_mov_b32 s43,0x20000
s_add_u32 s44,s10,0x800
s_addc_u32 s45,s11,0x0
s_and_b32 s45,s45,0xffff
s_mov_b32 s46,0x5ff800
s_mov_b32 s47,0x20000
s_lshl_b32 s96,s27,7
v_and_b32 v68,3,v1
v_mul_u32_u24 v68,8,v68
v_and_b32 v72,1,v1
v_bfe_u32 v3,v1,1,1
v_lshl_add_u32 v72,v72,1,v3
v_mul_u32_u24 v72,8,v72
s_lshl_b32 s48,s26,5
v_add_u32 v68,s48,v68
v_add_u32 v72,s48,v72
s_add_u32 s4,s8,0x0
s_addc_u32 s5,s9,0x0
s_and_b32 s5,s5,0xffff
s_mov_b32 s6,0x400000
s_mov_b32 s7,0x20000
v_lshrrev_b32 v57,3,v1
s_and_b32 s48,s26,1
v_xor_b32 v3,v57,v1
v_xor_b32 v3,s48,v3
v_and_b32 v3,7,v3
v_lshlrev_b32 v57,11,v57
v_lshl_add_u32 v57,v3,4,v57
s_lshl_b32 s56,s28,20
s_sub_u32 s56,s56,65536
s_and_b32 s56,s56,0x3fffff
s_lshl_b32 s57,s26,14
s_add_u32 s56,s56,s57
s_lshl_b32 s57,s26,10
v_lshrrev_b32 v64,3,v1
v_bfe_u32 v65,v1,4,1
s_and_b32 s48,s26,1
s_lshl_b32 s48,s48,1
v_or_b32 v65,s48,v65
v_lshlrev_b32 v65,1,v65
v_and_b32 v66,7,v1
v_xor_b32 v65,v65,v66
v_mul_u32_u24 v64,6144,v64
v_lshl_add_u32 v67,v65,4,v64
s_mul_i32 s48,s26,49152
s_add_u32 s96,s96,s48
s_mul_i32 s97,s26,3072
s_add_u32 m0,s57,24576
s_nop 0
buffer_load_dwordx4 v57,s[4:7],s56 offen offset:0 lds
s_add_u32 m0,s97,0
s_nop 0
buffer_load_dwordx4 v67,s[36:39],s96 offen offset:0 lds
buffer_load_dwordx4 v67,s[40:43],s96 offen offset:1024 lds
buffer_load_dwordx4 v67,s[44:47],s96 offen offset:2048 lds
s_add_u32 s96,s96,196608
s_add_u32 m0,s97,12288
s_nop 0
buffer_load_dwordx4 v67,s[36:39],s96 offen offset:0 lds
buffer_load_dwordx4 v67,s[40:43],s96 offen offset:1024 lds
buffer_load_dwordx4 v67,s[44:47],s96 offen offset:2048 lds
s_add_u32 s96,s96,196608
s_add_u32 m0,s57,28544
s_nop 0
buffer_load_dwordx4 v57,s[4:7],s56 offen offset:128 lds
s_add_u32 m0,s97,139264
s_nop 0
buffer_load_dwordx4 v67,s[36:39],s96 offen offset:0 lds
buffer_load_dwordx4 v67,s[40:43],s96 offen offset:1024 lds
buffer_load_dwordx4 v67,s[44:47],s96 offen offset:2048 lds
s_add_u32 s96,s96,196608
s_add_u32 m0,s97,151552
s_nop 0
buffer_load_dwordx4 v67,s[36:39],s96 offen offset:0 lds
buffer_load_dwordx4 v67,s[40:43],s96 offen offset:1024 lds
buffer_load_dwordx4 v67,s[44:47],s96 offen offset:2048 lds
s_add_u32 s96,s96,196608
s_add_u32 m0,s57,32512
s_nop 0
buffer_load_dwordx4 v57,s[4:7],s56 offen offset:256 lds
s_add_u32 m0,s97,90112
s_nop 0
buffer_load_dwordx4 v67,s[36:39],s96 offen offset:0 lds
buffer_load_dwordx4 v67,s[40:43],s96 offen offset:1024 lds
buffer_load_dwordx4 v67,s[44:47],s96 offen offset:2048 lds
s_add_u32 s96,s96,196608
s_add_u32 m0,s97,102400
s_nop 0
buffer_load_dwordx4 v67,s[36:39],s96 offen offset:0 lds
buffer_load_dwordx4 v67,s[40:43],s96 offen offset:1024 lds
buffer_load_dwordx4 v67,s[44:47],s96 offen offset:2048 lds
s_add_u32 s96,s96,196608
v_lshrrev_b32 v65,4,v1
v_bfe_u32 v64,v1,3,1
v_and_b32 v66,1,v65
v_lshl_add_u32 v64,v66,1,v64
v_lshlrev_b32 v64,5,v64
v_mul_u32_u24 v65,3072,v65
v_bfe_u32 v66,v1,2,2
v_lshl_add_u32 v65,v66,7,v65
v_xor_b32 v68,v64,v68
v_add_u32 v68,v65,v68
v_add_u32 v69,139264,v68
v_add_u32 v70,90112,v68
v_add_u32 v71,114688,v68
v_xor_b32 v72,v64,v72
v_add_u32 v72,v65,v72
v_add_u32 v73,139264,v72
v_add_u32 v74,90112,v72
v_add_u32 v75,114688,v72
s_lshl2_add_u32 s48,s27,s26
s_lshl_b32 s48,s48,9
s_add_u32 s68,s48,4648960
s_lshl2_add_u32 s49,s27,32
s_waitcnt lgkmcnt(0)
s_add_u32 s54,s52,0x7f4a7c15
s_addc_u32 s55,s53,0x9e3779b9
s_lshl_b32 s67,s49,5
s_add_u32 s67,s67,4898816
s_sub_u32 s51,s67,s68
s_add_u32 s92,s32,s51
s_addc_u32 s93,s33,0x10000
s_cmp_eq_u32 s26,0
s_cselect_b32 s94,1,0
s_mov_b32 s95,0x20000
v_mov_b32 v30,s54
v_mov_b32 v31,s55
v_cmp_gt_u32 vcc,12,v1
v_mul_u32_u24 v3,296960,v1
v_mov_b32 v12,0x7fff0000
v_cndmask_b32 v3,v12,v3,vcc
s_add_u32 s51,s67,76021760
buffer_store_dwordx2 v[30:31],v3,s[0:3],s51 offen sc1
s_sub_u32 s67,s68,296960
s_mul_i32 s49,s28,296960
s_add_u32 s68,s68,s49
s_sub_u32 s68,s68,2375680
s_mov_b32 s62,31
s_add_u32 s48,s28,3
s_and_b32 s48,s48,3
s_mul_i32 s48,s48,296960
s_add_u32 s66,s48,41721856
s_sub_u32 s48,s48,s49
s_add_u32 s69,s48,38010880
s_sub_u32 s70,0xfdce2000,s48
s_mov_b32 s72,s70
s_mov_b32 s63,0xfffba000
s_mul_i32 s64,s26,9216
s_add_u32 s65,s64,73728
s_xor_b32 s65,s65,s64
s_add_u32 s63,s63,s64
v_mov_b32 v4,v2
v_add_u32 v5,36864,v2
s_mov_b32 s71,s63
v_bfe_u32 v3,v1,1,3
v_mul_u32_u24 v6,1152,v3
v_lshrrev_b32 v3,1,v3
v_add_u32 v3,s26,v3
v_lshrrev_b32 v7,4,v1
v_lshl_add_u32 v3,v3,2,v7
v_and_b32 v3,15,v3
v_lshl_add_u32 v6,v3,2,v6
v_and_b32 v3,1,v1
v_lshl_add_u32 v6,v3,6,v6
v_add_u32 v6,1024,v6
s_lshl_b32 s48,s26,2
v_add_u32 v8,s48,v7
v_and_b32 v9,15,v1
v_add_u32 v3,64,v1
v_cmp_eq_u32 vcc,0,v9
v_add_u32 v7,73728,v6
v_xor_b32 v7,v7,v6
v_cndmask_b32 v8,v3,v8,vcc
v_lshlrev_b32 v8,2,v8
v_add_u32 v8,147456,v8
v_lshlrev_b32 v9,2,v9
v_add_u32 v9,147456,v9
v_lshrrev_b32 v10,4,v1
v_and_b32 v32,15,v1
v_and_b32 v33,1,v1
v_lshl_add_u32 v34,v10,2,v33
v_lshlrev_b32 v33,4,v33
v_mov_b32 v35,0xffff
v_lshlrev_b32 v33,v33,v35
v_cmp_le_u32 vcc,v34,v32
v_add_u32 v34,2,v34
s_nop 0
v_cndmask_b32 v124,0,v33,vcc
v_xor_b32 v126,v124,v33
v_cmp_le_u32 vcc,v34,v32
s_nop 1
v_cndmask_b32 v125,0,v33,vcc
v_xor_b32 v127,v125,v33
v_lshlrev_b32 v10,4,v10
v_add_u32 v10,147456,v10
s_lshl_b32 s48,s26,10
s_add_u32 s48,s48,148480
v_lshlrev_b32 v11,3,v1
v_add_u32 v11,s48,v11
s_lshl_b32 s48,s26,3
s_add_u32 s48,s48,0
v_add_u32 v0,s48,v1
v_lshrrev_b32 v0,2,v0
v_lshlrev_b32 v0,7,v0
v_cmp_gt_u32 vcc,8,v1
v_mov_b32 v3,0x7fff0000
s_nop 0
v_cndmask_b32 v0,v3,v0,vcc
v_cndmask_b32 v30,0,v30,vcc
v_cndmask_b32 v31,0,v31,vcc
v_mov_b32 v116,0xff61b1e6
v_mov_b32 v117,0xff61b1e6
v_mov_b32 v118,0xff61b1e6
v_mov_b32 v119,0xff61b1e6
v_mov_b32 v120,0xff61b1e6
v_mov_b32 v121,0xff61b1e6
v_mov_b32 v122,0xff61b1e6
v_mov_b32 v123,0xff61b1e6
v_cmp_ne_u32 vcc,0xffff,v124
s_nop 1
v_cndmask_b32 v116,0,v116,vcc
v_cmp_ne_u32 vcc,0xffff0000,v124
s_nop 1
v_cndmask_b32 v117,0,v117,vcc
v_cmp_ne_u32 vcc,0xffff,v125
s_nop 1
v_cndmask_b32 v118,0,v118,vcc
v_cmp_ne_u32 vcc,0xffff0000,v125
s_nop 1
v_cndmask_b32 v119,0,v119,vcc
v_cmp_ne_u32 vcc,0xffff,v126
s_nop 1
v_cndmask_b32 v120,0,v120,vcc
v_cmp_ne_u32 vcc,0xffff0000,v126
s_nop 1
v_cndmask_b32 v121,0,v121,vcc
v_cmp_ne_u32 vcc,0xffff,v127
s_nop 1
v_cndmask_b32 v122,0,v122,vcc
v_cmp_ne_u32 vcc,0xffff0000,v127
s_nop 1
v_cndmask_b32 v123,0,v123,vcc
v_bfe_u32 v3,v1,3,2
v_mul_u32_u24 v56,1187840,v3
v_cmp_eq_u32 vcc,3,v3
s_sub_u32 s48,0,s70
v_mov_b32 v3,s48
v_cndmask_b32 v56,v56,v3,vcc
v_and_b32 v3,7,v1
v_lshl_add_u32 v56,v3,7,v56
v_bfe_u32 v3,v1,3,1
v_lshrrev_b32 v58,4,v1
v_xor_b32 v3,v3,v58
v_xor_b32 v3,v3,v1
v_and_b32 v3,7,v3
v_and_b32 v58,15,v1
v_lshlrev_b32 v58,7,v58
v_lshl_add_u32 v58,v3,4,v58
v_xor_b32 v59,64,v58
v_add_u32 v58,24576,v58
v_add_u32 v59,24576,v59
v_cmp_gt_u32 vcc,32,v1
v_mov_b32 v1,0x7fff0000
s_nop 0
v_cndmask_b32 v1,v1,v2,vcc
s_mov_b32 s58,0x4003c000
s_mov_b32 s59,0x0
s_mov_b32 s60,0x80028000
s_mov_b32 s61,0x0
v_subrev_u32 v3,148480,v11
v_xor_b32 v3,1024,v3
v_add_u32 v3,148480,v3
s_add_u32 s74,s66,s72
s_sub_u32 s74,s66,286720
s_add_u32 s66,s66,s72
s_mov_b32 s72,1187840
s_waitcnt vmcnt(15)
s_barrier
s_add_u32 m0,s57,36480
s_nop 0
buffer_load_dwordx4 v57,s[4:7],s56 offen offset:384 lds
ds_read_b64_tr_b16 a[0:1],v68 offset:0
ds_read_b64_tr_b16 a[2:3],v68 offset:512
s_add_u32 m0,s97,114688
s_nop 0
buffer_load_dwordx4 v67,s[36:39],s96 offen offset:0 lds
ds_read_b64_tr_b16 a[4:5],v68 offset:1024
ds_read_b64_tr_b16 a[6:7],v68 offset:1536
buffer_load_dwordx4 v67,s[40:43],s96 offen offset:1024 lds
ds_read_b64_tr_b16 a[8:9],v72 offset:2048
ds_read_b64_tr_b16 a[10:11],v72 offset:2560
buffer_load_dwordx4 v67,s[44:47],s96 offen offset:2048 lds
ds_read_b64_tr_b16 a[12:13],v68 offset:12288
ds_read_b64_tr_b16 a[14:15],v68 offset:12800
ds_read_b64_tr_b16 a[16:17],v68 offset:13312
s_add_u32 s96,s96,196608
s_add_u32 m0,s97,126976
s_nop 0
buffer_load_dwordx4 v67,s[36:39],s96 offen offset:0 lds
ds_read_b64_tr_b16 a[18:19],v68 offset:13824
)#"
R"#(
ds_read_b64_tr_b16 a[20:21],v72 offset:14336
buffer_load_dwordx4 v67,s[40:43],s96 offen offset:1024 lds
ds_read_b64_tr_b16 a[22:23],v72 offset:14848
ds_read_b128 v[76:79],v58 offset:0
buffer_load_dwordx4 v67,s[44:47],s96 offen offset:2048 lds
ds_read_b128 v[80:83],v58 offset:2048
ds_read_b128 v[84:87],v59 offset:0
ds_read_b128 v[88:91],v59 offset:2048
s_add_u32 s96,s96,196608
s_waitcnt lgkmcnt(0)
s_waitcnt vmcnt(15)
s_barrier
s_add_u32 m0,s57,40448
s_nop 0
buffer_load_dwordx4 v57,s[4:7],s56 offen offset:512 lds
ds_read_b64_tr_b16 a[24:25],v69 offset:0
ds_read_b64_tr_b16 a[26:27],v69 offset:512
v_mfma_f32_16x16x32_bf16 v[20:23],a[0:3],v[16:19],0
v_mfma_f32_16x16x32_bf16 v[24:27],a[4:7],v[16:19],0
s_add_u32 m0,s97,0
s_nop 0
buffer_load_dwordx4 v67,s[36:39],s96 offen offset:0 lds
ds_read_b64_tr_b16 a[28:29],v69 offset:1024
ds_read_b64_tr_b16 a[30:31],v69 offset:1536
v_mfma_f32_16x16x32_bf16 v[32:35],v[16:19],a[8:11],0
v_mfma_f32_16x16x32_bf16 v[36:39],a[4:7],v[76:79],0
buffer_load_dwordx4 v67,s[40:43],s96 offen offset:1024 lds
ds_read_b64_tr_b16 a[32:33],v73 offset:2048
ds_read_b64_tr_b16 a[34:35],v73 offset:2560
v_mfma_f32_16x16x32_bf16 v[40:43],v[76:79],a[8:11],0
v_mfma_f32_16x16x32_bf16 v[44:47],a[0:3],v[80:83],0
buffer_load_dwordx4 v67,s[44:47],s96 offen offset:2048 lds
ds_read_b64_tr_b16 a[36:37],v69 offset:12288
ds_read_b64_tr_b16 a[38:39],v69 offset:12800
ds_read_b64_tr_b16 a[40:41],v69 offset:13312
v_mfma_f32_16x16x32_bf16 v[48:51],a[4:7],v[80:83],0
v_mfma_f32_16x16x32_bf16 v[52:55],v[80:83],a[8:11],0
v_mfma_f32_16x16x32_bf16 v[20:23],a[12:15],v[16:19],v[20:23]
s_add_u32 s96,s96,196608
s_add_u32 m0,s97,12288
s_nop 0
buffer_load_dwordx4 v67,s[36:39],s96 offen offset:0 lds
ds_read_b64_tr_b16 a[42:43],v69 offset:13824
ds_read_b64_tr_b16 a[44:45],v73 offset:14336
v_mfma_f32_16x16x32_bf16 v[24:27],a[16:19],v[16:19],v[24:27]
v_mfma_f32_16x16x32_bf16 v[32:35],v[16:19],a[20:23],v[32:35]
buffer_load_dwordx4 v67,s[40:43],s96 offen offset:1024 lds
ds_read_b64_tr_b16 a[46:47],v73 offset:14848
ds_read_b128 v[92:95],v58 offset:4096
v_mfma_f32_16x16x32_bf16 v[36:39],a[16:19],v[84:87],v[36:39]
v_mfma_f32_16x16x32_bf16 v[40:43],v[84:87],a[20:23],v[40:43]
buffer_load_dwordx4 v67,s[44:47],s96 offen offset:2048 lds
ds_read_b128 v[96:99],v58 offset:6144
ds_read_b128 v[100:103],v59 offset:4096
ds_read_b128 v[104:107],v59 offset:6144
v_mfma_f32_16x16x32_bf16 v[44:47],a[12:15],v[88:91],v[44:47]
v_mfma_f32_16x16x32_bf16 v[48:51],a[16:19],v[88:91],v[48:51]
v_mfma_f32_16x16x32_bf16 v[52:55],v[88:91],a[20:23],v[52:55]
s_add_u32 s96,s96,196608
s_waitcnt lgkmcnt(0)
s_waitcnt vmcnt(15)
s_barrier
s_add_u32 m0,s57,44416
s_nop 0
buffer_load_dwordx4 v57,s[4:7],s56 offen offset:640 lds
ds_read_b64_tr_b16 a[48:49],v70 offset:0
ds_read_b64_tr_b16 a[50:51],v70 offset:512
v_mfma_f32_16x16x32_bf16 v[20:23],a[24:27],v[16:19],v[20:23]
v_mfma_f32_16x16x32_bf16 v[24:27],a[28:31],v[16:19],v[24:27]
s_add_u32 m0,s97,139264
s_nop 0
buffer_load_dwordx4 v67,s[36:39],s96 offen offset:0 lds
ds_read_b64_tr_b16 a[52:53],v70 offset:1024
ds_read_b64_tr_b16 a[54:55],v70 offset:1536
v_mfma_f32_16x16x32_bf16 v[32:35],v[16:19],a[32:35],v[32:35]
v_mfma_f32_16x16x32_bf16 v[36:39],a[28:31],v[92:95],v[36:39]
buffer_load_dwordx4 v67,s[40:43],s96 offen offset:1024 lds
ds_read_b64_tr_b16 a[56:57],v74 offset:2048
ds_read_b64_tr_b16 a[58:59],v74 offset:2560
v_mfma_f32_16x16x32_bf16 v[40:43],v[92:95],a[32:35],v[40:43]
v_mfma_f32_16x16x32_bf16 v[44:47],a[24:27],v[96:99],v[44:47]
buffer_load_dwordx4 v67,s[44:47],s96 offen offset:2048 lds
ds_read_b64_tr_b16 a[60:61],v70 offset:12288
ds_read_b64_tr_b16 a[62:63],v70 offset:12800
ds_read_b64_tr_b16 a[64:65],v70 offset:13312
v_mfma_f32_16x16x32_bf16 v[48:51],a[28:31],v[96:99],v[48:51]
v_mfma_f32_16x16x32_bf16 v[52:55],v[96:99],a[32:35],v[52:55]
v_mfma_f32_16x16x32_bf16 v[20:23],a[36:39],v[16:19],v[20:23]
s_add_u32 s96,s96,196608
s_add_u32 m0,s97,151552
s_nop 0
buffer_load_dwordx4 v67,s[36:39],s96 offen offset:0 lds
ds_read_b64_tr_b16 a[66:67],v70 offset:13824
ds_read_b64_tr_b16 a[68:69],v74 offset:14336
v_mfma_f32_16x16x32_bf16 v[24:27],a[40:43],v[16:19],v[24:27]
v_mfma_f32_16x16x32_bf16 v[32:35],v[16:19],a[44:47],v[32:35]
buffer_load_dwordx4 v67,s[40:43],s96 offen offset:1024 lds
ds_read_b64_tr_b16 a[70:71],v74 offset:14848
ds_read_b128 v[76:79],v58 offset:8192
v_mfma_f32_16x16x32_bf16 v[36:39],a[40:43],v[100:103],v[36:39]
v_mfma_f32_16x16x32_bf16 v[40:43],v[100:103],a[44:47],v[40:43]
buffer_load_dwordx4 v67,s[44:47],s96 offen offset:2048 lds
ds_read_b128 v[80:83],v58 offset:10240
ds_read_b128 v[84:87],v59 offset:8192
ds_read_b128 v[88:91],v59 offset:10240
v_mfma_f32_16x16x32_bf16 v[44:47],a[36:39],v[104:107],v[44:47]
v_mfma_f32_16x16x32_bf16 v[48:51],a[40:43],v[104:107],v[48:51]
v_mfma_f32_16x16x32_bf16 v[52:55],v[104:107],a[44:47],v[52:55]
s_add_u32 s96,s96,196608
s_waitcnt lgkmcnt(0)
s_waitcnt vmcnt(14)
s_barrier
s_add_u32 m0,s57,48384
s_nop 0
buffer_load_dwordx4 v57,s[4:7],s56 offen offset:768 lds
ds_read_b64_tr_b16 a[72:73],v71 offset:0
ds_read_b64_tr_b16 a[74:75],v71 offset:512
v_mfma_f32_16x16x32_bf16 v[20:23],a[48:51],v[16:19],v[20:23]
v_mfma_f32_16x16x32_bf16 v[24:27],a[52:55],v[16:19],v[24:27]
s_add_u32 m0,s97,90112
s_nop 0
buffer_load_dwordx4 v67,s[36:39],s96 offen offset:0 lds
ds_read_b64_tr_b16 a[76:77],v71 offset:1024
ds_read_b64_tr_b16 a[78:79],v71 offset:1536
v_mfma_f32_16x16x32_bf16 v[32:35],v[16:19],a[56:59],v[32:35]
v_mfma_f32_16x16x32_bf16 v[36:39],a[52:55],v[76:79],v[36:39]
buffer_load_dwordx4 v67,s[40:43],s96 offen offset:1024 lds
ds_read_b64_tr_b16 a[80:81],v75 offset:2048
ds_read_b64_tr_b16 a[82:83],v75 offset:2560
v_mfma_f32_16x16x32_bf16 v[40:43],v[76:79],a[56:59],v[40:43]
v_mfma_f32_16x16x32_bf16 v[44:47],a[48:51],v[80:83],v[44:47]
buffer_load_dwordx4 v67,s[44:47],s96 offen offset:2048 lds
ds_read_b64_tr_b16 a[84:85],v71 offset:12288
ds_read_b64_tr_b16 a[86:87],v71 offset:12800
ds_read_b64_tr_b16 a[88:89],v71 offset:13312
v_mfma_f32_16x16x32_bf16 v[48:51],a[52:55],v[80:83],v[48:51]
v_mfma_f32_16x16x32_bf16 v[52:55],v[80:83],a[56:59],v[52:55]
v_mfma_f32_16x16x32_bf16 v[20:23],a[60:63],v[16:19],v[20:23]
s_add_u32 s96,s96,196608
s_add_u32 m0,s97,102400
s_nop 0
buffer_load_dwordx4 v67,s[36:39],s96 offen offset:0 lds
ds_read_b64_tr_b16 a[90:91],v71 offset:13824
ds_read_b64_tr_b16 a[92:93],v75 offset:14336
v_mfma_f32_16x16x32_bf16 v[24:27],a[64:67],v[16:19],v[24:27]
v_mfma_f32_16x16x32_bf16 v[32:35],v[16:19],a[68:71],v[32:35]
buffer_load_dwordx4 v67,s[40:43],s96 offen offset:1024 lds
ds_read_b64_tr_b16 a[94:95],v75 offset:14848
ds_read_b128 v[92:95],v58 offset:12288
v_mfma_f32_16x16x32_bf16 v[36:39],a[64:67],v[84:87],v[36:39]
v_mfma_f32_16x16x32_bf16 v[40:43],v[84:87],a[68:71],v[40:43]
buffer_load_dwordx4 v67,s[44:47],s96 offen offset:2048 lds
ds_read_b128 v[96:99],v58 offset:14336
ds_read_b128 v[100:103],v59 offset:12288
ds_read_b128 v[104:107],v59 offset:14336
v_mfma_f32_16x16x32_bf16 v[44:47],a[60:63],v[88:91],v[44:47]
v_mfma_f32_16x16x32_bf16 v[48:51],a[64:67],v[88:91],v[48:51]
v_mfma_f32_16x16x32_bf16 v[52:55],v[88:91],a[68:71],v[52:55]
s_add_u32 s96,s96,196608
s_waitcnt lgkmcnt(0)
s_waitcnt vmcnt(14)
s_barrier
s_add_u32 m0,s57,52352
s_nop 0
buffer_load_dwordx4 v57,s[4:7],s56 offen offset:896 lds
ds_read_b64_tr_b16 a[96:97],v68 offset:0
ds_read_b64_tr_b16 a[98:99],v68 offset:512
v_mfma_f32_16x16x32_bf16 v[20:23],a[72:75],v[16:19],v[20:23]
v_mfma_f32_16x16x32_bf16 v[24:27],a[76:79],v[16:19],v[24:27]
s_add_u32 m0,s97,114688
s_nop 0
buffer_load_dwordx4 v67,s[36:39],s96 offen offset:0 lds
ds_read_b64_tr_b16 a[100:101],v68 offset:1024
ds_read_b64_tr_b16 a[102:103],v68 offset:1536
v_mfma_f32_16x16x32_bf16 v[32:35],v[16:19],a[80:83],v[32:35]
v_mfma_f32_16x16x32_bf16 v[36:39],a[76:79],v[92:95],v[36:39]
buffer_load_dwordx4 v67,s[40:43],s96 offen offset:1024 lds
ds_read_b64_tr_b16 a[104:105],v72 offset:2048
ds_read_b64_tr_b16 a[106:107],v72 offset:2560
v_mfma_f32_16x16x32_bf16 v[40:43],v[92:95],a[80:83],v[40:43]
v_mfma_f32_16x16x32_bf16 v[44:47],a[72:75],v[96:99],v[44:47]
buffer_load_dwordx4 v67,s[44:47],s96 offen offset:2048 lds
ds_read_b64_tr_b16 a[108:109],v68 offset:12288
ds_read_b64_tr_b16 a[110:111],v68 offset:12800
ds_read_b64_tr_b16 a[112:113],v68 offset:13312
v_mfma_f32_16x16x32_bf16 v[48:51],a[76:79],v[96:99],v[48:51]
v_mfma_f32_16x16x32_bf16 v[52:55],v[96:99],a[80:83],v[52:55]
v_mfma_f32_16x16x32_bf16 v[20:23],a[84:87],v[16:19],v[20:23]
s_add_u32 s96,s96,196608
s_add_u32 m0,s97,126976
s_nop 0
buffer_load_dwordx4 v67,s[36:39],s96 offen offset:0 lds
ds_read_b64_tr_b16 a[114:115],v68 offset:13824
ds_read_b64_tr_b16 a[116:117],v72 offset:14336
v_mfma_f32_16x16x32_bf16 v[24:27],a[88:91],v[16:19],v[24:27]
v_mfma_f32_16x16x32_bf16 v[32:35],v[16:19],a[92:95],v[32:35]
buffer_load_dwordx4 v67,s[40:43],s96 offen offset:1024 lds
ds_read_b64_tr_b16 a[118:119],v72 offset:14848
ds_read_b128 v[76:79],v58 offset:16384
v_mfma_f32_16x16x32_bf16 v[36:39],a[88:91],v[100:103],v[36:39]
v_mfma_f32_16x16x32_bf16 v[40:43],v[100:103],a[92:95],v[40:43]
buffer_load_dwordx4 v67,s[44:47],s96 offen offset:2048 lds
ds_read_b128 v[80:83],v58 offset:18432
ds_read_b128 v[84:87],v59 offset:16384
ds_read_b128 v[88:91],v59 offset:18432
v_mfma_f32_16x16x32_bf16 v[44:47],a[84:87],v[104:107],v[44:47]
v_mfma_f32_16x16x32_bf16 v[48:51],a[88:91],v[104:107],v[48:51]
v_mfma_f32_16x16x32_bf16 v[52:55],v[104:107],a[92:95],v[52:55]
s_add_u32 s96,s96,196608
s_waitcnt lgkmcnt(0)
s_waitcnt vmcnt(14)
s_barrier
s_add_u32 m0,s57,56320
s_nop 0
buffer_load_dwordx4 v57,s[4:7],s56 offen offset:1024 lds
ds_read_b64_tr_b16 a[120:121],v69 offset:0
ds_read_b64_tr_b16 a[122:123],v69 offset:512
v_mfma_f32_16x16x32_bf16 v[20:23],a[96:99],v[16:19],v[20:23]
v_mfma_f32_16x16x32_bf16 v[24:27],a[100:103],v[16:19],v[24:27]
s_add_u32 m0,s97,0
s_nop 0
buffer_load_dwordx4 v67,s[36:39],s96 offen offset:0 lds
ds_read_b64_tr_b16 a[124:125],v69 offset:1024
ds_read_b64_tr_b16 a[126:127],v69 offset:1536
v_mfma_f32_16x16x32_bf16 v[32:35],v[16:19],a[104:107],v[32:35]
v_mfma_f32_16x16x32_bf16 v[36:39],a[100:103],v[76:79],v[36:39]
buffer_load_dwordx4 v67,s[40:43],s96 offen offset:1024 lds
ds_read_b64_tr_b16 a[128:129],v73 offset:2048
ds_read_b64_tr_b16 a[130:131],v73 offset:2560
v_mfma_f32_16x16x32_bf16 v[40:43],v[76:79],a[104:107],v[40:43]
v_mfma_f32_16x16x32_bf16 v[44:47],a[96:99],v[80:83],v[44:47]
buffer_load_dwordx4 v67,s[44:47],s96 offen offset:2048 lds
ds_read_b64_tr_b16 a[132:133],v69 offset:12288
ds_read_b64_tr_b16 a[134:135],v69 offset:12800
ds_read_b64_tr_b16 a[136:137],v69 offset:13312
v_mfma_f32_16x16x32_bf16 v[48:51],a[100:103],v[80:83],v[48:51]
v_mfma_f32_16x16x32_bf16 v[52:55],v[80:83],a[104:107],v[52:55]
v_mfma_f32_16x16x32_bf16 v[20:23],a[108:111],v[16:19],v[20:23]
s_add_u32 s96,s96,196608
s_add_u32 m0,s97,12288
s_nop 0
buffer_load_dwordx4 v67,s[36:39],s96 offen offset:0 lds
ds_read_b64_tr_b16 a[138:139],v69 offset:13824
ds_read_b64_tr_b16 a[140:141],v73 offset:14336
v_mfma_f32_16x16x32_bf16 v[24:27],a[112:115],v[16:19],v[24:27]
v_mfma_f32_16x16x32_bf16 v[32:35],v[16:19],a[116:119],v[32:35]
buffer_load_dwordx4 v67,s[40:43],s96 offen offset:1024 lds
ds_read_b64_tr_b16 a[142:143],v73 offset:14848
ds_read_b128 v[92:95],v58 offset:20480
v_mfma_f32_16x16x32_bf16 v[36:39],a[112:115],v[84:87],v[36:39]
v_mfma_f32_16x16x32_bf16 v[40:43],v[84:87],a[116:119],v[40:43]
buffer_load_dwordx4 v67,s[44:47],s96 offen offset:2048 lds
ds_read_b128 v[96:99],v58 offset:22528
ds_read_b128 v[100:103],v59 offset:20480
ds_read_b128 v[104:107],v59 offset:22528
v_mfma_f32_16x16x32_bf16 v[44:47],a[108:111],v[88:91],v[44:47]
v_mfma_f32_16x16x32_bf16 v[48:51],a[112:115],v[88:91],v[48:51]
v_mfma_f32_16x16x32_bf16 v[52:55],v[88:91],a[116:119],v[52:55]
s_add_u32 s96,s96,196608
s_waitcnt lgkmcnt(0)
s_waitcnt vmcnt(14)
s_barrier
s_add_u32 m0,s57,60288
s_nop 0
buffer_load_dwordx4 v57,s[4:7],s56 offen offset:1152 lds
ds_read_b64_tr_b16 a[144:145],v70 offset:0
ds_read_b64_tr_b16 a[146:147],v70 offset:512
v_mfma_f32_16x16x32_bf16 v[20:23],a[120:123],v[16:19],v[20:23]
v_mfma_f32_16x16x32_bf16 v[24:27],a[124:127],v[16:19],v[24:27]
s_add_u32 m0,s97,139264
s_nop 0
buffer_load_dwordx4 v67,s[36:39],s96 offen offset:0 lds
ds_read_b64_tr_b16 a[148:149],v70 offset:1024
ds_read_b64_tr_b16 a[150:151],v70 offset:1536
v_mfma_f32_16x16x32_bf16 v[32:35],v[16:19],a[128:131],v[32:35]
v_mfma_f32_16x16x32_bf16 v[36:39],a[124:127],v[92:95],v[36:39]
buffer_load_dwordx4 v67,s[40:43],s96 offen offset:1024 lds
ds_read_b64_tr_b16 a[152:153],v74 offset:2048
ds_read_b64_tr_b16 a[154:155],v74 offset:2560
v_mfma_f32_16x16x32_bf16 v[40:43],v[92:95],a[128:131],v[40:43]
v_mfma_f32_16x16x32_bf16 v[44:47],a[120:123],v[96:99],v[44:47]
buffer_load_dwordx4 v67,s[44:47],s96 offen offset:2048 lds
ds_read_b64_tr_b16 a[156:157],v70 offset:12288
ds_read_b64_tr_b16 a[158:159],v70 offset:12800
ds_read_b64_tr_b16 a[160:161],v70 offset:13312
v_mfma_f32_16x16x32_bf16 v[48:51],a[124:127],v[96:99],v[48:51]
v_mfma_f32_16x16x32_bf16 v[52:55],v[96:99],a[128:131],v[52:55]
v_mfma_f32_16x16x32_bf16 v[20:23],a[132:135],v[16:19],v[20:23]
s_add_u32 s96,s96,196608
s_add_u32 m0,s97,151552
s_nop 0
buffer_load_dwordx4 v67,s[36:39],s96 offen offset:0 lds
ds_read_b64_tr_b16 a[162:163],v70 offset:13824
ds_read_b64_tr_b16 a[164:165],v74 offset:14336
v_mfma_f32_16x16x32_bf16 v[24:27],a[136:139],v[16:19],v[24:27]
v_mfma_f32_16x16x32_bf16 v[32:35],v[16:19],a[140:143],v[32:35]
buffer_load_dwordx4 v67,s[40:43],s96 offen offset:1024 lds
ds_read_b64_tr_b16 a[166:167],v74 offset:14848
ds_read_b128 v[76:79],v58 offset:24576
v_mfma_f32_16x16x32_bf16 v[36:39],a[136:139],v[100:103],v[36:39]
v_mfma_f32_16x16x32_bf16 v[40:43],v[100:103],a[140:143],v[40:43]
buffer_load_dwordx4 v67,s[44:47],s96 offen offset:2048 lds
ds_read_b128 v[80:83],v58 offset:26624
ds_read_b128 v[84:87],v59 offset:24576
ds_read_b128 v[88:91],v59 offset:26624
v_mfma_f32_16x16x32_bf16 v[44:47],a[132:135],v[104:107],v[44:47]
v_mfma_f32_16x16x32_bf16 v[48:51],a[136:139],v[104:107],v[48:51]
v_mfma_f32_16x16x32_bf16 v[52:55],v[104:107],a[140:143],v[52:55]
s_add_u32 s96,s96,196608
s_waitcnt lgkmcnt(0)
s_waitcnt vmcnt(14)
s_barrier
s_add_u32 m0,s57,64256
s_nop 0
buffer_load_dwordx4 v57,s[4:7],s56 offen offset:1280 lds
ds_read_b64_tr_b16 a[168:169],v71 offset:0
ds_read_b64_tr_b16 a[170:171],v71 offset:512
v_mfma_f32_16x16x32_bf16 v[20:23],a[144:147],v[16:19],v[20:23]
v_mfma_f32_16x16x32_bf16 v[24:27],a[148:151],v[16:19],v[24:27]
s_add_u32 m0,s97,90112
s_nop 0
buffer_load_dwordx4 v67,s[36:39],s96 offen offset:0 lds
ds_read_b64_tr_b16 a[172:173],v71 offset:1024
ds_read_b64_tr_b16 a[174:175],v71 offset:1536
v_mfma_f32_16x16x32_bf16 v[32:35],v[16:19],a[152:155],v[32:35]
v_mfma_f32_16x16x32_bf16 v[36:39],a[148:151],v[76:79],v[36:39]
buffer_load_dwordx4 v67,s[40:43],s96 offen offset:1024 lds
ds_read_b64_tr_b16 a[176:177],v75 offset:2048
ds_read_b64_tr_b16 a[178:179],v75 offset:2560
v_mfma_f32_16x16x32_bf16 v[40:43],v[76:79],a[152:155],v[40:43]
v_mfma_f32_16x16x32_bf16 v[44:47],a[144:147],v[80:83],v[44:47]
buffer_load_dwordx4 v67,s[44:47],s96 offen offset:2048 lds
ds_read_b64_tr_b16 a[180:181],v71 offset:12288
ds_read_b64_tr_b16 a[182:183],v71 offset:12800
v_mfma_f32_16x16x32_bf16 v[48:51],a[148:151],v[80:83],v[48:51]
v_mfma_f32_16x16x32_bf16 v[52:55],v[80:83],a[152:155],v[52:55]
s_add_u32 s96,s96,196608
s_add_u32 m0,s97,102400
s_nop 0
buffer_load_dwordx4 v67,s[36:39],s96 offen offset:0 lds
ds_read_b64_tr_b16 a[184:185],v71 offset:13312
ds_read_b64_tr_b16 a[186:187],v71 offset:13824
v_mfma_f32_16x16x32_bf16 v[20:23],a[156:159],v[16:19],v[20:23]
v_mfma_f32_16x16x32_bf16 v[24:27],a[160:163],v[16:19],v[24:27]
buffer_load_dwordx4 v67,s[40:43],s96 offen offset:1024 lds
ds_read_b64_tr_b16 a[188:189],v75 offset:14336
ds_read_b64_tr_b16 a[190:191],v75 offset:14848
v_mfma_f32_16x16x32_bf16 v[32:35],v[16:19],a[164:167],v[32:35]
v_mfma_f32_16x16x32_bf16 v[36:39],a[160:163],v[84:87],v[36:39]
buffer_load_dwordx4 v67,s[44:47],s96 offen offset:2048 lds
ds_read_b128 v[92:95],v58 offset:28672
ds_read_b128 v[96:99],v58 offset:30720
v_mfma_f32_16x16x32_bf16 v[40:43],v[84:87],a[164:167],v[40:43]
v_mfma_f32_16x16x32_bf16 v[44:47],a[156:159],v[88:91],v[44:47]
s_add_u32 s96,s96,196608
buffer_load_dwordx2 v[60:61],v56,s[0:3],s66 offen sc1
ds_read_b128 v[100:103],v59 offset:28672
ds_read_b128 v[104:107],v59 offset:30720
v_mfma_f32_16x16x32_bf16 v[48:51],a[160:163],v[88:91],v[48:51]
v_mfma_f32_16x16x32_bf16 v[52:55],v[88:91],a[164:167],v[52:55]
s_waitcnt lgkmcnt(0)
s_waitcnt vmcnt(15)
s_barrier
s_add_u32 m0,s57,68224
s_nop 0
buffer_load_dwordx4 v57,s[4:7],s56 offen offset:1408 lds
ds_read_b64_tr_b16 a[192:193],v68 offset:0
ds_read_b64_tr_b16 a[194:195],v68 offset:512
v_mfma_f32_16x16x32_bf16 v[20:23],a[168:171],v[16:19],v[20:23]
v_mfma_f32_16x16x32_bf16 v[24:27],a[172:175],v[16:19],v[24:27]
s_add_u32 m0,s97,114688
s_nop 0
buffer_load_dwordx4 v67,s[36:39],s96 offen offset:0 lds
ds_read_b64_tr_b16 a[196:197],v68 offset:1024
ds_read_b64_tr_b16 a[198:199],v68 offset:1536
v_mfma_f32_16x16x32_bf16 v[32:35],v[16:19],a[176:179],v[32:35]
v_mfma_f32_16x16x32_bf16 v[36:39],a[172:175],v[92:95],v[36:39]
buffer_load_dwordx4 v67,s[40:43],s96 offen offset:1024 lds
ds_read_b64_tr_b16 a[200:201],v72 offset:2048
ds_read_b64_tr_b16 a[202:203],v72 offset:2560
v_mfma_f32_16x16x32_bf16 v[40:43],v[92:95],a[176:179],v[40:43]
v_mfma_f32_16x16x32_bf16 v[44:47],a[168:171],v[96:99],v[44:47]
buffer_load_dwordx4 v67,s[44:47],s96 offen offset:2048 lds
ds_read_b64_tr_b16 a[204:205],v68 offset:12288
ds_read_b64_tr_b16 a[206:207],v68 offset:12800
ds_read_b64_tr_b16 a[208:209],v68 offset:13312
v_mfma_f32_16x16x32_bf16 v[48:51],a[172:175],v[96:99],v[48:51]
v_mfma_f32_16x16x32_bf16 v[52:55],v[96:99],a[176:179],v[52:55]
v_mfma_f32_16x16x32_bf16 v[20:23],a[180:183],v[16:19],v[20:23]
s_add_u32 s96,s96,196608
s_add_u32 m0,s97,126976
s_nop 0
buffer_load_dwordx4 v67,s[36:39],s96 offen offset:0 lds
ds_read_b64_tr_b16 a[210:211],v68 offset:13824
ds_read_b64_tr_b16 a[212:213],v72 offset:14336
v_mfma_f32_16x16x32_bf16 v[24:27],a[184:187],v[16:19],v[24:27]
v_mfma_f32_16x16x32_bf16 v[32:35],v[16:19],a[188:191],v[32:35]
buffer_load_dwordx4 v67,s[40:43],s96 offen offset:1024 lds
ds_read_b64_tr_b16 a[214:215],v72 offset:14848
ds_read_b128 v[76:79],v58 offset:32768
v_mfma_f32_16x16x32_bf16 v[36:39],a[184:187],v[100:103],v[36:39]
v_mfma_f32_16x16x32_bf16 v[40:43],v[100:103],a[188:191],v[40:43]
buffer_load_dwordx4 v67,s[44:47],s96 offen offset:2048 lds
ds_read_b128 v[80:83],v58 offset:34816
ds_read_b128 v[84:87],v59 offset:32768
ds_read_b128 v[88:91],v59 offset:34816
v_mfma_f32_16x16x32_bf16 v[44:47],a[180:183],v[104:107],v[44:47]
v_mfma_f32_16x16x32_bf16 v[48:51],a[184:187],v[104:107],v[48:51]
v_mfma_f32_16x16x32_bf16 v[52:55],v[104:107],a[188:191],v[52:55]
s_add_u32 s96,s96,196608
s_waitcnt lgkmcnt(0)
s_waitcnt vmcnt(15)
s_barrier
s_add_u32 m0,s57,72192
s_nop 0
buffer_load_dwordx4 v57,s[4:7],s56 offen offset:1536 lds
ds_read_b64_tr_b16 a[216:217],v69 offset:0
ds_read_b64_tr_b16 a[218:219],v69 offset:512
v_mfma_f32_16x16x32_bf16 v[20:23],a[192:195],v[16:19],v[20:23]
v_mfma_f32_16x16x32_bf16 v[24:27],a[196:199],v[16:19],v[24:27]
s_add_u32 m0,s97,0
s_nop 0
buffer_load_dwordx4 v67,s[36:39],s96 offen offset:0 lds
ds_read_b64_tr_b16 a[220:221],v69 offset:1024
ds_read_b64_tr_b16 a[222:223],v69 offset:1536
v_mfma_f32_16x16x32_bf16 v[32:35],v[16:19],a[200:203],v[32:35]
v_mfma_f32_16x16x32_bf16 v[36:39],a[196:199],v[76:79],v[36:39]
buffer_load_dwordx4 v67,s[40:43],s96 offen offset:1024 lds
ds_read_b64_tr_b16 a[224:225],v73 offset:2048
ds_read_b64_tr_b16 a[226:227],v73 offset:2560
v_mfma_f32_16x16x32_bf16 v[40:43],v[76:79],a[200:203],v[40:43]
v_mfma_f32_16x16x32_bf16 v[44:47],a[192:195],v[80:83],v[44:47]
buffer_load_dwordx4 v67,s[44:47],s96 offen offset:2048 lds
ds_read_b64_tr_b16 a[228:229],v69 offset:12288
ds_read_b64_tr_b16 a[230:231],v69 offset:12800
ds_read_b64_tr_b16 a[232:233],v69 offset:13312
v_mfma_f32_16x16x32_bf16 v[48:51],a[196:199],v[80:83],v[48:51]
v_mfma_f32_16x16x32_bf16 v[52:55],v[80:83],a[200:203],v[52:55]
v_mfma_f32_16x16x32_bf16 v[20:23],a[204:207],v[16:19],v[20:23]
s_add_u32 s96,s96,196608
s_add_u32 m0,s97,12288
s_nop 0
buffer_load_dwordx4 v67,s[36:39],s96 offen offset:0 lds
ds_read_b64_tr_b16 a[234:235],v69 offset:13824
ds_read_b64_tr_b16 a[236:237],v73 offset:14336
v_mfma_f32_16x16x32_bf16 v[24:27],a[208:211],v[16:19],v[24:27]
v_mfma_f32_16x16x32_bf16 v[32:35],v[16:19],a[212:215],v[32:35]
buffer_load_dwordx4 v67,s[40:43],s96 offen offset:1024 lds
ds_read_b64_tr_b16 a[238:239],v73 offset:14848
ds_read_b128 v[92:95],v58 offset:36864
v_mfma_f32_16x16x32_bf16 v[36:39],a[208:211],v[84:87],v[36:39]
v_mfma_f32_16x16x32_bf16 v[40:43],v[84:87],a[212:215],v[40:43]
buffer_load_dwordx4 v67,s[44:47],s96 offen offset:2048 lds
ds_read_b128 v[96:99],v58 offset:38912
ds_read_b128 v[100:103],v59 offset:36864
ds_read_b128 v[104:107],v59 offset:38912
v_mfma_f32_16x16x32_bf16 v[44:47],a[204:207],v[88:91],v[44:47]
v_mfma_f32_16x16x32_bf16 v[48:51],a[208:211],v[88:91],v[48:51]
v_mfma_f32_16x16x32_bf16 v[52:55],v[88:91],a[212:215],v[52:55]
s_add_u32 s96,s96,196608
s_waitcnt lgkmcnt(0)
s_waitcnt vmcnt(15)
s_barrier
s_add_u32 m0,s57,76160
s_nop 0
buffer_load_dwordx4 v57,s[4:7],s56 offen offset:1664 lds
ds_read_b64_tr_b16 a[240:241],v70 offset:0
ds_read_b64_tr_b16 a[242:243],v70 offset:512
v_mfma_f32_16x16x32_bf16 v[20:23],a[216:219],v[16:19],v[20:23]
v_mfma_f32_16x16x32_bf16 v[24:27],a[220:223],v[16:19],v[24:27]
s_add_u32 m0,s97,139264
s_nop 0
buffer_load_dwordx4 v67,s[36:39],s96 offen offset:0 lds
ds_read_b64_tr_b16 a[244:245],v70 offset:1024
ds_read_b64_tr_b16 a[246:247],v70 offset:1536
v_mfma_f32_16x16x32_bf16 v[32:35],v[16:19],a[224:227],v[32:35]
v_mfma_f32_16x16x32_bf16 v[36:39],a[220:223],v[92:95],v[36:39]
buffer_load_dwordx4 v67,s[40:43],s96 offen offset:1024 lds
ds_read_b64_tr_b16 a[248:249],v74 offset:2048
ds_read_b64_tr_b16 a[250:251],v74 offset:2560
v_mfma_f32_16x16x32_bf16 v[40:43],v[92:95],a[224:227],v[40:43]
v_mfma_f32_16x16x32_bf16 v[44:47],a[216:219],v[96:99],v[44:47]
buffer_load_dwordx4 v67,s[44:47],s96 offen offset:2048 lds
ds_read_b64_tr_b16 a[252:253],v70 offset:12288
ds_read_b64_tr_b16 a[254:255],v70 offset:12800
ds_read_b64_tr_b16 v[128:129],v70 offset:13312
v_mfma_f32_16x16x32_bf16 v[48:51],a[220:223],v[96:99],v[48:51]
v_mfma_f32_16x16x32_bf16 v[52:55],v[96:99],a[224:227],v[52:55]
v_mfma_f32_16x16x32_bf16 v[20:23],a[228:231],v[16:19],v[20:23]
s_add_u32 s96,s96,196608
s_add_u32 m0,s97,151552
s_nop 0
buffer_load_dwordx4 v67,s[36:39],s96 offen offset:0 lds
ds_read_b64_tr_b16 v[130:131],v70 offset:13824
ds_read_b64_tr_b16 v[132:133],v74 offset:14336
v_mfma_f32_16x16x32_bf16 v[24:27],a[232:235],v[16:19],v[24:27]
v_mfma_f32_16x16x32_bf16 v[32:35],v[16:19],a[236:239],v[32:35]
buffer_load_dwordx4 v67,s[40:43],s96 offen offset:1024 lds
ds_read_b64_tr_b16 v[134:135],v74 offset:14848
ds_read_b128 v[76:79],v58 offset:40960
)#"
R"#(
v_mfma_f32_16x16x32_bf16 v[36:39],a[232:235],v[100:103],v[36:39]
v_mfma_f32_16x16x32_bf16 v[40:43],v[100:103],a[236:239],v[40:43]
buffer_load_dwordx4 v67,s[44:47],s96 offen offset:2048 lds
ds_read_b128 v[80:83],v58 offset:43008
ds_read_b128 v[84:87],v59 offset:40960
ds_read_b128 v[88:91],v59 offset:43008
v_mfma_f32_16x16x32_bf16 v[44:47],a[228:231],v[104:107],v[44:47]
v_mfma_f32_16x16x32_bf16 v[48:51],a[232:235],v[104:107],v[48:51]
v_mfma_f32_16x16x32_bf16 v[52:55],v[104:107],a[236:239],v[52:55]
s_add_u32 s96,s96,196608
s_waitcnt lgkmcnt(0)
s_waitcnt vmcnt(14)
s_barrier
s_add_u32 m0,s57,80128
s_nop 0
buffer_load_dwordx4 v57,s[4:7],s56 offen offset:1792 lds
ds_read_b64_tr_b16 v[136:137],v71 offset:0
ds_read_b64_tr_b16 v[138:139],v71 offset:512
v_mfma_f32_16x16x32_bf16 v[20:23],a[240:243],v[16:19],v[20:23]
v_mfma_f32_16x16x32_bf16 v[24:27],a[244:247],v[16:19],v[24:27]
s_add_u32 m0,s97,90112
s_nop 0
buffer_load_dwordx4 v67,s[36:39],s96 offen offset:0 lds
ds_read_b64_tr_b16 v[140:141],v71 offset:1024
ds_read_b64_tr_b16 v[142:143],v71 offset:1536
v_mfma_f32_16x16x32_bf16 v[32:35],v[16:19],a[248:251],v[32:35]
v_mfma_f32_16x16x32_bf16 v[36:39],a[244:247],v[76:79],v[36:39]
buffer_load_dwordx4 v67,s[40:43],s96 offen offset:1024 lds
ds_read_b64_tr_b16 v[144:145],v75 offset:2048
ds_read_b64_tr_b16 v[146:147],v75 offset:2560
v_mfma_f32_16x16x32_bf16 v[40:43],v[76:79],a[248:251],v[40:43]
v_mfma_f32_16x16x32_bf16 v[44:47],a[240:243],v[80:83],v[44:47]
buffer_load_dwordx4 v67,s[44:47],s96 offen offset:2048 lds
ds_read_b64_tr_b16 v[148:149],v71 offset:12288
ds_read_b64_tr_b16 v[150:151],v71 offset:12800
ds_read_b64_tr_b16 v[152:153],v71 offset:13312
v_mfma_f32_16x16x32_bf16 v[48:51],a[244:247],v[80:83],v[48:51]
v_mfma_f32_16x16x32_bf16 v[52:55],v[80:83],a[248:251],v[52:55]
v_mfma_f32_16x16x32_bf16 v[20:23],a[252:255],v[16:19],v[20:23]
s_add_u32 s96,s96,196608
s_add_u32 m0,s97,102400
s_nop 0
buffer_load_dwordx4 v67,s[36:39],s96 offen offset:0 lds
ds_read_b64_tr_b16 v[154:155],v71 offset:13824
ds_read_b64_tr_b16 v[156:157],v75 offset:14336
v_mfma_f32_16x16x32_bf16 v[24:27],v[128:131],v[16:19],v[24:27]
v_mfma_f32_16x16x32_bf16 v[32:35],v[16:19],v[132:135],v[32:35]
buffer_load_dwordx4 v67,s[40:43],s96 offen offset:1024 lds
ds_read_b64_tr_b16 v[158:159],v75 offset:14848
ds_read_b128 v[92:95],v58 offset:45056
v_mfma_f32_16x16x32_bf16 v[36:39],v[128:131],v[84:87],v[36:39]
v_mfma_f32_16x16x32_bf16 v[40:43],v[84:87],v[132:135],v[40:43]
buffer_load_dwordx4 v67,s[44:47],s96 offen offset:2048 lds
ds_read_b128 v[96:99],v58 offset:47104
ds_read_b128 v[100:103],v59 offset:45056
ds_read_b128 v[104:107],v59 offset:47104
v_mfma_f32_16x16x32_bf16 v[44:47],a[252:255],v[88:91],v[44:47]
v_mfma_f32_16x16x32_bf16 v[48:51],v[128:131],v[88:91],v[48:51]
v_mfma_f32_16x16x32_bf16 v[52:55],v[88:91],v[132:135],v[52:55]
s_add_u32 s96,s96,196608
s_waitcnt lgkmcnt(0)
s_waitcnt vmcnt(14)
s_barrier
s_add_u32 m0,s57,84096
s_nop 0
buffer_load_dwordx4 v57,s[4:7],s56 offen offset:1920 lds
ds_read_b64_tr_b16 v[160:161],v68 offset:0
v_mfma_f32_16x16x32_bf16 v[20:23],v[136:139],v[16:19],v[20:23]
s_add_u32 m0,s97,114688
s_nop 0
buffer_load_dwordx4 v67,s[36:39],s96 offen offset:0 lds
ds_read_b64_tr_b16 v[162:163],v68 offset:512
v_mfma_f32_16x16x32_bf16 v[24:27],v[140:143],v[16:19],v[24:27]
buffer_load_dwordx4 v67,s[40:43],s96 offen offset:1024 lds
ds_read_b64_tr_b16 v[164:165],v68 offset:1024
v_mfma_f32_16x16x32_bf16 v[32:35],v[16:19],v[144:147],v[32:35]
buffer_load_dwordx4 v67,s[44:47],s96 offen offset:2048 lds
ds_read_b64_tr_b16 v[166:167],v68 offset:1536
v_mfma_f32_16x16x32_bf16 v[36:39],v[140:143],v[92:95],v[36:39]
s_add_u32 s96,s96,196608
s_add_u32 m0,s97,126976
s_nop 0
buffer_load_dwordx4 v67,s[36:39],s96 offen offset:0 lds
ds_read_b64_tr_b16 v[168:169],v72 offset:2048
v_mfma_f32_16x16x32_bf16 v[40:43],v[92:95],v[144:147],v[40:43]
buffer_load_dwordx4 v67,s[40:43],s96 offen offset:1024 lds
ds_read_b64_tr_b16 v[170:171],v72 offset:2560
v_mfma_f32_16x16x32_bf16 v[44:47],v[136:139],v[96:99],v[44:47]
buffer_load_dwordx4 v67,s[44:47],s96 offen offset:2048 lds
ds_read_b64_tr_b16 v[172:173],v68 offset:12288
v_mfma_f32_16x16x32_bf16 v[48:51],v[140:143],v[96:99],v[48:51]
s_add_u32 s96,s96,196608
v_cmp_ne_u64 vcc,s[54:55],v[60:61]
s_cbranch_vccnz .L_rtqe
.L_bkqe:
buffer_load_dword v108,v6,s[32:35],s74 offen sc1
ds_read_b64_tr_b16 v[174:175],v68 offset:12800
v_mfma_f32_16x16x32_bf16 v[52:55],v[96:99],v[144:147],v[52:55]
s_add_u32 s74,s74,9216
buffer_load_dword v109,v6,s[32:35],s74 offen sc1
ds_read_b64_tr_b16 v[176:177],v68 offset:13312
v_mfma_f32_16x16x32_bf16 v[20:23],v[148:151],v[16:19],v[20:23]
s_add_u32 s74,s74,9216
buffer_load_dword v110,v6,s[32:35],s74 offen sc1
ds_read_b64_tr_b16 v[178:179],v68 offset:13824
v_mfma_f32_16x16x32_bf16 v[24:27],v[152:155],v[16:19],v[24:27]
s_add_u32 s74,s74,9216
buffer_load_dword v111,v6,s[32:35],s74 offen sc1
ds_read_b64_tr_b16 v[180:181],v72 offset:14336
v_mfma_f32_16x16x32_bf16 v[32:35],v[16:19],v[156:159],v[32:35]
s_sub_u32 s74,s66,286720
buffer_load_dword v112,v6,s[32:35],s74 offen sc1
ds_read_b64_tr_b16 v[182:183],v72 offset:14848
v_mfma_f32_16x16x32_bf16 v[36:39],v[152:155],v[100:103],v[36:39]
s_add_u32 s74,s74,9216
buffer_load_dword v113,v6,s[32:35],s74 offen sc1
ds_read_b128 v[76:79],v58 offset:49152
v_mfma_f32_16x16x32_bf16 v[40:43],v[100:103],v[156:159],v[40:43]
s_add_u32 s74,s74,9216
buffer_load_dword v114,v6,s[32:35],s74 offen sc1
ds_read_b128 v[80:83],v58 offset:51200
v_mfma_f32_16x16x32_bf16 v[44:47],v[148:151],v[104:107],v[44:47]
s_add_u32 s74,s74,9216
buffer_load_dword v115,v6,s[32:35],s74 offen sc1
ds_read_b128 v[84:87],v59 offset:49152
v_mfma_f32_16x16x32_bf16 v[48:51],v[152:155],v[104:107],v[48:51]
s_add_u32 s74,s74,9216
s_add_u32 s66,s66,1187840
s_add_u32 s63,s66,s71
s_add_u32 s66,s66,2375680
buffer_load_dwordx2 v[12:13],v0,s[0:3],s66 offen sc1
ds_read_b128 v[88:91],v59 offset:51200
v_mfma_f32_16x16x32_bf16 v[52:55],v[104:107],v[156:159],v[52:55]
s_waitcnt lgkmcnt(0)
s_waitcnt vmcnt(23)
s_barrier
s_add_u32 m0,s64,0
s_nop 0
buffer_load_dwordx4 v2,s[32:35],s63 offen offset:0 sc1 lds
ds_read_b64_tr_b16 v[184:185],v69 offset:0
v_mfma_f32_16x16x32_bf16 v[20:23],v[160:163],v[16:19],v[20:23]
buffer_load_dwordx4 v2,s[32:35],s63 offen offset:1024 sc1 lds
ds_read_b64_tr_b16 v[186:187],v69 offset:512
ds_read_b64_tr_b16 v[188:189],v69 offset:1024
v_mfma_f32_16x16x32_bf16 v[24:27],v[164:167],v[16:19],v[24:27]
v_mfma_f32_16x16x32_bf16 v[32:35],v[16:19],v[168:171],v[32:35]
buffer_load_dwordx4 v2,s[32:35],s63 offen offset:2048 sc1 lds
ds_read_b64_tr_b16 v[190:191],v69 offset:1536
ds_read_b64_tr_b16 v[192:193],v73 offset:2048
v_mfma_f32_16x16x32_bf16 v[36:39],v[164:167],v[76:79],v[36:39]
v_mfma_f32_16x16x32_bf16 v[40:43],v[76:79],v[168:171],v[40:43]
buffer_load_dwordx4 v2,s[32:35],s63 offen offset:3072 sc1 lds
ds_read_b64_tr_b16 v[194:195],v73 offset:2560
ds_read_b64_tr_b16 v[196:197],v69 offset:12288
v_mfma_f32_16x16x32_bf16 v[44:47],v[160:163],v[80:83],v[44:47]
v_mfma_f32_16x16x32_bf16 v[48:51],v[164:167],v[80:83],v[48:51]
s_add_u32 s63,s63,4096
s_add_u32 m0,s64,4096
s_nop 0
buffer_load_dwordx4 v2,s[32:35],s63 offen offset:0 sc1 lds
ds_read_b64_tr_b16 v[198:199],v69 offset:12800
v_mfma_f32_16x16x32_bf16 v[52:55],v[80:83],v[168:171],v[52:55]
buffer_load_dwordx4 v2,s[32:35],s63 offen offset:1024 sc1 lds
ds_read_b64_tr_b16 v[200:201],v69 offset:13312
ds_read_b64_tr_b16 v[202:203],v69 offset:13824
v_mfma_f32_16x16x32_bf16 v[20:23],v[172:175],v[16:19],v[20:23]
v_mfma_f32_16x16x32_bf16 v[24:27],v[176:179],v[16:19],v[24:27]
buffer_load_dwordx4 v2,s[32:35],s63 offen offset:2048 sc1 lds
ds_read_b64_tr_b16 v[204:205],v73 offset:14336
ds_read_b64_tr_b16 v[206:207],v73 offset:14848
v_mfma_f32_16x16x32_bf16 v[32:35],v[16:19],v[180:183],v[32:35]
v_mfma_f32_16x16x32_bf16 v[36:39],v[176:179],v[84:87],v[36:39]
buffer_load_dwordx4 v2,s[32:35],s63 offen offset:3072 sc1 lds
ds_read_b128 v[92:95],v58 offset:53248
ds_read_b128 v[96:99],v58 offset:55296
v_mfma_f32_16x16x32_bf16 v[40:43],v[84:87],v[180:183],v[40:43]
v_mfma_f32_16x16x32_bf16 v[44:47],v[172:175],v[88:91],v[44:47]
s_add_u32 s63,s63,4096
s_add_u32 m0,s64,8192
s_nop 0
buffer_load_dwordx4 v2,s[32:35],s63 offen offset:0 sc1 lds
ds_read_b128 v[100:103],v59 offset:53248
ds_read_b128 v[104:107],v59 offset:55296
v_mfma_f32_16x16x32_bf16 v[48:51],v[176:179],v[88:91],v[48:51]
v_mfma_f32_16x16x32_bf16 v[52:55],v[88:91],v[180:183],v[52:55]
s_waitcnt lgkmcnt(0)
s_waitcnt vmcnt(25)
s_barrier
s_add_u32 s63,s63,1179648
s_add_u32 m0,s64,36864
s_nop 0
buffer_load_dwordx4 v2,s[32:35],s63 offen offset:0 sc1 lds
ds_read_b64_tr_b16 v[208:209],v70 offset:0
ds_read_b64_tr_b16 v[210:211],v70 offset:512
ds_read_b64_tr_b16 v[212:213],v70 offset:1024
ds_read_b64_tr_b16 v[214:215],v70 offset:1536
v_mfma_f32_16x16x32_bf16 v[20:23],v[184:187],v[16:19],v[20:23]
v_mfma_f32_16x16x32_bf16 v[24:27],v[188:191],v[16:19],v[24:27]
v_mfma_f32_16x16x32_bf16 v[32:35],v[16:19],v[192:195],v[32:35]
v_mfma_f32_16x16x32_bf16 v[36:39],v[188:191],v[92:95],v[36:39]
buffer_load_dwordx4 v2,s[32:35],s63 offen offset:1024 sc1 lds
ds_read_b64_tr_b16 v[216:217],v74 offset:2048
ds_read_b64_tr_b16 v[218:219],v74 offset:2560
ds_read_b64_tr_b16 v[220:221],v70 offset:12288
ds_read_b64_tr_b16 v[222:223],v70 offset:12800
v_mfma_f32_16x16x32_bf16 v[40:43],v[92:95],v[192:195],v[40:43]
v_mfma_f32_16x16x32_bf16 v[44:47],v[184:187],v[96:99],v[44:47]
v_mfma_f32_16x16x32_bf16 v[48:51],v[188:191],v[96:99],v[48:51]
v_mfma_f32_16x16x32_bf16 v[52:55],v[96:99],v[192:195],v[52:55]
buffer_load_dwordx4 v2,s[32:35],s63 offen offset:2048 sc1 lds
ds_read_b64_tr_b16 v[224:225],v70 offset:13312
ds_read_b64_tr_b16 v[226:227],v70 offset:13824
ds_read_b64_tr_b16 v[228:229],v74 offset:14336
ds_read_b64_tr_b16 v[230:231],v74 offset:14848
v_mfma_f32_16x16x32_bf16 v[20:23],v[196:199],v[16:19],v[20:23]
v_mfma_f32_16x16x32_bf16 v[24:27],v[200:203],v[16:19],v[24:27]
v_mfma_f32_16x16x32_bf16 v[32:35],v[16:19],v[204:207],v[32:35]
v_mfma_f32_16x16x32_bf16 v[36:39],v[200:203],v[100:103],v[36:39]
buffer_load_dwordx4 v2,s[32:35],s63 offen offset:3072 sc1 lds
ds_read_b128 v[76:79],v58 offset:57344
ds_read_b128 v[80:83],v58 offset:59392
ds_read_b128 v[84:87],v59 offset:57344
ds_read_b128 v[88:91],v59 offset:59392
v_mfma_f32_16x16x32_bf16 v[40:43],v[100:103],v[204:207],v[40:43]
v_mfma_f32_16x16x32_bf16 v[44:47],v[196:199],v[104:107],v[44:47]
v_mfma_f32_16x16x32_bf16 v[48:51],v[200:203],v[104:107],v[48:51]
v_mfma_f32_16x16x32_bf16 v[52:55],v[104:107],v[204:207],v[52:55]
s_waitcnt lgkmcnt(0)
s_waitcnt vmcnt(22)
s_barrier
s_add_u32 s63,s63,4096
s_add_u32 m0,s64,40960
s_nop 0
buffer_load_dwordx4 v2,s[32:35],s63 offen offset:0 sc1 lds
ds_read_b64_tr_b16 v[232:233],v71 offset:0
ds_read_b64_tr_b16 v[234:235],v71 offset:512
ds_read_b64_tr_b16 v[236:237],v71 offset:1024
v_mfma_f32_16x16x32_bf16 v[20:23],v[208:211],v[16:19],v[20:23]
v_mfma_f32_16x16x32_bf16 v[24:27],v[212:215],v[16:19],v[24:27]
v_mfma_f32_16x16x32_bf16 v[32:35],v[16:19],v[216:219],v[32:35]
buffer_load_dwordx4 v2,s[32:35],s63 offen offset:1024 sc1 lds
ds_read_b64_tr_b16 v[238:239],v71 offset:1536
ds_read_b64_tr_b16 v[240:241],v75 offset:2048
ds_read_b64_tr_b16 v[242:243],v75 offset:2560
v_mfma_f32_16x16x32_bf16 v[36:39],v[212:215],v[76:79],v[36:39]
v_mfma_f32_16x16x32_bf16 v[40:43],v[76:79],v[216:219],v[40:43]
v_mfma_f32_16x16x32_bf16 v[44:47],v[208:211],v[80:83],v[44:47]
buffer_load_dwordx4 v2,s[32:35],s63 offen offset:2048 sc1 lds
ds_read_b64_tr_b16 v[244:245],v71 offset:12288
ds_read_b64_tr_b16 v[246:247],v71 offset:12800
ds_read_b64_tr_b16 v[248:249],v71 offset:13312
v_mfma_f32_16x16x32_bf16 v[48:51],v[212:215],v[80:83],v[48:51]
v_mfma_f32_16x16x32_bf16 v[52:55],v[80:83],v[216:219],v[52:55]
v_mfma_f32_16x16x32_bf16 v[20:23],v[220:223],v[16:19],v[20:23]
buffer_load_dwordx4 v2,s[32:35],s63 offen offset:3072 sc1 lds
ds_read_b64_tr_b16 v[250:251],v71 offset:13824
ds_read_b64_tr_b16 v[252:253],v75 offset:14336
ds_read_b64_tr_b16 v[254:255],v75 offset:14848
v_mfma_f32_16x16x32_bf16 v[24:27],v[224:227],v[16:19],v[24:27]
v_mfma_f32_16x16x32_bf16 v[32:35],v[16:19],v[228:231],v[32:35]
v_mfma_f32_16x16x32_bf16 v[36:39],v[224:227],v[84:87],v[36:39]
s_add_u32 s63,s63,4096
s_add_u32 m0,s64,45056
s_nop 0
buffer_load_dwordx4 v2,s[32:35],s63 offen offset:0 sc1 lds
ds_read_b128 v[92:95],v58 offset:61440
ds_read_b128 v[96:99],v58 offset:63488
ds_read_b128 v[100:103],v59 offset:61440
ds_read_b128 v[104:107],v59 offset:63488
v_mfma_f32_16x16x32_bf16 v[40:43],v[84:87],v[228:231],v[40:43]
v_mfma_f32_16x16x32_bf16 v[44:47],v[220:223],v[88:91],v[44:47]
v_mfma_f32_16x16x32_bf16 v[48:51],v[224:227],v[88:91],v[48:51]
v_mfma_f32_16x16x32_bf16 v[52:55],v[88:91],v[228:231],v[52:55]
s_xor_b32 s64,s64,s65
s_waitcnt lgkmcnt(0)
v_mfma_f32_16x16x32_bf16 v[20:23],v[232:235],v[16:19],v[20:23]
v_mfma_f32_16x16x32_bf16 v[24:27],v[236:239],v[16:19],v[24:27]
v_mfma_f32_16x16x32_bf16 v[32:35],v[16:19],v[240:243],v[32:35]
v_mfma_f32_16x16x32_bf16 v[36:39],v[236:239],v[92:95],v[36:39]
v_mfma_f32_16x16x32_bf16 v[40:43],v[92:95],v[240:243],v[40:43]
v_mfma_f32_16x16x32_bf16 v[44:47],v[232:235],v[96:99],v[44:47]
v_mfma_f32_16x16x32_bf16 v[48:51],v[236:239],v[96:99],v[48:51]
v_mfma_f32_16x16x32_bf16 v[52:55],v[96:99],v[240:243],v[52:55]
v_mfma_f32_16x16x32_bf16 v[20:23],v[244:247],v[16:19],v[20:23]
v_mfma_f32_16x16x32_bf16 v[24:27],v[248:251],v[16:19],v[24:27]
v_mfma_f32_16x16x32_bf16 v[32:35],v[16:19],v[252:255],v[32:35]
v_mfma_f32_16x16x32_bf16 v[36:39],v[248:251],v[100:103],v[36:39]
v_mfma_f32_16x16x32_bf16 v[40:43],v[100:103],v[252:255],v[40:43]
v_mfma_f32_16x16x32_bf16 v[44:47],v[244:247],v[104:107],v[44:47]
v_mfma_f32_16x16x32_bf16 v[48:51],v[248:251],v[104:107],v[48:51]
v_mfma_f32_16x16x32_bf16 v[52:55],v[104:107],v[252:255],v[52:55]
s_nop 2
v_mov_b32 v28,v32
s_waitcnt vmcnt(19)
v_add_f32 v108,v108,v109
v_add_f32 v110,v110,v111
v_add_f32 v108,v108,v110
s_nop 1
v_add_f32_dpp v108,v108,v108 row_ror:8 row_mask:0xf bank_mask:0xf
s_nop 1
v_add_f32_dpp v108,v108,v108 row_ror:4 row_mask:0xf bank_mask:0xf
s_nop 1
v_add_f32_dpp v108,v108,v108 row_ror:2 row_mask:0xf bank_mask:0xf
v_mul_f32 v108,0x3a800000,v108
v_mul_f32 v109,v108,v108
s_nop 1
v_sub_f32_dpp v109,v108,v109 quad_perm:[1,1,3,3] row_mask:0xf bank_mask:0xf
v_max_f32 v109,0,v109
v_cvt_pk_bf16_f32 v109,0,v109
v_add_f32 v109,0x3727c5ac,v109
v_cvt_pk_bf16_f32 v109,0,v109
v_sqrt_f32 v109,v109
v_cvt_pk_bf16_f32 v108,0,v108
v_cvt_pk_bf16_f32 v109,0,v109
v_rcp_f32 v109,v109
s_nop 0
ds_write2_b32 v8,v108,v109 offset0:0 offset1:16
v_add_f32 v112,v112,v113
v_add_f32 v114,v114,v115
v_add_f32 v112,v112,v114
s_nop 1
v_add_f32_dpp v112,v112,v112 row_ror:8 row_mask:0xf bank_mask:0xf
s_nop 1
v_add_f32_dpp v112,v112,v112 row_ror:4 row_mask:0xf bank_mask:0xf
s_nop 1
v_add_f32_dpp v112,v112,v112 row_ror:2 row_mask:0xf bank_mask:0xf
v_mul_f32 v112,0x3a800000,v112
v_mul_f32 v113,v112,v112
s_nop 1
v_sub_f32_dpp v113,v112,v113 quad_perm:[1,1,3,3] row_mask:0xf bank_mask:0xf
v_max_f32 v113,0,v113
v_cvt_pk_bf16_f32 v113,0,v113
v_add_f32 v113,0x3727c5ac,v113
v_cvt_pk_bf16_f32 v113,0,v113
v_sqrt_f32 v113,v113
v_cvt_pk_bf16_f32 v112,0,v112
v_cvt_pk_bf16_f32 v113,0,v113
v_rcp_f32 v113,v113
s_nop 0
ds_write2_b32 v8,v112,v113 offset0:32 offset1:48
s_waitcnt lgkmcnt(0)
s_barrier
ds_read2_b32 v[14:15],v9 offset0:0 offset1:16
ds_read_b128 v[92:95],v10 offset:0
ds_read_b128 v[96:99],v10 offset:64
s_add_u32 s68,s68,1187840
s_waitcnt lgkmcnt(0)
v_mul_f32 v29,0x3e8293ee,v15
v_fma_f32 v36,-v14,v24,v36
v_fma_f32 v37,-v14,v25,v37
v_fma_f32 v38,-v14,v26,v38
v_fma_f32 v39,-v14,v27,v39
v_fma_f32 v32,-v14,v20,v32
v_fma_f32 v33,-v14,v21,v33
v_fma_f32 v34,-v14,v22,v34
v_fma_f32 v35,-v14,v23,v35
v_mul_f32 v36,v36,v15
v_mul_f32 v37,v37,v15
v_mul_f32 v38,v38,v15
v_mul_f32 v39,v39,v15
v_mul_f32 v32,v32,v29
v_mul_f32 v33,v33,v29
v_mul_f32 v34,v34,v29
v_mul_f32 v35,v35,v29
v_cvt_pk_bf16_f32 v104,v36,v37
v_cvt_pk_bf16_f32 v105,v38,v39
v_cvt_pk_bf16_f32 v100,v32,v33
v_cvt_pk_bf16_f32 v101,v34,v35
ds_write2st64_b64 v11,v[100:101],v[104:105] offset0:0 offset1:1
v_fma_f32 v40,-v92,v28,v40
v_fma_f32 v41,-v93,v28,v41
v_fma_f32 v42,-v94,v28,v42
v_fma_f32 v43,-v95,v28,v43
v_mul_f32 v40,v40,v96
v_mul_f32 v41,v41,v97
v_mul_f32 v42,v42,v98
v_mul_f32 v43,v43,v99
s_waitcnt lgkmcnt(0)
s_barrier
ds_read_b64 v[102:103],v3
ds_read_b64 v[106:107],v3 offset:512
v_cvt_pk_bf16_f32 v112,v40,v41
v_cvt_pk_bf16_f32 v113,v42,v43
s_waitcnt lgkmcnt(0)
s_waitcnt vmcnt(9)
s_barrier
ds_read_b128 v[56:59],v4 offset:0
ds_read_b128 v[60:63],v4 offset:1152
ds_read_b128 v[64:67],v4 offset:2304
ds_read_b128 v[68:71],v4 offset:3456
ds_read_b128 v[72:75],v4 offset:4608
ds_read_b128 v[76:79],v4 offset:5760
.L_qgo:
.p2align 6
.L_ql:
s_bitcmp1_b64 s[58:59],s62
s_cbranch_scc1 .L_qs0
.L_qb0:
s_waitcnt lgkmcnt(2)
v_mfma_f32_16x16x32_bf16 v[32:35],a[0:3],v[56:59],0
ds_read_b128 v[80:83],v4 offset:6912
ds_read2_b32 v[14:15],v9 offset0:32 offset1:48
v_mfma_f32_16x16x32_bf16 v[32:35],a[12:15],v[60:63],v[32:35]
ds_read_b128 v[92:95],v10 offset:128
ds_read_b128 v[96:99],v10 offset:192
v_mfma_f32_16x16x32_bf16 v[36:39],a[16:19],v[60:63],0
s_add_u32 s68,s68,1187840
v_mfma_f32_16x16x32_bf16 v[36:39],a[4:7],v[56:59],v[36:39]
ds_read_b128 v[84:87],v4 offset:8064
v_mfma_f32_16x16x32_bf16 v[40:43],v[56:59],a[8:11],0
ds_read2st64_b32 v[88:89],v6 offset0:0 offset1:36
v_mfma_f32_16x16x32_bf16 v[40:43],v[60:63],a[20:23],v[40:43]
s_waitcnt vmcnt(4)
v_cmp_ne_u64 vcc,v[30:31],v[12:13]
v_mfma_f32_16x16x32_bf16 v[40:43],v[64:67],a[32:35],v[40:43]
ds_read_b128 v[56:59],v4 offset:9216
ds_read2st64_b32 v[90:91],v6 offset0:72 offset1:108
v_mfma_f32_16x16x32_bf16 v[40:43],v[68:71],a[44:47],v[40:43]
s_cbranch_vccnz .L_rtq0
.L_bkq0:
s_add_u32 s63,s66,s71
v_mfma_f32_16x16x32_bf16 v[36:39],a[40:43],v[68:71],v[36:39]
s_add_u32 s66,s66,s72
s_add_u32 m0,s64,0
v_mfma_f32_16x16x32_bf16 v[36:39],a[28:31],v[64:67],v[36:39]
ds_read_b128 v[60:63],v4 offset:10368
buffer_load_dwordx4 v2,s[32:35],s63 offen offset:0 sc1 lds
v_mfma_f32_16x16x32_bf16 v[32:35],a[24:27],v[64:67],v[32:35]
s_waitcnt lgkmcnt(5)
v_mul_f32 v29,0x3e8293ee,v15
v_mfma_f32_16x16x32_bf16 v[32:35],a[36:39],v[68:71],v[32:35]
v_fma_f32 v48,-v14,v24,v48
s_waitcnt lgkmcnt(4)
v_mfma_f32_16x16x32_bf16 v[32:35],a[48:51],v[72:75],v[32:35]
ds_read_b128 v[64:67],v4 offset:11520
v_fma_f32 v49,-v14,v25,v49
v_mfma_f32_16x16x32_bf16 v[32:35],a[60:63],v[76:79],v[32:35]
v_fma_f32 v50,-v14,v26,v50
v_fma_f32 v51,-v14,v27,v51
v_mfma_f32_16x16x32_bf16 v[36:39],a[64:67],v[76:79],v[36:39]
v_fma_f32 v44,-v14,v20,v44
v_fma_f32 v45,-v14,v21,v45
v_mfma_f32_16x16x32_bf16 v[36:39],a[52:55],v[72:75],v[36:39]
ds_read_b128 v[68:71],v4 offset:12672
v_fma_f32 v46,-v14,v22,v46
v_mfma_f32_16x16x32_bf16 v[40:43],v[72:75],a[56:59],v[40:43]
buffer_load_dwordx4 v2,s[32:35],s63 offen offset:1024 sc1 lds
v_fma_f32 v47,-v14,v23,v47
v_mfma_f32_16x16x32_bf16 v[40:43],v[76:79],a[68:71],v[40:43]
v_mul_f32 v48,v48,v15
v_mul_f32 v49,v49,v15
v_mfma_f32_16x16x32_bf16 v[40:43],v[80:83],a[80:83],v[40:43]
ds_read_b128 v[72:75],v4 offset:13824
v_mul_f32 v50,v50,v15
v_mfma_f32_16x16x32_bf16 v[40:43],v[84:87],a[92:95],v[40:43]
v_mul_f32 v51,v51,v15
v_mul_f32 v44,v44,v29
v_mfma_f32_16x16x32_bf16 v[36:39],a[88:91],v[84:87],v[36:39]
v_mul_f32 v45,v45,v29
v_mul_f32 v46,v46,v29
v_mfma_f32_16x16x32_bf16 v[36:39],a[76:79],v[80:83],v[36:39]
ds_read_b128 v[76:79],v4 offset:14976
v_mul_f32 v47,v47,v29
v_mfma_f32_16x16x32_bf16 v[32:35],a[72:75],v[80:83],v[32:35]
v_cvt_pk_bf16_f32 v108,v48,v49
v_cvt_pk_bf16_f32 v109,v50,v51
v_mfma_f32_16x16x32_bf16 v[32:35],a[84:87],v[84:87],v[32:35]
v_cvt_pk_bf16_f32 v100,v44,v45
s_waitcnt lgkmcnt(2)
v_mfma_f32_16x16x32_bf16 v[32:35],a[96:99],v[56:59],v[32:35]
ds_read_b128 v[80:83],v4 offset:16128
buffer_load_dwordx4 v2,s[32:35],s63 offen offset:2048 sc1 lds
v_mfma_f32_16x16x32_bf16 v[32:35],a[108:111],v[60:63],v[32:35]
v_cvt_pk_bf16_f32 v101,v46,v47
v_mfma_f32_16x16x32_bf16 v[36:39],a[112:115],v[60:63],v[36:39]
ds_write2st64_b64 v11,v[100:101],v[108:109] offset0:0 offset1:1
v_fma_f32 v52,-v92,v28,v52
v_mfma_f32_16x16x32_bf16 v[36:39],a[100:103],v[56:59],v[36:39]
ds_read_b128 v[84:87],v4 offset:17280
v_fma_f32 v53,-v93,v28,v53
v_mfma_f32_16x16x32_bf16 v[40:43],v[56:59],a[104:107],v[40:43]
v_fma_f32 v54,-v94,v28,v54
v_fma_f32 v55,-v95,v28,v55
v_mfma_f32_16x16x32_bf16 v[40:43],v[60:63],a[116:119],v[40:43]
v_mul_f32 v52,v52,v96
v_mul_f32 v53,v53,v97
v_mfma_f32_16x16x32_bf16 v[40:43],v[64:67],a[128:131],v[40:43]
ds_read_b128 v[56:59],v4 offset:18432
v_mul_f32 v54,v54,v98
v_mfma_f32_16x16x32_bf16 v[40:43],v[68:71],a[140:143],v[40:43]
v_mul_f32 v55,v55,v99
s_waitcnt lgkmcnt(2)
v_mfma_f32_16x16x32_bf16 v[36:39],a[136:139],v[68:71],v[36:39]
buffer_load_dwordx4 v2,s[32:35],s63 offen offset:3072 sc1 lds
s_waitcnt vmcnt(4)
v_mfma_f32_16x16x32_bf16 v[36:39],a[124:127],v[64:67],v[36:39]
ds_read_b128 v[60:63],v4 offset:19584
s_barrier
v_mfma_f32_16x16x32_bf16 v[32:35],a[120:123],v[64:67],v[32:35]
ds_read_b64 v[102:103],v3
)#"
R"#(
ds_read_b64 v[110:111],v3 offset:512
v_mfma_f32_16x16x32_bf16 v[32:35],a[132:135],v[68:71],v[32:35]
buffer_store_dwordx2 v[30:31],v2,s[92:95],s67 idxen sc1
s_waitcnt lgkmcnt(4)
v_mfma_f32_16x16x32_bf16 v[32:35],a[144:147],v[72:75],v[32:35]
ds_read_b128 v[64:67],v4 offset:20736
v_cvt_pk_bf16_f32 v114,v52,v53
v_mfma_f32_16x16x32_bf16 v[32:35],a[156:159],v[76:79],v[32:35]
v_cvt_pk_bf16_f32 v115,v54,v55
v_add_f32 v88,v88,v89
v_mfma_f32_16x16x32_bf16 v[36:39],a[160:163],v[76:79],v[36:39]
v_add_f32 v90,v90,v91
v_mfma_f32_16x16x32_bf16 v[36:39],a[148:151],v[72:75],v[36:39]
ds_read_b128 v[68:71],v4 offset:21888
s_add_u32 s63,s63,4096
v_mfma_f32_16x16x32_bf16 v[40:43],v[72:75],a[152:155],v[40:43]
s_add_u32 m0,s64,4096
v_add_f32 v88,v88,v90
v_mfma_f32_16x16x32_bf16 v[40:43],v[76:79],a[164:167],v[40:43]
buffer_load_dwordx4 v2,s[32:35],s63 offen offset:0 sc1 lds
v_add_f32_dpp v88,v88,v88 row_ror:8 row_mask:0xf bank_mask:0xf
v_mfma_f32_16x16x32_bf16 v[40:43],v[80:83],a[176:179],v[40:43]
ds_read_b128 v[72:75],v4 offset:23040
s_waitcnt lgkmcnt(3)
v_mfma_f32_16x16x32_bf16 v[48:51],v[104:107],v[100:103],v[120:123]
v_add_f32_dpp v88,v88,v88 row_ror:4 row_mask:0xf bank_mask:0xf
v_mfma_f32_16x16x32_bf16 v[44:47],v[108:111],v[100:103],v[116:119]
v_mfma_f32_16x16x32_bf16 v[40:43],v[84:87],a[188:191],v[40:43]
v_add_f32_dpp v88,v88,v88 row_ror:2 row_mask:0xf bank_mask:0xf
v_mfma_f32_16x16x32_bf16 v[36:39],a[184:187],v[84:87],v[36:39]
v_mul_f32 v88,0x3a800000,v88
v_mfma_f32_16x16x32_bf16 v[36:39],a[172:175],v[80:83],v[36:39]
ds_read_b128 v[76:79],v4 offset:24192
v_mul_f32 v89,v88,v88
v_mfma_f32_16x16x32_bf16 v[32:35],a[168:171],v[80:83],v[32:35]
v_max3_f32 v44,v44,v45,v48
v_max3_f32 v46,v46,v47,v50
v_mfma_f32_16x16x32_bf16 v[32:35],a[180:183],v[84:87],v[32:35]
v_max_f32 v44,v44,v49
s_waitcnt lgkmcnt(2)
v_mfma_f32_16x16x32_bf16 v[32:35],a[192:195],v[56:59],v[32:35]
ds_read_b128 v[80:83],v4 offset:25344
buffer_load_dwordx4 v2,s[32:35],s63 offen offset:1024 sc1 lds
v_mfma_f32_16x16x32_bf16 v[32:35],a[204:207],v[60:63],v[32:35]
v_max_f32 v46,v46,v51
v_sub_f32_dpp v89,v88,v89 quad_perm:[1,1,3,3] row_mask:0xf bank_mask:0xf
v_mfma_f32_16x16x32_bf16 v[36:39],a[208:211],v[60:63],v[36:39]
buffer_load_dwordx2 v[12:13],v0,s[0:3],s66 offen sc1
v_max_f32 v96,v44,v46
v_mfma_f32_16x16x32_bf16 v[36:39],a[196:199],v[56:59],v[36:39]
ds_read_b128 v[84:87],v4 offset:26496
v_max_f32 v97,v44,v46
v_mfma_f32_16x16x32_bf16 v[40:43],v[56:59],a[200:203],v[40:43]
v_max_f32 v89,0,v89
v_cvt_pk_bf16_f32 v88,0,v88
v_mfma_f32_16x16x32_bf16 v[40:43],v[60:63],a[212:215],v[40:43]
v_permlane16_swap_b32 v96,v97
v_mfma_f32_16x16x32_bf16 v[40:43],v[64:67],a[224:227],v[40:43]
ds_read_b128 v[56:59],v4 offset:27648
v_max_f32 v98,v96,v97
v_mfma_f32_16x16x32_bf16 v[40:43],v[68:71],a[236:239],v[40:43]
v_max_f32 v99,v96,v97
v_cvt_pk_bf16_f32 v89,0,v89
v_mfma_f32_16x16x32_bf16 v[36:39],a[232:235],v[68:71],v[36:39]
buffer_load_dwordx4 v2,s[32:35],s63 offen offset:2048 sc1 lds
v_add_f32 v89,0x3727c5ac,v89
v_mfma_f32_16x16x32_bf16 v[36:39],a[220:223],v[64:67],v[36:39]
ds_read_b128 v[60:63],v4 offset:28800
v_cvt_pk_bf16_f32 v89,0,v89
v_mfma_f32_16x16x32_bf16 v[32:35],a[216:219],v[64:67],v[32:35]
v_permlane32_swap_b32 v98,v99
v_mfma_f32_16x16x32_bf16 v[32:35],a[228:231],v[68:71],v[32:35]
v_max_f32 v96,v98,v99
s_waitcnt lgkmcnt(2)
v_mfma_f32_16x16x32_bf16 v[32:35],a[240:243],v[72:75],v[32:35]
ds_read_b128 v[64:67],v4 offset:29952
v_sub_f32 v44,v44,v96
v_mfma_f32_16x16x32_bf16 v[32:35],a[252:255],v[76:79],v[32:35]
v_exp_f32 v44,v44
v_mfma_f32_16x16x32_bf16 v[36:39],v[128:131],v[76:79],v[36:39]
v_sqrt_f32 v89,v89
v_mfma_f32_16x16x32_bf16 v[36:39],a[244:247],v[72:75],v[36:39]
ds_read_b128 v[68:71],v4 offset:31104
v_sub_f32 v46,v46,v96
v_mfma_f32_16x16x32_bf16 v[40:43],v[72:75],a[248:251],v[40:43]
buffer_load_dwordx4 v2,s[32:35],s63 offen offset:3072 sc1 lds
v_cvt_pk_bf16_f32 v44,v44,v44
v_mfma_f32_16x16x32_bf16 v[40:43],v[76:79],v[132:135],v[40:43]
v_exp_f32 v46,v46
v_mfma_f32_16x16x32_bf16 v[40:43],v[80:83],v[144:147],v[40:43]
ds_read_b128 v[72:75],v4 offset:32256
v_cvt_pk_bf16_f32 v46,v46,v46
v_mfma_f32_16x16x32_bf16 v[40:43],v[84:87],v[156:159],v[40:43]
v_and_b32 v94,v44,v124
v_and_b32 v92,v44,v126
v_mfma_f32_16x16x32_bf16 v[36:39],v[152:155],v[84:87],v[36:39]
v_and_b32 v95,v46,v125
v_and_b32 v93,v46,v127
v_mfma_f32_16x16x32_bf16 v[36:39],v[140:143],v[80:83],v[36:39]
ds_read_b128 v[76:79],v4 offset:33408
v_cvt_pk_bf16_f32 v89,0,v89
v_mfma_f32_16x16x32_bf16 v[48:51],v[16:19],v[92:95],0
v_rcp_f32 v89,v89
v_mfma_f32_16x16x32_bf16 v[52:55],v[112:115],v[92:95],0
ds_write2_b32 v8,v88,v89 offset0:0 offset1:16
v_mfma_f32_16x16x32_bf16 v[32:35],v[136:139],v[80:83],v[32:35]
v_mfma_f32_16x16x32_bf16 v[32:35],v[148:151],v[84:87],v[32:35]
s_add_u32 s63,s63,4096
s_waitcnt lgkmcnt(3)
v_mfma_f32_16x16x32_bf16 v[32:35],v[160:163],v[56:59],v[32:35]
ds_read_b128 v[80:83],v4 offset:34560
s_add_u32 m0,s64,8192
v_mfma_f32_16x16x32_bf16 v[32:35],v[172:175],v[60:63],v[32:35]
buffer_load_dwordx4 v2,s[32:35],s63 offen offset:0 sc1 lds
v_mfma_f32_16x16x32_bf16 v[36:39],v[176:179],v[60:63],v[36:39]
v_rcp_f32 v48,v48
v_mfma_f32_16x16x32_bf16 v[36:39],v[164:167],v[56:59],v[36:39]
ds_read_b128 v[84:87],v4 offset:35712
v_mul_f32 v52,v52,v48
v_mfma_f32_16x16x32_bf16 v[40:43],v[56:59],v[168:171],v[40:43]
v_xor_b32 v4,73728,v4
v_mul_f32 v53,v53,v48
v_mfma_f32_16x16x32_bf16 v[40:43],v[60:63],v[180:183],v[40:43]
s_waitcnt lgkmcnt(2)
s_barrier
v_mfma_f32_16x16x32_bf16 v[40:43],v[64:67],v[192:195],v[40:43]
ds_read_b128 v[56:59],v5 offset:0
v_mul_f32 v54,v54,v48
v_mfma_f32_16x16x32_bf16 v[40:43],v[68:71],v[204:207],v[40:43]
v_mul_f32 v55,v55,v48
v_cvt_pk_bf16_f32 v100,v52,v53
v_mfma_f32_16x16x32_bf16 v[36:39],v[200:203],v[68:71],v[36:39]
v_cvt_pk_bf16_f32 v101,v54,v55
v_mfma_f32_16x16x32_bf16 v[36:39],v[188:191],v[64:67],v[36:39]
ds_read_b128 v[60:63],v5 offset:1152
v_mfma_f32_16x16x32_bf16 v[32:35],v[184:187],v[64:67],v[32:35]
v_permlane32_swap_b32 v100,v102
v_mfma_f32_16x16x32_bf16 v[32:35],v[196:199],v[68:71],v[32:35]
s_waitcnt lgkmcnt(2)
v_mfma_f32_16x16x32_bf16 v[32:35],v[208:211],v[72:75],v[32:35]
ds_read_b128 v[64:67],v5 offset:2304
v_mfma_f32_16x16x32_bf16 v[32:35],v[220:223],v[76:79],v[32:35]
v_permlane32_swap_b32 v101,v103
v_mfma_f32_16x16x32_bf16 v[36:39],v[224:227],v[76:79],v[36:39]
buffer_store_dwordx4 v[100:103],v1,s[32:35],s68 offen sc1
s_mov_b32 s67,s68
v_mfma_f32_16x16x32_bf16 v[36:39],v[212:215],v[72:75],v[36:39]
ds_read_b128 v[68:71],v5 offset:3456
v_mfma_f32_16x16x32_bf16 v[40:43],v[72:75],v[216:219],v[40:43]
v_mfma_f32_16x16x32_bf16 v[40:43],v[76:79],v[228:231],v[40:43]
v_mfma_f32_16x16x32_bf16 v[40:43],v[80:83],v[240:243],v[40:43]
ds_read_b128 v[72:75],v5 offset:4608
v_mfma_f32_16x16x32_bf16 v[40:43],v[84:87],v[252:255],v[40:43]
v_mfma_f32_16x16x32_bf16 v[36:39],v[248:251],v[84:87],v[36:39]
v_mfma_f32_16x16x32_bf16 v[36:39],v[236:239],v[80:83],v[36:39]
ds_read_b128 v[76:79],v5 offset:5760
v_mfma_f32_16x16x32_bf16 v[32:35],v[232:235],v[80:83],v[32:35]
v_mfma_f32_16x16x32_bf16 v[32:35],v[244:247],v[84:87],v[32:35]
s_bitcmp1_b64 s[60:61],s62
s_cbranch_scc1 .L_qs1
.L_qb1:
s_waitcnt lgkmcnt(2)
v_mfma_f32_16x16x32_bf16 v[44:47],a[0:3],v[56:59],0
ds_read_b128 v[80:83],v5 offset:6912
ds_read2_b32 v[14:15],v9 offset0:0 offset1:16
v_mfma_f32_16x16x32_bf16 v[44:47],a[12:15],v[60:63],v[44:47]
ds_read_b128 v[92:95],v10 offset:0
ds_read_b128 v[96:99],v10 offset:64
v_mfma_f32_16x16x32_bf16 v[48:51],a[16:19],v[60:63],0
s_add_u32 s68,s68,1187840
v_mfma_f32_16x16x32_bf16 v[48:51],a[4:7],v[56:59],v[48:51]
ds_read_b128 v[84:87],v5 offset:8064
v_mfma_f32_16x16x32_bf16 v[52:55],v[56:59],a[8:11],0
ds_read2st64_b32 v[88:89],v6 offset0:144 offset1:180
v_mfma_f32_16x16x32_bf16 v[52:55],v[60:63],a[20:23],v[52:55]
s_waitcnt vmcnt(4)
v_cmp_ne_u64 vcc,v[30:31],v[12:13]
v_mfma_f32_16x16x32_bf16 v[52:55],v[64:67],a[32:35],v[52:55]
ds_read_b128 v[56:59],v5 offset:9216
ds_read2st64_b32 v[90:91],v6 offset0:216 offset1:252
v_mfma_f32_16x16x32_bf16 v[52:55],v[68:71],a[44:47],v[52:55]
s_cbranch_vccnz .L_rtq1
.L_bkq1:
s_add_u32 s63,s66,s71
v_mfma_f32_16x16x32_bf16 v[48:51],a[40:43],v[68:71],v[48:51]
s_add_u32 s66,s66,s72
s_add_u32 m0,s64,36864
v_mfma_f32_16x16x32_bf16 v[48:51],a[28:31],v[64:67],v[48:51]
ds_read_b128 v[60:63],v5 offset:10368
buffer_load_dwordx4 v2,s[32:35],s63 offen offset:0 sc1 lds
v_mfma_f32_16x16x32_bf16 v[44:47],a[24:27],v[64:67],v[44:47]
v_xor_b32 v6,v7,v6
s_waitcnt lgkmcnt(5)
v_mfma_f32_16x16x32_bf16 v[44:47],a[36:39],v[68:71],v[44:47]
v_mul_f32 v29,0x3e8293ee,v15
s_waitcnt lgkmcnt(4)
v_mfma_f32_16x16x32_bf16 v[44:47],a[48:51],v[72:75],v[44:47]
ds_read_b128 v[64:67],v5 offset:11520
v_fma_f32 v36,-v14,v24,v36
v_mfma_f32_16x16x32_bf16 v[44:47],a[60:63],v[76:79],v[44:47]
v_fma_f32 v37,-v14,v25,v37
v_fma_f32 v38,-v14,v26,v38
v_mfma_f32_16x16x32_bf16 v[48:51],a[64:67],v[76:79],v[48:51]
v_fma_f32 v39,-v14,v27,v39
v_fma_f32 v32,-v14,v20,v32
v_mfma_f32_16x16x32_bf16 v[48:51],a[52:55],v[72:75],v[48:51]
ds_read_b128 v[68:71],v5 offset:12672
v_fma_f32 v33,-v14,v21,v33
v_mfma_f32_16x16x32_bf16 v[52:55],v[72:75],a[56:59],v[52:55]
buffer_load_dwordx4 v2,s[32:35],s63 offen offset:1024 sc1 lds
v_fma_f32 v34,-v14,v22,v34
v_mfma_f32_16x16x32_bf16 v[52:55],v[76:79],a[68:71],v[52:55]
v_fma_f32 v35,-v14,v23,v35
v_mul_f32 v36,v36,v15
v_mfma_f32_16x16x32_bf16 v[52:55],v[80:83],a[80:83],v[52:55]
ds_read_b128 v[72:75],v5 offset:13824
v_mul_f32 v37,v37,v15
v_mfma_f32_16x16x32_bf16 v[52:55],v[84:87],a[92:95],v[52:55]
v_mul_f32 v38,v38,v15
v_mul_f32 v39,v39,v15
v_mfma_f32_16x16x32_bf16 v[48:51],a[88:91],v[84:87],v[48:51]
v_mul_f32 v32,v32,v29
v_mul_f32 v33,v33,v29
v_mfma_f32_16x16x32_bf16 v[48:51],a[76:79],v[80:83],v[48:51]
ds_read_b128 v[76:79],v5 offset:14976
v_mul_f32 v34,v34,v29
v_mfma_f32_16x16x32_bf16 v[44:47],a[72:75],v[80:83],v[44:47]
v_mul_f32 v35,v35,v29
v_cvt_pk_bf16_f32 v104,v36,v37
v_mfma_f32_16x16x32_bf16 v[44:47],a[84:87],v[84:87],v[44:47]
v_cvt_pk_bf16_f32 v105,v38,v39
s_waitcnt lgkmcnt(2)
v_mfma_f32_16x16x32_bf16 v[44:47],a[96:99],v[56:59],v[44:47]
ds_read_b128 v[80:83],v5 offset:16128
buffer_load_dwordx4 v2,s[32:35],s63 offen offset:2048 sc1 lds
v_mfma_f32_16x16x32_bf16 v[44:47],a[108:111],v[60:63],v[44:47]
v_cvt_pk_bf16_f32 v100,v32,v33
v_cvt_pk_bf16_f32 v101,v34,v35
v_mfma_f32_16x16x32_bf16 v[48:51],a[112:115],v[60:63],v[48:51]
ds_write2st64_b64 v11,v[100:101],v[104:105] offset0:0 offset1:1
v_fma_f32 v40,-v92,v28,v40
v_mfma_f32_16x16x32_bf16 v[48:51],a[100:103],v[56:59],v[48:51]
ds_read_b128 v[84:87],v5 offset:17280
v_fma_f32 v41,-v93,v28,v41
v_mfma_f32_16x16x32_bf16 v[52:55],v[56:59],a[104:107],v[52:55]
v_fma_f32 v42,-v94,v28,v42
v_fma_f32 v43,-v95,v28,v43
v_mfma_f32_16x16x32_bf16 v[52:55],v[60:63],a[116:119],v[52:55]
v_mul_f32 v40,v40,v96
v_mul_f32 v41,v41,v97
v_mfma_f32_16x16x32_bf16 v[52:55],v[64:67],a[128:131],v[52:55]
ds_read_b128 v[56:59],v5 offset:18432
v_mul_f32 v42,v42,v98
v_mfma_f32_16x16x32_bf16 v[52:55],v[68:71],a[140:143],v[52:55]
v_mul_f32 v43,v43,v99
s_waitcnt lgkmcnt(2)
v_mfma_f32_16x16x32_bf16 v[48:51],a[136:139],v[68:71],v[48:51]
buffer_load_dwordx4 v2,s[32:35],s63 offen offset:3072 sc1 lds
s_waitcnt vmcnt(4)
v_mfma_f32_16x16x32_bf16 v[48:51],a[124:127],v[64:67],v[48:51]
ds_read_b128 v[60:63],v5 offset:19584
s_barrier
v_mfma_f32_16x16x32_bf16 v[44:47],a[120:123],v[64:67],v[44:47]
ds_read_b64 v[102:103],v3
ds_read_b64 v[106:107],v3 offset:512
v_mfma_f32_16x16x32_bf16 v[44:47],a[132:135],v[68:71],v[44:47]
buffer_store_dwordx2 v[30:31],v2,s[92:95],s67 idxen sc1
s_waitcnt lgkmcnt(4)
v_mfma_f32_16x16x32_bf16 v[44:47],a[144:147],v[72:75],v[44:47]
ds_read_b128 v[64:67],v5 offset:20736
v_cvt_pk_bf16_f32 v112,v40,v41
v_mfma_f32_16x16x32_bf16 v[44:47],a[156:159],v[76:79],v[44:47]
v_cvt_pk_bf16_f32 v113,v42,v43
v_add_f32 v88,v88,v89
v_mfma_f32_16x16x32_bf16 v[48:51],a[160:163],v[76:79],v[48:51]
v_add_f32 v90,v90,v91
v_mfma_f32_16x16x32_bf16 v[48:51],a[148:151],v[72:75],v[48:51]
ds_read_b128 v[68:71],v5 offset:21888
s_add_u32 s63,s63,4096
v_mfma_f32_16x16x32_bf16 v[52:55],v[72:75],a[152:155],v[52:55]
s_add_u32 m0,s64,40960
v_add_f32 v88,v88,v90
v_mfma_f32_16x16x32_bf16 v[52:55],v[76:79],a[164:167],v[52:55]
buffer_load_dwordx4 v2,s[32:35],s63 offen offset:0 sc1 lds
v_add_f32_dpp v88,v88,v88 row_ror:8 row_mask:0xf bank_mask:0xf
v_mfma_f32_16x16x32_bf16 v[52:55],v[80:83],a[176:179],v[52:55]
ds_read_b128 v[72:75],v5 offset:23040
s_waitcnt lgkmcnt(3)
v_mfma_f32_16x16x32_bf16 v[36:39],v[108:111],v[100:103],v[120:123]
v_add_f32_dpp v88,v88,v88 row_ror:4 row_mask:0xf bank_mask:0xf
v_mfma_f32_16x16x32_bf16 v[32:35],v[104:107],v[100:103],v[116:119]
v_mfma_f32_16x16x32_bf16 v[52:55],v[84:87],a[188:191],v[52:55]
v_add_f32_dpp v88,v88,v88 row_ror:2 row_mask:0xf bank_mask:0xf
v_mfma_f32_16x16x32_bf16 v[48:51],a[184:187],v[84:87],v[48:51]
v_mul_f32 v88,0x3a800000,v88
v_mfma_f32_16x16x32_bf16 v[48:51],a[172:175],v[80:83],v[48:51]
ds_read_b128 v[76:79],v5 offset:24192
v_mul_f32 v89,v88,v88
v_mfma_f32_16x16x32_bf16 v[44:47],a[168:171],v[80:83],v[44:47]
v_max3_f32 v32,v32,v33,v36
v_max3_f32 v34,v34,v35,v38
v_mfma_f32_16x16x32_bf16 v[44:47],a[180:183],v[84:87],v[44:47]
v_max_f32 v32,v32,v37
s_waitcnt lgkmcnt(2)
v_mfma_f32_16x16x32_bf16 v[44:47],a[192:195],v[56:59],v[44:47]
ds_read_b128 v[80:83],v5 offset:25344
buffer_load_dwordx4 v2,s[32:35],s63 offen offset:1024 sc1 lds
v_mfma_f32_16x16x32_bf16 v[44:47],a[204:207],v[60:63],v[44:47]
v_max_f32 v34,v34,v39
v_sub_f32_dpp v89,v88,v89 quad_perm:[1,1,3,3] row_mask:0xf bank_mask:0xf
v_mfma_f32_16x16x32_bf16 v[48:51],a[208:211],v[60:63],v[48:51]
buffer_load_dwordx2 v[12:13],v0,s[0:3],s66 offen sc1
v_max_f32 v96,v32,v34
v_mfma_f32_16x16x32_bf16 v[48:51],a[196:199],v[56:59],v[48:51]
ds_read_b128 v[84:87],v5 offset:26496
v_max_f32 v97,v32,v34
v_mfma_f32_16x16x32_bf16 v[52:55],v[56:59],a[200:203],v[52:55]
v_max_f32 v89,0,v89
v_cvt_pk_bf16_f32 v88,0,v88
v_mfma_f32_16x16x32_bf16 v[52:55],v[60:63],a[212:215],v[52:55]
v_permlane16_swap_b32 v96,v97
v_mfma_f32_16x16x32_bf16 v[52:55],v[64:67],a[224:227],v[52:55]
ds_read_b128 v[56:59],v5 offset:27648
v_max_f32 v98,v96,v97
v_mfma_f32_16x16x32_bf16 v[52:55],v[68:71],a[236:239],v[52:55]
v_max_f32 v99,v96,v97
v_cvt_pk_bf16_f32 v89,0,v89
v_mfma_f32_16x16x32_bf16 v[48:51],a[232:235],v[68:71],v[48:51]
buffer_load_dwordx4 v2,s[32:35],s63 offen offset:2048 sc1 lds
v_add_f32 v89,0x3727c5ac,v89
v_mfma_f32_16x16x32_bf16 v[48:51],a[220:223],v[64:67],v[48:51]
ds_read_b128 v[60:63],v5 offset:28800
v_cvt_pk_bf16_f32 v89,0,v89
v_mfma_f32_16x16x32_bf16 v[44:47],a[216:219],v[64:67],v[44:47]
v_permlane32_swap_b32 v98,v99
v_mfma_f32_16x16x32_bf16 v[44:47],a[228:231],v[68:71],v[44:47]
v_max_f32 v96,v98,v99
s_waitcnt lgkmcnt(2)
v_mfma_f32_16x16x32_bf16 v[44:47],a[240:243],v[72:75],v[44:47]
ds_read_b128 v[64:67],v5 offset:29952
v_sub_f32 v32,v32,v96
v_mfma_f32_16x16x32_bf16 v[44:47],a[252:255],v[76:79],v[44:47]
v_exp_f32 v32,v32
v_mfma_f32_16x16x32_bf16 v[48:51],v[128:131],v[76:79],v[48:51]
v_sqrt_f32 v89,v89
v_mfma_f32_16x16x32_bf16 v[48:51],a[244:247],v[72:75],v[48:51]
ds_read_b128 v[68:71],v5 offset:31104
v_sub_f32 v34,v34,v96
v_mfma_f32_16x16x32_bf16 v[52:55],v[72:75],a[248:251],v[52:55]
buffer_load_dwordx4 v2,s[32:35],s63 offen offset:3072 sc1 lds
v_cvt_pk_bf16_f32 v32,v32,v32
v_mfma_f32_16x16x32_bf16 v[52:55],v[76:79],v[132:135],v[52:55]
v_exp_f32 v34,v34
v_mfma_f32_16x16x32_bf16 v[52:55],v[80:83],v[144:147],v[52:55]
ds_read_b128 v[72:75],v5 offset:32256
v_cvt_pk_bf16_f32 v34,v34,v34
v_mfma_f32_16x16x32_bf16 v[52:55],v[84:87],v[156:159],v[52:55]
v_and_b32 v92,v32,v124
v_and_b32 v94,v32,v126
v_mfma_f32_16x16x32_bf16 v[48:51],v[152:155],v[84:87],v[48:51]
v_and_b32 v93,v34,v125
v_and_b32 v95,v34,v127
v_mfma_f32_16x16x32_bf16 v[48:51],v[140:143],v[80:83],v[48:51]
ds_read_b128 v[76:79],v5 offset:33408
v_cvt_pk_bf16_f32 v89,0,v89
v_mfma_f32_16x16x32_bf16 v[36:39],v[16:19],v[92:95],0
v_rcp_f32 v89,v89
v_mfma_f32_16x16x32_bf16 v[40:43],v[112:115],v[92:95],0
ds_write2_b32 v8,v88,v89 offset0:32 offset1:48
v_mfma_f32_16x16x32_bf16 v[44:47],v[136:139],v[80:83],v[44:47]
v_mfma_f32_16x16x32_bf16 v[44:47],v[148:151],v[84:87],v[44:47]
s_add_u32 s63,s63,4096
s_waitcnt lgkmcnt(3)
v_mfma_f32_16x16x32_bf16 v[44:47],v[160:163],v[56:59],v[44:47]
ds_read_b128 v[80:83],v5 offset:34560
s_add_u32 m0,s64,45056
v_mfma_f32_16x16x32_bf16 v[44:47],v[172:175],v[60:63],v[44:47]
buffer_load_dwordx4 v2,s[32:35],s63 offen offset:0 sc1 lds
s_xor_b32 s64,s64,s65
v_mfma_f32_16x16x32_bf16 v[48:51],v[176:179],v[60:63],v[48:51]
v_rcp_f32 v36,v36
v_mfma_f32_16x16x32_bf16 v[48:51],v[164:167],v[56:59],v[48:51]
ds_read_b128 v[84:87],v5 offset:35712
v_mul_f32 v40,v40,v36
v_mfma_f32_16x16x32_bf16 v[52:55],v[56:59],v[168:171],v[52:55]
v_xor_b32 v5,73728,v5
v_mul_f32 v41,v41,v36
v_mfma_f32_16x16x32_bf16 v[52:55],v[60:63],v[180:183],v[52:55]
s_waitcnt lgkmcnt(2)
s_barrier
v_mfma_f32_16x16x32_bf16 v[52:55],v[64:67],v[192:195],v[52:55]
ds_read_b128 v[56:59],v4 offset:0
v_mul_f32 v42,v42,v36
v_mfma_f32_16x16x32_bf16 v[52:55],v[68:71],v[204:207],v[52:55]
v_mul_f32 v43,v43,v36
v_cvt_pk_bf16_f32 v100,v40,v41
v_mfma_f32_16x16x32_bf16 v[48:51],v[200:203],v[68:71],v[48:51]
v_cvt_pk_bf16_f32 v101,v42,v43
v_mfma_f32_16x16x32_bf16 v[48:51],v[188:191],v[64:67],v[48:51]
ds_read_b128 v[60:63],v4 offset:1152
v_mfma_f32_16x16x32_bf16 v[44:47],v[184:187],v[64:67],v[44:47]
v_permlane32_swap_b32 v100,v102
v_mfma_f32_16x16x32_bf16 v[44:47],v[196:199],v[68:71],v[44:47]
s_waitcnt lgkmcnt(2)
v_mfma_f32_16x16x32_bf16 v[44:47],v[208:211],v[72:75],v[44:47]
ds_read_b128 v[64:67],v4 offset:2304
v_mfma_f32_16x16x32_bf16 v[44:47],v[220:223],v[76:79],v[44:47]
v_permlane32_swap_b32 v101,v103
v_mfma_f32_16x16x32_bf16 v[48:51],v[224:227],v[76:79],v[48:51]
buffer_store_dwordx4 v[100:103],v1,s[32:35],s68 offen sc1
s_mov_b32 s67,s68
v_mfma_f32_16x16x32_bf16 v[48:51],v[212:215],v[72:75],v[48:51]
ds_read_b128 v[68:71],v4 offset:3456
v_mfma_f32_16x16x32_bf16 v[52:55],v[72:75],v[216:219],v[52:55]
v_mfma_f32_16x16x32_bf16 v[52:55],v[76:79],v[228:231],v[52:55]
v_mfma_f32_16x16x32_bf16 v[52:55],v[80:83],v[240:243],v[52:55]
ds_read_b128 v[72:75],v4 offset:4608
v_mfma_f32_16x16x32_bf16 v[52:55],v[84:87],v[252:255],v[52:55]
v_mfma_f32_16x16x32_bf16 v[48:51],v[248:251],v[84:87],v[48:51]
v_mfma_f32_16x16x32_bf16 v[48:51],v[236:239],v[80:83],v[48:51]
ds_read_b128 v[76:79],v4 offset:5760
v_mfma_f32_16x16x32_bf16 v[44:47],v[232:235],v[80:83],v[44:47]
s_sub_u32 s62,s62,1
v_mfma_f32_16x16x32_bf16 v[44:47],v[244:247],v[84:87],v[44:47]
s_cbranch_scc0 .L_ql
s_nop 1
ds_read2_b32 v[14:15],v9 offset0:32 offset1:48
ds_read_b128 v[92:95],v10 offset:128
ds_read_b128 v[96:99],v10 offset:192
s_add_u32 s68,s68,1187840
s_waitcnt lgkmcnt(0)
v_mul_f32 v29,0x3e8293ee,v15
v_fma_f32 v48,-v14,v24,v48
v_fma_f32 v49,-v14,v25,v49
v_fma_f32 v50,-v14,v26,v50
v_fma_f32 v51,-v14,v27,v51
v_fma_f32 v44,-v14,v20,v44
v_fma_f32 v45,-v14,v21,v45
v_fma_f32 v46,-v14,v22,v46
v_fma_f32 v47,-v14,v23,v47
v_mul_f32 v48,v48,v15
v_mul_f32 v49,v49,v15
v_mul_f32 v50,v50,v15
v_mul_f32 v51,v51,v15
v_mul_f32 v44,v44,v29
v_mul_f32 v45,v45,v29
v_mul_f32 v46,v46,v29
v_mul_f32 v47,v47,v29
v_cvt_pk_bf16_f32 v108,v48,v49
v_cvt_pk_bf16_f32 v109,v50,v51
v_cvt_pk_bf16_f32 v100,v44,v45
v_cvt_pk_bf16_f32 v101,v46,v47
ds_write2st64_b64 v11,v[100:101],v[108:109] offset0:0 offset1:1
v_fma_f32 v52,-v92,v28,v52
v_fma_f32 v53,-v93,v28,v53
v_fma_f32 v54,-v94,v28,v54
v_fma_f32 v55,-v95,v28,v55
v_mul_f32 v52,v52,v96
v_mul_f32 v53,v53,v97
v_mul_f32 v54,v54,v98
v_mul_f32 v55,v55,v99
s_waitcnt lgkmcnt(0)
s_waitcnt vmcnt(0)
s_barrier
ds_read_b64 v[102:103],v3
ds_read_b64 v[110:111],v3 offset:512
buffer_store_dwordx2 v[30:31],v2,s[92:95],s67 idxen sc1
v_cvt_pk_bf16_f32 v114,v52,v53
v_cvt_pk_bf16_f32 v115,v54,v55
s_waitcnt lgkmcnt(0)
v_mfma_f32_16x16x32_bf16 v[48:51],v[104:107],v[100:103],v[120:123]
v_mfma_f32_16x16x32_bf16 v[44:47],v[108:111],v[100:103],v[116:119]
s_nop 7
v_max3_f32 v44,v44,v45,v48
v_max3_f32 v46,v46,v47,v50
v_max_f32 v44,v44,v49
v_max_f32 v46,v46,v51
v_max_f32 v96,v44,v46
v_max_f32 v97,v44,v46
s_nop 1
v_permlane16_swap_b32 v96,v97
v_max_f32 v98,v96,v97
v_max_f32 v99,v96,v97
s_nop 1
v_permlane32_swap_b32 v98,v99
v_max_f32 v96,v98,v99
v_sub_f32 v44,v44,v96
v_sub_f32 v46,v46,v96
v_exp_f32 v44,v44
v_exp_f32 v46,v46
v_cvt_pk_bf16_f32 v44,v44,v44
v_cvt_pk_bf16_f32 v46,v46,v46
v_and_b32 v94,v44,v124
v_and_b32 v92,v44,v126
v_and_b32 v95,v46,v125
v_and_b32 v93,v46,v127
s_nop 1
v_mfma_f32_16x16x32_bf16 v[48:51],v[16:19],v[92:95],0
v_mfma_f32_16x16x32_bf16 v[52:55],v[112:115],v[92:95],0
s_nop 6
)#"
R"#(
v_rcp_f32 v48,v48
s_nop 0
v_mul_f32 v52,v52,v48
v_mul_f32 v53,v53,v48
v_mul_f32 v54,v54,v48
v_mul_f32 v55,v55,v48
v_cvt_pk_bf16_f32 v100,v52,v53
v_cvt_pk_bf16_f32 v101,v54,v55
s_nop 0
v_permlane32_swap_b32 v100,v102
v_permlane32_swap_b32 v101,v103
buffer_store_dwordx4 v[100:103],v1,s[32:35],s68 offen sc1
s_mov_b32 s67,s68
s_waitcnt vmcnt(0)
s_barrier
buffer_store_dwordx2 v[30:31],v2,s[92:95],s67 idxen sc1
s_branch .L_two
.L_w:
s_mov_b32 s96,0
s_add_u32 s36,s12,0x0
s_addc_u32 s37,s13,0x0
s_and_b32 s37,s37,0xffff
s_mov_b32 s38,0x200000
s_mov_b32 s39,0x20000
s_add_u32 s40,s12,0xfffffc80
s_addc_u32 s41,s13,0xffffffff
s_and_b32 s41,s41,0xffff
s_mov_b32 s42,0x200380
s_mov_b32 s43,0x20000
s_lshl_b32 s48,s27,8
s_add_u32 s96,s96,s48
v_and_b32 v8,3,v1
v_mul_u32_u24 v8,16,v8
s_and_b32 s48,s26,1
s_lshl_b32 s48,s48,6
s_lshr_b32 s49,s26,1
s_lshl_b32 s49,s49,10
s_add_u32 s48,s48,s49
v_add_u32 v8,s48,v8
v_lshrrev_b32 v4,3,v1
v_bfe_u32 v5,v1,4,1
s_and_b32 s48,s26,1
s_lshl_b32 s48,s48,1
v_or_b32 v5,s48,v5
v_lshlrev_b32 v5,1,v5
v_and_b32 v6,7,v1
v_xor_b32 v5,v5,v6
v_mul_u32_u24 v4,2048,v4
v_lshl_add_u32 v7,v5,4,v4
s_mul_i32 s48,s26,16384
s_add_u32 s96,s96,s48
s_mul_i32 s97,s26,2048
s_add_u32 m0,s97,0
s_nop 0
buffer_load_dwordx4 v7,s[36:39],s96 offen offset:0 lds
buffer_load_dwordx4 v7,s[40:43],s96 offen offset:1024 lds
s_add_u32 s96,s96,65536
s_add_u32 m0,s97,8192
s_nop 0
buffer_load_dwordx4 v7,s[36:39],s96 offen offset:0 lds
buffer_load_dwordx4 v7,s[40:43],s96 offen offset:1024 lds
s_add_u32 s96,s96,65536
s_add_u32 m0,s97,16384
s_nop 0
buffer_load_dwordx4 v7,s[36:39],s96 offen offset:0 lds
buffer_load_dwordx4 v7,s[40:43],s96 offen offset:1024 lds
s_add_u32 s96,s96,65536
s_add_u32 m0,s97,24576
s_nop 0
buffer_load_dwordx4 v7,s[36:39],s96 offen offset:0 lds
buffer_load_dwordx4 v7,s[40:43],s96 offen offset:1024 lds
s_add_u32 s96,s96,65536
s_add_u32 m0,s97,32768
s_nop 0
buffer_load_dwordx4 v7,s[36:39],s96 offen offset:0 lds
buffer_load_dwordx4 v7,s[40:43],s96 offen offset:1024 lds
s_add_u32 s96,s96,65536
s_add_u32 m0,s97,40960
s_nop 0
buffer_load_dwordx4 v7,s[36:39],s96 offen offset:0 lds
buffer_load_dwordx4 v7,s[40:43],s96 offen offset:1024 lds
s_add_u32 s96,s96,65536
s_add_u32 m0,s97,49152
s_nop 0
buffer_load_dwordx4 v7,s[36:39],s96 offen offset:0 lds
buffer_load_dwordx4 v7,s[40:43],s96 offen offset:1024 lds
s_add_u32 s96,s96,65536
s_add_u32 m0,s97,57344
s_nop 0
buffer_load_dwordx4 v7,s[36:39],s96 offen offset:0 lds
buffer_load_dwordx4 v7,s[40:43],s96 offen offset:1024 lds
s_add_u32 s96,s96,65536
s_add_u32 m0,s97,65536
s_nop 0
buffer_load_dwordx4 v7,s[36:39],s96 offen offset:0 lds
buffer_load_dwordx4 v7,s[40:43],s96 offen offset:1024 lds
s_add_u32 s96,s96,65536
s_add_u32 m0,s97,73728
s_nop 0
buffer_load_dwordx4 v7,s[36:39],s96 offen offset:0 lds
buffer_load_dwordx4 v7,s[40:43],s96 offen offset:1024 lds
s_add_u32 s96,s96,65536
s_add_u32 m0,s97,81920
s_nop 0
buffer_load_dwordx4 v7,s[36:39],s96 offen offset:0 lds
buffer_load_dwordx4 v7,s[40:43],s96 offen offset:1024 lds
s_add_u32 s96,s96,65536
s_add_u32 m0,s97,90112
s_nop 0
buffer_load_dwordx4 v7,s[36:39],s96 offen offset:0 lds
buffer_load_dwordx4 v7,s[40:43],s96 offen offset:1024 lds
s_add_u32 s96,s96,65536
s_add_u32 m0,s97,98304
s_nop 0
buffer_load_dwordx4 v7,s[36:39],s96 offen offset:0 lds
buffer_load_dwordx4 v7,s[40:43],s96 offen offset:1024 lds
s_add_u32 s96,s96,65536
s_add_u32 m0,s97,106496
s_nop 0
buffer_load_dwordx4 v7,s[36:39],s96 offen offset:0 lds
buffer_load_dwordx4 v7,s[40:43],s96 offen offset:1024 lds
s_add_u32 s96,s96,65536
v_lshrrev_b32 v5,4,v1
v_bfe_u32 v4,v1,3,1
v_and_b32 v6,1,v5
v_lshl_add_u32 v4,v6,1,v4
v_lshlrev_b32 v4,5,v4
v_mul_u32_u24 v5,2048,v5
v_bfe_u32 v6,v1,2,2
v_lshl_add_u32 v5,v6,7,v5
v_xor_b32 v8,v4,v8
v_add_u32 v8,v5,v8
v_add_u32 v9,65536,v8
s_waitcnt vmcnt(24)
s_barrier
s_add_u32 m0,s97,114688
s_nop 0
buffer_load_dwordx4 v7,s[36:39],s96 offen offset:0 lds
buffer_load_dwordx4 v7,s[40:43],s96 offen offset:1024 lds
s_add_u32 s96,s96,65536
s_add_u32 m0,s97,122880
s_nop 0
buffer_load_dwordx4 v7,s[36:39],s96 offen offset:0 lds
buffer_load_dwordx4 v7,s[40:43],s96 offen offset:1024 lds
s_add_u32 s96,s96,65536
ds_read_b64_tr_b16 a[0:1],v8 offset:0
ds_read_b64_tr_b16 a[2:3],v8 offset:512
ds_read_b64_tr_b16 a[4:5],v8 offset:8
ds_read_b64_tr_b16 a[6:7],v8 offset:520
ds_read_b64_tr_b16 a[8:9],v8 offset:8192
ds_read_b64_tr_b16 a[10:11],v8 offset:8704
ds_read_b64_tr_b16 a[12:13],v8 offset:8200
ds_read_b64_tr_b16 a[14:15],v8 offset:8712
s_waitcnt lgkmcnt(0)
s_waitcnt vmcnt(24)
s_barrier
s_add_u32 m0,s97,0
s_nop 0
buffer_load_dwordx4 v7,s[36:39],s96 offen offset:0 lds
buffer_load_dwordx4 v7,s[40:43],s96 offen offset:1024 lds
s_add_u32 s96,s96,65536
s_add_u32 m0,s97,8192
s_nop 0
buffer_load_dwordx4 v7,s[36:39],s96 offen offset:0 lds
buffer_load_dwordx4 v7,s[40:43],s96 offen offset:1024 lds
s_add_u32 s96,s96,65536
ds_read_b64_tr_b16 a[16:17],v8 offset:16384
ds_read_b64_tr_b16 a[18:19],v8 offset:16896
ds_read_b64_tr_b16 a[20:21],v8 offset:16392
ds_read_b64_tr_b16 a[22:23],v8 offset:16904
ds_read_b64_tr_b16 a[24:25],v8 offset:24576
ds_read_b64_tr_b16 a[26:27],v8 offset:25088
ds_read_b64_tr_b16 a[28:29],v8 offset:24584
ds_read_b64_tr_b16 a[30:31],v8 offset:25096
s_waitcnt lgkmcnt(0)
s_waitcnt vmcnt(24)
s_barrier
s_add_u32 m0,s97,16384
s_nop 0
buffer_load_dwordx4 v7,s[36:39],s96 offen offset:0 lds
buffer_load_dwordx4 v7,s[40:43],s96 offen offset:1024 lds
s_add_u32 s96,s96,65536
s_add_u32 m0,s97,24576
s_nop 0
buffer_load_dwordx4 v7,s[36:39],s96 offen offset:0 lds
buffer_load_dwordx4 v7,s[40:43],s96 offen offset:1024 lds
s_add_u32 s96,s96,65536
ds_read_b64_tr_b16 a[32:33],v8 offset:32768
ds_read_b64_tr_b16 a[34:35],v8 offset:33280
ds_read_b64_tr_b16 a[36:37],v8 offset:32776
ds_read_b64_tr_b16 a[38:39],v8 offset:33288
ds_read_b64_tr_b16 a[40:41],v8 offset:40960
ds_read_b64_tr_b16 a[42:43],v8 offset:41472
ds_read_b64_tr_b16 a[44:45],v8 offset:40968
ds_read_b64_tr_b16 a[46:47],v8 offset:41480
s_waitcnt lgkmcnt(0)
s_waitcnt vmcnt(24)
s_barrier
s_add_u32 m0,s97,32768
s_nop 0
buffer_load_dwordx4 v7,s[36:39],s96 offen offset:0 lds
buffer_load_dwordx4 v7,s[40:43],s96 offen offset:1024 lds
s_add_u32 s96,s96,65536
s_add_u32 m0,s97,40960
s_nop 0
buffer_load_dwordx4 v7,s[36:39],s96 offen offset:0 lds
buffer_load_dwordx4 v7,s[40:43],s96 offen offset:1024 lds
s_add_u32 s96,s96,65536
ds_read_b64_tr_b16 a[48:49],v8 offset:49152
ds_read_b64_tr_b16 a[50:51],v8 offset:49664
ds_read_b64_tr_b16 a[52:53],v8 offset:49160
ds_read_b64_tr_b16 a[54:55],v8 offset:49672
ds_read_b64_tr_b16 a[56:57],v8 offset:57344
ds_read_b64_tr_b16 a[58:59],v8 offset:57856
ds_read_b64_tr_b16 a[60:61],v8 offset:57352
ds_read_b64_tr_b16 a[62:63],v8 offset:57864
s_waitcnt lgkmcnt(0)
s_waitcnt vmcnt(24)
s_barrier
s_add_u32 m0,s97,49152
s_nop 0
buffer_load_dwordx4 v7,s[36:39],s96 offen offset:0 lds
buffer_load_dwordx4 v7,s[40:43],s96 offen offset:1024 lds
s_add_u32 s96,s96,65536
s_add_u32 m0,s97,57344
s_nop 0
buffer_load_dwordx4 v7,s[36:39],s96 offen offset:0 lds
buffer_load_dwordx4 v7,s[40:43],s96 offen offset:1024 lds
s_add_u32 s96,s96,65536
ds_read_b64_tr_b16 a[64:65],v9 offset:0
ds_read_b64_tr_b16 a[66:67],v9 offset:512
ds_read_b64_tr_b16 a[68:69],v9 offset:8
ds_read_b64_tr_b16 a[70:71],v9 offset:520
ds_read_b64_tr_b16 a[72:73],v9 offset:8192
ds_read_b64_tr_b16 a[74:75],v9 offset:8704
ds_read_b64_tr_b16 a[76:77],v9 offset:8200
ds_read_b64_tr_b16 a[78:79],v9 offset:8712
s_waitcnt lgkmcnt(0)
s_waitcnt vmcnt(24)
s_barrier
s_add_u32 m0,s97,65536
s_nop 0
buffer_load_dwordx4 v7,s[36:39],s96 offen offset:0 lds
buffer_load_dwordx4 v7,s[40:43],s96 offen offset:1024 lds
s_add_u32 s96,s96,65536
s_add_u32 m0,s97,73728
s_nop 0
buffer_load_dwordx4 v7,s[36:39],s96 offen offset:0 lds
buffer_load_dwordx4 v7,s[40:43],s96 offen offset:1024 lds
s_add_u32 s96,s96,65536
ds_read_b64_tr_b16 a[80:81],v9 offset:16384
ds_read_b64_tr_b16 a[82:83],v9 offset:16896
ds_read_b64_tr_b16 a[84:85],v9 offset:16392
ds_read_b64_tr_b16 a[86:87],v9 offset:16904
ds_read_b64_tr_b16 a[88:89],v9 offset:24576
ds_read_b64_tr_b16 a[90:91],v9 offset:25088
ds_read_b64_tr_b16 a[92:93],v9 offset:24584
ds_read_b64_tr_b16 a[94:95],v9 offset:25096
s_waitcnt lgkmcnt(0)
s_waitcnt vmcnt(24)
s_barrier
s_add_u32 m0,s97,81920
s_nop 0
buffer_load_dwordx4 v7,s[36:39],s96 offen offset:0 lds
buffer_load_dwordx4 v7,s[40:43],s96 offen offset:1024 lds
s_add_u32 s96,s96,65536
s_add_u32 m0,s97,90112
s_nop 0
buffer_load_dwordx4 v7,s[36:39],s96 offen offset:0 lds
buffer_load_dwordx4 v7,s[40:43],s96 offen offset:1024 lds
s_add_u32 s96,s96,65536
ds_read_b64_tr_b16 a[96:97],v9 offset:32768
ds_read_b64_tr_b16 a[98:99],v9 offset:33280
ds_read_b64_tr_b16 a[100:101],v9 offset:32776
ds_read_b64_tr_b16 a[102:103],v9 offset:33288
ds_read_b64_tr_b16 a[104:105],v9 offset:40960
ds_read_b64_tr_b16 a[106:107],v9 offset:41472
ds_read_b64_tr_b16 a[108:109],v9 offset:40968
ds_read_b64_tr_b16 a[110:111],v9 offset:41480
s_waitcnt lgkmcnt(0)
s_waitcnt vmcnt(24)
s_barrier
s_add_u32 m0,s97,98304
s_nop 0
buffer_load_dwordx4 v7,s[36:39],s96 offen offset:0 lds
buffer_load_dwordx4 v7,s[40:43],s96 offen offset:1024 lds
s_add_u32 s96,s96,65536
s_add_u32 m0,s97,106496
s_nop 0
buffer_load_dwordx4 v7,s[36:39],s96 offen offset:0 lds
buffer_load_dwordx4 v7,s[40:43],s96 offen offset:1024 lds
s_add_u32 s96,s96,65536
ds_read_b64_tr_b16 a[112:113],v9 offset:49152
ds_read_b64_tr_b16 a[114:115],v9 offset:49664
ds_read_b64_tr_b16 a[116:117],v9 offset:49160
ds_read_b64_tr_b16 a[118:119],v9 offset:49672
ds_read_b64_tr_b16 a[120:121],v9 offset:57344
ds_read_b64_tr_b16 a[122:123],v9 offset:57856
ds_read_b64_tr_b16 a[124:125],v9 offset:57352
ds_read_b64_tr_b16 a[126:127],v9 offset:57864
s_waitcnt lgkmcnt(0)
s_waitcnt vmcnt(24)
s_barrier
s_add_u32 m0,s97,114688
s_nop 0
buffer_load_dwordx4 v7,s[36:39],s96 offen offset:0 lds
buffer_load_dwordx4 v7,s[40:43],s96 offen offset:1024 lds
s_add_u32 s96,s96,65536
s_add_u32 m0,s97,122880
s_nop 0
buffer_load_dwordx4 v7,s[36:39],s96 offen offset:0 lds
buffer_load_dwordx4 v7,s[40:43],s96 offen offset:1024 lds
s_add_u32 s96,s96,65536
ds_read_b64_tr_b16 a[128:129],v8 offset:0
ds_read_b64_tr_b16 a[130:131],v8 offset:512
ds_read_b64_tr_b16 a[132:133],v8 offset:8
ds_read_b64_tr_b16 a[134:135],v8 offset:520
ds_read_b64_tr_b16 a[136:137],v8 offset:8192
ds_read_b64_tr_b16 a[138:139],v8 offset:8704
ds_read_b64_tr_b16 a[140:141],v8 offset:8200
ds_read_b64_tr_b16 a[142:143],v8 offset:8712
s_waitcnt lgkmcnt(0)
s_waitcnt vmcnt(24)
s_barrier
ds_read_b64_tr_b16 a[144:145],v8 offset:16384
ds_read_b64_tr_b16 a[146:147],v8 offset:16896
ds_read_b64_tr_b16 a[148:149],v8 offset:16392
ds_read_b64_tr_b16 a[150:151],v8 offset:16904
ds_read_b64_tr_b16 a[152:153],v8 offset:24576
ds_read_b64_tr_b16 a[154:155],v8 offset:25088
ds_read_b64_tr_b16 a[156:157],v8 offset:24584
ds_read_b64_tr_b16 a[158:159],v8 offset:25096
s_waitcnt lgkmcnt(0)
s_waitcnt vmcnt(20)
s_barrier
ds_read_b64_tr_b16 a[160:161],v8 offset:32768
ds_read_b64_tr_b16 a[162:163],v8 offset:33280
ds_read_b64_tr_b16 a[164:165],v8 offset:32776
ds_read_b64_tr_b16 a[166:167],v8 offset:33288
ds_read_b64_tr_b16 a[168:169],v8 offset:40960
ds_read_b64_tr_b16 a[170:171],v8 offset:41472
ds_read_b64_tr_b16 a[172:173],v8 offset:40968
ds_read_b64_tr_b16 a[174:175],v8 offset:41480
s_waitcnt lgkmcnt(0)
s_waitcnt vmcnt(16)
s_barrier
ds_read_b64_tr_b16 a[176:177],v8 offset:49152
ds_read_b64_tr_b16 a[178:179],v8 offset:49664
ds_read_b64_tr_b16 a[180:181],v8 offset:49160
ds_read_b64_tr_b16 a[182:183],v8 offset:49672
ds_read_b64_tr_b16 a[184:185],v8 offset:57344
ds_read_b64_tr_b16 a[186:187],v8 offset:57856
ds_read_b64_tr_b16 a[188:189],v8 offset:57352
ds_read_b64_tr_b16 a[190:191],v8 offset:57864
s_waitcnt lgkmcnt(0)
s_waitcnt vmcnt(12)
s_barrier
ds_read_b64_tr_b16 a[192:193],v9 offset:0
ds_read_b64_tr_b16 a[194:195],v9 offset:512
ds_read_b64_tr_b16 a[196:197],v9 offset:8
ds_read_b64_tr_b16 a[198:199],v9 offset:520
ds_read_b64_tr_b16 a[200:201],v9 offset:8192
ds_read_b64_tr_b16 a[202:203],v9 offset:8704
ds_read_b64_tr_b16 a[204:205],v9 offset:8200
ds_read_b64_tr_b16 a[206:207],v9 offset:8712
s_waitcnt lgkmcnt(0)
s_waitcnt vmcnt(8)
s_barrier
ds_read_b64_tr_b16 a[208:209],v9 offset:16384
ds_read_b64_tr_b16 a[210:211],v9 offset:16896
ds_read_b64_tr_b16 a[212:213],v9 offset:16392
ds_read_b64_tr_b16 a[214:215],v9 offset:16904
ds_read_b64_tr_b16 a[216:217],v9 offset:24576
ds_read_b64_tr_b16 a[218:219],v9 offset:25088
ds_read_b64_tr_b16 a[220:221],v9 offset:24584
ds_read_b64_tr_b16 a[222:223],v9 offset:25096
s_waitcnt lgkmcnt(0)
s_waitcnt vmcnt(4)
s_barrier
ds_read_b64_tr_b16 a[224:225],v9 offset:32768
ds_read_b64_tr_b16 a[226:227],v9 offset:33280
ds_read_b64_tr_b16 a[228:229],v9 offset:32776
ds_read_b64_tr_b16 a[230:231],v9 offset:33288
ds_read_b64_tr_b16 a[232:233],v9 offset:40960
ds_read_b64_tr_b16 a[234:235],v9 offset:41472
ds_read_b64_tr_b16 a[236:237],v9 offset:40968
ds_read_b64_tr_b16 a[238:239],v9 offset:41480
s_waitcnt lgkmcnt(0)
s_waitcnt vmcnt(0)
s_barrier
ds_read_b64_tr_b16 a[240:241],v9 offset:49152
ds_read_b64_tr_b16 a[242:243],v9 offset:49664
ds_read_b64_tr_b16 a[244:245],v9 offset:49160
ds_read_b64_tr_b16 a[246:247],v9 offset:49672
ds_read_b64_tr_b16 a[248:249],v9 offset:57344
ds_read_b64_tr_b16 a[250:251],v9 offset:57856
ds_read_b64_tr_b16 a[252:253],v9 offset:57352
ds_read_b64_tr_b16 a[254:255],v9 offset:57864
s_waitcnt lgkmcnt(0)
s_barrier
s_lshl2_add_u32 s50,s27,s26
s_and_b32 s51,s50,6
s_lshl_b32 s51,s51,1
v_add_u32 v3,s51,v1
v_bfi_b32 v3,15,v3,v1
v_lshlrev_b32 v3,2,v3
v_cmp_gt_u32 vcc,32,v1
v_mov_b32 v15,0x7fff0000
s_nop 0
v_cndmask_b32 v15,v15,v3,vcc
s_mul_i32 s48,s50,1152
s_add_u32 s68,s48,4681728
s_lshl2_add_u32 s49,s27,96
v_lshlrev_b32 v7,3,v1
s_mov_b32 s30,593920
s_mul_i32 s57,s28,296960
s_mov_b32 s56,76021760
s_cmp_lg_u32 s25,0
s_cmov_b32 s30,296960
s_cmov_b32 s57,81960960
s_cmov_b32 s56,81960960
s_lshl_b32 s31,s30,1
s_waitcnt lgkmcnt(0)
s_add_u32 s54,s52,0x7f4a7c15
s_addc_u32 s55,s53,0x9e3779b9
s_lshl_b32 s67,s49,5
s_add_u32 s67,s67,4898816
s_sub_u32 s51,s67,s68
s_add_u32 s92,s32,s51
s_addc_u32 s93,s33,0x10000
s_sub_u32 s92,s92,s30
s_subb_u32 s93,s93,0
s_cmp_eq_u32 s26,0
s_cselect_b32 s94,1,0
s_mov_b32 s95,0x20000
v_mov_b32 v16,s54
v_mov_b32 v17,s55
v_cmp_gt_u32 vcc,12,v1
v_mul_u32_u24 v3,296960,v1
v_mov_b32 v12,0x7fff0000
v_cndmask_b32 v3,v12,v3,vcc
s_add_u32 s51,s67,s56
buffer_store_dwordx2 v[16:17],v3,s[0:3],s51 offen sc1
s_sub_u32 s67,s68,296960
s_add_u32 s68,s68,s57
s_sub_u32 s68,s68,s31
s_sub_u32 s62,255,s28
s_lshr_b32 s62,s62,2
s_mov_b32 s49,s57
s_cmp_lg_u32 s25,0
s_cmov_b32 s62,-1
s_cmov_b32 s49,76021760
s_add_u32 s48,s48,s49
s_add_u32 s71,s48,4612096
s_add_u32 s63,s49,4648960
s_add_u32 s66,s49,4898816
s_mul_i32 s64,s26,8192
s_add_u32 s65,s64,65536
s_xor_b32 s65,s65,s64
s_add_u32 s63,s63,s64
v_mov_b32 v4,v2
v_add_u32 v6,4096,v2
v_and_b32 v14,15,v1
v_lshlrev_b32 v14,2,v14
s_mov_b32 s48,32
v_add_u32 v14,s48,v14
v_lshrrev_b32 v14,2,v14
v_lshlrev_b32 v14,7,v14
v_lshrrev_b32 v3,4,v1
v_mul_u32_u24 v3,s30,v3
v_add_u32 v14,v3,v14
v_cmp_gt_u32 vcc,32,v1
v_mov_b32 v3,0x7fff0000
s_nop 0
v_cndmask_b32 v14,v3,v14,vcc
v_cndmask_b32 v16,0,v16,vcc
v_cndmask_b32 v17,0,v17,vcc
s_mov_b64 s[8:9],s[62:63]
s_mov_b64 s[10:11],s[64:65]
s_mov_b64 s[12:13],s[66:67]
s_mov_b64 s[14:15],s[68:69]
s_mov_b64 s[16:17],s[70:71]
s_mov_b64 s[18:19],s[72:73]
s_mov_b64 s[20:21],s[74:75]
s_mov_b32 s62,0
s_mov_b64 vcc,0
s_mov_b64 exec,0
s_branch .L_wgo
.L_wbk:
s_mov_b64 exec,-1
s_mov_b64 s[62:63],s[8:9]
s_mov_b64 s[64:65],s[10:11]
s_mov_b64 s[66:67],s[12:13]
s_mov_b64 s[68:69],s[14:15]
s_mov_b64 s[70:71],s[16:17]
s_mov_b64 s[72:73],s[18:19]
s_mov_b64 s[74:75],s[20:21]
s_waitcnt vmcnt(0) lgkmcnt(0)
.L_wnd:
s_cmp_lg_u32 s26,1
)#"
R"#(
s_cbranch_scc1 .L_skwa
s_mov_b32 s48,20000
s_branch .L_rfwa
.L_rlwa:
s_sleep 2
.L_rfwa:
buffer_load_dwordx2 v[12:13],v14,s[0:3],s66 offen sc1
s_sub_u32 s48,s48,1
s_cbranch_scc1 .L_end
s_waitcnt vmcnt(0)
v_cmp_ne_u64 vcc,v[16:17],v[12:13]
s_cbranch_vccnz .L_rlwa
.L_skwa:
s_waitcnt vmcnt(0)
s_barrier
s_add_u32 s66,s66,s31
s_add_u32 m0,s64,0
s_nop 0
buffer_load_dwordx4 v2,s[32:35],s63 offen offset:0 sc1 lds
buffer_load_dwordx4 v2,s[32:35],s63 offen offset:1024 sc1 lds
buffer_load_dwordx4 v2,s[32:35],s63 offen offset:2048 sc1 lds
buffer_load_dwordx4 v2,s[32:35],s63 offen offset:3072 sc1 lds
s_add_u32 m0,s64,4096
s_nop 0
buffer_load_dwordx4 v6,s[32:35],s63 offen offset:0 sc1 lds
buffer_load_dwordx4 v6,s[32:35],s63 offen offset:1024 sc1 lds
buffer_load_dwordx4 v6,s[32:35],s63 offen offset:2048 sc1 lds
buffer_load_dwordx4 v6,s[32:35],s63 offen offset:3072 sc1 lds
s_add_u32 s63,s63,s30
buffer_load_dwordx4 v[48:51],v2,s[32:35],s71 offen sc1
s_add_u32 s71,s71,s30
buffer_load_dwordx4 v[52:55],v2,s[32:35],s71 offen sc1
s_add_u32 s71,s71,s30
s_add_u32 m0,s64,32768
s_nop 0
buffer_load_dwordx4 v2,s[32:35],s63 offen offset:0 sc1 lds
buffer_load_dwordx4 v2,s[32:35],s63 offen offset:1024 sc1 lds
buffer_load_dwordx4 v2,s[32:35],s63 offen offset:2048 sc1 lds
buffer_load_dwordx4 v2,s[32:35],s63 offen offset:3072 sc1 lds
s_add_u32 m0,s64,36864
s_nop 0
buffer_load_dwordx4 v6,s[32:35],s63 offen offset:0 sc1 lds
buffer_load_dwordx4 v6,s[32:35],s63 offen offset:1024 sc1 lds
buffer_load_dwordx4 v6,s[32:35],s63 offen offset:2048 sc1 lds
buffer_load_dwordx4 v6,s[32:35],s63 offen offset:3072 sc1 lds
s_add_u32 s63,s63,s30
s_xor_b32 s64,s64,s65
s_cmp_lg_u32 s26,1
s_cbranch_scc1 .L_skwc
s_mov_b32 s48,20000
s_branch .L_rfwc
.L_rlwc:
s_sleep 2
.L_rfwc:
buffer_load_dwordx2 v[12:13],v14,s[0:3],s66 offen sc1
s_sub_u32 s48,s48,1
s_cbranch_scc1 .L_end
s_waitcnt vmcnt(0)
v_cmp_ne_u64 vcc,v[16:17],v[12:13]
s_cbranch_vccnz .L_rlwc
.L_skwc:
s_waitcnt vmcnt(0)
s_barrier
s_add_u32 s66,s66,s31
s_add_u32 m0,s64,0
s_nop 0
buffer_load_dwordx4 v2,s[32:35],s63 offen offset:0 sc1 lds
buffer_load_dwordx4 v2,s[32:35],s63 offen offset:1024 sc1 lds
buffer_load_dwordx4 v2,s[32:35],s63 offen offset:2048 sc1 lds
buffer_load_dwordx4 v2,s[32:35],s63 offen offset:3072 sc1 lds
s_add_u32 m0,s64,4096
s_nop 0
buffer_load_dwordx4 v6,s[32:35],s63 offen offset:0 sc1 lds
s_barrier
ds_read_b128 v[128:131],v4 offset:0
ds_read_b128 v[132:135],v4 offset:1024
ds_read_b128 v[136:139],v4 offset:2048
ds_read_b128 v[140:143],v4 offset:3072
ds_read_b128 v[144:147],v4 offset:4096
ds_read_b128 v[148:151],v4 offset:5120
ds_read_b128 v[152:155],v4 offset:6144
ds_read_b128 v[156:159],v4 offset:7168
ds_read_b128 v[160:163],v4 offset:8192
ds_read_b128 v[164:167],v4 offset:9216
ds_read_b128 v[168:171],v4 offset:10240
ds_read_b128 v[172:175],v4 offset:11264
v_lshlrev_b32 v32,16,v48
v_and_b32 v33,0xffff0000,v48
v_lshlrev_b32 v34,16,v49
v_and_b32 v35,0xffff0000,v49
v_lshlrev_b32 v36,16,v50
v_and_b32 v37,0xffff0000,v50
v_lshlrev_b32 v38,16,v51
v_and_b32 v39,0xffff0000,v51
.L_wgo:
s_cmp_eq_u32 s26,1
s_cbranch_scc1 .L_wv1
s_cmp_eq_u32 s26,2
s_cbranch_scc1 .L_wv2
s_cmp_eq_u32 s26,3
s_cbranch_scc1 .L_wv3
.p2align 6
.L_wv0:
.L_wl_0:
s_waitcnt lgkmcnt(4)
v_mfma_f32_16x16x32_bf16 v[32:35],a[0:3],v[128:131],v[32:35]
ds_read_b128 v[176:179],v4 offset:12288
v_mfma_f32_16x16x32_bf16 v[32:35],a[8:11],v[132:135],v[32:35]
buffer_load_dwordx4 v6,s[32:35],s63 offen offset:1024 sc1 lds
v_mfma_f32_16x16x32_bf16 v[32:35],a[16:19],v[136:139],v[32:35]
ds_read_b128 v[180:183],v4 offset:13312
s_add_u32 s68,s68,s30
v_mfma_f32_16x16x32_bf16 v[32:35],a[24:27],v[140:143],v[32:35]
v_cvt_pk_bf16_f32 v56,v40,v41
v_cvt_pk_bf16_f32 v57,v42,v43
v_mfma_f32_16x16x32_bf16 v[36:39],a[28:31],v[140:143],v[36:39]
ds_read_b128 v[184:187],v4 offset:14336
v_cvt_pk_bf16_f32 v58,v44,v45
v_mfma_f32_16x16x32_bf16 v[36:39],a[20:23],v[136:139],v[36:39]
v_cvt_pk_bf16_f32 v59,v46,v47
v_add_f32 v60,v40,v41
v_mfma_f32_16x16x32_bf16 v[36:39],a[12:15],v[132:135],v[36:39]
ds_read_b128 v[188:191],v4 offset:15360
v_mul_f32 v61,v40,v40
v_mfma_f32_16x16x32_bf16 v[36:39],a[4:7],v[128:131],v[36:39]
v_fmac_f32 v61,v41,v41
v_add_f32 v60,v60,v42
v_mfma_f32_16x16x32_bf16 v[36:39],a[36:39],v[144:147],v[36:39]
ds_read_b128 v[128:131],v4 offset:16384
v_fmac_f32 v61,v42,v42
v_mfma_f32_16x16x32_bf16 v[36:39],a[44:47],v[148:151],v[36:39]
buffer_store_dwordx4 v[56:59],v2,s[32:35],s68 offen sc1
v_mfma_f32_16x16x32_bf16 v[36:39],a[52:55],v[152:155],v[36:39]
ds_read_b128 v[132:135],v4 offset:17408
v_add_f32 v60,v60,v43
v_mfma_f32_16x16x32_bf16 v[36:39],a[60:63],v[156:159],v[36:39]
v_fmac_f32 v61,v43,v43
v_add_f32 v60,v60,v44
v_mfma_f32_16x16x32_bf16 v[32:35],a[56:59],v[156:159],v[32:35]
ds_read_b128 v[136:139],v4 offset:18432
v_fmac_f32 v61,v44,v44
v_mfma_f32_16x16x32_bf16 v[32:35],a[48:51],v[152:155],v[32:35]
v_add_f32 v60,v60,v45
v_fmac_f32 v61,v45,v45
v_mfma_f32_16x16x32_bf16 v[32:35],a[40:43],v[148:151],v[32:35]
ds_read_b128 v[140:143],v4 offset:19456
v_add_f32 v60,v60,v46
v_mfma_f32_16x16x32_bf16 v[32:35],a[32:35],v[144:147],v[32:35]
v_fmac_f32 v61,v46,v46
s_waitcnt lgkmcnt(4)
v_mfma_f32_16x16x32_bf16 v[32:35],a[64:67],v[160:163],v[32:35]
ds_read_b128 v[144:147],v4 offset:20480
v_add_f32 v60,v60,v47
v_mfma_f32_16x16x32_bf16 v[32:35],a[72:75],v[164:167],v[32:35]
v_fmac_f32 v61,v47,v47
v_mfma_f32_16x16x32_bf16 v[32:35],a[80:83],v[168:171],v[32:35]
ds_read_b128 v[148:151],v4 offset:21504
v_mfma_f32_16x16x32_bf16 v[32:35],a[88:91],v[172:175],v[32:35]
v_permlane16_swap_b32 v60,v61
v_mfma_f32_16x16x32_bf16 v[36:39],a[92:95],v[172:175],v[36:39]
ds_read_b128 v[152:155],v4 offset:22528
v_add_f32 v60,v60,v61
v_mfma_f32_16x16x32_bf16 v[36:39],a[84:87],v[168:171],v[36:39]
buffer_load_dwordx4 v[48:51],v2,s[32:35],s71 offen sc1
v_mfma_f32_16x16x32_bf16 v[36:39],a[76:79],v[164:167],v[36:39]
ds_read_b128 v[156:159],v4 offset:23552
s_add_u32 s71,s71,s30
v_mfma_f32_16x16x32_bf16 v[36:39],a[68:71],v[160:163],v[36:39]
v_permlane32_swap_b32 v60,v61
v_mfma_f32_16x16x32_bf16 v[36:39],a[100:103],v[176:179],v[36:39]
ds_read_b128 v[160:163],v4 offset:24576
v_add_f32 v60,v60,v61
v_mfma_f32_16x16x32_bf16 v[36:39],a[108:111],v[180:183],v[36:39]
buffer_store_dword v60,v15,s[32:35],s68 offen offset:1024 sc1
v_mfma_f32_16x16x32_bf16 v[36:39],a[116:119],v[184:187],v[36:39]
ds_read_b128 v[164:167],v4 offset:25600
v_mfma_f32_16x16x32_bf16 v[36:39],a[124:127],v[188:191],v[36:39]
v_mfma_f32_16x16x32_bf16 v[32:35],a[120:123],v[188:191],v[32:35]
ds_read_b128 v[168:171],v4 offset:26624
v_mfma_f32_16x16x32_bf16 v[32:35],a[112:115],v[184:187],v[32:35]
buffer_load_dwordx4 v6,s[32:35],s63 offen offset:2048 sc1 lds
v_mfma_f32_16x16x32_bf16 v[32:35],a[104:107],v[180:183],v[32:35]
ds_read_b128 v[172:175],v4 offset:27648
v_mfma_f32_16x16x32_bf16 v[32:35],a[96:99],v[176:179],v[32:35]
s_waitcnt lgkmcnt(4)
v_mfma_f32_16x16x32_bf16 v[32:35],a[128:131],v[128:131],v[32:35]
ds_read_b128 v[176:179],v4 offset:28672
v_mfma_f32_16x16x32_bf16 v[32:35],a[136:139],v[132:135],v[32:35]
buffer_load_dwordx4 v6,s[32:35],s63 offen offset:3072 sc1 lds
v_mfma_f32_16x16x32_bf16 v[32:35],a[144:147],v[136:139],v[32:35]
ds_read_b128 v[180:183],v4 offset:29696
s_add_u32 s63,s63,s30
v_mfma_f32_16x16x32_bf16 v[32:35],a[152:155],v[140:143],v[32:35]
v_mfma_f32_16x16x32_bf16 v[36:39],a[156:159],v[140:143],v[36:39]
ds_read_b128 v[184:187],v4 offset:30720
v_mfma_f32_16x16x32_bf16 v[36:39],a[148:151],v[136:139],v[36:39]
v_mfma_f32_16x16x32_bf16 v[36:39],a[140:143],v[132:135],v[36:39]
ds_read_b128 v[188:191],v4 offset:31744
v_mfma_f32_16x16x32_bf16 v[36:39],a[132:135],v[128:131],v[36:39]
s_waitcnt vmcnt(12)
s_barrier
v_mfma_f32_16x16x32_bf16 v[36:39],a[164:167],v[144:147],v[36:39]
ds_read_b128 v[128:131],v4 offset:32768
v_lshlrev_b32 v40,16,v52
v_mfma_f32_16x16x32_bf16 v[36:39],a[172:175],v[148:151],v[36:39]
buffer_store_dwordx2 v[16:17],v2,s[92:95],s68 idxen sc1
v_mfma_f32_16x16x32_bf16 v[36:39],a[180:183],v[152:155],v[36:39]
ds_read_b128 v[132:135],v4 offset:33792
v_and_b32 v41,0xffff0000,v52
v_mfma_f32_16x16x32_bf16 v[36:39],a[188:191],v[156:159],v[36:39]
v_lshlrev_b32 v42,16,v53
v_and_b32 v43,0xffff0000,v53
v_mfma_f32_16x16x32_bf16 v[32:35],a[184:187],v[156:159],v[32:35]
ds_read_b128 v[136:139],v4 offset:34816
s_add_u32 m0,s64,32768
v_mfma_f32_16x16x32_bf16 v[32:35],a[176:179],v[152:155],v[32:35]
buffer_load_dwordx4 v2,s[32:35],s63 offen offset:0 sc1 lds
v_mfma_f32_16x16x32_bf16 v[32:35],a[168:171],v[148:151],v[32:35]
ds_read_b128 v[140:143],v4 offset:35840
v_lshlrev_b32 v44,16,v54
v_mfma_f32_16x16x32_bf16 v[32:35],a[160:163],v[144:147],v[32:35]
v_and_b32 v45,0xffff0000,v54
s_waitcnt lgkmcnt(4)
v_mfma_f32_16x16x32_bf16 v[32:35],a[192:195],v[160:163],v[32:35]
ds_read_b128 v[144:147],v4 offset:36864
v_lshlrev_b32 v46,16,v55
v_mfma_f32_16x16x32_bf16 v[32:35],a[200:203],v[164:167],v[32:35]
buffer_load_dwordx4 v2,s[32:35],s63 offen offset:1024 sc1 lds
v_mfma_f32_16x16x32_bf16 v[32:35],a[208:211],v[168:171],v[32:35]
ds_read_b128 v[148:151],v4 offset:37888
v_and_b32 v47,0xffff0000,v55
v_mfma_f32_16x16x32_bf16 v[32:35],a[216:219],v[172:175],v[32:35]
v_mfma_f32_16x16x32_bf16 v[36:39],a[220:223],v[172:175],v[36:39]
ds_read_b128 v[152:155],v4 offset:38912
v_mfma_f32_16x16x32_bf16 v[36:39],a[212:215],v[168:171],v[36:39]
buffer_load_dwordx4 v2,s[32:35],s63 offen offset:2048 sc1 lds
v_mfma_f32_16x16x32_bf16 v[36:39],a[204:207],v[164:167],v[36:39]
ds_read_b128 v[156:159],v4 offset:39936
v_mfma_f32_16x16x32_bf16 v[36:39],a[196:199],v[160:163],v[36:39]
v_mfma_f32_16x16x32_bf16 v[36:39],a[228:231],v[176:179],v[36:39]
ds_read_b128 v[160:163],v4 offset:40960
v_mfma_f32_16x16x32_bf16 v[36:39],a[236:239],v[180:183],v[36:39]
buffer_load_dwordx4 v2,s[32:35],s63 offen offset:3072 sc1 lds
v_mfma_f32_16x16x32_bf16 v[36:39],a[244:247],v[184:187],v[36:39]
ds_read_b128 v[164:167],v4 offset:41984
v_mfma_f32_16x16x32_bf16 v[36:39],a[252:255],v[188:191],v[36:39]
v_mfma_f32_16x16x32_bf16 v[32:35],a[248:251],v[188:191],v[32:35]
ds_read_b128 v[168:171],v4 offset:43008
s_add_u32 m0,s64,36864
v_mfma_f32_16x16x32_bf16 v[32:35],a[240:243],v[184:187],v[32:35]
buffer_load_dwordx4 v6,s[32:35],s63 offen offset:0 sc1 lds
v_mfma_f32_16x16x32_bf16 v[32:35],a[232:235],v[180:183],v[32:35]
ds_read_b128 v[172:175],v4 offset:44032
v_mfma_f32_16x16x32_bf16 v[32:35],a[224:227],v[176:179],v[32:35]
s_waitcnt lgkmcnt(4)
v_mfma_f32_16x16x32_bf16 v[40:43],a[0:3],v[128:131],v[40:43]
ds_read_b128 v[176:179],v4 offset:45056
v_mfma_f32_16x16x32_bf16 v[40:43],a[8:11],v[132:135],v[40:43]
buffer_load_dwordx4 v6,s[32:35],s63 offen offset:1024 sc1 lds
v_mfma_f32_16x16x32_bf16 v[40:43],a[16:19],v[136:139],v[40:43]
ds_read_b128 v[180:183],v4 offset:46080
s_add_u32 s68,s68,s30
v_mfma_f32_16x16x32_bf16 v[40:43],a[24:27],v[140:143],v[40:43]
v_cvt_pk_bf16_f32 v56,v32,v33
v_cvt_pk_bf16_f32 v57,v34,v35
v_mfma_f32_16x16x32_bf16 v[44:47],a[28:31],v[140:143],v[44:47]
ds_read_b128 v[184:187],v4 offset:47104
v_cvt_pk_bf16_f32 v58,v36,v37
v_mfma_f32_16x16x32_bf16 v[44:47],a[20:23],v[136:139],v[44:47]
v_cvt_pk_bf16_f32 v59,v38,v39
v_add_f32 v60,v32,v33
v_mfma_f32_16x16x32_bf16 v[44:47],a[12:15],v[132:135],v[44:47]
ds_read_b128 v[188:191],v4 offset:48128
v_mul_f32 v61,v32,v32
v_mfma_f32_16x16x32_bf16 v[44:47],a[4:7],v[128:131],v[44:47]
v_fmac_f32 v61,v33,v33
v_add_f32 v60,v60,v34
v_mfma_f32_16x16x32_bf16 v[44:47],a[36:39],v[144:147],v[44:47]
ds_read_b128 v[128:131],v4 offset:49152
v_fmac_f32 v61,v34,v34
v_mfma_f32_16x16x32_bf16 v[44:47],a[44:47],v[148:151],v[44:47]
buffer_store_dwordx4 v[56:59],v2,s[32:35],s68 offen sc1
v_mfma_f32_16x16x32_bf16 v[44:47],a[52:55],v[152:155],v[44:47]
ds_read_b128 v[132:135],v4 offset:50176
v_add_f32 v60,v60,v35
v_mfma_f32_16x16x32_bf16 v[44:47],a[60:63],v[156:159],v[44:47]
v_fmac_f32 v61,v35,v35
v_add_f32 v60,v60,v36
v_mfma_f32_16x16x32_bf16 v[40:43],a[56:59],v[156:159],v[40:43]
ds_read_b128 v[136:139],v4 offset:51200
v_fmac_f32 v61,v36,v36
v_mfma_f32_16x16x32_bf16 v[40:43],a[48:51],v[152:155],v[40:43]
v_add_f32 v60,v60,v37
v_fmac_f32 v61,v37,v37
v_mfma_f32_16x16x32_bf16 v[40:43],a[40:43],v[148:151],v[40:43]
ds_read_b128 v[140:143],v4 offset:52224
v_add_f32 v60,v60,v38
v_mfma_f32_16x16x32_bf16 v[40:43],a[32:35],v[144:147],v[40:43]
v_fmac_f32 v61,v38,v38
s_waitcnt lgkmcnt(4)
v_mfma_f32_16x16x32_bf16 v[40:43],a[64:67],v[160:163],v[40:43]
ds_read_b128 v[144:147],v4 offset:53248
v_add_f32 v60,v60,v39
v_mfma_f32_16x16x32_bf16 v[40:43],a[72:75],v[164:167],v[40:43]
v_fmac_f32 v61,v39,v39
v_mfma_f32_16x16x32_bf16 v[40:43],a[80:83],v[168:171],v[40:43]
ds_read_b128 v[148:151],v4 offset:54272
v_mfma_f32_16x16x32_bf16 v[40:43],a[88:91],v[172:175],v[40:43]
v_permlane16_swap_b32 v60,v61
v_mfma_f32_16x16x32_bf16 v[44:47],a[92:95],v[172:175],v[44:47]
ds_read_b128 v[152:155],v4 offset:55296
v_add_f32 v60,v60,v61
v_mfma_f32_16x16x32_bf16 v[44:47],a[84:87],v[168:171],v[44:47]
buffer_load_dwordx4 v[52:55],v2,s[32:35],s71 offen sc1
v_mfma_f32_16x16x32_bf16 v[44:47],a[76:79],v[164:167],v[44:47]
ds_read_b128 v[156:159],v4 offset:56320
s_add_u32 s71,s71,s30
v_mfma_f32_16x16x32_bf16 v[44:47],a[68:71],v[160:163],v[44:47]
v_permlane32_swap_b32 v60,v61
v_mfma_f32_16x16x32_bf16 v[44:47],a[100:103],v[176:179],v[44:47]
ds_read_b128 v[160:163],v4 offset:57344
v_add_f32 v60,v60,v61
v_mfma_f32_16x16x32_bf16 v[44:47],a[108:111],v[180:183],v[44:47]
buffer_store_dword v60,v15,s[32:35],s68 offen offset:1024 sc1
v_mfma_f32_16x16x32_bf16 v[44:47],a[116:119],v[184:187],v[44:47]
ds_read_b128 v[164:167],v4 offset:58368
v_mfma_f32_16x16x32_bf16 v[44:47],a[124:127],v[188:191],v[44:47]
v_mfma_f32_16x16x32_bf16 v[40:43],a[120:123],v[188:191],v[40:43]
ds_read_b128 v[168:171],v4 offset:59392
v_mfma_f32_16x16x32_bf16 v[40:43],a[112:115],v[184:187],v[40:43]
buffer_load_dwordx4 v6,s[32:35],s63 offen offset:2048 sc1 lds
v_mfma_f32_16x16x32_bf16 v[40:43],a[104:107],v[180:183],v[40:43]
ds_read_b128 v[172:175],v4 offset:60416
v_mfma_f32_16x16x32_bf16 v[40:43],a[96:99],v[176:179],v[40:43]
s_waitcnt lgkmcnt(4)
v_mfma_f32_16x16x32_bf16 v[40:43],a[128:131],v[128:131],v[40:43]
ds_read_b128 v[176:179],v4 offset:61440
v_mfma_f32_16x16x32_bf16 v[40:43],a[136:139],v[132:135],v[40:43]
buffer_load_dwordx4 v6,s[32:35],s63 offen offset:3072 sc1 lds
v_mfma_f32_16x16x32_bf16 v[40:43],a[144:147],v[136:139],v[40:43]
ds_read_b128 v[180:183],v4 offset:62464
s_add_u32 s63,s63,s30
v_mfma_f32_16x16x32_bf16 v[40:43],a[152:155],v[140:143],v[40:43]
s_xor_b32 s64,s64,s65
v_mfma_f32_16x16x32_bf16 v[44:47],a[156:159],v[140:143],v[44:47]
ds_read_b128 v[184:187],v4 offset:63488
v_mfma_f32_16x16x32_bf16 v[44:47],a[148:151],v[136:139],v[44:47]
v_mfma_f32_16x16x32_bf16 v[44:47],a[140:143],v[132:135],v[44:47]
ds_read_b128 v[188:191],v4 offset:64512
v_xor_b32 v4,65536,v4
v_mfma_f32_16x16x32_bf16 v[44:47],a[132:135],v[128:131],v[44:47]
s_waitcnt vmcnt(12)
s_barrier
v_mfma_f32_16x16x32_bf16 v[44:47],a[164:167],v[144:147],v[44:47]
ds_read_b128 v[128:131],v4 offset:0
v_lshlrev_b32 v32,16,v48
v_mfma_f32_16x16x32_bf16 v[44:47],a[172:175],v[148:151],v[44:47]
buffer_store_dwordx2 v[16:17],v2,s[92:95],s68 idxen sc1
v_mfma_f32_16x16x32_bf16 v[44:47],a[180:183],v[152:155],v[44:47]
ds_read_b128 v[132:135],v4 offset:1024
v_and_b32 v33,0xffff0000,v48
v_mfma_f32_16x16x32_bf16 v[44:47],a[188:191],v[156:159],v[44:47]
v_lshlrev_b32 v34,16,v49
v_and_b32 v35,0xffff0000,v49
v_mfma_f32_16x16x32_bf16 v[40:43],a[184:187],v[156:159],v[40:43]
ds_read_b128 v[136:139],v4 offset:2048
s_add_u32 m0,s64,0
v_mfma_f32_16x16x32_bf16 v[40:43],a[176:179],v[152:155],v[40:43]
buffer_load_dwordx4 v2,s[32:35],s63 offen offset:0 sc1 lds
v_mfma_f32_16x16x32_bf16 v[40:43],a[168:171],v[148:151],v[40:43]
ds_read_b128 v[140:143],v4 offset:3072
v_lshlrev_b32 v36,16,v50
v_mfma_f32_16x16x32_bf16 v[40:43],a[160:163],v[144:147],v[40:43]
v_and_b32 v37,0xffff0000,v50
s_waitcnt lgkmcnt(4)
v_mfma_f32_16x16x32_bf16 v[40:43],a[192:195],v[160:163],v[40:43]
ds_read_b128 v[144:147],v4 offset:4096
v_lshlrev_b32 v38,16,v51
v_mfma_f32_16x16x32_bf16 v[40:43],a[200:203],v[164:167],v[40:43]
buffer_load_dwordx4 v2,s[32:35],s63 offen offset:1024 sc1 lds
v_mfma_f32_16x16x32_bf16 v[40:43],a[208:211],v[168:171],v[40:43]
ds_read_b128 v[148:151],v4 offset:5120
v_and_b32 v39,0xffff0000,v51
v_mfma_f32_16x16x32_bf16 v[40:43],a[216:219],v[172:175],v[40:43]
v_mfma_f32_16x16x32_bf16 v[44:47],a[220:223],v[172:175],v[44:47]
ds_read_b128 v[152:155],v4 offset:6144
v_mfma_f32_16x16x32_bf16 v[44:47],a[212:215],v[168:171],v[44:47]
buffer_load_dwordx4 v2,s[32:35],s63 offen offset:2048 sc1 lds
v_mfma_f32_16x16x32_bf16 v[44:47],a[204:207],v[164:167],v[44:47]
ds_read_b128 v[156:159],v4 offset:7168
v_mfma_f32_16x16x32_bf16 v[44:47],a[196:199],v[160:163],v[44:47]
v_mfma_f32_16x16x32_bf16 v[44:47],a[228:231],v[176:179],v[44:47]
ds_read_b128 v[160:163],v4 offset:8192
v_mfma_f32_16x16x32_bf16 v[44:47],a[236:239],v[180:183],v[44:47]
buffer_load_dwordx4 v2,s[32:35],s63 offen offset:3072 sc1 lds
v_mfma_f32_16x16x32_bf16 v[44:47],a[244:247],v[184:187],v[44:47]
ds_read_b128 v[164:167],v4 offset:9216
v_mfma_f32_16x16x32_bf16 v[44:47],a[252:255],v[188:191],v[44:47]
v_mfma_f32_16x16x32_bf16 v[40:43],a[248:251],v[188:191],v[40:43]
ds_read_b128 v[168:171],v4 offset:10240
s_add_u32 m0,s64,4096
v_mfma_f32_16x16x32_bf16 v[40:43],a[240:243],v[184:187],v[40:43]
buffer_load_dwordx4 v6,s[32:35],s63 offen offset:0 sc1 lds
v_mfma_f32_16x16x32_bf16 v[40:43],a[232:235],v[180:183],v[40:43]
ds_read_b128 v[172:175],v4 offset:11264
s_sub_u32 s62,s62,1
v_mfma_f32_16x16x32_bf16 v[40:43],a[224:227],v[176:179],v[40:43]
s_cbranch_scc0 .L_wl_0
s_nop 1
s_add_u32 s68,s68,s30
s_nop 3
v_cvt_pk_bf16_f32 v56,v40,v41
v_cvt_pk_bf16_f32 v57,v42,v43
v_cvt_pk_bf16_f32 v58,v44,v45
v_cvt_pk_bf16_f32 v59,v46,v47
buffer_store_dwordx4 v[56:59],v2,s[32:35],s68 offen sc1
v_add_f32 v60,v40,v41
v_mul_f32 v61,v40,v40
v_fmac_f32 v61,v41,v41
v_add_f32 v60,v60,v42
v_fmac_f32 v61,v42,v42
v_add_f32 v60,v60,v43
v_fmac_f32 v61,v43,v43
v_add_f32 v60,v60,v44
v_fmac_f32 v61,v44,v44
v_add_f32 v60,v60,v45
v_fmac_f32 v61,v45,v45
v_add_f32 v60,v60,v46
v_fmac_f32 v61,v46,v46
v_add_f32 v60,v60,v47
v_fmac_f32 v61,v47,v47
s_nop 1
v_permlane16_swap_b32 v60,v61
v_add_f32 v60,v60,v61
s_nop 1
v_permlane32_swap_b32 v60,v61
v_add_f32 v60,v60,v61
buffer_store_dword v60,v15,s[32:35],s68 offen offset:1024 sc1
s_waitcnt vmcnt(10)
s_barrier
buffer_store_dwordx2 v[16:17],v2,s[92:95],s68 idxen sc1
s_add_u32 s68,s68,s30
s_waitcnt vmcnt(1)
s_barrier
buffer_store_dwordx2 v[16:17],v2,s[92:95],s68 idxen sc1
s_cbranch_execz .L_wbk
s_branch .L_end
.p2align 6
.L_wv1:
.L_wl_1:
s_waitcnt lgkmcnt(4)
v_mfma_f32_16x16x32_bf16 v[32:35],a[0:3],v[128:131],v[32:35]
ds_read_b128 v[176:179],v4 offset:12288
v_mfma_f32_16x16x32_bf16 v[32:35],a[8:11],v[132:135],v[32:35]
buffer_load_dwordx4 v6,s[32:35],s63 offen offset:1024 sc1 lds
v_mfma_f32_16x16x32_bf16 v[32:35],a[16:19],v[136:139],v[32:35]
ds_read_b128 v[180:183],v4 offset:13312
s_add_u32 s68,s68,s30
v_mfma_f32_16x16x32_bf16 v[32:35],a[24:27],v[140:143],v[32:35]
v_cvt_pk_bf16_f32 v56,v40,v41
v_cvt_pk_bf16_f32 v57,v42,v43
v_mfma_f32_16x16x32_bf16 v[36:39],a[28:31],v[140:143],v[36:39]
ds_read_b128 v[184:187],v4 offset:14336
v_cvt_pk_bf16_f32 v58,v44,v45
v_mfma_f32_16x16x32_bf16 v[36:39],a[20:23],v[136:139],v[36:39]
v_cvt_pk_bf16_f32 v59,v46,v47
v_add_f32 v60,v40,v41
v_mfma_f32_16x16x32_bf16 v[36:39],a[12:15],v[132:135],v[36:39]
ds_read_b128 v[188:191],v4 offset:15360
v_mul_f32 v61,v40,v40
v_mfma_f32_16x16x32_bf16 v[36:39],a[4:7],v[128:131],v[36:39]
v_fmac_f32 v61,v41,v41
v_add_f32 v60,v60,v42
v_mfma_f32_16x16x32_bf16 v[36:39],a[36:39],v[144:147],v[36:39]
ds_read_b128 v[128:131],v4 offset:16384
v_fmac_f32 v61,v42,v42
v_mfma_f32_16x16x32_bf16 v[36:39],a[44:47],v[148:151],v[36:39]
buffer_store_dwordx4 v[56:59],v2,s[32:35],s68 offen sc1
v_mfma_f32_16x16x32_bf16 v[36:39],a[52:55],v[152:155],v[36:39]
ds_read_b128 v[132:135],v4 offset:17408
v_add_f32 v60,v60,v43
v_mfma_f32_16x16x32_bf16 v[36:39],a[60:63],v[156:159],v[36:39]
v_fmac_f32 v61,v43,v43
v_add_f32 v60,v60,v44
v_mfma_f32_16x16x32_bf16 v[32:35],a[56:59],v[156:159],v[32:35]
ds_read_b128 v[136:139],v4 offset:18432
v_fmac_f32 v61,v44,v44
v_mfma_f32_16x16x32_bf16 v[32:35],a[48:51],v[152:155],v[32:35]
v_add_f32 v60,v60,v45
v_fmac_f32 v61,v45,v45
v_mfma_f32_16x16x32_bf16 v[32:35],a[40:43],v[148:151],v[32:35]
ds_read_b128 v[140:143],v4 offset:19456
v_add_f32 v60,v60,v46
v_mfma_f32_16x16x32_bf16 v[32:35],a[32:35],v[144:147],v[32:35]
v_fmac_f32 v61,v46,v46
s_waitcnt lgkmcnt(4)
v_mfma_f32_16x16x32_bf16 v[32:35],a[64:67],v[160:163],v[32:35]
ds_read_b128 v[144:147],v4 offset:20480
v_add_f32 v60,v60,v47
v_mfma_f32_16x16x32_bf16 v[32:35],a[72:75],v[164:167],v[32:35]
)#"
R"#(
v_fmac_f32 v61,v47,v47
v_mfma_f32_16x16x32_bf16 v[32:35],a[80:83],v[168:171],v[32:35]
ds_read_b128 v[148:151],v4 offset:21504
v_mfma_f32_16x16x32_bf16 v[32:35],a[88:91],v[172:175],v[32:35]
v_permlane16_swap_b32 v60,v61
v_mfma_f32_16x16x32_bf16 v[36:39],a[92:95],v[172:175],v[36:39]
ds_read_b128 v[152:155],v4 offset:22528
v_add_f32 v60,v60,v61
v_mfma_f32_16x16x32_bf16 v[36:39],a[84:87],v[168:171],v[36:39]
buffer_load_dwordx4 v[48:51],v2,s[32:35],s71 offen sc1
v_mfma_f32_16x16x32_bf16 v[36:39],a[76:79],v[164:167],v[36:39]
ds_read_b128 v[156:159],v4 offset:23552
s_add_u32 s71,s71,s30
v_mfma_f32_16x16x32_bf16 v[36:39],a[68:71],v[160:163],v[36:39]
v_permlane32_swap_b32 v60,v61
v_mfma_f32_16x16x32_bf16 v[36:39],a[100:103],v[176:179],v[36:39]
ds_read_b128 v[160:163],v4 offset:24576
v_add_f32 v60,v60,v61
v_mfma_f32_16x16x32_bf16 v[36:39],a[108:111],v[180:183],v[36:39]
buffer_store_dword v60,v15,s[32:35],s68 offen offset:1024 sc1
v_mfma_f32_16x16x32_bf16 v[36:39],a[116:119],v[184:187],v[36:39]
ds_read_b128 v[164:167],v4 offset:25600
v_mfma_f32_16x16x32_bf16 v[36:39],a[124:127],v[188:191],v[36:39]
v_mfma_f32_16x16x32_bf16 v[32:35],a[120:123],v[188:191],v[32:35]
ds_read_b128 v[168:171],v4 offset:26624
v_mfma_f32_16x16x32_bf16 v[32:35],a[112:115],v[184:187],v[32:35]
buffer_load_dwordx4 v6,s[32:35],s63 offen offset:2048 sc1 lds
v_mfma_f32_16x16x32_bf16 v[32:35],a[104:107],v[180:183],v[32:35]
ds_read_b128 v[172:175],v4 offset:27648
v_mfma_f32_16x16x32_bf16 v[32:35],a[96:99],v[176:179],v[32:35]
s_waitcnt lgkmcnt(4)
v_mfma_f32_16x16x32_bf16 v[32:35],a[128:131],v[128:131],v[32:35]
ds_read_b128 v[176:179],v4 offset:28672
v_mfma_f32_16x16x32_bf16 v[32:35],a[136:139],v[132:135],v[32:35]
buffer_load_dwordx4 v6,s[32:35],s63 offen offset:3072 sc1 lds
v_mfma_f32_16x16x32_bf16 v[32:35],a[144:147],v[136:139],v[32:35]
ds_read_b128 v[180:183],v4 offset:29696
s_add_u32 s63,s63,s30
v_mfma_f32_16x16x32_bf16 v[32:35],a[152:155],v[140:143],v[32:35]
v_mfma_f32_16x16x32_bf16 v[36:39],a[156:159],v[140:143],v[36:39]
ds_read_b128 v[184:187],v4 offset:30720
v_mfma_f32_16x16x32_bf16 v[36:39],a[148:151],v[136:139],v[36:39]
buffer_load_dwordx2 v[12:13],v14,s[0:3],s66 offen sc1
v_mfma_f32_16x16x32_bf16 v[36:39],a[140:143],v[132:135],v[36:39]
s_waitcnt vmcnt(12)
s_barrier
v_mfma_f32_16x16x32_bf16 v[36:39],a[132:135],v[128:131],v[36:39]
ds_read_b128 v[188:191],v4 offset:31744
v_mfma_f32_16x16x32_bf16 v[36:39],a[164:167],v[144:147],v[36:39]
ds_read_b128 v[128:131],v4 offset:32768
v_lshlrev_b32 v40,16,v52
v_mfma_f32_16x16x32_bf16 v[36:39],a[172:175],v[148:151],v[36:39]
v_and_b32 v41,0xffff0000,v52
v_lshlrev_b32 v42,16,v53
v_mfma_f32_16x16x32_bf16 v[36:39],a[180:183],v[152:155],v[36:39]
ds_read_b128 v[132:135],v4 offset:33792
v_and_b32 v43,0xffff0000,v53
v_mfma_f32_16x16x32_bf16 v[36:39],a[188:191],v[156:159],v[36:39]
v_lshlrev_b32 v44,16,v54
v_and_b32 v45,0xffff0000,v54
v_mfma_f32_16x16x32_bf16 v[32:35],a[184:187],v[156:159],v[32:35]
ds_read_b128 v[136:139],v4 offset:34816
s_add_u32 m0,s64,32768
v_mfma_f32_16x16x32_bf16 v[32:35],a[176:179],v[152:155],v[32:35]
buffer_load_dwordx4 v2,s[32:35],s63 offen offset:0 sc1 lds
v_mfma_f32_16x16x32_bf16 v[32:35],a[168:171],v[148:151],v[32:35]
ds_read_b128 v[140:143],v4 offset:35840
v_lshlrev_b32 v46,16,v55
v_mfma_f32_16x16x32_bf16 v[32:35],a[160:163],v[144:147],v[32:35]
v_and_b32 v47,0xffff0000,v55
s_waitcnt lgkmcnt(4)
v_mfma_f32_16x16x32_bf16 v[32:35],a[192:195],v[160:163],v[32:35]
ds_read_b128 v[144:147],v4 offset:36864
v_mfma_f32_16x16x32_bf16 v[32:35],a[200:203],v[164:167],v[32:35]
buffer_load_dwordx4 v2,s[32:35],s63 offen offset:1024 sc1 lds
v_mfma_f32_16x16x32_bf16 v[32:35],a[208:211],v[168:171],v[32:35]
ds_read_b128 v[148:151],v4 offset:37888
v_mfma_f32_16x16x32_bf16 v[32:35],a[216:219],v[172:175],v[32:35]
v_mfma_f32_16x16x32_bf16 v[36:39],a[220:223],v[172:175],v[36:39]
ds_read_b128 v[152:155],v4 offset:38912
v_mfma_f32_16x16x32_bf16 v[36:39],a[212:215],v[168:171],v[36:39]
buffer_load_dwordx4 v2,s[32:35],s63 offen offset:2048 sc1 lds
v_mfma_f32_16x16x32_bf16 v[36:39],a[204:207],v[164:167],v[36:39]
ds_read_b128 v[156:159],v4 offset:39936
v_mfma_f32_16x16x32_bf16 v[36:39],a[196:199],v[160:163],v[36:39]
v_mfma_f32_16x16x32_bf16 v[36:39],a[228:231],v[176:179],v[36:39]
ds_read_b128 v[160:163],v4 offset:40960
v_mfma_f32_16x16x32_bf16 v[36:39],a[236:239],v[180:183],v[36:39]
buffer_load_dwordx4 v2,s[32:35],s63 offen offset:3072 sc1 lds
v_mfma_f32_16x16x32_bf16 v[36:39],a[244:247],v[184:187],v[36:39]
ds_read_b128 v[164:167],v4 offset:41984
v_mfma_f32_16x16x32_bf16 v[36:39],a[252:255],v[188:191],v[36:39]
v_mfma_f32_16x16x32_bf16 v[32:35],a[248:251],v[188:191],v[32:35]
ds_read_b128 v[168:171],v4 offset:43008
s_add_u32 m0,s64,36864
v_mfma_f32_16x16x32_bf16 v[32:35],a[240:243],v[184:187],v[32:35]
buffer_load_dwordx4 v6,s[32:35],s63 offen offset:0 sc1 lds
v_mfma_f32_16x16x32_bf16 v[32:35],a[232:235],v[180:183],v[32:35]
ds_read_b128 v[172:175],v4 offset:44032
v_mfma_f32_16x16x32_bf16 v[32:35],a[224:227],v[176:179],v[32:35]
s_waitcnt lgkmcnt(4)
v_mfma_f32_16x16x32_bf16 v[40:43],a[0:3],v[128:131],v[40:43]
ds_read_b128 v[176:179],v4 offset:45056
v_mfma_f32_16x16x32_bf16 v[40:43],a[8:11],v[132:135],v[40:43]
buffer_load_dwordx4 v6,s[32:35],s63 offen offset:1024 sc1 lds
v_mfma_f32_16x16x32_bf16 v[40:43],a[16:19],v[136:139],v[40:43]
ds_read_b128 v[180:183],v4 offset:46080
s_add_u32 s68,s68,s30
v_mfma_f32_16x16x32_bf16 v[40:43],a[24:27],v[140:143],v[40:43]
v_cvt_pk_bf16_f32 v56,v32,v33
v_cvt_pk_bf16_f32 v57,v34,v35
v_mfma_f32_16x16x32_bf16 v[44:47],a[28:31],v[140:143],v[44:47]
ds_read_b128 v[184:187],v4 offset:47104
v_cvt_pk_bf16_f32 v58,v36,v37
v_mfma_f32_16x16x32_bf16 v[44:47],a[20:23],v[136:139],v[44:47]
v_cvt_pk_bf16_f32 v59,v38,v39
v_add_f32 v60,v32,v33
v_mfma_f32_16x16x32_bf16 v[44:47],a[12:15],v[132:135],v[44:47]
ds_read_b128 v[188:191],v4 offset:48128
v_mul_f32 v61,v32,v32
v_mfma_f32_16x16x32_bf16 v[44:47],a[4:7],v[128:131],v[44:47]
v_fmac_f32 v61,v33,v33
v_add_f32 v60,v60,v34
v_mfma_f32_16x16x32_bf16 v[44:47],a[36:39],v[144:147],v[44:47]
ds_read_b128 v[128:131],v4 offset:49152
v_fmac_f32 v61,v34,v34
v_mfma_f32_16x16x32_bf16 v[44:47],a[44:47],v[148:151],v[44:47]
buffer_store_dwordx4 v[56:59],v2,s[32:35],s68 offen sc1
v_mfma_f32_16x16x32_bf16 v[44:47],a[52:55],v[152:155],v[44:47]
ds_read_b128 v[132:135],v4 offset:50176
v_add_f32 v60,v60,v35
v_mfma_f32_16x16x32_bf16 v[44:47],a[60:63],v[156:159],v[44:47]
v_fmac_f32 v61,v35,v35
v_add_f32 v60,v60,v36
v_mfma_f32_16x16x32_bf16 v[40:43],a[56:59],v[156:159],v[40:43]
ds_read_b128 v[136:139],v4 offset:51200
v_fmac_f32 v61,v36,v36
v_mfma_f32_16x16x32_bf16 v[40:43],a[48:51],v[152:155],v[40:43]
v_add_f32 v60,v60,v37
v_fmac_f32 v61,v37,v37
v_mfma_f32_16x16x32_bf16 v[40:43],a[40:43],v[148:151],v[40:43]
ds_read_b128 v[140:143],v4 offset:52224
v_add_f32 v60,v60,v38
v_mfma_f32_16x16x32_bf16 v[40:43],a[32:35],v[144:147],v[40:43]
v_fmac_f32 v61,v38,v38
s_waitcnt lgkmcnt(4)
v_mfma_f32_16x16x32_bf16 v[40:43],a[64:67],v[160:163],v[40:43]
ds_read_b128 v[144:147],v4 offset:53248
v_add_f32 v60,v60,v39
v_mfma_f32_16x16x32_bf16 v[40:43],a[72:75],v[164:167],v[40:43]
v_fmac_f32 v61,v39,v39
v_mfma_f32_16x16x32_bf16 v[40:43],a[80:83],v[168:171],v[40:43]
ds_read_b128 v[148:151],v4 offset:54272
v_mfma_f32_16x16x32_bf16 v[40:43],a[88:91],v[172:175],v[40:43]
v_permlane16_swap_b32 v60,v61
v_mfma_f32_16x16x32_bf16 v[44:47],a[92:95],v[172:175],v[44:47]
ds_read_b128 v[152:155],v4 offset:55296
v_add_f32 v60,v60,v61
v_mfma_f32_16x16x32_bf16 v[44:47],a[84:87],v[168:171],v[44:47]
buffer_load_dwordx4 v[52:55],v2,s[32:35],s71 offen sc1
v_mfma_f32_16x16x32_bf16 v[44:47],a[76:79],v[164:167],v[44:47]
ds_read_b128 v[156:159],v4 offset:56320
s_add_u32 s71,s71,s30
v_mfma_f32_16x16x32_bf16 v[44:47],a[68:71],v[160:163],v[44:47]
v_permlane32_swap_b32 v60,v61
v_mfma_f32_16x16x32_bf16 v[44:47],a[100:103],v[176:179],v[44:47]
ds_read_b128 v[160:163],v4 offset:57344
v_add_f32 v60,v60,v61
v_mfma_f32_16x16x32_bf16 v[44:47],a[108:111],v[180:183],v[44:47]
buffer_store_dword v60,v15,s[32:35],s68 offen offset:1024 sc1
v_mfma_f32_16x16x32_bf16 v[44:47],a[116:119],v[184:187],v[44:47]
ds_read_b128 v[164:167],v4 offset:58368
v_mfma_f32_16x16x32_bf16 v[44:47],a[124:127],v[188:191],v[44:47]
v_mfma_f32_16x16x32_bf16 v[40:43],a[120:123],v[188:191],v[40:43]
ds_read_b128 v[168:171],v4 offset:59392
v_mfma_f32_16x16x32_bf16 v[40:43],a[112:115],v[184:187],v[40:43]
buffer_load_dwordx4 v6,s[32:35],s63 offen offset:2048 sc1 lds
v_mfma_f32_16x16x32_bf16 v[40:43],a[104:107],v[180:183],v[40:43]
ds_read_b128 v[172:175],v4 offset:60416
v_mfma_f32_16x16x32_bf16 v[40:43],a[96:99],v[176:179],v[40:43]
s_waitcnt lgkmcnt(4)
v_mfma_f32_16x16x32_bf16 v[40:43],a[128:131],v[128:131],v[40:43]
ds_read_b128 v[176:179],v4 offset:61440
v_mfma_f32_16x16x32_bf16 v[40:43],a[136:139],v[132:135],v[40:43]
buffer_load_dwordx4 v6,s[32:35],s63 offen offset:3072 sc1 lds
v_mfma_f32_16x16x32_bf16 v[40:43],a[144:147],v[136:139],v[40:43]
ds_read_b128 v[180:183],v4 offset:62464
s_waitcnt vmcnt(11)
v_mfma_f32_16x16x32_bf16 v[40:43],a[152:155],v[140:143],v[40:43]
v_cmp_ne_u64 vcc,v[16:17],v[12:13]
s_add_u32 s63,s63,s30
v_mfma_f32_16x16x32_bf16 v[44:47],a[156:159],v[140:143],v[44:47]
ds_read_b128 v[184:187],v4 offset:63488
s_cbranch_vccnz .L_rtw1_1
.L_bkw1_1:
v_mfma_f32_16x16x32_bf16 v[44:47],a[148:151],v[136:139],v[44:47]
s_add_u32 s66,s66,s31
s_xor_b32 s64,s64,s65
v_mfma_f32_16x16x32_bf16 v[44:47],a[140:143],v[132:135],v[44:47]
ds_read_b128 v[188:191],v4 offset:64512
s_barrier
v_mfma_f32_16x16x32_bf16 v[44:47],a[132:135],v[128:131],v[44:47]
v_xor_b32 v4,65536,v4
v_mfma_f32_16x16x32_bf16 v[44:47],a[164:167],v[144:147],v[44:47]
ds_read_b128 v[128:131],v4 offset:0
v_lshlrev_b32 v32,16,v48
v_mfma_f32_16x16x32_bf16 v[44:47],a[172:175],v[148:151],v[44:47]
v_and_b32 v33,0xffff0000,v48
v_lshlrev_b32 v34,16,v49
v_mfma_f32_16x16x32_bf16 v[44:47],a[180:183],v[152:155],v[44:47]
ds_read_b128 v[132:135],v4 offset:1024
v_and_b32 v35,0xffff0000,v49
v_mfma_f32_16x16x32_bf16 v[44:47],a[188:191],v[156:159],v[44:47]
v_lshlrev_b32 v36,16,v50
v_and_b32 v37,0xffff0000,v50
v_mfma_f32_16x16x32_bf16 v[40:43],a[184:187],v[156:159],v[40:43]
ds_read_b128 v[136:139],v4 offset:2048
s_add_u32 m0,s64,0
v_mfma_f32_16x16x32_bf16 v[40:43],a[176:179],v[152:155],v[40:43]
buffer_load_dwordx4 v2,s[32:35],s63 offen offset:0 sc1 lds
v_mfma_f32_16x16x32_bf16 v[40:43],a[168:171],v[148:151],v[40:43]
ds_read_b128 v[140:143],v4 offset:3072
v_lshlrev_b32 v38,16,v51
v_mfma_f32_16x16x32_bf16 v[40:43],a[160:163],v[144:147],v[40:43]
v_and_b32 v39,0xffff0000,v51
s_waitcnt lgkmcnt(4)
v_mfma_f32_16x16x32_bf16 v[40:43],a[192:195],v[160:163],v[40:43]
ds_read_b128 v[144:147],v4 offset:4096
v_mfma_f32_16x16x32_bf16 v[40:43],a[200:203],v[164:167],v[40:43]
buffer_load_dwordx4 v2,s[32:35],s63 offen offset:1024 sc1 lds
v_mfma_f32_16x16x32_bf16 v[40:43],a[208:211],v[168:171],v[40:43]
ds_read_b128 v[148:151],v4 offset:5120
v_mfma_f32_16x16x32_bf16 v[40:43],a[216:219],v[172:175],v[40:43]
v_mfma_f32_16x16x32_bf16 v[44:47],a[220:223],v[172:175],v[44:47]
ds_read_b128 v[152:155],v4 offset:6144
v_mfma_f32_16x16x32_bf16 v[44:47],a[212:215],v[168:171],v[44:47]
buffer_load_dwordx4 v2,s[32:35],s63 offen offset:2048 sc1 lds
v_mfma_f32_16x16x32_bf16 v[44:47],a[204:207],v[164:167],v[44:47]
ds_read_b128 v[156:159],v4 offset:7168
v_mfma_f32_16x16x32_bf16 v[44:47],a[196:199],v[160:163],v[44:47]
v_mfma_f32_16x16x32_bf16 v[44:47],a[228:231],v[176:179],v[44:47]
ds_read_b128 v[160:163],v4 offset:8192
v_mfma_f32_16x16x32_bf16 v[44:47],a[236:239],v[180:183],v[44:47]
buffer_load_dwordx4 v2,s[32:35],s63 offen offset:3072 sc1 lds
v_mfma_f32_16x16x32_bf16 v[44:47],a[244:247],v[184:187],v[44:47]
ds_read_b128 v[164:167],v4 offset:9216
v_mfma_f32_16x16x32_bf16 v[44:47],a[252:255],v[188:191],v[44:47]
v_mfma_f32_16x16x32_bf16 v[40:43],a[248:251],v[188:191],v[40:43]
ds_read_b128 v[168:171],v4 offset:10240
s_add_u32 m0,s64,4096
v_mfma_f32_16x16x32_bf16 v[40:43],a[240:243],v[184:187],v[40:43]
buffer_load_dwordx4 v6,s[32:35],s63 offen offset:0 sc1 lds
v_mfma_f32_16x16x32_bf16 v[40:43],a[232:235],v[180:183],v[40:43]
ds_read_b128 v[172:175],v4 offset:11264
s_sub_u32 s62,s62,1
v_mfma_f32_16x16x32_bf16 v[40:43],a[224:227],v[176:179],v[40:43]
s_cbranch_scc0 .L_wl_1
s_nop 1
s_add_u32 s68,s68,s30
s_nop 3
v_cvt_pk_bf16_f32 v56,v40,v41
v_cvt_pk_bf16_f32 v57,v42,v43
v_cvt_pk_bf16_f32 v58,v44,v45
v_cvt_pk_bf16_f32 v59,v46,v47
buffer_store_dwordx4 v[56:59],v2,s[32:35],s68 offen sc1
v_add_f32 v60,v40,v41
v_mul_f32 v61,v40,v40
v_fmac_f32 v61,v41,v41
v_add_f32 v60,v60,v42
v_fmac_f32 v61,v42,v42
v_add_f32 v60,v60,v43
v_fmac_f32 v61,v43,v43
v_add_f32 v60,v60,v44
v_fmac_f32 v61,v44,v44
v_add_f32 v60,v60,v45
v_fmac_f32 v61,v45,v45
v_add_f32 v60,v60,v46
v_fmac_f32 v61,v46,v46
v_add_f32 v60,v60,v47
v_fmac_f32 v61,v47,v47
s_nop 1
v_permlane16_swap_b32 v60,v61
v_add_f32 v60,v60,v61
s_nop 1
v_permlane32_swap_b32 v60,v61
v_add_f32 v60,v60,v61
buffer_store_dword v60,v15,s[32:35],s68 offen offset:1024 sc1
s_waitcnt vmcnt(9)
s_barrier
buffer_store_dwordx2 v[16:17],v2,s[92:95],s68 idxen sc1
s_add_u32 s68,s68,s30
s_waitcnt vmcnt(1)
s_barrier
buffer_store_dwordx2 v[16:17],v2,s[92:95],s68 idxen sc1
s_cbranch_execz .L_wbk
s_branch .L_end
.p2align 6
.L_wv2:
.L_wl_2:
s_waitcnt lgkmcnt(4)
v_mfma_f32_16x16x32_bf16 v[32:35],a[0:3],v[128:131],v[32:35]
ds_read_b128 v[176:179],v4 offset:12288
v_mfma_f32_16x16x32_bf16 v[32:35],a[8:11],v[132:135],v[32:35]
buffer_load_dwordx4 v6,s[32:35],s63 offen offset:1024 sc1 lds
v_mfma_f32_16x16x32_bf16 v[32:35],a[16:19],v[136:139],v[32:35]
ds_read_b128 v[180:183],v4 offset:13312
s_add_u32 s68,s68,s30
v_mfma_f32_16x16x32_bf16 v[32:35],a[24:27],v[140:143],v[32:35]
v_cvt_pk_bf16_f32 v56,v40,v41
v_cvt_pk_bf16_f32 v57,v42,v43
v_mfma_f32_16x16x32_bf16 v[36:39],a[28:31],v[140:143],v[36:39]
ds_read_b128 v[184:187],v4 offset:14336
v_cvt_pk_bf16_f32 v58,v44,v45
v_mfma_f32_16x16x32_bf16 v[36:39],a[20:23],v[136:139],v[36:39]
v_cvt_pk_bf16_f32 v59,v46,v47
v_add_f32 v60,v40,v41
v_mfma_f32_16x16x32_bf16 v[36:39],a[12:15],v[132:135],v[36:39]
ds_read_b128 v[188:191],v4 offset:15360
v_mul_f32 v61,v40,v40
v_mfma_f32_16x16x32_bf16 v[36:39],a[4:7],v[128:131],v[36:39]
v_fmac_f32 v61,v41,v41
v_add_f32 v60,v60,v42
v_mfma_f32_16x16x32_bf16 v[36:39],a[36:39],v[144:147],v[36:39]
ds_read_b128 v[128:131],v4 offset:16384
v_fmac_f32 v61,v42,v42
v_mfma_f32_16x16x32_bf16 v[36:39],a[44:47],v[148:151],v[36:39]
buffer_store_dwordx4 v[56:59],v2,s[32:35],s68 offen sc1
v_mfma_f32_16x16x32_bf16 v[36:39],a[52:55],v[152:155],v[36:39]
ds_read_b128 v[132:135],v4 offset:17408
v_add_f32 v60,v60,v43
v_mfma_f32_16x16x32_bf16 v[36:39],a[60:63],v[156:159],v[36:39]
v_fmac_f32 v61,v43,v43
v_add_f32 v60,v60,v44
v_mfma_f32_16x16x32_bf16 v[32:35],a[56:59],v[156:159],v[32:35]
ds_read_b128 v[136:139],v4 offset:18432
v_fmac_f32 v61,v44,v44
v_mfma_f32_16x16x32_bf16 v[32:35],a[48:51],v[152:155],v[32:35]
v_add_f32 v60,v60,v45
v_fmac_f32 v61,v45,v45
v_mfma_f32_16x16x32_bf16 v[32:35],a[40:43],v[148:151],v[32:35]
ds_read_b128 v[140:143],v4 offset:19456
v_add_f32 v60,v60,v46
v_mfma_f32_16x16x32_bf16 v[32:35],a[32:35],v[144:147],v[32:35]
v_fmac_f32 v61,v46,v46
s_waitcnt lgkmcnt(4)
v_mfma_f32_16x16x32_bf16 v[32:35],a[64:67],v[160:163],v[32:35]
ds_read_b128 v[144:147],v4 offset:20480
v_add_f32 v60,v60,v47
v_mfma_f32_16x16x32_bf16 v[32:35],a[72:75],v[164:167],v[32:35]
v_fmac_f32 v61,v47,v47
v_mfma_f32_16x16x32_bf16 v[32:35],a[80:83],v[168:171],v[32:35]
ds_read_b128 v[148:151],v4 offset:21504
v_mfma_f32_16x16x32_bf16 v[32:35],a[88:91],v[172:175],v[32:35]
v_permlane16_swap_b32 v60,v61
v_mfma_f32_16x16x32_bf16 v[36:39],a[92:95],v[172:175],v[36:39]
ds_read_b128 v[152:155],v4 offset:22528
v_add_f32 v60,v60,v61
v_mfma_f32_16x16x32_bf16 v[36:39],a[84:87],v[168:171],v[36:39]
buffer_load_dwordx4 v[48:51],v2,s[32:35],s71 offen sc1
v_mfma_f32_16x16x32_bf16 v[36:39],a[76:79],v[164:167],v[36:39]
ds_read_b128 v[156:159],v4 offset:23552
s_add_u32 s71,s71,s30
v_mfma_f32_16x16x32_bf16 v[36:39],a[68:71],v[160:163],v[36:39]
v_permlane32_swap_b32 v60,v61
v_mfma_f32_16x16x32_bf16 v[36:39],a[100:103],v[176:179],v[36:39]
ds_read_b128 v[160:163],v4 offset:24576
v_add_f32 v60,v60,v61
v_mfma_f32_16x16x32_bf16 v[36:39],a[108:111],v[180:183],v[36:39]
buffer_store_dword v60,v15,s[32:35],s68 offen offset:1024 sc1
v_mfma_f32_16x16x32_bf16 v[36:39],a[116:119],v[184:187],v[36:39]
ds_read_b128 v[164:167],v4 offset:25600
v_mfma_f32_16x16x32_bf16 v[36:39],a[124:127],v[188:191],v[36:39]
v_mfma_f32_16x16x32_bf16 v[32:35],a[120:123],v[188:191],v[32:35]
ds_read_b128 v[168:171],v4 offset:26624
v_mfma_f32_16x16x32_bf16 v[32:35],a[112:115],v[184:187],v[32:35]
buffer_load_dwordx4 v6,s[32:35],s63 offen offset:2048 sc1 lds
v_mfma_f32_16x16x32_bf16 v[32:35],a[104:107],v[180:183],v[32:35]
ds_read_b128 v[172:175],v4 offset:27648
v_mfma_f32_16x16x32_bf16 v[32:35],a[96:99],v[176:179],v[32:35]
s_waitcnt lgkmcnt(4)
v_mfma_f32_16x16x32_bf16 v[32:35],a[128:131],v[128:131],v[32:35]
ds_read_b128 v[176:179],v4 offset:28672
v_mfma_f32_16x16x32_bf16 v[32:35],a[136:139],v[132:135],v[32:35]
buffer_load_dwordx4 v6,s[32:35],s63 offen offset:3072 sc1 lds
v_mfma_f32_16x16x32_bf16 v[32:35],a[144:147],v[136:139],v[32:35]
ds_read_b128 v[180:183],v4 offset:29696
s_add_u32 s63,s63,s30
v_mfma_f32_16x16x32_bf16 v[32:35],a[152:155],v[140:143],v[32:35]
v_mfma_f32_16x16x32_bf16 v[36:39],a[156:159],v[140:143],v[36:39]
ds_read_b128 v[184:187],v4 offset:30720
v_mfma_f32_16x16x32_bf16 v[36:39],a[148:151],v[136:139],v[36:39]
s_waitcnt vmcnt(11)
s_barrier
v_mfma_f32_16x16x32_bf16 v[36:39],a[140:143],v[132:135],v[36:39]
ds_read_b128 v[188:191],v4 offset:31744
v_mfma_f32_16x16x32_bf16 v[36:39],a[132:135],v[128:131],v[36:39]
v_mfma_f32_16x16x32_bf16 v[36:39],a[164:167],v[144:147],v[36:39]
ds_read_b128 v[128:131],v4 offset:32768
v_lshlrev_b32 v40,16,v52
v_mfma_f32_16x16x32_bf16 v[36:39],a[172:175],v[148:151],v[36:39]
v_and_b32 v41,0xffff0000,v52
v_lshlrev_b32 v42,16,v53
v_mfma_f32_16x16x32_bf16 v[36:39],a[180:183],v[152:155],v[36:39]
ds_read_b128 v[132:135],v4 offset:33792
v_and_b32 v43,0xffff0000,v53
v_mfma_f32_16x16x32_bf16 v[36:39],a[188:191],v[156:159],v[36:39]
v_lshlrev_b32 v44,16,v54
v_and_b32 v45,0xffff0000,v54
v_mfma_f32_16x16x32_bf16 v[32:35],a[184:187],v[156:159],v[32:35]
ds_read_b128 v[136:139],v4 offset:34816
s_add_u32 m0,s64,32768
v_mfma_f32_16x16x32_bf16 v[32:35],a[176:179],v[152:155],v[32:35]
buffer_load_dwordx4 v2,s[32:35],s63 offen offset:0 sc1 lds
v_mfma_f32_16x16x32_bf16 v[32:35],a[168:171],v[148:151],v[32:35]
ds_read_b128 v[140:143],v4 offset:35840
v_lshlrev_b32 v46,16,v55
v_mfma_f32_16x16x32_bf16 v[32:35],a[160:163],v[144:147],v[32:35]
v_and_b32 v47,0xffff0000,v55
s_waitcnt lgkmcnt(4)
v_mfma_f32_16x16x32_bf16 v[32:35],a[192:195],v[160:163],v[32:35]
ds_read_b128 v[144:147],v4 offset:36864
v_mfma_f32_16x16x32_bf16 v[32:35],a[200:203],v[164:167],v[32:35]
buffer_load_dwordx4 v2,s[32:35],s63 offen offset:1024 sc1 lds
v_mfma_f32_16x16x32_bf16 v[32:35],a[208:211],v[168:171],v[32:35]
ds_read_b128 v[148:151],v4 offset:37888
v_mfma_f32_16x16x32_bf16 v[32:35],a[216:219],v[172:175],v[32:35]
v_mfma_f32_16x16x32_bf16 v[36:39],a[220:223],v[172:175],v[36:39]
ds_read_b128 v[152:155],v4 offset:38912
v_mfma_f32_16x16x32_bf16 v[36:39],a[212:215],v[168:171],v[36:39]
buffer_load_dwordx4 v2,s[32:35],s63 offen offset:2048 sc1 lds
v_mfma_f32_16x16x32_bf16 v[36:39],a[204:207],v[164:167],v[36:39]
ds_read_b128 v[156:159],v4 offset:39936
v_mfma_f32_16x16x32_bf16 v[36:39],a[196:199],v[160:163],v[36:39]
v_mfma_f32_16x16x32_bf16 v[36:39],a[228:231],v[176:179],v[36:39]
ds_read_b128 v[160:163],v4 offset:40960
v_mfma_f32_16x16x32_bf16 v[36:39],a[236:239],v[180:183],v[36:39]
buffer_load_dwordx4 v2,s[32:35],s63 offen offset:3072 sc1 lds
v_mfma_f32_16x16x32_bf16 v[36:39],a[244:247],v[184:187],v[36:39]
ds_read_b128 v[164:167],v4 offset:41984
v_mfma_f32_16x16x32_bf16 v[36:39],a[252:255],v[188:191],v[36:39]
v_mfma_f32_16x16x32_bf16 v[32:35],a[248:251],v[188:191],v[32:35]
ds_read_b128 v[168:171],v4 offset:43008
s_add_u32 m0,s64,36864
v_mfma_f32_16x16x32_bf16 v[32:35],a[240:243],v[184:187],v[32:35]
buffer_load_dwordx4 v6,s[32:35],s63 offen offset:0 sc1 lds
v_mfma_f32_16x16x32_bf16 v[32:35],a[232:235],v[180:183],v[32:35]
ds_read_b128 v[172:175],v4 offset:44032
v_mfma_f32_16x16x32_bf16 v[32:35],a[224:227],v[176:179],v[32:35]
s_waitcnt lgkmcnt(4)
v_mfma_f32_16x16x32_bf16 v[40:43],a[0:3],v[128:131],v[40:43]
ds_read_b128 v[176:179],v4 offset:45056
v_mfma_f32_16x16x32_bf16 v[40:43],a[8:11],v[132:135],v[40:43]
buffer_load_dwordx4 v6,s[32:35],s63 offen offset:1024 sc1 lds
v_mfma_f32_16x16x32_bf16 v[40:43],a[16:19],v[136:139],v[40:43]
ds_read_b128 v[180:183],v4 offset:46080
s_add_u32 s68,s68,s30
v_mfma_f32_16x16x32_bf16 v[40:43],a[24:27],v[140:143],v[40:43]
v_cvt_pk_bf16_f32 v56,v32,v33
v_cvt_pk_bf16_f32 v57,v34,v35
v_mfma_f32_16x16x32_bf16 v[44:47],a[28:31],v[140:143],v[44:47]
ds_read_b128 v[184:187],v4 offset:47104
v_cvt_pk_bf16_f32 v58,v36,v37
v_mfma_f32_16x16x32_bf16 v[44:47],a[20:23],v[136:139],v[44:47]
v_cvt_pk_bf16_f32 v59,v38,v39
v_add_f32 v60,v32,v33
v_mfma_f32_16x16x32_bf16 v[44:47],a[12:15],v[132:135],v[44:47]
ds_read_b128 v[188:191],v4 offset:48128
v_mul_f32 v61,v32,v32
v_mfma_f32_16x16x32_bf16 v[44:47],a[4:7],v[128:131],v[44:47]
v_fmac_f32 v61,v33,v33
v_add_f32 v60,v60,v34
v_mfma_f32_16x16x32_bf16 v[44:47],a[36:39],v[144:147],v[44:47]
ds_read_b128 v[128:131],v4 offset:49152
v_fmac_f32 v61,v34,v34
v_mfma_f32_16x16x32_bf16 v[44:47],a[44:47],v[148:151],v[44:47]
buffer_store_dwordx4 v[56:59],v2,s[32:35],s68 offen sc1
v_mfma_f32_16x16x32_bf16 v[44:47],a[52:55],v[152:155],v[44:47]
ds_read_b128 v[132:135],v4 offset:50176
v_add_f32 v60,v60,v35
v_mfma_f32_16x16x32_bf16 v[44:47],a[60:63],v[156:159],v[44:47]
v_fmac_f32 v61,v35,v35
v_add_f32 v60,v60,v36
v_mfma_f32_16x16x32_bf16 v[40:43],a[56:59],v[156:159],v[40:43]
ds_read_b128 v[136:139],v4 offset:51200
v_fmac_f32 v61,v36,v36
v_mfma_f32_16x16x32_bf16 v[40:43],a[48:51],v[152:155],v[40:43]
v_add_f32 v60,v60,v37
v_fmac_f32 v61,v37,v37
v_mfma_f32_16x16x32_bf16 v[40:43],a[40:43],v[148:151],v[40:43]
ds_read_b128 v[140:143],v4 offset:52224
v_add_f32 v60,v60,v38
v_mfma_f32_16x16x32_bf16 v[40:43],a[32:35],v[144:147],v[40:43]
v_fmac_f32 v61,v38,v38
s_waitcnt lgkmcnt(4)
v_mfma_f32_16x16x32_bf16 v[40:43],a[64:67],v[160:163],v[40:43]
ds_read_b128 v[144:147],v4 offset:53248
v_add_f32 v60,v60,v39
v_mfma_f32_16x16x32_bf16 v[40:43],a[72:75],v[164:167],v[40:43]
v_fmac_f32 v61,v39,v39
v_mfma_f32_16x16x32_bf16 v[40:43],a[80:83],v[168:171],v[40:43]
)#"
R"#(
ds_read_b128 v[148:151],v4 offset:54272
v_mfma_f32_16x16x32_bf16 v[40:43],a[88:91],v[172:175],v[40:43]
v_permlane16_swap_b32 v60,v61
v_mfma_f32_16x16x32_bf16 v[44:47],a[92:95],v[172:175],v[44:47]
ds_read_b128 v[152:155],v4 offset:55296
v_add_f32 v60,v60,v61
v_mfma_f32_16x16x32_bf16 v[44:47],a[84:87],v[168:171],v[44:47]
buffer_load_dwordx4 v[52:55],v2,s[32:35],s71 offen sc1
v_mfma_f32_16x16x32_bf16 v[44:47],a[76:79],v[164:167],v[44:47]
ds_read_b128 v[156:159],v4 offset:56320
s_add_u32 s71,s71,s30
v_mfma_f32_16x16x32_bf16 v[44:47],a[68:71],v[160:163],v[44:47]
v_permlane32_swap_b32 v60,v61
v_mfma_f32_16x16x32_bf16 v[44:47],a[100:103],v[176:179],v[44:47]
ds_read_b128 v[160:163],v4 offset:57344
v_add_f32 v60,v60,v61
v_mfma_f32_16x16x32_bf16 v[44:47],a[108:111],v[180:183],v[44:47]
buffer_store_dword v60,v15,s[32:35],s68 offen offset:1024 sc1
v_mfma_f32_16x16x32_bf16 v[44:47],a[116:119],v[184:187],v[44:47]
ds_read_b128 v[164:167],v4 offset:58368
v_mfma_f32_16x16x32_bf16 v[44:47],a[124:127],v[188:191],v[44:47]
v_mfma_f32_16x16x32_bf16 v[40:43],a[120:123],v[188:191],v[40:43]
ds_read_b128 v[168:171],v4 offset:59392
v_mfma_f32_16x16x32_bf16 v[40:43],a[112:115],v[184:187],v[40:43]
buffer_load_dwordx4 v6,s[32:35],s63 offen offset:2048 sc1 lds
v_mfma_f32_16x16x32_bf16 v[40:43],a[104:107],v[180:183],v[40:43]
ds_read_b128 v[172:175],v4 offset:60416
v_mfma_f32_16x16x32_bf16 v[40:43],a[96:99],v[176:179],v[40:43]
s_waitcnt lgkmcnt(4)
v_mfma_f32_16x16x32_bf16 v[40:43],a[128:131],v[128:131],v[40:43]
ds_read_b128 v[176:179],v4 offset:61440
v_mfma_f32_16x16x32_bf16 v[40:43],a[136:139],v[132:135],v[40:43]
buffer_load_dwordx4 v6,s[32:35],s63 offen offset:3072 sc1 lds
v_mfma_f32_16x16x32_bf16 v[40:43],a[144:147],v[136:139],v[40:43]
ds_read_b128 v[180:183],v4 offset:62464
s_add_u32 s63,s63,s30
v_mfma_f32_16x16x32_bf16 v[40:43],a[152:155],v[140:143],v[40:43]
s_xor_b32 s64,s64,s65
v_mfma_f32_16x16x32_bf16 v[44:47],a[156:159],v[140:143],v[44:47]
ds_read_b128 v[184:187],v4 offset:63488
v_mfma_f32_16x16x32_bf16 v[44:47],a[148:151],v[136:139],v[44:47]
s_waitcnt vmcnt(11)
s_barrier
v_mfma_f32_16x16x32_bf16 v[44:47],a[140:143],v[132:135],v[44:47]
ds_read_b128 v[188:191],v4 offset:64512
v_xor_b32 v4,65536,v4
v_mfma_f32_16x16x32_bf16 v[44:47],a[132:135],v[128:131],v[44:47]
v_mfma_f32_16x16x32_bf16 v[44:47],a[164:167],v[144:147],v[44:47]
ds_read_b128 v[128:131],v4 offset:0
v_lshlrev_b32 v32,16,v48
v_mfma_f32_16x16x32_bf16 v[44:47],a[172:175],v[148:151],v[44:47]
v_and_b32 v33,0xffff0000,v48
v_lshlrev_b32 v34,16,v49
v_mfma_f32_16x16x32_bf16 v[44:47],a[180:183],v[152:155],v[44:47]
ds_read_b128 v[132:135],v4 offset:1024
v_and_b32 v35,0xffff0000,v49
v_mfma_f32_16x16x32_bf16 v[44:47],a[188:191],v[156:159],v[44:47]
v_lshlrev_b32 v36,16,v50
v_and_b32 v37,0xffff0000,v50
v_mfma_f32_16x16x32_bf16 v[40:43],a[184:187],v[156:159],v[40:43]
ds_read_b128 v[136:139],v4 offset:2048
s_add_u32 m0,s64,0
v_mfma_f32_16x16x32_bf16 v[40:43],a[176:179],v[152:155],v[40:43]
buffer_load_dwordx4 v2,s[32:35],s63 offen offset:0 sc1 lds
v_mfma_f32_16x16x32_bf16 v[40:43],a[168:171],v[148:151],v[40:43]
ds_read_b128 v[140:143],v4 offset:3072
v_lshlrev_b32 v38,16,v51
v_mfma_f32_16x16x32_bf16 v[40:43],a[160:163],v[144:147],v[40:43]
v_and_b32 v39,0xffff0000,v51
s_waitcnt lgkmcnt(4)
v_mfma_f32_16x16x32_bf16 v[40:43],a[192:195],v[160:163],v[40:43]
ds_read_b128 v[144:147],v4 offset:4096
v_mfma_f32_16x16x32_bf16 v[40:43],a[200:203],v[164:167],v[40:43]
buffer_load_dwordx4 v2,s[32:35],s63 offen offset:1024 sc1 lds
v_mfma_f32_16x16x32_bf16 v[40:43],a[208:211],v[168:171],v[40:43]
ds_read_b128 v[148:151],v4 offset:5120
v_mfma_f32_16x16x32_bf16 v[40:43],a[216:219],v[172:175],v[40:43]
v_mfma_f32_16x16x32_bf16 v[44:47],a[220:223],v[172:175],v[44:47]
ds_read_b128 v[152:155],v4 offset:6144
v_mfma_f32_16x16x32_bf16 v[44:47],a[212:215],v[168:171],v[44:47]
buffer_load_dwordx4 v2,s[32:35],s63 offen offset:2048 sc1 lds
v_mfma_f32_16x16x32_bf16 v[44:47],a[204:207],v[164:167],v[44:47]
ds_read_b128 v[156:159],v4 offset:7168
v_mfma_f32_16x16x32_bf16 v[44:47],a[196:199],v[160:163],v[44:47]
v_mfma_f32_16x16x32_bf16 v[44:47],a[228:231],v[176:179],v[44:47]
ds_read_b128 v[160:163],v4 offset:8192
v_mfma_f32_16x16x32_bf16 v[44:47],a[236:239],v[180:183],v[44:47]
buffer_load_dwordx4 v2,s[32:35],s63 offen offset:3072 sc1 lds
v_mfma_f32_16x16x32_bf16 v[44:47],a[244:247],v[184:187],v[44:47]
ds_read_b128 v[164:167],v4 offset:9216
v_mfma_f32_16x16x32_bf16 v[44:47],a[252:255],v[188:191],v[44:47]
v_mfma_f32_16x16x32_bf16 v[40:43],a[248:251],v[188:191],v[40:43]
ds_read_b128 v[168:171],v4 offset:10240
s_add_u32 m0,s64,4096
v_mfma_f32_16x16x32_bf16 v[40:43],a[240:243],v[184:187],v[40:43]
buffer_load_dwordx4 v6,s[32:35],s63 offen offset:0 sc1 lds
v_mfma_f32_16x16x32_bf16 v[40:43],a[232:235],v[180:183],v[40:43]
ds_read_b128 v[172:175],v4 offset:11264
s_sub_u32 s62,s62,1
v_mfma_f32_16x16x32_bf16 v[40:43],a[224:227],v[176:179],v[40:43]
s_cbranch_scc0 .L_wl_2
s_nop 1
s_add_u32 s68,s68,s30
s_nop 3
v_cvt_pk_bf16_f32 v56,v40,v41
v_cvt_pk_bf16_f32 v57,v42,v43
v_cvt_pk_bf16_f32 v58,v44,v45
v_cvt_pk_bf16_f32 v59,v46,v47
buffer_store_dwordx4 v[56:59],v2,s[32:35],s68 offen sc1
v_add_f32 v60,v40,v41
v_mul_f32 v61,v40,v40
v_fmac_f32 v61,v41,v41
v_add_f32 v60,v60,v42
v_fmac_f32 v61,v42,v42
v_add_f32 v60,v60,v43
v_fmac_f32 v61,v43,v43
v_add_f32 v60,v60,v44
v_fmac_f32 v61,v44,v44
v_add_f32 v60,v60,v45
v_fmac_f32 v61,v45,v45
v_add_f32 v60,v60,v46
v_fmac_f32 v61,v46,v46
v_add_f32 v60,v60,v47
v_fmac_f32 v61,v47,v47
s_nop 1
v_permlane16_swap_b32 v60,v61
v_add_f32 v60,v60,v61
s_nop 1
v_permlane32_swap_b32 v60,v61
v_add_f32 v60,v60,v61
buffer_store_dword v60,v15,s[32:35],s68 offen offset:1024 sc1
s_waitcnt vmcnt(9)
s_barrier
buffer_store_dwordx2 v[16:17],v2,s[92:95],s68 idxen sc1
s_add_u32 s68,s68,s30
s_waitcnt vmcnt(1)
s_barrier
buffer_store_dwordx2 v[16:17],v2,s[92:95],s68 idxen sc1
s_cbranch_execz .L_wbk
s_branch .L_end
.p2align 6
.L_wv3:
.L_wl_3:
s_waitcnt lgkmcnt(4)
v_mfma_f32_16x16x32_bf16 v[32:35],a[0:3],v[128:131],v[32:35]
ds_read_b128 v[176:179],v4 offset:12288
v_mfma_f32_16x16x32_bf16 v[32:35],a[8:11],v[132:135],v[32:35]
buffer_load_dwordx4 v6,s[32:35],s63 offen offset:1024 sc1 lds
v_mfma_f32_16x16x32_bf16 v[32:35],a[16:19],v[136:139],v[32:35]
ds_read_b128 v[180:183],v4 offset:13312
s_add_u32 s68,s68,s30
v_mfma_f32_16x16x32_bf16 v[32:35],a[24:27],v[140:143],v[32:35]
v_cvt_pk_bf16_f32 v56,v40,v41
v_cvt_pk_bf16_f32 v57,v42,v43
v_mfma_f32_16x16x32_bf16 v[36:39],a[28:31],v[140:143],v[36:39]
ds_read_b128 v[184:187],v4 offset:14336
v_cvt_pk_bf16_f32 v58,v44,v45
v_mfma_f32_16x16x32_bf16 v[36:39],a[20:23],v[136:139],v[36:39]
v_cvt_pk_bf16_f32 v59,v46,v47
v_add_f32 v60,v40,v41
v_mfma_f32_16x16x32_bf16 v[36:39],a[12:15],v[132:135],v[36:39]
ds_read_b128 v[188:191],v4 offset:15360
v_mul_f32 v61,v40,v40
v_mfma_f32_16x16x32_bf16 v[36:39],a[4:7],v[128:131],v[36:39]
v_fmac_f32 v61,v41,v41
v_add_f32 v60,v60,v42
v_mfma_f32_16x16x32_bf16 v[36:39],a[36:39],v[144:147],v[36:39]
ds_read_b128 v[128:131],v4 offset:16384
v_fmac_f32 v61,v42,v42
v_mfma_f32_16x16x32_bf16 v[36:39],a[44:47],v[148:151],v[36:39]
buffer_store_dwordx4 v[56:59],v2,s[32:35],s68 offen sc1
v_mfma_f32_16x16x32_bf16 v[36:39],a[52:55],v[152:155],v[36:39]
ds_read_b128 v[132:135],v4 offset:17408
v_add_f32 v60,v60,v43
v_mfma_f32_16x16x32_bf16 v[36:39],a[60:63],v[156:159],v[36:39]
v_fmac_f32 v61,v43,v43
v_add_f32 v60,v60,v44
v_mfma_f32_16x16x32_bf16 v[32:35],a[56:59],v[156:159],v[32:35]
ds_read_b128 v[136:139],v4 offset:18432
v_fmac_f32 v61,v44,v44
v_mfma_f32_16x16x32_bf16 v[32:35],a[48:51],v[152:155],v[32:35]
v_add_f32 v60,v60,v45
v_fmac_f32 v61,v45,v45
v_mfma_f32_16x16x32_bf16 v[32:35],a[40:43],v[148:151],v[32:35]
ds_read_b128 v[140:143],v4 offset:19456
v_add_f32 v60,v60,v46
v_mfma_f32_16x16x32_bf16 v[32:35],a[32:35],v[144:147],v[32:35]
v_fmac_f32 v61,v46,v46
s_waitcnt lgkmcnt(4)
v_mfma_f32_16x16x32_bf16 v[32:35],a[64:67],v[160:163],v[32:35]
ds_read_b128 v[144:147],v4 offset:20480
v_add_f32 v60,v60,v47
v_mfma_f32_16x16x32_bf16 v[32:35],a[72:75],v[164:167],v[32:35]
v_fmac_f32 v61,v47,v47
v_mfma_f32_16x16x32_bf16 v[32:35],a[80:83],v[168:171],v[32:35]
ds_read_b128 v[148:151],v4 offset:21504
v_mfma_f32_16x16x32_bf16 v[32:35],a[88:91],v[172:175],v[32:35]
v_permlane16_swap_b32 v60,v61
v_mfma_f32_16x16x32_bf16 v[36:39],a[92:95],v[172:175],v[36:39]
ds_read_b128 v[152:155],v4 offset:22528
v_add_f32 v60,v60,v61
v_mfma_f32_16x16x32_bf16 v[36:39],a[84:87],v[168:171],v[36:39]
buffer_load_dwordx4 v[48:51],v2,s[32:35],s71 offen sc1
v_mfma_f32_16x16x32_bf16 v[36:39],a[76:79],v[164:167],v[36:39]
ds_read_b128 v[156:159],v4 offset:23552
s_add_u32 s71,s71,s30
v_mfma_f32_16x16x32_bf16 v[36:39],a[68:71],v[160:163],v[36:39]
v_permlane32_swap_b32 v60,v61
v_mfma_f32_16x16x32_bf16 v[36:39],a[100:103],v[176:179],v[36:39]
ds_read_b128 v[160:163],v4 offset:24576
v_add_f32 v60,v60,v61
v_mfma_f32_16x16x32_bf16 v[36:39],a[108:111],v[180:183],v[36:39]
buffer_store_dword v60,v15,s[32:35],s68 offen offset:1024 sc1
v_mfma_f32_16x16x32_bf16 v[36:39],a[116:119],v[184:187],v[36:39]
ds_read_b128 v[164:167],v4 offset:25600
v_mfma_f32_16x16x32_bf16 v[36:39],a[124:127],v[188:191],v[36:39]
v_mfma_f32_16x16x32_bf16 v[32:35],a[120:123],v[188:191],v[32:35]
ds_read_b128 v[168:171],v4 offset:26624
v_mfma_f32_16x16x32_bf16 v[32:35],a[112:115],v[184:187],v[32:35]
buffer_load_dwordx4 v6,s[32:35],s63 offen offset:2048 sc1 lds
v_mfma_f32_16x16x32_bf16 v[32:35],a[104:107],v[180:183],v[32:35]
ds_read_b128 v[172:175],v4 offset:27648
v_mfma_f32_16x16x32_bf16 v[32:35],a[96:99],v[176:179],v[32:35]
s_waitcnt lgkmcnt(4)
v_mfma_f32_16x16x32_bf16 v[32:35],a[128:131],v[128:131],v[32:35]
ds_read_b128 v[176:179],v4 offset:28672
v_mfma_f32_16x16x32_bf16 v[32:35],a[136:139],v[132:135],v[32:35]
buffer_load_dwordx4 v6,s[32:35],s63 offen offset:3072 sc1 lds
v_mfma_f32_16x16x32_bf16 v[32:35],a[144:147],v[136:139],v[32:35]
ds_read_b128 v[180:183],v4 offset:29696
s_add_u32 s63,s63,s30
v_mfma_f32_16x16x32_bf16 v[32:35],a[152:155],v[140:143],v[32:35]
v_mfma_f32_16x16x32_bf16 v[36:39],a[156:159],v[140:143],v[36:39]
s_waitcnt vmcnt(11)
s_barrier
v_mfma_f32_16x16x32_bf16 v[36:39],a[148:151],v[136:139],v[36:39]
ds_read_b128 v[184:187],v4 offset:30720
v_mfma_f32_16x16x32_bf16 v[36:39],a[140:143],v[132:135],v[36:39]
ds_read_b128 v[188:191],v4 offset:31744
v_mfma_f32_16x16x32_bf16 v[36:39],a[132:135],v[128:131],v[36:39]
v_mfma_f32_16x16x32_bf16 v[36:39],a[164:167],v[144:147],v[36:39]
ds_read_b128 v[128:131],v4 offset:32768
v_lshlrev_b32 v40,16,v52
v_mfma_f32_16x16x32_bf16 v[36:39],a[172:175],v[148:151],v[36:39]
v_and_b32 v41,0xffff0000,v52
v_lshlrev_b32 v42,16,v53
v_mfma_f32_16x16x32_bf16 v[36:39],a[180:183],v[152:155],v[36:39]
ds_read_b128 v[132:135],v4 offset:33792
v_and_b32 v43,0xffff0000,v53
v_mfma_f32_16x16x32_bf16 v[36:39],a[188:191],v[156:159],v[36:39]
v_lshlrev_b32 v44,16,v54
v_and_b32 v45,0xffff0000,v54
v_mfma_f32_16x16x32_bf16 v[32:35],a[184:187],v[156:159],v[32:35]
ds_read_b128 v[136:139],v4 offset:34816
s_add_u32 m0,s64,32768
v_mfma_f32_16x16x32_bf16 v[32:35],a[176:179],v[152:155],v[32:35]
buffer_load_dwordx4 v2,s[32:35],s63 offen offset:0 sc1 lds
v_mfma_f32_16x16x32_bf16 v[32:35],a[168:171],v[148:151],v[32:35]
ds_read_b128 v[140:143],v4 offset:35840
v_lshlrev_b32 v46,16,v55
v_mfma_f32_16x16x32_bf16 v[32:35],a[160:163],v[144:147],v[32:35]
v_and_b32 v47,0xffff0000,v55
s_waitcnt lgkmcnt(4)
v_mfma_f32_16x16x32_bf16 v[32:35],a[192:195],v[160:163],v[32:35]
ds_read_b128 v[144:147],v4 offset:36864
v_mfma_f32_16x16x32_bf16 v[32:35],a[200:203],v[164:167],v[32:35]
buffer_load_dwordx4 v2,s[32:35],s63 offen offset:1024 sc1 lds
v_mfma_f32_16x16x32_bf16 v[32:35],a[208:211],v[168:171],v[32:35]
ds_read_b128 v[148:151],v4 offset:37888
v_mfma_f32_16x16x32_bf16 v[32:35],a[216:219],v[172:175],v[32:35]
v_mfma_f32_16x16x32_bf16 v[36:39],a[220:223],v[172:175],v[36:39]
ds_read_b128 v[152:155],v4 offset:38912
v_mfma_f32_16x16x32_bf16 v[36:39],a[212:215],v[168:171],v[36:39]
buffer_load_dwordx4 v2,s[32:35],s63 offen offset:2048 sc1 lds
v_mfma_f32_16x16x32_bf16 v[36:39],a[204:207],v[164:167],v[36:39]
ds_read_b128 v[156:159],v4 offset:39936
v_mfma_f32_16x16x32_bf16 v[36:39],a[196:199],v[160:163],v[36:39]
v_mfma_f32_16x16x32_bf16 v[36:39],a[228:231],v[176:179],v[36:39]
ds_read_b128 v[160:163],v4 offset:40960
v_mfma_f32_16x16x32_bf16 v[36:39],a[236:239],v[180:183],v[36:39]
buffer_load_dwordx4 v2,s[32:35],s63 offen offset:3072 sc1 lds
v_mfma_f32_16x16x32_bf16 v[36:39],a[244:247],v[184:187],v[36:39]
ds_read_b128 v[164:167],v4 offset:41984
v_mfma_f32_16x16x32_bf16 v[36:39],a[252:255],v[188:191],v[36:39]
v_mfma_f32_16x16x32_bf16 v[32:35],a[248:251],v[188:191],v[32:35]
ds_read_b128 v[168:171],v4 offset:43008
s_add_u32 m0,s64,36864
v_mfma_f32_16x16x32_bf16 v[32:35],a[240:243],v[184:187],v[32:35]
buffer_load_dwordx4 v6,s[32:35],s63 offen offset:0 sc1 lds
v_mfma_f32_16x16x32_bf16 v[32:35],a[232:235],v[180:183],v[32:35]
ds_read_b128 v[172:175],v4 offset:44032
v_mfma_f32_16x16x32_bf16 v[32:35],a[224:227],v[176:179],v[32:35]
s_waitcnt lgkmcnt(4)
v_mfma_f32_16x16x32_bf16 v[40:43],a[0:3],v[128:131],v[40:43]
ds_read_b128 v[176:179],v4 offset:45056
v_mfma_f32_16x16x32_bf16 v[40:43],a[8:11],v[132:135],v[40:43]
buffer_load_dwordx4 v6,s[32:35],s63 offen offset:1024 sc1 lds
v_mfma_f32_16x16x32_bf16 v[40:43],a[16:19],v[136:139],v[40:43]
ds_read_b128 v[180:183],v4 offset:46080
s_add_u32 s68,s68,s30
v_mfma_f32_16x16x32_bf16 v[40:43],a[24:27],v[140:143],v[40:43]
v_cvt_pk_bf16_f32 v56,v32,v33
v_cvt_pk_bf16_f32 v57,v34,v35
v_mfma_f32_16x16x32_bf16 v[44:47],a[28:31],v[140:143],v[44:47]
ds_read_b128 v[184:187],v4 offset:47104
v_cvt_pk_bf16_f32 v58,v36,v37
v_mfma_f32_16x16x32_bf16 v[44:47],a[20:23],v[136:139],v[44:47]
v_cvt_pk_bf16_f32 v59,v38,v39
v_add_f32 v60,v32,v33
v_mfma_f32_16x16x32_bf16 v[44:47],a[12:15],v[132:135],v[44:47]
ds_read_b128 v[188:191],v4 offset:48128
v_mul_f32 v61,v32,v32
v_mfma_f32_16x16x32_bf16 v[44:47],a[4:7],v[128:131],v[44:47]
v_fmac_f32 v61,v33,v33
v_add_f32 v60,v60,v34
v_mfma_f32_16x16x32_bf16 v[44:47],a[36:39],v[144:147],v[44:47]
ds_read_b128 v[128:131],v4 offset:49152
v_fmac_f32 v61,v34,v34
v_mfma_f32_16x16x32_bf16 v[44:47],a[44:47],v[148:151],v[44:47]
buffer_store_dwordx4 v[56:59],v2,s[32:35],s68 offen sc1
v_mfma_f32_16x16x32_bf16 v[44:47],a[52:55],v[152:155],v[44:47]
ds_read_b128 v[132:135],v4 offset:50176
v_add_f32 v60,v60,v35
v_mfma_f32_16x16x32_bf16 v[44:47],a[60:63],v[156:159],v[44:47]
v_fmac_f32 v61,v35,v35
v_add_f32 v60,v60,v36
v_mfma_f32_16x16x32_bf16 v[40:43],a[56:59],v[156:159],v[40:43]
ds_read_b128 v[136:139],v4 offset:51200
v_fmac_f32 v61,v36,v36
v_mfma_f32_16x16x32_bf16 v[40:43],a[48:51],v[152:155],v[40:43]
v_add_f32 v60,v60,v37
v_fmac_f32 v61,v37,v37
v_mfma_f32_16x16x32_bf16 v[40:43],a[40:43],v[148:151],v[40:43]
ds_read_b128 v[140:143],v4 offset:52224
v_add_f32 v60,v60,v38
v_mfma_f32_16x16x32_bf16 v[40:43],a[32:35],v[144:147],v[40:43]
v_fmac_f32 v61,v38,v38
s_waitcnt lgkmcnt(4)
v_mfma_f32_16x16x32_bf16 v[40:43],a[64:67],v[160:163],v[40:43]
ds_read_b128 v[144:147],v4 offset:53248
v_add_f32 v60,v60,v39
v_mfma_f32_16x16x32_bf16 v[40:43],a[72:75],v[164:167],v[40:43]
v_fmac_f32 v61,v39,v39
v_mfma_f32_16x16x32_bf16 v[40:43],a[80:83],v[168:171],v[40:43]
ds_read_b128 v[148:151],v4 offset:54272
v_mfma_f32_16x16x32_bf16 v[40:43],a[88:91],v[172:175],v[40:43]
v_permlane16_swap_b32 v60,v61
v_mfma_f32_16x16x32_bf16 v[44:47],a[92:95],v[172:175],v[44:47]
ds_read_b128 v[152:155],v4 offset:55296
v_add_f32 v60,v60,v61
v_mfma_f32_16x16x32_bf16 v[44:47],a[84:87],v[168:171],v[44:47]
buffer_load_dwordx4 v[52:55],v2,s[32:35],s71 offen sc1
v_mfma_f32_16x16x32_bf16 v[44:47],a[76:79],v[164:167],v[44:47]
ds_read_b128 v[156:159],v4 offset:56320
s_add_u32 s71,s71,s30
v_mfma_f32_16x16x32_bf16 v[44:47],a[68:71],v[160:163],v[44:47]
v_permlane32_swap_b32 v60,v61
v_mfma_f32_16x16x32_bf16 v[44:47],a[100:103],v[176:179],v[44:47]
ds_read_b128 v[160:163],v4 offset:57344
v_add_f32 v60,v60,v61
v_mfma_f32_16x16x32_bf16 v[44:47],a[108:111],v[180:183],v[44:47]
buffer_store_dword v60,v15,s[32:35],s68 offen offset:1024 sc1
v_mfma_f32_16x16x32_bf16 v[44:47],a[116:119],v[184:187],v[44:47]
ds_read_b128 v[164:167],v4 offset:58368
v_mfma_f32_16x16x32_bf16 v[44:47],a[124:127],v[188:191],v[44:47]
v_mfma_f32_16x16x32_bf16 v[40:43],a[120:123],v[188:191],v[40:43]
ds_read_b128 v[168:171],v4 offset:59392
v_mfma_f32_16x16x32_bf16 v[40:43],a[112:115],v[184:187],v[40:43]
buffer_load_dwordx4 v6,s[32:35],s63 offen offset:2048 sc1 lds
v_mfma_f32_16x16x32_bf16 v[40:43],a[104:107],v[180:183],v[40:43]
ds_read_b128 v[172:175],v4 offset:60416
v_mfma_f32_16x16x32_bf16 v[40:43],a[96:99],v[176:179],v[40:43]
s_waitcnt lgkmcnt(4)
v_mfma_f32_16x16x32_bf16 v[40:43],a[128:131],v[128:131],v[40:43]
ds_read_b128 v[176:179],v4 offset:61440
v_mfma_f32_16x16x32_bf16 v[40:43],a[136:139],v[132:135],v[40:43]
buffer_load_dwordx4 v6,s[32:35],s63 offen offset:3072 sc1 lds
v_mfma_f32_16x16x32_bf16 v[40:43],a[144:147],v[136:139],v[40:43]
ds_read_b128 v[180:183],v4 offset:62464
s_add_u32 s63,s63,s30
v_mfma_f32_16x16x32_bf16 v[40:43],a[152:155],v[140:143],v[40:43]
s_xor_b32 s64,s64,s65
v_mfma_f32_16x16x32_bf16 v[44:47],a[156:159],v[140:143],v[44:47]
s_waitcnt vmcnt(11)
s_barrier
v_mfma_f32_16x16x32_bf16 v[44:47],a[148:151],v[136:139],v[44:47]
ds_read_b128 v[184:187],v4 offset:63488
v_mfma_f32_16x16x32_bf16 v[44:47],a[140:143],v[132:135],v[44:47]
ds_read_b128 v[188:191],v4 offset:64512
v_xor_b32 v4,65536,v4
v_mfma_f32_16x16x32_bf16 v[44:47],a[132:135],v[128:131],v[44:47]
v_mfma_f32_16x16x32_bf16 v[44:47],a[164:167],v[144:147],v[44:47]
ds_read_b128 v[128:131],v4 offset:0
v_lshlrev_b32 v32,16,v48
v_mfma_f32_16x16x32_bf16 v[44:47],a[172:175],v[148:151],v[44:47]
v_and_b32 v33,0xffff0000,v48
v_lshlrev_b32 v34,16,v49
v_mfma_f32_16x16x32_bf16 v[44:47],a[180:183],v[152:155],v[44:47]
ds_read_b128 v[132:135],v4 offset:1024
v_and_b32 v35,0xffff0000,v49
v_mfma_f32_16x16x32_bf16 v[44:47],a[188:191],v[156:159],v[44:47]
v_lshlrev_b32 v36,16,v50
v_and_b32 v37,0xffff0000,v50
v_mfma_f32_16x16x32_bf16 v[40:43],a[184:187],v[156:159],v[40:43]
ds_read_b128 v[136:139],v4 offset:2048
s_add_u32 m0,s64,0
v_mfma_f32_16x16x32_bf16 v[40:43],a[176:179],v[152:155],v[40:43]
buffer_load_dwordx4 v2,s[32:35],s63 offen offset:0 sc1 lds
v_mfma_f32_16x16x32_bf16 v[40:43],a[168:171],v[148:151],v[40:43]
ds_read_b128 v[140:143],v4 offset:3072
v_lshlrev_b32 v38,16,v51
v_mfma_f32_16x16x32_bf16 v[40:43],a[160:163],v[144:147],v[40:43]
v_and_b32 v39,0xffff0000,v51
s_waitcnt lgkmcnt(4)
v_mfma_f32_16x16x32_bf16 v[40:43],a[192:195],v[160:163],v[40:43]
ds_read_b128 v[144:147],v4 offset:4096
v_mfma_f32_16x16x32_bf16 v[40:43],a[200:203],v[164:167],v[40:43]
buffer_load_dwordx4 v2,s[32:35],s63 offen offset:1024 sc1 lds
v_mfma_f32_16x16x32_bf16 v[40:43],a[208:211],v[168:171],v[40:43]
ds_read_b128 v[148:151],v4 offset:5120
v_mfma_f32_16x16x32_bf16 v[40:43],a[216:219],v[172:175],v[40:43]
v_mfma_f32_16x16x32_bf16 v[44:47],a[220:223],v[172:175],v[44:47]
ds_read_b128 v[152:155],v4 offset:6144
v_mfma_f32_16x16x32_bf16 v[44:47],a[212:215],v[168:171],v[44:47]
buffer_load_dwordx4 v2,s[32:35],s63 offen offset:2048 sc1 lds
v_mfma_f32_16x16x32_bf16 v[44:47],a[204:207],v[164:167],v[44:47]
ds_read_b128 v[156:159],v4 offset:7168
v_mfma_f32_16x16x32_bf16 v[44:47],a[196:199],v[160:163],v[44:47]
v_mfma_f32_16x16x32_bf16 v[44:47],a[228:231],v[176:179],v[44:47]
ds_read_b128 v[160:163],v4 offset:8192
v_mfma_f32_16x16x32_bf16 v[44:47],a[236:239],v[180:183],v[44:47]
buffer_load_dwordx4 v2,s[32:35],s63 offen offset:3072 sc1 lds
v_mfma_f32_16x16x32_bf16 v[44:47],a[244:247],v[184:187],v[44:47]
ds_read_b128 v[164:167],v4 offset:9216
v_mfma_f32_16x16x32_bf16 v[44:47],a[252:255],v[188:191],v[44:47]
v_mfma_f32_16x16x32_bf16 v[40:43],a[248:251],v[188:191],v[40:43]
ds_read_b128 v[168:171],v4 offset:10240
s_add_u32 m0,s64,4096
v_mfma_f32_16x16x32_bf16 v[40:43],a[240:243],v[184:187],v[40:43]
buffer_load_dwordx4 v6,s[32:35],s63 offen offset:0 sc1 lds
v_mfma_f32_16x16x32_bf16 v[40:43],a[232:235],v[180:183],v[40:43]
ds_read_b128 v[172:175],v4 offset:11264
s_sub_u32 s62,s62,1
v_mfma_f32_16x16x32_bf16 v[40:43],a[224:227],v[176:179],v[40:43]
s_cbranch_scc0 .L_wl_3
s_nop 1
s_add_u32 s68,s68,s30
s_nop 3
v_cvt_pk_bf16_f32 v56,v40,v41
v_cvt_pk_bf16_f32 v57,v42,v43
v_cvt_pk_bf16_f32 v58,v44,v45
v_cvt_pk_bf16_f32 v59,v46,v47
buffer_store_dwordx4 v[56:59],v2,s[32:35],s68 offen sc1
v_add_f32 v60,v40,v41
v_mul_f32 v61,v40,v40
v_fmac_f32 v61,v41,v41
v_add_f32 v60,v60,v42
v_fmac_f32 v61,v42,v42
v_add_f32 v60,v60,v43
v_fmac_f32 v61,v43,v43
v_add_f32 v60,v60,v44
v_fmac_f32 v61,v44,v44
v_add_f32 v60,v60,v45
v_fmac_f32 v61,v45,v45
v_add_f32 v60,v60,v46
v_fmac_f32 v61,v46,v46
v_add_f32 v60,v60,v47
v_fmac_f32 v61,v47,v47
s_nop 1
v_permlane16_swap_b32 v60,v61
v_add_f32 v60,v60,v61
s_nop 1
v_permlane32_swap_b32 v60,v61
v_add_f32 v60,v60,v61
buffer_store_dword v60,v15,s[32:35],s68 offen offset:1024 sc1
s_waitcnt vmcnt(9)
s_barrier
buffer_store_dwordx2 v[16:17],v2,s[92:95],s68 idxen sc1
s_add_u32 s68,s68,s30
s_waitcnt vmcnt(1)
s_barrier
buffer_store_dwordx2 v[16:17],v2,s[92:95],s68 idxen sc1
s_cbranch_execz .L_wbk
s_branch .L_end
.L_h:
v_mov_b32 v16,0x3f803f80
v_mov_b32 v17,0x3f803f80
v_mov_b32 v18,0x3f803f80
v_mov_b32 v19,0x3f803f80
s_add_u32 s36,s14,0x0
s_addc_u32 s37,s15,0x0
s_and_b32 s37,s37,0xffff
s_mov_b32 s38,0x700000
s_mov_b32 s39,0x20000
s_add_u32 s40,s16,0xfffffc00
s_addc_u32 s41,s17,0xffffffff
s_and_b32 s41,s41,0xffff
s_mov_b32 s42,0x700400
s_mov_b32 s43,0x20000
s_lshl_b32 s96,s27,7
)#"
R"#(
s_add_u32 s96,s96,6912
v_and_b32 v8,1,v1
v_bfe_u32 v3,v1,1,1
v_lshl_add_u32 v8,v8,1,v3
v_mul_u32_u24 v8,8,v8
s_lshl_b32 s48,s26,5
v_add_u32 v8,s48,v8
v_bfe_u32 v3,v1,4,1
v_lshl_add_u32 v8,v3,10,v8
v_lshrrev_b32 v4,3,v1
v_bfe_u32 v5,v1,4,1
s_and_b32 s48,s26,1
s_lshl_b32 s48,s48,1
v_or_b32 v5,s48,v5
v_lshlrev_b32 v5,1,v5
v_and_b32 v6,7,v1
v_xor_b32 v5,v5,v6
v_mul_u32_u24 v4,7168,v4
v_lshl_add_u32 v7,v5,4,v4
s_mul_i32 s48,s26,57344
s_add_u32 s96,s96,s48
s_mul_i32 s97,s26,2048
s_add_u32 m0,s97,0
s_nop 0
buffer_load_dwordx4 v7,s[36:39],s96 offen offset:0 lds
buffer_load_dwordx4 v7,s[40:43],s96 offen offset:1024 lds
s_add_u32 s96,s96,229376
s_add_u32 m0,s97,8192
s_nop 0
buffer_load_dwordx4 v7,s[36:39],s96 offen offset:0 lds
buffer_load_dwordx4 v7,s[40:43],s96 offen offset:1024 lds
s_add_u32 s96,s96,229376
s_add_u32 m0,s97,16384
s_nop 0
buffer_load_dwordx4 v7,s[36:39],s96 offen offset:0 lds
buffer_load_dwordx4 v7,s[40:43],s96 offen offset:1024 lds
s_add_u32 s96,s96,229376
s_add_u32 m0,s97,24576
s_nop 0
buffer_load_dwordx4 v7,s[36:39],s96 offen offset:0 lds
buffer_load_dwordx4 v7,s[40:43],s96 offen offset:1024 lds
s_add_u32 s96,s96,229376
s_add_u32 m0,s97,32768
s_nop 0
buffer_load_dwordx4 v7,s[36:39],s96 offen offset:0 lds
buffer_load_dwordx4 v7,s[40:43],s96 offen offset:1024 lds
s_add_u32 s96,s96,229376
s_add_u32 m0,s97,40960
s_nop 0
buffer_load_dwordx4 v7,s[36:39],s96 offen offset:0 lds
buffer_load_dwordx4 v7,s[40:43],s96 offen offset:1024 lds
s_add_u32 s96,s96,229376
s_add_u32 m0,s97,49152
s_nop 0
buffer_load_dwordx4 v7,s[36:39],s96 offen offset:0 lds
buffer_load_dwordx4 v7,s[40:43],s96 offen offset:1024 lds
s_add_u32 s96,s96,229376
s_add_u32 m0,s97,57344
s_nop 0
buffer_load_dwordx4 v7,s[36:39],s96 offen offset:0 lds
buffer_load_dwordx4 v7,s[40:43],s96 offen offset:1024 lds
s_add_u32 s96,s96,229376
s_add_u32 m0,s97,65536
s_nop 0
buffer_load_dwordx4 v7,s[36:39],s96 offen offset:0 lds
buffer_load_dwordx4 v7,s[40:43],s96 offen offset:1024 lds
s_add_u32 s96,s96,229376
s_add_u32 m0,s97,73728
s_nop 0
buffer_load_dwordx4 v7,s[36:39],s96 offen offset:0 lds
buffer_load_dwordx4 v7,s[40:43],s96 offen offset:1024 lds
s_add_u32 s96,s96,229376
s_add_u32 m0,s97,81920
s_nop 0
buffer_load_dwordx4 v7,s[36:39],s96 offen offset:0 lds
buffer_load_dwordx4 v7,s[40:43],s96 offen offset:1024 lds
s_add_u32 s96,s96,229376
s_add_u32 m0,s97,90112
s_nop 0
buffer_load_dwordx4 v7,s[36:39],s96 offen offset:0 lds
buffer_load_dwordx4 v7,s[40:43],s96 offen offset:1024 lds
s_add_u32 s96,s96,229376
s_add_u32 m0,s97,98304
s_nop 0
buffer_load_dwordx4 v7,s[36:39],s96 offen offset:0 lds
buffer_load_dwordx4 v7,s[40:43],s96 offen offset:1024 lds
s_add_u32 s96,s96,229376
s_add_u32 m0,s97,106496
s_nop 0
buffer_load_dwordx4 v7,s[36:39],s96 offen offset:0 lds
buffer_load_dwordx4 v7,s[40:43],s96 offen offset:1024 lds
s_add_u32 s96,s96,229376
v_lshrrev_b32 v5,5,v1
v_bfe_u32 v4,v1,3,1
v_and_b32 v6,1,v5
v_lshl_add_u32 v4,v6,1,v4
v_lshlrev_b32 v4,5,v4
v_mul_u32_u24 v5,2048,v5
v_bfe_u32 v6,v1,2,2
v_lshl_add_u32 v5,v6,7,v5
v_xor_b32 v8,v4,v8
v_add_u32 v8,v5,v8
v_add_u32 v9,65536,v8
s_waitcnt vmcnt(24)
s_barrier
s_add_u32 m0,s97,114688
s_nop 0
buffer_load_dwordx4 v7,s[36:39],s96 offen offset:0 lds
buffer_load_dwordx4 v7,s[40:43],s96 offen offset:1024 lds
s_add_u32 s96,s96,229376
s_add_u32 m0,s97,122880
s_nop 0
buffer_load_dwordx4 v7,s[36:39],s96 offen offset:0 lds
buffer_load_dwordx4 v7,s[40:43],s96 offen offset:1024 lds
s_add_u32 s96,s96,229376
ds_read_b64_tr_b16 a[0:1],v8 offset:0
ds_read_b64_tr_b16 a[2:3],v8 offset:512
ds_read_b64_tr_b16 a[4:5],v8 offset:4096
ds_read_b64_tr_b16 a[6:7],v8 offset:4608
ds_read_b64_tr_b16 a[32:33],v8 offset:8192
ds_read_b64_tr_b16 a[34:35],v8 offset:8704
ds_read_b64_tr_b16 a[36:37],v8 offset:12288
ds_read_b64_tr_b16 a[38:39],v8 offset:12800
s_waitcnt lgkmcnt(0)
v_mfma_f32_32x32x16_bf16 v[112:127],a[0:3],v[16:19],0
v_mfma_f32_32x32x16_bf16 v[112:127],a[4:7],v[16:19],v[112:127]
v_mfma_f32_32x32x16_bf16 v[112:127],a[32:35],v[16:19],v[112:127]
v_mfma_f32_32x32x16_bf16 v[112:127],a[36:39],v[16:19],v[112:127]
s_waitcnt vmcnt(24)
s_barrier
s_add_u32 m0,s97,0
s_nop 0
buffer_load_dwordx4 v7,s[36:39],s96 offen offset:0 lds
buffer_load_dwordx4 v7,s[40:43],s96 offen offset:1024 lds
s_add_u32 s96,s96,229376
s_add_u32 m0,s97,8192
s_nop 0
buffer_load_dwordx4 v7,s[36:39],s96 offen offset:0 lds
buffer_load_dwordx4 v7,s[40:43],s96 offen offset:1024 lds
s_add_u32 s96,s96,229376
ds_read_b64_tr_b16 a[64:65],v8 offset:16384
ds_read_b64_tr_b16 a[66:67],v8 offset:16896
ds_read_b64_tr_b16 a[68:69],v8 offset:20480
ds_read_b64_tr_b16 a[70:71],v8 offset:20992
ds_read_b64_tr_b16 a[96:97],v8 offset:24576
ds_read_b64_tr_b16 a[98:99],v8 offset:25088
ds_read_b64_tr_b16 a[100:101],v8 offset:28672
ds_read_b64_tr_b16 a[102:103],v8 offset:29184
s_waitcnt lgkmcnt(0)
v_mfma_f32_32x32x16_bf16 v[112:127],a[64:67],v[16:19],v[112:127]
v_mfma_f32_32x32x16_bf16 v[112:127],a[68:71],v[16:19],v[112:127]
v_mfma_f32_32x32x16_bf16 v[112:127],a[96:99],v[16:19],v[112:127]
v_mfma_f32_32x32x16_bf16 v[112:127],a[100:103],v[16:19],v[112:127]
s_waitcnt vmcnt(24)
s_barrier
s_add_u32 m0,s97,16384
s_nop 0
buffer_load_dwordx4 v7,s[36:39],s96 offen offset:0 lds
buffer_load_dwordx4 v7,s[40:43],s96 offen offset:1024 lds
s_add_u32 s96,s96,229376
s_add_u32 m0,s97,24576
s_nop 0
buffer_load_dwordx4 v7,s[36:39],s96 offen offset:0 lds
buffer_load_dwordx4 v7,s[40:43],s96 offen offset:1024 lds
s_add_u32 s96,s96,229376
ds_read_b64_tr_b16 a[128:129],v8 offset:32768
ds_read_b64_tr_b16 a[130:131],v8 offset:33280
ds_read_b64_tr_b16 a[132:133],v8 offset:36864
ds_read_b64_tr_b16 a[134:135],v8 offset:37376
ds_read_b64_tr_b16 a[160:161],v8 offset:40960
ds_read_b64_tr_b16 a[162:163],v8 offset:41472
ds_read_b64_tr_b16 a[164:165],v8 offset:45056
ds_read_b64_tr_b16 a[166:167],v8 offset:45568
s_waitcnt lgkmcnt(0)
v_mfma_f32_32x32x16_bf16 v[112:127],a[128:131],v[16:19],v[112:127]
v_mfma_f32_32x32x16_bf16 v[112:127],a[132:135],v[16:19],v[112:127]
v_mfma_f32_32x32x16_bf16 v[112:127],a[160:163],v[16:19],v[112:127]
v_mfma_f32_32x32x16_bf16 v[112:127],a[164:167],v[16:19],v[112:127]
s_waitcnt vmcnt(24)
s_barrier
s_add_u32 m0,s97,32768
s_nop 0
buffer_load_dwordx4 v7,s[36:39],s96 offen offset:0 lds
buffer_load_dwordx4 v7,s[40:43],s96 offen offset:1024 lds
s_add_u32 s96,s96,229376
s_add_u32 m0,s97,40960
s_nop 0
buffer_load_dwordx4 v7,s[36:39],s96 offen offset:0 lds
buffer_load_dwordx4 v7,s[40:43],s96 offen offset:1024 lds
s_add_u32 s96,s96,229376
ds_read_b64_tr_b16 a[192:193],v8 offset:49152
ds_read_b64_tr_b16 a[194:195],v8 offset:49664
ds_read_b64_tr_b16 a[196:197],v8 offset:53248
ds_read_b64_tr_b16 a[198:199],v8 offset:53760
ds_read_b64_tr_b16 a[224:225],v8 offset:57344
ds_read_b64_tr_b16 a[226:227],v8 offset:57856
ds_read_b64_tr_b16 a[228:229],v8 offset:61440
ds_read_b64_tr_b16 a[230:231],v8 offset:61952
s_waitcnt lgkmcnt(0)
v_mfma_f32_32x32x16_bf16 v[112:127],a[192:195],v[16:19],v[112:127]
v_mfma_f32_32x32x16_bf16 v[112:127],a[196:199],v[16:19],v[112:127]
v_mfma_f32_32x32x16_bf16 v[112:127],a[224:227],v[16:19],v[112:127]
v_mfma_f32_32x32x16_bf16 v[112:127],a[228:231],v[16:19],v[112:127]
s_waitcnt vmcnt(24)
s_barrier
s_add_u32 m0,s97,49152
s_nop 0
buffer_load_dwordx4 v7,s[36:39],s96 offen offset:0 lds
buffer_load_dwordx4 v7,s[40:43],s96 offen offset:1024 lds
s_add_u32 s96,s96,229376
s_add_u32 m0,s97,57344
s_nop 0
buffer_load_dwordx4 v7,s[36:39],s96 offen offset:0 lds
buffer_load_dwordx4 v7,s[40:43],s96 offen offset:1024 lds
s_add_u32 s96,s96,229376
ds_read_b64_tr_b16 a[8:9],v9 offset:0
ds_read_b64_tr_b16 a[10:11],v9 offset:512
ds_read_b64_tr_b16 a[12:13],v9 offset:4096
ds_read_b64_tr_b16 a[14:15],v9 offset:4608
ds_read_b64_tr_b16 a[40:41],v9 offset:8192
ds_read_b64_tr_b16 a[42:43],v9 offset:8704
ds_read_b64_tr_b16 a[44:45],v9 offset:12288
ds_read_b64_tr_b16 a[46:47],v9 offset:12800
s_waitcnt lgkmcnt(0)
v_mfma_f32_32x32x16_bf16 v[112:127],a[8:11],v[16:19],v[112:127]
v_mfma_f32_32x32x16_bf16 v[112:127],a[12:15],v[16:19],v[112:127]
v_mfma_f32_32x32x16_bf16 v[112:127],a[40:43],v[16:19],v[112:127]
v_mfma_f32_32x32x16_bf16 v[112:127],a[44:47],v[16:19],v[112:127]
s_waitcnt vmcnt(24)
s_barrier
s_add_u32 m0,s97,65536
s_nop 0
buffer_load_dwordx4 v7,s[36:39],s96 offen offset:0 lds
buffer_load_dwordx4 v7,s[40:43],s96 offen offset:1024 lds
s_add_u32 s96,s96,229376
s_add_u32 m0,s97,73728
s_nop 0
buffer_load_dwordx4 v7,s[36:39],s96 offen offset:0 lds
buffer_load_dwordx4 v7,s[40:43],s96 offen offset:1024 lds
s_add_u32 s96,s96,229376
ds_read_b64_tr_b16 a[72:73],v9 offset:16384
ds_read_b64_tr_b16 a[74:75],v9 offset:16896
ds_read_b64_tr_b16 a[76:77],v9 offset:20480
ds_read_b64_tr_b16 a[78:79],v9 offset:20992
ds_read_b64_tr_b16 a[104:105],v9 offset:24576
ds_read_b64_tr_b16 a[106:107],v9 offset:25088
ds_read_b64_tr_b16 a[108:109],v9 offset:28672
ds_read_b64_tr_b16 a[110:111],v9 offset:29184
s_waitcnt lgkmcnt(0)
v_mfma_f32_32x32x16_bf16 v[112:127],a[72:75],v[16:19],v[112:127]
v_mfma_f32_32x32x16_bf16 v[112:127],a[76:79],v[16:19],v[112:127]
v_mfma_f32_32x32x16_bf16 v[112:127],a[104:107],v[16:19],v[112:127]
v_mfma_f32_32x32x16_bf16 v[112:127],a[108:111],v[16:19],v[112:127]
s_waitcnt vmcnt(24)
s_barrier
s_add_u32 m0,s97,81920
s_nop 0
buffer_load_dwordx4 v7,s[36:39],s96 offen offset:0 lds
buffer_load_dwordx4 v7,s[40:43],s96 offen offset:1024 lds
s_add_u32 s96,s96,229376
s_add_u32 m0,s97,90112
s_nop 0
buffer_load_dwordx4 v7,s[36:39],s96 offen offset:0 lds
buffer_load_dwordx4 v7,s[40:43],s96 offen offset:1024 lds
s_add_u32 s96,s96,229376
ds_read_b64_tr_b16 a[136:137],v9 offset:32768
ds_read_b64_tr_b16 a[138:139],v9 offset:33280
ds_read_b64_tr_b16 a[140:141],v9 offset:36864
ds_read_b64_tr_b16 a[142:143],v9 offset:37376
ds_read_b64_tr_b16 a[168:169],v9 offset:40960
ds_read_b64_tr_b16 a[170:171],v9 offset:41472
ds_read_b64_tr_b16 a[172:173],v9 offset:45056
ds_read_b64_tr_b16 a[174:175],v9 offset:45568
s_waitcnt lgkmcnt(0)
v_mfma_f32_32x32x16_bf16 v[112:127],a[136:139],v[16:19],v[112:127]
v_mfma_f32_32x32x16_bf16 v[112:127],a[140:143],v[16:19],v[112:127]
v_mfma_f32_32x32x16_bf16 v[112:127],a[168:171],v[16:19],v[112:127]
v_mfma_f32_32x32x16_bf16 v[112:127],a[172:175],v[16:19],v[112:127]
s_waitcnt vmcnt(24)
s_barrier
s_add_u32 m0,s97,98304
s_nop 0
buffer_load_dwordx4 v7,s[36:39],s96 offen offset:0 lds
buffer_load_dwordx4 v7,s[40:43],s96 offen offset:1024 lds
s_add_u32 s96,s96,229376
s_add_u32 m0,s97,106496
s_nop 0
buffer_load_dwordx4 v7,s[36:39],s96 offen offset:0 lds
buffer_load_dwordx4 v7,s[40:43],s96 offen offset:1024 lds
s_add_u32 s96,s96,229376
ds_read_b64_tr_b16 a[200:201],v9 offset:49152
ds_read_b64_tr_b16 a[202:203],v9 offset:49664
ds_read_b64_tr_b16 a[204:205],v9 offset:53248
ds_read_b64_tr_b16 a[206:207],v9 offset:53760
ds_read_b64_tr_b16 a[232:233],v9 offset:57344
ds_read_b64_tr_b16 a[234:235],v9 offset:57856
ds_read_b64_tr_b16 a[236:237],v9 offset:61440
ds_read_b64_tr_b16 a[238:239],v9 offset:61952
s_waitcnt lgkmcnt(0)
v_mfma_f32_32x32x16_bf16 v[112:127],a[200:203],v[16:19],v[112:127]
v_mfma_f32_32x32x16_bf16 v[112:127],a[204:207],v[16:19],v[112:127]
v_mfma_f32_32x32x16_bf16 v[112:127],a[232:235],v[16:19],v[112:127]
v_mfma_f32_32x32x16_bf16 v[112:127],a[236:239],v[16:19],v[112:127]
s_waitcnt vmcnt(24)
s_barrier
s_add_u32 m0,s97,114688
s_nop 0
buffer_load_dwordx4 v7,s[36:39],s96 offen offset:0 lds
buffer_load_dwordx4 v7,s[40:43],s96 offen offset:1024 lds
s_add_u32 s96,s96,229376
s_add_u32 m0,s97,122880
s_nop 0
buffer_load_dwordx4 v7,s[36:39],s96 offen offset:0 lds
buffer_load_dwordx4 v7,s[40:43],s96 offen offset:1024 lds
s_add_u32 s96,s96,229376
ds_read_b64_tr_b16 a[16:17],v8 offset:0
ds_read_b64_tr_b16 a[18:19],v8 offset:512
ds_read_b64_tr_b16 a[20:21],v8 offset:4096
ds_read_b64_tr_b16 a[22:23],v8 offset:4608
ds_read_b64_tr_b16 a[48:49],v8 offset:8192
ds_read_b64_tr_b16 a[50:51],v8 offset:8704
ds_read_b64_tr_b16 a[52:53],v8 offset:12288
ds_read_b64_tr_b16 a[54:55],v8 offset:12800
s_waitcnt lgkmcnt(0)
v_mfma_f32_32x32x16_bf16 v[112:127],a[16:19],v[16:19],v[112:127]
v_mfma_f32_32x32x16_bf16 v[112:127],a[20:23],v[16:19],v[112:127]
v_mfma_f32_32x32x16_bf16 v[112:127],a[48:51],v[16:19],v[112:127]
v_mfma_f32_32x32x16_bf16 v[112:127],a[52:55],v[16:19],v[112:127]
s_waitcnt vmcnt(24)
s_barrier
ds_read_b64_tr_b16 a[80:81],v8 offset:16384
ds_read_b64_tr_b16 a[82:83],v8 offset:16896
ds_read_b64_tr_b16 a[84:85],v8 offset:20480
ds_read_b64_tr_b16 a[86:87],v8 offset:20992
ds_read_b64_tr_b16 a[112:113],v8 offset:24576
ds_read_b64_tr_b16 a[114:115],v8 offset:25088
ds_read_b64_tr_b16 a[116:117],v8 offset:28672
ds_read_b64_tr_b16 a[118:119],v8 offset:29184
s_waitcnt lgkmcnt(0)
v_mfma_f32_32x32x16_bf16 v[112:127],a[80:83],v[16:19],v[112:127]
v_mfma_f32_32x32x16_bf16 v[112:127],a[84:87],v[16:19],v[112:127]
v_mfma_f32_32x32x16_bf16 v[112:127],a[112:115],v[16:19],v[112:127]
v_mfma_f32_32x32x16_bf16 v[112:127],a[116:119],v[16:19],v[112:127]
s_waitcnt vmcnt(20)
s_barrier
ds_read_b64_tr_b16 a[144:145],v8 offset:32768
ds_read_b64_tr_b16 a[146:147],v8 offset:33280
ds_read_b64_tr_b16 a[148:149],v8 offset:36864
ds_read_b64_tr_b16 a[150:151],v8 offset:37376
ds_read_b64_tr_b16 a[176:177],v8 offset:40960
ds_read_b64_tr_b16 a[178:179],v8 offset:41472
ds_read_b64_tr_b16 a[180:181],v8 offset:45056
ds_read_b64_tr_b16 a[182:183],v8 offset:45568
s_waitcnt lgkmcnt(0)
v_mfma_f32_32x32x16_bf16 v[112:127],a[144:147],v[16:19],v[112:127]
v_mfma_f32_32x32x16_bf16 v[112:127],a[148:151],v[16:19],v[112:127]
v_mfma_f32_32x32x16_bf16 v[112:127],a[176:179],v[16:19],v[112:127]
v_mfma_f32_32x32x16_bf16 v[112:127],a[180:183],v[16:19],v[112:127]
s_waitcnt vmcnt(16)
s_barrier
ds_read_b64_tr_b16 a[208:209],v8 offset:49152
ds_read_b64_tr_b16 a[210:211],v8 offset:49664
ds_read_b64_tr_b16 a[212:213],v8 offset:53248
ds_read_b64_tr_b16 a[214:215],v8 offset:53760
ds_read_b64_tr_b16 a[240:241],v8 offset:57344
ds_read_b64_tr_b16 a[242:243],v8 offset:57856
ds_read_b64_tr_b16 a[244:245],v8 offset:61440
ds_read_b64_tr_b16 a[246:247],v8 offset:61952
s_waitcnt lgkmcnt(0)
v_mfma_f32_32x32x16_bf16 v[112:127],a[208:211],v[16:19],v[112:127]
v_mfma_f32_32x32x16_bf16 v[112:127],a[212:215],v[16:19],v[112:127]
v_mfma_f32_32x32x16_bf16 v[112:127],a[240:243],v[16:19],v[112:127]
v_mfma_f32_32x32x16_bf16 v[112:127],a[244:247],v[16:19],v[112:127]
s_waitcnt vmcnt(12)
s_barrier
ds_read_b64_tr_b16 a[24:25],v9 offset:0
ds_read_b64_tr_b16 a[26:27],v9 offset:512
ds_read_b64_tr_b16 a[28:29],v9 offset:4096
ds_read_b64_tr_b16 a[30:31],v9 offset:4608
ds_read_b64_tr_b16 a[56:57],v9 offset:8192
ds_read_b64_tr_b16 a[58:59],v9 offset:8704
ds_read_b64_tr_b16 a[60:61],v9 offset:12288
ds_read_b64_tr_b16 a[62:63],v9 offset:12800
s_waitcnt lgkmcnt(0)
v_mfma_f32_32x32x16_bf16 v[112:127],a[24:27],v[16:19],v[112:127]
v_mfma_f32_32x32x16_bf16 v[112:127],a[28:31],v[16:19],v[112:127]
v_mfma_f32_32x32x16_bf16 v[112:127],a[56:59],v[16:19],v[112:127]
v_mfma_f32_32x32x16_bf16 v[112:127],a[60:63],v[16:19],v[112:127]
s_waitcnt vmcnt(8)
s_barrier
ds_read_b64_tr_b16 a[88:89],v9 offset:16384
ds_read_b64_tr_b16 a[90:91],v9 offset:16896
ds_read_b64_tr_b16 a[92:93],v9 offset:20480
ds_read_b64_tr_b16 a[94:95],v9 offset:20992
ds_read_b64_tr_b16 a[120:121],v9 offset:24576
ds_read_b64_tr_b16 a[122:123],v9 offset:25088
ds_read_b64_tr_b16 a[124:125],v9 offset:28672
ds_read_b64_tr_b16 a[126:127],v9 offset:29184
s_waitcnt lgkmcnt(0)
v_mfma_f32_32x32x16_bf16 v[112:127],a[88:91],v[16:19],v[112:127]
v_mfma_f32_32x32x16_bf16 v[112:127],a[92:95],v[16:19],v[112:127]
v_mfma_f32_32x32x16_bf16 v[112:127],a[120:123],v[16:19],v[112:127]
v_mfma_f32_32x32x16_bf16 v[112:127],a[124:127],v[16:19],v[112:127]
s_waitcnt vmcnt(4)
s_barrier
ds_read_b64_tr_b16 a[152:153],v9 offset:32768
ds_read_b64_tr_b16 a[154:155],v9 offset:33280
ds_read_b64_tr_b16 a[156:157],v9 offset:36864
ds_read_b64_tr_b16 a[158:159],v9 offset:37376
ds_read_b64_tr_b16 a[184:185],v9 offset:40960
ds_read_b64_tr_b16 a[186:187],v9 offset:41472
ds_read_b64_tr_b16 a[188:189],v9 offset:45056
ds_read_b64_tr_b16 a[190:191],v9 offset:45568
s_waitcnt lgkmcnt(0)
v_mfma_f32_32x32x16_bf16 v[112:127],a[152:155],v[16:19],v[112:127]
v_mfma_f32_32x32x16_bf16 v[112:127],a[156:159],v[16:19],v[112:127]
v_mfma_f32_32x32x16_bf16 v[112:127],a[184:187],v[16:19],v[112:127]
v_mfma_f32_32x32x16_bf16 v[112:127],a[188:191],v[16:19],v[112:127]
s_waitcnt vmcnt(0)
s_barrier
ds_read_b64_tr_b16 a[216:217],v9 offset:49152
ds_read_b64_tr_b16 a[218:219],v9 offset:49664
ds_read_b64_tr_b16 a[220:221],v9 offset:53248
ds_read_b64_tr_b16 a[222:223],v9 offset:53760
ds_read_b64_tr_b16 a[248:249],v9 offset:57344
ds_read_b64_tr_b16 a[250:251],v9 offset:57856
ds_read_b64_tr_b16 a[252:253],v9 offset:61440
ds_read_b64_tr_b16 a[254:255],v9 offset:61952
s_waitcnt lgkmcnt(0)
v_mfma_f32_32x32x16_bf16 v[112:127],a[216:219],v[16:19],v[112:127]
v_mfma_f32_32x32x16_bf16 v[112:127],a[220:223],v[16:19],v[112:127]
v_mfma_f32_32x32x16_bf16 v[112:127],a[248:251],v[16:19],v[112:127]
v_mfma_f32_32x32x16_bf16 v[112:127],a[252:255],v[16:19],v[112:127]
s_barrier
s_lshl2_add_u32 s49,s27,272
s_lshl2_add_u32 s68,s27,s26
s_lshl_b32 s68,s68,9
s_add_u32 s68,s68,4829184
s_mov_b32 s63,4681728
s_mov_b32 s30,1187840
s_mul_i32 s57,s28,593920
s_mov_b32 s56,76021760
s_cmp_lg_u32 s25,0
s_cmov_b32 s30,593920
s_cmov_b32 s57,81960960
s_cmov_b32 s56,81960960
s_lshl_b32 s31,s30,1
s_waitcnt lgkmcnt(0)
s_add_u32 s54,s52,0x7f4a7c15
s_addc_u32 s55,s53,0x9e3779b9
s_lshl_b32 s67,s49,5
s_add_u32 s67,s67,4898816
s_sub_u32 s51,s67,s68
s_add_u32 s92,s32,s51
s_addc_u32 s93,s33,0x10000
s_cmp_eq_u32 s26,0
s_cselect_b32 s94,1,0
s_mov_b32 s95,0x20000
v_mov_b32 v16,s54
v_mov_b32 v17,s55
v_cmp_gt_u32 vcc,12,v1
v_mul_u32_u24 v3,296960,v1
v_mov_b32 v12,0x7fff0000
v_cndmask_b32 v3,v12,v3,vcc
s_add_u32 s51,s67,s56
buffer_store_dwordx2 v[16:17],v3,s[0:3],s51 offen sc1
s_sub_u32 s67,s68,296960
s_sub_u32 s51,s51,s56
s_sub_u32 s51,s51,s68
v_mul_u32_u24 v3,296960,v1
v_add_u32 v3,s51,v3
v_subrev_u32 v18,0,v0
v_cmp_gt_u32 vcc,2,v18
v_mov_b32 v18,0x7fff0000
s_nop 0
v_cndmask_b32 v18,v18,v3,vcc
s_add_u32 s68,s68,s57
s_sub_u32 s68,s68,s31
s_add_u32 s63,s63,s57
s_add_u32 s66,s57,4898816
s_mul_i32 s64,s26,9216
s_add_u32 s63,s63,s64
s_mov_b32 s62,31
s_cmp_lg_u32 s28,0
s_cmov_b32 s62,31
v_and_b32 v4,0xf0,v2
v_lshrrev_b32 v3,5,v1
v_lshl_add_u32 v4,v3,8,v4
v_bfe_u32 v3,v1,4,1
v_mul_u32_u24 v15,296960,v3
v_add_u32 v15,v15,v4
v_add_u32 v15,0,v15
v_mul_u32_u24 v3,36864,v3
v_add_u32 v4,v3,v4
v_add_u32 v5,73728,v4
v_add_u32 v20,0,v2
v_add_u32 v21,296960,v2
v_add_u32 v22,4096,v2
v_add_u32 v23,301056,v2
v_add_u32 v24,5120,v2
)#"
R"#(
v_add_u32 v25,302080,v2
v_and_b32 v14,7,v1
s_mul_i32 s48,s26,8
s_add_u32 s48,s48,96
v_add_u32 v14,s48,v14
v_lshrrev_b32 v14,2,v14
v_lshlrev_b32 v14,7,v14
v_lshrrev_b32 v3,4,v1
v_mul_u32_u24 v3,296960,v3
v_add_u32 v14,v3,v14
v_cmp_gt_u32 vcc,32,v1
v_mov_b32 v3,0x7fff0000
s_nop 0
v_cndmask_b32 v14,v3,v14,vcc
v_cndmask_b32 v16,0,v16,vcc
v_cndmask_b32 v17,0,v17,vcc
v_bfe_u32 v3,v1,1,3
v_mul_u32_u24 v8,1152,v3
v_lshrrev_b32 v3,1,v3
v_add_u32 v3,s26,v3
v_lshrrev_b32 v9,4,v1
v_lshl_add_u32 v3,v3,2,v9
v_and_b32 v3,15,v3
v_lshl_add_u32 v8,v3,2,v8
v_and_b32 v3,1,v1
v_lshl_add_u32 v8,v3,6,v8
v_add_u32 v8,1024,v8
s_lshl_b32 s48,s26,2
v_add_u32 v10,s48,v9
v_and_b32 v11,15,v1
v_add_u32 v3,64,v1
v_cmp_eq_u32 vcc,0,v11
v_add_u32 v9,73728,v8
v_xor_b32 v9,v9,v8
v_cndmask_b32 v10,v3,v10,vcc
v_lshlrev_b32 v10,2,v10
v_add_u32 v10,147456,v10
v_lshlrev_b32 v11,2,v11
v_add_u32 v11,147456,v11
v_and_b32 v3,15,v1
v_cmp_eq_u32 vcc,8,v3
v_lshrrev_b32 v3,4,v1
s_lshl2_add_u32 s48,s26,36880
v_add_lshl_u32 v3,v3,s48,2
v_cndmask_b32 v10,v10,v3,vcc
v_and_b32 v3,16,v1
v_lshl_add_u32 v11,v3,2,v11
s_mov_b64 s[8:9],s[62:63]
s_mov_b64 s[10:11],s[64:65]
s_mov_b64 s[12:13],s[66:67]
s_mov_b64 s[14:15],s[68:69]
s_mov_b64 s[16:17],s[70:71]
s_mov_b64 s[18:19],s[72:73]
s_mov_b64 s[20:21],s[74:75]
s_mov_b32 s62,0
s_mov_b64 vcc,0
s_mov_b64 exec,0
s_branch .L_hgo
.L_hbk:
s_mov_b64 exec,-1
s_mov_b64 s[62:63],s[8:9]
s_mov_b64 s[64:65],s[10:11]
s_mov_b64 s[66:67],s[12:13]
s_mov_b64 s[68:69],s[14:15]
s_mov_b64 s[70:71],s[16:17]
s_mov_b64 s[72:73],s[18:19]
s_mov_b64 s[74:75],s[20:21]
s_waitcnt vmcnt(0) lgkmcnt(0)
.L_hnd:
s_mov_b32 s48,20000
s_branch .L_rfha
.L_rlha:
s_sleep 2
.L_rfha:
buffer_load_dwordx2 v[12:13],v14,s[0:3],s66 offen sc1
s_sub_u32 s48,s48,1
s_cbranch_scc1 .L_end
s_waitcnt vmcnt(0)
v_cmp_ne_u64 vcc,v[16:17],v[12:13]
s_cbranch_vccnz .L_rlha
s_add_u32 s66,s66,s30
s_add_u32 m0,s64,0
s_nop 0
buffer_load_dwordx4 v20,s[32:35],s63 offen offset:0 sc1 lds
buffer_load_dwordx4 v20,s[32:35],s63 offen offset:1024 sc1 lds
buffer_load_dwordx4 v20,s[32:35],s63 offen offset:2048 sc1 lds
buffer_load_dwordx4 v20,s[32:35],s63 offen offset:3072 sc1 lds
s_add_u32 m0,s64,36864
s_nop 0
buffer_load_dwordx4 v21,s[32:35],s63 offen offset:0 sc1 lds
buffer_load_dwordx4 v21,s[32:35],s63 offen offset:1024 sc1 lds
buffer_load_dwordx4 v21,s[32:35],s63 offen offset:2048 sc1 lds
buffer_load_dwordx4 v21,s[32:35],s63 offen offset:3072 sc1 lds
s_add_u32 m0,s64,4096
s_nop 0
buffer_load_dwordx4 v22,s[32:35],s63 offen offset:0 sc1 lds
s_add_u32 m0,s64,40960
s_nop 0
buffer_load_dwordx4 v23,s[32:35],s63 offen offset:0 sc1 lds
s_add_u32 m0,s64,5120
s_nop 0
buffer_load_dwordx4 v24,s[32:35],s63 offen offset:0 sc1 lds
buffer_load_dwordx4 v24,s[32:35],s63 offen offset:1024 sc1 lds
buffer_load_dwordx4 v24,s[32:35],s63 offen offset:2048 sc1 lds
buffer_load_dwordx4 v24,s[32:35],s63 offen offset:3072 sc1 lds
s_add_u32 m0,s64,41984
s_nop 0
buffer_load_dwordx4 v25,s[32:35],s63 offen offset:0 sc1 lds
buffer_load_dwordx4 v25,s[32:35],s63 offen offset:1024 sc1 lds
buffer_load_dwordx4 v25,s[32:35],s63 offen offset:2048 sc1 lds
buffer_load_dwordx4 v25,s[32:35],s63 offen offset:3072 sc1 lds
s_add_u32 s63,s63,s30
s_waitcnt vmcnt(8)
s_barrier
ds_read_b128 v[128:131],v4 offset:0
ds_read_b128 v[132:135],v4 offset:512
ds_read_b128 v[136:139],v4 offset:9216
ds_read_b128 v[140:143],v4 offset:9728
ds_read_b128 v[144:147],v4 offset:18432
ds_read_b128 v[148:151],v4 offset:18944
ds_read_b128 v[152:155],v4 offset:27648
ds_read_b128 v[156:159],v4 offset:28160
s_mov_b32 s48,20000
s_branch .L_rfhb
.L_rlhb:
s_sleep 2
.L_rfhb:
buffer_load_dwordx2 v[12:13],v14,s[0:3],s66 offen sc1
s_sub_u32 s48,s48,1
s_cbranch_scc1 .L_end
s_waitcnt vmcnt(0)
v_cmp_ne_u64 vcc,v[16:17],v[12:13]
s_cbranch_vccnz .L_rlhb
.L_hgo:
s_cmp_eq_u32 s26,1
s_cbranch_scc1 .L_hv1
s_cmp_eq_u32 s26,2
s_cbranch_scc1 .L_hv2
s_cmp_eq_u32 s26,3
s_cbranch_scc1 .L_hv3
.p2align 6
.L_hv0:
.L_hl_0:
s_waitcnt lgkmcnt(4)
v_mfma_f32_32x32x16_bf16 v[32:47],a[0:3],v[128:131],0
ds_read_b128 v[160:163],v4 offset:1152
s_waitcnt vmcnt(6)
v_cmp_ne_u64 vcc,v[16:17],v[12:13]
s_add_u32 s68,s68,s30
v_mfma_f32_32x32x16_bf16 v[32:47],a[4:7],v[132:135],v[32:47]
ds_read_b128 v[164:167],v4 offset:1664
s_cbranch_vccnz .L_rth0_0
.L_bkh0_0:
s_add_u32 s66,s66,s30
v_mfma_f32_32x32x16_bf16 v[32:47],a[8:11],v[136:139],v[32:47]
ds_read_b128 v[168:171],v4 offset:10368
s_add_u32 m0,s64,73728
v_fma_f32 v48,-v72,v112,v48
buffer_load_dwordx4 v20,s[32:35],s63 offen offset:0 sc1 lds
v_mfma_f32_32x32x16_bf16 v[32:47],a[12:15],v[140:143],v[32:47]
ds_read_b128 v[172:175],v4 offset:10880
v_fma_f32 v49,-v72,v113,v49
v_fma_f32 v50,-v72,v114,v50
v_fma_f32 v51,-v72,v115,v51
v_fma_f32 v52,-v72,v116,v52
s_waitcnt lgkmcnt(4)
v_mfma_f32_32x32x16_bf16 v[32:47],a[16:19],v[144:147],v[32:47]
buffer_load_dwordx4 v20,s[32:35],s63 offen offset:1024 sc1 lds
ds_read_b128 v[176:179],v4 offset:19584
v_fma_f32 v53,-v72,v117,v53
v_fma_f32 v54,-v72,v118,v54
v_mfma_f32_32x32x16_bf16 v[32:47],a[20:23],v[148:151],v[32:47]
ds_read_b128 v[180:183],v4 offset:20096
v_fma_f32 v55,-v72,v119,v55
v_fma_f32 v56,-v72,v120,v56
v_fma_f32 v57,-v72,v121,v57
v_fma_f32 v58,-v72,v122,v58
v_fma_f32 v59,-v72,v123,v59
v_mfma_f32_32x32x16_bf16 v[32:47],a[24:27],v[152:155],v[32:47]
buffer_load_dwordx4 v20,s[32:35],s63 offen offset:2048 sc1 lds
ds_read_b128 v[184:187],v4 offset:28800
v_fma_f32 v60,-v72,v124,v60
v_fma_f32 v61,-v72,v125,v61
v_mfma_f32_32x32x16_bf16 v[32:47],a[28:31],v[156:159],v[32:47]
ds_read_b128 v[188:191],v4 offset:29312
v_fma_f32 v62,-v72,v126,v62
v_fma_f32 v63,-v72,v127,v63
v_mul_f32 v76,v48,v73
v_mul_f32 v77,v49,v73
s_waitcnt lgkmcnt(4)
v_mfma_f32_32x32x16_bf16 v[32:47],a[32:35],v[160:163],v[32:47]
buffer_load_dwordx4 v20,s[32:35],s63 offen offset:3072 sc1 lds
ds_read_b128 v[128:131],v4 offset:2304
v_exp_f32 v76,v76
v_mfma_f32_32x32x16_bf16 v[32:47],a[36:39],v[164:167],v[32:47]
ds_read_b128 v[132:135],v4 offset:2816
v_exp_f32 v77,v77
v_mul_f32 v78,v50,v73
v_mul_f32 v79,v51,v73
v_mul_f32 v80,v52,v73
v_mfma_f32_32x32x16_bf16 v[32:47],a[40:43],v[168:171],v[32:47]
ds_read_b128 v[136:139],v4 offset:11520
s_add_u32 m0,s64,110592
v_exp_f32 v78,v78
v_exp_f32 v79,v79
v_mfma_f32_32x32x16_bf16 v[32:47],a[44:47],v[172:175],v[32:47]
ds_read_b128 v[140:143],v4 offset:12032
v_exp_f32 v80,v80
v_mul_f32 v81,v53,v73
v_mul_f32 v82,v54,v73
s_waitcnt lgkmcnt(4)
v_mfma_f32_32x32x16_bf16 v[32:47],a[48:51],v[176:179],v[32:47]
buffer_load_dwordx4 v21,s[32:35],s63 offen offset:0 sc1 lds
ds_read_b128 v[144:147],v4 offset:20736
v_exp_f32 v81,v81
v_mfma_f32_32x32x16_bf16 v[32:47],a[52:55],v[180:183],v[32:47]
ds_read_b128 v[148:151],v4 offset:21248
v_exp_f32 v82,v82
v_mul_f32 v83,v55,v73
v_mul_f32 v48,v48,v56
v_mul_f32 v49,v49,v57
v_mfma_f32_32x32x16_bf16 v[32:47],a[56:59],v[184:187],v[32:47]
buffer_load_dwordx4 v21,s[32:35],s63 offen offset:1024 sc1 lds
ds_read_b128 v[152:155],v4 offset:29952
v_exp_f32 v83,v83
v_mfma_f32_32x32x16_bf16 v[32:47],a[60:63],v[188:191],v[32:47]
ds_read_b128 v[156:159],v4 offset:30464
v_mul_f32 v50,v50,v58
v_mul_f32 v51,v51,v59
v_mul_f32 v52,v52,v60
v_mul_f32 v53,v53,v61
s_waitcnt lgkmcnt(4)
v_mfma_f32_32x32x16_bf16 v[32:47],a[64:67],v[128:131],v[32:47]
buffer_load_dwordx4 v21,s[32:35],s63 offen offset:2048 sc1 lds
ds_read_b128 v[160:163],v4 offset:3456
v_mul_f32 v54,v54,v62
v_mul_f32 v55,v55,v63
v_mfma_f32_32x32x16_bf16 v[32:47],a[68:71],v[132:135],v[32:47]
ds_read_b128 v[164:167],v4 offset:3968
v_fma_f32 v76,v76,v74,v74
v_fma_f32 v77,v77,v74,v74
v_rcp_f32 v76,v76
v_fma_f32 v78,v78,v74,v74
v_mfma_f32_32x32x16_bf16 v[32:47],a[72:75],v[136:139],v[32:47]
buffer_load_dwordx4 v21,s[32:35],s63 offen offset:3072 sc1 lds
ds_read_b128 v[168:171],v4 offset:12672
s_add_u32 m0,s64,77824
v_fma_f32 v79,v79,v74,v74
v_mfma_f32_32x32x16_bf16 v[32:47],a[76:79],v[140:143],v[32:47]
ds_read_b128 v[172:175],v4 offset:13184
v_rcp_f32 v77,v77
v_rcp_f32 v78,v78
s_waitcnt lgkmcnt(4)
v_mfma_f32_32x32x16_bf16 v[32:47],a[80:83],v[144:147],v[32:47]
buffer_load_dwordx4 v22,s[32:35],s63 offen offset:0 sc1 lds
ds_read_b128 v[176:179],v4 offset:21888
s_add_u32 m0,s64,114688
v_fma_f32 v80,v80,v74,v74
v_mfma_f32_32x32x16_bf16 v[32:47],a[84:87],v[148:151],v[32:47]
ds_read_b128 v[180:183],v4 offset:22400
v_rcp_f32 v79,v79
v_rcp_f32 v80,v80
v_fma_f32 v81,v81,v74,v74
v_mfma_f32_32x32x16_bf16 v[32:47],a[88:91],v[152:155],v[32:47]
buffer_load_dwordx4 v23,s[32:35],s63 offen offset:0 sc1 lds
ds_read_b128 v[184:187],v4 offset:31104
v_rcp_f32 v81,v81
v_mfma_f32_32x32x16_bf16 v[32:47],a[92:95],v[156:159],v[32:47]
ds_read_b128 v[188:191],v4 offset:31616
v_fma_f32 v82,v82,v74,v74
v_fma_f32 v83,v83,v74,v74
v_rcp_f32 v82,v82
s_waitcnt lgkmcnt(4)
v_mfma_f32_32x32x16_bf16 v[32:47],a[96:99],v[160:163],v[32:47]
s_waitcnt vmcnt(11)
s_barrier
ds_read_b128 v[128:131],v4 offset:4608
v_rcp_f32 v83,v83
v_mul_f32 v48,v48,v76
v_mfma_f32_32x32x16_bf16 v[32:47],a[100:103],v[164:167],v[32:47]
ds_read_b128 v[132:135],v4 offset:5120
v_mul_f32 v49,v49,v77
v_mul_f32 v50,v50,v78
v_mul_f32 v51,v51,v79
v_mul_f32 v52,v52,v80
v_mul_f32 v53,v53,v81
v_mfma_f32_32x32x16_bf16 v[32:47],a[104:107],v[168:171],v[32:47]
ds_read_b128 v[136:139],v4 offset:13824
s_add_u32 m0,s64,78848
v_mul_f32 v54,v54,v82
buffer_load_dwordx4 v24,s[32:35],s63 offen offset:0 sc1 lds
v_mfma_f32_32x32x16_bf16 v[32:47],a[108:111],v[172:175],v[32:47]
ds_read_b128 v[140:143],v4 offset:14336
v_mul_f32 v55,v55,v83
v_cvt_pk_bf16_f32 v84,v48,v49
v_cvt_pk_bf16_f32 v85,v50,v51
v_cvt_pk_bf16_f32 v86,v52,v53
s_waitcnt lgkmcnt(4)
v_mfma_f32_32x32x16_bf16 v[32:47],a[112:115],v[176:179],v[32:47]
ds_read_b128 v[144:147],v4 offset:23040
v_cvt_pk_bf16_f32 v87,v54,v55
ds_read2st64_b32 v[64:65],v8 offset0:0 offset1:36
ds_read2st64_b32 v[66:67],v8 offset0:72 offset1:108
ds_read2st64_b32 v[68:69],v8 offset0:144 offset1:180
ds_read2st64_b32 v[70:71],v8 offset0:216 offset1:252
v_mfma_f32_32x32x16_bf16 v[32:47],a[116:119],v[180:183],v[32:47]
ds_read_b128 v[148:151],v4 offset:23552
v_xor_b32 v8,v9,v8
v_mfma_f32_32x32x16_bf16 v[32:47],a[120:123],v[184:187],v[32:47]
buffer_store_dwordx4 v[84:87],v15,s[32:35],s68 offen sc1
ds_read_b128 v[152:155],v4 offset:32256
v_mfma_f32_32x32x16_bf16 v[32:47],a[124:127],v[188:191],v[32:47]
ds_read_b128 v[156:159],v4 offset:32768
s_waitcnt lgkmcnt(8)
v_mfma_f32_32x32x16_bf16 v[32:47],a[128:131],v[128:131],v[32:47]
buffer_load_dwordx4 v24,s[32:35],s63 offen offset:1024 sc1 lds
ds_read_b128 v[160:163],v4 offset:5760
v_mfma_f32_32x32x16_bf16 v[32:47],a[132:135],v[132:135],v[32:47]
ds_read_b128 v[164:167],v4 offset:6272
s_waitcnt lgkmcnt(5)
v_add_f32 v64,v64,v65
v_add_f32 v66,v66,v67
v_add_f32 v68,v68,v69
v_add_f32 v70,v70,v71
v_mfma_f32_32x32x16_bf16 v[32:47],a[136:139],v[136:139],v[32:47]
buffer_load_dwordx4 v24,s[32:35],s63 offen offset:2048 sc1 lds
ds_read_b128 v[168:171],v4 offset:14976
v_add_f32 v66,v64,v66
v_add_f32 v68,v68,v70
v_mfma_f32_32x32x16_bf16 v[32:47],a[140:143],v[140:143],v[32:47]
ds_read_b128 v[172:175],v4 offset:15488
v_add_f32_dpp v64,v66,v66 row_ror:8 row_mask:0xf bank_mask:0x3
s_waitcnt lgkmcnt(4)
v_mfma_f32_32x32x16_bf16 v[32:47],a[144:147],v[144:147],v[32:47]
buffer_load_dwordx2 v[12:13],v14,s[0:3],s66 offen sc1
ds_read_b128 v[176:179],v4 offset:24192
v_add_f32_dpp v64,v68,v68 row_ror:8 row_mask:0xf bank_mask:0xc
v_mfma_f32_32x32x16_bf16 v[32:47],a[148:151],v[148:151],v[32:47]
ds_read_b128 v[180:183],v4 offset:24704
v_add_f32_dpp v64,v64,v64 row_ror:12 row_mask:0xf bank_mask:0xf
v_mfma_f32_32x32x16_bf16 v[32:47],a[152:155],v[152:155],v[32:47]
buffer_load_dwordx4 v24,s[32:35],s63 offen offset:3072 sc1 lds
ds_read_b128 v[184:187],v4 offset:33408
v_add_f32_dpp v64,v64,v64 quad_perm:[2,3,0,1] row_mask:0xf bank_mask:0xf
v_mfma_f32_32x32x16_bf16 v[32:47],a[156:159],v[156:159],v[32:47]
ds_read_b128 v[188:191],v4 offset:33920
s_add_u32 m0,s64,115712
v_mul_f32 v64,0x3a800000,v64
s_waitcnt lgkmcnt(4)
v_mfma_f32_32x32x16_bf16 v[32:47],a[160:163],v[160:163],v[32:47]
buffer_load_dwordx4 v25,s[32:35],s63 offen offset:0 sc1 lds
ds_read_b128 v[128:131],v4 offset:6912
v_mul_f32 v65,v64,v64
v_mfma_f32_32x32x16_bf16 v[32:47],a[164:167],v[164:167],v[32:47]
ds_read_b128 v[132:135],v4 offset:7424
v_sub_f32_dpp v65,v64,v65 quad_perm:[1,1,3,3] row_mask:0xf bank_mask:0xf
v_mfma_f32_32x32x16_bf16 v[32:47],a[168:171],v[168:171],v[32:47]
ds_read_b128 v[136:139],v4 offset:16128
v_max_f32 v65,0,v65
v_mfma_f32_32x32x16_bf16 v[32:47],a[172:175],v[172:175],v[32:47]
ds_read_b128 v[140:143],v4 offset:16640
v_cvt_pk_bf16_f32 v65,0,v65
s_waitcnt lgkmcnt(4)
v_mfma_f32_32x32x16_bf16 v[32:47],a[176:179],v[176:179],v[32:47]
buffer_load_dwordx4 v25,s[32:35],s63 offen offset:1024 sc1 lds
ds_read_b128 v[144:147],v4 offset:25344
v_add_f32 v65,0x3727c5ac,v65
v_mfma_f32_32x32x16_bf16 v[32:47],a[180:183],v[180:183],v[32:47]
ds_read_b128 v[148:151],v4 offset:25856
v_cvt_pk_bf16_f32 v65,0,v65
v_mfma_f32_32x32x16_bf16 v[32:47],a[184:187],v[184:187],v[32:47]
buffer_load_dwordx4 v25,s[32:35],s63 offen offset:2048 sc1 lds
ds_read_b128 v[152:155],v4 offset:34560
v_sqrt_f32 v65,v65
v_mfma_f32_32x32x16_bf16 v[32:47],a[188:191],v[188:191],v[32:47]
ds_read_b128 v[156:159],v4 offset:35072
v_cvt_pk_bf16_f32 v65,v64,v65
s_waitcnt lgkmcnt(4)
v_mfma_f32_32x32x16_bf16 v[32:47],a[192:195],v[128:131],v[32:47]
ds_read_b128 v[160:163],v4 offset:8064
ds_write_b32 v10,v65 offset:0
v_mfma_f32_32x32x16_bf16 v[32:47],a[196:199],v[132:135],v[32:47]
ds_read_b128 v[164:167],v4 offset:8576
v_mfma_f32_32x32x16_bf16 v[32:47],a[200:203],v[136:139],v[32:47]
buffer_load_dwordx4 v25,s[32:35],s63 offen offset:3072 sc1 lds
ds_read_b128 v[168:171],v4 offset:17280
s_add_u32 s63,s63,s30
v_mfma_f32_32x32x16_bf16 v[32:47],a[204:207],v[140:143],v[32:47]
ds_read_b128 v[172:175],v4 offset:17792
s_waitcnt lgkmcnt(5)
v_mfma_f32_32x32x16_bf16 v[32:47],a[208:211],v[144:147],v[32:47]
ds_read_b128 v[176:179],v4 offset:26496
v_mfma_f32_32x32x16_bf16 v[32:47],a[212:215],v[148:151],v[32:47]
ds_read_b128 v[180:183],v4 offset:27008
v_mfma_f32_32x32x16_bf16 v[32:47],a[216:219],v[152:155],v[32:47]
ds_read_b128 v[184:187],v4 offset:35712
v_mfma_f32_32x32x16_bf16 v[32:47],a[220:223],v[156:159],v[32:47]
ds_read_b128 v[188:191],v4 offset:36224
s_waitcnt lgkmcnt(4)
v_mfma_f32_32x32x16_bf16 v[32:47],a[224:227],v[160:163],v[32:47]
s_waitcnt vmcnt(8)
s_barrier
buffer_store_dwordx2 v[16:17],v18,s[32:35],s68 offen sc1
ds_read_b128 v[128:131],v5 offset:0
v_mfma_f32_32x32x16_bf16 v[32:47],a[228:231],v[164:167],v[32:47]
ds_read_b128 v[132:135],v5 offset:512
ds_read_b32 v74,v11 offset:0
v_mfma_f32_32x32x16_bf16 v[32:47],a[232:235],v[168:171],v[32:47]
ds_read_b128 v[136:139],v5 offset:9216
v_mfma_f32_32x32x16_bf16 v[32:47],a[236:239],v[172:175],v[32:47]
ds_read_b128 v[140:143],v5 offset:9728
s_waitcnt lgkmcnt(5)
v_mfma_f32_32x32x16_bf16 v[32:47],a[240:243],v[176:179],v[32:47]
ds_read_b128 v[144:147],v5 offset:18432
s_waitcnt lgkmcnt(3)
v_lshlrev_b32 v72,16,v74
v_and_b32 v74,0xffff0000,v74
v_mfma_f32_32x32x16_bf16 v[32:47],a[244:247],v[180:183],v[32:47]
ds_read_b128 v[148:151],v5 offset:18944
v_rcp_f32 v73,v74
v_mul_f32 v74,v74,v74
v_mfma_f32_32x32x16_bf16 v[32:47],a[248:251],v[184:187],v[32:47]
ds_read_b128 v[152:155],v5 offset:27648
v_mul_f32 v73,0xbfb8aa3b,v73
v_mfma_f32_32x32x16_bf16 v[32:47],a[252:255],v[188:191],v[32:47]
ds_read_b128 v[156:159],v5 offset:28160
s_waitcnt lgkmcnt(4)
v_mfma_f32_32x32x16_bf16 v[48:63],a[0:3],v[128:131],0
ds_read_b128 v[160:163],v5 offset:1152
s_waitcnt vmcnt(6)
v_cmp_ne_u64 vcc,v[16:17],v[12:13]
s_add_u32 s68,s68,s30
v_mfma_f32_32x32x16_bf16 v[48:63],a[4:7],v[132:135],v[48:63]
ds_read_b128 v[164:167],v5 offset:1664
s_cbranch_vccnz .L_rth1_0
.L_bkh1_0:
s_add_u32 s66,s66,s30
v_mfma_f32_32x32x16_bf16 v[48:63],a[8:11],v[136:139],v[48:63]
ds_read_b128 v[168:171],v5 offset:10368
s_add_u32 m0,s64,0
v_fma_f32 v32,-v72,v112,v32
buffer_load_dwordx4 v20,s[32:35],s63 offen offset:0 sc1 lds
v_mfma_f32_32x32x16_bf16 v[48:63],a[12:15],v[140:143],v[48:63]
ds_read_b128 v[172:175],v5 offset:10880
v_fma_f32 v33,-v72,v113,v33
v_fma_f32 v34,-v72,v114,v34
v_fma_f32 v35,-v72,v115,v35
v_fma_f32 v36,-v72,v116,v36
s_waitcnt lgkmcnt(4)
v_mfma_f32_32x32x16_bf16 v[48:63],a[16:19],v[144:147],v[48:63]
buffer_load_dwordx4 v20,s[32:35],s63 offen offset:1024 sc1 lds
ds_read_b128 v[176:179],v5 offset:19584
v_fma_f32 v37,-v72,v117,v37
v_fma_f32 v38,-v72,v118,v38
v_mfma_f32_32x32x16_bf16 v[48:63],a[20:23],v[148:151],v[48:63]
ds_read_b128 v[180:183],v5 offset:20096
v_fma_f32 v39,-v72,v119,v39
v_fma_f32 v40,-v72,v120,v40
v_fma_f32 v41,-v72,v121,v41
v_fma_f32 v42,-v72,v122,v42
v_fma_f32 v43,-v72,v123,v43
v_mfma_f32_32x32x16_bf16 v[48:63],a[24:27],v[152:155],v[48:63]
buffer_load_dwordx4 v20,s[32:35],s63 offen offset:2048 sc1 lds
ds_read_b128 v[184:187],v5 offset:28800
v_fma_f32 v44,-v72,v124,v44
v_fma_f32 v45,-v72,v125,v45
v_mfma_f32_32x32x16_bf16 v[48:63],a[28:31],v[156:159],v[48:63]
ds_read_b128 v[188:191],v5 offset:29312
v_fma_f32 v46,-v72,v126,v46
v_fma_f32 v47,-v72,v127,v47
v_mul_f32 v76,v32,v73
v_mul_f32 v77,v33,v73
s_waitcnt lgkmcnt(4)
v_mfma_f32_32x32x16_bf16 v[48:63],a[32:35],v[160:163],v[48:63]
buffer_load_dwordx4 v20,s[32:35],s63 offen offset:3072 sc1 lds
ds_read_b128 v[128:131],v5 offset:2304
v_exp_f32 v76,v76
v_mfma_f32_32x32x16_bf16 v[48:63],a[36:39],v[164:167],v[48:63]
ds_read_b128 v[132:135],v5 offset:2816
v_exp_f32 v77,v77
v_mul_f32 v78,v34,v73
v_mul_f32 v79,v35,v73
v_mul_f32 v80,v36,v73
v_mfma_f32_32x32x16_bf16 v[48:63],a[40:43],v[168:171],v[48:63]
ds_read_b128 v[136:139],v5 offset:11520
s_add_u32 m0,s64,36864
v_exp_f32 v78,v78
v_exp_f32 v79,v79
v_mfma_f32_32x32x16_bf16 v[48:63],a[44:47],v[172:175],v[48:63]
ds_read_b128 v[140:143],v5 offset:12032
v_exp_f32 v80,v80
v_mul_f32 v81,v37,v73
v_mul_f32 v82,v38,v73
s_waitcnt lgkmcnt(4)
v_mfma_f32_32x32x16_bf16 v[48:63],a[48:51],v[176:179],v[48:63]
buffer_load_dwordx4 v21,s[32:35],s63 offen offset:0 sc1 lds
ds_read_b128 v[144:147],v5 offset:20736
v_exp_f32 v81,v81
v_mfma_f32_32x32x16_bf16 v[48:63],a[52:55],v[180:183],v[48:63]
ds_read_b128 v[148:151],v5 offset:21248
)#"
R"#(
v_exp_f32 v82,v82
v_mul_f32 v83,v39,v73
v_mul_f32 v32,v32,v40
v_mul_f32 v33,v33,v41
v_mfma_f32_32x32x16_bf16 v[48:63],a[56:59],v[184:187],v[48:63]
buffer_load_dwordx4 v21,s[32:35],s63 offen offset:1024 sc1 lds
ds_read_b128 v[152:155],v5 offset:29952
v_exp_f32 v83,v83
v_mfma_f32_32x32x16_bf16 v[48:63],a[60:63],v[188:191],v[48:63]
ds_read_b128 v[156:159],v5 offset:30464
v_mul_f32 v34,v34,v42
v_mul_f32 v35,v35,v43
v_mul_f32 v36,v36,v44
v_mul_f32 v37,v37,v45
s_waitcnt lgkmcnt(4)
v_mfma_f32_32x32x16_bf16 v[48:63],a[64:67],v[128:131],v[48:63]
buffer_load_dwordx4 v21,s[32:35],s63 offen offset:2048 sc1 lds
ds_read_b128 v[160:163],v5 offset:3456
v_mul_f32 v38,v38,v46
v_mul_f32 v39,v39,v47
v_mfma_f32_32x32x16_bf16 v[48:63],a[68:71],v[132:135],v[48:63]
ds_read_b128 v[164:167],v5 offset:3968
v_fma_f32 v76,v76,v74,v74
v_fma_f32 v77,v77,v74,v74
v_rcp_f32 v76,v76
v_fma_f32 v78,v78,v74,v74
v_mfma_f32_32x32x16_bf16 v[48:63],a[72:75],v[136:139],v[48:63]
buffer_load_dwordx4 v21,s[32:35],s63 offen offset:3072 sc1 lds
ds_read_b128 v[168:171],v5 offset:12672
s_add_u32 m0,s64,4096
v_fma_f32 v79,v79,v74,v74
v_mfma_f32_32x32x16_bf16 v[48:63],a[76:79],v[140:143],v[48:63]
ds_read_b128 v[172:175],v5 offset:13184
v_rcp_f32 v77,v77
v_rcp_f32 v78,v78
s_waitcnt lgkmcnt(4)
v_mfma_f32_32x32x16_bf16 v[48:63],a[80:83],v[144:147],v[48:63]
buffer_load_dwordx4 v22,s[32:35],s63 offen offset:0 sc1 lds
ds_read_b128 v[176:179],v5 offset:21888
s_add_u32 m0,s64,40960
v_fma_f32 v80,v80,v74,v74
v_mfma_f32_32x32x16_bf16 v[48:63],a[84:87],v[148:151],v[48:63]
ds_read_b128 v[180:183],v5 offset:22400
v_rcp_f32 v79,v79
v_rcp_f32 v80,v80
v_fma_f32 v81,v81,v74,v74
v_mfma_f32_32x32x16_bf16 v[48:63],a[88:91],v[152:155],v[48:63]
buffer_load_dwordx4 v23,s[32:35],s63 offen offset:0 sc1 lds
ds_read_b128 v[184:187],v5 offset:31104
v_rcp_f32 v81,v81
v_mfma_f32_32x32x16_bf16 v[48:63],a[92:95],v[156:159],v[48:63]
ds_read_b128 v[188:191],v5 offset:31616
v_fma_f32 v82,v82,v74,v74
v_fma_f32 v83,v83,v74,v74
v_rcp_f32 v82,v82
s_waitcnt lgkmcnt(4)
v_mfma_f32_32x32x16_bf16 v[48:63],a[96:99],v[160:163],v[48:63]
s_waitcnt vmcnt(11)
s_barrier
ds_read_b128 v[128:131],v5 offset:4608
v_rcp_f32 v83,v83
v_mul_f32 v32,v32,v76
v_mfma_f32_32x32x16_bf16 v[48:63],a[100:103],v[164:167],v[48:63]
ds_read_b128 v[132:135],v5 offset:5120
v_mul_f32 v33,v33,v77
v_mul_f32 v34,v34,v78
v_mul_f32 v35,v35,v79
v_mul_f32 v36,v36,v80
v_mul_f32 v37,v37,v81
v_mfma_f32_32x32x16_bf16 v[48:63],a[104:107],v[168:171],v[48:63]
ds_read_b128 v[136:139],v5 offset:13824
s_add_u32 m0,s64,5120
v_mul_f32 v38,v38,v82
buffer_load_dwordx4 v24,s[32:35],s63 offen offset:0 sc1 lds
v_mfma_f32_32x32x16_bf16 v[48:63],a[108:111],v[172:175],v[48:63]
ds_read_b128 v[140:143],v5 offset:14336
v_mul_f32 v39,v39,v83
v_cvt_pk_bf16_f32 v84,v32,v33
v_cvt_pk_bf16_f32 v85,v34,v35
v_cvt_pk_bf16_f32 v86,v36,v37
s_waitcnt lgkmcnt(4)
v_mfma_f32_32x32x16_bf16 v[48:63],a[112:115],v[176:179],v[48:63]
ds_read_b128 v[144:147],v5 offset:23040
v_cvt_pk_bf16_f32 v87,v38,v39
ds_read2st64_b32 v[64:65],v8 offset0:0 offset1:36
ds_read2st64_b32 v[66:67],v8 offset0:72 offset1:108
ds_read2st64_b32 v[68:69],v8 offset0:144 offset1:180
ds_read2st64_b32 v[70:71],v8 offset0:216 offset1:252
v_mfma_f32_32x32x16_bf16 v[48:63],a[116:119],v[180:183],v[48:63]
ds_read_b128 v[148:151],v5 offset:23552
v_xor_b32 v8,v9,v8
v_mfma_f32_32x32x16_bf16 v[48:63],a[120:123],v[184:187],v[48:63]
buffer_store_dwordx4 v[84:87],v15,s[32:35],s68 offen sc1
ds_read_b128 v[152:155],v5 offset:32256
v_mfma_f32_32x32x16_bf16 v[48:63],a[124:127],v[188:191],v[48:63]
ds_read_b128 v[156:159],v5 offset:32768
s_waitcnt lgkmcnt(8)
v_mfma_f32_32x32x16_bf16 v[48:63],a[128:131],v[128:131],v[48:63]
buffer_load_dwordx4 v24,s[32:35],s63 offen offset:1024 sc1 lds
ds_read_b128 v[160:163],v5 offset:5760
v_mfma_f32_32x32x16_bf16 v[48:63],a[132:135],v[132:135],v[48:63]
ds_read_b128 v[164:167],v5 offset:6272
s_waitcnt lgkmcnt(5)
v_add_f32 v64,v64,v65
v_add_f32 v66,v66,v67
v_add_f32 v68,v68,v69
v_add_f32 v70,v70,v71
v_mfma_f32_32x32x16_bf16 v[48:63],a[136:139],v[136:139],v[48:63]
buffer_load_dwordx4 v24,s[32:35],s63 offen offset:2048 sc1 lds
ds_read_b128 v[168:171],v5 offset:14976
v_add_f32 v66,v64,v66
v_add_f32 v68,v68,v70
v_mfma_f32_32x32x16_bf16 v[48:63],a[140:143],v[140:143],v[48:63]
ds_read_b128 v[172:175],v5 offset:15488
v_add_f32_dpp v64,v66,v66 row_ror:8 row_mask:0xf bank_mask:0x3
s_waitcnt lgkmcnt(4)
v_mfma_f32_32x32x16_bf16 v[48:63],a[144:147],v[144:147],v[48:63]
buffer_load_dwordx2 v[12:13],v14,s[0:3],s66 offen sc1
ds_read_b128 v[176:179],v5 offset:24192
v_add_f32_dpp v64,v68,v68 row_ror:8 row_mask:0xf bank_mask:0xc
v_mfma_f32_32x32x16_bf16 v[48:63],a[148:151],v[148:151],v[48:63]
ds_read_b128 v[180:183],v5 offset:24704
v_add_f32_dpp v64,v64,v64 row_ror:12 row_mask:0xf bank_mask:0xf
v_mfma_f32_32x32x16_bf16 v[48:63],a[152:155],v[152:155],v[48:63]
buffer_load_dwordx4 v24,s[32:35],s63 offen offset:3072 sc1 lds
ds_read_b128 v[184:187],v5 offset:33408
v_add_f32_dpp v64,v64,v64 quad_perm:[2,3,0,1] row_mask:0xf bank_mask:0xf
v_mfma_f32_32x32x16_bf16 v[48:63],a[156:159],v[156:159],v[48:63]
ds_read_b128 v[188:191],v5 offset:33920
s_add_u32 m0,s64,41984
v_mul_f32 v64,0x3a800000,v64
s_waitcnt lgkmcnt(4)
v_mfma_f32_32x32x16_bf16 v[48:63],a[160:163],v[160:163],v[48:63]
buffer_load_dwordx4 v25,s[32:35],s63 offen offset:0 sc1 lds
ds_read_b128 v[128:131],v5 offset:6912
v_mul_f32 v65,v64,v64
v_mfma_f32_32x32x16_bf16 v[48:63],a[164:167],v[164:167],v[48:63]
ds_read_b128 v[132:135],v5 offset:7424
v_sub_f32_dpp v65,v64,v65 quad_perm:[1,1,3,3] row_mask:0xf bank_mask:0xf
v_mfma_f32_32x32x16_bf16 v[48:63],a[168:171],v[168:171],v[48:63]
ds_read_b128 v[136:139],v5 offset:16128
v_max_f32 v65,0,v65
v_mfma_f32_32x32x16_bf16 v[48:63],a[172:175],v[172:175],v[48:63]
ds_read_b128 v[140:143],v5 offset:16640
v_cvt_pk_bf16_f32 v65,0,v65
s_waitcnt lgkmcnt(4)
v_mfma_f32_32x32x16_bf16 v[48:63],a[176:179],v[176:179],v[48:63]
buffer_load_dwordx4 v25,s[32:35],s63 offen offset:1024 sc1 lds
ds_read_b128 v[144:147],v5 offset:25344
v_add_f32 v65,0x3727c5ac,v65
v_mfma_f32_32x32x16_bf16 v[48:63],a[180:183],v[180:183],v[48:63]
ds_read_b128 v[148:151],v5 offset:25856
v_cvt_pk_bf16_f32 v65,0,v65
v_mfma_f32_32x32x16_bf16 v[48:63],a[184:187],v[184:187],v[48:63]
buffer_load_dwordx4 v25,s[32:35],s63 offen offset:2048 sc1 lds
ds_read_b128 v[152:155],v5 offset:34560
v_sqrt_f32 v65,v65
v_mfma_f32_32x32x16_bf16 v[48:63],a[188:191],v[188:191],v[48:63]
ds_read_b128 v[156:159],v5 offset:35072
v_cvt_pk_bf16_f32 v65,v64,v65
s_waitcnt lgkmcnt(4)
v_mfma_f32_32x32x16_bf16 v[48:63],a[192:195],v[128:131],v[48:63]
ds_read_b128 v[160:163],v5 offset:8064
ds_write_b32 v10,v65 offset:128
v_mfma_f32_32x32x16_bf16 v[48:63],a[196:199],v[132:135],v[48:63]
ds_read_b128 v[164:167],v5 offset:8576
v_mfma_f32_32x32x16_bf16 v[48:63],a[200:203],v[136:139],v[48:63]
buffer_load_dwordx4 v25,s[32:35],s63 offen offset:3072 sc1 lds
ds_read_b128 v[168:171],v5 offset:17280
s_add_u32 s63,s63,s30
v_mfma_f32_32x32x16_bf16 v[48:63],a[204:207],v[140:143],v[48:63]
ds_read_b128 v[172:175],v5 offset:17792
s_waitcnt lgkmcnt(5)
v_mfma_f32_32x32x16_bf16 v[48:63],a[208:211],v[144:147],v[48:63]
ds_read_b128 v[176:179],v5 offset:26496
v_mfma_f32_32x32x16_bf16 v[48:63],a[212:215],v[148:151],v[48:63]
ds_read_b128 v[180:183],v5 offset:27008
v_mfma_f32_32x32x16_bf16 v[48:63],a[216:219],v[152:155],v[48:63]
ds_read_b128 v[184:187],v5 offset:35712
v_mfma_f32_32x32x16_bf16 v[48:63],a[220:223],v[156:159],v[48:63]
ds_read_b128 v[188:191],v5 offset:36224
s_waitcnt lgkmcnt(4)
v_mfma_f32_32x32x16_bf16 v[48:63],a[224:227],v[160:163],v[48:63]
s_waitcnt vmcnt(8)
s_barrier
buffer_store_dwordx2 v[16:17],v18,s[32:35],s68 offen sc1
ds_read_b128 v[128:131],v4 offset:0
v_mfma_f32_32x32x16_bf16 v[48:63],a[228:231],v[164:167],v[48:63]
ds_read_b128 v[132:135],v4 offset:512
ds_read_b32 v74,v11 offset:128
v_mfma_f32_32x32x16_bf16 v[48:63],a[232:235],v[168:171],v[48:63]
ds_read_b128 v[136:139],v4 offset:9216
v_mfma_f32_32x32x16_bf16 v[48:63],a[236:239],v[172:175],v[48:63]
ds_read_b128 v[140:143],v4 offset:9728
s_waitcnt lgkmcnt(5)
v_mfma_f32_32x32x16_bf16 v[48:63],a[240:243],v[176:179],v[48:63]
ds_read_b128 v[144:147],v4 offset:18432
s_waitcnt lgkmcnt(3)
v_lshlrev_b32 v72,16,v74
v_and_b32 v74,0xffff0000,v74
v_mfma_f32_32x32x16_bf16 v[48:63],a[244:247],v[180:183],v[48:63]
ds_read_b128 v[148:151],v4 offset:18944
v_rcp_f32 v73,v74
v_mul_f32 v74,v74,v74
v_mfma_f32_32x32x16_bf16 v[48:63],a[248:251],v[184:187],v[48:63]
ds_read_b128 v[152:155],v4 offset:27648
v_mul_f32 v73,0xbfb8aa3b,v73
s_sub_u32 s62,s62,1
v_mfma_f32_32x32x16_bf16 v[48:63],a[252:255],v[188:191],v[48:63]
ds_read_b128 v[156:159],v4 offset:28160
s_cbranch_scc0 .L_hl_0
s_nop 1
s_add_u32 s68,s68,s30
s_nop 6
v_fma_f32 v48,-v72,v112,v48
v_fma_f32 v49,-v72,v113,v49
v_fma_f32 v50,-v72,v114,v50
v_fma_f32 v51,-v72,v115,v51
v_fma_f32 v52,-v72,v116,v52
v_fma_f32 v53,-v72,v117,v53
v_fma_f32 v54,-v72,v118,v54
v_fma_f32 v55,-v72,v119,v55
v_fma_f32 v56,-v72,v120,v56
v_fma_f32 v57,-v72,v121,v57
v_fma_f32 v58,-v72,v122,v58
v_fma_f32 v59,-v72,v123,v59
v_fma_f32 v60,-v72,v124,v60
v_fma_f32 v61,-v72,v125,v61
v_fma_f32 v62,-v72,v126,v62
v_fma_f32 v63,-v72,v127,v63
v_mul_f32 v76,v48,v73
v_mul_f32 v77,v49,v73
v_mul_f32 v78,v50,v73
v_mul_f32 v79,v51,v73
v_mul_f32 v80,v52,v73
v_mul_f32 v81,v53,v73
v_mul_f32 v82,v54,v73
v_mul_f32 v83,v55,v73
v_exp_f32 v76,v76
v_exp_f32 v77,v77
v_mul_f32 v48,v48,v56
v_mul_f32 v49,v49,v57
v_exp_f32 v78,v78
v_exp_f32 v79,v79
v_mul_f32 v50,v50,v58
v_mul_f32 v51,v51,v59
v_exp_f32 v80,v80
v_exp_f32 v81,v81
v_mul_f32 v52,v52,v60
v_mul_f32 v53,v53,v61
v_exp_f32 v82,v82
v_exp_f32 v83,v83
v_mul_f32 v54,v54,v62
v_mul_f32 v55,v55,v63
v_fma_f32 v76,v76,v74,v74
v_fma_f32 v77,v77,v74,v74
v_rcp_f32 v76,v76
v_rcp_f32 v77,v77
v_fma_f32 v78,v78,v74,v74
v_fma_f32 v79,v79,v74,v74
v_rcp_f32 v78,v78
v_rcp_f32 v79,v79
v_fma_f32 v80,v80,v74,v74
v_fma_f32 v81,v81,v74,v74
v_rcp_f32 v80,v80
v_rcp_f32 v81,v81
v_fma_f32 v82,v82,v74,v74
v_fma_f32 v83,v83,v74,v74
v_rcp_f32 v82,v82
v_rcp_f32 v83,v83
v_mul_f32 v48,v48,v76
v_mul_f32 v49,v49,v77
v_mul_f32 v50,v50,v78
v_mul_f32 v51,v51,v79
v_mul_f32 v52,v52,v80
v_mul_f32 v53,v53,v81
v_mul_f32 v54,v54,v82
v_mul_f32 v55,v55,v83
v_cvt_pk_bf16_f32 v84,v48,v49
v_cvt_pk_bf16_f32 v85,v50,v51
v_cvt_pk_bf16_f32 v86,v52,v53
v_cvt_pk_bf16_f32 v87,v54,v55
buffer_store_dwordx4 v[84:87],v15,s[32:35],s68 offen sc1
s_waitcnt vmcnt(0)
s_barrier
buffer_store_dwordx2 v[16:17],v18,s[32:35],s68 offen sc1
s_cbranch_execz .L_hbk
s_branch .L_end
.p2align 6
.L_hv1:
.L_hl_1:
s_waitcnt lgkmcnt(4)
v_mfma_f32_32x32x16_bf16 v[32:47],a[0:3],v[128:131],0
ds_read_b128 v[160:163],v4 offset:1152
s_waitcnt vmcnt(5)
v_cmp_ne_u64 vcc,v[16:17],v[12:13]
s_add_u32 s68,s68,s30
v_mfma_f32_32x32x16_bf16 v[32:47],a[4:7],v[132:135],v[32:47]
ds_read_b128 v[164:167],v4 offset:1664
s_cbranch_vccnz .L_rth0_1
.L_bkh0_1:
s_add_u32 s66,s66,s30
v_mfma_f32_32x32x16_bf16 v[32:47],a[8:11],v[136:139],v[32:47]
ds_read_b128 v[168:171],v4 offset:10368
s_add_u32 m0,s64,73728
v_fma_f32 v48,-v72,v112,v48
buffer_load_dwordx4 v20,s[32:35],s63 offen offset:0 sc1 lds
v_mfma_f32_32x32x16_bf16 v[32:47],a[12:15],v[140:143],v[32:47]
ds_read_b128 v[172:175],v4 offset:10880
v_fma_f32 v49,-v72,v113,v49
v_fma_f32 v50,-v72,v114,v50
v_fma_f32 v51,-v72,v115,v51
v_fma_f32 v52,-v72,v116,v52
s_waitcnt lgkmcnt(4)
v_mfma_f32_32x32x16_bf16 v[32:47],a[16:19],v[144:147],v[32:47]
buffer_load_dwordx4 v20,s[32:35],s63 offen offset:1024 sc1 lds
ds_read_b128 v[176:179],v4 offset:19584
v_fma_f32 v53,-v72,v117,v53
v_fma_f32 v54,-v72,v118,v54
v_mfma_f32_32x32x16_bf16 v[32:47],a[20:23],v[148:151],v[32:47]
ds_read_b128 v[180:183],v4 offset:20096
v_fma_f32 v55,-v72,v119,v55
v_fma_f32 v56,-v72,v120,v56
v_fma_f32 v57,-v72,v121,v57
v_fma_f32 v58,-v72,v122,v58
v_fma_f32 v59,-v72,v123,v59
v_mfma_f32_32x32x16_bf16 v[32:47],a[24:27],v[152:155],v[32:47]
buffer_load_dwordx4 v20,s[32:35],s63 offen offset:2048 sc1 lds
ds_read_b128 v[184:187],v4 offset:28800
v_fma_f32 v60,-v72,v124,v60
v_fma_f32 v61,-v72,v125,v61
v_mfma_f32_32x32x16_bf16 v[32:47],a[28:31],v[156:159],v[32:47]
ds_read_b128 v[188:191],v4 offset:29312
v_fma_f32 v62,-v72,v126,v62
v_fma_f32 v63,-v72,v127,v63
v_mul_f32 v76,v48,v73
v_mul_f32 v77,v49,v73
s_waitcnt lgkmcnt(4)
v_mfma_f32_32x32x16_bf16 v[32:47],a[32:35],v[160:163],v[32:47]
buffer_load_dwordx4 v20,s[32:35],s63 offen offset:3072 sc1 lds
ds_read_b128 v[128:131],v4 offset:2304
v_exp_f32 v76,v76
v_mfma_f32_32x32x16_bf16 v[32:47],a[36:39],v[164:167],v[32:47]
ds_read_b128 v[132:135],v4 offset:2816
v_exp_f32 v77,v77
v_mul_f32 v78,v50,v73
v_mul_f32 v79,v51,v73
v_mul_f32 v80,v52,v73
v_mfma_f32_32x32x16_bf16 v[32:47],a[40:43],v[168:171],v[32:47]
ds_read_b128 v[136:139],v4 offset:11520
s_add_u32 m0,s64,110592
v_exp_f32 v78,v78
v_exp_f32 v79,v79
v_mfma_f32_32x32x16_bf16 v[32:47],a[44:47],v[172:175],v[32:47]
ds_read_b128 v[140:143],v4 offset:12032
v_exp_f32 v80,v80
v_mul_f32 v81,v53,v73
v_mul_f32 v82,v54,v73
s_waitcnt lgkmcnt(4)
v_mfma_f32_32x32x16_bf16 v[32:47],a[48:51],v[176:179],v[32:47]
buffer_load_dwordx4 v21,s[32:35],s63 offen offset:0 sc1 lds
ds_read_b128 v[144:147],v4 offset:20736
v_exp_f32 v81,v81
v_mfma_f32_32x32x16_bf16 v[32:47],a[52:55],v[180:183],v[32:47]
ds_read_b128 v[148:151],v4 offset:21248
v_exp_f32 v82,v82
v_mul_f32 v83,v55,v73
v_mul_f32 v48,v48,v56
v_mul_f32 v49,v49,v57
v_mfma_f32_32x32x16_bf16 v[32:47],a[56:59],v[184:187],v[32:47]
buffer_load_dwordx4 v21,s[32:35],s63 offen offset:1024 sc1 lds
ds_read_b128 v[152:155],v4 offset:29952
v_exp_f32 v83,v83
v_mfma_f32_32x32x16_bf16 v[32:47],a[60:63],v[188:191],v[32:47]
ds_read_b128 v[156:159],v4 offset:30464
v_mul_f32 v50,v50,v58
v_mul_f32 v51,v51,v59
v_mul_f32 v52,v52,v60
v_mul_f32 v53,v53,v61
s_waitcnt lgkmcnt(4)
v_mfma_f32_32x32x16_bf16 v[32:47],a[64:67],v[128:131],v[32:47]
buffer_load_dwordx4 v21,s[32:35],s63 offen offset:2048 sc1 lds
ds_read_b128 v[160:163],v4 offset:3456
v_mul_f32 v54,v54,v62
v_mul_f32 v55,v55,v63
v_mfma_f32_32x32x16_bf16 v[32:47],a[68:71],v[132:135],v[32:47]
ds_read_b128 v[164:167],v4 offset:3968
v_fma_f32 v76,v76,v74,v74
v_fma_f32 v77,v77,v74,v74
v_rcp_f32 v76,v76
v_fma_f32 v78,v78,v74,v74
v_mfma_f32_32x32x16_bf16 v[32:47],a[72:75],v[136:139],v[32:47]
buffer_load_dwordx4 v21,s[32:35],s63 offen offset:3072 sc1 lds
ds_read_b128 v[168:171],v4 offset:12672
s_add_u32 m0,s64,77824
v_fma_f32 v79,v79,v74,v74
v_mfma_f32_32x32x16_bf16 v[32:47],a[76:79],v[140:143],v[32:47]
ds_read_b128 v[172:175],v4 offset:13184
v_rcp_f32 v77,v77
v_rcp_f32 v78,v78
s_waitcnt lgkmcnt(4)
v_mfma_f32_32x32x16_bf16 v[32:47],a[80:83],v[144:147],v[32:47]
buffer_load_dwordx4 v22,s[32:35],s63 offen offset:0 sc1 lds
ds_read_b128 v[176:179],v4 offset:21888
s_add_u32 m0,s64,114688
v_fma_f32 v80,v80,v74,v74
v_mfma_f32_32x32x16_bf16 v[32:47],a[84:87],v[148:151],v[32:47]
ds_read_b128 v[180:183],v4 offset:22400
v_rcp_f32 v79,v79
v_rcp_f32 v80,v80
v_fma_f32 v81,v81,v74,v74
v_mfma_f32_32x32x16_bf16 v[32:47],a[88:91],v[152:155],v[32:47]
buffer_load_dwordx4 v23,s[32:35],s63 offen offset:0 sc1 lds
ds_read_b128 v[184:187],v4 offset:31104
v_rcp_f32 v81,v81
v_mfma_f32_32x32x16_bf16 v[32:47],a[92:95],v[156:159],v[32:47]
ds_read_b128 v[188:191],v4 offset:31616
v_fma_f32 v82,v82,v74,v74
v_fma_f32 v83,v83,v74,v74
v_rcp_f32 v82,v82
s_waitcnt lgkmcnt(4)
s_waitcnt vmcnt(10)
s_barrier
v_mfma_f32_32x32x16_bf16 v[32:47],a[96:99],v[160:163],v[32:47]
ds_read_b128 v[128:131],v4 offset:4608
v_rcp_f32 v83,v83
v_mul_f32 v48,v48,v76
v_mfma_f32_32x32x16_bf16 v[32:47],a[100:103],v[164:167],v[32:47]
ds_read_b128 v[132:135],v4 offset:5120
v_mul_f32 v49,v49,v77
v_mul_f32 v50,v50,v78
v_mul_f32 v51,v51,v79
v_mul_f32 v52,v52,v80
v_mul_f32 v53,v53,v81
v_mfma_f32_32x32x16_bf16 v[32:47],a[104:107],v[168:171],v[32:47]
ds_read_b128 v[136:139],v4 offset:13824
s_add_u32 m0,s64,78848
v_mul_f32 v54,v54,v82
buffer_load_dwordx4 v24,s[32:35],s63 offen offset:0 sc1 lds
v_mfma_f32_32x32x16_bf16 v[32:47],a[108:111],v[172:175],v[32:47]
ds_read_b128 v[140:143],v4 offset:14336
v_mul_f32 v55,v55,v83
v_cvt_pk_bf16_f32 v84,v48,v49
v_cvt_pk_bf16_f32 v85,v50,v51
v_cvt_pk_bf16_f32 v86,v52,v53
s_waitcnt lgkmcnt(4)
v_mfma_f32_32x32x16_bf16 v[32:47],a[112:115],v[176:179],v[32:47]
ds_read_b128 v[144:147],v4 offset:23040
v_cvt_pk_bf16_f32 v87,v54,v55
ds_read2st64_b32 v[64:65],v8 offset0:0 offset1:36
ds_read2st64_b32 v[66:67],v8 offset0:72 offset1:108
ds_read2st64_b32 v[68:69],v8 offset0:144 offset1:180
ds_read2st64_b32 v[70:71],v8 offset0:216 offset1:252
v_mfma_f32_32x32x16_bf16 v[32:47],a[116:119],v[180:183],v[32:47]
ds_read_b128 v[148:151],v4 offset:23552
v_xor_b32 v8,v9,v8
v_mfma_f32_32x32x16_bf16 v[32:47],a[120:123],v[184:187],v[32:47]
buffer_store_dwordx4 v[84:87],v15,s[32:35],s68 offen sc1
ds_read_b128 v[152:155],v4 offset:32256
v_mfma_f32_32x32x16_bf16 v[32:47],a[124:127],v[188:191],v[32:47]
ds_read_b128 v[156:159],v4 offset:32768
s_waitcnt lgkmcnt(8)
v_mfma_f32_32x32x16_bf16 v[32:47],a[128:131],v[128:131],v[32:47]
buffer_load_dwordx4 v24,s[32:35],s63 offen offset:1024 sc1 lds
ds_read_b128 v[160:163],v4 offset:5760
v_mfma_f32_32x32x16_bf16 v[32:47],a[132:135],v[132:135],v[32:47]
ds_read_b128 v[164:167],v4 offset:6272
s_waitcnt lgkmcnt(5)
v_add_f32 v64,v64,v65
v_add_f32 v66,v66,v67
v_add_f32 v68,v68,v69
v_add_f32 v70,v70,v71
v_mfma_f32_32x32x16_bf16 v[32:47],a[136:139],v[136:139],v[32:47]
buffer_load_dwordx4 v24,s[32:35],s63 offen offset:2048 sc1 lds
ds_read_b128 v[168:171],v4 offset:14976
v_add_f32 v66,v64,v66
v_add_f32 v68,v68,v70
v_mfma_f32_32x32x16_bf16 v[32:47],a[140:143],v[140:143],v[32:47]
ds_read_b128 v[172:175],v4 offset:15488
v_add_f32_dpp v64,v66,v66 row_ror:8 row_mask:0xf bank_mask:0x3
s_waitcnt lgkmcnt(4)
v_mfma_f32_32x32x16_bf16 v[32:47],a[144:147],v[144:147],v[32:47]
buffer_load_dwordx2 v[12:13],v14,s[0:3],s66 offen sc1
ds_read_b128 v[176:179],v4 offset:24192
v_add_f32_dpp v64,v68,v68 row_ror:8 row_mask:0xf bank_mask:0xc
v_mfma_f32_32x32x16_bf16 v[32:47],a[148:151],v[148:151],v[32:47]
ds_read_b128 v[180:183],v4 offset:24704
v_add_f32_dpp v64,v64,v64 row_ror:12 row_mask:0xf bank_mask:0xf
v_mfma_f32_32x32x16_bf16 v[32:47],a[152:155],v[152:155],v[32:47]
buffer_load_dwordx4 v24,s[32:35],s63 offen offset:3072 sc1 lds
ds_read_b128 v[184:187],v4 offset:33408
v_add_f32_dpp v64,v64,v64 quad_perm:[2,3,0,1] row_mask:0xf bank_mask:0xf
v_mfma_f32_32x32x16_bf16 v[32:47],a[156:159],v[156:159],v[32:47]
ds_read_b128 v[188:191],v4 offset:33920
s_add_u32 m0,s64,115712
v_mul_f32 v64,0x3a800000,v64
s_waitcnt lgkmcnt(4)
v_mfma_f32_32x32x16_bf16 v[32:47],a[160:163],v[160:163],v[32:47]
buffer_load_dwordx4 v25,s[32:35],s63 offen offset:0 sc1 lds
ds_read_b128 v[128:131],v4 offset:6912
)#"
R"#(
v_mul_f32 v65,v64,v64
v_mfma_f32_32x32x16_bf16 v[32:47],a[164:167],v[164:167],v[32:47]
ds_read_b128 v[132:135],v4 offset:7424
v_sub_f32_dpp v65,v64,v65 quad_perm:[1,1,3,3] row_mask:0xf bank_mask:0xf
v_mfma_f32_32x32x16_bf16 v[32:47],a[168:171],v[168:171],v[32:47]
ds_read_b128 v[136:139],v4 offset:16128
v_max_f32 v65,0,v65
v_mfma_f32_32x32x16_bf16 v[32:47],a[172:175],v[172:175],v[32:47]
ds_read_b128 v[140:143],v4 offset:16640
v_cvt_pk_bf16_f32 v65,0,v65
s_waitcnt lgkmcnt(4)
v_mfma_f32_32x32x16_bf16 v[32:47],a[176:179],v[176:179],v[32:47]
buffer_load_dwordx4 v25,s[32:35],s63 offen offset:1024 sc1 lds
ds_read_b128 v[144:147],v4 offset:25344
v_add_f32 v65,0x3727c5ac,v65
v_mfma_f32_32x32x16_bf16 v[32:47],a[180:183],v[180:183],v[32:47]
ds_read_b128 v[148:151],v4 offset:25856
v_cvt_pk_bf16_f32 v65,0,v65
v_mfma_f32_32x32x16_bf16 v[32:47],a[184:187],v[184:187],v[32:47]
buffer_load_dwordx4 v25,s[32:35],s63 offen offset:2048 sc1 lds
ds_read_b128 v[152:155],v4 offset:34560
v_sqrt_f32 v65,v65
v_mfma_f32_32x32x16_bf16 v[32:47],a[188:191],v[188:191],v[32:47]
ds_read_b128 v[156:159],v4 offset:35072
v_cvt_pk_bf16_f32 v65,v64,v65
s_waitcnt lgkmcnt(4)
v_mfma_f32_32x32x16_bf16 v[32:47],a[192:195],v[128:131],v[32:47]
ds_read_b128 v[160:163],v4 offset:8064
ds_write_b32 v10,v65 offset:0
v_mfma_f32_32x32x16_bf16 v[32:47],a[196:199],v[132:135],v[32:47]
ds_read_b128 v[164:167],v4 offset:8576
v_mfma_f32_32x32x16_bf16 v[32:47],a[200:203],v[136:139],v[32:47]
buffer_load_dwordx4 v25,s[32:35],s63 offen offset:3072 sc1 lds
ds_read_b128 v[168:171],v4 offset:17280
s_add_u32 s63,s63,s30
v_mfma_f32_32x32x16_bf16 v[32:47],a[204:207],v[140:143],v[32:47]
ds_read_b128 v[172:175],v4 offset:17792
s_waitcnt lgkmcnt(5)
v_mfma_f32_32x32x16_bf16 v[32:47],a[208:211],v[144:147],v[32:47]
ds_read_b128 v[176:179],v4 offset:26496
v_mfma_f32_32x32x16_bf16 v[32:47],a[212:215],v[148:151],v[32:47]
ds_read_b128 v[180:183],v4 offset:27008
v_mfma_f32_32x32x16_bf16 v[32:47],a[216:219],v[152:155],v[32:47]
ds_read_b128 v[184:187],v4 offset:35712
v_mfma_f32_32x32x16_bf16 v[32:47],a[220:223],v[156:159],v[32:47]
ds_read_b128 v[188:191],v4 offset:36224
s_waitcnt lgkmcnt(4)
s_waitcnt vmcnt(8)
s_barrier
v_mfma_f32_32x32x16_bf16 v[32:47],a[224:227],v[160:163],v[32:47]
ds_read_b128 v[128:131],v5 offset:0
v_mfma_f32_32x32x16_bf16 v[32:47],a[228:231],v[164:167],v[32:47]
ds_read_b128 v[132:135],v5 offset:512
ds_read_b32 v74,v11 offset:0
v_mfma_f32_32x32x16_bf16 v[32:47],a[232:235],v[168:171],v[32:47]
ds_read_b128 v[136:139],v5 offset:9216
v_mfma_f32_32x32x16_bf16 v[32:47],a[236:239],v[172:175],v[32:47]
ds_read_b128 v[140:143],v5 offset:9728
s_waitcnt lgkmcnt(5)
v_mfma_f32_32x32x16_bf16 v[32:47],a[240:243],v[176:179],v[32:47]
ds_read_b128 v[144:147],v5 offset:18432
s_waitcnt lgkmcnt(3)
v_lshlrev_b32 v72,16,v74
v_and_b32 v74,0xffff0000,v74
v_mfma_f32_32x32x16_bf16 v[32:47],a[244:247],v[180:183],v[32:47]
ds_read_b128 v[148:151],v5 offset:18944
v_rcp_f32 v73,v74
v_mul_f32 v74,v74,v74
v_mfma_f32_32x32x16_bf16 v[32:47],a[248:251],v[184:187],v[32:47]
ds_read_b128 v[152:155],v5 offset:27648
v_mul_f32 v73,0xbfb8aa3b,v73
v_mfma_f32_32x32x16_bf16 v[32:47],a[252:255],v[188:191],v[32:47]
ds_read_b128 v[156:159],v5 offset:28160
s_waitcnt lgkmcnt(4)
v_mfma_f32_32x32x16_bf16 v[48:63],a[0:3],v[128:131],0
ds_read_b128 v[160:163],v5 offset:1152
s_waitcnt vmcnt(5)
v_cmp_ne_u64 vcc,v[16:17],v[12:13]
s_add_u32 s68,s68,s30
v_mfma_f32_32x32x16_bf16 v[48:63],a[4:7],v[132:135],v[48:63]
ds_read_b128 v[164:167],v5 offset:1664
s_cbranch_vccnz .L_rth1_1
.L_bkh1_1:
s_add_u32 s66,s66,s30
v_mfma_f32_32x32x16_bf16 v[48:63],a[8:11],v[136:139],v[48:63]
ds_read_b128 v[168:171],v5 offset:10368
s_add_u32 m0,s64,0
v_fma_f32 v32,-v72,v112,v32
buffer_load_dwordx4 v20,s[32:35],s63 offen offset:0 sc1 lds
v_mfma_f32_32x32x16_bf16 v[48:63],a[12:15],v[140:143],v[48:63]
ds_read_b128 v[172:175],v5 offset:10880
v_fma_f32 v33,-v72,v113,v33
v_fma_f32 v34,-v72,v114,v34
v_fma_f32 v35,-v72,v115,v35
v_fma_f32 v36,-v72,v116,v36
s_waitcnt lgkmcnt(4)
v_mfma_f32_32x32x16_bf16 v[48:63],a[16:19],v[144:147],v[48:63]
buffer_load_dwordx4 v20,s[32:35],s63 offen offset:1024 sc1 lds
ds_read_b128 v[176:179],v5 offset:19584
v_fma_f32 v37,-v72,v117,v37
v_fma_f32 v38,-v72,v118,v38
v_mfma_f32_32x32x16_bf16 v[48:63],a[20:23],v[148:151],v[48:63]
ds_read_b128 v[180:183],v5 offset:20096
v_fma_f32 v39,-v72,v119,v39
v_fma_f32 v40,-v72,v120,v40
v_fma_f32 v41,-v72,v121,v41
v_fma_f32 v42,-v72,v122,v42
v_fma_f32 v43,-v72,v123,v43
v_mfma_f32_32x32x16_bf16 v[48:63],a[24:27],v[152:155],v[48:63]
buffer_load_dwordx4 v20,s[32:35],s63 offen offset:2048 sc1 lds
ds_read_b128 v[184:187],v5 offset:28800
v_fma_f32 v44,-v72,v124,v44
v_fma_f32 v45,-v72,v125,v45
v_mfma_f32_32x32x16_bf16 v[48:63],a[28:31],v[156:159],v[48:63]
ds_read_b128 v[188:191],v5 offset:29312
v_fma_f32 v46,-v72,v126,v46
v_fma_f32 v47,-v72,v127,v47
v_mul_f32 v76,v32,v73
v_mul_f32 v77,v33,v73
s_waitcnt lgkmcnt(4)
v_mfma_f32_32x32x16_bf16 v[48:63],a[32:35],v[160:163],v[48:63]
buffer_load_dwordx4 v20,s[32:35],s63 offen offset:3072 sc1 lds
ds_read_b128 v[128:131],v5 offset:2304
v_exp_f32 v76,v76
v_mfma_f32_32x32x16_bf16 v[48:63],a[36:39],v[164:167],v[48:63]
ds_read_b128 v[132:135],v5 offset:2816
v_exp_f32 v77,v77
v_mul_f32 v78,v34,v73
v_mul_f32 v79,v35,v73
v_mul_f32 v80,v36,v73
v_mfma_f32_32x32x16_bf16 v[48:63],a[40:43],v[168:171],v[48:63]
ds_read_b128 v[136:139],v5 offset:11520
s_add_u32 m0,s64,36864
v_exp_f32 v78,v78
v_exp_f32 v79,v79
v_mfma_f32_32x32x16_bf16 v[48:63],a[44:47],v[172:175],v[48:63]
ds_read_b128 v[140:143],v5 offset:12032
v_exp_f32 v80,v80
v_mul_f32 v81,v37,v73
v_mul_f32 v82,v38,v73
s_waitcnt lgkmcnt(4)
v_mfma_f32_32x32x16_bf16 v[48:63],a[48:51],v[176:179],v[48:63]
buffer_load_dwordx4 v21,s[32:35],s63 offen offset:0 sc1 lds
ds_read_b128 v[144:147],v5 offset:20736
v_exp_f32 v81,v81
v_mfma_f32_32x32x16_bf16 v[48:63],a[52:55],v[180:183],v[48:63]
ds_read_b128 v[148:151],v5 offset:21248
v_exp_f32 v82,v82
v_mul_f32 v83,v39,v73
v_mul_f32 v32,v32,v40
v_mul_f32 v33,v33,v41
v_mfma_f32_32x32x16_bf16 v[48:63],a[56:59],v[184:187],v[48:63]
buffer_load_dwordx4 v21,s[32:35],s63 offen offset:1024 sc1 lds
ds_read_b128 v[152:155],v5 offset:29952
v_exp_f32 v83,v83
v_mfma_f32_32x32x16_bf16 v[48:63],a[60:63],v[188:191],v[48:63]
ds_read_b128 v[156:159],v5 offset:30464
v_mul_f32 v34,v34,v42
v_mul_f32 v35,v35,v43
v_mul_f32 v36,v36,v44
v_mul_f32 v37,v37,v45
s_waitcnt lgkmcnt(4)
v_mfma_f32_32x32x16_bf16 v[48:63],a[64:67],v[128:131],v[48:63]
buffer_load_dwordx4 v21,s[32:35],s63 offen offset:2048 sc1 lds
ds_read_b128 v[160:163],v5 offset:3456
v_mul_f32 v38,v38,v46
v_mul_f32 v39,v39,v47
v_mfma_f32_32x32x16_bf16 v[48:63],a[68:71],v[132:135],v[48:63]
ds_read_b128 v[164:167],v5 offset:3968
v_fma_f32 v76,v76,v74,v74
v_fma_f32 v77,v77,v74,v74
v_rcp_f32 v76,v76
v_fma_f32 v78,v78,v74,v74
v_mfma_f32_32x32x16_bf16 v[48:63],a[72:75],v[136:139],v[48:63]
buffer_load_dwordx4 v21,s[32:35],s63 offen offset:3072 sc1 lds
ds_read_b128 v[168:171],v5 offset:12672
s_add_u32 m0,s64,4096
v_fma_f32 v79,v79,v74,v74
v_mfma_f32_32x32x16_bf16 v[48:63],a[76:79],v[140:143],v[48:63]
ds_read_b128 v[172:175],v5 offset:13184
v_rcp_f32 v77,v77
v_rcp_f32 v78,v78
s_waitcnt lgkmcnt(4)
v_mfma_f32_32x32x16_bf16 v[48:63],a[80:83],v[144:147],v[48:63]
buffer_load_dwordx4 v22,s[32:35],s63 offen offset:0 sc1 lds
ds_read_b128 v[176:179],v5 offset:21888
s_add_u32 m0,s64,40960
v_fma_f32 v80,v80,v74,v74
v_mfma_f32_32x32x16_bf16 v[48:63],a[84:87],v[148:151],v[48:63]
ds_read_b128 v[180:183],v5 offset:22400
v_rcp_f32 v79,v79
v_rcp_f32 v80,v80
v_fma_f32 v81,v81,v74,v74
v_mfma_f32_32x32x16_bf16 v[48:63],a[88:91],v[152:155],v[48:63]
buffer_load_dwordx4 v23,s[32:35],s63 offen offset:0 sc1 lds
ds_read_b128 v[184:187],v5 offset:31104
v_rcp_f32 v81,v81
v_mfma_f32_32x32x16_bf16 v[48:63],a[92:95],v[156:159],v[48:63]
ds_read_b128 v[188:191],v5 offset:31616
v_fma_f32 v82,v82,v74,v74
v_fma_f32 v83,v83,v74,v74
v_rcp_f32 v82,v82
s_waitcnt lgkmcnt(4)
s_waitcnt vmcnt(10)
s_barrier
v_mfma_f32_32x32x16_bf16 v[48:63],a[96:99],v[160:163],v[48:63]
ds_read_b128 v[128:131],v5 offset:4608
v_rcp_f32 v83,v83
v_mul_f32 v32,v32,v76
v_mfma_f32_32x32x16_bf16 v[48:63],a[100:103],v[164:167],v[48:63]
ds_read_b128 v[132:135],v5 offset:5120
v_mul_f32 v33,v33,v77
v_mul_f32 v34,v34,v78
v_mul_f32 v35,v35,v79
v_mul_f32 v36,v36,v80
v_mul_f32 v37,v37,v81
v_mfma_f32_32x32x16_bf16 v[48:63],a[104:107],v[168:171],v[48:63]
ds_read_b128 v[136:139],v5 offset:13824
s_add_u32 m0,s64,5120
v_mul_f32 v38,v38,v82
buffer_load_dwordx4 v24,s[32:35],s63 offen offset:0 sc1 lds
v_mfma_f32_32x32x16_bf16 v[48:63],a[108:111],v[172:175],v[48:63]
ds_read_b128 v[140:143],v5 offset:14336
v_mul_f32 v39,v39,v83
v_cvt_pk_bf16_f32 v84,v32,v33
v_cvt_pk_bf16_f32 v85,v34,v35
v_cvt_pk_bf16_f32 v86,v36,v37
s_waitcnt lgkmcnt(4)
v_mfma_f32_32x32x16_bf16 v[48:63],a[112:115],v[176:179],v[48:63]
ds_read_b128 v[144:147],v5 offset:23040
v_cvt_pk_bf16_f32 v87,v38,v39
ds_read2st64_b32 v[64:65],v8 offset0:0 offset1:36
ds_read2st64_b32 v[66:67],v8 offset0:72 offset1:108
ds_read2st64_b32 v[68:69],v8 offset0:144 offset1:180
ds_read2st64_b32 v[70:71],v8 offset0:216 offset1:252
v_mfma_f32_32x32x16_bf16 v[48:63],a[116:119],v[180:183],v[48:63]
ds_read_b128 v[148:151],v5 offset:23552
v_xor_b32 v8,v9,v8
v_mfma_f32_32x32x16_bf16 v[48:63],a[120:123],v[184:187],v[48:63]
buffer_store_dwordx4 v[84:87],v15,s[32:35],s68 offen sc1
ds_read_b128 v[152:155],v5 offset:32256
v_mfma_f32_32x32x16_bf16 v[48:63],a[124:127],v[188:191],v[48:63]
ds_read_b128 v[156:159],v5 offset:32768
s_waitcnt lgkmcnt(8)
v_mfma_f32_32x32x16_bf16 v[48:63],a[128:131],v[128:131],v[48:63]
buffer_load_dwordx4 v24,s[32:35],s63 offen offset:1024 sc1 lds
ds_read_b128 v[160:163],v5 offset:5760
v_mfma_f32_32x32x16_bf16 v[48:63],a[132:135],v[132:135],v[48:63]
ds_read_b128 v[164:167],v5 offset:6272
s_waitcnt lgkmcnt(5)
v_add_f32 v64,v64,v65
v_add_f32 v66,v66,v67
v_add_f32 v68,v68,v69
v_add_f32 v70,v70,v71
v_mfma_f32_32x32x16_bf16 v[48:63],a[136:139],v[136:139],v[48:63]
buffer_load_dwordx4 v24,s[32:35],s63 offen offset:2048 sc1 lds
ds_read_b128 v[168:171],v5 offset:14976
v_add_f32 v66,v64,v66
v_add_f32 v68,v68,v70
v_mfma_f32_32x32x16_bf16 v[48:63],a[140:143],v[140:143],v[48:63]
ds_read_b128 v[172:175],v5 offset:15488
v_add_f32_dpp v64,v66,v66 row_ror:8 row_mask:0xf bank_mask:0x3
s_waitcnt lgkmcnt(4)
v_mfma_f32_32x32x16_bf16 v[48:63],a[144:147],v[144:147],v[48:63]
buffer_load_dwordx2 v[12:13],v14,s[0:3],s66 offen sc1
ds_read_b128 v[176:179],v5 offset:24192
v_add_f32_dpp v64,v68,v68 row_ror:8 row_mask:0xf bank_mask:0xc
v_mfma_f32_32x32x16_bf16 v[48:63],a[148:151],v[148:151],v[48:63]
ds_read_b128 v[180:183],v5 offset:24704
v_add_f32_dpp v64,v64,v64 row_ror:12 row_mask:0xf bank_mask:0xf
v_mfma_f32_32x32x16_bf16 v[48:63],a[152:155],v[152:155],v[48:63]
buffer_load_dwordx4 v24,s[32:35],s63 offen offset:3072 sc1 lds
ds_read_b128 v[184:187],v5 offset:33408
v_add_f32_dpp v64,v64,v64 quad_perm:[2,3,0,1] row_mask:0xf bank_mask:0xf
v_mfma_f32_32x32x16_bf16 v[48:63],a[156:159],v[156:159],v[48:63]
ds_read_b128 v[188:191],v5 offset:33920
s_add_u32 m0,s64,41984
v_mul_f32 v64,0x3a800000,v64
s_waitcnt lgkmcnt(4)
v_mfma_f32_32x32x16_bf16 v[48:63],a[160:163],v[160:163],v[48:63]
buffer_load_dwordx4 v25,s[32:35],s63 offen offset:0 sc1 lds
ds_read_b128 v[128:131],v5 offset:6912
v_mul_f32 v65,v64,v64
v_mfma_f32_32x32x16_bf16 v[48:63],a[164:167],v[164:167],v[48:63]
ds_read_b128 v[132:135],v5 offset:7424
v_sub_f32_dpp v65,v64,v65 quad_perm:[1,1,3,3] row_mask:0xf bank_mask:0xf
v_mfma_f32_32x32x16_bf16 v[48:63],a[168:171],v[168:171],v[48:63]
ds_read_b128 v[136:139],v5 offset:16128
v_max_f32 v65,0,v65
v_mfma_f32_32x32x16_bf16 v[48:63],a[172:175],v[172:175],v[48:63]
ds_read_b128 v[140:143],v5 offset:16640
v_cvt_pk_bf16_f32 v65,0,v65
s_waitcnt lgkmcnt(4)
v_mfma_f32_32x32x16_bf16 v[48:63],a[176:179],v[176:179],v[48:63]
buffer_load_dwordx4 v25,s[32:35],s63 offen offset:1024 sc1 lds
ds_read_b128 v[144:147],v5 offset:25344
v_add_f32 v65,0x3727c5ac,v65
v_mfma_f32_32x32x16_bf16 v[48:63],a[180:183],v[180:183],v[48:63]
ds_read_b128 v[148:151],v5 offset:25856
v_cvt_pk_bf16_f32 v65,0,v65
v_mfma_f32_32x32x16_bf16 v[48:63],a[184:187],v[184:187],v[48:63]
buffer_load_dwordx4 v25,s[32:35],s63 offen offset:2048 sc1 lds
ds_read_b128 v[152:155],v5 offset:34560
v_sqrt_f32 v65,v65
v_mfma_f32_32x32x16_bf16 v[48:63],a[188:191],v[188:191],v[48:63]
ds_read_b128 v[156:159],v5 offset:35072
v_cvt_pk_bf16_f32 v65,v64,v65
s_waitcnt lgkmcnt(4)
v_mfma_f32_32x32x16_bf16 v[48:63],a[192:195],v[128:131],v[48:63]
ds_read_b128 v[160:163],v5 offset:8064
ds_write_b32 v10,v65 offset:128
v_mfma_f32_32x32x16_bf16 v[48:63],a[196:199],v[132:135],v[48:63]
ds_read_b128 v[164:167],v5 offset:8576
v_mfma_f32_32x32x16_bf16 v[48:63],a[200:203],v[136:139],v[48:63]
buffer_load_dwordx4 v25,s[32:35],s63 offen offset:3072 sc1 lds
ds_read_b128 v[168:171],v5 offset:17280
s_add_u32 s63,s63,s30
v_mfma_f32_32x32x16_bf16 v[48:63],a[204:207],v[140:143],v[48:63]
ds_read_b128 v[172:175],v5 offset:17792
s_waitcnt lgkmcnt(5)
v_mfma_f32_32x32x16_bf16 v[48:63],a[208:211],v[144:147],v[48:63]
ds_read_b128 v[176:179],v5 offset:26496
v_mfma_f32_32x32x16_bf16 v[48:63],a[212:215],v[148:151],v[48:63]
ds_read_b128 v[180:183],v5 offset:27008
v_mfma_f32_32x32x16_bf16 v[48:63],a[216:219],v[152:155],v[48:63]
ds_read_b128 v[184:187],v5 offset:35712
v_mfma_f32_32x32x16_bf16 v[48:63],a[220:223],v[156:159],v[48:63]
ds_read_b128 v[188:191],v5 offset:36224
s_waitcnt lgkmcnt(4)
s_waitcnt vmcnt(8)
s_barrier
v_mfma_f32_32x32x16_bf16 v[48:63],a[224:227],v[160:163],v[48:63]
ds_read_b128 v[128:131],v4 offset:0
v_mfma_f32_32x32x16_bf16 v[48:63],a[228:231],v[164:167],v[48:63]
ds_read_b128 v[132:135],v4 offset:512
ds_read_b32 v74,v11 offset:128
v_mfma_f32_32x32x16_bf16 v[48:63],a[232:235],v[168:171],v[48:63]
ds_read_b128 v[136:139],v4 offset:9216
v_mfma_f32_32x32x16_bf16 v[48:63],a[236:239],v[172:175],v[48:63]
ds_read_b128 v[140:143],v4 offset:9728
s_waitcnt lgkmcnt(5)
v_mfma_f32_32x32x16_bf16 v[48:63],a[240:243],v[176:179],v[48:63]
ds_read_b128 v[144:147],v4 offset:18432
s_waitcnt lgkmcnt(3)
v_lshlrev_b32 v72,16,v74
v_and_b32 v74,0xffff0000,v74
v_mfma_f32_32x32x16_bf16 v[48:63],a[244:247],v[180:183],v[48:63]
ds_read_b128 v[148:151],v4 offset:18944
v_rcp_f32 v73,v74
v_mul_f32 v74,v74,v74
v_mfma_f32_32x32x16_bf16 v[48:63],a[248:251],v[184:187],v[48:63]
ds_read_b128 v[152:155],v4 offset:27648
v_mul_f32 v73,0xbfb8aa3b,v73
s_sub_u32 s62,s62,1
v_mfma_f32_32x32x16_bf16 v[48:63],a[252:255],v[188:191],v[48:63]
ds_read_b128 v[156:159],v4 offset:28160
s_cbranch_scc0 .L_hl_1
s_nop 1
s_add_u32 s68,s68,s30
s_nop 6
v_fma_f32 v48,-v72,v112,v48
v_fma_f32 v49,-v72,v113,v49
v_fma_f32 v50,-v72,v114,v50
v_fma_f32 v51,-v72,v115,v51
v_fma_f32 v52,-v72,v116,v52
v_fma_f32 v53,-v72,v117,v53
v_fma_f32 v54,-v72,v118,v54
v_fma_f32 v55,-v72,v119,v55
v_fma_f32 v56,-v72,v120,v56
v_fma_f32 v57,-v72,v121,v57
v_fma_f32 v58,-v72,v122,v58
v_fma_f32 v59,-v72,v123,v59
v_fma_f32 v60,-v72,v124,v60
v_fma_f32 v61,-v72,v125,v61
v_fma_f32 v62,-v72,v126,v62
v_fma_f32 v63,-v72,v127,v63
v_mul_f32 v76,v48,v73
v_mul_f32 v77,v49,v73
v_mul_f32 v78,v50,v73
v_mul_f32 v79,v51,v73
v_mul_f32 v80,v52,v73
v_mul_f32 v81,v53,v73
v_mul_f32 v82,v54,v73
v_mul_f32 v83,v55,v73
v_exp_f32 v76,v76
v_exp_f32 v77,v77
v_mul_f32 v48,v48,v56
v_mul_f32 v49,v49,v57
v_exp_f32 v78,v78
v_exp_f32 v79,v79
v_mul_f32 v50,v50,v58
v_mul_f32 v51,v51,v59
v_exp_f32 v80,v80
v_exp_f32 v81,v81
v_mul_f32 v52,v52,v60
v_mul_f32 v53,v53,v61
v_exp_f32 v82,v82
v_exp_f32 v83,v83
v_mul_f32 v54,v54,v62
v_mul_f32 v55,v55,v63
v_fma_f32 v76,v76,v74,v74
v_fma_f32 v77,v77,v74,v74
v_rcp_f32 v76,v76
v_rcp_f32 v77,v77
v_fma_f32 v78,v78,v74,v74
v_fma_f32 v79,v79,v74,v74
v_rcp_f32 v78,v78
v_rcp_f32 v79,v79
v_fma_f32 v80,v80,v74,v74
v_fma_f32 v81,v81,v74,v74
v_rcp_f32 v80,v80
v_rcp_f32 v81,v81
v_fma_f32 v82,v82,v74,v74
v_fma_f32 v83,v83,v74,v74
v_rcp_f32 v82,v82
v_rcp_f32 v83,v83
v_mul_f32 v48,v48,v76
v_mul_f32 v49,v49,v77
v_mul_f32 v50,v50,v78
v_mul_f32 v51,v51,v79
v_mul_f32 v52,v52,v80
v_mul_f32 v53,v53,v81
v_mul_f32 v54,v54,v82
v_mul_f32 v55,v55,v83
v_cvt_pk_bf16_f32 v84,v48,v49
v_cvt_pk_bf16_f32 v85,v50,v51
v_cvt_pk_bf16_f32 v86,v52,v53
v_cvt_pk_bf16_f32 v87,v54,v55
buffer_store_dwordx4 v[84:87],v15,s[32:35],s68 offen sc1
s_waitcnt vmcnt(0)
s_barrier
buffer_store_dwordx2 v[16:17],v18,s[32:35],s68 offen sc1
s_cbranch_execz .L_hbk
s_branch .L_end
.p2align 6
.L_hv2:
.L_hl_2:
s_waitcnt lgkmcnt(4)
v_mfma_f32_32x32x16_bf16 v[32:47],a[0:3],v[128:131],0
ds_read_b128 v[160:163],v4 offset:1152
s_waitcnt vmcnt(5)
v_cmp_ne_u64 vcc,v[16:17],v[12:13]
s_add_u32 s68,s68,s30
v_mfma_f32_32x32x16_bf16 v[32:47],a[4:7],v[132:135],v[32:47]
ds_read_b128 v[164:167],v4 offset:1664
s_cbranch_vccnz .L_rth0_2
.L_bkh0_2:
s_add_u32 s66,s66,s30
v_mfma_f32_32x32x16_bf16 v[32:47],a[8:11],v[136:139],v[32:47]
ds_read_b128 v[168:171],v4 offset:10368
s_add_u32 m0,s64,73728
v_fma_f32 v48,-v72,v112,v48
buffer_load_dwordx4 v20,s[32:35],s63 offen offset:0 sc1 lds
v_mfma_f32_32x32x16_bf16 v[32:47],a[12:15],v[140:143],v[32:47]
ds_read_b128 v[172:175],v4 offset:10880
v_fma_f32 v49,-v72,v113,v49
v_fma_f32 v50,-v72,v114,v50
v_fma_f32 v51,-v72,v115,v51
v_fma_f32 v52,-v72,v116,v52
s_waitcnt lgkmcnt(4)
v_mfma_f32_32x32x16_bf16 v[32:47],a[16:19],v[144:147],v[32:47]
buffer_load_dwordx4 v20,s[32:35],s63 offen offset:1024 sc1 lds
ds_read_b128 v[176:179],v4 offset:19584
v_fma_f32 v53,-v72,v117,v53
v_fma_f32 v54,-v72,v118,v54
v_mfma_f32_32x32x16_bf16 v[32:47],a[20:23],v[148:151],v[32:47]
ds_read_b128 v[180:183],v4 offset:20096
v_fma_f32 v55,-v72,v119,v55
v_fma_f32 v56,-v72,v120,v56
v_fma_f32 v57,-v72,v121,v57
v_fma_f32 v58,-v72,v122,v58
v_fma_f32 v59,-v72,v123,v59
v_mfma_f32_32x32x16_bf16 v[32:47],a[24:27],v[152:155],v[32:47]
buffer_load_dwordx4 v20,s[32:35],s63 offen offset:2048 sc1 lds
ds_read_b128 v[184:187],v4 offset:28800
v_fma_f32 v60,-v72,v124,v60
v_fma_f32 v61,-v72,v125,v61
v_mfma_f32_32x32x16_bf16 v[32:47],a[28:31],v[156:159],v[32:47]
ds_read_b128 v[188:191],v4 offset:29312
v_fma_f32 v62,-v72,v126,v62
v_fma_f32 v63,-v72,v127,v63
v_mul_f32 v76,v48,v73
v_mul_f32 v77,v49,v73
s_waitcnt lgkmcnt(4)
v_mfma_f32_32x32x16_bf16 v[32:47],a[32:35],v[160:163],v[32:47]
buffer_load_dwordx4 v20,s[32:35],s63 offen offset:3072 sc1 lds
ds_read_b128 v[128:131],v4 offset:2304
v_exp_f32 v76,v76
v_mfma_f32_32x32x16_bf16 v[32:47],a[36:39],v[164:167],v[32:47]
ds_read_b128 v[132:135],v4 offset:2816
v_exp_f32 v77,v77
v_mul_f32 v78,v50,v73
v_mul_f32 v79,v51,v73
v_mul_f32 v80,v52,v73
v_mfma_f32_32x32x16_bf16 v[32:47],a[40:43],v[168:171],v[32:47]
ds_read_b128 v[136:139],v4 offset:11520
s_add_u32 m0,s64,110592
v_exp_f32 v78,v78
v_exp_f32 v79,v79
v_mfma_f32_32x32x16_bf16 v[32:47],a[44:47],v[172:175],v[32:47]
)#"
R"#(
ds_read_b128 v[140:143],v4 offset:12032
v_exp_f32 v80,v80
v_mul_f32 v81,v53,v73
v_mul_f32 v82,v54,v73
s_waitcnt lgkmcnt(4)
v_mfma_f32_32x32x16_bf16 v[32:47],a[48:51],v[176:179],v[32:47]
buffer_load_dwordx4 v21,s[32:35],s63 offen offset:0 sc1 lds
ds_read_b128 v[144:147],v4 offset:20736
v_exp_f32 v81,v81
v_mfma_f32_32x32x16_bf16 v[32:47],a[52:55],v[180:183],v[32:47]
ds_read_b128 v[148:151],v4 offset:21248
v_exp_f32 v82,v82
v_mul_f32 v83,v55,v73
v_mul_f32 v48,v48,v56
v_mul_f32 v49,v49,v57
v_mfma_f32_32x32x16_bf16 v[32:47],a[56:59],v[184:187],v[32:47]
buffer_load_dwordx4 v21,s[32:35],s63 offen offset:1024 sc1 lds
ds_read_b128 v[152:155],v4 offset:29952
v_exp_f32 v83,v83
v_mfma_f32_32x32x16_bf16 v[32:47],a[60:63],v[188:191],v[32:47]
ds_read_b128 v[156:159],v4 offset:30464
v_mul_f32 v50,v50,v58
v_mul_f32 v51,v51,v59
v_mul_f32 v52,v52,v60
v_mul_f32 v53,v53,v61
s_waitcnt lgkmcnt(4)
v_mfma_f32_32x32x16_bf16 v[32:47],a[64:67],v[128:131],v[32:47]
buffer_load_dwordx4 v21,s[32:35],s63 offen offset:2048 sc1 lds
ds_read_b128 v[160:163],v4 offset:3456
v_mul_f32 v54,v54,v62
v_mul_f32 v55,v55,v63
v_mfma_f32_32x32x16_bf16 v[32:47],a[68:71],v[132:135],v[32:47]
ds_read_b128 v[164:167],v4 offset:3968
v_fma_f32 v76,v76,v74,v74
v_fma_f32 v77,v77,v74,v74
v_rcp_f32 v76,v76
v_fma_f32 v78,v78,v74,v74
v_mfma_f32_32x32x16_bf16 v[32:47],a[72:75],v[136:139],v[32:47]
buffer_load_dwordx4 v21,s[32:35],s63 offen offset:3072 sc1 lds
ds_read_b128 v[168:171],v4 offset:12672
s_add_u32 m0,s64,77824
v_fma_f32 v79,v79,v74,v74
v_mfma_f32_32x32x16_bf16 v[32:47],a[76:79],v[140:143],v[32:47]
ds_read_b128 v[172:175],v4 offset:13184
v_rcp_f32 v77,v77
v_rcp_f32 v78,v78
s_waitcnt lgkmcnt(4)
v_mfma_f32_32x32x16_bf16 v[32:47],a[80:83],v[144:147],v[32:47]
buffer_load_dwordx4 v22,s[32:35],s63 offen offset:0 sc1 lds
ds_read_b128 v[176:179],v4 offset:21888
s_add_u32 m0,s64,114688
v_fma_f32 v80,v80,v74,v74
v_mfma_f32_32x32x16_bf16 v[32:47],a[84:87],v[148:151],v[32:47]
ds_read_b128 v[180:183],v4 offset:22400
v_rcp_f32 v79,v79
v_rcp_f32 v80,v80
v_fma_f32 v81,v81,v74,v74
v_mfma_f32_32x32x16_bf16 v[32:47],a[88:91],v[152:155],v[32:47]
buffer_load_dwordx4 v23,s[32:35],s63 offen offset:0 sc1 lds
ds_read_b128 v[184:187],v4 offset:31104
v_rcp_f32 v81,v81
s_waitcnt vmcnt(10)
s_barrier
v_mfma_f32_32x32x16_bf16 v[32:47],a[92:95],v[156:159],v[32:47]
ds_read_b128 v[188:191],v4 offset:31616
v_fma_f32 v82,v82,v74,v74
v_fma_f32 v83,v83,v74,v74
v_rcp_f32 v82,v82
s_waitcnt lgkmcnt(4)
v_mfma_f32_32x32x16_bf16 v[32:47],a[96:99],v[160:163],v[32:47]
ds_read_b128 v[128:131],v4 offset:4608
v_rcp_f32 v83,v83
v_mul_f32 v48,v48,v76
v_mfma_f32_32x32x16_bf16 v[32:47],a[100:103],v[164:167],v[32:47]
ds_read_b128 v[132:135],v4 offset:5120
v_mul_f32 v49,v49,v77
v_mul_f32 v50,v50,v78
v_mul_f32 v51,v51,v79
v_mul_f32 v52,v52,v80
v_mul_f32 v53,v53,v81
v_mfma_f32_32x32x16_bf16 v[32:47],a[104:107],v[168:171],v[32:47]
ds_read_b128 v[136:139],v4 offset:13824
s_add_u32 m0,s64,78848
v_mul_f32 v54,v54,v82
buffer_load_dwordx4 v24,s[32:35],s63 offen offset:0 sc1 lds
v_mfma_f32_32x32x16_bf16 v[32:47],a[108:111],v[172:175],v[32:47]
ds_read_b128 v[140:143],v4 offset:14336
v_mul_f32 v55,v55,v83
v_cvt_pk_bf16_f32 v84,v48,v49
v_cvt_pk_bf16_f32 v85,v50,v51
v_cvt_pk_bf16_f32 v86,v52,v53
s_waitcnt lgkmcnt(4)
v_mfma_f32_32x32x16_bf16 v[32:47],a[112:115],v[176:179],v[32:47]
ds_read_b128 v[144:147],v4 offset:23040
v_cvt_pk_bf16_f32 v87,v54,v55
ds_read2st64_b32 v[64:65],v8 offset0:0 offset1:36
ds_read2st64_b32 v[66:67],v8 offset0:72 offset1:108
ds_read2st64_b32 v[68:69],v8 offset0:144 offset1:180
ds_read2st64_b32 v[70:71],v8 offset0:216 offset1:252
v_mfma_f32_32x32x16_bf16 v[32:47],a[116:119],v[180:183],v[32:47]
ds_read_b128 v[148:151],v4 offset:23552
v_xor_b32 v8,v9,v8
v_mfma_f32_32x32x16_bf16 v[32:47],a[120:123],v[184:187],v[32:47]
buffer_store_dwordx4 v[84:87],v15,s[32:35],s68 offen sc1
ds_read_b128 v[152:155],v4 offset:32256
v_mfma_f32_32x32x16_bf16 v[32:47],a[124:127],v[188:191],v[32:47]
ds_read_b128 v[156:159],v4 offset:32768
s_waitcnt lgkmcnt(8)
v_mfma_f32_32x32x16_bf16 v[32:47],a[128:131],v[128:131],v[32:47]
buffer_load_dwordx4 v24,s[32:35],s63 offen offset:1024 sc1 lds
ds_read_b128 v[160:163],v4 offset:5760
v_mfma_f32_32x32x16_bf16 v[32:47],a[132:135],v[132:135],v[32:47]
ds_read_b128 v[164:167],v4 offset:6272
s_waitcnt lgkmcnt(5)
v_add_f32 v64,v64,v65
v_add_f32 v66,v66,v67
v_add_f32 v68,v68,v69
v_add_f32 v70,v70,v71
v_mfma_f32_32x32x16_bf16 v[32:47],a[136:139],v[136:139],v[32:47]
buffer_load_dwordx4 v24,s[32:35],s63 offen offset:2048 sc1 lds
ds_read_b128 v[168:171],v4 offset:14976
v_add_f32 v66,v64,v66
v_add_f32 v68,v68,v70
v_mfma_f32_32x32x16_bf16 v[32:47],a[140:143],v[140:143],v[32:47]
ds_read_b128 v[172:175],v4 offset:15488
v_add_f32_dpp v64,v66,v66 row_ror:8 row_mask:0xf bank_mask:0x3
s_waitcnt lgkmcnt(4)
v_mfma_f32_32x32x16_bf16 v[32:47],a[144:147],v[144:147],v[32:47]
buffer_load_dwordx2 v[12:13],v14,s[0:3],s66 offen sc1
ds_read_b128 v[176:179],v4 offset:24192
v_add_f32_dpp v64,v68,v68 row_ror:8 row_mask:0xf bank_mask:0xc
v_mfma_f32_32x32x16_bf16 v[32:47],a[148:151],v[148:151],v[32:47]
ds_read_b128 v[180:183],v4 offset:24704
v_add_f32_dpp v64,v64,v64 row_ror:12 row_mask:0xf bank_mask:0xf
v_mfma_f32_32x32x16_bf16 v[32:47],a[152:155],v[152:155],v[32:47]
buffer_load_dwordx4 v24,s[32:35],s63 offen offset:3072 sc1 lds
ds_read_b128 v[184:187],v4 offset:33408
v_add_f32_dpp v64,v64,v64 quad_perm:[2,3,0,1] row_mask:0xf bank_mask:0xf
v_mfma_f32_32x32x16_bf16 v[32:47],a[156:159],v[156:159],v[32:47]
ds_read_b128 v[188:191],v4 offset:33920
s_add_u32 m0,s64,115712
v_mul_f32 v64,0x3a800000,v64
s_waitcnt lgkmcnt(4)
v_mfma_f32_32x32x16_bf16 v[32:47],a[160:163],v[160:163],v[32:47]
buffer_load_dwordx4 v25,s[32:35],s63 offen offset:0 sc1 lds
ds_read_b128 v[128:131],v4 offset:6912
v_mul_f32 v65,v64,v64
v_mfma_f32_32x32x16_bf16 v[32:47],a[164:167],v[164:167],v[32:47]
ds_read_b128 v[132:135],v4 offset:7424
v_sub_f32_dpp v65,v64,v65 quad_perm:[1,1,3,3] row_mask:0xf bank_mask:0xf
v_mfma_f32_32x32x16_bf16 v[32:47],a[168:171],v[168:171],v[32:47]
ds_read_b128 v[136:139],v4 offset:16128
v_max_f32 v65,0,v65
v_mfma_f32_32x32x16_bf16 v[32:47],a[172:175],v[172:175],v[32:47]
ds_read_b128 v[140:143],v4 offset:16640
v_cvt_pk_bf16_f32 v65,0,v65
s_waitcnt lgkmcnt(4)
v_mfma_f32_32x32x16_bf16 v[32:47],a[176:179],v[176:179],v[32:47]
buffer_load_dwordx4 v25,s[32:35],s63 offen offset:1024 sc1 lds
ds_read_b128 v[144:147],v4 offset:25344
v_add_f32 v65,0x3727c5ac,v65
v_mfma_f32_32x32x16_bf16 v[32:47],a[180:183],v[180:183],v[32:47]
ds_read_b128 v[148:151],v4 offset:25856
v_cvt_pk_bf16_f32 v65,0,v65
v_mfma_f32_32x32x16_bf16 v[32:47],a[184:187],v[184:187],v[32:47]
buffer_load_dwordx4 v25,s[32:35],s63 offen offset:2048 sc1 lds
ds_read_b128 v[152:155],v4 offset:34560
v_sqrt_f32 v65,v65
v_mfma_f32_32x32x16_bf16 v[32:47],a[188:191],v[188:191],v[32:47]
ds_read_b128 v[156:159],v4 offset:35072
v_cvt_pk_bf16_f32 v65,v64,v65
s_waitcnt lgkmcnt(4)
v_mfma_f32_32x32x16_bf16 v[32:47],a[192:195],v[128:131],v[32:47]
ds_read_b128 v[160:163],v4 offset:8064
ds_write_b32 v10,v65 offset:0
v_mfma_f32_32x32x16_bf16 v[32:47],a[196:199],v[132:135],v[32:47]
ds_read_b128 v[164:167],v4 offset:8576
v_mfma_f32_32x32x16_bf16 v[32:47],a[200:203],v[136:139],v[32:47]
buffer_load_dwordx4 v25,s[32:35],s63 offen offset:3072 sc1 lds
ds_read_b128 v[168:171],v4 offset:17280
s_add_u32 s63,s63,s30
v_mfma_f32_32x32x16_bf16 v[32:47],a[204:207],v[140:143],v[32:47]
ds_read_b128 v[172:175],v4 offset:17792
s_waitcnt lgkmcnt(5)
v_mfma_f32_32x32x16_bf16 v[32:47],a[208:211],v[144:147],v[32:47]
ds_read_b128 v[176:179],v4 offset:26496
v_mfma_f32_32x32x16_bf16 v[32:47],a[212:215],v[148:151],v[32:47]
ds_read_b128 v[180:183],v4 offset:27008
v_mfma_f32_32x32x16_bf16 v[32:47],a[216:219],v[152:155],v[32:47]
ds_read_b128 v[184:187],v4 offset:35712
s_waitcnt vmcnt(8)
s_waitcnt lgkmcnt(6)
s_barrier
v_mfma_f32_32x32x16_bf16 v[32:47],a[220:223],v[156:159],v[32:47]
ds_read_b128 v[188:191],v4 offset:36224
s_waitcnt lgkmcnt(4)
v_mfma_f32_32x32x16_bf16 v[32:47],a[224:227],v[160:163],v[32:47]
ds_read_b128 v[128:131],v5 offset:0
v_mfma_f32_32x32x16_bf16 v[32:47],a[228:231],v[164:167],v[32:47]
ds_read_b128 v[132:135],v5 offset:512
ds_read_b32 v74,v11 offset:0
v_mfma_f32_32x32x16_bf16 v[32:47],a[232:235],v[168:171],v[32:47]
ds_read_b128 v[136:139],v5 offset:9216
v_mfma_f32_32x32x16_bf16 v[32:47],a[236:239],v[172:175],v[32:47]
ds_read_b128 v[140:143],v5 offset:9728
s_waitcnt lgkmcnt(5)
v_mfma_f32_32x32x16_bf16 v[32:47],a[240:243],v[176:179],v[32:47]
ds_read_b128 v[144:147],v5 offset:18432
s_waitcnt lgkmcnt(3)
v_lshlrev_b32 v72,16,v74
v_and_b32 v74,0xffff0000,v74
v_mfma_f32_32x32x16_bf16 v[32:47],a[244:247],v[180:183],v[32:47]
ds_read_b128 v[148:151],v5 offset:18944
v_rcp_f32 v73,v74
v_mul_f32 v74,v74,v74
v_mfma_f32_32x32x16_bf16 v[32:47],a[248:251],v[184:187],v[32:47]
ds_read_b128 v[152:155],v5 offset:27648
v_mul_f32 v73,0xbfb8aa3b,v73
v_mfma_f32_32x32x16_bf16 v[32:47],a[252:255],v[188:191],v[32:47]
ds_read_b128 v[156:159],v5 offset:28160
s_waitcnt lgkmcnt(4)
v_mfma_f32_32x32x16_bf16 v[48:63],a[0:3],v[128:131],0
ds_read_b128 v[160:163],v5 offset:1152
s_waitcnt vmcnt(5)
v_cmp_ne_u64 vcc,v[16:17],v[12:13]
s_add_u32 s68,s68,s30
v_mfma_f32_32x32x16_bf16 v[48:63],a[4:7],v[132:135],v[48:63]
ds_read_b128 v[164:167],v5 offset:1664
s_cbranch_vccnz .L_rth1_2
.L_bkh1_2:
s_add_u32 s66,s66,s30
v_mfma_f32_32x32x16_bf16 v[48:63],a[8:11],v[136:139],v[48:63]
ds_read_b128 v[168:171],v5 offset:10368
s_add_u32 m0,s64,0
v_fma_f32 v32,-v72,v112,v32
buffer_load_dwordx4 v20,s[32:35],s63 offen offset:0 sc1 lds
v_mfma_f32_32x32x16_bf16 v[48:63],a[12:15],v[140:143],v[48:63]
ds_read_b128 v[172:175],v5 offset:10880
v_fma_f32 v33,-v72,v113,v33
v_fma_f32 v34,-v72,v114,v34
v_fma_f32 v35,-v72,v115,v35
v_fma_f32 v36,-v72,v116,v36
s_waitcnt lgkmcnt(4)
v_mfma_f32_32x32x16_bf16 v[48:63],a[16:19],v[144:147],v[48:63]
buffer_load_dwordx4 v20,s[32:35],s63 offen offset:1024 sc1 lds
ds_read_b128 v[176:179],v5 offset:19584
v_fma_f32 v37,-v72,v117,v37
v_fma_f32 v38,-v72,v118,v38
v_mfma_f32_32x32x16_bf16 v[48:63],a[20:23],v[148:151],v[48:63]
ds_read_b128 v[180:183],v5 offset:20096
v_fma_f32 v39,-v72,v119,v39
v_fma_f32 v40,-v72,v120,v40
v_fma_f32 v41,-v72,v121,v41
v_fma_f32 v42,-v72,v122,v42
v_fma_f32 v43,-v72,v123,v43
v_mfma_f32_32x32x16_bf16 v[48:63],a[24:27],v[152:155],v[48:63]
buffer_load_dwordx4 v20,s[32:35],s63 offen offset:2048 sc1 lds
ds_read_b128 v[184:187],v5 offset:28800
v_fma_f32 v44,-v72,v124,v44
v_fma_f32 v45,-v72,v125,v45
v_mfma_f32_32x32x16_bf16 v[48:63],a[28:31],v[156:159],v[48:63]
ds_read_b128 v[188:191],v5 offset:29312
v_fma_f32 v46,-v72,v126,v46
v_fma_f32 v47,-v72,v127,v47
v_mul_f32 v76,v32,v73
v_mul_f32 v77,v33,v73
s_waitcnt lgkmcnt(4)
v_mfma_f32_32x32x16_bf16 v[48:63],a[32:35],v[160:163],v[48:63]
buffer_load_dwordx4 v20,s[32:35],s63 offen offset:3072 sc1 lds
ds_read_b128 v[128:131],v5 offset:2304
v_exp_f32 v76,v76
v_mfma_f32_32x32x16_bf16 v[48:63],a[36:39],v[164:167],v[48:63]
ds_read_b128 v[132:135],v5 offset:2816
v_exp_f32 v77,v77
v_mul_f32 v78,v34,v73
v_mul_f32 v79,v35,v73
v_mul_f32 v80,v36,v73
v_mfma_f32_32x32x16_bf16 v[48:63],a[40:43],v[168:171],v[48:63]
ds_read_b128 v[136:139],v5 offset:11520
s_add_u32 m0,s64,36864
v_exp_f32 v78,v78
v_exp_f32 v79,v79
v_mfma_f32_32x32x16_bf16 v[48:63],a[44:47],v[172:175],v[48:63]
ds_read_b128 v[140:143],v5 offset:12032
v_exp_f32 v80,v80
v_mul_f32 v81,v37,v73
v_mul_f32 v82,v38,v73
s_waitcnt lgkmcnt(4)
v_mfma_f32_32x32x16_bf16 v[48:63],a[48:51],v[176:179],v[48:63]
buffer_load_dwordx4 v21,s[32:35],s63 offen offset:0 sc1 lds
ds_read_b128 v[144:147],v5 offset:20736
v_exp_f32 v81,v81
v_mfma_f32_32x32x16_bf16 v[48:63],a[52:55],v[180:183],v[48:63]
ds_read_b128 v[148:151],v5 offset:21248
v_exp_f32 v82,v82
v_mul_f32 v83,v39,v73
v_mul_f32 v32,v32,v40
v_mul_f32 v33,v33,v41
v_mfma_f32_32x32x16_bf16 v[48:63],a[56:59],v[184:187],v[48:63]
buffer_load_dwordx4 v21,s[32:35],s63 offen offset:1024 sc1 lds
ds_read_b128 v[152:155],v5 offset:29952
v_exp_f32 v83,v83
v_mfma_f32_32x32x16_bf16 v[48:63],a[60:63],v[188:191],v[48:63]
ds_read_b128 v[156:159],v5 offset:30464
v_mul_f32 v34,v34,v42
v_mul_f32 v35,v35,v43
v_mul_f32 v36,v36,v44
v_mul_f32 v37,v37,v45
s_waitcnt lgkmcnt(4)
v_mfma_f32_32x32x16_bf16 v[48:63],a[64:67],v[128:131],v[48:63]
buffer_load_dwordx4 v21,s[32:35],s63 offen offset:2048 sc1 lds
ds_read_b128 v[160:163],v5 offset:3456
v_mul_f32 v38,v38,v46
v_mul_f32 v39,v39,v47
v_mfma_f32_32x32x16_bf16 v[48:63],a[68:71],v[132:135],v[48:63]
ds_read_b128 v[164:167],v5 offset:3968
v_fma_f32 v76,v76,v74,v74
v_fma_f32 v77,v77,v74,v74
v_rcp_f32 v76,v76
v_fma_f32 v78,v78,v74,v74
v_mfma_f32_32x32x16_bf16 v[48:63],a[72:75],v[136:139],v[48:63]
buffer_load_dwordx4 v21,s[32:35],s63 offen offset:3072 sc1 lds
ds_read_b128 v[168:171],v5 offset:12672
s_add_u32 m0,s64,4096
v_fma_f32 v79,v79,v74,v74
v_mfma_f32_32x32x16_bf16 v[48:63],a[76:79],v[140:143],v[48:63]
ds_read_b128 v[172:175],v5 offset:13184
v_rcp_f32 v77,v77
v_rcp_f32 v78,v78
s_waitcnt lgkmcnt(4)
v_mfma_f32_32x32x16_bf16 v[48:63],a[80:83],v[144:147],v[48:63]
buffer_load_dwordx4 v22,s[32:35],s63 offen offset:0 sc1 lds
ds_read_b128 v[176:179],v5 offset:21888
s_add_u32 m0,s64,40960
v_fma_f32 v80,v80,v74,v74
v_mfma_f32_32x32x16_bf16 v[48:63],a[84:87],v[148:151],v[48:63]
ds_read_b128 v[180:183],v5 offset:22400
v_rcp_f32 v79,v79
v_rcp_f32 v80,v80
v_fma_f32 v81,v81,v74,v74
v_mfma_f32_32x32x16_bf16 v[48:63],a[88:91],v[152:155],v[48:63]
buffer_load_dwordx4 v23,s[32:35],s63 offen offset:0 sc1 lds
ds_read_b128 v[184:187],v5 offset:31104
v_rcp_f32 v81,v81
s_waitcnt vmcnt(10)
s_barrier
v_mfma_f32_32x32x16_bf16 v[48:63],a[92:95],v[156:159],v[48:63]
ds_read_b128 v[188:191],v5 offset:31616
v_fma_f32 v82,v82,v74,v74
v_fma_f32 v83,v83,v74,v74
v_rcp_f32 v82,v82
s_waitcnt lgkmcnt(4)
v_mfma_f32_32x32x16_bf16 v[48:63],a[96:99],v[160:163],v[48:63]
ds_read_b128 v[128:131],v5 offset:4608
v_rcp_f32 v83,v83
v_mul_f32 v32,v32,v76
v_mfma_f32_32x32x16_bf16 v[48:63],a[100:103],v[164:167],v[48:63]
ds_read_b128 v[132:135],v5 offset:5120
v_mul_f32 v33,v33,v77
v_mul_f32 v34,v34,v78
v_mul_f32 v35,v35,v79
v_mul_f32 v36,v36,v80
v_mul_f32 v37,v37,v81
v_mfma_f32_32x32x16_bf16 v[48:63],a[104:107],v[168:171],v[48:63]
ds_read_b128 v[136:139],v5 offset:13824
s_add_u32 m0,s64,5120
v_mul_f32 v38,v38,v82
buffer_load_dwordx4 v24,s[32:35],s63 offen offset:0 sc1 lds
v_mfma_f32_32x32x16_bf16 v[48:63],a[108:111],v[172:175],v[48:63]
ds_read_b128 v[140:143],v5 offset:14336
v_mul_f32 v39,v39,v83
v_cvt_pk_bf16_f32 v84,v32,v33
v_cvt_pk_bf16_f32 v85,v34,v35
v_cvt_pk_bf16_f32 v86,v36,v37
s_waitcnt lgkmcnt(4)
v_mfma_f32_32x32x16_bf16 v[48:63],a[112:115],v[176:179],v[48:63]
ds_read_b128 v[144:147],v5 offset:23040
v_cvt_pk_bf16_f32 v87,v38,v39
ds_read2st64_b32 v[64:65],v8 offset0:0 offset1:36
ds_read2st64_b32 v[66:67],v8 offset0:72 offset1:108
ds_read2st64_b32 v[68:69],v8 offset0:144 offset1:180
ds_read2st64_b32 v[70:71],v8 offset0:216 offset1:252
v_mfma_f32_32x32x16_bf16 v[48:63],a[116:119],v[180:183],v[48:63]
ds_read_b128 v[148:151],v5 offset:23552
v_xor_b32 v8,v9,v8
v_mfma_f32_32x32x16_bf16 v[48:63],a[120:123],v[184:187],v[48:63]
buffer_store_dwordx4 v[84:87],v15,s[32:35],s68 offen sc1
ds_read_b128 v[152:155],v5 offset:32256
v_mfma_f32_32x32x16_bf16 v[48:63],a[124:127],v[188:191],v[48:63]
ds_read_b128 v[156:159],v5 offset:32768
s_waitcnt lgkmcnt(8)
v_mfma_f32_32x32x16_bf16 v[48:63],a[128:131],v[128:131],v[48:63]
buffer_load_dwordx4 v24,s[32:35],s63 offen offset:1024 sc1 lds
ds_read_b128 v[160:163],v5 offset:5760
v_mfma_f32_32x32x16_bf16 v[48:63],a[132:135],v[132:135],v[48:63]
ds_read_b128 v[164:167],v5 offset:6272
s_waitcnt lgkmcnt(5)
v_add_f32 v64,v64,v65
v_add_f32 v66,v66,v67
v_add_f32 v68,v68,v69
v_add_f32 v70,v70,v71
v_mfma_f32_32x32x16_bf16 v[48:63],a[136:139],v[136:139],v[48:63]
buffer_load_dwordx4 v24,s[32:35],s63 offen offset:2048 sc1 lds
ds_read_b128 v[168:171],v5 offset:14976
v_add_f32 v66,v64,v66
v_add_f32 v68,v68,v70
v_mfma_f32_32x32x16_bf16 v[48:63],a[140:143],v[140:143],v[48:63]
ds_read_b128 v[172:175],v5 offset:15488
v_add_f32_dpp v64,v66,v66 row_ror:8 row_mask:0xf bank_mask:0x3
s_waitcnt lgkmcnt(4)
v_mfma_f32_32x32x16_bf16 v[48:63],a[144:147],v[144:147],v[48:63]
buffer_load_dwordx2 v[12:13],v14,s[0:3],s66 offen sc1
ds_read_b128 v[176:179],v5 offset:24192
v_add_f32_dpp v64,v68,v68 row_ror:8 row_mask:0xf bank_mask:0xc
v_mfma_f32_32x32x16_bf16 v[48:63],a[148:151],v[148:151],v[48:63]
ds_read_b128 v[180:183],v5 offset:24704
v_add_f32_dpp v64,v64,v64 row_ror:12 row_mask:0xf bank_mask:0xf
v_mfma_f32_32x32x16_bf16 v[48:63],a[152:155],v[152:155],v[48:63]
buffer_load_dwordx4 v24,s[32:35],s63 offen offset:3072 sc1 lds
ds_read_b128 v[184:187],v5 offset:33408
v_add_f32_dpp v64,v64,v64 quad_perm:[2,3,0,1] row_mask:0xf bank_mask:0xf
v_mfma_f32_32x32x16_bf16 v[48:63],a[156:159],v[156:159],v[48:63]
ds_read_b128 v[188:191],v5 offset:33920
s_add_u32 m0,s64,41984
v_mul_f32 v64,0x3a800000,v64
s_waitcnt lgkmcnt(4)
v_mfma_f32_32x32x16_bf16 v[48:63],a[160:163],v[160:163],v[48:63]
buffer_load_dwordx4 v25,s[32:35],s63 offen offset:0 sc1 lds
ds_read_b128 v[128:131],v5 offset:6912
v_mul_f32 v65,v64,v64
v_mfma_f32_32x32x16_bf16 v[48:63],a[164:167],v[164:167],v[48:63]
ds_read_b128 v[132:135],v5 offset:7424
v_sub_f32_dpp v65,v64,v65 quad_perm:[1,1,3,3] row_mask:0xf bank_mask:0xf
v_mfma_f32_32x32x16_bf16 v[48:63],a[168:171],v[168:171],v[48:63]
ds_read_b128 v[136:139],v5 offset:16128
v_max_f32 v65,0,v65
v_mfma_f32_32x32x16_bf16 v[48:63],a[172:175],v[172:175],v[48:63]
ds_read_b128 v[140:143],v5 offset:16640
v_cvt_pk_bf16_f32 v65,0,v65
s_waitcnt lgkmcnt(4)
v_mfma_f32_32x32x16_bf16 v[48:63],a[176:179],v[176:179],v[48:63]
buffer_load_dwordx4 v25,s[32:35],s63 offen offset:1024 sc1 lds
ds_read_b128 v[144:147],v5 offset:25344
v_add_f32 v65,0x3727c5ac,v65
v_mfma_f32_32x32x16_bf16 v[48:63],a[180:183],v[180:183],v[48:63]
ds_read_b128 v[148:151],v5 offset:25856
v_cvt_pk_bf16_f32 v65,0,v65
v_mfma_f32_32x32x16_bf16 v[48:63],a[184:187],v[184:187],v[48:63]
buffer_load_dwordx4 v25,s[32:35],s63 offen offset:2048 sc1 lds
ds_read_b128 v[152:155],v5 offset:34560
v_sqrt_f32 v65,v65
v_mfma_f32_32x32x16_bf16 v[48:63],a[188:191],v[188:191],v[48:63]
ds_read_b128 v[156:159],v5 offset:35072
v_cvt_pk_bf16_f32 v65,v64,v65
s_waitcnt lgkmcnt(4)
v_mfma_f32_32x32x16_bf16 v[48:63],a[192:195],v[128:131],v[48:63]
ds_read_b128 v[160:163],v5 offset:8064
ds_write_b32 v10,v65 offset:128
v_mfma_f32_32x32x16_bf16 v[48:63],a[196:199],v[132:135],v[48:63]
ds_read_b128 v[164:167],v5 offset:8576
v_mfma_f32_32x32x16_bf16 v[48:63],a[200:203],v[136:139],v[48:63]
buffer_load_dwordx4 v25,s[32:35],s63 offen offset:3072 sc1 lds
ds_read_b128 v[168:171],v5 offset:17280
s_add_u32 s63,s63,s30
v_mfma_f32_32x32x16_bf16 v[48:63],a[204:207],v[140:143],v[48:63]
ds_read_b128 v[172:175],v5 offset:17792
s_waitcnt lgkmcnt(5)
v_mfma_f32_32x32x16_bf16 v[48:63],a[208:211],v[144:147],v[48:63]
ds_read_b128 v[176:179],v5 offset:26496
v_mfma_f32_32x32x16_bf16 v[48:63],a[212:215],v[148:151],v[48:63]
ds_read_b128 v[180:183],v5 offset:27008
v_mfma_f32_32x32x16_bf16 v[48:63],a[216:219],v[152:155],v[48:63]
ds_read_b128 v[184:187],v5 offset:35712
s_waitcnt vmcnt(8)
s_waitcnt lgkmcnt(6)
s_barrier
v_mfma_f32_32x32x16_bf16 v[48:63],a[220:223],v[156:159],v[48:63]
ds_read_b128 v[188:191],v5 offset:36224
s_waitcnt lgkmcnt(4)
v_mfma_f32_32x32x16_bf16 v[48:63],a[224:227],v[160:163],v[48:63]
ds_read_b128 v[128:131],v4 offset:0
v_mfma_f32_32x32x16_bf16 v[48:63],a[228:231],v[164:167],v[48:63]
ds_read_b128 v[132:135],v4 offset:512
ds_read_b32 v74,v11 offset:128
v_mfma_f32_32x32x16_bf16 v[48:63],a[232:235],v[168:171],v[48:63]
ds_read_b128 v[136:139],v4 offset:9216
v_mfma_f32_32x32x16_bf16 v[48:63],a[236:239],v[172:175],v[48:63]
ds_read_b128 v[140:143],v4 offset:9728
s_waitcnt lgkmcnt(5)
v_mfma_f32_32x32x16_bf16 v[48:63],a[240:243],v[176:179],v[48:63]
ds_read_b128 v[144:147],v4 offset:18432
s_waitcnt lgkmcnt(3)
v_lshlrev_b32 v72,16,v74
v_and_b32 v74,0xffff0000,v74
v_mfma_f32_32x32x16_bf16 v[48:63],a[244:247],v[180:183],v[48:63]
ds_read_b128 v[148:151],v4 offset:18944
v_rcp_f32 v73,v74
v_mul_f32 v74,v74,v74
v_mfma_f32_32x32x16_bf16 v[48:63],a[248:251],v[184:187],v[48:63]
ds_read_b128 v[152:155],v4 offset:27648
)#"
R"#(
v_mul_f32 v73,0xbfb8aa3b,v73
s_sub_u32 s62,s62,1
v_mfma_f32_32x32x16_bf16 v[48:63],a[252:255],v[188:191],v[48:63]
ds_read_b128 v[156:159],v4 offset:28160
s_cbranch_scc0 .L_hl_2
s_nop 1
s_add_u32 s68,s68,s30
s_nop 6
v_fma_f32 v48,-v72,v112,v48
v_fma_f32 v49,-v72,v113,v49
v_fma_f32 v50,-v72,v114,v50
v_fma_f32 v51,-v72,v115,v51
v_fma_f32 v52,-v72,v116,v52
v_fma_f32 v53,-v72,v117,v53
v_fma_f32 v54,-v72,v118,v54
v_fma_f32 v55,-v72,v119,v55
v_fma_f32 v56,-v72,v120,v56
v_fma_f32 v57,-v72,v121,v57
v_fma_f32 v58,-v72,v122,v58
v_fma_f32 v59,-v72,v123,v59
v_fma_f32 v60,-v72,v124,v60
v_fma_f32 v61,-v72,v125,v61
v_fma_f32 v62,-v72,v126,v62
v_fma_f32 v63,-v72,v127,v63
v_mul_f32 v76,v48,v73
v_mul_f32 v77,v49,v73
v_mul_f32 v78,v50,v73
v_mul_f32 v79,v51,v73
v_mul_f32 v80,v52,v73
v_mul_f32 v81,v53,v73
v_mul_f32 v82,v54,v73
v_mul_f32 v83,v55,v73
v_exp_f32 v76,v76
v_exp_f32 v77,v77
v_mul_f32 v48,v48,v56
v_mul_f32 v49,v49,v57
v_exp_f32 v78,v78
v_exp_f32 v79,v79
v_mul_f32 v50,v50,v58
v_mul_f32 v51,v51,v59
v_exp_f32 v80,v80
v_exp_f32 v81,v81
v_mul_f32 v52,v52,v60
v_mul_f32 v53,v53,v61
v_exp_f32 v82,v82
v_exp_f32 v83,v83
v_mul_f32 v54,v54,v62
v_mul_f32 v55,v55,v63
v_fma_f32 v76,v76,v74,v74
v_fma_f32 v77,v77,v74,v74
v_rcp_f32 v76,v76
v_rcp_f32 v77,v77
v_fma_f32 v78,v78,v74,v74
v_fma_f32 v79,v79,v74,v74
v_rcp_f32 v78,v78
v_rcp_f32 v79,v79
v_fma_f32 v80,v80,v74,v74
v_fma_f32 v81,v81,v74,v74
v_rcp_f32 v80,v80
v_rcp_f32 v81,v81
v_fma_f32 v82,v82,v74,v74
v_fma_f32 v83,v83,v74,v74
v_rcp_f32 v82,v82
v_rcp_f32 v83,v83
v_mul_f32 v48,v48,v76
v_mul_f32 v49,v49,v77
v_mul_f32 v50,v50,v78
v_mul_f32 v51,v51,v79
v_mul_f32 v52,v52,v80
v_mul_f32 v53,v53,v81
v_mul_f32 v54,v54,v82
v_mul_f32 v55,v55,v83
v_cvt_pk_bf16_f32 v84,v48,v49
v_cvt_pk_bf16_f32 v85,v50,v51
v_cvt_pk_bf16_f32 v86,v52,v53
v_cvt_pk_bf16_f32 v87,v54,v55
buffer_store_dwordx4 v[84:87],v15,s[32:35],s68 offen sc1
s_waitcnt vmcnt(0)
s_barrier
buffer_store_dwordx2 v[16:17],v18,s[32:35],s68 offen sc1
s_cbranch_execz .L_hbk
s_branch .L_end
.p2align 6
.L_hv3:
.L_hl_3:
s_waitcnt lgkmcnt(4)
v_mfma_f32_32x32x16_bf16 v[32:47],a[0:3],v[128:131],0
ds_read_b128 v[160:163],v4 offset:1152
s_waitcnt vmcnt(5)
v_cmp_ne_u64 vcc,v[16:17],v[12:13]
s_add_u32 s68,s68,s30
v_mfma_f32_32x32x16_bf16 v[32:47],a[4:7],v[132:135],v[32:47]
ds_read_b128 v[164:167],v4 offset:1664
s_cbranch_vccnz .L_rth0_3
.L_bkh0_3:
s_add_u32 s66,s66,s30
v_mfma_f32_32x32x16_bf16 v[32:47],a[8:11],v[136:139],v[32:47]
ds_read_b128 v[168:171],v4 offset:10368
s_add_u32 m0,s64,73728
v_fma_f32 v48,-v72,v112,v48
buffer_load_dwordx4 v20,s[32:35],s63 offen offset:0 sc1 lds
v_mfma_f32_32x32x16_bf16 v[32:47],a[12:15],v[140:143],v[32:47]
ds_read_b128 v[172:175],v4 offset:10880
v_fma_f32 v49,-v72,v113,v49
v_fma_f32 v50,-v72,v114,v50
v_fma_f32 v51,-v72,v115,v51
v_fma_f32 v52,-v72,v116,v52
s_waitcnt lgkmcnt(4)
v_mfma_f32_32x32x16_bf16 v[32:47],a[16:19],v[144:147],v[32:47]
buffer_load_dwordx4 v20,s[32:35],s63 offen offset:1024 sc1 lds
ds_read_b128 v[176:179],v4 offset:19584
v_fma_f32 v53,-v72,v117,v53
v_fma_f32 v54,-v72,v118,v54
v_mfma_f32_32x32x16_bf16 v[32:47],a[20:23],v[148:151],v[32:47]
ds_read_b128 v[180:183],v4 offset:20096
v_fma_f32 v55,-v72,v119,v55
v_fma_f32 v56,-v72,v120,v56
v_fma_f32 v57,-v72,v121,v57
v_fma_f32 v58,-v72,v122,v58
v_fma_f32 v59,-v72,v123,v59
v_mfma_f32_32x32x16_bf16 v[32:47],a[24:27],v[152:155],v[32:47]
buffer_load_dwordx4 v20,s[32:35],s63 offen offset:2048 sc1 lds
ds_read_b128 v[184:187],v4 offset:28800
v_fma_f32 v60,-v72,v124,v60
v_fma_f32 v61,-v72,v125,v61
v_mfma_f32_32x32x16_bf16 v[32:47],a[28:31],v[156:159],v[32:47]
ds_read_b128 v[188:191],v4 offset:29312
v_fma_f32 v62,-v72,v126,v62
v_fma_f32 v63,-v72,v127,v63
v_mul_f32 v76,v48,v73
v_mul_f32 v77,v49,v73
s_waitcnt lgkmcnt(4)
v_mfma_f32_32x32x16_bf16 v[32:47],a[32:35],v[160:163],v[32:47]
buffer_load_dwordx4 v20,s[32:35],s63 offen offset:3072 sc1 lds
ds_read_b128 v[128:131],v4 offset:2304
v_exp_f32 v76,v76
v_mfma_f32_32x32x16_bf16 v[32:47],a[36:39],v[164:167],v[32:47]
ds_read_b128 v[132:135],v4 offset:2816
v_exp_f32 v77,v77
v_mul_f32 v78,v50,v73
v_mul_f32 v79,v51,v73
v_mul_f32 v80,v52,v73
v_mfma_f32_32x32x16_bf16 v[32:47],a[40:43],v[168:171],v[32:47]
ds_read_b128 v[136:139],v4 offset:11520
s_add_u32 m0,s64,110592
v_exp_f32 v78,v78
v_exp_f32 v79,v79
v_mfma_f32_32x32x16_bf16 v[32:47],a[44:47],v[172:175],v[32:47]
ds_read_b128 v[140:143],v4 offset:12032
v_exp_f32 v80,v80
v_mul_f32 v81,v53,v73
v_mul_f32 v82,v54,v73
s_waitcnt lgkmcnt(4)
v_mfma_f32_32x32x16_bf16 v[32:47],a[48:51],v[176:179],v[32:47]
buffer_load_dwordx4 v21,s[32:35],s63 offen offset:0 sc1 lds
ds_read_b128 v[144:147],v4 offset:20736
v_exp_f32 v81,v81
v_mfma_f32_32x32x16_bf16 v[32:47],a[52:55],v[180:183],v[32:47]
ds_read_b128 v[148:151],v4 offset:21248
v_exp_f32 v82,v82
v_mul_f32 v83,v55,v73
v_mul_f32 v48,v48,v56
v_mul_f32 v49,v49,v57
v_mfma_f32_32x32x16_bf16 v[32:47],a[56:59],v[184:187],v[32:47]
buffer_load_dwordx4 v21,s[32:35],s63 offen offset:1024 sc1 lds
ds_read_b128 v[152:155],v4 offset:29952
v_exp_f32 v83,v83
v_mfma_f32_32x32x16_bf16 v[32:47],a[60:63],v[188:191],v[32:47]
ds_read_b128 v[156:159],v4 offset:30464
v_mul_f32 v50,v50,v58
v_mul_f32 v51,v51,v59
v_mul_f32 v52,v52,v60
v_mul_f32 v53,v53,v61
s_waitcnt lgkmcnt(4)
v_mfma_f32_32x32x16_bf16 v[32:47],a[64:67],v[128:131],v[32:47]
buffer_load_dwordx4 v21,s[32:35],s63 offen offset:2048 sc1 lds
ds_read_b128 v[160:163],v4 offset:3456
v_mul_f32 v54,v54,v62
v_mul_f32 v55,v55,v63
v_mfma_f32_32x32x16_bf16 v[32:47],a[68:71],v[132:135],v[32:47]
ds_read_b128 v[164:167],v4 offset:3968
v_fma_f32 v76,v76,v74,v74
v_fma_f32 v77,v77,v74,v74
v_rcp_f32 v76,v76
v_fma_f32 v78,v78,v74,v74
v_mfma_f32_32x32x16_bf16 v[32:47],a[72:75],v[136:139],v[32:47]
buffer_load_dwordx4 v21,s[32:35],s63 offen offset:3072 sc1 lds
ds_read_b128 v[168:171],v4 offset:12672
s_add_u32 m0,s64,77824
v_fma_f32 v79,v79,v74,v74
v_mfma_f32_32x32x16_bf16 v[32:47],a[76:79],v[140:143],v[32:47]
ds_read_b128 v[172:175],v4 offset:13184
v_rcp_f32 v77,v77
v_rcp_f32 v78,v78
s_waitcnt lgkmcnt(4)
v_mfma_f32_32x32x16_bf16 v[32:47],a[80:83],v[144:147],v[32:47]
buffer_load_dwordx4 v22,s[32:35],s63 offen offset:0 sc1 lds
ds_read_b128 v[176:179],v4 offset:21888
s_add_u32 m0,s64,114688
v_fma_f32 v80,v80,v74,v74
v_mfma_f32_32x32x16_bf16 v[32:47],a[84:87],v[148:151],v[32:47]
ds_read_b128 v[180:183],v4 offset:22400
v_rcp_f32 v79,v79
v_rcp_f32 v80,v80
v_fma_f32 v81,v81,v74,v74
s_waitcnt vmcnt(9)
s_barrier
v_mfma_f32_32x32x16_bf16 v[32:47],a[88:91],v[152:155],v[32:47]
buffer_load_dwordx4 v23,s[32:35],s63 offen offset:0 sc1 lds
ds_read_b128 v[184:187],v4 offset:31104
v_rcp_f32 v81,v81
v_mfma_f32_32x32x16_bf16 v[32:47],a[92:95],v[156:159],v[32:47]
ds_read_b128 v[188:191],v4 offset:31616
v_fma_f32 v82,v82,v74,v74
v_fma_f32 v83,v83,v74,v74
v_rcp_f32 v82,v82
s_waitcnt lgkmcnt(4)
v_mfma_f32_32x32x16_bf16 v[32:47],a[96:99],v[160:163],v[32:47]
ds_read_b128 v[128:131],v4 offset:4608
v_rcp_f32 v83,v83
v_mul_f32 v48,v48,v76
v_mfma_f32_32x32x16_bf16 v[32:47],a[100:103],v[164:167],v[32:47]
ds_read_b128 v[132:135],v4 offset:5120
v_mul_f32 v49,v49,v77
v_mul_f32 v50,v50,v78
v_mul_f32 v51,v51,v79
v_mul_f32 v52,v52,v80
v_mul_f32 v53,v53,v81
v_mfma_f32_32x32x16_bf16 v[32:47],a[104:107],v[168:171],v[32:47]
ds_read_b128 v[136:139],v4 offset:13824
s_add_u32 m0,s64,78848
v_mul_f32 v54,v54,v82
buffer_load_dwordx4 v24,s[32:35],s63 offen offset:0 sc1 lds
v_mfma_f32_32x32x16_bf16 v[32:47],a[108:111],v[172:175],v[32:47]
ds_read_b128 v[140:143],v4 offset:14336
v_mul_f32 v55,v55,v83
v_cvt_pk_bf16_f32 v84,v48,v49
v_cvt_pk_bf16_f32 v85,v50,v51
v_cvt_pk_bf16_f32 v86,v52,v53
s_waitcnt lgkmcnt(4)
v_mfma_f32_32x32x16_bf16 v[32:47],a[112:115],v[176:179],v[32:47]
ds_read_b128 v[144:147],v4 offset:23040
v_cvt_pk_bf16_f32 v87,v54,v55
ds_read2st64_b32 v[64:65],v8 offset0:0 offset1:36
ds_read2st64_b32 v[66:67],v8 offset0:72 offset1:108
ds_read2st64_b32 v[68:69],v8 offset0:144 offset1:180
ds_read2st64_b32 v[70:71],v8 offset0:216 offset1:252
v_mfma_f32_32x32x16_bf16 v[32:47],a[116:119],v[180:183],v[32:47]
ds_read_b128 v[148:151],v4 offset:23552
v_xor_b32 v8,v9,v8
v_mfma_f32_32x32x16_bf16 v[32:47],a[120:123],v[184:187],v[32:47]
buffer_store_dwordx4 v[84:87],v15,s[32:35],s68 offen sc1
ds_read_b128 v[152:155],v4 offset:32256
v_mfma_f32_32x32x16_bf16 v[32:47],a[124:127],v[188:191],v[32:47]
ds_read_b128 v[156:159],v4 offset:32768
s_waitcnt lgkmcnt(8)
v_mfma_f32_32x32x16_bf16 v[32:47],a[128:131],v[128:131],v[32:47]
buffer_load_dwordx4 v24,s[32:35],s63 offen offset:1024 sc1 lds
ds_read_b128 v[160:163],v4 offset:5760
v_mfma_f32_32x32x16_bf16 v[32:47],a[132:135],v[132:135],v[32:47]
ds_read_b128 v[164:167],v4 offset:6272
s_waitcnt lgkmcnt(5)
v_add_f32 v64,v64,v65
v_add_f32 v66,v66,v67
v_add_f32 v68,v68,v69
v_add_f32 v70,v70,v71
v_mfma_f32_32x32x16_bf16 v[32:47],a[136:139],v[136:139],v[32:47]
buffer_load_dwordx4 v24,s[32:35],s63 offen offset:2048 sc1 lds
ds_read_b128 v[168:171],v4 offset:14976
v_add_f32 v66,v64,v66
v_add_f32 v68,v68,v70
v_mfma_f32_32x32x16_bf16 v[32:47],a[140:143],v[140:143],v[32:47]
ds_read_b128 v[172:175],v4 offset:15488
v_add_f32_dpp v64,v66,v66 row_ror:8 row_mask:0xf bank_mask:0x3
s_waitcnt lgkmcnt(4)
v_mfma_f32_32x32x16_bf16 v[32:47],a[144:147],v[144:147],v[32:47]
buffer_load_dwordx2 v[12:13],v14,s[0:3],s66 offen sc1
ds_read_b128 v[176:179],v4 offset:24192
v_add_f32_dpp v64,v68,v68 row_ror:8 row_mask:0xf bank_mask:0xc
v_mfma_f32_32x32x16_bf16 v[32:47],a[148:151],v[148:151],v[32:47]
ds_read_b128 v[180:183],v4 offset:24704
v_add_f32_dpp v64,v64,v64 row_ror:12 row_mask:0xf bank_mask:0xf
v_mfma_f32_32x32x16_bf16 v[32:47],a[152:155],v[152:155],v[32:47]
buffer_load_dwordx4 v24,s[32:35],s63 offen offset:3072 sc1 lds
ds_read_b128 v[184:187],v4 offset:33408
v_add_f32_dpp v64,v64,v64 quad_perm:[2,3,0,1] row_mask:0xf bank_mask:0xf
v_mfma_f32_32x32x16_bf16 v[32:47],a[156:159],v[156:159],v[32:47]
ds_read_b128 v[188:191],v4 offset:33920
s_add_u32 m0,s64,115712
v_mul_f32 v64,0x3a800000,v64
s_waitcnt lgkmcnt(4)
v_mfma_f32_32x32x16_bf16 v[32:47],a[160:163],v[160:163],v[32:47]
buffer_load_dwordx4 v25,s[32:35],s63 offen offset:0 sc1 lds
ds_read_b128 v[128:131],v4 offset:6912
v_mul_f32 v65,v64,v64
v_mfma_f32_32x32x16_bf16 v[32:47],a[164:167],v[164:167],v[32:47]
ds_read_b128 v[132:135],v4 offset:7424
v_sub_f32_dpp v65,v64,v65 quad_perm:[1,1,3,3] row_mask:0xf bank_mask:0xf
v_mfma_f32_32x32x16_bf16 v[32:47],a[168:171],v[168:171],v[32:47]
ds_read_b128 v[136:139],v4 offset:16128
v_max_f32 v65,0,v65
v_mfma_f32_32x32x16_bf16 v[32:47],a[172:175],v[172:175],v[32:47]
ds_read_b128 v[140:143],v4 offset:16640
v_cvt_pk_bf16_f32 v65,0,v65
s_waitcnt lgkmcnt(4)
v_mfma_f32_32x32x16_bf16 v[32:47],a[176:179],v[176:179],v[32:47]
buffer_load_dwordx4 v25,s[32:35],s63 offen offset:1024 sc1 lds
ds_read_b128 v[144:147],v4 offset:25344
v_add_f32 v65,0x3727c5ac,v65
v_mfma_f32_32x32x16_bf16 v[32:47],a[180:183],v[180:183],v[32:47]
ds_read_b128 v[148:151],v4 offset:25856
v_cvt_pk_bf16_f32 v65,0,v65
v_mfma_f32_32x32x16_bf16 v[32:47],a[184:187],v[184:187],v[32:47]
buffer_load_dwordx4 v25,s[32:35],s63 offen offset:2048 sc1 lds
ds_read_b128 v[152:155],v4 offset:34560
v_sqrt_f32 v65,v65
v_mfma_f32_32x32x16_bf16 v[32:47],a[188:191],v[188:191],v[32:47]
ds_read_b128 v[156:159],v4 offset:35072
v_cvt_pk_bf16_f32 v65,v64,v65
s_waitcnt lgkmcnt(4)
v_mfma_f32_32x32x16_bf16 v[32:47],a[192:195],v[128:131],v[32:47]
ds_read_b128 v[160:163],v4 offset:8064
ds_write_b32 v10,v65 offset:0
v_mfma_f32_32x32x16_bf16 v[32:47],a[196:199],v[132:135],v[32:47]
ds_read_b128 v[164:167],v4 offset:8576
v_mfma_f32_32x32x16_bf16 v[32:47],a[200:203],v[136:139],v[32:47]
buffer_load_dwordx4 v25,s[32:35],s63 offen offset:3072 sc1 lds
ds_read_b128 v[168:171],v4 offset:17280
s_add_u32 s63,s63,s30
v_mfma_f32_32x32x16_bf16 v[32:47],a[204:207],v[140:143],v[32:47]
ds_read_b128 v[172:175],v4 offset:17792
s_waitcnt lgkmcnt(5)
v_mfma_f32_32x32x16_bf16 v[32:47],a[208:211],v[144:147],v[32:47]
ds_read_b128 v[176:179],v4 offset:26496
v_mfma_f32_32x32x16_bf16 v[32:47],a[212:215],v[148:151],v[32:47]
ds_read_b128 v[180:183],v4 offset:27008
s_waitcnt vmcnt(8)
s_waitcnt lgkmcnt(5)
s_barrier
v_mfma_f32_32x32x16_bf16 v[32:47],a[216:219],v[152:155],v[32:47]
ds_read_b128 v[184:187],v4 offset:35712
v_mfma_f32_32x32x16_bf16 v[32:47],a[220:223],v[156:159],v[32:47]
ds_read_b128 v[188:191],v4 offset:36224
s_waitcnt lgkmcnt(4)
v_mfma_f32_32x32x16_bf16 v[32:47],a[224:227],v[160:163],v[32:47]
ds_read_b128 v[128:131],v5 offset:0
v_mfma_f32_32x32x16_bf16 v[32:47],a[228:231],v[164:167],v[32:47]
ds_read_b128 v[132:135],v5 offset:512
ds_read_b32 v74,v11 offset:0
v_mfma_f32_32x32x16_bf16 v[32:47],a[232:235],v[168:171],v[32:47]
ds_read_b128 v[136:139],v5 offset:9216
v_mfma_f32_32x32x16_bf16 v[32:47],a[236:239],v[172:175],v[32:47]
ds_read_b128 v[140:143],v5 offset:9728
s_waitcnt lgkmcnt(5)
v_mfma_f32_32x32x16_bf16 v[32:47],a[240:243],v[176:179],v[32:47]
ds_read_b128 v[144:147],v5 offset:18432
s_waitcnt lgkmcnt(3)
v_lshlrev_b32 v72,16,v74
v_and_b32 v74,0xffff0000,v74
v_mfma_f32_32x32x16_bf16 v[32:47],a[244:247],v[180:183],v[32:47]
ds_read_b128 v[148:151],v5 offset:18944
v_rcp_f32 v73,v74
v_mul_f32 v74,v74,v74
v_mfma_f32_32x32x16_bf16 v[32:47],a[248:251],v[184:187],v[32:47]
ds_read_b128 v[152:155],v5 offset:27648
v_mul_f32 v73,0xbfb8aa3b,v73
v_mfma_f32_32x32x16_bf16 v[32:47],a[252:255],v[188:191],v[32:47]
ds_read_b128 v[156:159],v5 offset:28160
s_waitcnt lgkmcnt(4)
v_mfma_f32_32x32x16_bf16 v[48:63],a[0:3],v[128:131],0
ds_read_b128 v[160:163],v5 offset:1152
s_waitcnt vmcnt(5)
v_cmp_ne_u64 vcc,v[16:17],v[12:13]
s_add_u32 s68,s68,s30
v_mfma_f32_32x32x16_bf16 v[48:63],a[4:7],v[132:135],v[48:63]
ds_read_b128 v[164:167],v5 offset:1664
s_cbranch_vccnz .L_rth1_3
.L_bkh1_3:
s_add_u32 s66,s66,s30
v_mfma_f32_32x32x16_bf16 v[48:63],a[8:11],v[136:139],v[48:63]
ds_read_b128 v[168:171],v5 offset:10368
s_add_u32 m0,s64,0
v_fma_f32 v32,-v72,v112,v32
buffer_load_dwordx4 v20,s[32:35],s63 offen offset:0 sc1 lds
v_mfma_f32_32x32x16_bf16 v[48:63],a[12:15],v[140:143],v[48:63]
ds_read_b128 v[172:175],v5 offset:10880
v_fma_f32 v33,-v72,v113,v33
v_fma_f32 v34,-v72,v114,v34
v_fma_f32 v35,-v72,v115,v35
v_fma_f32 v36,-v72,v116,v36
s_waitcnt lgkmcnt(4)
v_mfma_f32_32x32x16_bf16 v[48:63],a[16:19],v[144:147],v[48:63]
buffer_load_dwordx4 v20,s[32:35],s63 offen offset:1024 sc1 lds
ds_read_b128 v[176:179],v5 offset:19584
v_fma_f32 v37,-v72,v117,v37
v_fma_f32 v38,-v72,v118,v38
v_mfma_f32_32x32x16_bf16 v[48:63],a[20:23],v[148:151],v[48:63]
ds_read_b128 v[180:183],v5 offset:20096
v_fma_f32 v39,-v72,v119,v39
v_fma_f32 v40,-v72,v120,v40
v_fma_f32 v41,-v72,v121,v41
v_fma_f32 v42,-v72,v122,v42
v_fma_f32 v43,-v72,v123,v43
v_mfma_f32_32x32x16_bf16 v[48:63],a[24:27],v[152:155],v[48:63]
buffer_load_dwordx4 v20,s[32:35],s63 offen offset:2048 sc1 lds
ds_read_b128 v[184:187],v5 offset:28800
v_fma_f32 v44,-v72,v124,v44
v_fma_f32 v45,-v72,v125,v45
v_mfma_f32_32x32x16_bf16 v[48:63],a[28:31],v[156:159],v[48:63]
ds_read_b128 v[188:191],v5 offset:29312
v_fma_f32 v46,-v72,v126,v46
v_fma_f32 v47,-v72,v127,v47
v_mul_f32 v76,v32,v73
v_mul_f32 v77,v33,v73
s_waitcnt lgkmcnt(4)
v_mfma_f32_32x32x16_bf16 v[48:63],a[32:35],v[160:163],v[48:63]
buffer_load_dwordx4 v20,s[32:35],s63 offen offset:3072 sc1 lds
ds_read_b128 v[128:131],v5 offset:2304
v_exp_f32 v76,v76
v_mfma_f32_32x32x16_bf16 v[48:63],a[36:39],v[164:167],v[48:63]
ds_read_b128 v[132:135],v5 offset:2816
v_exp_f32 v77,v77
v_mul_f32 v78,v34,v73
v_mul_f32 v79,v35,v73
v_mul_f32 v80,v36,v73
v_mfma_f32_32x32x16_bf16 v[48:63],a[40:43],v[168:171],v[48:63]
ds_read_b128 v[136:139],v5 offset:11520
s_add_u32 m0,s64,36864
v_exp_f32 v78,v78
v_exp_f32 v79,v79
v_mfma_f32_32x32x16_bf16 v[48:63],a[44:47],v[172:175],v[48:63]
ds_read_b128 v[140:143],v5 offset:12032
v_exp_f32 v80,v80
v_mul_f32 v81,v37,v73
v_mul_f32 v82,v38,v73
s_waitcnt lgkmcnt(4)
v_mfma_f32_32x32x16_bf16 v[48:63],a[48:51],v[176:179],v[48:63]
buffer_load_dwordx4 v21,s[32:35],s63 offen offset:0 sc1 lds
ds_read_b128 v[144:147],v5 offset:20736
v_exp_f32 v81,v81
v_mfma_f32_32x32x16_bf16 v[48:63],a[52:55],v[180:183],v[48:63]
ds_read_b128 v[148:151],v5 offset:21248
v_exp_f32 v82,v82
v_mul_f32 v83,v39,v73
v_mul_f32 v32,v32,v40
v_mul_f32 v33,v33,v41
v_mfma_f32_32x32x16_bf16 v[48:63],a[56:59],v[184:187],v[48:63]
buffer_load_dwordx4 v21,s[32:35],s63 offen offset:1024 sc1 lds
ds_read_b128 v[152:155],v5 offset:29952
v_exp_f32 v83,v83
v_mfma_f32_32x32x16_bf16 v[48:63],a[60:63],v[188:191],v[48:63]
ds_read_b128 v[156:159],v5 offset:30464
v_mul_f32 v34,v34,v42
v_mul_f32 v35,v35,v43
v_mul_f32 v36,v36,v44
v_mul_f32 v37,v37,v45
s_waitcnt lgkmcnt(4)
v_mfma_f32_32x32x16_bf16 v[48:63],a[64:67],v[128:131],v[48:63]
buffer_load_dwordx4 v21,s[32:35],s63 offen offset:2048 sc1 lds
ds_read_b128 v[160:163],v5 offset:3456
v_mul_f32 v38,v38,v46
v_mul_f32 v39,v39,v47
v_mfma_f32_32x32x16_bf16 v[48:63],a[68:71],v[132:135],v[48:63]
ds_read_b128 v[164:167],v5 offset:3968
v_fma_f32 v76,v76,v74,v74
v_fma_f32 v77,v77,v74,v74
v_rcp_f32 v76,v76
v_fma_f32 v78,v78,v74,v74
v_mfma_f32_32x32x16_bf16 v[48:63],a[72:75],v[136:139],v[48:63]
buffer_load_dwordx4 v21,s[32:35],s63 offen offset:3072 sc1 lds
ds_read_b128 v[168:171],v5 offset:12672
s_add_u32 m0,s64,4096
v_fma_f32 v79,v79,v74,v74
v_mfma_f32_32x32x16_bf16 v[48:63],a[76:79],v[140:143],v[48:63]
ds_read_b128 v[172:175],v5 offset:13184
v_rcp_f32 v77,v77
v_rcp_f32 v78,v78
s_waitcnt lgkmcnt(4)
v_mfma_f32_32x32x16_bf16 v[48:63],a[80:83],v[144:147],v[48:63]
buffer_load_dwordx4 v22,s[32:35],s63 offen offset:0 sc1 lds
ds_read_b128 v[176:179],v5 offset:21888
s_add_u32 m0,s64,40960
v_fma_f32 v80,v80,v74,v74
v_mfma_f32_32x32x16_bf16 v[48:63],a[84:87],v[148:151],v[48:63]
ds_read_b128 v[180:183],v5 offset:22400
v_rcp_f32 v79,v79
v_rcp_f32 v80,v80
v_fma_f32 v81,v81,v74,v74
s_waitcnt vmcnt(9)
s_barrier
v_mfma_f32_32x32x16_bf16 v[48:63],a[88:91],v[152:155],v[48:63]
buffer_load_dwordx4 v23,s[32:35],s63 offen offset:0 sc1 lds
ds_read_b128 v[184:187],v5 offset:31104
v_rcp_f32 v81,v81
v_mfma_f32_32x32x16_bf16 v[48:63],a[92:95],v[156:159],v[48:63]
ds_read_b128 v[188:191],v5 offset:31616
v_fma_f32 v82,v82,v74,v74
v_fma_f32 v83,v83,v74,v74
v_rcp_f32 v82,v82
s_waitcnt lgkmcnt(4)
)#"
R"#(
v_mfma_f32_32x32x16_bf16 v[48:63],a[96:99],v[160:163],v[48:63]
ds_read_b128 v[128:131],v5 offset:4608
v_rcp_f32 v83,v83
v_mul_f32 v32,v32,v76
v_mfma_f32_32x32x16_bf16 v[48:63],a[100:103],v[164:167],v[48:63]
ds_read_b128 v[132:135],v5 offset:5120
v_mul_f32 v33,v33,v77
v_mul_f32 v34,v34,v78
v_mul_f32 v35,v35,v79
v_mul_f32 v36,v36,v80
v_mul_f32 v37,v37,v81
v_mfma_f32_32x32x16_bf16 v[48:63],a[104:107],v[168:171],v[48:63]
ds_read_b128 v[136:139],v5 offset:13824
s_add_u32 m0,s64,5120
v_mul_f32 v38,v38,v82
buffer_load_dwordx4 v24,s[32:35],s63 offen offset:0 sc1 lds
v_mfma_f32_32x32x16_bf16 v[48:63],a[108:111],v[172:175],v[48:63]
ds_read_b128 v[140:143],v5 offset:14336
v_mul_f32 v39,v39,v83
v_cvt_pk_bf16_f32 v84,v32,v33
v_cvt_pk_bf16_f32 v85,v34,v35
v_cvt_pk_bf16_f32 v86,v36,v37
s_waitcnt lgkmcnt(4)
v_mfma_f32_32x32x16_bf16 v[48:63],a[112:115],v[176:179],v[48:63]
ds_read_b128 v[144:147],v5 offset:23040
v_cvt_pk_bf16_f32 v87,v38,v39
ds_read2st64_b32 v[64:65],v8 offset0:0 offset1:36
ds_read2st64_b32 v[66:67],v8 offset0:72 offset1:108
ds_read2st64_b32 v[68:69],v8 offset0:144 offset1:180
ds_read2st64_b32 v[70:71],v8 offset0:216 offset1:252
v_mfma_f32_32x32x16_bf16 v[48:63],a[116:119],v[180:183],v[48:63]
ds_read_b128 v[148:151],v5 offset:23552
v_xor_b32 v8,v9,v8
v_mfma_f32_32x32x16_bf16 v[48:63],a[120:123],v[184:187],v[48:63]
buffer_store_dwordx4 v[84:87],v15,s[32:35],s68 offen sc1
ds_read_b128 v[152:155],v5 offset:32256
v_mfma_f32_32x32x16_bf16 v[48:63],a[124:127],v[188:191],v[48:63]
ds_read_b128 v[156:159],v5 offset:32768
s_waitcnt lgkmcnt(8)
v_mfma_f32_32x32x16_bf16 v[48:63],a[128:131],v[128:131],v[48:63]
buffer_load_dwordx4 v24,s[32:35],s63 offen offset:1024 sc1 lds
ds_read_b128 v[160:163],v5 offset:5760
v_mfma_f32_32x32x16_bf16 v[48:63],a[132:135],v[132:135],v[48:63]
ds_read_b128 v[164:167],v5 offset:6272
s_waitcnt lgkmcnt(5)
v_add_f32 v64,v64,v65
v_add_f32 v66,v66,v67
v_add_f32 v68,v68,v69
v_add_f32 v70,v70,v71
v_mfma_f32_32x32x16_bf16 v[48:63],a[136:139],v[136:139],v[48:63]
buffer_load_dwordx4 v24,s[32:35],s63 offen offset:2048 sc1 lds
ds_read_b128 v[168:171],v5 offset:14976
v_add_f32 v66,v64,v66
v_add_f32 v68,v68,v70
v_mfma_f32_32x32x16_bf16 v[48:63],a[140:143],v[140:143],v[48:63]
ds_read_b128 v[172:175],v5 offset:15488
v_add_f32_dpp v64,v66,v66 row_ror:8 row_mask:0xf bank_mask:0x3
s_waitcnt lgkmcnt(4)
v_mfma_f32_32x32x16_bf16 v[48:63],a[144:147],v[144:147],v[48:63]
buffer_load_dwordx2 v[12:13],v14,s[0:3],s66 offen sc1
ds_read_b128 v[176:179],v5 offset:24192
v_add_f32_dpp v64,v68,v68 row_ror:8 row_mask:0xf bank_mask:0xc
v_mfma_f32_32x32x16_bf16 v[48:63],a[148:151],v[148:151],v[48:63]
ds_read_b128 v[180:183],v5 offset:24704
v_add_f32_dpp v64,v64,v64 row_ror:12 row_mask:0xf bank_mask:0xf
v_mfma_f32_32x32x16_bf16 v[48:63],a[152:155],v[152:155],v[48:63]
buffer_load_dwordx4 v24,s[32:35],s63 offen offset:3072 sc1 lds
ds_read_b128 v[184:187],v5 offset:33408
v_add_f32_dpp v64,v64,v64 quad_perm:[2,3,0,1] row_mask:0xf bank_mask:0xf
v_mfma_f32_32x32x16_bf16 v[48:63],a[156:159],v[156:159],v[48:63]
ds_read_b128 v[188:191],v5 offset:33920
s_add_u32 m0,s64,41984
v_mul_f32 v64,0x3a800000,v64
s_waitcnt lgkmcnt(4)
v_mfma_f32_32x32x16_bf16 v[48:63],a[160:163],v[160:163],v[48:63]
buffer_load_dwordx4 v25,s[32:35],s63 offen offset:0 sc1 lds
ds_read_b128 v[128:131],v5 offset:6912
v_mul_f32 v65,v64,v64
v_mfma_f32_32x32x16_bf16 v[48:63],a[164:167],v[164:167],v[48:63]
ds_read_b128 v[132:135],v5 offset:7424
v_sub_f32_dpp v65,v64,v65 quad_perm:[1,1,3,3] row_mask:0xf bank_mask:0xf
v_mfma_f32_32x32x16_bf16 v[48:63],a[168:171],v[168:171],v[48:63]
ds_read_b128 v[136:139],v5 offset:16128
v_max_f32 v65,0,v65
v_mfma_f32_32x32x16_bf16 v[48:63],a[172:175],v[172:175],v[48:63]
ds_read_b128 v[140:143],v5 offset:16640
v_cvt_pk_bf16_f32 v65,0,v65
s_waitcnt lgkmcnt(4)
v_mfma_f32_32x32x16_bf16 v[48:63],a[176:179],v[176:179],v[48:63]
buffer_load_dwordx4 v25,s[32:35],s63 offen offset:1024 sc1 lds
ds_read_b128 v[144:147],v5 offset:25344
v_add_f32 v65,0x3727c5ac,v65
v_mfma_f32_32x32x16_bf16 v[48:63],a[180:183],v[180:183],v[48:63]
ds_read_b128 v[148:151],v5 offset:25856
v_cvt_pk_bf16_f32 v65,0,v65
v_mfma_f32_32x32x16_bf16 v[48:63],a[184:187],v[184:187],v[48:63]
buffer_load_dwordx4 v25,s[32:35],s63 offen offset:2048 sc1 lds
ds_read_b128 v[152:155],v5 offset:34560
v_sqrt_f32 v65,v65
v_mfma_f32_32x32x16_bf16 v[48:63],a[188:191],v[188:191],v[48:63]
ds_read_b128 v[156:159],v5 offset:35072
v_cvt_pk_bf16_f32 v65,v64,v65
s_waitcnt lgkmcnt(4)
v_mfma_f32_32x32x16_bf16 v[48:63],a[192:195],v[128:131],v[48:63]
ds_read_b128 v[160:163],v5 offset:8064
ds_write_b32 v10,v65 offset:128
v_mfma_f32_32x32x16_bf16 v[48:63],a[196:199],v[132:135],v[48:63]
ds_read_b128 v[164:167],v5 offset:8576
v_mfma_f32_32x32x16_bf16 v[48:63],a[200:203],v[136:139],v[48:63]
buffer_load_dwordx4 v25,s[32:35],s63 offen offset:3072 sc1 lds
ds_read_b128 v[168:171],v5 offset:17280
s_add_u32 s63,s63,s30
v_mfma_f32_32x32x16_bf16 v[48:63],a[204:207],v[140:143],v[48:63]
ds_read_b128 v[172:175],v5 offset:17792
s_waitcnt lgkmcnt(5)
v_mfma_f32_32x32x16_bf16 v[48:63],a[208:211],v[144:147],v[48:63]
ds_read_b128 v[176:179],v5 offset:26496
v_mfma_f32_32x32x16_bf16 v[48:63],a[212:215],v[148:151],v[48:63]
ds_read_b128 v[180:183],v5 offset:27008
s_waitcnt vmcnt(8)
s_waitcnt lgkmcnt(5)
s_barrier
v_mfma_f32_32x32x16_bf16 v[48:63],a[216:219],v[152:155],v[48:63]
ds_read_b128 v[184:187],v5 offset:35712
v_mfma_f32_32x32x16_bf16 v[48:63],a[220:223],v[156:159],v[48:63]
ds_read_b128 v[188:191],v5 offset:36224
s_waitcnt lgkmcnt(4)
v_mfma_f32_32x32x16_bf16 v[48:63],a[224:227],v[160:163],v[48:63]
ds_read_b128 v[128:131],v4 offset:0
v_mfma_f32_32x32x16_bf16 v[48:63],a[228:231],v[164:167],v[48:63]
ds_read_b128 v[132:135],v4 offset:512
ds_read_b32 v74,v11 offset:128
v_mfma_f32_32x32x16_bf16 v[48:63],a[232:235],v[168:171],v[48:63]
ds_read_b128 v[136:139],v4 offset:9216
v_mfma_f32_32x32x16_bf16 v[48:63],a[236:239],v[172:175],v[48:63]
ds_read_b128 v[140:143],v4 offset:9728
s_waitcnt lgkmcnt(5)
v_mfma_f32_32x32x16_bf16 v[48:63],a[240:243],v[176:179],v[48:63]
ds_read_b128 v[144:147],v4 offset:18432
s_waitcnt lgkmcnt(3)
v_lshlrev_b32 v72,16,v74
v_and_b32 v74,0xffff0000,v74
v_mfma_f32_32x32x16_bf16 v[48:63],a[244:247],v[180:183],v[48:63]
ds_read_b128 v[148:151],v4 offset:18944
v_rcp_f32 v73,v74
v_mul_f32 v74,v74,v74
v_mfma_f32_32x32x16_bf16 v[48:63],a[248:251],v[184:187],v[48:63]
ds_read_b128 v[152:155],v4 offset:27648
v_mul_f32 v73,0xbfb8aa3b,v73
s_sub_u32 s62,s62,1
v_mfma_f32_32x32x16_bf16 v[48:63],a[252:255],v[188:191],v[48:63]
ds_read_b128 v[156:159],v4 offset:28160
s_cbranch_scc0 .L_hl_3
s_nop 1
s_add_u32 s68,s68,s30
s_nop 6
v_fma_f32 v48,-v72,v112,v48
v_fma_f32 v49,-v72,v113,v49
v_fma_f32 v50,-v72,v114,v50
v_fma_f32 v51,-v72,v115,v51
v_fma_f32 v52,-v72,v116,v52
v_fma_f32 v53,-v72,v117,v53
v_fma_f32 v54,-v72,v118,v54
v_fma_f32 v55,-v72,v119,v55
v_fma_f32 v56,-v72,v120,v56
v_fma_f32 v57,-v72,v121,v57
v_fma_f32 v58,-v72,v122,v58
v_fma_f32 v59,-v72,v123,v59
v_fma_f32 v60,-v72,v124,v60
v_fma_f32 v61,-v72,v125,v61
v_fma_f32 v62,-v72,v126,v62
v_fma_f32 v63,-v72,v127,v63
v_mul_f32 v76,v48,v73
v_mul_f32 v77,v49,v73
v_mul_f32 v78,v50,v73
v_mul_f32 v79,v51,v73
v_mul_f32 v80,v52,v73
v_mul_f32 v81,v53,v73
v_mul_f32 v82,v54,v73
v_mul_f32 v83,v55,v73
v_exp_f32 v76,v76
v_exp_f32 v77,v77
v_mul_f32 v48,v48,v56
v_mul_f32 v49,v49,v57
v_exp_f32 v78,v78
v_exp_f32 v79,v79
v_mul_f32 v50,v50,v58
v_mul_f32 v51,v51,v59
v_exp_f32 v80,v80
v_exp_f32 v81,v81
v_mul_f32 v52,v52,v60
v_mul_f32 v53,v53,v61
v_exp_f32 v82,v82
v_exp_f32 v83,v83
v_mul_f32 v54,v54,v62
v_mul_f32 v55,v55,v63
v_fma_f32 v76,v76,v74,v74
v_fma_f32 v77,v77,v74,v74
v_rcp_f32 v76,v76
v_rcp_f32 v77,v77
v_fma_f32 v78,v78,v74,v74
v_fma_f32 v79,v79,v74,v74
v_rcp_f32 v78,v78
v_rcp_f32 v79,v79
v_fma_f32 v80,v80,v74,v74
v_fma_f32 v81,v81,v74,v74
v_rcp_f32 v80,v80
v_rcp_f32 v81,v81
v_fma_f32 v82,v82,v74,v74
v_fma_f32 v83,v83,v74,v74
v_rcp_f32 v82,v82
v_rcp_f32 v83,v83
v_mul_f32 v48,v48,v76
v_mul_f32 v49,v49,v77
v_mul_f32 v50,v50,v78
v_mul_f32 v51,v51,v79
v_mul_f32 v52,v52,v80
v_mul_f32 v53,v53,v81
v_mul_f32 v54,v54,v82
v_mul_f32 v55,v55,v83
v_cvt_pk_bf16_f32 v84,v48,v49
v_cvt_pk_bf16_f32 v85,v50,v51
v_cvt_pk_bf16_f32 v86,v52,v53
v_cvt_pk_bf16_f32 v87,v54,v55
buffer_store_dwordx4 v[84:87],v15,s[32:35],s68 offen sc1
s_waitcnt vmcnt(0)
s_barrier
buffer_store_dwordx2 v[16:17],v18,s[32:35],s68 offen sc1
s_cbranch_execz .L_hbk
s_branch .L_end
.L_rtg0_1:
s_mov_b32 s48,20000
.L_rlg0_1:
s_sleep 8
.L_rfg0_1:
buffer_load_dwordx2 v[12:13],v14,s[0:3],s66 offen sc1
s_sub_u32 s48,s48,1
s_cbranch_scc1 .L_end
s_waitcnt vmcnt(0)
v_cmp_ne_u64 vcc,v[16:17],v[12:13]
s_cbranch_vccnz .L_rlg0_1
s_branch .L_bkg0_1
.L_rtg1_2:
s_mov_b32 s48,20000
.L_rlg1_2:
s_sleep 8
.L_rfg1_2:
buffer_load_dwordx2 v[12:13],v14,s[0:3],s66 offen sc1
s_sub_u32 s48,s48,1
s_cbranch_scc1 .L_end
s_waitcnt vmcnt(0)
v_cmp_ne_u64 vcc,v[16:17],v[12:13]
s_cbranch_vccnz .L_rlg1_2
s_branch .L_bkg1_2
.L_rtqe:
s_mov_b32 s48,20000
s_branch .L_rfqe
.L_rlqe:
s_sleep 2
.L_rfqe:
buffer_load_dwordx2 v[60:61],v56,s[0:3],s66 offen sc1
s_sub_u32 s48,s48,1
s_cbranch_scc1 .L_end
s_waitcnt vmcnt(0)
v_cmp_ne_u64 vcc,s[54:55],v[60:61]
s_cbranch_vccnz .L_rlqe
s_branch .L_bkqe
.L_rtq0:
s_mov_b32 s48,20000
.L_rlq0:
s_sleep 8
.L_rfq0:
buffer_load_dwordx2 v[12:13],v0,s[0:3],s66 offen sc1
s_sub_u32 s48,s48,1
s_cbranch_scc1 .L_end
s_waitcnt vmcnt(0)
v_cmp_ne_u64 vcc,v[30:31],v[12:13]
s_cbranch_vccnz .L_rlq0
s_branch .L_bkq0
.L_rtq1:
s_mov_b32 s48,20000
.L_rlq1:
s_sleep 8
.L_rfq1:
buffer_load_dwordx2 v[12:13],v0,s[0:3],s66 offen sc1
s_sub_u32 s48,s48,1
s_cbranch_scc1 .L_end
s_waitcnt vmcnt(0)
v_cmp_ne_u64 vcc,v[30:31],v[12:13]
s_cbranch_vccnz .L_rlq1
s_branch .L_bkq1
.L_qs0:
s_cmp_lg_u32 s62,30
s_cbranch_scc1 .L_qn4
v_mov_b32 v120,0xff61b1e6
v_mov_b32 v121,0xff61b1e6
v_mov_b32 v122,0xff61b1e6
v_mov_b32 v123,0xff61b1e6
v_cmp_ne_u32 vcc,0xffff,v126
s_nop 1
v_cndmask_b32 v120,0,v120,vcc
v_cmp_ne_u32 vcc,0xffff0000,v126
s_nop 1
v_cndmask_b32 v121,0,v121,vcc
v_cmp_ne_u32 vcc,0xffff,v127
s_nop 1
v_cndmask_b32 v122,0,v122,vcc
v_cmp_ne_u32 vcc,0xffff0000,v127
s_nop 1
v_cndmask_b32 v123,0,v123,vcc
s_nop 1
s_branch .L_qb0
.L_qn4:
s_cmp_lg_u32 s62,17
s_cbranch_scc1 .L_qn30
s_mov_b32 s72,s69
s_nop 1
s_branch .L_qb0
.L_qn30:
s_cmp_lg_u32 s62,16
s_cbranch_scc1 .L_qn32
s_mov_b32 s72,1187840
s_nop 1
s_branch .L_qb0
.L_qn32:
s_cmp_lg_u32 s62,15
s_cbranch_scc1 .L_qn34
s_sub_u32 s68,s68,39198720
s_nop 1
s_branch .L_qb0
.L_qn34:
s_cmp_lg_u32 s62,14
s_cbranch_scc1 .L_qn36
v_mov_b32 v120,0xff61b1e6
v_mov_b32 v121,0xff61b1e6
v_mov_b32 v122,0xff61b1e6
v_mov_b32 v123,0xff61b1e6
v_cmp_ne_u32 vcc,0xffff,v126
s_nop 1
v_cndmask_b32 v120,0,v120,vcc
v_cmp_ne_u32 vcc,0xffff0000,v126
s_nop 1
v_cndmask_b32 v121,0,v121,vcc
v_cmp_ne_u32 vcc,0xffff,v127
s_nop 1
v_cndmask_b32 v122,0,v122,vcc
v_cmp_ne_u32 vcc,0xffff0000,v127
s_nop 1
v_cndmask_b32 v123,0,v123,vcc
s_nop 1
s_branch .L_qb0
.L_qn36:
s_branch .L_qb0
.L_qs1:
s_cmp_lg_u32 s62,31
s_cbranch_scc1 .L_qn3
s_cmp_lg_u32 s28,0
s_cbranch_scc1 .L_qb1
v_mov_b32 v120,0xff61b1e6
v_mov_b32 v121,0xff61b1e6
v_mov_b32 v122,0xff61b1e6
v_mov_b32 v123,0xff61b1e6
s_nop 1
s_branch .L_qb1
.L_qn3:
s_cmp_lg_u32 s62,17
s_cbranch_scc1 .L_qn31
s_mov_b32 s72,s70
s_nop 1
s_branch .L_qb1
.L_qn31:
s_cmp_lg_u32 s62,15
s_cbranch_scc1 .L_qn35
s_add_u32 s68,s68,38010880
s_cmp_lg_u32 s28,0
s_cbranch_scc1 .L_qb1
v_mov_b32 v120,0xff61b1e6
v_mov_b32 v121,0xff61b1e6
v_mov_b32 v122,0xff61b1e6
v_mov_b32 v123,0xff61b1e6
s_nop 1
s_branch .L_qb1
.L_qn35:
s_branch .L_qb1
.L_rtw1_1:
s_mov_b32 s48,20000
.L_rlw1_1:
s_sleep 8
.L_rfw1_1:
buffer_load_dwordx2 v[12:13],v14,s[0:3],s66 offen sc1
s_sub_u32 s48,s48,1
s_cbranch_scc1 .L_end
s_waitcnt vmcnt(0)
v_cmp_ne_u64 vcc,v[16:17],v[12:13]
s_cbranch_vccnz .L_rlw1_1
s_branch .L_bkw1_1
.L_rth0_0:
s_mov_b32 s48,20000
.L_rlh0_0:
s_sleep 8
.L_rfh0_0:
buffer_load_dwordx2 v[12:13],v14,s[0:3],s66 offen sc1
s_sub_u32 s48,s48,1
s_cbranch_scc1 .L_end
s_waitcnt vmcnt(0)
v_cmp_ne_u64 vcc,v[16:17],v[12:13]
s_cbranch_vccnz .L_rlh0_0
s_branch .L_bkh0_0
.L_rth1_0:
s_mov_b32 s48,20000
.L_rlh1_0:
s_sleep 8
.L_rfh1_0:
buffer_load_dwordx2 v[12:13],v14,s[0:3],s66 offen sc1
s_sub_u32 s48,s48,1
s_cbranch_scc1 .L_end
s_waitcnt vmcnt(0)
v_cmp_ne_u64 vcc,v[16:17],v[12:13]
s_cbranch_vccnz .L_rlh1_0
s_branch .L_bkh1_0
.L_rth0_1:
s_mov_b32 s48,20000
.L_rlh0_1:
s_sleep 8
.L_rfh0_1:
buffer_load_dwordx2 v[12:13],v14,s[0:3],s66 offen sc1
s_sub_u32 s48,s48,1
s_cbranch_scc1 .L_end
s_waitcnt vmcnt(0)
v_cmp_ne_u64 vcc,v[16:17],v[12:13]
s_cbranch_vccnz .L_rlh0_1
s_branch .L_bkh0_1
.L_rth1_1:
s_mov_b32 s48,20000
.L_rlh1_1:
s_sleep 8
.L_rfh1_1:
buffer_load_dwordx2 v[12:13],v14,s[0:3],s66 offen sc1
s_sub_u32 s48,s48,1
s_cbranch_scc1 .L_end
s_waitcnt vmcnt(0)
v_cmp_ne_u64 vcc,v[16:17],v[12:13]
s_cbranch_vccnz .L_rlh1_1
s_branch .L_bkh1_1
.L_rth0_2:
s_mov_b32 s48,20000
.L_rlh0_2:
s_sleep 8
.L_rfh0_2:
buffer_load_dwordx2 v[12:13],v14,s[0:3],s66 offen sc1
s_sub_u32 s48,s48,1
s_cbranch_scc1 .L_end
s_waitcnt vmcnt(0)
v_cmp_ne_u64 vcc,v[16:17],v[12:13]
s_cbranch_vccnz .L_rlh0_2
s_branch .L_bkh0_2
.L_rth1_2:
s_mov_b32 s48,20000
.L_rlh1_2:
s_sleep 8
.L_rfh1_2:
buffer_load_dwordx2 v[12:13],v14,s[0:3],s66 offen sc1
s_sub_u32 s48,s48,1
s_cbranch_scc1 .L_end
s_waitcnt vmcnt(0)
v_cmp_ne_u64 vcc,v[16:17],v[12:13]
s_cbranch_vccnz .L_rlh1_2
s_branch .L_bkh1_2
.L_rth0_3:
s_mov_b32 s48,20000
.L_rlh0_3:
s_sleep 8
.L_rfh0_3:
buffer_load_dwordx2 v[12:13],v14,s[0:3],s66 offen sc1
s_sub_u32 s48,s48,1
s_cbranch_scc1 .L_end
s_waitcnt vmcnt(0)
v_cmp_ne_u64 vcc,v[16:17],v[12:13]
s_cbranch_vccnz .L_rlh0_3
s_branch .L_bkh0_3
.L_rth1_3:
s_mov_b32 s48,20000
.L_rlh1_3:
s_sleep 8
.L_rfh1_3:
buffer_load_dwordx2 v[12:13],v14,s[0:3],s66 offen sc1
s_sub_u32 s48,s48,1
s_cbranch_scc1 .L_end
s_waitcnt vmcnt(0)
v_cmp_ne_u64 vcc,v[16:17],v[12:13]
s_cbranch_vccnz .L_rlh1_3
s_branch .L_bkh1_3
.L_rtl0_1:
s_mov_b32 s48,20000
.L_rll0_1:
s_sleep 8
.L_rfl0_1:
buffer_load_dwordx2 v[6:7],v0,s[0:3],s66 offen sc1
s_sub_u32 s48,s48,1
s_cbranch_scc1 .L_end
)#"
R"#(
s_waitcnt vmcnt(0)
v_cmp_ne_u64 vcc,v[8:9],v[6:7]
s_cbranch_vccnz .L_rll0_1
s_branch .L_bkl0_1
.L_rtl1_1:
s_mov_b32 s48,20000
.L_rll1_1:
s_sleep 8
.L_rfl1_1:
buffer_load_dwordx2 v[6:7],v0,s[0:3],s66 offen sc1
s_sub_u32 s48,s48,1
s_cbranch_scc1 .L_end
s_waitcnt vmcnt(0)
v_cmp_ne_u64 vcc,v[8:9],v[6:7]
s_cbranch_vccnz .L_rll1_1
s_branch .L_bkl1_1
.L_rtm1_1:
s_cmp_eq_u32 s62,0
s_cbranch_scc1 .L_bkm1_1
s_mov_b32 s48,20000
.L_rlm1_1:
s_sleep 8
.L_rfm1_1:
buffer_load_dwordx2 v[6:7],v0,s[0:3],s66 offen sc1
s_sub_u32 s48,s48,1
s_cbranch_scc1 .L_end
s_waitcnt vmcnt(0)
v_cmp_ne_u64 vcc,v[8:9],v[6:7]
s_cbranch_vccnz .L_rlm1_1
s_branch .L_bkm1_1
.L_rtm0_1:
s_cmp_eq_u32 s62,0
s_cbranch_scc1 .L_bkm0_1
s_mov_b32 s48,20000
.L_rlm0_1:
s_sleep 8
.L_rfm0_1:
buffer_load_dwordx2 v[6:7],v0,s[0:3],s66 offen sc1
s_sub_u32 s48,s48,1
s_cbranch_scc1 .L_end
s_waitcnt vmcnt(0)
v_cmp_ne_u64 vcc,v[8:9],v[6:7]
s_cbranch_vccnz .L_rlm0_1
s_branch .L_bkm0_1
.L_rta0_1:
s_mov_b32 s48,20000
.L_rla0_1:
s_sleep 8
.L_rfa0_1:
buffer_load_dwordx2 v[6:7],v0,s[0:3],s66 offen sc1
s_sub_u32 s48,s48,1
s_cbranch_scc1 .L_end
s_waitcnt vmcnt(0)
v_cmp_ne_u64 vcc,v[8:9],v[6:7]
s_cbranch_vccnz .L_rla0_1
s_branch .L_bka0_1
.L_rta1_1:
s_mov_b32 s48,20000
.L_rla1_1:
s_sleep 8
.L_rfa1_1:
buffer_load_dwordx2 v[6:7],v0,s[0:3],s66 offen sc1
s_sub_u32 s48,s48,1
s_cbranch_scc1 .L_end
s_waitcnt vmcnt(0)
v_cmp_ne_u64 vcc,v[8:9],v[6:7]
s_cbranch_vccnz .L_rla1_1
s_branch .L_bka1_1
.L_rtb1_1:
s_cmp_eq_u32 s62,0
s_cbranch_scc1 .L_bkb1_1
s_mov_b32 s48,20000
.L_rlb1_1:
s_sleep 8
.L_rfb1_1:
buffer_load_dwordx2 v[6:7],v0,s[0:3],s66 offen sc1
s_sub_u32 s48,s48,1
s_cbranch_scc1 .L_end
s_waitcnt vmcnt(0)
v_cmp_ne_u64 vcc,v[8:9],v[6:7]
s_cbranch_vccnz .L_rlb1_1
s_branch .L_bkb1_1
.L_rtb0_1:
s_cmp_eq_u32 s62,0
s_cbranch_scc1 .L_bkb0_1
s_mov_b32 s48,20000
.L_rlb0_1:
s_sleep 8
.L_rfb0_1:
buffer_load_dwordx2 v[6:7],v0,s[0:3],s66 offen sc1
s_sub_u32 s48,s48,1
s_cbranch_scc1 .L_end
s_waitcnt vmcnt(0)
v_cmp_ne_u64 vcc,v[8:9],v[6:7]
s_cbranch_vccnz .L_rlb0_1
s_branch .L_bkb0_1
.L_two:
v_lshrrev_b32 v1,4,v2
s_mov_b32 s25,1
s_mov_b32 s73,0
s_bfe_u32 s28,s24,0x20001
s_bfe_u32 s27,s24,0x30003
s_bfe_u32 s29,s24,0x10006
s_lshl2_add_u32 s50,s27,s26
s_add_u32 s48,s28,136
s_mul_i32 s48,s48,296960
s_cmp_eq_u32 s29,0
s_cselect_b32 s48,s48,78397440
s_mul_i32 s70,s50,1152
s_add_u32 s70,s70,s48
v_mov_b32 v21,0x7fff0000
s_branch .L_d
.L_end:
s_waitcnt vmcnt(0) lgkmcnt(0)
s_endpgm
.L_d:
s_lshl_b32 s65,s50,11
s_mul_i32 s48,s50,1152
s_add_u32 s48,s48,4612096
s_add_u32 s65,s65,4833280
s_cmp_eq_u32 s29,0
s_cselect_b32 s48,s65,s48
s_cselect_b32 s49,280,0
s_lshl2_add_u32 s49,s27,s49
s_waitcnt lgkmcnt(0)
s_add_u32 s54,s52,0x7f4a7c15
s_addc_u32 s55,s53,0x9e3779b9
s_lshl_b32 s67,s49,5
s_add_u32 s67,s67,4898816
s_sub_u32 s51,s67,s48
s_add_u32 s92,s32,s51
s_addc_u32 s93,s33,0x10000
s_cmp_eq_u32 s26,0
s_cselect_b32 s94,1,0
s_mov_b32 s95,0x20000
v_mov_b32 v8,s54
v_mov_b32 v9,s55
v_cmp_gt_u32 vcc,12,v1
v_mul_u32_u24 v3,296960,v1
v_mov_b32 v6,0x7fff0000
v_cndmask_b32 v3,v6,v3,vcc
s_add_u32 s51,s67,76021760
buffer_store_dwordx2 v[8:9],v3,s[0:3],s51 offen sc1
s_sub_u32 s67,s48,296960
s_mov_b32 s30,1187840
s_mov_b32 s31,1175552
s_mul_i32 s57,s25,136
s_add_u32 s57,s57,s28
s_mul_i32 s57,s57,296960
s_mul_i32 s48,s25,2375680
s_add_u32 s65,s65,s48
s_add_u32 s65,s65,s57
s_mul_i32 s96,s29,3670016
s_add_u32 s36,s18,0x0
s_addc_u32 s37,s19,0x0
s_and_b32 s37,s37,0xffff
s_mov_b32 s38,0x700000
s_mov_b32 s39,0x20000
s_add_u32 s40,s18,0xfffffc80
s_addc_u32 s41,s19,0xffffffff
s_and_b32 s41,s41,0xffff
s_mov_b32 s42,0x700380
s_mov_b32 s43,0x20000
s_lshl_b32 s48,s27,8
s_add_u32 s96,s96,s48
v_and_b32 v8,3,v1
v_mul_u32_u24 v8,16,v8
s_and_b32 s48,s26,1
s_lshl_b32 s48,s48,6
s_lshr_b32 s49,s26,1
s_lshl_b32 s49,s49,10
s_add_u32 s48,s48,s49
v_add_u32 v8,s48,v8
v_lshrrev_b32 v4,3,v1
v_bfe_u32 v5,v1,4,1
s_and_b32 s48,s26,1
s_lshl_b32 s48,s48,1
v_or_b32 v5,s48,v5
v_lshlrev_b32 v5,1,v5
v_and_b32 v6,7,v1
v_xor_b32 v5,v5,v6
v_mul_u32_u24 v4,2048,v4
v_lshl_add_u32 v7,v5,4,v4
s_mul_i32 s48,s26,16384
s_add_u32 s96,s96,s48
s_mul_i32 s97,s26,2048
s_add_u32 m0,s97,0
s_nop 0
buffer_load_dwordx4 v7,s[36:39],s96 offen offset:0 lds
buffer_load_dwordx4 v7,s[40:43],s96 offen offset:1024 lds
s_add_u32 s96,s96,65536
s_add_u32 m0,s97,8192
s_nop 0
buffer_load_dwordx4 v7,s[36:39],s96 offen offset:0 lds
buffer_load_dwordx4 v7,s[40:43],s96 offen offset:1024 lds
s_add_u32 s96,s96,65536
s_add_u32 m0,s97,16384
s_nop 0
buffer_load_dwordx4 v7,s[36:39],s96 offen offset:0 lds
buffer_load_dwordx4 v7,s[40:43],s96 offen offset:1024 lds
s_add_u32 s96,s96,65536
s_add_u32 m0,s97,24576
s_nop 0
buffer_load_dwordx4 v7,s[36:39],s96 offen offset:0 lds
buffer_load_dwordx4 v7,s[40:43],s96 offen offset:1024 lds
s_add_u32 s96,s96,65536
s_add_u32 m0,s97,32768
s_nop 0
buffer_load_dwordx4 v7,s[36:39],s96 offen offset:0 lds
buffer_load_dwordx4 v7,s[40:43],s96 offen offset:1024 lds
s_add_u32 s96,s96,65536
s_add_u32 m0,s97,40960
s_nop 0
buffer_load_dwordx4 v7,s[36:39],s96 offen offset:0 lds
buffer_load_dwordx4 v7,s[40:43],s96 offen offset:1024 lds
s_add_u32 s96,s96,65536
s_add_u32 m0,s97,49152
s_nop 0
buffer_load_dwordx4 v7,s[36:39],s96 offen offset:0 lds
buffer_load_dwordx4 v7,s[40:43],s96 offen offset:1024 lds
s_add_u32 s96,s96,65536
s_add_u32 m0,s97,57344
s_nop 0
buffer_load_dwordx4 v7,s[36:39],s96 offen offset:0 lds
buffer_load_dwordx4 v7,s[40:43],s96 offen offset:1024 lds
s_add_u32 s96,s96,65536
s_add_u32 m0,s97,65536
s_nop 0
buffer_load_dwordx4 v7,s[36:39],s96 offen offset:0 lds
buffer_load_dwordx4 v7,s[40:43],s96 offen offset:1024 lds
s_add_u32 s96,s96,65536
s_add_u32 m0,s97,73728
s_nop 0
buffer_load_dwordx4 v7,s[36:39],s96 offen offset:0 lds
buffer_load_dwordx4 v7,s[40:43],s96 offen offset:1024 lds
s_add_u32 s96,s96,65536
s_add_u32 m0,s97,81920
s_nop 0
buffer_load_dwordx4 v7,s[36:39],s96 offen offset:0 lds
buffer_load_dwordx4 v7,s[40:43],s96 offen offset:1024 lds
s_add_u32 s96,s96,65536
s_add_u32 m0,s97,90112
s_nop 0
buffer_load_dwordx4 v7,s[36:39],s96 offen offset:0 lds
buffer_load_dwordx4 v7,s[40:43],s96 offen offset:1024 lds
s_add_u32 s96,s96,65536
s_add_u32 m0,s97,98304
s_nop 0
buffer_load_dwordx4 v7,s[36:39],s96 offen offset:0 lds
buffer_load_dwordx4 v7,s[40:43],s96 offen offset:1024 lds
s_add_u32 s96,s96,65536
s_add_u32 m0,s97,106496
s_nop 0
buffer_load_dwordx4 v7,s[36:39],s96 offen offset:0 lds
buffer_load_dwordx4 v7,s[40:43],s96 offen offset:1024 lds
s_add_u32 s96,s96,65536
v_lshrrev_b32 v5,4,v1
v_bfe_u32 v4,v1,3,1
v_and_b32 v6,1,v5
v_lshl_add_u32 v4,v6,1,v4
v_lshlrev_b32 v4,5,v4
v_mul_u32_u24 v5,2048,v5
v_bfe_u32 v6,v1,2,2
v_lshl_add_u32 v5,v6,7,v5
v_xor_b32 v8,v4,v8
v_add_u32 v8,v5,v8
v_add_u32 v9,65536,v8
s_waitcnt vmcnt(24)
s_barrier
s_add_u32 m0,s97,114688
s_nop 0
buffer_load_dwordx4 v7,s[36:39],s96 offen offset:0 lds
buffer_load_dwordx4 v7,s[40:43],s96 offen offset:1024 lds
s_add_u32 s96,s96,65536
s_add_u32 m0,s97,122880
s_nop 0
buffer_load_dwordx4 v7,s[36:39],s96 offen offset:0 lds
buffer_load_dwordx4 v7,s[40:43],s96 offen offset:1024 lds
s_add_u32 s96,s96,65536
ds_read_b64_tr_b16 a[0:1],v8 offset:0
ds_read_b64_tr_b16 a[2:3],v8 offset:512
ds_read_b64_tr_b16 a[4:5],v8 offset:8
ds_read_b64_tr_b16 a[6:7],v8 offset:520
ds_read_b64_tr_b16 a[8:9],v8 offset:8192
ds_read_b64_tr_b16 a[10:11],v8 offset:8704
ds_read_b64_tr_b16 a[12:13],v8 offset:8200
ds_read_b64_tr_b16 a[14:15],v8 offset:8712
s_waitcnt lgkmcnt(0)
s_waitcnt vmcnt(24)
s_barrier
s_add_u32 m0,s97,0
s_nop 0
buffer_load_dwordx4 v7,s[36:39],s96 offen offset:0 lds
buffer_load_dwordx4 v7,s[40:43],s96 offen offset:1024 lds
s_add_u32 s96,s96,65536
s_add_u32 m0,s97,8192
s_nop 0
buffer_load_dwordx4 v7,s[36:39],s96 offen offset:0 lds
buffer_load_dwordx4 v7,s[40:43],s96 offen offset:1024 lds
s_add_u32 s96,s96,65536
ds_read_b64_tr_b16 a[16:17],v8 offset:16384
ds_read_b64_tr_b16 a[18:19],v8 offset:16896
ds_read_b64_tr_b16 a[20:21],v8 offset:16392
ds_read_b64_tr_b16 a[22:23],v8 offset:16904
ds_read_b64_tr_b16 a[24:25],v8 offset:24576
ds_read_b64_tr_b16 a[26:27],v8 offset:25088
ds_read_b64_tr_b16 a[28:29],v8 offset:24584
ds_read_b64_tr_b16 a[30:31],v8 offset:25096
s_waitcnt lgkmcnt(0)
s_waitcnt vmcnt(24)
s_barrier
s_add_u32 m0,s97,16384
s_nop 0
buffer_load_dwordx4 v7,s[36:39],s96 offen offset:0 lds
buffer_load_dwordx4 v7,s[40:43],s96 offen offset:1024 lds
s_add_u32 s96,s96,65536
s_add_u32 m0,s97,24576
s_nop 0
buffer_load_dwordx4 v7,s[36:39],s96 offen offset:0 lds
buffer_load_dwordx4 v7,s[40:43],s96 offen offset:1024 lds
s_add_u32 s96,s96,65536
ds_read_b64_tr_b16 a[32:33],v8 offset:32768
ds_read_b64_tr_b16 a[34:35],v8 offset:33280
ds_read_b64_tr_b16 a[36:37],v8 offset:32776
ds_read_b64_tr_b16 a[38:39],v8 offset:33288
ds_read_b64_tr_b16 a[40:41],v8 offset:40960
ds_read_b64_tr_b16 a[42:43],v8 offset:41472
ds_read_b64_tr_b16 a[44:45],v8 offset:40968
ds_read_b64_tr_b16 a[46:47],v8 offset:41480
s_waitcnt lgkmcnt(0)
s_waitcnt vmcnt(24)
s_barrier
s_add_u32 m0,s97,32768
s_nop 0
buffer_load_dwordx4 v7,s[36:39],s96 offen offset:0 lds
buffer_load_dwordx4 v7,s[40:43],s96 offen offset:1024 lds
s_add_u32 s96,s96,65536
s_add_u32 m0,s97,40960
s_nop 0
buffer_load_dwordx4 v7,s[36:39],s96 offen offset:0 lds
buffer_load_dwordx4 v7,s[40:43],s96 offen offset:1024 lds
s_add_u32 s96,s96,65536
ds_read_b64_tr_b16 a[48:49],v8 offset:49152
ds_read_b64_tr_b16 a[50:51],v8 offset:49664
ds_read_b64_tr_b16 a[52:53],v8 offset:49160
ds_read_b64_tr_b16 a[54:55],v8 offset:49672
ds_read_b64_tr_b16 a[56:57],v8 offset:57344
ds_read_b64_tr_b16 a[58:59],v8 offset:57856
ds_read_b64_tr_b16 a[60:61],v8 offset:57352
ds_read_b64_tr_b16 a[62:63],v8 offset:57864
s_waitcnt lgkmcnt(0)
s_waitcnt vmcnt(24)
s_barrier
s_add_u32 m0,s97,49152
s_nop 0
buffer_load_dwordx4 v7,s[36:39],s96 offen offset:0 lds
buffer_load_dwordx4 v7,s[40:43],s96 offen offset:1024 lds
s_add_u32 s96,s96,65536
s_add_u32 m0,s97,57344
s_nop 0
buffer_load_dwordx4 v7,s[36:39],s96 offen offset:0 lds
buffer_load_dwordx4 v7,s[40:43],s96 offen offset:1024 lds
s_add_u32 s96,s96,65536
ds_read_b64_tr_b16 a[64:65],v9 offset:0
ds_read_b64_tr_b16 a[66:67],v9 offset:512
ds_read_b64_tr_b16 a[68:69],v9 offset:8
ds_read_b64_tr_b16 a[70:71],v9 offset:520
ds_read_b64_tr_b16 a[72:73],v9 offset:8192
ds_read_b64_tr_b16 a[74:75],v9 offset:8704
ds_read_b64_tr_b16 a[76:77],v9 offset:8200
ds_read_b64_tr_b16 a[78:79],v9 offset:8712
s_waitcnt lgkmcnt(0)
s_waitcnt vmcnt(24)
s_barrier
s_add_u32 m0,s97,65536
s_nop 0
buffer_load_dwordx4 v7,s[36:39],s96 offen offset:0 lds
buffer_load_dwordx4 v7,s[40:43],s96 offen offset:1024 lds
s_add_u32 s96,s96,65536
s_add_u32 m0,s97,73728
s_nop 0
buffer_load_dwordx4 v7,s[36:39],s96 offen offset:0 lds
buffer_load_dwordx4 v7,s[40:43],s96 offen offset:1024 lds
s_add_u32 s96,s96,65536
ds_read_b64_tr_b16 a[80:81],v9 offset:16384
ds_read_b64_tr_b16 a[82:83],v9 offset:16896
ds_read_b64_tr_b16 a[84:85],v9 offset:16392
ds_read_b64_tr_b16 a[86:87],v9 offset:16904
ds_read_b64_tr_b16 a[88:89],v9 offset:24576
ds_read_b64_tr_b16 a[90:91],v9 offset:25088
ds_read_b64_tr_b16 a[92:93],v9 offset:24584
ds_read_b64_tr_b16 a[94:95],v9 offset:25096
s_waitcnt lgkmcnt(0)
s_waitcnt vmcnt(24)
s_barrier
s_add_u32 m0,s97,81920
s_nop 0
buffer_load_dwordx4 v7,s[36:39],s96 offen offset:0 lds
buffer_load_dwordx4 v7,s[40:43],s96 offen offset:1024 lds
s_add_u32 s96,s96,65536
s_add_u32 m0,s97,90112
s_nop 0
buffer_load_dwordx4 v7,s[36:39],s96 offen offset:0 lds
buffer_load_dwordx4 v7,s[40:43],s96 offen offset:1024 lds
s_add_u32 s96,s96,65536
ds_read_b64_tr_b16 a[96:97],v9 offset:32768
ds_read_b64_tr_b16 a[98:99],v9 offset:33280
ds_read_b64_tr_b16 a[100:101],v9 offset:32776
ds_read_b64_tr_b16 a[102:103],v9 offset:33288
ds_read_b64_tr_b16 a[104:105],v9 offset:40960
ds_read_b64_tr_b16 a[106:107],v9 offset:41472
ds_read_b64_tr_b16 a[108:109],v9 offset:40968
ds_read_b64_tr_b16 a[110:111],v9 offset:41480
s_waitcnt lgkmcnt(0)
s_waitcnt vmcnt(24)
s_barrier
s_add_u32 m0,s97,98304
s_nop 0
buffer_load_dwordx4 v7,s[36:39],s96 offen offset:0 lds
buffer_load_dwordx4 v7,s[40:43],s96 offen offset:1024 lds
s_add_u32 s96,s96,65536
s_add_u32 m0,s97,106496
s_nop 0
buffer_load_dwordx4 v7,s[36:39],s96 offen offset:0 lds
buffer_load_dwordx4 v7,s[40:43],s96 offen offset:1024 lds
s_add_u32 s96,s96,65536
ds_read_b64_tr_b16 a[112:113],v9 offset:49152
ds_read_b64_tr_b16 a[114:115],v9 offset:49664
ds_read_b64_tr_b16 a[116:117],v9 offset:49160
ds_read_b64_tr_b16 a[118:119],v9 offset:49672
ds_read_b64_tr_b16 a[120:121],v9 offset:57344
ds_read_b64_tr_b16 a[122:123],v9 offset:57856
ds_read_b64_tr_b16 a[124:125],v9 offset:57352
ds_read_b64_tr_b16 a[126:127],v9 offset:57864
s_waitcnt lgkmcnt(0)
s_waitcnt vmcnt(24)
s_barrier
s_add_u32 m0,s97,114688
s_nop 0
buffer_load_dwordx4 v7,s[36:39],s96 offen offset:0 lds
buffer_load_dwordx4 v7,s[40:43],s96 offen offset:1024 lds
s_add_u32 s96,s96,65536
s_add_u32 m0,s97,122880
s_nop 0
buffer_load_dwordx4 v7,s[36:39],s96 offen offset:0 lds
buffer_load_dwordx4 v7,s[40:43],s96 offen offset:1024 lds
s_add_u32 s96,s96,65536
ds_read_b64_tr_b16 a[128:129],v8 offset:0
ds_read_b64_tr_b16 a[130:131],v8 offset:512
ds_read_b64_tr_b16 a[132:133],v8 offset:8
ds_read_b64_tr_b16 a[134:135],v8 offset:520
ds_read_b64_tr_b16 a[136:137],v8 offset:8192
ds_read_b64_tr_b16 a[138:139],v8 offset:8704
ds_read_b64_tr_b16 a[140:141],v8 offset:8200
ds_read_b64_tr_b16 a[142:143],v8 offset:8712
s_waitcnt lgkmcnt(0)
s_waitcnt vmcnt(24)
s_barrier
s_add_u32 m0,s97,0
s_nop 0
buffer_load_dwordx4 v7,s[36:39],s96 offen offset:0 lds
buffer_load_dwordx4 v7,s[40:43],s96 offen offset:1024 lds
s_add_u32 s96,s96,65536
s_add_u32 m0,s97,8192
s_nop 0
buffer_load_dwordx4 v7,s[36:39],s96 offen offset:0 lds
buffer_load_dwordx4 v7,s[40:43],s96 offen offset:1024 lds
s_add_u32 s96,s96,65536
ds_read_b64_tr_b16 a[144:145],v8 offset:16384
ds_read_b64_tr_b16 a[146:147],v8 offset:16896
ds_read_b64_tr_b16 a[148:149],v8 offset:16392
ds_read_b64_tr_b16 a[150:151],v8 offset:16904
ds_read_b64_tr_b16 a[152:153],v8 offset:24576
ds_read_b64_tr_b16 a[154:155],v8 offset:25088
ds_read_b64_tr_b16 a[156:157],v8 offset:24584
ds_read_b64_tr_b16 a[158:159],v8 offset:25096
s_waitcnt lgkmcnt(0)
s_waitcnt vmcnt(24)
s_barrier
s_add_u32 m0,s97,16384
s_nop 0
buffer_load_dwordx4 v7,s[36:39],s96 offen offset:0 lds
buffer_load_dwordx4 v7,s[40:43],s96 offen offset:1024 lds
s_add_u32 s96,s96,65536
s_add_u32 m0,s97,24576
s_nop 0
buffer_load_dwordx4 v7,s[36:39],s96 offen offset:0 lds
buffer_load_dwordx4 v7,s[40:43],s96 offen offset:1024 lds
s_add_u32 s96,s96,65536
ds_read_b64_tr_b16 a[160:161],v8 offset:32768
ds_read_b64_tr_b16 a[162:163],v8 offset:33280
ds_read_b64_tr_b16 a[164:165],v8 offset:32776
ds_read_b64_tr_b16 a[166:167],v8 offset:33288
ds_read_b64_tr_b16 a[168:169],v8 offset:40960
ds_read_b64_tr_b16 a[170:171],v8 offset:41472
ds_read_b64_tr_b16 a[172:173],v8 offset:40968
ds_read_b64_tr_b16 a[174:175],v8 offset:41480
s_waitcnt lgkmcnt(0)
s_waitcnt vmcnt(24)
s_barrier
)#"
R"#(
s_add_u32 m0,s97,32768
s_nop 0
buffer_load_dwordx4 v7,s[36:39],s96 offen offset:0 lds
buffer_load_dwordx4 v7,s[40:43],s96 offen offset:1024 lds
s_add_u32 s96,s96,65536
s_add_u32 m0,s97,40960
s_nop 0
buffer_load_dwordx4 v7,s[36:39],s96 offen offset:0 lds
buffer_load_dwordx4 v7,s[40:43],s96 offen offset:1024 lds
s_add_u32 s96,s96,65536
ds_read_b64_tr_b16 a[176:177],v8 offset:49152
ds_read_b64_tr_b16 a[178:179],v8 offset:49664
ds_read_b64_tr_b16 a[180:181],v8 offset:49160
ds_read_b64_tr_b16 a[182:183],v8 offset:49672
ds_read_b64_tr_b16 a[184:185],v8 offset:57344
ds_read_b64_tr_b16 a[186:187],v8 offset:57856
ds_read_b64_tr_b16 a[188:189],v8 offset:57352
ds_read_b64_tr_b16 a[190:191],v8 offset:57864
s_waitcnt lgkmcnt(0)
s_waitcnt vmcnt(24)
s_barrier
s_add_u32 m0,s97,49152
s_nop 0
buffer_load_dwordx4 v7,s[36:39],s96 offen offset:0 lds
buffer_load_dwordx4 v7,s[40:43],s96 offen offset:1024 lds
s_add_u32 s96,s96,65536
s_add_u32 m0,s97,57344
s_nop 0
buffer_load_dwordx4 v7,s[36:39],s96 offen offset:0 lds
buffer_load_dwordx4 v7,s[40:43],s96 offen offset:1024 lds
s_add_u32 s96,s96,65536
ds_read_b64_tr_b16 a[192:193],v9 offset:0
ds_read_b64_tr_b16 a[194:195],v9 offset:512
ds_read_b64_tr_b16 a[196:197],v9 offset:8
ds_read_b64_tr_b16 a[198:199],v9 offset:520
ds_read_b64_tr_b16 a[200:201],v9 offset:8192
ds_read_b64_tr_b16 a[202:203],v9 offset:8704
ds_read_b64_tr_b16 a[204:205],v9 offset:8200
ds_read_b64_tr_b16 a[206:207],v9 offset:8712
s_waitcnt lgkmcnt(0)
s_waitcnt vmcnt(24)
s_barrier
s_add_u32 m0,s97,65536
s_nop 0
buffer_load_dwordx4 v7,s[36:39],s96 offen offset:0 lds
buffer_load_dwordx4 v7,s[40:43],s96 offen offset:1024 lds
s_add_u32 s96,s96,65536
s_add_u32 m0,s97,73728
s_nop 0
buffer_load_dwordx4 v7,s[36:39],s96 offen offset:0 lds
buffer_load_dwordx4 v7,s[40:43],s96 offen offset:1024 lds
s_add_u32 s96,s96,65536
ds_read_b64_tr_b16 a[208:209],v9 offset:16384
ds_read_b64_tr_b16 a[210:211],v9 offset:16896
ds_read_b64_tr_b16 a[212:213],v9 offset:16392
ds_read_b64_tr_b16 a[214:215],v9 offset:16904
ds_read_b64_tr_b16 a[216:217],v9 offset:24576
ds_read_b64_tr_b16 a[218:219],v9 offset:25088
ds_read_b64_tr_b16 a[220:221],v9 offset:24584
ds_read_b64_tr_b16 a[222:223],v9 offset:25096
s_waitcnt lgkmcnt(0)
s_waitcnt vmcnt(24)
s_barrier
s_add_u32 m0,s97,81920
s_nop 0
buffer_load_dwordx4 v7,s[36:39],s96 offen offset:0 lds
buffer_load_dwordx4 v7,s[40:43],s96 offen offset:1024 lds
s_add_u32 s96,s96,65536
s_add_u32 m0,s97,90112
s_nop 0
buffer_load_dwordx4 v7,s[36:39],s96 offen offset:0 lds
buffer_load_dwordx4 v7,s[40:43],s96 offen offset:1024 lds
s_add_u32 s96,s96,65536
ds_read_b64_tr_b16 a[224:225],v9 offset:32768
ds_read_b64_tr_b16 a[226:227],v9 offset:33280
ds_read_b64_tr_b16 a[228:229],v9 offset:32776
ds_read_b64_tr_b16 a[230:231],v9 offset:33288
ds_read_b64_tr_b16 a[232:233],v9 offset:40960
ds_read_b64_tr_b16 a[234:235],v9 offset:41472
ds_read_b64_tr_b16 a[236:237],v9 offset:40968
ds_read_b64_tr_b16 a[238:239],v9 offset:41480
s_waitcnt lgkmcnt(0)
s_waitcnt vmcnt(24)
s_barrier
s_add_u32 m0,s97,98304
s_nop 0
buffer_load_dwordx4 v7,s[36:39],s96 offen offset:0 lds
buffer_load_dwordx4 v7,s[40:43],s96 offen offset:1024 lds
s_add_u32 s96,s96,65536
s_add_u32 m0,s97,106496
s_nop 0
buffer_load_dwordx4 v7,s[36:39],s96 offen offset:0 lds
buffer_load_dwordx4 v7,s[40:43],s96 offen offset:1024 lds
s_add_u32 s96,s96,65536
ds_read_b64_tr_b16 a[240:241],v9 offset:49152
ds_read_b64_tr_b16 a[242:243],v9 offset:49664
ds_read_b64_tr_b16 a[244:245],v9 offset:49160
ds_read_b64_tr_b16 a[246:247],v9 offset:49672
ds_read_b64_tr_b16 a[248:249],v9 offset:57344
ds_read_b64_tr_b16 a[250:251],v9 offset:57856
ds_read_b64_tr_b16 a[252:253],v9 offset:57352
ds_read_b64_tr_b16 a[254:255],v9 offset:57864
s_waitcnt lgkmcnt(0)
s_waitcnt vmcnt(24)
s_barrier
s_add_u32 m0,s97,114688
s_nop 0
buffer_load_dwordx4 v7,s[36:39],s96 offen offset:0 lds
buffer_load_dwordx4 v7,s[40:43],s96 offen offset:1024 lds
s_add_u32 s96,s96,65536
s_add_u32 m0,s97,122880
s_nop 0
buffer_load_dwordx4 v7,s[36:39],s96 offen offset:0 lds
buffer_load_dwordx4 v7,s[40:43],s96 offen offset:1024 lds
s_add_u32 s96,s96,65536
ds_read_b64_tr_b16 v[64:65],v8 offset:0
ds_read_b64_tr_b16 v[66:67],v8 offset:512
ds_read_b64_tr_b16 v[68:69],v8 offset:8
ds_read_b64_tr_b16 v[70:71],v8 offset:520
ds_read_b64_tr_b16 v[72:73],v8 offset:8192
ds_read_b64_tr_b16 v[74:75],v8 offset:8704
ds_read_b64_tr_b16 v[76:77],v8 offset:8200
ds_read_b64_tr_b16 v[78:79],v8 offset:8712
s_waitcnt lgkmcnt(0)
s_waitcnt vmcnt(24)
s_barrier
s_add_u32 m0,s97,0
s_nop 0
buffer_load_dwordx4 v7,s[36:39],s96 offen offset:0 lds
buffer_load_dwordx4 v7,s[40:43],s96 offen offset:1024 lds
s_add_u32 s96,s96,65536
s_add_u32 m0,s97,8192
s_nop 0
buffer_load_dwordx4 v7,s[36:39],s96 offen offset:0 lds
buffer_load_dwordx4 v7,s[40:43],s96 offen offset:1024 lds
s_add_u32 s96,s96,65536
ds_read_b64_tr_b16 v[80:81],v8 offset:16384
ds_read_b64_tr_b16 v[82:83],v8 offset:16896
ds_read_b64_tr_b16 v[84:85],v8 offset:16392
ds_read_b64_tr_b16 v[86:87],v8 offset:16904
ds_read_b64_tr_b16 v[88:89],v8 offset:24576
ds_read_b64_tr_b16 v[90:91],v8 offset:25088
ds_read_b64_tr_b16 v[92:93],v8 offset:24584
ds_read_b64_tr_b16 v[94:95],v8 offset:25096
s_waitcnt lgkmcnt(0)
s_waitcnt vmcnt(24)
s_barrier
s_add_u32 m0,s97,16384
s_nop 0
buffer_load_dwordx4 v7,s[36:39],s96 offen offset:0 lds
buffer_load_dwordx4 v7,s[40:43],s96 offen offset:1024 lds
s_add_u32 s96,s96,65536
s_add_u32 m0,s97,24576
s_nop 0
buffer_load_dwordx4 v7,s[36:39],s96 offen offset:0 lds
buffer_load_dwordx4 v7,s[40:43],s96 offen offset:1024 lds
s_add_u32 s96,s96,65536
ds_read_b64_tr_b16 v[96:97],v8 offset:32768
ds_read_b64_tr_b16 v[98:99],v8 offset:33280
ds_read_b64_tr_b16 v[100:101],v8 offset:32776
ds_read_b64_tr_b16 v[102:103],v8 offset:33288
ds_read_b64_tr_b16 v[104:105],v8 offset:40960
ds_read_b64_tr_b16 v[106:107],v8 offset:41472
ds_read_b64_tr_b16 v[108:109],v8 offset:40968
ds_read_b64_tr_b16 v[110:111],v8 offset:41480
s_waitcnt lgkmcnt(0)
s_waitcnt vmcnt(24)
s_barrier
s_add_u32 m0,s97,32768
s_nop 0
buffer_load_dwordx4 v7,s[36:39],s96 offen offset:0 lds
buffer_load_dwordx4 v7,s[40:43],s96 offen offset:1024 lds
s_add_u32 s96,s96,65536
s_add_u32 m0,s97,40960
s_nop 0
buffer_load_dwordx4 v7,s[36:39],s96 offen offset:0 lds
buffer_load_dwordx4 v7,s[40:43],s96 offen offset:1024 lds
s_add_u32 s96,s96,65536
ds_read_b64_tr_b16 v[112:113],v8 offset:49152
ds_read_b64_tr_b16 v[114:115],v8 offset:49664
ds_read_b64_tr_b16 v[116:117],v8 offset:49160
ds_read_b64_tr_b16 v[118:119],v8 offset:49672
ds_read_b64_tr_b16 v[120:121],v8 offset:57344
ds_read_b64_tr_b16 v[122:123],v8 offset:57856
ds_read_b64_tr_b16 v[124:125],v8 offset:57352
ds_read_b64_tr_b16 v[126:127],v8 offset:57864
s_waitcnt lgkmcnt(0)
s_waitcnt vmcnt(24)
s_barrier
s_add_u32 m0,s97,49152
s_nop 0
buffer_load_dwordx4 v7,s[36:39],s96 offen offset:0 lds
buffer_load_dwordx4 v7,s[40:43],s96 offen offset:1024 lds
s_add_u32 s96,s96,65536
s_add_u32 m0,s97,57344
s_nop 0
buffer_load_dwordx4 v7,s[36:39],s96 offen offset:0 lds
buffer_load_dwordx4 v7,s[40:43],s96 offen offset:1024 lds
s_add_u32 s96,s96,65536
ds_read_b64_tr_b16 v[128:129],v9 offset:0
ds_read_b64_tr_b16 v[130:131],v9 offset:512
ds_read_b64_tr_b16 v[132:133],v9 offset:8
ds_read_b64_tr_b16 v[134:135],v9 offset:520
ds_read_b64_tr_b16 v[136:137],v9 offset:8192
ds_read_b64_tr_b16 v[138:139],v9 offset:8704
ds_read_b64_tr_b16 v[140:141],v9 offset:8200
ds_read_b64_tr_b16 v[142:143],v9 offset:8712
s_waitcnt lgkmcnt(0)
s_waitcnt vmcnt(24)
s_barrier
ds_read_b64_tr_b16 v[144:145],v9 offset:16384
ds_read_b64_tr_b16 v[146:147],v9 offset:16896
ds_read_b64_tr_b16 v[148:149],v9 offset:16392
ds_read_b64_tr_b16 v[150:151],v9 offset:16904
ds_read_b64_tr_b16 v[152:153],v9 offset:24576
ds_read_b64_tr_b16 v[154:155],v9 offset:25088
ds_read_b64_tr_b16 v[156:157],v9 offset:24584
ds_read_b64_tr_b16 v[158:159],v9 offset:25096
s_waitcnt lgkmcnt(0)
s_waitcnt vmcnt(20)
s_barrier
ds_read_b64_tr_b16 v[160:161],v9 offset:32768
ds_read_b64_tr_b16 v[162:163],v9 offset:33280
ds_read_b64_tr_b16 v[164:165],v9 offset:32776
ds_read_b64_tr_b16 v[166:167],v9 offset:33288
ds_read_b64_tr_b16 v[168:169],v9 offset:40960
ds_read_b64_tr_b16 v[170:171],v9 offset:41472
ds_read_b64_tr_b16 v[172:173],v9 offset:40968
ds_read_b64_tr_b16 v[174:175],v9 offset:41480
s_waitcnt lgkmcnt(0)
s_waitcnt vmcnt(16)
s_barrier
ds_read_b64_tr_b16 v[176:177],v9 offset:49152
ds_read_b64_tr_b16 v[178:179],v9 offset:49664
ds_read_b64_tr_b16 v[180:181],v9 offset:49160
ds_read_b64_tr_b16 v[182:183],v9 offset:49672
ds_read_b64_tr_b16 v[184:185],v9 offset:57344
ds_read_b64_tr_b16 v[186:187],v9 offset:57856
ds_read_b64_tr_b16 v[188:189],v9 offset:57352
ds_read_b64_tr_b16 v[190:191],v9 offset:57864
s_waitcnt lgkmcnt(0)
s_waitcnt vmcnt(12)
s_barrier
ds_read_b64_tr_b16 v[192:193],v8 offset:0
ds_read_b64_tr_b16 v[194:195],v8 offset:512
ds_read_b64_tr_b16 v[196:197],v8 offset:8
ds_read_b64_tr_b16 v[198:199],v8 offset:520
ds_read_b64_tr_b16 v[200:201],v8 offset:8192
ds_read_b64_tr_b16 v[202:203],v8 offset:8704
ds_read_b64_tr_b16 v[204:205],v8 offset:8200
ds_read_b64_tr_b16 v[206:207],v8 offset:8712
s_waitcnt lgkmcnt(0)
s_waitcnt vmcnt(8)
s_barrier
ds_read_b64_tr_b16 v[208:209],v8 offset:16384
ds_read_b64_tr_b16 v[210:211],v8 offset:16896
ds_read_b64_tr_b16 v[212:213],v8 offset:16392
ds_read_b64_tr_b16 v[214:215],v8 offset:16904
ds_read_b64_tr_b16 v[216:217],v8 offset:24576
ds_read_b64_tr_b16 v[218:219],v8 offset:25088
ds_read_b64_tr_b16 v[220:221],v8 offset:24584
ds_read_b64_tr_b16 v[222:223],v8 offset:25096
s_waitcnt lgkmcnt(0)
s_waitcnt vmcnt(4)
s_barrier
ds_read_b64_tr_b16 v[224:225],v8 offset:32768
ds_read_b64_tr_b16 v[226:227],v8 offset:33280
ds_read_b64_tr_b16 v[228:229],v8 offset:32776
ds_read_b64_tr_b16 v[230:231],v8 offset:33288
ds_read_b64_tr_b16 v[232:233],v8 offset:40960
ds_read_b64_tr_b16 v[234:235],v8 offset:41472
ds_read_b64_tr_b16 v[236:237],v8 offset:40968
ds_read_b64_tr_b16 v[238:239],v8 offset:41480
s_waitcnt lgkmcnt(0)
s_waitcnt vmcnt(0)
s_barrier
ds_read_b64_tr_b16 v[240:241],v8 offset:49152
ds_read_b64_tr_b16 v[242:243],v8 offset:49664
ds_read_b64_tr_b16 v[244:245],v8 offset:49160
ds_read_b64_tr_b16 v[246:247],v8 offset:49672
ds_read_b64_tr_b16 v[248:249],v8 offset:57344
ds_read_b64_tr_b16 v[250:251],v8 offset:57856
ds_read_b64_tr_b16 v[252:253],v8 offset:57352
ds_read_b64_tr_b16 v[254:255],v8 offset:57864
s_waitcnt lgkmcnt(0)
s_barrier
v_mov_b32 v8,s54
v_mov_b32 v9,s55
s_lshl2_add_u32 s48,s29,s26
s_mul_i32 s63,s48,14336
s_add_u32 s48,s29,18
s_mul_i32 s49,s29,72
s_add_u32 s49,s49,128
v_lshl_add_u32 v0,v1,2,s49
s_add_u32 s49,s50,280
v_writelane_b32 v0,s49,63
v_lshrrev_b32 v0,2,v0
v_lshlrev_b32 v0,7,v0
v_mov_b32 v3,2375680
v_writelane_b32 v3,0,63
v_add_u32 v0,v3,v0
v_cmp_ge_u32 vcc,s48,v1
s_andn2_b32 s49,s29,s25
s_lshl_b32 s49,s49,31
s_nop 4
s_or_b32 vcc_hi,vcc_hi,s49
s_nop 4
v_mov_b32 v3,0x7fff0000
v_cndmask_b32 v0,v3,v0,vcc
v_cndmask_b32 v8,0,v8,vcc
v_cndmask_b32 v9,0,v9,vcc
v_mov_b32 v3,v21
s_add_u32 s63,s63,s57
s_add_u32 s63,s63,4718592
s_add_u32 s66,s57,2523136
s_mul_i32 s64,s26,14336
s_lshl_b32 s74,s26,11
s_add_u32 s74,s74,114688
v_add_u32 v5,s74,v2
v_add_u32 v4,57344,v2
s_add_u32 s44,s20,0x0
s_addc_u32 s45,s21,0x0
s_and_b32 s45,s45,0xffff
s_mov_b32 s46,0x400000
s_mov_b32 s47,0x20000
s_mov_b32 s62,16
s_sub_u32 s58,111,s28
s_lshr_b32 s58,s58,2
s_lshl_b32 s59,s28,20
s_mov_b32 s75,32768
s_mul_i32 s48,s25,65536
s_add_u32 s59,s59,s48
s_cmp_lg_u32 s25,0
s_cmov_b32 s62,0
s_cmov_b32 s58,-1
s_cbranch_scc1 .L_x
s_cmp_lg_u32 s29,0
s_cbranch_scc1 .L_dh
s_cmp_lg_u32 s25,0
s_cbranch_scc1 .L_dn
s_lshl_b32 s48,s27,7
s_add_u32 s48,s48,s66
s_add_u32 s48,s48,8960
v_add_u32 v10,34,v1
v_mul_u32_u24 v10,1187840,v10
v_writelane_b32 v10,0,32
s_cmp_eq_u32 s26,0
s_cselect_b32 s49,33,0
v_cmp_gt_u32 vcc,s49,v1
v_mov_b32 v11,0x7fff0000
v_mov_b32 v12,s54
v_mov_b32 v13,s55
v_cndmask_b32 v10,v11,v10,vcc
buffer_store_dwordx2 v[12:13],v10,s[32:35],s48 offen sc1
.L_dn:
v_lshrrev_b32 v10,4,v1
v_and_b32 v1,15,v1
s_lshl_b32 s48,s50,6
v_lshl_add_u32 v1,v1,2,s48
v_lshl_add_u32 v1,v10,12,v1
s_add_u32 s71,s70,4681728
s_sub_u32 s68,s65,2375680
s_and_b32 s65,s59,0x3f8000
s_cmp_lg_u32 s26,1
s_cbranch_scc1 .L_skdl
s_mov_b32 s48,20000
s_branch .L_rfdl
.L_rldl:
s_sleep 2
.L_rfdl:
buffer_load_dwordx2 v[6:7],v0,s[0:3],s66 offen sc1
s_sub_u32 s48,s48,1
s_cbranch_scc1 .L_end
s_waitcnt vmcnt(0)
v_cmp_ne_u64 vcc,v[8:9],v[6:7]
s_cbranch_vccnz .L_rldl
.L_skdl:
s_waitcnt vmcnt(0)
s_barrier
s_add_u32 s66,s66,s30
s_add_u32 m0,s64,0
s_nop 0
buffer_load_dwordx4 v2,s[32:35],s63 offen offset:0 sc1 lds
buffer_load_dwordx4 v2,s[32:35],s63 offen offset:1024 sc1 lds
buffer_load_dwordx4 v2,s[32:35],s63 offen offset:2048 sc1 lds
buffer_load_dwordx4 v2,s[32:35],s63 offen offset:3072 sc1 lds
s_add_u32 s63,s63,4096
s_add_u32 m0,s64,4096
s_nop 0
buffer_load_dwordx4 v2,s[32:35],s63 offen offset:0 sc1 lds
buffer_load_dwordx4 v2,s[32:35],s63 offen offset:1024 sc1 lds
buffer_load_dwordx4 v2,s[32:35],s63 offen offset:2048 sc1 lds
buffer_load_dwordx4 v2,s[32:35],s63 offen offset:3072 sc1 lds
s_add_u32 s63,s63,4096
s_add_u32 m0,s64,8192
s_nop 0
buffer_load_dwordx4 v2,s[32:35],s63 offen offset:0 sc1 lds
buffer_load_dwordx4 v2,s[32:35],s63 offen offset:1024 sc1 lds
buffer_load_dwordx4 v2,s[32:35],s63 offen offset:2048 sc1 lds
buffer_load_dwordx4 v2,s[32:35],s63 offen offset:3072 sc1 lds
s_add_u32 s63,s63,4096
s_add_u32 m0,s64,12288
s_nop 0
buffer_load_dwordx4 v2,s[32:35],s63 offen offset:0 sc1 lds
buffer_load_dwordx4 v2,s[32:35],s63 offen offset:1024 sc1 lds
s_add_u32 s63,s63,s31
buffer_load_dwordx4 v[12:15],v2,s[32:35],s71 offen sc1
s_add_u32 s71,s71,s30
buffer_load_dwordx2 v[6:7],v0,s[0:3],s66 offen sc1
s_waitcnt vmcnt(0)
s_barrier
v_lshlrev_b32 v16,16,v12
v_and_b32 v17,0xffff0000,v12
v_lshlrev_b32 v18,16,v13
v_and_b32 v19,0xffff0000,v13
v_lshlrev_b32 v20,16,v14
v_and_b32 v21,0xffff0000,v14
v_lshlrev_b32 v22,16,v15
v_and_b32 v23,0xffff0000,v15
ds_read_b128 v[32:35],v2 offset:0
ds_read_b128 v[36:39],v2 offset:14336
ds_read_b128 v[40:43],v2 offset:28672
ds_read_b128 v[44:47],v2 offset:43008
ds_read_b128 v[48:51],v2 offset:1024
ds_read_b128 v[52:55],v2 offset:15360
.L_dlgo:
s_cmp_eq_u32 s26,1
s_cbranch_scc1 .L_dlv1
s_cmp_eq_u32 s26,2
s_cbranch_scc1 .L_dlv2
s_cmp_eq_u32 s26,3
s_cbranch_scc1 .L_dlv3
.p2align 6
.L_dlv0:
.L_dll_0:
s_waitcnt lgkmcnt(4)
v_mfma_f32_16x16x32_bf16 v[16:19],a[0:3],v[32:35],v[16:19]
ds_read_b128 v[56:59],v2 offset:29696
v_mfma_f32_16x16x32_bf16 v[16:19],a[112:115],v[36:39],v[16:19]
v_mfma_f32_16x16x32_bf16 v[20:23],a[116:119],v[36:39],v[20:23]
ds_read_b128 v[60:63],v2 offset:44032
v_mfma_f32_16x16x32_bf16 v[20:23],a[4:7],v[32:35],v[20:23]
s_add_u32 s68,s68,1187840
s_waitcnt lgkmcnt(4)
v_mfma_f32_16x16x32_bf16 v[20:23],a[228:231],v[40:43],v[20:23]
ds_read_b128 v[32:35],v2 offset:2048
v_mfma_f32_16x16x32_bf16 v[20:23],v[148:151],v[44:47],v[20:23]
buffer_store_dwordx4 v[28:31],v2,s[32:35],s68 offen offset:1024 sc1
v_mfma_f32_16x16x32_bf16 v[16:19],v[144:147],v[44:47],v[16:19]
ds_read_b128 v[36:39],v2 offset:16384
v_mfma_f32_16x16x32_bf16 v[16:19],a[224:227],v[40:43],v[16:19]
s_waitcnt lgkmcnt(4)
v_mfma_f32_16x16x32_bf16 v[16:19],a[8:11],v[48:51],v[16:19]
ds_read_b128 v[40:43],v2 offset:30720
v_mfma_f32_16x16x32_bf16 v[16:19],a[120:123],v[52:55],v[16:19]
buffer_store_dwordx4 v[24:27],v2,s[32:35],s68 offen sc1
s_mov_b32 s67,s68
v_mfma_f32_16x16x32_bf16 v[20:23],a[124:127],v[52:55],v[20:23]
ds_read_b128 v[44:47],v2 offset:45056
v_mfma_f32_16x16x32_bf16 v[20:23],a[12:15],v[48:51],v[20:23]
s_waitcnt lgkmcnt(4)
v_mfma_f32_16x16x32_bf16 v[20:23],a[236:239],v[56:59],v[20:23]
ds_read_b128 v[48:51],v2 offset:3072
v_mfma_f32_16x16x32_bf16 v[20:23],v[156:159],v[60:63],v[20:23]
v_mfma_f32_16x16x32_bf16 v[16:19],v[152:155],v[60:63],v[16:19]
ds_read_b128 v[52:55],v2 offset:17408
v_mfma_f32_16x16x32_bf16 v[16:19],a[232:235],v[56:59],v[16:19]
s_waitcnt lgkmcnt(4)
v_mfma_f32_16x16x32_bf16 v[16:19],a[16:19],v[32:35],v[16:19]
ds_read_b128 v[56:59],v2 offset:31744
v_mfma_f32_16x16x32_bf16 v[16:19],a[128:131],v[36:39],v[16:19]
v_mfma_f32_16x16x32_bf16 v[20:23],a[132:135],v[36:39],v[20:23]
ds_read_b128 v[60:63],v2 offset:46080
v_mfma_f32_16x16x32_bf16 v[20:23],a[20:23],v[32:35],v[20:23]
s_waitcnt lgkmcnt(4)
s_waitcnt vmcnt(3)
s_barrier
v_mfma_f32_16x16x32_bf16 v[20:23],a[244:247],v[40:43],v[20:23]
ds_read_b128 v[32:35],v2 offset:4096
s_add_u32 m0,s64,57344
v_mfma_f32_16x16x32_bf16 v[20:23],v[164:167],v[44:47],v[20:23]
buffer_load_dwordx4 v2,s[32:35],s63 offen offset:0 sc1 lds
v_mfma_f32_16x16x32_bf16 v[16:19],v[160:163],v[44:47],v[16:19]
ds_read_b128 v[36:39],v2 offset:18432
v_mfma_f32_16x16x32_bf16 v[16:19],a[240:243],v[40:43],v[16:19]
s_waitcnt lgkmcnt(4)
v_mfma_f32_16x16x32_bf16 v[16:19],a[24:27],v[48:51],v[16:19]
ds_read_b128 v[40:43],v2 offset:32768
v_mfma_f32_16x16x32_bf16 v[16:19],a[136:139],v[52:55],v[16:19]
v_mfma_f32_16x16x32_bf16 v[20:23],a[140:143],v[52:55],v[20:23]
ds_read_b128 v[44:47],v2 offset:47104
v_mfma_f32_16x16x32_bf16 v[20:23],a[28:31],v[48:51],v[20:23]
s_waitcnt lgkmcnt(4)
v_mfma_f32_16x16x32_bf16 v[20:23],a[252:255],v[56:59],v[20:23]
ds_read_b128 v[48:51],v2 offset:5120
v_mfma_f32_16x16x32_bf16 v[20:23],v[172:175],v[60:63],v[20:23]
buffer_load_dwordx4 v2,s[32:35],s63 offen offset:1024 sc1 lds
v_mfma_f32_16x16x32_bf16 v[16:19],v[168:171],v[60:63],v[16:19]
ds_read_b128 v[52:55],v2 offset:19456
v_mfma_f32_16x16x32_bf16 v[16:19],a[248:251],v[56:59],v[16:19]
)#"
R"#(
s_waitcnt lgkmcnt(4)
v_mfma_f32_16x16x32_bf16 v[16:19],a[32:35],v[32:35],v[16:19]
ds_read_b128 v[56:59],v2 offset:33792
v_mfma_f32_16x16x32_bf16 v[16:19],a[144:147],v[36:39],v[16:19]
buffer_load_dwordx4 v2,s[32:35],s63 offen offset:2048 sc1 lds
v_mfma_f32_16x16x32_bf16 v[20:23],a[148:151],v[36:39],v[20:23]
ds_read_b128 v[60:63],v2 offset:48128
v_mfma_f32_16x16x32_bf16 v[20:23],a[36:39],v[32:35],v[20:23]
s_waitcnt lgkmcnt(4)
v_mfma_f32_16x16x32_bf16 v[20:23],v[68:71],v[40:43],v[20:23]
ds_read_b128 v[32:35],v2 offset:6144
v_mfma_f32_16x16x32_bf16 v[20:23],v[180:183],v[44:47],v[20:23]
buffer_load_dwordx4 v2,s[32:35],s63 offen offset:3072 sc1 lds
v_mfma_f32_16x16x32_bf16 v[16:19],v[176:179],v[44:47],v[16:19]
ds_read_b128 v[36:39],v2 offset:20480
v_mfma_f32_16x16x32_bf16 v[16:19],v[64:67],v[40:43],v[16:19]
s_waitcnt lgkmcnt(4)
v_mfma_f32_16x16x32_bf16 v[16:19],a[40:43],v[48:51],v[16:19]
ds_read_b128 v[40:43],v2 offset:34816
v_mfma_f32_16x16x32_bf16 v[16:19],a[152:155],v[52:55],v[16:19]
buffer_load_dwordx4 v[12:15],v2,s[32:35],s71 offen sc1
s_add_u32 s71,s71,s30
v_mfma_f32_16x16x32_bf16 v[20:23],a[156:159],v[52:55],v[20:23]
ds_read_b128 v[44:47],v2 offset:49152
s_add_u32 s63,s63,4096
v_mfma_f32_16x16x32_bf16 v[20:23],a[44:47],v[48:51],v[20:23]
s_add_u32 m0,s64,61440
s_waitcnt lgkmcnt(4)
v_mfma_f32_16x16x32_bf16 v[20:23],v[76:79],v[56:59],v[20:23]
ds_read_b128 v[48:51],v2 offset:7168
v_mfma_f32_16x16x32_bf16 v[20:23],v[188:191],v[60:63],v[20:23]
buffer_load_dwordx4 v2,s[32:35],s63 offen offset:0 sc1 lds
v_mfma_f32_16x16x32_bf16 v[16:19],v[184:187],v[60:63],v[16:19]
ds_read_b128 v[52:55],v2 offset:21504
v_mfma_f32_16x16x32_bf16 v[16:19],v[72:75],v[56:59],v[16:19]
s_waitcnt lgkmcnt(4)
v_mfma_f32_16x16x32_bf16 v[16:19],a[48:51],v[32:35],v[16:19]
ds_read_b128 v[56:59],v2 offset:35840
v_mfma_f32_16x16x32_bf16 v[16:19],a[160:163],v[36:39],v[16:19]
buffer_load_dwordx4 v2,s[32:35],s63 offen offset:1024 sc1 lds
v_mfma_f32_16x16x32_bf16 v[20:23],a[164:167],v[36:39],v[20:23]
ds_read_b128 v[60:63],v2 offset:50176
v_mfma_f32_16x16x32_bf16 v[20:23],a[52:55],v[32:35],v[20:23]
s_waitcnt lgkmcnt(4)
v_mfma_f32_16x16x32_bf16 v[20:23],v[84:87],v[40:43],v[20:23]
ds_read_b128 v[32:35],v2 offset:8192
v_mfma_f32_16x16x32_bf16 v[20:23],v[196:199],v[44:47],v[20:23]
buffer_load_dwordx4 v2,s[32:35],s63 offen offset:2048 sc1 lds
v_mfma_f32_16x16x32_bf16 v[16:19],v[192:195],v[44:47],v[16:19]
ds_read_b128 v[36:39],v2 offset:22528
v_mfma_f32_16x16x32_bf16 v[16:19],v[80:83],v[40:43],v[16:19]
s_waitcnt lgkmcnt(4)
v_mfma_f32_16x16x32_bf16 v[16:19],a[56:59],v[48:51],v[16:19]
ds_read_b128 v[40:43],v2 offset:36864
v_mfma_f32_16x16x32_bf16 v[16:19],a[168:171],v[52:55],v[16:19]
buffer_load_dwordx4 v2,s[32:35],s63 offen offset:3072 sc1 lds
v_mfma_f32_16x16x32_bf16 v[20:23],a[172:175],v[52:55],v[20:23]
ds_read_b128 v[44:47],v2 offset:51200
v_mfma_f32_16x16x32_bf16 v[20:23],a[60:63],v[48:51],v[20:23]
s_waitcnt lgkmcnt(4)
v_mfma_f32_16x16x32_bf16 v[20:23],v[92:95],v[56:59],v[20:23]
ds_read_b128 v[48:51],v2 offset:9216
v_mfma_f32_16x16x32_bf16 v[20:23],v[204:207],v[60:63],v[20:23]
s_add_u32 s63,s63,4096
s_add_u32 m0,s64,65536
v_mfma_f32_16x16x32_bf16 v[16:19],v[200:203],v[60:63],v[16:19]
ds_read_b128 v[52:55],v2 offset:23552
v_mfma_f32_16x16x32_bf16 v[16:19],v[88:91],v[56:59],v[16:19]
s_waitcnt lgkmcnt(4)
v_mfma_f32_16x16x32_bf16 v[16:19],a[64:67],v[32:35],v[16:19]
ds_read_b128 v[56:59],v2 offset:37888
v_mfma_f32_16x16x32_bf16 v[16:19],a[176:179],v[36:39],v[16:19]
buffer_load_dwordx4 v2,s[32:35],s63 offen offset:0 sc1 lds
v_mfma_f32_16x16x32_bf16 v[20:23],a[180:183],v[36:39],v[20:23]
ds_read_b128 v[60:63],v2 offset:52224
v_mfma_f32_16x16x32_bf16 v[20:23],a[68:71],v[32:35],v[20:23]
s_waitcnt lgkmcnt(4)
v_mfma_f32_16x16x32_bf16 v[20:23],v[100:103],v[40:43],v[20:23]
ds_read_b128 v[32:35],v2 offset:10240
v_mfma_f32_16x16x32_bf16 v[20:23],v[212:215],v[44:47],v[20:23]
buffer_load_dwordx4 v2,s[32:35],s63 offen offset:1024 sc1 lds
v_mfma_f32_16x16x32_bf16 v[16:19],v[208:211],v[44:47],v[16:19]
ds_read_b128 v[36:39],v2 offset:24576
v_mfma_f32_16x16x32_bf16 v[16:19],v[96:99],v[40:43],v[16:19]
s_waitcnt lgkmcnt(4)
v_mfma_f32_16x16x32_bf16 v[16:19],a[72:75],v[48:51],v[16:19]
ds_read_b128 v[40:43],v2 offset:38912
v_mfma_f32_16x16x32_bf16 v[16:19],a[184:187],v[52:55],v[16:19]
buffer_load_dwordx4 v2,s[32:35],s63 offen offset:2048 sc1 lds
v_mfma_f32_16x16x32_bf16 v[20:23],a[188:191],v[52:55],v[20:23]
ds_read_b128 v[44:47],v2 offset:53248
v_mfma_f32_16x16x32_bf16 v[20:23],a[76:79],v[48:51],v[20:23]
s_waitcnt lgkmcnt(4)
v_mfma_f32_16x16x32_bf16 v[20:23],v[108:111],v[56:59],v[20:23]
ds_read_b128 v[48:51],v2 offset:11264
v_mfma_f32_16x16x32_bf16 v[20:23],v[220:223],v[60:63],v[20:23]
v_mfma_f32_16x16x32_bf16 v[16:19],v[216:219],v[60:63],v[16:19]
ds_read_b128 v[52:55],v2 offset:25600
v_mfma_f32_16x16x32_bf16 v[16:19],v[104:107],v[56:59],v[16:19]
s_waitcnt lgkmcnt(4)
v_mfma_f32_16x16x32_bf16 v[16:19],a[80:83],v[32:35],v[16:19]
ds_read_b128 v[56:59],v2 offset:39936
v_mfma_f32_16x16x32_bf16 v[16:19],a[192:195],v[36:39],v[16:19]
buffer_load_dwordx4 v2,s[32:35],s63 offen offset:3072 sc1 lds
s_add_u32 s63,s63,4096
v_mfma_f32_16x16x32_bf16 v[20:23],a[196:199],v[36:39],v[20:23]
ds_read_b128 v[60:63],v2 offset:54272
s_add_u32 m0,s64,69632
v_mfma_f32_16x16x32_bf16 v[20:23],a[84:87],v[32:35],v[20:23]
s_waitcnt lgkmcnt(4)
v_mfma_f32_16x16x32_bf16 v[20:23],v[116:119],v[40:43],v[20:23]
ds_read_b128 v[32:35],v2 offset:12288
v_mfma_f32_16x16x32_bf16 v[20:23],v[228:231],v[44:47],v[20:23]
buffer_load_dwordx4 v2,s[32:35],s63 offen offset:0 sc1 lds
v_mfma_f32_16x16x32_bf16 v[16:19],v[224:227],v[44:47],v[16:19]
ds_read_b128 v[36:39],v2 offset:26624
v_mfma_f32_16x16x32_bf16 v[16:19],v[112:115],v[40:43],v[16:19]
s_waitcnt lgkmcnt(4)
v_mfma_f32_16x16x32_bf16 v[16:19],a[88:91],v[48:51],v[16:19]
ds_read_b128 v[40:43],v2 offset:40960
v_mfma_f32_16x16x32_bf16 v[16:19],a[200:203],v[52:55],v[16:19]
buffer_load_dwordx4 v2,s[32:35],s63 offen offset:1024 sc1 lds
s_add_u32 s63,s63,s31
v_mfma_f32_16x16x32_bf16 v[20:23],a[204:207],v[52:55],v[20:23]
ds_read_b128 v[44:47],v2 offset:55296
v_mfma_f32_16x16x32_bf16 v[20:23],a[92:95],v[48:51],v[20:23]
s_waitcnt lgkmcnt(4)
v_mfma_f32_16x16x32_bf16 v[20:23],v[124:127],v[56:59],v[20:23]
ds_read_b128 v[48:51],v2 offset:13312
s_waitcnt vmcnt(10)
v_mfma_f32_16x16x32_bf16 v[20:23],v[236:239],v[60:63],v[20:23]
v_lshlrev_b32 v24,16,v12
v_and_b32 v25,0xffff0000,v12
v_mfma_f32_16x16x32_bf16 v[16:19],v[232:235],v[60:63],v[16:19]
ds_read_b128 v[52:55],v2 offset:27648
v_lshlrev_b32 v26,16,v13
v_mfma_f32_16x16x32_bf16 v[16:19],v[120:123],v[56:59],v[16:19]
v_and_b32 v27,0xffff0000,v13
s_waitcnt lgkmcnt(4)
v_mfma_f32_16x16x32_bf16 v[16:19],a[96:99],v[32:35],v[16:19]
ds_read_b128 v[56:59],v2 offset:41984
v_lshlrev_b32 v28,16,v14
v_mfma_f32_16x16x32_bf16 v[16:19],a[208:211],v[36:39],v[16:19]
v_and_b32 v29,0xffff0000,v14
v_lshlrev_b32 v30,16,v15
v_mfma_f32_16x16x32_bf16 v[20:23],a[212:215],v[36:39],v[20:23]
ds_read_b128 v[60:63],v2 offset:56320
v_and_b32 v31,0xffff0000,v15
v_mfma_f32_16x16x32_bf16 v[20:23],a[100:103],v[32:35],v[20:23]
s_waitcnt lgkmcnt(4)
s_barrier
v_mfma_f32_16x16x32_bf16 v[20:23],v[132:135],v[40:43],v[20:23]
ds_read_b128 v[32:35],v4 offset:0
v_mfma_f32_16x16x32_bf16 v[20:23],v[244:247],v[44:47],v[20:23]
v_mfma_f32_16x16x32_bf16 v[16:19],v[240:243],v[44:47],v[16:19]
ds_read_b128 v[36:39],v4 offset:14336
v_mfma_f32_16x16x32_bf16 v[16:19],v[128:131],v[40:43],v[16:19]
s_waitcnt lgkmcnt(4)
v_mfma_f32_16x16x32_bf16 v[16:19],a[104:107],v[48:51],v[16:19]
ds_read_b128 v[40:43],v4 offset:28672
v_mfma_f32_16x16x32_bf16 v[16:19],a[216:219],v[52:55],v[16:19]
buffer_store_dwordx2 v[8:9],v2,s[92:95],s67 idxen sc1
v_mfma_f32_16x16x32_bf16 v[20:23],a[220:223],v[52:55],v[20:23]
ds_read_b128 v[44:47],v4 offset:43008
v_mfma_f32_16x16x32_bf16 v[20:23],a[108:111],v[48:51],v[20:23]
s_waitcnt lgkmcnt(4)
v_mfma_f32_16x16x32_bf16 v[20:23],v[140:143],v[56:59],v[20:23]
ds_read_b128 v[48:51],v4 offset:1024
v_mfma_f32_16x16x32_bf16 v[20:23],v[252:255],v[60:63],v[20:23]
v_mfma_f32_16x16x32_bf16 v[16:19],v[248:251],v[60:63],v[16:19]
ds_read_b128 v[52:55],v4 offset:15360
s_sub_u32 s62,s62,1
v_mfma_f32_16x16x32_bf16 v[16:19],v[136:139],v[56:59],v[16:19]
s_cbranch_scc1 .L_dlb_0
s_waitcnt lgkmcnt(4)
v_mfma_f32_16x16x32_bf16 v[24:27],a[0:3],v[32:35],v[24:27]
ds_read_b128 v[56:59],v4 offset:29696
v_mfma_f32_16x16x32_bf16 v[24:27],a[112:115],v[36:39],v[24:27]
v_mfma_f32_16x16x32_bf16 v[28:31],a[116:119],v[36:39],v[28:31]
ds_read_b128 v[60:63],v4 offset:44032
v_mfma_f32_16x16x32_bf16 v[28:31],a[4:7],v[32:35],v[28:31]
s_add_u32 s68,s68,1187840
s_waitcnt lgkmcnt(4)
v_mfma_f32_16x16x32_bf16 v[28:31],a[228:231],v[40:43],v[28:31]
ds_read_b128 v[32:35],v4 offset:2048
v_mfma_f32_16x16x32_bf16 v[28:31],v[148:151],v[44:47],v[28:31]
buffer_store_dwordx4 v[20:23],v2,s[32:35],s68 offen offset:1024 sc1
v_mfma_f32_16x16x32_bf16 v[24:27],v[144:147],v[44:47],v[24:27]
ds_read_b128 v[36:39],v4 offset:16384
v_mfma_f32_16x16x32_bf16 v[24:27],a[224:227],v[40:43],v[24:27]
s_waitcnt lgkmcnt(4)
v_mfma_f32_16x16x32_bf16 v[24:27],a[8:11],v[48:51],v[24:27]
ds_read_b128 v[40:43],v4 offset:30720
v_mfma_f32_16x16x32_bf16 v[24:27],a[120:123],v[52:55],v[24:27]
buffer_store_dwordx4 v[16:19],v2,s[32:35],s68 offen sc1
s_mov_b32 s67,s68
v_mfma_f32_16x16x32_bf16 v[28:31],a[124:127],v[52:55],v[28:31]
ds_read_b128 v[44:47],v4 offset:45056
v_mfma_f32_16x16x32_bf16 v[28:31],a[12:15],v[48:51],v[28:31]
s_waitcnt lgkmcnt(4)
v_mfma_f32_16x16x32_bf16 v[28:31],a[236:239],v[56:59],v[28:31]
ds_read_b128 v[48:51],v4 offset:3072
v_mfma_f32_16x16x32_bf16 v[28:31],v[156:159],v[60:63],v[28:31]
v_mfma_f32_16x16x32_bf16 v[24:27],v[152:155],v[60:63],v[24:27]
ds_read_b128 v[52:55],v4 offset:17408
v_mfma_f32_16x16x32_bf16 v[24:27],a[232:235],v[56:59],v[24:27]
s_waitcnt lgkmcnt(4)
v_mfma_f32_16x16x32_bf16 v[24:27],a[16:19],v[32:35],v[24:27]
ds_read_b128 v[56:59],v4 offset:31744
v_mfma_f32_16x16x32_bf16 v[24:27],a[128:131],v[36:39],v[24:27]
v_mfma_f32_16x16x32_bf16 v[28:31],a[132:135],v[36:39],v[28:31]
ds_read_b128 v[60:63],v4 offset:46080
v_mfma_f32_16x16x32_bf16 v[28:31],a[20:23],v[32:35],v[28:31]
s_waitcnt lgkmcnt(4)
s_waitcnt vmcnt(3)
s_barrier
v_mfma_f32_16x16x32_bf16 v[28:31],a[244:247],v[40:43],v[28:31]
ds_read_b128 v[32:35],v4 offset:4096
s_add_u32 m0,s64,0
v_mfma_f32_16x16x32_bf16 v[28:31],v[164:167],v[44:47],v[28:31]
buffer_load_dwordx4 v2,s[32:35],s63 offen offset:0 sc1 lds
v_mfma_f32_16x16x32_bf16 v[24:27],v[160:163],v[44:47],v[24:27]
ds_read_b128 v[36:39],v4 offset:18432
v_mfma_f32_16x16x32_bf16 v[24:27],a[240:243],v[40:43],v[24:27]
s_waitcnt lgkmcnt(4)
v_mfma_f32_16x16x32_bf16 v[24:27],a[24:27],v[48:51],v[24:27]
ds_read_b128 v[40:43],v4 offset:32768
v_mfma_f32_16x16x32_bf16 v[24:27],a[136:139],v[52:55],v[24:27]
v_mfma_f32_16x16x32_bf16 v[28:31],a[140:143],v[52:55],v[28:31]
ds_read_b128 v[44:47],v4 offset:47104
v_mfma_f32_16x16x32_bf16 v[28:31],a[28:31],v[48:51],v[28:31]
s_waitcnt lgkmcnt(4)
v_mfma_f32_16x16x32_bf16 v[28:31],a[252:255],v[56:59],v[28:31]
ds_read_b128 v[48:51],v4 offset:5120
v_mfma_f32_16x16x32_bf16 v[28:31],v[172:175],v[60:63],v[28:31]
buffer_load_dwordx4 v2,s[32:35],s63 offen offset:1024 sc1 lds
v_mfma_f32_16x16x32_bf16 v[24:27],v[168:171],v[60:63],v[24:27]
ds_read_b128 v[52:55],v4 offset:19456
v_mfma_f32_16x16x32_bf16 v[24:27],a[248:251],v[56:59],v[24:27]
s_waitcnt lgkmcnt(4)
v_mfma_f32_16x16x32_bf16 v[24:27],a[32:35],v[32:35],v[24:27]
ds_read_b128 v[56:59],v4 offset:33792
v_mfma_f32_16x16x32_bf16 v[24:27],a[144:147],v[36:39],v[24:27]
buffer_load_dwordx4 v2,s[32:35],s63 offen offset:2048 sc1 lds
v_mfma_f32_16x16x32_bf16 v[28:31],a[148:151],v[36:39],v[28:31]
ds_read_b128 v[60:63],v4 offset:48128
v_mfma_f32_16x16x32_bf16 v[28:31],a[36:39],v[32:35],v[28:31]
s_waitcnt lgkmcnt(4)
v_mfma_f32_16x16x32_bf16 v[28:31],v[68:71],v[40:43],v[28:31]
ds_read_b128 v[32:35],v4 offset:6144
v_mfma_f32_16x16x32_bf16 v[28:31],v[180:183],v[44:47],v[28:31]
buffer_load_dwordx4 v2,s[32:35],s63 offen offset:3072 sc1 lds
v_mfma_f32_16x16x32_bf16 v[24:27],v[176:179],v[44:47],v[24:27]
ds_read_b128 v[36:39],v4 offset:20480
v_mfma_f32_16x16x32_bf16 v[24:27],v[64:67],v[40:43],v[24:27]
s_waitcnt lgkmcnt(4)
v_mfma_f32_16x16x32_bf16 v[24:27],a[40:43],v[48:51],v[24:27]
ds_read_b128 v[40:43],v4 offset:34816
v_mfma_f32_16x16x32_bf16 v[24:27],a[152:155],v[52:55],v[24:27]
buffer_load_dwordx4 v[12:15],v2,s[32:35],s71 offen sc1
s_add_u32 s71,s71,s30
v_mfma_f32_16x16x32_bf16 v[28:31],a[156:159],v[52:55],v[28:31]
ds_read_b128 v[44:47],v4 offset:49152
s_add_u32 s63,s63,4096
v_mfma_f32_16x16x32_bf16 v[28:31],a[44:47],v[48:51],v[28:31]
s_add_u32 m0,s64,4096
s_waitcnt lgkmcnt(4)
v_mfma_f32_16x16x32_bf16 v[28:31],v[76:79],v[56:59],v[28:31]
ds_read_b128 v[48:51],v4 offset:7168
v_mfma_f32_16x16x32_bf16 v[28:31],v[188:191],v[60:63],v[28:31]
buffer_load_dwordx4 v2,s[32:35],s63 offen offset:0 sc1 lds
v_mfma_f32_16x16x32_bf16 v[24:27],v[184:187],v[60:63],v[24:27]
ds_read_b128 v[52:55],v4 offset:21504
v_mfma_f32_16x16x32_bf16 v[24:27],v[72:75],v[56:59],v[24:27]
s_waitcnt lgkmcnt(4)
v_mfma_f32_16x16x32_bf16 v[24:27],a[48:51],v[32:35],v[24:27]
ds_read_b128 v[56:59],v4 offset:35840
v_mfma_f32_16x16x32_bf16 v[24:27],a[160:163],v[36:39],v[24:27]
buffer_load_dwordx4 v2,s[32:35],s63 offen offset:1024 sc1 lds
v_mfma_f32_16x16x32_bf16 v[28:31],a[164:167],v[36:39],v[28:31]
ds_read_b128 v[60:63],v4 offset:50176
v_mfma_f32_16x16x32_bf16 v[28:31],a[52:55],v[32:35],v[28:31]
s_waitcnt lgkmcnt(4)
v_mfma_f32_16x16x32_bf16 v[28:31],v[84:87],v[40:43],v[28:31]
ds_read_b128 v[32:35],v4 offset:8192
v_mfma_f32_16x16x32_bf16 v[28:31],v[196:199],v[44:47],v[28:31]
buffer_load_dwordx4 v2,s[32:35],s63 offen offset:2048 sc1 lds
v_mfma_f32_16x16x32_bf16 v[24:27],v[192:195],v[44:47],v[24:27]
ds_read_b128 v[36:39],v4 offset:22528
v_mfma_f32_16x16x32_bf16 v[24:27],v[80:83],v[40:43],v[24:27]
s_waitcnt lgkmcnt(4)
v_mfma_f32_16x16x32_bf16 v[24:27],a[56:59],v[48:51],v[24:27]
ds_read_b128 v[40:43],v4 offset:36864
v_mfma_f32_16x16x32_bf16 v[24:27],a[168:171],v[52:55],v[24:27]
buffer_load_dwordx4 v2,s[32:35],s63 offen offset:3072 sc1 lds
v_mfma_f32_16x16x32_bf16 v[28:31],a[172:175],v[52:55],v[28:31]
ds_read_b128 v[44:47],v4 offset:51200
v_mfma_f32_16x16x32_bf16 v[28:31],a[60:63],v[48:51],v[28:31]
s_waitcnt lgkmcnt(4)
v_mfma_f32_16x16x32_bf16 v[28:31],v[92:95],v[56:59],v[28:31]
ds_read_b128 v[48:51],v4 offset:9216
v_mfma_f32_16x16x32_bf16 v[28:31],v[204:207],v[60:63],v[28:31]
s_add_u32 s63,s63,4096
s_add_u32 m0,s64,8192
v_mfma_f32_16x16x32_bf16 v[24:27],v[200:203],v[60:63],v[24:27]
ds_read_b128 v[52:55],v4 offset:23552
v_mfma_f32_16x16x32_bf16 v[24:27],v[88:91],v[56:59],v[24:27]
s_waitcnt lgkmcnt(4)
v_mfma_f32_16x16x32_bf16 v[24:27],a[64:67],v[32:35],v[24:27]
ds_read_b128 v[56:59],v4 offset:37888
v_mfma_f32_16x16x32_bf16 v[24:27],a[176:179],v[36:39],v[24:27]
buffer_load_dwordx4 v2,s[32:35],s63 offen offset:0 sc1 lds
v_mfma_f32_16x16x32_bf16 v[28:31],a[180:183],v[36:39],v[28:31]
ds_read_b128 v[60:63],v4 offset:52224
v_mfma_f32_16x16x32_bf16 v[28:31],a[68:71],v[32:35],v[28:31]
s_waitcnt lgkmcnt(4)
v_mfma_f32_16x16x32_bf16 v[28:31],v[100:103],v[40:43],v[28:31]
ds_read_b128 v[32:35],v4 offset:10240
v_mfma_f32_16x16x32_bf16 v[28:31],v[212:215],v[44:47],v[28:31]
buffer_load_dwordx4 v2,s[32:35],s63 offen offset:1024 sc1 lds
v_mfma_f32_16x16x32_bf16 v[24:27],v[208:211],v[44:47],v[24:27]
ds_read_b128 v[36:39],v4 offset:24576
v_mfma_f32_16x16x32_bf16 v[24:27],v[96:99],v[40:43],v[24:27]
s_waitcnt lgkmcnt(4)
v_mfma_f32_16x16x32_bf16 v[24:27],a[72:75],v[48:51],v[24:27]
ds_read_b128 v[40:43],v4 offset:38912
v_mfma_f32_16x16x32_bf16 v[24:27],a[184:187],v[52:55],v[24:27]
buffer_load_dwordx4 v2,s[32:35],s63 offen offset:2048 sc1 lds
v_mfma_f32_16x16x32_bf16 v[28:31],a[188:191],v[52:55],v[28:31]
ds_read_b128 v[44:47],v4 offset:53248
v_mfma_f32_16x16x32_bf16 v[28:31],a[76:79],v[48:51],v[28:31]
s_waitcnt lgkmcnt(4)
v_mfma_f32_16x16x32_bf16 v[28:31],v[108:111],v[56:59],v[28:31]
ds_read_b128 v[48:51],v4 offset:11264
v_mfma_f32_16x16x32_bf16 v[28:31],v[220:223],v[60:63],v[28:31]
v_mfma_f32_16x16x32_bf16 v[24:27],v[216:219],v[60:63],v[24:27]
ds_read_b128 v[52:55],v4 offset:25600
v_mfma_f32_16x16x32_bf16 v[24:27],v[104:107],v[56:59],v[24:27]
s_waitcnt lgkmcnt(4)
v_mfma_f32_16x16x32_bf16 v[24:27],a[80:83],v[32:35],v[24:27]
ds_read_b128 v[56:59],v4 offset:39936
v_mfma_f32_16x16x32_bf16 v[24:27],a[192:195],v[36:39],v[24:27]
buffer_load_dwordx4 v2,s[32:35],s63 offen offset:3072 sc1 lds
s_add_u32 s63,s63,4096
v_mfma_f32_16x16x32_bf16 v[28:31],a[196:199],v[36:39],v[28:31]
ds_read_b128 v[60:63],v4 offset:54272
s_add_u32 m0,s64,12288
v_mfma_f32_16x16x32_bf16 v[28:31],a[84:87],v[32:35],v[28:31]
s_waitcnt lgkmcnt(4)
v_mfma_f32_16x16x32_bf16 v[28:31],v[116:119],v[40:43],v[28:31]
ds_read_b128 v[32:35],v4 offset:12288
v_mfma_f32_16x16x32_bf16 v[28:31],v[228:231],v[44:47],v[28:31]
buffer_load_dwordx4 v2,s[32:35],s63 offen offset:0 sc1 lds
v_mfma_f32_16x16x32_bf16 v[24:27],v[224:227],v[44:47],v[24:27]
ds_read_b128 v[36:39],v4 offset:26624
v_mfma_f32_16x16x32_bf16 v[24:27],v[112:115],v[40:43],v[24:27]
s_waitcnt lgkmcnt(4)
v_mfma_f32_16x16x32_bf16 v[24:27],a[88:91],v[48:51],v[24:27]
ds_read_b128 v[40:43],v4 offset:40960
v_mfma_f32_16x16x32_bf16 v[24:27],a[200:203],v[52:55],v[24:27]
buffer_load_dwordx4 v2,s[32:35],s63 offen offset:1024 sc1 lds
s_add_u32 s63,s63,s31
v_mfma_f32_16x16x32_bf16 v[28:31],a[204:207],v[52:55],v[28:31]
ds_read_b128 v[44:47],v4 offset:55296
v_mfma_f32_16x16x32_bf16 v[28:31],a[92:95],v[48:51],v[28:31]
s_waitcnt lgkmcnt(4)
v_mfma_f32_16x16x32_bf16 v[28:31],v[124:127],v[56:59],v[28:31]
ds_read_b128 v[48:51],v4 offset:13312
s_waitcnt vmcnt(10)
v_mfma_f32_16x16x32_bf16 v[28:31],v[236:239],v[60:63],v[28:31]
v_lshlrev_b32 v16,16,v12
v_and_b32 v17,0xffff0000,v12
v_mfma_f32_16x16x32_bf16 v[24:27],v[232:235],v[60:63],v[24:27]
ds_read_b128 v[52:55],v4 offset:27648
v_lshlrev_b32 v18,16,v13
v_mfma_f32_16x16x32_bf16 v[24:27],v[120:123],v[56:59],v[24:27]
v_and_b32 v19,0xffff0000,v13
s_waitcnt lgkmcnt(4)
v_mfma_f32_16x16x32_bf16 v[24:27],a[96:99],v[32:35],v[24:27]
ds_read_b128 v[56:59],v4 offset:41984
v_lshlrev_b32 v20,16,v14
v_mfma_f32_16x16x32_bf16 v[24:27],a[208:211],v[36:39],v[24:27]
v_and_b32 v21,0xffff0000,v14
v_lshlrev_b32 v22,16,v15
v_mfma_f32_16x16x32_bf16 v[28:31],a[212:215],v[36:39],v[28:31]
ds_read_b128 v[60:63],v4 offset:56320
v_and_b32 v23,0xffff0000,v15
v_mfma_f32_16x16x32_bf16 v[28:31],a[100:103],v[32:35],v[28:31]
s_waitcnt lgkmcnt(4)
s_barrier
v_mfma_f32_16x16x32_bf16 v[28:31],v[132:135],v[40:43],v[28:31]
ds_read_b128 v[32:35],v2 offset:0
v_mfma_f32_16x16x32_bf16 v[28:31],v[244:247],v[44:47],v[28:31]
v_mfma_f32_16x16x32_bf16 v[24:27],v[240:243],v[44:47],v[24:27]
ds_read_b128 v[36:39],v2 offset:14336
v_mfma_f32_16x16x32_bf16 v[24:27],v[128:131],v[40:43],v[24:27]
s_waitcnt lgkmcnt(4)
v_mfma_f32_16x16x32_bf16 v[24:27],a[104:107],v[48:51],v[24:27]
ds_read_b128 v[40:43],v2 offset:28672
v_mfma_f32_16x16x32_bf16 v[24:27],a[216:219],v[52:55],v[24:27]
buffer_store_dwordx2 v[8:9],v2,s[92:95],s67 idxen sc1
v_mfma_f32_16x16x32_bf16 v[28:31],a[220:223],v[52:55],v[28:31]
ds_read_b128 v[44:47],v2 offset:43008
v_mfma_f32_16x16x32_bf16 v[28:31],a[108:111],v[48:51],v[28:31]
s_waitcnt lgkmcnt(4)
v_mfma_f32_16x16x32_bf16 v[28:31],v[140:143],v[56:59],v[28:31]
ds_read_b128 v[48:51],v2 offset:1024
v_mfma_f32_16x16x32_bf16 v[28:31],v[252:255],v[60:63],v[28:31]
v_mfma_f32_16x16x32_bf16 v[24:27],v[248:251],v[60:63],v[24:27]
ds_read_b128 v[52:55],v2 offset:15360
v_mfma_f32_16x16x32_bf16 v[24:27],v[136:139],v[56:59],v[24:27]
s_branch .L_dll_0
.L_dlb_0:
s_mov_b32 s62,s58
s_mov_b32 s70,s59
s_add_u32 s74,s74,8192
v_lshrrev_b32 v5,8,v2
v_lshl_add_u32 v5,v5,4,s74
v_and_b32 v3,128,v2
v_lshl_add_u32 v5,v3,2,v5
v_and_b32 v3,16,v2
v_lshl_add_u32 v5,v3,4,v5
v_and_b32 v3,96,v2
v_lshl_add_u32 v5,v3,1,v5
v_lshrrev_b32 v3,2,v2
v_add_u32 v3,s74,v3
s_add_u32 s66,s66,2375680
s_add_u32 s63,s63,2375680
s_add_u32 s71,s71,2375680
s_mov_b32 s68,98304
s_mov_b32 s34,79446016
.p2align 6
.L_dml_0:
s_waitcnt lgkmcnt(4)
v_mfma_f32_16x16x32_bf16 v[24:27],a[0:3],v[32:35],v[24:27]
ds_read_b128 v[56:59],v4 offset:29696
v_mfma_f32_16x16x32_bf16 v[24:27],a[112:115],v[36:39],v[24:27]
v_mfma_f32_16x16x32_bf16 v[28:31],a[116:119],v[36:39],v[28:31]
ds_read_b128 v[60:63],v4 offset:44032
v_mfma_f32_16x16x32_bf16 v[28:31],a[4:7],v[32:35],v[28:31]
v_cvt_pk_bf16_f32 v15,v22,v23
s_waitcnt lgkmcnt(4)
v_mfma_f32_16x16x32_bf16 v[28:31],a[228:231],v[40:43],v[28:31]
ds_read_b128 v[32:35],v4 offset:2048
v_cvt_pk_bf16_f32 v14,v20,v21
v_mfma_f32_16x16x32_bf16 v[28:31],v[148:151],v[44:47],v[28:31]
v_cvt_pk_bf16_f32 v13,v18,v19
v_cvt_pk_bf16_f32 v12,v16,v17
v_mfma_f32_16x16x32_bf16 v[24:27],v[144:147],v[44:47],v[24:27]
ds_read_b128 v[36:39],v4 offset:16384
ds_write_b128 v5,v[12:15]
v_mfma_f32_16x16x32_bf16 v[24:27],a[224:227],v[40:43],v[24:27]
ds_read2st64_b32 v[12:13],v3 offset0:0 offset1:1
s_waitcnt lgkmcnt(6)
v_mfma_f32_16x16x32_bf16 v[24:27],a[8:11],v[48:51],v[24:27]
ds_read_b128 v[40:43],v4 offset:30720
ds_read2st64_b32 v[14:15],v3 offset0:2 offset1:3
v_mfma_f32_16x16x32_bf16 v[24:27],a[120:123],v[52:55],v[24:27]
v_mfma_f32_16x16x32_bf16 v[28:31],a[124:127],v[52:55],v[28:31]
ds_read_b128 v[44:47],v4 offset:45056
v_mfma_f32_16x16x32_bf16 v[28:31],a[12:15],v[48:51],v[28:31]
s_waitcnt lgkmcnt(7)
v_mfma_f32_16x16x32_bf16 v[28:31],a[236:239],v[56:59],v[28:31]
ds_read_b128 v[48:51],v4 offset:3072
v_mfma_f32_16x16x32_bf16 v[28:31],v[156:159],v[60:63],v[28:31]
s_waitcnt lgkmcnt(2)
buffer_atomic_pk_add_bf16 v12,v1,s[44:47],s65 offen offset:0 sc1
v_mfma_f32_16x16x32_bf16 v[24:27],v[152:155],v[60:63],v[24:27]
ds_read_b128 v[52:55],v4 offset:17408
s_add_u32 s70,s70,s75
v_mfma_f32_16x16x32_bf16 v[24:27],a[232:235],v[56:59],v[24:27]
buffer_atomic_pk_add_bf16 v13,v1,s[44:47],s65 offen offset:2048 sc1
v_mfma_f32_16x16x32_bf16 v[24:27],a[16:19],v[32:35],v[24:27]
ds_read_b128 v[56:59],v4 offset:31744
s_add_u32 s65,s65,16384
v_mfma_f32_16x16x32_bf16 v[24:27],a[128:131],v[36:39],v[24:27]
buffer_atomic_pk_add_bf16 v14,v1,s[44:47],s65 offen offset:0 sc1
v_mfma_f32_16x16x32_bf16 v[28:31],a[132:135],v[36:39],v[28:31]
ds_read_b128 v[60:63],v4 offset:46080
v_mfma_f32_16x16x32_bf16 v[28:31],a[20:23],v[32:35],v[28:31]
s_waitcnt lgkmcnt(4)
s_waitcnt vmcnt(3)
s_barrier
v_mfma_f32_16x16x32_bf16 v[28:31],a[244:247],v[40:43],v[28:31]
ds_read_b128 v[32:35],v4 offset:4096
s_add_u32 m0,s64,0
v_mfma_f32_16x16x32_bf16 v[28:31],v[164:167],v[44:47],v[28:31]
buffer_atomic_pk_add_bf16 v15,v1,s[44:47],s65 offen offset:2048 sc1
s_and_b32 s65,s70,0x3f8000
v_mfma_f32_16x16x32_bf16 v[24:27],v[160:163],v[44:47],v[24:27]
ds_read_b128 v[36:39],v4 offset:18432
v_mfma_f32_16x16x32_bf16 v[24:27],a[240:243],v[40:43],v[24:27]
s_waitcnt lgkmcnt(4)
v_mfma_f32_16x16x32_bf16 v[24:27],a[24:27],v[48:51],v[24:27]
ds_read_b128 v[40:43],v4 offset:32768
v_mfma_f32_16x16x32_bf16 v[24:27],a[136:139],v[52:55],v[24:27]
buffer_load_dwordx4 v2,s[32:35],s63 offen offset:0 sc1 lds
v_mfma_f32_16x16x32_bf16 v[28:31],a[140:143],v[52:55],v[28:31]
ds_read_b128 v[44:47],v4 offset:47104
v_mfma_f32_16x16x32_bf16 v[28:31],a[28:31],v[48:51],v[28:31]
)#"
R"#(
s_waitcnt lgkmcnt(4)
v_mfma_f32_16x16x32_bf16 v[28:31],a[252:255],v[56:59],v[28:31]
ds_read_b128 v[48:51],v4 offset:5120
v_mfma_f32_16x16x32_bf16 v[28:31],v[172:175],v[60:63],v[28:31]
buffer_load_dwordx4 v2,s[32:35],s63 offen offset:1024 sc1 lds
v_mfma_f32_16x16x32_bf16 v[24:27],v[168:171],v[60:63],v[24:27]
ds_read_b128 v[52:55],v4 offset:19456
v_mfma_f32_16x16x32_bf16 v[24:27],a[248:251],v[56:59],v[24:27]
s_waitcnt lgkmcnt(4)
v_mfma_f32_16x16x32_bf16 v[24:27],a[32:35],v[32:35],v[24:27]
ds_read_b128 v[56:59],v4 offset:33792
v_mfma_f32_16x16x32_bf16 v[24:27],a[144:147],v[36:39],v[24:27]
buffer_load_dwordx4 v2,s[32:35],s63 offen offset:2048 sc1 lds
v_mfma_f32_16x16x32_bf16 v[28:31],a[148:151],v[36:39],v[28:31]
ds_read_b128 v[60:63],v4 offset:48128
v_mfma_f32_16x16x32_bf16 v[28:31],a[36:39],v[32:35],v[28:31]
s_waitcnt lgkmcnt(4)
v_mfma_f32_16x16x32_bf16 v[28:31],v[68:71],v[40:43],v[28:31]
ds_read_b128 v[32:35],v4 offset:6144
v_mfma_f32_16x16x32_bf16 v[28:31],v[180:183],v[44:47],v[28:31]
buffer_load_dwordx4 v2,s[32:35],s63 offen offset:3072 sc1 lds
v_mfma_f32_16x16x32_bf16 v[24:27],v[176:179],v[44:47],v[24:27]
ds_read_b128 v[36:39],v4 offset:20480
v_mfma_f32_16x16x32_bf16 v[24:27],v[64:67],v[40:43],v[24:27]
s_waitcnt lgkmcnt(4)
v_mfma_f32_16x16x32_bf16 v[24:27],a[40:43],v[48:51],v[24:27]
ds_read_b128 v[40:43],v4 offset:34816
v_mfma_f32_16x16x32_bf16 v[24:27],a[152:155],v[52:55],v[24:27]
buffer_load_dwordx4 v[12:15],v2,s[32:35],s71 offen sc1
s_add_u32 s71,s71,s30
v_mfma_f32_16x16x32_bf16 v[28:31],a[156:159],v[52:55],v[28:31]
ds_read_b128 v[44:47],v4 offset:49152
s_add_u32 s63,s63,4096
v_mfma_f32_16x16x32_bf16 v[28:31],a[44:47],v[48:51],v[28:31]
s_add_u32 m0,s64,4096
s_waitcnt lgkmcnt(4)
v_mfma_f32_16x16x32_bf16 v[28:31],v[76:79],v[56:59],v[28:31]
ds_read_b128 v[48:51],v4 offset:7168
v_mfma_f32_16x16x32_bf16 v[28:31],v[188:191],v[60:63],v[28:31]
buffer_load_dwordx4 v2,s[32:35],s63 offen offset:0 sc1 lds
v_mfma_f32_16x16x32_bf16 v[24:27],v[184:187],v[60:63],v[24:27]
ds_read_b128 v[52:55],v4 offset:21504
v_mfma_f32_16x16x32_bf16 v[24:27],v[72:75],v[56:59],v[24:27]
s_waitcnt lgkmcnt(4)
v_mfma_f32_16x16x32_bf16 v[24:27],a[48:51],v[32:35],v[24:27]
ds_read_b128 v[56:59],v4 offset:35840
v_mfma_f32_16x16x32_bf16 v[24:27],a[160:163],v[36:39],v[24:27]
buffer_load_dwordx4 v2,s[32:35],s63 offen offset:1024 sc1 lds
v_mfma_f32_16x16x32_bf16 v[28:31],a[164:167],v[36:39],v[28:31]
ds_read_b128 v[60:63],v4 offset:50176
v_mfma_f32_16x16x32_bf16 v[28:31],a[52:55],v[32:35],v[28:31]
s_waitcnt lgkmcnt(4)
v_mfma_f32_16x16x32_bf16 v[28:31],v[84:87],v[40:43],v[28:31]
ds_read_b128 v[32:35],v4 offset:8192
v_mfma_f32_16x16x32_bf16 v[28:31],v[196:199],v[44:47],v[28:31]
buffer_load_dwordx4 v2,s[32:35],s63 offen offset:2048 sc1 lds
v_mfma_f32_16x16x32_bf16 v[24:27],v[192:195],v[44:47],v[24:27]
ds_read_b128 v[36:39],v4 offset:22528
v_mfma_f32_16x16x32_bf16 v[24:27],v[80:83],v[40:43],v[24:27]
s_waitcnt lgkmcnt(4)
v_mfma_f32_16x16x32_bf16 v[24:27],a[56:59],v[48:51],v[24:27]
ds_read_b128 v[40:43],v4 offset:36864
v_mfma_f32_16x16x32_bf16 v[24:27],a[168:171],v[52:55],v[24:27]
buffer_load_dwordx4 v2,s[32:35],s63 offen offset:3072 sc1 lds
v_mfma_f32_16x16x32_bf16 v[28:31],a[172:175],v[52:55],v[28:31]
ds_read_b128 v[44:47],v4 offset:51200
v_mfma_f32_16x16x32_bf16 v[28:31],a[60:63],v[48:51],v[28:31]
s_waitcnt lgkmcnt(4)
v_mfma_f32_16x16x32_bf16 v[28:31],v[92:95],v[56:59],v[28:31]
ds_read_b128 v[48:51],v4 offset:9216
v_mfma_f32_16x16x32_bf16 v[28:31],v[204:207],v[60:63],v[28:31]
s_add_u32 s63,s63,4096
s_add_u32 m0,s64,8192
v_mfma_f32_16x16x32_bf16 v[24:27],v[200:203],v[60:63],v[24:27]
ds_read_b128 v[52:55],v4 offset:23552
v_mfma_f32_16x16x32_bf16 v[24:27],v[88:91],v[56:59],v[24:27]
s_waitcnt lgkmcnt(4)
v_mfma_f32_16x16x32_bf16 v[24:27],a[64:67],v[32:35],v[24:27]
ds_read_b128 v[56:59],v4 offset:37888
v_mfma_f32_16x16x32_bf16 v[24:27],a[176:179],v[36:39],v[24:27]
buffer_load_dwordx4 v2,s[32:35],s63 offen offset:0 sc1 lds
v_mfma_f32_16x16x32_bf16 v[28:31],a[180:183],v[36:39],v[28:31]
ds_read_b128 v[60:63],v4 offset:52224
v_mfma_f32_16x16x32_bf16 v[28:31],a[68:71],v[32:35],v[28:31]
s_waitcnt lgkmcnt(4)
v_mfma_f32_16x16x32_bf16 v[28:31],v[100:103],v[40:43],v[28:31]
ds_read_b128 v[32:35],v4 offset:10240
v_mfma_f32_16x16x32_bf16 v[28:31],v[212:215],v[44:47],v[28:31]
buffer_load_dwordx4 v2,s[32:35],s63 offen offset:1024 sc1 lds
v_mfma_f32_16x16x32_bf16 v[24:27],v[208:211],v[44:47],v[24:27]
ds_read_b128 v[36:39],v4 offset:24576
v_mfma_f32_16x16x32_bf16 v[24:27],v[96:99],v[40:43],v[24:27]
s_waitcnt lgkmcnt(4)
v_mfma_f32_16x16x32_bf16 v[24:27],a[72:75],v[48:51],v[24:27]
ds_read_b128 v[40:43],v4 offset:38912
v_mfma_f32_16x16x32_bf16 v[24:27],a[184:187],v[52:55],v[24:27]
buffer_load_dwordx4 v2,s[32:35],s63 offen offset:2048 sc1 lds
v_mfma_f32_16x16x32_bf16 v[28:31],a[188:191],v[52:55],v[28:31]
ds_read_b128 v[44:47],v4 offset:53248
v_mfma_f32_16x16x32_bf16 v[28:31],a[76:79],v[48:51],v[28:31]
s_waitcnt lgkmcnt(4)
v_mfma_f32_16x16x32_bf16 v[28:31],v[108:111],v[56:59],v[28:31]
ds_read_b128 v[48:51],v4 offset:11264
v_mfma_f32_16x16x32_bf16 v[28:31],v[220:223],v[60:63],v[28:31]
v_mfma_f32_16x16x32_bf16 v[24:27],v[216:219],v[60:63],v[24:27]
ds_read_b128 v[52:55],v4 offset:25600
v_mfma_f32_16x16x32_bf16 v[24:27],v[104:107],v[56:59],v[24:27]
s_waitcnt lgkmcnt(4)
v_mfma_f32_16x16x32_bf16 v[24:27],a[80:83],v[32:35],v[24:27]
ds_read_b128 v[56:59],v4 offset:39936
v_mfma_f32_16x16x32_bf16 v[24:27],a[192:195],v[36:39],v[24:27]
buffer_load_dwordx4 v2,s[32:35],s63 offen offset:3072 sc1 lds
s_add_u32 s63,s63,4096
v_mfma_f32_16x16x32_bf16 v[28:31],a[196:199],v[36:39],v[28:31]
ds_read_b128 v[60:63],v4 offset:54272
s_add_u32 m0,s64,12288
v_mfma_f32_16x16x32_bf16 v[28:31],a[84:87],v[32:35],v[28:31]
s_waitcnt lgkmcnt(4)
v_mfma_f32_16x16x32_bf16 v[28:31],v[116:119],v[40:43],v[28:31]
ds_read_b128 v[32:35],v4 offset:12288
v_mfma_f32_16x16x32_bf16 v[28:31],v[228:231],v[44:47],v[28:31]
buffer_load_dwordx4 v2,s[32:35],s63 offen offset:0 sc1 lds
v_mfma_f32_16x16x32_bf16 v[24:27],v[224:227],v[44:47],v[24:27]
ds_read_b128 v[36:39],v4 offset:26624
v_mfma_f32_16x16x32_bf16 v[24:27],v[112:115],v[40:43],v[24:27]
s_waitcnt lgkmcnt(4)
v_mfma_f32_16x16x32_bf16 v[24:27],a[88:91],v[48:51],v[24:27]
ds_read_b128 v[40:43],v4 offset:40960
v_mfma_f32_16x16x32_bf16 v[24:27],a[200:203],v[52:55],v[24:27]
buffer_load_dwordx4 v2,s[32:35],s63 offen offset:1024 sc1 lds
s_add_u32 s63,s63,s31
v_mfma_f32_16x16x32_bf16 v[28:31],a[204:207],v[52:55],v[28:31]
ds_read_b128 v[44:47],v4 offset:55296
v_mfma_f32_16x16x32_bf16 v[28:31],a[92:95],v[48:51],v[28:31]
s_waitcnt lgkmcnt(4)
v_mfma_f32_16x16x32_bf16 v[28:31],v[124:127],v[56:59],v[28:31]
ds_read_b128 v[48:51],v4 offset:13312
s_waitcnt vmcnt(10)
v_mfma_f32_16x16x32_bf16 v[28:31],v[236:239],v[60:63],v[28:31]
v_lshlrev_b32 v16,16,v12
v_and_b32 v17,0xffff0000,v12
v_mfma_f32_16x16x32_bf16 v[24:27],v[232:235],v[60:63],v[24:27]
ds_read_b128 v[52:55],v4 offset:27648
v_lshlrev_b32 v18,16,v13
v_mfma_f32_16x16x32_bf16 v[24:27],v[120:123],v[56:59],v[24:27]
v_and_b32 v19,0xffff0000,v13
s_waitcnt lgkmcnt(4)
v_mfma_f32_16x16x32_bf16 v[24:27],a[96:99],v[32:35],v[24:27]
ds_read_b128 v[56:59],v4 offset:41984
v_lshlrev_b32 v20,16,v14
v_mfma_f32_16x16x32_bf16 v[24:27],a[208:211],v[36:39],v[24:27]
v_and_b32 v21,0xffff0000,v14
v_lshlrev_b32 v22,16,v15
v_mfma_f32_16x16x32_bf16 v[28:31],a[212:215],v[36:39],v[28:31]
ds_read_b128 v[60:63],v4 offset:56320
v_and_b32 v23,0xffff0000,v15
v_mfma_f32_16x16x32_bf16 v[28:31],a[100:103],v[32:35],v[28:31]
s_waitcnt lgkmcnt(4)
s_barrier
v_mfma_f32_16x16x32_bf16 v[28:31],v[132:135],v[40:43],v[28:31]
ds_read_b128 v[32:35],v2 offset:0
v_mfma_f32_16x16x32_bf16 v[28:31],v[244:247],v[44:47],v[28:31]
v_mfma_f32_16x16x32_bf16 v[24:27],v[240:243],v[44:47],v[24:27]
ds_read_b128 v[36:39],v2 offset:14336
v_mfma_f32_16x16x32_bf16 v[24:27],v[128:131],v[40:43],v[24:27]
s_waitcnt lgkmcnt(4)
v_mfma_f32_16x16x32_bf16 v[24:27],a[104:107],v[48:51],v[24:27]
ds_read_b128 v[40:43],v2 offset:28672
v_mfma_f32_16x16x32_bf16 v[24:27],a[216:219],v[52:55],v[24:27]
v_mfma_f32_16x16x32_bf16 v[28:31],a[220:223],v[52:55],v[28:31]
ds_read_b128 v[44:47],v2 offset:43008
v_mfma_f32_16x16x32_bf16 v[28:31],a[108:111],v[48:51],v[28:31]
s_waitcnt lgkmcnt(4)
v_mfma_f32_16x16x32_bf16 v[28:31],v[140:143],v[56:59],v[28:31]
ds_read_b128 v[48:51],v2 offset:1024
v_mfma_f32_16x16x32_bf16 v[28:31],v[252:255],v[60:63],v[28:31]
v_mfma_f32_16x16x32_bf16 v[24:27],v[248:251],v[60:63],v[24:27]
ds_read_b128 v[52:55],v2 offset:15360
s_sub_u32 s62,s62,1
v_mfma_f32_16x16x32_bf16 v[24:27],v[136:139],v[56:59],v[24:27]
s_cbranch_scc1 .L_dmt_0
s_waitcnt lgkmcnt(4)
v_mfma_f32_16x16x32_bf16 v[16:19],a[0:3],v[32:35],v[16:19]
ds_read_b128 v[56:59],v2 offset:29696
v_mfma_f32_16x16x32_bf16 v[16:19],a[112:115],v[36:39],v[16:19]
v_mfma_f32_16x16x32_bf16 v[20:23],a[116:119],v[36:39],v[20:23]
ds_read_b128 v[60:63],v2 offset:44032
v_mfma_f32_16x16x32_bf16 v[20:23],a[4:7],v[32:35],v[20:23]
v_cvt_pk_bf16_f32 v15,v30,v31
s_waitcnt lgkmcnt(4)
v_mfma_f32_16x16x32_bf16 v[20:23],a[228:231],v[40:43],v[20:23]
ds_read_b128 v[32:35],v2 offset:2048
v_cvt_pk_bf16_f32 v14,v28,v29
v_mfma_f32_16x16x32_bf16 v[20:23],v[148:151],v[44:47],v[20:23]
v_cvt_pk_bf16_f32 v13,v26,v27
v_cvt_pk_bf16_f32 v12,v24,v25
v_mfma_f32_16x16x32_bf16 v[16:19],v[144:147],v[44:47],v[16:19]
ds_read_b128 v[36:39],v2 offset:16384
ds_write_b128 v5,v[12:15]
v_mfma_f32_16x16x32_bf16 v[16:19],a[224:227],v[40:43],v[16:19]
ds_read2st64_b32 v[12:13],v3 offset0:0 offset1:1
s_waitcnt lgkmcnt(6)
v_mfma_f32_16x16x32_bf16 v[16:19],a[8:11],v[48:51],v[16:19]
ds_read_b128 v[40:43],v2 offset:30720
ds_read2st64_b32 v[14:15],v3 offset0:2 offset1:3
v_mfma_f32_16x16x32_bf16 v[16:19],a[120:123],v[52:55],v[16:19]
v_mfma_f32_16x16x32_bf16 v[20:23],a[124:127],v[52:55],v[20:23]
ds_read_b128 v[44:47],v2 offset:45056
v_mfma_f32_16x16x32_bf16 v[20:23],a[12:15],v[48:51],v[20:23]
s_waitcnt lgkmcnt(7)
v_mfma_f32_16x16x32_bf16 v[20:23],a[236:239],v[56:59],v[20:23]
ds_read_b128 v[48:51],v2 offset:3072
v_mfma_f32_16x16x32_bf16 v[20:23],v[156:159],v[60:63],v[20:23]
s_waitcnt lgkmcnt(2)
buffer_atomic_pk_add_bf16 v12,v1,s[44:47],s65 offen offset:0 sc1
v_mfma_f32_16x16x32_bf16 v[16:19],v[152:155],v[60:63],v[16:19]
ds_read_b128 v[52:55],v2 offset:17408
s_add_u32 s70,s70,s68
v_mfma_f32_16x16x32_bf16 v[16:19],a[232:235],v[56:59],v[16:19]
buffer_atomic_pk_add_bf16 v13,v1,s[44:47],s65 offen offset:2048 sc1
v_mfma_f32_16x16x32_bf16 v[16:19],a[16:19],v[32:35],v[16:19]
ds_read_b128 v[56:59],v2 offset:31744
s_add_u32 s65,s65,16384
v_mfma_f32_16x16x32_bf16 v[16:19],a[128:131],v[36:39],v[16:19]
buffer_atomic_pk_add_bf16 v14,v1,s[44:47],s65 offen offset:0 sc1
s_mov_b32 s68,s75
v_mfma_f32_16x16x32_bf16 v[20:23],a[132:135],v[36:39],v[20:23]
ds_read_b128 v[60:63],v2 offset:46080
v_mfma_f32_16x16x32_bf16 v[20:23],a[20:23],v[32:35],v[20:23]
s_waitcnt lgkmcnt(4)
s_waitcnt vmcnt(3)
s_barrier
v_mfma_f32_16x16x32_bf16 v[20:23],a[244:247],v[40:43],v[20:23]
ds_read_b128 v[32:35],v2 offset:4096
s_add_u32 m0,s64,57344
v_mfma_f32_16x16x32_bf16 v[20:23],v[164:167],v[44:47],v[20:23]
buffer_atomic_pk_add_bf16 v15,v1,s[44:47],s65 offen offset:2048 sc1
s_and_b32 s65,s70,0x3f8000
v_mfma_f32_16x16x32_bf16 v[16:19],v[160:163],v[44:47],v[16:19]
ds_read_b128 v[36:39],v2 offset:18432
v_mfma_f32_16x16x32_bf16 v[16:19],a[240:243],v[40:43],v[16:19]
s_waitcnt lgkmcnt(4)
v_mfma_f32_16x16x32_bf16 v[16:19],a[24:27],v[48:51],v[16:19]
ds_read_b128 v[40:43],v2 offset:32768
v_mfma_f32_16x16x32_bf16 v[16:19],a[136:139],v[52:55],v[16:19]
buffer_load_dwordx4 v2,s[32:35],s63 offen offset:0 sc1 lds
v_mfma_f32_16x16x32_bf16 v[20:23],a[140:143],v[52:55],v[20:23]
ds_read_b128 v[44:47],v2 offset:47104
v_mfma_f32_16x16x32_bf16 v[20:23],a[28:31],v[48:51],v[20:23]
s_waitcnt lgkmcnt(4)
v_mfma_f32_16x16x32_bf16 v[20:23],a[252:255],v[56:59],v[20:23]
ds_read_b128 v[48:51],v2 offset:5120
v_mfma_f32_16x16x32_bf16 v[20:23],v[172:175],v[60:63],v[20:23]
buffer_load_dwordx4 v2,s[32:35],s63 offen offset:1024 sc1 lds
v_mfma_f32_16x16x32_bf16 v[16:19],v[168:171],v[60:63],v[16:19]
ds_read_b128 v[52:55],v2 offset:19456
v_mfma_f32_16x16x32_bf16 v[16:19],a[248:251],v[56:59],v[16:19]
s_waitcnt lgkmcnt(4)
v_mfma_f32_16x16x32_bf16 v[16:19],a[32:35],v[32:35],v[16:19]
ds_read_b128 v[56:59],v2 offset:33792
v_mfma_f32_16x16x32_bf16 v[16:19],a[144:147],v[36:39],v[16:19]
buffer_load_dwordx4 v2,s[32:35],s63 offen offset:2048 sc1 lds
v_mfma_f32_16x16x32_bf16 v[20:23],a[148:151],v[36:39],v[20:23]
ds_read_b128 v[60:63],v2 offset:48128
v_mfma_f32_16x16x32_bf16 v[20:23],a[36:39],v[32:35],v[20:23]
s_waitcnt lgkmcnt(4)
v_mfma_f32_16x16x32_bf16 v[20:23],v[68:71],v[40:43],v[20:23]
ds_read_b128 v[32:35],v2 offset:6144
v_mfma_f32_16x16x32_bf16 v[20:23],v[180:183],v[44:47],v[20:23]
buffer_load_dwordx4 v2,s[32:35],s63 offen offset:3072 sc1 lds
v_mfma_f32_16x16x32_bf16 v[16:19],v[176:179],v[44:47],v[16:19]
ds_read_b128 v[36:39],v2 offset:20480
v_mfma_f32_16x16x32_bf16 v[16:19],v[64:67],v[40:43],v[16:19]
s_waitcnt lgkmcnt(4)
v_mfma_f32_16x16x32_bf16 v[16:19],a[40:43],v[48:51],v[16:19]
ds_read_b128 v[40:43],v2 offset:34816
v_mfma_f32_16x16x32_bf16 v[16:19],a[152:155],v[52:55],v[16:19]
buffer_load_dwordx4 v[12:15],v2,s[32:35],s71 offen sc1
s_add_u32 s71,s71,s30
v_mfma_f32_16x16x32_bf16 v[20:23],a[156:159],v[52:55],v[20:23]
ds_read_b128 v[44:47],v2 offset:49152
s_add_u32 s63,s63,4096
v_mfma_f32_16x16x32_bf16 v[20:23],a[44:47],v[48:51],v[20:23]
s_add_u32 m0,s64,61440
s_waitcnt lgkmcnt(4)
v_mfma_f32_16x16x32_bf16 v[20:23],v[76:79],v[56:59],v[20:23]
ds_read_b128 v[48:51],v2 offset:7168
v_mfma_f32_16x16x32_bf16 v[20:23],v[188:191],v[60:63],v[20:23]
buffer_load_dwordx4 v2,s[32:35],s63 offen offset:0 sc1 lds
v_mfma_f32_16x16x32_bf16 v[16:19],v[184:187],v[60:63],v[16:19]
ds_read_b128 v[52:55],v2 offset:21504
v_mfma_f32_16x16x32_bf16 v[16:19],v[72:75],v[56:59],v[16:19]
s_waitcnt lgkmcnt(4)
v_mfma_f32_16x16x32_bf16 v[16:19],a[48:51],v[32:35],v[16:19]
ds_read_b128 v[56:59],v2 offset:35840
v_mfma_f32_16x16x32_bf16 v[16:19],a[160:163],v[36:39],v[16:19]
buffer_load_dwordx4 v2,s[32:35],s63 offen offset:1024 sc1 lds
v_mfma_f32_16x16x32_bf16 v[20:23],a[164:167],v[36:39],v[20:23]
ds_read_b128 v[60:63],v2 offset:50176
v_mfma_f32_16x16x32_bf16 v[20:23],a[52:55],v[32:35],v[20:23]
s_waitcnt lgkmcnt(4)
v_mfma_f32_16x16x32_bf16 v[20:23],v[84:87],v[40:43],v[20:23]
ds_read_b128 v[32:35],v2 offset:8192
v_mfma_f32_16x16x32_bf16 v[20:23],v[196:199],v[44:47],v[20:23]
buffer_load_dwordx4 v2,s[32:35],s63 offen offset:2048 sc1 lds
v_mfma_f32_16x16x32_bf16 v[16:19],v[192:195],v[44:47],v[16:19]
ds_read_b128 v[36:39],v2 offset:22528
v_mfma_f32_16x16x32_bf16 v[16:19],v[80:83],v[40:43],v[16:19]
s_waitcnt lgkmcnt(4)
v_mfma_f32_16x16x32_bf16 v[16:19],a[56:59],v[48:51],v[16:19]
ds_read_b128 v[40:43],v2 offset:36864
v_mfma_f32_16x16x32_bf16 v[16:19],a[168:171],v[52:55],v[16:19]
buffer_load_dwordx4 v2,s[32:35],s63 offen offset:3072 sc1 lds
v_mfma_f32_16x16x32_bf16 v[20:23],a[172:175],v[52:55],v[20:23]
ds_read_b128 v[44:47],v2 offset:51200
v_mfma_f32_16x16x32_bf16 v[20:23],a[60:63],v[48:51],v[20:23]
s_waitcnt lgkmcnt(4)
v_mfma_f32_16x16x32_bf16 v[20:23],v[92:95],v[56:59],v[20:23]
ds_read_b128 v[48:51],v2 offset:9216
v_mfma_f32_16x16x32_bf16 v[20:23],v[204:207],v[60:63],v[20:23]
s_add_u32 s63,s63,4096
s_add_u32 m0,s64,65536
v_mfma_f32_16x16x32_bf16 v[16:19],v[200:203],v[60:63],v[16:19]
ds_read_b128 v[52:55],v2 offset:23552
v_mfma_f32_16x16x32_bf16 v[16:19],v[88:91],v[56:59],v[16:19]
s_waitcnt lgkmcnt(4)
v_mfma_f32_16x16x32_bf16 v[16:19],a[64:67],v[32:35],v[16:19]
ds_read_b128 v[56:59],v2 offset:37888
v_mfma_f32_16x16x32_bf16 v[16:19],a[176:179],v[36:39],v[16:19]
buffer_load_dwordx4 v2,s[32:35],s63 offen offset:0 sc1 lds
v_mfma_f32_16x16x32_bf16 v[20:23],a[180:183],v[36:39],v[20:23]
ds_read_b128 v[60:63],v2 offset:52224
v_mfma_f32_16x16x32_bf16 v[20:23],a[68:71],v[32:35],v[20:23]
s_waitcnt lgkmcnt(4)
v_mfma_f32_16x16x32_bf16 v[20:23],v[100:103],v[40:43],v[20:23]
ds_read_b128 v[32:35],v2 offset:10240
v_mfma_f32_16x16x32_bf16 v[20:23],v[212:215],v[44:47],v[20:23]
buffer_load_dwordx4 v2,s[32:35],s63 offen offset:1024 sc1 lds
v_mfma_f32_16x16x32_bf16 v[16:19],v[208:211],v[44:47],v[16:19]
ds_read_b128 v[36:39],v2 offset:24576
v_mfma_f32_16x16x32_bf16 v[16:19],v[96:99],v[40:43],v[16:19]
s_waitcnt lgkmcnt(4)
v_mfma_f32_16x16x32_bf16 v[16:19],a[72:75],v[48:51],v[16:19]
ds_read_b128 v[40:43],v2 offset:38912
v_mfma_f32_16x16x32_bf16 v[16:19],a[184:187],v[52:55],v[16:19]
buffer_load_dwordx4 v2,s[32:35],s63 offen offset:2048 sc1 lds
v_mfma_f32_16x16x32_bf16 v[20:23],a[188:191],v[52:55],v[20:23]
ds_read_b128 v[44:47],v2 offset:53248
v_mfma_f32_16x16x32_bf16 v[20:23],a[76:79],v[48:51],v[20:23]
s_waitcnt lgkmcnt(4)
v_mfma_f32_16x16x32_bf16 v[20:23],v[108:111],v[56:59],v[20:23]
ds_read_b128 v[48:51],v2 offset:11264
v_mfma_f32_16x16x32_bf16 v[20:23],v[220:223],v[60:63],v[20:23]
v_mfma_f32_16x16x32_bf16 v[16:19],v[216:219],v[60:63],v[16:19]
ds_read_b128 v[52:55],v2 offset:25600
v_mfma_f32_16x16x32_bf16 v[16:19],v[104:107],v[56:59],v[16:19]
s_waitcnt lgkmcnt(4)
v_mfma_f32_16x16x32_bf16 v[16:19],a[80:83],v[32:35],v[16:19]
ds_read_b128 v[56:59],v2 offset:39936
v_mfma_f32_16x16x32_bf16 v[16:19],a[192:195],v[36:39],v[16:19]
buffer_load_dwordx4 v2,s[32:35],s63 offen offset:3072 sc1 lds
s_add_u32 s63,s63,4096
v_mfma_f32_16x16x32_bf16 v[20:23],a[196:199],v[36:39],v[20:23]
ds_read_b128 v[60:63],v2 offset:54272
s_add_u32 m0,s64,69632
v_mfma_f32_16x16x32_bf16 v[20:23],a[84:87],v[32:35],v[20:23]
s_waitcnt lgkmcnt(4)
v_mfma_f32_16x16x32_bf16 v[20:23],v[116:119],v[40:43],v[20:23]
ds_read_b128 v[32:35],v2 offset:12288
v_mfma_f32_16x16x32_bf16 v[20:23],v[228:231],v[44:47],v[20:23]
buffer_load_dwordx4 v2,s[32:35],s63 offen offset:0 sc1 lds
v_mfma_f32_16x16x32_bf16 v[16:19],v[224:227],v[44:47],v[16:19]
ds_read_b128 v[36:39],v2 offset:26624
v_mfma_f32_16x16x32_bf16 v[16:19],v[112:115],v[40:43],v[16:19]
s_waitcnt lgkmcnt(4)
v_mfma_f32_16x16x32_bf16 v[16:19],a[88:91],v[48:51],v[16:19]
ds_read_b128 v[40:43],v2 offset:40960
v_mfma_f32_16x16x32_bf16 v[16:19],a[200:203],v[52:55],v[16:19]
buffer_load_dwordx4 v2,s[32:35],s63 offen offset:1024 sc1 lds
s_add_u32 s63,s63,s31
v_mfma_f32_16x16x32_bf16 v[20:23],a[204:207],v[52:55],v[20:23]
ds_read_b128 v[44:47],v2 offset:55296
v_mfma_f32_16x16x32_bf16 v[20:23],a[92:95],v[48:51],v[20:23]
s_waitcnt lgkmcnt(4)
v_mfma_f32_16x16x32_bf16 v[20:23],v[124:127],v[56:59],v[20:23]
ds_read_b128 v[48:51],v2 offset:13312
s_waitcnt vmcnt(10)
v_mfma_f32_16x16x32_bf16 v[20:23],v[236:239],v[60:63],v[20:23]
v_lshlrev_b32 v24,16,v12
v_and_b32 v25,0xffff0000,v12
v_mfma_f32_16x16x32_bf16 v[16:19],v[232:235],v[60:63],v[16:19]
ds_read_b128 v[52:55],v2 offset:27648
v_lshlrev_b32 v26,16,v13
v_mfma_f32_16x16x32_bf16 v[16:19],v[120:123],v[56:59],v[16:19]
v_and_b32 v27,0xffff0000,v13
s_waitcnt lgkmcnt(4)
v_mfma_f32_16x16x32_bf16 v[16:19],a[96:99],v[32:35],v[16:19]
ds_read_b128 v[56:59],v2 offset:41984
v_lshlrev_b32 v28,16,v14
v_mfma_f32_16x16x32_bf16 v[16:19],a[208:211],v[36:39],v[16:19]
v_and_b32 v29,0xffff0000,v14
v_lshlrev_b32 v30,16,v15
v_mfma_f32_16x16x32_bf16 v[20:23],a[212:215],v[36:39],v[20:23]
ds_read_b128 v[60:63],v2 offset:56320
v_and_b32 v31,0xffff0000,v15
v_mfma_f32_16x16x32_bf16 v[20:23],a[100:103],v[32:35],v[20:23]
s_waitcnt lgkmcnt(4)
s_barrier
v_mfma_f32_16x16x32_bf16 v[20:23],v[132:135],v[40:43],v[20:23]
ds_read_b128 v[32:35],v4 offset:0
v_mfma_f32_16x16x32_bf16 v[20:23],v[244:247],v[44:47],v[20:23]
v_mfma_f32_16x16x32_bf16 v[16:19],v[240:243],v[44:47],v[16:19]
ds_read_b128 v[36:39],v4 offset:14336
v_mfma_f32_16x16x32_bf16 v[16:19],v[128:131],v[40:43],v[16:19]
s_waitcnt lgkmcnt(4)
v_mfma_f32_16x16x32_bf16 v[16:19],a[104:107],v[48:51],v[16:19]
ds_read_b128 v[40:43],v4 offset:28672
v_mfma_f32_16x16x32_bf16 v[16:19],a[216:219],v[52:55],v[16:19]
v_mfma_f32_16x16x32_bf16 v[20:23],a[220:223],v[52:55],v[20:23]
ds_read_b128 v[44:47],v4 offset:43008
v_mfma_f32_16x16x32_bf16 v[20:23],a[108:111],v[48:51],v[20:23]
s_waitcnt lgkmcnt(4)
v_mfma_f32_16x16x32_bf16 v[20:23],v[140:143],v[56:59],v[20:23]
ds_read_b128 v[48:51],v4 offset:1024
v_mfma_f32_16x16x32_bf16 v[20:23],v[252:255],v[60:63],v[20:23]
v_mfma_f32_16x16x32_bf16 v[16:19],v[248:251],v[60:63],v[16:19]
ds_read_b128 v[52:55],v4 offset:15360
s_sub_u32 s62,s62,1
v_mfma_f32_16x16x32_bf16 v[16:19],v[136:139],v[56:59],v[16:19]
s_cbranch_scc0 .L_dml_0
.L_dmu_0:
s_nop 1
s_nop 0
v_cvt_pk_bf16_f32 v15,v22,v23
v_cvt_pk_bf16_f32 v14,v20,v21
s_nop 1
v_cvt_pk_bf16_f32 v13,v18,v19
v_cvt_pk_bf16_f32 v12,v16,v17
ds_write_b128 v5,v[12:15]
ds_read2st64_b32 v[12:13],v3 offset0:0 offset1:1
ds_read2st64_b32 v[14:15],v3 offset0:2 offset1:3
s_waitcnt lgkmcnt(0)
buffer_atomic_pk_add_bf16 v12,v1,s[44:47],s65 offen offset:0 sc1
s_add_u32 s70,s70,s75
buffer_atomic_pk_add_bf16 v13,v1,s[44:47],s65 offen offset:2048 sc1
s_add_u32 s65,s65,16384
buffer_atomic_pk_add_bf16 v14,v1,s[44:47],s65 offen offset:0 sc1
buffer_atomic_pk_add_bf16 v15,v1,s[44:47],s65 offen offset:2048 sc1
s_and_b32 s65,s70,0x3f8000
s_branch .L_end
.L_dmt_0:
s_nop 1
s_nop 0
v_cvt_pk_bf16_f32 v15,v30,v31
v_cvt_pk_bf16_f32 v14,v28,v29
s_nop 1
v_cvt_pk_bf16_f32 v13,v26,v27
v_cvt_pk_bf16_f32 v12,v24,v25
ds_write_b128 v5,v[12:15]
ds_read2st64_b32 v[12:13],v3 offset0:0 offset1:1
ds_read2st64_b32 v[14:15],v3 offset0:2 offset1:3
s_waitcnt lgkmcnt(0)
buffer_atomic_pk_add_bf16 v12,v1,s[44:47],s65 offen offset:0 sc1
s_add_u32 s70,s70,s68
buffer_atomic_pk_add_bf16 v13,v1,s[44:47],s65 offen offset:2048 sc1
s_add_u32 s65,s65,16384
buffer_atomic_pk_add_bf16 v14,v1,s[44:47],s65 offen offset:0 sc1
s_mov_b32 s68,s75
buffer_atomic_pk_add_bf16 v15,v1,s[44:47],s65 offen offset:2048 sc1
s_and_b32 s65,s70,0x3f8000
s_branch .L_end
.p2align 6
.L_dlv1:
.L_dll_1:
s_waitcnt lgkmcnt(4)
v_mfma_f32_16x16x32_bf16 v[16:19],a[0:3],v[32:35],v[16:19]
ds_read_b128 v[56:59],v2 offset:29696
v_mfma_f32_16x16x32_bf16 v[16:19],a[112:115],v[36:39],v[16:19]
v_mfma_f32_16x16x32_bf16 v[20:23],a[116:119],v[36:39],v[20:23]
ds_read_b128 v[60:63],v2 offset:44032
v_mfma_f32_16x16x32_bf16 v[20:23],a[4:7],v[32:35],v[20:23]
s_add_u32 s68,s68,1187840
s_waitcnt lgkmcnt(4)
v_mfma_f32_16x16x32_bf16 v[20:23],a[228:231],v[40:43],v[20:23]
ds_read_b128 v[32:35],v2 offset:2048
v_mfma_f32_16x16x32_bf16 v[20:23],v[148:151],v[44:47],v[20:23]
buffer_store_dwordx4 v[28:31],v2,s[32:35],s68 offen offset:1024 sc1
v_mfma_f32_16x16x32_bf16 v[16:19],v[144:147],v[44:47],v[16:19]
ds_read_b128 v[36:39],v2 offset:16384
v_mfma_f32_16x16x32_bf16 v[16:19],a[224:227],v[40:43],v[16:19]
s_waitcnt lgkmcnt(4)
v_mfma_f32_16x16x32_bf16 v[16:19],a[8:11],v[48:51],v[16:19]
ds_read_b128 v[40:43],v2 offset:30720
v_mfma_f32_16x16x32_bf16 v[16:19],a[120:123],v[52:55],v[16:19]
buffer_store_dwordx4 v[24:27],v2,s[32:35],s68 offen sc1
s_mov_b32 s67,s68
v_mfma_f32_16x16x32_bf16 v[20:23],a[124:127],v[52:55],v[20:23]
ds_read_b128 v[44:47],v2 offset:45056
v_mfma_f32_16x16x32_bf16 v[20:23],a[12:15],v[48:51],v[20:23]
s_waitcnt lgkmcnt(4)
)#"
R"#(
v_mfma_f32_16x16x32_bf16 v[20:23],a[236:239],v[56:59],v[20:23]
ds_read_b128 v[48:51],v2 offset:3072
v_mfma_f32_16x16x32_bf16 v[20:23],v[156:159],v[60:63],v[20:23]
v_mfma_f32_16x16x32_bf16 v[16:19],v[152:155],v[60:63],v[16:19]
ds_read_b128 v[52:55],v2 offset:17408
s_waitcnt vmcnt(5)
v_mfma_f32_16x16x32_bf16 v[16:19],a[232:235],v[56:59],v[16:19]
v_cmp_ne_u64 vcc,v[8:9],v[6:7]
s_waitcnt lgkmcnt(4)
v_mfma_f32_16x16x32_bf16 v[16:19],a[16:19],v[32:35],v[16:19]
ds_read_b128 v[56:59],v2 offset:31744
s_cbranch_vccnz .L_rtl0_1
.L_bkl0_1:
v_mfma_f32_16x16x32_bf16 v[16:19],a[128:131],v[36:39],v[16:19]
s_add_u32 s66,s66,s30
v_mfma_f32_16x16x32_bf16 v[20:23],a[132:135],v[36:39],v[20:23]
s_waitcnt vmcnt(2)
s_barrier
v_mfma_f32_16x16x32_bf16 v[20:23],a[20:23],v[32:35],v[20:23]
ds_read_b128 v[60:63],v2 offset:46080
s_waitcnt lgkmcnt(4)
v_mfma_f32_16x16x32_bf16 v[20:23],a[244:247],v[40:43],v[20:23]
ds_read_b128 v[32:35],v2 offset:4096
s_add_u32 m0,s64,57344
v_mfma_f32_16x16x32_bf16 v[20:23],v[164:167],v[44:47],v[20:23]
buffer_load_dwordx4 v2,s[32:35],s63 offen offset:0 sc1 lds
v_mfma_f32_16x16x32_bf16 v[16:19],v[160:163],v[44:47],v[16:19]
ds_read_b128 v[36:39],v2 offset:18432
v_mfma_f32_16x16x32_bf16 v[16:19],a[240:243],v[40:43],v[16:19]
s_waitcnt lgkmcnt(4)
v_mfma_f32_16x16x32_bf16 v[16:19],a[24:27],v[48:51],v[16:19]
ds_read_b128 v[40:43],v2 offset:32768
v_mfma_f32_16x16x32_bf16 v[16:19],a[136:139],v[52:55],v[16:19]
v_mfma_f32_16x16x32_bf16 v[20:23],a[140:143],v[52:55],v[20:23]
ds_read_b128 v[44:47],v2 offset:47104
v_mfma_f32_16x16x32_bf16 v[20:23],a[28:31],v[48:51],v[20:23]
s_waitcnt lgkmcnt(4)
v_mfma_f32_16x16x32_bf16 v[20:23],a[252:255],v[56:59],v[20:23]
ds_read_b128 v[48:51],v2 offset:5120
v_mfma_f32_16x16x32_bf16 v[20:23],v[172:175],v[60:63],v[20:23]
buffer_load_dwordx4 v2,s[32:35],s63 offen offset:1024 sc1 lds
v_mfma_f32_16x16x32_bf16 v[16:19],v[168:171],v[60:63],v[16:19]
ds_read_b128 v[52:55],v2 offset:19456
v_mfma_f32_16x16x32_bf16 v[16:19],a[248:251],v[56:59],v[16:19]
s_waitcnt lgkmcnt(4)
v_mfma_f32_16x16x32_bf16 v[16:19],a[32:35],v[32:35],v[16:19]
ds_read_b128 v[56:59],v2 offset:33792
v_mfma_f32_16x16x32_bf16 v[16:19],a[144:147],v[36:39],v[16:19]
buffer_load_dwordx4 v2,s[32:35],s63 offen offset:2048 sc1 lds
v_mfma_f32_16x16x32_bf16 v[20:23],a[148:151],v[36:39],v[20:23]
ds_read_b128 v[60:63],v2 offset:48128
v_mfma_f32_16x16x32_bf16 v[20:23],a[36:39],v[32:35],v[20:23]
s_waitcnt lgkmcnt(4)
v_mfma_f32_16x16x32_bf16 v[20:23],v[68:71],v[40:43],v[20:23]
ds_read_b128 v[32:35],v2 offset:6144
v_mfma_f32_16x16x32_bf16 v[20:23],v[180:183],v[44:47],v[20:23]
buffer_load_dwordx4 v2,s[32:35],s63 offen offset:3072 sc1 lds
v_mfma_f32_16x16x32_bf16 v[16:19],v[176:179],v[44:47],v[16:19]
ds_read_b128 v[36:39],v2 offset:20480
v_mfma_f32_16x16x32_bf16 v[16:19],v[64:67],v[40:43],v[16:19]
s_waitcnt lgkmcnt(4)
v_mfma_f32_16x16x32_bf16 v[16:19],a[40:43],v[48:51],v[16:19]
ds_read_b128 v[40:43],v2 offset:34816
v_mfma_f32_16x16x32_bf16 v[16:19],a[152:155],v[52:55],v[16:19]
buffer_load_dwordx4 v[12:15],v2,s[32:35],s71 offen sc1
s_add_u32 s71,s71,s30
v_mfma_f32_16x16x32_bf16 v[20:23],a[156:159],v[52:55],v[20:23]
ds_read_b128 v[44:47],v2 offset:49152
s_add_u32 s63,s63,4096
v_mfma_f32_16x16x32_bf16 v[20:23],a[44:47],v[48:51],v[20:23]
s_add_u32 m0,s64,61440
s_waitcnt lgkmcnt(4)
v_mfma_f32_16x16x32_bf16 v[20:23],v[76:79],v[56:59],v[20:23]
ds_read_b128 v[48:51],v2 offset:7168
v_mfma_f32_16x16x32_bf16 v[20:23],v[188:191],v[60:63],v[20:23]
buffer_load_dwordx4 v2,s[32:35],s63 offen offset:0 sc1 lds
v_mfma_f32_16x16x32_bf16 v[16:19],v[184:187],v[60:63],v[16:19]
ds_read_b128 v[52:55],v2 offset:21504
v_mfma_f32_16x16x32_bf16 v[16:19],v[72:75],v[56:59],v[16:19]
s_waitcnt lgkmcnt(4)
v_mfma_f32_16x16x32_bf16 v[16:19],a[48:51],v[32:35],v[16:19]
ds_read_b128 v[56:59],v2 offset:35840
v_mfma_f32_16x16x32_bf16 v[16:19],a[160:163],v[36:39],v[16:19]
buffer_load_dwordx4 v2,s[32:35],s63 offen offset:1024 sc1 lds
v_mfma_f32_16x16x32_bf16 v[20:23],a[164:167],v[36:39],v[20:23]
ds_read_b128 v[60:63],v2 offset:50176
v_mfma_f32_16x16x32_bf16 v[20:23],a[52:55],v[32:35],v[20:23]
s_waitcnt lgkmcnt(4)
v_mfma_f32_16x16x32_bf16 v[20:23],v[84:87],v[40:43],v[20:23]
ds_read_b128 v[32:35],v2 offset:8192
v_mfma_f32_16x16x32_bf16 v[20:23],v[196:199],v[44:47],v[20:23]
buffer_load_dwordx4 v2,s[32:35],s63 offen offset:2048 sc1 lds
v_mfma_f32_16x16x32_bf16 v[16:19],v[192:195],v[44:47],v[16:19]
ds_read_b128 v[36:39],v2 offset:22528
v_mfma_f32_16x16x32_bf16 v[16:19],v[80:83],v[40:43],v[16:19]
s_waitcnt lgkmcnt(4)
v_mfma_f32_16x16x32_bf16 v[16:19],a[56:59],v[48:51],v[16:19]
ds_read_b128 v[40:43],v2 offset:36864
v_mfma_f32_16x16x32_bf16 v[16:19],a[168:171],v[52:55],v[16:19]
buffer_load_dwordx4 v2,s[32:35],s63 offen offset:3072 sc1 lds
v_mfma_f32_16x16x32_bf16 v[20:23],a[172:175],v[52:55],v[20:23]
ds_read_b128 v[44:47],v2 offset:51200
v_mfma_f32_16x16x32_bf16 v[20:23],a[60:63],v[48:51],v[20:23]
s_waitcnt lgkmcnt(4)
v_mfma_f32_16x16x32_bf16 v[20:23],v[92:95],v[56:59],v[20:23]
ds_read_b128 v[48:51],v2 offset:9216
v_mfma_f32_16x16x32_bf16 v[20:23],v[204:207],v[60:63],v[20:23]
s_add_u32 s63,s63,4096
s_add_u32 m0,s64,65536
v_mfma_f32_16x16x32_bf16 v[16:19],v[200:203],v[60:63],v[16:19]
ds_read_b128 v[52:55],v2 offset:23552
v_mfma_f32_16x16x32_bf16 v[16:19],v[88:91],v[56:59],v[16:19]
s_waitcnt lgkmcnt(4)
v_mfma_f32_16x16x32_bf16 v[16:19],a[64:67],v[32:35],v[16:19]
ds_read_b128 v[56:59],v2 offset:37888
v_mfma_f32_16x16x32_bf16 v[16:19],a[176:179],v[36:39],v[16:19]
buffer_load_dwordx4 v2,s[32:35],s63 offen offset:0 sc1 lds
v_mfma_f32_16x16x32_bf16 v[20:23],a[180:183],v[36:39],v[20:23]
ds_read_b128 v[60:63],v2 offset:52224
v_mfma_f32_16x16x32_bf16 v[20:23],a[68:71],v[32:35],v[20:23]
s_waitcnt lgkmcnt(4)
v_mfma_f32_16x16x32_bf16 v[20:23],v[100:103],v[40:43],v[20:23]
ds_read_b128 v[32:35],v2 offset:10240
v_mfma_f32_16x16x32_bf16 v[20:23],v[212:215],v[44:47],v[20:23]
buffer_load_dwordx4 v2,s[32:35],s63 offen offset:1024 sc1 lds
v_mfma_f32_16x16x32_bf16 v[16:19],v[208:211],v[44:47],v[16:19]
ds_read_b128 v[36:39],v2 offset:24576
v_mfma_f32_16x16x32_bf16 v[16:19],v[96:99],v[40:43],v[16:19]
s_waitcnt lgkmcnt(4)
v_mfma_f32_16x16x32_bf16 v[16:19],a[72:75],v[48:51],v[16:19]
ds_read_b128 v[40:43],v2 offset:38912
v_mfma_f32_16x16x32_bf16 v[16:19],a[184:187],v[52:55],v[16:19]
buffer_load_dwordx4 v2,s[32:35],s63 offen offset:2048 sc1 lds
v_mfma_f32_16x16x32_bf16 v[20:23],a[188:191],v[52:55],v[20:23]
ds_read_b128 v[44:47],v2 offset:53248
v_mfma_f32_16x16x32_bf16 v[20:23],a[76:79],v[48:51],v[20:23]
s_waitcnt lgkmcnt(4)
v_mfma_f32_16x16x32_bf16 v[20:23],v[108:111],v[56:59],v[20:23]
ds_read_b128 v[48:51],v2 offset:11264
v_mfma_f32_16x16x32_bf16 v[20:23],v[220:223],v[60:63],v[20:23]
buffer_load_dwordx2 v[6:7],v0,s[0:3],s66 offen sc1
v_mfma_f32_16x16x32_bf16 v[16:19],v[216:219],v[60:63],v[16:19]
ds_read_b128 v[52:55],v2 offset:25600
v_mfma_f32_16x16x32_bf16 v[16:19],v[104:107],v[56:59],v[16:19]
s_waitcnt lgkmcnt(4)
v_mfma_f32_16x16x32_bf16 v[16:19],a[80:83],v[32:35],v[16:19]
ds_read_b128 v[56:59],v2 offset:39936
v_mfma_f32_16x16x32_bf16 v[16:19],a[192:195],v[36:39],v[16:19]
buffer_load_dwordx4 v2,s[32:35],s63 offen offset:3072 sc1 lds
s_add_u32 s63,s63,4096
v_mfma_f32_16x16x32_bf16 v[20:23],a[196:199],v[36:39],v[20:23]
ds_read_b128 v[60:63],v2 offset:54272
s_add_u32 m0,s64,69632
v_mfma_f32_16x16x32_bf16 v[20:23],a[84:87],v[32:35],v[20:23]
s_waitcnt lgkmcnt(4)
v_mfma_f32_16x16x32_bf16 v[20:23],v[116:119],v[40:43],v[20:23]
ds_read_b128 v[32:35],v2 offset:12288
v_mfma_f32_16x16x32_bf16 v[20:23],v[228:231],v[44:47],v[20:23]
buffer_load_dwordx4 v2,s[32:35],s63 offen offset:0 sc1 lds
v_mfma_f32_16x16x32_bf16 v[16:19],v[224:227],v[44:47],v[16:19]
ds_read_b128 v[36:39],v2 offset:26624
v_mfma_f32_16x16x32_bf16 v[16:19],v[112:115],v[40:43],v[16:19]
s_waitcnt lgkmcnt(4)
v_mfma_f32_16x16x32_bf16 v[16:19],a[88:91],v[48:51],v[16:19]
ds_read_b128 v[40:43],v2 offset:40960
v_mfma_f32_16x16x32_bf16 v[16:19],a[200:203],v[52:55],v[16:19]
buffer_load_dwordx4 v2,s[32:35],s63 offen offset:1024 sc1 lds
s_add_u32 s63,s63,s31
v_mfma_f32_16x16x32_bf16 v[20:23],a[204:207],v[52:55],v[20:23]
ds_read_b128 v[44:47],v2 offset:55296
v_mfma_f32_16x16x32_bf16 v[20:23],a[92:95],v[48:51],v[20:23]
s_waitcnt lgkmcnt(4)
v_mfma_f32_16x16x32_bf16 v[20:23],v[124:127],v[56:59],v[20:23]
ds_read_b128 v[48:51],v2 offset:13312
s_waitcnt vmcnt(11)
v_mfma_f32_16x16x32_bf16 v[20:23],v[236:239],v[60:63],v[20:23]
v_lshlrev_b32 v24,16,v12
v_and_b32 v25,0xffff0000,v12
v_mfma_f32_16x16x32_bf16 v[16:19],v[232:235],v[60:63],v[16:19]
ds_read_b128 v[52:55],v2 offset:27648
v_lshlrev_b32 v26,16,v13
v_mfma_f32_16x16x32_bf16 v[16:19],v[120:123],v[56:59],v[16:19]
v_and_b32 v27,0xffff0000,v13
s_waitcnt lgkmcnt(4)
v_mfma_f32_16x16x32_bf16 v[16:19],a[96:99],v[32:35],v[16:19]
ds_read_b128 v[56:59],v2 offset:41984
v_lshlrev_b32 v28,16,v14
v_mfma_f32_16x16x32_bf16 v[16:19],a[208:211],v[36:39],v[16:19]
v_and_b32 v29,0xffff0000,v14
v_lshlrev_b32 v30,16,v15
v_mfma_f32_16x16x32_bf16 v[20:23],a[212:215],v[36:39],v[20:23]
s_barrier
v_mfma_f32_16x16x32_bf16 v[20:23],a[100:103],v[32:35],v[20:23]
ds_read_b128 v[60:63],v2 offset:56320
s_waitcnt lgkmcnt(4)
v_mfma_f32_16x16x32_bf16 v[20:23],v[132:135],v[40:43],v[20:23]
ds_read_b128 v[32:35],v4 offset:0
v_and_b32 v31,0xffff0000,v15
v_mfma_f32_16x16x32_bf16 v[20:23],v[244:247],v[44:47],v[20:23]
v_mfma_f32_16x16x32_bf16 v[16:19],v[240:243],v[44:47],v[16:19]
ds_read_b128 v[36:39],v4 offset:14336
v_mfma_f32_16x16x32_bf16 v[16:19],v[128:131],v[40:43],v[16:19]
s_waitcnt lgkmcnt(4)
v_mfma_f32_16x16x32_bf16 v[16:19],a[104:107],v[48:51],v[16:19]
ds_read_b128 v[40:43],v4 offset:28672
v_mfma_f32_16x16x32_bf16 v[16:19],a[216:219],v[52:55],v[16:19]
v_mfma_f32_16x16x32_bf16 v[20:23],a[220:223],v[52:55],v[20:23]
ds_read_b128 v[44:47],v4 offset:43008
v_mfma_f32_16x16x32_bf16 v[20:23],a[108:111],v[48:51],v[20:23]
s_waitcnt lgkmcnt(4)
v_mfma_f32_16x16x32_bf16 v[20:23],v[140:143],v[56:59],v[20:23]
ds_read_b128 v[48:51],v4 offset:1024
v_mfma_f32_16x16x32_bf16 v[20:23],v[252:255],v[60:63],v[20:23]
v_mfma_f32_16x16x32_bf16 v[16:19],v[248:251],v[60:63],v[16:19]
ds_read_b128 v[52:55],v4 offset:15360
s_sub_u32 s62,s62,1
v_mfma_f32_16x16x32_bf16 v[16:19],v[136:139],v[56:59],v[16:19]
s_cbranch_scc1 .L_dlb_1
s_waitcnt lgkmcnt(4)
v_mfma_f32_16x16x32_bf16 v[24:27],a[0:3],v[32:35],v[24:27]
ds_read_b128 v[56:59],v4 offset:29696
v_mfma_f32_16x16x32_bf16 v[24:27],a[112:115],v[36:39],v[24:27]
v_mfma_f32_16x16x32_bf16 v[28:31],a[116:119],v[36:39],v[28:31]
ds_read_b128 v[60:63],v4 offset:44032
v_mfma_f32_16x16x32_bf16 v[28:31],a[4:7],v[32:35],v[28:31]
s_add_u32 s68,s68,1187840
s_waitcnt lgkmcnt(4)
v_mfma_f32_16x16x32_bf16 v[28:31],a[228:231],v[40:43],v[28:31]
ds_read_b128 v[32:35],v4 offset:2048
v_mfma_f32_16x16x32_bf16 v[28:31],v[148:151],v[44:47],v[28:31]
buffer_store_dwordx4 v[20:23],v2,s[32:35],s68 offen offset:1024 sc1
v_mfma_f32_16x16x32_bf16 v[24:27],v[144:147],v[44:47],v[24:27]
ds_read_b128 v[36:39],v4 offset:16384
v_mfma_f32_16x16x32_bf16 v[24:27],a[224:227],v[40:43],v[24:27]
s_waitcnt lgkmcnt(4)
v_mfma_f32_16x16x32_bf16 v[24:27],a[8:11],v[48:51],v[24:27]
ds_read_b128 v[40:43],v4 offset:30720
v_mfma_f32_16x16x32_bf16 v[24:27],a[120:123],v[52:55],v[24:27]
buffer_store_dwordx4 v[16:19],v2,s[32:35],s68 offen sc1
s_mov_b32 s67,s68
v_mfma_f32_16x16x32_bf16 v[28:31],a[124:127],v[52:55],v[28:31]
ds_read_b128 v[44:47],v4 offset:45056
v_mfma_f32_16x16x32_bf16 v[28:31],a[12:15],v[48:51],v[28:31]
s_waitcnt lgkmcnt(4)
v_mfma_f32_16x16x32_bf16 v[28:31],a[236:239],v[56:59],v[28:31]
ds_read_b128 v[48:51],v4 offset:3072
v_mfma_f32_16x16x32_bf16 v[28:31],v[156:159],v[60:63],v[28:31]
v_mfma_f32_16x16x32_bf16 v[24:27],v[152:155],v[60:63],v[24:27]
ds_read_b128 v[52:55],v4 offset:17408
s_waitcnt vmcnt(5)
v_mfma_f32_16x16x32_bf16 v[24:27],a[232:235],v[56:59],v[24:27]
v_cmp_ne_u64 vcc,v[8:9],v[6:7]
s_waitcnt lgkmcnt(4)
v_mfma_f32_16x16x32_bf16 v[24:27],a[16:19],v[32:35],v[24:27]
ds_read_b128 v[56:59],v4 offset:31744
s_cbranch_vccnz .L_rtl1_1
.L_bkl1_1:
v_mfma_f32_16x16x32_bf16 v[24:27],a[128:131],v[36:39],v[24:27]
s_add_u32 s66,s66,s30
v_mfma_f32_16x16x32_bf16 v[28:31],a[132:135],v[36:39],v[28:31]
s_waitcnt vmcnt(2)
s_barrier
v_mfma_f32_16x16x32_bf16 v[28:31],a[20:23],v[32:35],v[28:31]
ds_read_b128 v[60:63],v4 offset:46080
s_waitcnt lgkmcnt(4)
v_mfma_f32_16x16x32_bf16 v[28:31],a[244:247],v[40:43],v[28:31]
ds_read_b128 v[32:35],v4 offset:4096
s_add_u32 m0,s64,0
v_mfma_f32_16x16x32_bf16 v[28:31],v[164:167],v[44:47],v[28:31]
buffer_load_dwordx4 v2,s[32:35],s63 offen offset:0 sc1 lds
v_mfma_f32_16x16x32_bf16 v[24:27],v[160:163],v[44:47],v[24:27]
ds_read_b128 v[36:39],v4 offset:18432
v_mfma_f32_16x16x32_bf16 v[24:27],a[240:243],v[40:43],v[24:27]
s_waitcnt lgkmcnt(4)
v_mfma_f32_16x16x32_bf16 v[24:27],a[24:27],v[48:51],v[24:27]
ds_read_b128 v[40:43],v4 offset:32768
v_mfma_f32_16x16x32_bf16 v[24:27],a[136:139],v[52:55],v[24:27]
v_mfma_f32_16x16x32_bf16 v[28:31],a[140:143],v[52:55],v[28:31]
ds_read_b128 v[44:47],v4 offset:47104
v_mfma_f32_16x16x32_bf16 v[28:31],a[28:31],v[48:51],v[28:31]
s_waitcnt lgkmcnt(4)
v_mfma_f32_16x16x32_bf16 v[28:31],a[252:255],v[56:59],v[28:31]
ds_read_b128 v[48:51],v4 offset:5120
v_mfma_f32_16x16x32_bf16 v[28:31],v[172:175],v[60:63],v[28:31]
buffer_load_dwordx4 v2,s[32:35],s63 offen offset:1024 sc1 lds
v_mfma_f32_16x16x32_bf16 v[24:27],v[168:171],v[60:63],v[24:27]
ds_read_b128 v[52:55],v4 offset:19456
v_mfma_f32_16x16x32_bf16 v[24:27],a[248:251],v[56:59],v[24:27]
s_waitcnt lgkmcnt(4)
v_mfma_f32_16x16x32_bf16 v[24:27],a[32:35],v[32:35],v[24:27]
ds_read_b128 v[56:59],v4 offset:33792
v_mfma_f32_16x16x32_bf16 v[24:27],a[144:147],v[36:39],v[24:27]
buffer_load_dwordx4 v2,s[32:35],s63 offen offset:2048 sc1 lds
v_mfma_f32_16x16x32_bf16 v[28:31],a[148:151],v[36:39],v[28:31]
ds_read_b128 v[60:63],v4 offset:48128
v_mfma_f32_16x16x32_bf16 v[28:31],a[36:39],v[32:35],v[28:31]
s_waitcnt lgkmcnt(4)
v_mfma_f32_16x16x32_bf16 v[28:31],v[68:71],v[40:43],v[28:31]
ds_read_b128 v[32:35],v4 offset:6144
v_mfma_f32_16x16x32_bf16 v[28:31],v[180:183],v[44:47],v[28:31]
buffer_load_dwordx4 v2,s[32:35],s63 offen offset:3072 sc1 lds
v_mfma_f32_16x16x32_bf16 v[24:27],v[176:179],v[44:47],v[24:27]
ds_read_b128 v[36:39],v4 offset:20480
v_mfma_f32_16x16x32_bf16 v[24:27],v[64:67],v[40:43],v[24:27]
s_waitcnt lgkmcnt(4)
v_mfma_f32_16x16x32_bf16 v[24:27],a[40:43],v[48:51],v[24:27]
ds_read_b128 v[40:43],v4 offset:34816
v_mfma_f32_16x16x32_bf16 v[24:27],a[152:155],v[52:55],v[24:27]
buffer_load_dwordx4 v[12:15],v2,s[32:35],s71 offen sc1
s_add_u32 s71,s71,s30
v_mfma_f32_16x16x32_bf16 v[28:31],a[156:159],v[52:55],v[28:31]
ds_read_b128 v[44:47],v4 offset:49152
s_add_u32 s63,s63,4096
v_mfma_f32_16x16x32_bf16 v[28:31],a[44:47],v[48:51],v[28:31]
s_add_u32 m0,s64,4096
s_waitcnt lgkmcnt(4)
v_mfma_f32_16x16x32_bf16 v[28:31],v[76:79],v[56:59],v[28:31]
ds_read_b128 v[48:51],v4 offset:7168
v_mfma_f32_16x16x32_bf16 v[28:31],v[188:191],v[60:63],v[28:31]
buffer_load_dwordx4 v2,s[32:35],s63 offen offset:0 sc1 lds
v_mfma_f32_16x16x32_bf16 v[24:27],v[184:187],v[60:63],v[24:27]
ds_read_b128 v[52:55],v4 offset:21504
v_mfma_f32_16x16x32_bf16 v[24:27],v[72:75],v[56:59],v[24:27]
s_waitcnt lgkmcnt(4)
v_mfma_f32_16x16x32_bf16 v[24:27],a[48:51],v[32:35],v[24:27]
ds_read_b128 v[56:59],v4 offset:35840
v_mfma_f32_16x16x32_bf16 v[24:27],a[160:163],v[36:39],v[24:27]
buffer_load_dwordx4 v2,s[32:35],s63 offen offset:1024 sc1 lds
v_mfma_f32_16x16x32_bf16 v[28:31],a[164:167],v[36:39],v[28:31]
ds_read_b128 v[60:63],v4 offset:50176
v_mfma_f32_16x16x32_bf16 v[28:31],a[52:55],v[32:35],v[28:31]
s_waitcnt lgkmcnt(4)
v_mfma_f32_16x16x32_bf16 v[28:31],v[84:87],v[40:43],v[28:31]
ds_read_b128 v[32:35],v4 offset:8192
v_mfma_f32_16x16x32_bf16 v[28:31],v[196:199],v[44:47],v[28:31]
buffer_load_dwordx4 v2,s[32:35],s63 offen offset:2048 sc1 lds
v_mfma_f32_16x16x32_bf16 v[24:27],v[192:195],v[44:47],v[24:27]
ds_read_b128 v[36:39],v4 offset:22528
v_mfma_f32_16x16x32_bf16 v[24:27],v[80:83],v[40:43],v[24:27]
s_waitcnt lgkmcnt(4)
v_mfma_f32_16x16x32_bf16 v[24:27],a[56:59],v[48:51],v[24:27]
ds_read_b128 v[40:43],v4 offset:36864
v_mfma_f32_16x16x32_bf16 v[24:27],a[168:171],v[52:55],v[24:27]
buffer_load_dwordx4 v2,s[32:35],s63 offen offset:3072 sc1 lds
v_mfma_f32_16x16x32_bf16 v[28:31],a[172:175],v[52:55],v[28:31]
ds_read_b128 v[44:47],v4 offset:51200
v_mfma_f32_16x16x32_bf16 v[28:31],a[60:63],v[48:51],v[28:31]
s_waitcnt lgkmcnt(4)
v_mfma_f32_16x16x32_bf16 v[28:31],v[92:95],v[56:59],v[28:31]
ds_read_b128 v[48:51],v4 offset:9216
v_mfma_f32_16x16x32_bf16 v[28:31],v[204:207],v[60:63],v[28:31]
s_add_u32 s63,s63,4096
s_add_u32 m0,s64,8192
v_mfma_f32_16x16x32_bf16 v[24:27],v[200:203],v[60:63],v[24:27]
ds_read_b128 v[52:55],v4 offset:23552
v_mfma_f32_16x16x32_bf16 v[24:27],v[88:91],v[56:59],v[24:27]
s_waitcnt lgkmcnt(4)
v_mfma_f32_16x16x32_bf16 v[24:27],a[64:67],v[32:35],v[24:27]
ds_read_b128 v[56:59],v4 offset:37888
v_mfma_f32_16x16x32_bf16 v[24:27],a[176:179],v[36:39],v[24:27]
buffer_load_dwordx4 v2,s[32:35],s63 offen offset:0 sc1 lds
v_mfma_f32_16x16x32_bf16 v[28:31],a[180:183],v[36:39],v[28:31]
ds_read_b128 v[60:63],v4 offset:52224
v_mfma_f32_16x16x32_bf16 v[28:31],a[68:71],v[32:35],v[28:31]
s_waitcnt lgkmcnt(4)
v_mfma_f32_16x16x32_bf16 v[28:31],v[100:103],v[40:43],v[28:31]
ds_read_b128 v[32:35],v4 offset:10240
v_mfma_f32_16x16x32_bf16 v[28:31],v[212:215],v[44:47],v[28:31]
buffer_load_dwordx4 v2,s[32:35],s63 offen offset:1024 sc1 lds
v_mfma_f32_16x16x32_bf16 v[24:27],v[208:211],v[44:47],v[24:27]
ds_read_b128 v[36:39],v4 offset:24576
v_mfma_f32_16x16x32_bf16 v[24:27],v[96:99],v[40:43],v[24:27]
s_waitcnt lgkmcnt(4)
v_mfma_f32_16x16x32_bf16 v[24:27],a[72:75],v[48:51],v[24:27]
ds_read_b128 v[40:43],v4 offset:38912
v_mfma_f32_16x16x32_bf16 v[24:27],a[184:187],v[52:55],v[24:27]
buffer_load_dwordx4 v2,s[32:35],s63 offen offset:2048 sc1 lds
v_mfma_f32_16x16x32_bf16 v[28:31],a[188:191],v[52:55],v[28:31]
ds_read_b128 v[44:47],v4 offset:53248
v_mfma_f32_16x16x32_bf16 v[28:31],a[76:79],v[48:51],v[28:31]
s_waitcnt lgkmcnt(4)
v_mfma_f32_16x16x32_bf16 v[28:31],v[108:111],v[56:59],v[28:31]
ds_read_b128 v[48:51],v4 offset:11264
v_mfma_f32_16x16x32_bf16 v[28:31],v[220:223],v[60:63],v[28:31]
buffer_load_dwordx2 v[6:7],v0,s[0:3],s66 offen sc1
v_mfma_f32_16x16x32_bf16 v[24:27],v[216:219],v[60:63],v[24:27]
ds_read_b128 v[52:55],v4 offset:25600
v_mfma_f32_16x16x32_bf16 v[24:27],v[104:107],v[56:59],v[24:27]
s_waitcnt lgkmcnt(4)
v_mfma_f32_16x16x32_bf16 v[24:27],a[80:83],v[32:35],v[24:27]
ds_read_b128 v[56:59],v4 offset:39936
v_mfma_f32_16x16x32_bf16 v[24:27],a[192:195],v[36:39],v[24:27]
buffer_load_dwordx4 v2,s[32:35],s63 offen offset:3072 sc1 lds
s_add_u32 s63,s63,4096
v_mfma_f32_16x16x32_bf16 v[28:31],a[196:199],v[36:39],v[28:31]
ds_read_b128 v[60:63],v4 offset:54272
s_add_u32 m0,s64,12288
v_mfma_f32_16x16x32_bf16 v[28:31],a[84:87],v[32:35],v[28:31]
s_waitcnt lgkmcnt(4)
v_mfma_f32_16x16x32_bf16 v[28:31],v[116:119],v[40:43],v[28:31]
ds_read_b128 v[32:35],v4 offset:12288
v_mfma_f32_16x16x32_bf16 v[28:31],v[228:231],v[44:47],v[28:31]
buffer_load_dwordx4 v2,s[32:35],s63 offen offset:0 sc1 lds
v_mfma_f32_16x16x32_bf16 v[24:27],v[224:227],v[44:47],v[24:27]
ds_read_b128 v[36:39],v4 offset:26624
v_mfma_f32_16x16x32_bf16 v[24:27],v[112:115],v[40:43],v[24:27]
s_waitcnt lgkmcnt(4)
v_mfma_f32_16x16x32_bf16 v[24:27],a[88:91],v[48:51],v[24:27]
ds_read_b128 v[40:43],v4 offset:40960
v_mfma_f32_16x16x32_bf16 v[24:27],a[200:203],v[52:55],v[24:27]
buffer_load_dwordx4 v2,s[32:35],s63 offen offset:1024 sc1 lds
s_add_u32 s63,s63,s31
v_mfma_f32_16x16x32_bf16 v[28:31],a[204:207],v[52:55],v[28:31]
ds_read_b128 v[44:47],v4 offset:55296
v_mfma_f32_16x16x32_bf16 v[28:31],a[92:95],v[48:51],v[28:31]
s_waitcnt lgkmcnt(4)
v_mfma_f32_16x16x32_bf16 v[28:31],v[124:127],v[56:59],v[28:31]
ds_read_b128 v[48:51],v4 offset:13312
s_waitcnt vmcnt(11)
v_mfma_f32_16x16x32_bf16 v[28:31],v[236:239],v[60:63],v[28:31]
v_lshlrev_b32 v16,16,v12
v_and_b32 v17,0xffff0000,v12
v_mfma_f32_16x16x32_bf16 v[24:27],v[232:235],v[60:63],v[24:27]
ds_read_b128 v[52:55],v4 offset:27648
v_lshlrev_b32 v18,16,v13
v_mfma_f32_16x16x32_bf16 v[24:27],v[120:123],v[56:59],v[24:27]
v_and_b32 v19,0xffff0000,v13
s_waitcnt lgkmcnt(4)
v_mfma_f32_16x16x32_bf16 v[24:27],a[96:99],v[32:35],v[24:27]
ds_read_b128 v[56:59],v4 offset:41984
v_lshlrev_b32 v20,16,v14
v_mfma_f32_16x16x32_bf16 v[24:27],a[208:211],v[36:39],v[24:27]
v_and_b32 v21,0xffff0000,v14
v_lshlrev_b32 v22,16,v15
v_mfma_f32_16x16x32_bf16 v[28:31],a[212:215],v[36:39],v[28:31]
s_barrier
v_mfma_f32_16x16x32_bf16 v[28:31],a[100:103],v[32:35],v[28:31]
ds_read_b128 v[60:63],v4 offset:56320
s_waitcnt lgkmcnt(4)
v_mfma_f32_16x16x32_bf16 v[28:31],v[132:135],v[40:43],v[28:31]
ds_read_b128 v[32:35],v2 offset:0
v_and_b32 v23,0xffff0000,v15
v_mfma_f32_16x16x32_bf16 v[28:31],v[244:247],v[44:47],v[28:31]
v_mfma_f32_16x16x32_bf16 v[24:27],v[240:243],v[44:47],v[24:27]
ds_read_b128 v[36:39],v2 offset:14336
v_mfma_f32_16x16x32_bf16 v[24:27],v[128:131],v[40:43],v[24:27]
s_waitcnt lgkmcnt(4)
v_mfma_f32_16x16x32_bf16 v[24:27],a[104:107],v[48:51],v[24:27]
ds_read_b128 v[40:43],v2 offset:28672
v_mfma_f32_16x16x32_bf16 v[24:27],a[216:219],v[52:55],v[24:27]
v_mfma_f32_16x16x32_bf16 v[28:31],a[220:223],v[52:55],v[28:31]
ds_read_b128 v[44:47],v2 offset:43008
v_mfma_f32_16x16x32_bf16 v[28:31],a[108:111],v[48:51],v[28:31]
s_waitcnt lgkmcnt(4)
v_mfma_f32_16x16x32_bf16 v[28:31],v[140:143],v[56:59],v[28:31]
ds_read_b128 v[48:51],v2 offset:1024
v_mfma_f32_16x16x32_bf16 v[28:31],v[252:255],v[60:63],v[28:31]
v_mfma_f32_16x16x32_bf16 v[24:27],v[248:251],v[60:63],v[24:27]
ds_read_b128 v[52:55],v2 offset:15360
v_mfma_f32_16x16x32_bf16 v[24:27],v[136:139],v[56:59],v[24:27]
s_branch .L_dll_1
.L_dlb_1:
s_mov_b32 s62,s58
s_mov_b32 s70,s59
s_add_u32 s74,s74,8192
v_lshrrev_b32 v5,8,v2
v_lshl_add_u32 v5,v5,4,s74
v_and_b32 v3,128,v2
v_lshl_add_u32 v5,v3,2,v5
v_and_b32 v3,16,v2
v_lshl_add_u32 v5,v3,4,v5
v_and_b32 v3,96,v2
v_lshl_add_u32 v5,v3,1,v5
v_lshrrev_b32 v3,2,v2
v_add_u32 v3,s74,v3
s_add_u32 s66,s66,2375680
s_add_u32 s63,s63,2375680
s_add_u32 s71,s71,2375680
s_mov_b32 s68,98304
s_waitcnt vmcnt(3)
v_not_b32 v6,v8
s_mov_b32 s34,79446016
.p2align 6
.L_dml_1:
s_waitcnt lgkmcnt(4)
v_mfma_f32_16x16x32_bf16 v[24:27],a[0:3],v[32:35],v[24:27]
ds_read_b128 v[56:59],v4 offset:29696
v_mfma_f32_16x16x32_bf16 v[24:27],a[112:115],v[36:39],v[24:27]
v_mfma_f32_16x16x32_bf16 v[28:31],a[116:119],v[36:39],v[28:31]
ds_read_b128 v[60:63],v4 offset:44032
v_mfma_f32_16x16x32_bf16 v[28:31],a[4:7],v[32:35],v[28:31]
v_cvt_pk_bf16_f32 v15,v22,v23
s_waitcnt lgkmcnt(4)
v_mfma_f32_16x16x32_bf16 v[28:31],a[228:231],v[40:43],v[28:31]
ds_read_b128 v[32:35],v4 offset:2048
v_cvt_pk_bf16_f32 v14,v20,v21
v_mfma_f32_16x16x32_bf16 v[28:31],v[148:151],v[44:47],v[28:31]
v_cvt_pk_bf16_f32 v13,v18,v19
v_cvt_pk_bf16_f32 v12,v16,v17
v_mfma_f32_16x16x32_bf16 v[24:27],v[144:147],v[44:47],v[24:27]
)#"
R"#(
ds_read_b128 v[36:39],v4 offset:16384
ds_write_b128 v5,v[12:15]
v_mfma_f32_16x16x32_bf16 v[24:27],a[224:227],v[40:43],v[24:27]
ds_read2st64_b32 v[12:13],v3 offset0:0 offset1:1
s_waitcnt lgkmcnt(6)
v_mfma_f32_16x16x32_bf16 v[24:27],a[8:11],v[48:51],v[24:27]
ds_read_b128 v[40:43],v4 offset:30720
ds_read2st64_b32 v[14:15],v3 offset0:2 offset1:3
v_mfma_f32_16x16x32_bf16 v[24:27],a[120:123],v[52:55],v[24:27]
v_mfma_f32_16x16x32_bf16 v[28:31],a[124:127],v[52:55],v[28:31]
ds_read_b128 v[44:47],v4 offset:45056
v_mfma_f32_16x16x32_bf16 v[28:31],a[12:15],v[48:51],v[28:31]
s_waitcnt lgkmcnt(7)
v_mfma_f32_16x16x32_bf16 v[28:31],a[236:239],v[56:59],v[28:31]
ds_read_b128 v[48:51],v4 offset:3072
v_mfma_f32_16x16x32_bf16 v[28:31],v[156:159],v[60:63],v[28:31]
s_waitcnt lgkmcnt(2)
buffer_atomic_pk_add_bf16 v12,v1,s[44:47],s65 offen offset:0 sc1
v_mfma_f32_16x16x32_bf16 v[24:27],v[152:155],v[60:63],v[24:27]
ds_read_b128 v[52:55],v4 offset:17408
s_waitcnt vmcnt(4)
v_mfma_f32_16x16x32_bf16 v[24:27],a[232:235],v[56:59],v[24:27]
v_cmp_ne_u64 vcc,v[8:9],v[6:7]
v_mfma_f32_16x16x32_bf16 v[24:27],a[16:19],v[32:35],v[24:27]
ds_read_b128 v[56:59],v4 offset:31744
s_cbranch_vccnz .L_rtm1_1
.L_bkm1_1:
v_mfma_f32_16x16x32_bf16 v[24:27],a[128:131],v[36:39],v[24:27]
s_add_u32 s66,s66,s30
s_add_u32 s70,s70,s75
v_mfma_f32_16x16x32_bf16 v[28:31],a[132:135],v[36:39],v[28:31]
s_waitcnt vmcnt(1)
s_barrier
v_mfma_f32_16x16x32_bf16 v[28:31],a[20:23],v[32:35],v[28:31]
buffer_atomic_pk_add_bf16 v13,v1,s[44:47],s65 offen offset:2048 sc1
s_waitcnt lgkmcnt(3)
v_mfma_f32_16x16x32_bf16 v[28:31],a[244:247],v[40:43],v[28:31]
ds_read_b128 v[60:63],v4 offset:46080
ds_read_b128 v[32:35],v4 offset:4096
v_mfma_f32_16x16x32_bf16 v[28:31],v[164:167],v[44:47],v[28:31]
s_add_u32 m0,s64,0
s_add_u32 s65,s65,16384
v_mfma_f32_16x16x32_bf16 v[24:27],v[160:163],v[44:47],v[24:27]
ds_read_b128 v[36:39],v4 offset:18432
v_mfma_f32_16x16x32_bf16 v[24:27],a[240:243],v[40:43],v[24:27]
buffer_atomic_pk_add_bf16 v14,v1,s[44:47],s65 offen offset:0 sc1
s_waitcnt lgkmcnt(4)
v_mfma_f32_16x16x32_bf16 v[24:27],a[24:27],v[48:51],v[24:27]
ds_read_b128 v[40:43],v4 offset:32768
v_mfma_f32_16x16x32_bf16 v[24:27],a[136:139],v[52:55],v[24:27]
buffer_atomic_pk_add_bf16 v15,v1,s[44:47],s65 offen offset:2048 sc1
s_and_b32 s65,s70,0x3f8000
v_mfma_f32_16x16x32_bf16 v[28:31],a[140:143],v[52:55],v[28:31]
ds_read_b128 v[44:47],v4 offset:47104
v_mfma_f32_16x16x32_bf16 v[28:31],a[28:31],v[48:51],v[28:31]
s_waitcnt lgkmcnt(4)
v_mfma_f32_16x16x32_bf16 v[28:31],a[252:255],v[56:59],v[28:31]
ds_read_b128 v[48:51],v4 offset:5120
v_mfma_f32_16x16x32_bf16 v[28:31],v[172:175],v[60:63],v[28:31]
buffer_load_dwordx4 v2,s[32:35],s63 offen offset:0 sc1 lds
v_mfma_f32_16x16x32_bf16 v[24:27],v[168:171],v[60:63],v[24:27]
ds_read_b128 v[52:55],v4 offset:19456
v_mfma_f32_16x16x32_bf16 v[24:27],a[248:251],v[56:59],v[24:27]
s_waitcnt lgkmcnt(4)
v_mfma_f32_16x16x32_bf16 v[24:27],a[32:35],v[32:35],v[24:27]
ds_read_b128 v[56:59],v4 offset:33792
v_mfma_f32_16x16x32_bf16 v[24:27],a[144:147],v[36:39],v[24:27]
buffer_load_dwordx4 v2,s[32:35],s63 offen offset:1024 sc1 lds
v_mfma_f32_16x16x32_bf16 v[28:31],a[148:151],v[36:39],v[28:31]
ds_read_b128 v[60:63],v4 offset:48128
v_mfma_f32_16x16x32_bf16 v[28:31],a[36:39],v[32:35],v[28:31]
s_waitcnt lgkmcnt(4)
v_mfma_f32_16x16x32_bf16 v[28:31],v[68:71],v[40:43],v[28:31]
ds_read_b128 v[32:35],v4 offset:6144
v_mfma_f32_16x16x32_bf16 v[28:31],v[180:183],v[44:47],v[28:31]
buffer_load_dwordx4 v2,s[32:35],s63 offen offset:2048 sc1 lds
v_mfma_f32_16x16x32_bf16 v[24:27],v[176:179],v[44:47],v[24:27]
ds_read_b128 v[36:39],v4 offset:20480
v_mfma_f32_16x16x32_bf16 v[24:27],v[64:67],v[40:43],v[24:27]
s_waitcnt lgkmcnt(4)
v_mfma_f32_16x16x32_bf16 v[24:27],a[40:43],v[48:51],v[24:27]
ds_read_b128 v[40:43],v4 offset:34816
v_mfma_f32_16x16x32_bf16 v[24:27],a[152:155],v[52:55],v[24:27]
buffer_load_dwordx4 v2,s[32:35],s63 offen offset:3072 sc1 lds
v_mfma_f32_16x16x32_bf16 v[28:31],a[156:159],v[52:55],v[28:31]
ds_read_b128 v[44:47],v4 offset:49152
v_mfma_f32_16x16x32_bf16 v[28:31],a[44:47],v[48:51],v[28:31]
s_waitcnt lgkmcnt(4)
v_mfma_f32_16x16x32_bf16 v[28:31],v[76:79],v[56:59],v[28:31]
ds_read_b128 v[48:51],v4 offset:7168
v_mfma_f32_16x16x32_bf16 v[28:31],v[188:191],v[60:63],v[28:31]
buffer_load_dwordx4 v[12:15],v2,s[32:35],s71 offen sc1
s_add_u32 s71,s71,s30
v_mfma_f32_16x16x32_bf16 v[24:27],v[184:187],v[60:63],v[24:27]
ds_read_b128 v[52:55],v4 offset:21504
s_add_u32 s63,s63,4096
v_mfma_f32_16x16x32_bf16 v[24:27],v[72:75],v[56:59],v[24:27]
s_add_u32 m0,s64,4096
s_waitcnt lgkmcnt(4)
v_mfma_f32_16x16x32_bf16 v[24:27],a[48:51],v[32:35],v[24:27]
ds_read_b128 v[56:59],v4 offset:35840
v_mfma_f32_16x16x32_bf16 v[24:27],a[160:163],v[36:39],v[24:27]
buffer_load_dwordx4 v2,s[32:35],s63 offen offset:0 sc1 lds
v_mfma_f32_16x16x32_bf16 v[28:31],a[164:167],v[36:39],v[28:31]
ds_read_b128 v[60:63],v4 offset:50176
v_mfma_f32_16x16x32_bf16 v[28:31],a[52:55],v[32:35],v[28:31]
s_waitcnt lgkmcnt(4)
v_mfma_f32_16x16x32_bf16 v[28:31],v[84:87],v[40:43],v[28:31]
ds_read_b128 v[32:35],v4 offset:8192
v_mfma_f32_16x16x32_bf16 v[28:31],v[196:199],v[44:47],v[28:31]
buffer_load_dwordx4 v2,s[32:35],s63 offen offset:1024 sc1 lds
v_mfma_f32_16x16x32_bf16 v[24:27],v[192:195],v[44:47],v[24:27]
ds_read_b128 v[36:39],v4 offset:22528
v_mfma_f32_16x16x32_bf16 v[24:27],v[80:83],v[40:43],v[24:27]
s_waitcnt lgkmcnt(4)
v_mfma_f32_16x16x32_bf16 v[24:27],a[56:59],v[48:51],v[24:27]
ds_read_b128 v[40:43],v4 offset:36864
v_mfma_f32_16x16x32_bf16 v[24:27],a[168:171],v[52:55],v[24:27]
buffer_load_dwordx4 v2,s[32:35],s63 offen offset:2048 sc1 lds
v_mfma_f32_16x16x32_bf16 v[28:31],a[172:175],v[52:55],v[28:31]
ds_read_b128 v[44:47],v4 offset:51200
v_mfma_f32_16x16x32_bf16 v[28:31],a[60:63],v[48:51],v[28:31]
s_waitcnt lgkmcnt(4)
v_mfma_f32_16x16x32_bf16 v[28:31],v[92:95],v[56:59],v[28:31]
ds_read_b128 v[48:51],v4 offset:9216
v_mfma_f32_16x16x32_bf16 v[28:31],v[204:207],v[60:63],v[28:31]
buffer_load_dwordx4 v2,s[32:35],s63 offen offset:3072 sc1 lds
s_add_u32 s63,s63,4096
v_mfma_f32_16x16x32_bf16 v[24:27],v[200:203],v[60:63],v[24:27]
ds_read_b128 v[52:55],v4 offset:23552
s_add_u32 m0,s64,8192
v_mfma_f32_16x16x32_bf16 v[24:27],v[88:91],v[56:59],v[24:27]
s_waitcnt lgkmcnt(4)
v_mfma_f32_16x16x32_bf16 v[24:27],a[64:67],v[32:35],v[24:27]
ds_read_b128 v[56:59],v4 offset:37888
v_mfma_f32_16x16x32_bf16 v[24:27],a[176:179],v[36:39],v[24:27]
buffer_load_dwordx4 v2,s[32:35],s63 offen offset:0 sc1 lds
v_mfma_f32_16x16x32_bf16 v[28:31],a[180:183],v[36:39],v[28:31]
ds_read_b128 v[60:63],v4 offset:52224
v_mfma_f32_16x16x32_bf16 v[28:31],a[68:71],v[32:35],v[28:31]
s_waitcnt lgkmcnt(4)
v_mfma_f32_16x16x32_bf16 v[28:31],v[100:103],v[40:43],v[28:31]
ds_read_b128 v[32:35],v4 offset:10240
v_mfma_f32_16x16x32_bf16 v[28:31],v[212:215],v[44:47],v[28:31]
buffer_load_dwordx4 v2,s[32:35],s63 offen offset:1024 sc1 lds
v_mfma_f32_16x16x32_bf16 v[24:27],v[208:211],v[44:47],v[24:27]
ds_read_b128 v[36:39],v4 offset:24576
v_mfma_f32_16x16x32_bf16 v[24:27],v[96:99],v[40:43],v[24:27]
s_waitcnt lgkmcnt(4)
v_mfma_f32_16x16x32_bf16 v[24:27],a[72:75],v[48:51],v[24:27]
ds_read_b128 v[40:43],v4 offset:38912
v_mfma_f32_16x16x32_bf16 v[24:27],a[184:187],v[52:55],v[24:27]
buffer_load_dwordx4 v2,s[32:35],s63 offen offset:2048 sc1 lds
v_mfma_f32_16x16x32_bf16 v[28:31],a[188:191],v[52:55],v[28:31]
ds_read_b128 v[44:47],v4 offset:53248
v_mfma_f32_16x16x32_bf16 v[28:31],a[76:79],v[48:51],v[28:31]
s_waitcnt lgkmcnt(4)
v_mfma_f32_16x16x32_bf16 v[28:31],v[108:111],v[56:59],v[28:31]
ds_read_b128 v[48:51],v4 offset:11264
v_mfma_f32_16x16x32_bf16 v[28:31],v[220:223],v[60:63],v[28:31]
buffer_load_dwordx2 v[6:7],v0,s[0:3],s66 offen sc1
v_mfma_f32_16x16x32_bf16 v[24:27],v[216:219],v[60:63],v[24:27]
ds_read_b128 v[52:55],v4 offset:25600
v_mfma_f32_16x16x32_bf16 v[24:27],v[104:107],v[56:59],v[24:27]
s_waitcnt lgkmcnt(4)
v_mfma_f32_16x16x32_bf16 v[24:27],a[80:83],v[32:35],v[24:27]
ds_read_b128 v[56:59],v4 offset:39936
v_mfma_f32_16x16x32_bf16 v[24:27],a[192:195],v[36:39],v[24:27]
buffer_load_dwordx4 v2,s[32:35],s63 offen offset:3072 sc1 lds
s_add_u32 s63,s63,4096
v_mfma_f32_16x16x32_bf16 v[28:31],a[196:199],v[36:39],v[28:31]
ds_read_b128 v[60:63],v4 offset:54272
s_add_u32 m0,s64,12288
v_mfma_f32_16x16x32_bf16 v[28:31],a[84:87],v[32:35],v[28:31]
s_waitcnt lgkmcnt(4)
v_mfma_f32_16x16x32_bf16 v[28:31],v[116:119],v[40:43],v[28:31]
ds_read_b128 v[32:35],v4 offset:12288
v_mfma_f32_16x16x32_bf16 v[28:31],v[228:231],v[44:47],v[28:31]
buffer_load_dwordx4 v2,s[32:35],s63 offen offset:0 sc1 lds
v_mfma_f32_16x16x32_bf16 v[24:27],v[224:227],v[44:47],v[24:27]
ds_read_b128 v[36:39],v4 offset:26624
v_mfma_f32_16x16x32_bf16 v[24:27],v[112:115],v[40:43],v[24:27]
s_waitcnt lgkmcnt(4)
v_mfma_f32_16x16x32_bf16 v[24:27],a[88:91],v[48:51],v[24:27]
ds_read_b128 v[40:43],v4 offset:40960
v_mfma_f32_16x16x32_bf16 v[24:27],a[200:203],v[52:55],v[24:27]
buffer_load_dwordx4 v2,s[32:35],s63 offen offset:1024 sc1 lds
s_add_u32 s63,s63,s31
v_mfma_f32_16x16x32_bf16 v[28:31],a[204:207],v[52:55],v[28:31]
ds_read_b128 v[44:47],v4 offset:55296
v_mfma_f32_16x16x32_bf16 v[28:31],a[92:95],v[48:51],v[28:31]
s_waitcnt lgkmcnt(4)
v_mfma_f32_16x16x32_bf16 v[28:31],v[124:127],v[56:59],v[28:31]
ds_read_b128 v[48:51],v4 offset:13312
s_waitcnt vmcnt(11)
v_mfma_f32_16x16x32_bf16 v[28:31],v[236:239],v[60:63],v[28:31]
v_lshlrev_b32 v16,16,v12
v_and_b32 v17,0xffff0000,v12
v_mfma_f32_16x16x32_bf16 v[24:27],v[232:235],v[60:63],v[24:27]
ds_read_b128 v[52:55],v4 offset:27648
v_lshlrev_b32 v18,16,v13
v_mfma_f32_16x16x32_bf16 v[24:27],v[120:123],v[56:59],v[24:27]
v_and_b32 v19,0xffff0000,v13
s_waitcnt lgkmcnt(4)
v_mfma_f32_16x16x32_bf16 v[24:27],a[96:99],v[32:35],v[24:27]
ds_read_b128 v[56:59],v4 offset:41984
v_lshlrev_b32 v20,16,v14
v_mfma_f32_16x16x32_bf16 v[24:27],a[208:211],v[36:39],v[24:27]
v_and_b32 v21,0xffff0000,v14
v_lshlrev_b32 v22,16,v15
v_mfma_f32_16x16x32_bf16 v[28:31],a[212:215],v[36:39],v[28:31]
s_barrier
v_mfma_f32_16x16x32_bf16 v[28:31],a[100:103],v[32:35],v[28:31]
ds_read_b128 v[60:63],v4 offset:56320
s_waitcnt lgkmcnt(4)
v_mfma_f32_16x16x32_bf16 v[28:31],v[132:135],v[40:43],v[28:31]
ds_read_b128 v[32:35],v2 offset:0
v_and_b32 v23,0xffff0000,v15
v_mfma_f32_16x16x32_bf16 v[28:31],v[244:247],v[44:47],v[28:31]
v_mfma_f32_16x16x32_bf16 v[24:27],v[240:243],v[44:47],v[24:27]
ds_read_b128 v[36:39],v2 offset:14336
v_mfma_f32_16x16x32_bf16 v[24:27],v[128:131],v[40:43],v[24:27]
s_waitcnt lgkmcnt(4)
v_mfma_f32_16x16x32_bf16 v[24:27],a[104:107],v[48:51],v[24:27]
ds_read_b128 v[40:43],v2 offset:28672
v_mfma_f32_16x16x32_bf16 v[24:27],a[216:219],v[52:55],v[24:27]
v_mfma_f32_16x16x32_bf16 v[28:31],a[220:223],v[52:55],v[28:31]
ds_read_b128 v[44:47],v2 offset:43008
v_mfma_f32_16x16x32_bf16 v[28:31],a[108:111],v[48:51],v[28:31]
s_waitcnt lgkmcnt(4)
v_mfma_f32_16x16x32_bf16 v[28:31],v[140:143],v[56:59],v[28:31]
ds_read_b128 v[48:51],v2 offset:1024
v_mfma_f32_16x16x32_bf16 v[28:31],v[252:255],v[60:63],v[28:31]
v_mfma_f32_16x16x32_bf16 v[24:27],v[248:251],v[60:63],v[24:27]
ds_read_b128 v[52:55],v2 offset:15360
s_sub_u32 s62,s62,1
v_mfma_f32_16x16x32_bf16 v[24:27],v[136:139],v[56:59],v[24:27]
s_cbranch_scc1 .L_dmt_1
s_waitcnt lgkmcnt(4)
v_mfma_f32_16x16x32_bf16 v[16:19],a[0:3],v[32:35],v[16:19]
ds_read_b128 v[56:59],v2 offset:29696
v_mfma_f32_16x16x32_bf16 v[16:19],a[112:115],v[36:39],v[16:19]
v_mfma_f32_16x16x32_bf16 v[20:23],a[116:119],v[36:39],v[20:23]
ds_read_b128 v[60:63],v2 offset:44032
v_mfma_f32_16x16x32_bf16 v[20:23],a[4:7],v[32:35],v[20:23]
v_cvt_pk_bf16_f32 v15,v30,v31
s_waitcnt lgkmcnt(4)
v_mfma_f32_16x16x32_bf16 v[20:23],a[228:231],v[40:43],v[20:23]
ds_read_b128 v[32:35],v2 offset:2048
v_cvt_pk_bf16_f32 v14,v28,v29
v_mfma_f32_16x16x32_bf16 v[20:23],v[148:151],v[44:47],v[20:23]
v_cvt_pk_bf16_f32 v13,v26,v27
v_cvt_pk_bf16_f32 v12,v24,v25
v_mfma_f32_16x16x32_bf16 v[16:19],v[144:147],v[44:47],v[16:19]
ds_read_b128 v[36:39],v2 offset:16384
ds_write_b128 v5,v[12:15]
v_mfma_f32_16x16x32_bf16 v[16:19],a[224:227],v[40:43],v[16:19]
ds_read2st64_b32 v[12:13],v3 offset0:0 offset1:1
s_waitcnt lgkmcnt(6)
v_mfma_f32_16x16x32_bf16 v[16:19],a[8:11],v[48:51],v[16:19]
ds_read_b128 v[40:43],v2 offset:30720
ds_read2st64_b32 v[14:15],v3 offset0:2 offset1:3
v_mfma_f32_16x16x32_bf16 v[16:19],a[120:123],v[52:55],v[16:19]
v_mfma_f32_16x16x32_bf16 v[20:23],a[124:127],v[52:55],v[20:23]
ds_read_b128 v[44:47],v2 offset:45056
v_mfma_f32_16x16x32_bf16 v[20:23],a[12:15],v[48:51],v[20:23]
s_waitcnt lgkmcnt(7)
v_mfma_f32_16x16x32_bf16 v[20:23],a[236:239],v[56:59],v[20:23]
ds_read_b128 v[48:51],v2 offset:3072
v_mfma_f32_16x16x32_bf16 v[20:23],v[156:159],v[60:63],v[20:23]
s_waitcnt lgkmcnt(2)
buffer_atomic_pk_add_bf16 v12,v1,s[44:47],s65 offen offset:0 sc1
v_mfma_f32_16x16x32_bf16 v[16:19],v[152:155],v[60:63],v[16:19]
ds_read_b128 v[52:55],v2 offset:17408
s_waitcnt vmcnt(4)
v_mfma_f32_16x16x32_bf16 v[16:19],a[232:235],v[56:59],v[16:19]
v_cmp_ne_u64 vcc,v[8:9],v[6:7]
v_mfma_f32_16x16x32_bf16 v[16:19],a[16:19],v[32:35],v[16:19]
ds_read_b128 v[56:59],v2 offset:31744
s_cbranch_vccnz .L_rtm0_1
.L_bkm0_1:
v_mfma_f32_16x16x32_bf16 v[16:19],a[128:131],v[36:39],v[16:19]
s_add_u32 s66,s66,s30
s_add_u32 s70,s70,s68
v_mfma_f32_16x16x32_bf16 v[20:23],a[132:135],v[36:39],v[20:23]
s_waitcnt vmcnt(1)
s_barrier
v_mfma_f32_16x16x32_bf16 v[20:23],a[20:23],v[32:35],v[20:23]
buffer_atomic_pk_add_bf16 v13,v1,s[44:47],s65 offen offset:2048 sc1
s_waitcnt lgkmcnt(3)
v_mfma_f32_16x16x32_bf16 v[20:23],a[244:247],v[40:43],v[20:23]
ds_read_b128 v[60:63],v2 offset:46080
ds_read_b128 v[32:35],v2 offset:4096
v_mfma_f32_16x16x32_bf16 v[20:23],v[164:167],v[44:47],v[20:23]
s_add_u32 m0,s64,57344
s_add_u32 s65,s65,16384
v_mfma_f32_16x16x32_bf16 v[16:19],v[160:163],v[44:47],v[16:19]
ds_read_b128 v[36:39],v2 offset:18432
v_mfma_f32_16x16x32_bf16 v[16:19],a[240:243],v[40:43],v[16:19]
buffer_atomic_pk_add_bf16 v14,v1,s[44:47],s65 offen offset:0 sc1
s_waitcnt lgkmcnt(4)
v_mfma_f32_16x16x32_bf16 v[16:19],a[24:27],v[48:51],v[16:19]
ds_read_b128 v[40:43],v2 offset:32768
s_mov_b32 s68,s75
v_mfma_f32_16x16x32_bf16 v[16:19],a[136:139],v[52:55],v[16:19]
buffer_atomic_pk_add_bf16 v15,v1,s[44:47],s65 offen offset:2048 sc1
s_and_b32 s65,s70,0x3f8000
v_mfma_f32_16x16x32_bf16 v[20:23],a[140:143],v[52:55],v[20:23]
ds_read_b128 v[44:47],v2 offset:47104
v_mfma_f32_16x16x32_bf16 v[20:23],a[28:31],v[48:51],v[20:23]
s_waitcnt lgkmcnt(4)
v_mfma_f32_16x16x32_bf16 v[20:23],a[252:255],v[56:59],v[20:23]
ds_read_b128 v[48:51],v2 offset:5120
v_mfma_f32_16x16x32_bf16 v[20:23],v[172:175],v[60:63],v[20:23]
buffer_load_dwordx4 v2,s[32:35],s63 offen offset:0 sc1 lds
v_mfma_f32_16x16x32_bf16 v[16:19],v[168:171],v[60:63],v[16:19]
ds_read_b128 v[52:55],v2 offset:19456
v_mfma_f32_16x16x32_bf16 v[16:19],a[248:251],v[56:59],v[16:19]
s_waitcnt lgkmcnt(4)
v_mfma_f32_16x16x32_bf16 v[16:19],a[32:35],v[32:35],v[16:19]
ds_read_b128 v[56:59],v2 offset:33792
v_mfma_f32_16x16x32_bf16 v[16:19],a[144:147],v[36:39],v[16:19]
buffer_load_dwordx4 v2,s[32:35],s63 offen offset:1024 sc1 lds
v_mfma_f32_16x16x32_bf16 v[20:23],a[148:151],v[36:39],v[20:23]
ds_read_b128 v[60:63],v2 offset:48128
v_mfma_f32_16x16x32_bf16 v[20:23],a[36:39],v[32:35],v[20:23]
s_waitcnt lgkmcnt(4)
v_mfma_f32_16x16x32_bf16 v[20:23],v[68:71],v[40:43],v[20:23]
ds_read_b128 v[32:35],v2 offset:6144
v_mfma_f32_16x16x32_bf16 v[20:23],v[180:183],v[44:47],v[20:23]
buffer_load_dwordx4 v2,s[32:35],s63 offen offset:2048 sc1 lds
v_mfma_f32_16x16x32_bf16 v[16:19],v[176:179],v[44:47],v[16:19]
ds_read_b128 v[36:39],v2 offset:20480
v_mfma_f32_16x16x32_bf16 v[16:19],v[64:67],v[40:43],v[16:19]
s_waitcnt lgkmcnt(4)
v_mfma_f32_16x16x32_bf16 v[16:19],a[40:43],v[48:51],v[16:19]
ds_read_b128 v[40:43],v2 offset:34816
v_mfma_f32_16x16x32_bf16 v[16:19],a[152:155],v[52:55],v[16:19]
buffer_load_dwordx4 v2,s[32:35],s63 offen offset:3072 sc1 lds
v_mfma_f32_16x16x32_bf16 v[20:23],a[156:159],v[52:55],v[20:23]
ds_read_b128 v[44:47],v2 offset:49152
v_mfma_f32_16x16x32_bf16 v[20:23],a[44:47],v[48:51],v[20:23]
s_waitcnt lgkmcnt(4)
v_mfma_f32_16x16x32_bf16 v[20:23],v[76:79],v[56:59],v[20:23]
ds_read_b128 v[48:51],v2 offset:7168
v_mfma_f32_16x16x32_bf16 v[20:23],v[188:191],v[60:63],v[20:23]
buffer_load_dwordx4 v[12:15],v2,s[32:35],s71 offen sc1
s_add_u32 s71,s71,s30
v_mfma_f32_16x16x32_bf16 v[16:19],v[184:187],v[60:63],v[16:19]
ds_read_b128 v[52:55],v2 offset:21504
s_add_u32 s63,s63,4096
v_mfma_f32_16x16x32_bf16 v[16:19],v[72:75],v[56:59],v[16:19]
s_add_u32 m0,s64,61440
s_waitcnt lgkmcnt(4)
v_mfma_f32_16x16x32_bf16 v[16:19],a[48:51],v[32:35],v[16:19]
ds_read_b128 v[56:59],v2 offset:35840
v_mfma_f32_16x16x32_bf16 v[16:19],a[160:163],v[36:39],v[16:19]
buffer_load_dwordx4 v2,s[32:35],s63 offen offset:0 sc1 lds
v_mfma_f32_16x16x32_bf16 v[20:23],a[164:167],v[36:39],v[20:23]
ds_read_b128 v[60:63],v2 offset:50176
v_mfma_f32_16x16x32_bf16 v[20:23],a[52:55],v[32:35],v[20:23]
s_waitcnt lgkmcnt(4)
v_mfma_f32_16x16x32_bf16 v[20:23],v[84:87],v[40:43],v[20:23]
ds_read_b128 v[32:35],v2 offset:8192
v_mfma_f32_16x16x32_bf16 v[20:23],v[196:199],v[44:47],v[20:23]
buffer_load_dwordx4 v2,s[32:35],s63 offen offset:1024 sc1 lds
v_mfma_f32_16x16x32_bf16 v[16:19],v[192:195],v[44:47],v[16:19]
ds_read_b128 v[36:39],v2 offset:22528
v_mfma_f32_16x16x32_bf16 v[16:19],v[80:83],v[40:43],v[16:19]
s_waitcnt lgkmcnt(4)
v_mfma_f32_16x16x32_bf16 v[16:19],a[56:59],v[48:51],v[16:19]
ds_read_b128 v[40:43],v2 offset:36864
v_mfma_f32_16x16x32_bf16 v[16:19],a[168:171],v[52:55],v[16:19]
buffer_load_dwordx4 v2,s[32:35],s63 offen offset:2048 sc1 lds
v_mfma_f32_16x16x32_bf16 v[20:23],a[172:175],v[52:55],v[20:23]
ds_read_b128 v[44:47],v2 offset:51200
v_mfma_f32_16x16x32_bf16 v[20:23],a[60:63],v[48:51],v[20:23]
s_waitcnt lgkmcnt(4)
v_mfma_f32_16x16x32_bf16 v[20:23],v[92:95],v[56:59],v[20:23]
ds_read_b128 v[48:51],v2 offset:9216
v_mfma_f32_16x16x32_bf16 v[20:23],v[204:207],v[60:63],v[20:23]
buffer_load_dwordx4 v2,s[32:35],s63 offen offset:3072 sc1 lds
s_add_u32 s63,s63,4096
v_mfma_f32_16x16x32_bf16 v[16:19],v[200:203],v[60:63],v[16:19]
ds_read_b128 v[52:55],v2 offset:23552
s_add_u32 m0,s64,65536
v_mfma_f32_16x16x32_bf16 v[16:19],v[88:91],v[56:59],v[16:19]
s_waitcnt lgkmcnt(4)
v_mfma_f32_16x16x32_bf16 v[16:19],a[64:67],v[32:35],v[16:19]
ds_read_b128 v[56:59],v2 offset:37888
v_mfma_f32_16x16x32_bf16 v[16:19],a[176:179],v[36:39],v[16:19]
buffer_load_dwordx4 v2,s[32:35],s63 offen offset:0 sc1 lds
v_mfma_f32_16x16x32_bf16 v[20:23],a[180:183],v[36:39],v[20:23]
ds_read_b128 v[60:63],v2 offset:52224
v_mfma_f32_16x16x32_bf16 v[20:23],a[68:71],v[32:35],v[20:23]
s_waitcnt lgkmcnt(4)
v_mfma_f32_16x16x32_bf16 v[20:23],v[100:103],v[40:43],v[20:23]
ds_read_b128 v[32:35],v2 offset:10240
v_mfma_f32_16x16x32_bf16 v[20:23],v[212:215],v[44:47],v[20:23]
buffer_load_dwordx4 v2,s[32:35],s63 offen offset:1024 sc1 lds
v_mfma_f32_16x16x32_bf16 v[16:19],v[208:211],v[44:47],v[16:19]
ds_read_b128 v[36:39],v2 offset:24576
v_mfma_f32_16x16x32_bf16 v[16:19],v[96:99],v[40:43],v[16:19]
s_waitcnt lgkmcnt(4)
v_mfma_f32_16x16x32_bf16 v[16:19],a[72:75],v[48:51],v[16:19]
ds_read_b128 v[40:43],v2 offset:38912
v_mfma_f32_16x16x32_bf16 v[16:19],a[184:187],v[52:55],v[16:19]
buffer_load_dwordx4 v2,s[32:35],s63 offen offset:2048 sc1 lds
v_mfma_f32_16x16x32_bf16 v[20:23],a[188:191],v[52:55],v[20:23]
ds_read_b128 v[44:47],v2 offset:53248
v_mfma_f32_16x16x32_bf16 v[20:23],a[76:79],v[48:51],v[20:23]
s_waitcnt lgkmcnt(4)
v_mfma_f32_16x16x32_bf16 v[20:23],v[108:111],v[56:59],v[20:23]
ds_read_b128 v[48:51],v2 offset:11264
v_mfma_f32_16x16x32_bf16 v[20:23],v[220:223],v[60:63],v[20:23]
buffer_load_dwordx2 v[6:7],v0,s[0:3],s66 offen sc1
v_mfma_f32_16x16x32_bf16 v[16:19],v[216:219],v[60:63],v[16:19]
ds_read_b128 v[52:55],v2 offset:25600
v_mfma_f32_16x16x32_bf16 v[16:19],v[104:107],v[56:59],v[16:19]
s_waitcnt lgkmcnt(4)
v_mfma_f32_16x16x32_bf16 v[16:19],a[80:83],v[32:35],v[16:19]
ds_read_b128 v[56:59],v2 offset:39936
v_mfma_f32_16x16x32_bf16 v[16:19],a[192:195],v[36:39],v[16:19]
buffer_load_dwordx4 v2,s[32:35],s63 offen offset:3072 sc1 lds
s_add_u32 s63,s63,4096
v_mfma_f32_16x16x32_bf16 v[20:23],a[196:199],v[36:39],v[20:23]
ds_read_b128 v[60:63],v2 offset:54272
s_add_u32 m0,s64,69632
v_mfma_f32_16x16x32_bf16 v[20:23],a[84:87],v[32:35],v[20:23]
s_waitcnt lgkmcnt(4)
v_mfma_f32_16x16x32_bf16 v[20:23],v[116:119],v[40:43],v[20:23]
ds_read_b128 v[32:35],v2 offset:12288
v_mfma_f32_16x16x32_bf16 v[20:23],v[228:231],v[44:47],v[20:23]
buffer_load_dwordx4 v2,s[32:35],s63 offen offset:0 sc1 lds
v_mfma_f32_16x16x32_bf16 v[16:19],v[224:227],v[44:47],v[16:19]
ds_read_b128 v[36:39],v2 offset:26624
v_mfma_f32_16x16x32_bf16 v[16:19],v[112:115],v[40:43],v[16:19]
s_waitcnt lgkmcnt(4)
v_mfma_f32_16x16x32_bf16 v[16:19],a[88:91],v[48:51],v[16:19]
ds_read_b128 v[40:43],v2 offset:40960
v_mfma_f32_16x16x32_bf16 v[16:19],a[200:203],v[52:55],v[16:19]
buffer_load_dwordx4 v2,s[32:35],s63 offen offset:1024 sc1 lds
s_add_u32 s63,s63,s31
v_mfma_f32_16x16x32_bf16 v[20:23],a[204:207],v[52:55],v[20:23]
ds_read_b128 v[44:47],v2 offset:55296
v_mfma_f32_16x16x32_bf16 v[20:23],a[92:95],v[48:51],v[20:23]
s_waitcnt lgkmcnt(4)
v_mfma_f32_16x16x32_bf16 v[20:23],v[124:127],v[56:59],v[20:23]
ds_read_b128 v[48:51],v2 offset:13312
s_waitcnt vmcnt(11)
v_mfma_f32_16x16x32_bf16 v[20:23],v[236:239],v[60:63],v[20:23]
v_lshlrev_b32 v24,16,v12
v_and_b32 v25,0xffff0000,v12
v_mfma_f32_16x16x32_bf16 v[16:19],v[232:235],v[60:63],v[16:19]
ds_read_b128 v[52:55],v2 offset:27648
v_lshlrev_b32 v26,16,v13
v_mfma_f32_16x16x32_bf16 v[16:19],v[120:123],v[56:59],v[16:19]
v_and_b32 v27,0xffff0000,v13
s_waitcnt lgkmcnt(4)
v_mfma_f32_16x16x32_bf16 v[16:19],a[96:99],v[32:35],v[16:19]
ds_read_b128 v[56:59],v2 offset:41984
v_lshlrev_b32 v28,16,v14
v_mfma_f32_16x16x32_bf16 v[16:19],a[208:211],v[36:39],v[16:19]
v_and_b32 v29,0xffff0000,v14
v_lshlrev_b32 v30,16,v15
v_mfma_f32_16x16x32_bf16 v[20:23],a[212:215],v[36:39],v[20:23]
s_barrier
v_mfma_f32_16x16x32_bf16 v[20:23],a[100:103],v[32:35],v[20:23]
ds_read_b128 v[60:63],v2 offset:56320
s_waitcnt lgkmcnt(4)
v_mfma_f32_16x16x32_bf16 v[20:23],v[132:135],v[40:43],v[20:23]
ds_read_b128 v[32:35],v4 offset:0
v_and_b32 v31,0xffff0000,v15
v_mfma_f32_16x16x32_bf16 v[20:23],v[244:247],v[44:47],v[20:23]
v_mfma_f32_16x16x32_bf16 v[16:19],v[240:243],v[44:47],v[16:19]
ds_read_b128 v[36:39],v4 offset:14336
v_mfma_f32_16x16x32_bf16 v[16:19],v[128:131],v[40:43],v[16:19]
s_waitcnt lgkmcnt(4)
v_mfma_f32_16x16x32_bf16 v[16:19],a[104:107],v[48:51],v[16:19]
ds_read_b128 v[40:43],v4 offset:28672
v_mfma_f32_16x16x32_bf16 v[16:19],a[216:219],v[52:55],v[16:19]
v_mfma_f32_16x16x32_bf16 v[20:23],a[220:223],v[52:55],v[20:23]
ds_read_b128 v[44:47],v4 offset:43008
v_mfma_f32_16x16x32_bf16 v[20:23],a[108:111],v[48:51],v[20:23]
s_waitcnt lgkmcnt(4)
v_mfma_f32_16x16x32_bf16 v[20:23],v[140:143],v[56:59],v[20:23]
ds_read_b128 v[48:51],v4 offset:1024
v_mfma_f32_16x16x32_bf16 v[20:23],v[252:255],v[60:63],v[20:23]
v_mfma_f32_16x16x32_bf16 v[16:19],v[248:251],v[60:63],v[16:19]
ds_read_b128 v[52:55],v4 offset:15360
s_sub_u32 s62,s62,1
v_mfma_f32_16x16x32_bf16 v[16:19],v[136:139],v[56:59],v[16:19]
s_cbranch_scc0 .L_dml_1
.L_dmu_1:
s_nop 1
s_nop 0
v_cvt_pk_bf16_f32 v15,v22,v23
v_cvt_pk_bf16_f32 v14,v20,v21
s_nop 1
v_cvt_pk_bf16_f32 v13,v18,v19
)#"
R"#(
v_cvt_pk_bf16_f32 v12,v16,v17
ds_write_b128 v5,v[12:15]
ds_read2st64_b32 v[12:13],v3 offset0:0 offset1:1
ds_read2st64_b32 v[14:15],v3 offset0:2 offset1:3
s_waitcnt lgkmcnt(0)
buffer_atomic_pk_add_bf16 v12,v1,s[44:47],s65 offen offset:0 sc1
s_add_u32 s70,s70,s75
buffer_atomic_pk_add_bf16 v13,v1,s[44:47],s65 offen offset:2048 sc1
s_add_u32 s65,s65,16384
buffer_atomic_pk_add_bf16 v14,v1,s[44:47],s65 offen offset:0 sc1
buffer_atomic_pk_add_bf16 v15,v1,s[44:47],s65 offen offset:2048 sc1
s_and_b32 s65,s70,0x3f8000
s_branch .L_end
.L_dmt_1:
s_nop 1
s_nop 0
v_cvt_pk_bf16_f32 v15,v30,v31
v_cvt_pk_bf16_f32 v14,v28,v29
s_nop 1
v_cvt_pk_bf16_f32 v13,v26,v27
v_cvt_pk_bf16_f32 v12,v24,v25
ds_write_b128 v5,v[12:15]
ds_read2st64_b32 v[12:13],v3 offset0:0 offset1:1
ds_read2st64_b32 v[14:15],v3 offset0:2 offset1:3
s_waitcnt lgkmcnt(0)
buffer_atomic_pk_add_bf16 v12,v1,s[44:47],s65 offen offset:0 sc1
s_add_u32 s70,s70,s68
buffer_atomic_pk_add_bf16 v13,v1,s[44:47],s65 offen offset:2048 sc1
s_add_u32 s65,s65,16384
buffer_atomic_pk_add_bf16 v14,v1,s[44:47],s65 offen offset:0 sc1
s_mov_b32 s68,s75
buffer_atomic_pk_add_bf16 v15,v1,s[44:47],s65 offen offset:2048 sc1
s_and_b32 s65,s70,0x3f8000
s_branch .L_end
.p2align 6
.L_dlv2:
.L_dll_2:
s_waitcnt lgkmcnt(4)
v_mfma_f32_16x16x32_bf16 v[16:19],a[0:3],v[32:35],v[16:19]
ds_read_b128 v[56:59],v2 offset:29696
v_mfma_f32_16x16x32_bf16 v[16:19],a[112:115],v[36:39],v[16:19]
v_mfma_f32_16x16x32_bf16 v[20:23],a[116:119],v[36:39],v[20:23]
ds_read_b128 v[60:63],v2 offset:44032
v_mfma_f32_16x16x32_bf16 v[20:23],a[4:7],v[32:35],v[20:23]
s_add_u32 s68,s68,1187840
s_waitcnt lgkmcnt(4)
v_mfma_f32_16x16x32_bf16 v[20:23],a[228:231],v[40:43],v[20:23]
ds_read_b128 v[32:35],v2 offset:2048
v_mfma_f32_16x16x32_bf16 v[20:23],v[148:151],v[44:47],v[20:23]
buffer_store_dwordx4 v[28:31],v2,s[32:35],s68 offen offset:1024 sc1
v_mfma_f32_16x16x32_bf16 v[16:19],v[144:147],v[44:47],v[16:19]
ds_read_b128 v[36:39],v2 offset:16384
v_mfma_f32_16x16x32_bf16 v[16:19],a[224:227],v[40:43],v[16:19]
s_waitcnt lgkmcnt(4)
v_mfma_f32_16x16x32_bf16 v[16:19],a[8:11],v[48:51],v[16:19]
ds_read_b128 v[40:43],v2 offset:30720
v_mfma_f32_16x16x32_bf16 v[16:19],a[120:123],v[52:55],v[16:19]
buffer_store_dwordx4 v[24:27],v2,s[32:35],s68 offen sc1
s_mov_b32 s67,s68
v_mfma_f32_16x16x32_bf16 v[20:23],a[124:127],v[52:55],v[20:23]
ds_read_b128 v[44:47],v2 offset:45056
v_mfma_f32_16x16x32_bf16 v[20:23],a[12:15],v[48:51],v[20:23]
s_waitcnt lgkmcnt(4)
v_mfma_f32_16x16x32_bf16 v[20:23],a[236:239],v[56:59],v[20:23]
ds_read_b128 v[48:51],v2 offset:3072
v_mfma_f32_16x16x32_bf16 v[20:23],v[156:159],v[60:63],v[20:23]
v_mfma_f32_16x16x32_bf16 v[16:19],v[152:155],v[60:63],v[16:19]
ds_read_b128 v[52:55],v2 offset:17408
v_mfma_f32_16x16x32_bf16 v[16:19],a[232:235],v[56:59],v[16:19]
s_waitcnt lgkmcnt(4)
v_mfma_f32_16x16x32_bf16 v[16:19],a[16:19],v[32:35],v[16:19]
ds_read_b128 v[56:59],v2 offset:31744
v_mfma_f32_16x16x32_bf16 v[16:19],a[128:131],v[36:39],v[16:19]
s_waitcnt vmcnt(2)
s_barrier
v_mfma_f32_16x16x32_bf16 v[20:23],a[132:135],v[36:39],v[20:23]
ds_read_b128 v[60:63],v2 offset:46080
v_mfma_f32_16x16x32_bf16 v[20:23],a[20:23],v[32:35],v[20:23]
s_waitcnt lgkmcnt(4)
v_mfma_f32_16x16x32_bf16 v[20:23],a[244:247],v[40:43],v[20:23]
ds_read_b128 v[32:35],v2 offset:4096
s_add_u32 m0,s64,57344
v_mfma_f32_16x16x32_bf16 v[20:23],v[164:167],v[44:47],v[20:23]
buffer_load_dwordx4 v2,s[32:35],s63 offen offset:0 sc1 lds
v_mfma_f32_16x16x32_bf16 v[16:19],v[160:163],v[44:47],v[16:19]
ds_read_b128 v[36:39],v2 offset:18432
v_mfma_f32_16x16x32_bf16 v[16:19],a[240:243],v[40:43],v[16:19]
s_waitcnt lgkmcnt(4)
v_mfma_f32_16x16x32_bf16 v[16:19],a[24:27],v[48:51],v[16:19]
ds_read_b128 v[40:43],v2 offset:32768
v_mfma_f32_16x16x32_bf16 v[16:19],a[136:139],v[52:55],v[16:19]
v_mfma_f32_16x16x32_bf16 v[20:23],a[140:143],v[52:55],v[20:23]
ds_read_b128 v[44:47],v2 offset:47104
v_mfma_f32_16x16x32_bf16 v[20:23],a[28:31],v[48:51],v[20:23]
s_waitcnt lgkmcnt(4)
v_mfma_f32_16x16x32_bf16 v[20:23],a[252:255],v[56:59],v[20:23]
ds_read_b128 v[48:51],v2 offset:5120
v_mfma_f32_16x16x32_bf16 v[20:23],v[172:175],v[60:63],v[20:23]
buffer_load_dwordx4 v2,s[32:35],s63 offen offset:1024 sc1 lds
v_mfma_f32_16x16x32_bf16 v[16:19],v[168:171],v[60:63],v[16:19]
ds_read_b128 v[52:55],v2 offset:19456
v_mfma_f32_16x16x32_bf16 v[16:19],a[248:251],v[56:59],v[16:19]
s_waitcnt lgkmcnt(4)
v_mfma_f32_16x16x32_bf16 v[16:19],a[32:35],v[32:35],v[16:19]
ds_read_b128 v[56:59],v2 offset:33792
v_mfma_f32_16x16x32_bf16 v[16:19],a[144:147],v[36:39],v[16:19]
buffer_load_dwordx4 v2,s[32:35],s63 offen offset:2048 sc1 lds
v_mfma_f32_16x16x32_bf16 v[20:23],a[148:151],v[36:39],v[20:23]
ds_read_b128 v[60:63],v2 offset:48128
v_mfma_f32_16x16x32_bf16 v[20:23],a[36:39],v[32:35],v[20:23]
s_waitcnt lgkmcnt(4)
v_mfma_f32_16x16x32_bf16 v[20:23],v[68:71],v[40:43],v[20:23]
ds_read_b128 v[32:35],v2 offset:6144
v_mfma_f32_16x16x32_bf16 v[20:23],v[180:183],v[44:47],v[20:23]
buffer_load_dwordx4 v2,s[32:35],s63 offen offset:3072 sc1 lds
v_mfma_f32_16x16x32_bf16 v[16:19],v[176:179],v[44:47],v[16:19]
ds_read_b128 v[36:39],v2 offset:20480
v_mfma_f32_16x16x32_bf16 v[16:19],v[64:67],v[40:43],v[16:19]
s_waitcnt lgkmcnt(4)
v_mfma_f32_16x16x32_bf16 v[16:19],a[40:43],v[48:51],v[16:19]
ds_read_b128 v[40:43],v2 offset:34816
v_mfma_f32_16x16x32_bf16 v[16:19],a[152:155],v[52:55],v[16:19]
buffer_load_dwordx4 v[12:15],v2,s[32:35],s71 offen sc1
s_add_u32 s71,s71,s30
v_mfma_f32_16x16x32_bf16 v[20:23],a[156:159],v[52:55],v[20:23]
ds_read_b128 v[44:47],v2 offset:49152
s_add_u32 s63,s63,4096
v_mfma_f32_16x16x32_bf16 v[20:23],a[44:47],v[48:51],v[20:23]
s_add_u32 m0,s64,61440
s_waitcnt lgkmcnt(4)
v_mfma_f32_16x16x32_bf16 v[20:23],v[76:79],v[56:59],v[20:23]
ds_read_b128 v[48:51],v2 offset:7168
v_mfma_f32_16x16x32_bf16 v[20:23],v[188:191],v[60:63],v[20:23]
buffer_load_dwordx4 v2,s[32:35],s63 offen offset:0 sc1 lds
v_mfma_f32_16x16x32_bf16 v[16:19],v[184:187],v[60:63],v[16:19]
ds_read_b128 v[52:55],v2 offset:21504
v_mfma_f32_16x16x32_bf16 v[16:19],v[72:75],v[56:59],v[16:19]
s_waitcnt lgkmcnt(4)
v_mfma_f32_16x16x32_bf16 v[16:19],a[48:51],v[32:35],v[16:19]
ds_read_b128 v[56:59],v2 offset:35840
v_mfma_f32_16x16x32_bf16 v[16:19],a[160:163],v[36:39],v[16:19]
buffer_load_dwordx4 v2,s[32:35],s63 offen offset:1024 sc1 lds
v_mfma_f32_16x16x32_bf16 v[20:23],a[164:167],v[36:39],v[20:23]
ds_read_b128 v[60:63],v2 offset:50176
v_mfma_f32_16x16x32_bf16 v[20:23],a[52:55],v[32:35],v[20:23]
s_waitcnt lgkmcnt(4)
v_mfma_f32_16x16x32_bf16 v[20:23],v[84:87],v[40:43],v[20:23]
ds_read_b128 v[32:35],v2 offset:8192
v_mfma_f32_16x16x32_bf16 v[20:23],v[196:199],v[44:47],v[20:23]
buffer_load_dwordx4 v2,s[32:35],s63 offen offset:2048 sc1 lds
v_mfma_f32_16x16x32_bf16 v[16:19],v[192:195],v[44:47],v[16:19]
ds_read_b128 v[36:39],v2 offset:22528
v_mfma_f32_16x16x32_bf16 v[16:19],v[80:83],v[40:43],v[16:19]
s_waitcnt lgkmcnt(4)
v_mfma_f32_16x16x32_bf16 v[16:19],a[56:59],v[48:51],v[16:19]
ds_read_b128 v[40:43],v2 offset:36864
v_mfma_f32_16x16x32_bf16 v[16:19],a[168:171],v[52:55],v[16:19]
buffer_load_dwordx4 v2,s[32:35],s63 offen offset:3072 sc1 lds
v_mfma_f32_16x16x32_bf16 v[20:23],a[172:175],v[52:55],v[20:23]
ds_read_b128 v[44:47],v2 offset:51200
v_mfma_f32_16x16x32_bf16 v[20:23],a[60:63],v[48:51],v[20:23]
s_waitcnt lgkmcnt(4)
v_mfma_f32_16x16x32_bf16 v[20:23],v[92:95],v[56:59],v[20:23]
ds_read_b128 v[48:51],v2 offset:9216
v_mfma_f32_16x16x32_bf16 v[20:23],v[204:207],v[60:63],v[20:23]
s_add_u32 s63,s63,4096
s_add_u32 m0,s64,65536
v_mfma_f32_16x16x32_bf16 v[16:19],v[200:203],v[60:63],v[16:19]
ds_read_b128 v[52:55],v2 offset:23552
v_mfma_f32_16x16x32_bf16 v[16:19],v[88:91],v[56:59],v[16:19]
s_waitcnt lgkmcnt(4)
v_mfma_f32_16x16x32_bf16 v[16:19],a[64:67],v[32:35],v[16:19]
ds_read_b128 v[56:59],v2 offset:37888
v_mfma_f32_16x16x32_bf16 v[16:19],a[176:179],v[36:39],v[16:19]
buffer_load_dwordx4 v2,s[32:35],s63 offen offset:0 sc1 lds
v_mfma_f32_16x16x32_bf16 v[20:23],a[180:183],v[36:39],v[20:23]
ds_read_b128 v[60:63],v2 offset:52224
v_mfma_f32_16x16x32_bf16 v[20:23],a[68:71],v[32:35],v[20:23]
s_waitcnt lgkmcnt(4)
v_mfma_f32_16x16x32_bf16 v[20:23],v[100:103],v[40:43],v[20:23]
ds_read_b128 v[32:35],v2 offset:10240
v_mfma_f32_16x16x32_bf16 v[20:23],v[212:215],v[44:47],v[20:23]
buffer_load_dwordx4 v2,s[32:35],s63 offen offset:1024 sc1 lds
v_mfma_f32_16x16x32_bf16 v[16:19],v[208:211],v[44:47],v[16:19]
ds_read_b128 v[36:39],v2 offset:24576
v_mfma_f32_16x16x32_bf16 v[16:19],v[96:99],v[40:43],v[16:19]
s_waitcnt lgkmcnt(4)
v_mfma_f32_16x16x32_bf16 v[16:19],a[72:75],v[48:51],v[16:19]
ds_read_b128 v[40:43],v2 offset:38912
v_mfma_f32_16x16x32_bf16 v[16:19],a[184:187],v[52:55],v[16:19]
buffer_load_dwordx4 v2,s[32:35],s63 offen offset:2048 sc1 lds
v_mfma_f32_16x16x32_bf16 v[20:23],a[188:191],v[52:55],v[20:23]
ds_read_b128 v[44:47],v2 offset:53248
v_mfma_f32_16x16x32_bf16 v[20:23],a[76:79],v[48:51],v[20:23]
s_waitcnt lgkmcnt(4)
v_mfma_f32_16x16x32_bf16 v[20:23],v[108:111],v[56:59],v[20:23]
ds_read_b128 v[48:51],v2 offset:11264
v_mfma_f32_16x16x32_bf16 v[20:23],v[220:223],v[60:63],v[20:23]
v_mfma_f32_16x16x32_bf16 v[16:19],v[216:219],v[60:63],v[16:19]
ds_read_b128 v[52:55],v2 offset:25600
v_mfma_f32_16x16x32_bf16 v[16:19],v[104:107],v[56:59],v[16:19]
s_waitcnt lgkmcnt(4)
v_mfma_f32_16x16x32_bf16 v[16:19],a[80:83],v[32:35],v[16:19]
ds_read_b128 v[56:59],v2 offset:39936
v_mfma_f32_16x16x32_bf16 v[16:19],a[192:195],v[36:39],v[16:19]
buffer_load_dwordx4 v2,s[32:35],s63 offen offset:3072 sc1 lds
s_add_u32 s63,s63,4096
v_mfma_f32_16x16x32_bf16 v[20:23],a[196:199],v[36:39],v[20:23]
ds_read_b128 v[60:63],v2 offset:54272
s_add_u32 m0,s64,69632
v_mfma_f32_16x16x32_bf16 v[20:23],a[84:87],v[32:35],v[20:23]
s_waitcnt lgkmcnt(4)
v_mfma_f32_16x16x32_bf16 v[20:23],v[116:119],v[40:43],v[20:23]
ds_read_b128 v[32:35],v2 offset:12288
v_mfma_f32_16x16x32_bf16 v[20:23],v[228:231],v[44:47],v[20:23]
buffer_load_dwordx4 v2,s[32:35],s63 offen offset:0 sc1 lds
v_mfma_f32_16x16x32_bf16 v[16:19],v[224:227],v[44:47],v[16:19]
ds_read_b128 v[36:39],v2 offset:26624
v_mfma_f32_16x16x32_bf16 v[16:19],v[112:115],v[40:43],v[16:19]
s_waitcnt lgkmcnt(4)
v_mfma_f32_16x16x32_bf16 v[16:19],a[88:91],v[48:51],v[16:19]
ds_read_b128 v[40:43],v2 offset:40960
v_mfma_f32_16x16x32_bf16 v[16:19],a[200:203],v[52:55],v[16:19]
buffer_load_dwordx4 v2,s[32:35],s63 offen offset:1024 sc1 lds
s_add_u32 s63,s63,s31
v_mfma_f32_16x16x32_bf16 v[20:23],a[204:207],v[52:55],v[20:23]
ds_read_b128 v[44:47],v2 offset:55296
v_mfma_f32_16x16x32_bf16 v[20:23],a[92:95],v[48:51],v[20:23]
s_waitcnt lgkmcnt(4)
v_mfma_f32_16x16x32_bf16 v[20:23],v[124:127],v[56:59],v[20:23]
ds_read_b128 v[48:51],v2 offset:13312
s_waitcnt vmcnt(10)
v_mfma_f32_16x16x32_bf16 v[20:23],v[236:239],v[60:63],v[20:23]
v_lshlrev_b32 v24,16,v12
v_and_b32 v25,0xffff0000,v12
v_mfma_f32_16x16x32_bf16 v[16:19],v[232:235],v[60:63],v[16:19]
ds_read_b128 v[52:55],v2 offset:27648
v_lshlrev_b32 v26,16,v13
v_mfma_f32_16x16x32_bf16 v[16:19],v[120:123],v[56:59],v[16:19]
v_and_b32 v27,0xffff0000,v13
s_waitcnt lgkmcnt(4)
v_mfma_f32_16x16x32_bf16 v[16:19],a[96:99],v[32:35],v[16:19]
ds_read_b128 v[56:59],v2 offset:41984
v_lshlrev_b32 v28,16,v14
v_mfma_f32_16x16x32_bf16 v[16:19],a[208:211],v[36:39],v[16:19]
s_barrier
v_mfma_f32_16x16x32_bf16 v[20:23],a[212:215],v[36:39],v[20:23]
ds_read_b128 v[60:63],v2 offset:56320
v_and_b32 v29,0xffff0000,v14
v_mfma_f32_16x16x32_bf16 v[20:23],a[100:103],v[32:35],v[20:23]
v_lshlrev_b32 v30,16,v15
s_waitcnt lgkmcnt(4)
v_mfma_f32_16x16x32_bf16 v[20:23],v[132:135],v[40:43],v[20:23]
ds_read_b128 v[32:35],v4 offset:0
v_and_b32 v31,0xffff0000,v15
v_mfma_f32_16x16x32_bf16 v[20:23],v[244:247],v[44:47],v[20:23]
v_mfma_f32_16x16x32_bf16 v[16:19],v[240:243],v[44:47],v[16:19]
ds_read_b128 v[36:39],v4 offset:14336
v_mfma_f32_16x16x32_bf16 v[16:19],v[128:131],v[40:43],v[16:19]
s_waitcnt lgkmcnt(4)
v_mfma_f32_16x16x32_bf16 v[16:19],a[104:107],v[48:51],v[16:19]
ds_read_b128 v[40:43],v4 offset:28672
v_mfma_f32_16x16x32_bf16 v[16:19],a[216:219],v[52:55],v[16:19]
v_mfma_f32_16x16x32_bf16 v[20:23],a[220:223],v[52:55],v[20:23]
ds_read_b128 v[44:47],v4 offset:43008
v_mfma_f32_16x16x32_bf16 v[20:23],a[108:111],v[48:51],v[20:23]
s_waitcnt lgkmcnt(4)
v_mfma_f32_16x16x32_bf16 v[20:23],v[140:143],v[56:59],v[20:23]
ds_read_b128 v[48:51],v4 offset:1024
v_mfma_f32_16x16x32_bf16 v[20:23],v[252:255],v[60:63],v[20:23]
v_mfma_f32_16x16x32_bf16 v[16:19],v[248:251],v[60:63],v[16:19]
ds_read_b128 v[52:55],v4 offset:15360
s_sub_u32 s62,s62,1
v_mfma_f32_16x16x32_bf16 v[16:19],v[136:139],v[56:59],v[16:19]
s_cbranch_scc1 .L_dlb_2
s_waitcnt lgkmcnt(4)
v_mfma_f32_16x16x32_bf16 v[24:27],a[0:3],v[32:35],v[24:27]
ds_read_b128 v[56:59],v4 offset:29696
v_mfma_f32_16x16x32_bf16 v[24:27],a[112:115],v[36:39],v[24:27]
v_mfma_f32_16x16x32_bf16 v[28:31],a[116:119],v[36:39],v[28:31]
ds_read_b128 v[60:63],v4 offset:44032
v_mfma_f32_16x16x32_bf16 v[28:31],a[4:7],v[32:35],v[28:31]
s_add_u32 s68,s68,1187840
s_waitcnt lgkmcnt(4)
v_mfma_f32_16x16x32_bf16 v[28:31],a[228:231],v[40:43],v[28:31]
ds_read_b128 v[32:35],v4 offset:2048
v_mfma_f32_16x16x32_bf16 v[28:31],v[148:151],v[44:47],v[28:31]
buffer_store_dwordx4 v[20:23],v2,s[32:35],s68 offen offset:1024 sc1
v_mfma_f32_16x16x32_bf16 v[24:27],v[144:147],v[44:47],v[24:27]
ds_read_b128 v[36:39],v4 offset:16384
v_mfma_f32_16x16x32_bf16 v[24:27],a[224:227],v[40:43],v[24:27]
s_waitcnt lgkmcnt(4)
v_mfma_f32_16x16x32_bf16 v[24:27],a[8:11],v[48:51],v[24:27]
ds_read_b128 v[40:43],v4 offset:30720
v_mfma_f32_16x16x32_bf16 v[24:27],a[120:123],v[52:55],v[24:27]
buffer_store_dwordx4 v[16:19],v2,s[32:35],s68 offen sc1
s_mov_b32 s67,s68
v_mfma_f32_16x16x32_bf16 v[28:31],a[124:127],v[52:55],v[28:31]
ds_read_b128 v[44:47],v4 offset:45056
v_mfma_f32_16x16x32_bf16 v[28:31],a[12:15],v[48:51],v[28:31]
s_waitcnt lgkmcnt(4)
v_mfma_f32_16x16x32_bf16 v[28:31],a[236:239],v[56:59],v[28:31]
ds_read_b128 v[48:51],v4 offset:3072
v_mfma_f32_16x16x32_bf16 v[28:31],v[156:159],v[60:63],v[28:31]
v_mfma_f32_16x16x32_bf16 v[24:27],v[152:155],v[60:63],v[24:27]
ds_read_b128 v[52:55],v4 offset:17408
v_mfma_f32_16x16x32_bf16 v[24:27],a[232:235],v[56:59],v[24:27]
s_waitcnt lgkmcnt(4)
v_mfma_f32_16x16x32_bf16 v[24:27],a[16:19],v[32:35],v[24:27]
ds_read_b128 v[56:59],v4 offset:31744
v_mfma_f32_16x16x32_bf16 v[24:27],a[128:131],v[36:39],v[24:27]
s_waitcnt vmcnt(2)
s_barrier
v_mfma_f32_16x16x32_bf16 v[28:31],a[132:135],v[36:39],v[28:31]
ds_read_b128 v[60:63],v4 offset:46080
v_mfma_f32_16x16x32_bf16 v[28:31],a[20:23],v[32:35],v[28:31]
s_waitcnt lgkmcnt(4)
v_mfma_f32_16x16x32_bf16 v[28:31],a[244:247],v[40:43],v[28:31]
ds_read_b128 v[32:35],v4 offset:4096
s_add_u32 m0,s64,0
v_mfma_f32_16x16x32_bf16 v[28:31],v[164:167],v[44:47],v[28:31]
buffer_load_dwordx4 v2,s[32:35],s63 offen offset:0 sc1 lds
v_mfma_f32_16x16x32_bf16 v[24:27],v[160:163],v[44:47],v[24:27]
ds_read_b128 v[36:39],v4 offset:18432
v_mfma_f32_16x16x32_bf16 v[24:27],a[240:243],v[40:43],v[24:27]
s_waitcnt lgkmcnt(4)
v_mfma_f32_16x16x32_bf16 v[24:27],a[24:27],v[48:51],v[24:27]
ds_read_b128 v[40:43],v4 offset:32768
v_mfma_f32_16x16x32_bf16 v[24:27],a[136:139],v[52:55],v[24:27]
v_mfma_f32_16x16x32_bf16 v[28:31],a[140:143],v[52:55],v[28:31]
ds_read_b128 v[44:47],v4 offset:47104
v_mfma_f32_16x16x32_bf16 v[28:31],a[28:31],v[48:51],v[28:31]
s_waitcnt lgkmcnt(4)
v_mfma_f32_16x16x32_bf16 v[28:31],a[252:255],v[56:59],v[28:31]
ds_read_b128 v[48:51],v4 offset:5120
v_mfma_f32_16x16x32_bf16 v[28:31],v[172:175],v[60:63],v[28:31]
buffer_load_dwordx4 v2,s[32:35],s63 offen offset:1024 sc1 lds
v_mfma_f32_16x16x32_bf16 v[24:27],v[168:171],v[60:63],v[24:27]
ds_read_b128 v[52:55],v4 offset:19456
v_mfma_f32_16x16x32_bf16 v[24:27],a[248:251],v[56:59],v[24:27]
s_waitcnt lgkmcnt(4)
v_mfma_f32_16x16x32_bf16 v[24:27],a[32:35],v[32:35],v[24:27]
ds_read_b128 v[56:59],v4 offset:33792
v_mfma_f32_16x16x32_bf16 v[24:27],a[144:147],v[36:39],v[24:27]
buffer_load_dwordx4 v2,s[32:35],s63 offen offset:2048 sc1 lds
v_mfma_f32_16x16x32_bf16 v[28:31],a[148:151],v[36:39],v[28:31]
ds_read_b128 v[60:63],v4 offset:48128
v_mfma_f32_16x16x32_bf16 v[28:31],a[36:39],v[32:35],v[28:31]
s_waitcnt lgkmcnt(4)
v_mfma_f32_16x16x32_bf16 v[28:31],v[68:71],v[40:43],v[28:31]
ds_read_b128 v[32:35],v4 offset:6144
v_mfma_f32_16x16x32_bf16 v[28:31],v[180:183],v[44:47],v[28:31]
buffer_load_dwordx4 v2,s[32:35],s63 offen offset:3072 sc1 lds
v_mfma_f32_16x16x32_bf16 v[24:27],v[176:179],v[44:47],v[24:27]
ds_read_b128 v[36:39],v4 offset:20480
v_mfma_f32_16x16x32_bf16 v[24:27],v[64:67],v[40:43],v[24:27]
s_waitcnt lgkmcnt(4)
v_mfma_f32_16x16x32_bf16 v[24:27],a[40:43],v[48:51],v[24:27]
ds_read_b128 v[40:43],v4 offset:34816
v_mfma_f32_16x16x32_bf16 v[24:27],a[152:155],v[52:55],v[24:27]
buffer_load_dwordx4 v[12:15],v2,s[32:35],s71 offen sc1
s_add_u32 s71,s71,s30
v_mfma_f32_16x16x32_bf16 v[28:31],a[156:159],v[52:55],v[28:31]
ds_read_b128 v[44:47],v4 offset:49152
s_add_u32 s63,s63,4096
v_mfma_f32_16x16x32_bf16 v[28:31],a[44:47],v[48:51],v[28:31]
s_add_u32 m0,s64,4096
s_waitcnt lgkmcnt(4)
v_mfma_f32_16x16x32_bf16 v[28:31],v[76:79],v[56:59],v[28:31]
ds_read_b128 v[48:51],v4 offset:7168
v_mfma_f32_16x16x32_bf16 v[28:31],v[188:191],v[60:63],v[28:31]
buffer_load_dwordx4 v2,s[32:35],s63 offen offset:0 sc1 lds
v_mfma_f32_16x16x32_bf16 v[24:27],v[184:187],v[60:63],v[24:27]
ds_read_b128 v[52:55],v4 offset:21504
v_mfma_f32_16x16x32_bf16 v[24:27],v[72:75],v[56:59],v[24:27]
s_waitcnt lgkmcnt(4)
v_mfma_f32_16x16x32_bf16 v[24:27],a[48:51],v[32:35],v[24:27]
ds_read_b128 v[56:59],v4 offset:35840
v_mfma_f32_16x16x32_bf16 v[24:27],a[160:163],v[36:39],v[24:27]
buffer_load_dwordx4 v2,s[32:35],s63 offen offset:1024 sc1 lds
v_mfma_f32_16x16x32_bf16 v[28:31],a[164:167],v[36:39],v[28:31]
ds_read_b128 v[60:63],v4 offset:50176
v_mfma_f32_16x16x32_bf16 v[28:31],a[52:55],v[32:35],v[28:31]
s_waitcnt lgkmcnt(4)
v_mfma_f32_16x16x32_bf16 v[28:31],v[84:87],v[40:43],v[28:31]
ds_read_b128 v[32:35],v4 offset:8192
v_mfma_f32_16x16x32_bf16 v[28:31],v[196:199],v[44:47],v[28:31]
buffer_load_dwordx4 v2,s[32:35],s63 offen offset:2048 sc1 lds
v_mfma_f32_16x16x32_bf16 v[24:27],v[192:195],v[44:47],v[24:27]
ds_read_b128 v[36:39],v4 offset:22528
v_mfma_f32_16x16x32_bf16 v[24:27],v[80:83],v[40:43],v[24:27]
s_waitcnt lgkmcnt(4)
v_mfma_f32_16x16x32_bf16 v[24:27],a[56:59],v[48:51],v[24:27]
ds_read_b128 v[40:43],v4 offset:36864
v_mfma_f32_16x16x32_bf16 v[24:27],a[168:171],v[52:55],v[24:27]
buffer_load_dwordx4 v2,s[32:35],s63 offen offset:3072 sc1 lds
v_mfma_f32_16x16x32_bf16 v[28:31],a[172:175],v[52:55],v[28:31]
ds_read_b128 v[44:47],v4 offset:51200
v_mfma_f32_16x16x32_bf16 v[28:31],a[60:63],v[48:51],v[28:31]
s_waitcnt lgkmcnt(4)
v_mfma_f32_16x16x32_bf16 v[28:31],v[92:95],v[56:59],v[28:31]
ds_read_b128 v[48:51],v4 offset:9216
v_mfma_f32_16x16x32_bf16 v[28:31],v[204:207],v[60:63],v[28:31]
s_add_u32 s63,s63,4096
s_add_u32 m0,s64,8192
v_mfma_f32_16x16x32_bf16 v[24:27],v[200:203],v[60:63],v[24:27]
ds_read_b128 v[52:55],v4 offset:23552
v_mfma_f32_16x16x32_bf16 v[24:27],v[88:91],v[56:59],v[24:27]
s_waitcnt lgkmcnt(4)
v_mfma_f32_16x16x32_bf16 v[24:27],a[64:67],v[32:35],v[24:27]
ds_read_b128 v[56:59],v4 offset:37888
v_mfma_f32_16x16x32_bf16 v[24:27],a[176:179],v[36:39],v[24:27]
buffer_load_dwordx4 v2,s[32:35],s63 offen offset:0 sc1 lds
v_mfma_f32_16x16x32_bf16 v[28:31],a[180:183],v[36:39],v[28:31]
ds_read_b128 v[60:63],v4 offset:52224
v_mfma_f32_16x16x32_bf16 v[28:31],a[68:71],v[32:35],v[28:31]
s_waitcnt lgkmcnt(4)
v_mfma_f32_16x16x32_bf16 v[28:31],v[100:103],v[40:43],v[28:31]
ds_read_b128 v[32:35],v4 offset:10240
v_mfma_f32_16x16x32_bf16 v[28:31],v[212:215],v[44:47],v[28:31]
buffer_load_dwordx4 v2,s[32:35],s63 offen offset:1024 sc1 lds
v_mfma_f32_16x16x32_bf16 v[24:27],v[208:211],v[44:47],v[24:27]
ds_read_b128 v[36:39],v4 offset:24576
v_mfma_f32_16x16x32_bf16 v[24:27],v[96:99],v[40:43],v[24:27]
s_waitcnt lgkmcnt(4)
v_mfma_f32_16x16x32_bf16 v[24:27],a[72:75],v[48:51],v[24:27]
ds_read_b128 v[40:43],v4 offset:38912
v_mfma_f32_16x16x32_bf16 v[24:27],a[184:187],v[52:55],v[24:27]
buffer_load_dwordx4 v2,s[32:35],s63 offen offset:2048 sc1 lds
v_mfma_f32_16x16x32_bf16 v[28:31],a[188:191],v[52:55],v[28:31]
ds_read_b128 v[44:47],v4 offset:53248
v_mfma_f32_16x16x32_bf16 v[28:31],a[76:79],v[48:51],v[28:31]
s_waitcnt lgkmcnt(4)
v_mfma_f32_16x16x32_bf16 v[28:31],v[108:111],v[56:59],v[28:31]
ds_read_b128 v[48:51],v4 offset:11264
v_mfma_f32_16x16x32_bf16 v[28:31],v[220:223],v[60:63],v[28:31]
v_mfma_f32_16x16x32_bf16 v[24:27],v[216:219],v[60:63],v[24:27]
ds_read_b128 v[52:55],v4 offset:25600
v_mfma_f32_16x16x32_bf16 v[24:27],v[104:107],v[56:59],v[24:27]
s_waitcnt lgkmcnt(4)
v_mfma_f32_16x16x32_bf16 v[24:27],a[80:83],v[32:35],v[24:27]
ds_read_b128 v[56:59],v4 offset:39936
v_mfma_f32_16x16x32_bf16 v[24:27],a[192:195],v[36:39],v[24:27]
buffer_load_dwordx4 v2,s[32:35],s63 offen offset:3072 sc1 lds
s_add_u32 s63,s63,4096
v_mfma_f32_16x16x32_bf16 v[28:31],a[196:199],v[36:39],v[28:31]
ds_read_b128 v[60:63],v4 offset:54272
s_add_u32 m0,s64,12288
v_mfma_f32_16x16x32_bf16 v[28:31],a[84:87],v[32:35],v[28:31]
s_waitcnt lgkmcnt(4)
v_mfma_f32_16x16x32_bf16 v[28:31],v[116:119],v[40:43],v[28:31]
ds_read_b128 v[32:35],v4 offset:12288
v_mfma_f32_16x16x32_bf16 v[28:31],v[228:231],v[44:47],v[28:31]
buffer_load_dwordx4 v2,s[32:35],s63 offen offset:0 sc1 lds
v_mfma_f32_16x16x32_bf16 v[24:27],v[224:227],v[44:47],v[24:27]
ds_read_b128 v[36:39],v4 offset:26624
v_mfma_f32_16x16x32_bf16 v[24:27],v[112:115],v[40:43],v[24:27]
s_waitcnt lgkmcnt(4)
v_mfma_f32_16x16x32_bf16 v[24:27],a[88:91],v[48:51],v[24:27]
ds_read_b128 v[40:43],v4 offset:40960
v_mfma_f32_16x16x32_bf16 v[24:27],a[200:203],v[52:55],v[24:27]
buffer_load_dwordx4 v2,s[32:35],s63 offen offset:1024 sc1 lds
s_add_u32 s63,s63,s31
v_mfma_f32_16x16x32_bf16 v[28:31],a[204:207],v[52:55],v[28:31]
ds_read_b128 v[44:47],v4 offset:55296
v_mfma_f32_16x16x32_bf16 v[28:31],a[92:95],v[48:51],v[28:31]
s_waitcnt lgkmcnt(4)
v_mfma_f32_16x16x32_bf16 v[28:31],v[124:127],v[56:59],v[28:31]
ds_read_b128 v[48:51],v4 offset:13312
s_waitcnt vmcnt(10)
v_mfma_f32_16x16x32_bf16 v[28:31],v[236:239],v[60:63],v[28:31]
v_lshlrev_b32 v16,16,v12
v_and_b32 v17,0xffff0000,v12
v_mfma_f32_16x16x32_bf16 v[24:27],v[232:235],v[60:63],v[24:27]
ds_read_b128 v[52:55],v4 offset:27648
v_lshlrev_b32 v18,16,v13
v_mfma_f32_16x16x32_bf16 v[24:27],v[120:123],v[56:59],v[24:27]
v_and_b32 v19,0xffff0000,v13
s_waitcnt lgkmcnt(4)
v_mfma_f32_16x16x32_bf16 v[24:27],a[96:99],v[32:35],v[24:27]
ds_read_b128 v[56:59],v4 offset:41984
v_lshlrev_b32 v20,16,v14
v_mfma_f32_16x16x32_bf16 v[24:27],a[208:211],v[36:39],v[24:27]
s_barrier
v_mfma_f32_16x16x32_bf16 v[28:31],a[212:215],v[36:39],v[28:31]
ds_read_b128 v[60:63],v4 offset:56320
v_and_b32 v21,0xffff0000,v14
v_mfma_f32_16x16x32_bf16 v[28:31],a[100:103],v[32:35],v[28:31]
v_lshlrev_b32 v22,16,v15
s_waitcnt lgkmcnt(4)
v_mfma_f32_16x16x32_bf16 v[28:31],v[132:135],v[40:43],v[28:31]
ds_read_b128 v[32:35],v2 offset:0
v_and_b32 v23,0xffff0000,v15
v_mfma_f32_16x16x32_bf16 v[28:31],v[244:247],v[44:47],v[28:31]
v_mfma_f32_16x16x32_bf16 v[24:27],v[240:243],v[44:47],v[24:27]
ds_read_b128 v[36:39],v2 offset:14336
v_mfma_f32_16x16x32_bf16 v[24:27],v[128:131],v[40:43],v[24:27]
s_waitcnt lgkmcnt(4)
v_mfma_f32_16x16x32_bf16 v[24:27],a[104:107],v[48:51],v[24:27]
ds_read_b128 v[40:43],v2 offset:28672
)#"
R"#(
v_mfma_f32_16x16x32_bf16 v[24:27],a[216:219],v[52:55],v[24:27]
v_mfma_f32_16x16x32_bf16 v[28:31],a[220:223],v[52:55],v[28:31]
ds_read_b128 v[44:47],v2 offset:43008
v_mfma_f32_16x16x32_bf16 v[28:31],a[108:111],v[48:51],v[28:31]
s_waitcnt lgkmcnt(4)
v_mfma_f32_16x16x32_bf16 v[28:31],v[140:143],v[56:59],v[28:31]
ds_read_b128 v[48:51],v2 offset:1024
v_mfma_f32_16x16x32_bf16 v[28:31],v[252:255],v[60:63],v[28:31]
v_mfma_f32_16x16x32_bf16 v[24:27],v[248:251],v[60:63],v[24:27]
ds_read_b128 v[52:55],v2 offset:15360
v_mfma_f32_16x16x32_bf16 v[24:27],v[136:139],v[56:59],v[24:27]
s_branch .L_dll_2
.L_dlb_2:
s_mov_b32 s62,s58
s_mov_b32 s70,s59
s_add_u32 s74,s74,8192
v_lshrrev_b32 v5,8,v2
v_lshl_add_u32 v5,v5,4,s74
v_and_b32 v3,128,v2
v_lshl_add_u32 v5,v3,2,v5
v_and_b32 v3,16,v2
v_lshl_add_u32 v5,v3,4,v5
v_and_b32 v3,96,v2
v_lshl_add_u32 v5,v3,1,v5
v_lshrrev_b32 v3,2,v2
v_add_u32 v3,s74,v3
s_add_u32 s66,s66,2375680
s_add_u32 s63,s63,2375680
s_add_u32 s71,s71,2375680
s_mov_b32 s68,98304
s_mov_b32 s34,79446016
.p2align 6
.L_dml_2:
s_waitcnt lgkmcnt(4)
v_mfma_f32_16x16x32_bf16 v[24:27],a[0:3],v[32:35],v[24:27]
ds_read_b128 v[56:59],v4 offset:29696
v_mfma_f32_16x16x32_bf16 v[24:27],a[112:115],v[36:39],v[24:27]
v_mfma_f32_16x16x32_bf16 v[28:31],a[116:119],v[36:39],v[28:31]
ds_read_b128 v[60:63],v4 offset:44032
v_mfma_f32_16x16x32_bf16 v[28:31],a[4:7],v[32:35],v[28:31]
v_cvt_pk_bf16_f32 v15,v22,v23
s_waitcnt lgkmcnt(4)
v_mfma_f32_16x16x32_bf16 v[28:31],a[228:231],v[40:43],v[28:31]
ds_read_b128 v[32:35],v4 offset:2048
v_cvt_pk_bf16_f32 v14,v20,v21
v_mfma_f32_16x16x32_bf16 v[28:31],v[148:151],v[44:47],v[28:31]
v_cvt_pk_bf16_f32 v13,v18,v19
v_cvt_pk_bf16_f32 v12,v16,v17
v_mfma_f32_16x16x32_bf16 v[24:27],v[144:147],v[44:47],v[24:27]
ds_read_b128 v[36:39],v4 offset:16384
ds_write_b128 v5,v[12:15]
v_mfma_f32_16x16x32_bf16 v[24:27],a[224:227],v[40:43],v[24:27]
ds_read2st64_b32 v[12:13],v3 offset0:0 offset1:1
s_waitcnt lgkmcnt(6)
v_mfma_f32_16x16x32_bf16 v[24:27],a[8:11],v[48:51],v[24:27]
ds_read_b128 v[40:43],v4 offset:30720
ds_read2st64_b32 v[14:15],v3 offset0:2 offset1:3
v_mfma_f32_16x16x32_bf16 v[24:27],a[120:123],v[52:55],v[24:27]
v_mfma_f32_16x16x32_bf16 v[28:31],a[124:127],v[52:55],v[28:31]
ds_read_b128 v[44:47],v4 offset:45056
v_mfma_f32_16x16x32_bf16 v[28:31],a[12:15],v[48:51],v[28:31]
s_waitcnt lgkmcnt(7)
v_mfma_f32_16x16x32_bf16 v[28:31],a[236:239],v[56:59],v[28:31]
ds_read_b128 v[48:51],v4 offset:3072
v_mfma_f32_16x16x32_bf16 v[28:31],v[156:159],v[60:63],v[28:31]
s_waitcnt lgkmcnt(2)
buffer_atomic_pk_add_bf16 v12,v1,s[44:47],s65 offen offset:0 sc1
v_mfma_f32_16x16x32_bf16 v[24:27],v[152:155],v[60:63],v[24:27]
ds_read_b128 v[52:55],v4 offset:17408
s_add_u32 s70,s70,s75
v_mfma_f32_16x16x32_bf16 v[24:27],a[232:235],v[56:59],v[24:27]
buffer_atomic_pk_add_bf16 v13,v1,s[44:47],s65 offen offset:2048 sc1
v_mfma_f32_16x16x32_bf16 v[24:27],a[16:19],v[32:35],v[24:27]
ds_read_b128 v[56:59],v4 offset:31744
s_add_u32 s65,s65,16384
v_mfma_f32_16x16x32_bf16 v[24:27],a[128:131],v[36:39],v[24:27]
s_waitcnt vmcnt(2)
s_barrier
v_mfma_f32_16x16x32_bf16 v[28:31],a[132:135],v[36:39],v[28:31]
ds_read_b128 v[60:63],v4 offset:46080
v_mfma_f32_16x16x32_bf16 v[28:31],a[20:23],v[32:35],v[28:31]
buffer_atomic_pk_add_bf16 v14,v1,s[44:47],s65 offen offset:0 sc1
s_waitcnt lgkmcnt(4)
v_mfma_f32_16x16x32_bf16 v[28:31],a[244:247],v[40:43],v[28:31]
ds_read_b128 v[32:35],v4 offset:4096
s_add_u32 m0,s64,0
v_mfma_f32_16x16x32_bf16 v[28:31],v[164:167],v[44:47],v[28:31]
buffer_atomic_pk_add_bf16 v15,v1,s[44:47],s65 offen offset:2048 sc1
s_and_b32 s65,s70,0x3f8000
v_mfma_f32_16x16x32_bf16 v[24:27],v[160:163],v[44:47],v[24:27]
ds_read_b128 v[36:39],v4 offset:18432
v_mfma_f32_16x16x32_bf16 v[24:27],a[240:243],v[40:43],v[24:27]
s_waitcnt lgkmcnt(4)
v_mfma_f32_16x16x32_bf16 v[24:27],a[24:27],v[48:51],v[24:27]
ds_read_b128 v[40:43],v4 offset:32768
v_mfma_f32_16x16x32_bf16 v[24:27],a[136:139],v[52:55],v[24:27]
buffer_load_dwordx4 v2,s[32:35],s63 offen offset:0 sc1 lds
v_mfma_f32_16x16x32_bf16 v[28:31],a[140:143],v[52:55],v[28:31]
ds_read_b128 v[44:47],v4 offset:47104
v_mfma_f32_16x16x32_bf16 v[28:31],a[28:31],v[48:51],v[28:31]
s_waitcnt lgkmcnt(4)
v_mfma_f32_16x16x32_bf16 v[28:31],a[252:255],v[56:59],v[28:31]
ds_read_b128 v[48:51],v4 offset:5120
v_mfma_f32_16x16x32_bf16 v[28:31],v[172:175],v[60:63],v[28:31]
buffer_load_dwordx4 v2,s[32:35],s63 offen offset:1024 sc1 lds
v_mfma_f32_16x16x32_bf16 v[24:27],v[168:171],v[60:63],v[24:27]
ds_read_b128 v[52:55],v4 offset:19456
v_mfma_f32_16x16x32_bf16 v[24:27],a[248:251],v[56:59],v[24:27]
s_waitcnt lgkmcnt(4)
v_mfma_f32_16x16x32_bf16 v[24:27],a[32:35],v[32:35],v[24:27]
ds_read_b128 v[56:59],v4 offset:33792
v_mfma_f32_16x16x32_bf16 v[24:27],a[144:147],v[36:39],v[24:27]
buffer_load_dwordx4 v2,s[32:35],s63 offen offset:2048 sc1 lds
v_mfma_f32_16x16x32_bf16 v[28:31],a[148:151],v[36:39],v[28:31]
ds_read_b128 v[60:63],v4 offset:48128
v_mfma_f32_16x16x32_bf16 v[28:31],a[36:39],v[32:35],v[28:31]
s_waitcnt lgkmcnt(4)
v_mfma_f32_16x16x32_bf16 v[28:31],v[68:71],v[40:43],v[28:31]
ds_read_b128 v[32:35],v4 offset:6144
v_mfma_f32_16x16x32_bf16 v[28:31],v[180:183],v[44:47],v[28:31]
buffer_load_dwordx4 v2,s[32:35],s63 offen offset:3072 sc1 lds
v_mfma_f32_16x16x32_bf16 v[24:27],v[176:179],v[44:47],v[24:27]
ds_read_b128 v[36:39],v4 offset:20480
v_mfma_f32_16x16x32_bf16 v[24:27],v[64:67],v[40:43],v[24:27]
s_waitcnt lgkmcnt(4)
v_mfma_f32_16x16x32_bf16 v[24:27],a[40:43],v[48:51],v[24:27]
ds_read_b128 v[40:43],v4 offset:34816
v_mfma_f32_16x16x32_bf16 v[24:27],a[152:155],v[52:55],v[24:27]
buffer_load_dwordx4 v[12:15],v2,s[32:35],s71 offen sc1
s_add_u32 s71,s71,s30
v_mfma_f32_16x16x32_bf16 v[28:31],a[156:159],v[52:55],v[28:31]
ds_read_b128 v[44:47],v4 offset:49152
s_add_u32 s63,s63,4096
v_mfma_f32_16x16x32_bf16 v[28:31],a[44:47],v[48:51],v[28:31]
s_add_u32 m0,s64,4096
s_waitcnt lgkmcnt(4)
v_mfma_f32_16x16x32_bf16 v[28:31],v[76:79],v[56:59],v[28:31]
ds_read_b128 v[48:51],v4 offset:7168
v_mfma_f32_16x16x32_bf16 v[28:31],v[188:191],v[60:63],v[28:31]
buffer_load_dwordx4 v2,s[32:35],s63 offen offset:0 sc1 lds
v_mfma_f32_16x16x32_bf16 v[24:27],v[184:187],v[60:63],v[24:27]
ds_read_b128 v[52:55],v4 offset:21504
v_mfma_f32_16x16x32_bf16 v[24:27],v[72:75],v[56:59],v[24:27]
s_waitcnt lgkmcnt(4)
v_mfma_f32_16x16x32_bf16 v[24:27],a[48:51],v[32:35],v[24:27]
ds_read_b128 v[56:59],v4 offset:35840
v_mfma_f32_16x16x32_bf16 v[24:27],a[160:163],v[36:39],v[24:27]
buffer_load_dwordx4 v2,s[32:35],s63 offen offset:1024 sc1 lds
v_mfma_f32_16x16x32_bf16 v[28:31],a[164:167],v[36:39],v[28:31]
ds_read_b128 v[60:63],v4 offset:50176
v_mfma_f32_16x16x32_bf16 v[28:31],a[52:55],v[32:35],v[28:31]
s_waitcnt lgkmcnt(4)
v_mfma_f32_16x16x32_bf16 v[28:31],v[84:87],v[40:43],v[28:31]
ds_read_b128 v[32:35],v4 offset:8192
v_mfma_f32_16x16x32_bf16 v[28:31],v[196:199],v[44:47],v[28:31]
buffer_load_dwordx4 v2,s[32:35],s63 offen offset:2048 sc1 lds
v_mfma_f32_16x16x32_bf16 v[24:27],v[192:195],v[44:47],v[24:27]
ds_read_b128 v[36:39],v4 offset:22528
v_mfma_f32_16x16x32_bf16 v[24:27],v[80:83],v[40:43],v[24:27]
s_waitcnt lgkmcnt(4)
v_mfma_f32_16x16x32_bf16 v[24:27],a[56:59],v[48:51],v[24:27]
ds_read_b128 v[40:43],v4 offset:36864
v_mfma_f32_16x16x32_bf16 v[24:27],a[168:171],v[52:55],v[24:27]
buffer_load_dwordx4 v2,s[32:35],s63 offen offset:3072 sc1 lds
v_mfma_f32_16x16x32_bf16 v[28:31],a[172:175],v[52:55],v[28:31]
ds_read_b128 v[44:47],v4 offset:51200
v_mfma_f32_16x16x32_bf16 v[28:31],a[60:63],v[48:51],v[28:31]
s_waitcnt lgkmcnt(4)
v_mfma_f32_16x16x32_bf16 v[28:31],v[92:95],v[56:59],v[28:31]
ds_read_b128 v[48:51],v4 offset:9216
v_mfma_f32_16x16x32_bf16 v[28:31],v[204:207],v[60:63],v[28:31]
s_add_u32 s63,s63,4096
s_add_u32 m0,s64,8192
v_mfma_f32_16x16x32_bf16 v[24:27],v[200:203],v[60:63],v[24:27]
ds_read_b128 v[52:55],v4 offset:23552
v_mfma_f32_16x16x32_bf16 v[24:27],v[88:91],v[56:59],v[24:27]
s_waitcnt lgkmcnt(4)
v_mfma_f32_16x16x32_bf16 v[24:27],a[64:67],v[32:35],v[24:27]
ds_read_b128 v[56:59],v4 offset:37888
v_mfma_f32_16x16x32_bf16 v[24:27],a[176:179],v[36:39],v[24:27]
buffer_load_dwordx4 v2,s[32:35],s63 offen offset:0 sc1 lds
v_mfma_f32_16x16x32_bf16 v[28:31],a[180:183],v[36:39],v[28:31]
ds_read_b128 v[60:63],v4 offset:52224
v_mfma_f32_16x16x32_bf16 v[28:31],a[68:71],v[32:35],v[28:31]
s_waitcnt lgkmcnt(4)
v_mfma_f32_16x16x32_bf16 v[28:31],v[100:103],v[40:43],v[28:31]
ds_read_b128 v[32:35],v4 offset:10240
v_mfma_f32_16x16x32_bf16 v[28:31],v[212:215],v[44:47],v[28:31]
buffer_load_dwordx4 v2,s[32:35],s63 offen offset:1024 sc1 lds
v_mfma_f32_16x16x32_bf16 v[24:27],v[208:211],v[44:47],v[24:27]
ds_read_b128 v[36:39],v4 offset:24576
v_mfma_f32_16x16x32_bf16 v[24:27],v[96:99],v[40:43],v[24:27]
s_waitcnt lgkmcnt(4)
v_mfma_f32_16x16x32_bf16 v[24:27],a[72:75],v[48:51],v[24:27]
ds_read_b128 v[40:43],v4 offset:38912
v_mfma_f32_16x16x32_bf16 v[24:27],a[184:187],v[52:55],v[24:27]
buffer_load_dwordx4 v2,s[32:35],s63 offen offset:2048 sc1 lds
v_mfma_f32_16x16x32_bf16 v[28:31],a[188:191],v[52:55],v[28:31]
ds_read_b128 v[44:47],v4 offset:53248
v_mfma_f32_16x16x32_bf16 v[28:31],a[76:79],v[48:51],v[28:31]
s_waitcnt lgkmcnt(4)
v_mfma_f32_16x16x32_bf16 v[28:31],v[108:111],v[56:59],v[28:31]
ds_read_b128 v[48:51],v4 offset:11264
v_mfma_f32_16x16x32_bf16 v[28:31],v[220:223],v[60:63],v[28:31]
v_mfma_f32_16x16x32_bf16 v[24:27],v[216:219],v[60:63],v[24:27]
ds_read_b128 v[52:55],v4 offset:25600
v_mfma_f32_16x16x32_bf16 v[24:27],v[104:107],v[56:59],v[24:27]
s_waitcnt lgkmcnt(4)
v_mfma_f32_16x16x32_bf16 v[24:27],a[80:83],v[32:35],v[24:27]
ds_read_b128 v[56:59],v4 offset:39936
v_mfma_f32_16x16x32_bf16 v[24:27],a[192:195],v[36:39],v[24:27]
buffer_load_dwordx4 v2,s[32:35],s63 offen offset:3072 sc1 lds
s_add_u32 s63,s63,4096
v_mfma_f32_16x16x32_bf16 v[28:31],a[196:199],v[36:39],v[28:31]
ds_read_b128 v[60:63],v4 offset:54272
s_add_u32 m0,s64,12288
v_mfma_f32_16x16x32_bf16 v[28:31],a[84:87],v[32:35],v[28:31]
s_waitcnt lgkmcnt(4)
v_mfma_f32_16x16x32_bf16 v[28:31],v[116:119],v[40:43],v[28:31]
ds_read_b128 v[32:35],v4 offset:12288
v_mfma_f32_16x16x32_bf16 v[28:31],v[228:231],v[44:47],v[28:31]
buffer_load_dwordx4 v2,s[32:35],s63 offen offset:0 sc1 lds
v_mfma_f32_16x16x32_bf16 v[24:27],v[224:227],v[44:47],v[24:27]
ds_read_b128 v[36:39],v4 offset:26624
v_mfma_f32_16x16x32_bf16 v[24:27],v[112:115],v[40:43],v[24:27]
s_waitcnt lgkmcnt(4)
v_mfma_f32_16x16x32_bf16 v[24:27],a[88:91],v[48:51],v[24:27]
ds_read_b128 v[40:43],v4 offset:40960
v_mfma_f32_16x16x32_bf16 v[24:27],a[200:203],v[52:55],v[24:27]
buffer_load_dwordx4 v2,s[32:35],s63 offen offset:1024 sc1 lds
s_add_u32 s63,s63,s31
v_mfma_f32_16x16x32_bf16 v[28:31],a[204:207],v[52:55],v[28:31]
ds_read_b128 v[44:47],v4 offset:55296
v_mfma_f32_16x16x32_bf16 v[28:31],a[92:95],v[48:51],v[28:31]
s_waitcnt lgkmcnt(4)
v_mfma_f32_16x16x32_bf16 v[28:31],v[124:127],v[56:59],v[28:31]
ds_read_b128 v[48:51],v4 offset:13312
s_waitcnt vmcnt(10)
v_mfma_f32_16x16x32_bf16 v[28:31],v[236:239],v[60:63],v[28:31]
v_lshlrev_b32 v16,16,v12
v_and_b32 v17,0xffff0000,v12
v_mfma_f32_16x16x32_bf16 v[24:27],v[232:235],v[60:63],v[24:27]
ds_read_b128 v[52:55],v4 offset:27648
v_lshlrev_b32 v18,16,v13
v_mfma_f32_16x16x32_bf16 v[24:27],v[120:123],v[56:59],v[24:27]
v_and_b32 v19,0xffff0000,v13
s_waitcnt lgkmcnt(4)
v_mfma_f32_16x16x32_bf16 v[24:27],a[96:99],v[32:35],v[24:27]
ds_read_b128 v[56:59],v4 offset:41984
v_lshlrev_b32 v20,16,v14
v_mfma_f32_16x16x32_bf16 v[24:27],a[208:211],v[36:39],v[24:27]
s_barrier
v_mfma_f32_16x16x32_bf16 v[28:31],a[212:215],v[36:39],v[28:31]
ds_read_b128 v[60:63],v4 offset:56320
v_and_b32 v21,0xffff0000,v14
v_mfma_f32_16x16x32_bf16 v[28:31],a[100:103],v[32:35],v[28:31]
v_lshlrev_b32 v22,16,v15
s_waitcnt lgkmcnt(4)
v_mfma_f32_16x16x32_bf16 v[28:31],v[132:135],v[40:43],v[28:31]
ds_read_b128 v[32:35],v2 offset:0
v_and_b32 v23,0xffff0000,v15
v_mfma_f32_16x16x32_bf16 v[28:31],v[244:247],v[44:47],v[28:31]
v_mfma_f32_16x16x32_bf16 v[24:27],v[240:243],v[44:47],v[24:27]
ds_read_b128 v[36:39],v2 offset:14336
v_mfma_f32_16x16x32_bf16 v[24:27],v[128:131],v[40:43],v[24:27]
s_waitcnt lgkmcnt(4)
v_mfma_f32_16x16x32_bf16 v[24:27],a[104:107],v[48:51],v[24:27]
ds_read_b128 v[40:43],v2 offset:28672
v_mfma_f32_16x16x32_bf16 v[24:27],a[216:219],v[52:55],v[24:27]
v_mfma_f32_16x16x32_bf16 v[28:31],a[220:223],v[52:55],v[28:31]
ds_read_b128 v[44:47],v2 offset:43008
v_mfma_f32_16x16x32_bf16 v[28:31],a[108:111],v[48:51],v[28:31]
s_waitcnt lgkmcnt(4)
v_mfma_f32_16x16x32_bf16 v[28:31],v[140:143],v[56:59],v[28:31]
ds_read_b128 v[48:51],v2 offset:1024
v_mfma_f32_16x16x32_bf16 v[28:31],v[252:255],v[60:63],v[28:31]
v_mfma_f32_16x16x32_bf16 v[24:27],v[248:251],v[60:63],v[24:27]
ds_read_b128 v[52:55],v2 offset:15360
s_sub_u32 s62,s62,1
v_mfma_f32_16x16x32_bf16 v[24:27],v[136:139],v[56:59],v[24:27]
s_cbranch_scc1 .L_dmt_2
s_waitcnt lgkmcnt(4)
v_mfma_f32_16x16x32_bf16 v[16:19],a[0:3],v[32:35],v[16:19]
ds_read_b128 v[56:59],v2 offset:29696
v_mfma_f32_16x16x32_bf16 v[16:19],a[112:115],v[36:39],v[16:19]
v_mfma_f32_16x16x32_bf16 v[20:23],a[116:119],v[36:39],v[20:23]
ds_read_b128 v[60:63],v2 offset:44032
v_mfma_f32_16x16x32_bf16 v[20:23],a[4:7],v[32:35],v[20:23]
v_cvt_pk_bf16_f32 v15,v30,v31
s_waitcnt lgkmcnt(4)
v_mfma_f32_16x16x32_bf16 v[20:23],a[228:231],v[40:43],v[20:23]
ds_read_b128 v[32:35],v2 offset:2048
v_cvt_pk_bf16_f32 v14,v28,v29
v_mfma_f32_16x16x32_bf16 v[20:23],v[148:151],v[44:47],v[20:23]
v_cvt_pk_bf16_f32 v13,v26,v27
v_cvt_pk_bf16_f32 v12,v24,v25
v_mfma_f32_16x16x32_bf16 v[16:19],v[144:147],v[44:47],v[16:19]
ds_read_b128 v[36:39],v2 offset:16384
ds_write_b128 v5,v[12:15]
v_mfma_f32_16x16x32_bf16 v[16:19],a[224:227],v[40:43],v[16:19]
ds_read2st64_b32 v[12:13],v3 offset0:0 offset1:1
s_waitcnt lgkmcnt(6)
v_mfma_f32_16x16x32_bf16 v[16:19],a[8:11],v[48:51],v[16:19]
ds_read_b128 v[40:43],v2 offset:30720
ds_read2st64_b32 v[14:15],v3 offset0:2 offset1:3
v_mfma_f32_16x16x32_bf16 v[16:19],a[120:123],v[52:55],v[16:19]
v_mfma_f32_16x16x32_bf16 v[20:23],a[124:127],v[52:55],v[20:23]
ds_read_b128 v[44:47],v2 offset:45056
v_mfma_f32_16x16x32_bf16 v[20:23],a[12:15],v[48:51],v[20:23]
s_waitcnt lgkmcnt(7)
v_mfma_f32_16x16x32_bf16 v[20:23],a[236:239],v[56:59],v[20:23]
ds_read_b128 v[48:51],v2 offset:3072
v_mfma_f32_16x16x32_bf16 v[20:23],v[156:159],v[60:63],v[20:23]
s_waitcnt lgkmcnt(2)
buffer_atomic_pk_add_bf16 v12,v1,s[44:47],s65 offen offset:0 sc1
v_mfma_f32_16x16x32_bf16 v[16:19],v[152:155],v[60:63],v[16:19]
ds_read_b128 v[52:55],v2 offset:17408
s_add_u32 s70,s70,s68
v_mfma_f32_16x16x32_bf16 v[16:19],a[232:235],v[56:59],v[16:19]
buffer_atomic_pk_add_bf16 v13,v1,s[44:47],s65 offen offset:2048 sc1
v_mfma_f32_16x16x32_bf16 v[16:19],a[16:19],v[32:35],v[16:19]
ds_read_b128 v[56:59],v2 offset:31744
s_add_u32 s65,s65,16384
v_mfma_f32_16x16x32_bf16 v[16:19],a[128:131],v[36:39],v[16:19]
s_waitcnt vmcnt(2)
s_barrier
v_mfma_f32_16x16x32_bf16 v[20:23],a[132:135],v[36:39],v[20:23]
ds_read_b128 v[60:63],v2 offset:46080
v_mfma_f32_16x16x32_bf16 v[20:23],a[20:23],v[32:35],v[20:23]
buffer_atomic_pk_add_bf16 v14,v1,s[44:47],s65 offen offset:0 sc1
s_waitcnt lgkmcnt(4)
v_mfma_f32_16x16x32_bf16 v[20:23],a[244:247],v[40:43],v[20:23]
ds_read_b128 v[32:35],v2 offset:4096
s_add_u32 m0,s64,57344
v_mfma_f32_16x16x32_bf16 v[20:23],v[164:167],v[44:47],v[20:23]
buffer_load_dwordx4 v2,s[32:35],s63 offen offset:0 sc1 lds
s_mov_b32 s68,s75
v_mfma_f32_16x16x32_bf16 v[16:19],v[160:163],v[44:47],v[16:19]
ds_read_b128 v[36:39],v2 offset:18432
v_mfma_f32_16x16x32_bf16 v[16:19],a[240:243],v[40:43],v[16:19]
buffer_atomic_pk_add_bf16 v15,v1,s[44:47],s65 offen offset:2048 sc1
s_waitcnt lgkmcnt(4)
v_mfma_f32_16x16x32_bf16 v[16:19],a[24:27],v[48:51],v[16:19]
ds_read_b128 v[40:43],v2 offset:32768
s_and_b32 s65,s70,0x3f8000
v_mfma_f32_16x16x32_bf16 v[16:19],a[136:139],v[52:55],v[16:19]
v_mfma_f32_16x16x32_bf16 v[20:23],a[140:143],v[52:55],v[20:23]
ds_read_b128 v[44:47],v2 offset:47104
v_mfma_f32_16x16x32_bf16 v[20:23],a[28:31],v[48:51],v[20:23]
s_waitcnt lgkmcnt(4)
v_mfma_f32_16x16x32_bf16 v[20:23],a[252:255],v[56:59],v[20:23]
ds_read_b128 v[48:51],v2 offset:5120
v_mfma_f32_16x16x32_bf16 v[20:23],v[172:175],v[60:63],v[20:23]
buffer_load_dwordx4 v2,s[32:35],s63 offen offset:1024 sc1 lds
v_mfma_f32_16x16x32_bf16 v[16:19],v[168:171],v[60:63],v[16:19]
ds_read_b128 v[52:55],v2 offset:19456
v_mfma_f32_16x16x32_bf16 v[16:19],a[248:251],v[56:59],v[16:19]
s_waitcnt lgkmcnt(4)
v_mfma_f32_16x16x32_bf16 v[16:19],a[32:35],v[32:35],v[16:19]
ds_read_b128 v[56:59],v2 offset:33792
v_mfma_f32_16x16x32_bf16 v[16:19],a[144:147],v[36:39],v[16:19]
buffer_load_dwordx4 v2,s[32:35],s63 offen offset:2048 sc1 lds
v_mfma_f32_16x16x32_bf16 v[20:23],a[148:151],v[36:39],v[20:23]
ds_read_b128 v[60:63],v2 offset:48128
v_mfma_f32_16x16x32_bf16 v[20:23],a[36:39],v[32:35],v[20:23]
s_waitcnt lgkmcnt(4)
v_mfma_f32_16x16x32_bf16 v[20:23],v[68:71],v[40:43],v[20:23]
ds_read_b128 v[32:35],v2 offset:6144
v_mfma_f32_16x16x32_bf16 v[20:23],v[180:183],v[44:47],v[20:23]
buffer_load_dwordx4 v2,s[32:35],s63 offen offset:3072 sc1 lds
v_mfma_f32_16x16x32_bf16 v[16:19],v[176:179],v[44:47],v[16:19]
ds_read_b128 v[36:39],v2 offset:20480
v_mfma_f32_16x16x32_bf16 v[16:19],v[64:67],v[40:43],v[16:19]
s_waitcnt lgkmcnt(4)
v_mfma_f32_16x16x32_bf16 v[16:19],a[40:43],v[48:51],v[16:19]
ds_read_b128 v[40:43],v2 offset:34816
v_mfma_f32_16x16x32_bf16 v[16:19],a[152:155],v[52:55],v[16:19]
buffer_load_dwordx4 v[12:15],v2,s[32:35],s71 offen sc1
s_add_u32 s71,s71,s30
v_mfma_f32_16x16x32_bf16 v[20:23],a[156:159],v[52:55],v[20:23]
ds_read_b128 v[44:47],v2 offset:49152
s_add_u32 s63,s63,4096
v_mfma_f32_16x16x32_bf16 v[20:23],a[44:47],v[48:51],v[20:23]
s_add_u32 m0,s64,61440
s_waitcnt lgkmcnt(4)
v_mfma_f32_16x16x32_bf16 v[20:23],v[76:79],v[56:59],v[20:23]
ds_read_b128 v[48:51],v2 offset:7168
v_mfma_f32_16x16x32_bf16 v[20:23],v[188:191],v[60:63],v[20:23]
buffer_load_dwordx4 v2,s[32:35],s63 offen offset:0 sc1 lds
v_mfma_f32_16x16x32_bf16 v[16:19],v[184:187],v[60:63],v[16:19]
ds_read_b128 v[52:55],v2 offset:21504
v_mfma_f32_16x16x32_bf16 v[16:19],v[72:75],v[56:59],v[16:19]
s_waitcnt lgkmcnt(4)
v_mfma_f32_16x16x32_bf16 v[16:19],a[48:51],v[32:35],v[16:19]
ds_read_b128 v[56:59],v2 offset:35840
v_mfma_f32_16x16x32_bf16 v[16:19],a[160:163],v[36:39],v[16:19]
buffer_load_dwordx4 v2,s[32:35],s63 offen offset:1024 sc1 lds
v_mfma_f32_16x16x32_bf16 v[20:23],a[164:167],v[36:39],v[20:23]
ds_read_b128 v[60:63],v2 offset:50176
v_mfma_f32_16x16x32_bf16 v[20:23],a[52:55],v[32:35],v[20:23]
s_waitcnt lgkmcnt(4)
v_mfma_f32_16x16x32_bf16 v[20:23],v[84:87],v[40:43],v[20:23]
ds_read_b128 v[32:35],v2 offset:8192
v_mfma_f32_16x16x32_bf16 v[20:23],v[196:199],v[44:47],v[20:23]
buffer_load_dwordx4 v2,s[32:35],s63 offen offset:2048 sc1 lds
v_mfma_f32_16x16x32_bf16 v[16:19],v[192:195],v[44:47],v[16:19]
ds_read_b128 v[36:39],v2 offset:22528
v_mfma_f32_16x16x32_bf16 v[16:19],v[80:83],v[40:43],v[16:19]
s_waitcnt lgkmcnt(4)
v_mfma_f32_16x16x32_bf16 v[16:19],a[56:59],v[48:51],v[16:19]
ds_read_b128 v[40:43],v2 offset:36864
v_mfma_f32_16x16x32_bf16 v[16:19],a[168:171],v[52:55],v[16:19]
buffer_load_dwordx4 v2,s[32:35],s63 offen offset:3072 sc1 lds
v_mfma_f32_16x16x32_bf16 v[20:23],a[172:175],v[52:55],v[20:23]
ds_read_b128 v[44:47],v2 offset:51200
v_mfma_f32_16x16x32_bf16 v[20:23],a[60:63],v[48:51],v[20:23]
s_waitcnt lgkmcnt(4)
v_mfma_f32_16x16x32_bf16 v[20:23],v[92:95],v[56:59],v[20:23]
ds_read_b128 v[48:51],v2 offset:9216
v_mfma_f32_16x16x32_bf16 v[20:23],v[204:207],v[60:63],v[20:23]
s_add_u32 s63,s63,4096
s_add_u32 m0,s64,65536
v_mfma_f32_16x16x32_bf16 v[16:19],v[200:203],v[60:63],v[16:19]
ds_read_b128 v[52:55],v2 offset:23552
v_mfma_f32_16x16x32_bf16 v[16:19],v[88:91],v[56:59],v[16:19]
s_waitcnt lgkmcnt(4)
v_mfma_f32_16x16x32_bf16 v[16:19],a[64:67],v[32:35],v[16:19]
ds_read_b128 v[56:59],v2 offset:37888
v_mfma_f32_16x16x32_bf16 v[16:19],a[176:179],v[36:39],v[16:19]
buffer_load_dwordx4 v2,s[32:35],s63 offen offset:0 sc1 lds
v_mfma_f32_16x16x32_bf16 v[20:23],a[180:183],v[36:39],v[20:23]
ds_read_b128 v[60:63],v2 offset:52224
v_mfma_f32_16x16x32_bf16 v[20:23],a[68:71],v[32:35],v[20:23]
s_waitcnt lgkmcnt(4)
v_mfma_f32_16x16x32_bf16 v[20:23],v[100:103],v[40:43],v[20:23]
ds_read_b128 v[32:35],v2 offset:10240
v_mfma_f32_16x16x32_bf16 v[20:23],v[212:215],v[44:47],v[20:23]
buffer_load_dwordx4 v2,s[32:35],s63 offen offset:1024 sc1 lds
v_mfma_f32_16x16x32_bf16 v[16:19],v[208:211],v[44:47],v[16:19]
ds_read_b128 v[36:39],v2 offset:24576
v_mfma_f32_16x16x32_bf16 v[16:19],v[96:99],v[40:43],v[16:19]
s_waitcnt lgkmcnt(4)
v_mfma_f32_16x16x32_bf16 v[16:19],a[72:75],v[48:51],v[16:19]
ds_read_b128 v[40:43],v2 offset:38912
v_mfma_f32_16x16x32_bf16 v[16:19],a[184:187],v[52:55],v[16:19]
buffer_load_dwordx4 v2,s[32:35],s63 offen offset:2048 sc1 lds
v_mfma_f32_16x16x32_bf16 v[20:23],a[188:191],v[52:55],v[20:23]
ds_read_b128 v[44:47],v2 offset:53248
v_mfma_f32_16x16x32_bf16 v[20:23],a[76:79],v[48:51],v[20:23]
s_waitcnt lgkmcnt(4)
v_mfma_f32_16x16x32_bf16 v[20:23],v[108:111],v[56:59],v[20:23]
ds_read_b128 v[48:51],v2 offset:11264
v_mfma_f32_16x16x32_bf16 v[20:23],v[220:223],v[60:63],v[20:23]
v_mfma_f32_16x16x32_bf16 v[16:19],v[216:219],v[60:63],v[16:19]
ds_read_b128 v[52:55],v2 offset:25600
v_mfma_f32_16x16x32_bf16 v[16:19],v[104:107],v[56:59],v[16:19]
s_waitcnt lgkmcnt(4)
v_mfma_f32_16x16x32_bf16 v[16:19],a[80:83],v[32:35],v[16:19]
ds_read_b128 v[56:59],v2 offset:39936
v_mfma_f32_16x16x32_bf16 v[16:19],a[192:195],v[36:39],v[16:19]
buffer_load_dwordx4 v2,s[32:35],s63 offen offset:3072 sc1 lds
s_add_u32 s63,s63,4096
v_mfma_f32_16x16x32_bf16 v[20:23],a[196:199],v[36:39],v[20:23]
ds_read_b128 v[60:63],v2 offset:54272
s_add_u32 m0,s64,69632
v_mfma_f32_16x16x32_bf16 v[20:23],a[84:87],v[32:35],v[20:23]
s_waitcnt lgkmcnt(4)
v_mfma_f32_16x16x32_bf16 v[20:23],v[116:119],v[40:43],v[20:23]
ds_read_b128 v[32:35],v2 offset:12288
v_mfma_f32_16x16x32_bf16 v[20:23],v[228:231],v[44:47],v[20:23]
buffer_load_dwordx4 v2,s[32:35],s63 offen offset:0 sc1 lds
v_mfma_f32_16x16x32_bf16 v[16:19],v[224:227],v[44:47],v[16:19]
ds_read_b128 v[36:39],v2 offset:26624
v_mfma_f32_16x16x32_bf16 v[16:19],v[112:115],v[40:43],v[16:19]
s_waitcnt lgkmcnt(4)
v_mfma_f32_16x16x32_bf16 v[16:19],a[88:91],v[48:51],v[16:19]
ds_read_b128 v[40:43],v2 offset:40960
v_mfma_f32_16x16x32_bf16 v[16:19],a[200:203],v[52:55],v[16:19]
buffer_load_dwordx4 v2,s[32:35],s63 offen offset:1024 sc1 lds
s_add_u32 s63,s63,s31
v_mfma_f32_16x16x32_bf16 v[20:23],a[204:207],v[52:55],v[20:23]
ds_read_b128 v[44:47],v2 offset:55296
v_mfma_f32_16x16x32_bf16 v[20:23],a[92:95],v[48:51],v[20:23]
s_waitcnt lgkmcnt(4)
v_mfma_f32_16x16x32_bf16 v[20:23],v[124:127],v[56:59],v[20:23]
ds_read_b128 v[48:51],v2 offset:13312
s_waitcnt vmcnt(10)
v_mfma_f32_16x16x32_bf16 v[20:23],v[236:239],v[60:63],v[20:23]
v_lshlrev_b32 v24,16,v12
v_and_b32 v25,0xffff0000,v12
v_mfma_f32_16x16x32_bf16 v[16:19],v[232:235],v[60:63],v[16:19]
ds_read_b128 v[52:55],v2 offset:27648
v_lshlrev_b32 v26,16,v13
v_mfma_f32_16x16x32_bf16 v[16:19],v[120:123],v[56:59],v[16:19]
v_and_b32 v27,0xffff0000,v13
s_waitcnt lgkmcnt(4)
v_mfma_f32_16x16x32_bf16 v[16:19],a[96:99],v[32:35],v[16:19]
ds_read_b128 v[56:59],v2 offset:41984
v_lshlrev_b32 v28,16,v14
v_mfma_f32_16x16x32_bf16 v[16:19],a[208:211],v[36:39],v[16:19]
)#"
R"#(
s_barrier
v_mfma_f32_16x16x32_bf16 v[20:23],a[212:215],v[36:39],v[20:23]
ds_read_b128 v[60:63],v2 offset:56320
v_and_b32 v29,0xffff0000,v14
v_mfma_f32_16x16x32_bf16 v[20:23],a[100:103],v[32:35],v[20:23]
v_lshlrev_b32 v30,16,v15
s_waitcnt lgkmcnt(4)
v_mfma_f32_16x16x32_bf16 v[20:23],v[132:135],v[40:43],v[20:23]
ds_read_b128 v[32:35],v4 offset:0
v_and_b32 v31,0xffff0000,v15
v_mfma_f32_16x16x32_bf16 v[20:23],v[244:247],v[44:47],v[20:23]
v_mfma_f32_16x16x32_bf16 v[16:19],v[240:243],v[44:47],v[16:19]
ds_read_b128 v[36:39],v4 offset:14336
v_mfma_f32_16x16x32_bf16 v[16:19],v[128:131],v[40:43],v[16:19]
s_waitcnt lgkmcnt(4)
v_mfma_f32_16x16x32_bf16 v[16:19],a[104:107],v[48:51],v[16:19]
ds_read_b128 v[40:43],v4 offset:28672
v_mfma_f32_16x16x32_bf16 v[16:19],a[216:219],v[52:55],v[16:19]
v_mfma_f32_16x16x32_bf16 v[20:23],a[220:223],v[52:55],v[20:23]
ds_read_b128 v[44:47],v4 offset:43008
v_mfma_f32_16x16x32_bf16 v[20:23],a[108:111],v[48:51],v[20:23]
s_waitcnt lgkmcnt(4)
v_mfma_f32_16x16x32_bf16 v[20:23],v[140:143],v[56:59],v[20:23]
ds_read_b128 v[48:51],v4 offset:1024
v_mfma_f32_16x16x32_bf16 v[20:23],v[252:255],v[60:63],v[20:23]
v_mfma_f32_16x16x32_bf16 v[16:19],v[248:251],v[60:63],v[16:19]
ds_read_b128 v[52:55],v4 offset:15360
s_sub_u32 s62,s62,1
v_mfma_f32_16x16x32_bf16 v[16:19],v[136:139],v[56:59],v[16:19]
s_cbranch_scc0 .L_dml_2
.L_dmu_2:
s_nop 1
s_nop 0
v_cvt_pk_bf16_f32 v15,v22,v23
v_cvt_pk_bf16_f32 v14,v20,v21
s_nop 1
v_cvt_pk_bf16_f32 v13,v18,v19
v_cvt_pk_bf16_f32 v12,v16,v17
ds_write_b128 v5,v[12:15]
ds_read2st64_b32 v[12:13],v3 offset0:0 offset1:1
ds_read2st64_b32 v[14:15],v3 offset0:2 offset1:3
s_waitcnt lgkmcnt(0)
buffer_atomic_pk_add_bf16 v12,v1,s[44:47],s65 offen offset:0 sc1
s_add_u32 s70,s70,s75
buffer_atomic_pk_add_bf16 v13,v1,s[44:47],s65 offen offset:2048 sc1
s_add_u32 s65,s65,16384
buffer_atomic_pk_add_bf16 v14,v1,s[44:47],s65 offen offset:0 sc1
buffer_atomic_pk_add_bf16 v15,v1,s[44:47],s65 offen offset:2048 sc1
s_and_b32 s65,s70,0x3f8000
s_branch .L_end
.L_dmt_2:
s_nop 1
s_nop 0
v_cvt_pk_bf16_f32 v15,v30,v31
v_cvt_pk_bf16_f32 v14,v28,v29
s_nop 1
v_cvt_pk_bf16_f32 v13,v26,v27
v_cvt_pk_bf16_f32 v12,v24,v25
ds_write_b128 v5,v[12:15]
ds_read2st64_b32 v[12:13],v3 offset0:0 offset1:1
ds_read2st64_b32 v[14:15],v3 offset0:2 offset1:3
s_waitcnt lgkmcnt(0)
buffer_atomic_pk_add_bf16 v12,v1,s[44:47],s65 offen offset:0 sc1
s_add_u32 s70,s70,s68
buffer_atomic_pk_add_bf16 v13,v1,s[44:47],s65 offen offset:2048 sc1
s_add_u32 s65,s65,16384
buffer_atomic_pk_add_bf16 v14,v1,s[44:47],s65 offen offset:0 sc1
s_mov_b32 s68,s75
buffer_atomic_pk_add_bf16 v15,v1,s[44:47],s65 offen offset:2048 sc1
s_and_b32 s65,s70,0x3f8000
s_branch .L_end
.p2align 6
.L_dlv3:
.L_dll_3:
s_waitcnt lgkmcnt(4)
v_mfma_f32_16x16x32_bf16 v[16:19],a[0:3],v[32:35],v[16:19]
ds_read_b128 v[56:59],v2 offset:29696
v_mfma_f32_16x16x32_bf16 v[16:19],a[112:115],v[36:39],v[16:19]
v_mfma_f32_16x16x32_bf16 v[20:23],a[116:119],v[36:39],v[20:23]
ds_read_b128 v[60:63],v2 offset:44032
v_mfma_f32_16x16x32_bf16 v[20:23],a[4:7],v[32:35],v[20:23]
s_add_u32 s68,s68,1187840
s_waitcnt lgkmcnt(4)
v_mfma_f32_16x16x32_bf16 v[20:23],a[228:231],v[40:43],v[20:23]
ds_read_b128 v[32:35],v2 offset:2048
v_mfma_f32_16x16x32_bf16 v[20:23],v[148:151],v[44:47],v[20:23]
buffer_store_dwordx4 v[28:31],v2,s[32:35],s68 offen offset:1024 sc1
v_mfma_f32_16x16x32_bf16 v[16:19],v[144:147],v[44:47],v[16:19]
ds_read_b128 v[36:39],v2 offset:16384
v_mfma_f32_16x16x32_bf16 v[16:19],a[224:227],v[40:43],v[16:19]
s_waitcnt lgkmcnt(4)
v_mfma_f32_16x16x32_bf16 v[16:19],a[8:11],v[48:51],v[16:19]
ds_read_b128 v[40:43],v2 offset:30720
v_mfma_f32_16x16x32_bf16 v[16:19],a[120:123],v[52:55],v[16:19]
buffer_store_dwordx4 v[24:27],v2,s[32:35],s68 offen sc1
s_mov_b32 s67,s68
v_mfma_f32_16x16x32_bf16 v[20:23],a[124:127],v[52:55],v[20:23]
ds_read_b128 v[44:47],v2 offset:45056
v_mfma_f32_16x16x32_bf16 v[20:23],a[12:15],v[48:51],v[20:23]
s_waitcnt lgkmcnt(4)
v_mfma_f32_16x16x32_bf16 v[20:23],a[236:239],v[56:59],v[20:23]
ds_read_b128 v[48:51],v2 offset:3072
v_mfma_f32_16x16x32_bf16 v[20:23],v[156:159],v[60:63],v[20:23]
v_mfma_f32_16x16x32_bf16 v[16:19],v[152:155],v[60:63],v[16:19]
ds_read_b128 v[52:55],v2 offset:17408
v_mfma_f32_16x16x32_bf16 v[16:19],a[232:235],v[56:59],v[16:19]
s_waitcnt lgkmcnt(4)
v_mfma_f32_16x16x32_bf16 v[16:19],a[16:19],v[32:35],v[16:19]
s_waitcnt vmcnt(2)
s_barrier
v_mfma_f32_16x16x32_bf16 v[16:19],a[128:131],v[36:39],v[16:19]
ds_read_b128 v[56:59],v2 offset:31744
v_mfma_f32_16x16x32_bf16 v[20:23],a[132:135],v[36:39],v[20:23]
ds_read_b128 v[60:63],v2 offset:46080
v_mfma_f32_16x16x32_bf16 v[20:23],a[20:23],v[32:35],v[20:23]
s_waitcnt lgkmcnt(4)
v_mfma_f32_16x16x32_bf16 v[20:23],a[244:247],v[40:43],v[20:23]
ds_read_b128 v[32:35],v2 offset:4096
s_add_u32 m0,s64,57344
v_mfma_f32_16x16x32_bf16 v[20:23],v[164:167],v[44:47],v[20:23]
buffer_load_dwordx4 v2,s[32:35],s63 offen offset:0 sc1 lds
v_mfma_f32_16x16x32_bf16 v[16:19],v[160:163],v[44:47],v[16:19]
ds_read_b128 v[36:39],v2 offset:18432
v_mfma_f32_16x16x32_bf16 v[16:19],a[240:243],v[40:43],v[16:19]
s_waitcnt lgkmcnt(4)
v_mfma_f32_16x16x32_bf16 v[16:19],a[24:27],v[48:51],v[16:19]
ds_read_b128 v[40:43],v2 offset:32768
v_mfma_f32_16x16x32_bf16 v[16:19],a[136:139],v[52:55],v[16:19]
v_mfma_f32_16x16x32_bf16 v[20:23],a[140:143],v[52:55],v[20:23]
ds_read_b128 v[44:47],v2 offset:47104
v_mfma_f32_16x16x32_bf16 v[20:23],a[28:31],v[48:51],v[20:23]
s_waitcnt lgkmcnt(4)
v_mfma_f32_16x16x32_bf16 v[20:23],a[252:255],v[56:59],v[20:23]
ds_read_b128 v[48:51],v2 offset:5120
v_mfma_f32_16x16x32_bf16 v[20:23],v[172:175],v[60:63],v[20:23]
buffer_load_dwordx4 v2,s[32:35],s63 offen offset:1024 sc1 lds
v_mfma_f32_16x16x32_bf16 v[16:19],v[168:171],v[60:63],v[16:19]
ds_read_b128 v[52:55],v2 offset:19456
v_mfma_f32_16x16x32_bf16 v[16:19],a[248:251],v[56:59],v[16:19]
s_waitcnt lgkmcnt(4)
v_mfma_f32_16x16x32_bf16 v[16:19],a[32:35],v[32:35],v[16:19]
ds_read_b128 v[56:59],v2 offset:33792
v_mfma_f32_16x16x32_bf16 v[16:19],a[144:147],v[36:39],v[16:19]
buffer_load_dwordx4 v2,s[32:35],s63 offen offset:2048 sc1 lds
v_mfma_f32_16x16x32_bf16 v[20:23],a[148:151],v[36:39],v[20:23]
ds_read_b128 v[60:63],v2 offset:48128
v_mfma_f32_16x16x32_bf16 v[20:23],a[36:39],v[32:35],v[20:23]
s_waitcnt lgkmcnt(4)
v_mfma_f32_16x16x32_bf16 v[20:23],v[68:71],v[40:43],v[20:23]
ds_read_b128 v[32:35],v2 offset:6144
v_mfma_f32_16x16x32_bf16 v[20:23],v[180:183],v[44:47],v[20:23]
buffer_load_dwordx4 v2,s[32:35],s63 offen offset:3072 sc1 lds
v_mfma_f32_16x16x32_bf16 v[16:19],v[176:179],v[44:47],v[16:19]
ds_read_b128 v[36:39],v2 offset:20480
v_mfma_f32_16x16x32_bf16 v[16:19],v[64:67],v[40:43],v[16:19]
s_waitcnt lgkmcnt(4)
v_mfma_f32_16x16x32_bf16 v[16:19],a[40:43],v[48:51],v[16:19]
ds_read_b128 v[40:43],v2 offset:34816
v_mfma_f32_16x16x32_bf16 v[16:19],a[152:155],v[52:55],v[16:19]
buffer_load_dwordx4 v[12:15],v2,s[32:35],s71 offen sc1
s_add_u32 s71,s71,s30
v_mfma_f32_16x16x32_bf16 v[20:23],a[156:159],v[52:55],v[20:23]
ds_read_b128 v[44:47],v2 offset:49152
s_add_u32 s63,s63,4096
v_mfma_f32_16x16x32_bf16 v[20:23],a[44:47],v[48:51],v[20:23]
s_add_u32 m0,s64,61440
s_waitcnt lgkmcnt(4)
v_mfma_f32_16x16x32_bf16 v[20:23],v[76:79],v[56:59],v[20:23]
ds_read_b128 v[48:51],v2 offset:7168
v_mfma_f32_16x16x32_bf16 v[20:23],v[188:191],v[60:63],v[20:23]
buffer_load_dwordx4 v2,s[32:35],s63 offen offset:0 sc1 lds
v_mfma_f32_16x16x32_bf16 v[16:19],v[184:187],v[60:63],v[16:19]
ds_read_b128 v[52:55],v2 offset:21504
v_mfma_f32_16x16x32_bf16 v[16:19],v[72:75],v[56:59],v[16:19]
s_waitcnt lgkmcnt(4)
v_mfma_f32_16x16x32_bf16 v[16:19],a[48:51],v[32:35],v[16:19]
ds_read_b128 v[56:59],v2 offset:35840
v_mfma_f32_16x16x32_bf16 v[16:19],a[160:163],v[36:39],v[16:19]
buffer_load_dwordx4 v2,s[32:35],s63 offen offset:1024 sc1 lds
v_mfma_f32_16x16x32_bf16 v[20:23],a[164:167],v[36:39],v[20:23]
ds_read_b128 v[60:63],v2 offset:50176
v_mfma_f32_16x16x32_bf16 v[20:23],a[52:55],v[32:35],v[20:23]
s_waitcnt lgkmcnt(4)
v_mfma_f32_16x16x32_bf16 v[20:23],v[84:87],v[40:43],v[20:23]
ds_read_b128 v[32:35],v2 offset:8192
v_mfma_f32_16x16x32_bf16 v[20:23],v[196:199],v[44:47],v[20:23]
buffer_load_dwordx4 v2,s[32:35],s63 offen offset:2048 sc1 lds
v_mfma_f32_16x16x32_bf16 v[16:19],v[192:195],v[44:47],v[16:19]
ds_read_b128 v[36:39],v2 offset:22528
v_mfma_f32_16x16x32_bf16 v[16:19],v[80:83],v[40:43],v[16:19]
s_waitcnt lgkmcnt(4)
v_mfma_f32_16x16x32_bf16 v[16:19],a[56:59],v[48:51],v[16:19]
ds_read_b128 v[40:43],v2 offset:36864
v_mfma_f32_16x16x32_bf16 v[16:19],a[168:171],v[52:55],v[16:19]
buffer_load_dwordx4 v2,s[32:35],s63 offen offset:3072 sc1 lds
v_mfma_f32_16x16x32_bf16 v[20:23],a[172:175],v[52:55],v[20:23]
ds_read_b128 v[44:47],v2 offset:51200
v_mfma_f32_16x16x32_bf16 v[20:23],a[60:63],v[48:51],v[20:23]
s_waitcnt lgkmcnt(4)
v_mfma_f32_16x16x32_bf16 v[20:23],v[92:95],v[56:59],v[20:23]
ds_read_b128 v[48:51],v2 offset:9216
v_mfma_f32_16x16x32_bf16 v[20:23],v[204:207],v[60:63],v[20:23]
s_add_u32 s63,s63,4096
s_add_u32 m0,s64,65536
v_mfma_f32_16x16x32_bf16 v[16:19],v[200:203],v[60:63],v[16:19]
ds_read_b128 v[52:55],v2 offset:23552
v_mfma_f32_16x16x32_bf16 v[16:19],v[88:91],v[56:59],v[16:19]
s_waitcnt lgkmcnt(4)
v_mfma_f32_16x16x32_bf16 v[16:19],a[64:67],v[32:35],v[16:19]
ds_read_b128 v[56:59],v2 offset:37888
v_mfma_f32_16x16x32_bf16 v[16:19],a[176:179],v[36:39],v[16:19]
buffer_load_dwordx4 v2,s[32:35],s63 offen offset:0 sc1 lds
v_mfma_f32_16x16x32_bf16 v[20:23],a[180:183],v[36:39],v[20:23]
ds_read_b128 v[60:63],v2 offset:52224
v_mfma_f32_16x16x32_bf16 v[20:23],a[68:71],v[32:35],v[20:23]
s_waitcnt lgkmcnt(4)
v_mfma_f32_16x16x32_bf16 v[20:23],v[100:103],v[40:43],v[20:23]
ds_read_b128 v[32:35],v2 offset:10240
v_mfma_f32_16x16x32_bf16 v[20:23],v[212:215],v[44:47],v[20:23]
buffer_load_dwordx4 v2,s[32:35],s63 offen offset:1024 sc1 lds
v_mfma_f32_16x16x32_bf16 v[16:19],v[208:211],v[44:47],v[16:19]
ds_read_b128 v[36:39],v2 offset:24576
v_mfma_f32_16x16x32_bf16 v[16:19],v[96:99],v[40:43],v[16:19]
s_waitcnt lgkmcnt(4)
v_mfma_f32_16x16x32_bf16 v[16:19],a[72:75],v[48:51],v[16:19]
ds_read_b128 v[40:43],v2 offset:38912
v_mfma_f32_16x16x32_bf16 v[16:19],a[184:187],v[52:55],v[16:19]
buffer_load_dwordx4 v2,s[32:35],s63 offen offset:2048 sc1 lds
v_mfma_f32_16x16x32_bf16 v[20:23],a[188:191],v[52:55],v[20:23]
ds_read_b128 v[44:47],v2 offset:53248
v_mfma_f32_16x16x32_bf16 v[20:23],a[76:79],v[48:51],v[20:23]
s_waitcnt lgkmcnt(4)
v_mfma_f32_16x16x32_bf16 v[20:23],v[108:111],v[56:59],v[20:23]
ds_read_b128 v[48:51],v2 offset:11264
v_mfma_f32_16x16x32_bf16 v[20:23],v[220:223],v[60:63],v[20:23]
v_mfma_f32_16x16x32_bf16 v[16:19],v[216:219],v[60:63],v[16:19]
ds_read_b128 v[52:55],v2 offset:25600
v_mfma_f32_16x16x32_bf16 v[16:19],v[104:107],v[56:59],v[16:19]
s_waitcnt lgkmcnt(4)
v_mfma_f32_16x16x32_bf16 v[16:19],a[80:83],v[32:35],v[16:19]
ds_read_b128 v[56:59],v2 offset:39936
v_mfma_f32_16x16x32_bf16 v[16:19],a[192:195],v[36:39],v[16:19]
buffer_load_dwordx4 v2,s[32:35],s63 offen offset:3072 sc1 lds
s_add_u32 s63,s63,4096
v_mfma_f32_16x16x32_bf16 v[20:23],a[196:199],v[36:39],v[20:23]
ds_read_b128 v[60:63],v2 offset:54272
s_add_u32 m0,s64,69632
v_mfma_f32_16x16x32_bf16 v[20:23],a[84:87],v[32:35],v[20:23]
s_waitcnt lgkmcnt(4)
v_mfma_f32_16x16x32_bf16 v[20:23],v[116:119],v[40:43],v[20:23]
ds_read_b128 v[32:35],v2 offset:12288
v_mfma_f32_16x16x32_bf16 v[20:23],v[228:231],v[44:47],v[20:23]
buffer_load_dwordx4 v2,s[32:35],s63 offen offset:0 sc1 lds
v_mfma_f32_16x16x32_bf16 v[16:19],v[224:227],v[44:47],v[16:19]
ds_read_b128 v[36:39],v2 offset:26624
v_mfma_f32_16x16x32_bf16 v[16:19],v[112:115],v[40:43],v[16:19]
s_waitcnt lgkmcnt(4)
v_mfma_f32_16x16x32_bf16 v[16:19],a[88:91],v[48:51],v[16:19]
ds_read_b128 v[40:43],v2 offset:40960
v_mfma_f32_16x16x32_bf16 v[16:19],a[200:203],v[52:55],v[16:19]
buffer_load_dwordx4 v2,s[32:35],s63 offen offset:1024 sc1 lds
s_add_u32 s63,s63,s31
v_mfma_f32_16x16x32_bf16 v[20:23],a[204:207],v[52:55],v[20:23]
ds_read_b128 v[44:47],v2 offset:55296
v_mfma_f32_16x16x32_bf16 v[20:23],a[92:95],v[48:51],v[20:23]
s_waitcnt lgkmcnt(4)
v_mfma_f32_16x16x32_bf16 v[20:23],v[124:127],v[56:59],v[20:23]
ds_read_b128 v[48:51],v2 offset:13312
s_waitcnt vmcnt(10)
v_mfma_f32_16x16x32_bf16 v[20:23],v[236:239],v[60:63],v[20:23]
v_lshlrev_b32 v24,16,v12
v_and_b32 v25,0xffff0000,v12
v_mfma_f32_16x16x32_bf16 v[16:19],v[232:235],v[60:63],v[16:19]
ds_read_b128 v[52:55],v2 offset:27648
v_lshlrev_b32 v26,16,v13
v_mfma_f32_16x16x32_bf16 v[16:19],v[120:123],v[56:59],v[16:19]
v_and_b32 v27,0xffff0000,v13
s_waitcnt lgkmcnt(4)
v_mfma_f32_16x16x32_bf16 v[16:19],a[96:99],v[32:35],v[16:19]
s_barrier
v_mfma_f32_16x16x32_bf16 v[16:19],a[208:211],v[36:39],v[16:19]
ds_read_b128 v[56:59],v2 offset:41984
v_lshlrev_b32 v28,16,v14
v_mfma_f32_16x16x32_bf16 v[20:23],a[212:215],v[36:39],v[20:23]
ds_read_b128 v[60:63],v2 offset:56320
v_and_b32 v29,0xffff0000,v14
v_mfma_f32_16x16x32_bf16 v[20:23],a[100:103],v[32:35],v[20:23]
v_lshlrev_b32 v30,16,v15
s_waitcnt lgkmcnt(4)
v_mfma_f32_16x16x32_bf16 v[20:23],v[132:135],v[40:43],v[20:23]
ds_read_b128 v[32:35],v4 offset:0
v_and_b32 v31,0xffff0000,v15
v_mfma_f32_16x16x32_bf16 v[20:23],v[244:247],v[44:47],v[20:23]
v_mfma_f32_16x16x32_bf16 v[16:19],v[240:243],v[44:47],v[16:19]
ds_read_b128 v[36:39],v4 offset:14336
v_mfma_f32_16x16x32_bf16 v[16:19],v[128:131],v[40:43],v[16:19]
s_waitcnt lgkmcnt(4)
v_mfma_f32_16x16x32_bf16 v[16:19],a[104:107],v[48:51],v[16:19]
ds_read_b128 v[40:43],v4 offset:28672
v_mfma_f32_16x16x32_bf16 v[16:19],a[216:219],v[52:55],v[16:19]
v_mfma_f32_16x16x32_bf16 v[20:23],a[220:223],v[52:55],v[20:23]
ds_read_b128 v[44:47],v4 offset:43008
v_mfma_f32_16x16x32_bf16 v[20:23],a[108:111],v[48:51],v[20:23]
s_waitcnt lgkmcnt(4)
v_mfma_f32_16x16x32_bf16 v[20:23],v[140:143],v[56:59],v[20:23]
ds_read_b128 v[48:51],v4 offset:1024
v_mfma_f32_16x16x32_bf16 v[20:23],v[252:255],v[60:63],v[20:23]
v_mfma_f32_16x16x32_bf16 v[16:19],v[248:251],v[60:63],v[16:19]
ds_read_b128 v[52:55],v4 offset:15360
s_sub_u32 s62,s62,1
v_mfma_f32_16x16x32_bf16 v[16:19],v[136:139],v[56:59],v[16:19]
s_cbranch_scc1 .L_dlb_3
s_waitcnt lgkmcnt(4)
v_mfma_f32_16x16x32_bf16 v[24:27],a[0:3],v[32:35],v[24:27]
ds_read_b128 v[56:59],v4 offset:29696
v_mfma_f32_16x16x32_bf16 v[24:27],a[112:115],v[36:39],v[24:27]
v_mfma_f32_16x16x32_bf16 v[28:31],a[116:119],v[36:39],v[28:31]
ds_read_b128 v[60:63],v4 offset:44032
v_mfma_f32_16x16x32_bf16 v[28:31],a[4:7],v[32:35],v[28:31]
s_add_u32 s68,s68,1187840
s_waitcnt lgkmcnt(4)
v_mfma_f32_16x16x32_bf16 v[28:31],a[228:231],v[40:43],v[28:31]
ds_read_b128 v[32:35],v4 offset:2048
v_mfma_f32_16x16x32_bf16 v[28:31],v[148:151],v[44:47],v[28:31]
buffer_store_dwordx4 v[20:23],v2,s[32:35],s68 offen offset:1024 sc1
v_mfma_f32_16x16x32_bf16 v[24:27],v[144:147],v[44:47],v[24:27]
ds_read_b128 v[36:39],v4 offset:16384
v_mfma_f32_16x16x32_bf16 v[24:27],a[224:227],v[40:43],v[24:27]
s_waitcnt lgkmcnt(4)
v_mfma_f32_16x16x32_bf16 v[24:27],a[8:11],v[48:51],v[24:27]
ds_read_b128 v[40:43],v4 offset:30720
v_mfma_f32_16x16x32_bf16 v[24:27],a[120:123],v[52:55],v[24:27]
buffer_store_dwordx4 v[16:19],v2,s[32:35],s68 offen sc1
s_mov_b32 s67,s68
v_mfma_f32_16x16x32_bf16 v[28:31],a[124:127],v[52:55],v[28:31]
ds_read_b128 v[44:47],v4 offset:45056
v_mfma_f32_16x16x32_bf16 v[28:31],a[12:15],v[48:51],v[28:31]
s_waitcnt lgkmcnt(4)
v_mfma_f32_16x16x32_bf16 v[28:31],a[236:239],v[56:59],v[28:31]
ds_read_b128 v[48:51],v4 offset:3072
v_mfma_f32_16x16x32_bf16 v[28:31],v[156:159],v[60:63],v[28:31]
v_mfma_f32_16x16x32_bf16 v[24:27],v[152:155],v[60:63],v[24:27]
ds_read_b128 v[52:55],v4 offset:17408
v_mfma_f32_16x16x32_bf16 v[24:27],a[232:235],v[56:59],v[24:27]
s_waitcnt lgkmcnt(4)
v_mfma_f32_16x16x32_bf16 v[24:27],a[16:19],v[32:35],v[24:27]
s_waitcnt vmcnt(2)
s_barrier
v_mfma_f32_16x16x32_bf16 v[24:27],a[128:131],v[36:39],v[24:27]
ds_read_b128 v[56:59],v4 offset:31744
v_mfma_f32_16x16x32_bf16 v[28:31],a[132:135],v[36:39],v[28:31]
ds_read_b128 v[60:63],v4 offset:46080
v_mfma_f32_16x16x32_bf16 v[28:31],a[20:23],v[32:35],v[28:31]
s_waitcnt lgkmcnt(4)
v_mfma_f32_16x16x32_bf16 v[28:31],a[244:247],v[40:43],v[28:31]
ds_read_b128 v[32:35],v4 offset:4096
s_add_u32 m0,s64,0
v_mfma_f32_16x16x32_bf16 v[28:31],v[164:167],v[44:47],v[28:31]
buffer_load_dwordx4 v2,s[32:35],s63 offen offset:0 sc1 lds
v_mfma_f32_16x16x32_bf16 v[24:27],v[160:163],v[44:47],v[24:27]
ds_read_b128 v[36:39],v4 offset:18432
v_mfma_f32_16x16x32_bf16 v[24:27],a[240:243],v[40:43],v[24:27]
s_waitcnt lgkmcnt(4)
v_mfma_f32_16x16x32_bf16 v[24:27],a[24:27],v[48:51],v[24:27]
ds_read_b128 v[40:43],v4 offset:32768
v_mfma_f32_16x16x32_bf16 v[24:27],a[136:139],v[52:55],v[24:27]
v_mfma_f32_16x16x32_bf16 v[28:31],a[140:143],v[52:55],v[28:31]
ds_read_b128 v[44:47],v4 offset:47104
v_mfma_f32_16x16x32_bf16 v[28:31],a[28:31],v[48:51],v[28:31]
s_waitcnt lgkmcnt(4)
v_mfma_f32_16x16x32_bf16 v[28:31],a[252:255],v[56:59],v[28:31]
ds_read_b128 v[48:51],v4 offset:5120
v_mfma_f32_16x16x32_bf16 v[28:31],v[172:175],v[60:63],v[28:31]
buffer_load_dwordx4 v2,s[32:35],s63 offen offset:1024 sc1 lds
v_mfma_f32_16x16x32_bf16 v[24:27],v[168:171],v[60:63],v[24:27]
ds_read_b128 v[52:55],v4 offset:19456
v_mfma_f32_16x16x32_bf16 v[24:27],a[248:251],v[56:59],v[24:27]
s_waitcnt lgkmcnt(4)
v_mfma_f32_16x16x32_bf16 v[24:27],a[32:35],v[32:35],v[24:27]
ds_read_b128 v[56:59],v4 offset:33792
v_mfma_f32_16x16x32_bf16 v[24:27],a[144:147],v[36:39],v[24:27]
buffer_load_dwordx4 v2,s[32:35],s63 offen offset:2048 sc1 lds
v_mfma_f32_16x16x32_bf16 v[28:31],a[148:151],v[36:39],v[28:31]
ds_read_b128 v[60:63],v4 offset:48128
v_mfma_f32_16x16x32_bf16 v[28:31],a[36:39],v[32:35],v[28:31]
s_waitcnt lgkmcnt(4)
v_mfma_f32_16x16x32_bf16 v[28:31],v[68:71],v[40:43],v[28:31]
ds_read_b128 v[32:35],v4 offset:6144
v_mfma_f32_16x16x32_bf16 v[28:31],v[180:183],v[44:47],v[28:31]
buffer_load_dwordx4 v2,s[32:35],s63 offen offset:3072 sc1 lds
v_mfma_f32_16x16x32_bf16 v[24:27],v[176:179],v[44:47],v[24:27]
ds_read_b128 v[36:39],v4 offset:20480
v_mfma_f32_16x16x32_bf16 v[24:27],v[64:67],v[40:43],v[24:27]
s_waitcnt lgkmcnt(4)
v_mfma_f32_16x16x32_bf16 v[24:27],a[40:43],v[48:51],v[24:27]
ds_read_b128 v[40:43],v4 offset:34816
v_mfma_f32_16x16x32_bf16 v[24:27],a[152:155],v[52:55],v[24:27]
buffer_load_dwordx4 v[12:15],v2,s[32:35],s71 offen sc1
s_add_u32 s71,s71,s30
v_mfma_f32_16x16x32_bf16 v[28:31],a[156:159],v[52:55],v[28:31]
ds_read_b128 v[44:47],v4 offset:49152
s_add_u32 s63,s63,4096
v_mfma_f32_16x16x32_bf16 v[28:31],a[44:47],v[48:51],v[28:31]
s_add_u32 m0,s64,4096
s_waitcnt lgkmcnt(4)
v_mfma_f32_16x16x32_bf16 v[28:31],v[76:79],v[56:59],v[28:31]
ds_read_b128 v[48:51],v4 offset:7168
v_mfma_f32_16x16x32_bf16 v[28:31],v[188:191],v[60:63],v[28:31]
buffer_load_dwordx4 v2,s[32:35],s63 offen offset:0 sc1 lds
v_mfma_f32_16x16x32_bf16 v[24:27],v[184:187],v[60:63],v[24:27]
ds_read_b128 v[52:55],v4 offset:21504
v_mfma_f32_16x16x32_bf16 v[24:27],v[72:75],v[56:59],v[24:27]
s_waitcnt lgkmcnt(4)
v_mfma_f32_16x16x32_bf16 v[24:27],a[48:51],v[32:35],v[24:27]
ds_read_b128 v[56:59],v4 offset:35840
v_mfma_f32_16x16x32_bf16 v[24:27],a[160:163],v[36:39],v[24:27]
buffer_load_dwordx4 v2,s[32:35],s63 offen offset:1024 sc1 lds
v_mfma_f32_16x16x32_bf16 v[28:31],a[164:167],v[36:39],v[28:31]
ds_read_b128 v[60:63],v4 offset:50176
v_mfma_f32_16x16x32_bf16 v[28:31],a[52:55],v[32:35],v[28:31]
s_waitcnt lgkmcnt(4)
v_mfma_f32_16x16x32_bf16 v[28:31],v[84:87],v[40:43],v[28:31]
ds_read_b128 v[32:35],v4 offset:8192
v_mfma_f32_16x16x32_bf16 v[28:31],v[196:199],v[44:47],v[28:31]
buffer_load_dwordx4 v2,s[32:35],s63 offen offset:2048 sc1 lds
v_mfma_f32_16x16x32_bf16 v[24:27],v[192:195],v[44:47],v[24:27]
ds_read_b128 v[36:39],v4 offset:22528
v_mfma_f32_16x16x32_bf16 v[24:27],v[80:83],v[40:43],v[24:27]
s_waitcnt lgkmcnt(4)
v_mfma_f32_16x16x32_bf16 v[24:27],a[56:59],v[48:51],v[24:27]
ds_read_b128 v[40:43],v4 offset:36864
v_mfma_f32_16x16x32_bf16 v[24:27],a[168:171],v[52:55],v[24:27]
buffer_load_dwordx4 v2,s[32:35],s63 offen offset:3072 sc1 lds
v_mfma_f32_16x16x32_bf16 v[28:31],a[172:175],v[52:55],v[28:31]
ds_read_b128 v[44:47],v4 offset:51200
v_mfma_f32_16x16x32_bf16 v[28:31],a[60:63],v[48:51],v[28:31]
s_waitcnt lgkmcnt(4)
v_mfma_f32_16x16x32_bf16 v[28:31],v[92:95],v[56:59],v[28:31]
ds_read_b128 v[48:51],v4 offset:9216
v_mfma_f32_16x16x32_bf16 v[28:31],v[204:207],v[60:63],v[28:31]
s_add_u32 s63,s63,4096
s_add_u32 m0,s64,8192
v_mfma_f32_16x16x32_bf16 v[24:27],v[200:203],v[60:63],v[24:27]
ds_read_b128 v[52:55],v4 offset:23552
v_mfma_f32_16x16x32_bf16 v[24:27],v[88:91],v[56:59],v[24:27]
s_waitcnt lgkmcnt(4)
v_mfma_f32_16x16x32_bf16 v[24:27],a[64:67],v[32:35],v[24:27]
ds_read_b128 v[56:59],v4 offset:37888
v_mfma_f32_16x16x32_bf16 v[24:27],a[176:179],v[36:39],v[24:27]
buffer_load_dwordx4 v2,s[32:35],s63 offen offset:0 sc1 lds
v_mfma_f32_16x16x32_bf16 v[28:31],a[180:183],v[36:39],v[28:31]
ds_read_b128 v[60:63],v4 offset:52224
v_mfma_f32_16x16x32_bf16 v[28:31],a[68:71],v[32:35],v[28:31]
s_waitcnt lgkmcnt(4)
v_mfma_f32_16x16x32_bf16 v[28:31],v[100:103],v[40:43],v[28:31]
ds_read_b128 v[32:35],v4 offset:10240
v_mfma_f32_16x16x32_bf16 v[28:31],v[212:215],v[44:47],v[28:31]
buffer_load_dwordx4 v2,s[32:35],s63 offen offset:1024 sc1 lds
v_mfma_f32_16x16x32_bf16 v[24:27],v[208:211],v[44:47],v[24:27]
ds_read_b128 v[36:39],v4 offset:24576
v_mfma_f32_16x16x32_bf16 v[24:27],v[96:99],v[40:43],v[24:27]
s_waitcnt lgkmcnt(4)
v_mfma_f32_16x16x32_bf16 v[24:27],a[72:75],v[48:51],v[24:27]
ds_read_b128 v[40:43],v4 offset:38912
v_mfma_f32_16x16x32_bf16 v[24:27],a[184:187],v[52:55],v[24:27]
buffer_load_dwordx4 v2,s[32:35],s63 offen offset:2048 sc1 lds
v_mfma_f32_16x16x32_bf16 v[28:31],a[188:191],v[52:55],v[28:31]
ds_read_b128 v[44:47],v4 offset:53248
v_mfma_f32_16x16x32_bf16 v[28:31],a[76:79],v[48:51],v[28:31]
s_waitcnt lgkmcnt(4)
v_mfma_f32_16x16x32_bf16 v[28:31],v[108:111],v[56:59],v[28:31]
ds_read_b128 v[48:51],v4 offset:11264
v_mfma_f32_16x16x32_bf16 v[28:31],v[220:223],v[60:63],v[28:31]
v_mfma_f32_16x16x32_bf16 v[24:27],v[216:219],v[60:63],v[24:27]
ds_read_b128 v[52:55],v4 offset:25600
v_mfma_f32_16x16x32_bf16 v[24:27],v[104:107],v[56:59],v[24:27]
s_waitcnt lgkmcnt(4)
v_mfma_f32_16x16x32_bf16 v[24:27],a[80:83],v[32:35],v[24:27]
ds_read_b128 v[56:59],v4 offset:39936
v_mfma_f32_16x16x32_bf16 v[24:27],a[192:195],v[36:39],v[24:27]
buffer_load_dwordx4 v2,s[32:35],s63 offen offset:3072 sc1 lds
s_add_u32 s63,s63,4096
v_mfma_f32_16x16x32_bf16 v[28:31],a[196:199],v[36:39],v[28:31]
ds_read_b128 v[60:63],v4 offset:54272
s_add_u32 m0,s64,12288
v_mfma_f32_16x16x32_bf16 v[28:31],a[84:87],v[32:35],v[28:31]
s_waitcnt lgkmcnt(4)
v_mfma_f32_16x16x32_bf16 v[28:31],v[116:119],v[40:43],v[28:31]
ds_read_b128 v[32:35],v4 offset:12288
v_mfma_f32_16x16x32_bf16 v[28:31],v[228:231],v[44:47],v[28:31]
buffer_load_dwordx4 v2,s[32:35],s63 offen offset:0 sc1 lds
v_mfma_f32_16x16x32_bf16 v[24:27],v[224:227],v[44:47],v[24:27]
ds_read_b128 v[36:39],v4 offset:26624
v_mfma_f32_16x16x32_bf16 v[24:27],v[112:115],v[40:43],v[24:27]
s_waitcnt lgkmcnt(4)
v_mfma_f32_16x16x32_bf16 v[24:27],a[88:91],v[48:51],v[24:27]
ds_read_b128 v[40:43],v4 offset:40960
v_mfma_f32_16x16x32_bf16 v[24:27],a[200:203],v[52:55],v[24:27]
buffer_load_dwordx4 v2,s[32:35],s63 offen offset:1024 sc1 lds
s_add_u32 s63,s63,s31
)#"
R"#(
v_mfma_f32_16x16x32_bf16 v[28:31],a[204:207],v[52:55],v[28:31]
ds_read_b128 v[44:47],v4 offset:55296
v_mfma_f32_16x16x32_bf16 v[28:31],a[92:95],v[48:51],v[28:31]
s_waitcnt lgkmcnt(4)
v_mfma_f32_16x16x32_bf16 v[28:31],v[124:127],v[56:59],v[28:31]
ds_read_b128 v[48:51],v4 offset:13312
s_waitcnt vmcnt(10)
v_mfma_f32_16x16x32_bf16 v[28:31],v[236:239],v[60:63],v[28:31]
v_lshlrev_b32 v16,16,v12
v_and_b32 v17,0xffff0000,v12
v_mfma_f32_16x16x32_bf16 v[24:27],v[232:235],v[60:63],v[24:27]
ds_read_b128 v[52:55],v4 offset:27648
v_lshlrev_b32 v18,16,v13
v_mfma_f32_16x16x32_bf16 v[24:27],v[120:123],v[56:59],v[24:27]
v_and_b32 v19,0xffff0000,v13
s_waitcnt lgkmcnt(4)
v_mfma_f32_16x16x32_bf16 v[24:27],a[96:99],v[32:35],v[24:27]
s_barrier
v_mfma_f32_16x16x32_bf16 v[24:27],a[208:211],v[36:39],v[24:27]
ds_read_b128 v[56:59],v4 offset:41984
v_lshlrev_b32 v20,16,v14
v_mfma_f32_16x16x32_bf16 v[28:31],a[212:215],v[36:39],v[28:31]
ds_read_b128 v[60:63],v4 offset:56320
v_and_b32 v21,0xffff0000,v14
v_mfma_f32_16x16x32_bf16 v[28:31],a[100:103],v[32:35],v[28:31]
v_lshlrev_b32 v22,16,v15
s_waitcnt lgkmcnt(4)
v_mfma_f32_16x16x32_bf16 v[28:31],v[132:135],v[40:43],v[28:31]
ds_read_b128 v[32:35],v2 offset:0
v_and_b32 v23,0xffff0000,v15
v_mfma_f32_16x16x32_bf16 v[28:31],v[244:247],v[44:47],v[28:31]
v_mfma_f32_16x16x32_bf16 v[24:27],v[240:243],v[44:47],v[24:27]
ds_read_b128 v[36:39],v2 offset:14336
v_mfma_f32_16x16x32_bf16 v[24:27],v[128:131],v[40:43],v[24:27]
s_waitcnt lgkmcnt(4)
v_mfma_f32_16x16x32_bf16 v[24:27],a[104:107],v[48:51],v[24:27]
ds_read_b128 v[40:43],v2 offset:28672
v_mfma_f32_16x16x32_bf16 v[24:27],a[216:219],v[52:55],v[24:27]
v_mfma_f32_16x16x32_bf16 v[28:31],a[220:223],v[52:55],v[28:31]
ds_read_b128 v[44:47],v2 offset:43008
v_mfma_f32_16x16x32_bf16 v[28:31],a[108:111],v[48:51],v[28:31]
s_waitcnt lgkmcnt(4)
v_mfma_f32_16x16x32_bf16 v[28:31],v[140:143],v[56:59],v[28:31]
ds_read_b128 v[48:51],v2 offset:1024
v_mfma_f32_16x16x32_bf16 v[28:31],v[252:255],v[60:63],v[28:31]
v_mfma_f32_16x16x32_bf16 v[24:27],v[248:251],v[60:63],v[24:27]
ds_read_b128 v[52:55],v2 offset:15360
v_mfma_f32_16x16x32_bf16 v[24:27],v[136:139],v[56:59],v[24:27]
s_branch .L_dll_3
.L_dlb_3:
s_mov_b32 s62,s58
s_mov_b32 s70,s59
s_add_u32 s74,s74,8192
v_lshrrev_b32 v5,8,v2
v_lshl_add_u32 v5,v5,4,s74
v_and_b32 v3,128,v2
v_lshl_add_u32 v5,v3,2,v5
v_and_b32 v3,16,v2
v_lshl_add_u32 v5,v3,4,v5
v_and_b32 v3,96,v2
v_lshl_add_u32 v5,v3,1,v5
v_lshrrev_b32 v3,2,v2
v_add_u32 v3,s74,v3
s_add_u32 s66,s66,2375680
s_add_u32 s63,s63,2375680
s_add_u32 s71,s71,2375680
s_mov_b32 s68,98304
s_mov_b32 s34,79446016
.p2align 6
.L_dml_3:
s_waitcnt lgkmcnt(4)
v_mfma_f32_16x16x32_bf16 v[24:27],a[0:3],v[32:35],v[24:27]
ds_read_b128 v[56:59],v4 offset:29696
v_mfma_f32_16x16x32_bf16 v[24:27],a[112:115],v[36:39],v[24:27]
v_mfma_f32_16x16x32_bf16 v[28:31],a[116:119],v[36:39],v[28:31]
ds_read_b128 v[60:63],v4 offset:44032
v_mfma_f32_16x16x32_bf16 v[28:31],a[4:7],v[32:35],v[28:31]
v_cvt_pk_bf16_f32 v15,v22,v23
s_waitcnt lgkmcnt(4)
v_mfma_f32_16x16x32_bf16 v[28:31],a[228:231],v[40:43],v[28:31]
ds_read_b128 v[32:35],v4 offset:2048
v_cvt_pk_bf16_f32 v14,v20,v21
v_mfma_f32_16x16x32_bf16 v[28:31],v[148:151],v[44:47],v[28:31]
v_cvt_pk_bf16_f32 v13,v18,v19
v_cvt_pk_bf16_f32 v12,v16,v17
v_mfma_f32_16x16x32_bf16 v[24:27],v[144:147],v[44:47],v[24:27]
ds_read_b128 v[36:39],v4 offset:16384
ds_write_b128 v5,v[12:15]
v_mfma_f32_16x16x32_bf16 v[24:27],a[224:227],v[40:43],v[24:27]
ds_read2st64_b32 v[12:13],v3 offset0:0 offset1:1
s_waitcnt lgkmcnt(6)
v_mfma_f32_16x16x32_bf16 v[24:27],a[8:11],v[48:51],v[24:27]
ds_read_b128 v[40:43],v4 offset:30720
ds_read2st64_b32 v[14:15],v3 offset0:2 offset1:3
v_mfma_f32_16x16x32_bf16 v[24:27],a[120:123],v[52:55],v[24:27]
v_mfma_f32_16x16x32_bf16 v[28:31],a[124:127],v[52:55],v[28:31]
ds_read_b128 v[44:47],v4 offset:45056
v_mfma_f32_16x16x32_bf16 v[28:31],a[12:15],v[48:51],v[28:31]
s_waitcnt lgkmcnt(7)
v_mfma_f32_16x16x32_bf16 v[28:31],a[236:239],v[56:59],v[28:31]
ds_read_b128 v[48:51],v4 offset:3072
v_mfma_f32_16x16x32_bf16 v[28:31],v[156:159],v[60:63],v[28:31]
s_waitcnt lgkmcnt(2)
buffer_atomic_pk_add_bf16 v12,v1,s[44:47],s65 offen offset:0 sc1
v_mfma_f32_16x16x32_bf16 v[24:27],v[152:155],v[60:63],v[24:27]
ds_read_b128 v[52:55],v4 offset:17408
s_add_u32 s70,s70,s75
v_mfma_f32_16x16x32_bf16 v[24:27],a[232:235],v[56:59],v[24:27]
buffer_atomic_pk_add_bf16 v13,v1,s[44:47],s65 offen offset:2048 sc1
v_mfma_f32_16x16x32_bf16 v[24:27],a[16:19],v[32:35],v[24:27]
s_waitcnt vmcnt(2)
s_barrier
v_mfma_f32_16x16x32_bf16 v[24:27],a[128:131],v[36:39],v[24:27]
ds_read_b128 v[56:59],v4 offset:31744
s_add_u32 s65,s65,16384
v_mfma_f32_16x16x32_bf16 v[28:31],a[132:135],v[36:39],v[28:31]
ds_read_b128 v[60:63],v4 offset:46080
v_mfma_f32_16x16x32_bf16 v[28:31],a[20:23],v[32:35],v[28:31]
buffer_atomic_pk_add_bf16 v14,v1,s[44:47],s65 offen offset:0 sc1
s_waitcnt lgkmcnt(4)
v_mfma_f32_16x16x32_bf16 v[28:31],a[244:247],v[40:43],v[28:31]
ds_read_b128 v[32:35],v4 offset:4096
s_add_u32 m0,s64,0
v_mfma_f32_16x16x32_bf16 v[28:31],v[164:167],v[44:47],v[28:31]
buffer_atomic_pk_add_bf16 v15,v1,s[44:47],s65 offen offset:2048 sc1
s_and_b32 s65,s70,0x3f8000
v_mfma_f32_16x16x32_bf16 v[24:27],v[160:163],v[44:47],v[24:27]
ds_read_b128 v[36:39],v4 offset:18432
v_mfma_f32_16x16x32_bf16 v[24:27],a[240:243],v[40:43],v[24:27]
s_waitcnt lgkmcnt(4)
v_mfma_f32_16x16x32_bf16 v[24:27],a[24:27],v[48:51],v[24:27]
ds_read_b128 v[40:43],v4 offset:32768
v_mfma_f32_16x16x32_bf16 v[24:27],a[136:139],v[52:55],v[24:27]
buffer_load_dwordx4 v2,s[32:35],s63 offen offset:0 sc1 lds
v_mfma_f32_16x16x32_bf16 v[28:31],a[140:143],v[52:55],v[28:31]
ds_read_b128 v[44:47],v4 offset:47104
v_mfma_f32_16x16x32_bf16 v[28:31],a[28:31],v[48:51],v[28:31]
s_waitcnt lgkmcnt(4)
v_mfma_f32_16x16x32_bf16 v[28:31],a[252:255],v[56:59],v[28:31]
ds_read_b128 v[48:51],v4 offset:5120
v_mfma_f32_16x16x32_bf16 v[28:31],v[172:175],v[60:63],v[28:31]
buffer_load_dwordx4 v2,s[32:35],s63 offen offset:1024 sc1 lds
v_mfma_f32_16x16x32_bf16 v[24:27],v[168:171],v[60:63],v[24:27]
ds_read_b128 v[52:55],v4 offset:19456
v_mfma_f32_16x16x32_bf16 v[24:27],a[248:251],v[56:59],v[24:27]
s_waitcnt lgkmcnt(4)
v_mfma_f32_16x16x32_bf16 v[24:27],a[32:35],v[32:35],v[24:27]
ds_read_b128 v[56:59],v4 offset:33792
v_mfma_f32_16x16x32_bf16 v[24:27],a[144:147],v[36:39],v[24:27]
buffer_load_dwordx4 v2,s[32:35],s63 offen offset:2048 sc1 lds
v_mfma_f32_16x16x32_bf16 v[28:31],a[148:151],v[36:39],v[28:31]
ds_read_b128 v[60:63],v4 offset:48128
v_mfma_f32_16x16x32_bf16 v[28:31],a[36:39],v[32:35],v[28:31]
s_waitcnt lgkmcnt(4)
v_mfma_f32_16x16x32_bf16 v[28:31],v[68:71],v[40:43],v[28:31]
ds_read_b128 v[32:35],v4 offset:6144
v_mfma_f32_16x16x32_bf16 v[28:31],v[180:183],v[44:47],v[28:31]
buffer_load_dwordx4 v2,s[32:35],s63 offen offset:3072 sc1 lds
v_mfma_f32_16x16x32_bf16 v[24:27],v[176:179],v[44:47],v[24:27]
ds_read_b128 v[36:39],v4 offset:20480
v_mfma_f32_16x16x32_bf16 v[24:27],v[64:67],v[40:43],v[24:27]
s_waitcnt lgkmcnt(4)
v_mfma_f32_16x16x32_bf16 v[24:27],a[40:43],v[48:51],v[24:27]
ds_read_b128 v[40:43],v4 offset:34816
v_mfma_f32_16x16x32_bf16 v[24:27],a[152:155],v[52:55],v[24:27]
buffer_load_dwordx4 v[12:15],v2,s[32:35],s71 offen sc1
s_add_u32 s71,s71,s30
v_mfma_f32_16x16x32_bf16 v[28:31],a[156:159],v[52:55],v[28:31]
ds_read_b128 v[44:47],v4 offset:49152
s_add_u32 s63,s63,4096
v_mfma_f32_16x16x32_bf16 v[28:31],a[44:47],v[48:51],v[28:31]
s_add_u32 m0,s64,4096
s_waitcnt lgkmcnt(4)
v_mfma_f32_16x16x32_bf16 v[28:31],v[76:79],v[56:59],v[28:31]
ds_read_b128 v[48:51],v4 offset:7168
v_mfma_f32_16x16x32_bf16 v[28:31],v[188:191],v[60:63],v[28:31]
buffer_load_dwordx4 v2,s[32:35],s63 offen offset:0 sc1 lds
v_mfma_f32_16x16x32_bf16 v[24:27],v[184:187],v[60:63],v[24:27]
ds_read_b128 v[52:55],v4 offset:21504
v_mfma_f32_16x16x32_bf16 v[24:27],v[72:75],v[56:59],v[24:27]
s_waitcnt lgkmcnt(4)
v_mfma_f32_16x16x32_bf16 v[24:27],a[48:51],v[32:35],v[24:27]
ds_read_b128 v[56:59],v4 offset:35840
v_mfma_f32_16x16x32_bf16 v[24:27],a[160:163],v[36:39],v[24:27]
buffer_load_dwordx4 v2,s[32:35],s63 offen offset:1024 sc1 lds
v_mfma_f32_16x16x32_bf16 v[28:31],a[164:167],v[36:39],v[28:31]
ds_read_b128 v[60:63],v4 offset:50176
v_mfma_f32_16x16x32_bf16 v[28:31],a[52:55],v[32:35],v[28:31]
s_waitcnt lgkmcnt(4)
v_mfma_f32_16x16x32_bf16 v[28:31],v[84:87],v[40:43],v[28:31]
ds_read_b128 v[32:35],v4 offset:8192
v_mfma_f32_16x16x32_bf16 v[28:31],v[196:199],v[44:47],v[28:31]
buffer_load_dwordx4 v2,s[32:35],s63 offen offset:2048 sc1 lds
v_mfma_f32_16x16x32_bf16 v[24:27],v[192:195],v[44:47],v[24:27]
ds_read_b128 v[36:39],v4 offset:22528
v_mfma_f32_16x16x32_bf16 v[24:27],v[80:83],v[40:43],v[24:27]
s_waitcnt lgkmcnt(4)
v_mfma_f32_16x16x32_bf16 v[24:27],a[56:59],v[48:51],v[24:27]
ds_read_b128 v[40:43],v4 offset:36864
v_mfma_f32_16x16x32_bf16 v[24:27],a[168:171],v[52:55],v[24:27]
buffer_load_dwordx4 v2,s[32:35],s63 offen offset:3072 sc1 lds
v_mfma_f32_16x16x32_bf16 v[28:31],a[172:175],v[52:55],v[28:31]
ds_read_b128 v[44:47],v4 offset:51200
v_mfma_f32_16x16x32_bf16 v[28:31],a[60:63],v[48:51],v[28:31]
s_waitcnt lgkmcnt(4)
v_mfma_f32_16x16x32_bf16 v[28:31],v[92:95],v[56:59],v[28:31]
ds_read_b128 v[48:51],v4 offset:9216
v_mfma_f32_16x16x32_bf16 v[28:31],v[204:207],v[60:63],v[28:31]
s_add_u32 s63,s63,4096
s_add_u32 m0,s64,8192
v_mfma_f32_16x16x32_bf16 v[24:27],v[200:203],v[60:63],v[24:27]
ds_read_b128 v[52:55],v4 offset:23552
v_mfma_f32_16x16x32_bf16 v[24:27],v[88:91],v[56:59],v[24:27]
s_waitcnt lgkmcnt(4)
v_mfma_f32_16x16x32_bf16 v[24:27],a[64:67],v[32:35],v[24:27]
ds_read_b128 v[56:59],v4 offset:37888
v_mfma_f32_16x16x32_bf16 v[24:27],a[176:179],v[36:39],v[24:27]
buffer_load_dwordx4 v2,s[32:35],s63 offen offset:0 sc1 lds
v_mfma_f32_16x16x32_bf16 v[28:31],a[180:183],v[36:39],v[28:31]
ds_read_b128 v[60:63],v4 offset:52224
v_mfma_f32_16x16x32_bf16 v[28:31],a[68:71],v[32:35],v[28:31]
s_waitcnt lgkmcnt(4)
v_mfma_f32_16x16x32_bf16 v[28:31],v[100:103],v[40:43],v[28:31]
ds_read_b128 v[32:35],v4 offset:10240
v_mfma_f32_16x16x32_bf16 v[28:31],v[212:215],v[44:47],v[28:31]
buffer_load_dwordx4 v2,s[32:35],s63 offen offset:1024 sc1 lds
v_mfma_f32_16x16x32_bf16 v[24:27],v[208:211],v[44:47],v[24:27]
ds_read_b128 v[36:39],v4 offset:24576
v_mfma_f32_16x16x32_bf16 v[24:27],v[96:99],v[40:43],v[24:27]
s_waitcnt lgkmcnt(4)
v_mfma_f32_16x16x32_bf16 v[24:27],a[72:75],v[48:51],v[24:27]
ds_read_b128 v[40:43],v4 offset:38912
v_mfma_f32_16x16x32_bf16 v[24:27],a[184:187],v[52:55],v[24:27]
buffer_load_dwordx4 v2,s[32:35],s63 offen offset:2048 sc1 lds
v_mfma_f32_16x16x32_bf16 v[28:31],a[188:191],v[52:55],v[28:31]
ds_read_b128 v[44:47],v4 offset:53248
v_mfma_f32_16x16x32_bf16 v[28:31],a[76:79],v[48:51],v[28:31]
s_waitcnt lgkmcnt(4)
v_mfma_f32_16x16x32_bf16 v[28:31],v[108:111],v[56:59],v[28:31]
ds_read_b128 v[48:51],v4 offset:11264
v_mfma_f32_16x16x32_bf16 v[28:31],v[220:223],v[60:63],v[28:31]
v_mfma_f32_16x16x32_bf16 v[24:27],v[216:219],v[60:63],v[24:27]
ds_read_b128 v[52:55],v4 offset:25600
v_mfma_f32_16x16x32_bf16 v[24:27],v[104:107],v[56:59],v[24:27]
s_waitcnt lgkmcnt(4)
v_mfma_f32_16x16x32_bf16 v[24:27],a[80:83],v[32:35],v[24:27]
ds_read_b128 v[56:59],v4 offset:39936
v_mfma_f32_16x16x32_bf16 v[24:27],a[192:195],v[36:39],v[24:27]
buffer_load_dwordx4 v2,s[32:35],s63 offen offset:3072 sc1 lds
s_add_u32 s63,s63,4096
v_mfma_f32_16x16x32_bf16 v[28:31],a[196:199],v[36:39],v[28:31]
ds_read_b128 v[60:63],v4 offset:54272
s_add_u32 m0,s64,12288
v_mfma_f32_16x16x32_bf16 v[28:31],a[84:87],v[32:35],v[28:31]
s_waitcnt lgkmcnt(4)
v_mfma_f32_16x16x32_bf16 v[28:31],v[116:119],v[40:43],v[28:31]
ds_read_b128 v[32:35],v4 offset:12288
v_mfma_f32_16x16x32_bf16 v[28:31],v[228:231],v[44:47],v[28:31]
buffer_load_dwordx4 v2,s[32:35],s63 offen offset:0 sc1 lds
v_mfma_f32_16x16x32_bf16 v[24:27],v[224:227],v[44:47],v[24:27]
ds_read_b128 v[36:39],v4 offset:26624
v_mfma_f32_16x16x32_bf16 v[24:27],v[112:115],v[40:43],v[24:27]
s_waitcnt lgkmcnt(4)
v_mfma_f32_16x16x32_bf16 v[24:27],a[88:91],v[48:51],v[24:27]
ds_read_b128 v[40:43],v4 offset:40960
v_mfma_f32_16x16x32_bf16 v[24:27],a[200:203],v[52:55],v[24:27]
buffer_load_dwordx4 v2,s[32:35],s63 offen offset:1024 sc1 lds
s_add_u32 s63,s63,s31
v_mfma_f32_16x16x32_bf16 v[28:31],a[204:207],v[52:55],v[28:31]
ds_read_b128 v[44:47],v4 offset:55296
v_mfma_f32_16x16x32_bf16 v[28:31],a[92:95],v[48:51],v[28:31]
s_waitcnt lgkmcnt(4)
v_mfma_f32_16x16x32_bf16 v[28:31],v[124:127],v[56:59],v[28:31]
ds_read_b128 v[48:51],v4 offset:13312
s_waitcnt vmcnt(10)
v_mfma_f32_16x16x32_bf16 v[28:31],v[236:239],v[60:63],v[28:31]
v_lshlrev_b32 v16,16,v12
v_and_b32 v17,0xffff0000,v12
v_mfma_f32_16x16x32_bf16 v[24:27],v[232:235],v[60:63],v[24:27]
ds_read_b128 v[52:55],v4 offset:27648
v_lshlrev_b32 v18,16,v13
v_mfma_f32_16x16x32_bf16 v[24:27],v[120:123],v[56:59],v[24:27]
v_and_b32 v19,0xffff0000,v13
s_waitcnt lgkmcnt(4)
v_mfma_f32_16x16x32_bf16 v[24:27],a[96:99],v[32:35],v[24:27]
s_barrier
v_mfma_f32_16x16x32_bf16 v[24:27],a[208:211],v[36:39],v[24:27]
ds_read_b128 v[56:59],v4 offset:41984
v_lshlrev_b32 v20,16,v14
v_mfma_f32_16x16x32_bf16 v[28:31],a[212:215],v[36:39],v[28:31]
ds_read_b128 v[60:63],v4 offset:56320
v_and_b32 v21,0xffff0000,v14
v_mfma_f32_16x16x32_bf16 v[28:31],a[100:103],v[32:35],v[28:31]
v_lshlrev_b32 v22,16,v15
s_waitcnt lgkmcnt(4)
v_mfma_f32_16x16x32_bf16 v[28:31],v[132:135],v[40:43],v[28:31]
ds_read_b128 v[32:35],v2 offset:0
v_and_b32 v23,0xffff0000,v15
v_mfma_f32_16x16x32_bf16 v[28:31],v[244:247],v[44:47],v[28:31]
v_mfma_f32_16x16x32_bf16 v[24:27],v[240:243],v[44:47],v[24:27]
ds_read_b128 v[36:39],v2 offset:14336
v_mfma_f32_16x16x32_bf16 v[24:27],v[128:131],v[40:43],v[24:27]
s_waitcnt lgkmcnt(4)
v_mfma_f32_16x16x32_bf16 v[24:27],a[104:107],v[48:51],v[24:27]
ds_read_b128 v[40:43],v2 offset:28672
v_mfma_f32_16x16x32_bf16 v[24:27],a[216:219],v[52:55],v[24:27]
v_mfma_f32_16x16x32_bf16 v[28:31],a[220:223],v[52:55],v[28:31]
ds_read_b128 v[44:47],v2 offset:43008
v_mfma_f32_16x16x32_bf16 v[28:31],a[108:111],v[48:51],v[28:31]
s_waitcnt lgkmcnt(4)
v_mfma_f32_16x16x32_bf16 v[28:31],v[140:143],v[56:59],v[28:31]
ds_read_b128 v[48:51],v2 offset:1024
v_mfma_f32_16x16x32_bf16 v[28:31],v[252:255],v[60:63],v[28:31]
v_mfma_f32_16x16x32_bf16 v[24:27],v[248:251],v[60:63],v[24:27]
ds_read_b128 v[52:55],v2 offset:15360
s_sub_u32 s62,s62,1
v_mfma_f32_16x16x32_bf16 v[24:27],v[136:139],v[56:59],v[24:27]
s_cbranch_scc1 .L_dmt_3
s_waitcnt lgkmcnt(4)
v_mfma_f32_16x16x32_bf16 v[16:19],a[0:3],v[32:35],v[16:19]
ds_read_b128 v[56:59],v2 offset:29696
v_mfma_f32_16x16x32_bf16 v[16:19],a[112:115],v[36:39],v[16:19]
v_mfma_f32_16x16x32_bf16 v[20:23],a[116:119],v[36:39],v[20:23]
ds_read_b128 v[60:63],v2 offset:44032
v_mfma_f32_16x16x32_bf16 v[20:23],a[4:7],v[32:35],v[20:23]
v_cvt_pk_bf16_f32 v15,v30,v31
s_waitcnt lgkmcnt(4)
v_mfma_f32_16x16x32_bf16 v[20:23],a[228:231],v[40:43],v[20:23]
ds_read_b128 v[32:35],v2 offset:2048
v_cvt_pk_bf16_f32 v14,v28,v29
v_mfma_f32_16x16x32_bf16 v[20:23],v[148:151],v[44:47],v[20:23]
v_cvt_pk_bf16_f32 v13,v26,v27
v_cvt_pk_bf16_f32 v12,v24,v25
v_mfma_f32_16x16x32_bf16 v[16:19],v[144:147],v[44:47],v[16:19]
ds_read_b128 v[36:39],v2 offset:16384
ds_write_b128 v5,v[12:15]
v_mfma_f32_16x16x32_bf16 v[16:19],a[224:227],v[40:43],v[16:19]
ds_read2st64_b32 v[12:13],v3 offset0:0 offset1:1
s_waitcnt lgkmcnt(6)
v_mfma_f32_16x16x32_bf16 v[16:19],a[8:11],v[48:51],v[16:19]
ds_read_b128 v[40:43],v2 offset:30720
ds_read2st64_b32 v[14:15],v3 offset0:2 offset1:3
v_mfma_f32_16x16x32_bf16 v[16:19],a[120:123],v[52:55],v[16:19]
v_mfma_f32_16x16x32_bf16 v[20:23],a[124:127],v[52:55],v[20:23]
ds_read_b128 v[44:47],v2 offset:45056
v_mfma_f32_16x16x32_bf16 v[20:23],a[12:15],v[48:51],v[20:23]
s_waitcnt lgkmcnt(7)
v_mfma_f32_16x16x32_bf16 v[20:23],a[236:239],v[56:59],v[20:23]
ds_read_b128 v[48:51],v2 offset:3072
v_mfma_f32_16x16x32_bf16 v[20:23],v[156:159],v[60:63],v[20:23]
s_waitcnt lgkmcnt(2)
buffer_atomic_pk_add_bf16 v12,v1,s[44:47],s65 offen offset:0 sc1
v_mfma_f32_16x16x32_bf16 v[16:19],v[152:155],v[60:63],v[16:19]
ds_read_b128 v[52:55],v2 offset:17408
s_add_u32 s70,s70,s68
v_mfma_f32_16x16x32_bf16 v[16:19],a[232:235],v[56:59],v[16:19]
buffer_atomic_pk_add_bf16 v13,v1,s[44:47],s65 offen offset:2048 sc1
v_mfma_f32_16x16x32_bf16 v[16:19],a[16:19],v[32:35],v[16:19]
s_waitcnt vmcnt(2)
s_barrier
v_mfma_f32_16x16x32_bf16 v[16:19],a[128:131],v[36:39],v[16:19]
ds_read_b128 v[56:59],v2 offset:31744
s_add_u32 s65,s65,16384
v_mfma_f32_16x16x32_bf16 v[20:23],a[132:135],v[36:39],v[20:23]
ds_read_b128 v[60:63],v2 offset:46080
v_mfma_f32_16x16x32_bf16 v[20:23],a[20:23],v[32:35],v[20:23]
buffer_atomic_pk_add_bf16 v14,v1,s[44:47],s65 offen offset:0 sc1
s_waitcnt lgkmcnt(4)
v_mfma_f32_16x16x32_bf16 v[20:23],a[244:247],v[40:43],v[20:23]
ds_read_b128 v[32:35],v2 offset:4096
s_add_u32 m0,s64,57344
v_mfma_f32_16x16x32_bf16 v[20:23],v[164:167],v[44:47],v[20:23]
buffer_load_dwordx4 v2,s[32:35],s63 offen offset:0 sc1 lds
s_mov_b32 s68,s75
v_mfma_f32_16x16x32_bf16 v[16:19],v[160:163],v[44:47],v[16:19]
ds_read_b128 v[36:39],v2 offset:18432
v_mfma_f32_16x16x32_bf16 v[16:19],a[240:243],v[40:43],v[16:19]
buffer_atomic_pk_add_bf16 v15,v1,s[44:47],s65 offen offset:2048 sc1
s_waitcnt lgkmcnt(4)
v_mfma_f32_16x16x32_bf16 v[16:19],a[24:27],v[48:51],v[16:19]
ds_read_b128 v[40:43],v2 offset:32768
s_and_b32 s65,s70,0x3f8000
v_mfma_f32_16x16x32_bf16 v[16:19],a[136:139],v[52:55],v[16:19]
v_mfma_f32_16x16x32_bf16 v[20:23],a[140:143],v[52:55],v[20:23]
ds_read_b128 v[44:47],v2 offset:47104
v_mfma_f32_16x16x32_bf16 v[20:23],a[28:31],v[48:51],v[20:23]
s_waitcnt lgkmcnt(4)
v_mfma_f32_16x16x32_bf16 v[20:23],a[252:255],v[56:59],v[20:23]
ds_read_b128 v[48:51],v2 offset:5120
v_mfma_f32_16x16x32_bf16 v[20:23],v[172:175],v[60:63],v[20:23]
buffer_load_dwordx4 v2,s[32:35],s63 offen offset:1024 sc1 lds
v_mfma_f32_16x16x32_bf16 v[16:19],v[168:171],v[60:63],v[16:19]
ds_read_b128 v[52:55],v2 offset:19456
v_mfma_f32_16x16x32_bf16 v[16:19],a[248:251],v[56:59],v[16:19]
s_waitcnt lgkmcnt(4)
v_mfma_f32_16x16x32_bf16 v[16:19],a[32:35],v[32:35],v[16:19]
ds_read_b128 v[56:59],v2 offset:33792
v_mfma_f32_16x16x32_bf16 v[16:19],a[144:147],v[36:39],v[16:19]
buffer_load_dwordx4 v2,s[32:35],s63 offen offset:2048 sc1 lds
v_mfma_f32_16x16x32_bf16 v[20:23],a[148:151],v[36:39],v[20:23]
ds_read_b128 v[60:63],v2 offset:48128
v_mfma_f32_16x16x32_bf16 v[20:23],a[36:39],v[32:35],v[20:23]
s_waitcnt lgkmcnt(4)
v_mfma_f32_16x16x32_bf16 v[20:23],v[68:71],v[40:43],v[20:23]
ds_read_b128 v[32:35],v2 offset:6144
v_mfma_f32_16x16x32_bf16 v[20:23],v[180:183],v[44:47],v[20:23]
buffer_load_dwordx4 v2,s[32:35],s63 offen offset:3072 sc1 lds
v_mfma_f32_16x16x32_bf16 v[16:19],v[176:179],v[44:47],v[16:19]
ds_read_b128 v[36:39],v2 offset:20480
v_mfma_f32_16x16x32_bf16 v[16:19],v[64:67],v[40:43],v[16:19]
s_waitcnt lgkmcnt(4)
v_mfma_f32_16x16x32_bf16 v[16:19],a[40:43],v[48:51],v[16:19]
ds_read_b128 v[40:43],v2 offset:34816
v_mfma_f32_16x16x32_bf16 v[16:19],a[152:155],v[52:55],v[16:19]
buffer_load_dwordx4 v[12:15],v2,s[32:35],s71 offen sc1
s_add_u32 s71,s71,s30
v_mfma_f32_16x16x32_bf16 v[20:23],a[156:159],v[52:55],v[20:23]
ds_read_b128 v[44:47],v2 offset:49152
s_add_u32 s63,s63,4096
v_mfma_f32_16x16x32_bf16 v[20:23],a[44:47],v[48:51],v[20:23]
s_add_u32 m0,s64,61440
s_waitcnt lgkmcnt(4)
v_mfma_f32_16x16x32_bf16 v[20:23],v[76:79],v[56:59],v[20:23]
ds_read_b128 v[48:51],v2 offset:7168
v_mfma_f32_16x16x32_bf16 v[20:23],v[188:191],v[60:63],v[20:23]
buffer_load_dwordx4 v2,s[32:35],s63 offen offset:0 sc1 lds
v_mfma_f32_16x16x32_bf16 v[16:19],v[184:187],v[60:63],v[16:19]
ds_read_b128 v[52:55],v2 offset:21504
v_mfma_f32_16x16x32_bf16 v[16:19],v[72:75],v[56:59],v[16:19]
s_waitcnt lgkmcnt(4)
v_mfma_f32_16x16x32_bf16 v[16:19],a[48:51],v[32:35],v[16:19]
ds_read_b128 v[56:59],v2 offset:35840
v_mfma_f32_16x16x32_bf16 v[16:19],a[160:163],v[36:39],v[16:19]
buffer_load_dwordx4 v2,s[32:35],s63 offen offset:1024 sc1 lds
v_mfma_f32_16x16x32_bf16 v[20:23],a[164:167],v[36:39],v[20:23]
ds_read_b128 v[60:63],v2 offset:50176
v_mfma_f32_16x16x32_bf16 v[20:23],a[52:55],v[32:35],v[20:23]
s_waitcnt lgkmcnt(4)
v_mfma_f32_16x16x32_bf16 v[20:23],v[84:87],v[40:43],v[20:23]
ds_read_b128 v[32:35],v2 offset:8192
v_mfma_f32_16x16x32_bf16 v[20:23],v[196:199],v[44:47],v[20:23]
buffer_load_dwordx4 v2,s[32:35],s63 offen offset:2048 sc1 lds
v_mfma_f32_16x16x32_bf16 v[16:19],v[192:195],v[44:47],v[16:19]
ds_read_b128 v[36:39],v2 offset:22528
v_mfma_f32_16x16x32_bf16 v[16:19],v[80:83],v[40:43],v[16:19]
s_waitcnt lgkmcnt(4)
v_mfma_f32_16x16x32_bf16 v[16:19],a[56:59],v[48:51],v[16:19]
ds_read_b128 v[40:43],v2 offset:36864
v_mfma_f32_16x16x32_bf16 v[16:19],a[168:171],v[52:55],v[16:19]
buffer_load_dwordx4 v2,s[32:35],s63 offen offset:3072 sc1 lds
v_mfma_f32_16x16x32_bf16 v[20:23],a[172:175],v[52:55],v[20:23]
ds_read_b128 v[44:47],v2 offset:51200
v_mfma_f32_16x16x32_bf16 v[20:23],a[60:63],v[48:51],v[20:23]
s_waitcnt lgkmcnt(4)
v_mfma_f32_16x16x32_bf16 v[20:23],v[92:95],v[56:59],v[20:23]
ds_read_b128 v[48:51],v2 offset:9216
v_mfma_f32_16x16x32_bf16 v[20:23],v[204:207],v[60:63],v[20:23]
s_add_u32 s63,s63,4096
s_add_u32 m0,s64,65536
v_mfma_f32_16x16x32_bf16 v[16:19],v[200:203],v[60:63],v[16:19]
ds_read_b128 v[52:55],v2 offset:23552
v_mfma_f32_16x16x32_bf16 v[16:19],v[88:91],v[56:59],v[16:19]
s_waitcnt lgkmcnt(4)
v_mfma_f32_16x16x32_bf16 v[16:19],a[64:67],v[32:35],v[16:19]
ds_read_b128 v[56:59],v2 offset:37888
v_mfma_f32_16x16x32_bf16 v[16:19],a[176:179],v[36:39],v[16:19]
buffer_load_dwordx4 v2,s[32:35],s63 offen offset:0 sc1 lds
v_mfma_f32_16x16x32_bf16 v[20:23],a[180:183],v[36:39],v[20:23]
ds_read_b128 v[60:63],v2 offset:52224
v_mfma_f32_16x16x32_bf16 v[20:23],a[68:71],v[32:35],v[20:23]
s_waitcnt lgkmcnt(4)
v_mfma_f32_16x16x32_bf16 v[20:23],v[100:103],v[40:43],v[20:23]
ds_read_b128 v[32:35],v2 offset:10240
v_mfma_f32_16x16x32_bf16 v[20:23],v[212:215],v[44:47],v[20:23]
buffer_load_dwordx4 v2,s[32:35],s63 offen offset:1024 sc1 lds
v_mfma_f32_16x16x32_bf16 v[16:19],v[208:211],v[44:47],v[16:19]
ds_read_b128 v[36:39],v2 offset:24576
v_mfma_f32_16x16x32_bf16 v[16:19],v[96:99],v[40:43],v[16:19]
s_waitcnt lgkmcnt(4)
v_mfma_f32_16x16x32_bf16 v[16:19],a[72:75],v[48:51],v[16:19]
ds_read_b128 v[40:43],v2 offset:38912
v_mfma_f32_16x16x32_bf16 v[16:19],a[184:187],v[52:55],v[16:19]
buffer_load_dwordx4 v2,s[32:35],s63 offen offset:2048 sc1 lds
v_mfma_f32_16x16x32_bf16 v[20:23],a[188:191],v[52:55],v[20:23]
ds_read_b128 v[44:47],v2 offset:53248
v_mfma_f32_16x16x32_bf16 v[20:23],a[76:79],v[48:51],v[20:23]
s_waitcnt lgkmcnt(4)
v_mfma_f32_16x16x32_bf16 v[20:23],v[108:111],v[56:59],v[20:23]
ds_read_b128 v[48:51],v2 offset:11264
v_mfma_f32_16x16x32_bf16 v[20:23],v[220:223],v[60:63],v[20:23]
v_mfma_f32_16x16x32_bf16 v[16:19],v[216:219],v[60:63],v[16:19]
ds_read_b128 v[52:55],v2 offset:25600
v_mfma_f32_16x16x32_bf16 v[16:19],v[104:107],v[56:59],v[16:19]
s_waitcnt lgkmcnt(4)
v_mfma_f32_16x16x32_bf16 v[16:19],a[80:83],v[32:35],v[16:19]
ds_read_b128 v[56:59],v2 offset:39936
v_mfma_f32_16x16x32_bf16 v[16:19],a[192:195],v[36:39],v[16:19]
buffer_load_dwordx4 v2,s[32:35],s63 offen offset:3072 sc1 lds
s_add_u32 s63,s63,4096
v_mfma_f32_16x16x32_bf16 v[20:23],a[196:199],v[36:39],v[20:23]
)#"
R"#(
ds_read_b128 v[60:63],v2 offset:54272
s_add_u32 m0,s64,69632
v_mfma_f32_16x16x32_bf16 v[20:23],a[84:87],v[32:35],v[20:23]
s_waitcnt lgkmcnt(4)
v_mfma_f32_16x16x32_bf16 v[20:23],v[116:119],v[40:43],v[20:23]
ds_read_b128 v[32:35],v2 offset:12288
v_mfma_f32_16x16x32_bf16 v[20:23],v[228:231],v[44:47],v[20:23]
buffer_load_dwordx4 v2,s[32:35],s63 offen offset:0 sc1 lds
v_mfma_f32_16x16x32_bf16 v[16:19],v[224:227],v[44:47],v[16:19]
ds_read_b128 v[36:39],v2 offset:26624
v_mfma_f32_16x16x32_bf16 v[16:19],v[112:115],v[40:43],v[16:19]
s_waitcnt lgkmcnt(4)
v_mfma_f32_16x16x32_bf16 v[16:19],a[88:91],v[48:51],v[16:19]
ds_read_b128 v[40:43],v2 offset:40960
v_mfma_f32_16x16x32_bf16 v[16:19],a[200:203],v[52:55],v[16:19]
buffer_load_dwordx4 v2,s[32:35],s63 offen offset:1024 sc1 lds
s_add_u32 s63,s63,s31
v_mfma_f32_16x16x32_bf16 v[20:23],a[204:207],v[52:55],v[20:23]
ds_read_b128 v[44:47],v2 offset:55296
v_mfma_f32_16x16x32_bf16 v[20:23],a[92:95],v[48:51],v[20:23]
s_waitcnt lgkmcnt(4)
v_mfma_f32_16x16x32_bf16 v[20:23],v[124:127],v[56:59],v[20:23]
ds_read_b128 v[48:51],v2 offset:13312
s_waitcnt vmcnt(10)
v_mfma_f32_16x16x32_bf16 v[20:23],v[236:239],v[60:63],v[20:23]
v_lshlrev_b32 v24,16,v12
v_and_b32 v25,0xffff0000,v12
v_mfma_f32_16x16x32_bf16 v[16:19],v[232:235],v[60:63],v[16:19]
ds_read_b128 v[52:55],v2 offset:27648
v_lshlrev_b32 v26,16,v13
v_mfma_f32_16x16x32_bf16 v[16:19],v[120:123],v[56:59],v[16:19]
v_and_b32 v27,0xffff0000,v13
s_waitcnt lgkmcnt(4)
v_mfma_f32_16x16x32_bf16 v[16:19],a[96:99],v[32:35],v[16:19]
s_barrier
v_mfma_f32_16x16x32_bf16 v[16:19],a[208:211],v[36:39],v[16:19]
ds_read_b128 v[56:59],v2 offset:41984
v_lshlrev_b32 v28,16,v14
v_mfma_f32_16x16x32_bf16 v[20:23],a[212:215],v[36:39],v[20:23]
ds_read_b128 v[60:63],v2 offset:56320
v_and_b32 v29,0xffff0000,v14
v_mfma_f32_16x16x32_bf16 v[20:23],a[100:103],v[32:35],v[20:23]
v_lshlrev_b32 v30,16,v15
s_waitcnt lgkmcnt(4)
v_mfma_f32_16x16x32_bf16 v[20:23],v[132:135],v[40:43],v[20:23]
ds_read_b128 v[32:35],v4 offset:0
v_and_b32 v31,0xffff0000,v15
v_mfma_f32_16x16x32_bf16 v[20:23],v[244:247],v[44:47],v[20:23]
v_mfma_f32_16x16x32_bf16 v[16:19],v[240:243],v[44:47],v[16:19]
ds_read_b128 v[36:39],v4 offset:14336
v_mfma_f32_16x16x32_bf16 v[16:19],v[128:131],v[40:43],v[16:19]
s_waitcnt lgkmcnt(4)
v_mfma_f32_16x16x32_bf16 v[16:19],a[104:107],v[48:51],v[16:19]
ds_read_b128 v[40:43],v4 offset:28672
v_mfma_f32_16x16x32_bf16 v[16:19],a[216:219],v[52:55],v[16:19]
v_mfma_f32_16x16x32_bf16 v[20:23],a[220:223],v[52:55],v[20:23]
ds_read_b128 v[44:47],v4 offset:43008
v_mfma_f32_16x16x32_bf16 v[20:23],a[108:111],v[48:51],v[20:23]
s_waitcnt lgkmcnt(4)
v_mfma_f32_16x16x32_bf16 v[20:23],v[140:143],v[56:59],v[20:23]
ds_read_b128 v[48:51],v4 offset:1024
v_mfma_f32_16x16x32_bf16 v[20:23],v[252:255],v[60:63],v[20:23]
v_mfma_f32_16x16x32_bf16 v[16:19],v[248:251],v[60:63],v[16:19]
ds_read_b128 v[52:55],v4 offset:15360
s_sub_u32 s62,s62,1
v_mfma_f32_16x16x32_bf16 v[16:19],v[136:139],v[56:59],v[16:19]
s_cbranch_scc0 .L_dml_3
.L_dmu_3:
s_nop 1
s_nop 0
v_cvt_pk_bf16_f32 v15,v22,v23
v_cvt_pk_bf16_f32 v14,v20,v21
s_nop 1
v_cvt_pk_bf16_f32 v13,v18,v19
v_cvt_pk_bf16_f32 v12,v16,v17
ds_write_b128 v5,v[12:15]
ds_read2st64_b32 v[12:13],v3 offset0:0 offset1:1
ds_read2st64_b32 v[14:15],v3 offset0:2 offset1:3
s_waitcnt lgkmcnt(0)
buffer_atomic_pk_add_bf16 v12,v1,s[44:47],s65 offen offset:0 sc1
s_add_u32 s70,s70,s75
buffer_atomic_pk_add_bf16 v13,v1,s[44:47],s65 offen offset:2048 sc1
s_add_u32 s65,s65,16384
buffer_atomic_pk_add_bf16 v14,v1,s[44:47],s65 offen offset:0 sc1
buffer_atomic_pk_add_bf16 v15,v1,s[44:47],s65 offen offset:2048 sc1
s_and_b32 s65,s70,0x3f8000
s_branch .L_end
.L_dmt_3:
s_nop 1
s_nop 0
v_cvt_pk_bf16_f32 v15,v30,v31
v_cvt_pk_bf16_f32 v14,v28,v29
s_nop 1
v_cvt_pk_bf16_f32 v13,v26,v27
v_cvt_pk_bf16_f32 v12,v24,v25
ds_write_b128 v5,v[12:15]
ds_read2st64_b32 v[12:13],v3 offset0:0 offset1:1
ds_read2st64_b32 v[14:15],v3 offset0:2 offset1:3
s_waitcnt lgkmcnt(0)
buffer_atomic_pk_add_bf16 v12,v1,s[44:47],s65 offen offset:0 sc1
s_add_u32 s70,s70,s68
buffer_atomic_pk_add_bf16 v13,v1,s[44:47],s65 offen offset:2048 sc1
s_add_u32 s65,s65,16384
buffer_atomic_pk_add_bf16 v14,v1,s[44:47],s65 offen offset:0 sc1
s_mov_b32 s68,s75
buffer_atomic_pk_add_bf16 v15,v1,s[44:47],s65 offen offset:2048 sc1
s_and_b32 s65,s70,0x3f8000
s_branch .L_end
.L_dh:
s_cmp_lg_u32 s25,0
s_cbranch_scc1 .L_dy
v_mov_b32 v12,0
v_mov_b32 v13,0
v_mov_b32 v14,0
v_mov_b32 v15,0
v_lshrrev_b32 v10,4,v1
v_and_b32 v11,0xf0,v2
v_lshl_add_u32 v10,v10,11,v11
s_lshl2_add_u32 s48,s28,s26
s_lshl_b32 s48,s48,10
s_add_u32 s48,s48,s27
s_lshl_b32 s48,s48,8
s_mov_b32 s49,31
.L_dz:
buffer_store_dwordx4 v[12:15],v10,s[44:47],s48 offen sc1
s_add_u32 s48,s48,8192
s_sub_u32 s49,s49,1
s_cbranch_scc0 .L_dz
.L_dy:
v_lshrrev_b32 v10,4,v1
v_and_b32 v1,15,v1
s_lshl_b32 s48,s50,6
v_lshl_add_u32 v1,v1,2,s48
v_lshl_add_u32 v1,v10,12,v1
s_sub_u32 s71,s65,1187840
s_add_u32 s69,s69,38010880
s_add_u32 s68,s70,2236416
s_mov_b32 s67,s68
s_mov_b32 s72,0x3f803f80
s_add_u32 s70,s70,42622976
s_and_b32 s65,s59,0x3f8000
s_cmp_lg_u32 s26,1
s_cbranch_scc1 .L_skda
s_mov_b32 s48,20000
s_branch .L_rfda
.L_rlda:
s_sleep 2
.L_rfda:
buffer_load_dwordx2 v[6:7],v0,s[0:3],s66 offen sc1
s_sub_u32 s48,s48,1
s_cbranch_scc1 .L_end
s_waitcnt vmcnt(0)
v_cmp_ne_u64 vcc,v[8:9],v[6:7]
s_cbranch_vccnz .L_rlda
.L_skda:
s_waitcnt vmcnt(0)
s_barrier
s_add_u32 s66,s66,s30
s_add_u32 m0,s64,0
s_nop 0
buffer_load_dwordx4 v2,s[32:35],s63 offen offset:0 sc1 lds
buffer_load_dwordx4 v2,s[32:35],s63 offen offset:1024 sc1 lds
buffer_load_dwordx4 v2,s[32:35],s63 offen offset:2048 sc1 lds
buffer_load_dwordx4 v2,s[32:35],s63 offen offset:3072 sc1 lds
s_add_u32 s63,s63,4096
s_add_u32 m0,s64,4096
s_nop 0
buffer_load_dwordx4 v2,s[32:35],s63 offen offset:0 sc1 lds
buffer_load_dwordx4 v2,s[32:35],s63 offen offset:1024 sc1 lds
buffer_load_dwordx4 v2,s[32:35],s63 offen offset:2048 sc1 lds
buffer_load_dwordx4 v2,s[32:35],s63 offen offset:3072 sc1 lds
s_add_u32 s63,s63,4096
s_add_u32 m0,s64,8192
s_nop 0
buffer_load_dwordx4 v2,s[32:35],s63 offen offset:0 sc1 lds
buffer_load_dwordx4 v2,s[32:35],s63 offen offset:1024 sc1 lds
buffer_load_dwordx4 v2,s[32:35],s63 offen offset:2048 sc1 lds
buffer_load_dwordx4 v2,s[32:35],s63 offen offset:3072 sc1 lds
s_add_u32 s63,s63,4096
s_add_u32 m0,s64,12288
s_nop 0
buffer_load_dwordx4 v2,s[32:35],s63 offen offset:0 sc1 lds
buffer_load_dwordx4 v2,s[32:35],s63 offen offset:1024 sc1 lds
s_add_u32 s63,s63,s31
buffer_load_dwordx2 v[6:7],v0,s[0:3],s66 offen sc1
s_waitcnt vmcnt(0)
s_barrier
ds_read_b128 v[32:35],v2 offset:0
ds_read_b128 v[36:39],v2 offset:14336
ds_read_b128 v[40:43],v2 offset:28672
ds_read_b128 v[44:47],v2 offset:43008
ds_read_b128 v[48:51],v2 offset:1024
ds_read_b128 v[52:55],v2 offset:15360
.L_dhgo:
s_cmp_eq_u32 s26,1
s_cbranch_scc1 .L_dhv1
s_cmp_eq_u32 s26,2
s_cbranch_scc1 .L_dhv2
s_cmp_eq_u32 s26,3
s_cbranch_scc1 .L_dhv3
.p2align 6
.L_dhv0:
.L_dal_0:
s_waitcnt lgkmcnt(4)
v_mfma_f32_16x16x32_bf16 v[16:19],a[0:3],v[32:35],0
ds_read_b128 v[56:59],v2 offset:29696
v_dot2_f32_bf16 v10,v12,s72,0
v_mfma_f32_16x16x32_bf16 v[16:19],a[112:115],v[36:39],v[16:19]
v_dot2_f32_bf16 v11,v12,v12,0
v_dot2_f32_bf16 v10,v13,s72,v10
v_mfma_f32_16x16x32_bf16 v[20:23],a[116:119],v[36:39],0
ds_read_b128 v[60:63],v2 offset:44032
v_dot2_f32_bf16 v11,v13,v13,v11
v_mfma_f32_16x16x32_bf16 v[20:23],a[4:7],v[32:35],v[20:23]
v_dot2_f32_bf16 v10,v14,s72,v10
s_waitcnt lgkmcnt(4)
v_mfma_f32_16x16x32_bf16 v[20:23],a[228:231],v[40:43],v[20:23]
ds_read_b128 v[32:35],v2 offset:2048
v_dot2_f32_bf16 v11,v14,v14,v11
v_mfma_f32_16x16x32_bf16 v[20:23],v[148:151],v[44:47],v[20:23]
v_dot2_f32_bf16 v10,v15,s72,v10
v_dot2_f32_bf16 v11,v15,v15,v11
v_mfma_f32_16x16x32_bf16 v[16:19],v[144:147],v[44:47],v[16:19]
ds_read_b128 v[36:39],v2 offset:16384
v_mfma_f32_16x16x32_bf16 v[16:19],a[224:227],v[40:43],v[16:19]
v_permlane16_swap_b32 v10,v11
s_waitcnt lgkmcnt(4)
v_mfma_f32_16x16x32_bf16 v[16:19],a[8:11],v[48:51],v[16:19]
ds_read_b128 v[40:43],v2 offset:30720
v_add_f32 v10,v10,v11
v_mfma_f32_16x16x32_bf16 v[16:19],a[120:123],v[52:55],v[16:19]
v_mfma_f32_16x16x32_bf16 v[20:23],a[124:127],v[52:55],v[20:23]
ds_read_b128 v[44:47],v2 offset:45056
v_permlane32_swap_b32 v10,v11
v_mfma_f32_16x16x32_bf16 v[20:23],a[12:15],v[48:51],v[20:23]
v_add_f32 v10,v10,v11
s_waitcnt lgkmcnt(4)
v_mfma_f32_16x16x32_bf16 v[20:23],a[236:239],v[56:59],v[20:23]
ds_read_b128 v[48:51],v2 offset:3072
v_mfma_f32_16x16x32_bf16 v[20:23],v[156:159],v[60:63],v[20:23]
buffer_store_dword v10,v3,s[32:35],s67 offen offset:1024 sc1
v_mfma_f32_16x16x32_bf16 v[16:19],v[152:155],v[60:63],v[16:19]
ds_read_b128 v[52:55],v2 offset:17408
v_mfma_f32_16x16x32_bf16 v[16:19],a[232:235],v[56:59],v[16:19]
s_waitcnt lgkmcnt(4)
v_mfma_f32_16x16x32_bf16 v[16:19],a[16:19],v[32:35],v[16:19]
ds_read_b128 v[56:59],v2 offset:31744
v_mfma_f32_16x16x32_bf16 v[16:19],a[128:131],v[36:39],v[16:19]
v_mfma_f32_16x16x32_bf16 v[20:23],a[132:135],v[36:39],v[20:23]
ds_read_b128 v[60:63],v2 offset:46080
v_mfma_f32_16x16x32_bf16 v[20:23],a[20:23],v[32:35],v[20:23]
s_waitcnt lgkmcnt(4)
s_waitcnt vmcnt(3)
s_barrier
v_mfma_f32_16x16x32_bf16 v[20:23],a[244:247],v[40:43],v[20:23]
ds_read_b128 v[32:35],v2 offset:4096
s_mov_b32 m0,s74
v_mfma_f32_16x16x32_bf16 v[20:23],v[164:167],v[44:47],v[20:23]
buffer_load_dwordx4 v2,s[32:35],s71 offen offset:0 sc1 lds
v_mfma_f32_16x16x32_bf16 v[16:19],v[160:163],v[44:47],v[16:19]
ds_read_b128 v[36:39],v2 offset:18432
v_mfma_f32_16x16x32_bf16 v[16:19],a[240:243],v[40:43],v[16:19]
s_waitcnt lgkmcnt(4)
v_mfma_f32_16x16x32_bf16 v[16:19],a[24:27],v[48:51],v[16:19]
ds_read_b128 v[40:43],v2 offset:32768
v_mfma_f32_16x16x32_bf16 v[16:19],a[136:139],v[52:55],v[16:19]
buffer_load_dwordx4 v2,s[32:35],s71 offen offset:1024 sc1 lds
s_add_u32 s71,s71,s30
v_mfma_f32_16x16x32_bf16 v[20:23],a[140:143],v[52:55],v[20:23]
ds_read_b128 v[44:47],v2 offset:47104
s_add_u32 m0,s64,57344
v_mfma_f32_16x16x32_bf16 v[20:23],a[28:31],v[48:51],v[20:23]
s_waitcnt lgkmcnt(4)
v_mfma_f32_16x16x32_bf16 v[20:23],a[252:255],v[56:59],v[20:23]
ds_read_b128 v[48:51],v2 offset:5120
v_mfma_f32_16x16x32_bf16 v[20:23],v[172:175],v[60:63],v[20:23]
buffer_load_dwordx4 v2,s[32:35],s63 offen offset:0 sc1 lds
v_mfma_f32_16x16x32_bf16 v[16:19],v[168:171],v[60:63],v[16:19]
ds_read_b128 v[52:55],v2 offset:19456
v_mfma_f32_16x16x32_bf16 v[16:19],a[248:251],v[56:59],v[16:19]
s_waitcnt lgkmcnt(4)
v_mfma_f32_16x16x32_bf16 v[16:19],a[32:35],v[32:35],v[16:19]
ds_read_b128 v[56:59],v2 offset:33792
v_mfma_f32_16x16x32_bf16 v[16:19],a[144:147],v[36:39],v[16:19]
buffer_load_dwordx4 v2,s[32:35],s63 offen offset:1024 sc1 lds
v_mfma_f32_16x16x32_bf16 v[20:23],a[148:151],v[36:39],v[20:23]
ds_read_b128 v[60:63],v2 offset:48128
v_mfma_f32_16x16x32_bf16 v[20:23],a[36:39],v[32:35],v[20:23]
s_waitcnt lgkmcnt(4)
v_mfma_f32_16x16x32_bf16 v[20:23],v[68:71],v[40:43],v[20:23]
ds_read_b128 v[32:35],v2 offset:6144
v_mfma_f32_16x16x32_bf16 v[20:23],v[180:183],v[44:47],v[20:23]
buffer_load_dwordx4 v2,s[32:35],s63 offen offset:2048 sc1 lds
v_mfma_f32_16x16x32_bf16 v[16:19],v[176:179],v[44:47],v[16:19]
ds_read_b128 v[36:39],v2 offset:20480
v_mfma_f32_16x16x32_bf16 v[16:19],v[64:67],v[40:43],v[16:19]
s_waitcnt lgkmcnt(4)
v_mfma_f32_16x16x32_bf16 v[16:19],a[40:43],v[48:51],v[16:19]
ds_read_b128 v[40:43],v2 offset:34816
v_mfma_f32_16x16x32_bf16 v[16:19],a[152:155],v[52:55],v[16:19]
buffer_load_dwordx4 v2,s[32:35],s63 offen offset:3072 sc1 lds
s_add_u32 s63,s63,4096
v_mfma_f32_16x16x32_bf16 v[20:23],a[156:159],v[52:55],v[20:23]
ds_read_b128 v[44:47],v2 offset:49152
s_add_u32 m0,s64,61440
v_mfma_f32_16x16x32_bf16 v[20:23],a[44:47],v[48:51],v[20:23]
s_waitcnt lgkmcnt(4)
v_mfma_f32_16x16x32_bf16 v[20:23],v[76:79],v[56:59],v[20:23]
ds_read_b128 v[48:51],v2 offset:7168
v_mfma_f32_16x16x32_bf16 v[20:23],v[188:191],v[60:63],v[20:23]
buffer_load_dwordx4 v2,s[32:35],s63 offen offset:0 sc1 lds
v_mfma_f32_16x16x32_bf16 v[16:19],v[184:187],v[60:63],v[16:19]
ds_read_b128 v[52:55],v2 offset:21504
v_mfma_f32_16x16x32_bf16 v[16:19],v[72:75],v[56:59],v[16:19]
s_waitcnt lgkmcnt(4)
v_mfma_f32_16x16x32_bf16 v[16:19],a[48:51],v[32:35],v[16:19]
ds_read_b128 v[56:59],v2 offset:35840
v_mfma_f32_16x16x32_bf16 v[16:19],a[160:163],v[36:39],v[16:19]
buffer_load_dwordx4 v2,s[32:35],s63 offen offset:1024 sc1 lds
v_mfma_f32_16x16x32_bf16 v[20:23],a[164:167],v[36:39],v[20:23]
ds_read_b128 v[60:63],v2 offset:50176
v_mfma_f32_16x16x32_bf16 v[20:23],a[52:55],v[32:35],v[20:23]
s_waitcnt lgkmcnt(4)
v_mfma_f32_16x16x32_bf16 v[20:23],v[84:87],v[40:43],v[20:23]
ds_read_b128 v[32:35],v2 offset:8192
v_mfma_f32_16x16x32_bf16 v[20:23],v[196:199],v[44:47],v[20:23]
buffer_load_dwordx4 v2,s[32:35],s63 offen offset:2048 sc1 lds
v_mfma_f32_16x16x32_bf16 v[16:19],v[192:195],v[44:47],v[16:19]
ds_read_b128 v[36:39],v2 offset:22528
v_mfma_f32_16x16x32_bf16 v[16:19],v[80:83],v[40:43],v[16:19]
s_waitcnt lgkmcnt(4)
v_mfma_f32_16x16x32_bf16 v[16:19],a[56:59],v[48:51],v[16:19]
ds_read_b128 v[40:43],v2 offset:36864
v_mfma_f32_16x16x32_bf16 v[16:19],a[168:171],v[52:55],v[16:19]
buffer_load_dwordx4 v2,s[32:35],s63 offen offset:3072 sc1 lds
v_mfma_f32_16x16x32_bf16 v[20:23],a[172:175],v[52:55],v[20:23]
ds_read_b128 v[44:47],v2 offset:51200
v_mfma_f32_16x16x32_bf16 v[20:23],a[60:63],v[48:51],v[20:23]
s_waitcnt lgkmcnt(4)
v_mfma_f32_16x16x32_bf16 v[20:23],v[92:95],v[56:59],v[20:23]
ds_read_b128 v[48:51],v2 offset:9216
v_mfma_f32_16x16x32_bf16 v[20:23],v[204:207],v[60:63],v[20:23]
s_add_u32 s63,s63,4096
s_add_u32 m0,s64,65536
v_mfma_f32_16x16x32_bf16 v[16:19],v[200:203],v[60:63],v[16:19]
ds_read_b128 v[52:55],v2 offset:23552
v_mfma_f32_16x16x32_bf16 v[16:19],v[88:91],v[56:59],v[16:19]
s_waitcnt lgkmcnt(4)
v_mfma_f32_16x16x32_bf16 v[16:19],a[64:67],v[32:35],v[16:19]
ds_read_b128 v[56:59],v2 offset:37888
v_mfma_f32_16x16x32_bf16 v[16:19],a[176:179],v[36:39],v[16:19]
buffer_load_dwordx4 v2,s[32:35],s63 offen offset:0 sc1 lds
v_mfma_f32_16x16x32_bf16 v[20:23],a[180:183],v[36:39],v[20:23]
ds_read_b128 v[60:63],v2 offset:52224
v_mfma_f32_16x16x32_bf16 v[20:23],a[68:71],v[32:35],v[20:23]
s_waitcnt lgkmcnt(4)
v_mfma_f32_16x16x32_bf16 v[20:23],v[100:103],v[40:43],v[20:23]
ds_read_b128 v[32:35],v2 offset:10240
v_mfma_f32_16x16x32_bf16 v[20:23],v[212:215],v[44:47],v[20:23]
buffer_load_dwordx4 v2,s[32:35],s63 offen offset:1024 sc1 lds
v_mfma_f32_16x16x32_bf16 v[16:19],v[208:211],v[44:47],v[16:19]
ds_read_b128 v[36:39],v2 offset:24576
v_mfma_f32_16x16x32_bf16 v[16:19],v[96:99],v[40:43],v[16:19]
s_waitcnt lgkmcnt(4)
v_mfma_f32_16x16x32_bf16 v[16:19],a[72:75],v[48:51],v[16:19]
ds_read_b128 v[40:43],v2 offset:38912
s_waitcnt vmcnt(10)
v_mfma_f32_16x16x32_bf16 v[16:19],a[184:187],v[52:55],v[16:19]
buffer_load_dwordx4 v2,s[32:35],s63 offen offset:2048 sc1 lds
ds_read_b128 v[12:15],v5 offset:0
v_mfma_f32_16x16x32_bf16 v[20:23],a[188:191],v[52:55],v[20:23]
ds_read_b128 v[44:47],v2 offset:53248
v_mfma_f32_16x16x32_bf16 v[20:23],a[76:79],v[48:51],v[20:23]
s_waitcnt lgkmcnt(5)
v_mfma_f32_16x16x32_bf16 v[20:23],v[108:111],v[56:59],v[20:23]
ds_read_b128 v[48:51],v2 offset:11264
v_mfma_f32_16x16x32_bf16 v[20:23],v[220:223],v[60:63],v[20:23]
v_mfma_f32_16x16x32_bf16 v[16:19],v[216:219],v[60:63],v[16:19]
ds_read_b128 v[52:55],v2 offset:25600
s_waitcnt lgkmcnt(3)
v_mfma_f32_16x16x32_bf16 v[16:19],v[104:107],v[56:59],v[16:19]
v_add_f32 v24,v24,v12
v_mfma_f32_16x16x32_bf16 v[16:19],a[80:83],v[32:35],v[16:19]
ds_read_b128 v[56:59],v2 offset:39936
v_add_f32 v25,v25,v13
v_mfma_f32_16x16x32_bf16 v[16:19],a[192:195],v[36:39],v[16:19]
buffer_load_dwordx4 v2,s[32:35],s63 offen offset:3072 sc1 lds
s_add_u32 s63,s63,4096
v_mfma_f32_16x16x32_bf16 v[20:23],a[196:199],v[36:39],v[20:23]
ds_read_b128 v[60:63],v2 offset:54272
s_add_u32 m0,s64,69632
v_mfma_f32_16x16x32_bf16 v[20:23],a[84:87],v[32:35],v[20:23]
v_add_f32 v26,v26,v14
s_waitcnt lgkmcnt(4)
v_mfma_f32_16x16x32_bf16 v[20:23],v[116:119],v[40:43],v[20:23]
ds_read_b128 v[32:35],v2 offset:12288
v_add_f32 v27,v27,v15
v_mfma_f32_16x16x32_bf16 v[20:23],v[228:231],v[44:47],v[20:23]
buffer_load_dwordx4 v2,s[32:35],s63 offen offset:0 sc1 lds
ds_read_b128 v[12:15],v5 offset:1024
v_mfma_f32_16x16x32_bf16 v[16:19],v[224:227],v[44:47],v[16:19]
ds_read_b128 v[36:39],v2 offset:26624
v_mfma_f32_16x16x32_bf16 v[16:19],v[112:115],v[40:43],v[16:19]
s_waitcnt lgkmcnt(5)
v_mfma_f32_16x16x32_bf16 v[16:19],a[88:91],v[48:51],v[16:19]
ds_read_b128 v[40:43],v2 offset:40960
v_mfma_f32_16x16x32_bf16 v[16:19],a[200:203],v[52:55],v[16:19]
buffer_load_dwordx4 v2,s[32:35],s63 offen offset:1024 sc1 lds
s_add_u32 s63,s63,s31
v_mfma_f32_16x16x32_bf16 v[20:23],a[204:207],v[52:55],v[20:23]
ds_read_b128 v[44:47],v2 offset:55296
s_waitcnt lgkmcnt(3)
v_mfma_f32_16x16x32_bf16 v[20:23],a[92:95],v[48:51],v[20:23]
v_add_f32 v28,v28,v12
v_mfma_f32_16x16x32_bf16 v[20:23],v[124:127],v[56:59],v[20:23]
ds_read_b128 v[48:51],v2 offset:13312
v_add_f32 v29,v29,v13
v_mfma_f32_16x16x32_bf16 v[20:23],v[236:239],v[60:63],v[20:23]
v_add_f32 v30,v30,v14
v_add_f32 v31,v31,v15
v_mfma_f32_16x16x32_bf16 v[16:19],v[232:235],v[60:63],v[16:19]
ds_read_b128 v[52:55],v2 offset:27648
v_cvt_pk_bf16_f32 v12,v24,v25
v_mfma_f32_16x16x32_bf16 v[16:19],v[120:123],v[56:59],v[16:19]
v_cvt_pk_bf16_f32 v13,v26,v27
s_waitcnt lgkmcnt(4)
v_mfma_f32_16x16x32_bf16 v[16:19],a[96:99],v[32:35],v[16:19]
ds_read_b128 v[56:59],v2 offset:41984
v_cvt_pk_bf16_f32 v14,v28,v29
v_mfma_f32_16x16x32_bf16 v[16:19],a[208:211],v[36:39],v[16:19]
v_cvt_pk_bf16_f32 v15,v30,v31
v_mfma_f32_16x16x32_bf16 v[20:23],a[212:215],v[36:39],v[20:23]
ds_read_b128 v[60:63],v2 offset:56320
v_mfma_f32_16x16x32_bf16 v[20:23],a[100:103],v[32:35],v[20:23]
s_waitcnt lgkmcnt(4)
s_waitcnt vmcnt(10)
s_barrier
v_mfma_f32_16x16x32_bf16 v[20:23],v[132:135],v[40:43],v[20:23]
ds_read_b128 v[32:35],v4 offset:0
v_mfma_f32_16x16x32_bf16 v[20:23],v[244:247],v[44:47],v[20:23]
buffer_store_dwordx4 v[12:15],v2,s[32:35],s68 offen sc1
v_mfma_f32_16x16x32_bf16 v[16:19],v[240:243],v[44:47],v[16:19]
ds_read_b128 v[36:39],v4 offset:14336
v_mfma_f32_16x16x32_bf16 v[16:19],v[128:131],v[40:43],v[16:19]
s_waitcnt lgkmcnt(4)
v_mfma_f32_16x16x32_bf16 v[16:19],a[104:107],v[48:51],v[16:19]
ds_read_b128 v[40:43],v4 offset:28672
v_mfma_f32_16x16x32_bf16 v[16:19],a[216:219],v[52:55],v[16:19]
buffer_store_dwordx2 v[8:9],v2,s[92:95],s67 idxen sc1
v_mfma_f32_16x16x32_bf16 v[20:23],a[220:223],v[52:55],v[20:23]
ds_read_b128 v[44:47],v4 offset:43008
s_mov_b32 s67,s68
v_mfma_f32_16x16x32_bf16 v[20:23],a[108:111],v[48:51],v[20:23]
s_mov_b32 s68,s70
s_waitcnt lgkmcnt(4)
v_mfma_f32_16x16x32_bf16 v[20:23],v[140:143],v[56:59],v[20:23]
ds_read_b128 v[48:51],v4 offset:1024
s_add_u32 s70,s70,1187840
v_mfma_f32_16x16x32_bf16 v[20:23],v[252:255],v[60:63],v[20:23]
v_mfma_f32_16x16x32_bf16 v[16:19],v[248:251],v[60:63],v[16:19]
ds_read_b128 v[52:55],v4 offset:15360
s_sub_u32 s62,s62,1
v_mfma_f32_16x16x32_bf16 v[16:19],v[136:139],v[56:59],v[16:19]
s_cbranch_scc1 .L_dab_0
s_waitcnt lgkmcnt(4)
v_mfma_f32_16x16x32_bf16 v[24:27],a[0:3],v[32:35],0
ds_read_b128 v[56:59],v4 offset:29696
v_dot2_f32_bf16 v10,v12,s72,0
v_mfma_f32_16x16x32_bf16 v[24:27],a[112:115],v[36:39],v[24:27]
v_dot2_f32_bf16 v11,v12,v12,0
v_dot2_f32_bf16 v10,v13,s72,v10
v_mfma_f32_16x16x32_bf16 v[28:31],a[116:119],v[36:39],0
ds_read_b128 v[60:63],v4 offset:44032
v_dot2_f32_bf16 v11,v13,v13,v11
v_mfma_f32_16x16x32_bf16 v[28:31],a[4:7],v[32:35],v[28:31]
v_dot2_f32_bf16 v10,v14,s72,v10
s_waitcnt lgkmcnt(4)
v_mfma_f32_16x16x32_bf16 v[28:31],a[228:231],v[40:43],v[28:31]
ds_read_b128 v[32:35],v4 offset:2048
v_dot2_f32_bf16 v11,v14,v14,v11
v_mfma_f32_16x16x32_bf16 v[28:31],v[148:151],v[44:47],v[28:31]
v_dot2_f32_bf16 v10,v15,s72,v10
v_dot2_f32_bf16 v11,v15,v15,v11
v_mfma_f32_16x16x32_bf16 v[24:27],v[144:147],v[44:47],v[24:27]
ds_read_b128 v[36:39],v4 offset:16384
v_mfma_f32_16x16x32_bf16 v[24:27],a[224:227],v[40:43],v[24:27]
v_permlane16_swap_b32 v10,v11
s_waitcnt lgkmcnt(4)
v_mfma_f32_16x16x32_bf16 v[24:27],a[8:11],v[48:51],v[24:27]
ds_read_b128 v[40:43],v4 offset:30720
v_add_f32 v10,v10,v11
v_mfma_f32_16x16x32_bf16 v[24:27],a[120:123],v[52:55],v[24:27]
v_mfma_f32_16x16x32_bf16 v[28:31],a[124:127],v[52:55],v[28:31]
ds_read_b128 v[44:47],v4 offset:45056
v_permlane32_swap_b32 v10,v11
v_mfma_f32_16x16x32_bf16 v[28:31],a[12:15],v[48:51],v[28:31]
v_add_f32 v10,v10,v11
s_waitcnt lgkmcnt(4)
v_mfma_f32_16x16x32_bf16 v[28:31],a[236:239],v[56:59],v[28:31]
ds_read_b128 v[48:51],v4 offset:3072
)#"
R"#(
v_mfma_f32_16x16x32_bf16 v[28:31],v[156:159],v[60:63],v[28:31]
buffer_store_dword v10,v3,s[32:35],s67 offen offset:1024 sc1
v_mfma_f32_16x16x32_bf16 v[24:27],v[152:155],v[60:63],v[24:27]
ds_read_b128 v[52:55],v4 offset:17408
v_mfma_f32_16x16x32_bf16 v[24:27],a[232:235],v[56:59],v[24:27]
s_waitcnt lgkmcnt(4)
v_mfma_f32_16x16x32_bf16 v[24:27],a[16:19],v[32:35],v[24:27]
ds_read_b128 v[56:59],v4 offset:31744
v_mfma_f32_16x16x32_bf16 v[24:27],a[128:131],v[36:39],v[24:27]
v_mfma_f32_16x16x32_bf16 v[28:31],a[132:135],v[36:39],v[28:31]
ds_read_b128 v[60:63],v4 offset:46080
v_mfma_f32_16x16x32_bf16 v[28:31],a[20:23],v[32:35],v[28:31]
s_waitcnt lgkmcnt(4)
s_waitcnt vmcnt(3)
s_barrier
v_mfma_f32_16x16x32_bf16 v[28:31],a[244:247],v[40:43],v[28:31]
ds_read_b128 v[32:35],v4 offset:4096
s_mov_b32 m0,s74
v_mfma_f32_16x16x32_bf16 v[28:31],v[164:167],v[44:47],v[28:31]
buffer_load_dwordx4 v2,s[32:35],s71 offen offset:0 sc1 lds
v_mfma_f32_16x16x32_bf16 v[24:27],v[160:163],v[44:47],v[24:27]
ds_read_b128 v[36:39],v4 offset:18432
v_mfma_f32_16x16x32_bf16 v[24:27],a[240:243],v[40:43],v[24:27]
s_waitcnt lgkmcnt(4)
v_mfma_f32_16x16x32_bf16 v[24:27],a[24:27],v[48:51],v[24:27]
ds_read_b128 v[40:43],v4 offset:32768
v_mfma_f32_16x16x32_bf16 v[24:27],a[136:139],v[52:55],v[24:27]
buffer_load_dwordx4 v2,s[32:35],s71 offen offset:1024 sc1 lds
s_add_u32 s71,s71,s30
v_mfma_f32_16x16x32_bf16 v[28:31],a[140:143],v[52:55],v[28:31]
ds_read_b128 v[44:47],v4 offset:47104
s_add_u32 m0,s64,0
v_mfma_f32_16x16x32_bf16 v[28:31],a[28:31],v[48:51],v[28:31]
s_waitcnt lgkmcnt(4)
v_mfma_f32_16x16x32_bf16 v[28:31],a[252:255],v[56:59],v[28:31]
ds_read_b128 v[48:51],v4 offset:5120
v_mfma_f32_16x16x32_bf16 v[28:31],v[172:175],v[60:63],v[28:31]
buffer_load_dwordx4 v2,s[32:35],s63 offen offset:0 sc1 lds
v_mfma_f32_16x16x32_bf16 v[24:27],v[168:171],v[60:63],v[24:27]
ds_read_b128 v[52:55],v4 offset:19456
v_mfma_f32_16x16x32_bf16 v[24:27],a[248:251],v[56:59],v[24:27]
s_waitcnt lgkmcnt(4)
v_mfma_f32_16x16x32_bf16 v[24:27],a[32:35],v[32:35],v[24:27]
ds_read_b128 v[56:59],v4 offset:33792
v_mfma_f32_16x16x32_bf16 v[24:27],a[144:147],v[36:39],v[24:27]
buffer_load_dwordx4 v2,s[32:35],s63 offen offset:1024 sc1 lds
v_mfma_f32_16x16x32_bf16 v[28:31],a[148:151],v[36:39],v[28:31]
ds_read_b128 v[60:63],v4 offset:48128
v_mfma_f32_16x16x32_bf16 v[28:31],a[36:39],v[32:35],v[28:31]
s_waitcnt lgkmcnt(4)
v_mfma_f32_16x16x32_bf16 v[28:31],v[68:71],v[40:43],v[28:31]
ds_read_b128 v[32:35],v4 offset:6144
v_mfma_f32_16x16x32_bf16 v[28:31],v[180:183],v[44:47],v[28:31]
buffer_load_dwordx4 v2,s[32:35],s63 offen offset:2048 sc1 lds
v_mfma_f32_16x16x32_bf16 v[24:27],v[176:179],v[44:47],v[24:27]
ds_read_b128 v[36:39],v4 offset:20480
v_mfma_f32_16x16x32_bf16 v[24:27],v[64:67],v[40:43],v[24:27]
s_waitcnt lgkmcnt(4)
v_mfma_f32_16x16x32_bf16 v[24:27],a[40:43],v[48:51],v[24:27]
ds_read_b128 v[40:43],v4 offset:34816
v_mfma_f32_16x16x32_bf16 v[24:27],a[152:155],v[52:55],v[24:27]
buffer_load_dwordx4 v2,s[32:35],s63 offen offset:3072 sc1 lds
s_add_u32 s63,s63,4096
v_mfma_f32_16x16x32_bf16 v[28:31],a[156:159],v[52:55],v[28:31]
ds_read_b128 v[44:47],v4 offset:49152
s_add_u32 m0,s64,4096
v_mfma_f32_16x16x32_bf16 v[28:31],a[44:47],v[48:51],v[28:31]
s_waitcnt lgkmcnt(4)
v_mfma_f32_16x16x32_bf16 v[28:31],v[76:79],v[56:59],v[28:31]
ds_read_b128 v[48:51],v4 offset:7168
v_mfma_f32_16x16x32_bf16 v[28:31],v[188:191],v[60:63],v[28:31]
buffer_load_dwordx4 v2,s[32:35],s63 offen offset:0 sc1 lds
v_mfma_f32_16x16x32_bf16 v[24:27],v[184:187],v[60:63],v[24:27]
ds_read_b128 v[52:55],v4 offset:21504
v_mfma_f32_16x16x32_bf16 v[24:27],v[72:75],v[56:59],v[24:27]
s_waitcnt lgkmcnt(4)
v_mfma_f32_16x16x32_bf16 v[24:27],a[48:51],v[32:35],v[24:27]
ds_read_b128 v[56:59],v4 offset:35840
v_mfma_f32_16x16x32_bf16 v[24:27],a[160:163],v[36:39],v[24:27]
buffer_load_dwordx4 v2,s[32:35],s63 offen offset:1024 sc1 lds
v_mfma_f32_16x16x32_bf16 v[28:31],a[164:167],v[36:39],v[28:31]
ds_read_b128 v[60:63],v4 offset:50176
v_mfma_f32_16x16x32_bf16 v[28:31],a[52:55],v[32:35],v[28:31]
s_waitcnt lgkmcnt(4)
v_mfma_f32_16x16x32_bf16 v[28:31],v[84:87],v[40:43],v[28:31]
ds_read_b128 v[32:35],v4 offset:8192
v_mfma_f32_16x16x32_bf16 v[28:31],v[196:199],v[44:47],v[28:31]
buffer_load_dwordx4 v2,s[32:35],s63 offen offset:2048 sc1 lds
v_mfma_f32_16x16x32_bf16 v[24:27],v[192:195],v[44:47],v[24:27]
ds_read_b128 v[36:39],v4 offset:22528
v_mfma_f32_16x16x32_bf16 v[24:27],v[80:83],v[40:43],v[24:27]
s_waitcnt lgkmcnt(4)
v_mfma_f32_16x16x32_bf16 v[24:27],a[56:59],v[48:51],v[24:27]
ds_read_b128 v[40:43],v4 offset:36864
v_mfma_f32_16x16x32_bf16 v[24:27],a[168:171],v[52:55],v[24:27]
buffer_load_dwordx4 v2,s[32:35],s63 offen offset:3072 sc1 lds
v_mfma_f32_16x16x32_bf16 v[28:31],a[172:175],v[52:55],v[28:31]
ds_read_b128 v[44:47],v4 offset:51200
v_mfma_f32_16x16x32_bf16 v[28:31],a[60:63],v[48:51],v[28:31]
s_waitcnt lgkmcnt(4)
v_mfma_f32_16x16x32_bf16 v[28:31],v[92:95],v[56:59],v[28:31]
ds_read_b128 v[48:51],v4 offset:9216
v_mfma_f32_16x16x32_bf16 v[28:31],v[204:207],v[60:63],v[28:31]
s_add_u32 s63,s63,4096
s_add_u32 m0,s64,8192
v_mfma_f32_16x16x32_bf16 v[24:27],v[200:203],v[60:63],v[24:27]
ds_read_b128 v[52:55],v4 offset:23552
v_mfma_f32_16x16x32_bf16 v[24:27],v[88:91],v[56:59],v[24:27]
s_waitcnt lgkmcnt(4)
v_mfma_f32_16x16x32_bf16 v[24:27],a[64:67],v[32:35],v[24:27]
ds_read_b128 v[56:59],v4 offset:37888
v_mfma_f32_16x16x32_bf16 v[24:27],a[176:179],v[36:39],v[24:27]
buffer_load_dwordx4 v2,s[32:35],s63 offen offset:0 sc1 lds
v_mfma_f32_16x16x32_bf16 v[28:31],a[180:183],v[36:39],v[28:31]
ds_read_b128 v[60:63],v4 offset:52224
v_mfma_f32_16x16x32_bf16 v[28:31],a[68:71],v[32:35],v[28:31]
s_waitcnt lgkmcnt(4)
v_mfma_f32_16x16x32_bf16 v[28:31],v[100:103],v[40:43],v[28:31]
ds_read_b128 v[32:35],v4 offset:10240
v_mfma_f32_16x16x32_bf16 v[28:31],v[212:215],v[44:47],v[28:31]
buffer_load_dwordx4 v2,s[32:35],s63 offen offset:1024 sc1 lds
v_mfma_f32_16x16x32_bf16 v[24:27],v[208:211],v[44:47],v[24:27]
ds_read_b128 v[36:39],v4 offset:24576
v_mfma_f32_16x16x32_bf16 v[24:27],v[96:99],v[40:43],v[24:27]
s_waitcnt lgkmcnt(4)
v_mfma_f32_16x16x32_bf16 v[24:27],a[72:75],v[48:51],v[24:27]
ds_read_b128 v[40:43],v4 offset:38912
s_waitcnt vmcnt(10)
v_mfma_f32_16x16x32_bf16 v[24:27],a[184:187],v[52:55],v[24:27]
buffer_load_dwordx4 v2,s[32:35],s63 offen offset:2048 sc1 lds
ds_read_b128 v[12:15],v5 offset:0
v_mfma_f32_16x16x32_bf16 v[28:31],a[188:191],v[52:55],v[28:31]
ds_read_b128 v[44:47],v4 offset:53248
v_mfma_f32_16x16x32_bf16 v[28:31],a[76:79],v[48:51],v[28:31]
s_waitcnt lgkmcnt(5)
v_mfma_f32_16x16x32_bf16 v[28:31],v[108:111],v[56:59],v[28:31]
ds_read_b128 v[48:51],v4 offset:11264
v_mfma_f32_16x16x32_bf16 v[28:31],v[220:223],v[60:63],v[28:31]
v_mfma_f32_16x16x32_bf16 v[24:27],v[216:219],v[60:63],v[24:27]
ds_read_b128 v[52:55],v4 offset:25600
s_waitcnt lgkmcnt(3)
v_mfma_f32_16x16x32_bf16 v[24:27],v[104:107],v[56:59],v[24:27]
v_add_f32 v16,v16,v12
v_mfma_f32_16x16x32_bf16 v[24:27],a[80:83],v[32:35],v[24:27]
ds_read_b128 v[56:59],v4 offset:39936
v_add_f32 v17,v17,v13
v_mfma_f32_16x16x32_bf16 v[24:27],a[192:195],v[36:39],v[24:27]
buffer_load_dwordx4 v2,s[32:35],s63 offen offset:3072 sc1 lds
s_add_u32 s63,s63,4096
v_mfma_f32_16x16x32_bf16 v[28:31],a[196:199],v[36:39],v[28:31]
ds_read_b128 v[60:63],v4 offset:54272
s_add_u32 m0,s64,12288
v_mfma_f32_16x16x32_bf16 v[28:31],a[84:87],v[32:35],v[28:31]
v_add_f32 v18,v18,v14
s_waitcnt lgkmcnt(4)
v_mfma_f32_16x16x32_bf16 v[28:31],v[116:119],v[40:43],v[28:31]
ds_read_b128 v[32:35],v4 offset:12288
v_add_f32 v19,v19,v15
v_mfma_f32_16x16x32_bf16 v[28:31],v[228:231],v[44:47],v[28:31]
buffer_load_dwordx4 v2,s[32:35],s63 offen offset:0 sc1 lds
ds_read_b128 v[12:15],v5 offset:1024
v_mfma_f32_16x16x32_bf16 v[24:27],v[224:227],v[44:47],v[24:27]
ds_read_b128 v[36:39],v4 offset:26624
v_mfma_f32_16x16x32_bf16 v[24:27],v[112:115],v[40:43],v[24:27]
s_waitcnt lgkmcnt(5)
v_mfma_f32_16x16x32_bf16 v[24:27],a[88:91],v[48:51],v[24:27]
ds_read_b128 v[40:43],v4 offset:40960
v_mfma_f32_16x16x32_bf16 v[24:27],a[200:203],v[52:55],v[24:27]
buffer_load_dwordx4 v2,s[32:35],s63 offen offset:1024 sc1 lds
s_add_u32 s63,s63,s31
v_mfma_f32_16x16x32_bf16 v[28:31],a[204:207],v[52:55],v[28:31]
ds_read_b128 v[44:47],v4 offset:55296
s_waitcnt lgkmcnt(3)
v_mfma_f32_16x16x32_bf16 v[28:31],a[92:95],v[48:51],v[28:31]
v_add_f32 v20,v20,v12
v_mfma_f32_16x16x32_bf16 v[28:31],v[124:127],v[56:59],v[28:31]
ds_read_b128 v[48:51],v4 offset:13312
v_add_f32 v21,v21,v13
v_mfma_f32_16x16x32_bf16 v[28:31],v[236:239],v[60:63],v[28:31]
v_add_f32 v22,v22,v14
v_add_f32 v23,v23,v15
v_mfma_f32_16x16x32_bf16 v[24:27],v[232:235],v[60:63],v[24:27]
ds_read_b128 v[52:55],v4 offset:27648
v_cvt_pk_bf16_f32 v12,v16,v17
v_mfma_f32_16x16x32_bf16 v[24:27],v[120:123],v[56:59],v[24:27]
v_cvt_pk_bf16_f32 v13,v18,v19
s_waitcnt lgkmcnt(4)
v_mfma_f32_16x16x32_bf16 v[24:27],a[96:99],v[32:35],v[24:27]
ds_read_b128 v[56:59],v4 offset:41984
v_cvt_pk_bf16_f32 v14,v20,v21
v_mfma_f32_16x16x32_bf16 v[24:27],a[208:211],v[36:39],v[24:27]
v_cvt_pk_bf16_f32 v15,v22,v23
v_mfma_f32_16x16x32_bf16 v[28:31],a[212:215],v[36:39],v[28:31]
ds_read_b128 v[60:63],v4 offset:56320
v_mfma_f32_16x16x32_bf16 v[28:31],a[100:103],v[32:35],v[28:31]
s_waitcnt lgkmcnt(4)
s_waitcnt vmcnt(10)
s_barrier
v_mfma_f32_16x16x32_bf16 v[28:31],v[132:135],v[40:43],v[28:31]
ds_read_b128 v[32:35],v2 offset:0
v_mfma_f32_16x16x32_bf16 v[28:31],v[244:247],v[44:47],v[28:31]
buffer_store_dwordx4 v[12:15],v2,s[32:35],s69 offen sc1
v_mfma_f32_16x16x32_bf16 v[24:27],v[240:243],v[44:47],v[24:27]
ds_read_b128 v[36:39],v2 offset:14336
v_mfma_f32_16x16x32_bf16 v[24:27],v[128:131],v[40:43],v[24:27]
s_waitcnt lgkmcnt(4)
v_mfma_f32_16x16x32_bf16 v[24:27],a[104:107],v[48:51],v[24:27]
ds_read_b128 v[40:43],v2 offset:28672
v_mfma_f32_16x16x32_bf16 v[24:27],a[216:219],v[52:55],v[24:27]
buffer_store_dwordx2 v[8:9],v2,s[92:95],s67 idxen sc1
v_mfma_f32_16x16x32_bf16 v[28:31],a[220:223],v[52:55],v[28:31]
ds_read_b128 v[44:47],v2 offset:43008
s_mov_b32 s67,s69
v_mfma_f32_16x16x32_bf16 v[28:31],a[108:111],v[48:51],v[28:31]
s_mov_b32 s69,s70
s_waitcnt lgkmcnt(4)
v_mfma_f32_16x16x32_bf16 v[28:31],v[140:143],v[56:59],v[28:31]
ds_read_b128 v[48:51],v2 offset:1024
s_add_u32 s70,s70,1187840
v_mfma_f32_16x16x32_bf16 v[28:31],v[252:255],v[60:63],v[28:31]
v_mfma_f32_16x16x32_bf16 v[24:27],v[248:251],v[60:63],v[24:27]
ds_read_b128 v[52:55],v2 offset:15360
v_mfma_f32_16x16x32_bf16 v[24:27],v[136:139],v[56:59],v[24:27]
s_branch .L_dal_0
.L_dab_0:
v_dot2_f32_bf16 v10,v12,s72,0
v_dot2_f32_bf16 v11,v12,v12,0
v_dot2_f32_bf16 v10,v13,s72,v10
v_dot2_f32_bf16 v11,v13,v13,v11
v_dot2_f32_bf16 v10,v14,s72,v10
v_dot2_f32_bf16 v11,v14,v14,v11
v_dot2_f32_bf16 v10,v15,s72,v10
v_dot2_f32_bf16 v11,v15,v15,v11
s_nop 2
v_permlane16_swap_b32 v10,v11
v_add_f32 v10,v10,v11
s_nop 1
v_permlane32_swap_b32 v10,v11
v_add_f32 v10,v10,v11
buffer_store_dword v10,v3,s[32:35],s67 offen offset:1024 sc1
s_mov_b32 s62,s58
s_mov_b32 s70,s59
s_add_u32 s74,s74,8192
v_lshrrev_b32 v5,8,v2
v_lshl_add_u32 v5,v5,4,s74
v_and_b32 v3,128,v2
v_lshl_add_u32 v5,v3,2,v5
v_and_b32 v3,16,v2
v_lshl_add_u32 v5,v3,4,v5
v_and_b32 v3,96,v2
v_lshl_add_u32 v5,v3,1,v5
v_lshrrev_b32 v3,2,v2
v_add_u32 v3,s74,v3
s_add_u32 s66,s66,2375680
s_add_u32 s63,s63,2375680
s_add_u32 s71,s71,2375680
s_mov_b32 s68,98304
s_mov_b32 s34,79446016
.p2align 6
.L_dbl_0:
s_waitcnt lgkmcnt(4)
v_mfma_f32_16x16x32_bf16 v[24:27],a[0:3],v[32:35],0
ds_read_b128 v[56:59],v4 offset:29696
v_mfma_f32_16x16x32_bf16 v[24:27],a[112:115],v[36:39],v[24:27]
v_mfma_f32_16x16x32_bf16 v[28:31],a[116:119],v[36:39],0
ds_read_b128 v[60:63],v4 offset:44032
v_mfma_f32_16x16x32_bf16 v[28:31],a[4:7],v[32:35],v[28:31]
v_cvt_pk_bf16_f32 v15,v22,v23
s_waitcnt lgkmcnt(4)
v_mfma_f32_16x16x32_bf16 v[28:31],a[228:231],v[40:43],v[28:31]
ds_read_b128 v[32:35],v4 offset:2048
v_cvt_pk_bf16_f32 v14,v20,v21
v_mfma_f32_16x16x32_bf16 v[28:31],v[148:151],v[44:47],v[28:31]
v_cvt_pk_bf16_f32 v13,v18,v19
v_cvt_pk_bf16_f32 v12,v16,v17
v_mfma_f32_16x16x32_bf16 v[24:27],v[144:147],v[44:47],v[24:27]
ds_read_b128 v[36:39],v4 offset:16384
ds_write_b128 v5,v[12:15]
v_mfma_f32_16x16x32_bf16 v[24:27],a[224:227],v[40:43],v[24:27]
ds_read2st64_b32 v[12:13],v3 offset0:0 offset1:1
s_waitcnt lgkmcnt(6)
v_mfma_f32_16x16x32_bf16 v[24:27],a[8:11],v[48:51],v[24:27]
ds_read_b128 v[40:43],v4 offset:30720
ds_read2st64_b32 v[14:15],v3 offset0:2 offset1:3
v_mfma_f32_16x16x32_bf16 v[24:27],a[120:123],v[52:55],v[24:27]
v_mfma_f32_16x16x32_bf16 v[28:31],a[124:127],v[52:55],v[28:31]
ds_read_b128 v[44:47],v4 offset:45056
v_mfma_f32_16x16x32_bf16 v[28:31],a[12:15],v[48:51],v[28:31]
s_waitcnt lgkmcnt(7)
v_mfma_f32_16x16x32_bf16 v[28:31],a[236:239],v[56:59],v[28:31]
ds_read_b128 v[48:51],v4 offset:3072
v_mfma_f32_16x16x32_bf16 v[28:31],v[156:159],v[60:63],v[28:31]
s_waitcnt lgkmcnt(2)
buffer_atomic_pk_add_bf16 v12,v1,s[44:47],s65 offen offset:0 sc1
v_mfma_f32_16x16x32_bf16 v[24:27],v[152:155],v[60:63],v[24:27]
ds_read_b128 v[52:55],v4 offset:17408
s_add_u32 s70,s70,s75
v_mfma_f32_16x16x32_bf16 v[24:27],a[232:235],v[56:59],v[24:27]
buffer_atomic_pk_add_bf16 v13,v1,s[44:47],s65 offen offset:2048 sc1
v_mfma_f32_16x16x32_bf16 v[24:27],a[16:19],v[32:35],v[24:27]
ds_read_b128 v[56:59],v4 offset:31744
s_add_u32 s65,s65,16384
v_mfma_f32_16x16x32_bf16 v[24:27],a[128:131],v[36:39],v[24:27]
buffer_atomic_pk_add_bf16 v14,v1,s[44:47],s65 offen offset:0 sc1
v_mfma_f32_16x16x32_bf16 v[28:31],a[132:135],v[36:39],v[28:31]
ds_read_b128 v[60:63],v4 offset:46080
v_mfma_f32_16x16x32_bf16 v[28:31],a[20:23],v[32:35],v[28:31]
s_waitcnt lgkmcnt(4)
s_waitcnt vmcnt(3)
s_barrier
v_mfma_f32_16x16x32_bf16 v[28:31],a[244:247],v[40:43],v[28:31]
ds_read_b128 v[32:35],v4 offset:4096
s_add_u32 m0,s64,0
v_mfma_f32_16x16x32_bf16 v[28:31],v[164:167],v[44:47],v[28:31]
buffer_atomic_pk_add_bf16 v15,v1,s[44:47],s65 offen offset:2048 sc1
s_and_b32 s65,s70,0x3f8000
v_mfma_f32_16x16x32_bf16 v[24:27],v[160:163],v[44:47],v[24:27]
ds_read_b128 v[36:39],v4 offset:18432
v_mfma_f32_16x16x32_bf16 v[24:27],a[240:243],v[40:43],v[24:27]
s_waitcnt lgkmcnt(4)
v_mfma_f32_16x16x32_bf16 v[24:27],a[24:27],v[48:51],v[24:27]
ds_read_b128 v[40:43],v4 offset:32768
v_mfma_f32_16x16x32_bf16 v[24:27],a[136:139],v[52:55],v[24:27]
buffer_load_dwordx4 v2,s[32:35],s63 offen offset:0 sc1 lds
v_mfma_f32_16x16x32_bf16 v[28:31],a[140:143],v[52:55],v[28:31]
ds_read_b128 v[44:47],v4 offset:47104
v_mfma_f32_16x16x32_bf16 v[28:31],a[28:31],v[48:51],v[28:31]
s_waitcnt lgkmcnt(4)
v_mfma_f32_16x16x32_bf16 v[28:31],a[252:255],v[56:59],v[28:31]
ds_read_b128 v[48:51],v4 offset:5120
v_mfma_f32_16x16x32_bf16 v[28:31],v[172:175],v[60:63],v[28:31]
buffer_load_dwordx4 v2,s[32:35],s63 offen offset:1024 sc1 lds
v_mfma_f32_16x16x32_bf16 v[24:27],v[168:171],v[60:63],v[24:27]
ds_read_b128 v[52:55],v4 offset:19456
v_mfma_f32_16x16x32_bf16 v[24:27],a[248:251],v[56:59],v[24:27]
s_waitcnt lgkmcnt(4)
v_mfma_f32_16x16x32_bf16 v[24:27],a[32:35],v[32:35],v[24:27]
ds_read_b128 v[56:59],v4 offset:33792
v_mfma_f32_16x16x32_bf16 v[24:27],a[144:147],v[36:39],v[24:27]
buffer_load_dwordx4 v2,s[32:35],s63 offen offset:2048 sc1 lds
v_mfma_f32_16x16x32_bf16 v[28:31],a[148:151],v[36:39],v[28:31]
ds_read_b128 v[60:63],v4 offset:48128
v_mfma_f32_16x16x32_bf16 v[28:31],a[36:39],v[32:35],v[28:31]
s_waitcnt lgkmcnt(4)
v_mfma_f32_16x16x32_bf16 v[28:31],v[68:71],v[40:43],v[28:31]
ds_read_b128 v[32:35],v4 offset:6144
v_mfma_f32_16x16x32_bf16 v[28:31],v[180:183],v[44:47],v[28:31]
buffer_load_dwordx4 v2,s[32:35],s63 offen offset:3072 sc1 lds
v_mfma_f32_16x16x32_bf16 v[24:27],v[176:179],v[44:47],v[24:27]
ds_read_b128 v[36:39],v4 offset:20480
v_mfma_f32_16x16x32_bf16 v[24:27],v[64:67],v[40:43],v[24:27]
s_waitcnt lgkmcnt(4)
v_mfma_f32_16x16x32_bf16 v[24:27],a[40:43],v[48:51],v[24:27]
ds_read_b128 v[40:43],v4 offset:34816
s_add_u32 s63,s63,4096
v_mfma_f32_16x16x32_bf16 v[24:27],a[152:155],v[52:55],v[24:27]
s_add_u32 m0,s64,4096
v_mfma_f32_16x16x32_bf16 v[28:31],a[156:159],v[52:55],v[28:31]
ds_read_b128 v[44:47],v4 offset:49152
v_mfma_f32_16x16x32_bf16 v[28:31],a[44:47],v[48:51],v[28:31]
s_waitcnt lgkmcnt(4)
v_mfma_f32_16x16x32_bf16 v[28:31],v[76:79],v[56:59],v[28:31]
ds_read_b128 v[48:51],v4 offset:7168
v_mfma_f32_16x16x32_bf16 v[28:31],v[188:191],v[60:63],v[28:31]
buffer_load_dwordx4 v2,s[32:35],s63 offen offset:0 sc1 lds
v_mfma_f32_16x16x32_bf16 v[24:27],v[184:187],v[60:63],v[24:27]
ds_read_b128 v[52:55],v4 offset:21504
v_mfma_f32_16x16x32_bf16 v[24:27],v[72:75],v[56:59],v[24:27]
s_waitcnt lgkmcnt(4)
v_mfma_f32_16x16x32_bf16 v[24:27],a[48:51],v[32:35],v[24:27]
ds_read_b128 v[56:59],v4 offset:35840
v_mfma_f32_16x16x32_bf16 v[24:27],a[160:163],v[36:39],v[24:27]
buffer_load_dwordx4 v2,s[32:35],s63 offen offset:1024 sc1 lds
v_mfma_f32_16x16x32_bf16 v[28:31],a[164:167],v[36:39],v[28:31]
ds_read_b128 v[60:63],v4 offset:50176
v_mfma_f32_16x16x32_bf16 v[28:31],a[52:55],v[32:35],v[28:31]
s_waitcnt lgkmcnt(4)
v_mfma_f32_16x16x32_bf16 v[28:31],v[84:87],v[40:43],v[28:31]
ds_read_b128 v[32:35],v4 offset:8192
v_mfma_f32_16x16x32_bf16 v[28:31],v[196:199],v[44:47],v[28:31]
buffer_load_dwordx4 v2,s[32:35],s63 offen offset:2048 sc1 lds
v_mfma_f32_16x16x32_bf16 v[24:27],v[192:195],v[44:47],v[24:27]
ds_read_b128 v[36:39],v4 offset:22528
v_mfma_f32_16x16x32_bf16 v[24:27],v[80:83],v[40:43],v[24:27]
s_waitcnt lgkmcnt(4)
v_mfma_f32_16x16x32_bf16 v[24:27],a[56:59],v[48:51],v[24:27]
ds_read_b128 v[40:43],v4 offset:36864
v_mfma_f32_16x16x32_bf16 v[24:27],a[168:171],v[52:55],v[24:27]
buffer_load_dwordx4 v2,s[32:35],s63 offen offset:3072 sc1 lds
v_mfma_f32_16x16x32_bf16 v[28:31],a[172:175],v[52:55],v[28:31]
ds_read_b128 v[44:47],v4 offset:51200
v_mfma_f32_16x16x32_bf16 v[28:31],a[60:63],v[48:51],v[28:31]
s_waitcnt lgkmcnt(4)
v_mfma_f32_16x16x32_bf16 v[28:31],v[92:95],v[56:59],v[28:31]
ds_read_b128 v[48:51],v4 offset:9216
v_mfma_f32_16x16x32_bf16 v[28:31],v[204:207],v[60:63],v[28:31]
s_add_u32 s63,s63,4096
s_add_u32 m0,s64,8192
v_mfma_f32_16x16x32_bf16 v[24:27],v[200:203],v[60:63],v[24:27]
ds_read_b128 v[52:55],v4 offset:23552
v_mfma_f32_16x16x32_bf16 v[24:27],v[88:91],v[56:59],v[24:27]
s_waitcnt lgkmcnt(4)
v_mfma_f32_16x16x32_bf16 v[24:27],a[64:67],v[32:35],v[24:27]
ds_read_b128 v[56:59],v4 offset:37888
v_mfma_f32_16x16x32_bf16 v[24:27],a[176:179],v[36:39],v[24:27]
buffer_load_dwordx4 v2,s[32:35],s63 offen offset:0 sc1 lds
v_mfma_f32_16x16x32_bf16 v[28:31],a[180:183],v[36:39],v[28:31]
ds_read_b128 v[60:63],v4 offset:52224
v_mfma_f32_16x16x32_bf16 v[28:31],a[68:71],v[32:35],v[28:31]
s_waitcnt lgkmcnt(4)
v_mfma_f32_16x16x32_bf16 v[28:31],v[100:103],v[40:43],v[28:31]
ds_read_b128 v[32:35],v4 offset:10240
v_mfma_f32_16x16x32_bf16 v[28:31],v[212:215],v[44:47],v[28:31]
buffer_load_dwordx4 v2,s[32:35],s63 offen offset:1024 sc1 lds
v_mfma_f32_16x16x32_bf16 v[24:27],v[208:211],v[44:47],v[24:27]
ds_read_b128 v[36:39],v4 offset:24576
v_mfma_f32_16x16x32_bf16 v[24:27],v[96:99],v[40:43],v[24:27]
s_waitcnt lgkmcnt(4)
v_mfma_f32_16x16x32_bf16 v[24:27],a[72:75],v[48:51],v[24:27]
ds_read_b128 v[40:43],v4 offset:38912
v_mfma_f32_16x16x32_bf16 v[24:27],a[184:187],v[52:55],v[24:27]
buffer_load_dwordx4 v2,s[32:35],s63 offen offset:2048 sc1 lds
v_mfma_f32_16x16x32_bf16 v[28:31],a[188:191],v[52:55],v[28:31]
ds_read_b128 v[44:47],v4 offset:53248
v_mfma_f32_16x16x32_bf16 v[28:31],a[76:79],v[48:51],v[28:31]
s_waitcnt lgkmcnt(4)
v_mfma_f32_16x16x32_bf16 v[28:31],v[108:111],v[56:59],v[28:31]
ds_read_b128 v[48:51],v4 offset:11264
v_mfma_f32_16x16x32_bf16 v[28:31],v[220:223],v[60:63],v[28:31]
v_mfma_f32_16x16x32_bf16 v[24:27],v[216:219],v[60:63],v[24:27]
ds_read_b128 v[52:55],v4 offset:25600
v_mfma_f32_16x16x32_bf16 v[24:27],v[104:107],v[56:59],v[24:27]
s_waitcnt lgkmcnt(4)
v_mfma_f32_16x16x32_bf16 v[24:27],a[80:83],v[32:35],v[24:27]
ds_read_b128 v[56:59],v4 offset:39936
v_mfma_f32_16x16x32_bf16 v[24:27],a[192:195],v[36:39],v[24:27]
buffer_load_dwordx4 v2,s[32:35],s63 offen offset:3072 sc1 lds
s_add_u32 s63,s63,4096
v_mfma_f32_16x16x32_bf16 v[28:31],a[196:199],v[36:39],v[28:31]
ds_read_b128 v[60:63],v4 offset:54272
s_add_u32 m0,s64,12288
v_mfma_f32_16x16x32_bf16 v[28:31],a[84:87],v[32:35],v[28:31]
s_waitcnt lgkmcnt(4)
v_mfma_f32_16x16x32_bf16 v[28:31],v[116:119],v[40:43],v[28:31]
ds_read_b128 v[32:35],v4 offset:12288
v_mfma_f32_16x16x32_bf16 v[28:31],v[228:231],v[44:47],v[28:31]
buffer_load_dwordx4 v2,s[32:35],s63 offen offset:0 sc1 lds
v_mfma_f32_16x16x32_bf16 v[24:27],v[224:227],v[44:47],v[24:27]
ds_read_b128 v[36:39],v4 offset:26624
v_mfma_f32_16x16x32_bf16 v[24:27],v[112:115],v[40:43],v[24:27]
s_waitcnt lgkmcnt(4)
v_mfma_f32_16x16x32_bf16 v[24:27],a[88:91],v[48:51],v[24:27]
ds_read_b128 v[40:43],v4 offset:40960
v_mfma_f32_16x16x32_bf16 v[24:27],a[200:203],v[52:55],v[24:27]
buffer_load_dwordx4 v2,s[32:35],s63 offen offset:1024 sc1 lds
s_add_u32 s63,s63,s31
v_mfma_f32_16x16x32_bf16 v[28:31],a[204:207],v[52:55],v[28:31]
ds_read_b128 v[44:47],v4 offset:55296
v_mfma_f32_16x16x32_bf16 v[28:31],a[92:95],v[48:51],v[28:31]
s_waitcnt lgkmcnt(4)
v_mfma_f32_16x16x32_bf16 v[28:31],v[124:127],v[56:59],v[28:31]
ds_read_b128 v[48:51],v4 offset:13312
v_mfma_f32_16x16x32_bf16 v[28:31],v[236:239],v[60:63],v[28:31]
v_mfma_f32_16x16x32_bf16 v[24:27],v[232:235],v[60:63],v[24:27]
ds_read_b128 v[52:55],v4 offset:27648
v_mfma_f32_16x16x32_bf16 v[24:27],v[120:123],v[56:59],v[24:27]
s_waitcnt lgkmcnt(4)
v_mfma_f32_16x16x32_bf16 v[24:27],a[96:99],v[32:35],v[24:27]
ds_read_b128 v[56:59],v4 offset:41984
v_mfma_f32_16x16x32_bf16 v[24:27],a[208:211],v[36:39],v[24:27]
v_mfma_f32_16x16x32_bf16 v[28:31],a[212:215],v[36:39],v[28:31]
ds_read_b128 v[60:63],v4 offset:56320
v_mfma_f32_16x16x32_bf16 v[28:31],a[100:103],v[32:35],v[28:31]
s_waitcnt lgkmcnt(4)
s_waitcnt vmcnt(10)
s_barrier
v_mfma_f32_16x16x32_bf16 v[28:31],v[132:135],v[40:43],v[28:31]
ds_read_b128 v[32:35],v2 offset:0
v_mfma_f32_16x16x32_bf16 v[28:31],v[244:247],v[44:47],v[28:31]
v_mfma_f32_16x16x32_bf16 v[24:27],v[240:243],v[44:47],v[24:27]
ds_read_b128 v[36:39],v2 offset:14336
v_mfma_f32_16x16x32_bf16 v[24:27],v[128:131],v[40:43],v[24:27]
s_waitcnt lgkmcnt(4)
v_mfma_f32_16x16x32_bf16 v[24:27],a[104:107],v[48:51],v[24:27]
ds_read_b128 v[40:43],v2 offset:28672
v_mfma_f32_16x16x32_bf16 v[24:27],a[216:219],v[52:55],v[24:27]
buffer_store_dwordx2 v[8:9],v2,s[92:95],s67 idxen sc1
v_mfma_f32_16x16x32_bf16 v[28:31],a[220:223],v[52:55],v[28:31]
ds_read_b128 v[44:47],v2 offset:43008
v_mfma_f32_16x16x32_bf16 v[28:31],a[108:111],v[48:51],v[28:31]
s_waitcnt lgkmcnt(4)
v_mfma_f32_16x16x32_bf16 v[28:31],v[140:143],v[56:59],v[28:31]
ds_read_b128 v[48:51],v2 offset:1024
v_mfma_f32_16x16x32_bf16 v[28:31],v[252:255],v[60:63],v[28:31]
v_mfma_f32_16x16x32_bf16 v[24:27],v[248:251],v[60:63],v[24:27]
ds_read_b128 v[52:55],v2 offset:15360
s_sub_u32 s62,s62,1
v_mfma_f32_16x16x32_bf16 v[24:27],v[136:139],v[56:59],v[24:27]
s_cbranch_scc1 .L_dbt_0
s_waitcnt lgkmcnt(4)
)#"
R"#(
v_mfma_f32_16x16x32_bf16 v[16:19],a[0:3],v[32:35],0
ds_read_b128 v[56:59],v2 offset:29696
v_mfma_f32_16x16x32_bf16 v[16:19],a[112:115],v[36:39],v[16:19]
v_mfma_f32_16x16x32_bf16 v[20:23],a[116:119],v[36:39],0
ds_read_b128 v[60:63],v2 offset:44032
v_mfma_f32_16x16x32_bf16 v[20:23],a[4:7],v[32:35],v[20:23]
v_cvt_pk_bf16_f32 v15,v30,v31
s_waitcnt lgkmcnt(4)
v_mfma_f32_16x16x32_bf16 v[20:23],a[228:231],v[40:43],v[20:23]
ds_read_b128 v[32:35],v2 offset:2048
v_cvt_pk_bf16_f32 v14,v28,v29
v_mfma_f32_16x16x32_bf16 v[20:23],v[148:151],v[44:47],v[20:23]
v_cvt_pk_bf16_f32 v13,v26,v27
v_cvt_pk_bf16_f32 v12,v24,v25
v_mfma_f32_16x16x32_bf16 v[16:19],v[144:147],v[44:47],v[16:19]
ds_read_b128 v[36:39],v2 offset:16384
ds_write_b128 v5,v[12:15]
v_mfma_f32_16x16x32_bf16 v[16:19],a[224:227],v[40:43],v[16:19]
ds_read2st64_b32 v[12:13],v3 offset0:0 offset1:1
s_waitcnt lgkmcnt(6)
v_mfma_f32_16x16x32_bf16 v[16:19],a[8:11],v[48:51],v[16:19]
ds_read_b128 v[40:43],v2 offset:30720
ds_read2st64_b32 v[14:15],v3 offset0:2 offset1:3
v_mfma_f32_16x16x32_bf16 v[16:19],a[120:123],v[52:55],v[16:19]
v_mfma_f32_16x16x32_bf16 v[20:23],a[124:127],v[52:55],v[20:23]
ds_read_b128 v[44:47],v2 offset:45056
v_mfma_f32_16x16x32_bf16 v[20:23],a[12:15],v[48:51],v[20:23]
s_waitcnt lgkmcnt(7)
v_mfma_f32_16x16x32_bf16 v[20:23],a[236:239],v[56:59],v[20:23]
ds_read_b128 v[48:51],v2 offset:3072
v_mfma_f32_16x16x32_bf16 v[20:23],v[156:159],v[60:63],v[20:23]
s_waitcnt lgkmcnt(2)
buffer_atomic_pk_add_bf16 v12,v1,s[44:47],s65 offen offset:0 sc1
v_mfma_f32_16x16x32_bf16 v[16:19],v[152:155],v[60:63],v[16:19]
ds_read_b128 v[52:55],v2 offset:17408
s_add_u32 s70,s70,s68
v_mfma_f32_16x16x32_bf16 v[16:19],a[232:235],v[56:59],v[16:19]
buffer_atomic_pk_add_bf16 v13,v1,s[44:47],s65 offen offset:2048 sc1
v_mfma_f32_16x16x32_bf16 v[16:19],a[16:19],v[32:35],v[16:19]
ds_read_b128 v[56:59],v2 offset:31744
s_add_u32 s65,s65,16384
v_mfma_f32_16x16x32_bf16 v[16:19],a[128:131],v[36:39],v[16:19]
buffer_atomic_pk_add_bf16 v14,v1,s[44:47],s65 offen offset:0 sc1
s_mov_b32 s68,s75
v_mfma_f32_16x16x32_bf16 v[20:23],a[132:135],v[36:39],v[20:23]
ds_read_b128 v[60:63],v2 offset:46080
v_mfma_f32_16x16x32_bf16 v[20:23],a[20:23],v[32:35],v[20:23]
s_waitcnt lgkmcnt(4)
s_waitcnt vmcnt(4)
s_barrier
v_mfma_f32_16x16x32_bf16 v[20:23],a[244:247],v[40:43],v[20:23]
ds_read_b128 v[32:35],v2 offset:4096
s_add_u32 m0,s64,57344
v_mfma_f32_16x16x32_bf16 v[20:23],v[164:167],v[44:47],v[20:23]
buffer_atomic_pk_add_bf16 v15,v1,s[44:47],s65 offen offset:2048 sc1
s_and_b32 s65,s70,0x3f8000
v_mfma_f32_16x16x32_bf16 v[16:19],v[160:163],v[44:47],v[16:19]
ds_read_b128 v[36:39],v2 offset:18432
v_mfma_f32_16x16x32_bf16 v[16:19],a[240:243],v[40:43],v[16:19]
s_waitcnt lgkmcnt(4)
v_mfma_f32_16x16x32_bf16 v[16:19],a[24:27],v[48:51],v[16:19]
ds_read_b128 v[40:43],v2 offset:32768
v_mfma_f32_16x16x32_bf16 v[16:19],a[136:139],v[52:55],v[16:19]
buffer_load_dwordx4 v2,s[32:35],s63 offen offset:0 sc1 lds
v_mfma_f32_16x16x32_bf16 v[20:23],a[140:143],v[52:55],v[20:23]
ds_read_b128 v[44:47],v2 offset:47104
v_mfma_f32_16x16x32_bf16 v[20:23],a[28:31],v[48:51],v[20:23]
s_waitcnt lgkmcnt(4)
v_mfma_f32_16x16x32_bf16 v[20:23],a[252:255],v[56:59],v[20:23]
ds_read_b128 v[48:51],v2 offset:5120
v_mfma_f32_16x16x32_bf16 v[20:23],v[172:175],v[60:63],v[20:23]
buffer_load_dwordx4 v2,s[32:35],s63 offen offset:1024 sc1 lds
v_mfma_f32_16x16x32_bf16 v[16:19],v[168:171],v[60:63],v[16:19]
ds_read_b128 v[52:55],v2 offset:19456
v_mfma_f32_16x16x32_bf16 v[16:19],a[248:251],v[56:59],v[16:19]
s_waitcnt lgkmcnt(4)
v_mfma_f32_16x16x32_bf16 v[16:19],a[32:35],v[32:35],v[16:19]
ds_read_b128 v[56:59],v2 offset:33792
v_mfma_f32_16x16x32_bf16 v[16:19],a[144:147],v[36:39],v[16:19]
buffer_load_dwordx4 v2,s[32:35],s63 offen offset:2048 sc1 lds
v_mfma_f32_16x16x32_bf16 v[20:23],a[148:151],v[36:39],v[20:23]
ds_read_b128 v[60:63],v2 offset:48128
v_mfma_f32_16x16x32_bf16 v[20:23],a[36:39],v[32:35],v[20:23]
s_waitcnt lgkmcnt(4)
v_mfma_f32_16x16x32_bf16 v[20:23],v[68:71],v[40:43],v[20:23]
ds_read_b128 v[32:35],v2 offset:6144
v_mfma_f32_16x16x32_bf16 v[20:23],v[180:183],v[44:47],v[20:23]
buffer_load_dwordx4 v2,s[32:35],s63 offen offset:3072 sc1 lds
v_mfma_f32_16x16x32_bf16 v[16:19],v[176:179],v[44:47],v[16:19]
ds_read_b128 v[36:39],v2 offset:20480
v_mfma_f32_16x16x32_bf16 v[16:19],v[64:67],v[40:43],v[16:19]
s_waitcnt lgkmcnt(4)
v_mfma_f32_16x16x32_bf16 v[16:19],a[40:43],v[48:51],v[16:19]
ds_read_b128 v[40:43],v2 offset:34816
s_add_u32 s63,s63,4096
v_mfma_f32_16x16x32_bf16 v[16:19],a[152:155],v[52:55],v[16:19]
s_add_u32 m0,s64,61440
v_mfma_f32_16x16x32_bf16 v[20:23],a[156:159],v[52:55],v[20:23]
ds_read_b128 v[44:47],v2 offset:49152
v_mfma_f32_16x16x32_bf16 v[20:23],a[44:47],v[48:51],v[20:23]
s_waitcnt lgkmcnt(4)
v_mfma_f32_16x16x32_bf16 v[20:23],v[76:79],v[56:59],v[20:23]
ds_read_b128 v[48:51],v2 offset:7168
v_mfma_f32_16x16x32_bf16 v[20:23],v[188:191],v[60:63],v[20:23]
buffer_load_dwordx4 v2,s[32:35],s63 offen offset:0 sc1 lds
v_mfma_f32_16x16x32_bf16 v[16:19],v[184:187],v[60:63],v[16:19]
ds_read_b128 v[52:55],v2 offset:21504
v_mfma_f32_16x16x32_bf16 v[16:19],v[72:75],v[56:59],v[16:19]
s_waitcnt lgkmcnt(4)
v_mfma_f32_16x16x32_bf16 v[16:19],a[48:51],v[32:35],v[16:19]
ds_read_b128 v[56:59],v2 offset:35840
v_mfma_f32_16x16x32_bf16 v[16:19],a[160:163],v[36:39],v[16:19]
buffer_load_dwordx4 v2,s[32:35],s63 offen offset:1024 sc1 lds
v_mfma_f32_16x16x32_bf16 v[20:23],a[164:167],v[36:39],v[20:23]
ds_read_b128 v[60:63],v2 offset:50176
v_mfma_f32_16x16x32_bf16 v[20:23],a[52:55],v[32:35],v[20:23]
s_waitcnt lgkmcnt(4)
v_mfma_f32_16x16x32_bf16 v[20:23],v[84:87],v[40:43],v[20:23]
ds_read_b128 v[32:35],v2 offset:8192
v_mfma_f32_16x16x32_bf16 v[20:23],v[196:199],v[44:47],v[20:23]
buffer_load_dwordx4 v2,s[32:35],s63 offen offset:2048 sc1 lds
v_mfma_f32_16x16x32_bf16 v[16:19],v[192:195],v[44:47],v[16:19]
ds_read_b128 v[36:39],v2 offset:22528
v_mfma_f32_16x16x32_bf16 v[16:19],v[80:83],v[40:43],v[16:19]
s_waitcnt lgkmcnt(4)
v_mfma_f32_16x16x32_bf16 v[16:19],a[56:59],v[48:51],v[16:19]
ds_read_b128 v[40:43],v2 offset:36864
v_mfma_f32_16x16x32_bf16 v[16:19],a[168:171],v[52:55],v[16:19]
buffer_load_dwordx4 v2,s[32:35],s63 offen offset:3072 sc1 lds
v_mfma_f32_16x16x32_bf16 v[20:23],a[172:175],v[52:55],v[20:23]
ds_read_b128 v[44:47],v2 offset:51200
v_mfma_f32_16x16x32_bf16 v[20:23],a[60:63],v[48:51],v[20:23]
s_waitcnt lgkmcnt(4)
v_mfma_f32_16x16x32_bf16 v[20:23],v[92:95],v[56:59],v[20:23]
ds_read_b128 v[48:51],v2 offset:9216
v_mfma_f32_16x16x32_bf16 v[20:23],v[204:207],v[60:63],v[20:23]
s_add_u32 s63,s63,4096
s_add_u32 m0,s64,65536
v_mfma_f32_16x16x32_bf16 v[16:19],v[200:203],v[60:63],v[16:19]
ds_read_b128 v[52:55],v2 offset:23552
v_mfma_f32_16x16x32_bf16 v[16:19],v[88:91],v[56:59],v[16:19]
s_waitcnt lgkmcnt(4)
v_mfma_f32_16x16x32_bf16 v[16:19],a[64:67],v[32:35],v[16:19]
ds_read_b128 v[56:59],v2 offset:37888
v_mfma_f32_16x16x32_bf16 v[16:19],a[176:179],v[36:39],v[16:19]
buffer_load_dwordx4 v2,s[32:35],s63 offen offset:0 sc1 lds
v_mfma_f32_16x16x32_bf16 v[20:23],a[180:183],v[36:39],v[20:23]
ds_read_b128 v[60:63],v2 offset:52224
v_mfma_f32_16x16x32_bf16 v[20:23],a[68:71],v[32:35],v[20:23]
s_waitcnt lgkmcnt(4)
v_mfma_f32_16x16x32_bf16 v[20:23],v[100:103],v[40:43],v[20:23]
ds_read_b128 v[32:35],v2 offset:10240
v_mfma_f32_16x16x32_bf16 v[20:23],v[212:215],v[44:47],v[20:23]
buffer_load_dwordx4 v2,s[32:35],s63 offen offset:1024 sc1 lds
v_mfma_f32_16x16x32_bf16 v[16:19],v[208:211],v[44:47],v[16:19]
ds_read_b128 v[36:39],v2 offset:24576
v_mfma_f32_16x16x32_bf16 v[16:19],v[96:99],v[40:43],v[16:19]
s_waitcnt lgkmcnt(4)
v_mfma_f32_16x16x32_bf16 v[16:19],a[72:75],v[48:51],v[16:19]
ds_read_b128 v[40:43],v2 offset:38912
v_mfma_f32_16x16x32_bf16 v[16:19],a[184:187],v[52:55],v[16:19]
buffer_load_dwordx4 v2,s[32:35],s63 offen offset:2048 sc1 lds
v_mfma_f32_16x16x32_bf16 v[20:23],a[188:191],v[52:55],v[20:23]
ds_read_b128 v[44:47],v2 offset:53248
v_mfma_f32_16x16x32_bf16 v[20:23],a[76:79],v[48:51],v[20:23]
s_waitcnt lgkmcnt(4)
v_mfma_f32_16x16x32_bf16 v[20:23],v[108:111],v[56:59],v[20:23]
ds_read_b128 v[48:51],v2 offset:11264
v_mfma_f32_16x16x32_bf16 v[20:23],v[220:223],v[60:63],v[20:23]
v_mfma_f32_16x16x32_bf16 v[16:19],v[216:219],v[60:63],v[16:19]
ds_read_b128 v[52:55],v2 offset:25600
v_mfma_f32_16x16x32_bf16 v[16:19],v[104:107],v[56:59],v[16:19]
s_waitcnt lgkmcnt(4)
v_mfma_f32_16x16x32_bf16 v[16:19],a[80:83],v[32:35],v[16:19]
ds_read_b128 v[56:59],v2 offset:39936
v_mfma_f32_16x16x32_bf16 v[16:19],a[192:195],v[36:39],v[16:19]
buffer_load_dwordx4 v2,s[32:35],s63 offen offset:3072 sc1 lds
s_add_u32 s63,s63,4096
v_mfma_f32_16x16x32_bf16 v[20:23],a[196:199],v[36:39],v[20:23]
ds_read_b128 v[60:63],v2 offset:54272
s_add_u32 m0,s64,69632
v_mfma_f32_16x16x32_bf16 v[20:23],a[84:87],v[32:35],v[20:23]
s_waitcnt lgkmcnt(4)
v_mfma_f32_16x16x32_bf16 v[20:23],v[116:119],v[40:43],v[20:23]
ds_read_b128 v[32:35],v2 offset:12288
v_mfma_f32_16x16x32_bf16 v[20:23],v[228:231],v[44:47],v[20:23]
buffer_load_dwordx4 v2,s[32:35],s63 offen offset:0 sc1 lds
v_mfma_f32_16x16x32_bf16 v[16:19],v[224:227],v[44:47],v[16:19]
ds_read_b128 v[36:39],v2 offset:26624
v_mfma_f32_16x16x32_bf16 v[16:19],v[112:115],v[40:43],v[16:19]
s_waitcnt lgkmcnt(4)
v_mfma_f32_16x16x32_bf16 v[16:19],a[88:91],v[48:51],v[16:19]
ds_read_b128 v[40:43],v2 offset:40960
v_mfma_f32_16x16x32_bf16 v[16:19],a[200:203],v[52:55],v[16:19]
buffer_load_dwordx4 v2,s[32:35],s63 offen offset:1024 sc1 lds
s_add_u32 s63,s63,s31
v_mfma_f32_16x16x32_bf16 v[20:23],a[204:207],v[52:55],v[20:23]
ds_read_b128 v[44:47],v2 offset:55296
v_mfma_f32_16x16x32_bf16 v[20:23],a[92:95],v[48:51],v[20:23]
s_waitcnt lgkmcnt(4)
v_mfma_f32_16x16x32_bf16 v[20:23],v[124:127],v[56:59],v[20:23]
ds_read_b128 v[48:51],v2 offset:13312
v_mfma_f32_16x16x32_bf16 v[20:23],v[236:239],v[60:63],v[20:23]
v_mfma_f32_16x16x32_bf16 v[16:19],v[232:235],v[60:63],v[16:19]
ds_read_b128 v[52:55],v2 offset:27648
v_mfma_f32_16x16x32_bf16 v[16:19],v[120:123],v[56:59],v[16:19]
s_waitcnt lgkmcnt(4)
v_mfma_f32_16x16x32_bf16 v[16:19],a[96:99],v[32:35],v[16:19]
ds_read_b128 v[56:59],v2 offset:41984
v_mfma_f32_16x16x32_bf16 v[16:19],a[208:211],v[36:39],v[16:19]
v_mfma_f32_16x16x32_bf16 v[20:23],a[212:215],v[36:39],v[20:23]
ds_read_b128 v[60:63],v2 offset:56320
v_mfma_f32_16x16x32_bf16 v[20:23],a[100:103],v[32:35],v[20:23]
s_waitcnt lgkmcnt(4)
s_waitcnt vmcnt(10)
s_barrier
v_mfma_f32_16x16x32_bf16 v[20:23],v[132:135],v[40:43],v[20:23]
ds_read_b128 v[32:35],v4 offset:0
v_mfma_f32_16x16x32_bf16 v[20:23],v[244:247],v[44:47],v[20:23]
v_mfma_f32_16x16x32_bf16 v[16:19],v[240:243],v[44:47],v[16:19]
ds_read_b128 v[36:39],v4 offset:14336
v_mfma_f32_16x16x32_bf16 v[16:19],v[128:131],v[40:43],v[16:19]
s_waitcnt lgkmcnt(4)
v_mfma_f32_16x16x32_bf16 v[16:19],a[104:107],v[48:51],v[16:19]
ds_read_b128 v[40:43],v4 offset:28672
v_mfma_f32_16x16x32_bf16 v[16:19],a[216:219],v[52:55],v[16:19]
v_mfma_f32_16x16x32_bf16 v[20:23],a[220:223],v[52:55],v[20:23]
ds_read_b128 v[44:47],v4 offset:43008
v_mfma_f32_16x16x32_bf16 v[20:23],a[108:111],v[48:51],v[20:23]
s_waitcnt lgkmcnt(4)
v_mfma_f32_16x16x32_bf16 v[20:23],v[140:143],v[56:59],v[20:23]
ds_read_b128 v[48:51],v4 offset:1024
v_mfma_f32_16x16x32_bf16 v[20:23],v[252:255],v[60:63],v[20:23]
v_mfma_f32_16x16x32_bf16 v[16:19],v[248:251],v[60:63],v[16:19]
ds_read_b128 v[52:55],v4 offset:15360
s_sub_u32 s62,s62,1
v_mfma_f32_16x16x32_bf16 v[16:19],v[136:139],v[56:59],v[16:19]
s_cbranch_scc0 .L_dbl_0
.L_dbu_0:
s_nop 1
s_nop 0
v_cvt_pk_bf16_f32 v15,v22,v23
v_cvt_pk_bf16_f32 v14,v20,v21
s_nop 1
v_cvt_pk_bf16_f32 v13,v18,v19
v_cvt_pk_bf16_f32 v12,v16,v17
ds_write_b128 v5,v[12:15]
ds_read2st64_b32 v[12:13],v3 offset0:0 offset1:1
ds_read2st64_b32 v[14:15],v3 offset0:2 offset1:3
s_waitcnt lgkmcnt(0)
buffer_atomic_pk_add_bf16 v12,v1,s[44:47],s65 offen offset:0 sc1
s_add_u32 s70,s70,s75
buffer_atomic_pk_add_bf16 v13,v1,s[44:47],s65 offen offset:2048 sc1
s_add_u32 s65,s65,16384
buffer_atomic_pk_add_bf16 v14,v1,s[44:47],s65 offen offset:0 sc1
buffer_atomic_pk_add_bf16 v15,v1,s[44:47],s65 offen offset:2048 sc1
s_and_b32 s65,s70,0x3f8000
s_or_b32 s48,s28,s27
s_or_b32 s48,s48,s26
s_cbranch_scc1 .L_end
v_mov_b32 v8,s54
v_mov_b32 v9,s55
buffer_store_dwordx2 v[8:9],off,s[32:35],0 sc1
s_branch .L_end
.L_dbt_0:
s_nop 1
s_nop 0
v_cvt_pk_bf16_f32 v15,v30,v31
v_cvt_pk_bf16_f32 v14,v28,v29
s_nop 1
v_cvt_pk_bf16_f32 v13,v26,v27
v_cvt_pk_bf16_f32 v12,v24,v25
ds_write_b128 v5,v[12:15]
ds_read2st64_b32 v[12:13],v3 offset0:0 offset1:1
ds_read2st64_b32 v[14:15],v3 offset0:2 offset1:3
s_waitcnt lgkmcnt(0)
buffer_atomic_pk_add_bf16 v12,v1,s[44:47],s65 offen offset:0 sc1
s_add_u32 s70,s70,s68
buffer_atomic_pk_add_bf16 v13,v1,s[44:47],s65 offen offset:2048 sc1
s_add_u32 s65,s65,16384
buffer_atomic_pk_add_bf16 v14,v1,s[44:47],s65 offen offset:0 sc1
s_mov_b32 s68,s75
buffer_atomic_pk_add_bf16 v15,v1,s[44:47],s65 offen offset:2048 sc1
s_and_b32 s65,s70,0x3f8000
s_or_b32 s48,s28,s27
s_or_b32 s48,s48,s26
s_cbranch_scc1 .L_end
v_mov_b32 v8,s54
v_mov_b32 v9,s55
buffer_store_dwordx2 v[8:9],off,s[32:35],0 sc1
s_branch .L_end
.p2align 6
.L_dhv1:
.L_dal_1:
s_waitcnt lgkmcnt(4)
v_mfma_f32_16x16x32_bf16 v[16:19],a[0:3],v[32:35],0
ds_read_b128 v[56:59],v2 offset:29696
v_dot2_f32_bf16 v10,v12,s72,0
v_mfma_f32_16x16x32_bf16 v[16:19],a[112:115],v[36:39],v[16:19]
v_dot2_f32_bf16 v11,v12,v12,0
v_dot2_f32_bf16 v10,v13,s72,v10
v_mfma_f32_16x16x32_bf16 v[20:23],a[116:119],v[36:39],0
ds_read_b128 v[60:63],v2 offset:44032
v_dot2_f32_bf16 v11,v13,v13,v11
v_mfma_f32_16x16x32_bf16 v[20:23],a[4:7],v[32:35],v[20:23]
v_dot2_f32_bf16 v10,v14,s72,v10
s_waitcnt lgkmcnt(4)
v_mfma_f32_16x16x32_bf16 v[20:23],a[228:231],v[40:43],v[20:23]
ds_read_b128 v[32:35],v2 offset:2048
v_dot2_f32_bf16 v11,v14,v14,v11
v_mfma_f32_16x16x32_bf16 v[20:23],v[148:151],v[44:47],v[20:23]
v_dot2_f32_bf16 v10,v15,s72,v10
v_dot2_f32_bf16 v11,v15,v15,v11
v_mfma_f32_16x16x32_bf16 v[16:19],v[144:147],v[44:47],v[16:19]
ds_read_b128 v[36:39],v2 offset:16384
v_mfma_f32_16x16x32_bf16 v[16:19],a[224:227],v[40:43],v[16:19]
v_permlane16_swap_b32 v10,v11
s_waitcnt lgkmcnt(4)
v_mfma_f32_16x16x32_bf16 v[16:19],a[8:11],v[48:51],v[16:19]
ds_read_b128 v[40:43],v2 offset:30720
v_add_f32 v10,v10,v11
v_mfma_f32_16x16x32_bf16 v[16:19],a[120:123],v[52:55],v[16:19]
v_mfma_f32_16x16x32_bf16 v[20:23],a[124:127],v[52:55],v[20:23]
ds_read_b128 v[44:47],v2 offset:45056
v_permlane32_swap_b32 v10,v11
v_mfma_f32_16x16x32_bf16 v[20:23],a[12:15],v[48:51],v[20:23]
v_add_f32 v10,v10,v11
s_waitcnt lgkmcnt(4)
v_mfma_f32_16x16x32_bf16 v[20:23],a[236:239],v[56:59],v[20:23]
ds_read_b128 v[48:51],v2 offset:3072
v_mfma_f32_16x16x32_bf16 v[20:23],v[156:159],v[60:63],v[20:23]
buffer_store_dword v10,v3,s[32:35],s67 offen offset:1024 sc1
v_mfma_f32_16x16x32_bf16 v[16:19],v[152:155],v[60:63],v[16:19]
ds_read_b128 v[52:55],v2 offset:17408
s_waitcnt vmcnt(5)
v_mfma_f32_16x16x32_bf16 v[16:19],a[232:235],v[56:59],v[16:19]
v_cmp_ne_u64 vcc,v[8:9],v[6:7]
s_waitcnt lgkmcnt(4)
v_mfma_f32_16x16x32_bf16 v[16:19],a[16:19],v[32:35],v[16:19]
ds_read_b128 v[56:59],v2 offset:31744
s_cbranch_vccnz .L_rta0_1
.L_bka0_1:
v_mfma_f32_16x16x32_bf16 v[16:19],a[128:131],v[36:39],v[16:19]
s_add_u32 s66,s66,s30
v_mfma_f32_16x16x32_bf16 v[20:23],a[132:135],v[36:39],v[20:23]
s_waitcnt vmcnt(2)
s_barrier
v_mfma_f32_16x16x32_bf16 v[20:23],a[20:23],v[32:35],v[20:23]
ds_read_b128 v[60:63],v2 offset:46080
s_waitcnt lgkmcnt(4)
v_mfma_f32_16x16x32_bf16 v[20:23],a[244:247],v[40:43],v[20:23]
ds_read_b128 v[32:35],v2 offset:4096
s_mov_b32 m0,s74
v_mfma_f32_16x16x32_bf16 v[20:23],v[164:167],v[44:47],v[20:23]
buffer_load_dwordx4 v2,s[32:35],s71 offen offset:0 sc1 lds
v_mfma_f32_16x16x32_bf16 v[16:19],v[160:163],v[44:47],v[16:19]
ds_read_b128 v[36:39],v2 offset:18432
v_mfma_f32_16x16x32_bf16 v[16:19],a[240:243],v[40:43],v[16:19]
s_waitcnt lgkmcnt(4)
v_mfma_f32_16x16x32_bf16 v[16:19],a[24:27],v[48:51],v[16:19]
ds_read_b128 v[40:43],v2 offset:32768
v_mfma_f32_16x16x32_bf16 v[16:19],a[136:139],v[52:55],v[16:19]
buffer_load_dwordx4 v2,s[32:35],s71 offen offset:1024 sc1 lds
s_add_u32 s71,s71,s30
v_mfma_f32_16x16x32_bf16 v[20:23],a[140:143],v[52:55],v[20:23]
ds_read_b128 v[44:47],v2 offset:47104
s_add_u32 m0,s64,57344
v_mfma_f32_16x16x32_bf16 v[20:23],a[28:31],v[48:51],v[20:23]
s_waitcnt lgkmcnt(4)
v_mfma_f32_16x16x32_bf16 v[20:23],a[252:255],v[56:59],v[20:23]
ds_read_b128 v[48:51],v2 offset:5120
v_mfma_f32_16x16x32_bf16 v[20:23],v[172:175],v[60:63],v[20:23]
buffer_load_dwordx4 v2,s[32:35],s63 offen offset:0 sc1 lds
v_mfma_f32_16x16x32_bf16 v[16:19],v[168:171],v[60:63],v[16:19]
ds_read_b128 v[52:55],v2 offset:19456
v_mfma_f32_16x16x32_bf16 v[16:19],a[248:251],v[56:59],v[16:19]
s_waitcnt lgkmcnt(4)
v_mfma_f32_16x16x32_bf16 v[16:19],a[32:35],v[32:35],v[16:19]
ds_read_b128 v[56:59],v2 offset:33792
v_mfma_f32_16x16x32_bf16 v[16:19],a[144:147],v[36:39],v[16:19]
buffer_load_dwordx4 v2,s[32:35],s63 offen offset:1024 sc1 lds
v_mfma_f32_16x16x32_bf16 v[20:23],a[148:151],v[36:39],v[20:23]
ds_read_b128 v[60:63],v2 offset:48128
v_mfma_f32_16x16x32_bf16 v[20:23],a[36:39],v[32:35],v[20:23]
s_waitcnt lgkmcnt(4)
v_mfma_f32_16x16x32_bf16 v[20:23],v[68:71],v[40:43],v[20:23]
ds_read_b128 v[32:35],v2 offset:6144
v_mfma_f32_16x16x32_bf16 v[20:23],v[180:183],v[44:47],v[20:23]
buffer_load_dwordx4 v2,s[32:35],s63 offen offset:2048 sc1 lds
v_mfma_f32_16x16x32_bf16 v[16:19],v[176:179],v[44:47],v[16:19]
ds_read_b128 v[36:39],v2 offset:20480
v_mfma_f32_16x16x32_bf16 v[16:19],v[64:67],v[40:43],v[16:19]
s_waitcnt lgkmcnt(4)
v_mfma_f32_16x16x32_bf16 v[16:19],a[40:43],v[48:51],v[16:19]
ds_read_b128 v[40:43],v2 offset:34816
v_mfma_f32_16x16x32_bf16 v[16:19],a[152:155],v[52:55],v[16:19]
buffer_load_dwordx4 v2,s[32:35],s63 offen offset:3072 sc1 lds
s_add_u32 s63,s63,4096
v_mfma_f32_16x16x32_bf16 v[20:23],a[156:159],v[52:55],v[20:23]
ds_read_b128 v[44:47],v2 offset:49152
s_add_u32 m0,s64,61440
v_mfma_f32_16x16x32_bf16 v[20:23],a[44:47],v[48:51],v[20:23]
s_waitcnt lgkmcnt(4)
v_mfma_f32_16x16x32_bf16 v[20:23],v[76:79],v[56:59],v[20:23]
ds_read_b128 v[48:51],v2 offset:7168
v_mfma_f32_16x16x32_bf16 v[20:23],v[188:191],v[60:63],v[20:23]
buffer_load_dwordx4 v2,s[32:35],s63 offen offset:0 sc1 lds
v_mfma_f32_16x16x32_bf16 v[16:19],v[184:187],v[60:63],v[16:19]
ds_read_b128 v[52:55],v2 offset:21504
v_mfma_f32_16x16x32_bf16 v[16:19],v[72:75],v[56:59],v[16:19]
s_waitcnt lgkmcnt(4)
v_mfma_f32_16x16x32_bf16 v[16:19],a[48:51],v[32:35],v[16:19]
ds_read_b128 v[56:59],v2 offset:35840
v_mfma_f32_16x16x32_bf16 v[16:19],a[160:163],v[36:39],v[16:19]
buffer_load_dwordx4 v2,s[32:35],s63 offen offset:1024 sc1 lds
v_mfma_f32_16x16x32_bf16 v[20:23],a[164:167],v[36:39],v[20:23]
ds_read_b128 v[60:63],v2 offset:50176
v_mfma_f32_16x16x32_bf16 v[20:23],a[52:55],v[32:35],v[20:23]
s_waitcnt lgkmcnt(4)
v_mfma_f32_16x16x32_bf16 v[20:23],v[84:87],v[40:43],v[20:23]
ds_read_b128 v[32:35],v2 offset:8192
v_mfma_f32_16x16x32_bf16 v[20:23],v[196:199],v[44:47],v[20:23]
buffer_load_dwordx4 v2,s[32:35],s63 offen offset:2048 sc1 lds
v_mfma_f32_16x16x32_bf16 v[16:19],v[192:195],v[44:47],v[16:19]
ds_read_b128 v[36:39],v2 offset:22528
v_mfma_f32_16x16x32_bf16 v[16:19],v[80:83],v[40:43],v[16:19]
s_waitcnt lgkmcnt(4)
v_mfma_f32_16x16x32_bf16 v[16:19],a[56:59],v[48:51],v[16:19]
ds_read_b128 v[40:43],v2 offset:36864
v_mfma_f32_16x16x32_bf16 v[16:19],a[168:171],v[52:55],v[16:19]
buffer_load_dwordx4 v2,s[32:35],s63 offen offset:3072 sc1 lds
v_mfma_f32_16x16x32_bf16 v[20:23],a[172:175],v[52:55],v[20:23]
ds_read_b128 v[44:47],v2 offset:51200
v_mfma_f32_16x16x32_bf16 v[20:23],a[60:63],v[48:51],v[20:23]
s_waitcnt lgkmcnt(4)
v_mfma_f32_16x16x32_bf16 v[20:23],v[92:95],v[56:59],v[20:23]
ds_read_b128 v[48:51],v2 offset:9216
v_mfma_f32_16x16x32_bf16 v[20:23],v[204:207],v[60:63],v[20:23]
s_add_u32 s63,s63,4096
s_add_u32 m0,s64,65536
v_mfma_f32_16x16x32_bf16 v[16:19],v[200:203],v[60:63],v[16:19]
ds_read_b128 v[52:55],v2 offset:23552
v_mfma_f32_16x16x32_bf16 v[16:19],v[88:91],v[56:59],v[16:19]
s_waitcnt lgkmcnt(4)
v_mfma_f32_16x16x32_bf16 v[16:19],a[64:67],v[32:35],v[16:19]
ds_read_b128 v[56:59],v2 offset:37888
v_mfma_f32_16x16x32_bf16 v[16:19],a[176:179],v[36:39],v[16:19]
buffer_load_dwordx4 v2,s[32:35],s63 offen offset:0 sc1 lds
v_mfma_f32_16x16x32_bf16 v[20:23],a[180:183],v[36:39],v[20:23]
ds_read_b128 v[60:63],v2 offset:52224
v_mfma_f32_16x16x32_bf16 v[20:23],a[68:71],v[32:35],v[20:23]
s_waitcnt lgkmcnt(4)
v_mfma_f32_16x16x32_bf16 v[20:23],v[100:103],v[40:43],v[20:23]
ds_read_b128 v[32:35],v2 offset:10240
v_mfma_f32_16x16x32_bf16 v[20:23],v[212:215],v[44:47],v[20:23]
buffer_load_dwordx4 v2,s[32:35],s63 offen offset:1024 sc1 lds
v_mfma_f32_16x16x32_bf16 v[16:19],v[208:211],v[44:47],v[16:19]
ds_read_b128 v[36:39],v2 offset:24576
v_mfma_f32_16x16x32_bf16 v[16:19],v[96:99],v[40:43],v[16:19]
s_waitcnt lgkmcnt(4)
v_mfma_f32_16x16x32_bf16 v[16:19],a[72:75],v[48:51],v[16:19]
ds_read_b128 v[40:43],v2 offset:38912
s_waitcnt vmcnt(10)
v_mfma_f32_16x16x32_bf16 v[16:19],a[184:187],v[52:55],v[16:19]
buffer_load_dwordx4 v2,s[32:35],s63 offen offset:2048 sc1 lds
ds_read_b128 v[12:15],v5 offset:0
v_mfma_f32_16x16x32_bf16 v[20:23],a[188:191],v[52:55],v[20:23]
ds_read_b128 v[44:47],v2 offset:53248
v_mfma_f32_16x16x32_bf16 v[20:23],a[76:79],v[48:51],v[20:23]
s_waitcnt lgkmcnt(5)
v_mfma_f32_16x16x32_bf16 v[20:23],v[108:111],v[56:59],v[20:23]
ds_read_b128 v[48:51],v2 offset:11264
v_mfma_f32_16x16x32_bf16 v[20:23],v[220:223],v[60:63],v[20:23]
buffer_load_dwordx2 v[6:7],v0,s[0:3],s66 offen sc1
v_mfma_f32_16x16x32_bf16 v[16:19],v[216:219],v[60:63],v[16:19]
ds_read_b128 v[52:55],v2 offset:25600
s_waitcnt lgkmcnt(3)
v_mfma_f32_16x16x32_bf16 v[16:19],v[104:107],v[56:59],v[16:19]
v_add_f32 v24,v24,v12
v_mfma_f32_16x16x32_bf16 v[16:19],a[80:83],v[32:35],v[16:19]
ds_read_b128 v[56:59],v2 offset:39936
v_add_f32 v25,v25,v13
v_mfma_f32_16x16x32_bf16 v[16:19],a[192:195],v[36:39],v[16:19]
buffer_load_dwordx4 v2,s[32:35],s63 offen offset:3072 sc1 lds
s_add_u32 s63,s63,4096
v_mfma_f32_16x16x32_bf16 v[20:23],a[196:199],v[36:39],v[20:23]
ds_read_b128 v[60:63],v2 offset:54272
s_add_u32 m0,s64,69632
v_mfma_f32_16x16x32_bf16 v[20:23],a[84:87],v[32:35],v[20:23]
v_add_f32 v26,v26,v14
s_waitcnt lgkmcnt(4)
v_mfma_f32_16x16x32_bf16 v[20:23],v[116:119],v[40:43],v[20:23]
ds_read_b128 v[32:35],v2 offset:12288
v_add_f32 v27,v27,v15
v_mfma_f32_16x16x32_bf16 v[20:23],v[228:231],v[44:47],v[20:23]
buffer_load_dwordx4 v2,s[32:35],s63 offen offset:0 sc1 lds
ds_read_b128 v[12:15],v5 offset:1024
v_mfma_f32_16x16x32_bf16 v[16:19],v[224:227],v[44:47],v[16:19]
ds_read_b128 v[36:39],v2 offset:26624
v_mfma_f32_16x16x32_bf16 v[16:19],v[112:115],v[40:43],v[16:19]
)#"
R"#(
s_waitcnt lgkmcnt(5)
v_mfma_f32_16x16x32_bf16 v[16:19],a[88:91],v[48:51],v[16:19]
ds_read_b128 v[40:43],v2 offset:40960
v_mfma_f32_16x16x32_bf16 v[16:19],a[200:203],v[52:55],v[16:19]
buffer_load_dwordx4 v2,s[32:35],s63 offen offset:1024 sc1 lds
s_add_u32 s63,s63,s31
v_mfma_f32_16x16x32_bf16 v[20:23],a[204:207],v[52:55],v[20:23]
ds_read_b128 v[44:47],v2 offset:55296
s_waitcnt lgkmcnt(3)
v_mfma_f32_16x16x32_bf16 v[20:23],a[92:95],v[48:51],v[20:23]
v_add_f32 v28,v28,v12
v_mfma_f32_16x16x32_bf16 v[20:23],v[124:127],v[56:59],v[20:23]
ds_read_b128 v[48:51],v2 offset:13312
v_add_f32 v29,v29,v13
v_mfma_f32_16x16x32_bf16 v[20:23],v[236:239],v[60:63],v[20:23]
v_add_f32 v30,v30,v14
v_add_f32 v31,v31,v15
v_mfma_f32_16x16x32_bf16 v[16:19],v[232:235],v[60:63],v[16:19]
ds_read_b128 v[52:55],v2 offset:27648
v_cvt_pk_bf16_f32 v12,v24,v25
v_mfma_f32_16x16x32_bf16 v[16:19],v[120:123],v[56:59],v[16:19]
v_cvt_pk_bf16_f32 v13,v26,v27
s_waitcnt lgkmcnt(4)
v_mfma_f32_16x16x32_bf16 v[16:19],a[96:99],v[32:35],v[16:19]
ds_read_b128 v[56:59],v2 offset:41984
v_cvt_pk_bf16_f32 v14,v28,v29
v_mfma_f32_16x16x32_bf16 v[16:19],a[208:211],v[36:39],v[16:19]
v_cvt_pk_bf16_f32 v15,v30,v31
v_mfma_f32_16x16x32_bf16 v[20:23],a[212:215],v[36:39],v[20:23]
s_waitcnt vmcnt(11)
s_barrier
v_mfma_f32_16x16x32_bf16 v[20:23],a[100:103],v[32:35],v[20:23]
ds_read_b128 v[60:63],v2 offset:56320
s_waitcnt lgkmcnt(4)
v_mfma_f32_16x16x32_bf16 v[20:23],v[132:135],v[40:43],v[20:23]
ds_read_b128 v[32:35],v4 offset:0
v_mfma_f32_16x16x32_bf16 v[20:23],v[244:247],v[44:47],v[20:23]
buffer_store_dwordx4 v[12:15],v2,s[32:35],s68 offen sc1
v_mfma_f32_16x16x32_bf16 v[16:19],v[240:243],v[44:47],v[16:19]
ds_read_b128 v[36:39],v4 offset:14336
v_mfma_f32_16x16x32_bf16 v[16:19],v[128:131],v[40:43],v[16:19]
s_waitcnt lgkmcnt(4)
v_mfma_f32_16x16x32_bf16 v[16:19],a[104:107],v[48:51],v[16:19]
ds_read_b128 v[40:43],v4 offset:28672
v_mfma_f32_16x16x32_bf16 v[16:19],a[216:219],v[52:55],v[16:19]
v_mfma_f32_16x16x32_bf16 v[20:23],a[220:223],v[52:55],v[20:23]
ds_read_b128 v[44:47],v4 offset:43008
s_mov_b32 s67,s68
v_mfma_f32_16x16x32_bf16 v[20:23],a[108:111],v[48:51],v[20:23]
s_mov_b32 s68,s70
s_waitcnt lgkmcnt(4)
v_mfma_f32_16x16x32_bf16 v[20:23],v[140:143],v[56:59],v[20:23]
ds_read_b128 v[48:51],v4 offset:1024
s_add_u32 s70,s70,1187840
v_mfma_f32_16x16x32_bf16 v[20:23],v[252:255],v[60:63],v[20:23]
v_mfma_f32_16x16x32_bf16 v[16:19],v[248:251],v[60:63],v[16:19]
ds_read_b128 v[52:55],v4 offset:15360
s_sub_u32 s62,s62,1
v_mfma_f32_16x16x32_bf16 v[16:19],v[136:139],v[56:59],v[16:19]
s_cbranch_scc1 .L_dab_1
s_waitcnt lgkmcnt(4)
v_mfma_f32_16x16x32_bf16 v[24:27],a[0:3],v[32:35],0
ds_read_b128 v[56:59],v4 offset:29696
v_dot2_f32_bf16 v10,v12,s72,0
v_mfma_f32_16x16x32_bf16 v[24:27],a[112:115],v[36:39],v[24:27]
v_dot2_f32_bf16 v11,v12,v12,0
v_dot2_f32_bf16 v10,v13,s72,v10
v_mfma_f32_16x16x32_bf16 v[28:31],a[116:119],v[36:39],0
ds_read_b128 v[60:63],v4 offset:44032
v_dot2_f32_bf16 v11,v13,v13,v11
v_mfma_f32_16x16x32_bf16 v[28:31],a[4:7],v[32:35],v[28:31]
v_dot2_f32_bf16 v10,v14,s72,v10
s_waitcnt lgkmcnt(4)
v_mfma_f32_16x16x32_bf16 v[28:31],a[228:231],v[40:43],v[28:31]
ds_read_b128 v[32:35],v4 offset:2048
v_dot2_f32_bf16 v11,v14,v14,v11
v_mfma_f32_16x16x32_bf16 v[28:31],v[148:151],v[44:47],v[28:31]
v_dot2_f32_bf16 v10,v15,s72,v10
v_dot2_f32_bf16 v11,v15,v15,v11
v_mfma_f32_16x16x32_bf16 v[24:27],v[144:147],v[44:47],v[24:27]
ds_read_b128 v[36:39],v4 offset:16384
v_mfma_f32_16x16x32_bf16 v[24:27],a[224:227],v[40:43],v[24:27]
v_permlane16_swap_b32 v10,v11
s_waitcnt lgkmcnt(4)
v_mfma_f32_16x16x32_bf16 v[24:27],a[8:11],v[48:51],v[24:27]
ds_read_b128 v[40:43],v4 offset:30720
v_add_f32 v10,v10,v11
v_mfma_f32_16x16x32_bf16 v[24:27],a[120:123],v[52:55],v[24:27]
v_mfma_f32_16x16x32_bf16 v[28:31],a[124:127],v[52:55],v[28:31]
ds_read_b128 v[44:47],v4 offset:45056
v_permlane32_swap_b32 v10,v11
v_mfma_f32_16x16x32_bf16 v[28:31],a[12:15],v[48:51],v[28:31]
v_add_f32 v10,v10,v11
s_waitcnt lgkmcnt(4)
v_mfma_f32_16x16x32_bf16 v[28:31],a[236:239],v[56:59],v[28:31]
ds_read_b128 v[48:51],v4 offset:3072
v_mfma_f32_16x16x32_bf16 v[28:31],v[156:159],v[60:63],v[28:31]
buffer_store_dword v10,v3,s[32:35],s67 offen offset:1024 sc1
v_mfma_f32_16x16x32_bf16 v[24:27],v[152:155],v[60:63],v[24:27]
ds_read_b128 v[52:55],v4 offset:17408
s_waitcnt vmcnt(5)
v_mfma_f32_16x16x32_bf16 v[24:27],a[232:235],v[56:59],v[24:27]
v_cmp_ne_u64 vcc,v[8:9],v[6:7]
s_waitcnt lgkmcnt(4)
v_mfma_f32_16x16x32_bf16 v[24:27],a[16:19],v[32:35],v[24:27]
ds_read_b128 v[56:59],v4 offset:31744
s_cbranch_vccnz .L_rta1_1
.L_bka1_1:
v_mfma_f32_16x16x32_bf16 v[24:27],a[128:131],v[36:39],v[24:27]
s_add_u32 s66,s66,s30
v_mfma_f32_16x16x32_bf16 v[28:31],a[132:135],v[36:39],v[28:31]
s_waitcnt vmcnt(2)
s_barrier
v_mfma_f32_16x16x32_bf16 v[28:31],a[20:23],v[32:35],v[28:31]
ds_read_b128 v[60:63],v4 offset:46080
s_waitcnt lgkmcnt(4)
v_mfma_f32_16x16x32_bf16 v[28:31],a[244:247],v[40:43],v[28:31]
ds_read_b128 v[32:35],v4 offset:4096
s_mov_b32 m0,s74
v_mfma_f32_16x16x32_bf16 v[28:31],v[164:167],v[44:47],v[28:31]
buffer_load_dwordx4 v2,s[32:35],s71 offen offset:0 sc1 lds
v_mfma_f32_16x16x32_bf16 v[24:27],v[160:163],v[44:47],v[24:27]
ds_read_b128 v[36:39],v4 offset:18432
v_mfma_f32_16x16x32_bf16 v[24:27],a[240:243],v[40:43],v[24:27]
s_waitcnt lgkmcnt(4)
v_mfma_f32_16x16x32_bf16 v[24:27],a[24:27],v[48:51],v[24:27]
ds_read_b128 v[40:43],v4 offset:32768
v_mfma_f32_16x16x32_bf16 v[24:27],a[136:139],v[52:55],v[24:27]
buffer_load_dwordx4 v2,s[32:35],s71 offen offset:1024 sc1 lds
s_add_u32 s71,s71,s30
v_mfma_f32_16x16x32_bf16 v[28:31],a[140:143],v[52:55],v[28:31]
ds_read_b128 v[44:47],v4 offset:47104
s_add_u32 m0,s64,0
v_mfma_f32_16x16x32_bf16 v[28:31],a[28:31],v[48:51],v[28:31]
s_waitcnt lgkmcnt(4)
v_mfma_f32_16x16x32_bf16 v[28:31],a[252:255],v[56:59],v[28:31]
ds_read_b128 v[48:51],v4 offset:5120
v_mfma_f32_16x16x32_bf16 v[28:31],v[172:175],v[60:63],v[28:31]
buffer_load_dwordx4 v2,s[32:35],s63 offen offset:0 sc1 lds
v_mfma_f32_16x16x32_bf16 v[24:27],v[168:171],v[60:63],v[24:27]
ds_read_b128 v[52:55],v4 offset:19456
v_mfma_f32_16x16x32_bf16 v[24:27],a[248:251],v[56:59],v[24:27]
s_waitcnt lgkmcnt(4)
v_mfma_f32_16x16x32_bf16 v[24:27],a[32:35],v[32:35],v[24:27]
ds_read_b128 v[56:59],v4 offset:33792
v_mfma_f32_16x16x32_bf16 v[24:27],a[144:147],v[36:39],v[24:27]
buffer_load_dwordx4 v2,s[32:35],s63 offen offset:1024 sc1 lds
v_mfma_f32_16x16x32_bf16 v[28:31],a[148:151],v[36:39],v[28:31]
ds_read_b128 v[60:63],v4 offset:48128
v_mfma_f32_16x16x32_bf16 v[28:31],a[36:39],v[32:35],v[28:31]
s_waitcnt lgkmcnt(4)
v_mfma_f32_16x16x32_bf16 v[28:31],v[68:71],v[40:43],v[28:31]
ds_read_b128 v[32:35],v4 offset:6144
v_mfma_f32_16x16x32_bf16 v[28:31],v[180:183],v[44:47],v[28:31]
buffer_load_dwordx4 v2,s[32:35],s63 offen offset:2048 sc1 lds
v_mfma_f32_16x16x32_bf16 v[24:27],v[176:179],v[44:47],v[24:27]
ds_read_b128 v[36:39],v4 offset:20480
v_mfma_f32_16x16x32_bf16 v[24:27],v[64:67],v[40:43],v[24:27]
s_waitcnt lgkmcnt(4)
v_mfma_f32_16x16x32_bf16 v[24:27],a[40:43],v[48:51],v[24:27]
ds_read_b128 v[40:43],v4 offset:34816
v_mfma_f32_16x16x32_bf16 v[24:27],a[152:155],v[52:55],v[24:27]
buffer_load_dwordx4 v2,s[32:35],s63 offen offset:3072 sc1 lds
s_add_u32 s63,s63,4096
v_mfma_f32_16x16x32_bf16 v[28:31],a[156:159],v[52:55],v[28:31]
ds_read_b128 v[44:47],v4 offset:49152
s_add_u32 m0,s64,4096
v_mfma_f32_16x16x32_bf16 v[28:31],a[44:47],v[48:51],v[28:31]
s_waitcnt lgkmcnt(4)
v_mfma_f32_16x16x32_bf16 v[28:31],v[76:79],v[56:59],v[28:31]
ds_read_b128 v[48:51],v4 offset:7168
v_mfma_f32_16x16x32_bf16 v[28:31],v[188:191],v[60:63],v[28:31]
buffer_load_dwordx4 v2,s[32:35],s63 offen offset:0 sc1 lds
v_mfma_f32_16x16x32_bf16 v[24:27],v[184:187],v[60:63],v[24:27]
ds_read_b128 v[52:55],v4 offset:21504
v_mfma_f32_16x16x32_bf16 v[24:27],v[72:75],v[56:59],v[24:27]
s_waitcnt lgkmcnt(4)
v_mfma_f32_16x16x32_bf16 v[24:27],a[48:51],v[32:35],v[24:27]
ds_read_b128 v[56:59],v4 offset:35840
v_mfma_f32_16x16x32_bf16 v[24:27],a[160:163],v[36:39],v[24:27]
buffer_load_dwordx4 v2,s[32:35],s63 offen offset:1024 sc1 lds
v_mfma_f32_16x16x32_bf16 v[28:31],a[164:167],v[36:39],v[28:31]
ds_read_b128 v[60:63],v4 offset:50176
v_mfma_f32_16x16x32_bf16 v[28:31],a[52:55],v[32:35],v[28:31]
s_waitcnt lgkmcnt(4)
v_mfma_f32_16x16x32_bf16 v[28:31],v[84:87],v[40:43],v[28:31]
ds_read_b128 v[32:35],v4 offset:8192
v_mfma_f32_16x16x32_bf16 v[28:31],v[196:199],v[44:47],v[28:31]
buffer_load_dwordx4 v2,s[32:35],s63 offen offset:2048 sc1 lds
v_mfma_f32_16x16x32_bf16 v[24:27],v[192:195],v[44:47],v[24:27]
ds_read_b128 v[36:39],v4 offset:22528
v_mfma_f32_16x16x32_bf16 v[24:27],v[80:83],v[40:43],v[24:27]
s_waitcnt lgkmcnt(4)
v_mfma_f32_16x16x32_bf16 v[24:27],a[56:59],v[48:51],v[24:27]
ds_read_b128 v[40:43],v4 offset:36864
v_mfma_f32_16x16x32_bf16 v[24:27],a[168:171],v[52:55],v[24:27]
buffer_load_dwordx4 v2,s[32:35],s63 offen offset:3072 sc1 lds
v_mfma_f32_16x16x32_bf16 v[28:31],a[172:175],v[52:55],v[28:31]
ds_read_b128 v[44:47],v4 offset:51200
v_mfma_f32_16x16x32_bf16 v[28:31],a[60:63],v[48:51],v[28:31]
s_waitcnt lgkmcnt(4)
v_mfma_f32_16x16x32_bf16 v[28:31],v[92:95],v[56:59],v[28:31]
ds_read_b128 v[48:51],v4 offset:9216
v_mfma_f32_16x16x32_bf16 v[28:31],v[204:207],v[60:63],v[28:31]
s_add_u32 s63,s63,4096
s_add_u32 m0,s64,8192
v_mfma_f32_16x16x32_bf16 v[24:27],v[200:203],v[60:63],v[24:27]
ds_read_b128 v[52:55],v4 offset:23552
v_mfma_f32_16x16x32_bf16 v[24:27],v[88:91],v[56:59],v[24:27]
s_waitcnt lgkmcnt(4)
v_mfma_f32_16x16x32_bf16 v[24:27],a[64:67],v[32:35],v[24:27]
ds_read_b128 v[56:59],v4 offset:37888
v_mfma_f32_16x16x32_bf16 v[24:27],a[176:179],v[36:39],v[24:27]
buffer_load_dwordx4 v2,s[32:35],s63 offen offset:0 sc1 lds
v_mfma_f32_16x16x32_bf16 v[28:31],a[180:183],v[36:39],v[28:31]
ds_read_b128 v[60:63],v4 offset:52224
v_mfma_f32_16x16x32_bf16 v[28:31],a[68:71],v[32:35],v[28:31]
s_waitcnt lgkmcnt(4)
v_mfma_f32_16x16x32_bf16 v[28:31],v[100:103],v[40:43],v[28:31]
ds_read_b128 v[32:35],v4 offset:10240
v_mfma_f32_16x16x32_bf16 v[28:31],v[212:215],v[44:47],v[28:31]
buffer_load_dwordx4 v2,s[32:35],s63 offen offset:1024 sc1 lds
v_mfma_f32_16x16x32_bf16 v[24:27],v[208:211],v[44:47],v[24:27]
ds_read_b128 v[36:39],v4 offset:24576
v_mfma_f32_16x16x32_bf16 v[24:27],v[96:99],v[40:43],v[24:27]
s_waitcnt lgkmcnt(4)
v_mfma_f32_16x16x32_bf16 v[24:27],a[72:75],v[48:51],v[24:27]
ds_read_b128 v[40:43],v4 offset:38912
s_waitcnt vmcnt(10)
v_mfma_f32_16x16x32_bf16 v[24:27],a[184:187],v[52:55],v[24:27]
buffer_load_dwordx4 v2,s[32:35],s63 offen offset:2048 sc1 lds
ds_read_b128 v[12:15],v5 offset:0
v_mfma_f32_16x16x32_bf16 v[28:31],a[188:191],v[52:55],v[28:31]
ds_read_b128 v[44:47],v4 offset:53248
v_mfma_f32_16x16x32_bf16 v[28:31],a[76:79],v[48:51],v[28:31]
s_waitcnt lgkmcnt(5)
v_mfma_f32_16x16x32_bf16 v[28:31],v[108:111],v[56:59],v[28:31]
ds_read_b128 v[48:51],v4 offset:11264
v_mfma_f32_16x16x32_bf16 v[28:31],v[220:223],v[60:63],v[28:31]
buffer_load_dwordx2 v[6:7],v0,s[0:3],s66 offen sc1
v_mfma_f32_16x16x32_bf16 v[24:27],v[216:219],v[60:63],v[24:27]
ds_read_b128 v[52:55],v4 offset:25600
s_waitcnt lgkmcnt(3)
v_mfma_f32_16x16x32_bf16 v[24:27],v[104:107],v[56:59],v[24:27]
v_add_f32 v16,v16,v12
v_mfma_f32_16x16x32_bf16 v[24:27],a[80:83],v[32:35],v[24:27]
ds_read_b128 v[56:59],v4 offset:39936
v_add_f32 v17,v17,v13
v_mfma_f32_16x16x32_bf16 v[24:27],a[192:195],v[36:39],v[24:27]
buffer_load_dwordx4 v2,s[32:35],s63 offen offset:3072 sc1 lds
s_add_u32 s63,s63,4096
v_mfma_f32_16x16x32_bf16 v[28:31],a[196:199],v[36:39],v[28:31]
ds_read_b128 v[60:63],v4 offset:54272
s_add_u32 m0,s64,12288
v_mfma_f32_16x16x32_bf16 v[28:31],a[84:87],v[32:35],v[28:31]
v_add_f32 v18,v18,v14
s_waitcnt lgkmcnt(4)
v_mfma_f32_16x16x32_bf16 v[28:31],v[116:119],v[40:43],v[28:31]
ds_read_b128 v[32:35],v4 offset:12288
v_add_f32 v19,v19,v15
v_mfma_f32_16x16x32_bf16 v[28:31],v[228:231],v[44:47],v[28:31]
buffer_load_dwordx4 v2,s[32:35],s63 offen offset:0 sc1 lds
ds_read_b128 v[12:15],v5 offset:1024
v_mfma_f32_16x16x32_bf16 v[24:27],v[224:227],v[44:47],v[24:27]
ds_read_b128 v[36:39],v4 offset:26624
v_mfma_f32_16x16x32_bf16 v[24:27],v[112:115],v[40:43],v[24:27]
s_waitcnt lgkmcnt(5)
v_mfma_f32_16x16x32_bf16 v[24:27],a[88:91],v[48:51],v[24:27]
ds_read_b128 v[40:43],v4 offset:40960
v_mfma_f32_16x16x32_bf16 v[24:27],a[200:203],v[52:55],v[24:27]
buffer_load_dwordx4 v2,s[32:35],s63 offen offset:1024 sc1 lds
s_add_u32 s63,s63,s31
v_mfma_f32_16x16x32_bf16 v[28:31],a[204:207],v[52:55],v[28:31]
ds_read_b128 v[44:47],v4 offset:55296
s_waitcnt lgkmcnt(3)
v_mfma_f32_16x16x32_bf16 v[28:31],a[92:95],v[48:51],v[28:31]
v_add_f32 v20,v20,v12
v_mfma_f32_16x16x32_bf16 v[28:31],v[124:127],v[56:59],v[28:31]
ds_read_b128 v[48:51],v4 offset:13312
v_add_f32 v21,v21,v13
v_mfma_f32_16x16x32_bf16 v[28:31],v[236:239],v[60:63],v[28:31]
v_add_f32 v22,v22,v14
v_add_f32 v23,v23,v15
v_mfma_f32_16x16x32_bf16 v[24:27],v[232:235],v[60:63],v[24:27]
ds_read_b128 v[52:55],v4 offset:27648
v_cvt_pk_bf16_f32 v12,v16,v17
v_mfma_f32_16x16x32_bf16 v[24:27],v[120:123],v[56:59],v[24:27]
v_cvt_pk_bf16_f32 v13,v18,v19
s_waitcnt lgkmcnt(4)
v_mfma_f32_16x16x32_bf16 v[24:27],a[96:99],v[32:35],v[24:27]
ds_read_b128 v[56:59],v4 offset:41984
v_cvt_pk_bf16_f32 v14,v20,v21
v_mfma_f32_16x16x32_bf16 v[24:27],a[208:211],v[36:39],v[24:27]
v_cvt_pk_bf16_f32 v15,v22,v23
v_mfma_f32_16x16x32_bf16 v[28:31],a[212:215],v[36:39],v[28:31]
s_waitcnt vmcnt(11)
s_barrier
v_mfma_f32_16x16x32_bf16 v[28:31],a[100:103],v[32:35],v[28:31]
ds_read_b128 v[60:63],v4 offset:56320
s_waitcnt lgkmcnt(4)
v_mfma_f32_16x16x32_bf16 v[28:31],v[132:135],v[40:43],v[28:31]
ds_read_b128 v[32:35],v2 offset:0
v_mfma_f32_16x16x32_bf16 v[28:31],v[244:247],v[44:47],v[28:31]
buffer_store_dwordx4 v[12:15],v2,s[32:35],s69 offen sc1
v_mfma_f32_16x16x32_bf16 v[24:27],v[240:243],v[44:47],v[24:27]
ds_read_b128 v[36:39],v2 offset:14336
v_mfma_f32_16x16x32_bf16 v[24:27],v[128:131],v[40:43],v[24:27]
s_waitcnt lgkmcnt(4)
v_mfma_f32_16x16x32_bf16 v[24:27],a[104:107],v[48:51],v[24:27]
ds_read_b128 v[40:43],v2 offset:28672
v_mfma_f32_16x16x32_bf16 v[24:27],a[216:219],v[52:55],v[24:27]
v_mfma_f32_16x16x32_bf16 v[28:31],a[220:223],v[52:55],v[28:31]
ds_read_b128 v[44:47],v2 offset:43008
s_mov_b32 s67,s69
v_mfma_f32_16x16x32_bf16 v[28:31],a[108:111],v[48:51],v[28:31]
s_mov_b32 s69,s70
s_waitcnt lgkmcnt(4)
v_mfma_f32_16x16x32_bf16 v[28:31],v[140:143],v[56:59],v[28:31]
ds_read_b128 v[48:51],v2 offset:1024
s_add_u32 s70,s70,1187840
v_mfma_f32_16x16x32_bf16 v[28:31],v[252:255],v[60:63],v[28:31]
v_mfma_f32_16x16x32_bf16 v[24:27],v[248:251],v[60:63],v[24:27]
ds_read_b128 v[52:55],v2 offset:15360
v_mfma_f32_16x16x32_bf16 v[24:27],v[136:139],v[56:59],v[24:27]
s_branch .L_dal_1
.L_dab_1:
v_dot2_f32_bf16 v10,v12,s72,0
v_dot2_f32_bf16 v11,v12,v12,0
v_dot2_f32_bf16 v10,v13,s72,v10
v_dot2_f32_bf16 v11,v13,v13,v11
v_dot2_f32_bf16 v10,v14,s72,v10
v_dot2_f32_bf16 v11,v14,v14,v11
v_dot2_f32_bf16 v10,v15,s72,v10
v_dot2_f32_bf16 v11,v15,v15,v11
s_nop 2
v_permlane16_swap_b32 v10,v11
v_add_f32 v10,v10,v11
s_nop 1
v_permlane32_swap_b32 v10,v11
v_add_f32 v10,v10,v11
buffer_store_dword v10,v3,s[32:35],s67 offen offset:1024 sc1
s_mov_b32 s62,s58
s_mov_b32 s70,s59
s_add_u32 s74,s74,8192
v_lshrrev_b32 v5,8,v2
v_lshl_add_u32 v5,v5,4,s74
v_and_b32 v3,128,v2
v_lshl_add_u32 v5,v3,2,v5
v_and_b32 v3,16,v2
v_lshl_add_u32 v5,v3,4,v5
v_and_b32 v3,96,v2
v_lshl_add_u32 v5,v3,1,v5
v_lshrrev_b32 v3,2,v2
v_add_u32 v3,s74,v3
s_add_u32 s66,s66,2375680
s_add_u32 s63,s63,2375680
s_add_u32 s71,s71,2375680
s_mov_b32 s68,98304
s_waitcnt vmcnt(5)
v_not_b32 v6,v8
s_mov_b32 s34,79446016
.p2align 6
.L_dbl_1:
s_waitcnt lgkmcnt(4)
v_mfma_f32_16x16x32_bf16 v[24:27],a[0:3],v[32:35],0
ds_read_b128 v[56:59],v4 offset:29696
v_mfma_f32_16x16x32_bf16 v[24:27],a[112:115],v[36:39],v[24:27]
v_mfma_f32_16x16x32_bf16 v[28:31],a[116:119],v[36:39],0
ds_read_b128 v[60:63],v4 offset:44032
v_mfma_f32_16x16x32_bf16 v[28:31],a[4:7],v[32:35],v[28:31]
v_cvt_pk_bf16_f32 v15,v22,v23
s_waitcnt lgkmcnt(4)
v_mfma_f32_16x16x32_bf16 v[28:31],a[228:231],v[40:43],v[28:31]
ds_read_b128 v[32:35],v4 offset:2048
v_cvt_pk_bf16_f32 v14,v20,v21
v_mfma_f32_16x16x32_bf16 v[28:31],v[148:151],v[44:47],v[28:31]
v_cvt_pk_bf16_f32 v13,v18,v19
v_cvt_pk_bf16_f32 v12,v16,v17
v_mfma_f32_16x16x32_bf16 v[24:27],v[144:147],v[44:47],v[24:27]
ds_read_b128 v[36:39],v4 offset:16384
ds_write_b128 v5,v[12:15]
v_mfma_f32_16x16x32_bf16 v[24:27],a[224:227],v[40:43],v[24:27]
ds_read2st64_b32 v[12:13],v3 offset0:0 offset1:1
s_waitcnt lgkmcnt(6)
v_mfma_f32_16x16x32_bf16 v[24:27],a[8:11],v[48:51],v[24:27]
ds_read_b128 v[40:43],v4 offset:30720
ds_read2st64_b32 v[14:15],v3 offset0:2 offset1:3
v_mfma_f32_16x16x32_bf16 v[24:27],a[120:123],v[52:55],v[24:27]
v_mfma_f32_16x16x32_bf16 v[28:31],a[124:127],v[52:55],v[28:31]
ds_read_b128 v[44:47],v4 offset:45056
v_mfma_f32_16x16x32_bf16 v[28:31],a[12:15],v[48:51],v[28:31]
s_waitcnt lgkmcnt(7)
v_mfma_f32_16x16x32_bf16 v[28:31],a[236:239],v[56:59],v[28:31]
ds_read_b128 v[48:51],v4 offset:3072
v_mfma_f32_16x16x32_bf16 v[28:31],v[156:159],v[60:63],v[28:31]
s_waitcnt lgkmcnt(2)
buffer_atomic_pk_add_bf16 v12,v1,s[44:47],s65 offen offset:0 sc1
v_mfma_f32_16x16x32_bf16 v[24:27],v[152:155],v[60:63],v[24:27]
ds_read_b128 v[52:55],v4 offset:17408
s_waitcnt vmcnt(4)
v_mfma_f32_16x16x32_bf16 v[24:27],a[232:235],v[56:59],v[24:27]
v_cmp_ne_u64 vcc,v[8:9],v[6:7]
v_mfma_f32_16x16x32_bf16 v[24:27],a[16:19],v[32:35],v[24:27]
ds_read_b128 v[56:59],v4 offset:31744
s_cbranch_vccnz .L_rtb1_1
.L_bkb1_1:
v_mfma_f32_16x16x32_bf16 v[24:27],a[128:131],v[36:39],v[24:27]
s_add_u32 s66,s66,s30
s_add_u32 s70,s70,s75
v_mfma_f32_16x16x32_bf16 v[28:31],a[132:135],v[36:39],v[28:31]
s_waitcnt vmcnt(1)
s_barrier
v_mfma_f32_16x16x32_bf16 v[28:31],a[20:23],v[32:35],v[28:31]
buffer_atomic_pk_add_bf16 v13,v1,s[44:47],s65 offen offset:2048 sc1
s_waitcnt lgkmcnt(3)
v_mfma_f32_16x16x32_bf16 v[28:31],a[244:247],v[40:43],v[28:31]
ds_read_b128 v[60:63],v4 offset:46080
ds_read_b128 v[32:35],v4 offset:4096
v_mfma_f32_16x16x32_bf16 v[28:31],v[164:167],v[44:47],v[28:31]
s_add_u32 m0,s64,0
s_add_u32 s65,s65,16384
v_mfma_f32_16x16x32_bf16 v[24:27],v[160:163],v[44:47],v[24:27]
ds_read_b128 v[36:39],v4 offset:18432
v_mfma_f32_16x16x32_bf16 v[24:27],a[240:243],v[40:43],v[24:27]
buffer_atomic_pk_add_bf16 v14,v1,s[44:47],s65 offen offset:0 sc1
s_waitcnt lgkmcnt(4)
v_mfma_f32_16x16x32_bf16 v[24:27],a[24:27],v[48:51],v[24:27]
ds_read_b128 v[40:43],v4 offset:32768
v_mfma_f32_16x16x32_bf16 v[24:27],a[136:139],v[52:55],v[24:27]
buffer_atomic_pk_add_bf16 v15,v1,s[44:47],s65 offen offset:2048 sc1
s_and_b32 s65,s70,0x3f8000
v_mfma_f32_16x16x32_bf16 v[28:31],a[140:143],v[52:55],v[28:31]
ds_read_b128 v[44:47],v4 offset:47104
v_mfma_f32_16x16x32_bf16 v[28:31],a[28:31],v[48:51],v[28:31]
s_waitcnt lgkmcnt(4)
v_mfma_f32_16x16x32_bf16 v[28:31],a[252:255],v[56:59],v[28:31]
ds_read_b128 v[48:51],v4 offset:5120
v_mfma_f32_16x16x32_bf16 v[28:31],v[172:175],v[60:63],v[28:31]
buffer_load_dwordx4 v2,s[32:35],s63 offen offset:0 sc1 lds
v_mfma_f32_16x16x32_bf16 v[24:27],v[168:171],v[60:63],v[24:27]
ds_read_b128 v[52:55],v4 offset:19456
v_mfma_f32_16x16x32_bf16 v[24:27],a[248:251],v[56:59],v[24:27]
s_waitcnt lgkmcnt(4)
v_mfma_f32_16x16x32_bf16 v[24:27],a[32:35],v[32:35],v[24:27]
ds_read_b128 v[56:59],v4 offset:33792
v_mfma_f32_16x16x32_bf16 v[24:27],a[144:147],v[36:39],v[24:27]
buffer_load_dwordx4 v2,s[32:35],s63 offen offset:1024 sc1 lds
v_mfma_f32_16x16x32_bf16 v[28:31],a[148:151],v[36:39],v[28:31]
ds_read_b128 v[60:63],v4 offset:48128
v_mfma_f32_16x16x32_bf16 v[28:31],a[36:39],v[32:35],v[28:31]
s_waitcnt lgkmcnt(4)
v_mfma_f32_16x16x32_bf16 v[28:31],v[68:71],v[40:43],v[28:31]
ds_read_b128 v[32:35],v4 offset:6144
v_mfma_f32_16x16x32_bf16 v[28:31],v[180:183],v[44:47],v[28:31]
buffer_load_dwordx4 v2,s[32:35],s63 offen offset:2048 sc1 lds
v_mfma_f32_16x16x32_bf16 v[24:27],v[176:179],v[44:47],v[24:27]
ds_read_b128 v[36:39],v4 offset:20480
v_mfma_f32_16x16x32_bf16 v[24:27],v[64:67],v[40:43],v[24:27]
s_waitcnt lgkmcnt(4)
v_mfma_f32_16x16x32_bf16 v[24:27],a[40:43],v[48:51],v[24:27]
ds_read_b128 v[40:43],v4 offset:34816
v_mfma_f32_16x16x32_bf16 v[24:27],a[152:155],v[52:55],v[24:27]
buffer_load_dwordx4 v2,s[32:35],s63 offen offset:3072 sc1 lds
s_add_u32 s63,s63,4096
v_mfma_f32_16x16x32_bf16 v[28:31],a[156:159],v[52:55],v[28:31]
ds_read_b128 v[44:47],v4 offset:49152
s_add_u32 m0,s64,4096
v_mfma_f32_16x16x32_bf16 v[28:31],a[44:47],v[48:51],v[28:31]
s_waitcnt lgkmcnt(4)
v_mfma_f32_16x16x32_bf16 v[28:31],v[76:79],v[56:59],v[28:31]
ds_read_b128 v[48:51],v4 offset:7168
v_mfma_f32_16x16x32_bf16 v[28:31],v[188:191],v[60:63],v[28:31]
buffer_load_dwordx4 v2,s[32:35],s63 offen offset:0 sc1 lds
v_mfma_f32_16x16x32_bf16 v[24:27],v[184:187],v[60:63],v[24:27]
ds_read_b128 v[52:55],v4 offset:21504
v_mfma_f32_16x16x32_bf16 v[24:27],v[72:75],v[56:59],v[24:27]
s_waitcnt lgkmcnt(4)
v_mfma_f32_16x16x32_bf16 v[24:27],a[48:51],v[32:35],v[24:27]
ds_read_b128 v[56:59],v4 offset:35840
v_mfma_f32_16x16x32_bf16 v[24:27],a[160:163],v[36:39],v[24:27]
buffer_load_dwordx4 v2,s[32:35],s63 offen offset:1024 sc1 lds
v_mfma_f32_16x16x32_bf16 v[28:31],a[164:167],v[36:39],v[28:31]
ds_read_b128 v[60:63],v4 offset:50176
v_mfma_f32_16x16x32_bf16 v[28:31],a[52:55],v[32:35],v[28:31]
s_waitcnt lgkmcnt(4)
v_mfma_f32_16x16x32_bf16 v[28:31],v[84:87],v[40:43],v[28:31]
ds_read_b128 v[32:35],v4 offset:8192
v_mfma_f32_16x16x32_bf16 v[28:31],v[196:199],v[44:47],v[28:31]
buffer_load_dwordx4 v2,s[32:35],s63 offen offset:2048 sc1 lds
v_mfma_f32_16x16x32_bf16 v[24:27],v[192:195],v[44:47],v[24:27]
ds_read_b128 v[36:39],v4 offset:22528
v_mfma_f32_16x16x32_bf16 v[24:27],v[80:83],v[40:43],v[24:27]
s_waitcnt lgkmcnt(4)
v_mfma_f32_16x16x32_bf16 v[24:27],a[56:59],v[48:51],v[24:27]
ds_read_b128 v[40:43],v4 offset:36864
v_mfma_f32_16x16x32_bf16 v[24:27],a[168:171],v[52:55],v[24:27]
buffer_load_dwordx4 v2,s[32:35],s63 offen offset:3072 sc1 lds
v_mfma_f32_16x16x32_bf16 v[28:31],a[172:175],v[52:55],v[28:31]
ds_read_b128 v[44:47],v4 offset:51200
v_mfma_f32_16x16x32_bf16 v[28:31],a[60:63],v[48:51],v[28:31]
)#"
R"#(
s_waitcnt lgkmcnt(4)
v_mfma_f32_16x16x32_bf16 v[28:31],v[92:95],v[56:59],v[28:31]
ds_read_b128 v[48:51],v4 offset:9216
v_mfma_f32_16x16x32_bf16 v[28:31],v[204:207],v[60:63],v[28:31]
s_add_u32 s63,s63,4096
s_add_u32 m0,s64,8192
v_mfma_f32_16x16x32_bf16 v[24:27],v[200:203],v[60:63],v[24:27]
ds_read_b128 v[52:55],v4 offset:23552
v_mfma_f32_16x16x32_bf16 v[24:27],v[88:91],v[56:59],v[24:27]
s_waitcnt lgkmcnt(4)
v_mfma_f32_16x16x32_bf16 v[24:27],a[64:67],v[32:35],v[24:27]
ds_read_b128 v[56:59],v4 offset:37888
v_mfma_f32_16x16x32_bf16 v[24:27],a[176:179],v[36:39],v[24:27]
buffer_load_dwordx4 v2,s[32:35],s63 offen offset:0 sc1 lds
v_mfma_f32_16x16x32_bf16 v[28:31],a[180:183],v[36:39],v[28:31]
ds_read_b128 v[60:63],v4 offset:52224
v_mfma_f32_16x16x32_bf16 v[28:31],a[68:71],v[32:35],v[28:31]
s_waitcnt lgkmcnt(4)
v_mfma_f32_16x16x32_bf16 v[28:31],v[100:103],v[40:43],v[28:31]
ds_read_b128 v[32:35],v4 offset:10240
v_mfma_f32_16x16x32_bf16 v[28:31],v[212:215],v[44:47],v[28:31]
buffer_load_dwordx4 v2,s[32:35],s63 offen offset:1024 sc1 lds
v_mfma_f32_16x16x32_bf16 v[24:27],v[208:211],v[44:47],v[24:27]
ds_read_b128 v[36:39],v4 offset:24576
v_mfma_f32_16x16x32_bf16 v[24:27],v[96:99],v[40:43],v[24:27]
s_waitcnt lgkmcnt(4)
v_mfma_f32_16x16x32_bf16 v[24:27],a[72:75],v[48:51],v[24:27]
ds_read_b128 v[40:43],v4 offset:38912
v_mfma_f32_16x16x32_bf16 v[24:27],a[184:187],v[52:55],v[24:27]
buffer_load_dwordx4 v2,s[32:35],s63 offen offset:2048 sc1 lds
v_mfma_f32_16x16x32_bf16 v[28:31],a[188:191],v[52:55],v[28:31]
ds_read_b128 v[44:47],v4 offset:53248
v_mfma_f32_16x16x32_bf16 v[28:31],a[76:79],v[48:51],v[28:31]
s_waitcnt lgkmcnt(4)
v_mfma_f32_16x16x32_bf16 v[28:31],v[108:111],v[56:59],v[28:31]
ds_read_b128 v[48:51],v4 offset:11264
v_mfma_f32_16x16x32_bf16 v[28:31],v[220:223],v[60:63],v[28:31]
buffer_load_dwordx2 v[6:7],v0,s[0:3],s66 offen sc1
v_mfma_f32_16x16x32_bf16 v[24:27],v[216:219],v[60:63],v[24:27]
ds_read_b128 v[52:55],v4 offset:25600
v_mfma_f32_16x16x32_bf16 v[24:27],v[104:107],v[56:59],v[24:27]
s_waitcnt lgkmcnt(4)
v_mfma_f32_16x16x32_bf16 v[24:27],a[80:83],v[32:35],v[24:27]
ds_read_b128 v[56:59],v4 offset:39936
v_mfma_f32_16x16x32_bf16 v[24:27],a[192:195],v[36:39],v[24:27]
buffer_load_dwordx4 v2,s[32:35],s63 offen offset:3072 sc1 lds
s_add_u32 s63,s63,4096
v_mfma_f32_16x16x32_bf16 v[28:31],a[196:199],v[36:39],v[28:31]
ds_read_b128 v[60:63],v4 offset:54272
s_add_u32 m0,s64,12288
v_mfma_f32_16x16x32_bf16 v[28:31],a[84:87],v[32:35],v[28:31]
s_waitcnt lgkmcnt(4)
v_mfma_f32_16x16x32_bf16 v[28:31],v[116:119],v[40:43],v[28:31]
ds_read_b128 v[32:35],v4 offset:12288
v_mfma_f32_16x16x32_bf16 v[28:31],v[228:231],v[44:47],v[28:31]
buffer_load_dwordx4 v2,s[32:35],s63 offen offset:0 sc1 lds
v_mfma_f32_16x16x32_bf16 v[24:27],v[224:227],v[44:47],v[24:27]
ds_read_b128 v[36:39],v4 offset:26624
v_mfma_f32_16x16x32_bf16 v[24:27],v[112:115],v[40:43],v[24:27]
s_waitcnt lgkmcnt(4)
v_mfma_f32_16x16x32_bf16 v[24:27],a[88:91],v[48:51],v[24:27]
ds_read_b128 v[40:43],v4 offset:40960
v_mfma_f32_16x16x32_bf16 v[24:27],a[200:203],v[52:55],v[24:27]
buffer_load_dwordx4 v2,s[32:35],s63 offen offset:1024 sc1 lds
s_add_u32 s63,s63,s31
v_mfma_f32_16x16x32_bf16 v[28:31],a[204:207],v[52:55],v[28:31]
ds_read_b128 v[44:47],v4 offset:55296
v_mfma_f32_16x16x32_bf16 v[28:31],a[92:95],v[48:51],v[28:31]
s_waitcnt lgkmcnt(4)
v_mfma_f32_16x16x32_bf16 v[28:31],v[124:127],v[56:59],v[28:31]
ds_read_b128 v[48:51],v4 offset:13312
v_mfma_f32_16x16x32_bf16 v[28:31],v[236:239],v[60:63],v[28:31]
v_mfma_f32_16x16x32_bf16 v[24:27],v[232:235],v[60:63],v[24:27]
ds_read_b128 v[52:55],v4 offset:27648
v_mfma_f32_16x16x32_bf16 v[24:27],v[120:123],v[56:59],v[24:27]
s_waitcnt lgkmcnt(4)
v_mfma_f32_16x16x32_bf16 v[24:27],a[96:99],v[32:35],v[24:27]
ds_read_b128 v[56:59],v4 offset:41984
v_mfma_f32_16x16x32_bf16 v[24:27],a[208:211],v[36:39],v[24:27]
v_mfma_f32_16x16x32_bf16 v[28:31],a[212:215],v[36:39],v[28:31]
s_waitcnt vmcnt(11)
s_barrier
v_mfma_f32_16x16x32_bf16 v[28:31],a[100:103],v[32:35],v[28:31]
ds_read_b128 v[60:63],v4 offset:56320
s_waitcnt lgkmcnt(4)
v_mfma_f32_16x16x32_bf16 v[28:31],v[132:135],v[40:43],v[28:31]
ds_read_b128 v[32:35],v2 offset:0
v_mfma_f32_16x16x32_bf16 v[28:31],v[244:247],v[44:47],v[28:31]
v_mfma_f32_16x16x32_bf16 v[24:27],v[240:243],v[44:47],v[24:27]
ds_read_b128 v[36:39],v2 offset:14336
v_mfma_f32_16x16x32_bf16 v[24:27],v[128:131],v[40:43],v[24:27]
s_waitcnt lgkmcnt(4)
v_mfma_f32_16x16x32_bf16 v[24:27],a[104:107],v[48:51],v[24:27]
ds_read_b128 v[40:43],v2 offset:28672
v_mfma_f32_16x16x32_bf16 v[24:27],a[216:219],v[52:55],v[24:27]
v_mfma_f32_16x16x32_bf16 v[28:31],a[220:223],v[52:55],v[28:31]
ds_read_b128 v[44:47],v2 offset:43008
v_mfma_f32_16x16x32_bf16 v[28:31],a[108:111],v[48:51],v[28:31]
s_waitcnt lgkmcnt(4)
v_mfma_f32_16x16x32_bf16 v[28:31],v[140:143],v[56:59],v[28:31]
ds_read_b128 v[48:51],v2 offset:1024
v_mfma_f32_16x16x32_bf16 v[28:31],v[252:255],v[60:63],v[28:31]
v_mfma_f32_16x16x32_bf16 v[24:27],v[248:251],v[60:63],v[24:27]
ds_read_b128 v[52:55],v2 offset:15360
s_sub_u32 s62,s62,1
v_mfma_f32_16x16x32_bf16 v[24:27],v[136:139],v[56:59],v[24:27]
s_cbranch_scc1 .L_dbt_1
s_waitcnt lgkmcnt(4)
v_mfma_f32_16x16x32_bf16 v[16:19],a[0:3],v[32:35],0
ds_read_b128 v[56:59],v2 offset:29696
v_mfma_f32_16x16x32_bf16 v[16:19],a[112:115],v[36:39],v[16:19]
v_mfma_f32_16x16x32_bf16 v[20:23],a[116:119],v[36:39],0
ds_read_b128 v[60:63],v2 offset:44032
v_mfma_f32_16x16x32_bf16 v[20:23],a[4:7],v[32:35],v[20:23]
v_cvt_pk_bf16_f32 v15,v30,v31
s_waitcnt lgkmcnt(4)
v_mfma_f32_16x16x32_bf16 v[20:23],a[228:231],v[40:43],v[20:23]
ds_read_b128 v[32:35],v2 offset:2048
v_cvt_pk_bf16_f32 v14,v28,v29
v_mfma_f32_16x16x32_bf16 v[20:23],v[148:151],v[44:47],v[20:23]
v_cvt_pk_bf16_f32 v13,v26,v27
v_cvt_pk_bf16_f32 v12,v24,v25
v_mfma_f32_16x16x32_bf16 v[16:19],v[144:147],v[44:47],v[16:19]
ds_read_b128 v[36:39],v2 offset:16384
ds_write_b128 v5,v[12:15]
v_mfma_f32_16x16x32_bf16 v[16:19],a[224:227],v[40:43],v[16:19]
ds_read2st64_b32 v[12:13],v3 offset0:0 offset1:1
s_waitcnt lgkmcnt(6)
v_mfma_f32_16x16x32_bf16 v[16:19],a[8:11],v[48:51],v[16:19]
ds_read_b128 v[40:43],v2 offset:30720
ds_read2st64_b32 v[14:15],v3 offset0:2 offset1:3
v_mfma_f32_16x16x32_bf16 v[16:19],a[120:123],v[52:55],v[16:19]
v_mfma_f32_16x16x32_bf16 v[20:23],a[124:127],v[52:55],v[20:23]
ds_read_b128 v[44:47],v2 offset:45056
v_mfma_f32_16x16x32_bf16 v[20:23],a[12:15],v[48:51],v[20:23]
s_waitcnt lgkmcnt(7)
v_mfma_f32_16x16x32_bf16 v[20:23],a[236:239],v[56:59],v[20:23]
ds_read_b128 v[48:51],v2 offset:3072
v_mfma_f32_16x16x32_bf16 v[20:23],v[156:159],v[60:63],v[20:23]
s_waitcnt lgkmcnt(2)
buffer_atomic_pk_add_bf16 v12,v1,s[44:47],s65 offen offset:0 sc1
v_mfma_f32_16x16x32_bf16 v[16:19],v[152:155],v[60:63],v[16:19]
ds_read_b128 v[52:55],v2 offset:17408
s_waitcnt vmcnt(4)
v_mfma_f32_16x16x32_bf16 v[16:19],a[232:235],v[56:59],v[16:19]
v_cmp_ne_u64 vcc,v[8:9],v[6:7]
v_mfma_f32_16x16x32_bf16 v[16:19],a[16:19],v[32:35],v[16:19]
ds_read_b128 v[56:59],v2 offset:31744
s_cbranch_vccnz .L_rtb0_1
.L_bkb0_1:
v_mfma_f32_16x16x32_bf16 v[16:19],a[128:131],v[36:39],v[16:19]
s_add_u32 s66,s66,s30
s_add_u32 s70,s70,s68
v_mfma_f32_16x16x32_bf16 v[20:23],a[132:135],v[36:39],v[20:23]
s_waitcnt vmcnt(1)
s_barrier
v_mfma_f32_16x16x32_bf16 v[20:23],a[20:23],v[32:35],v[20:23]
buffer_atomic_pk_add_bf16 v13,v1,s[44:47],s65 offen offset:2048 sc1
s_waitcnt lgkmcnt(3)
v_mfma_f32_16x16x32_bf16 v[20:23],a[244:247],v[40:43],v[20:23]
ds_read_b128 v[60:63],v2 offset:46080
ds_read_b128 v[32:35],v2 offset:4096
v_mfma_f32_16x16x32_bf16 v[20:23],v[164:167],v[44:47],v[20:23]
s_add_u32 m0,s64,57344
s_add_u32 s65,s65,16384
v_mfma_f32_16x16x32_bf16 v[16:19],v[160:163],v[44:47],v[16:19]
ds_read_b128 v[36:39],v2 offset:18432
v_mfma_f32_16x16x32_bf16 v[16:19],a[240:243],v[40:43],v[16:19]
buffer_atomic_pk_add_bf16 v14,v1,s[44:47],s65 offen offset:0 sc1
s_waitcnt lgkmcnt(4)
v_mfma_f32_16x16x32_bf16 v[16:19],a[24:27],v[48:51],v[16:19]
ds_read_b128 v[40:43],v2 offset:32768
s_mov_b32 s68,s75
v_mfma_f32_16x16x32_bf16 v[16:19],a[136:139],v[52:55],v[16:19]
buffer_atomic_pk_add_bf16 v15,v1,s[44:47],s65 offen offset:2048 sc1
s_and_b32 s65,s70,0x3f8000
v_mfma_f32_16x16x32_bf16 v[20:23],a[140:143],v[52:55],v[20:23]
ds_read_b128 v[44:47],v2 offset:47104
v_mfma_f32_16x16x32_bf16 v[20:23],a[28:31],v[48:51],v[20:23]
s_waitcnt lgkmcnt(4)
v_mfma_f32_16x16x32_bf16 v[20:23],a[252:255],v[56:59],v[20:23]
ds_read_b128 v[48:51],v2 offset:5120
v_mfma_f32_16x16x32_bf16 v[20:23],v[172:175],v[60:63],v[20:23]
buffer_load_dwordx4 v2,s[32:35],s63 offen offset:0 sc1 lds
v_mfma_f32_16x16x32_bf16 v[16:19],v[168:171],v[60:63],v[16:19]
ds_read_b128 v[52:55],v2 offset:19456
v_mfma_f32_16x16x32_bf16 v[16:19],a[248:251],v[56:59],v[16:19]
s_waitcnt lgkmcnt(4)
v_mfma_f32_16x16x32_bf16 v[16:19],a[32:35],v[32:35],v[16:19]
ds_read_b128 v[56:59],v2 offset:33792
v_mfma_f32_16x16x32_bf16 v[16:19],a[144:147],v[36:39],v[16:19]
buffer_load_dwordx4 v2,s[32:35],s63 offen offset:1024 sc1 lds
v_mfma_f32_16x16x32_bf16 v[20:23],a[148:151],v[36:39],v[20:23]
ds_read_b128 v[60:63],v2 offset:48128
v_mfma_f32_16x16x32_bf16 v[20:23],a[36:39],v[32:35],v[20:23]
s_waitcnt lgkmcnt(4)
v_mfma_f32_16x16x32_bf16 v[20:23],v[68:71],v[40:43],v[20:23]
ds_read_b128 v[32:35],v2 offset:6144
v_mfma_f32_16x16x32_bf16 v[20:23],v[180:183],v[44:47],v[20:23]
buffer_load_dwordx4 v2,s[32:35],s63 offen offset:2048 sc1 lds
v_mfma_f32_16x16x32_bf16 v[16:19],v[176:179],v[44:47],v[16:19]
ds_read_b128 v[36:39],v2 offset:20480
v_mfma_f32_16x16x32_bf16 v[16:19],v[64:67],v[40:43],v[16:19]
s_waitcnt lgkmcnt(4)
v_mfma_f32_16x16x32_bf16 v[16:19],a[40:43],v[48:51],v[16:19]
ds_read_b128 v[40:43],v2 offset:34816
v_mfma_f32_16x16x32_bf16 v[16:19],a[152:155],v[52:55],v[16:19]
buffer_load_dwordx4 v2,s[32:35],s63 offen offset:3072 sc1 lds
s_add_u32 s63,s63,4096
v_mfma_f32_16x16x32_bf16 v[20:23],a[156:159],v[52:55],v[20:23]
ds_read_b128 v[44:47],v2 offset:49152
s_add_u32 m0,s64,61440
v_mfma_f32_16x16x32_bf16 v[20:23],a[44:47],v[48:51],v[20:23]
s_waitcnt lgkmcnt(4)
v_mfma_f32_16x16x32_bf16 v[20:23],v[76:79],v[56:59],v[20:23]
ds_read_b128 v[48:51],v2 offset:7168
v_mfma_f32_16x16x32_bf16 v[20:23],v[188:191],v[60:63],v[20:23]
buffer_load_dwordx4 v2,s[32:35],s63 offen offset:0 sc1 lds
v_mfma_f32_16x16x32_bf16 v[16:19],v[184:187],v[60:63],v[16:19]
ds_read_b128 v[52:55],v2 offset:21504
v_mfma_f32_16x16x32_bf16 v[16:19],v[72:75],v[56:59],v[16:19]
s_waitcnt lgkmcnt(4)
v_mfma_f32_16x16x32_bf16 v[16:19],a[48:51],v[32:35],v[16:19]
ds_read_b128 v[56:59],v2 offset:35840
v_mfma_f32_16x16x32_bf16 v[16:19],a[160:163],v[36:39],v[16:19]
buffer_load_dwordx4 v2,s[32:35],s63 offen offset:1024 sc1 lds
v_mfma_f32_16x16x32_bf16 v[20:23],a[164:167],v[36:39],v[20:23]
ds_read_b128 v[60:63],v2 offset:50176
v_mfma_f32_16x16x32_bf16 v[20:23],a[52:55],v[32:35],v[20:23]
s_waitcnt lgkmcnt(4)
v_mfma_f32_16x16x32_bf16 v[20:23],v[84:87],v[40:43],v[20:23]
ds_read_b128 v[32:35],v2 offset:8192
v_mfma_f32_16x16x32_bf16 v[20:23],v[196:199],v[44:47],v[20:23]
buffer_load_dwordx4 v2,s[32:35],s63 offen offset:2048 sc1 lds
v_mfma_f32_16x16x32_bf16 v[16:19],v[192:195],v[44:47],v[16:19]
ds_read_b128 v[36:39],v2 offset:22528
v_mfma_f32_16x16x32_bf16 v[16:19],v[80:83],v[40:43],v[16:19]
s_waitcnt lgkmcnt(4)
v_mfma_f32_16x16x32_bf16 v[16:19],a[56:59],v[48:51],v[16:19]
ds_read_b128 v[40:43],v2 offset:36864
v_mfma_f32_16x16x32_bf16 v[16:19],a[168:171],v[52:55],v[16:19]
buffer_load_dwordx4 v2,s[32:35],s63 offen offset:3072 sc1 lds
v_mfma_f32_16x16x32_bf16 v[20:23],a[172:175],v[52:55],v[20:23]
ds_read_b128 v[44:47],v2 offset:51200
v_mfma_f32_16x16x32_bf16 v[20:23],a[60:63],v[48:51],v[20:23]
s_waitcnt lgkmcnt(4)
v_mfma_f32_16x16x32_bf16 v[20:23],v[92:95],v[56:59],v[20:23]
ds_read_b128 v[48:51],v2 offset:9216
v_mfma_f32_16x16x32_bf16 v[20:23],v[204:207],v[60:63],v[20:23]
s_add_u32 s63,s63,4096
s_add_u32 m0,s64,65536
v_mfma_f32_16x16x32_bf16 v[16:19],v[200:203],v[60:63],v[16:19]
ds_read_b128 v[52:55],v2 offset:23552
v_mfma_f32_16x16x32_bf16 v[16:19],v[88:91],v[56:59],v[16:19]
s_waitcnt lgkmcnt(4)
v_mfma_f32_16x16x32_bf16 v[16:19],a[64:67],v[32:35],v[16:19]
ds_read_b128 v[56:59],v2 offset:37888
v_mfma_f32_16x16x32_bf16 v[16:19],a[176:179],v[36:39],v[16:19]
buffer_load_dwordx4 v2,s[32:35],s63 offen offset:0 sc1 lds
v_mfma_f32_16x16x32_bf16 v[20:23],a[180:183],v[36:39],v[20:23]
ds_read_b128 v[60:63],v2 offset:52224
v_mfma_f32_16x16x32_bf16 v[20:23],a[68:71],v[32:35],v[20:23]
s_waitcnt lgkmcnt(4)
v_mfma_f32_16x16x32_bf16 v[20:23],v[100:103],v[40:43],v[20:23]
ds_read_b128 v[32:35],v2 offset:10240
v_mfma_f32_16x16x32_bf16 v[20:23],v[212:215],v[44:47],v[20:23]
buffer_load_dwordx4 v2,s[32:35],s63 offen offset:1024 sc1 lds
v_mfma_f32_16x16x32_bf16 v[16:19],v[208:211],v[44:47],v[16:19]
ds_read_b128 v[36:39],v2 offset:24576
v_mfma_f32_16x16x32_bf16 v[16:19],v[96:99],v[40:43],v[16:19]
s_waitcnt lgkmcnt(4)
v_mfma_f32_16x16x32_bf16 v[16:19],a[72:75],v[48:51],v[16:19]
ds_read_b128 v[40:43],v2 offset:38912
v_mfma_f32_16x16x32_bf16 v[16:19],a[184:187],v[52:55],v[16:19]
buffer_load_dwordx4 v2,s[32:35],s63 offen offset:2048 sc1 lds
v_mfma_f32_16x16x32_bf16 v[20:23],a[188:191],v[52:55],v[20:23]
ds_read_b128 v[44:47],v2 offset:53248
v_mfma_f32_16x16x32_bf16 v[20:23],a[76:79],v[48:51],v[20:23]
s_waitcnt lgkmcnt(4)
v_mfma_f32_16x16x32_bf16 v[20:23],v[108:111],v[56:59],v[20:23]
ds_read_b128 v[48:51],v2 offset:11264
v_mfma_f32_16x16x32_bf16 v[20:23],v[220:223],v[60:63],v[20:23]
buffer_load_dwordx2 v[6:7],v0,s[0:3],s66 offen sc1
v_mfma_f32_16x16x32_bf16 v[16:19],v[216:219],v[60:63],v[16:19]
ds_read_b128 v[52:55],v2 offset:25600
v_mfma_f32_16x16x32_bf16 v[16:19],v[104:107],v[56:59],v[16:19]
s_waitcnt lgkmcnt(4)
v_mfma_f32_16x16x32_bf16 v[16:19],a[80:83],v[32:35],v[16:19]
ds_read_b128 v[56:59],v2 offset:39936
v_mfma_f32_16x16x32_bf16 v[16:19],a[192:195],v[36:39],v[16:19]
buffer_load_dwordx4 v2,s[32:35],s63 offen offset:3072 sc1 lds
s_add_u32 s63,s63,4096
v_mfma_f32_16x16x32_bf16 v[20:23],a[196:199],v[36:39],v[20:23]
ds_read_b128 v[60:63],v2 offset:54272
s_add_u32 m0,s64,69632
v_mfma_f32_16x16x32_bf16 v[20:23],a[84:87],v[32:35],v[20:23]
s_waitcnt lgkmcnt(4)
v_mfma_f32_16x16x32_bf16 v[20:23],v[116:119],v[40:43],v[20:23]
ds_read_b128 v[32:35],v2 offset:12288
v_mfma_f32_16x16x32_bf16 v[20:23],v[228:231],v[44:47],v[20:23]
buffer_load_dwordx4 v2,s[32:35],s63 offen offset:0 sc1 lds
v_mfma_f32_16x16x32_bf16 v[16:19],v[224:227],v[44:47],v[16:19]
ds_read_b128 v[36:39],v2 offset:26624
v_mfma_f32_16x16x32_bf16 v[16:19],v[112:115],v[40:43],v[16:19]
s_waitcnt lgkmcnt(4)
v_mfma_f32_16x16x32_bf16 v[16:19],a[88:91],v[48:51],v[16:19]
ds_read_b128 v[40:43],v2 offset:40960
v_mfma_f32_16x16x32_bf16 v[16:19],a[200:203],v[52:55],v[16:19]
buffer_load_dwordx4 v2,s[32:35],s63 offen offset:1024 sc1 lds
s_add_u32 s63,s63,s31
v_mfma_f32_16x16x32_bf16 v[20:23],a[204:207],v[52:55],v[20:23]
ds_read_b128 v[44:47],v2 offset:55296
v_mfma_f32_16x16x32_bf16 v[20:23],a[92:95],v[48:51],v[20:23]
s_waitcnt lgkmcnt(4)
v_mfma_f32_16x16x32_bf16 v[20:23],v[124:127],v[56:59],v[20:23]
ds_read_b128 v[48:51],v2 offset:13312
v_mfma_f32_16x16x32_bf16 v[20:23],v[236:239],v[60:63],v[20:23]
v_mfma_f32_16x16x32_bf16 v[16:19],v[232:235],v[60:63],v[16:19]
ds_read_b128 v[52:55],v2 offset:27648
v_mfma_f32_16x16x32_bf16 v[16:19],v[120:123],v[56:59],v[16:19]
s_waitcnt lgkmcnt(4)
v_mfma_f32_16x16x32_bf16 v[16:19],a[96:99],v[32:35],v[16:19]
ds_read_b128 v[56:59],v2 offset:41984
v_mfma_f32_16x16x32_bf16 v[16:19],a[208:211],v[36:39],v[16:19]
v_mfma_f32_16x16x32_bf16 v[20:23],a[212:215],v[36:39],v[20:23]
s_waitcnt vmcnt(11)
s_barrier
v_mfma_f32_16x16x32_bf16 v[20:23],a[100:103],v[32:35],v[20:23]
ds_read_b128 v[60:63],v2 offset:56320
s_waitcnt lgkmcnt(4)
v_mfma_f32_16x16x32_bf16 v[20:23],v[132:135],v[40:43],v[20:23]
ds_read_b128 v[32:35],v4 offset:0
v_mfma_f32_16x16x32_bf16 v[20:23],v[244:247],v[44:47],v[20:23]
v_mfma_f32_16x16x32_bf16 v[16:19],v[240:243],v[44:47],v[16:19]
ds_read_b128 v[36:39],v4 offset:14336
v_mfma_f32_16x16x32_bf16 v[16:19],v[128:131],v[40:43],v[16:19]
s_waitcnt lgkmcnt(4)
v_mfma_f32_16x16x32_bf16 v[16:19],a[104:107],v[48:51],v[16:19]
ds_read_b128 v[40:43],v4 offset:28672
v_mfma_f32_16x16x32_bf16 v[16:19],a[216:219],v[52:55],v[16:19]
v_mfma_f32_16x16x32_bf16 v[20:23],a[220:223],v[52:55],v[20:23]
ds_read_b128 v[44:47],v4 offset:43008
v_mfma_f32_16x16x32_bf16 v[20:23],a[108:111],v[48:51],v[20:23]
s_waitcnt lgkmcnt(4)
v_mfma_f32_16x16x32_bf16 v[20:23],v[140:143],v[56:59],v[20:23]
ds_read_b128 v[48:51],v4 offset:1024
v_mfma_f32_16x16x32_bf16 v[20:23],v[252:255],v[60:63],v[20:23]
v_mfma_f32_16x16x32_bf16 v[16:19],v[248:251],v[60:63],v[16:19]
ds_read_b128 v[52:55],v4 offset:15360
s_sub_u32 s62,s62,1
v_mfma_f32_16x16x32_bf16 v[16:19],v[136:139],v[56:59],v[16:19]
s_cbranch_scc0 .L_dbl_1
.L_dbu_1:
s_nop 1
s_nop 0
v_cvt_pk_bf16_f32 v15,v22,v23
v_cvt_pk_bf16_f32 v14,v20,v21
s_nop 1
v_cvt_pk_bf16_f32 v13,v18,v19
v_cvt_pk_bf16_f32 v12,v16,v17
ds_write_b128 v5,v[12:15]
ds_read2st64_b32 v[12:13],v3 offset0:0 offset1:1
ds_read2st64_b32 v[14:15],v3 offset0:2 offset1:3
s_waitcnt lgkmcnt(0)
buffer_atomic_pk_add_bf16 v12,v1,s[44:47],s65 offen offset:0 sc1
s_add_u32 s70,s70,s75
buffer_atomic_pk_add_bf16 v13,v1,s[44:47],s65 offen offset:2048 sc1
s_add_u32 s65,s65,16384
buffer_atomic_pk_add_bf16 v14,v1,s[44:47],s65 offen offset:0 sc1
buffer_atomic_pk_add_bf16 v15,v1,s[44:47],s65 offen offset:2048 sc1
s_and_b32 s65,s70,0x3f8000
s_or_b32 s48,s28,s27
s_or_b32 s48,s48,s26
s_cbranch_scc1 .L_end
v_mov_b32 v8,s54
v_mov_b32 v9,s55
buffer_store_dwordx2 v[8:9],off,s[32:35],0 sc1
s_branch .L_end
.L_dbt_1:
s_nop 1
s_nop 0
v_cvt_pk_bf16_f32 v15,v30,v31
v_cvt_pk_bf16_f32 v14,v28,v29
s_nop 1
v_cvt_pk_bf16_f32 v13,v26,v27
v_cvt_pk_bf16_f32 v12,v24,v25
ds_write_b128 v5,v[12:15]
ds_read2st64_b32 v[12:13],v3 offset0:0 offset1:1
ds_read2st64_b32 v[14:15],v3 offset0:2 offset1:3
s_waitcnt lgkmcnt(0)
buffer_atomic_pk_add_bf16 v12,v1,s[44:47],s65 offen offset:0 sc1
s_add_u32 s70,s70,s68
buffer_atomic_pk_add_bf16 v13,v1,s[44:47],s65 offen offset:2048 sc1
s_add_u32 s65,s65,16384
buffer_atomic_pk_add_bf16 v14,v1,s[44:47],s65 offen offset:0 sc1
s_mov_b32 s68,s75
buffer_atomic_pk_add_bf16 v15,v1,s[44:47],s65 offen offset:2048 sc1
s_and_b32 s65,s70,0x3f8000
s_or_b32 s48,s28,s27
s_or_b32 s48,s48,s26
s_cbranch_scc1 .L_end
v_mov_b32 v8,s54
v_mov_b32 v9,s55
buffer_store_dwordx2 v[8:9],off,s[32:35],0 sc1
s_branch .L_end
.p2align 6
.L_dhv2:
.L_dal_2:
s_waitcnt lgkmcnt(4)
v_mfma_f32_16x16x32_bf16 v[16:19],a[0:3],v[32:35],0
ds_read_b128 v[56:59],v2 offset:29696
v_dot2_f32_bf16 v10,v12,s72,0
v_mfma_f32_16x16x32_bf16 v[16:19],a[112:115],v[36:39],v[16:19]
v_dot2_f32_bf16 v11,v12,v12,0
v_dot2_f32_bf16 v10,v13,s72,v10
v_mfma_f32_16x16x32_bf16 v[20:23],a[116:119],v[36:39],0
ds_read_b128 v[60:63],v2 offset:44032
v_dot2_f32_bf16 v11,v13,v13,v11
v_mfma_f32_16x16x32_bf16 v[20:23],a[4:7],v[32:35],v[20:23]
v_dot2_f32_bf16 v10,v14,s72,v10
s_waitcnt lgkmcnt(4)
v_mfma_f32_16x16x32_bf16 v[20:23],a[228:231],v[40:43],v[20:23]
ds_read_b128 v[32:35],v2 offset:2048
v_dot2_f32_bf16 v11,v14,v14,v11
v_mfma_f32_16x16x32_bf16 v[20:23],v[148:151],v[44:47],v[20:23]
v_dot2_f32_bf16 v10,v15,s72,v10
v_dot2_f32_bf16 v11,v15,v15,v11
v_mfma_f32_16x16x32_bf16 v[16:19],v[144:147],v[44:47],v[16:19]
ds_read_b128 v[36:39],v2 offset:16384
v_mfma_f32_16x16x32_bf16 v[16:19],a[224:227],v[40:43],v[16:19]
v_permlane16_swap_b32 v10,v11
s_waitcnt lgkmcnt(4)
v_mfma_f32_16x16x32_bf16 v[16:19],a[8:11],v[48:51],v[16:19]
ds_read_b128 v[40:43],v2 offset:30720
v_add_f32 v10,v10,v11
v_mfma_f32_16x16x32_bf16 v[16:19],a[120:123],v[52:55],v[16:19]
v_mfma_f32_16x16x32_bf16 v[20:23],a[124:127],v[52:55],v[20:23]
ds_read_b128 v[44:47],v2 offset:45056
v_permlane32_swap_b32 v10,v11
v_mfma_f32_16x16x32_bf16 v[20:23],a[12:15],v[48:51],v[20:23]
v_add_f32 v10,v10,v11
s_waitcnt lgkmcnt(4)
v_mfma_f32_16x16x32_bf16 v[20:23],a[236:239],v[56:59],v[20:23]
ds_read_b128 v[48:51],v2 offset:3072
v_mfma_f32_16x16x32_bf16 v[20:23],v[156:159],v[60:63],v[20:23]
buffer_store_dword v10,v3,s[32:35],s67 offen offset:1024 sc1
v_mfma_f32_16x16x32_bf16 v[16:19],v[152:155],v[60:63],v[16:19]
ds_read_b128 v[52:55],v2 offset:17408
v_mfma_f32_16x16x32_bf16 v[16:19],a[232:235],v[56:59],v[16:19]
s_waitcnt lgkmcnt(4)
v_mfma_f32_16x16x32_bf16 v[16:19],a[16:19],v[32:35],v[16:19]
ds_read_b128 v[56:59],v2 offset:31744
v_mfma_f32_16x16x32_bf16 v[16:19],a[128:131],v[36:39],v[16:19]
s_waitcnt vmcnt(2)
s_barrier
v_mfma_f32_16x16x32_bf16 v[20:23],a[132:135],v[36:39],v[20:23]
ds_read_b128 v[60:63],v2 offset:46080
v_mfma_f32_16x16x32_bf16 v[20:23],a[20:23],v[32:35],v[20:23]
s_waitcnt lgkmcnt(4)
v_mfma_f32_16x16x32_bf16 v[20:23],a[244:247],v[40:43],v[20:23]
ds_read_b128 v[32:35],v2 offset:4096
s_mov_b32 m0,s74
v_mfma_f32_16x16x32_bf16 v[20:23],v[164:167],v[44:47],v[20:23]
buffer_load_dwordx4 v2,s[32:35],s71 offen offset:0 sc1 lds
v_mfma_f32_16x16x32_bf16 v[16:19],v[160:163],v[44:47],v[16:19]
ds_read_b128 v[36:39],v2 offset:18432
v_mfma_f32_16x16x32_bf16 v[16:19],a[240:243],v[40:43],v[16:19]
s_waitcnt lgkmcnt(4)
v_mfma_f32_16x16x32_bf16 v[16:19],a[24:27],v[48:51],v[16:19]
ds_read_b128 v[40:43],v2 offset:32768
v_mfma_f32_16x16x32_bf16 v[16:19],a[136:139],v[52:55],v[16:19]
buffer_load_dwordx4 v2,s[32:35],s71 offen offset:1024 sc1 lds
s_add_u32 s71,s71,s30
v_mfma_f32_16x16x32_bf16 v[20:23],a[140:143],v[52:55],v[20:23]
ds_read_b128 v[44:47],v2 offset:47104
s_add_u32 m0,s64,57344
v_mfma_f32_16x16x32_bf16 v[20:23],a[28:31],v[48:51],v[20:23]
s_waitcnt lgkmcnt(4)
v_mfma_f32_16x16x32_bf16 v[20:23],a[252:255],v[56:59],v[20:23]
ds_read_b128 v[48:51],v2 offset:5120
v_mfma_f32_16x16x32_bf16 v[20:23],v[172:175],v[60:63],v[20:23]
buffer_load_dwordx4 v2,s[32:35],s63 offen offset:0 sc1 lds
v_mfma_f32_16x16x32_bf16 v[16:19],v[168:171],v[60:63],v[16:19]
ds_read_b128 v[52:55],v2 offset:19456
v_mfma_f32_16x16x32_bf16 v[16:19],a[248:251],v[56:59],v[16:19]
s_waitcnt lgkmcnt(4)
v_mfma_f32_16x16x32_bf16 v[16:19],a[32:35],v[32:35],v[16:19]
ds_read_b128 v[56:59],v2 offset:33792
v_mfma_f32_16x16x32_bf16 v[16:19],a[144:147],v[36:39],v[16:19]
buffer_load_dwordx4 v2,s[32:35],s63 offen offset:1024 sc1 lds
v_mfma_f32_16x16x32_bf16 v[20:23],a[148:151],v[36:39],v[20:23]
ds_read_b128 v[60:63],v2 offset:48128
v_mfma_f32_16x16x32_bf16 v[20:23],a[36:39],v[32:35],v[20:23]
s_waitcnt lgkmcnt(4)
v_mfma_f32_16x16x32_bf16 v[20:23],v[68:71],v[40:43],v[20:23]
ds_read_b128 v[32:35],v2 offset:6144
v_mfma_f32_16x16x32_bf16 v[20:23],v[180:183],v[44:47],v[20:23]
buffer_load_dwordx4 v2,s[32:35],s63 offen offset:2048 sc1 lds
v_mfma_f32_16x16x32_bf16 v[16:19],v[176:179],v[44:47],v[16:19]
ds_read_b128 v[36:39],v2 offset:20480
)#"
R"#(
v_mfma_f32_16x16x32_bf16 v[16:19],v[64:67],v[40:43],v[16:19]
s_waitcnt lgkmcnt(4)
v_mfma_f32_16x16x32_bf16 v[16:19],a[40:43],v[48:51],v[16:19]
ds_read_b128 v[40:43],v2 offset:34816
v_mfma_f32_16x16x32_bf16 v[16:19],a[152:155],v[52:55],v[16:19]
buffer_load_dwordx4 v2,s[32:35],s63 offen offset:3072 sc1 lds
s_add_u32 s63,s63,4096
v_mfma_f32_16x16x32_bf16 v[20:23],a[156:159],v[52:55],v[20:23]
ds_read_b128 v[44:47],v2 offset:49152
s_add_u32 m0,s64,61440
v_mfma_f32_16x16x32_bf16 v[20:23],a[44:47],v[48:51],v[20:23]
s_waitcnt lgkmcnt(4)
v_mfma_f32_16x16x32_bf16 v[20:23],v[76:79],v[56:59],v[20:23]
ds_read_b128 v[48:51],v2 offset:7168
v_mfma_f32_16x16x32_bf16 v[20:23],v[188:191],v[60:63],v[20:23]
buffer_load_dwordx4 v2,s[32:35],s63 offen offset:0 sc1 lds
v_mfma_f32_16x16x32_bf16 v[16:19],v[184:187],v[60:63],v[16:19]
ds_read_b128 v[52:55],v2 offset:21504
v_mfma_f32_16x16x32_bf16 v[16:19],v[72:75],v[56:59],v[16:19]
s_waitcnt lgkmcnt(4)
v_mfma_f32_16x16x32_bf16 v[16:19],a[48:51],v[32:35],v[16:19]
ds_read_b128 v[56:59],v2 offset:35840
v_mfma_f32_16x16x32_bf16 v[16:19],a[160:163],v[36:39],v[16:19]
buffer_load_dwordx4 v2,s[32:35],s63 offen offset:1024 sc1 lds
v_mfma_f32_16x16x32_bf16 v[20:23],a[164:167],v[36:39],v[20:23]
ds_read_b128 v[60:63],v2 offset:50176
v_mfma_f32_16x16x32_bf16 v[20:23],a[52:55],v[32:35],v[20:23]
s_waitcnt lgkmcnt(4)
v_mfma_f32_16x16x32_bf16 v[20:23],v[84:87],v[40:43],v[20:23]
ds_read_b128 v[32:35],v2 offset:8192
v_mfma_f32_16x16x32_bf16 v[20:23],v[196:199],v[44:47],v[20:23]
buffer_load_dwordx4 v2,s[32:35],s63 offen offset:2048 sc1 lds
v_mfma_f32_16x16x32_bf16 v[16:19],v[192:195],v[44:47],v[16:19]
ds_read_b128 v[36:39],v2 offset:22528
v_mfma_f32_16x16x32_bf16 v[16:19],v[80:83],v[40:43],v[16:19]
s_waitcnt lgkmcnt(4)
v_mfma_f32_16x16x32_bf16 v[16:19],a[56:59],v[48:51],v[16:19]
ds_read_b128 v[40:43],v2 offset:36864
v_mfma_f32_16x16x32_bf16 v[16:19],a[168:171],v[52:55],v[16:19]
buffer_load_dwordx4 v2,s[32:35],s63 offen offset:3072 sc1 lds
v_mfma_f32_16x16x32_bf16 v[20:23],a[172:175],v[52:55],v[20:23]
ds_read_b128 v[44:47],v2 offset:51200
v_mfma_f32_16x16x32_bf16 v[20:23],a[60:63],v[48:51],v[20:23]
s_waitcnt lgkmcnt(4)
v_mfma_f32_16x16x32_bf16 v[20:23],v[92:95],v[56:59],v[20:23]
ds_read_b128 v[48:51],v2 offset:9216
v_mfma_f32_16x16x32_bf16 v[20:23],v[204:207],v[60:63],v[20:23]
s_add_u32 s63,s63,4096
s_add_u32 m0,s64,65536
v_mfma_f32_16x16x32_bf16 v[16:19],v[200:203],v[60:63],v[16:19]
ds_read_b128 v[52:55],v2 offset:23552
v_mfma_f32_16x16x32_bf16 v[16:19],v[88:91],v[56:59],v[16:19]
s_waitcnt lgkmcnt(4)
v_mfma_f32_16x16x32_bf16 v[16:19],a[64:67],v[32:35],v[16:19]
ds_read_b128 v[56:59],v2 offset:37888
v_mfma_f32_16x16x32_bf16 v[16:19],a[176:179],v[36:39],v[16:19]
buffer_load_dwordx4 v2,s[32:35],s63 offen offset:0 sc1 lds
v_mfma_f32_16x16x32_bf16 v[20:23],a[180:183],v[36:39],v[20:23]
ds_read_b128 v[60:63],v2 offset:52224
v_mfma_f32_16x16x32_bf16 v[20:23],a[68:71],v[32:35],v[20:23]
s_waitcnt lgkmcnt(4)
v_mfma_f32_16x16x32_bf16 v[20:23],v[100:103],v[40:43],v[20:23]
ds_read_b128 v[32:35],v2 offset:10240
v_mfma_f32_16x16x32_bf16 v[20:23],v[212:215],v[44:47],v[20:23]
buffer_load_dwordx4 v2,s[32:35],s63 offen offset:1024 sc1 lds
v_mfma_f32_16x16x32_bf16 v[16:19],v[208:211],v[44:47],v[16:19]
ds_read_b128 v[36:39],v2 offset:24576
v_mfma_f32_16x16x32_bf16 v[16:19],v[96:99],v[40:43],v[16:19]
s_waitcnt lgkmcnt(4)
v_mfma_f32_16x16x32_bf16 v[16:19],a[72:75],v[48:51],v[16:19]
ds_read_b128 v[40:43],v2 offset:38912
s_waitcnt vmcnt(10)
v_mfma_f32_16x16x32_bf16 v[16:19],a[184:187],v[52:55],v[16:19]
buffer_load_dwordx4 v2,s[32:35],s63 offen offset:2048 sc1 lds
ds_read_b128 v[12:15],v5 offset:0
v_mfma_f32_16x16x32_bf16 v[20:23],a[188:191],v[52:55],v[20:23]
ds_read_b128 v[44:47],v2 offset:53248
v_mfma_f32_16x16x32_bf16 v[20:23],a[76:79],v[48:51],v[20:23]
s_waitcnt lgkmcnt(5)
v_mfma_f32_16x16x32_bf16 v[20:23],v[108:111],v[56:59],v[20:23]
ds_read_b128 v[48:51],v2 offset:11264
v_mfma_f32_16x16x32_bf16 v[20:23],v[220:223],v[60:63],v[20:23]
v_mfma_f32_16x16x32_bf16 v[16:19],v[216:219],v[60:63],v[16:19]
ds_read_b128 v[52:55],v2 offset:25600
s_waitcnt lgkmcnt(3)
v_mfma_f32_16x16x32_bf16 v[16:19],v[104:107],v[56:59],v[16:19]
v_add_f32 v24,v24,v12
v_mfma_f32_16x16x32_bf16 v[16:19],a[80:83],v[32:35],v[16:19]
ds_read_b128 v[56:59],v2 offset:39936
v_add_f32 v25,v25,v13
v_mfma_f32_16x16x32_bf16 v[16:19],a[192:195],v[36:39],v[16:19]
buffer_load_dwordx4 v2,s[32:35],s63 offen offset:3072 sc1 lds
s_add_u32 s63,s63,4096
v_mfma_f32_16x16x32_bf16 v[20:23],a[196:199],v[36:39],v[20:23]
ds_read_b128 v[60:63],v2 offset:54272
s_add_u32 m0,s64,69632
v_mfma_f32_16x16x32_bf16 v[20:23],a[84:87],v[32:35],v[20:23]
v_add_f32 v26,v26,v14
s_waitcnt lgkmcnt(4)
v_mfma_f32_16x16x32_bf16 v[20:23],v[116:119],v[40:43],v[20:23]
ds_read_b128 v[32:35],v2 offset:12288
v_add_f32 v27,v27,v15
v_mfma_f32_16x16x32_bf16 v[20:23],v[228:231],v[44:47],v[20:23]
buffer_load_dwordx4 v2,s[32:35],s63 offen offset:0 sc1 lds
ds_read_b128 v[12:15],v5 offset:1024
v_mfma_f32_16x16x32_bf16 v[16:19],v[224:227],v[44:47],v[16:19]
ds_read_b128 v[36:39],v2 offset:26624
v_mfma_f32_16x16x32_bf16 v[16:19],v[112:115],v[40:43],v[16:19]
s_waitcnt lgkmcnt(5)
v_mfma_f32_16x16x32_bf16 v[16:19],a[88:91],v[48:51],v[16:19]
ds_read_b128 v[40:43],v2 offset:40960
v_mfma_f32_16x16x32_bf16 v[16:19],a[200:203],v[52:55],v[16:19]
buffer_load_dwordx4 v2,s[32:35],s63 offen offset:1024 sc1 lds
s_add_u32 s63,s63,s31
v_mfma_f32_16x16x32_bf16 v[20:23],a[204:207],v[52:55],v[20:23]
ds_read_b128 v[44:47],v2 offset:55296
s_waitcnt lgkmcnt(3)
v_mfma_f32_16x16x32_bf16 v[20:23],a[92:95],v[48:51],v[20:23]
v_add_f32 v28,v28,v12
v_mfma_f32_16x16x32_bf16 v[20:23],v[124:127],v[56:59],v[20:23]
ds_read_b128 v[48:51],v2 offset:13312
v_add_f32 v29,v29,v13
v_mfma_f32_16x16x32_bf16 v[20:23],v[236:239],v[60:63],v[20:23]
v_add_f32 v30,v30,v14
v_add_f32 v31,v31,v15
v_mfma_f32_16x16x32_bf16 v[16:19],v[232:235],v[60:63],v[16:19]
ds_read_b128 v[52:55],v2 offset:27648
v_cvt_pk_bf16_f32 v12,v24,v25
v_mfma_f32_16x16x32_bf16 v[16:19],v[120:123],v[56:59],v[16:19]
v_cvt_pk_bf16_f32 v13,v26,v27
s_waitcnt lgkmcnt(4)
v_mfma_f32_16x16x32_bf16 v[16:19],a[96:99],v[32:35],v[16:19]
ds_read_b128 v[56:59],v2 offset:41984
v_cvt_pk_bf16_f32 v14,v28,v29
v_mfma_f32_16x16x32_bf16 v[16:19],a[208:211],v[36:39],v[16:19]
s_waitcnt vmcnt(10)
s_barrier
v_mfma_f32_16x16x32_bf16 v[20:23],a[212:215],v[36:39],v[20:23]
ds_read_b128 v[60:63],v2 offset:56320
v_cvt_pk_bf16_f32 v15,v30,v31
v_mfma_f32_16x16x32_bf16 v[20:23],a[100:103],v[32:35],v[20:23]
s_waitcnt lgkmcnt(4)
v_mfma_f32_16x16x32_bf16 v[20:23],v[132:135],v[40:43],v[20:23]
ds_read_b128 v[32:35],v4 offset:0
v_mfma_f32_16x16x32_bf16 v[20:23],v[244:247],v[44:47],v[20:23]
buffer_store_dwordx4 v[12:15],v2,s[32:35],s68 offen sc1
v_mfma_f32_16x16x32_bf16 v[16:19],v[240:243],v[44:47],v[16:19]
ds_read_b128 v[36:39],v4 offset:14336
v_mfma_f32_16x16x32_bf16 v[16:19],v[128:131],v[40:43],v[16:19]
s_waitcnt lgkmcnt(4)
v_mfma_f32_16x16x32_bf16 v[16:19],a[104:107],v[48:51],v[16:19]
ds_read_b128 v[40:43],v4 offset:28672
v_mfma_f32_16x16x32_bf16 v[16:19],a[216:219],v[52:55],v[16:19]
v_mfma_f32_16x16x32_bf16 v[20:23],a[220:223],v[52:55],v[20:23]
ds_read_b128 v[44:47],v4 offset:43008
s_mov_b32 s67,s68
v_mfma_f32_16x16x32_bf16 v[20:23],a[108:111],v[48:51],v[20:23]
s_mov_b32 s68,s70
s_waitcnt lgkmcnt(4)
v_mfma_f32_16x16x32_bf16 v[20:23],v[140:143],v[56:59],v[20:23]
ds_read_b128 v[48:51],v4 offset:1024
s_add_u32 s70,s70,1187840
v_mfma_f32_16x16x32_bf16 v[20:23],v[252:255],v[60:63],v[20:23]
v_mfma_f32_16x16x32_bf16 v[16:19],v[248:251],v[60:63],v[16:19]
ds_read_b128 v[52:55],v4 offset:15360
s_sub_u32 s62,s62,1
v_mfma_f32_16x16x32_bf16 v[16:19],v[136:139],v[56:59],v[16:19]
s_cbranch_scc1 .L_dab_2
s_waitcnt lgkmcnt(4)
v_mfma_f32_16x16x32_bf16 v[24:27],a[0:3],v[32:35],0
ds_read_b128 v[56:59],v4 offset:29696
v_dot2_f32_bf16 v10,v12,s72,0
v_mfma_f32_16x16x32_bf16 v[24:27],a[112:115],v[36:39],v[24:27]
v_dot2_f32_bf16 v11,v12,v12,0
v_dot2_f32_bf16 v10,v13,s72,v10
v_mfma_f32_16x16x32_bf16 v[28:31],a[116:119],v[36:39],0
ds_read_b128 v[60:63],v4 offset:44032
v_dot2_f32_bf16 v11,v13,v13,v11
v_mfma_f32_16x16x32_bf16 v[28:31],a[4:7],v[32:35],v[28:31]
v_dot2_f32_bf16 v10,v14,s72,v10
s_waitcnt lgkmcnt(4)
v_mfma_f32_16x16x32_bf16 v[28:31],a[228:231],v[40:43],v[28:31]
ds_read_b128 v[32:35],v4 offset:2048
v_dot2_f32_bf16 v11,v14,v14,v11
v_mfma_f32_16x16x32_bf16 v[28:31],v[148:151],v[44:47],v[28:31]
v_dot2_f32_bf16 v10,v15,s72,v10
v_dot2_f32_bf16 v11,v15,v15,v11
v_mfma_f32_16x16x32_bf16 v[24:27],v[144:147],v[44:47],v[24:27]
ds_read_b128 v[36:39],v4 offset:16384
v_mfma_f32_16x16x32_bf16 v[24:27],a[224:227],v[40:43],v[24:27]
v_permlane16_swap_b32 v10,v11
s_waitcnt lgkmcnt(4)
v_mfma_f32_16x16x32_bf16 v[24:27],a[8:11],v[48:51],v[24:27]
ds_read_b128 v[40:43],v4 offset:30720
v_add_f32 v10,v10,v11
v_mfma_f32_16x16x32_bf16 v[24:27],a[120:123],v[52:55],v[24:27]
v_mfma_f32_16x16x32_bf16 v[28:31],a[124:127],v[52:55],v[28:31]
ds_read_b128 v[44:47],v4 offset:45056
v_permlane32_swap_b32 v10,v11
v_mfma_f32_16x16x32_bf16 v[28:31],a[12:15],v[48:51],v[28:31]
v_add_f32 v10,v10,v11
s_waitcnt lgkmcnt(4)
v_mfma_f32_16x16x32_bf16 v[28:31],a[236:239],v[56:59],v[28:31]
ds_read_b128 v[48:51],v4 offset:3072
v_mfma_f32_16x16x32_bf16 v[28:31],v[156:159],v[60:63],v[28:31]
buffer_store_dword v10,v3,s[32:35],s67 offen offset:1024 sc1
v_mfma_f32_16x16x32_bf16 v[24:27],v[152:155],v[60:63],v[24:27]
ds_read_b128 v[52:55],v4 offset:17408
v_mfma_f32_16x16x32_bf16 v[24:27],a[232:235],v[56:59],v[24:27]
s_waitcnt lgkmcnt(4)
v_mfma_f32_16x16x32_bf16 v[24:27],a[16:19],v[32:35],v[24:27]
ds_read_b128 v[56:59],v4 offset:31744
v_mfma_f32_16x16x32_bf16 v[24:27],a[128:131],v[36:39],v[24:27]
s_waitcnt vmcnt(2)
s_barrier
v_mfma_f32_16x16x32_bf16 v[28:31],a[132:135],v[36:39],v[28:31]
ds_read_b128 v[60:63],v4 offset:46080
v_mfma_f32_16x16x32_bf16 v[28:31],a[20:23],v[32:35],v[28:31]
s_waitcnt lgkmcnt(4)
v_mfma_f32_16x16x32_bf16 v[28:31],a[244:247],v[40:43],v[28:31]
ds_read_b128 v[32:35],v4 offset:4096
s_mov_b32 m0,s74
v_mfma_f32_16x16x32_bf16 v[28:31],v[164:167],v[44:47],v[28:31]
buffer_load_dwordx4 v2,s[32:35],s71 offen offset:0 sc1 lds
v_mfma_f32_16x16x32_bf16 v[24:27],v[160:163],v[44:47],v[24:27]
ds_read_b128 v[36:39],v4 offset:18432
v_mfma_f32_16x16x32_bf16 v[24:27],a[240:243],v[40:43],v[24:27]
s_waitcnt lgkmcnt(4)
v_mfma_f32_16x16x32_bf16 v[24:27],a[24:27],v[48:51],v[24:27]
ds_read_b128 v[40:43],v4 offset:32768
v_mfma_f32_16x16x32_bf16 v[24:27],a[136:139],v[52:55],v[24:27]
buffer_load_dwordx4 v2,s[32:35],s71 offen offset:1024 sc1 lds
s_add_u32 s71,s71,s30
v_mfma_f32_16x16x32_bf16 v[28:31],a[140:143],v[52:55],v[28:31]
ds_read_b128 v[44:47],v4 offset:47104
s_add_u32 m0,s64,0
v_mfma_f32_16x16x32_bf16 v[28:31],a[28:31],v[48:51],v[28:31]
s_waitcnt lgkmcnt(4)
v_mfma_f32_16x16x32_bf16 v[28:31],a[252:255],v[56:59],v[28:31]
ds_read_b128 v[48:51],v4 offset:5120
v_mfma_f32_16x16x32_bf16 v[28:31],v[172:175],v[60:63],v[28:31]
buffer_load_dwordx4 v2,s[32:35],s63 offen offset:0 sc1 lds
v_mfma_f32_16x16x32_bf16 v[24:27],v[168:171],v[60:63],v[24:27]
ds_read_b128 v[52:55],v4 offset:19456
v_mfma_f32_16x16x32_bf16 v[24:27],a[248:251],v[56:59],v[24:27]
s_waitcnt lgkmcnt(4)
v_mfma_f32_16x16x32_bf16 v[24:27],a[32:35],v[32:35],v[24:27]
ds_read_b128 v[56:59],v4 offset:33792
v_mfma_f32_16x16x32_bf16 v[24:27],a[144:147],v[36:39],v[24:27]
buffer_load_dwordx4 v2,s[32:35],s63 offen offset:1024 sc1 lds
v_mfma_f32_16x16x32_bf16 v[28:31],a[148:151],v[36:39],v[28:31]
ds_read_b128 v[60:63],v4 offset:48128
v_mfma_f32_16x16x32_bf16 v[28:31],a[36:39],v[32:35],v[28:31]
s_waitcnt lgkmcnt(4)
v_mfma_f32_16x16x32_bf16 v[28:31],v[68:71],v[40:43],v[28:31]
ds_read_b128 v[32:35],v4 offset:6144
v_mfma_f32_16x16x32_bf16 v[28:31],v[180:183],v[44:47],v[28:31]
buffer_load_dwordx4 v2,s[32:35],s63 offen offset:2048 sc1 lds
v_mfma_f32_16x16x32_bf16 v[24:27],v[176:179],v[44:47],v[24:27]
ds_read_b128 v[36:39],v4 offset:20480
v_mfma_f32_16x16x32_bf16 v[24:27],v[64:67],v[40:43],v[24:27]
s_waitcnt lgkmcnt(4)
v_mfma_f32_16x16x32_bf16 v[24:27],a[40:43],v[48:51],v[24:27]
ds_read_b128 v[40:43],v4 offset:34816
v_mfma_f32_16x16x32_bf16 v[24:27],a[152:155],v[52:55],v[24:27]
buffer_load_dwordx4 v2,s[32:35],s63 offen offset:3072 sc1 lds
s_add_u32 s63,s63,4096
v_mfma_f32_16x16x32_bf16 v[28:31],a[156:159],v[52:55],v[28:31]
ds_read_b128 v[44:47],v4 offset:49152
s_add_u32 m0,s64,4096
v_mfma_f32_16x16x32_bf16 v[28:31],a[44:47],v[48:51],v[28:31]
s_waitcnt lgkmcnt(4)
v_mfma_f32_16x16x32_bf16 v[28:31],v[76:79],v[56:59],v[28:31]
ds_read_b128 v[48:51],v4 offset:7168
v_mfma_f32_16x16x32_bf16 v[28:31],v[188:191],v[60:63],v[28:31]
buffer_load_dwordx4 v2,s[32:35],s63 offen offset:0 sc1 lds
v_mfma_f32_16x16x32_bf16 v[24:27],v[184:187],v[60:63],v[24:27]
ds_read_b128 v[52:55],v4 offset:21504
v_mfma_f32_16x16x32_bf16 v[24:27],v[72:75],v[56:59],v[24:27]
s_waitcnt lgkmcnt(4)
v_mfma_f32_16x16x32_bf16 v[24:27],a[48:51],v[32:35],v[24:27]
ds_read_b128 v[56:59],v4 offset:35840
v_mfma_f32_16x16x32_bf16 v[24:27],a[160:163],v[36:39],v[24:27]
buffer_load_dwordx4 v2,s[32:35],s63 offen offset:1024 sc1 lds
v_mfma_f32_16x16x32_bf16 v[28:31],a[164:167],v[36:39],v[28:31]
ds_read_b128 v[60:63],v4 offset:50176
v_mfma_f32_16x16x32_bf16 v[28:31],a[52:55],v[32:35],v[28:31]
s_waitcnt lgkmcnt(4)
v_mfma_f32_16x16x32_bf16 v[28:31],v[84:87],v[40:43],v[28:31]
ds_read_b128 v[32:35],v4 offset:8192
v_mfma_f32_16x16x32_bf16 v[28:31],v[196:199],v[44:47],v[28:31]
buffer_load_dwordx4 v2,s[32:35],s63 offen offset:2048 sc1 lds
v_mfma_f32_16x16x32_bf16 v[24:27],v[192:195],v[44:47],v[24:27]
ds_read_b128 v[36:39],v4 offset:22528
v_mfma_f32_16x16x32_bf16 v[24:27],v[80:83],v[40:43],v[24:27]
s_waitcnt lgkmcnt(4)
v_mfma_f32_16x16x32_bf16 v[24:27],a[56:59],v[48:51],v[24:27]
ds_read_b128 v[40:43],v4 offset:36864
v_mfma_f32_16x16x32_bf16 v[24:27],a[168:171],v[52:55],v[24:27]
buffer_load_dwordx4 v2,s[32:35],s63 offen offset:3072 sc1 lds
v_mfma_f32_16x16x32_bf16 v[28:31],a[172:175],v[52:55],v[28:31]
ds_read_b128 v[44:47],v4 offset:51200
v_mfma_f32_16x16x32_bf16 v[28:31],a[60:63],v[48:51],v[28:31]
s_waitcnt lgkmcnt(4)
v_mfma_f32_16x16x32_bf16 v[28:31],v[92:95],v[56:59],v[28:31]
ds_read_b128 v[48:51],v4 offset:9216
v_mfma_f32_16x16x32_bf16 v[28:31],v[204:207],v[60:63],v[28:31]
s_add_u32 s63,s63,4096
s_add_u32 m0,s64,8192
v_mfma_f32_16x16x32_bf16 v[24:27],v[200:203],v[60:63],v[24:27]
ds_read_b128 v[52:55],v4 offset:23552
v_mfma_f32_16x16x32_bf16 v[24:27],v[88:91],v[56:59],v[24:27]
s_waitcnt lgkmcnt(4)
v_mfma_f32_16x16x32_bf16 v[24:27],a[64:67],v[32:35],v[24:27]
ds_read_b128 v[56:59],v4 offset:37888
v_mfma_f32_16x16x32_bf16 v[24:27],a[176:179],v[36:39],v[24:27]
buffer_load_dwordx4 v2,s[32:35],s63 offen offset:0 sc1 lds
v_mfma_f32_16x16x32_bf16 v[28:31],a[180:183],v[36:39],v[28:31]
ds_read_b128 v[60:63],v4 offset:52224
v_mfma_f32_16x16x32_bf16 v[28:31],a[68:71],v[32:35],v[28:31]
s_waitcnt lgkmcnt(4)
v_mfma_f32_16x16x32_bf16 v[28:31],v[100:103],v[40:43],v[28:31]
ds_read_b128 v[32:35],v4 offset:10240
v_mfma_f32_16x16x32_bf16 v[28:31],v[212:215],v[44:47],v[28:31]
buffer_load_dwordx4 v2,s[32:35],s63 offen offset:1024 sc1 lds
v_mfma_f32_16x16x32_bf16 v[24:27],v[208:211],v[44:47],v[24:27]
ds_read_b128 v[36:39],v4 offset:24576
v_mfma_f32_16x16x32_bf16 v[24:27],v[96:99],v[40:43],v[24:27]
s_waitcnt lgkmcnt(4)
v_mfma_f32_16x16x32_bf16 v[24:27],a[72:75],v[48:51],v[24:27]
ds_read_b128 v[40:43],v4 offset:38912
s_waitcnt vmcnt(10)
v_mfma_f32_16x16x32_bf16 v[24:27],a[184:187],v[52:55],v[24:27]
buffer_load_dwordx4 v2,s[32:35],s63 offen offset:2048 sc1 lds
ds_read_b128 v[12:15],v5 offset:0
v_mfma_f32_16x16x32_bf16 v[28:31],a[188:191],v[52:55],v[28:31]
ds_read_b128 v[44:47],v4 offset:53248
v_mfma_f32_16x16x32_bf16 v[28:31],a[76:79],v[48:51],v[28:31]
s_waitcnt lgkmcnt(5)
v_mfma_f32_16x16x32_bf16 v[28:31],v[108:111],v[56:59],v[28:31]
ds_read_b128 v[48:51],v4 offset:11264
v_mfma_f32_16x16x32_bf16 v[28:31],v[220:223],v[60:63],v[28:31]
v_mfma_f32_16x16x32_bf16 v[24:27],v[216:219],v[60:63],v[24:27]
ds_read_b128 v[52:55],v4 offset:25600
s_waitcnt lgkmcnt(3)
v_mfma_f32_16x16x32_bf16 v[24:27],v[104:107],v[56:59],v[24:27]
v_add_f32 v16,v16,v12
v_mfma_f32_16x16x32_bf16 v[24:27],a[80:83],v[32:35],v[24:27]
ds_read_b128 v[56:59],v4 offset:39936
v_add_f32 v17,v17,v13
v_mfma_f32_16x16x32_bf16 v[24:27],a[192:195],v[36:39],v[24:27]
buffer_load_dwordx4 v2,s[32:35],s63 offen offset:3072 sc1 lds
s_add_u32 s63,s63,4096
v_mfma_f32_16x16x32_bf16 v[28:31],a[196:199],v[36:39],v[28:31]
ds_read_b128 v[60:63],v4 offset:54272
s_add_u32 m0,s64,12288
v_mfma_f32_16x16x32_bf16 v[28:31],a[84:87],v[32:35],v[28:31]
v_add_f32 v18,v18,v14
s_waitcnt lgkmcnt(4)
v_mfma_f32_16x16x32_bf16 v[28:31],v[116:119],v[40:43],v[28:31]
ds_read_b128 v[32:35],v4 offset:12288
v_add_f32 v19,v19,v15
v_mfma_f32_16x16x32_bf16 v[28:31],v[228:231],v[44:47],v[28:31]
buffer_load_dwordx4 v2,s[32:35],s63 offen offset:0 sc1 lds
ds_read_b128 v[12:15],v5 offset:1024
v_mfma_f32_16x16x32_bf16 v[24:27],v[224:227],v[44:47],v[24:27]
ds_read_b128 v[36:39],v4 offset:26624
v_mfma_f32_16x16x32_bf16 v[24:27],v[112:115],v[40:43],v[24:27]
s_waitcnt lgkmcnt(5)
v_mfma_f32_16x16x32_bf16 v[24:27],a[88:91],v[48:51],v[24:27]
ds_read_b128 v[40:43],v4 offset:40960
v_mfma_f32_16x16x32_bf16 v[24:27],a[200:203],v[52:55],v[24:27]
buffer_load_dwordx4 v2,s[32:35],s63 offen offset:1024 sc1 lds
s_add_u32 s63,s63,s31
v_mfma_f32_16x16x32_bf16 v[28:31],a[204:207],v[52:55],v[28:31]
ds_read_b128 v[44:47],v4 offset:55296
s_waitcnt lgkmcnt(3)
v_mfma_f32_16x16x32_bf16 v[28:31],a[92:95],v[48:51],v[28:31]
v_add_f32 v20,v20,v12
v_mfma_f32_16x16x32_bf16 v[28:31],v[124:127],v[56:59],v[28:31]
ds_read_b128 v[48:51],v4 offset:13312
v_add_f32 v21,v21,v13
v_mfma_f32_16x16x32_bf16 v[28:31],v[236:239],v[60:63],v[28:31]
v_add_f32 v22,v22,v14
v_add_f32 v23,v23,v15
v_mfma_f32_16x16x32_bf16 v[24:27],v[232:235],v[60:63],v[24:27]
ds_read_b128 v[52:55],v4 offset:27648
v_cvt_pk_bf16_f32 v12,v16,v17
v_mfma_f32_16x16x32_bf16 v[24:27],v[120:123],v[56:59],v[24:27]
v_cvt_pk_bf16_f32 v13,v18,v19
s_waitcnt lgkmcnt(4)
v_mfma_f32_16x16x32_bf16 v[24:27],a[96:99],v[32:35],v[24:27]
ds_read_b128 v[56:59],v4 offset:41984
v_cvt_pk_bf16_f32 v14,v20,v21
v_mfma_f32_16x16x32_bf16 v[24:27],a[208:211],v[36:39],v[24:27]
s_waitcnt vmcnt(10)
s_barrier
v_mfma_f32_16x16x32_bf16 v[28:31],a[212:215],v[36:39],v[28:31]
ds_read_b128 v[60:63],v4 offset:56320
v_cvt_pk_bf16_f32 v15,v22,v23
v_mfma_f32_16x16x32_bf16 v[28:31],a[100:103],v[32:35],v[28:31]
s_waitcnt lgkmcnt(4)
v_mfma_f32_16x16x32_bf16 v[28:31],v[132:135],v[40:43],v[28:31]
ds_read_b128 v[32:35],v2 offset:0
v_mfma_f32_16x16x32_bf16 v[28:31],v[244:247],v[44:47],v[28:31]
buffer_store_dwordx4 v[12:15],v2,s[32:35],s69 offen sc1
v_mfma_f32_16x16x32_bf16 v[24:27],v[240:243],v[44:47],v[24:27]
ds_read_b128 v[36:39],v2 offset:14336
v_mfma_f32_16x16x32_bf16 v[24:27],v[128:131],v[40:43],v[24:27]
s_waitcnt lgkmcnt(4)
v_mfma_f32_16x16x32_bf16 v[24:27],a[104:107],v[48:51],v[24:27]
ds_read_b128 v[40:43],v2 offset:28672
v_mfma_f32_16x16x32_bf16 v[24:27],a[216:219],v[52:55],v[24:27]
v_mfma_f32_16x16x32_bf16 v[28:31],a[220:223],v[52:55],v[28:31]
ds_read_b128 v[44:47],v2 offset:43008
s_mov_b32 s67,s69
v_mfma_f32_16x16x32_bf16 v[28:31],a[108:111],v[48:51],v[28:31]
s_mov_b32 s69,s70
s_waitcnt lgkmcnt(4)
v_mfma_f32_16x16x32_bf16 v[28:31],v[140:143],v[56:59],v[28:31]
ds_read_b128 v[48:51],v2 offset:1024
s_add_u32 s70,s70,1187840
v_mfma_f32_16x16x32_bf16 v[28:31],v[252:255],v[60:63],v[28:31]
v_mfma_f32_16x16x32_bf16 v[24:27],v[248:251],v[60:63],v[24:27]
ds_read_b128 v[52:55],v2 offset:15360
v_mfma_f32_16x16x32_bf16 v[24:27],v[136:139],v[56:59],v[24:27]
s_branch .L_dal_2
.L_dab_2:
v_dot2_f32_bf16 v10,v12,s72,0
v_dot2_f32_bf16 v11,v12,v12,0
v_dot2_f32_bf16 v10,v13,s72,v10
v_dot2_f32_bf16 v11,v13,v13,v11
v_dot2_f32_bf16 v10,v14,s72,v10
v_dot2_f32_bf16 v11,v14,v14,v11
v_dot2_f32_bf16 v10,v15,s72,v10
v_dot2_f32_bf16 v11,v15,v15,v11
s_nop 2
v_permlane16_swap_b32 v10,v11
v_add_f32 v10,v10,v11
s_nop 1
v_permlane32_swap_b32 v10,v11
v_add_f32 v10,v10,v11
buffer_store_dword v10,v3,s[32:35],s67 offen offset:1024 sc1
s_mov_b32 s62,s58
s_mov_b32 s70,s59
s_add_u32 s74,s74,8192
v_lshrrev_b32 v5,8,v2
v_lshl_add_u32 v5,v5,4,s74
v_and_b32 v3,128,v2
v_lshl_add_u32 v5,v3,2,v5
v_and_b32 v3,16,v2
v_lshl_add_u32 v5,v3,4,v5
v_and_b32 v3,96,v2
v_lshl_add_u32 v5,v3,1,v5
v_lshrrev_b32 v3,2,v2
v_add_u32 v3,s74,v3
s_add_u32 s66,s66,2375680
s_add_u32 s63,s63,2375680
s_add_u32 s71,s71,2375680
s_mov_b32 s68,98304
s_mov_b32 s34,79446016
.p2align 6
.L_dbl_2:
s_waitcnt lgkmcnt(4)
v_mfma_f32_16x16x32_bf16 v[24:27],a[0:3],v[32:35],0
ds_read_b128 v[56:59],v4 offset:29696
v_mfma_f32_16x16x32_bf16 v[24:27],a[112:115],v[36:39],v[24:27]
v_mfma_f32_16x16x32_bf16 v[28:31],a[116:119],v[36:39],0
ds_read_b128 v[60:63],v4 offset:44032
v_mfma_f32_16x16x32_bf16 v[28:31],a[4:7],v[32:35],v[28:31]
v_cvt_pk_bf16_f32 v15,v22,v23
s_waitcnt lgkmcnt(4)
v_mfma_f32_16x16x32_bf16 v[28:31],a[228:231],v[40:43],v[28:31]
ds_read_b128 v[32:35],v4 offset:2048
v_cvt_pk_bf16_f32 v14,v20,v21
v_mfma_f32_16x16x32_bf16 v[28:31],v[148:151],v[44:47],v[28:31]
v_cvt_pk_bf16_f32 v13,v18,v19
v_cvt_pk_bf16_f32 v12,v16,v17
v_mfma_f32_16x16x32_bf16 v[24:27],v[144:147],v[44:47],v[24:27]
ds_read_b128 v[36:39],v4 offset:16384
ds_write_b128 v5,v[12:15]
v_mfma_f32_16x16x32_bf16 v[24:27],a[224:227],v[40:43],v[24:27]
ds_read2st64_b32 v[12:13],v3 offset0:0 offset1:1
s_waitcnt lgkmcnt(6)
v_mfma_f32_16x16x32_bf16 v[24:27],a[8:11],v[48:51],v[24:27]
ds_read_b128 v[40:43],v4 offset:30720
ds_read2st64_b32 v[14:15],v3 offset0:2 offset1:3
v_mfma_f32_16x16x32_bf16 v[24:27],a[120:123],v[52:55],v[24:27]
v_mfma_f32_16x16x32_bf16 v[28:31],a[124:127],v[52:55],v[28:31]
ds_read_b128 v[44:47],v4 offset:45056
v_mfma_f32_16x16x32_bf16 v[28:31],a[12:15],v[48:51],v[28:31]
s_waitcnt lgkmcnt(7)
v_mfma_f32_16x16x32_bf16 v[28:31],a[236:239],v[56:59],v[28:31]
ds_read_b128 v[48:51],v4 offset:3072
v_mfma_f32_16x16x32_bf16 v[28:31],v[156:159],v[60:63],v[28:31]
s_waitcnt lgkmcnt(2)
buffer_atomic_pk_add_bf16 v12,v1,s[44:47],s65 offen offset:0 sc1
v_mfma_f32_16x16x32_bf16 v[24:27],v[152:155],v[60:63],v[24:27]
ds_read_b128 v[52:55],v4 offset:17408
s_add_u32 s70,s70,s75
)#"
R"#(
v_mfma_f32_16x16x32_bf16 v[24:27],a[232:235],v[56:59],v[24:27]
buffer_atomic_pk_add_bf16 v13,v1,s[44:47],s65 offen offset:2048 sc1
v_mfma_f32_16x16x32_bf16 v[24:27],a[16:19],v[32:35],v[24:27]
ds_read_b128 v[56:59],v4 offset:31744
s_add_u32 s65,s65,16384
v_mfma_f32_16x16x32_bf16 v[24:27],a[128:131],v[36:39],v[24:27]
s_waitcnt vmcnt(2)
s_barrier
v_mfma_f32_16x16x32_bf16 v[28:31],a[132:135],v[36:39],v[28:31]
ds_read_b128 v[60:63],v4 offset:46080
v_mfma_f32_16x16x32_bf16 v[28:31],a[20:23],v[32:35],v[28:31]
buffer_atomic_pk_add_bf16 v14,v1,s[44:47],s65 offen offset:0 sc1
s_waitcnt lgkmcnt(4)
v_mfma_f32_16x16x32_bf16 v[28:31],a[244:247],v[40:43],v[28:31]
ds_read_b128 v[32:35],v4 offset:4096
s_add_u32 m0,s64,0
v_mfma_f32_16x16x32_bf16 v[28:31],v[164:167],v[44:47],v[28:31]
buffer_atomic_pk_add_bf16 v15,v1,s[44:47],s65 offen offset:2048 sc1
s_and_b32 s65,s70,0x3f8000
v_mfma_f32_16x16x32_bf16 v[24:27],v[160:163],v[44:47],v[24:27]
ds_read_b128 v[36:39],v4 offset:18432
v_mfma_f32_16x16x32_bf16 v[24:27],a[240:243],v[40:43],v[24:27]
s_waitcnt lgkmcnt(4)
v_mfma_f32_16x16x32_bf16 v[24:27],a[24:27],v[48:51],v[24:27]
ds_read_b128 v[40:43],v4 offset:32768
v_mfma_f32_16x16x32_bf16 v[24:27],a[136:139],v[52:55],v[24:27]
buffer_load_dwordx4 v2,s[32:35],s63 offen offset:0 sc1 lds
v_mfma_f32_16x16x32_bf16 v[28:31],a[140:143],v[52:55],v[28:31]
ds_read_b128 v[44:47],v4 offset:47104
v_mfma_f32_16x16x32_bf16 v[28:31],a[28:31],v[48:51],v[28:31]
s_waitcnt lgkmcnt(4)
v_mfma_f32_16x16x32_bf16 v[28:31],a[252:255],v[56:59],v[28:31]
ds_read_b128 v[48:51],v4 offset:5120
v_mfma_f32_16x16x32_bf16 v[28:31],v[172:175],v[60:63],v[28:31]
buffer_load_dwordx4 v2,s[32:35],s63 offen offset:1024 sc1 lds
v_mfma_f32_16x16x32_bf16 v[24:27],v[168:171],v[60:63],v[24:27]
ds_read_b128 v[52:55],v4 offset:19456
v_mfma_f32_16x16x32_bf16 v[24:27],a[248:251],v[56:59],v[24:27]
s_waitcnt lgkmcnt(4)
v_mfma_f32_16x16x32_bf16 v[24:27],a[32:35],v[32:35],v[24:27]
ds_read_b128 v[56:59],v4 offset:33792
v_mfma_f32_16x16x32_bf16 v[24:27],a[144:147],v[36:39],v[24:27]
buffer_load_dwordx4 v2,s[32:35],s63 offen offset:2048 sc1 lds
v_mfma_f32_16x16x32_bf16 v[28:31],a[148:151],v[36:39],v[28:31]
ds_read_b128 v[60:63],v4 offset:48128
v_mfma_f32_16x16x32_bf16 v[28:31],a[36:39],v[32:35],v[28:31]
s_waitcnt lgkmcnt(4)
v_mfma_f32_16x16x32_bf16 v[28:31],v[68:71],v[40:43],v[28:31]
ds_read_b128 v[32:35],v4 offset:6144
v_mfma_f32_16x16x32_bf16 v[28:31],v[180:183],v[44:47],v[28:31]
buffer_load_dwordx4 v2,s[32:35],s63 offen offset:3072 sc1 lds
v_mfma_f32_16x16x32_bf16 v[24:27],v[176:179],v[44:47],v[24:27]
ds_read_b128 v[36:39],v4 offset:20480
v_mfma_f32_16x16x32_bf16 v[24:27],v[64:67],v[40:43],v[24:27]
s_waitcnt lgkmcnt(4)
v_mfma_f32_16x16x32_bf16 v[24:27],a[40:43],v[48:51],v[24:27]
ds_read_b128 v[40:43],v4 offset:34816
s_add_u32 s63,s63,4096
v_mfma_f32_16x16x32_bf16 v[24:27],a[152:155],v[52:55],v[24:27]
s_add_u32 m0,s64,4096
v_mfma_f32_16x16x32_bf16 v[28:31],a[156:159],v[52:55],v[28:31]
ds_read_b128 v[44:47],v4 offset:49152
v_mfma_f32_16x16x32_bf16 v[28:31],a[44:47],v[48:51],v[28:31]
s_waitcnt lgkmcnt(4)
v_mfma_f32_16x16x32_bf16 v[28:31],v[76:79],v[56:59],v[28:31]
ds_read_b128 v[48:51],v4 offset:7168
v_mfma_f32_16x16x32_bf16 v[28:31],v[188:191],v[60:63],v[28:31]
buffer_load_dwordx4 v2,s[32:35],s63 offen offset:0 sc1 lds
v_mfma_f32_16x16x32_bf16 v[24:27],v[184:187],v[60:63],v[24:27]
ds_read_b128 v[52:55],v4 offset:21504
v_mfma_f32_16x16x32_bf16 v[24:27],v[72:75],v[56:59],v[24:27]
s_waitcnt lgkmcnt(4)
v_mfma_f32_16x16x32_bf16 v[24:27],a[48:51],v[32:35],v[24:27]
ds_read_b128 v[56:59],v4 offset:35840
v_mfma_f32_16x16x32_bf16 v[24:27],a[160:163],v[36:39],v[24:27]
buffer_load_dwordx4 v2,s[32:35],s63 offen offset:1024 sc1 lds
v_mfma_f32_16x16x32_bf16 v[28:31],a[164:167],v[36:39],v[28:31]
ds_read_b128 v[60:63],v4 offset:50176
v_mfma_f32_16x16x32_bf16 v[28:31],a[52:55],v[32:35],v[28:31]
s_waitcnt lgkmcnt(4)
v_mfma_f32_16x16x32_bf16 v[28:31],v[84:87],v[40:43],v[28:31]
ds_read_b128 v[32:35],v4 offset:8192
v_mfma_f32_16x16x32_bf16 v[28:31],v[196:199],v[44:47],v[28:31]
buffer_load_dwordx4 v2,s[32:35],s63 offen offset:2048 sc1 lds
v_mfma_f32_16x16x32_bf16 v[24:27],v[192:195],v[44:47],v[24:27]
ds_read_b128 v[36:39],v4 offset:22528
v_mfma_f32_16x16x32_bf16 v[24:27],v[80:83],v[40:43],v[24:27]
s_waitcnt lgkmcnt(4)
v_mfma_f32_16x16x32_bf16 v[24:27],a[56:59],v[48:51],v[24:27]
ds_read_b128 v[40:43],v4 offset:36864
v_mfma_f32_16x16x32_bf16 v[24:27],a[168:171],v[52:55],v[24:27]
buffer_load_dwordx4 v2,s[32:35],s63 offen offset:3072 sc1 lds
v_mfma_f32_16x16x32_bf16 v[28:31],a[172:175],v[52:55],v[28:31]
ds_read_b128 v[44:47],v4 offset:51200
v_mfma_f32_16x16x32_bf16 v[28:31],a[60:63],v[48:51],v[28:31]
s_waitcnt lgkmcnt(4)
v_mfma_f32_16x16x32_bf16 v[28:31],v[92:95],v[56:59],v[28:31]
ds_read_b128 v[48:51],v4 offset:9216
v_mfma_f32_16x16x32_bf16 v[28:31],v[204:207],v[60:63],v[28:31]
s_add_u32 s63,s63,4096
s_add_u32 m0,s64,8192
v_mfma_f32_16x16x32_bf16 v[24:27],v[200:203],v[60:63],v[24:27]
ds_read_b128 v[52:55],v4 offset:23552
v_mfma_f32_16x16x32_bf16 v[24:27],v[88:91],v[56:59],v[24:27]
s_waitcnt lgkmcnt(4)
v_mfma_f32_16x16x32_bf16 v[24:27],a[64:67],v[32:35],v[24:27]
ds_read_b128 v[56:59],v4 offset:37888
v_mfma_f32_16x16x32_bf16 v[24:27],a[176:179],v[36:39],v[24:27]
buffer_load_dwordx4 v2,s[32:35],s63 offen offset:0 sc1 lds
v_mfma_f32_16x16x32_bf16 v[28:31],a[180:183],v[36:39],v[28:31]
ds_read_b128 v[60:63],v4 offset:52224
v_mfma_f32_16x16x32_bf16 v[28:31],a[68:71],v[32:35],v[28:31]
s_waitcnt lgkmcnt(4)
v_mfma_f32_16x16x32_bf16 v[28:31],v[100:103],v[40:43],v[28:31]
ds_read_b128 v[32:35],v4 offset:10240
v_mfma_f32_16x16x32_bf16 v[28:31],v[212:215],v[44:47],v[28:31]
buffer_load_dwordx4 v2,s[32:35],s63 offen offset:1024 sc1 lds
v_mfma_f32_16x16x32_bf16 v[24:27],v[208:211],v[44:47],v[24:27]
ds_read_b128 v[36:39],v4 offset:24576
v_mfma_f32_16x16x32_bf16 v[24:27],v[96:99],v[40:43],v[24:27]
s_waitcnt lgkmcnt(4)
v_mfma_f32_16x16x32_bf16 v[24:27],a[72:75],v[48:51],v[24:27]
ds_read_b128 v[40:43],v4 offset:38912
v_mfma_f32_16x16x32_bf16 v[24:27],a[184:187],v[52:55],v[24:27]
buffer_load_dwordx4 v2,s[32:35],s63 offen offset:2048 sc1 lds
v_mfma_f32_16x16x32_bf16 v[28:31],a[188:191],v[52:55],v[28:31]
ds_read_b128 v[44:47],v4 offset:53248
v_mfma_f32_16x16x32_bf16 v[28:31],a[76:79],v[48:51],v[28:31]
s_waitcnt lgkmcnt(4)
v_mfma_f32_16x16x32_bf16 v[28:31],v[108:111],v[56:59],v[28:31]
ds_read_b128 v[48:51],v4 offset:11264
v_mfma_f32_16x16x32_bf16 v[28:31],v[220:223],v[60:63],v[28:31]
v_mfma_f32_16x16x32_bf16 v[24:27],v[216:219],v[60:63],v[24:27]
ds_read_b128 v[52:55],v4 offset:25600
v_mfma_f32_16x16x32_bf16 v[24:27],v[104:107],v[56:59],v[24:27]
s_waitcnt lgkmcnt(4)
v_mfma_f32_16x16x32_bf16 v[24:27],a[80:83],v[32:35],v[24:27]
ds_read_b128 v[56:59],v4 offset:39936
v_mfma_f32_16x16x32_bf16 v[24:27],a[192:195],v[36:39],v[24:27]
buffer_load_dwordx4 v2,s[32:35],s63 offen offset:3072 sc1 lds
s_add_u32 s63,s63,4096
v_mfma_f32_16x16x32_bf16 v[28:31],a[196:199],v[36:39],v[28:31]
ds_read_b128 v[60:63],v4 offset:54272
s_add_u32 m0,s64,12288
v_mfma_f32_16x16x32_bf16 v[28:31],a[84:87],v[32:35],v[28:31]
s_waitcnt lgkmcnt(4)
v_mfma_f32_16x16x32_bf16 v[28:31],v[116:119],v[40:43],v[28:31]
ds_read_b128 v[32:35],v4 offset:12288
v_mfma_f32_16x16x32_bf16 v[28:31],v[228:231],v[44:47],v[28:31]
buffer_load_dwordx4 v2,s[32:35],s63 offen offset:0 sc1 lds
v_mfma_f32_16x16x32_bf16 v[24:27],v[224:227],v[44:47],v[24:27]
ds_read_b128 v[36:39],v4 offset:26624
v_mfma_f32_16x16x32_bf16 v[24:27],v[112:115],v[40:43],v[24:27]
s_waitcnt lgkmcnt(4)
v_mfma_f32_16x16x32_bf16 v[24:27],a[88:91],v[48:51],v[24:27]
ds_read_b128 v[40:43],v4 offset:40960
v_mfma_f32_16x16x32_bf16 v[24:27],a[200:203],v[52:55],v[24:27]
buffer_load_dwordx4 v2,s[32:35],s63 offen offset:1024 sc1 lds
s_add_u32 s63,s63,s31
v_mfma_f32_16x16x32_bf16 v[28:31],a[204:207],v[52:55],v[28:31]
ds_read_b128 v[44:47],v4 offset:55296
v_mfma_f32_16x16x32_bf16 v[28:31],a[92:95],v[48:51],v[28:31]
s_waitcnt lgkmcnt(4)
v_mfma_f32_16x16x32_bf16 v[28:31],v[124:127],v[56:59],v[28:31]
ds_read_b128 v[48:51],v4 offset:13312
v_mfma_f32_16x16x32_bf16 v[28:31],v[236:239],v[60:63],v[28:31]
v_mfma_f32_16x16x32_bf16 v[24:27],v[232:235],v[60:63],v[24:27]
ds_read_b128 v[52:55],v4 offset:27648
v_mfma_f32_16x16x32_bf16 v[24:27],v[120:123],v[56:59],v[24:27]
s_waitcnt lgkmcnt(4)
v_mfma_f32_16x16x32_bf16 v[24:27],a[96:99],v[32:35],v[24:27]
ds_read_b128 v[56:59],v4 offset:41984
v_mfma_f32_16x16x32_bf16 v[24:27],a[208:211],v[36:39],v[24:27]
s_waitcnt vmcnt(10)
s_barrier
v_mfma_f32_16x16x32_bf16 v[28:31],a[212:215],v[36:39],v[28:31]
ds_read_b128 v[60:63],v4 offset:56320
v_mfma_f32_16x16x32_bf16 v[28:31],a[100:103],v[32:35],v[28:31]
s_waitcnt lgkmcnt(4)
v_mfma_f32_16x16x32_bf16 v[28:31],v[132:135],v[40:43],v[28:31]
ds_read_b128 v[32:35],v2 offset:0
v_mfma_f32_16x16x32_bf16 v[28:31],v[244:247],v[44:47],v[28:31]
v_mfma_f32_16x16x32_bf16 v[24:27],v[240:243],v[44:47],v[24:27]
ds_read_b128 v[36:39],v2 offset:14336
v_mfma_f32_16x16x32_bf16 v[24:27],v[128:131],v[40:43],v[24:27]
s_waitcnt lgkmcnt(4)
v_mfma_f32_16x16x32_bf16 v[24:27],a[104:107],v[48:51],v[24:27]
ds_read_b128 v[40:43],v2 offset:28672
v_mfma_f32_16x16x32_bf16 v[24:27],a[216:219],v[52:55],v[24:27]
v_mfma_f32_16x16x32_bf16 v[28:31],a[220:223],v[52:55],v[28:31]
ds_read_b128 v[44:47],v2 offset:43008
v_mfma_f32_16x16x32_bf16 v[28:31],a[108:111],v[48:51],v[28:31]
s_waitcnt lgkmcnt(4)
v_mfma_f32_16x16x32_bf16 v[28:31],v[140:143],v[56:59],v[28:31]
ds_read_b128 v[48:51],v2 offset:1024
v_mfma_f32_16x16x32_bf16 v[28:31],v[252:255],v[60:63],v[28:31]
v_mfma_f32_16x16x32_bf16 v[24:27],v[248:251],v[60:63],v[24:27]
ds_read_b128 v[52:55],v2 offset:15360
s_sub_u32 s62,s62,1
v_mfma_f32_16x16x32_bf16 v[24:27],v[136:139],v[56:59],v[24:27]
s_cbranch_scc1 .L_dbt_2
s_waitcnt lgkmcnt(4)
v_mfma_f32_16x16x32_bf16 v[16:19],a[0:3],v[32:35],0
ds_read_b128 v[56:59],v2 offset:29696
v_mfma_f32_16x16x32_bf16 v[16:19],a[112:115],v[36:39],v[16:19]
v_mfma_f32_16x16x32_bf16 v[20:23],a[116:119],v[36:39],0
ds_read_b128 v[60:63],v2 offset:44032
v_mfma_f32_16x16x32_bf16 v[20:23],a[4:7],v[32:35],v[20:23]
v_cvt_pk_bf16_f32 v15,v30,v31
s_waitcnt lgkmcnt(4)
v_mfma_f32_16x16x32_bf16 v[20:23],a[228:231],v[40:43],v[20:23]
ds_read_b128 v[32:35],v2 offset:2048
v_cvt_pk_bf16_f32 v14,v28,v29
v_mfma_f32_16x16x32_bf16 v[20:23],v[148:151],v[44:47],v[20:23]
v_cvt_pk_bf16_f32 v13,v26,v27
v_cvt_pk_bf16_f32 v12,v24,v25
v_mfma_f32_16x16x32_bf16 v[16:19],v[144:147],v[44:47],v[16:19]
ds_read_b128 v[36:39],v2 offset:16384
ds_write_b128 v5,v[12:15]
v_mfma_f32_16x16x32_bf16 v[16:19],a[224:227],v[40:43],v[16:19]
ds_read2st64_b32 v[12:13],v3 offset0:0 offset1:1
s_waitcnt lgkmcnt(6)
v_mfma_f32_16x16x32_bf16 v[16:19],a[8:11],v[48:51],v[16:19]
ds_read_b128 v[40:43],v2 offset:30720
ds_read2st64_b32 v[14:15],v3 offset0:2 offset1:3
v_mfma_f32_16x16x32_bf16 v[16:19],a[120:123],v[52:55],v[16:19]
v_mfma_f32_16x16x32_bf16 v[20:23],a[124:127],v[52:55],v[20:23]
ds_read_b128 v[44:47],v2 offset:45056
v_mfma_f32_16x16x32_bf16 v[20:23],a[12:15],v[48:51],v[20:23]
s_waitcnt lgkmcnt(7)
v_mfma_f32_16x16x32_bf16 v[20:23],a[236:239],v[56:59],v[20:23]
ds_read_b128 v[48:51],v2 offset:3072
v_mfma_f32_16x16x32_bf16 v[20:23],v[156:159],v[60:63],v[20:23]
s_waitcnt lgkmcnt(2)
buffer_atomic_pk_add_bf16 v12,v1,s[44:47],s65 offen offset:0 sc1
v_mfma_f32_16x16x32_bf16 v[16:19],v[152:155],v[60:63],v[16:19]
ds_read_b128 v[52:55],v2 offset:17408
s_add_u32 s70,s70,s68
v_mfma_f32_16x16x32_bf16 v[16:19],a[232:235],v[56:59],v[16:19]
buffer_atomic_pk_add_bf16 v13,v1,s[44:47],s65 offen offset:2048 sc1
v_mfma_f32_16x16x32_bf16 v[16:19],a[16:19],v[32:35],v[16:19]
ds_read_b128 v[56:59],v2 offset:31744
s_add_u32 s65,s65,16384
v_mfma_f32_16x16x32_bf16 v[16:19],a[128:131],v[36:39],v[16:19]
s_waitcnt vmcnt(2)
s_barrier
v_mfma_f32_16x16x32_bf16 v[20:23],a[132:135],v[36:39],v[20:23]
ds_read_b128 v[60:63],v2 offset:46080
v_mfma_f32_16x16x32_bf16 v[20:23],a[20:23],v[32:35],v[20:23]
buffer_atomic_pk_add_bf16 v14,v1,s[44:47],s65 offen offset:0 sc1
s_waitcnt lgkmcnt(4)
v_mfma_f32_16x16x32_bf16 v[20:23],a[244:247],v[40:43],v[20:23]
ds_read_b128 v[32:35],v2 offset:4096
s_add_u32 m0,s64,57344
v_mfma_f32_16x16x32_bf16 v[20:23],v[164:167],v[44:47],v[20:23]
buffer_load_dwordx4 v2,s[32:35],s63 offen offset:0 sc1 lds
s_mov_b32 s68,s75
v_mfma_f32_16x16x32_bf16 v[16:19],v[160:163],v[44:47],v[16:19]
ds_read_b128 v[36:39],v2 offset:18432
v_mfma_f32_16x16x32_bf16 v[16:19],a[240:243],v[40:43],v[16:19]
buffer_atomic_pk_add_bf16 v15,v1,s[44:47],s65 offen offset:2048 sc1
s_waitcnt lgkmcnt(4)
v_mfma_f32_16x16x32_bf16 v[16:19],a[24:27],v[48:51],v[16:19]
ds_read_b128 v[40:43],v2 offset:32768
s_and_b32 s65,s70,0x3f8000
v_mfma_f32_16x16x32_bf16 v[16:19],a[136:139],v[52:55],v[16:19]
v_mfma_f32_16x16x32_bf16 v[20:23],a[140:143],v[52:55],v[20:23]
ds_read_b128 v[44:47],v2 offset:47104
v_mfma_f32_16x16x32_bf16 v[20:23],a[28:31],v[48:51],v[20:23]
s_waitcnt lgkmcnt(4)
v_mfma_f32_16x16x32_bf16 v[20:23],a[252:255],v[56:59],v[20:23]
ds_read_b128 v[48:51],v2 offset:5120
v_mfma_f32_16x16x32_bf16 v[20:23],v[172:175],v[60:63],v[20:23]
buffer_load_dwordx4 v2,s[32:35],s63 offen offset:1024 sc1 lds
v_mfma_f32_16x16x32_bf16 v[16:19],v[168:171],v[60:63],v[16:19]
ds_read_b128 v[52:55],v2 offset:19456
v_mfma_f32_16x16x32_bf16 v[16:19],a[248:251],v[56:59],v[16:19]
s_waitcnt lgkmcnt(4)
v_mfma_f32_16x16x32_bf16 v[16:19],a[32:35],v[32:35],v[16:19]
ds_read_b128 v[56:59],v2 offset:33792
v_mfma_f32_16x16x32_bf16 v[16:19],a[144:147],v[36:39],v[16:19]
buffer_load_dwordx4 v2,s[32:35],s63 offen offset:2048 sc1 lds
v_mfma_f32_16x16x32_bf16 v[20:23],a[148:151],v[36:39],v[20:23]
ds_read_b128 v[60:63],v2 offset:48128
v_mfma_f32_16x16x32_bf16 v[20:23],a[36:39],v[32:35],v[20:23]
s_waitcnt lgkmcnt(4)
v_mfma_f32_16x16x32_bf16 v[20:23],v[68:71],v[40:43],v[20:23]
ds_read_b128 v[32:35],v2 offset:6144
v_mfma_f32_16x16x32_bf16 v[20:23],v[180:183],v[44:47],v[20:23]
buffer_load_dwordx4 v2,s[32:35],s63 offen offset:3072 sc1 lds
v_mfma_f32_16x16x32_bf16 v[16:19],v[176:179],v[44:47],v[16:19]
ds_read_b128 v[36:39],v2 offset:20480
v_mfma_f32_16x16x32_bf16 v[16:19],v[64:67],v[40:43],v[16:19]
s_waitcnt lgkmcnt(4)
v_mfma_f32_16x16x32_bf16 v[16:19],a[40:43],v[48:51],v[16:19]
ds_read_b128 v[40:43],v2 offset:34816
s_add_u32 s63,s63,4096
v_mfma_f32_16x16x32_bf16 v[16:19],a[152:155],v[52:55],v[16:19]
s_add_u32 m0,s64,61440
v_mfma_f32_16x16x32_bf16 v[20:23],a[156:159],v[52:55],v[20:23]
ds_read_b128 v[44:47],v2 offset:49152
v_mfma_f32_16x16x32_bf16 v[20:23],a[44:47],v[48:51],v[20:23]
s_waitcnt lgkmcnt(4)
v_mfma_f32_16x16x32_bf16 v[20:23],v[76:79],v[56:59],v[20:23]
ds_read_b128 v[48:51],v2 offset:7168
v_mfma_f32_16x16x32_bf16 v[20:23],v[188:191],v[60:63],v[20:23]
buffer_load_dwordx4 v2,s[32:35],s63 offen offset:0 sc1 lds
v_mfma_f32_16x16x32_bf16 v[16:19],v[184:187],v[60:63],v[16:19]
ds_read_b128 v[52:55],v2 offset:21504
v_mfma_f32_16x16x32_bf16 v[16:19],v[72:75],v[56:59],v[16:19]
s_waitcnt lgkmcnt(4)
v_mfma_f32_16x16x32_bf16 v[16:19],a[48:51],v[32:35],v[16:19]
ds_read_b128 v[56:59],v2 offset:35840
v_mfma_f32_16x16x32_bf16 v[16:19],a[160:163],v[36:39],v[16:19]
buffer_load_dwordx4 v2,s[32:35],s63 offen offset:1024 sc1 lds
v_mfma_f32_16x16x32_bf16 v[20:23],a[164:167],v[36:39],v[20:23]
ds_read_b128 v[60:63],v2 offset:50176
v_mfma_f32_16x16x32_bf16 v[20:23],a[52:55],v[32:35],v[20:23]
s_waitcnt lgkmcnt(4)
v_mfma_f32_16x16x32_bf16 v[20:23],v[84:87],v[40:43],v[20:23]
ds_read_b128 v[32:35],v2 offset:8192
v_mfma_f32_16x16x32_bf16 v[20:23],v[196:199],v[44:47],v[20:23]
buffer_load_dwordx4 v2,s[32:35],s63 offen offset:2048 sc1 lds
v_mfma_f32_16x16x32_bf16 v[16:19],v[192:195],v[44:47],v[16:19]
ds_read_b128 v[36:39],v2 offset:22528
v_mfma_f32_16x16x32_bf16 v[16:19],v[80:83],v[40:43],v[16:19]
s_waitcnt lgkmcnt(4)
v_mfma_f32_16x16x32_bf16 v[16:19],a[56:59],v[48:51],v[16:19]
ds_read_b128 v[40:43],v2 offset:36864
v_mfma_f32_16x16x32_bf16 v[16:19],a[168:171],v[52:55],v[16:19]
buffer_load_dwordx4 v2,s[32:35],s63 offen offset:3072 sc1 lds
v_mfma_f32_16x16x32_bf16 v[20:23],a[172:175],v[52:55],v[20:23]
ds_read_b128 v[44:47],v2 offset:51200
v_mfma_f32_16x16x32_bf16 v[20:23],a[60:63],v[48:51],v[20:23]
s_waitcnt lgkmcnt(4)
v_mfma_f32_16x16x32_bf16 v[20:23],v[92:95],v[56:59],v[20:23]
ds_read_b128 v[48:51],v2 offset:9216
v_mfma_f32_16x16x32_bf16 v[20:23],v[204:207],v[60:63],v[20:23]
s_add_u32 s63,s63,4096
s_add_u32 m0,s64,65536
v_mfma_f32_16x16x32_bf16 v[16:19],v[200:203],v[60:63],v[16:19]
ds_read_b128 v[52:55],v2 offset:23552
v_mfma_f32_16x16x32_bf16 v[16:19],v[88:91],v[56:59],v[16:19]
s_waitcnt lgkmcnt(4)
v_mfma_f32_16x16x32_bf16 v[16:19],a[64:67],v[32:35],v[16:19]
ds_read_b128 v[56:59],v2 offset:37888
v_mfma_f32_16x16x32_bf16 v[16:19],a[176:179],v[36:39],v[16:19]
buffer_load_dwordx4 v2,s[32:35],s63 offen offset:0 sc1 lds
v_mfma_f32_16x16x32_bf16 v[20:23],a[180:183],v[36:39],v[20:23]
ds_read_b128 v[60:63],v2 offset:52224
v_mfma_f32_16x16x32_bf16 v[20:23],a[68:71],v[32:35],v[20:23]
s_waitcnt lgkmcnt(4)
v_mfma_f32_16x16x32_bf16 v[20:23],v[100:103],v[40:43],v[20:23]
ds_read_b128 v[32:35],v2 offset:10240
v_mfma_f32_16x16x32_bf16 v[20:23],v[212:215],v[44:47],v[20:23]
buffer_load_dwordx4 v2,s[32:35],s63 offen offset:1024 sc1 lds
v_mfma_f32_16x16x32_bf16 v[16:19],v[208:211],v[44:47],v[16:19]
ds_read_b128 v[36:39],v2 offset:24576
v_mfma_f32_16x16x32_bf16 v[16:19],v[96:99],v[40:43],v[16:19]
s_waitcnt lgkmcnt(4)
v_mfma_f32_16x16x32_bf16 v[16:19],a[72:75],v[48:51],v[16:19]
ds_read_b128 v[40:43],v2 offset:38912
v_mfma_f32_16x16x32_bf16 v[16:19],a[184:187],v[52:55],v[16:19]
buffer_load_dwordx4 v2,s[32:35],s63 offen offset:2048 sc1 lds
v_mfma_f32_16x16x32_bf16 v[20:23],a[188:191],v[52:55],v[20:23]
ds_read_b128 v[44:47],v2 offset:53248
v_mfma_f32_16x16x32_bf16 v[20:23],a[76:79],v[48:51],v[20:23]
s_waitcnt lgkmcnt(4)
v_mfma_f32_16x16x32_bf16 v[20:23],v[108:111],v[56:59],v[20:23]
ds_read_b128 v[48:51],v2 offset:11264
v_mfma_f32_16x16x32_bf16 v[20:23],v[220:223],v[60:63],v[20:23]
v_mfma_f32_16x16x32_bf16 v[16:19],v[216:219],v[60:63],v[16:19]
ds_read_b128 v[52:55],v2 offset:25600
v_mfma_f32_16x16x32_bf16 v[16:19],v[104:107],v[56:59],v[16:19]
s_waitcnt lgkmcnt(4)
v_mfma_f32_16x16x32_bf16 v[16:19],a[80:83],v[32:35],v[16:19]
ds_read_b128 v[56:59],v2 offset:39936
v_mfma_f32_16x16x32_bf16 v[16:19],a[192:195],v[36:39],v[16:19]
buffer_load_dwordx4 v2,s[32:35],s63 offen offset:3072 sc1 lds
s_add_u32 s63,s63,4096
v_mfma_f32_16x16x32_bf16 v[20:23],a[196:199],v[36:39],v[20:23]
ds_read_b128 v[60:63],v2 offset:54272
s_add_u32 m0,s64,69632
v_mfma_f32_16x16x32_bf16 v[20:23],a[84:87],v[32:35],v[20:23]
s_waitcnt lgkmcnt(4)
v_mfma_f32_16x16x32_bf16 v[20:23],v[116:119],v[40:43],v[20:23]
ds_read_b128 v[32:35],v2 offset:12288
v_mfma_f32_16x16x32_bf16 v[20:23],v[228:231],v[44:47],v[20:23]
buffer_load_dwordx4 v2,s[32:35],s63 offen offset:0 sc1 lds
v_mfma_f32_16x16x32_bf16 v[16:19],v[224:227],v[44:47],v[16:19]
ds_read_b128 v[36:39],v2 offset:26624
v_mfma_f32_16x16x32_bf16 v[16:19],v[112:115],v[40:43],v[16:19]
s_waitcnt lgkmcnt(4)
v_mfma_f32_16x16x32_bf16 v[16:19],a[88:91],v[48:51],v[16:19]
ds_read_b128 v[40:43],v2 offset:40960
v_mfma_f32_16x16x32_bf16 v[16:19],a[200:203],v[52:55],v[16:19]
buffer_load_dwordx4 v2,s[32:35],s63 offen offset:1024 sc1 lds
s_add_u32 s63,s63,s31
v_mfma_f32_16x16x32_bf16 v[20:23],a[204:207],v[52:55],v[20:23]
ds_read_b128 v[44:47],v2 offset:55296
v_mfma_f32_16x16x32_bf16 v[20:23],a[92:95],v[48:51],v[20:23]
s_waitcnt lgkmcnt(4)
v_mfma_f32_16x16x32_bf16 v[20:23],v[124:127],v[56:59],v[20:23]
ds_read_b128 v[48:51],v2 offset:13312
v_mfma_f32_16x16x32_bf16 v[20:23],v[236:239],v[60:63],v[20:23]
v_mfma_f32_16x16x32_bf16 v[16:19],v[232:235],v[60:63],v[16:19]
ds_read_b128 v[52:55],v2 offset:27648
v_mfma_f32_16x16x32_bf16 v[16:19],v[120:123],v[56:59],v[16:19]
s_waitcnt lgkmcnt(4)
v_mfma_f32_16x16x32_bf16 v[16:19],a[96:99],v[32:35],v[16:19]
ds_read_b128 v[56:59],v2 offset:41984
v_mfma_f32_16x16x32_bf16 v[16:19],a[208:211],v[36:39],v[16:19]
s_waitcnt vmcnt(10)
s_barrier
v_mfma_f32_16x16x32_bf16 v[20:23],a[212:215],v[36:39],v[20:23]
ds_read_b128 v[60:63],v2 offset:56320
v_mfma_f32_16x16x32_bf16 v[20:23],a[100:103],v[32:35],v[20:23]
s_waitcnt lgkmcnt(4)
v_mfma_f32_16x16x32_bf16 v[20:23],v[132:135],v[40:43],v[20:23]
ds_read_b128 v[32:35],v4 offset:0
v_mfma_f32_16x16x32_bf16 v[20:23],v[244:247],v[44:47],v[20:23]
v_mfma_f32_16x16x32_bf16 v[16:19],v[240:243],v[44:47],v[16:19]
ds_read_b128 v[36:39],v4 offset:14336
v_mfma_f32_16x16x32_bf16 v[16:19],v[128:131],v[40:43],v[16:19]
s_waitcnt lgkmcnt(4)
v_mfma_f32_16x16x32_bf16 v[16:19],a[104:107],v[48:51],v[16:19]
ds_read_b128 v[40:43],v4 offset:28672
v_mfma_f32_16x16x32_bf16 v[16:19],a[216:219],v[52:55],v[16:19]
v_mfma_f32_16x16x32_bf16 v[20:23],a[220:223],v[52:55],v[20:23]
ds_read_b128 v[44:47],v4 offset:43008
v_mfma_f32_16x16x32_bf16 v[20:23],a[108:111],v[48:51],v[20:23]
s_waitcnt lgkmcnt(4)
v_mfma_f32_16x16x32_bf16 v[20:23],v[140:143],v[56:59],v[20:23]
ds_read_b128 v[48:51],v4 offset:1024
v_mfma_f32_16x16x32_bf16 v[20:23],v[252:255],v[60:63],v[20:23]
v_mfma_f32_16x16x32_bf16 v[16:19],v[248:251],v[60:63],v[16:19]
ds_read_b128 v[52:55],v4 offset:15360
s_sub_u32 s62,s62,1
v_mfma_f32_16x16x32_bf16 v[16:19],v[136:139],v[56:59],v[16:19]
s_cbranch_scc0 .L_dbl_2
.L_dbu_2:
s_nop 1
s_nop 0
v_cvt_pk_bf16_f32 v15,v22,v23
v_cvt_pk_bf16_f32 v14,v20,v21
s_nop 1
v_cvt_pk_bf16_f32 v13,v18,v19
v_cvt_pk_bf16_f32 v12,v16,v17
ds_write_b128 v5,v[12:15]
ds_read2st64_b32 v[12:13],v3 offset0:0 offset1:1
ds_read2st64_b32 v[14:15],v3 offset0:2 offset1:3
s_waitcnt lgkmcnt(0)
buffer_atomic_pk_add_bf16 v12,v1,s[44:47],s65 offen offset:0 sc1
s_add_u32 s70,s70,s75
buffer_atomic_pk_add_bf16 v13,v1,s[44:47],s65 offen offset:2048 sc1
s_add_u32 s65,s65,16384
buffer_atomic_pk_add_bf16 v14,v1,s[44:47],s65 offen offset:0 sc1
buffer_atomic_pk_add_bf16 v15,v1,s[44:47],s65 offen offset:2048 sc1
s_and_b32 s65,s70,0x3f8000
s_or_b32 s48,s28,s27
s_or_b32 s48,s48,s26
s_cbranch_scc1 .L_end
v_mov_b32 v8,s54
v_mov_b32 v9,s55
buffer_store_dwordx2 v[8:9],off,s[32:35],0 sc1
s_branch .L_end
.L_dbt_2:
s_nop 1
s_nop 0
v_cvt_pk_bf16_f32 v15,v30,v31
v_cvt_pk_bf16_f32 v14,v28,v29
s_nop 1
v_cvt_pk_bf16_f32 v13,v26,v27
v_cvt_pk_bf16_f32 v12,v24,v25
ds_write_b128 v5,v[12:15]
ds_read2st64_b32 v[12:13],v3 offset0:0 offset1:1
ds_read2st64_b32 v[14:15],v3 offset0:2 offset1:3
s_waitcnt lgkmcnt(0)
buffer_atomic_pk_add_bf16 v12,v1,s[44:47],s65 offen offset:0 sc1
s_add_u32 s70,s70,s68
buffer_atomic_pk_add_bf16 v13,v1,s[44:47],s65 offen offset:2048 sc1
s_add_u32 s65,s65,16384
buffer_atomic_pk_add_bf16 v14,v1,s[44:47],s65 offen offset:0 sc1
s_mov_b32 s68,s75
buffer_atomic_pk_add_bf16 v15,v1,s[44:47],s65 offen offset:2048 sc1
s_and_b32 s65,s70,0x3f8000
s_or_b32 s48,s28,s27
s_or_b32 s48,s48,s26
s_cbranch_scc1 .L_end
v_mov_b32 v8,s54
v_mov_b32 v9,s55
buffer_store_dwordx2 v[8:9],off,s[32:35],0 sc1
s_branch .L_end
.p2align 6
.L_dhv3:
.L_dal_3:
s_waitcnt lgkmcnt(4)
v_mfma_f32_16x16x32_bf16 v[16:19],a[0:3],v[32:35],0
ds_read_b128 v[56:59],v2 offset:29696
v_dot2_f32_bf16 v10,v12,s72,0
)#"
R"#(
v_mfma_f32_16x16x32_bf16 v[16:19],a[112:115],v[36:39],v[16:19]
v_dot2_f32_bf16 v11,v12,v12,0
v_dot2_f32_bf16 v10,v13,s72,v10
v_mfma_f32_16x16x32_bf16 v[20:23],a[116:119],v[36:39],0
ds_read_b128 v[60:63],v2 offset:44032
v_dot2_f32_bf16 v11,v13,v13,v11
v_mfma_f32_16x16x32_bf16 v[20:23],a[4:7],v[32:35],v[20:23]
v_dot2_f32_bf16 v10,v14,s72,v10
s_waitcnt lgkmcnt(4)
v_mfma_f32_16x16x32_bf16 v[20:23],a[228:231],v[40:43],v[20:23]
ds_read_b128 v[32:35],v2 offset:2048
v_dot2_f32_bf16 v11,v14,v14,v11
v_mfma_f32_16x16x32_bf16 v[20:23],v[148:151],v[44:47],v[20:23]
v_dot2_f32_bf16 v10,v15,s72,v10
v_dot2_f32_bf16 v11,v15,v15,v11
v_mfma_f32_16x16x32_bf16 v[16:19],v[144:147],v[44:47],v[16:19]
ds_read_b128 v[36:39],v2 offset:16384
v_mfma_f32_16x16x32_bf16 v[16:19],a[224:227],v[40:43],v[16:19]
v_permlane16_swap_b32 v10,v11
s_waitcnt lgkmcnt(4)
v_mfma_f32_16x16x32_bf16 v[16:19],a[8:11],v[48:51],v[16:19]
ds_read_b128 v[40:43],v2 offset:30720
v_add_f32 v10,v10,v11
v_mfma_f32_16x16x32_bf16 v[16:19],a[120:123],v[52:55],v[16:19]
v_mfma_f32_16x16x32_bf16 v[20:23],a[124:127],v[52:55],v[20:23]
ds_read_b128 v[44:47],v2 offset:45056
v_permlane32_swap_b32 v10,v11
v_mfma_f32_16x16x32_bf16 v[20:23],a[12:15],v[48:51],v[20:23]
v_add_f32 v10,v10,v11
s_waitcnt lgkmcnt(4)
v_mfma_f32_16x16x32_bf16 v[20:23],a[236:239],v[56:59],v[20:23]
ds_read_b128 v[48:51],v2 offset:3072
v_mfma_f32_16x16x32_bf16 v[20:23],v[156:159],v[60:63],v[20:23]
buffer_store_dword v10,v3,s[32:35],s67 offen offset:1024 sc1
v_mfma_f32_16x16x32_bf16 v[16:19],v[152:155],v[60:63],v[16:19]
ds_read_b128 v[52:55],v2 offset:17408
v_mfma_f32_16x16x32_bf16 v[16:19],a[232:235],v[56:59],v[16:19]
s_waitcnt lgkmcnt(4)
v_mfma_f32_16x16x32_bf16 v[16:19],a[16:19],v[32:35],v[16:19]
s_waitcnt vmcnt(2)
s_barrier
v_mfma_f32_16x16x32_bf16 v[16:19],a[128:131],v[36:39],v[16:19]
ds_read_b128 v[56:59],v2 offset:31744
v_mfma_f32_16x16x32_bf16 v[20:23],a[132:135],v[36:39],v[20:23]
ds_read_b128 v[60:63],v2 offset:46080
v_mfma_f32_16x16x32_bf16 v[20:23],a[20:23],v[32:35],v[20:23]
s_waitcnt lgkmcnt(4)
v_mfma_f32_16x16x32_bf16 v[20:23],a[244:247],v[40:43],v[20:23]
ds_read_b128 v[32:35],v2 offset:4096
s_mov_b32 m0,s74
v_mfma_f32_16x16x32_bf16 v[20:23],v[164:167],v[44:47],v[20:23]
buffer_load_dwordx4 v2,s[32:35],s71 offen offset:0 sc1 lds
v_mfma_f32_16x16x32_bf16 v[16:19],v[160:163],v[44:47],v[16:19]
ds_read_b128 v[36:39],v2 offset:18432
v_mfma_f32_16x16x32_bf16 v[16:19],a[240:243],v[40:43],v[16:19]
s_waitcnt lgkmcnt(4)
v_mfma_f32_16x16x32_bf16 v[16:19],a[24:27],v[48:51],v[16:19]
ds_read_b128 v[40:43],v2 offset:32768
v_mfma_f32_16x16x32_bf16 v[16:19],a[136:139],v[52:55],v[16:19]
buffer_load_dwordx4 v2,s[32:35],s71 offen offset:1024 sc1 lds
s_add_u32 s71,s71,s30
v_mfma_f32_16x16x32_bf16 v[20:23],a[140:143],v[52:55],v[20:23]
ds_read_b128 v[44:47],v2 offset:47104
s_add_u32 m0,s64,57344
v_mfma_f32_16x16x32_bf16 v[20:23],a[28:31],v[48:51],v[20:23]
s_waitcnt lgkmcnt(4)
v_mfma_f32_16x16x32_bf16 v[20:23],a[252:255],v[56:59],v[20:23]
ds_read_b128 v[48:51],v2 offset:5120
v_mfma_f32_16x16x32_bf16 v[20:23],v[172:175],v[60:63],v[20:23]
buffer_load_dwordx4 v2,s[32:35],s63 offen offset:0 sc1 lds
v_mfma_f32_16x16x32_bf16 v[16:19],v[168:171],v[60:63],v[16:19]
ds_read_b128 v[52:55],v2 offset:19456
v_mfma_f32_16x16x32_bf16 v[16:19],a[248:251],v[56:59],v[16:19]
s_waitcnt lgkmcnt(4)
v_mfma_f32_16x16x32_bf16 v[16:19],a[32:35],v[32:35],v[16:19]
ds_read_b128 v[56:59],v2 offset:33792
v_mfma_f32_16x16x32_bf16 v[16:19],a[144:147],v[36:39],v[16:19]
buffer_load_dwordx4 v2,s[32:35],s63 offen offset:1024 sc1 lds
v_mfma_f32_16x16x32_bf16 v[20:23],a[148:151],v[36:39],v[20:23]
ds_read_b128 v[60:63],v2 offset:48128
v_mfma_f32_16x16x32_bf16 v[20:23],a[36:39],v[32:35],v[20:23]
s_waitcnt lgkmcnt(4)
v_mfma_f32_16x16x32_bf16 v[20:23],v[68:71],v[40:43],v[20:23]
ds_read_b128 v[32:35],v2 offset:6144
v_mfma_f32_16x16x32_bf16 v[20:23],v[180:183],v[44:47],v[20:23]
buffer_load_dwordx4 v2,s[32:35],s63 offen offset:2048 sc1 lds
v_mfma_f32_16x16x32_bf16 v[16:19],v[176:179],v[44:47],v[16:19]
ds_read_b128 v[36:39],v2 offset:20480
v_mfma_f32_16x16x32_bf16 v[16:19],v[64:67],v[40:43],v[16:19]
s_waitcnt lgkmcnt(4)
v_mfma_f32_16x16x32_bf16 v[16:19],a[40:43],v[48:51],v[16:19]
ds_read_b128 v[40:43],v2 offset:34816
v_mfma_f32_16x16x32_bf16 v[16:19],a[152:155],v[52:55],v[16:19]
buffer_load_dwordx4 v2,s[32:35],s63 offen offset:3072 sc1 lds
s_add_u32 s63,s63,4096
v_mfma_f32_16x16x32_bf16 v[20:23],a[156:159],v[52:55],v[20:23]
ds_read_b128 v[44:47],v2 offset:49152
s_add_u32 m0,s64,61440
v_mfma_f32_16x16x32_bf16 v[20:23],a[44:47],v[48:51],v[20:23]
s_waitcnt lgkmcnt(4)
v_mfma_f32_16x16x32_bf16 v[20:23],v[76:79],v[56:59],v[20:23]
ds_read_b128 v[48:51],v2 offset:7168
v_mfma_f32_16x16x32_bf16 v[20:23],v[188:191],v[60:63],v[20:23]
buffer_load_dwordx4 v2,s[32:35],s63 offen offset:0 sc1 lds
v_mfma_f32_16x16x32_bf16 v[16:19],v[184:187],v[60:63],v[16:19]
ds_read_b128 v[52:55],v2 offset:21504
v_mfma_f32_16x16x32_bf16 v[16:19],v[72:75],v[56:59],v[16:19]
s_waitcnt lgkmcnt(4)
v_mfma_f32_16x16x32_bf16 v[16:19],a[48:51],v[32:35],v[16:19]
ds_read_b128 v[56:59],v2 offset:35840
v_mfma_f32_16x16x32_bf16 v[16:19],a[160:163],v[36:39],v[16:19]
buffer_load_dwordx4 v2,s[32:35],s63 offen offset:1024 sc1 lds
v_mfma_f32_16x16x32_bf16 v[20:23],a[164:167],v[36:39],v[20:23]
ds_read_b128 v[60:63],v2 offset:50176
v_mfma_f32_16x16x32_bf16 v[20:23],a[52:55],v[32:35],v[20:23]
s_waitcnt lgkmcnt(4)
v_mfma_f32_16x16x32_bf16 v[20:23],v[84:87],v[40:43],v[20:23]
ds_read_b128 v[32:35],v2 offset:8192
v_mfma_f32_16x16x32_bf16 v[20:23],v[196:199],v[44:47],v[20:23]
buffer_load_dwordx4 v2,s[32:35],s63 offen offset:2048 sc1 lds
v_mfma_f32_16x16x32_bf16 v[16:19],v[192:195],v[44:47],v[16:19]
ds_read_b128 v[36:39],v2 offset:22528
v_mfma_f32_16x16x32_bf16 v[16:19],v[80:83],v[40:43],v[16:19]
s_waitcnt lgkmcnt(4)
v_mfma_f32_16x16x32_bf16 v[16:19],a[56:59],v[48:51],v[16:19]
ds_read_b128 v[40:43],v2 offset:36864
v_mfma_f32_16x16x32_bf16 v[16:19],a[168:171],v[52:55],v[16:19]
buffer_load_dwordx4 v2,s[32:35],s63 offen offset:3072 sc1 lds
v_mfma_f32_16x16x32_bf16 v[20:23],a[172:175],v[52:55],v[20:23]
ds_read_b128 v[44:47],v2 offset:51200
v_mfma_f32_16x16x32_bf16 v[20:23],a[60:63],v[48:51],v[20:23]
s_waitcnt lgkmcnt(4)
v_mfma_f32_16x16x32_bf16 v[20:23],v[92:95],v[56:59],v[20:23]
ds_read_b128 v[48:51],v2 offset:9216
v_mfma_f32_16x16x32_bf16 v[20:23],v[204:207],v[60:63],v[20:23]
s_add_u32 s63,s63,4096
s_add_u32 m0,s64,65536
v_mfma_f32_16x16x32_bf16 v[16:19],v[200:203],v[60:63],v[16:19]
ds_read_b128 v[52:55],v2 offset:23552
v_mfma_f32_16x16x32_bf16 v[16:19],v[88:91],v[56:59],v[16:19]
s_waitcnt lgkmcnt(4)
v_mfma_f32_16x16x32_bf16 v[16:19],a[64:67],v[32:35],v[16:19]
ds_read_b128 v[56:59],v2 offset:37888
v_mfma_f32_16x16x32_bf16 v[16:19],a[176:179],v[36:39],v[16:19]
buffer_load_dwordx4 v2,s[32:35],s63 offen offset:0 sc1 lds
v_mfma_f32_16x16x32_bf16 v[20:23],a[180:183],v[36:39],v[20:23]
ds_read_b128 v[60:63],v2 offset:52224
v_mfma_f32_16x16x32_bf16 v[20:23],a[68:71],v[32:35],v[20:23]
s_waitcnt lgkmcnt(4)
v_mfma_f32_16x16x32_bf16 v[20:23],v[100:103],v[40:43],v[20:23]
ds_read_b128 v[32:35],v2 offset:10240
v_mfma_f32_16x16x32_bf16 v[20:23],v[212:215],v[44:47],v[20:23]
buffer_load_dwordx4 v2,s[32:35],s63 offen offset:1024 sc1 lds
v_mfma_f32_16x16x32_bf16 v[16:19],v[208:211],v[44:47],v[16:19]
ds_read_b128 v[36:39],v2 offset:24576
v_mfma_f32_16x16x32_bf16 v[16:19],v[96:99],v[40:43],v[16:19]
s_waitcnt lgkmcnt(4)
v_mfma_f32_16x16x32_bf16 v[16:19],a[72:75],v[48:51],v[16:19]
ds_read_b128 v[40:43],v2 offset:38912
s_waitcnt vmcnt(10)
v_mfma_f32_16x16x32_bf16 v[16:19],a[184:187],v[52:55],v[16:19]
buffer_load_dwordx4 v2,s[32:35],s63 offen offset:2048 sc1 lds
ds_read_b128 v[12:15],v5 offset:0
v_mfma_f32_16x16x32_bf16 v[20:23],a[188:191],v[52:55],v[20:23]
ds_read_b128 v[44:47],v2 offset:53248
v_mfma_f32_16x16x32_bf16 v[20:23],a[76:79],v[48:51],v[20:23]
s_waitcnt lgkmcnt(5)
v_mfma_f32_16x16x32_bf16 v[20:23],v[108:111],v[56:59],v[20:23]
ds_read_b128 v[48:51],v2 offset:11264
v_mfma_f32_16x16x32_bf16 v[20:23],v[220:223],v[60:63],v[20:23]
v_mfma_f32_16x16x32_bf16 v[16:19],v[216:219],v[60:63],v[16:19]
ds_read_b128 v[52:55],v2 offset:25600
s_waitcnt lgkmcnt(3)
v_mfma_f32_16x16x32_bf16 v[16:19],v[104:107],v[56:59],v[16:19]
v_add_f32 v24,v24,v12
v_mfma_f32_16x16x32_bf16 v[16:19],a[80:83],v[32:35],v[16:19]
ds_read_b128 v[56:59],v2 offset:39936
v_add_f32 v25,v25,v13
v_mfma_f32_16x16x32_bf16 v[16:19],a[192:195],v[36:39],v[16:19]
buffer_load_dwordx4 v2,s[32:35],s63 offen offset:3072 sc1 lds
s_add_u32 s63,s63,4096
v_mfma_f32_16x16x32_bf16 v[20:23],a[196:199],v[36:39],v[20:23]
ds_read_b128 v[60:63],v2 offset:54272
s_add_u32 m0,s64,69632
v_mfma_f32_16x16x32_bf16 v[20:23],a[84:87],v[32:35],v[20:23]
v_add_f32 v26,v26,v14
s_waitcnt lgkmcnt(4)
v_mfma_f32_16x16x32_bf16 v[20:23],v[116:119],v[40:43],v[20:23]
ds_read_b128 v[32:35],v2 offset:12288
v_add_f32 v27,v27,v15
v_mfma_f32_16x16x32_bf16 v[20:23],v[228:231],v[44:47],v[20:23]
buffer_load_dwordx4 v2,s[32:35],s63 offen offset:0 sc1 lds
ds_read_b128 v[12:15],v5 offset:1024
v_mfma_f32_16x16x32_bf16 v[16:19],v[224:227],v[44:47],v[16:19]
ds_read_b128 v[36:39],v2 offset:26624
v_mfma_f32_16x16x32_bf16 v[16:19],v[112:115],v[40:43],v[16:19]
s_waitcnt lgkmcnt(5)
v_mfma_f32_16x16x32_bf16 v[16:19],a[88:91],v[48:51],v[16:19]
ds_read_b128 v[40:43],v2 offset:40960
v_mfma_f32_16x16x32_bf16 v[16:19],a[200:203],v[52:55],v[16:19]
buffer_load_dwordx4 v2,s[32:35],s63 offen offset:1024 sc1 lds
s_add_u32 s63,s63,s31
v_mfma_f32_16x16x32_bf16 v[20:23],a[204:207],v[52:55],v[20:23]
ds_read_b128 v[44:47],v2 offset:55296
s_waitcnt lgkmcnt(3)
v_mfma_f32_16x16x32_bf16 v[20:23],a[92:95],v[48:51],v[20:23]
v_add_f32 v28,v28,v12
v_mfma_f32_16x16x32_bf16 v[20:23],v[124:127],v[56:59],v[20:23]
ds_read_b128 v[48:51],v2 offset:13312
v_add_f32 v29,v29,v13
v_mfma_f32_16x16x32_bf16 v[20:23],v[236:239],v[60:63],v[20:23]
v_add_f32 v30,v30,v14
v_add_f32 v31,v31,v15
v_mfma_f32_16x16x32_bf16 v[16:19],v[232:235],v[60:63],v[16:19]
ds_read_b128 v[52:55],v2 offset:27648
v_cvt_pk_bf16_f32 v12,v24,v25
v_mfma_f32_16x16x32_bf16 v[16:19],v[120:123],v[56:59],v[16:19]
v_cvt_pk_bf16_f32 v13,v26,v27
s_waitcnt lgkmcnt(4)
v_mfma_f32_16x16x32_bf16 v[16:19],a[96:99],v[32:35],v[16:19]
s_waitcnt vmcnt(10)
s_barrier
v_mfma_f32_16x16x32_bf16 v[16:19],a[208:211],v[36:39],v[16:19]
ds_read_b128 v[56:59],v2 offset:41984
v_cvt_pk_bf16_f32 v14,v28,v29
v_mfma_f32_16x16x32_bf16 v[20:23],a[212:215],v[36:39],v[20:23]
ds_read_b128 v[60:63],v2 offset:56320
v_cvt_pk_bf16_f32 v15,v30,v31
v_mfma_f32_16x16x32_bf16 v[20:23],a[100:103],v[32:35],v[20:23]
s_waitcnt lgkmcnt(4)
v_mfma_f32_16x16x32_bf16 v[20:23],v[132:135],v[40:43],v[20:23]
ds_read_b128 v[32:35],v4 offset:0
v_mfma_f32_16x16x32_bf16 v[20:23],v[244:247],v[44:47],v[20:23]
buffer_store_dwordx4 v[12:15],v2,s[32:35],s68 offen sc1
v_mfma_f32_16x16x32_bf16 v[16:19],v[240:243],v[44:47],v[16:19]
ds_read_b128 v[36:39],v4 offset:14336
v_mfma_f32_16x16x32_bf16 v[16:19],v[128:131],v[40:43],v[16:19]
s_waitcnt lgkmcnt(4)
v_mfma_f32_16x16x32_bf16 v[16:19],a[104:107],v[48:51],v[16:19]
ds_read_b128 v[40:43],v4 offset:28672
v_mfma_f32_16x16x32_bf16 v[16:19],a[216:219],v[52:55],v[16:19]
v_mfma_f32_16x16x32_bf16 v[20:23],a[220:223],v[52:55],v[20:23]
ds_read_b128 v[44:47],v4 offset:43008
s_mov_b32 s67,s68
v_mfma_f32_16x16x32_bf16 v[20:23],a[108:111],v[48:51],v[20:23]
s_mov_b32 s68,s70
s_waitcnt lgkmcnt(4)
v_mfma_f32_16x16x32_bf16 v[20:23],v[140:143],v[56:59],v[20:23]
ds_read_b128 v[48:51],v4 offset:1024
s_add_u32 s70,s70,1187840
v_mfma_f32_16x16x32_bf16 v[20:23],v[252:255],v[60:63],v[20:23]
v_mfma_f32_16x16x32_bf16 v[16:19],v[248:251],v[60:63],v[16:19]
ds_read_b128 v[52:55],v4 offset:15360
s_sub_u32 s62,s62,1
v_mfma_f32_16x16x32_bf16 v[16:19],v[136:139],v[56:59],v[16:19]
s_cbranch_scc1 .L_dab_3
s_waitcnt lgkmcnt(4)
v_mfma_f32_16x16x32_bf16 v[24:27],a[0:3],v[32:35],0
ds_read_b128 v[56:59],v4 offset:29696
v_dot2_f32_bf16 v10,v12,s72,0
v_mfma_f32_16x16x32_bf16 v[24:27],a[112:115],v[36:39],v[24:27]
v_dot2_f32_bf16 v11,v12,v12,0
v_dot2_f32_bf16 v10,v13,s72,v10
v_mfma_f32_16x16x32_bf16 v[28:31],a[116:119],v[36:39],0
ds_read_b128 v[60:63],v4 offset:44032
v_dot2_f32_bf16 v11,v13,v13,v11
v_mfma_f32_16x16x32_bf16 v[28:31],a[4:7],v[32:35],v[28:31]
v_dot2_f32_bf16 v10,v14,s72,v10
s_waitcnt lgkmcnt(4)
v_mfma_f32_16x16x32_bf16 v[28:31],a[228:231],v[40:43],v[28:31]
ds_read_b128 v[32:35],v4 offset:2048
v_dot2_f32_bf16 v11,v14,v14,v11
v_mfma_f32_16x16x32_bf16 v[28:31],v[148:151],v[44:47],v[28:31]
v_dot2_f32_bf16 v10,v15,s72,v10
v_dot2_f32_bf16 v11,v15,v15,v11
v_mfma_f32_16x16x32_bf16 v[24:27],v[144:147],v[44:47],v[24:27]
ds_read_b128 v[36:39],v4 offset:16384
v_mfma_f32_16x16x32_bf16 v[24:27],a[224:227],v[40:43],v[24:27]
v_permlane16_swap_b32 v10,v11
s_waitcnt lgkmcnt(4)
v_mfma_f32_16x16x32_bf16 v[24:27],a[8:11],v[48:51],v[24:27]
ds_read_b128 v[40:43],v4 offset:30720
v_add_f32 v10,v10,v11
v_mfma_f32_16x16x32_bf16 v[24:27],a[120:123],v[52:55],v[24:27]
v_mfma_f32_16x16x32_bf16 v[28:31],a[124:127],v[52:55],v[28:31]
ds_read_b128 v[44:47],v4 offset:45056
v_permlane32_swap_b32 v10,v11
v_mfma_f32_16x16x32_bf16 v[28:31],a[12:15],v[48:51],v[28:31]
v_add_f32 v10,v10,v11
s_waitcnt lgkmcnt(4)
v_mfma_f32_16x16x32_bf16 v[28:31],a[236:239],v[56:59],v[28:31]
ds_read_b128 v[48:51],v4 offset:3072
v_mfma_f32_16x16x32_bf16 v[28:31],v[156:159],v[60:63],v[28:31]
buffer_store_dword v10,v3,s[32:35],s67 offen offset:1024 sc1
v_mfma_f32_16x16x32_bf16 v[24:27],v[152:155],v[60:63],v[24:27]
ds_read_b128 v[52:55],v4 offset:17408
v_mfma_f32_16x16x32_bf16 v[24:27],a[232:235],v[56:59],v[24:27]
s_waitcnt lgkmcnt(4)
v_mfma_f32_16x16x32_bf16 v[24:27],a[16:19],v[32:35],v[24:27]
s_waitcnt vmcnt(2)
s_barrier
v_mfma_f32_16x16x32_bf16 v[24:27],a[128:131],v[36:39],v[24:27]
ds_read_b128 v[56:59],v4 offset:31744
v_mfma_f32_16x16x32_bf16 v[28:31],a[132:135],v[36:39],v[28:31]
ds_read_b128 v[60:63],v4 offset:46080
v_mfma_f32_16x16x32_bf16 v[28:31],a[20:23],v[32:35],v[28:31]
s_waitcnt lgkmcnt(4)
v_mfma_f32_16x16x32_bf16 v[28:31],a[244:247],v[40:43],v[28:31]
ds_read_b128 v[32:35],v4 offset:4096
s_mov_b32 m0,s74
v_mfma_f32_16x16x32_bf16 v[28:31],v[164:167],v[44:47],v[28:31]
buffer_load_dwordx4 v2,s[32:35],s71 offen offset:0 sc1 lds
v_mfma_f32_16x16x32_bf16 v[24:27],v[160:163],v[44:47],v[24:27]
ds_read_b128 v[36:39],v4 offset:18432
v_mfma_f32_16x16x32_bf16 v[24:27],a[240:243],v[40:43],v[24:27]
s_waitcnt lgkmcnt(4)
v_mfma_f32_16x16x32_bf16 v[24:27],a[24:27],v[48:51],v[24:27]
ds_read_b128 v[40:43],v4 offset:32768
v_mfma_f32_16x16x32_bf16 v[24:27],a[136:139],v[52:55],v[24:27]
buffer_load_dwordx4 v2,s[32:35],s71 offen offset:1024 sc1 lds
s_add_u32 s71,s71,s30
v_mfma_f32_16x16x32_bf16 v[28:31],a[140:143],v[52:55],v[28:31]
ds_read_b128 v[44:47],v4 offset:47104
s_add_u32 m0,s64,0
v_mfma_f32_16x16x32_bf16 v[28:31],a[28:31],v[48:51],v[28:31]
s_waitcnt lgkmcnt(4)
v_mfma_f32_16x16x32_bf16 v[28:31],a[252:255],v[56:59],v[28:31]
ds_read_b128 v[48:51],v4 offset:5120
v_mfma_f32_16x16x32_bf16 v[28:31],v[172:175],v[60:63],v[28:31]
buffer_load_dwordx4 v2,s[32:35],s63 offen offset:0 sc1 lds
v_mfma_f32_16x16x32_bf16 v[24:27],v[168:171],v[60:63],v[24:27]
ds_read_b128 v[52:55],v4 offset:19456
v_mfma_f32_16x16x32_bf16 v[24:27],a[248:251],v[56:59],v[24:27]
s_waitcnt lgkmcnt(4)
v_mfma_f32_16x16x32_bf16 v[24:27],a[32:35],v[32:35],v[24:27]
ds_read_b128 v[56:59],v4 offset:33792
v_mfma_f32_16x16x32_bf16 v[24:27],a[144:147],v[36:39],v[24:27]
buffer_load_dwordx4 v2,s[32:35],s63 offen offset:1024 sc1 lds
v_mfma_f32_16x16x32_bf16 v[28:31],a[148:151],v[36:39],v[28:31]
ds_read_b128 v[60:63],v4 offset:48128
v_mfma_f32_16x16x32_bf16 v[28:31],a[36:39],v[32:35],v[28:31]
s_waitcnt lgkmcnt(4)
v_mfma_f32_16x16x32_bf16 v[28:31],v[68:71],v[40:43],v[28:31]
ds_read_b128 v[32:35],v4 offset:6144
v_mfma_f32_16x16x32_bf16 v[28:31],v[180:183],v[44:47],v[28:31]
buffer_load_dwordx4 v2,s[32:35],s63 offen offset:2048 sc1 lds
v_mfma_f32_16x16x32_bf16 v[24:27],v[176:179],v[44:47],v[24:27]
ds_read_b128 v[36:39],v4 offset:20480
v_mfma_f32_16x16x32_bf16 v[24:27],v[64:67],v[40:43],v[24:27]
s_waitcnt lgkmcnt(4)
v_mfma_f32_16x16x32_bf16 v[24:27],a[40:43],v[48:51],v[24:27]
ds_read_b128 v[40:43],v4 offset:34816
v_mfma_f32_16x16x32_bf16 v[24:27],a[152:155],v[52:55],v[24:27]
buffer_load_dwordx4 v2,s[32:35],s63 offen offset:3072 sc1 lds
s_add_u32 s63,s63,4096
v_mfma_f32_16x16x32_bf16 v[28:31],a[156:159],v[52:55],v[28:31]
ds_read_b128 v[44:47],v4 offset:49152
s_add_u32 m0,s64,4096
v_mfma_f32_16x16x32_bf16 v[28:31],a[44:47],v[48:51],v[28:31]
s_waitcnt lgkmcnt(4)
v_mfma_f32_16x16x32_bf16 v[28:31],v[76:79],v[56:59],v[28:31]
ds_read_b128 v[48:51],v4 offset:7168
v_mfma_f32_16x16x32_bf16 v[28:31],v[188:191],v[60:63],v[28:31]
buffer_load_dwordx4 v2,s[32:35],s63 offen offset:0 sc1 lds
v_mfma_f32_16x16x32_bf16 v[24:27],v[184:187],v[60:63],v[24:27]
ds_read_b128 v[52:55],v4 offset:21504
v_mfma_f32_16x16x32_bf16 v[24:27],v[72:75],v[56:59],v[24:27]
s_waitcnt lgkmcnt(4)
v_mfma_f32_16x16x32_bf16 v[24:27],a[48:51],v[32:35],v[24:27]
ds_read_b128 v[56:59],v4 offset:35840
v_mfma_f32_16x16x32_bf16 v[24:27],a[160:163],v[36:39],v[24:27]
buffer_load_dwordx4 v2,s[32:35],s63 offen offset:1024 sc1 lds
v_mfma_f32_16x16x32_bf16 v[28:31],a[164:167],v[36:39],v[28:31]
ds_read_b128 v[60:63],v4 offset:50176
v_mfma_f32_16x16x32_bf16 v[28:31],a[52:55],v[32:35],v[28:31]
s_waitcnt lgkmcnt(4)
v_mfma_f32_16x16x32_bf16 v[28:31],v[84:87],v[40:43],v[28:31]
ds_read_b128 v[32:35],v4 offset:8192
v_mfma_f32_16x16x32_bf16 v[28:31],v[196:199],v[44:47],v[28:31]
buffer_load_dwordx4 v2,s[32:35],s63 offen offset:2048 sc1 lds
v_mfma_f32_16x16x32_bf16 v[24:27],v[192:195],v[44:47],v[24:27]
ds_read_b128 v[36:39],v4 offset:22528
v_mfma_f32_16x16x32_bf16 v[24:27],v[80:83],v[40:43],v[24:27]
s_waitcnt lgkmcnt(4)
v_mfma_f32_16x16x32_bf16 v[24:27],a[56:59],v[48:51],v[24:27]
ds_read_b128 v[40:43],v4 offset:36864
v_mfma_f32_16x16x32_bf16 v[24:27],a[168:171],v[52:55],v[24:27]
buffer_load_dwordx4 v2,s[32:35],s63 offen offset:3072 sc1 lds
v_mfma_f32_16x16x32_bf16 v[28:31],a[172:175],v[52:55],v[28:31]
ds_read_b128 v[44:47],v4 offset:51200
v_mfma_f32_16x16x32_bf16 v[28:31],a[60:63],v[48:51],v[28:31]
s_waitcnt lgkmcnt(4)
v_mfma_f32_16x16x32_bf16 v[28:31],v[92:95],v[56:59],v[28:31]
ds_read_b128 v[48:51],v4 offset:9216
v_mfma_f32_16x16x32_bf16 v[28:31],v[204:207],v[60:63],v[28:31]
s_add_u32 s63,s63,4096
s_add_u32 m0,s64,8192
v_mfma_f32_16x16x32_bf16 v[24:27],v[200:203],v[60:63],v[24:27]
ds_read_b128 v[52:55],v4 offset:23552
v_mfma_f32_16x16x32_bf16 v[24:27],v[88:91],v[56:59],v[24:27]
s_waitcnt lgkmcnt(4)
v_mfma_f32_16x16x32_bf16 v[24:27],a[64:67],v[32:35],v[24:27]
ds_read_b128 v[56:59],v4 offset:37888
v_mfma_f32_16x16x32_bf16 v[24:27],a[176:179],v[36:39],v[24:27]
buffer_load_dwordx4 v2,s[32:35],s63 offen offset:0 sc1 lds
v_mfma_f32_16x16x32_bf16 v[28:31],a[180:183],v[36:39],v[28:31]
ds_read_b128 v[60:63],v4 offset:52224
v_mfma_f32_16x16x32_bf16 v[28:31],a[68:71],v[32:35],v[28:31]
s_waitcnt lgkmcnt(4)
v_mfma_f32_16x16x32_bf16 v[28:31],v[100:103],v[40:43],v[28:31]
ds_read_b128 v[32:35],v4 offset:10240
v_mfma_f32_16x16x32_bf16 v[28:31],v[212:215],v[44:47],v[28:31]
buffer_load_dwordx4 v2,s[32:35],s63 offen offset:1024 sc1 lds
v_mfma_f32_16x16x32_bf16 v[24:27],v[208:211],v[44:47],v[24:27]
ds_read_b128 v[36:39],v4 offset:24576
v_mfma_f32_16x16x32_bf16 v[24:27],v[96:99],v[40:43],v[24:27]
s_waitcnt lgkmcnt(4)
v_mfma_f32_16x16x32_bf16 v[24:27],a[72:75],v[48:51],v[24:27]
ds_read_b128 v[40:43],v4 offset:38912
s_waitcnt vmcnt(10)
v_mfma_f32_16x16x32_bf16 v[24:27],a[184:187],v[52:55],v[24:27]
buffer_load_dwordx4 v2,s[32:35],s63 offen offset:2048 sc1 lds
ds_read_b128 v[12:15],v5 offset:0
v_mfma_f32_16x16x32_bf16 v[28:31],a[188:191],v[52:55],v[28:31]
ds_read_b128 v[44:47],v4 offset:53248
v_mfma_f32_16x16x32_bf16 v[28:31],a[76:79],v[48:51],v[28:31]
s_waitcnt lgkmcnt(5)
v_mfma_f32_16x16x32_bf16 v[28:31],v[108:111],v[56:59],v[28:31]
ds_read_b128 v[48:51],v4 offset:11264
v_mfma_f32_16x16x32_bf16 v[28:31],v[220:223],v[60:63],v[28:31]
v_mfma_f32_16x16x32_bf16 v[24:27],v[216:219],v[60:63],v[24:27]
ds_read_b128 v[52:55],v4 offset:25600
s_waitcnt lgkmcnt(3)
v_mfma_f32_16x16x32_bf16 v[24:27],v[104:107],v[56:59],v[24:27]
v_add_f32 v16,v16,v12
v_mfma_f32_16x16x32_bf16 v[24:27],a[80:83],v[32:35],v[24:27]
ds_read_b128 v[56:59],v4 offset:39936
v_add_f32 v17,v17,v13
v_mfma_f32_16x16x32_bf16 v[24:27],a[192:195],v[36:39],v[24:27]
buffer_load_dwordx4 v2,s[32:35],s63 offen offset:3072 sc1 lds
s_add_u32 s63,s63,4096
v_mfma_f32_16x16x32_bf16 v[28:31],a[196:199],v[36:39],v[28:31]
ds_read_b128 v[60:63],v4 offset:54272
s_add_u32 m0,s64,12288
v_mfma_f32_16x16x32_bf16 v[28:31],a[84:87],v[32:35],v[28:31]
v_add_f32 v18,v18,v14
s_waitcnt lgkmcnt(4)
v_mfma_f32_16x16x32_bf16 v[28:31],v[116:119],v[40:43],v[28:31]
ds_read_b128 v[32:35],v4 offset:12288
v_add_f32 v19,v19,v15
v_mfma_f32_16x16x32_bf16 v[28:31],v[228:231],v[44:47],v[28:31]
buffer_load_dwordx4 v2,s[32:35],s63 offen offset:0 sc1 lds
ds_read_b128 v[12:15],v5 offset:1024
v_mfma_f32_16x16x32_bf16 v[24:27],v[224:227],v[44:47],v[24:27]
ds_read_b128 v[36:39],v4 offset:26624
v_mfma_f32_16x16x32_bf16 v[24:27],v[112:115],v[40:43],v[24:27]
s_waitcnt lgkmcnt(5)
v_mfma_f32_16x16x32_bf16 v[24:27],a[88:91],v[48:51],v[24:27]
ds_read_b128 v[40:43],v4 offset:40960
v_mfma_f32_16x16x32_bf16 v[24:27],a[200:203],v[52:55],v[24:27]
buffer_load_dwordx4 v2,s[32:35],s63 offen offset:1024 sc1 lds
s_add_u32 s63,s63,s31
v_mfma_f32_16x16x32_bf16 v[28:31],a[204:207],v[52:55],v[28:31]
ds_read_b128 v[44:47],v4 offset:55296
s_waitcnt lgkmcnt(3)
v_mfma_f32_16x16x32_bf16 v[28:31],a[92:95],v[48:51],v[28:31]
v_add_f32 v20,v20,v12
v_mfma_f32_16x16x32_bf16 v[28:31],v[124:127],v[56:59],v[28:31]
ds_read_b128 v[48:51],v4 offset:13312
v_add_f32 v21,v21,v13
v_mfma_f32_16x16x32_bf16 v[28:31],v[236:239],v[60:63],v[28:31]
v_add_f32 v22,v22,v14
v_add_f32 v23,v23,v15
v_mfma_f32_16x16x32_bf16 v[24:27],v[232:235],v[60:63],v[24:27]
ds_read_b128 v[52:55],v4 offset:27648
v_cvt_pk_bf16_f32 v12,v16,v17
v_mfma_f32_16x16x32_bf16 v[24:27],v[120:123],v[56:59],v[24:27]
v_cvt_pk_bf16_f32 v13,v18,v19
s_waitcnt lgkmcnt(4)
v_mfma_f32_16x16x32_bf16 v[24:27],a[96:99],v[32:35],v[24:27]
s_waitcnt vmcnt(10)
s_barrier
v_mfma_f32_16x16x32_bf16 v[24:27],a[208:211],v[36:39],v[24:27]
ds_read_b128 v[56:59],v4 offset:41984
v_cvt_pk_bf16_f32 v14,v20,v21
v_mfma_f32_16x16x32_bf16 v[28:31],a[212:215],v[36:39],v[28:31]
ds_read_b128 v[60:63],v4 offset:56320
v_cvt_pk_bf16_f32 v15,v22,v23
v_mfma_f32_16x16x32_bf16 v[28:31],a[100:103],v[32:35],v[28:31]
s_waitcnt lgkmcnt(4)
v_mfma_f32_16x16x32_bf16 v[28:31],v[132:135],v[40:43],v[28:31]
ds_read_b128 v[32:35],v2 offset:0
v_mfma_f32_16x16x32_bf16 v[28:31],v[244:247],v[44:47],v[28:31]
buffer_store_dwordx4 v[12:15],v2,s[32:35],s69 offen sc1
v_mfma_f32_16x16x32_bf16 v[24:27],v[240:243],v[44:47],v[24:27]
ds_read_b128 v[36:39],v2 offset:14336
v_mfma_f32_16x16x32_bf16 v[24:27],v[128:131],v[40:43],v[24:27]
s_waitcnt lgkmcnt(4)
v_mfma_f32_16x16x32_bf16 v[24:27],a[104:107],v[48:51],v[24:27]
ds_read_b128 v[40:43],v2 offset:28672
)#"
R"#(
v_mfma_f32_16x16x32_bf16 v[24:27],a[216:219],v[52:55],v[24:27]
v_mfma_f32_16x16x32_bf16 v[28:31],a[220:223],v[52:55],v[28:31]
ds_read_b128 v[44:47],v2 offset:43008
s_mov_b32 s67,s69
v_mfma_f32_16x16x32_bf16 v[28:31],a[108:111],v[48:51],v[28:31]
s_mov_b32 s69,s70
s_waitcnt lgkmcnt(4)
v_mfma_f32_16x16x32_bf16 v[28:31],v[140:143],v[56:59],v[28:31]
ds_read_b128 v[48:51],v2 offset:1024
s_add_u32 s70,s70,1187840
v_mfma_f32_16x16x32_bf16 v[28:31],v[252:255],v[60:63],v[28:31]
v_mfma_f32_16x16x32_bf16 v[24:27],v[248:251],v[60:63],v[24:27]
ds_read_b128 v[52:55],v2 offset:15360
v_mfma_f32_16x16x32_bf16 v[24:27],v[136:139],v[56:59],v[24:27]
s_branch .L_dal_3
.L_dab_3:
v_dot2_f32_bf16 v10,v12,s72,0
v_dot2_f32_bf16 v11,v12,v12,0
v_dot2_f32_bf16 v10,v13,s72,v10
v_dot2_f32_bf16 v11,v13,v13,v11
v_dot2_f32_bf16 v10,v14,s72,v10
v_dot2_f32_bf16 v11,v14,v14,v11
v_dot2_f32_bf16 v10,v15,s72,v10
v_dot2_f32_bf16 v11,v15,v15,v11
s_nop 2
v_permlane16_swap_b32 v10,v11
v_add_f32 v10,v10,v11
s_nop 1
v_permlane32_swap_b32 v10,v11
v_add_f32 v10,v10,v11
buffer_store_dword v10,v3,s[32:35],s67 offen offset:1024 sc1
s_mov_b32 s62,s58
s_mov_b32 s70,s59
s_add_u32 s74,s74,8192
v_lshrrev_b32 v5,8,v2
v_lshl_add_u32 v5,v5,4,s74
v_and_b32 v3,128,v2
v_lshl_add_u32 v5,v3,2,v5
v_and_b32 v3,16,v2
v_lshl_add_u32 v5,v3,4,v5
v_and_b32 v3,96,v2
v_lshl_add_u32 v5,v3,1,v5
v_lshrrev_b32 v3,2,v2
v_add_u32 v3,s74,v3
s_add_u32 s66,s66,2375680
s_add_u32 s63,s63,2375680
s_add_u32 s71,s71,2375680
s_mov_b32 s68,98304
s_mov_b32 s34,79446016
.p2align 6
.L_dbl_3:
s_waitcnt lgkmcnt(4)
v_mfma_f32_16x16x32_bf16 v[24:27],a[0:3],v[32:35],0
ds_read_b128 v[56:59],v4 offset:29696
v_mfma_f32_16x16x32_bf16 v[24:27],a[112:115],v[36:39],v[24:27]
v_mfma_f32_16x16x32_bf16 v[28:31],a[116:119],v[36:39],0
ds_read_b128 v[60:63],v4 offset:44032
v_mfma_f32_16x16x32_bf16 v[28:31],a[4:7],v[32:35],v[28:31]
v_cvt_pk_bf16_f32 v15,v22,v23
s_waitcnt lgkmcnt(4)
v_mfma_f32_16x16x32_bf16 v[28:31],a[228:231],v[40:43],v[28:31]
ds_read_b128 v[32:35],v4 offset:2048
v_cvt_pk_bf16_f32 v14,v20,v21
v_mfma_f32_16x16x32_bf16 v[28:31],v[148:151],v[44:47],v[28:31]
v_cvt_pk_bf16_f32 v13,v18,v19
v_cvt_pk_bf16_f32 v12,v16,v17
v_mfma_f32_16x16x32_bf16 v[24:27],v[144:147],v[44:47],v[24:27]
ds_read_b128 v[36:39],v4 offset:16384
ds_write_b128 v5,v[12:15]
v_mfma_f32_16x16x32_bf16 v[24:27],a[224:227],v[40:43],v[24:27]
ds_read2st64_b32 v[12:13],v3 offset0:0 offset1:1
s_waitcnt lgkmcnt(6)
v_mfma_f32_16x16x32_bf16 v[24:27],a[8:11],v[48:51],v[24:27]
ds_read_b128 v[40:43],v4 offset:30720
ds_read2st64_b32 v[14:15],v3 offset0:2 offset1:3
v_mfma_f32_16x16x32_bf16 v[24:27],a[120:123],v[52:55],v[24:27]
v_mfma_f32_16x16x32_bf16 v[28:31],a[124:127],v[52:55],v[28:31]
ds_read_b128 v[44:47],v4 offset:45056
v_mfma_f32_16x16x32_bf16 v[28:31],a[12:15],v[48:51],v[28:31]
s_waitcnt lgkmcnt(7)
v_mfma_f32_16x16x32_bf16 v[28:31],a[236:239],v[56:59],v[28:31]
ds_read_b128 v[48:51],v4 offset:3072
v_mfma_f32_16x16x32_bf16 v[28:31],v[156:159],v[60:63],v[28:31]
s_waitcnt lgkmcnt(2)
buffer_atomic_pk_add_bf16 v12,v1,s[44:47],s65 offen offset:0 sc1
v_mfma_f32_16x16x32_bf16 v[24:27],v[152:155],v[60:63],v[24:27]
ds_read_b128 v[52:55],v4 offset:17408
s_add_u32 s70,s70,s75
v_mfma_f32_16x16x32_bf16 v[24:27],a[232:235],v[56:59],v[24:27]
buffer_atomic_pk_add_bf16 v13,v1,s[44:47],s65 offen offset:2048 sc1
v_mfma_f32_16x16x32_bf16 v[24:27],a[16:19],v[32:35],v[24:27]
s_waitcnt vmcnt(2)
s_barrier
v_mfma_f32_16x16x32_bf16 v[24:27],a[128:131],v[36:39],v[24:27]
ds_read_b128 v[56:59],v4 offset:31744
s_add_u32 s65,s65,16384
v_mfma_f32_16x16x32_bf16 v[28:31],a[132:135],v[36:39],v[28:31]
ds_read_b128 v[60:63],v4 offset:46080
v_mfma_f32_16x16x32_bf16 v[28:31],a[20:23],v[32:35],v[28:31]
buffer_atomic_pk_add_bf16 v14,v1,s[44:47],s65 offen offset:0 sc1
s_waitcnt lgkmcnt(4)
v_mfma_f32_16x16x32_bf16 v[28:31],a[244:247],v[40:43],v[28:31]
ds_read_b128 v[32:35],v4 offset:4096
s_add_u32 m0,s64,0
v_mfma_f32_16x16x32_bf16 v[28:31],v[164:167],v[44:47],v[28:31]
buffer_atomic_pk_add_bf16 v15,v1,s[44:47],s65 offen offset:2048 sc1
s_and_b32 s65,s70,0x3f8000
v_mfma_f32_16x16x32_bf16 v[24:27],v[160:163],v[44:47],v[24:27]
ds_read_b128 v[36:39],v4 offset:18432
v_mfma_f32_16x16x32_bf16 v[24:27],a[240:243],v[40:43],v[24:27]
s_waitcnt lgkmcnt(4)
v_mfma_f32_16x16x32_bf16 v[24:27],a[24:27],v[48:51],v[24:27]
ds_read_b128 v[40:43],v4 offset:32768
v_mfma_f32_16x16x32_bf16 v[24:27],a[136:139],v[52:55],v[24:27]
buffer_load_dwordx4 v2,s[32:35],s63 offen offset:0 sc1 lds
v_mfma_f32_16x16x32_bf16 v[28:31],a[140:143],v[52:55],v[28:31]
ds_read_b128 v[44:47],v4 offset:47104
v_mfma_f32_16x16x32_bf16 v[28:31],a[28:31],v[48:51],v[28:31]
s_waitcnt lgkmcnt(4)
v_mfma_f32_16x16x32_bf16 v[28:31],a[252:255],v[56:59],v[28:31]
ds_read_b128 v[48:51],v4 offset:5120
v_mfma_f32_16x16x32_bf16 v[28:31],v[172:175],v[60:63],v[28:31]
buffer_load_dwordx4 v2,s[32:35],s63 offen offset:1024 sc1 lds
v_mfma_f32_16x16x32_bf16 v[24:27],v[168:171],v[60:63],v[24:27]
ds_read_b128 v[52:55],v4 offset:19456
v_mfma_f32_16x16x32_bf16 v[24:27],a[248:251],v[56:59],v[24:27]
s_waitcnt lgkmcnt(4)
v_mfma_f32_16x16x32_bf16 v[24:27],a[32:35],v[32:35],v[24:27]
ds_read_b128 v[56:59],v4 offset:33792
v_mfma_f32_16x16x32_bf16 v[24:27],a[144:147],v[36:39],v[24:27]
buffer_load_dwordx4 v2,s[32:35],s63 offen offset:2048 sc1 lds
v_mfma_f32_16x16x32_bf16 v[28:31],a[148:151],v[36:39],v[28:31]
ds_read_b128 v[60:63],v4 offset:48128
v_mfma_f32_16x16x32_bf16 v[28:31],a[36:39],v[32:35],v[28:31]
s_waitcnt lgkmcnt(4)
v_mfma_f32_16x16x32_bf16 v[28:31],v[68:71],v[40:43],v[28:31]
ds_read_b128 v[32:35],v4 offset:6144
v_mfma_f32_16x16x32_bf16 v[28:31],v[180:183],v[44:47],v[28:31]
buffer_load_dwordx4 v2,s[32:35],s63 offen offset:3072 sc1 lds
v_mfma_f32_16x16x32_bf16 v[24:27],v[176:179],v[44:47],v[24:27]
ds_read_b128 v[36:39],v4 offset:20480
v_mfma_f32_16x16x32_bf16 v[24:27],v[64:67],v[40:43],v[24:27]
s_waitcnt lgkmcnt(4)
v_mfma_f32_16x16x32_bf16 v[24:27],a[40:43],v[48:51],v[24:27]
ds_read_b128 v[40:43],v4 offset:34816
s_add_u32 s63,s63,4096
v_mfma_f32_16x16x32_bf16 v[24:27],a[152:155],v[52:55],v[24:27]
s_add_u32 m0,s64,4096
v_mfma_f32_16x16x32_bf16 v[28:31],a[156:159],v[52:55],v[28:31]
ds_read_b128 v[44:47],v4 offset:49152
v_mfma_f32_16x16x32_bf16 v[28:31],a[44:47],v[48:51],v[28:31]
s_waitcnt lgkmcnt(4)
v_mfma_f32_16x16x32_bf16 v[28:31],v[76:79],v[56:59],v[28:31]
ds_read_b128 v[48:51],v4 offset:7168
v_mfma_f32_16x16x32_bf16 v[28:31],v[188:191],v[60:63],v[28:31]
buffer_load_dwordx4 v2,s[32:35],s63 offen offset:0 sc1 lds
v_mfma_f32_16x16x32_bf16 v[24:27],v[184:187],v[60:63],v[24:27]
ds_read_b128 v[52:55],v4 offset:21504
v_mfma_f32_16x16x32_bf16 v[24:27],v[72:75],v[56:59],v[24:27]
s_waitcnt lgkmcnt(4)
v_mfma_f32_16x16x32_bf16 v[24:27],a[48:51],v[32:35],v[24:27]
ds_read_b128 v[56:59],v4 offset:35840
v_mfma_f32_16x16x32_bf16 v[24:27],a[160:163],v[36:39],v[24:27]
buffer_load_dwordx4 v2,s[32:35],s63 offen offset:1024 sc1 lds
v_mfma_f32_16x16x32_bf16 v[28:31],a[164:167],v[36:39],v[28:31]
ds_read_b128 v[60:63],v4 offset:50176
v_mfma_f32_16x16x32_bf16 v[28:31],a[52:55],v[32:35],v[28:31]
s_waitcnt lgkmcnt(4)
v_mfma_f32_16x16x32_bf16 v[28:31],v[84:87],v[40:43],v[28:31]
ds_read_b128 v[32:35],v4 offset:8192
v_mfma_f32_16x16x32_bf16 v[28:31],v[196:199],v[44:47],v[28:31]
buffer_load_dwordx4 v2,s[32:35],s63 offen offset:2048 sc1 lds
v_mfma_f32_16x16x32_bf16 v[24:27],v[192:195],v[44:47],v[24:27]
ds_read_b128 v[36:39],v4 offset:22528
v_mfma_f32_16x16x32_bf16 v[24:27],v[80:83],v[40:43],v[24:27]
s_waitcnt lgkmcnt(4)
v_mfma_f32_16x16x32_bf16 v[24:27],a[56:59],v[48:51],v[24:27]
ds_read_b128 v[40:43],v4 offset:36864
v_mfma_f32_16x16x32_bf16 v[24:27],a[168:171],v[52:55],v[24:27]
buffer_load_dwordx4 v2,s[32:35],s63 offen offset:3072 sc1 lds
v_mfma_f32_16x16x32_bf16 v[28:31],a[172:175],v[52:55],v[28:31]
ds_read_b128 v[44:47],v4 offset:51200
v_mfma_f32_16x16x32_bf16 v[28:31],a[60:63],v[48:51],v[28:31]
s_waitcnt lgkmcnt(4)
v_mfma_f32_16x16x32_bf16 v[28:31],v[92:95],v[56:59],v[28:31]
ds_read_b128 v[48:51],v4 offset:9216
v_mfma_f32_16x16x32_bf16 v[28:31],v[204:207],v[60:63],v[28:31]
s_add_u32 s63,s63,4096
s_add_u32 m0,s64,8192
v_mfma_f32_16x16x32_bf16 v[24:27],v[200:203],v[60:63],v[24:27]
ds_read_b128 v[52:55],v4 offset:23552
v_mfma_f32_16x16x32_bf16 v[24:27],v[88:91],v[56:59],v[24:27]
s_waitcnt lgkmcnt(4)
v_mfma_f32_16x16x32_bf16 v[24:27],a[64:67],v[32:35],v[24:27]
ds_read_b128 v[56:59],v4 offset:37888
v_mfma_f32_16x16x32_bf16 v[24:27],a[176:179],v[36:39],v[24:27]
buffer_load_dwordx4 v2,s[32:35],s63 offen offset:0 sc1 lds
v_mfma_f32_16x16x32_bf16 v[28:31],a[180:183],v[36:39],v[28:31]
ds_read_b128 v[60:63],v4 offset:52224
v_mfma_f32_16x16x32_bf16 v[28:31],a[68:71],v[32:35],v[28:31]
s_waitcnt lgkmcnt(4)
v_mfma_f32_16x16x32_bf16 v[28:31],v[100:103],v[40:43],v[28:31]
ds_read_b128 v[32:35],v4 offset:10240
v_mfma_f32_16x16x32_bf16 v[28:31],v[212:215],v[44:47],v[28:31]
buffer_load_dwordx4 v2,s[32:35],s63 offen offset:1024 sc1 lds
v_mfma_f32_16x16x32_bf16 v[24:27],v[208:211],v[44:47],v[24:27]
ds_read_b128 v[36:39],v4 offset:24576
v_mfma_f32_16x16x32_bf16 v[24:27],v[96:99],v[40:43],v[24:27]
s_waitcnt lgkmcnt(4)
v_mfma_f32_16x16x32_bf16 v[24:27],a[72:75],v[48:51],v[24:27]
ds_read_b128 v[40:43],v4 offset:38912
v_mfma_f32_16x16x32_bf16 v[24:27],a[184:187],v[52:55],v[24:27]
buffer_load_dwordx4 v2,s[32:35],s63 offen offset:2048 sc1 lds
v_mfma_f32_16x16x32_bf16 v[28:31],a[188:191],v[52:55],v[28:31]
ds_read_b128 v[44:47],v4 offset:53248
v_mfma_f32_16x16x32_bf16 v[28:31],a[76:79],v[48:51],v[28:31]
s_waitcnt lgkmcnt(4)
v_mfma_f32_16x16x32_bf16 v[28:31],v[108:111],v[56:59],v[28:31]
ds_read_b128 v[48:51],v4 offset:11264
v_mfma_f32_16x16x32_bf16 v[28:31],v[220:223],v[60:63],v[28:31]
v_mfma_f32_16x16x32_bf16 v[24:27],v[216:219],v[60:63],v[24:27]
ds_read_b128 v[52:55],v4 offset:25600
v_mfma_f32_16x16x32_bf16 v[24:27],v[104:107],v[56:59],v[24:27]
s_waitcnt lgkmcnt(4)
v_mfma_f32_16x16x32_bf16 v[24:27],a[80:83],v[32:35],v[24:27]
ds_read_b128 v[56:59],v4 offset:39936
v_mfma_f32_16x16x32_bf16 v[24:27],a[192:195],v[36:39],v[24:27]
buffer_load_dwordx4 v2,s[32:35],s63 offen offset:3072 sc1 lds
s_add_u32 s63,s63,4096
v_mfma_f32_16x16x32_bf16 v[28:31],a[196:199],v[36:39],v[28:31]
ds_read_b128 v[60:63],v4 offset:54272
s_add_u32 m0,s64,12288
v_mfma_f32_16x16x32_bf16 v[28:31],a[84:87],v[32:35],v[28:31]
s_waitcnt lgkmcnt(4)
v_mfma_f32_16x16x32_bf16 v[28:31],v[116:119],v[40:43],v[28:31]
ds_read_b128 v[32:35],v4 offset:12288
v_mfma_f32_16x16x32_bf16 v[28:31],v[228:231],v[44:47],v[28:31]
buffer_load_dwordx4 v2,s[32:35],s63 offen offset:0 sc1 lds
v_mfma_f32_16x16x32_bf16 v[24:27],v[224:227],v[44:47],v[24:27]
ds_read_b128 v[36:39],v4 offset:26624
v_mfma_f32_16x16x32_bf16 v[24:27],v[112:115],v[40:43],v[24:27]
s_waitcnt lgkmcnt(4)
v_mfma_f32_16x16x32_bf16 v[24:27],a[88:91],v[48:51],v[24:27]
ds_read_b128 v[40:43],v4 offset:40960
v_mfma_f32_16x16x32_bf16 v[24:27],a[200:203],v[52:55],v[24:27]
buffer_load_dwordx4 v2,s[32:35],s63 offen offset:1024 sc1 lds
s_add_u32 s63,s63,s31
v_mfma_f32_16x16x32_bf16 v[28:31],a[204:207],v[52:55],v[28:31]
ds_read_b128 v[44:47],v4 offset:55296
v_mfma_f32_16x16x32_bf16 v[28:31],a[92:95],v[48:51],v[28:31]
s_waitcnt lgkmcnt(4)
v_mfma_f32_16x16x32_bf16 v[28:31],v[124:127],v[56:59],v[28:31]
ds_read_b128 v[48:51],v4 offset:13312
v_mfma_f32_16x16x32_bf16 v[28:31],v[236:239],v[60:63],v[28:31]
v_mfma_f32_16x16x32_bf16 v[24:27],v[232:235],v[60:63],v[24:27]
ds_read_b128 v[52:55],v4 offset:27648
v_mfma_f32_16x16x32_bf16 v[24:27],v[120:123],v[56:59],v[24:27]
s_waitcnt lgkmcnt(4)
v_mfma_f32_16x16x32_bf16 v[24:27],a[96:99],v[32:35],v[24:27]
s_waitcnt vmcnt(10)
s_barrier
v_mfma_f32_16x16x32_bf16 v[24:27],a[208:211],v[36:39],v[24:27]
ds_read_b128 v[56:59],v4 offset:41984
v_mfma_f32_16x16x32_bf16 v[28:31],a[212:215],v[36:39],v[28:31]
ds_read_b128 v[60:63],v4 offset:56320
v_mfma_f32_16x16x32_bf16 v[28:31],a[100:103],v[32:35],v[28:31]
s_waitcnt lgkmcnt(4)
v_mfma_f32_16x16x32_bf16 v[28:31],v[132:135],v[40:43],v[28:31]
ds_read_b128 v[32:35],v2 offset:0
v_mfma_f32_16x16x32_bf16 v[28:31],v[244:247],v[44:47],v[28:31]
v_mfma_f32_16x16x32_bf16 v[24:27],v[240:243],v[44:47],v[24:27]
ds_read_b128 v[36:39],v2 offset:14336
v_mfma_f32_16x16x32_bf16 v[24:27],v[128:131],v[40:43],v[24:27]
s_waitcnt lgkmcnt(4)
v_mfma_f32_16x16x32_bf16 v[24:27],a[104:107],v[48:51],v[24:27]
ds_read_b128 v[40:43],v2 offset:28672
v_mfma_f32_16x16x32_bf16 v[24:27],a[216:219],v[52:55],v[24:27]
v_mfma_f32_16x16x32_bf16 v[28:31],a[220:223],v[52:55],v[28:31]
ds_read_b128 v[44:47],v2 offset:43008
v_mfma_f32_16x16x32_bf16 v[28:31],a[108:111],v[48:51],v[28:31]
s_waitcnt lgkmcnt(4)
v_mfma_f32_16x16x32_bf16 v[28:31],v[140:143],v[56:59],v[28:31]
ds_read_b128 v[48:51],v2 offset:1024
v_mfma_f32_16x16x32_bf16 v[28:31],v[252:255],v[60:63],v[28:31]
v_mfma_f32_16x16x32_bf16 v[24:27],v[248:251],v[60:63],v[24:27]
ds_read_b128 v[52:55],v2 offset:15360
s_sub_u32 s62,s62,1
v_mfma_f32_16x16x32_bf16 v[24:27],v[136:139],v[56:59],v[24:27]
s_cbranch_scc1 .L_dbt_3
s_waitcnt lgkmcnt(4)
v_mfma_f32_16x16x32_bf16 v[16:19],a[0:3],v[32:35],0
ds_read_b128 v[56:59],v2 offset:29696
v_mfma_f32_16x16x32_bf16 v[16:19],a[112:115],v[36:39],v[16:19]
v_mfma_f32_16x16x32_bf16 v[20:23],a[116:119],v[36:39],0
ds_read_b128 v[60:63],v2 offset:44032
v_mfma_f32_16x16x32_bf16 v[20:23],a[4:7],v[32:35],v[20:23]
v_cvt_pk_bf16_f32 v15,v30,v31
s_waitcnt lgkmcnt(4)
v_mfma_f32_16x16x32_bf16 v[20:23],a[228:231],v[40:43],v[20:23]
ds_read_b128 v[32:35],v2 offset:2048
v_cvt_pk_bf16_f32 v14,v28,v29
v_mfma_f32_16x16x32_bf16 v[20:23],v[148:151],v[44:47],v[20:23]
v_cvt_pk_bf16_f32 v13,v26,v27
v_cvt_pk_bf16_f32 v12,v24,v25
v_mfma_f32_16x16x32_bf16 v[16:19],v[144:147],v[44:47],v[16:19]
ds_read_b128 v[36:39],v2 offset:16384
ds_write_b128 v5,v[12:15]
v_mfma_f32_16x16x32_bf16 v[16:19],a[224:227],v[40:43],v[16:19]
ds_read2st64_b32 v[12:13],v3 offset0:0 offset1:1
s_waitcnt lgkmcnt(6)
v_mfma_f32_16x16x32_bf16 v[16:19],a[8:11],v[48:51],v[16:19]
ds_read_b128 v[40:43],v2 offset:30720
ds_read2st64_b32 v[14:15],v3 offset0:2 offset1:3
v_mfma_f32_16x16x32_bf16 v[16:19],a[120:123],v[52:55],v[16:19]
v_mfma_f32_16x16x32_bf16 v[20:23],a[124:127],v[52:55],v[20:23]
ds_read_b128 v[44:47],v2 offset:45056
v_mfma_f32_16x16x32_bf16 v[20:23],a[12:15],v[48:51],v[20:23]
s_waitcnt lgkmcnt(7)
v_mfma_f32_16x16x32_bf16 v[20:23],a[236:239],v[56:59],v[20:23]
ds_read_b128 v[48:51],v2 offset:3072
v_mfma_f32_16x16x32_bf16 v[20:23],v[156:159],v[60:63],v[20:23]
s_waitcnt lgkmcnt(2)
buffer_atomic_pk_add_bf16 v12,v1,s[44:47],s65 offen offset:0 sc1
v_mfma_f32_16x16x32_bf16 v[16:19],v[152:155],v[60:63],v[16:19]
ds_read_b128 v[52:55],v2 offset:17408
s_add_u32 s70,s70,s68
v_mfma_f32_16x16x32_bf16 v[16:19],a[232:235],v[56:59],v[16:19]
buffer_atomic_pk_add_bf16 v13,v1,s[44:47],s65 offen offset:2048 sc1
v_mfma_f32_16x16x32_bf16 v[16:19],a[16:19],v[32:35],v[16:19]
s_waitcnt vmcnt(2)
s_barrier
v_mfma_f32_16x16x32_bf16 v[16:19],a[128:131],v[36:39],v[16:19]
ds_read_b128 v[56:59],v2 offset:31744
s_add_u32 s65,s65,16384
v_mfma_f32_16x16x32_bf16 v[20:23],a[132:135],v[36:39],v[20:23]
ds_read_b128 v[60:63],v2 offset:46080
v_mfma_f32_16x16x32_bf16 v[20:23],a[20:23],v[32:35],v[20:23]
buffer_atomic_pk_add_bf16 v14,v1,s[44:47],s65 offen offset:0 sc1
s_waitcnt lgkmcnt(4)
v_mfma_f32_16x16x32_bf16 v[20:23],a[244:247],v[40:43],v[20:23]
ds_read_b128 v[32:35],v2 offset:4096
s_add_u32 m0,s64,57344
v_mfma_f32_16x16x32_bf16 v[20:23],v[164:167],v[44:47],v[20:23]
buffer_load_dwordx4 v2,s[32:35],s63 offen offset:0 sc1 lds
s_mov_b32 s68,s75
v_mfma_f32_16x16x32_bf16 v[16:19],v[160:163],v[44:47],v[16:19]
ds_read_b128 v[36:39],v2 offset:18432
v_mfma_f32_16x16x32_bf16 v[16:19],a[240:243],v[40:43],v[16:19]
buffer_atomic_pk_add_bf16 v15,v1,s[44:47],s65 offen offset:2048 sc1
s_waitcnt lgkmcnt(4)
v_mfma_f32_16x16x32_bf16 v[16:19],a[24:27],v[48:51],v[16:19]
ds_read_b128 v[40:43],v2 offset:32768
s_and_b32 s65,s70,0x3f8000
v_mfma_f32_16x16x32_bf16 v[16:19],a[136:139],v[52:55],v[16:19]
v_mfma_f32_16x16x32_bf16 v[20:23],a[140:143],v[52:55],v[20:23]
ds_read_b128 v[44:47],v2 offset:47104
v_mfma_f32_16x16x32_bf16 v[20:23],a[28:31],v[48:51],v[20:23]
s_waitcnt lgkmcnt(4)
v_mfma_f32_16x16x32_bf16 v[20:23],a[252:255],v[56:59],v[20:23]
ds_read_b128 v[48:51],v2 offset:5120
v_mfma_f32_16x16x32_bf16 v[20:23],v[172:175],v[60:63],v[20:23]
buffer_load_dwordx4 v2,s[32:35],s63 offen offset:1024 sc1 lds
v_mfma_f32_16x16x32_bf16 v[16:19],v[168:171],v[60:63],v[16:19]
ds_read_b128 v[52:55],v2 offset:19456
v_mfma_f32_16x16x32_bf16 v[16:19],a[248:251],v[56:59],v[16:19]
s_waitcnt lgkmcnt(4)
v_mfma_f32_16x16x32_bf16 v[16:19],a[32:35],v[32:35],v[16:19]
ds_read_b128 v[56:59],v2 offset:33792
v_mfma_f32_16x16x32_bf16 v[16:19],a[144:147],v[36:39],v[16:19]
buffer_load_dwordx4 v2,s[32:35],s63 offen offset:2048 sc1 lds
v_mfma_f32_16x16x32_bf16 v[20:23],a[148:151],v[36:39],v[20:23]
ds_read_b128 v[60:63],v2 offset:48128
v_mfma_f32_16x16x32_bf16 v[20:23],a[36:39],v[32:35],v[20:23]
s_waitcnt lgkmcnt(4)
v_mfma_f32_16x16x32_bf16 v[20:23],v[68:71],v[40:43],v[20:23]
ds_read_b128 v[32:35],v2 offset:6144
v_mfma_f32_16x16x32_bf16 v[20:23],v[180:183],v[44:47],v[20:23]
buffer_load_dwordx4 v2,s[32:35],s63 offen offset:3072 sc1 lds
v_mfma_f32_16x16x32_bf16 v[16:19],v[176:179],v[44:47],v[16:19]
ds_read_b128 v[36:39],v2 offset:20480
v_mfma_f32_16x16x32_bf16 v[16:19],v[64:67],v[40:43],v[16:19]
s_waitcnt lgkmcnt(4)
v_mfma_f32_16x16x32_bf16 v[16:19],a[40:43],v[48:51],v[16:19]
ds_read_b128 v[40:43],v2 offset:34816
s_add_u32 s63,s63,4096
v_mfma_f32_16x16x32_bf16 v[16:19],a[152:155],v[52:55],v[16:19]
s_add_u32 m0,s64,61440
v_mfma_f32_16x16x32_bf16 v[20:23],a[156:159],v[52:55],v[20:23]
ds_read_b128 v[44:47],v2 offset:49152
v_mfma_f32_16x16x32_bf16 v[20:23],a[44:47],v[48:51],v[20:23]
s_waitcnt lgkmcnt(4)
v_mfma_f32_16x16x32_bf16 v[20:23],v[76:79],v[56:59],v[20:23]
ds_read_b128 v[48:51],v2 offset:7168
v_mfma_f32_16x16x32_bf16 v[20:23],v[188:191],v[60:63],v[20:23]
buffer_load_dwordx4 v2,s[32:35],s63 offen offset:0 sc1 lds
v_mfma_f32_16x16x32_bf16 v[16:19],v[184:187],v[60:63],v[16:19]
ds_read_b128 v[52:55],v2 offset:21504
v_mfma_f32_16x16x32_bf16 v[16:19],v[72:75],v[56:59],v[16:19]
s_waitcnt lgkmcnt(4)
v_mfma_f32_16x16x32_bf16 v[16:19],a[48:51],v[32:35],v[16:19]
ds_read_b128 v[56:59],v2 offset:35840
v_mfma_f32_16x16x32_bf16 v[16:19],a[160:163],v[36:39],v[16:19]
buffer_load_dwordx4 v2,s[32:35],s63 offen offset:1024 sc1 lds
v_mfma_f32_16x16x32_bf16 v[20:23],a[164:167],v[36:39],v[20:23]
ds_read_b128 v[60:63],v2 offset:50176
v_mfma_f32_16x16x32_bf16 v[20:23],a[52:55],v[32:35],v[20:23]
s_waitcnt lgkmcnt(4)
v_mfma_f32_16x16x32_bf16 v[20:23],v[84:87],v[40:43],v[20:23]
ds_read_b128 v[32:35],v2 offset:8192
v_mfma_f32_16x16x32_bf16 v[20:23],v[196:199],v[44:47],v[20:23]
buffer_load_dwordx4 v2,s[32:35],s63 offen offset:2048 sc1 lds
v_mfma_f32_16x16x32_bf16 v[16:19],v[192:195],v[44:47],v[16:19]
ds_read_b128 v[36:39],v2 offset:22528
v_mfma_f32_16x16x32_bf16 v[16:19],v[80:83],v[40:43],v[16:19]
s_waitcnt lgkmcnt(4)
v_mfma_f32_16x16x32_bf16 v[16:19],a[56:59],v[48:51],v[16:19]
ds_read_b128 v[40:43],v2 offset:36864
v_mfma_f32_16x16x32_bf16 v[16:19],a[168:171],v[52:55],v[16:19]
buffer_load_dwordx4 v2,s[32:35],s63 offen offset:3072 sc1 lds
v_mfma_f32_16x16x32_bf16 v[20:23],a[172:175],v[52:55],v[20:23]
ds_read_b128 v[44:47],v2 offset:51200
v_mfma_f32_16x16x32_bf16 v[20:23],a[60:63],v[48:51],v[20:23]
s_waitcnt lgkmcnt(4)
v_mfma_f32_16x16x32_bf16 v[20:23],v[92:95],v[56:59],v[20:23]
ds_read_b128 v[48:51],v2 offset:9216
v_mfma_f32_16x16x32_bf16 v[20:23],v[204:207],v[60:63],v[20:23]
s_add_u32 s63,s63,4096
s_add_u32 m0,s64,65536
v_mfma_f32_16x16x32_bf16 v[16:19],v[200:203],v[60:63],v[16:19]
ds_read_b128 v[52:55],v2 offset:23552
v_mfma_f32_16x16x32_bf16 v[16:19],v[88:91],v[56:59],v[16:19]
s_waitcnt lgkmcnt(4)
v_mfma_f32_16x16x32_bf16 v[16:19],a[64:67],v[32:35],v[16:19]
ds_read_b128 v[56:59],v2 offset:37888
v_mfma_f32_16x16x32_bf16 v[16:19],a[176:179],v[36:39],v[16:19]
buffer_load_dwordx4 v2,s[32:35],s63 offen offset:0 sc1 lds
v_mfma_f32_16x16x32_bf16 v[20:23],a[180:183],v[36:39],v[20:23]
ds_read_b128 v[60:63],v2 offset:52224
v_mfma_f32_16x16x32_bf16 v[20:23],a[68:71],v[32:35],v[20:23]
s_waitcnt lgkmcnt(4)
v_mfma_f32_16x16x32_bf16 v[20:23],v[100:103],v[40:43],v[20:23]
ds_read_b128 v[32:35],v2 offset:10240
v_mfma_f32_16x16x32_bf16 v[20:23],v[212:215],v[44:47],v[20:23]
buffer_load_dwordx4 v2,s[32:35],s63 offen offset:1024 sc1 lds
v_mfma_f32_16x16x32_bf16 v[16:19],v[208:211],v[44:47],v[16:19]
ds_read_b128 v[36:39],v2 offset:24576
v_mfma_f32_16x16x32_bf16 v[16:19],v[96:99],v[40:43],v[16:19]
s_waitcnt lgkmcnt(4)
v_mfma_f32_16x16x32_bf16 v[16:19],a[72:75],v[48:51],v[16:19]
ds_read_b128 v[40:43],v2 offset:38912
v_mfma_f32_16x16x32_bf16 v[16:19],a[184:187],v[52:55],v[16:19]
buffer_load_dwordx4 v2,s[32:35],s63 offen offset:2048 sc1 lds
v_mfma_f32_16x16x32_bf16 v[20:23],a[188:191],v[52:55],v[20:23]
ds_read_b128 v[44:47],v2 offset:53248
v_mfma_f32_16x16x32_bf16 v[20:23],a[76:79],v[48:51],v[20:23]
s_waitcnt lgkmcnt(4)
v_mfma_f32_16x16x32_bf16 v[20:23],v[108:111],v[56:59],v[20:23]
ds_read_b128 v[48:51],v2 offset:11264
v_mfma_f32_16x16x32_bf16 v[20:23],v[220:223],v[60:63],v[20:23]
v_mfma_f32_16x16x32_bf16 v[16:19],v[216:219],v[60:63],v[16:19]
ds_read_b128 v[52:55],v2 offset:25600
v_mfma_f32_16x16x32_bf16 v[16:19],v[104:107],v[56:59],v[16:19]
s_waitcnt lgkmcnt(4)
v_mfma_f32_16x16x32_bf16 v[16:19],a[80:83],v[32:35],v[16:19]
ds_read_b128 v[56:59],v2 offset:39936
v_mfma_f32_16x16x32_bf16 v[16:19],a[192:195],v[36:39],v[16:19]
buffer_load_dwordx4 v2,s[32:35],s63 offen offset:3072 sc1 lds
s_add_u32 s63,s63,4096
v_mfma_f32_16x16x32_bf16 v[20:23],a[196:199],v[36:39],v[20:23]
ds_read_b128 v[60:63],v2 offset:54272
s_add_u32 m0,s64,69632
v_mfma_f32_16x16x32_bf16 v[20:23],a[84:87],v[32:35],v[20:23]
s_waitcnt lgkmcnt(4)
v_mfma_f32_16x16x32_bf16 v[20:23],v[116:119],v[40:43],v[20:23]
ds_read_b128 v[32:35],v2 offset:12288
v_mfma_f32_16x16x32_bf16 v[20:23],v[228:231],v[44:47],v[20:23]
buffer_load_dwordx4 v2,s[32:35],s63 offen offset:0 sc1 lds
v_mfma_f32_16x16x32_bf16 v[16:19],v[224:227],v[44:47],v[16:19]
ds_read_b128 v[36:39],v2 offset:26624
v_mfma_f32_16x16x32_bf16 v[16:19],v[112:115],v[40:43],v[16:19]
s_waitcnt lgkmcnt(4)
v_mfma_f32_16x16x32_bf16 v[16:19],a[88:91],v[48:51],v[16:19]
ds_read_b128 v[40:43],v2 offset:40960
v_mfma_f32_16x16x32_bf16 v[16:19],a[200:203],v[52:55],v[16:19]
buffer_load_dwordx4 v2,s[32:35],s63 offen offset:1024 sc1 lds
s_add_u32 s63,s63,s31
v_mfma_f32_16x16x32_bf16 v[20:23],a[204:207],v[52:55],v[20:23]
ds_read_b128 v[44:47],v2 offset:55296
v_mfma_f32_16x16x32_bf16 v[20:23],a[92:95],v[48:51],v[20:23]
s_waitcnt lgkmcnt(4)
v_mfma_f32_16x16x32_bf16 v[20:23],v[124:127],v[56:59],v[20:23]
ds_read_b128 v[48:51],v2 offset:13312
v_mfma_f32_16x16x32_bf16 v[20:23],v[236:239],v[60:63],v[20:23]
v_mfma_f32_16x16x32_bf16 v[16:19],v[232:235],v[60:63],v[16:19]
ds_read_b128 v[52:55],v2 offset:27648
v_mfma_f32_16x16x32_bf16 v[16:19],v[120:123],v[56:59],v[16:19]
s_waitcnt lgkmcnt(4)
v_mfma_f32_16x16x32_bf16 v[16:19],a[96:99],v[32:35],v[16:19]
s_waitcnt vmcnt(10)
s_barrier
)#"
R"#(
v_mfma_f32_16x16x32_bf16 v[16:19],a[208:211],v[36:39],v[16:19]
ds_read_b128 v[56:59],v2 offset:41984
v_mfma_f32_16x16x32_bf16 v[20:23],a[212:215],v[36:39],v[20:23]
ds_read_b128 v[60:63],v2 offset:56320
v_mfma_f32_16x16x32_bf16 v[20:23],a[100:103],v[32:35],v[20:23]
s_waitcnt lgkmcnt(4)
v_mfma_f32_16x16x32_bf16 v[20:23],v[132:135],v[40:43],v[20:23]
ds_read_b128 v[32:35],v4 offset:0
v_mfma_f32_16x16x32_bf16 v[20:23],v[244:247],v[44:47],v[20:23]
v_mfma_f32_16x16x32_bf16 v[16:19],v[240:243],v[44:47],v[16:19]
ds_read_b128 v[36:39],v4 offset:14336
v_mfma_f32_16x16x32_bf16 v[16:19],v[128:131],v[40:43],v[16:19]
s_waitcnt lgkmcnt(4)
v_mfma_f32_16x16x32_bf16 v[16:19],a[104:107],v[48:51],v[16:19]
ds_read_b128 v[40:43],v4 offset:28672
v_mfma_f32_16x16x32_bf16 v[16:19],a[216:219],v[52:55],v[16:19]
v_mfma_f32_16x16x32_bf16 v[20:23],a[220:223],v[52:55],v[20:23]
ds_read_b128 v[44:47],v4 offset:43008
v_mfma_f32_16x16x32_bf16 v[20:23],a[108:111],v[48:51],v[20:23]
s_waitcnt lgkmcnt(4)
v_mfma_f32_16x16x32_bf16 v[20:23],v[140:143],v[56:59],v[20:23]
ds_read_b128 v[48:51],v4 offset:1024
v_mfma_f32_16x16x32_bf16 v[20:23],v[252:255],v[60:63],v[20:23]
v_mfma_f32_16x16x32_bf16 v[16:19],v[248:251],v[60:63],v[16:19]
ds_read_b128 v[52:55],v4 offset:15360
s_sub_u32 s62,s62,1
v_mfma_f32_16x16x32_bf16 v[16:19],v[136:139],v[56:59],v[16:19]
s_cbranch_scc0 .L_dbl_3
.L_dbu_3:
s_nop 1
s_nop 0
v_cvt_pk_bf16_f32 v15,v22,v23
v_cvt_pk_bf16_f32 v14,v20,v21
s_nop 1
v_cvt_pk_bf16_f32 v13,v18,v19
v_cvt_pk_bf16_f32 v12,v16,v17
ds_write_b128 v5,v[12:15]
ds_read2st64_b32 v[12:13],v3 offset0:0 offset1:1
ds_read2st64_b32 v[14:15],v3 offset0:2 offset1:3
s_waitcnt lgkmcnt(0)
buffer_atomic_pk_add_bf16 v12,v1,s[44:47],s65 offen offset:0 sc1
s_add_u32 s70,s70,s75
buffer_atomic_pk_add_bf16 v13,v1,s[44:47],s65 offen offset:2048 sc1
s_add_u32 s65,s65,16384
buffer_atomic_pk_add_bf16 v14,v1,s[44:47],s65 offen offset:0 sc1
buffer_atomic_pk_add_bf16 v15,v1,s[44:47],s65 offen offset:2048 sc1
s_and_b32 s65,s70,0x3f8000
s_or_b32 s48,s28,s27
s_or_b32 s48,s48,s26
s_cbranch_scc1 .L_end
v_mov_b32 v8,s54
v_mov_b32 v9,s55
buffer_store_dwordx2 v[8:9],off,s[32:35],0 sc1
s_branch .L_end
.L_dbt_3:
s_nop 1
s_nop 0
v_cvt_pk_bf16_f32 v15,v30,v31
v_cvt_pk_bf16_f32 v14,v28,v29
s_nop 1
v_cvt_pk_bf16_f32 v13,v26,v27
v_cvt_pk_bf16_f32 v12,v24,v25
ds_write_b128 v5,v[12:15]
ds_read2st64_b32 v[12:13],v3 offset0:0 offset1:1
ds_read2st64_b32 v[14:15],v3 offset0:2 offset1:3
s_waitcnt lgkmcnt(0)
buffer_atomic_pk_add_bf16 v12,v1,s[44:47],s65 offen offset:0 sc1
s_add_u32 s70,s70,s68
buffer_atomic_pk_add_bf16 v13,v1,s[44:47],s65 offen offset:2048 sc1
s_add_u32 s65,s65,16384
buffer_atomic_pk_add_bf16 v14,v1,s[44:47],s65 offen offset:0 sc1
s_mov_b32 s68,s75
buffer_atomic_pk_add_bf16 v15,v1,s[44:47],s65 offen offset:2048 sc1
s_and_b32 s65,s70,0x3f8000
s_or_b32 s48,s28,s27
s_or_b32 s48,s48,s26
s_cbranch_scc1 .L_end
v_mov_b32 v8,s54
v_mov_b32 v9,s55
buffer_store_dwordx2 v[8:9],off,s[32:35],0 sc1
s_branch .L_end
.L_x:
v_lshrrev_b32 v10,4,v1
v_and_b32 v1,15,v1
s_lshl_b32 s48,s50,6
v_lshl_add_u32 v1,v1,2,s48
v_lshl_add_u32 v1,v10,12,v1
s_add_u32 s71,s70,4681728
s_mov_b32 s65,s59
s_mov_b32 s70,s59
s_mov_b32 s62,2
s_add_u32 s74,s74,8192
v_lshrrev_b32 v5,8,v2
v_lshl_add_u32 v5,v5,4,s74
v_and_b32 v3,128,v2
v_lshl_add_u32 v5,v3,2,v5
v_and_b32 v3,16,v2
v_lshl_add_u32 v5,v3,4,v5
v_and_b32 v3,96,v2
v_lshl_add_u32 v5,v3,1,v5
v_lshrrev_b32 v3,2,v2
v_add_u32 v3,s74,v3
.L_xa:
s_cmp_lg_u32 s26,1
s_cbranch_scc1 .L_skx
s_mov_b32 s48,20000
s_branch .L_rfx
.L_rlx:
s_sleep 2
.L_rfx:
buffer_load_dwordx2 v[6:7],v0,s[0:3],s66 offen sc1
s_sub_u32 s48,s48,1
s_cbranch_scc1 .L_end
s_waitcnt vmcnt(0)
v_cmp_ne_u64 vcc,v[8:9],v[6:7]
s_cbranch_vccnz .L_rlx
.L_skx:
s_waitcnt vmcnt(0)
s_barrier
s_add_u32 s66,s66,s30
.L_xgo:
buffer_load_dwordx4 v[12:15],v2,s[32:35],s71 offen sc1
s_add_u32 s71,s71,s30
s_add_u32 m0,s64,0
s_nop 0
buffer_load_dwordx4 v2,s[32:35],s63 offen offset:0 sc1 lds
buffer_load_dwordx4 v2,s[32:35],s63 offen offset:1024 sc1 lds
buffer_load_dwordx4 v2,s[32:35],s63 offen offset:2048 sc1 lds
buffer_load_dwordx4 v2,s[32:35],s63 offen offset:3072 sc1 lds
s_add_u32 s63,s63,4096
s_add_u32 m0,s64,4096
s_nop 0
buffer_load_dwordx4 v2,s[32:35],s63 offen offset:0 sc1 lds
buffer_load_dwordx4 v2,s[32:35],s63 offen offset:1024 sc1 lds
buffer_load_dwordx4 v2,s[32:35],s63 offen offset:2048 sc1 lds
buffer_load_dwordx4 v2,s[32:35],s63 offen offset:3072 sc1 lds
s_add_u32 s63,s63,4096
s_add_u32 m0,s64,8192
s_nop 0
buffer_load_dwordx4 v2,s[32:35],s63 offen offset:0 sc1 lds
buffer_load_dwordx4 v2,s[32:35],s63 offen offset:1024 sc1 lds
buffer_load_dwordx4 v2,s[32:35],s63 offen offset:2048 sc1 lds
buffer_load_dwordx4 v2,s[32:35],s63 offen offset:3072 sc1 lds
s_add_u32 s63,s63,4096
s_add_u32 m0,s64,12288
s_nop 0
buffer_load_dwordx4 v2,s[32:35],s63 offen offset:0 sc1 lds
buffer_load_dwordx4 v2,s[32:35],s63 offen offset:1024 sc1 lds
s_add_u32 s63,s63,s31
s_waitcnt vmcnt(14)
v_lshlrev_b32 v16,16,v12
v_and_b32 v17,0xffff0000,v12
v_lshlrev_b32 v18,16,v13
v_and_b32 v19,0xffff0000,v13
v_lshlrev_b32 v20,16,v14
v_and_b32 v21,0xffff0000,v14
v_lshlrev_b32 v22,16,v15
v_and_b32 v23,0xffff0000,v15
s_waitcnt vmcnt(13)
s_barrier
ds_read_b128 v[32:35],v2 offset:0
ds_read_b128 v[36:39],v2 offset:14336
ds_read_b128 v[40:43],v2 offset:28672
ds_read_b128 v[44:47],v2 offset:43008
s_waitcnt vmcnt(11)
s_barrier
ds_read_b128 v[48:51],v2 offset:1024
ds_read_b128 v[52:55],v2 offset:15360
s_waitcnt lgkmcnt(4)
v_mfma_f32_16x16x32_bf16 v[16:19],a[0:3],v[32:35],v[16:19]
ds_read_b128 v[56:59],v2 offset:29696
v_mfma_f32_16x16x32_bf16 v[16:19],a[112:115],v[36:39],v[16:19]
v_mfma_f32_16x16x32_bf16 v[20:23],a[116:119],v[36:39],v[20:23]
ds_read_b128 v[60:63],v2 offset:44032
v_mfma_f32_16x16x32_bf16 v[20:23],a[4:7],v[32:35],v[20:23]
s_waitcnt lgkmcnt(4)
v_mfma_f32_16x16x32_bf16 v[20:23],a[228:231],v[40:43],v[20:23]
ds_read_b128 v[32:35],v2 offset:2048
v_mfma_f32_16x16x32_bf16 v[20:23],v[148:151],v[44:47],v[20:23]
v_mfma_f32_16x16x32_bf16 v[16:19],v[144:147],v[44:47],v[16:19]
ds_read_b128 v[36:39],v2 offset:16384
v_mfma_f32_16x16x32_bf16 v[16:19],a[224:227],v[40:43],v[16:19]
s_waitcnt lgkmcnt(4)
v_mfma_f32_16x16x32_bf16 v[16:19],a[8:11],v[48:51],v[16:19]
ds_read_b128 v[40:43],v2 offset:30720
v_mfma_f32_16x16x32_bf16 v[16:19],a[120:123],v[52:55],v[16:19]
v_mfma_f32_16x16x32_bf16 v[20:23],a[124:127],v[52:55],v[20:23]
ds_read_b128 v[44:47],v2 offset:45056
v_mfma_f32_16x16x32_bf16 v[20:23],a[12:15],v[48:51],v[20:23]
s_waitcnt vmcnt(9)
s_waitcnt lgkmcnt(4)
v_mfma_f32_16x16x32_bf16 v[20:23],a[236:239],v[56:59],v[20:23]
s_barrier
ds_read_b128 v[48:51],v2 offset:3072
v_mfma_f32_16x16x32_bf16 v[20:23],v[156:159],v[60:63],v[20:23]
v_mfma_f32_16x16x32_bf16 v[16:19],v[152:155],v[60:63],v[16:19]
ds_read_b128 v[52:55],v2 offset:17408
v_mfma_f32_16x16x32_bf16 v[16:19],a[232:235],v[56:59],v[16:19]
s_waitcnt lgkmcnt(4)
v_mfma_f32_16x16x32_bf16 v[16:19],a[16:19],v[32:35],v[16:19]
ds_read_b128 v[56:59],v2 offset:31744
v_mfma_f32_16x16x32_bf16 v[16:19],a[128:131],v[36:39],v[16:19]
v_mfma_f32_16x16x32_bf16 v[20:23],a[132:135],v[36:39],v[20:23]
ds_read_b128 v[60:63],v2 offset:46080
v_mfma_f32_16x16x32_bf16 v[20:23],a[20:23],v[32:35],v[20:23]
s_waitcnt lgkmcnt(4)
v_mfma_f32_16x16x32_bf16 v[20:23],a[244:247],v[40:43],v[20:23]
ds_read_b128 v[32:35],v2 offset:4096
v_mfma_f32_16x16x32_bf16 v[20:23],v[164:167],v[44:47],v[20:23]
v_mfma_f32_16x16x32_bf16 v[16:19],v[160:163],v[44:47],v[16:19]
ds_read_b128 v[36:39],v2 offset:18432
v_mfma_f32_16x16x32_bf16 v[16:19],a[240:243],v[40:43],v[16:19]
s_waitcnt lgkmcnt(4)
v_mfma_f32_16x16x32_bf16 v[16:19],a[24:27],v[48:51],v[16:19]
ds_read_b128 v[40:43],v2 offset:32768
v_mfma_f32_16x16x32_bf16 v[16:19],a[136:139],v[52:55],v[16:19]
v_mfma_f32_16x16x32_bf16 v[20:23],a[140:143],v[52:55],v[20:23]
ds_read_b128 v[44:47],v2 offset:47104
v_mfma_f32_16x16x32_bf16 v[20:23],a[28:31],v[48:51],v[20:23]
s_waitcnt vmcnt(7)
s_waitcnt lgkmcnt(4)
v_mfma_f32_16x16x32_bf16 v[20:23],a[252:255],v[56:59],v[20:23]
s_barrier
ds_read_b128 v[48:51],v2 offset:5120
v_mfma_f32_16x16x32_bf16 v[20:23],v[172:175],v[60:63],v[20:23]
v_mfma_f32_16x16x32_bf16 v[16:19],v[168:171],v[60:63],v[16:19]
ds_read_b128 v[52:55],v2 offset:19456
v_mfma_f32_16x16x32_bf16 v[16:19],a[248:251],v[56:59],v[16:19]
s_waitcnt lgkmcnt(4)
v_mfma_f32_16x16x32_bf16 v[16:19],a[32:35],v[32:35],v[16:19]
ds_read_b128 v[56:59],v2 offset:33792
v_mfma_f32_16x16x32_bf16 v[16:19],a[144:147],v[36:39],v[16:19]
v_mfma_f32_16x16x32_bf16 v[20:23],a[148:151],v[36:39],v[20:23]
ds_read_b128 v[60:63],v2 offset:48128
v_mfma_f32_16x16x32_bf16 v[20:23],a[36:39],v[32:35],v[20:23]
s_waitcnt lgkmcnt(4)
v_mfma_f32_16x16x32_bf16 v[20:23],v[68:71],v[40:43],v[20:23]
ds_read_b128 v[32:35],v2 offset:6144
v_mfma_f32_16x16x32_bf16 v[20:23],v[180:183],v[44:47],v[20:23]
v_mfma_f32_16x16x32_bf16 v[16:19],v[176:179],v[44:47],v[16:19]
ds_read_b128 v[36:39],v2 offset:20480
v_mfma_f32_16x16x32_bf16 v[16:19],v[64:67],v[40:43],v[16:19]
s_waitcnt lgkmcnt(4)
v_mfma_f32_16x16x32_bf16 v[16:19],a[40:43],v[48:51],v[16:19]
ds_read_b128 v[40:43],v2 offset:34816
v_mfma_f32_16x16x32_bf16 v[16:19],a[152:155],v[52:55],v[16:19]
v_mfma_f32_16x16x32_bf16 v[20:23],a[156:159],v[52:55],v[20:23]
ds_read_b128 v[44:47],v2 offset:49152
v_mfma_f32_16x16x32_bf16 v[20:23],a[44:47],v[48:51],v[20:23]
s_waitcnt vmcnt(5)
s_waitcnt lgkmcnt(4)
v_mfma_f32_16x16x32_bf16 v[20:23],v[76:79],v[56:59],v[20:23]
s_barrier
ds_read_b128 v[48:51],v2 offset:7168
v_mfma_f32_16x16x32_bf16 v[20:23],v[188:191],v[60:63],v[20:23]
v_mfma_f32_16x16x32_bf16 v[16:19],v[184:187],v[60:63],v[16:19]
ds_read_b128 v[52:55],v2 offset:21504
v_mfma_f32_16x16x32_bf16 v[16:19],v[72:75],v[56:59],v[16:19]
s_waitcnt lgkmcnt(4)
v_mfma_f32_16x16x32_bf16 v[16:19],a[48:51],v[32:35],v[16:19]
ds_read_b128 v[56:59],v2 offset:35840
v_mfma_f32_16x16x32_bf16 v[16:19],a[160:163],v[36:39],v[16:19]
v_mfma_f32_16x16x32_bf16 v[20:23],a[164:167],v[36:39],v[20:23]
ds_read_b128 v[60:63],v2 offset:50176
v_mfma_f32_16x16x32_bf16 v[20:23],a[52:55],v[32:35],v[20:23]
s_waitcnt lgkmcnt(4)
v_mfma_f32_16x16x32_bf16 v[20:23],v[84:87],v[40:43],v[20:23]
ds_read_b128 v[32:35],v2 offset:8192
v_mfma_f32_16x16x32_bf16 v[20:23],v[196:199],v[44:47],v[20:23]
v_mfma_f32_16x16x32_bf16 v[16:19],v[192:195],v[44:47],v[16:19]
ds_read_b128 v[36:39],v2 offset:22528
v_mfma_f32_16x16x32_bf16 v[16:19],v[80:83],v[40:43],v[16:19]
s_waitcnt lgkmcnt(4)
v_mfma_f32_16x16x32_bf16 v[16:19],a[56:59],v[48:51],v[16:19]
ds_read_b128 v[40:43],v2 offset:36864
v_mfma_f32_16x16x32_bf16 v[16:19],a[168:171],v[52:55],v[16:19]
v_mfma_f32_16x16x32_bf16 v[20:23],a[172:175],v[52:55],v[20:23]
ds_read_b128 v[44:47],v2 offset:51200
v_mfma_f32_16x16x32_bf16 v[20:23],a[60:63],v[48:51],v[20:23]
s_waitcnt vmcnt(3)
s_waitcnt lgkmcnt(4)
v_mfma_f32_16x16x32_bf16 v[20:23],v[92:95],v[56:59],v[20:23]
s_barrier
ds_read_b128 v[48:51],v2 offset:9216
v_mfma_f32_16x16x32_bf16 v[20:23],v[204:207],v[60:63],v[20:23]
v_mfma_f32_16x16x32_bf16 v[16:19],v[200:203],v[60:63],v[16:19]
ds_read_b128 v[52:55],v2 offset:23552
v_mfma_f32_16x16x32_bf16 v[16:19],v[88:91],v[56:59],v[16:19]
s_waitcnt lgkmcnt(4)
v_mfma_f32_16x16x32_bf16 v[16:19],a[64:67],v[32:35],v[16:19]
ds_read_b128 v[56:59],v2 offset:37888
v_mfma_f32_16x16x32_bf16 v[16:19],a[176:179],v[36:39],v[16:19]
v_mfma_f32_16x16x32_bf16 v[20:23],a[180:183],v[36:39],v[20:23]
ds_read_b128 v[60:63],v2 offset:52224
v_mfma_f32_16x16x32_bf16 v[20:23],a[68:71],v[32:35],v[20:23]
s_waitcnt lgkmcnt(4)
v_mfma_f32_16x16x32_bf16 v[20:23],v[100:103],v[40:43],v[20:23]
ds_read_b128 v[32:35],v2 offset:10240
v_mfma_f32_16x16x32_bf16 v[20:23],v[212:215],v[44:47],v[20:23]
v_mfma_f32_16x16x32_bf16 v[16:19],v[208:211],v[44:47],v[16:19]
ds_read_b128 v[36:39],v2 offset:24576
v_mfma_f32_16x16x32_bf16 v[16:19],v[96:99],v[40:43],v[16:19]
s_waitcnt lgkmcnt(4)
v_mfma_f32_16x16x32_bf16 v[16:19],a[72:75],v[48:51],v[16:19]
ds_read_b128 v[40:43],v2 offset:38912
v_mfma_f32_16x16x32_bf16 v[16:19],a[184:187],v[52:55],v[16:19]
v_mfma_f32_16x16x32_bf16 v[20:23],a[188:191],v[52:55],v[20:23]
ds_read_b128 v[44:47],v2 offset:53248
v_mfma_f32_16x16x32_bf16 v[20:23],a[76:79],v[48:51],v[20:23]
s_waitcnt vmcnt(1)
s_waitcnt lgkmcnt(4)
v_mfma_f32_16x16x32_bf16 v[20:23],v[108:111],v[56:59],v[20:23]
s_barrier
ds_read_b128 v[48:51],v2 offset:11264
v_mfma_f32_16x16x32_bf16 v[20:23],v[220:223],v[60:63],v[20:23]
v_mfma_f32_16x16x32_bf16 v[16:19],v[216:219],v[60:63],v[16:19]
ds_read_b128 v[52:55],v2 offset:25600
v_mfma_f32_16x16x32_bf16 v[16:19],v[104:107],v[56:59],v[16:19]
s_waitcnt lgkmcnt(4)
v_mfma_f32_16x16x32_bf16 v[16:19],a[80:83],v[32:35],v[16:19]
ds_read_b128 v[56:59],v2 offset:39936
v_mfma_f32_16x16x32_bf16 v[16:19],a[192:195],v[36:39],v[16:19]
v_mfma_f32_16x16x32_bf16 v[20:23],a[196:199],v[36:39],v[20:23]
ds_read_b128 v[60:63],v2 offset:54272
v_mfma_f32_16x16x32_bf16 v[20:23],a[84:87],v[32:35],v[20:23]
s_waitcnt lgkmcnt(4)
v_mfma_f32_16x16x32_bf16 v[20:23],v[116:119],v[40:43],v[20:23]
ds_read_b128 v[32:35],v2 offset:12288
v_mfma_f32_16x16x32_bf16 v[20:23],v[228:231],v[44:47],v[20:23]
v_mfma_f32_16x16x32_bf16 v[16:19],v[224:227],v[44:47],v[16:19]
ds_read_b128 v[36:39],v2 offset:26624
v_mfma_f32_16x16x32_bf16 v[16:19],v[112:115],v[40:43],v[16:19]
s_waitcnt lgkmcnt(4)
v_mfma_f32_16x16x32_bf16 v[16:19],a[88:91],v[48:51],v[16:19]
ds_read_b128 v[40:43],v2 offset:40960
v_mfma_f32_16x16x32_bf16 v[16:19],a[200:203],v[52:55],v[16:19]
v_mfma_f32_16x16x32_bf16 v[20:23],a[204:207],v[52:55],v[20:23]
ds_read_b128 v[44:47],v2 offset:55296
v_mfma_f32_16x16x32_bf16 v[20:23],a[92:95],v[48:51],v[20:23]
s_waitcnt vmcnt(0)
s_waitcnt lgkmcnt(4)
v_mfma_f32_16x16x32_bf16 v[20:23],v[124:127],v[56:59],v[20:23]
s_barrier
ds_read_b128 v[48:51],v2 offset:13312
v_mfma_f32_16x16x32_bf16 v[20:23],v[236:239],v[60:63],v[20:23]
v_mfma_f32_16x16x32_bf16 v[16:19],v[232:235],v[60:63],v[16:19]
ds_read_b128 v[52:55],v2 offset:27648
v_mfma_f32_16x16x32_bf16 v[16:19],v[120:123],v[56:59],v[16:19]
s_waitcnt lgkmcnt(4)
v_mfma_f32_16x16x32_bf16 v[16:19],a[96:99],v[32:35],v[16:19]
ds_read_b128 v[56:59],v2 offset:41984
v_mfma_f32_16x16x32_bf16 v[16:19],a[208:211],v[36:39],v[16:19]
v_mfma_f32_16x16x32_bf16 v[20:23],a[212:215],v[36:39],v[20:23]
ds_read_b128 v[60:63],v2 offset:56320
v_mfma_f32_16x16x32_bf16 v[20:23],a[100:103],v[32:35],v[20:23]
s_waitcnt lgkmcnt(4)
v_mfma_f32_16x16x32_bf16 v[20:23],v[132:135],v[40:43],v[20:23]
v_mfma_f32_16x16x32_bf16 v[20:23],v[244:247],v[44:47],v[20:23]
v_mfma_f32_16x16x32_bf16 v[16:19],v[240:243],v[44:47],v[16:19]
v_mfma_f32_16x16x32_bf16 v[16:19],v[128:131],v[40:43],v[16:19]
s_waitcnt lgkmcnt(2)
v_mfma_f32_16x16x32_bf16 v[16:19],a[104:107],v[48:51],v[16:19]
v_mfma_f32_16x16x32_bf16 v[16:19],a[216:219],v[52:55],v[16:19]
v_mfma_f32_16x16x32_bf16 v[20:23],a[220:223],v[52:55],v[20:23]
v_mfma_f32_16x16x32_bf16 v[20:23],a[108:111],v[48:51],v[20:23]
s_waitcnt lgkmcnt(0)
v_mfma_f32_16x16x32_bf16 v[20:23],v[140:143],v[56:59],v[20:23]
v_mfma_f32_16x16x32_bf16 v[20:23],v[252:255],v[60:63],v[20:23]
v_mfma_f32_16x16x32_bf16 v[16:19],v[248:251],v[60:63],v[16:19]
v_mfma_f32_16x16x32_bf16 v[16:19],v[136:139],v[56:59],v[16:19]
s_nop 1
s_nop 3
v_cvt_pk_bf16_f32 v15,v22,v23
v_cvt_pk_bf16_f32 v14,v20,v21
v_cvt_pk_bf16_f32 v13,v18,v19
v_cvt_pk_bf16_f32 v12,v16,v17
ds_write_b128 v5,v[12:15]
ds_read2st64_b32 v[12:13],v3 offset0:0 offset1:1
ds_read2st64_b32 v[14:15],v3 offset0:2 offset1:3
s_waitcnt lgkmcnt(0)
buffer_atomic_pk_add_bf16 v12,v1,s[44:47],s65 offen offset:0 sc1
s_add_u32 s70,s70,s75
buffer_atomic_pk_add_bf16 v13,v1,s[44:47],s65 offen offset:2048 sc1
s_add_u32 s65,s65,16384
buffer_atomic_pk_add_bf16 v14,v1,s[44:47],s65 offen offset:0 sc1
buffer_atomic_pk_add_bf16 v15,v1,s[44:47],s65 offen offset:2048 sc1
s_and_b32 s65,s70,0x3f8000
s_sub_u32 s62,s62,1
s_cbranch_scc1 .L_end
s_cmp_lg_u32 s62,0
s_cbranch_scc1 .L_xa
s_add_u32 s66,s66,32071680
s_add_u32 s63,s63,32071680
s_add_u32 s71,s71,32071680
s_add_u32 s70,s70,884736
s_mov_b32 s65,s70
s_branch .L_xa
.L_last:
)#"
    : "+{v0}"(tid)
    : "{s[8:9]}"(x), "{s[10:11]}"(wq), "{s[12:13]}"(wo), "{s[14:15]}"(wg), "{s[16:17]}"(wu), "{s[18:19]}"(wd),
      "{s[20:21]}"(out), "{s[22:23]}"(ws), "{s24}"(blockIdx.x)
    : "v1", "v2", "v3", "v4", "v5", "v6", "v7", "v8", "v9", "v10", "v11", "v12", "v13", "v14", "v15", "v16", "v17", "v18", "v19", "v20", "v21", "v22", "v23", "v24", "v25", "v26", "v27", "v28", "v29", "v30", "v31", "v32", "v33", "v34", "v35", "v36", "v37", "v38", "v39", "v40", "v41", "v42", "v43", "v44", "v45", "v46", "v47", "v48", "v49", "v50", "v51", "v52", "v53", "v54", "v55", "v56", "v57", "v58", "v59", "v60", "v61", "v62", "v63", "v64", "v65", "v66", "v67", "v68", "v69", "v70", "v71", "v72", "v73", "v74", "v75", "v76", "v77", "v78", "v79", "v80", "v81", "v82", "v83", "v84", "v85", "v86", "v87", "v88", "v89", "v90", "v91", "v92", "v93", "v94", "v95", "v96", "v97", "v98", "v99", "v100", "v101", "v102", "v103", "v104", "v105", "v106", "v107", "v108", "v109", "v110", "v111", "v112", "v113", "v114", "v115", "v116", "v117", "v118", "v119", "v120", "v121", "v122", "v123", "v124", "v125", "v126", "v127", "v128", "v129", "v130", "v131", "v132", "v133", "v134", "v135", "v136", "v137", "v138", "v139", "v140", "v141", "v142", "v143", "v144", "v145", "v146", "v147", "v148", "v149", "v150", "v151", "v152", "v153", "v154", "v155", "v156", "v157", "v158", "v159", "v160", "v161", "v162", "v163", "v164", "v165", "v166", "v167", "v168", "v169", "v170", "v171", "v172", "v173", "v174", "v175", "v176", "v177", "v178", "v179", "v180", "v181", "v182", "v183", "v184", "v185", "v186", "v187", "v188", "v189", "v190", "v191", "v192", "v193", "v194", "v195", "v196", "v197", "v198", "v199", "v200", "v201", "v202", "v203", "v204", "v205", "v206", "v207", "v208", "v209", "v210", "v211", "v212", "v213", "v214", "v215", "v216", "v217", "v218", "v219", "v220", "v221", "v222", "v223", "v224", "v225", "v226", "v227", "v228", "v229", "v230", "v231", "v232", "v233", "v234", "v235", "v236", "v237", "v238", "v239", "v240", "v241", "v242", "v243", "v244", "v245", "v246", "v247", "v248", "v249", "v250", "v251", "v252", "v253", "v254", "v255", "a0", "a1", "a2", "a3", "a4", "a5", "a6", "a7", "a8", "a9", "a10", "a11", "a12", "a13", "a14", "a15", "a16", "a17", "a18", "a19", "a20", "a21", "a22", "a23", "a24", "a25", "a26", "a27", "a28", "a29", "a30", "a31", "a32", "a33", "a34", "a35", "a36", "a37", "a38", "a39", "a40", "a41", "a42", "a43", "a44", "a45", "a46", "a47", "a48", "a49", "a50", "a51", "a52", "a53", "a54", "a55", "a56", "a57", "a58", "a59", "a60", "a61", "a62", "a63", "a64", "a65", "a66", "a67", "a68", "a69", "a70", "a71", "a72", "a73", "a74", "a75", "a76", "a77", "a78", "a79", "a80", "a81", "a82", "a83", "a84", "a85", "a86", "a87", "a88", "a89", "a90", "a91", "a92", "a93", "a94", "a95", "a96", "a97", "a98", "a99", "a100", "a101", "a102", "a103", "a104", "a105", "a106", "a107", "a108", "a109", "a110", "a111", "a112", "a113", "a114", "a115", "a116", "a117", "a118", "a119", "a120", "a121", "a122", "a123", "a124", "a125", "a126", "a127", "a128", "a129", "a130", "a131", "a132", "a133", "a134", "a135", "a136", "a137", "a138", "a139", "a140", "a141", "a142", "a143", "a144", "a145", "a146", "a147", "a148", "a149", "a150", "a151", "a152", "a153", "a154", "a155", "a156", "a157", "a158", "a159", "a160", "a161", "a162", "a163", "a164", "a165", "a166", "a167", "a168", "a169", "a170", "a171", "a172", "a173", "a174", "a175", "a176", "a177", "a178", "a179", "a180", "a181", "a182", "a183", "a184", "a185", "a186", "a187", "a188", "a189", "a190", "a191", "a192", "a193", "a194", "a195", "a196", "a197", "a198", "a199", "a200", "a201", "a202", "a203", "a204", "a205", "a206", "a207", "a208", "a209", "a210", "a211", "a212", "a213", "a214", "a215", "a216", "a217", "a218", "a219", "a220", "a221", "a222", "a223", "a224", "a225", "a226", "a227", "a228", "a229", "a230", "a231", "a232", "a233", "a234", "a235", "a236", "a237", "a238", "a239", "a240", "a241", "a242", "a243", "a244", "a245", "a246", "a247", "a248", "a249", "a250", "a251", "a252", "a253", "a254", "a255", "s25", "s26", "s27", "s28", "s29", "s30", "s31", "s32", "s33", "s34", "s35", "s36", "s37", "s38", "s39", "s40", "s41", "s42", "s43", "s44", "s45", "s46", "s47", "s48", "s49", "s50", "s51", "s52", "s53", "s54", "s55", "s56", "s57", "s58", "s59", "s60", "s61", "s62", "s63", "s64", "s65", "s66", "s67", "s68", "s69", "s70", "s71", "s72", "s73", "s74", "s75", "s76", "s77", "s78", "s79", "s80", "s81", "s82", "s83", "s84", "s85", "s86", "s87", "s88", "s89", "s90", "s91", "s92", "s93", "s94", "s95", "s96", "s97", "s98", "s99", "vcc", "scc", "m0", "memory");
}

extern "C" void kernel_launch(void* const* d_in, const int* in_sizes, int n_in, void* d_out, int out_size,
                              void* d_ws, size_t ws_size, hipStream_t stream) {
    constexpr int smem = 163840;
    (void)hipFuncSetAttribute((const void*)body_k, hipFuncAttributeMaxDynamicSharedMemorySize, smem);
    body_k<<<256, 256, smem, stream>>>(d_in[0], d_in[1], d_in[2], d_in[4], d_in[5], d_in[6], d_out, d_ws);
}
```

```cpp
#include <hip/hip_runtime.h>
#pragma clang diagnostic ignored "-Winline-asm"
#define W_XI 4612096
#define W_O 4648960
#define W_XA 4681728
#define W_H 4718592
#define W_P 4833280
#define W_EB 296960
#define W_HK 0
#define W_SIDE 276
#define W_HL 3
#define W_GS 0
#define W_MAP 1
extern "C" __global__ __launch_bounds__(256, 1) void body_k(const void* x, const void* wq, const void* wo, const void* wg, const void* wu,
                                                            const void* wd, void* out, void* ws) {
    unsigned tid = threadIdx.x;
    asm volatile(
R"#(
s_load_dwordx2 s[52:53],s[22:23],0x0
v_lshrrev_b32 v3,6,v0
v_and_b32 v1,63,v0
v_readfirstlane_b32 s26,v3
v_lshlrev_b32 v2,4,v1
s_add_u32 s32,s22,0x0
s_addc_u32 s33,s23,0x0
s_and_b32 s33,s33,0xffff
s_mov_b32 s34,0x4ce6000
s_mov_b32 s35,0x20000
s_mov_b64 s[0:1],s[32:33]
s_mov_b32 s2,84197376
s_mov_b32 s3,0x20000
s_mov_b32 s73,0
s_mov_b32 s25,0
s_lshr_b32 s49,s24,3
s_cmp_lt_u32 s49,16
s_cbranch_scc1 .L_nc
s_bfe_u32 s28,s24,0x20001
s_and_b32 s27,s49,7
s_bfe_u32 s29,s49,0x10003
s_mov_b32 s62,0
s_mov_b32 s74,1
s_bitcmp1_b32 s24,0
s_cbranch_scc0 .L_nt
s_getpc_b64 s[40:41]
.L_ct16:
s_and_b32 s41,s41,0xffff
s_mov_b32 s42,.L_last-.L_ct16
s_mov_b32 s43,0x20000
s_sub_u32 s48,s49,16
s_lshl_b32 s48,s48,8
v_add_lshl_u32 v5,v0,s48,7
buffer_load_dword v10,v5,s[40:43],0 offen
s_add_u32 s36,s10,0x0
s_addc_u32 s37,s11,0x0
s_and_b32 s37,s37,0xffff
s_mov_b32 s38,0x600000
s_mov_b32 s39,0x20000
s_lshl_b32 s48,s49,8
s_sub_u32 s48,s48,4096
v_add_u32 v4,s48,v0
s_lshl_b32 s48,s28,9
s_add_u32 s44,s8,0x0
s_addc_u32 s45,s9,0x0
s_and_b32 s45,s45,0xffff
s_mov_b32 s46,0x400000
s_mov_b32 s47,0x20000
v_lshrrev_b32 v5,10,v4
v_bfe_u32 v6,v4,1,9
v_lshl_add_u32 v5,v5,13,v6
v_subrev_u32 v5,512,v5
v_and_b32 v5,0x7fff,v5
v_lshlrev_b32 v5,7,v5
buffer_load_dword v10,v5,s[44:47],0 offen
v_mul_u32_u24 v5,43691,v4
v_lshrrev_b32 v5,19,v5
v_mul_u32_u24 v6,12,v5
v_sub_u32 v6,v4,v6
v_mul_u32_u24 v5,48,v5
v_lshrrev_b32 v7,2,v6
v_mul_u32_u24 v7,12,v7
v_add3_u32 v5,v5,v6,v7
v_lshlrev_b32 v5,7,v5
v_add_u32 v4,4096,v4
buffer_load_dword v10,v5,s[36:39],s48 offen
v_mul_u32_u24 v5,43691,v4
v_lshrrev_b32 v5,19,v5
v_mul_u32_u24 v6,12,v5
v_sub_u32 v6,v4,v6
v_mul_u32_u24 v5,48,v5
v_lshrrev_b32 v7,2,v6
v_mul_u32_u24 v7,12,v7
v_add3_u32 v5,v5,v6,v7
v_lshlrev_b32 v5,7,v5
v_add_u32 v4,4096,v4
buffer_load_dword v10,v5,s[36:39],s48 offen
v_mul_u32_u24 v5,43691,v4
v_lshrrev_b32 v5,19,v5
v_mul_u32_u24 v6,12,v5
v_sub_u32 v6,v4,v6
v_mul_u32_u24 v5,48,v5
v_lshrrev_b32 v7,2,v6
v_mul_u32_u24 v7,12,v7
v_add3_u32 v5,v5,v6,v7
v_lshlrev_b32 v5,7,v5
v_add_u32 v4,4096,v4
buffer_load_dword v10,v5,s[36:39],s48 offen
s_mov_b32 s62,1
s_mov_b32 s74,4
.L_nt:
s_lshl2_add_u32 s50,s27,s26
s_mul_i32 s70,s50,1152
v_and_b32 v20,15,v1
v_lshlrev_b32 v20,11,v20
v_and_b32 v21,48,v1
s_lshl_b32 s48,s50,6
v_add3_u32 v20,v20,v21,s48
s_add_u32 s36,s8,0x0
s_addc_u32 s37,s9,0x0
s_and_b32 s37,s37,0xffff
s_mov_b32 s38,0x400000
s_mov_b32 s39,0x20000
s_lshl2_add_u32 s48,s28,s62
s_lshl_b32 s48,s48,3
s_sub_u32 s48,s48,s29
s_add_u32 s48,s48,127
s_lshl_b32 s66,s27,7
s_add_u32 s66,s66,286720
s_sub_u32 s66,s66,s70
s_add_u32 s49,s28,3
s_and_b32 s49,s49,3
s_mul_i32 s49,s49,296960
s_add_u32 s69,s70,s49
s_add_u32 s69,s69,41435136
s_mul_i32 s49,s28,296960
s_add_u32 s70,s70,s49
s_and_b32 s51,s50,6
s_lshl_b32 s51,s51,1
v_add_u32 v3,s51,v1
v_bfi_b32 v3,15,v3,v1
v_lshlrev_b32 v3,2,v3
v_cmp_gt_u32 vcc,32,v1
v_mov_b32 v21,0x7fff0000
s_nop 0
v_cndmask_b32 v21,v21,v3,vcc
s_mul_i32 s49,s29,1187840
s_sub_u32 s71,s70,s49
s_add_u32 s71,s71,4612096
v_mul_u32_u24 v24,2375680,v1
v_add_u32 v24,s71,v24
s_cmp_eq_u32 s29,0
s_cselect_b32 s68,s71,s69
v_writelane_b32 v24,s68,0
s_mul_i32 s49,s62,9502720
s_add_u32 s71,s71,s49
s_cmp_eq_u32 s62,0
s_cselect_b32 s68,s68,s71
s_add_u32 s71,s71,2375680
v_lshrrev_b32 v22,2,v0
v_mov_b32 v23,0x3f803f80
.L_dc:
s_and_b32 s49,s48,127
s_lshl_b32 s49,s49,15
s_add_u32 s48,s48,2
buffer_load_dwordx4 v[32:35],v20,s[36:39],s49 offen
s_and_b32 s49,s48,127
s_lshl_b32 s49,s49,15
s_add_u32 s48,s48,2
buffer_load_dwordx4 v[36:39],v20,s[36:39],s49 offen
s_and_b32 s49,s48,127
s_lshl_b32 s49,s49,15
s_add_u32 s48,s48,2
buffer_load_dwordx4 v[40:43],v20,s[36:39],s49 offen
s_and_b32 s49,s48,127
s_lshl_b32 s49,s49,15
s_add_u32 s48,s48,2
buffer_load_dwordx4 v[44:47],v20,s[36:39],s49 offen
v_cmp_eq_u32 vcc,s62,v22
v_mov_b32 v25,0x7fff0000
s_add_u32 s62,s62,1
v_cndmask_b32 v25,v25,v24,vcc
s_waitcnt lgkmcnt(0)
s_add_u32 s54,s52,0x7f4a7c15
s_addc_u32 s55,s53,0x9e3779b9
v_mov_b32 v8,s54
v_mov_b32 v9,s55
s_waitcnt vmcnt(0)
v_dot2_f32_bf16 v48,v32,v23,0
v_dot2_f32_bf16 v49,v32,v32,0
v_dot2_f32_bf16 v50,v36,v23,0
v_dot2_f32_bf16 v51,v36,v36,0
v_dot2_f32_bf16 v52,v40,v23,0
v_dot2_f32_bf16 v53,v40,v40,0
v_dot2_f32_bf16 v54,v44,v23,0
v_dot2_f32_bf16 v55,v44,v44,0
v_dot2_f32_bf16 v48,v33,v23,v48
v_dot2_f32_bf16 v49,v33,v33,v49
v_dot2_f32_bf16 v50,v37,v23,v50
v_dot2_f32_bf16 v51,v37,v37,v51
v_dot2_f32_bf16 v52,v41,v23,v52
v_dot2_f32_bf16 v53,v41,v41,v53
v_dot2_f32_bf16 v54,v45,v23,v54
v_dot2_f32_bf16 v55,v45,v45,v55
v_dot2_f32_bf16 v48,v34,v23,v48
v_dot2_f32_bf16 v49,v34,v34,v49
v_dot2_f32_bf16 v50,v38,v23,v50
v_dot2_f32_bf16 v51,v38,v38,v51
v_dot2_f32_bf16 v52,v42,v23,v52
v_dot2_f32_bf16 v53,v42,v42,v53
v_dot2_f32_bf16 v54,v46,v23,v54
v_dot2_f32_bf16 v55,v46,v46,v55
v_dot2_f32_bf16 v48,v35,v23,v48
v_dot2_f32_bf16 v49,v35,v35,v49
v_dot2_f32_bf16 v50,v39,v23,v50
v_dot2_f32_bf16 v51,v39,v39,v51
v_dot2_f32_bf16 v52,v43,v23,v52
v_dot2_f32_bf16 v53,v43,v43,v53
v_dot2_f32_bf16 v54,v47,v23,v54
v_dot2_f32_bf16 v55,v47,v47,v55
v_permlane16_swap_b32 v48,v49
v_permlane16_swap_b32 v50,v51
v_permlane16_swap_b32 v52,v53
v_permlane16_swap_b32 v54,v55
v_add_f32 v48,v48,v49
v_add_f32 v50,v50,v51
v_add_f32 v52,v52,v53
v_add_f32 v54,v54,v55
v_permlane32_swap_b32 v48,v49
v_permlane32_swap_b32 v50,v51
v_permlane32_swap_b32 v52,v53
v_permlane32_swap_b32 v54,v55
v_add_f32 v48,v48,v49
v_add_f32 v50,v50,v51
v_add_f32 v52,v52,v53
v_add_f32 v54,v54,v55
buffer_store_dwordx4 v[32:35],v2,s[32:35],s68 offen sc1
buffer_store_dword v48,v21,s[32:35],s68 offen offset:1024 sc1
s_mov_b32 s68,s71
s_add_u32 s71,s71,2375680
buffer_store_dwordx4 v[36:39],v2,s[32:35],s68 offen sc1
buffer_store_dword v50,v21,s[32:35],s68 offen offset:1024 sc1
s_mov_b32 s68,s71
s_add_u32 s71,s71,2375680
buffer_store_dwordx4 v[40:43],v2,s[32:35],s68 offen sc1
buffer_store_dword v52,v21,s[32:35],s68 offen offset:1024 sc1
s_mov_b32 s68,s71
s_add_u32 s71,s71,2375680
buffer_store_dwordx4 v[44:47],v2,s[32:35],s68 offen sc1
buffer_store_dword v54,v21,s[32:35],s68 offen offset:1024 sc1
s_mov_b32 s68,s71
s_add_u32 s71,s71,2375680
s_waitcnt vmcnt(0)
s_barrier
buffer_store_dwordx2 v[8:9],v25,s[32:35],s66 offen sc1
s_cmp_lt_u32 s62,s74
s_cbranch_scc1 .L_dc
s_bitcmp1_b32 s24,0
s_cbranch_scc1 .L_d
s_lshr_b32 s49,s24,3
.L_nc:
s_bfe_u32 s50,s24,0x20001
s_and_b32 s27,s49,3
s_lshl2_add_u32 s27,s50,s27
s_lshr_b32 s28,s49,2
s_bitcmp1_b32 s24,0
s_cbranch_scc1 .L_q
s_and_b32 s27,s49,1
s_lshl1_add_u32 s27,s50,s27
s_lshr_b32 s28,s49,1
s_cmp_lt_u32 s49,4
s_cbranch_scc1 .L_w
s_cmp_ge_u32 s49,16
s_cbranch_scc1 .L_ns
s_getpc_b64 s[40:41]
.L_ct5:
s_and_b32 s41,s41,0xffff
s_mov_b32 s42,.L_last-.L_ct5
s_mov_b32 s43,0x20000
s_sub_u32 s48,s49,5
s_lshl_b32 s48,s48,8
v_add_lshl_u32 v5,v0,s48,7
buffer_load_dword v120,v5,s[40:43],0 offen
s_add_u32 s36,s12,0x0
s_addc_u32 s37,s13,0x0
s_and_b32 s37,s37,0xffff
s_mov_b32 s38,0x200000
s_mov_b32 s39,0x20000
s_sub_u32 s48,s49,4
s_lshl_b32 s48,s48,17
v_lshrrev_b32 v5,2,v0
v_and_b32 v6,3,v0
v_lshl_add_u32 v5,v5,4,v6
v_lshl_add_u32 v5,v5,7,s48
s_lshl_b32 s48,s50,9
v_add_u32 v6,1572864,v5
buffer_load_dword v120,v5,s[36:39],s48 offen
buffer_load_dword v120,v6,s[36:39],s48 offen
s_lshl2_add_u32 s48,s49,s50
s_lshl_b32 s48,s48,8
s_sub_u32 s48,s48,4096
v_add_u32 v4,s48,v0
v_mul_u32_u24 v5,52429,v4
v_lshrrev_b32 v5,22,v5
v_mul_u32_u24 v5,2240,v5
v_add_u32 v5,v5,v4
v_lshlrev_b32 v5,7,v5
v_add_u32 v5,4898816,v5
v_add_u32 v4,12288,v4
buffer_load_dword v120,v5,s[0:3],0 offen sc1
v_mul_u32_u24 v5,52429,v4
v_lshrrev_b32 v5,22,v5
v_mul_u32_u24 v5,2240,v5
v_add_u32 v5,v5,v4
v_lshlrev_b32 v5,7,v5
v_add_u32 v5,4898816,v5
v_add_u32 v4,12288,v4
buffer_load_dword v120,v5,s[0:3],0 offen sc1
s_sleep 10
.L_ns:
s_lshr_b32 s27,s50,1
s_and_b32 s28,s50,1
s_cmp_eq_u32 s49,4
s_cbranch_scc1 .L_h
s_sub_u32 s27,s49,14
s_lshr_b32 s27,s27,1
s_and_b32 s28,s49,1
s_add_u32 s28,s28,1
s_sub_u32 s48,s49,5
s_cmp_lt_u32 s49,14
s_cselect_b32 s27,s48,s27
s_cselect_b32 s28,0,s28
s_mul_i32 s50,s50,9
s_add_u32 s27,s27,s50
.L_g:
v_mov_b32 v16,0x3f803f80
v_mov_b32 v17,0x3f803f80
v_mov_b32 v18,0x3f803f80
v_mov_b32 v19,0x3f803f80
s_add_u32 s36,s14,0x0
s_addc_u32 s37,s15,0x0
s_and_b32 s37,s37,0xffff
s_mov_b32 s38,0x700000
s_mov_b32 s39,0x20000
s_add_u32 s40,s16,0x0
s_addc_u32 s41,s17,0x0
s_and_b32 s41,s41,0xffff
s_mov_b32 s42,0x700000
s_mov_b32 s43,0x20000
v_add_u32 v8,0,v1
v_mul_u32_u24 v9,5462,v8
v_lshrrev_b32 v9,16,v9
v_mul_u32_u24 v10,12,v9
v_sub_u32 v10,v8,v10
v_mul_u32_u24 v9,7168,v9
v_lshl_add_u32 v9,v10,4,v9
v_subrev_u32 v4,0,v9
v_add_u32 v8,64,v1
v_mul_u32_u24 v9,5462,v8
v_lshrrev_b32 v9,16,v9
v_mul_u32_u24 v10,12,v9
v_sub_u32 v10,v8,v10
v_mul_u32_u24 v9,7168,v9
v_lshl_add_u32 v9,v10,4,v9
v_subrev_u32 v5,1024,v9
v_add_u32 v8,128,v1
v_mul_u32_u24 v9,5462,v8
v_lshrrev_b32 v9,16,v9
v_mul_u32_u24 v10,12,v9
v_sub_u32 v10,v8,v10
v_mul_u32_u24 v9,7168,v9
v_lshl_add_u32 v9,v10,4,v9
v_subrev_u32 v6,2048,v9
s_mul_i32 s48,s26,114688
s_mul_i32 s49,s27,192
s_min_u32 s49,s49,6976
s_add_u32 s63,s48,s49
s_mul_i32 s64,s26,3072
s_mul_i32 s48,s28,11
s_lshr_b32 s48,s48,1
s_mul_i32 s49,s48,458752
s_add_u32 s63,s63,s49
s_and_b32 s49,s48,3
s_mul_i32 s49,s49,24576
s_add_u32 s49,s49,s64
s_add_u32 s48,s48,1
s_add_u32 m0,s49,0
s_nop 0
buffer_load_dwordx4 v4,s[36:39],s63 offen offset:0 lds
buffer_load_dwordx4 v5,s[36:39],s63 offen offset:1024 lds
buffer_load_dwordx4 v6,s[36:39],s63 offen offset:2048 lds
s_add_u32 m0,s49,12288
s_nop 0
buffer_load_dwordx4 v4,s[40:43],s63 offen offset:0 lds
buffer_load_dwordx4 v5,s[40:43],s63 offen offset:1024 lds
buffer_load_dwordx4 v6,s[40:43],s63 offen offset:2048 lds
s_add_u32 s63,s63,458752
s_and_b32 s49,s48,3
s_mul_i32 s49,s49,24576
s_add_u32 s49,s49,s64
s_add_u32 s48,s48,1
s_add_u32 m0,s49,0
s_nop 0
buffer_load_dwordx4 v4,s[36:39],s63 offen offset:0 lds
buffer_load_dwordx4 v5,s[36:39],s63 offen offset:1024 lds
buffer_load_dwordx4 v6,s[36:39],s63 offen offset:2048 lds
s_add_u32 m0,s49,12288
s_nop 0
buffer_load_dwordx4 v4,s[40:43],s63 offen offset:0 lds
buffer_load_dwordx4 v5,s[40:43],s63 offen offset:1024 lds
buffer_load_dwordx4 v6,s[40:43],s63 offen offset:2048 lds
s_add_u32 s63,s63,458752
s_and_b32 s49,s48,3
s_mul_i32 s49,s49,24576
s_add_u32 s49,s49,s64
s_add_u32 s48,s48,1
s_add_u32 m0,s49,0
s_nop 0
buffer_load_dwordx4 v4,s[36:39],s63 offen offset:0 lds
buffer_load_dwordx4 v5,s[36:39],s63 offen offset:1024 lds
buffer_load_dwordx4 v6,s[36:39],s63 offen offset:2048 lds
s_add_u32 m0,s49,12288
s_nop 0
buffer_load_dwordx4 v4,s[40:43],s63 offen offset:0 lds
buffer_load_dwordx4 v5,s[40:43],s63 offen offset:1024 lds
buffer_load_dwordx4 v6,s[40:43],s63 offen offset:2048 lds
s_add_u32 s63,s63,458752
v_mov_b32 v20,0
v_mov_b32 v21,0
v_mov_b32 v22,0
v_mov_b32 v23,0
v_mov_b32 v24,0
v_mov_b32 v25,0
v_mov_b32 v26,0
v_mov_b32 v27,0
v_mov_b32 v28,0
v_mov_b32 v29,0
v_mov_b32 v30,0
v_mov_b32 v31,0
v_and_b32 v12,15,v1
v_lshrrev_b32 v13,4,v1
v_lshrrev_b32 v14,2,v12
v_and_b32 v15,3,v12
v_lshl_add_u32 v13,v13,3,v14
v_mul_u32_u24 v13,192,v13
s_mul_i32 s48,s26,48
v_add_u32 v13,s48,v13
v_and_b32 v14,1,v15
v_lshrrev_b32 v15,1,v15
v_lshl_add_u32 v13,v14,3,v13
v_lshl_add_u32 v8,v15,5,v13
v_mul_u32_u24 v15,12288,v15
v_add3_u32 v9,v13,v15,16
v_add_u32 v10,49152,v8
v_add_u32 v11,49152,v9
v_add_u32 v12,98304,v8
v_add_u32 v13,98304,v9
s_cmp_eq_u32 s28,1
s_cbranch_scc1 .L_gw5
s_cmp_eq_u32 s28,2
s_cbranch_scc1 .L_gw11
.L_gw0:
s_waitcnt vmcnt(12)
s_barrier
ds_read_b64_tr_b16 a[0:1],v8 offset:0
ds_read_b64_tr_b16 a[2:3],v8 offset:768
ds_read_b64_tr_b16 a[4:5],v8 offset:12288
ds_read_b64_tr_b16 a[6:7],v8 offset:13056
ds_read_b64_tr_b16 a[8:9],v9 offset:0
ds_read_b64_tr_b16 a[10:11],v9 offset:768
ds_read_b64_tr_b16 a[12:13],v8 offset:6144
ds_read_b64_tr_b16 a[14:15],v8 offset:6912
ds_read_b64_tr_b16 a[16:17],v8 offset:18432
ds_read_b64_tr_b16 a[18:19],v8 offset:19200
ds_read_b64_tr_b16 a[20:21],v9 offset:6144
ds_read_b64_tr_b16 a[22:23],v9 offset:6912
s_add_u32 m0,s64,73728
s_nop 0
buffer_load_dwordx4 v4,s[36:39],s63 offen offset:0 lds
buffer_load_dwordx4 v5,s[36:39],s63 offen offset:1024 lds
buffer_load_dwordx4 v6,s[36:39],s63 offen offset:2048 lds
s_add_u32 m0,s64,86016
s_nop 0
buffer_load_dwordx4 v4,s[40:43],s63 offen offset:0 lds
buffer_load_dwordx4 v5,s[40:43],s63 offen offset:1024 lds
buffer_load_dwordx4 v6,s[40:43],s63 offen offset:2048 lds
s_add_u32 s63,s63,458752
.L_gy0:
s_waitcnt lgkmcnt(0)
v_mfma_f32_16x16x32_bf16 v[20:23],a[0:3],v[16:19],v[20:23]
v_mfma_f32_16x16x32_bf16 v[24:27],a[4:7],v[16:19],v[24:27]
v_mfma_f32_16x16x32_bf16 v[28:31],a[8:11],v[16:19],v[28:31]
v_mfma_f32_16x16x32_bf16 v[20:23],a[12:15],v[16:19],v[20:23]
v_mfma_f32_16x16x32_bf16 v[24:27],a[16:19],v[16:19],v[24:27]
v_mfma_f32_16x16x32_bf16 v[28:31],a[20:23],v[16:19],v[28:31]
.L_gw1:
s_waitcnt vmcnt(12)
s_barrier
ds_read_b64_tr_b16 a[24:25],v8 offset:24576
ds_read_b64_tr_b16 a[26:27],v8 offset:25344
ds_read_b64_tr_b16 a[28:29],v8 offset:36864
ds_read_b64_tr_b16 a[30:31],v8 offset:37632
ds_read_b64_tr_b16 a[32:33],v9 offset:24576
ds_read_b64_tr_b16 a[34:35],v9 offset:25344
ds_read_b64_tr_b16 a[36:37],v8 offset:30720
ds_read_b64_tr_b16 a[38:39],v8 offset:31488
ds_read_b64_tr_b16 a[40:41],v8 offset:43008
ds_read_b64_tr_b16 a[42:43],v8 offset:43776
ds_read_b64_tr_b16 a[44:45],v9 offset:30720
ds_read_b64_tr_b16 a[46:47],v9 offset:31488
s_add_u32 m0,s64,0
s_nop 0
buffer_load_dwordx4 v4,s[36:39],s63 offen offset:0 lds
buffer_load_dwordx4 v5,s[36:39],s63 offen offset:1024 lds
buffer_load_dwordx4 v6,s[36:39],s63 offen offset:2048 lds
s_add_u32 m0,s64,12288
s_nop 0
buffer_load_dwordx4 v4,s[40:43],s63 offen offset:0 lds
buffer_load_dwordx4 v5,s[40:43],s63 offen offset:1024 lds
buffer_load_dwordx4 v6,s[40:43],s63 offen offset:2048 lds
)#"
R"#(
s_add_u32 s63,s63,458752
.L_gy1:
s_waitcnt lgkmcnt(0)
v_mfma_f32_16x16x32_bf16 v[20:23],a[24:27],v[16:19],v[20:23]
v_mfma_f32_16x16x32_bf16 v[24:27],a[28:31],v[16:19],v[24:27]
v_mfma_f32_16x16x32_bf16 v[28:31],a[32:35],v[16:19],v[28:31]
v_mfma_f32_16x16x32_bf16 v[20:23],a[36:39],v[16:19],v[20:23]
v_mfma_f32_16x16x32_bf16 v[24:27],a[40:43],v[16:19],v[24:27]
v_mfma_f32_16x16x32_bf16 v[28:31],a[44:47],v[16:19],v[28:31]
.L_gw2:
s_waitcnt vmcnt(12)
s_cmp_eq_u32 s28,1
s_barrier
ds_read_b64_tr_b16 a[48:49],v10 offset:0
ds_read_b64_tr_b16 a[50:51],v10 offset:768
ds_read_b64_tr_b16 a[52:53],v10 offset:12288
ds_read_b64_tr_b16 a[54:55],v10 offset:13056
ds_read_b64_tr_b16 a[56:57],v11 offset:0
ds_read_b64_tr_b16 a[58:59],v11 offset:768
ds_read_b64_tr_b16 a[60:61],v10 offset:6144
ds_read_b64_tr_b16 a[62:63],v10 offset:6912
ds_read_b64_tr_b16 a[64:65],v10 offset:18432
ds_read_b64_tr_b16 a[66:67],v10 offset:19200
ds_read_b64_tr_b16 a[68:69],v11 offset:6144
ds_read_b64_tr_b16 a[70:71],v11 offset:6912
s_cbranch_scc1 .L_gy2
s_add_u32 m0,s64,24576
s_nop 0
buffer_load_dwordx4 v4,s[36:39],s63 offen offset:0 lds
buffer_load_dwordx4 v5,s[36:39],s63 offen offset:1024 lds
buffer_load_dwordx4 v6,s[36:39],s63 offen offset:2048 lds
s_add_u32 m0,s64,36864
s_nop 0
buffer_load_dwordx4 v4,s[40:43],s63 offen offset:0 lds
buffer_load_dwordx4 v5,s[40:43],s63 offen offset:1024 lds
buffer_load_dwordx4 v6,s[40:43],s63 offen offset:2048 lds
s_add_u32 s63,s63,458752
.L_gy2:
s_waitcnt lgkmcnt(0)
v_mfma_f32_16x16x32_bf16 v[20:23],a[48:51],v[16:19],v[20:23]
v_mfma_f32_16x16x32_bf16 v[24:27],a[52:55],v[16:19],v[24:27]
v_mfma_f32_16x16x32_bf16 v[28:31],a[56:59],v[16:19],v[28:31]
v_mfma_f32_16x16x32_bf16 v[20:23],a[60:63],v[16:19],v[20:23]
v_mfma_f32_16x16x32_bf16 v[24:27],a[64:67],v[16:19],v[24:27]
v_mfma_f32_16x16x32_bf16 v[28:31],a[68:71],v[16:19],v[28:31]
.L_gw3:
s_waitcnt vmcnt(12)
s_cmp_eq_u32 s28,1
s_cbranch_scc0 .L_gx3
s_waitcnt vmcnt(6)
.L_gx3:
s_barrier
ds_read_b64_tr_b16 a[72:73],v10 offset:24576
ds_read_b64_tr_b16 a[74:75],v10 offset:25344
ds_read_b64_tr_b16 a[76:77],v10 offset:36864
ds_read_b64_tr_b16 a[78:79],v10 offset:37632
ds_read_b64_tr_b16 a[80:81],v11 offset:24576
ds_read_b64_tr_b16 a[82:83],v11 offset:25344
ds_read_b64_tr_b16 a[84:85],v10 offset:30720
ds_read_b64_tr_b16 a[86:87],v10 offset:31488
ds_read_b64_tr_b16 a[88:89],v10 offset:43008
ds_read_b64_tr_b16 a[90:91],v10 offset:43776
ds_read_b64_tr_b16 a[92:93],v11 offset:30720
ds_read_b64_tr_b16 a[94:95],v11 offset:31488
s_cbranch_scc1 .L_gy3
s_add_u32 m0,s64,49152
s_nop 0
buffer_load_dwordx4 v4,s[36:39],s63 offen offset:0 lds
buffer_load_dwordx4 v5,s[36:39],s63 offen offset:1024 lds
buffer_load_dwordx4 v6,s[36:39],s63 offen offset:2048 lds
s_add_u32 m0,s64,61440
s_nop 0
buffer_load_dwordx4 v4,s[40:43],s63 offen offset:0 lds
buffer_load_dwordx4 v5,s[40:43],s63 offen offset:1024 lds
buffer_load_dwordx4 v6,s[40:43],s63 offen offset:2048 lds
s_add_u32 s63,s63,458752
.L_gy3:
s_waitcnt lgkmcnt(0)
v_mfma_f32_16x16x32_bf16 v[20:23],a[72:75],v[16:19],v[20:23]
v_mfma_f32_16x16x32_bf16 v[24:27],a[76:79],v[16:19],v[24:27]
v_mfma_f32_16x16x32_bf16 v[28:31],a[80:83],v[16:19],v[28:31]
v_mfma_f32_16x16x32_bf16 v[20:23],a[84:87],v[16:19],v[20:23]
v_mfma_f32_16x16x32_bf16 v[24:27],a[88:91],v[16:19],v[24:27]
v_mfma_f32_16x16x32_bf16 v[28:31],a[92:95],v[16:19],v[28:31]
.L_gw4:
s_waitcnt vmcnt(12)
s_cmp_eq_u32 s28,1
s_cbranch_scc0 .L_gx4
s_waitcnt vmcnt(0)
.L_gx4:
s_barrier
ds_read_b64_tr_b16 a[96:97],v8 offset:0
ds_read_b64_tr_b16 a[98:99],v8 offset:768
ds_read_b64_tr_b16 a[100:101],v8 offset:12288
ds_read_b64_tr_b16 a[102:103],v8 offset:13056
ds_read_b64_tr_b16 a[104:105],v9 offset:0
ds_read_b64_tr_b16 a[106:107],v9 offset:768
ds_read_b64_tr_b16 a[108:109],v8 offset:6144
ds_read_b64_tr_b16 a[110:111],v8 offset:6912
ds_read_b64_tr_b16 a[112:113],v8 offset:18432
ds_read_b64_tr_b16 a[114:115],v8 offset:19200
ds_read_b64_tr_b16 a[116:117],v9 offset:6144
ds_read_b64_tr_b16 a[118:119],v9 offset:6912
s_cbranch_scc1 .L_gy4
s_add_u32 m0,s64,73728
s_nop 0
buffer_load_dwordx4 v4,s[36:39],s63 offen offset:0 lds
buffer_load_dwordx4 v5,s[36:39],s63 offen offset:1024 lds
buffer_load_dwordx4 v6,s[36:39],s63 offen offset:2048 lds
s_add_u32 m0,s64,86016
s_nop 0
buffer_load_dwordx4 v4,s[40:43],s63 offen offset:0 lds
buffer_load_dwordx4 v5,s[40:43],s63 offen offset:1024 lds
buffer_load_dwordx4 v6,s[40:43],s63 offen offset:2048 lds
s_add_u32 s63,s63,458752
.L_gy4:
s_waitcnt lgkmcnt(0)
v_mfma_f32_16x16x32_bf16 v[20:23],a[96:99],v[16:19],v[20:23]
v_mfma_f32_16x16x32_bf16 v[24:27],a[100:103],v[16:19],v[24:27]
v_mfma_f32_16x16x32_bf16 v[28:31],a[104:107],v[16:19],v[28:31]
v_mfma_f32_16x16x32_bf16 v[20:23],a[108:111],v[16:19],v[20:23]
v_mfma_f32_16x16x32_bf16 v[24:27],a[112:115],v[16:19],v[24:27]
v_mfma_f32_16x16x32_bf16 v[28:31],a[116:119],v[16:19],v[28:31]
s_cmp_eq_u32 s28,1
s_cbranch_scc1 .L_gz
.L_gw5:
s_waitcnt vmcnt(12)
s_barrier
ds_read_b64_tr_b16 a[120:121],v8 offset:24576
ds_read_b64_tr_b16 a[122:123],v8 offset:25344
ds_read_b64_tr_b16 a[124:125],v8 offset:36864
ds_read_b64_tr_b16 a[126:127],v8 offset:37632
ds_read_b64_tr_b16 a[128:129],v9 offset:24576
ds_read_b64_tr_b16 a[130:131],v9 offset:25344
ds_read_b64_tr_b16 a[132:133],v8 offset:30720
ds_read_b64_tr_b16 a[134:135],v8 offset:31488
ds_read_b64_tr_b16 a[136:137],v8 offset:43008
ds_read_b64_tr_b16 a[138:139],v8 offset:43776
ds_read_b64_tr_b16 a[140:141],v9 offset:30720
ds_read_b64_tr_b16 a[142:143],v9 offset:31488
s_add_u32 m0,s64,0
s_nop 0
buffer_load_dwordx4 v4,s[36:39],s63 offen offset:0 lds
buffer_load_dwordx4 v5,s[36:39],s63 offen offset:1024 lds
buffer_load_dwordx4 v6,s[36:39],s63 offen offset:2048 lds
s_add_u32 m0,s64,12288
s_nop 0
buffer_load_dwordx4 v4,s[40:43],s63 offen offset:0 lds
buffer_load_dwordx4 v5,s[40:43],s63 offen offset:1024 lds
buffer_load_dwordx4 v6,s[40:43],s63 offen offset:2048 lds
s_add_u32 s63,s63,458752
.L_gy5:
s_waitcnt lgkmcnt(0)
v_mfma_f32_16x16x32_bf16 v[20:23],a[120:123],v[16:19],v[20:23]
v_mfma_f32_16x16x32_bf16 v[24:27],a[124:127],v[16:19],v[24:27]
v_mfma_f32_16x16x32_bf16 v[28:31],a[128:131],v[16:19],v[28:31]
v_mfma_f32_16x16x32_bf16 v[20:23],a[132:135],v[16:19],v[20:23]
v_mfma_f32_16x16x32_bf16 v[24:27],a[136:139],v[16:19],v[24:27]
v_mfma_f32_16x16x32_bf16 v[28:31],a[140:143],v[16:19],v[28:31]
.L_gw6:
s_waitcnt vmcnt(12)
s_barrier
ds_read_b64_tr_b16 a[144:145],v10 offset:0
ds_read_b64_tr_b16 a[146:147],v10 offset:768
ds_read_b64_tr_b16 a[148:149],v10 offset:12288
ds_read_b64_tr_b16 a[150:151],v10 offset:13056
ds_read_b64_tr_b16 a[152:153],v11 offset:0
ds_read_b64_tr_b16 a[154:155],v11 offset:768
ds_read_b64_tr_b16 a[156:157],v10 offset:6144
ds_read_b64_tr_b16 a[158:159],v10 offset:6912
ds_read_b64_tr_b16 a[160:161],v10 offset:18432
ds_read_b64_tr_b16 a[162:163],v10 offset:19200
ds_read_b64_tr_b16 a[164:165],v11 offset:6144
ds_read_b64_tr_b16 a[166:167],v11 offset:6912
s_add_u32 m0,s64,24576
s_nop 0
buffer_load_dwordx4 v4,s[36:39],s63 offen offset:0 lds
buffer_load_dwordx4 v5,s[36:39],s63 offen offset:1024 lds
buffer_load_dwordx4 v6,s[36:39],s63 offen offset:2048 lds
s_add_u32 m0,s64,36864
s_nop 0
buffer_load_dwordx4 v4,s[40:43],s63 offen offset:0 lds
buffer_load_dwordx4 v5,s[40:43],s63 offen offset:1024 lds
buffer_load_dwordx4 v6,s[40:43],s63 offen offset:2048 lds
s_add_u32 s63,s63,458752
.L_gy6:
s_waitcnt lgkmcnt(0)
v_mfma_f32_16x16x32_bf16 v[20:23],a[144:147],v[16:19],v[20:23]
v_mfma_f32_16x16x32_bf16 v[24:27],a[148:151],v[16:19],v[24:27]
v_mfma_f32_16x16x32_bf16 v[28:31],a[152:155],v[16:19],v[28:31]
v_mfma_f32_16x16x32_bf16 v[20:23],a[156:159],v[16:19],v[20:23]
v_mfma_f32_16x16x32_bf16 v[24:27],a[160:163],v[16:19],v[24:27]
v_mfma_f32_16x16x32_bf16 v[28:31],a[164:167],v[16:19],v[28:31]
.L_gw7:
s_waitcnt vmcnt(12)
s_barrier
ds_read_b64_tr_b16 a[168:169],v10 offset:24576
ds_read_b64_tr_b16 a[170:171],v10 offset:25344
ds_read_b64_tr_b16 a[172:173],v10 offset:36864
ds_read_b64_tr_b16 a[174:175],v10 offset:37632
ds_read_b64_tr_b16 a[176:177],v11 offset:24576
ds_read_b64_tr_b16 a[178:179],v11 offset:25344
ds_read_b64_tr_b16 a[180:181],v10 offset:30720
ds_read_b64_tr_b16 a[182:183],v10 offset:31488
ds_read_b64_tr_b16 a[184:185],v10 offset:43008
ds_read_b64_tr_b16 a[186:187],v10 offset:43776
ds_read_b64_tr_b16 a[188:189],v11 offset:30720
ds_read_b64_tr_b16 a[190:191],v11 offset:31488
s_add_u32 m0,s64,49152
s_nop 0
buffer_load_dwordx4 v4,s[36:39],s63 offen offset:0 lds
buffer_load_dwordx4 v5,s[36:39],s63 offen offset:1024 lds
buffer_load_dwordx4 v6,s[36:39],s63 offen offset:2048 lds
s_add_u32 m0,s64,61440
s_nop 0
buffer_load_dwordx4 v4,s[40:43],s63 offen offset:0 lds
buffer_load_dwordx4 v5,s[40:43],s63 offen offset:1024 lds
buffer_load_dwordx4 v6,s[40:43],s63 offen offset:2048 lds
s_add_u32 s63,s63,458752
.L_gy7:
s_waitcnt lgkmcnt(0)
v_mfma_f32_16x16x32_bf16 v[20:23],a[168:171],v[16:19],v[20:23]
v_mfma_f32_16x16x32_bf16 v[24:27],a[172:175],v[16:19],v[24:27]
v_mfma_f32_16x16x32_bf16 v[28:31],a[176:179],v[16:19],v[28:31]
v_mfma_f32_16x16x32_bf16 v[20:23],a[180:183],v[16:19],v[20:23]
v_mfma_f32_16x16x32_bf16 v[24:27],a[184:187],v[16:19],v[24:27]
v_mfma_f32_16x16x32_bf16 v[28:31],a[188:191],v[16:19],v[28:31]
.L_gw8:
s_waitcnt vmcnt(12)
s_cmp_eq_u32 s28,2
s_barrier
ds_read_b64_tr_b16 a[192:193],v8 offset:0
ds_read_b64_tr_b16 a[194:195],v8 offset:768
ds_read_b64_tr_b16 a[196:197],v8 offset:12288
ds_read_b64_tr_b16 a[198:199],v8 offset:13056
ds_read_b64_tr_b16 a[200:201],v9 offset:0
ds_read_b64_tr_b16 a[202:203],v9 offset:768
ds_read_b64_tr_b16 a[204:205],v8 offset:6144
ds_read_b64_tr_b16 a[206:207],v8 offset:6912
ds_read_b64_tr_b16 a[208:209],v8 offset:18432
ds_read_b64_tr_b16 a[210:211],v8 offset:19200
ds_read_b64_tr_b16 a[212:213],v9 offset:6144
ds_read_b64_tr_b16 a[214:215],v9 offset:6912
s_cbranch_scc1 .L_gy8
s_add_u32 m0,s64,73728
s_nop 0
buffer_load_dwordx4 v4,s[36:39],s63 offen offset:0 lds
buffer_load_dwordx4 v5,s[36:39],s63 offen offset:1024 lds
buffer_load_dwordx4 v6,s[36:39],s63 offen offset:2048 lds
s_add_u32 m0,s64,86016
s_nop 0
buffer_load_dwordx4 v4,s[40:43],s63 offen offset:0 lds
buffer_load_dwordx4 v5,s[40:43],s63 offen offset:1024 lds
buffer_load_dwordx4 v6,s[40:43],s63 offen offset:2048 lds
s_add_u32 s63,s63,458752
.L_gy8:
s_waitcnt lgkmcnt(0)
v_mfma_f32_16x16x32_bf16 v[20:23],a[192:195],v[16:19],v[20:23]
v_mfma_f32_16x16x32_bf16 v[24:27],a[196:199],v[16:19],v[24:27]
v_mfma_f32_16x16x32_bf16 v[28:31],a[200:203],v[16:19],v[28:31]
v_mfma_f32_16x16x32_bf16 v[20:23],a[204:207],v[16:19],v[20:23]
v_mfma_f32_16x16x32_bf16 v[24:27],a[208:211],v[16:19],v[24:27]
v_mfma_f32_16x16x32_bf16 v[28:31],a[212:215],v[16:19],v[28:31]
.L_gw9:
s_waitcnt vmcnt(12)
s_cmp_eq_u32 s28,2
s_cbranch_scc0 .L_gx9
s_waitcnt vmcnt(6)
.L_gx9:
s_barrier
ds_read_b64_tr_b16 a[216:217],v8 offset:24576
ds_read_b64_tr_b16 a[218:219],v8 offset:25344
ds_read_b64_tr_b16 a[220:221],v8 offset:36864
ds_read_b64_tr_b16 a[222:223],v8 offset:37632
ds_read_b64_tr_b16 a[224:225],v9 offset:24576
ds_read_b64_tr_b16 a[226:227],v9 offset:25344
ds_read_b64_tr_b16 a[228:229],v8 offset:30720
ds_read_b64_tr_b16 a[230:231],v8 offset:31488
ds_read_b64_tr_b16 a[232:233],v8 offset:43008
ds_read_b64_tr_b16 a[234:235],v8 offset:43776
ds_read_b64_tr_b16 a[236:237],v9 offset:30720
ds_read_b64_tr_b16 a[238:239],v9 offset:31488
s_cbranch_scc1 .L_gy9
s_add_u32 m0,s64,0
s_nop 0
buffer_load_dwordx4 v4,s[36:39],s63 offen offset:0 lds
buffer_load_dwordx4 v5,s[36:39],s63 offen offset:1024 lds
buffer_load_dwordx4 v6,s[36:39],s63 offen offset:2048 lds
s_add_u32 m0,s64,12288
s_nop 0
buffer_load_dwordx4 v4,s[40:43],s63 offen offset:0 lds
buffer_load_dwordx4 v5,s[40:43],s63 offen offset:1024 lds
buffer_load_dwordx4 v6,s[40:43],s63 offen offset:2048 lds
s_add_u32 s63,s63,458752
.L_gy9:
s_waitcnt lgkmcnt(0)
v_mfma_f32_16x16x32_bf16 v[20:23],a[216:219],v[16:19],v[20:23]
v_mfma_f32_16x16x32_bf16 v[24:27],a[220:223],v[16:19],v[24:27]
v_mfma_f32_16x16x32_bf16 v[28:31],a[224:227],v[16:19],v[28:31]
v_mfma_f32_16x16x32_bf16 v[20:23],a[228:231],v[16:19],v[20:23]
v_mfma_f32_16x16x32_bf16 v[24:27],a[232:235],v[16:19],v[24:27]
v_mfma_f32_16x16x32_bf16 v[28:31],a[236:239],v[16:19],v[28:31]
.L_gw10:
s_waitcnt vmcnt(12)
s_cmp_eq_u32 s28,2
s_cbranch_scc0 .L_gx10
s_waitcnt vmcnt(0)
.L_gx10:
s_barrier
ds_read_b64_tr_b16 a[240:241],v10 offset:0
ds_read_b64_tr_b16 a[242:243],v10 offset:768
ds_read_b64_tr_b16 a[244:245],v10 offset:12288
ds_read_b64_tr_b16 a[246:247],v10 offset:13056
ds_read_b64_tr_b16 a[248:249],v11 offset:0
ds_read_b64_tr_b16 a[250:251],v11 offset:768
ds_read_b64_tr_b16 a[252:253],v10 offset:6144
ds_read_b64_tr_b16 a[254:255],v10 offset:6912
ds_read_b64_tr_b16 v[128:129],v10 offset:18432
ds_read_b64_tr_b16 v[130:131],v10 offset:19200
ds_read_b64_tr_b16 v[132:133],v11 offset:6144
ds_read_b64_tr_b16 v[134:135],v11 offset:6912
s_cbranch_scc1 .L_gy10
s_add_u32 m0,s64,24576
s_nop 0
buffer_load_dwordx4 v4,s[36:39],s63 offen offset:0 lds
buffer_load_dwordx4 v5,s[36:39],s63 offen offset:1024 lds
buffer_load_dwordx4 v6,s[36:39],s63 offen offset:2048 lds
s_add_u32 m0,s64,36864
s_nop 0
buffer_load_dwordx4 v4,s[40:43],s63 offen offset:0 lds
buffer_load_dwordx4 v5,s[40:43],s63 offen offset:1024 lds
buffer_load_dwordx4 v6,s[40:43],s63 offen offset:2048 lds
s_add_u32 s63,s63,458752
.L_gy10:
s_waitcnt lgkmcnt(0)
v_mfma_f32_16x16x32_bf16 v[20:23],a[240:243],v[16:19],v[20:23]
v_mfma_f32_16x16x32_bf16 v[24:27],a[244:247],v[16:19],v[24:27]
v_mfma_f32_16x16x32_bf16 v[28:31],a[248:251],v[16:19],v[28:31]
v_mfma_f32_16x16x32_bf16 v[20:23],a[252:255],v[16:19],v[20:23]
v_mfma_f32_16x16x32_bf16 v[24:27],v[128:131],v[16:19],v[24:27]
v_mfma_f32_16x16x32_bf16 v[28:31],v[132:135],v[16:19],v[28:31]
s_cmp_eq_u32 s28,2
s_cbranch_scc1 .L_gz
.L_gw11:
s_waitcnt vmcnt(12)
s_barrier
ds_read_b64_tr_b16 v[136:137],v10 offset:24576
ds_read_b64_tr_b16 v[138:139],v10 offset:25344
ds_read_b64_tr_b16 v[140:141],v10 offset:36864
ds_read_b64_tr_b16 v[142:143],v10 offset:37632
ds_read_b64_tr_b16 v[144:145],v11 offset:24576
ds_read_b64_tr_b16 v[146:147],v11 offset:25344
ds_read_b64_tr_b16 v[148:149],v10 offset:30720
ds_read_b64_tr_b16 v[150:151],v10 offset:31488
ds_read_b64_tr_b16 v[152:153],v10 offset:43008
ds_read_b64_tr_b16 v[154:155],v10 offset:43776
ds_read_b64_tr_b16 v[156:157],v11 offset:30720
ds_read_b64_tr_b16 v[158:159],v11 offset:31488
s_add_u32 m0,s64,49152
s_nop 0
buffer_load_dwordx4 v4,s[36:39],s63 offen offset:0 lds
buffer_load_dwordx4 v5,s[36:39],s63 offen offset:1024 lds
buffer_load_dwordx4 v6,s[36:39],s63 offen offset:2048 lds
s_add_u32 m0,s64,61440
s_nop 0
buffer_load_dwordx4 v4,s[40:43],s63 offen offset:0 lds
buffer_load_dwordx4 v5,s[40:43],s63 offen offset:1024 lds
buffer_load_dwordx4 v6,s[40:43],s63 offen offset:2048 lds
s_add_u32 s63,s63,458752
.L_gy11:
s_waitcnt lgkmcnt(0)
v_mfma_f32_16x16x32_bf16 v[20:23],v[136:139],v[16:19],v[20:23]
v_mfma_f32_16x16x32_bf16 v[24:27],v[140:143],v[16:19],v[24:27]
v_mfma_f32_16x16x32_bf16 v[28:31],v[144:147],v[16:19],v[28:31]
v_mfma_f32_16x16x32_bf16 v[20:23],v[148:151],v[16:19],v[20:23]
v_mfma_f32_16x16x32_bf16 v[24:27],v[152:155],v[16:19],v[24:27]
v_mfma_f32_16x16x32_bf16 v[28:31],v[156:159],v[16:19],v[28:31]
.L_gw12:
s_waitcnt vmcnt(12)
s_barrier
ds_read_b64_tr_b16 v[160:161],v8 offset:0
ds_read_b64_tr_b16 v[162:163],v8 offset:768
ds_read_b64_tr_b16 v[164:165],v8 offset:12288
ds_read_b64_tr_b16 v[166:167],v8 offset:13056
ds_read_b64_tr_b16 v[168:169],v9 offset:0
ds_read_b64_tr_b16 v[170:171],v9 offset:768
ds_read_b64_tr_b16 v[172:173],v8 offset:6144
ds_read_b64_tr_b16 v[174:175],v8 offset:6912
ds_read_b64_tr_b16 v[176:177],v8 offset:18432
ds_read_b64_tr_b16 v[178:179],v8 offset:19200
ds_read_b64_tr_b16 v[180:181],v9 offset:6144
ds_read_b64_tr_b16 v[182:183],v9 offset:6912
s_add_u32 m0,s64,73728
s_nop 0
buffer_load_dwordx4 v4,s[36:39],s63 offen offset:0 lds
buffer_load_dwordx4 v5,s[36:39],s63 offen offset:1024 lds
buffer_load_dwordx4 v6,s[36:39],s63 offen offset:2048 lds
s_add_u32 m0,s64,86016
s_nop 0
buffer_load_dwordx4 v4,s[40:43],s63 offen offset:0 lds
buffer_load_dwordx4 v5,s[40:43],s63 offen offset:1024 lds
buffer_load_dwordx4 v6,s[40:43],s63 offen offset:2048 lds
s_sub_u32 s63,s63,6881280
.L_gy12:
s_waitcnt lgkmcnt(0)
v_mfma_f32_16x16x32_bf16 v[20:23],v[160:163],v[16:19],v[20:23]
v_mfma_f32_16x16x32_bf16 v[24:27],v[164:167],v[16:19],v[24:27]
v_mfma_f32_16x16x32_bf16 v[28:31],v[168:171],v[16:19],v[28:31]
v_mfma_f32_16x16x32_bf16 v[20:23],v[172:175],v[16:19],v[20:23]
v_mfma_f32_16x16x32_bf16 v[24:27],v[176:179],v[16:19],v[24:27]
v_mfma_f32_16x16x32_bf16 v[28:31],v[180:183],v[16:19],v[28:31]
.L_gw13:
s_waitcnt vmcnt(12)
s_cmp_eq_u32 s28,0
s_barrier
ds_read_b64_tr_b16 v[184:185],v8 offset:24576
ds_read_b64_tr_b16 v[186:187],v8 offset:25344
ds_read_b64_tr_b16 v[188:189],v8 offset:36864
ds_read_b64_tr_b16 v[190:191],v8 offset:37632
ds_read_b64_tr_b16 v[192:193],v9 offset:24576
ds_read_b64_tr_b16 v[194:195],v9 offset:25344
ds_read_b64_tr_b16 v[196:197],v8 offset:30720
ds_read_b64_tr_b16 v[198:199],v8 offset:31488
ds_read_b64_tr_b16 v[200:201],v8 offset:43008
ds_read_b64_tr_b16 v[202:203],v8 offset:43776
ds_read_b64_tr_b16 v[204:205],v9 offset:30720
ds_read_b64_tr_b16 v[206:207],v9 offset:31488
s_cbranch_scc1 .L_gy13
s_add_u32 m0,s64,0
s_nop 0
buffer_load_dwordx4 v4,s[36:39],s63 offen offset:0 lds
buffer_load_dwordx4 v5,s[36:39],s63 offen offset:1024 lds
buffer_load_dwordx4 v6,s[36:39],s63 offen offset:2048 lds
s_add_u32 m0,s64,12288
s_nop 0
buffer_load_dwordx4 v4,s[40:43],s63 offen offset:0 lds
buffer_load_dwordx4 v5,s[40:43],s63 offen offset:1024 lds
buffer_load_dwordx4 v6,s[40:43],s63 offen offset:2048 lds
s_add_u32 s63,s63,458752
.L_gy13:
s_waitcnt lgkmcnt(0)
v_mfma_f32_16x16x32_bf16 v[20:23],v[184:187],v[16:19],v[20:23]
v_mfma_f32_16x16x32_bf16 v[24:27],v[188:191],v[16:19],v[24:27]
v_mfma_f32_16x16x32_bf16 v[28:31],v[192:195],v[16:19],v[28:31]
v_mfma_f32_16x16x32_bf16 v[20:23],v[196:199],v[16:19],v[20:23]
v_mfma_f32_16x16x32_bf16 v[24:27],v[200:203],v[16:19],v[24:27]
v_mfma_f32_16x16x32_bf16 v[28:31],v[204:207],v[16:19],v[28:31]
.L_gw14:
s_waitcnt vmcnt(12)
s_cmp_eq_u32 s28,0
s_cbranch_scc0 .L_gx14
s_waitcnt vmcnt(6)
.L_gx14:
s_barrier
ds_read_b64_tr_b16 v[208:209],v10 offset:0
ds_read_b64_tr_b16 v[210:211],v10 offset:768
ds_read_b64_tr_b16 v[212:213],v10 offset:12288
ds_read_b64_tr_b16 v[214:215],v10 offset:13056
ds_read_b64_tr_b16 v[216:217],v11 offset:0
ds_read_b64_tr_b16 v[218:219],v11 offset:768
ds_read_b64_tr_b16 v[220:221],v10 offset:6144
ds_read_b64_tr_b16 v[222:223],v10 offset:6912
ds_read_b64_tr_b16 v[224:225],v10 offset:18432
ds_read_b64_tr_b16 v[226:227],v10 offset:19200
ds_read_b64_tr_b16 v[228:229],v11 offset:6144
ds_read_b64_tr_b16 v[230:231],v11 offset:6912
s_cbranch_scc1 .L_gy14
s_add_u32 m0,s64,24576
s_nop 0
buffer_load_dwordx4 v4,s[36:39],s63 offen offset:0 lds
buffer_load_dwordx4 v5,s[36:39],s63 offen offset:1024 lds
buffer_load_dwordx4 v6,s[36:39],s63 offen offset:2048 lds
s_add_u32 m0,s64,36864
s_nop 0
buffer_load_dwordx4 v4,s[40:43],s63 offen offset:0 lds
buffer_load_dwordx4 v5,s[40:43],s63 offen offset:1024 lds
buffer_load_dwordx4 v6,s[40:43],s63 offen offset:2048 lds
s_add_u32 s63,s63,458752
.L_gy14:
s_waitcnt lgkmcnt(0)
v_mfma_f32_16x16x32_bf16 v[20:23],v[208:211],v[16:19],v[20:23]
v_mfma_f32_16x16x32_bf16 v[24:27],v[212:215],v[16:19],v[24:27]
v_mfma_f32_16x16x32_bf16 v[28:31],v[216:219],v[16:19],v[28:31]
v_mfma_f32_16x16x32_bf16 v[20:23],v[220:223],v[16:19],v[20:23]
v_mfma_f32_16x16x32_bf16 v[24:27],v[224:227],v[16:19],v[24:27]
v_mfma_f32_16x16x32_bf16 v[28:31],v[228:231],v[16:19],v[28:31]
.L_gw15:
s_waitcnt vmcnt(12)
s_cmp_eq_u32 s28,0
s_cbranch_scc0 .L_gx15
s_waitcnt vmcnt(0)
.L_gx15:
s_barrier
ds_read_b64_tr_b16 v[232:233],v10 offset:24576
ds_read_b64_tr_b16 v[234:235],v10 offset:25344
ds_read_b64_tr_b16 v[236:237],v10 offset:36864
ds_read_b64_tr_b16 v[238:239],v10 offset:37632
ds_read_b64_tr_b16 v[240:241],v11 offset:24576
ds_read_b64_tr_b16 v[242:243],v11 offset:25344
ds_read_b64_tr_b16 v[244:245],v10 offset:30720
)#"
R"#(
ds_read_b64_tr_b16 v[246:247],v10 offset:31488
ds_read_b64_tr_b16 v[248:249],v10 offset:43008
ds_read_b64_tr_b16 v[250:251],v10 offset:43776
ds_read_b64_tr_b16 v[252:253],v11 offset:30720
ds_read_b64_tr_b16 v[254:255],v11 offset:31488
s_cbranch_scc1 .L_gy15
s_add_u32 m0,s64,49152
s_nop 0
buffer_load_dwordx4 v4,s[36:39],s63 offen offset:0 lds
buffer_load_dwordx4 v5,s[36:39],s63 offen offset:1024 lds
buffer_load_dwordx4 v6,s[36:39],s63 offen offset:2048 lds
s_add_u32 m0,s64,61440
s_nop 0
buffer_load_dwordx4 v4,s[40:43],s63 offen offset:0 lds
buffer_load_dwordx4 v5,s[40:43],s63 offen offset:1024 lds
buffer_load_dwordx4 v6,s[40:43],s63 offen offset:2048 lds
s_add_u32 s63,s63,458752
.L_gy15:
s_waitcnt lgkmcnt(0)
v_mfma_f32_16x16x32_bf16 v[20:23],v[232:235],v[16:19],v[20:23]
v_mfma_f32_16x16x32_bf16 v[24:27],v[236:239],v[16:19],v[24:27]
v_mfma_f32_16x16x32_bf16 v[28:31],v[240:243],v[16:19],v[28:31]
v_mfma_f32_16x16x32_bf16 v[20:23],v[244:247],v[16:19],v[20:23]
v_mfma_f32_16x16x32_bf16 v[24:27],v[248:251],v[16:19],v[24:27]
v_mfma_f32_16x16x32_bf16 v[28:31],v[252:255],v[16:19],v[28:31]
s_cmp_eq_u32 s28,0
s_cbranch_scc0 .L_gw0
.L_gz:
s_barrier
s_lshl2_add_u32 s49,s27,128
s_mul_i32 s68,s27,3072
s_min_u32 s68,s68,111616
s_mul_i32 s48,s26,768
s_cmp_le_u32 s28,0
s_addc_u32 s62,84,0
s_mov_b32 s30,890880
s_mul_i32 s57,s28,296960
s_mov_b32 s56,76021760
s_cmp_lg_u32 s25,0
s_cmov_b32 s30,296960
s_cmov_b32 s57,81960960
s_cmov_b32 s56,81960960
s_lshl_b32 s31,s30,1
s_cmp_lg_u32 s25,0
s_cmov_b32 s62,-1
v_cmp_gt_u32 vcc,48,v1
v_mov_b32 v15,0x7fff0000
s_nop 0
v_cndmask_b32 v15,v15,v2,vcc
s_add_u32 s68,s68,s48
s_add_u32 s68,s68,4718592
s_waitcnt lgkmcnt(0)
s_add_u32 s54,s52,0x7f4a7c15
s_addc_u32 s55,s53,0x9e3779b9
s_lshl_b32 s67,s49,5
s_add_u32 s67,s67,4898816
s_sub_u32 s51,s67,s68
s_add_u32 s92,s32,s51
s_addc_u32 s93,s33,0x10000
s_sub_u32 s92,s92,s30
s_subb_u32 s93,s93,0
s_cmp_eq_u32 s26,3
s_cselect_b32 s94,1,0
s_mov_b32 s95,0x20000
v_mov_b32 v16,s54
v_mov_b32 v17,s55
v_cmp_gt_u32 vcc,12,v1
v_mul_u32_u24 v3,296960,v1
v_mov_b32 v12,0x7fff0000
v_cndmask_b32 v3,v12,v3,vcc
s_add_u32 s51,s67,s56
buffer_store_dwordx2 v[16:17],v3,s[0:3],s51 offen sc1
s_sub_u32 s67,s68,296960
s_add_u32 s68,s68,s57
s_sub_u32 s68,s68,s31
s_add_u32 s63,s57,4681728
s_add_u32 s66,s57,4898816
s_mul_i32 s64,s26,9216
s_add_u32 s65,s64,73728
s_xor_b32 s65,s65,s64
s_add_u32 s63,s63,s64
v_mov_b32 v4,v2
v_add_u32 v5,36864,v2
v_add_u32 v6,4096,v2
v_add_u32 v7,8192,v2
v_bfe_u32 v3,v1,1,3
v_mul_u32_u24 v8,1152,v3
v_lshrrev_b32 v3,1,v3
v_add_u32 v3,s26,v3
v_lshrrev_b32 v9,4,v1
v_lshl_add_u32 v3,v3,2,v9
v_and_b32 v3,15,v3
v_lshl_add_u32 v8,v3,2,v8
v_and_b32 v3,1,v1
v_lshl_add_u32 v8,v3,6,v8
v_add_u32 v8,1024,v8
s_lshl_b32 s48,s26,2
v_add_u32 v10,s48,v9
v_and_b32 v11,15,v1
v_add_u32 v3,64,v1
v_cmp_eq_u32 vcc,0,v11
v_add_u32 v9,73728,v8
v_xor_b32 v9,v9,v8
v_cndmask_b32 v10,v3,v10,vcc
v_lshlrev_b32 v10,2,v10
v_add_u32 v10,147456,v10
v_lshlrev_b32 v11,2,v11
v_add_u32 v11,147456,v11
v_lshlrev_b32 v14,2,v1
v_add_u32 v14,96,v14
v_lshrrev_b32 v14,2,v14
v_lshlrev_b32 v14,7,v14
v_cmp_gt_u32 vcc,8,v1
v_mov_b32 v3,0x7fff0000
s_nop 0
v_cndmask_b32 v14,v3,v14,vcc
v_cndmask_b32 v16,0,v16,vcc
v_cndmask_b32 v17,0,v17,vcc
s_cmp_lg_u32 s26,1
s_cbranch_scc1 .L_skga
s_mov_b32 s48,20000
s_branch .L_rfga
.L_rlga:
s_sleep 2
.L_rfga:
buffer_load_dwordx2 v[12:13],v14,s[0:3],s66 offen sc1
s_sub_u32 s48,s48,1
s_cbranch_scc1 .L_end
s_waitcnt vmcnt(0)
v_cmp_ne_u64 vcc,v[16:17],v[12:13]
s_cbranch_vccnz .L_rlga
.L_skga:
s_waitcnt vmcnt(0)
s_barrier
s_add_u32 s66,s66,s30
s_add_u32 m0,s64,0
s_nop 0
buffer_load_dwordx4 v2,s[32:35],s63 offen offset:0 sc1 lds
buffer_load_dwordx4 v2,s[32:35],s63 offen offset:1024 sc1 lds
buffer_load_dwordx4 v2,s[32:35],s63 offen offset:2048 sc1 lds
buffer_load_dwordx4 v2,s[32:35],s63 offen offset:3072 sc1 lds
s_add_u32 m0,s64,4096
s_nop 0
buffer_load_dwordx4 v6,s[32:35],s63 offen offset:0 sc1 lds
buffer_load_dwordx4 v6,s[32:35],s63 offen offset:1024 sc1 lds
buffer_load_dwordx4 v6,s[32:35],s63 offen offset:2048 sc1 lds
buffer_load_dwordx4 v6,s[32:35],s63 offen offset:3072 sc1 lds
s_add_u32 m0,s64,8192
s_nop 0
buffer_load_dwordx4 v7,s[32:35],s63 offen offset:0 sc1 lds
s_add_u32 s63,s63,s30
s_cmp_lg_u32 s26,2
s_cbranch_scc1 .L_skgb
s_mov_b32 s48,20000
s_branch .L_rfgb
.L_rlgb:
s_sleep 2
.L_rfgb:
buffer_load_dwordx2 v[12:13],v14,s[0:3],s66 offen sc1
s_sub_u32 s48,s48,1
s_cbranch_scc1 .L_end
s_waitcnt vmcnt(0)
v_cmp_ne_u64 vcc,v[16:17],v[12:13]
s_cbranch_vccnz .L_rlgb
.L_skgb:
s_waitcnt vmcnt(0)
s_add_u32 s66,s66,s30
s_barrier
s_add_u32 m0,s64,36864
s_nop 0
buffer_load_dwordx4 v2,s[32:35],s63 offen offset:0 sc1 lds
buffer_load_dwordx4 v2,s[32:35],s63 offen offset:1024 sc1 lds
buffer_load_dwordx4 v2,s[32:35],s63 offen offset:2048 sc1 lds
buffer_load_dwordx4 v2,s[32:35],s63 offen offset:3072 sc1 lds
s_add_u32 m0,s64,40960
s_nop 0
buffer_load_dwordx4 v6,s[32:35],s63 offen offset:0 sc1 lds
buffer_load_dwordx4 v6,s[32:35],s63 offen offset:1024 sc1 lds
ds_read2st64_b32 v[56:57],v8 offset0:0 offset1:36
ds_read2st64_b32 v[58:59],v8 offset0:72 offset1:108
s_waitcnt lgkmcnt(0)
v_add_f32 v56,v56,v57
v_add_f32 v58,v58,v59
v_add_f32 v56,v56,v58
s_nop 1
v_add_f32_dpp v56,v56,v56 row_ror:8 row_mask:0xf bank_mask:0xf
s_nop 1
v_add_f32_dpp v56,v56,v56 row_ror:4 row_mask:0xf bank_mask:0xf
ds_read_b128 v[76:79],v4 offset:0
ds_read_b128 v[80:83],v4 offset:1152
ds_read_b128 v[84:87],v4 offset:2304
ds_read_b128 v[88:91],v4 offset:3456
ds_read_b128 v[92:95],v4 offset:4608
ds_read_b128 v[96:99],v4 offset:5760
ds_read_b128 v[100:103],v4 offset:6912
ds_read_b128 v[104:107],v4 offset:8064
s_waitcnt lgkmcnt(0)
s_cmp_eq_u32 s26,2
s_cselect_b32 s48,s30,0
s_add_u32 s66,s66,s48
.L_ggo:
s_cmp_eq_u32 s26,1
s_cbranch_scc1 .L_gv1
s_cmp_eq_u32 s26,2
s_cbranch_scc1 .L_gv2
s_cmp_eq_u32 s26,3
s_cbranch_scc1 .L_gv3
.p2align 6
.L_gv0:
.L_gl_0:
s_waitcnt lgkmcnt(4)
v_mfma_f32_16x16x32_bf16 v[32:35],a[0:3],v[76:79],0
ds_read_b128 v[108:111],v4 offset:9216
s_add_u32 s68,s68,s30
v_mfma_f32_16x16x32_bf16 v[32:35],a[12:15],v[80:83],v[32:35]
buffer_load_dwordx4 v6,s[32:35],s63 offen offset:2048 sc1 lds
v_fma_f32 v52,-v60,v28,v52
v_mfma_f32_16x16x32_bf16 v[32:35],a[24:27],v[84:87],v[32:35]
v_fma_f32 v53,-v60,v29,v53
v_fma_f32 v54,-v60,v30,v54
v_mfma_f32_16x16x32_bf16 v[32:35],a[36:39],v[88:91],v[32:35]
ds_read_b128 v[112:115],v4 offset:10368
v_fma_f32 v55,-v60,v31,v55
v_mfma_f32_16x16x32_bf16 v[36:39],a[40:43],v[88:91],0
v_permlane32_swap_b32 v52,v54
v_mfma_f32_16x16x32_bf16 v[36:39],a[28:31],v[84:87],v[36:39]
buffer_load_dwordx4 v6,s[32:35],s63 offen offset:3072 sc1 lds
v_fma_f32 v48,-v60,v24,v48
v_mfma_f32_16x16x32_bf16 v[36:39],a[16:19],v[80:83],v[36:39]
ds_read_b128 v[116:119],v4 offset:11520
v_fma_f32 v49,-v60,v25,v49
v_mfma_f32_16x16x32_bf16 v[36:39],a[4:7],v[76:79],v[36:39]
v_permlane32_swap_b32 v53,v55
v_mfma_f32_16x16x32_bf16 v[40:43],a[8:11],v[76:79],0
v_fma_f32 v50,-v60,v26,v50
v_fma_f32 v51,-v60,v27,v51
v_mfma_f32_16x16x32_bf16 v[40:43],a[20:23],v[80:83],v[40:43]
ds_read_b128 v[120:123],v4 offset:12672
v_fma_f32 v44,-v60,v20,v44
v_mfma_f32_16x16x32_bf16 v[40:43],a[32:35],v[84:87],v[40:43]
v_fma_f32 v45,-v60,v21,v45
v_fma_f32 v46,-v60,v22,v46
v_mfma_f32_16x16x32_bf16 v[40:43],a[44:47],v[88:91],v[40:43]
s_waitcnt lgkmcnt(4)
s_waitcnt vmcnt(8)
s_barrier
v_mfma_f32_16x16x32_bf16 v[40:43],a[56:59],v[92:95],v[40:43]
ds_read_b128 v[124:127],v4 offset:13824
s_add_u32 m0,s64,45056
v_mfma_f32_16x16x32_bf16 v[40:43],a[68:71],v[96:99],v[40:43]
buffer_load_dwordx4 v7,s[32:35],s63 offen offset:0 sc1 lds
s_add_u32 s63,s63,s30
v_mfma_f32_16x16x32_bf16 v[40:43],a[80:83],v[100:103],v[40:43]
s_xor_b32 s64,s64,s65
v_fma_f32 v47,-v60,v23,v47
v_mfma_f32_16x16x32_bf16 v[40:43],a[92:95],v[104:107],v[40:43]
ds_read_b128 v[76:79],v4 offset:14976
v_mul_f32 v64,v44,v61
v_mfma_f32_16x16x32_bf16 v[36:39],a[88:91],v[104:107],v[36:39]
v_exp_f32 v64,v64
v_mfma_f32_16x16x32_bf16 v[36:39],a[76:79],v[100:103],v[36:39]
v_mul_f32 v65,v45,v61
v_mul_f32 v66,v46,v61
v_mfma_f32_16x16x32_bf16 v[36:39],a[64:67],v[96:99],v[36:39]
ds_read_b128 v[80:83],v4 offset:16128
v_mul_f32 v67,v47,v61
v_mfma_f32_16x16x32_bf16 v[36:39],a[52:55],v[92:95],v[36:39]
v_exp_f32 v65,v65
v_mfma_f32_16x16x32_bf16 v[32:35],a[48:51],v[92:95],v[32:35]
v_exp_f32 v66,v66
v_mfma_f32_16x16x32_bf16 v[32:35],a[60:63],v[96:99],v[32:35]
ds_read_b128 v[84:87],v4 offset:17280
v_mul_f32 v68,v52,v61
v_mfma_f32_16x16x32_bf16 v[32:35],a[72:75],v[100:103],v[32:35]
v_exp_f32 v67,v67
v_mfma_f32_16x16x32_bf16 v[32:35],a[84:87],v[104:107],v[32:35]
v_mul_f32 v69,v53,v61
s_waitcnt lgkmcnt(4)
v_mfma_f32_16x16x32_bf16 v[32:35],a[96:99],v[108:111],v[32:35]
ds_read_b128 v[88:91],v4 offset:18432
v_mul_f32 v44,v44,v48
v_mfma_f32_16x16x32_bf16 v[32:35],a[108:111],v[112:115],v[32:35]
v_exp_f32 v68,v68
v_mfma_f32_16x16x32_bf16 v[32:35],a[120:123],v[116:119],v[32:35]
v_exp_f32 v69,v69
v_mfma_f32_16x16x32_bf16 v[32:35],a[132:135],v[120:123],v[32:35]
ds_read_b128 v[92:95],v4 offset:19584
v_mul_f32 v45,v45,v49
v_mfma_f32_16x16x32_bf16 v[36:39],a[136:139],v[120:123],v[36:39]
v_mul_f32 v46,v46,v50
v_mul_f32 v47,v47,v51
v_mfma_f32_16x16x32_bf16 v[36:39],a[124:127],v[116:119],v[36:39]
v_mul_f32 v52,v52,v54
v_mul_f32 v53,v53,v55
v_mfma_f32_16x16x32_bf16 v[36:39],a[112:115],v[112:115],v[36:39]
ds_read_b128 v[96:99],v4 offset:20736
v_fma_f32 v64,v64,v62,v62
v_mfma_f32_16x16x32_bf16 v[36:39],a[100:103],v[108:111],v[36:39]
v_rcp_f32 v64,v64
v_mfma_f32_16x16x32_bf16 v[40:43],a[104:107],v[108:111],v[40:43]
v_fma_f32 v65,v65,v62,v62
v_fma_f32 v66,v66,v62,v62
v_mfma_f32_16x16x32_bf16 v[40:43],a[116:119],v[112:115],v[40:43]
ds_read_b128 v[100:103],v4 offset:21888
v_fma_f32 v67,v67,v62,v62
v_mfma_f32_16x16x32_bf16 v[40:43],a[128:131],v[116:119],v[40:43]
v_rcp_f32 v65,v65
v_mfma_f32_16x16x32_bf16 v[40:43],a[140:143],v[120:123],v[40:43]
v_fma_f32 v68,v68,v62,v62
s_waitcnt lgkmcnt(4)
v_mfma_f32_16x16x32_bf16 v[40:43],a[152:155],v[124:127],v[40:43]
ds_read_b128 v[104:107],v4 offset:23040
v_fma_f32 v69,v69,v62,v62
v_mfma_f32_16x16x32_bf16 v[40:43],a[164:167],v[76:79],v[40:43]
v_rcp_f32 v66,v66
v_mfma_f32_16x16x32_bf16 v[40:43],a[176:179],v[80:83],v[40:43]
v_rcp_f32 v67,v67
v_mfma_f32_16x16x32_bf16 v[40:43],a[188:191],v[84:87],v[40:43]
ds_read_b128 v[108:111],v4 offset:24192
v_mul_f32 v44,v44,v64
v_mfma_f32_16x16x32_bf16 v[36:39],a[184:187],v[84:87],v[36:39]
v_rcp_f32 v68,v68
v_mfma_f32_16x16x32_bf16 v[36:39],a[172:175],v[80:83],v[36:39]
v_rcp_f32 v69,v69
v_mfma_f32_16x16x32_bf16 v[36:39],a[160:163],v[76:79],v[36:39]
ds_read_b128 v[112:115],v4 offset:25344
v_mul_f32 v45,v45,v65
v_mfma_f32_16x16x32_bf16 v[36:39],a[148:151],v[124:127],v[36:39]
v_mul_f32 v46,v46,v66
v_mul_f32 v47,v47,v67
v_mfma_f32_16x16x32_bf16 v[32:35],a[144:147],v[124:127],v[32:35]
v_mul_f32 v52,v52,v68
v_mul_f32 v53,v53,v69
v_mfma_f32_16x16x32_bf16 v[32:35],a[156:159],v[76:79],v[32:35]
ds_read_b128 v[116:119],v4 offset:26496
v_cvt_pk_bf16_f32 v74,v52,v53
v_mfma_f32_16x16x32_bf16 v[32:35],a[168:171],v[80:83],v[32:35]
v_cvt_pk_bf16_f32 v72,v44,v45
v_cvt_pk_bf16_f32 v73,v46,v47
v_mfma_f32_16x16x32_bf16 v[32:35],a[180:183],v[84:87],v[32:35]
v_add_f32_dpp v56,v56,v56 row_ror:2 row_mask:0xf bank_mask:0xf
s_waitcnt lgkmcnt(4)
v_mfma_f32_16x16x32_bf16 v[32:35],a[192:195],v[88:91],v[32:35]
ds_read_b128 v[120:123],v4 offset:27648
v_mul_f32 v56,0x3a800000,v56
v_mfma_f32_16x16x32_bf16 v[32:35],a[204:207],v[92:95],v[32:35]
v_permlane32_swap_b32 v74,v75
v_mfma_f32_16x16x32_bf16 v[32:35],a[216:219],v[96:99],v[32:35]
v_mul_f32 v57,v56,v56
v_mfma_f32_16x16x32_bf16 v[32:35],a[228:231],v[100:103],v[32:35]
ds_read_b128 v[124:127],v4 offset:28800
v_sub_f32_dpp v57,v56,v57 quad_perm:[1,1,3,3] row_mask:0xf bank_mask:0xf
v_mfma_f32_16x16x32_bf16 v[36:39],a[232:235],v[100:103],v[36:39]
v_permlane16_swap_b32 v72,v74
v_mfma_f32_16x16x32_bf16 v[36:39],a[220:223],v[96:99],v[36:39]
v_permlane16_swap_b32 v73,v75
v_mfma_f32_16x16x32_bf16 v[36:39],a[208:211],v[92:95],v[36:39]
buffer_store_dwordx4 v[72:75],v15,s[32:35],s68 offen sc1
ds_read_b128 v[76:79],v4 offset:29952
v_mfma_f32_16x16x32_bf16 v[36:39],a[196:199],v[88:91],v[36:39]
v_max_f32 v57,0,v57
v_mfma_f32_16x16x32_bf16 v[40:43],a[200:203],v[88:91],v[40:43]
v_cvt_pk_bf16_f32 v57,0,v57
v_mfma_f32_16x16x32_bf16 v[40:43],a[212:215],v[92:95],v[40:43]
ds_read_b128 v[80:83],v4 offset:31104
v_add_f32 v57,0x3727c5ac,v57
v_mfma_f32_16x16x32_bf16 v[40:43],a[224:227],v[96:99],v[40:43]
v_cvt_pk_bf16_f32 v57,0,v57
v_mfma_f32_16x16x32_bf16 v[40:43],a[236:239],v[100:103],v[40:43]
s_waitcnt lgkmcnt(4)
v_mfma_f32_16x16x32_bf16 v[40:43],a[248:251],v[104:107],v[40:43]
ds_read_b128 v[84:87],v4 offset:32256
v_mfma_f32_16x16x32_bf16 v[40:43],v[132:135],v[108:111],v[40:43]
v_sqrt_f32 v57,v57
v_mfma_f32_16x16x32_bf16 v[40:43],v[144:147],v[112:115],v[40:43]
v_cvt_pk_bf16_f32 v57,v56,v57
v_mfma_f32_16x16x32_bf16 v[40:43],v[156:159],v[116:119],v[40:43]
ds_read_b128 v[88:91],v4 offset:33408
ds_write_b32 v10,v57 offset:0
v_mfma_f32_16x16x32_bf16 v[36:39],v[152:155],v[116:119],v[36:39]
v_mfma_f32_16x16x32_bf16 v[36:39],v[140:143],v[112:115],v[36:39]
v_mfma_f32_16x16x32_bf16 v[36:39],v[128:131],v[108:111],v[36:39]
ds_read_b128 v[92:95],v4 offset:34560
v_mfma_f32_16x16x32_bf16 v[36:39],a[244:247],v[104:107],v[36:39]
v_mfma_f32_16x16x32_bf16 v[32:35],a[240:243],v[104:107],v[32:35]
v_mfma_f32_16x16x32_bf16 v[32:35],a[252:255],v[108:111],v[32:35]
ds_read_b128 v[96:99],v4 offset:35712
v_mfma_f32_16x16x32_bf16 v[32:35],v[136:139],v[112:115],v[32:35]
v_xor_b32 v4,73728,v4
v_mfma_f32_16x16x32_bf16 v[32:35],v[148:151],v[116:119],v[32:35]
s_waitcnt lgkmcnt(5)
s_waitcnt vmcnt(1)
s_waitcnt lgkmcnt(2)
s_barrier
v_mfma_f32_16x16x32_bf16 v[32:35],v[160:163],v[120:123],v[32:35]
ds_read_b128 v[100:103],v5 offset:0
s_add_u32 m0,s64,0
v_mfma_f32_16x16x32_bf16 v[32:35],v[172:175],v[124:127],v[32:35]
buffer_load_dwordx4 v2,s[32:35],s63 offen offset:0 sc1 lds
v_mfma_f32_16x16x32_bf16 v[32:35],v[184:187],v[76:79],v[32:35]
ds_read_b32 v62,v11 offset:0
v_mfma_f32_16x16x32_bf16 v[32:35],v[196:199],v[80:83],v[32:35]
ds_read_b128 v[104:107],v5 offset:1152
v_mfma_f32_16x16x32_bf16 v[36:39],v[200:203],v[80:83],v[36:39]
v_mfma_f32_16x16x32_bf16 v[36:39],v[188:191],v[76:79],v[36:39]
buffer_load_dwordx4 v2,s[32:35],s63 offen offset:1024 sc1 lds
v_mfma_f32_16x16x32_bf16 v[36:39],v[176:179],v[124:127],v[36:39]
ds_read_b128 v[108:111],v5 offset:2304
ds_read2st64_b32 v[56:57],v8 offset0:144 offset1:180
v_mfma_f32_16x16x32_bf16 v[36:39],v[164:167],v[120:123],v[36:39]
ds_read2st64_b32 v[58:59],v8 offset0:216 offset1:252
v_xor_b32 v8,v9,v8
v_mfma_f32_16x16x32_bf16 v[40:43],v[168:171],v[120:123],v[40:43]
s_waitcnt lgkmcnt(4)
v_lshlrev_b32 v60,16,v62
v_mfma_f32_16x16x32_bf16 v[40:43],v[180:183],v[124:127],v[40:43]
buffer_load_dwordx4 v2,s[32:35],s63 offen offset:2048 sc1 lds
ds_read_b128 v[112:115],v5 offset:3456
v_mfma_f32_16x16x32_bf16 v[40:43],v[192:195],v[76:79],v[40:43]
v_and_b32 v62,0xffff0000,v62
v_mfma_f32_16x16x32_bf16 v[40:43],v[204:207],v[80:83],v[40:43]
v_mfma_f32_16x16x32_bf16 v[40:43],v[216:219],v[84:87],v[40:43]
ds_read_b128 v[116:119],v5 offset:4608
v_mfma_f32_16x16x32_bf16 v[40:43],v[228:231],v[88:91],v[40:43]
buffer_load_dwordx4 v2,s[32:35],s63 offen offset:3072 sc1 lds
v_mfma_f32_16x16x32_bf16 v[40:43],v[240:243],v[92:95],v[40:43]
v_rcp_f32 v61,v62
v_mfma_f32_16x16x32_bf16 v[40:43],v[252:255],v[96:99],v[40:43]
ds_read_b128 v[120:123],v5 offset:5760
v_mul_f32 v62,v62,v62
v_mfma_f32_16x16x32_bf16 v[36:39],v[248:251],v[96:99],v[36:39]
s_add_u32 m0,s64,4096
s_waitcnt lgkmcnt(3)
v_add_f32 v56,v56,v57
v_mfma_f32_16x16x32_bf16 v[36:39],v[236:239],v[92:95],v[36:39]
buffer_load_dwordx4 v6,s[32:35],s63 offen offset:0 sc1 lds
v_add_f32 v58,v58,v59
v_mfma_f32_16x16x32_bf16 v[36:39],v[224:227],v[88:91],v[36:39]
ds_read_b128 v[124:127],v5 offset:6912
v_add_f32 v56,v56,v58
v_mfma_f32_16x16x32_bf16 v[36:39],v[212:215],v[84:87],v[36:39]
v_mul_f32 v61,0xbfb8aa3b,v61
v_add_f32_dpp v56,v56,v56 row_ror:8 row_mask:0xf bank_mask:0xf
v_mfma_f32_16x16x32_bf16 v[32:35],v[208:211],v[84:87],v[32:35]
v_mfma_f32_16x16x32_bf16 v[32:35],v[220:223],v[88:91],v[32:35]
buffer_load_dwordx4 v6,s[32:35],s63 offen offset:1024 sc1 lds
ds_read_b128 v[76:79],v5 offset:8064
v_mfma_f32_16x16x32_bf16 v[32:35],v[232:235],v[92:95],v[32:35]
v_add_f32_dpp v56,v56,v56 row_ror:4 row_mask:0xf bank_mask:0xf
s_sub_u32 s62,s62,1
v_mfma_f32_16x16x32_bf16 v[32:35],v[244:247],v[96:99],v[32:35]
s_cbranch_scc1 .L_gt0_0
.L_gm_0:
s_waitcnt lgkmcnt(4)
v_mfma_f32_16x16x32_bf16 v[44:47],a[0:3],v[100:103],0
ds_read_b128 v[80:83],v5 offset:9216
s_add_u32 s68,s68,s30
v_mfma_f32_16x16x32_bf16 v[44:47],a[12:15],v[104:107],v[44:47]
buffer_load_dwordx4 v6,s[32:35],s63 offen offset:2048 sc1 lds
v_fma_f32 v40,-v60,v28,v40
v_mfma_f32_16x16x32_bf16 v[44:47],a[24:27],v[108:111],v[44:47]
v_fma_f32 v41,-v60,v29,v41
v_fma_f32 v42,-v60,v30,v42
v_mfma_f32_16x16x32_bf16 v[44:47],a[36:39],v[112:115],v[44:47]
ds_read_b128 v[84:87],v5 offset:10368
v_fma_f32 v43,-v60,v31,v43
v_mfma_f32_16x16x32_bf16 v[48:51],a[40:43],v[112:115],0
v_permlane32_swap_b32 v40,v42
v_mfma_f32_16x16x32_bf16 v[48:51],a[28:31],v[108:111],v[48:51]
buffer_load_dwordx4 v6,s[32:35],s63 offen offset:3072 sc1 lds
v_fma_f32 v36,-v60,v24,v36
v_mfma_f32_16x16x32_bf16 v[48:51],a[16:19],v[104:107],v[48:51]
ds_read_b128 v[88:91],v5 offset:11520
v_fma_f32 v37,-v60,v25,v37
v_mfma_f32_16x16x32_bf16 v[48:51],a[4:7],v[100:103],v[48:51]
v_permlane32_swap_b32 v41,v43
v_mfma_f32_16x16x32_bf16 v[52:55],a[8:11],v[100:103],0
v_fma_f32 v38,-v60,v26,v38
v_fma_f32 v39,-v60,v27,v39
v_mfma_f32_16x16x32_bf16 v[52:55],a[20:23],v[104:107],v[52:55]
ds_read_b128 v[92:95],v5 offset:12672
v_fma_f32 v32,-v60,v20,v32
v_mfma_f32_16x16x32_bf16 v[52:55],a[32:35],v[108:111],v[52:55]
v_fma_f32 v33,-v60,v21,v33
v_fma_f32 v34,-v60,v22,v34
v_mfma_f32_16x16x32_bf16 v[52:55],a[44:47],v[112:115],v[52:55]
s_waitcnt lgkmcnt(4)
s_waitcnt vmcnt(8)
s_barrier
v_mfma_f32_16x16x32_bf16 v[52:55],a[56:59],v[116:119],v[52:55]
ds_read_b128 v[96:99],v5 offset:13824
s_add_u32 m0,s64,8192
v_mfma_f32_16x16x32_bf16 v[52:55],a[68:71],v[120:123],v[52:55]
buffer_load_dwordx4 v7,s[32:35],s63 offen offset:0 sc1 lds
s_add_u32 s63,s63,s30
v_mfma_f32_16x16x32_bf16 v[52:55],a[80:83],v[124:127],v[52:55]
v_fma_f32 v35,-v60,v23,v35
v_mul_f32 v64,v32,v61
v_mfma_f32_16x16x32_bf16 v[52:55],a[92:95],v[76:79],v[52:55]
ds_read_b128 v[100:103],v5 offset:14976
)#"
R"#(
v_mul_f32 v65,v33,v61
v_mfma_f32_16x16x32_bf16 v[48:51],a[88:91],v[76:79],v[48:51]
v_exp_f32 v64,v64
v_mfma_f32_16x16x32_bf16 v[48:51],a[76:79],v[124:127],v[48:51]
v_exp_f32 v65,v65
v_mfma_f32_16x16x32_bf16 v[48:51],a[64:67],v[120:123],v[48:51]
ds_read_b128 v[104:107],v5 offset:16128
v_mul_f32 v66,v34,v61
v_mfma_f32_16x16x32_bf16 v[48:51],a[52:55],v[116:119],v[48:51]
v_exp_f32 v66,v66
v_mfma_f32_16x16x32_bf16 v[44:47],a[48:51],v[116:119],v[44:47]
v_mul_f32 v67,v35,v61
v_mul_f32 v68,v40,v61
v_mfma_f32_16x16x32_bf16 v[44:47],a[60:63],v[120:123],v[44:47]
ds_read_b128 v[108:111],v5 offset:17280
v_mul_f32 v69,v41,v61
v_mfma_f32_16x16x32_bf16 v[44:47],a[72:75],v[124:127],v[44:47]
v_exp_f32 v67,v67
v_mfma_f32_16x16x32_bf16 v[44:47],a[84:87],v[76:79],v[44:47]
v_mul_f32 v32,v32,v36
s_waitcnt lgkmcnt(4)
v_mfma_f32_16x16x32_bf16 v[44:47],a[96:99],v[80:83],v[44:47]
ds_read_b128 v[112:115],v5 offset:18432
v_mul_f32 v33,v33,v37
v_mfma_f32_16x16x32_bf16 v[44:47],a[108:111],v[84:87],v[44:47]
v_exp_f32 v68,v68
v_mfma_f32_16x16x32_bf16 v[44:47],a[120:123],v[88:91],v[44:47]
v_exp_f32 v69,v69
v_mfma_f32_16x16x32_bf16 v[44:47],a[132:135],v[92:95],v[44:47]
ds_read_b128 v[116:119],v5 offset:19584
v_mul_f32 v34,v34,v38
v_mfma_f32_16x16x32_bf16 v[48:51],a[136:139],v[92:95],v[48:51]
v_mul_f32 v35,v35,v39
v_mul_f32 v40,v40,v42
v_mfma_f32_16x16x32_bf16 v[48:51],a[124:127],v[88:91],v[48:51]
v_mul_f32 v41,v41,v43
v_fma_f32 v64,v64,v62,v62
v_mfma_f32_16x16x32_bf16 v[48:51],a[112:115],v[84:87],v[48:51]
ds_read_b128 v[120:123],v5 offset:20736
v_fma_f32 v65,v65,v62,v62
v_mfma_f32_16x16x32_bf16 v[48:51],a[100:103],v[80:83],v[48:51]
v_rcp_f32 v64,v64
v_mfma_f32_16x16x32_bf16 v[52:55],a[104:107],v[80:83],v[52:55]
v_rcp_f32 v65,v65
v_mfma_f32_16x16x32_bf16 v[52:55],a[116:119],v[84:87],v[52:55]
ds_read_b128 v[124:127],v5 offset:21888
v_fma_f32 v66,v66,v62,v62
v_mfma_f32_16x16x32_bf16 v[52:55],a[128:131],v[88:91],v[52:55]
v_rcp_f32 v66,v66
v_mfma_f32_16x16x32_bf16 v[52:55],a[140:143],v[92:95],v[52:55]
v_fma_f32 v67,v67,v62,v62
s_waitcnt lgkmcnt(4)
v_mfma_f32_16x16x32_bf16 v[52:55],a[152:155],v[96:99],v[52:55]
ds_read_b128 v[76:79],v5 offset:23040
v_fma_f32 v68,v68,v62,v62
v_mfma_f32_16x16x32_bf16 v[52:55],a[164:167],v[100:103],v[52:55]
v_rcp_f32 v67,v67
v_mfma_f32_16x16x32_bf16 v[52:55],a[176:179],v[104:107],v[52:55]
v_rcp_f32 v68,v68
v_mfma_f32_16x16x32_bf16 v[52:55],a[188:191],v[108:111],v[52:55]
ds_read_b128 v[80:83],v5 offset:24192
v_fma_f32 v69,v69,v62,v62
v_mfma_f32_16x16x32_bf16 v[48:51],a[184:187],v[108:111],v[48:51]
v_rcp_f32 v69,v69
v_mfma_f32_16x16x32_bf16 v[48:51],a[172:175],v[104:107],v[48:51]
v_mul_f32 v32,v32,v64
v_mul_f32 v33,v33,v65
v_mfma_f32_16x16x32_bf16 v[48:51],a[160:163],v[100:103],v[48:51]
ds_read_b128 v[84:87],v5 offset:25344
v_mul_f32 v34,v34,v66
v_mfma_f32_16x16x32_bf16 v[48:51],a[148:151],v[96:99],v[48:51]
v_mul_f32 v35,v35,v67
v_mul_f32 v40,v40,v68
v_mfma_f32_16x16x32_bf16 v[44:47],a[144:147],v[96:99],v[44:47]
v_mul_f32 v41,v41,v69
v_cvt_pk_bf16_f32 v72,v32,v33
v_mfma_f32_16x16x32_bf16 v[44:47],a[156:159],v[100:103],v[44:47]
ds_read_b128 v[88:91],v5 offset:26496
v_cvt_pk_bf16_f32 v74,v40,v41
v_mfma_f32_16x16x32_bf16 v[44:47],a[168:171],v[104:107],v[44:47]
v_cvt_pk_bf16_f32 v73,v34,v35
v_add_f32_dpp v56,v56,v56 row_ror:2 row_mask:0xf bank_mask:0xf
v_mfma_f32_16x16x32_bf16 v[44:47],a[180:183],v[108:111],v[44:47]
v_mul_f32 v56,0x3a800000,v56
s_waitcnt lgkmcnt(4)
v_mfma_f32_16x16x32_bf16 v[44:47],a[192:195],v[112:115],v[44:47]
ds_read_b128 v[92:95],v5 offset:27648
v_mul_f32 v57,v56,v56
v_mfma_f32_16x16x32_bf16 v[44:47],a[204:207],v[116:119],v[44:47]
v_permlane32_swap_b32 v74,v75
v_mfma_f32_16x16x32_bf16 v[44:47],a[216:219],v[120:123],v[44:47]
v_sub_f32_dpp v57,v56,v57 quad_perm:[1,1,3,3] row_mask:0xf bank_mask:0xf
v_mfma_f32_16x16x32_bf16 v[44:47],a[228:231],v[124:127],v[44:47]
ds_read_b128 v[96:99],v5 offset:28800
v_max_f32 v57,0,v57
v_mfma_f32_16x16x32_bf16 v[48:51],a[232:235],v[124:127],v[48:51]
v_permlane16_swap_b32 v72,v74
v_mfma_f32_16x16x32_bf16 v[48:51],a[220:223],v[120:123],v[48:51]
v_permlane16_swap_b32 v73,v75
v_mfma_f32_16x16x32_bf16 v[48:51],a[208:211],v[116:119],v[48:51]
buffer_store_dwordx4 v[72:75],v15,s[32:35],s68 offen sc1
ds_read_b128 v[100:103],v5 offset:29952
v_mfma_f32_16x16x32_bf16 v[48:51],a[196:199],v[112:115],v[48:51]
v_cvt_pk_bf16_f32 v57,0,v57
v_mfma_f32_16x16x32_bf16 v[52:55],a[200:203],v[112:115],v[52:55]
v_add_f32 v57,0x3727c5ac,v57
v_mfma_f32_16x16x32_bf16 v[52:55],a[212:215],v[116:119],v[52:55]
ds_read_b128 v[104:107],v5 offset:31104
v_cvt_pk_bf16_f32 v57,0,v57
v_mfma_f32_16x16x32_bf16 v[52:55],a[224:227],v[120:123],v[52:55]
v_sqrt_f32 v57,v57
v_mfma_f32_16x16x32_bf16 v[52:55],a[236:239],v[124:127],v[52:55]
v_cvt_pk_bf16_f32 v57,v56,v57
s_waitcnt lgkmcnt(4)
v_mfma_f32_16x16x32_bf16 v[52:55],a[248:251],v[76:79],v[52:55]
ds_read_b128 v[108:111],v5 offset:32256
ds_write_b32 v10,v57 offset:128
v_mfma_f32_16x16x32_bf16 v[52:55],v[132:135],v[80:83],v[52:55]
v_mfma_f32_16x16x32_bf16 v[52:55],v[144:147],v[84:87],v[52:55]
v_mfma_f32_16x16x32_bf16 v[52:55],v[156:159],v[88:91],v[52:55]
ds_read_b128 v[112:115],v5 offset:33408
v_mfma_f32_16x16x32_bf16 v[48:51],v[152:155],v[88:91],v[48:51]
v_mfma_f32_16x16x32_bf16 v[48:51],v[140:143],v[84:87],v[48:51]
v_mfma_f32_16x16x32_bf16 v[48:51],v[128:131],v[80:83],v[48:51]
ds_read_b128 v[116:119],v5 offset:34560
v_mfma_f32_16x16x32_bf16 v[48:51],a[244:247],v[76:79],v[48:51]
v_mfma_f32_16x16x32_bf16 v[44:47],a[240:243],v[76:79],v[44:47]
v_mfma_f32_16x16x32_bf16 v[44:47],a[252:255],v[80:83],v[44:47]
ds_read_b128 v[120:123],v5 offset:35712
v_mfma_f32_16x16x32_bf16 v[44:47],v[136:139],v[84:87],v[44:47]
v_xor_b32 v5,73728,v5
v_mfma_f32_16x16x32_bf16 v[44:47],v[148:151],v[88:91],v[44:47]
s_waitcnt lgkmcnt(5)
s_waitcnt vmcnt(1)
s_waitcnt lgkmcnt(3)
s_barrier
v_mfma_f32_16x16x32_bf16 v[44:47],v[160:163],v[92:95],v[44:47]
ds_read_b128 v[76:79],v4 offset:0
s_add_u32 m0,s64,36864
v_mfma_f32_16x16x32_bf16 v[44:47],v[172:175],v[96:99],v[44:47]
buffer_load_dwordx4 v2,s[32:35],s63 offen offset:0 sc1 lds
v_mfma_f32_16x16x32_bf16 v[44:47],v[184:187],v[100:103],v[44:47]
ds_read_b32 v62,v11 offset:128
v_mfma_f32_16x16x32_bf16 v[44:47],v[196:199],v[104:107],v[44:47]
ds_read_b128 v[80:83],v4 offset:1152
v_mfma_f32_16x16x32_bf16 v[48:51],v[200:203],v[104:107],v[48:51]
v_mfma_f32_16x16x32_bf16 v[48:51],v[188:191],v[100:103],v[48:51]
buffer_load_dwordx4 v2,s[32:35],s63 offen offset:1024 sc1 lds
v_mfma_f32_16x16x32_bf16 v[48:51],v[176:179],v[96:99],v[48:51]
ds_read_b128 v[84:87],v4 offset:2304
ds_read2st64_b32 v[56:57],v8 offset0:0 offset1:36
v_mfma_f32_16x16x32_bf16 v[48:51],v[164:167],v[92:95],v[48:51]
ds_read2st64_b32 v[58:59],v8 offset0:72 offset1:108
v_mfma_f32_16x16x32_bf16 v[52:55],v[168:171],v[92:95],v[52:55]
s_waitcnt lgkmcnt(4)
v_lshlrev_b32 v60,16,v62
v_mfma_f32_16x16x32_bf16 v[52:55],v[180:183],v[96:99],v[52:55]
buffer_load_dwordx4 v2,s[32:35],s63 offen offset:2048 sc1 lds
ds_read_b128 v[88:91],v4 offset:3456
v_mfma_f32_16x16x32_bf16 v[52:55],v[192:195],v[100:103],v[52:55]
v_and_b32 v62,0xffff0000,v62
v_mfma_f32_16x16x32_bf16 v[52:55],v[204:207],v[104:107],v[52:55]
v_mfma_f32_16x16x32_bf16 v[52:55],v[216:219],v[108:111],v[52:55]
ds_read_b128 v[92:95],v4 offset:4608
v_mfma_f32_16x16x32_bf16 v[52:55],v[228:231],v[112:115],v[52:55]
buffer_load_dwordx4 v2,s[32:35],s63 offen offset:3072 sc1 lds
v_mfma_f32_16x16x32_bf16 v[52:55],v[240:243],v[116:119],v[52:55]
v_rcp_f32 v61,v62
v_mfma_f32_16x16x32_bf16 v[52:55],v[252:255],v[120:123],v[52:55]
ds_read_b128 v[96:99],v4 offset:5760
v_mul_f32 v62,v62,v62
v_mfma_f32_16x16x32_bf16 v[48:51],v[248:251],v[120:123],v[48:51]
s_add_u32 m0,s64,40960
s_waitcnt lgkmcnt(3)
v_add_f32 v56,v56,v57
v_mfma_f32_16x16x32_bf16 v[48:51],v[236:239],v[116:119],v[48:51]
buffer_load_dwordx4 v6,s[32:35],s63 offen offset:0 sc1 lds
v_add_f32 v58,v58,v59
v_mfma_f32_16x16x32_bf16 v[48:51],v[224:227],v[112:115],v[48:51]
ds_read_b128 v[100:103],v4 offset:6912
v_add_f32 v56,v56,v58
v_mfma_f32_16x16x32_bf16 v[48:51],v[212:215],v[108:111],v[48:51]
v_mul_f32 v61,0xbfb8aa3b,v61
v_add_f32_dpp v56,v56,v56 row_ror:8 row_mask:0xf bank_mask:0xf
v_mfma_f32_16x16x32_bf16 v[44:47],v[208:211],v[108:111],v[44:47]
v_mfma_f32_16x16x32_bf16 v[44:47],v[220:223],v[112:115],v[44:47]
buffer_load_dwordx4 v6,s[32:35],s63 offen offset:1024 sc1 lds
ds_read_b128 v[104:107],v4 offset:8064
v_mfma_f32_16x16x32_bf16 v[44:47],v[232:235],v[116:119],v[44:47]
v_add_f32_dpp v56,v56,v56 row_ror:4 row_mask:0xf bank_mask:0xf
s_sub_u32 s62,s62,1
v_mfma_f32_16x16x32_bf16 v[44:47],v[244:247],v[120:123],v[44:47]
s_cbranch_scc0 .L_gl_0
s_nop 1
s_add_u32 s68,s68,s30
s_waitcnt vmcnt(6)
s_barrier
buffer_store_dwordx2 v[16:17],v2,s[92:95],s68 idxen sc1
v_fma_f32 v52,-v60,v28,v52
v_fma_f32 v53,-v60,v29,v53
v_fma_f32 v54,-v60,v30,v54
v_fma_f32 v55,-v60,v31,v55
v_fma_f32 v48,-v60,v24,v48
v_fma_f32 v49,-v60,v25,v49
v_fma_f32 v50,-v60,v26,v50
v_fma_f32 v51,-v60,v27,v51
v_fma_f32 v44,-v60,v20,v44
v_fma_f32 v45,-v60,v21,v45
v_fma_f32 v46,-v60,v22,v46
v_fma_f32 v47,-v60,v23,v47
v_permlane32_swap_b32 v52,v54
v_permlane32_swap_b32 v53,v55
v_mul_f32 v64,v44,v61
v_mul_f32 v65,v45,v61
v_mul_f32 v66,v46,v61
v_mul_f32 v67,v47,v61
v_mul_f32 v68,v52,v61
v_mul_f32 v69,v53,v61
v_exp_f32 v64,v64
v_exp_f32 v65,v65
v_mul_f32 v44,v44,v48
v_mul_f32 v45,v45,v49
v_exp_f32 v66,v66
v_exp_f32 v67,v67
v_mul_f32 v46,v46,v50
v_mul_f32 v47,v47,v51
v_exp_f32 v68,v68
v_exp_f32 v69,v69
v_mul_f32 v52,v52,v54
v_mul_f32 v53,v53,v55
v_fma_f32 v64,v64,v62,v62
v_fma_f32 v65,v65,v62,v62
v_rcp_f32 v64,v64
v_rcp_f32 v65,v65
v_fma_f32 v66,v66,v62,v62
v_fma_f32 v67,v67,v62,v62
v_rcp_f32 v66,v66
v_rcp_f32 v67,v67
v_fma_f32 v68,v68,v62,v62
v_fma_f32 v69,v69,v62,v62
v_rcp_f32 v68,v68
v_rcp_f32 v69,v69
v_mul_f32 v44,v44,v64
v_mul_f32 v45,v45,v65
v_mul_f32 v46,v46,v66
v_mul_f32 v47,v47,v67
v_mul_f32 v52,v52,v68
v_mul_f32 v53,v53,v69
v_cvt_pk_bf16_f32 v74,v52,v53
v_cvt_pk_bf16_f32 v72,v44,v45
v_cvt_pk_bf16_f32 v73,v46,v47
v_permlane32_swap_b32 v74,v75
s_nop 1
v_permlane16_swap_b32 v72,v74
v_permlane16_swap_b32 v73,v75
buffer_store_dwordx4 v[72:75],v15,s[32:35],s68 offen sc1
s_add_u32 s68,s68,s30
s_waitcnt vmcnt(0)
s_barrier
buffer_store_dwordx2 v[16:17],v2,s[92:95],s68 idxen sc1
s_branch .L_end
.L_gt0_0:
s_nop 1
s_add_u32 s68,s68,s30
s_waitcnt vmcnt(6)
s_barrier
buffer_store_dwordx2 v[16:17],v2,s[92:95],s68 idxen sc1
v_fma_f32 v40,-v60,v28,v40
v_fma_f32 v41,-v60,v29,v41
v_fma_f32 v42,-v60,v30,v42
v_fma_f32 v43,-v60,v31,v43
v_fma_f32 v36,-v60,v24,v36
v_fma_f32 v37,-v60,v25,v37
v_fma_f32 v38,-v60,v26,v38
v_fma_f32 v39,-v60,v27,v39
v_fma_f32 v32,-v60,v20,v32
v_fma_f32 v33,-v60,v21,v33
v_fma_f32 v34,-v60,v22,v34
v_fma_f32 v35,-v60,v23,v35
v_permlane32_swap_b32 v40,v42
v_permlane32_swap_b32 v41,v43
v_mul_f32 v64,v32,v61
v_mul_f32 v65,v33,v61
v_mul_f32 v66,v34,v61
v_mul_f32 v67,v35,v61
v_mul_f32 v68,v40,v61
v_mul_f32 v69,v41,v61
v_exp_f32 v64,v64
v_exp_f32 v65,v65
v_mul_f32 v32,v32,v36
v_mul_f32 v33,v33,v37
v_exp_f32 v66,v66
v_exp_f32 v67,v67
v_mul_f32 v34,v34,v38
v_mul_f32 v35,v35,v39
v_exp_f32 v68,v68
v_exp_f32 v69,v69
v_mul_f32 v40,v40,v42
v_mul_f32 v41,v41,v43
v_fma_f32 v64,v64,v62,v62
v_fma_f32 v65,v65,v62,v62
v_rcp_f32 v64,v64
v_rcp_f32 v65,v65
v_fma_f32 v66,v66,v62,v62
v_fma_f32 v67,v67,v62,v62
v_rcp_f32 v66,v66
v_rcp_f32 v67,v67
v_fma_f32 v68,v68,v62,v62
v_fma_f32 v69,v69,v62,v62
v_rcp_f32 v68,v68
v_rcp_f32 v69,v69
v_mul_f32 v32,v32,v64
v_mul_f32 v33,v33,v65
v_mul_f32 v34,v34,v66
v_mul_f32 v35,v35,v67
v_mul_f32 v40,v40,v68
v_mul_f32 v41,v41,v69
v_cvt_pk_bf16_f32 v74,v40,v41
v_cvt_pk_bf16_f32 v72,v32,v33
v_cvt_pk_bf16_f32 v73,v34,v35
v_permlane32_swap_b32 v74,v75
s_nop 1
v_permlane16_swap_b32 v72,v74
v_permlane16_swap_b32 v73,v75
buffer_store_dwordx4 v[72:75],v15,s[32:35],s68 offen sc1
s_add_u32 s68,s68,s30
s_waitcnt vmcnt(0)
s_barrier
buffer_store_dwordx2 v[16:17],v2,s[92:95],s68 idxen sc1
s_branch .L_end
.p2align 6
.L_gv1:
.L_gl_1:
s_waitcnt lgkmcnt(4)
v_mfma_f32_16x16x32_bf16 v[32:35],a[0:3],v[76:79],0
ds_read_b128 v[108:111],v4 offset:9216
s_add_u32 s68,s68,s30
v_mfma_f32_16x16x32_bf16 v[32:35],a[12:15],v[80:83],v[32:35]
buffer_load_dwordx4 v6,s[32:35],s63 offen offset:2048 sc1 lds
v_fma_f32 v52,-v60,v28,v52
v_mfma_f32_16x16x32_bf16 v[32:35],a[24:27],v[84:87],v[32:35]
v_fma_f32 v53,-v60,v29,v53
v_fma_f32 v54,-v60,v30,v54
v_mfma_f32_16x16x32_bf16 v[32:35],a[36:39],v[88:91],v[32:35]
ds_read_b128 v[112:115],v4 offset:10368
v_fma_f32 v55,-v60,v31,v55
v_mfma_f32_16x16x32_bf16 v[36:39],a[40:43],v[88:91],0
v_permlane32_swap_b32 v52,v54
v_mfma_f32_16x16x32_bf16 v[36:39],a[28:31],v[84:87],v[36:39]
buffer_load_dwordx4 v6,s[32:35],s63 offen offset:3072 sc1 lds
v_fma_f32 v48,-v60,v24,v48
v_mfma_f32_16x16x32_bf16 v[36:39],a[16:19],v[80:83],v[36:39]
ds_read_b128 v[116:119],v4 offset:11520
v_fma_f32 v49,-v60,v25,v49
v_mfma_f32_16x16x32_bf16 v[36:39],a[4:7],v[76:79],v[36:39]
v_permlane32_swap_b32 v53,v55
v_mfma_f32_16x16x32_bf16 v[40:43],a[8:11],v[76:79],0
v_fma_f32 v50,-v60,v26,v50
v_fma_f32 v51,-v60,v27,v51
v_mfma_f32_16x16x32_bf16 v[40:43],a[20:23],v[80:83],v[40:43]
ds_read_b128 v[120:123],v4 offset:12672
v_fma_f32 v44,-v60,v20,v44
v_mfma_f32_16x16x32_bf16 v[40:43],a[32:35],v[84:87],v[40:43]
s_waitcnt vmcnt(8)
s_barrier
v_mfma_f32_16x16x32_bf16 v[40:43],a[44:47],v[88:91],v[40:43]
v_fma_f32 v45,-v60,v21,v45
s_waitcnt lgkmcnt(4)
v_mfma_f32_16x16x32_bf16 v[40:43],a[56:59],v[92:95],v[40:43]
ds_read_b128 v[124:127],v4 offset:13824
s_add_u32 m0,s64,45056
v_mfma_f32_16x16x32_bf16 v[40:43],a[68:71],v[96:99],v[40:43]
buffer_load_dwordx4 v7,s[32:35],s63 offen offset:0 sc1 lds
s_add_u32 s63,s63,s30
v_mfma_f32_16x16x32_bf16 v[40:43],a[80:83],v[100:103],v[40:43]
s_xor_b32 s64,s64,s65
v_fma_f32 v46,-v60,v22,v46
v_mfma_f32_16x16x32_bf16 v[40:43],a[92:95],v[104:107],v[40:43]
ds_read_b128 v[76:79],v4 offset:14976
v_fma_f32 v47,-v60,v23,v47
v_mfma_f32_16x16x32_bf16 v[36:39],a[88:91],v[104:107],v[36:39]
v_mul_f32 v64,v44,v61
v_mul_f32 v65,v45,v61
v_mfma_f32_16x16x32_bf16 v[36:39],a[76:79],v[100:103],v[36:39]
buffer_load_dwordx2 v[12:13],v14,s[0:3],s66 offen sc1
v_mul_f32 v66,v46,v61
v_mfma_f32_16x16x32_bf16 v[36:39],a[64:67],v[96:99],v[36:39]
ds_read_b128 v[80:83],v4 offset:16128
v_mul_f32 v67,v47,v61
v_mfma_f32_16x16x32_bf16 v[36:39],a[52:55],v[92:95],v[36:39]
v_exp_f32 v64,v64
v_mfma_f32_16x16x32_bf16 v[32:35],a[48:51],v[92:95],v[32:35]
v_exp_f32 v65,v65
v_mfma_f32_16x16x32_bf16 v[32:35],a[60:63],v[96:99],v[32:35]
ds_read_b128 v[84:87],v4 offset:17280
v_mul_f32 v68,v52,v61
v_mfma_f32_16x16x32_bf16 v[32:35],a[72:75],v[100:103],v[32:35]
v_exp_f32 v66,v66
v_mfma_f32_16x16x32_bf16 v[32:35],a[84:87],v[104:107],v[32:35]
v_mul_f32 v69,v53,v61
s_waitcnt lgkmcnt(4)
v_mfma_f32_16x16x32_bf16 v[32:35],a[96:99],v[108:111],v[32:35]
ds_read_b128 v[88:91],v4 offset:18432
v_mul_f32 v44,v44,v48
v_mfma_f32_16x16x32_bf16 v[32:35],a[108:111],v[112:115],v[32:35]
v_exp_f32 v67,v67
v_mfma_f32_16x16x32_bf16 v[32:35],a[120:123],v[116:119],v[32:35]
v_exp_f32 v68,v68
v_mfma_f32_16x16x32_bf16 v[32:35],a[132:135],v[120:123],v[32:35]
ds_read_b128 v[92:95],v4 offset:19584
v_mul_f32 v45,v45,v49
v_mfma_f32_16x16x32_bf16 v[36:39],a[136:139],v[120:123],v[36:39]
v_exp_f32 v69,v69
v_mfma_f32_16x16x32_bf16 v[36:39],a[124:127],v[116:119],v[36:39]
v_mul_f32 v46,v46,v50
v_mul_f32 v47,v47,v51
v_mfma_f32_16x16x32_bf16 v[36:39],a[112:115],v[112:115],v[36:39]
ds_read_b128 v[96:99],v4 offset:20736
v_mul_f32 v52,v52,v54
v_mfma_f32_16x16x32_bf16 v[36:39],a[100:103],v[108:111],v[36:39]
v_mul_f32 v53,v53,v55
v_fma_f32 v64,v64,v62,v62
v_mfma_f32_16x16x32_bf16 v[40:43],a[104:107],v[108:111],v[40:43]
v_rcp_f32 v64,v64
v_mfma_f32_16x16x32_bf16 v[40:43],a[116:119],v[112:115],v[40:43]
ds_read_b128 v[100:103],v4 offset:21888
v_fma_f32 v65,v65,v62,v62
v_mfma_f32_16x16x32_bf16 v[40:43],a[128:131],v[116:119],v[40:43]
v_rcp_f32 v65,v65
v_mfma_f32_16x16x32_bf16 v[40:43],a[140:143],v[120:123],v[40:43]
v_fma_f32 v66,v66,v62,v62
s_waitcnt lgkmcnt(4)
v_mfma_f32_16x16x32_bf16 v[40:43],a[152:155],v[124:127],v[40:43]
ds_read_b128 v[104:107],v4 offset:23040
v_fma_f32 v67,v67,v62,v62
v_mfma_f32_16x16x32_bf16 v[40:43],a[164:167],v[76:79],v[40:43]
v_rcp_f32 v66,v66
v_mfma_f32_16x16x32_bf16 v[40:43],a[176:179],v[80:83],v[40:43]
v_rcp_f32 v67,v67
v_mfma_f32_16x16x32_bf16 v[40:43],a[188:191],v[84:87],v[40:43]
ds_read_b128 v[108:111],v4 offset:24192
v_fma_f32 v68,v68,v62,v62
v_mfma_f32_16x16x32_bf16 v[36:39],a[184:187],v[84:87],v[36:39]
v_rcp_f32 v68,v68
v_mfma_f32_16x16x32_bf16 v[36:39],a[172:175],v[80:83],v[36:39]
v_fma_f32 v69,v69,v62,v62
v_mul_f32 v44,v44,v64
v_mfma_f32_16x16x32_bf16 v[36:39],a[160:163],v[76:79],v[36:39]
ds_read_b128 v[112:115],v4 offset:25344
v_mul_f32 v45,v45,v65
v_mfma_f32_16x16x32_bf16 v[36:39],a[148:151],v[124:127],v[36:39]
v_rcp_f32 v69,v69
v_mfma_f32_16x16x32_bf16 v[32:35],a[144:147],v[124:127],v[32:35]
v_mul_f32 v46,v46,v66
v_mul_f32 v47,v47,v67
v_mfma_f32_16x16x32_bf16 v[32:35],a[156:159],v[76:79],v[32:35]
ds_read_b128 v[116:119],v4 offset:26496
v_mul_f32 v52,v52,v68
v_mfma_f32_16x16x32_bf16 v[32:35],a[168:171],v[80:83],v[32:35]
v_mul_f32 v53,v53,v69
v_cvt_pk_bf16_f32 v72,v44,v45
v_mfma_f32_16x16x32_bf16 v[32:35],a[180:183],v[84:87],v[32:35]
v_cvt_pk_bf16_f32 v74,v52,v53
s_waitcnt lgkmcnt(4)
v_mfma_f32_16x16x32_bf16 v[32:35],a[192:195],v[88:91],v[32:35]
ds_read_b128 v[120:123],v4 offset:27648
v_cvt_pk_bf16_f32 v73,v46,v47
v_mfma_f32_16x16x32_bf16 v[32:35],a[204:207],v[92:95],v[32:35]
v_permlane32_swap_b32 v74,v75
v_mfma_f32_16x16x32_bf16 v[32:35],a[216:219],v[96:99],v[32:35]
v_add_f32_dpp v56,v56,v56 row_ror:2 row_mask:0xf bank_mask:0xf
v_mfma_f32_16x16x32_bf16 v[32:35],a[228:231],v[100:103],v[32:35]
ds_read_b128 v[124:127],v4 offset:28800
v_mul_f32 v56,0x3a800000,v56
v_mfma_f32_16x16x32_bf16 v[36:39],a[232:235],v[100:103],v[36:39]
v_permlane16_swap_b32 v72,v74
v_mfma_f32_16x16x32_bf16 v[36:39],a[220:223],v[96:99],v[36:39]
v_permlane16_swap_b32 v73,v75
v_mfma_f32_16x16x32_bf16 v[36:39],a[208:211],v[92:95],v[36:39]
buffer_store_dwordx4 v[72:75],v15,s[32:35],s68 offen sc1
ds_read_b128 v[76:79],v4 offset:29952
v_mfma_f32_16x16x32_bf16 v[36:39],a[196:199],v[88:91],v[36:39]
v_mul_f32 v57,v56,v56
v_mfma_f32_16x16x32_bf16 v[40:43],a[200:203],v[88:91],v[40:43]
v_mfma_f32_16x16x32_bf16 v[40:43],a[212:215],v[92:95],v[40:43]
ds_read_b128 v[80:83],v4 offset:31104
v_sub_f32_dpp v57,v56,v57 quad_perm:[1,1,3,3] row_mask:0xf bank_mask:0xf
v_mfma_f32_16x16x32_bf16 v[40:43],a[224:227],v[96:99],v[40:43]
v_max_f32 v57,0,v57
v_mfma_f32_16x16x32_bf16 v[40:43],a[236:239],v[100:103],v[40:43]
v_cvt_pk_bf16_f32 v57,0,v57
s_waitcnt lgkmcnt(4)
v_mfma_f32_16x16x32_bf16 v[40:43],a[248:251],v[104:107],v[40:43]
ds_read_b128 v[84:87],v4 offset:32256
v_add_f32 v57,0x3727c5ac,v57
v_mfma_f32_16x16x32_bf16 v[40:43],v[132:135],v[108:111],v[40:43]
v_cvt_pk_bf16_f32 v57,0,v57
v_mfma_f32_16x16x32_bf16 v[40:43],v[144:147],v[112:115],v[40:43]
v_sqrt_f32 v57,v57
v_mfma_f32_16x16x32_bf16 v[40:43],v[156:159],v[116:119],v[40:43]
)#"
R"#(
ds_read_b128 v[88:91],v4 offset:33408
v_cvt_pk_bf16_f32 v57,v56,v57
v_mfma_f32_16x16x32_bf16 v[36:39],v[152:155],v[116:119],v[36:39]
ds_write_b32 v10,v57 offset:0
v_mfma_f32_16x16x32_bf16 v[36:39],v[140:143],v[112:115],v[36:39]
v_mfma_f32_16x16x32_bf16 v[36:39],v[128:131],v[108:111],v[36:39]
ds_read_b128 v[92:95],v4 offset:34560
s_waitcnt vmcnt(1)
v_mfma_f32_16x16x32_bf16 v[36:39],a[244:247],v[104:107],v[36:39]
v_cmp_ne_u64 vcc,v[16:17],v[12:13]
v_mfma_f32_16x16x32_bf16 v[32:35],a[240:243],v[104:107],v[32:35]
s_cbranch_vccnz .L_rtg0_1
.L_bkg0_1:
s_add_u32 s66,s66,s31
v_mfma_f32_16x16x32_bf16 v[32:35],a[252:255],v[108:111],v[32:35]
ds_read_b128 v[96:99],v4 offset:35712
v_mfma_f32_16x16x32_bf16 v[32:35],v[136:139],v[112:115],v[32:35]
v_xor_b32 v4,73728,v4
s_waitcnt lgkmcnt(2)
s_barrier
v_mfma_f32_16x16x32_bf16 v[32:35],v[148:151],v[116:119],v[32:35]
v_mfma_f32_16x16x32_bf16 v[32:35],v[160:163],v[120:123],v[32:35]
ds_read_b128 v[100:103],v5 offset:0
s_add_u32 m0,s64,0
v_mfma_f32_16x16x32_bf16 v[32:35],v[172:175],v[124:127],v[32:35]
buffer_load_dwordx4 v2,s[32:35],s63 offen offset:0 sc1 lds
v_mfma_f32_16x16x32_bf16 v[32:35],v[184:187],v[76:79],v[32:35]
ds_read_b32 v62,v11 offset:0
v_mfma_f32_16x16x32_bf16 v[32:35],v[196:199],v[80:83],v[32:35]
ds_read_b128 v[104:107],v5 offset:1152
v_mfma_f32_16x16x32_bf16 v[36:39],v[200:203],v[80:83],v[36:39]
v_mfma_f32_16x16x32_bf16 v[36:39],v[188:191],v[76:79],v[36:39]
buffer_load_dwordx4 v2,s[32:35],s63 offen offset:1024 sc1 lds
v_mfma_f32_16x16x32_bf16 v[36:39],v[176:179],v[124:127],v[36:39]
ds_read_b128 v[108:111],v5 offset:2304
ds_read2st64_b32 v[56:57],v8 offset0:144 offset1:180
v_mfma_f32_16x16x32_bf16 v[36:39],v[164:167],v[120:123],v[36:39]
ds_read2st64_b32 v[58:59],v8 offset0:216 offset1:252
v_xor_b32 v8,v9,v8
v_mfma_f32_16x16x32_bf16 v[40:43],v[168:171],v[120:123],v[40:43]
s_waitcnt lgkmcnt(4)
v_lshlrev_b32 v60,16,v62
v_mfma_f32_16x16x32_bf16 v[40:43],v[180:183],v[124:127],v[40:43]
buffer_load_dwordx4 v2,s[32:35],s63 offen offset:2048 sc1 lds
ds_read_b128 v[112:115],v5 offset:3456
v_mfma_f32_16x16x32_bf16 v[40:43],v[192:195],v[76:79],v[40:43]
v_and_b32 v62,0xffff0000,v62
v_mfma_f32_16x16x32_bf16 v[40:43],v[204:207],v[80:83],v[40:43]
v_mfma_f32_16x16x32_bf16 v[40:43],v[216:219],v[84:87],v[40:43]
ds_read_b128 v[116:119],v5 offset:4608
v_mfma_f32_16x16x32_bf16 v[40:43],v[228:231],v[88:91],v[40:43]
buffer_load_dwordx4 v2,s[32:35],s63 offen offset:3072 sc1 lds
v_mfma_f32_16x16x32_bf16 v[40:43],v[240:243],v[92:95],v[40:43]
v_rcp_f32 v61,v62
v_mfma_f32_16x16x32_bf16 v[40:43],v[252:255],v[96:99],v[40:43]
ds_read_b128 v[120:123],v5 offset:5760
v_mul_f32 v62,v62,v62
v_mfma_f32_16x16x32_bf16 v[36:39],v[248:251],v[96:99],v[36:39]
s_add_u32 m0,s64,4096
s_waitcnt lgkmcnt(3)
v_add_f32 v56,v56,v57
v_mfma_f32_16x16x32_bf16 v[36:39],v[236:239],v[92:95],v[36:39]
buffer_load_dwordx4 v6,s[32:35],s63 offen offset:0 sc1 lds
v_add_f32 v58,v58,v59
v_mfma_f32_16x16x32_bf16 v[36:39],v[224:227],v[88:91],v[36:39]
ds_read_b128 v[124:127],v5 offset:6912
v_add_f32 v56,v56,v58
v_mfma_f32_16x16x32_bf16 v[36:39],v[212:215],v[84:87],v[36:39]
v_mul_f32 v61,0xbfb8aa3b,v61
v_add_f32_dpp v56,v56,v56 row_ror:8 row_mask:0xf bank_mask:0xf
v_mfma_f32_16x16x32_bf16 v[32:35],v[208:211],v[84:87],v[32:35]
v_mfma_f32_16x16x32_bf16 v[32:35],v[220:223],v[88:91],v[32:35]
buffer_load_dwordx4 v6,s[32:35],s63 offen offset:1024 sc1 lds
ds_read_b128 v[76:79],v5 offset:8064
v_mfma_f32_16x16x32_bf16 v[32:35],v[232:235],v[92:95],v[32:35]
v_add_f32_dpp v56,v56,v56 row_ror:4 row_mask:0xf bank_mask:0xf
s_sub_u32 s62,s62,1
v_mfma_f32_16x16x32_bf16 v[32:35],v[244:247],v[96:99],v[32:35]
s_cbranch_scc1 .L_gt0_1
.L_gm_1:
s_waitcnt lgkmcnt(4)
v_mfma_f32_16x16x32_bf16 v[44:47],a[0:3],v[100:103],0
ds_read_b128 v[80:83],v5 offset:9216
s_add_u32 s68,s68,s30
v_mfma_f32_16x16x32_bf16 v[44:47],a[12:15],v[104:107],v[44:47]
buffer_load_dwordx4 v6,s[32:35],s63 offen offset:2048 sc1 lds
v_fma_f32 v40,-v60,v28,v40
v_mfma_f32_16x16x32_bf16 v[44:47],a[24:27],v[108:111],v[44:47]
v_fma_f32 v41,-v60,v29,v41
v_fma_f32 v42,-v60,v30,v42
v_mfma_f32_16x16x32_bf16 v[44:47],a[36:39],v[112:115],v[44:47]
ds_read_b128 v[84:87],v5 offset:10368
v_fma_f32 v43,-v60,v31,v43
v_mfma_f32_16x16x32_bf16 v[48:51],a[40:43],v[112:115],0
v_permlane32_swap_b32 v40,v42
v_mfma_f32_16x16x32_bf16 v[48:51],a[28:31],v[108:111],v[48:51]
buffer_load_dwordx4 v6,s[32:35],s63 offen offset:3072 sc1 lds
v_fma_f32 v36,-v60,v24,v36
v_mfma_f32_16x16x32_bf16 v[48:51],a[16:19],v[104:107],v[48:51]
ds_read_b128 v[88:91],v5 offset:11520
v_fma_f32 v37,-v60,v25,v37
v_mfma_f32_16x16x32_bf16 v[48:51],a[4:7],v[100:103],v[48:51]
v_permlane32_swap_b32 v41,v43
v_mfma_f32_16x16x32_bf16 v[52:55],a[8:11],v[100:103],0
v_fma_f32 v38,-v60,v26,v38
v_fma_f32 v39,-v60,v27,v39
v_mfma_f32_16x16x32_bf16 v[52:55],a[20:23],v[104:107],v[52:55]
ds_read_b128 v[92:95],v5 offset:12672
v_fma_f32 v32,-v60,v20,v32
v_mfma_f32_16x16x32_bf16 v[52:55],a[32:35],v[108:111],v[52:55]
s_waitcnt vmcnt(8)
s_barrier
v_mfma_f32_16x16x32_bf16 v[52:55],a[44:47],v[112:115],v[52:55]
v_fma_f32 v33,-v60,v21,v33
s_waitcnt lgkmcnt(4)
v_mfma_f32_16x16x32_bf16 v[52:55],a[56:59],v[116:119],v[52:55]
ds_read_b128 v[96:99],v5 offset:13824
s_add_u32 m0,s64,8192
v_mfma_f32_16x16x32_bf16 v[52:55],a[68:71],v[120:123],v[52:55]
buffer_load_dwordx4 v7,s[32:35],s63 offen offset:0 sc1 lds
s_add_u32 s63,s63,s30
v_mfma_f32_16x16x32_bf16 v[52:55],a[80:83],v[124:127],v[52:55]
v_fma_f32 v34,-v60,v22,v34
v_fma_f32 v35,-v60,v23,v35
v_mfma_f32_16x16x32_bf16 v[52:55],a[92:95],v[76:79],v[52:55]
ds_read_b128 v[100:103],v5 offset:14976
v_mul_f32 v64,v32,v61
v_mfma_f32_16x16x32_bf16 v[48:51],a[88:91],v[76:79],v[48:51]
v_exp_f32 v64,v64
v_mfma_f32_16x16x32_bf16 v[48:51],a[76:79],v[124:127],v[48:51]
v_mul_f32 v65,v33,v61
v_mul_f32 v66,v34,v61
v_mfma_f32_16x16x32_bf16 v[48:51],a[64:67],v[120:123],v[48:51]
ds_read_b128 v[104:107],v5 offset:16128
v_mul_f32 v67,v35,v61
v_mfma_f32_16x16x32_bf16 v[48:51],a[52:55],v[116:119],v[48:51]
v_exp_f32 v65,v65
v_mfma_f32_16x16x32_bf16 v[44:47],a[48:51],v[116:119],v[44:47]
v_exp_f32 v66,v66
v_mfma_f32_16x16x32_bf16 v[44:47],a[60:63],v[120:123],v[44:47]
ds_read_b128 v[108:111],v5 offset:17280
v_mul_f32 v68,v40,v61
v_mfma_f32_16x16x32_bf16 v[44:47],a[72:75],v[124:127],v[44:47]
v_exp_f32 v67,v67
v_mfma_f32_16x16x32_bf16 v[44:47],a[84:87],v[76:79],v[44:47]
v_mul_f32 v69,v41,v61
s_waitcnt lgkmcnt(4)
v_mfma_f32_16x16x32_bf16 v[44:47],a[96:99],v[80:83],v[44:47]
ds_read_b128 v[112:115],v5 offset:18432
v_mul_f32 v32,v32,v36
v_mfma_f32_16x16x32_bf16 v[44:47],a[108:111],v[84:87],v[44:47]
v_exp_f32 v68,v68
v_mfma_f32_16x16x32_bf16 v[44:47],a[120:123],v[88:91],v[44:47]
v_exp_f32 v69,v69
v_mfma_f32_16x16x32_bf16 v[44:47],a[132:135],v[92:95],v[44:47]
ds_read_b128 v[116:119],v5 offset:19584
v_mul_f32 v33,v33,v37
v_mfma_f32_16x16x32_bf16 v[48:51],a[136:139],v[92:95],v[48:51]
v_mul_f32 v34,v34,v38
v_mul_f32 v35,v35,v39
v_mfma_f32_16x16x32_bf16 v[48:51],a[124:127],v[88:91],v[48:51]
v_mul_f32 v40,v40,v42
v_mul_f32 v41,v41,v43
v_mfma_f32_16x16x32_bf16 v[48:51],a[112:115],v[84:87],v[48:51]
ds_read_b128 v[120:123],v5 offset:20736
v_fma_f32 v64,v64,v62,v62
v_mfma_f32_16x16x32_bf16 v[48:51],a[100:103],v[80:83],v[48:51]
v_rcp_f32 v64,v64
v_mfma_f32_16x16x32_bf16 v[52:55],a[104:107],v[80:83],v[52:55]
v_fma_f32 v65,v65,v62,v62
v_fma_f32 v66,v66,v62,v62
v_mfma_f32_16x16x32_bf16 v[52:55],a[116:119],v[84:87],v[52:55]
ds_read_b128 v[124:127],v5 offset:21888
v_fma_f32 v67,v67,v62,v62
v_mfma_f32_16x16x32_bf16 v[52:55],a[128:131],v[88:91],v[52:55]
v_rcp_f32 v65,v65
v_mfma_f32_16x16x32_bf16 v[52:55],a[140:143],v[92:95],v[52:55]
v_fma_f32 v68,v68,v62,v62
s_waitcnt lgkmcnt(4)
v_mfma_f32_16x16x32_bf16 v[52:55],a[152:155],v[96:99],v[52:55]
ds_read_b128 v[76:79],v5 offset:23040
v_fma_f32 v69,v69,v62,v62
v_mfma_f32_16x16x32_bf16 v[52:55],a[164:167],v[100:103],v[52:55]
v_rcp_f32 v66,v66
v_mfma_f32_16x16x32_bf16 v[52:55],a[176:179],v[104:107],v[52:55]
v_rcp_f32 v67,v67
v_mfma_f32_16x16x32_bf16 v[52:55],a[188:191],v[108:111],v[52:55]
ds_read_b128 v[80:83],v5 offset:24192
v_mul_f32 v32,v32,v64
v_mfma_f32_16x16x32_bf16 v[48:51],a[184:187],v[108:111],v[48:51]
v_rcp_f32 v68,v68
v_mfma_f32_16x16x32_bf16 v[48:51],a[172:175],v[104:107],v[48:51]
v_rcp_f32 v69,v69
v_mfma_f32_16x16x32_bf16 v[48:51],a[160:163],v[100:103],v[48:51]
ds_read_b128 v[84:87],v5 offset:25344
v_mul_f32 v33,v33,v65
v_mfma_f32_16x16x32_bf16 v[48:51],a[148:151],v[96:99],v[48:51]
v_mul_f32 v34,v34,v66
v_mul_f32 v35,v35,v67
v_mfma_f32_16x16x32_bf16 v[44:47],a[144:147],v[96:99],v[44:47]
v_mul_f32 v40,v40,v68
v_mul_f32 v41,v41,v69
v_mfma_f32_16x16x32_bf16 v[44:47],a[156:159],v[100:103],v[44:47]
ds_read_b128 v[88:91],v5 offset:26496
v_cvt_pk_bf16_f32 v74,v40,v41
v_mfma_f32_16x16x32_bf16 v[44:47],a[168:171],v[104:107],v[44:47]
v_cvt_pk_bf16_f32 v72,v32,v33
v_cvt_pk_bf16_f32 v73,v34,v35
v_mfma_f32_16x16x32_bf16 v[44:47],a[180:183],v[108:111],v[44:47]
v_add_f32_dpp v56,v56,v56 row_ror:2 row_mask:0xf bank_mask:0xf
s_waitcnt lgkmcnt(4)
v_mfma_f32_16x16x32_bf16 v[44:47],a[192:195],v[112:115],v[44:47]
ds_read_b128 v[92:95],v5 offset:27648
v_mul_f32 v56,0x3a800000,v56
v_mfma_f32_16x16x32_bf16 v[44:47],a[204:207],v[116:119],v[44:47]
v_permlane32_swap_b32 v74,v75
v_mfma_f32_16x16x32_bf16 v[44:47],a[216:219],v[120:123],v[44:47]
v_mul_f32 v57,v56,v56
v_mfma_f32_16x16x32_bf16 v[44:47],a[228:231],v[124:127],v[44:47]
ds_read_b128 v[96:99],v5 offset:28800
v_sub_f32_dpp v57,v56,v57 quad_perm:[1,1,3,3] row_mask:0xf bank_mask:0xf
v_mfma_f32_16x16x32_bf16 v[48:51],a[232:235],v[124:127],v[48:51]
v_permlane16_swap_b32 v72,v74
v_mfma_f32_16x16x32_bf16 v[48:51],a[220:223],v[120:123],v[48:51]
v_permlane16_swap_b32 v73,v75
v_mfma_f32_16x16x32_bf16 v[48:51],a[208:211],v[116:119],v[48:51]
buffer_store_dwordx4 v[72:75],v15,s[32:35],s68 offen sc1
ds_read_b128 v[100:103],v5 offset:29952
v_mfma_f32_16x16x32_bf16 v[48:51],a[196:199],v[112:115],v[48:51]
v_max_f32 v57,0,v57
v_mfma_f32_16x16x32_bf16 v[52:55],a[200:203],v[112:115],v[52:55]
v_cvt_pk_bf16_f32 v57,0,v57
v_mfma_f32_16x16x32_bf16 v[52:55],a[212:215],v[116:119],v[52:55]
ds_read_b128 v[104:107],v5 offset:31104
v_add_f32 v57,0x3727c5ac,v57
v_mfma_f32_16x16x32_bf16 v[52:55],a[224:227],v[120:123],v[52:55]
v_cvt_pk_bf16_f32 v57,0,v57
v_mfma_f32_16x16x32_bf16 v[52:55],a[236:239],v[124:127],v[52:55]
s_waitcnt lgkmcnt(4)
v_mfma_f32_16x16x32_bf16 v[52:55],a[248:251],v[76:79],v[52:55]
ds_read_b128 v[108:111],v5 offset:32256
v_mfma_f32_16x16x32_bf16 v[52:55],v[132:135],v[80:83],v[52:55]
v_sqrt_f32 v57,v57
v_mfma_f32_16x16x32_bf16 v[52:55],v[144:147],v[84:87],v[52:55]
v_cvt_pk_bf16_f32 v57,v56,v57
v_mfma_f32_16x16x32_bf16 v[52:55],v[156:159],v[88:91],v[52:55]
ds_read_b128 v[112:115],v5 offset:33408
ds_write_b32 v10,v57 offset:128
v_mfma_f32_16x16x32_bf16 v[48:51],v[152:155],v[88:91],v[48:51]
v_mfma_f32_16x16x32_bf16 v[48:51],v[140:143],v[84:87],v[48:51]
v_mfma_f32_16x16x32_bf16 v[48:51],v[128:131],v[80:83],v[48:51]
ds_read_b128 v[116:119],v5 offset:34560
v_mfma_f32_16x16x32_bf16 v[48:51],a[244:247],v[76:79],v[48:51]
v_mfma_f32_16x16x32_bf16 v[44:47],a[240:243],v[76:79],v[44:47]
v_mfma_f32_16x16x32_bf16 v[44:47],a[252:255],v[80:83],v[44:47]
ds_read_b128 v[120:123],v5 offset:35712
v_mfma_f32_16x16x32_bf16 v[44:47],v[136:139],v[84:87],v[44:47]
s_waitcnt vmcnt(1)
s_waitcnt lgkmcnt(2)
s_barrier
v_mfma_f32_16x16x32_bf16 v[44:47],v[148:151],v[88:91],v[44:47]
v_xor_b32 v5,73728,v5
v_mfma_f32_16x16x32_bf16 v[44:47],v[160:163],v[92:95],v[44:47]
ds_read_b128 v[76:79],v4 offset:0
s_add_u32 m0,s64,36864
v_mfma_f32_16x16x32_bf16 v[44:47],v[172:175],v[96:99],v[44:47]
buffer_load_dwordx4 v2,s[32:35],s63 offen offset:0 sc1 lds
v_mfma_f32_16x16x32_bf16 v[44:47],v[184:187],v[100:103],v[44:47]
ds_read_b32 v62,v11 offset:128
v_mfma_f32_16x16x32_bf16 v[44:47],v[196:199],v[104:107],v[44:47]
ds_read_b128 v[80:83],v4 offset:1152
v_mfma_f32_16x16x32_bf16 v[48:51],v[200:203],v[104:107],v[48:51]
v_mfma_f32_16x16x32_bf16 v[48:51],v[188:191],v[100:103],v[48:51]
buffer_load_dwordx4 v2,s[32:35],s63 offen offset:1024 sc1 lds
v_mfma_f32_16x16x32_bf16 v[48:51],v[176:179],v[96:99],v[48:51]
ds_read_b128 v[84:87],v4 offset:2304
ds_read2st64_b32 v[56:57],v8 offset0:0 offset1:36
v_mfma_f32_16x16x32_bf16 v[48:51],v[164:167],v[92:95],v[48:51]
ds_read2st64_b32 v[58:59],v8 offset0:72 offset1:108
v_mfma_f32_16x16x32_bf16 v[52:55],v[168:171],v[92:95],v[52:55]
s_waitcnt lgkmcnt(4)
v_lshlrev_b32 v60,16,v62
v_mfma_f32_16x16x32_bf16 v[52:55],v[180:183],v[96:99],v[52:55]
buffer_load_dwordx4 v2,s[32:35],s63 offen offset:2048 sc1 lds
ds_read_b128 v[88:91],v4 offset:3456
v_mfma_f32_16x16x32_bf16 v[52:55],v[192:195],v[100:103],v[52:55]
v_and_b32 v62,0xffff0000,v62
v_mfma_f32_16x16x32_bf16 v[52:55],v[204:207],v[104:107],v[52:55]
v_mfma_f32_16x16x32_bf16 v[52:55],v[216:219],v[108:111],v[52:55]
ds_read_b128 v[92:95],v4 offset:4608
v_mfma_f32_16x16x32_bf16 v[52:55],v[228:231],v[112:115],v[52:55]
buffer_load_dwordx4 v2,s[32:35],s63 offen offset:3072 sc1 lds
v_mfma_f32_16x16x32_bf16 v[52:55],v[240:243],v[116:119],v[52:55]
v_rcp_f32 v61,v62
v_mfma_f32_16x16x32_bf16 v[52:55],v[252:255],v[120:123],v[52:55]
ds_read_b128 v[96:99],v4 offset:5760
v_mul_f32 v62,v62,v62
v_mfma_f32_16x16x32_bf16 v[48:51],v[248:251],v[120:123],v[48:51]
s_add_u32 m0,s64,40960
s_waitcnt lgkmcnt(3)
v_add_f32 v56,v56,v57
v_mfma_f32_16x16x32_bf16 v[48:51],v[236:239],v[116:119],v[48:51]
buffer_load_dwordx4 v6,s[32:35],s63 offen offset:0 sc1 lds
v_add_f32 v58,v58,v59
v_mfma_f32_16x16x32_bf16 v[48:51],v[224:227],v[112:115],v[48:51]
ds_read_b128 v[100:103],v4 offset:6912
v_add_f32 v56,v56,v58
v_mfma_f32_16x16x32_bf16 v[48:51],v[212:215],v[108:111],v[48:51]
v_mul_f32 v61,0xbfb8aa3b,v61
v_add_f32_dpp v56,v56,v56 row_ror:8 row_mask:0xf bank_mask:0xf
v_mfma_f32_16x16x32_bf16 v[44:47],v[208:211],v[108:111],v[44:47]
v_mfma_f32_16x16x32_bf16 v[44:47],v[220:223],v[112:115],v[44:47]
buffer_load_dwordx4 v6,s[32:35],s63 offen offset:1024 sc1 lds
ds_read_b128 v[104:107],v4 offset:8064
v_mfma_f32_16x16x32_bf16 v[44:47],v[232:235],v[116:119],v[44:47]
v_add_f32_dpp v56,v56,v56 row_ror:4 row_mask:0xf bank_mask:0xf
s_sub_u32 s62,s62,1
v_mfma_f32_16x16x32_bf16 v[44:47],v[244:247],v[120:123],v[44:47]
s_cbranch_scc0 .L_gl_1
s_nop 1
s_add_u32 s68,s68,s30
s_waitcnt vmcnt(6)
s_barrier
buffer_store_dwordx2 v[16:17],v2,s[92:95],s68 idxen sc1
v_fma_f32 v52,-v60,v28,v52
v_fma_f32 v53,-v60,v29,v53
v_fma_f32 v54,-v60,v30,v54
v_fma_f32 v55,-v60,v31,v55
v_fma_f32 v48,-v60,v24,v48
v_fma_f32 v49,-v60,v25,v49
v_fma_f32 v50,-v60,v26,v50
v_fma_f32 v51,-v60,v27,v51
v_fma_f32 v44,-v60,v20,v44
v_fma_f32 v45,-v60,v21,v45
v_fma_f32 v46,-v60,v22,v46
v_fma_f32 v47,-v60,v23,v47
v_permlane32_swap_b32 v52,v54
v_permlane32_swap_b32 v53,v55
v_mul_f32 v64,v44,v61
v_mul_f32 v65,v45,v61
v_mul_f32 v66,v46,v61
v_mul_f32 v67,v47,v61
v_mul_f32 v68,v52,v61
v_mul_f32 v69,v53,v61
v_exp_f32 v64,v64
v_exp_f32 v65,v65
v_mul_f32 v44,v44,v48
v_mul_f32 v45,v45,v49
v_exp_f32 v66,v66
v_exp_f32 v67,v67
v_mul_f32 v46,v46,v50
v_mul_f32 v47,v47,v51
v_exp_f32 v68,v68
v_exp_f32 v69,v69
v_mul_f32 v52,v52,v54
v_mul_f32 v53,v53,v55
v_fma_f32 v64,v64,v62,v62
v_fma_f32 v65,v65,v62,v62
v_rcp_f32 v64,v64
v_rcp_f32 v65,v65
v_fma_f32 v66,v66,v62,v62
v_fma_f32 v67,v67,v62,v62
v_rcp_f32 v66,v66
v_rcp_f32 v67,v67
v_fma_f32 v68,v68,v62,v62
v_fma_f32 v69,v69,v62,v62
v_rcp_f32 v68,v68
v_rcp_f32 v69,v69
v_mul_f32 v44,v44,v64
v_mul_f32 v45,v45,v65
v_mul_f32 v46,v46,v66
v_mul_f32 v47,v47,v67
v_mul_f32 v52,v52,v68
v_mul_f32 v53,v53,v69
v_cvt_pk_bf16_f32 v74,v52,v53
v_cvt_pk_bf16_f32 v72,v44,v45
v_cvt_pk_bf16_f32 v73,v46,v47
v_permlane32_swap_b32 v74,v75
s_nop 1
v_permlane16_swap_b32 v72,v74
v_permlane16_swap_b32 v73,v75
buffer_store_dwordx4 v[72:75],v15,s[32:35],s68 offen sc1
s_add_u32 s68,s68,s30
s_waitcnt vmcnt(0)
s_barrier
buffer_store_dwordx2 v[16:17],v2,s[92:95],s68 idxen sc1
s_branch .L_end
.L_gt0_1:
s_nop 1
s_add_u32 s68,s68,s30
s_waitcnt vmcnt(6)
s_barrier
buffer_store_dwordx2 v[16:17],v2,s[92:95],s68 idxen sc1
v_fma_f32 v40,-v60,v28,v40
v_fma_f32 v41,-v60,v29,v41
v_fma_f32 v42,-v60,v30,v42
v_fma_f32 v43,-v60,v31,v43
v_fma_f32 v36,-v60,v24,v36
v_fma_f32 v37,-v60,v25,v37
v_fma_f32 v38,-v60,v26,v38
v_fma_f32 v39,-v60,v27,v39
v_fma_f32 v32,-v60,v20,v32
v_fma_f32 v33,-v60,v21,v33
v_fma_f32 v34,-v60,v22,v34
v_fma_f32 v35,-v60,v23,v35
v_permlane32_swap_b32 v40,v42
v_permlane32_swap_b32 v41,v43
v_mul_f32 v64,v32,v61
v_mul_f32 v65,v33,v61
v_mul_f32 v66,v34,v61
v_mul_f32 v67,v35,v61
v_mul_f32 v68,v40,v61
v_mul_f32 v69,v41,v61
v_exp_f32 v64,v64
v_exp_f32 v65,v65
v_mul_f32 v32,v32,v36
v_mul_f32 v33,v33,v37
v_exp_f32 v66,v66
v_exp_f32 v67,v67
v_mul_f32 v34,v34,v38
v_mul_f32 v35,v35,v39
v_exp_f32 v68,v68
v_exp_f32 v69,v69
v_mul_f32 v40,v40,v42
v_mul_f32 v41,v41,v43
v_fma_f32 v64,v64,v62,v62
v_fma_f32 v65,v65,v62,v62
v_rcp_f32 v64,v64
v_rcp_f32 v65,v65
v_fma_f32 v66,v66,v62,v62
v_fma_f32 v67,v67,v62,v62
v_rcp_f32 v66,v66
v_rcp_f32 v67,v67
v_fma_f32 v68,v68,v62,v62
v_fma_f32 v69,v69,v62,v62
v_rcp_f32 v68,v68
v_rcp_f32 v69,v69
v_mul_f32 v32,v32,v64
v_mul_f32 v33,v33,v65
v_mul_f32 v34,v34,v66
v_mul_f32 v35,v35,v67
v_mul_f32 v40,v40,v68
v_mul_f32 v41,v41,v69
v_cvt_pk_bf16_f32 v74,v40,v41
v_cvt_pk_bf16_f32 v72,v32,v33
v_cvt_pk_bf16_f32 v73,v34,v35
v_permlane32_swap_b32 v74,v75
s_nop 1
v_permlane16_swap_b32 v72,v74
v_permlane16_swap_b32 v73,v75
buffer_store_dwordx4 v[72:75],v15,s[32:35],s68 offen sc1
s_add_u32 s68,s68,s30
s_waitcnt vmcnt(0)
s_barrier
buffer_store_dwordx2 v[16:17],v2,s[92:95],s68 idxen sc1
s_branch .L_end
.p2align 6
.L_gv2:
.L_gl_2:
s_waitcnt lgkmcnt(4)
v_mfma_f32_16x16x32_bf16 v[32:35],a[0:3],v[76:79],0
ds_read_b128 v[108:111],v4 offset:9216
s_add_u32 s68,s68,s30
v_mfma_f32_16x16x32_bf16 v[32:35],a[12:15],v[80:83],v[32:35]
buffer_load_dwordx4 v6,s[32:35],s63 offen offset:2048 sc1 lds
v_fma_f32 v52,-v60,v28,v52
v_mfma_f32_16x16x32_bf16 v[32:35],a[24:27],v[84:87],v[32:35]
v_fma_f32 v53,-v60,v29,v53
v_fma_f32 v54,-v60,v30,v54
v_mfma_f32_16x16x32_bf16 v[32:35],a[36:39],v[88:91],v[32:35]
ds_read_b128 v[112:115],v4 offset:10368
v_fma_f32 v55,-v60,v31,v55
v_mfma_f32_16x16x32_bf16 v[36:39],a[40:43],v[88:91],0
v_permlane32_swap_b32 v52,v54
v_mfma_f32_16x16x32_bf16 v[36:39],a[28:31],v[84:87],v[36:39]
buffer_load_dwordx4 v6,s[32:35],s63 offen offset:3072 sc1 lds
v_fma_f32 v48,-v60,v24,v48
v_mfma_f32_16x16x32_bf16 v[36:39],a[16:19],v[80:83],v[36:39]
ds_read_b128 v[116:119],v4 offset:11520
v_fma_f32 v49,-v60,v25,v49
v_mfma_f32_16x16x32_bf16 v[36:39],a[4:7],v[76:79],v[36:39]
v_permlane32_swap_b32 v53,v55
v_mfma_f32_16x16x32_bf16 v[40:43],a[8:11],v[76:79],0
v_fma_f32 v50,-v60,v26,v50
v_fma_f32 v51,-v60,v27,v51
v_mfma_f32_16x16x32_bf16 v[40:43],a[20:23],v[80:83],v[40:43]
s_waitcnt vmcnt(8)
s_barrier
v_mfma_f32_16x16x32_bf16 v[40:43],a[32:35],v[84:87],v[40:43]
ds_read_b128 v[120:123],v4 offset:12672
v_fma_f32 v44,-v60,v20,v44
v_mfma_f32_16x16x32_bf16 v[40:43],a[44:47],v[88:91],v[40:43]
v_fma_f32 v45,-v60,v21,v45
s_waitcnt lgkmcnt(4)
v_mfma_f32_16x16x32_bf16 v[40:43],a[56:59],v[92:95],v[40:43]
ds_read_b128 v[124:127],v4 offset:13824
s_add_u32 m0,s64,45056
v_mfma_f32_16x16x32_bf16 v[40:43],a[68:71],v[96:99],v[40:43]
buffer_load_dwordx4 v7,s[32:35],s63 offen offset:0 sc1 lds
s_add_u32 s63,s63,s30
)#"
R"#(
v_mfma_f32_16x16x32_bf16 v[40:43],a[80:83],v[100:103],v[40:43]
s_xor_b32 s64,s64,s65
v_fma_f32 v46,-v60,v22,v46
v_mfma_f32_16x16x32_bf16 v[40:43],a[92:95],v[104:107],v[40:43]
ds_read_b128 v[76:79],v4 offset:14976
v_fma_f32 v47,-v60,v23,v47
v_mfma_f32_16x16x32_bf16 v[36:39],a[88:91],v[104:107],v[36:39]
v_mul_f32 v64,v44,v61
v_mul_f32 v65,v45,v61
v_mfma_f32_16x16x32_bf16 v[36:39],a[76:79],v[100:103],v[36:39]
v_exp_f32 v64,v64
v_mfma_f32_16x16x32_bf16 v[36:39],a[64:67],v[96:99],v[36:39]
ds_read_b128 v[80:83],v4 offset:16128
v_mul_f32 v66,v46,v61
v_mfma_f32_16x16x32_bf16 v[36:39],a[52:55],v[92:95],v[36:39]
v_exp_f32 v65,v65
v_mfma_f32_16x16x32_bf16 v[32:35],a[48:51],v[92:95],v[32:35]
v_exp_f32 v66,v66
v_mfma_f32_16x16x32_bf16 v[32:35],a[60:63],v[96:99],v[32:35]
ds_read_b128 v[84:87],v4 offset:17280
v_mul_f32 v67,v47,v61
v_mfma_f32_16x16x32_bf16 v[32:35],a[72:75],v[100:103],v[32:35]
v_exp_f32 v67,v67
v_mfma_f32_16x16x32_bf16 v[32:35],a[84:87],v[104:107],v[32:35]
v_mul_f32 v68,v52,v61
s_waitcnt lgkmcnt(4)
v_mfma_f32_16x16x32_bf16 v[32:35],a[96:99],v[108:111],v[32:35]
ds_read_b128 v[88:91],v4 offset:18432
v_mul_f32 v69,v53,v61
v_mfma_f32_16x16x32_bf16 v[32:35],a[108:111],v[112:115],v[32:35]
v_exp_f32 v68,v68
v_mfma_f32_16x16x32_bf16 v[32:35],a[120:123],v[116:119],v[32:35]
v_exp_f32 v69,v69
v_mfma_f32_16x16x32_bf16 v[32:35],a[132:135],v[120:123],v[32:35]
ds_read_b128 v[92:95],v4 offset:19584
v_mul_f32 v44,v44,v48
v_mfma_f32_16x16x32_bf16 v[36:39],a[136:139],v[120:123],v[36:39]
v_mul_f32 v45,v45,v49
v_mul_f32 v46,v46,v50
v_mfma_f32_16x16x32_bf16 v[36:39],a[124:127],v[116:119],v[36:39]
v_mul_f32 v47,v47,v51
v_mul_f32 v52,v52,v54
v_mfma_f32_16x16x32_bf16 v[36:39],a[112:115],v[112:115],v[36:39]
ds_read_b128 v[96:99],v4 offset:20736
v_mul_f32 v53,v53,v55
v_mfma_f32_16x16x32_bf16 v[36:39],a[100:103],v[108:111],v[36:39]
v_fma_f32 v64,v64,v62,v62
v_fma_f32 v65,v65,v62,v62
v_mfma_f32_16x16x32_bf16 v[40:43],a[104:107],v[108:111],v[40:43]
v_rcp_f32 v64,v64
v_mfma_f32_16x16x32_bf16 v[40:43],a[116:119],v[112:115],v[40:43]
ds_read_b128 v[100:103],v4 offset:21888
v_fma_f32 v66,v66,v62,v62
v_mfma_f32_16x16x32_bf16 v[40:43],a[128:131],v[116:119],v[40:43]
v_rcp_f32 v65,v65
v_mfma_f32_16x16x32_bf16 v[40:43],a[140:143],v[120:123],v[40:43]
v_fma_f32 v67,v67,v62,v62
s_waitcnt lgkmcnt(4)
v_mfma_f32_16x16x32_bf16 v[40:43],a[152:155],v[124:127],v[40:43]
ds_read_b128 v[104:107],v4 offset:23040
v_fma_f32 v68,v68,v62,v62
v_mfma_f32_16x16x32_bf16 v[40:43],a[164:167],v[76:79],v[40:43]
v_rcp_f32 v66,v66
v_mfma_f32_16x16x32_bf16 v[40:43],a[176:179],v[80:83],v[40:43]
v_rcp_f32 v67,v67
v_mfma_f32_16x16x32_bf16 v[40:43],a[188:191],v[84:87],v[40:43]
ds_read_b128 v[108:111],v4 offset:24192
v_fma_f32 v69,v69,v62,v62
v_mfma_f32_16x16x32_bf16 v[36:39],a[184:187],v[84:87],v[36:39]
v_rcp_f32 v68,v68
v_mfma_f32_16x16x32_bf16 v[36:39],a[172:175],v[80:83],v[36:39]
v_rcp_f32 v69,v69
v_mfma_f32_16x16x32_bf16 v[36:39],a[160:163],v[76:79],v[36:39]
ds_read_b128 v[112:115],v4 offset:25344
v_mul_f32 v44,v44,v64
v_mfma_f32_16x16x32_bf16 v[36:39],a[148:151],v[124:127],v[36:39]
v_mul_f32 v45,v45,v65
v_mul_f32 v46,v46,v66
v_mfma_f32_16x16x32_bf16 v[32:35],a[144:147],v[124:127],v[32:35]
v_mul_f32 v47,v47,v67
v_mul_f32 v52,v52,v68
v_mfma_f32_16x16x32_bf16 v[32:35],a[156:159],v[76:79],v[32:35]
ds_read_b128 v[116:119],v4 offset:26496
v_mul_f32 v53,v53,v69
v_mfma_f32_16x16x32_bf16 v[32:35],a[168:171],v[80:83],v[32:35]
v_cvt_pk_bf16_f32 v74,v52,v53
v_cvt_pk_bf16_f32 v72,v44,v45
v_mfma_f32_16x16x32_bf16 v[32:35],a[180:183],v[84:87],v[32:35]
v_cvt_pk_bf16_f32 v73,v46,v47
s_waitcnt lgkmcnt(4)
v_mfma_f32_16x16x32_bf16 v[32:35],a[192:195],v[88:91],v[32:35]
ds_read_b128 v[120:123],v4 offset:27648
v_add_f32_dpp v56,v56,v56 row_ror:2 row_mask:0xf bank_mask:0xf
v_mfma_f32_16x16x32_bf16 v[32:35],a[204:207],v[92:95],v[32:35]
v_permlane32_swap_b32 v74,v75
v_mfma_f32_16x16x32_bf16 v[32:35],a[216:219],v[96:99],v[32:35]
v_mul_f32 v56,0x3a800000,v56
v_mfma_f32_16x16x32_bf16 v[32:35],a[228:231],v[100:103],v[32:35]
ds_read_b128 v[124:127],v4 offset:28800
v_mul_f32 v57,v56,v56
v_mfma_f32_16x16x32_bf16 v[36:39],a[232:235],v[100:103],v[36:39]
v_permlane16_swap_b32 v72,v74
v_mfma_f32_16x16x32_bf16 v[36:39],a[220:223],v[96:99],v[36:39]
v_permlane16_swap_b32 v73,v75
v_mfma_f32_16x16x32_bf16 v[36:39],a[208:211],v[92:95],v[36:39]
buffer_store_dwordx4 v[72:75],v15,s[32:35],s68 offen sc1
ds_read_b128 v[76:79],v4 offset:29952
v_mfma_f32_16x16x32_bf16 v[36:39],a[196:199],v[88:91],v[36:39]
v_sub_f32_dpp v57,v56,v57 quad_perm:[1,1,3,3] row_mask:0xf bank_mask:0xf
v_mfma_f32_16x16x32_bf16 v[40:43],a[200:203],v[88:91],v[40:43]
v_max_f32 v57,0,v57
v_mfma_f32_16x16x32_bf16 v[40:43],a[212:215],v[92:95],v[40:43]
ds_read_b128 v[80:83],v4 offset:31104
v_cvt_pk_bf16_f32 v57,0,v57
v_mfma_f32_16x16x32_bf16 v[40:43],a[224:227],v[96:99],v[40:43]
v_add_f32 v57,0x3727c5ac,v57
v_mfma_f32_16x16x32_bf16 v[40:43],a[236:239],v[100:103],v[40:43]
v_cvt_pk_bf16_f32 v57,0,v57
s_waitcnt lgkmcnt(4)
v_mfma_f32_16x16x32_bf16 v[40:43],a[248:251],v[104:107],v[40:43]
ds_read_b128 v[84:87],v4 offset:32256
v_mfma_f32_16x16x32_bf16 v[40:43],v[132:135],v[108:111],v[40:43]
v_sqrt_f32 v57,v57
v_mfma_f32_16x16x32_bf16 v[40:43],v[144:147],v[112:115],v[40:43]
v_cvt_pk_bf16_f32 v57,v56,v57
v_mfma_f32_16x16x32_bf16 v[40:43],v[156:159],v[116:119],v[40:43]
ds_read_b128 v[88:91],v4 offset:33408
ds_write_b32 v10,v57 offset:0
v_mfma_f32_16x16x32_bf16 v[36:39],v[152:155],v[116:119],v[36:39]
v_mfma_f32_16x16x32_bf16 v[36:39],v[140:143],v[112:115],v[36:39]
v_mfma_f32_16x16x32_bf16 v[36:39],v[128:131],v[108:111],v[36:39]
ds_read_b128 v[92:95],v4 offset:34560
v_mfma_f32_16x16x32_bf16 v[36:39],a[244:247],v[104:107],v[36:39]
v_mfma_f32_16x16x32_bf16 v[32:35],a[240:243],v[104:107],v[32:35]
v_mfma_f32_16x16x32_bf16 v[32:35],a[252:255],v[108:111],v[32:35]
s_waitcnt vmcnt(1)
s_waitcnt lgkmcnt(1)
s_barrier
v_mfma_f32_16x16x32_bf16 v[32:35],v[136:139],v[112:115],v[32:35]
ds_read_b128 v[96:99],v4 offset:35712
v_xor_b32 v4,73728,v4
v_mfma_f32_16x16x32_bf16 v[32:35],v[148:151],v[116:119],v[32:35]
v_mfma_f32_16x16x32_bf16 v[32:35],v[160:163],v[120:123],v[32:35]
ds_read_b128 v[100:103],v5 offset:0
s_add_u32 m0,s64,0
v_mfma_f32_16x16x32_bf16 v[32:35],v[172:175],v[124:127],v[32:35]
buffer_load_dwordx4 v2,s[32:35],s63 offen offset:0 sc1 lds
v_mfma_f32_16x16x32_bf16 v[32:35],v[184:187],v[76:79],v[32:35]
ds_read_b32 v62,v11 offset:0
v_mfma_f32_16x16x32_bf16 v[32:35],v[196:199],v[80:83],v[32:35]
ds_read_b128 v[104:107],v5 offset:1152
v_mfma_f32_16x16x32_bf16 v[36:39],v[200:203],v[80:83],v[36:39]
v_mfma_f32_16x16x32_bf16 v[36:39],v[188:191],v[76:79],v[36:39]
buffer_load_dwordx4 v2,s[32:35],s63 offen offset:1024 sc1 lds
v_mfma_f32_16x16x32_bf16 v[36:39],v[176:179],v[124:127],v[36:39]
ds_read_b128 v[108:111],v5 offset:2304
ds_read2st64_b32 v[56:57],v8 offset0:144 offset1:180
v_mfma_f32_16x16x32_bf16 v[36:39],v[164:167],v[120:123],v[36:39]
ds_read2st64_b32 v[58:59],v8 offset0:216 offset1:252
v_xor_b32 v8,v9,v8
v_mfma_f32_16x16x32_bf16 v[40:43],v[168:171],v[120:123],v[40:43]
s_waitcnt lgkmcnt(4)
v_lshlrev_b32 v60,16,v62
v_mfma_f32_16x16x32_bf16 v[40:43],v[180:183],v[124:127],v[40:43]
buffer_load_dwordx4 v2,s[32:35],s63 offen offset:2048 sc1 lds
ds_read_b128 v[112:115],v5 offset:3456
v_mfma_f32_16x16x32_bf16 v[40:43],v[192:195],v[76:79],v[40:43]
v_and_b32 v62,0xffff0000,v62
v_mfma_f32_16x16x32_bf16 v[40:43],v[204:207],v[80:83],v[40:43]
v_mfma_f32_16x16x32_bf16 v[40:43],v[216:219],v[84:87],v[40:43]
ds_read_b128 v[116:119],v5 offset:4608
v_mfma_f32_16x16x32_bf16 v[40:43],v[228:231],v[88:91],v[40:43]
buffer_load_dwordx4 v2,s[32:35],s63 offen offset:3072 sc1 lds
v_mfma_f32_16x16x32_bf16 v[40:43],v[240:243],v[92:95],v[40:43]
v_rcp_f32 v61,v62
v_mfma_f32_16x16x32_bf16 v[40:43],v[252:255],v[96:99],v[40:43]
ds_read_b128 v[120:123],v5 offset:5760
v_mul_f32 v62,v62,v62
v_mfma_f32_16x16x32_bf16 v[36:39],v[248:251],v[96:99],v[36:39]
s_add_u32 m0,s64,4096
s_waitcnt lgkmcnt(3)
v_add_f32 v56,v56,v57
v_mfma_f32_16x16x32_bf16 v[36:39],v[236:239],v[92:95],v[36:39]
buffer_load_dwordx4 v6,s[32:35],s63 offen offset:0 sc1 lds
v_add_f32 v58,v58,v59
v_mfma_f32_16x16x32_bf16 v[36:39],v[224:227],v[88:91],v[36:39]
ds_read_b128 v[124:127],v5 offset:6912
v_add_f32 v56,v56,v58
v_mfma_f32_16x16x32_bf16 v[36:39],v[212:215],v[84:87],v[36:39]
v_mul_f32 v61,0xbfb8aa3b,v61
v_add_f32_dpp v56,v56,v56 row_ror:8 row_mask:0xf bank_mask:0xf
v_mfma_f32_16x16x32_bf16 v[32:35],v[208:211],v[84:87],v[32:35]
v_mfma_f32_16x16x32_bf16 v[32:35],v[220:223],v[88:91],v[32:35]
buffer_load_dwordx4 v6,s[32:35],s63 offen offset:1024 sc1 lds
ds_read_b128 v[76:79],v5 offset:8064
v_mfma_f32_16x16x32_bf16 v[32:35],v[232:235],v[92:95],v[32:35]
v_add_f32_dpp v56,v56,v56 row_ror:4 row_mask:0xf bank_mask:0xf
s_sub_u32 s62,s62,1
v_mfma_f32_16x16x32_bf16 v[32:35],v[244:247],v[96:99],v[32:35]
s_cbranch_scc1 .L_gt0_2
.L_gm_2:
s_waitcnt lgkmcnt(4)
v_mfma_f32_16x16x32_bf16 v[44:47],a[0:3],v[100:103],0
ds_read_b128 v[80:83],v5 offset:9216
s_add_u32 s68,s68,s30
v_mfma_f32_16x16x32_bf16 v[44:47],a[12:15],v[104:107],v[44:47]
buffer_load_dwordx4 v6,s[32:35],s63 offen offset:2048 sc1 lds
v_fma_f32 v40,-v60,v28,v40
v_mfma_f32_16x16x32_bf16 v[44:47],a[24:27],v[108:111],v[44:47]
v_fma_f32 v41,-v60,v29,v41
v_fma_f32 v42,-v60,v30,v42
v_mfma_f32_16x16x32_bf16 v[44:47],a[36:39],v[112:115],v[44:47]
ds_read_b128 v[84:87],v5 offset:10368
v_fma_f32 v43,-v60,v31,v43
v_mfma_f32_16x16x32_bf16 v[48:51],a[40:43],v[112:115],0
v_permlane32_swap_b32 v40,v42
v_mfma_f32_16x16x32_bf16 v[48:51],a[28:31],v[108:111],v[48:51]
buffer_load_dwordx4 v6,s[32:35],s63 offen offset:3072 sc1 lds
v_fma_f32 v36,-v60,v24,v36
v_mfma_f32_16x16x32_bf16 v[48:51],a[16:19],v[104:107],v[48:51]
ds_read_b128 v[88:91],v5 offset:11520
v_fma_f32 v37,-v60,v25,v37
v_mfma_f32_16x16x32_bf16 v[48:51],a[4:7],v[100:103],v[48:51]
v_permlane32_swap_b32 v41,v43
v_mfma_f32_16x16x32_bf16 v[52:55],a[8:11],v[100:103],0
v_fma_f32 v38,-v60,v26,v38
v_fma_f32 v39,-v60,v27,v39
v_mfma_f32_16x16x32_bf16 v[52:55],a[20:23],v[104:107],v[52:55]
s_waitcnt vmcnt(8)
s_barrier
v_mfma_f32_16x16x32_bf16 v[52:55],a[32:35],v[108:111],v[52:55]
ds_read_b128 v[92:95],v5 offset:12672
v_fma_f32 v32,-v60,v20,v32
v_mfma_f32_16x16x32_bf16 v[52:55],a[44:47],v[112:115],v[52:55]
v_fma_f32 v33,-v60,v21,v33
s_waitcnt lgkmcnt(4)
v_mfma_f32_16x16x32_bf16 v[52:55],a[56:59],v[116:119],v[52:55]
ds_read_b128 v[96:99],v5 offset:13824
s_add_u32 m0,s64,8192
v_mfma_f32_16x16x32_bf16 v[52:55],a[68:71],v[120:123],v[52:55]
buffer_load_dwordx4 v7,s[32:35],s63 offen offset:0 sc1 lds
s_add_u32 s63,s63,s30
v_mfma_f32_16x16x32_bf16 v[52:55],a[80:83],v[124:127],v[52:55]
v_fma_f32 v34,-v60,v22,v34
v_fma_f32 v35,-v60,v23,v35
v_mfma_f32_16x16x32_bf16 v[52:55],a[92:95],v[76:79],v[52:55]
ds_read_b128 v[100:103],v5 offset:14976
v_mul_f32 v64,v32,v61
v_mfma_f32_16x16x32_bf16 v[48:51],a[88:91],v[76:79],v[48:51]
v_exp_f32 v64,v64
v_mfma_f32_16x16x32_bf16 v[48:51],a[76:79],v[124:127],v[48:51]
buffer_load_dwordx2 v[12:13],v14,s[0:3],s66 offen sc1
v_mul_f32 v65,v33,v61
v_mfma_f32_16x16x32_bf16 v[48:51],a[64:67],v[120:123],v[48:51]
ds_read_b128 v[104:107],v5 offset:16128
v_mul_f32 v66,v34,v61
v_mfma_f32_16x16x32_bf16 v[48:51],a[52:55],v[116:119],v[48:51]
v_exp_f32 v65,v65
v_mfma_f32_16x16x32_bf16 v[44:47],a[48:51],v[116:119],v[44:47]
v_exp_f32 v66,v66
v_mfma_f32_16x16x32_bf16 v[44:47],a[60:63],v[120:123],v[44:47]
ds_read_b128 v[108:111],v5 offset:17280
v_mul_f32 v67,v35,v61
v_mfma_f32_16x16x32_bf16 v[44:47],a[72:75],v[124:127],v[44:47]
v_exp_f32 v67,v67
v_mfma_f32_16x16x32_bf16 v[44:47],a[84:87],v[76:79],v[44:47]
v_mul_f32 v68,v40,v61
s_waitcnt lgkmcnt(4)
v_mfma_f32_16x16x32_bf16 v[44:47],a[96:99],v[80:83],v[44:47]
ds_read_b128 v[112:115],v5 offset:18432
v_mul_f32 v69,v41,v61
v_mfma_f32_16x16x32_bf16 v[44:47],a[108:111],v[84:87],v[44:47]
v_exp_f32 v68,v68
v_mfma_f32_16x16x32_bf16 v[44:47],a[120:123],v[88:91],v[44:47]
v_exp_f32 v69,v69
v_mfma_f32_16x16x32_bf16 v[44:47],a[132:135],v[92:95],v[44:47]
ds_read_b128 v[116:119],v5 offset:19584
v_mul_f32 v32,v32,v36
v_mfma_f32_16x16x32_bf16 v[48:51],a[136:139],v[92:95],v[48:51]
v_mul_f32 v33,v33,v37
v_mul_f32 v34,v34,v38
v_mfma_f32_16x16x32_bf16 v[48:51],a[124:127],v[88:91],v[48:51]
v_mul_f32 v35,v35,v39
v_mul_f32 v40,v40,v42
v_mfma_f32_16x16x32_bf16 v[48:51],a[112:115],v[84:87],v[48:51]
ds_read_b128 v[120:123],v5 offset:20736
v_mul_f32 v41,v41,v43
v_mfma_f32_16x16x32_bf16 v[48:51],a[100:103],v[80:83],v[48:51]
v_fma_f32 v64,v64,v62,v62
v_fma_f32 v65,v65,v62,v62
v_mfma_f32_16x16x32_bf16 v[52:55],a[104:107],v[80:83],v[52:55]
v_rcp_f32 v64,v64
v_mfma_f32_16x16x32_bf16 v[52:55],a[116:119],v[84:87],v[52:55]
ds_read_b128 v[124:127],v5 offset:21888
v_fma_f32 v66,v66,v62,v62
v_mfma_f32_16x16x32_bf16 v[52:55],a[128:131],v[88:91],v[52:55]
v_rcp_f32 v65,v65
v_mfma_f32_16x16x32_bf16 v[52:55],a[140:143],v[92:95],v[52:55]
v_fma_f32 v67,v67,v62,v62
s_waitcnt lgkmcnt(4)
v_mfma_f32_16x16x32_bf16 v[52:55],a[152:155],v[96:99],v[52:55]
ds_read_b128 v[76:79],v5 offset:23040
v_fma_f32 v68,v68,v62,v62
v_mfma_f32_16x16x32_bf16 v[52:55],a[164:167],v[100:103],v[52:55]
v_rcp_f32 v66,v66
v_mfma_f32_16x16x32_bf16 v[52:55],a[176:179],v[104:107],v[52:55]
v_rcp_f32 v67,v67
v_mfma_f32_16x16x32_bf16 v[52:55],a[188:191],v[108:111],v[52:55]
ds_read_b128 v[80:83],v5 offset:24192
v_fma_f32 v69,v69,v62,v62
v_mfma_f32_16x16x32_bf16 v[48:51],a[184:187],v[108:111],v[48:51]
v_rcp_f32 v68,v68
v_mfma_f32_16x16x32_bf16 v[48:51],a[172:175],v[104:107],v[48:51]
v_rcp_f32 v69,v69
v_mfma_f32_16x16x32_bf16 v[48:51],a[160:163],v[100:103],v[48:51]
ds_read_b128 v[84:87],v5 offset:25344
v_mul_f32 v32,v32,v64
v_mfma_f32_16x16x32_bf16 v[48:51],a[148:151],v[96:99],v[48:51]
v_mul_f32 v33,v33,v65
v_mul_f32 v34,v34,v66
v_mfma_f32_16x16x32_bf16 v[44:47],a[144:147],v[96:99],v[44:47]
v_mul_f32 v35,v35,v67
v_mul_f32 v40,v40,v68
v_mfma_f32_16x16x32_bf16 v[44:47],a[156:159],v[100:103],v[44:47]
ds_read_b128 v[88:91],v5 offset:26496
v_mul_f32 v41,v41,v69
v_mfma_f32_16x16x32_bf16 v[44:47],a[168:171],v[104:107],v[44:47]
v_cvt_pk_bf16_f32 v74,v40,v41
v_cvt_pk_bf16_f32 v72,v32,v33
v_mfma_f32_16x16x32_bf16 v[44:47],a[180:183],v[108:111],v[44:47]
v_cvt_pk_bf16_f32 v73,v34,v35
s_waitcnt lgkmcnt(4)
v_mfma_f32_16x16x32_bf16 v[44:47],a[192:195],v[112:115],v[44:47]
ds_read_b128 v[92:95],v5 offset:27648
v_add_f32_dpp v56,v56,v56 row_ror:2 row_mask:0xf bank_mask:0xf
v_mfma_f32_16x16x32_bf16 v[44:47],a[204:207],v[116:119],v[44:47]
v_permlane32_swap_b32 v74,v75
v_mfma_f32_16x16x32_bf16 v[44:47],a[216:219],v[120:123],v[44:47]
v_mul_f32 v56,0x3a800000,v56
v_mfma_f32_16x16x32_bf16 v[44:47],a[228:231],v[124:127],v[44:47]
ds_read_b128 v[96:99],v5 offset:28800
v_mul_f32 v57,v56,v56
v_mfma_f32_16x16x32_bf16 v[48:51],a[232:235],v[124:127],v[48:51]
v_permlane16_swap_b32 v72,v74
v_mfma_f32_16x16x32_bf16 v[48:51],a[220:223],v[120:123],v[48:51]
v_permlane16_swap_b32 v73,v75
v_mfma_f32_16x16x32_bf16 v[48:51],a[208:211],v[116:119],v[48:51]
buffer_store_dwordx4 v[72:75],v15,s[32:35],s68 offen sc1
ds_read_b128 v[100:103],v5 offset:29952
v_mfma_f32_16x16x32_bf16 v[48:51],a[196:199],v[112:115],v[48:51]
v_sub_f32_dpp v57,v56,v57 quad_perm:[1,1,3,3] row_mask:0xf bank_mask:0xf
v_mfma_f32_16x16x32_bf16 v[52:55],a[200:203],v[112:115],v[52:55]
v_max_f32 v57,0,v57
v_mfma_f32_16x16x32_bf16 v[52:55],a[212:215],v[116:119],v[52:55]
ds_read_b128 v[104:107],v5 offset:31104
v_cvt_pk_bf16_f32 v57,0,v57
v_mfma_f32_16x16x32_bf16 v[52:55],a[224:227],v[120:123],v[52:55]
v_add_f32 v57,0x3727c5ac,v57
v_mfma_f32_16x16x32_bf16 v[52:55],a[236:239],v[124:127],v[52:55]
v_cvt_pk_bf16_f32 v57,0,v57
s_waitcnt lgkmcnt(4)
v_mfma_f32_16x16x32_bf16 v[52:55],a[248:251],v[76:79],v[52:55]
ds_read_b128 v[108:111],v5 offset:32256
v_mfma_f32_16x16x32_bf16 v[52:55],v[132:135],v[80:83],v[52:55]
v_sqrt_f32 v57,v57
v_mfma_f32_16x16x32_bf16 v[52:55],v[144:147],v[84:87],v[52:55]
v_cvt_pk_bf16_f32 v57,v56,v57
v_mfma_f32_16x16x32_bf16 v[52:55],v[156:159],v[88:91],v[52:55]
ds_read_b128 v[112:115],v5 offset:33408
ds_write_b32 v10,v57 offset:128
v_mfma_f32_16x16x32_bf16 v[48:51],v[152:155],v[88:91],v[48:51]
v_mfma_f32_16x16x32_bf16 v[48:51],v[140:143],v[84:87],v[48:51]
s_waitcnt vmcnt(1)
v_cmp_ne_u64 vcc,v[16:17],v[12:13]
v_mfma_f32_16x16x32_bf16 v[48:51],v[128:131],v[80:83],v[48:51]
ds_read_b128 v[116:119],v5 offset:34560
v_mfma_f32_16x16x32_bf16 v[48:51],a[244:247],v[76:79],v[48:51]
s_cbranch_vccnz .L_rtg1_2
.L_bkg1_2:
s_add_u32 s66,s66,s31
v_mfma_f32_16x16x32_bf16 v[44:47],a[240:243],v[76:79],v[44:47]
v_mfma_f32_16x16x32_bf16 v[44:47],a[252:255],v[80:83],v[44:47]
ds_read_b128 v[120:123],v5 offset:35712
s_waitcnt lgkmcnt(2)
s_barrier
v_mfma_f32_16x16x32_bf16 v[44:47],v[136:139],v[84:87],v[44:47]
v_xor_b32 v5,73728,v5
v_mfma_f32_16x16x32_bf16 v[44:47],v[148:151],v[88:91],v[44:47]
v_mfma_f32_16x16x32_bf16 v[44:47],v[160:163],v[92:95],v[44:47]
ds_read_b128 v[76:79],v4 offset:0
s_add_u32 m0,s64,36864
v_mfma_f32_16x16x32_bf16 v[44:47],v[172:175],v[96:99],v[44:47]
buffer_load_dwordx4 v2,s[32:35],s63 offen offset:0 sc1 lds
v_mfma_f32_16x16x32_bf16 v[44:47],v[184:187],v[100:103],v[44:47]
ds_read_b32 v62,v11 offset:128
v_mfma_f32_16x16x32_bf16 v[44:47],v[196:199],v[104:107],v[44:47]
ds_read_b128 v[80:83],v4 offset:1152
v_mfma_f32_16x16x32_bf16 v[48:51],v[200:203],v[104:107],v[48:51]
v_mfma_f32_16x16x32_bf16 v[48:51],v[188:191],v[100:103],v[48:51]
buffer_load_dwordx4 v2,s[32:35],s63 offen offset:1024 sc1 lds
v_mfma_f32_16x16x32_bf16 v[48:51],v[176:179],v[96:99],v[48:51]
ds_read_b128 v[84:87],v4 offset:2304
ds_read2st64_b32 v[56:57],v8 offset0:0 offset1:36
v_mfma_f32_16x16x32_bf16 v[48:51],v[164:167],v[92:95],v[48:51]
ds_read2st64_b32 v[58:59],v8 offset0:72 offset1:108
v_mfma_f32_16x16x32_bf16 v[52:55],v[168:171],v[92:95],v[52:55]
s_waitcnt lgkmcnt(4)
v_lshlrev_b32 v60,16,v62
v_mfma_f32_16x16x32_bf16 v[52:55],v[180:183],v[96:99],v[52:55]
buffer_load_dwordx4 v2,s[32:35],s63 offen offset:2048 sc1 lds
ds_read_b128 v[88:91],v4 offset:3456
v_mfma_f32_16x16x32_bf16 v[52:55],v[192:195],v[100:103],v[52:55]
v_and_b32 v62,0xffff0000,v62
v_mfma_f32_16x16x32_bf16 v[52:55],v[204:207],v[104:107],v[52:55]
v_mfma_f32_16x16x32_bf16 v[52:55],v[216:219],v[108:111],v[52:55]
ds_read_b128 v[92:95],v4 offset:4608
v_mfma_f32_16x16x32_bf16 v[52:55],v[228:231],v[112:115],v[52:55]
buffer_load_dwordx4 v2,s[32:35],s63 offen offset:3072 sc1 lds
v_mfma_f32_16x16x32_bf16 v[52:55],v[240:243],v[116:119],v[52:55]
v_rcp_f32 v61,v62
v_mfma_f32_16x16x32_bf16 v[52:55],v[252:255],v[120:123],v[52:55]
ds_read_b128 v[96:99],v4 offset:5760
v_mul_f32 v62,v62,v62
v_mfma_f32_16x16x32_bf16 v[48:51],v[248:251],v[120:123],v[48:51]
s_add_u32 m0,s64,40960
s_waitcnt lgkmcnt(3)
v_add_f32 v56,v56,v57
v_mfma_f32_16x16x32_bf16 v[48:51],v[236:239],v[116:119],v[48:51]
buffer_load_dwordx4 v6,s[32:35],s63 offen offset:0 sc1 lds
v_add_f32 v58,v58,v59
v_mfma_f32_16x16x32_bf16 v[48:51],v[224:227],v[112:115],v[48:51]
ds_read_b128 v[100:103],v4 offset:6912
v_add_f32 v56,v56,v58
v_mfma_f32_16x16x32_bf16 v[48:51],v[212:215],v[108:111],v[48:51]
v_mul_f32 v61,0xbfb8aa3b,v61
v_add_f32_dpp v56,v56,v56 row_ror:8 row_mask:0xf bank_mask:0xf
v_mfma_f32_16x16x32_bf16 v[44:47],v[208:211],v[108:111],v[44:47]
v_mfma_f32_16x16x32_bf16 v[44:47],v[220:223],v[112:115],v[44:47]
buffer_load_dwordx4 v6,s[32:35],s63 offen offset:1024 sc1 lds
ds_read_b128 v[104:107],v4 offset:8064
v_mfma_f32_16x16x32_bf16 v[44:47],v[232:235],v[116:119],v[44:47]
v_add_f32_dpp v56,v56,v56 row_ror:4 row_mask:0xf bank_mask:0xf
s_sub_u32 s62,s62,1
v_mfma_f32_16x16x32_bf16 v[44:47],v[244:247],v[120:123],v[44:47]
s_cbranch_scc0 .L_gl_2
s_nop 1
s_add_u32 s68,s68,s30
s_waitcnt vmcnt(6)
s_barrier
buffer_store_dwordx2 v[16:17],v2,s[92:95],s68 idxen sc1
v_fma_f32 v52,-v60,v28,v52
v_fma_f32 v53,-v60,v29,v53
v_fma_f32 v54,-v60,v30,v54
v_fma_f32 v55,-v60,v31,v55
v_fma_f32 v48,-v60,v24,v48
v_fma_f32 v49,-v60,v25,v49
v_fma_f32 v50,-v60,v26,v50
v_fma_f32 v51,-v60,v27,v51
v_fma_f32 v44,-v60,v20,v44
v_fma_f32 v45,-v60,v21,v45
v_fma_f32 v46,-v60,v22,v46
v_fma_f32 v47,-v60,v23,v47
v_permlane32_swap_b32 v52,v54
v_permlane32_swap_b32 v53,v55
v_mul_f32 v64,v44,v61
v_mul_f32 v65,v45,v61
v_mul_f32 v66,v46,v61
v_mul_f32 v67,v47,v61
v_mul_f32 v68,v52,v61
v_mul_f32 v69,v53,v61
v_exp_f32 v64,v64
v_exp_f32 v65,v65
v_mul_f32 v44,v44,v48
v_mul_f32 v45,v45,v49
v_exp_f32 v66,v66
v_exp_f32 v67,v67
v_mul_f32 v46,v46,v50
v_mul_f32 v47,v47,v51
v_exp_f32 v68,v68
v_exp_f32 v69,v69
v_mul_f32 v52,v52,v54
v_mul_f32 v53,v53,v55
v_fma_f32 v64,v64,v62,v62
v_fma_f32 v65,v65,v62,v62
v_rcp_f32 v64,v64
v_rcp_f32 v65,v65
v_fma_f32 v66,v66,v62,v62
v_fma_f32 v67,v67,v62,v62
v_rcp_f32 v66,v66
v_rcp_f32 v67,v67
v_fma_f32 v68,v68,v62,v62
v_fma_f32 v69,v69,v62,v62
v_rcp_f32 v68,v68
v_rcp_f32 v69,v69
v_mul_f32 v44,v44,v64
v_mul_f32 v45,v45,v65
v_mul_f32 v46,v46,v66
v_mul_f32 v47,v47,v67
v_mul_f32 v52,v52,v68
v_mul_f32 v53,v53,v69
)#"
R"#(
v_cvt_pk_bf16_f32 v74,v52,v53
v_cvt_pk_bf16_f32 v72,v44,v45
v_cvt_pk_bf16_f32 v73,v46,v47
v_permlane32_swap_b32 v74,v75
s_nop 1
v_permlane16_swap_b32 v72,v74
v_permlane16_swap_b32 v73,v75
buffer_store_dwordx4 v[72:75],v15,s[32:35],s68 offen sc1
s_add_u32 s68,s68,s30
s_waitcnt vmcnt(0)
s_barrier
buffer_store_dwordx2 v[16:17],v2,s[92:95],s68 idxen sc1
s_branch .L_end
.L_gt0_2:
s_nop 1
s_add_u32 s68,s68,s30
s_waitcnt vmcnt(6)
s_barrier
buffer_store_dwordx2 v[16:17],v2,s[92:95],s68 idxen sc1
v_fma_f32 v40,-v60,v28,v40
v_fma_f32 v41,-v60,v29,v41
v_fma_f32 v42,-v60,v30,v42
v_fma_f32 v43,-v60,v31,v43
v_fma_f32 v36,-v60,v24,v36
v_fma_f32 v37,-v60,v25,v37
v_fma_f32 v38,-v60,v26,v38
v_fma_f32 v39,-v60,v27,v39
v_fma_f32 v32,-v60,v20,v32
v_fma_f32 v33,-v60,v21,v33
v_fma_f32 v34,-v60,v22,v34
v_fma_f32 v35,-v60,v23,v35
v_permlane32_swap_b32 v40,v42
v_permlane32_swap_b32 v41,v43
v_mul_f32 v64,v32,v61
v_mul_f32 v65,v33,v61
v_mul_f32 v66,v34,v61
v_mul_f32 v67,v35,v61
v_mul_f32 v68,v40,v61
v_mul_f32 v69,v41,v61
v_exp_f32 v64,v64
v_exp_f32 v65,v65
v_mul_f32 v32,v32,v36
v_mul_f32 v33,v33,v37
v_exp_f32 v66,v66
v_exp_f32 v67,v67
v_mul_f32 v34,v34,v38
v_mul_f32 v35,v35,v39
v_exp_f32 v68,v68
v_exp_f32 v69,v69
v_mul_f32 v40,v40,v42
v_mul_f32 v41,v41,v43
v_fma_f32 v64,v64,v62,v62
v_fma_f32 v65,v65,v62,v62
v_rcp_f32 v64,v64
v_rcp_f32 v65,v65
v_fma_f32 v66,v66,v62,v62
v_fma_f32 v67,v67,v62,v62
v_rcp_f32 v66,v66
v_rcp_f32 v67,v67
v_fma_f32 v68,v68,v62,v62
v_fma_f32 v69,v69,v62,v62
v_rcp_f32 v68,v68
v_rcp_f32 v69,v69
v_mul_f32 v32,v32,v64
v_mul_f32 v33,v33,v65
v_mul_f32 v34,v34,v66
v_mul_f32 v35,v35,v67
v_mul_f32 v40,v40,v68
v_mul_f32 v41,v41,v69
v_cvt_pk_bf16_f32 v74,v40,v41
v_cvt_pk_bf16_f32 v72,v32,v33
v_cvt_pk_bf16_f32 v73,v34,v35
v_permlane32_swap_b32 v74,v75
s_nop 1
v_permlane16_swap_b32 v72,v74
v_permlane16_swap_b32 v73,v75
buffer_store_dwordx4 v[72:75],v15,s[32:35],s68 offen sc1
s_add_u32 s68,s68,s30
s_waitcnt vmcnt(0)
s_barrier
buffer_store_dwordx2 v[16:17],v2,s[92:95],s68 idxen sc1
s_branch .L_end
.p2align 6
.L_gv3:
.L_gl_3:
s_waitcnt lgkmcnt(4)
v_mfma_f32_16x16x32_bf16 v[32:35],a[0:3],v[76:79],0
ds_read_b128 v[108:111],v4 offset:9216
s_add_u32 s68,s68,s30
v_mfma_f32_16x16x32_bf16 v[32:35],a[12:15],v[80:83],v[32:35]
buffer_load_dwordx4 v6,s[32:35],s63 offen offset:2048 sc1 lds
v_fma_f32 v52,-v60,v28,v52
v_mfma_f32_16x16x32_bf16 v[32:35],a[24:27],v[84:87],v[32:35]
v_fma_f32 v53,-v60,v29,v53
v_fma_f32 v54,-v60,v30,v54
v_mfma_f32_16x16x32_bf16 v[32:35],a[36:39],v[88:91],v[32:35]
ds_read_b128 v[112:115],v4 offset:10368
v_fma_f32 v55,-v60,v31,v55
v_mfma_f32_16x16x32_bf16 v[36:39],a[40:43],v[88:91],0
v_permlane32_swap_b32 v52,v54
v_mfma_f32_16x16x32_bf16 v[36:39],a[28:31],v[84:87],v[36:39]
buffer_load_dwordx4 v6,s[32:35],s63 offen offset:3072 sc1 lds
v_fma_f32 v48,-v60,v24,v48
v_mfma_f32_16x16x32_bf16 v[36:39],a[16:19],v[80:83],v[36:39]
ds_read_b128 v[116:119],v4 offset:11520
v_fma_f32 v49,-v60,v25,v49
v_mfma_f32_16x16x32_bf16 v[36:39],a[4:7],v[76:79],v[36:39]
v_permlane32_swap_b32 v53,v55
v_mfma_f32_16x16x32_bf16 v[40:43],a[8:11],v[76:79],0
s_waitcnt vmcnt(8)
s_barrier
v_mfma_f32_16x16x32_bf16 v[40:43],a[20:23],v[80:83],v[40:43]
buffer_store_dwordx2 v[16:17],v2,s[92:95],s68 idxen sc1
ds_read_b128 v[120:123],v4 offset:12672
v_mfma_f32_16x16x32_bf16 v[40:43],a[32:35],v[84:87],v[40:43]
v_fma_f32 v50,-v60,v26,v50
v_fma_f32 v51,-v60,v27,v51
v_mfma_f32_16x16x32_bf16 v[40:43],a[44:47],v[88:91],v[40:43]
v_fma_f32 v44,-v60,v20,v44
s_waitcnt lgkmcnt(4)
v_mfma_f32_16x16x32_bf16 v[40:43],a[56:59],v[92:95],v[40:43]
ds_read_b128 v[124:127],v4 offset:13824
s_add_u32 m0,s64,45056
v_mfma_f32_16x16x32_bf16 v[40:43],a[68:71],v[96:99],v[40:43]
buffer_load_dwordx4 v7,s[32:35],s63 offen offset:0 sc1 lds
s_add_u32 s63,s63,s30
v_mfma_f32_16x16x32_bf16 v[40:43],a[80:83],v[100:103],v[40:43]
s_xor_b32 s64,s64,s65
v_fma_f32 v45,-v60,v21,v45
v_mfma_f32_16x16x32_bf16 v[40:43],a[92:95],v[104:107],v[40:43]
ds_read_b128 v[76:79],v4 offset:14976
v_fma_f32 v46,-v60,v22,v46
v_mfma_f32_16x16x32_bf16 v[36:39],a[88:91],v[104:107],v[36:39]
v_fma_f32 v47,-v60,v23,v47
v_mul_f32 v64,v44,v61
v_mfma_f32_16x16x32_bf16 v[36:39],a[76:79],v[100:103],v[36:39]
v_exp_f32 v64,v64
v_mfma_f32_16x16x32_bf16 v[36:39],a[64:67],v[96:99],v[36:39]
ds_read_b128 v[80:83],v4 offset:16128
v_mul_f32 v65,v45,v61
v_mfma_f32_16x16x32_bf16 v[36:39],a[52:55],v[92:95],v[36:39]
v_exp_f32 v65,v65
v_mfma_f32_16x16x32_bf16 v[32:35],a[48:51],v[92:95],v[32:35]
v_mul_f32 v66,v46,v61
v_mul_f32 v67,v47,v61
v_mfma_f32_16x16x32_bf16 v[32:35],a[60:63],v[96:99],v[32:35]
ds_read_b128 v[84:87],v4 offset:17280
v_mul_f32 v68,v52,v61
v_mfma_f32_16x16x32_bf16 v[32:35],a[72:75],v[100:103],v[32:35]
v_exp_f32 v66,v66
v_mfma_f32_16x16x32_bf16 v[32:35],a[84:87],v[104:107],v[32:35]
v_mul_f32 v69,v53,v61
s_waitcnt lgkmcnt(4)
v_mfma_f32_16x16x32_bf16 v[32:35],a[96:99],v[108:111],v[32:35]
ds_read_b128 v[88:91],v4 offset:18432
v_mul_f32 v44,v44,v48
v_mfma_f32_16x16x32_bf16 v[32:35],a[108:111],v[112:115],v[32:35]
v_exp_f32 v67,v67
v_mfma_f32_16x16x32_bf16 v[32:35],a[120:123],v[116:119],v[32:35]
v_exp_f32 v68,v68
v_mfma_f32_16x16x32_bf16 v[32:35],a[132:135],v[120:123],v[32:35]
ds_read_b128 v[92:95],v4 offset:19584
v_mul_f32 v45,v45,v49
v_mfma_f32_16x16x32_bf16 v[36:39],a[136:139],v[120:123],v[36:39]
v_exp_f32 v69,v69
v_mfma_f32_16x16x32_bf16 v[36:39],a[124:127],v[116:119],v[36:39]
v_mul_f32 v46,v46,v50
v_mul_f32 v47,v47,v51
v_mfma_f32_16x16x32_bf16 v[36:39],a[112:115],v[112:115],v[36:39]
ds_read_b128 v[96:99],v4 offset:20736
v_mul_f32 v52,v52,v54
v_mfma_f32_16x16x32_bf16 v[36:39],a[100:103],v[108:111],v[36:39]
v_mul_f32 v53,v53,v55
v_fma_f32 v64,v64,v62,v62
v_mfma_f32_16x16x32_bf16 v[40:43],a[104:107],v[108:111],v[40:43]
v_rcp_f32 v64,v64
v_mfma_f32_16x16x32_bf16 v[40:43],a[116:119],v[112:115],v[40:43]
ds_read_b128 v[100:103],v4 offset:21888
v_fma_f32 v65,v65,v62,v62
v_mfma_f32_16x16x32_bf16 v[40:43],a[128:131],v[116:119],v[40:43]
v_rcp_f32 v65,v65
v_mfma_f32_16x16x32_bf16 v[40:43],a[140:143],v[120:123],v[40:43]
v_fma_f32 v66,v66,v62,v62
s_waitcnt lgkmcnt(4)
v_mfma_f32_16x16x32_bf16 v[40:43],a[152:155],v[124:127],v[40:43]
ds_read_b128 v[104:107],v4 offset:23040
v_fma_f32 v67,v67,v62,v62
v_mfma_f32_16x16x32_bf16 v[40:43],a[164:167],v[76:79],v[40:43]
v_rcp_f32 v66,v66
v_mfma_f32_16x16x32_bf16 v[40:43],a[176:179],v[80:83],v[40:43]
v_rcp_f32 v67,v67
v_mfma_f32_16x16x32_bf16 v[40:43],a[188:191],v[84:87],v[40:43]
ds_read_b128 v[108:111],v4 offset:24192
v_fma_f32 v68,v68,v62,v62
v_mfma_f32_16x16x32_bf16 v[36:39],a[184:187],v[84:87],v[36:39]
v_rcp_f32 v68,v68
v_mfma_f32_16x16x32_bf16 v[36:39],a[172:175],v[80:83],v[36:39]
v_fma_f32 v69,v69,v62,v62
v_mul_f32 v44,v44,v64
v_mfma_f32_16x16x32_bf16 v[36:39],a[160:163],v[76:79],v[36:39]
ds_read_b128 v[112:115],v4 offset:25344
v_mul_f32 v45,v45,v65
v_mfma_f32_16x16x32_bf16 v[36:39],a[148:151],v[124:127],v[36:39]
v_rcp_f32 v69,v69
v_mfma_f32_16x16x32_bf16 v[32:35],a[144:147],v[124:127],v[32:35]
v_mul_f32 v46,v46,v66
v_mul_f32 v47,v47,v67
v_mfma_f32_16x16x32_bf16 v[32:35],a[156:159],v[76:79],v[32:35]
ds_read_b128 v[116:119],v4 offset:26496
v_mul_f32 v52,v52,v68
v_mfma_f32_16x16x32_bf16 v[32:35],a[168:171],v[80:83],v[32:35]
v_mul_f32 v53,v53,v69
v_cvt_pk_bf16_f32 v72,v44,v45
v_mfma_f32_16x16x32_bf16 v[32:35],a[180:183],v[84:87],v[32:35]
v_cvt_pk_bf16_f32 v74,v52,v53
s_waitcnt lgkmcnt(4)
v_mfma_f32_16x16x32_bf16 v[32:35],a[192:195],v[88:91],v[32:35]
ds_read_b128 v[120:123],v4 offset:27648
v_cvt_pk_bf16_f32 v73,v46,v47
v_mfma_f32_16x16x32_bf16 v[32:35],a[204:207],v[92:95],v[32:35]
v_permlane32_swap_b32 v74,v75
v_mfma_f32_16x16x32_bf16 v[32:35],a[216:219],v[96:99],v[32:35]
v_add_f32_dpp v56,v56,v56 row_ror:2 row_mask:0xf bank_mask:0xf
v_mfma_f32_16x16x32_bf16 v[32:35],a[228:231],v[100:103],v[32:35]
ds_read_b128 v[124:127],v4 offset:28800
v_mul_f32 v56,0x3a800000,v56
v_mfma_f32_16x16x32_bf16 v[36:39],a[232:235],v[100:103],v[36:39]
v_permlane16_swap_b32 v72,v74
v_mfma_f32_16x16x32_bf16 v[36:39],a[220:223],v[96:99],v[36:39]
v_permlane16_swap_b32 v73,v75
v_mfma_f32_16x16x32_bf16 v[36:39],a[208:211],v[92:95],v[36:39]
buffer_store_dwordx4 v[72:75],v15,s[32:35],s68 offen sc1
ds_read_b128 v[76:79],v4 offset:29952
v_mfma_f32_16x16x32_bf16 v[36:39],a[196:199],v[88:91],v[36:39]
v_mul_f32 v57,v56,v56
v_mfma_f32_16x16x32_bf16 v[40:43],a[200:203],v[88:91],v[40:43]
v_mfma_f32_16x16x32_bf16 v[40:43],a[212:215],v[92:95],v[40:43]
ds_read_b128 v[80:83],v4 offset:31104
v_sub_f32_dpp v57,v56,v57 quad_perm:[1,1,3,3] row_mask:0xf bank_mask:0xf
v_mfma_f32_16x16x32_bf16 v[40:43],a[224:227],v[96:99],v[40:43]
v_max_f32 v57,0,v57
v_mfma_f32_16x16x32_bf16 v[40:43],a[236:239],v[100:103],v[40:43]
v_cvt_pk_bf16_f32 v57,0,v57
s_waitcnt lgkmcnt(4)
v_mfma_f32_16x16x32_bf16 v[40:43],a[248:251],v[104:107],v[40:43]
ds_read_b128 v[84:87],v4 offset:32256
v_add_f32 v57,0x3727c5ac,v57
v_mfma_f32_16x16x32_bf16 v[40:43],v[132:135],v[108:111],v[40:43]
v_cvt_pk_bf16_f32 v57,0,v57
v_mfma_f32_16x16x32_bf16 v[40:43],v[144:147],v[112:115],v[40:43]
v_sqrt_f32 v57,v57
v_mfma_f32_16x16x32_bf16 v[40:43],v[156:159],v[116:119],v[40:43]
ds_read_b128 v[88:91],v4 offset:33408
v_cvt_pk_bf16_f32 v57,v56,v57
v_mfma_f32_16x16x32_bf16 v[36:39],v[152:155],v[116:119],v[36:39]
ds_write_b32 v10,v57 offset:0
v_mfma_f32_16x16x32_bf16 v[36:39],v[140:143],v[112:115],v[36:39]
v_mfma_f32_16x16x32_bf16 v[36:39],v[128:131],v[108:111],v[36:39]
ds_read_b128 v[92:95],v4 offset:34560
v_mfma_f32_16x16x32_bf16 v[36:39],a[244:247],v[104:107],v[36:39]
v_mfma_f32_16x16x32_bf16 v[32:35],a[240:243],v[104:107],v[32:35]
s_waitcnt vmcnt(1)
s_waitcnt lgkmcnt(1)
s_barrier
v_mfma_f32_16x16x32_bf16 v[32:35],a[252:255],v[108:111],v[32:35]
ds_read_b128 v[96:99],v4 offset:35712
v_mfma_f32_16x16x32_bf16 v[32:35],v[136:139],v[112:115],v[32:35]
v_xor_b32 v4,73728,v4
v_mfma_f32_16x16x32_bf16 v[32:35],v[148:151],v[116:119],v[32:35]
v_mfma_f32_16x16x32_bf16 v[32:35],v[160:163],v[120:123],v[32:35]
ds_read_b128 v[100:103],v5 offset:0
s_add_u32 m0,s64,0
v_mfma_f32_16x16x32_bf16 v[32:35],v[172:175],v[124:127],v[32:35]
buffer_load_dwordx4 v2,s[32:35],s63 offen offset:0 sc1 lds
v_mfma_f32_16x16x32_bf16 v[32:35],v[184:187],v[76:79],v[32:35]
ds_read_b32 v62,v11 offset:0
v_mfma_f32_16x16x32_bf16 v[32:35],v[196:199],v[80:83],v[32:35]
ds_read_b128 v[104:107],v5 offset:1152
v_mfma_f32_16x16x32_bf16 v[36:39],v[200:203],v[80:83],v[36:39]
v_mfma_f32_16x16x32_bf16 v[36:39],v[188:191],v[76:79],v[36:39]
buffer_load_dwordx4 v2,s[32:35],s63 offen offset:1024 sc1 lds
v_mfma_f32_16x16x32_bf16 v[36:39],v[176:179],v[124:127],v[36:39]
ds_read_b128 v[108:111],v5 offset:2304
ds_read2st64_b32 v[56:57],v8 offset0:144 offset1:180
v_mfma_f32_16x16x32_bf16 v[36:39],v[164:167],v[120:123],v[36:39]
ds_read2st64_b32 v[58:59],v8 offset0:216 offset1:252
v_xor_b32 v8,v9,v8
v_mfma_f32_16x16x32_bf16 v[40:43],v[168:171],v[120:123],v[40:43]
s_waitcnt lgkmcnt(4)
v_lshlrev_b32 v60,16,v62
v_mfma_f32_16x16x32_bf16 v[40:43],v[180:183],v[124:127],v[40:43]
buffer_load_dwordx4 v2,s[32:35],s63 offen offset:2048 sc1 lds
ds_read_b128 v[112:115],v5 offset:3456
v_mfma_f32_16x16x32_bf16 v[40:43],v[192:195],v[76:79],v[40:43]
v_and_b32 v62,0xffff0000,v62
v_mfma_f32_16x16x32_bf16 v[40:43],v[204:207],v[80:83],v[40:43]
v_mfma_f32_16x16x32_bf16 v[40:43],v[216:219],v[84:87],v[40:43]
ds_read_b128 v[116:119],v5 offset:4608
v_mfma_f32_16x16x32_bf16 v[40:43],v[228:231],v[88:91],v[40:43]
buffer_load_dwordx4 v2,s[32:35],s63 offen offset:3072 sc1 lds
v_mfma_f32_16x16x32_bf16 v[40:43],v[240:243],v[92:95],v[40:43]
v_rcp_f32 v61,v62
v_mfma_f32_16x16x32_bf16 v[40:43],v[252:255],v[96:99],v[40:43]
ds_read_b128 v[120:123],v5 offset:5760
v_mul_f32 v62,v62,v62
v_mfma_f32_16x16x32_bf16 v[36:39],v[248:251],v[96:99],v[36:39]
s_add_u32 m0,s64,4096
s_waitcnt lgkmcnt(3)
v_add_f32 v56,v56,v57
v_mfma_f32_16x16x32_bf16 v[36:39],v[236:239],v[92:95],v[36:39]
buffer_load_dwordx4 v6,s[32:35],s63 offen offset:0 sc1 lds
v_add_f32 v58,v58,v59
v_mfma_f32_16x16x32_bf16 v[36:39],v[224:227],v[88:91],v[36:39]
ds_read_b128 v[124:127],v5 offset:6912
v_add_f32 v56,v56,v58
v_mfma_f32_16x16x32_bf16 v[36:39],v[212:215],v[84:87],v[36:39]
v_mul_f32 v61,0xbfb8aa3b,v61
v_add_f32_dpp v56,v56,v56 row_ror:8 row_mask:0xf bank_mask:0xf
v_mfma_f32_16x16x32_bf16 v[32:35],v[208:211],v[84:87],v[32:35]
v_mfma_f32_16x16x32_bf16 v[32:35],v[220:223],v[88:91],v[32:35]
buffer_load_dwordx4 v6,s[32:35],s63 offen offset:1024 sc1 lds
ds_read_b128 v[76:79],v5 offset:8064
v_mfma_f32_16x16x32_bf16 v[32:35],v[232:235],v[92:95],v[32:35]
v_add_f32_dpp v56,v56,v56 row_ror:4 row_mask:0xf bank_mask:0xf
s_sub_u32 s62,s62,1
v_mfma_f32_16x16x32_bf16 v[32:35],v[244:247],v[96:99],v[32:35]
s_cbranch_scc1 .L_gt0_3
.L_gm_3:
s_waitcnt lgkmcnt(4)
v_mfma_f32_16x16x32_bf16 v[44:47],a[0:3],v[100:103],0
ds_read_b128 v[80:83],v5 offset:9216
s_add_u32 s68,s68,s30
v_mfma_f32_16x16x32_bf16 v[44:47],a[12:15],v[104:107],v[44:47]
buffer_load_dwordx4 v6,s[32:35],s63 offen offset:2048 sc1 lds
v_fma_f32 v40,-v60,v28,v40
v_mfma_f32_16x16x32_bf16 v[44:47],a[24:27],v[108:111],v[44:47]
v_fma_f32 v41,-v60,v29,v41
v_fma_f32 v42,-v60,v30,v42
v_mfma_f32_16x16x32_bf16 v[44:47],a[36:39],v[112:115],v[44:47]
ds_read_b128 v[84:87],v5 offset:10368
v_fma_f32 v43,-v60,v31,v43
v_mfma_f32_16x16x32_bf16 v[48:51],a[40:43],v[112:115],0
v_permlane32_swap_b32 v40,v42
v_mfma_f32_16x16x32_bf16 v[48:51],a[28:31],v[108:111],v[48:51]
buffer_load_dwordx4 v6,s[32:35],s63 offen offset:3072 sc1 lds
v_fma_f32 v36,-v60,v24,v36
v_mfma_f32_16x16x32_bf16 v[48:51],a[16:19],v[104:107],v[48:51]
ds_read_b128 v[88:91],v5 offset:11520
v_fma_f32 v37,-v60,v25,v37
v_mfma_f32_16x16x32_bf16 v[48:51],a[4:7],v[100:103],v[48:51]
v_permlane32_swap_b32 v41,v43
v_mfma_f32_16x16x32_bf16 v[52:55],a[8:11],v[100:103],0
s_waitcnt vmcnt(8)
s_barrier
v_mfma_f32_16x16x32_bf16 v[52:55],a[20:23],v[104:107],v[52:55]
buffer_store_dwordx2 v[16:17],v2,s[92:95],s68 idxen sc1
ds_read_b128 v[92:95],v5 offset:12672
v_mfma_f32_16x16x32_bf16 v[52:55],a[32:35],v[108:111],v[52:55]
v_fma_f32 v38,-v60,v26,v38
v_fma_f32 v39,-v60,v27,v39
v_mfma_f32_16x16x32_bf16 v[52:55],a[44:47],v[112:115],v[52:55]
v_fma_f32 v32,-v60,v20,v32
s_waitcnt lgkmcnt(4)
v_mfma_f32_16x16x32_bf16 v[52:55],a[56:59],v[116:119],v[52:55]
ds_read_b128 v[96:99],v5 offset:13824
s_add_u32 m0,s64,8192
v_mfma_f32_16x16x32_bf16 v[52:55],a[68:71],v[120:123],v[52:55]
buffer_load_dwordx4 v7,s[32:35],s63 offen offset:0 sc1 lds
s_add_u32 s63,s63,s30
v_mfma_f32_16x16x32_bf16 v[52:55],a[80:83],v[124:127],v[52:55]
v_fma_f32 v33,-v60,v21,v33
v_fma_f32 v34,-v60,v22,v34
v_mfma_f32_16x16x32_bf16 v[52:55],a[92:95],v[76:79],v[52:55]
ds_read_b128 v[100:103],v5 offset:14976
v_fma_f32 v35,-v60,v23,v35
v_mfma_f32_16x16x32_bf16 v[48:51],a[88:91],v[76:79],v[48:51]
v_mul_f32 v64,v32,v61
v_mul_f32 v65,v33,v61
v_mfma_f32_16x16x32_bf16 v[48:51],a[76:79],v[124:127],v[48:51]
v_exp_f32 v64,v64
v_mfma_f32_16x16x32_bf16 v[48:51],a[64:67],v[120:123],v[48:51]
ds_read_b128 v[104:107],v5 offset:16128
v_mul_f32 v66,v34,v61
v_mfma_f32_16x16x32_bf16 v[48:51],a[52:55],v[116:119],v[48:51]
v_exp_f32 v65,v65
v_mfma_f32_16x16x32_bf16 v[44:47],a[48:51],v[116:119],v[44:47]
v_exp_f32 v66,v66
v_mfma_f32_16x16x32_bf16 v[44:47],a[60:63],v[120:123],v[44:47]
ds_read_b128 v[108:111],v5 offset:17280
v_mul_f32 v67,v35,v61
v_mfma_f32_16x16x32_bf16 v[44:47],a[72:75],v[124:127],v[44:47]
v_exp_f32 v67,v67
v_mfma_f32_16x16x32_bf16 v[44:47],a[84:87],v[76:79],v[44:47]
v_mul_f32 v68,v40,v61
s_waitcnt lgkmcnt(4)
v_mfma_f32_16x16x32_bf16 v[44:47],a[96:99],v[80:83],v[44:47]
ds_read_b128 v[112:115],v5 offset:18432
v_mul_f32 v69,v41,v61
v_mfma_f32_16x16x32_bf16 v[44:47],a[108:111],v[84:87],v[44:47]
v_exp_f32 v68,v68
v_mfma_f32_16x16x32_bf16 v[44:47],a[120:123],v[88:91],v[44:47]
v_exp_f32 v69,v69
v_mfma_f32_16x16x32_bf16 v[44:47],a[132:135],v[92:95],v[44:47]
ds_read_b128 v[116:119],v5 offset:19584
v_mul_f32 v32,v32,v36
v_mfma_f32_16x16x32_bf16 v[48:51],a[136:139],v[92:95],v[48:51]
v_mul_f32 v33,v33,v37
v_mul_f32 v34,v34,v38
v_mfma_f32_16x16x32_bf16 v[48:51],a[124:127],v[88:91],v[48:51]
v_mul_f32 v35,v35,v39
v_mul_f32 v40,v40,v42
v_mfma_f32_16x16x32_bf16 v[48:51],a[112:115],v[84:87],v[48:51]
ds_read_b128 v[120:123],v5 offset:20736
v_mul_f32 v41,v41,v43
v_mfma_f32_16x16x32_bf16 v[48:51],a[100:103],v[80:83],v[48:51]
v_fma_f32 v64,v64,v62,v62
v_fma_f32 v65,v65,v62,v62
v_mfma_f32_16x16x32_bf16 v[52:55],a[104:107],v[80:83],v[52:55]
v_rcp_f32 v64,v64
v_mfma_f32_16x16x32_bf16 v[52:55],a[116:119],v[84:87],v[52:55]
ds_read_b128 v[124:127],v5 offset:21888
v_fma_f32 v66,v66,v62,v62
v_mfma_f32_16x16x32_bf16 v[52:55],a[128:131],v[88:91],v[52:55]
v_rcp_f32 v65,v65
v_mfma_f32_16x16x32_bf16 v[52:55],a[140:143],v[92:95],v[52:55]
v_fma_f32 v67,v67,v62,v62
s_waitcnt lgkmcnt(4)
v_mfma_f32_16x16x32_bf16 v[52:55],a[152:155],v[96:99],v[52:55]
ds_read_b128 v[76:79],v5 offset:23040
v_fma_f32 v68,v68,v62,v62
v_mfma_f32_16x16x32_bf16 v[52:55],a[164:167],v[100:103],v[52:55]
v_rcp_f32 v66,v66
v_mfma_f32_16x16x32_bf16 v[52:55],a[176:179],v[104:107],v[52:55]
v_rcp_f32 v67,v67
v_mfma_f32_16x16x32_bf16 v[52:55],a[188:191],v[108:111],v[52:55]
ds_read_b128 v[80:83],v5 offset:24192
v_fma_f32 v69,v69,v62,v62
v_mfma_f32_16x16x32_bf16 v[48:51],a[184:187],v[108:111],v[48:51]
v_rcp_f32 v68,v68
v_mfma_f32_16x16x32_bf16 v[48:51],a[172:175],v[104:107],v[48:51]
v_rcp_f32 v69,v69
v_mfma_f32_16x16x32_bf16 v[48:51],a[160:163],v[100:103],v[48:51]
ds_read_b128 v[84:87],v5 offset:25344
v_mul_f32 v32,v32,v64
v_mfma_f32_16x16x32_bf16 v[48:51],a[148:151],v[96:99],v[48:51]
v_mul_f32 v33,v33,v65
v_mul_f32 v34,v34,v66
v_mfma_f32_16x16x32_bf16 v[44:47],a[144:147],v[96:99],v[44:47]
v_mul_f32 v35,v35,v67
v_mul_f32 v40,v40,v68
v_mfma_f32_16x16x32_bf16 v[44:47],a[156:159],v[100:103],v[44:47]
ds_read_b128 v[88:91],v5 offset:26496
v_mul_f32 v41,v41,v69
v_mfma_f32_16x16x32_bf16 v[44:47],a[168:171],v[104:107],v[44:47]
v_cvt_pk_bf16_f32 v74,v40,v41
v_cvt_pk_bf16_f32 v72,v32,v33
v_mfma_f32_16x16x32_bf16 v[44:47],a[180:183],v[108:111],v[44:47]
v_cvt_pk_bf16_f32 v73,v34,v35
s_waitcnt lgkmcnt(4)
v_mfma_f32_16x16x32_bf16 v[44:47],a[192:195],v[112:115],v[44:47]
ds_read_b128 v[92:95],v5 offset:27648
v_add_f32_dpp v56,v56,v56 row_ror:2 row_mask:0xf bank_mask:0xf
v_mfma_f32_16x16x32_bf16 v[44:47],a[204:207],v[116:119],v[44:47]
v_permlane32_swap_b32 v74,v75
v_mfma_f32_16x16x32_bf16 v[44:47],a[216:219],v[120:123],v[44:47]
v_mul_f32 v56,0x3a800000,v56
v_mfma_f32_16x16x32_bf16 v[44:47],a[228:231],v[124:127],v[44:47]
ds_read_b128 v[96:99],v5 offset:28800
v_mul_f32 v57,v56,v56
v_mfma_f32_16x16x32_bf16 v[48:51],a[232:235],v[124:127],v[48:51]
v_permlane16_swap_b32 v72,v74
v_mfma_f32_16x16x32_bf16 v[48:51],a[220:223],v[120:123],v[48:51]
v_permlane16_swap_b32 v73,v75
v_mfma_f32_16x16x32_bf16 v[48:51],a[208:211],v[116:119],v[48:51]
buffer_store_dwordx4 v[72:75],v15,s[32:35],s68 offen sc1
ds_read_b128 v[100:103],v5 offset:29952
v_mfma_f32_16x16x32_bf16 v[48:51],a[196:199],v[112:115],v[48:51]
v_sub_f32_dpp v57,v56,v57 quad_perm:[1,1,3,3] row_mask:0xf bank_mask:0xf
v_mfma_f32_16x16x32_bf16 v[52:55],a[200:203],v[112:115],v[52:55]
v_max_f32 v57,0,v57
v_mfma_f32_16x16x32_bf16 v[52:55],a[212:215],v[116:119],v[52:55]
ds_read_b128 v[104:107],v5 offset:31104
v_cvt_pk_bf16_f32 v57,0,v57
v_mfma_f32_16x16x32_bf16 v[52:55],a[224:227],v[120:123],v[52:55]
v_add_f32 v57,0x3727c5ac,v57
v_mfma_f32_16x16x32_bf16 v[52:55],a[236:239],v[124:127],v[52:55]
v_cvt_pk_bf16_f32 v57,0,v57
s_waitcnt lgkmcnt(4)
v_mfma_f32_16x16x32_bf16 v[52:55],a[248:251],v[76:79],v[52:55]
ds_read_b128 v[108:111],v5 offset:32256
v_mfma_f32_16x16x32_bf16 v[52:55],v[132:135],v[80:83],v[52:55]
v_sqrt_f32 v57,v57
v_mfma_f32_16x16x32_bf16 v[52:55],v[144:147],v[84:87],v[52:55]
v_cvt_pk_bf16_f32 v57,v56,v57
v_mfma_f32_16x16x32_bf16 v[52:55],v[156:159],v[88:91],v[52:55]
ds_read_b128 v[112:115],v5 offset:33408
ds_write_b32 v10,v57 offset:128
v_mfma_f32_16x16x32_bf16 v[48:51],v[152:155],v[88:91],v[48:51]
v_mfma_f32_16x16x32_bf16 v[48:51],v[140:143],v[84:87],v[48:51]
v_mfma_f32_16x16x32_bf16 v[48:51],v[128:131],v[80:83],v[48:51]
)#"
R"#(
ds_read_b128 v[116:119],v5 offset:34560
v_mfma_f32_16x16x32_bf16 v[48:51],a[244:247],v[76:79],v[48:51]
v_mfma_f32_16x16x32_bf16 v[44:47],a[240:243],v[76:79],v[44:47]
s_waitcnt vmcnt(1)
s_waitcnt lgkmcnt(1)
s_barrier
v_mfma_f32_16x16x32_bf16 v[44:47],a[252:255],v[80:83],v[44:47]
ds_read_b128 v[120:123],v5 offset:35712
v_mfma_f32_16x16x32_bf16 v[44:47],v[136:139],v[84:87],v[44:47]
v_xor_b32 v5,73728,v5
v_mfma_f32_16x16x32_bf16 v[44:47],v[148:151],v[88:91],v[44:47]
v_mfma_f32_16x16x32_bf16 v[44:47],v[160:163],v[92:95],v[44:47]
ds_read_b128 v[76:79],v4 offset:0
s_add_u32 m0,s64,36864
v_mfma_f32_16x16x32_bf16 v[44:47],v[172:175],v[96:99],v[44:47]
buffer_load_dwordx4 v2,s[32:35],s63 offen offset:0 sc1 lds
v_mfma_f32_16x16x32_bf16 v[44:47],v[184:187],v[100:103],v[44:47]
ds_read_b32 v62,v11 offset:128
v_mfma_f32_16x16x32_bf16 v[44:47],v[196:199],v[104:107],v[44:47]
ds_read_b128 v[80:83],v4 offset:1152
v_mfma_f32_16x16x32_bf16 v[48:51],v[200:203],v[104:107],v[48:51]
v_mfma_f32_16x16x32_bf16 v[48:51],v[188:191],v[100:103],v[48:51]
buffer_load_dwordx4 v2,s[32:35],s63 offen offset:1024 sc1 lds
v_mfma_f32_16x16x32_bf16 v[48:51],v[176:179],v[96:99],v[48:51]
ds_read_b128 v[84:87],v4 offset:2304
ds_read2st64_b32 v[56:57],v8 offset0:0 offset1:36
v_mfma_f32_16x16x32_bf16 v[48:51],v[164:167],v[92:95],v[48:51]
ds_read2st64_b32 v[58:59],v8 offset0:72 offset1:108
v_mfma_f32_16x16x32_bf16 v[52:55],v[168:171],v[92:95],v[52:55]
s_waitcnt lgkmcnt(4)
v_lshlrev_b32 v60,16,v62
v_mfma_f32_16x16x32_bf16 v[52:55],v[180:183],v[96:99],v[52:55]
buffer_load_dwordx4 v2,s[32:35],s63 offen offset:2048 sc1 lds
ds_read_b128 v[88:91],v4 offset:3456
v_mfma_f32_16x16x32_bf16 v[52:55],v[192:195],v[100:103],v[52:55]
v_and_b32 v62,0xffff0000,v62
v_mfma_f32_16x16x32_bf16 v[52:55],v[204:207],v[104:107],v[52:55]
v_mfma_f32_16x16x32_bf16 v[52:55],v[216:219],v[108:111],v[52:55]
ds_read_b128 v[92:95],v4 offset:4608
v_mfma_f32_16x16x32_bf16 v[52:55],v[228:231],v[112:115],v[52:55]
buffer_load_dwordx4 v2,s[32:35],s63 offen offset:3072 sc1 lds
v_mfma_f32_16x16x32_bf16 v[52:55],v[240:243],v[116:119],v[52:55]
v_rcp_f32 v61,v62
v_mfma_f32_16x16x32_bf16 v[52:55],v[252:255],v[120:123],v[52:55]
ds_read_b128 v[96:99],v4 offset:5760
v_mul_f32 v62,v62,v62
v_mfma_f32_16x16x32_bf16 v[48:51],v[248:251],v[120:123],v[48:51]
s_add_u32 m0,s64,40960
s_waitcnt lgkmcnt(3)
v_add_f32 v56,v56,v57
v_mfma_f32_16x16x32_bf16 v[48:51],v[236:239],v[116:119],v[48:51]
buffer_load_dwordx4 v6,s[32:35],s63 offen offset:0 sc1 lds
v_add_f32 v58,v58,v59
v_mfma_f32_16x16x32_bf16 v[48:51],v[224:227],v[112:115],v[48:51]
ds_read_b128 v[100:103],v4 offset:6912
v_add_f32 v56,v56,v58
v_mfma_f32_16x16x32_bf16 v[48:51],v[212:215],v[108:111],v[48:51]
v_mul_f32 v61,0xbfb8aa3b,v61
v_add_f32_dpp v56,v56,v56 row_ror:8 row_mask:0xf bank_mask:0xf
v_mfma_f32_16x16x32_bf16 v[44:47],v[208:211],v[108:111],v[44:47]
v_mfma_f32_16x16x32_bf16 v[44:47],v[220:223],v[112:115],v[44:47]
buffer_load_dwordx4 v6,s[32:35],s63 offen offset:1024 sc1 lds
ds_read_b128 v[104:107],v4 offset:8064
v_mfma_f32_16x16x32_bf16 v[44:47],v[232:235],v[116:119],v[44:47]
v_add_f32_dpp v56,v56,v56 row_ror:4 row_mask:0xf bank_mask:0xf
s_sub_u32 s62,s62,1
v_mfma_f32_16x16x32_bf16 v[44:47],v[244:247],v[120:123],v[44:47]
s_cbranch_scc0 .L_gl_3
s_nop 1
s_add_u32 s68,s68,s30
s_waitcnt vmcnt(6)
s_barrier
buffer_store_dwordx2 v[16:17],v2,s[92:95],s68 idxen sc1
v_fma_f32 v52,-v60,v28,v52
v_fma_f32 v53,-v60,v29,v53
v_fma_f32 v54,-v60,v30,v54
v_fma_f32 v55,-v60,v31,v55
v_fma_f32 v48,-v60,v24,v48
v_fma_f32 v49,-v60,v25,v49
v_fma_f32 v50,-v60,v26,v50
v_fma_f32 v51,-v60,v27,v51
v_fma_f32 v44,-v60,v20,v44
v_fma_f32 v45,-v60,v21,v45
v_fma_f32 v46,-v60,v22,v46
v_fma_f32 v47,-v60,v23,v47
v_permlane32_swap_b32 v52,v54
v_permlane32_swap_b32 v53,v55
v_mul_f32 v64,v44,v61
v_mul_f32 v65,v45,v61
v_mul_f32 v66,v46,v61
v_mul_f32 v67,v47,v61
v_mul_f32 v68,v52,v61
v_mul_f32 v69,v53,v61
v_exp_f32 v64,v64
v_exp_f32 v65,v65
v_mul_f32 v44,v44,v48
v_mul_f32 v45,v45,v49
v_exp_f32 v66,v66
v_exp_f32 v67,v67
v_mul_f32 v46,v46,v50
v_mul_f32 v47,v47,v51
v_exp_f32 v68,v68
v_exp_f32 v69,v69
v_mul_f32 v52,v52,v54
v_mul_f32 v53,v53,v55
v_fma_f32 v64,v64,v62,v62
v_fma_f32 v65,v65,v62,v62
v_rcp_f32 v64,v64
v_rcp_f32 v65,v65
v_fma_f32 v66,v66,v62,v62
v_fma_f32 v67,v67,v62,v62
v_rcp_f32 v66,v66
v_rcp_f32 v67,v67
v_fma_f32 v68,v68,v62,v62
v_fma_f32 v69,v69,v62,v62
v_rcp_f32 v68,v68
v_rcp_f32 v69,v69
v_mul_f32 v44,v44,v64
v_mul_f32 v45,v45,v65
v_mul_f32 v46,v46,v66
v_mul_f32 v47,v47,v67
v_mul_f32 v52,v52,v68
v_mul_f32 v53,v53,v69
v_cvt_pk_bf16_f32 v74,v52,v53
v_cvt_pk_bf16_f32 v72,v44,v45
v_cvt_pk_bf16_f32 v73,v46,v47
v_permlane32_swap_b32 v74,v75
s_nop 1
v_permlane16_swap_b32 v72,v74
v_permlane16_swap_b32 v73,v75
buffer_store_dwordx4 v[72:75],v15,s[32:35],s68 offen sc1
s_add_u32 s68,s68,s30
s_waitcnt vmcnt(0)
s_barrier
buffer_store_dwordx2 v[16:17],v2,s[92:95],s68 idxen sc1
s_branch .L_end
.L_gt0_3:
s_nop 1
s_add_u32 s68,s68,s30
s_waitcnt vmcnt(6)
s_barrier
buffer_store_dwordx2 v[16:17],v2,s[92:95],s68 idxen sc1
v_fma_f32 v40,-v60,v28,v40
v_fma_f32 v41,-v60,v29,v41
v_fma_f32 v42,-v60,v30,v42
v_fma_f32 v43,-v60,v31,v43
v_fma_f32 v36,-v60,v24,v36
v_fma_f32 v37,-v60,v25,v37
v_fma_f32 v38,-v60,v26,v38
v_fma_f32 v39,-v60,v27,v39
v_fma_f32 v32,-v60,v20,v32
v_fma_f32 v33,-v60,v21,v33
v_fma_f32 v34,-v60,v22,v34
v_fma_f32 v35,-v60,v23,v35
v_permlane32_swap_b32 v40,v42
v_permlane32_swap_b32 v41,v43
v_mul_f32 v64,v32,v61
v_mul_f32 v65,v33,v61
v_mul_f32 v66,v34,v61
v_mul_f32 v67,v35,v61
v_mul_f32 v68,v40,v61
v_mul_f32 v69,v41,v61
v_exp_f32 v64,v64
v_exp_f32 v65,v65
v_mul_f32 v32,v32,v36
v_mul_f32 v33,v33,v37
v_exp_f32 v66,v66
v_exp_f32 v67,v67
v_mul_f32 v34,v34,v38
v_mul_f32 v35,v35,v39
v_exp_f32 v68,v68
v_exp_f32 v69,v69
v_mul_f32 v40,v40,v42
v_mul_f32 v41,v41,v43
v_fma_f32 v64,v64,v62,v62
v_fma_f32 v65,v65,v62,v62
v_rcp_f32 v64,v64
v_rcp_f32 v65,v65
v_fma_f32 v66,v66,v62,v62
v_fma_f32 v67,v67,v62,v62
v_rcp_f32 v66,v66
v_rcp_f32 v67,v67
v_fma_f32 v68,v68,v62,v62
v_fma_f32 v69,v69,v62,v62
v_rcp_f32 v68,v68
v_rcp_f32 v69,v69
v_mul_f32 v32,v32,v64
v_mul_f32 v33,v33,v65
v_mul_f32 v34,v34,v66
v_mul_f32 v35,v35,v67
v_mul_f32 v40,v40,v68
v_mul_f32 v41,v41,v69
v_cvt_pk_bf16_f32 v74,v40,v41
v_cvt_pk_bf16_f32 v72,v32,v33
v_cvt_pk_bf16_f32 v73,v34,v35
v_permlane32_swap_b32 v74,v75
s_nop 1
v_permlane16_swap_b32 v72,v74
v_permlane16_swap_b32 v73,v75
buffer_store_dwordx4 v[72:75],v15,s[32:35],s68 offen sc1
s_add_u32 s68,s68,s30
s_waitcnt vmcnt(0)
s_barrier
buffer_store_dwordx2 v[16:17],v2,s[92:95],s68 idxen sc1
s_branch .L_end
.L_q:
v_mov_b32 v16,0x3f803f80
v_mov_b32 v17,0x3f803f80
v_mov_b32 v18,0x3f803f80
v_mov_b32 v19,0x3f803f80
s_add_u32 s36,s10,0x0
s_addc_u32 s37,s11,0x0
s_and_b32 s37,s37,0xffff
s_mov_b32 s38,0x600000
s_mov_b32 s39,0x20000
s_add_u32 s40,s10,0x400
s_addc_u32 s41,s11,0x0
s_and_b32 s41,s41,0xffff
s_mov_b32 s42,0x5ffc00
s_mov_b32 s43,0x20000
s_add_u32 s44,s10,0x800
s_addc_u32 s45,s11,0x0
s_and_b32 s45,s45,0xffff
s_mov_b32 s46,0x5ff800
s_mov_b32 s47,0x20000
s_lshl_b32 s96,s27,7
v_and_b32 v68,3,v1
v_mul_u32_u24 v68,8,v68
v_and_b32 v72,1,v1
v_bfe_u32 v3,v1,1,1
v_lshl_add_u32 v72,v72,1,v3
v_mul_u32_u24 v72,8,v72
s_lshl_b32 s48,s26,5
v_add_u32 v68,s48,v68
v_add_u32 v72,s48,v72
s_add_u32 s4,s8,0x0
s_addc_u32 s5,s9,0x0
s_and_b32 s5,s5,0xffff
s_mov_b32 s6,0x400000
s_mov_b32 s7,0x20000
v_lshrrev_b32 v57,3,v1
s_and_b32 s48,s26,1
v_xor_b32 v3,v57,v1
v_xor_b32 v3,s48,v3
v_and_b32 v3,7,v3
v_lshlrev_b32 v57,11,v57
v_lshl_add_u32 v57,v3,4,v57
s_lshl_b32 s56,s28,20
s_sub_u32 s56,s56,65536
s_and_b32 s56,s56,0x3fffff
s_lshl_b32 s57,s26,14
s_add_u32 s56,s56,s57
s_lshl_b32 s57,s26,10
v_lshrrev_b32 v64,3,v1
v_bfe_u32 v65,v1,4,1
s_and_b32 s48,s26,1
s_lshl_b32 s48,s48,1
v_or_b32 v65,s48,v65
v_lshlrev_b32 v65,1,v65
v_and_b32 v66,7,v1
v_xor_b32 v65,v65,v66
v_mul_u32_u24 v64,6144,v64
v_lshl_add_u32 v67,v65,4,v64
s_mul_i32 s48,s26,49152
s_add_u32 s96,s96,s48
s_mul_i32 s97,s26,3072
s_add_u32 m0,s57,24576
s_nop 0
buffer_load_dwordx4 v57,s[4:7],s56 offen offset:0 lds
s_add_u32 m0,s97,0
s_nop 0
buffer_load_dwordx4 v67,s[36:39],s96 offen offset:0 lds
buffer_load_dwordx4 v67,s[40:43],s96 offen offset:1024 lds
buffer_load_dwordx4 v67,s[44:47],s96 offen offset:2048 lds
s_add_u32 s96,s96,196608
s_add_u32 m0,s97,12288
s_nop 0
buffer_load_dwordx4 v67,s[36:39],s96 offen offset:0 lds
buffer_load_dwordx4 v67,s[40:43],s96 offen offset:1024 lds
buffer_load_dwordx4 v67,s[44:47],s96 offen offset:2048 lds
s_add_u32 s96,s96,196608
s_add_u32 m0,s57,28544
s_nop 0
buffer_load_dwordx4 v57,s[4:7],s56 offen offset:128 lds
s_add_u32 m0,s97,139264
s_nop 0
buffer_load_dwordx4 v67,s[36:39],s96 offen offset:0 lds
buffer_load_dwordx4 v67,s[40:43],s96 offen offset:1024 lds
buffer_load_dwordx4 v67,s[44:47],s96 offen offset:2048 lds
s_add_u32 s96,s96,196608
s_add_u32 m0,s97,151552
s_nop 0
buffer_load_dwordx4 v67,s[36:39],s96 offen offset:0 lds
buffer_load_dwordx4 v67,s[40:43],s96 offen offset:1024 lds
buffer_load_dwordx4 v67,s[44:47],s96 offen offset:2048 lds
s_add_u32 s96,s96,196608
s_add_u32 m0,s57,32512
s_nop 0
buffer_load_dwordx4 v57,s[4:7],s56 offen offset:256 lds
s_add_u32 m0,s97,90112
s_nop 0
buffer_load_dwordx4 v67,s[36:39],s96 offen offset:0 lds
buffer_load_dwordx4 v67,s[40:43],s96 offen offset:1024 lds
buffer_load_dwordx4 v67,s[44:47],s96 offen offset:2048 lds
s_add_u32 s96,s96,196608
s_add_u32 m0,s97,102400
s_nop 0
buffer_load_dwordx4 v67,s[36:39],s96 offen offset:0 lds
buffer_load_dwordx4 v67,s[40:43],s96 offen offset:1024 lds
buffer_load_dwordx4 v67,s[44:47],s96 offen offset:2048 lds
s_add_u32 s96,s96,196608
v_lshrrev_b32 v65,4,v1
v_bfe_u32 v64,v1,3,1
v_and_b32 v66,1,v65
v_lshl_add_u32 v64,v66,1,v64
v_lshlrev_b32 v64,5,v64
v_mul_u32_u24 v65,3072,v65
v_bfe_u32 v66,v1,2,2
v_lshl_add_u32 v65,v66,7,v65
v_xor_b32 v68,v64,v68
v_add_u32 v68,v65,v68
v_add_u32 v69,139264,v68
v_add_u32 v70,90112,v68
v_add_u32 v71,114688,v68
v_xor_b32 v72,v64,v72
v_add_u32 v72,v65,v72
v_add_u32 v73,139264,v72
v_add_u32 v74,90112,v72
v_add_u32 v75,114688,v72
s_lshl2_add_u32 s48,s27,s26
s_lshl_b32 s48,s48,9
s_add_u32 s68,s48,4648960
s_lshl2_add_u32 s49,s27,32
s_waitcnt lgkmcnt(0)
s_add_u32 s54,s52,0x7f4a7c15
s_addc_u32 s55,s53,0x9e3779b9
s_lshl_b32 s67,s49,5
s_add_u32 s67,s67,4898816
s_sub_u32 s51,s67,s68
s_add_u32 s92,s32,s51
s_addc_u32 s93,s33,0x10000
s_cmp_eq_u32 s26,0
s_cselect_b32 s94,1,0
s_mov_b32 s95,0x20000
v_mov_b32 v30,s54
v_mov_b32 v31,s55
v_cmp_gt_u32 vcc,12,v1
v_mul_u32_u24 v3,296960,v1
v_mov_b32 v12,0x7fff0000
v_cndmask_b32 v3,v12,v3,vcc
s_add_u32 s51,s67,76021760
buffer_store_dwordx2 v[30:31],v3,s[0:3],s51 offen sc1
s_sub_u32 s67,s68,296960
s_mul_i32 s49,s28,296960
s_add_u32 s68,s68,s49
s_sub_u32 s68,s68,2375680
s_mov_b32 s62,31
s_add_u32 s48,s28,3
s_and_b32 s48,s48,3
s_mul_i32 s48,s48,296960
s_add_u32 s66,s48,41721856
s_sub_u32 s48,s48,s49
s_add_u32 s69,s48,38010880
s_sub_u32 s70,0xfdce2000,s48
s_mov_b32 s72,s70
s_mov_b32 s63,0xfffba000
s_mul_i32 s64,s26,9216
s_add_u32 s65,s64,73728
s_xor_b32 s65,s65,s64
s_add_u32 s63,s63,s64
v_mov_b32 v4,v2
v_add_u32 v5,36864,v2
s_mov_b32 s71,s63
v_bfe_u32 v3,v1,1,3
v_mul_u32_u24 v6,1152,v3
v_lshrrev_b32 v3,1,v3
v_add_u32 v3,s26,v3
v_lshrrev_b32 v7,4,v1
v_lshl_add_u32 v3,v3,2,v7
v_and_b32 v3,15,v3
v_lshl_add_u32 v6,v3,2,v6
v_and_b32 v3,1,v1
v_lshl_add_u32 v6,v3,6,v6
v_add_u32 v6,1024,v6
s_lshl_b32 s48,s26,2
v_add_u32 v8,s48,v7
v_and_b32 v9,15,v1
v_add_u32 v3,64,v1
v_cmp_eq_u32 vcc,0,v9
v_add_u32 v7,73728,v6
v_xor_b32 v7,v7,v6
v_cndmask_b32 v8,v3,v8,vcc
v_lshlrev_b32 v8,2,v8
v_add_u32 v8,147456,v8
v_lshlrev_b32 v9,2,v9
v_add_u32 v9,147456,v9
v_lshrrev_b32 v10,4,v1
v_and_b32 v32,15,v1
v_and_b32 v33,1,v1
v_lshl_add_u32 v34,v10,2,v33
v_lshlrev_b32 v33,4,v33
v_mov_b32 v35,0xffff
v_lshlrev_b32 v33,v33,v35
v_cmp_le_u32 vcc,v34,v32
v_add_u32 v34,2,v34
s_nop 0
v_cndmask_b32 v124,0,v33,vcc
v_xor_b32 v126,v124,v33
v_cmp_le_u32 vcc,v34,v32
s_nop 1
v_cndmask_b32 v125,0,v33,vcc
v_xor_b32 v127,v125,v33
v_lshlrev_b32 v10,4,v10
v_add_u32 v10,147456,v10
s_lshl_b32 s48,s26,10
s_add_u32 s48,s48,148480
v_lshlrev_b32 v11,3,v1
v_add_u32 v11,s48,v11
s_lshl_b32 s48,s26,3
s_add_u32 s48,s48,0
v_add_u32 v0,s48,v1
v_lshrrev_b32 v0,2,v0
v_lshlrev_b32 v0,7,v0
v_cmp_gt_u32 vcc,8,v1
v_mov_b32 v3,0x7fff0000
s_nop 0
v_cndmask_b32 v0,v3,v0,vcc
v_cndmask_b32 v30,0,v30,vcc
v_cndmask_b32 v31,0,v31,vcc
v_mov_b32 v116,0xff61b1e6
v_mov_b32 v117,0xff61b1e6
v_mov_b32 v118,0xff61b1e6
v_mov_b32 v119,0xff61b1e6
v_mov_b32 v120,0xff61b1e6
v_mov_b32 v121,0xff61b1e6
v_mov_b32 v122,0xff61b1e6
v_mov_b32 v123,0xff61b1e6
v_cmp_ne_u32 vcc,0xffff,v124
s_nop 1
v_cndmask_b32 v116,0,v116,vcc
v_cmp_ne_u32 vcc,0xffff0000,v124
s_nop 1
v_cndmask_b32 v117,0,v117,vcc
v_cmp_ne_u32 vcc,0xffff,v125
s_nop 1
v_cndmask_b32 v118,0,v118,vcc
v_cmp_ne_u32 vcc,0xffff0000,v125
s_nop 1
v_cndmask_b32 v119,0,v119,vcc
v_cmp_ne_u32 vcc,0xffff,v126
s_nop 1
v_cndmask_b32 v120,0,v120,vcc
v_cmp_ne_u32 vcc,0xffff0000,v126
s_nop 1
v_cndmask_b32 v121,0,v121,vcc
v_cmp_ne_u32 vcc,0xffff,v127
s_nop 1
v_cndmask_b32 v122,0,v122,vcc
v_cmp_ne_u32 vcc,0xffff0000,v127
s_nop 1
v_cndmask_b32 v123,0,v123,vcc
v_bfe_u32 v3,v1,3,2
v_mul_u32_u24 v56,1187840,v3
v_cmp_eq_u32 vcc,3,v3
s_sub_u32 s48,0,s70
v_mov_b32 v3,s48
v_cndmask_b32 v56,v56,v3,vcc
v_and_b32 v3,7,v1
v_lshl_add_u32 v56,v3,7,v56
v_bfe_u32 v3,v1,3,1
v_lshrrev_b32 v58,4,v1
v_xor_b32 v3,v3,v58
v_xor_b32 v3,v3,v1
v_and_b32 v3,7,v3
v_and_b32 v58,15,v1
v_lshlrev_b32 v58,7,v58
v_lshl_add_u32 v58,v3,4,v58
v_xor_b32 v59,64,v58
v_add_u32 v58,24576,v58
v_add_u32 v59,24576,v59
v_cmp_gt_u32 vcc,32,v1
v_mov_b32 v1,0x7fff0000
s_nop 0
v_cndmask_b32 v1,v1,v2,vcc
s_mov_b32 s58,0x4003c000
s_mov_b32 s59,0x0
s_mov_b32 s60,0x80028000
s_mov_b32 s61,0x0
v_subrev_u32 v3,148480,v11
v_xor_b32 v3,1024,v3
v_add_u32 v3,148480,v3
s_add_u32 s74,s66,s72
s_sub_u32 s74,s66,286720
s_add_u32 s66,s66,s72
s_mov_b32 s72,1187840
s_waitcnt vmcnt(15)
s_barrier
s_add_u32 m0,s57,36480
s_nop 0
buffer_load_dwordx4 v57,s[4:7],s56 offen offset:384 lds
)#"
R"#(
ds_read_b64_tr_b16 a[0:1],v68 offset:0
ds_read_b64_tr_b16 a[2:3],v68 offset:512
s_add_u32 m0,s97,114688
s_nop 0
buffer_load_dwordx4 v67,s[36:39],s96 offen offset:0 lds
ds_read_b64_tr_b16 a[4:5],v68 offset:1024
ds_read_b64_tr_b16 a[6:7],v68 offset:1536
buffer_load_dwordx4 v67,s[40:43],s96 offen offset:1024 lds
ds_read_b64_tr_b16 a[8:9],v72 offset:2048
ds_read_b64_tr_b16 a[10:11],v72 offset:2560
buffer_load_dwordx4 v67,s[44:47],s96 offen offset:2048 lds
ds_read_b64_tr_b16 a[12:13],v68 offset:12288
ds_read_b64_tr_b16 a[14:15],v68 offset:12800
ds_read_b64_tr_b16 a[16:17],v68 offset:13312
s_add_u32 s96,s96,196608
s_add_u32 m0,s97,126976
s_nop 0
buffer_load_dwordx4 v67,s[36:39],s96 offen offset:0 lds
ds_read_b64_tr_b16 a[18:19],v68 offset:13824
ds_read_b64_tr_b16 a[20:21],v72 offset:14336
buffer_load_dwordx4 v67,s[40:43],s96 offen offset:1024 lds
ds_read_b64_tr_b16 a[22:23],v72 offset:14848
ds_read_b128 v[76:79],v58 offset:0
buffer_load_dwordx4 v67,s[44:47],s96 offen offset:2048 lds
ds_read_b128 v[80:83],v58 offset:2048
ds_read_b128 v[84:87],v59 offset:0
ds_read_b128 v[88:91],v59 offset:2048
s_add_u32 s96,s96,196608
s_waitcnt lgkmcnt(0)
s_waitcnt vmcnt(15)
s_barrier
s_add_u32 m0,s57,40448
s_nop 0
buffer_load_dwordx4 v57,s[4:7],s56 offen offset:512 lds
ds_read_b64_tr_b16 a[24:25],v69 offset:0
ds_read_b64_tr_b16 a[26:27],v69 offset:512
v_mfma_f32_16x16x32_bf16 v[20:23],a[0:3],v[16:19],0
v_mfma_f32_16x16x32_bf16 v[24:27],a[4:7],v[16:19],0
s_add_u32 m0,s97,0
s_nop 0
buffer_load_dwordx4 v67,s[36:39],s96 offen offset:0 lds
ds_read_b64_tr_b16 a[28:29],v69 offset:1024
ds_read_b64_tr_b16 a[30:31],v69 offset:1536
v_mfma_f32_16x16x32_bf16 v[32:35],v[16:19],a[8:11],0
v_mfma_f32_16x16x32_bf16 v[36:39],a[4:7],v[76:79],0
buffer_load_dwordx4 v67,s[40:43],s96 offen offset:1024 lds
ds_read_b64_tr_b16 a[32:33],v73 offset:2048
ds_read_b64_tr_b16 a[34:35],v73 offset:2560
v_mfma_f32_16x16x32_bf16 v[40:43],v[76:79],a[8:11],0
v_mfma_f32_16x16x32_bf16 v[44:47],a[0:3],v[80:83],0
buffer_load_dwordx4 v67,s[44:47],s96 offen offset:2048 lds
ds_read_b64_tr_b16 a[36:37],v69 offset:12288
ds_read_b64_tr_b16 a[38:39],v69 offset:12800
ds_read_b64_tr_b16 a[40:41],v69 offset:13312
v_mfma_f32_16x16x32_bf16 v[48:51],a[4:7],v[80:83],0
v_mfma_f32_16x16x32_bf16 v[52:55],v[80:83],a[8:11],0
v_mfma_f32_16x16x32_bf16 v[20:23],a[12:15],v[16:19],v[20:23]
s_add_u32 s96,s96,196608
s_add_u32 m0,s97,12288
s_nop 0
buffer_load_dwordx4 v67,s[36:39],s96 offen offset:0 lds
ds_read_b64_tr_b16 a[42:43],v69 offset:13824
ds_read_b64_tr_b16 a[44:45],v73 offset:14336
v_mfma_f32_16x16x32_bf16 v[24:27],a[16:19],v[16:19],v[24:27]
v_mfma_f32_16x16x32_bf16 v[32:35],v[16:19],a[20:23],v[32:35]
buffer_load_dwordx4 v67,s[40:43],s96 offen offset:1024 lds
ds_read_b64_tr_b16 a[46:47],v73 offset:14848
ds_read_b128 v[92:95],v58 offset:4096
v_mfma_f32_16x16x32_bf16 v[36:39],a[16:19],v[84:87],v[36:39]
v_mfma_f32_16x16x32_bf16 v[40:43],v[84:87],a[20:23],v[40:43]
buffer_load_dwordx4 v67,s[44:47],s96 offen offset:2048 lds
ds_read_b128 v[96:99],v58 offset:6144
ds_read_b128 v[100:103],v59 offset:4096
ds_read_b128 v[104:107],v59 offset:6144
v_mfma_f32_16x16x32_bf16 v[44:47],a[12:15],v[88:91],v[44:47]
v_mfma_f32_16x16x32_bf16 v[48:51],a[16:19],v[88:91],v[48:51]
v_mfma_f32_16x16x32_bf16 v[52:55],v[88:91],a[20:23],v[52:55]
s_add_u32 s96,s96,196608
s_waitcnt lgkmcnt(0)
s_waitcnt vmcnt(15)
s_barrier
s_add_u32 m0,s57,44416
s_nop 0
buffer_load_dwordx4 v57,s[4:7],s56 offen offset:640 lds
ds_read_b64_tr_b16 a[48:49],v70 offset:0
ds_read_b64_tr_b16 a[50:51],v70 offset:512
v_mfma_f32_16x16x32_bf16 v[20:23],a[24:27],v[16:19],v[20:23]
v_mfma_f32_16x16x32_bf16 v[24:27],a[28:31],v[16:19],v[24:27]
s_add_u32 m0,s97,139264
s_nop 0
buffer_load_dwordx4 v67,s[36:39],s96 offen offset:0 lds
ds_read_b64_tr_b16 a[52:53],v70 offset:1024
ds_read_b64_tr_b16 a[54:55],v70 offset:1536
v_mfma_f32_16x16x32_bf16 v[32:35],v[16:19],a[32:35],v[32:35]
v_mfma_f32_16x16x32_bf16 v[36:39],a[28:31],v[92:95],v[36:39]
buffer_load_dwordx4 v67,s[40:43],s96 offen offset:1024 lds
ds_read_b64_tr_b16 a[56:57],v74 offset:2048
ds_read_b64_tr_b16 a[58:59],v74 offset:2560
v_mfma_f32_16x16x32_bf16 v[40:43],v[92:95],a[32:35],v[40:43]
v_mfma_f32_16x16x32_bf16 v[44:47],a[24:27],v[96:99],v[44:47]
buffer_load_dwordx4 v67,s[44:47],s96 offen offset:2048 lds
ds_read_b64_tr_b16 a[60:61],v70 offset:12288
ds_read_b64_tr_b16 a[62:63],v70 offset:12800
ds_read_b64_tr_b16 a[64:65],v70 offset:13312
v_mfma_f32_16x16x32_bf16 v[48:51],a[28:31],v[96:99],v[48:51]
v_mfma_f32_16x16x32_bf16 v[52:55],v[96:99],a[32:35],v[52:55]
v_mfma_f32_16x16x32_bf16 v[20:23],a[36:39],v[16:19],v[20:23]
s_add_u32 s96,s96,196608
s_add_u32 m0,s97,151552
s_nop 0
buffer_load_dwordx4 v67,s[36:39],s96 offen offset:0 lds
ds_read_b64_tr_b16 a[66:67],v70 offset:13824
ds_read_b64_tr_b16 a[68:69],v74 offset:14336
v_mfma_f32_16x16x32_bf16 v[24:27],a[40:43],v[16:19],v[24:27]
v_mfma_f32_16x16x32_bf16 v[32:35],v[16:19],a[44:47],v[32:35]
buffer_load_dwordx4 v67,s[40:43],s96 offen offset:1024 lds
ds_read_b64_tr_b16 a[70:71],v74 offset:14848
ds_read_b128 v[76:79],v58 offset:8192
v_mfma_f32_16x16x32_bf16 v[36:39],a[40:43],v[100:103],v[36:39]
v_mfma_f32_16x16x32_bf16 v[40:43],v[100:103],a[44:47],v[40:43]
buffer_load_dwordx4 v67,s[44:47],s96 offen offset:2048 lds
ds_read_b128 v[80:83],v58 offset:10240
ds_read_b128 v[84:87],v59 offset:8192
ds_read_b128 v[88:91],v59 offset:10240
v_mfma_f32_16x16x32_bf16 v[44:47],a[36:39],v[104:107],v[44:47]
v_mfma_f32_16x16x32_bf16 v[48:51],a[40:43],v[104:107],v[48:51]
v_mfma_f32_16x16x32_bf16 v[52:55],v[104:107],a[44:47],v[52:55]
s_add_u32 s96,s96,196608
s_waitcnt lgkmcnt(0)
s_waitcnt vmcnt(14)
s_barrier
s_add_u32 m0,s57,48384
s_nop 0
buffer_load_dwordx4 v57,s[4:7],s56 offen offset:768 lds
ds_read_b64_tr_b16 a[72:73],v71 offset:0
ds_read_b64_tr_b16 a[74:75],v71 offset:512
v_mfma_f32_16x16x32_bf16 v[20:23],a[48:51],v[16:19],v[20:23]
v_mfma_f32_16x16x32_bf16 v[24:27],a[52:55],v[16:19],v[24:27]
s_add_u32 m0,s97,90112
s_nop 0
buffer_load_dwordx4 v67,s[36:39],s96 offen offset:0 lds
ds_read_b64_tr_b16 a[76:77],v71 offset:1024
ds_read_b64_tr_b16 a[78:79],v71 offset:1536
v_mfma_f32_16x16x32_bf16 v[32:35],v[16:19],a[56:59],v[32:35]
v_mfma_f32_16x16x32_bf16 v[36:39],a[52:55],v[76:79],v[36:39]
buffer_load_dwordx4 v67,s[40:43],s96 offen offset:1024 lds
ds_read_b64_tr_b16 a[80:81],v75 offset:2048
ds_read_b64_tr_b16 a[82:83],v75 offset:2560
v_mfma_f32_16x16x32_bf16 v[40:43],v[76:79],a[56:59],v[40:43]
v_mfma_f32_16x16x32_bf16 v[44:47],a[48:51],v[80:83],v[44:47]
buffer_load_dwordx4 v67,s[44:47],s96 offen offset:2048 lds
ds_read_b64_tr_b16 a[84:85],v71 offset:12288
ds_read_b64_tr_b16 a[86:87],v71 offset:12800
ds_read_b64_tr_b16 a[88:89],v71 offset:13312
v_mfma_f32_16x16x32_bf16 v[48:51],a[52:55],v[80:83],v[48:51]
v_mfma_f32_16x16x32_bf16 v[52:55],v[80:83],a[56:59],v[52:55]
v_mfma_f32_16x16x32_bf16 v[20:23],a[60:63],v[16:19],v[20:23]
s_add_u32 s96,s96,196608
s_add_u32 m0,s97,102400
s_nop 0
buffer_load_dwordx4 v67,s[36:39],s96 offen offset:0 lds
ds_read_b64_tr_b16 a[90:91],v71 offset:13824
ds_read_b64_tr_b16 a[92:93],v75 offset:14336
v_mfma_f32_16x16x32_bf16 v[24:27],a[64:67],v[16:19],v[24:27]
v_mfma_f32_16x16x32_bf16 v[32:35],v[16:19],a[68:71],v[32:35]
buffer_load_dwordx4 v67,s[40:43],s96 offen offset:1024 lds
ds_read_b64_tr_b16 a[94:95],v75 offset:14848
ds_read_b128 v[92:95],v58 offset:12288
v_mfma_f32_16x16x32_bf16 v[36:39],a[64:67],v[84:87],v[36:39]
v_mfma_f32_16x16x32_bf16 v[40:43],v[84:87],a[68:71],v[40:43]
buffer_load_dwordx4 v67,s[44:47],s96 offen offset:2048 lds
ds_read_b128 v[96:99],v58 offset:14336
ds_read_b128 v[100:103],v59 offset:12288
ds_read_b128 v[104:107],v59 offset:14336
v_mfma_f32_16x16x32_bf16 v[44:47],a[60:63],v[88:91],v[44:47]
v_mfma_f32_16x16x32_bf16 v[48:51],a[64:67],v[88:91],v[48:51]
v_mfma_f32_16x16x32_bf16 v[52:55],v[88:91],a[68:71],v[52:55]
s_add_u32 s96,s96,196608
s_waitcnt lgkmcnt(0)
s_waitcnt vmcnt(14)
s_barrier
s_add_u32 m0,s57,52352
s_nop 0
buffer_load_dwordx4 v57,s[4:7],s56 offen offset:896 lds
ds_read_b64_tr_b16 a[96:97],v68 offset:0
ds_read_b64_tr_b16 a[98:99],v68 offset:512
v_mfma_f32_16x16x32_bf16 v[20:23],a[72:75],v[16:19],v[20:23]
v_mfma_f32_16x16x32_bf16 v[24:27],a[76:79],v[16:19],v[24:27]
s_add_u32 m0,s97,114688
s_nop 0
buffer_load_dwordx4 v67,s[36:39],s96 offen offset:0 lds
ds_read_b64_tr_b16 a[100:101],v68 offset:1024
ds_read_b64_tr_b16 a[102:103],v68 offset:1536
v_mfma_f32_16x16x32_bf16 v[32:35],v[16:19],a[80:83],v[32:35]
v_mfma_f32_16x16x32_bf16 v[36:39],a[76:79],v[92:95],v[36:39]
buffer_load_dwordx4 v67,s[40:43],s96 offen offset:1024 lds
ds_read_b64_tr_b16 a[104:105],v72 offset:2048
ds_read_b64_tr_b16 a[106:107],v72 offset:2560
v_mfma_f32_16x16x32_bf16 v[40:43],v[92:95],a[80:83],v[40:43]
v_mfma_f32_16x16x32_bf16 v[44:47],a[72:75],v[96:99],v[44:47]
buffer_load_dwordx4 v67,s[44:47],s96 offen offset:2048 lds
ds_read_b64_tr_b16 a[108:109],v68 offset:12288
ds_read_b64_tr_b16 a[110:111],v68 offset:12800
ds_read_b64_tr_b16 a[112:113],v68 offset:13312
v_mfma_f32_16x16x32_bf16 v[48:51],a[76:79],v[96:99],v[48:51]
v_mfma_f32_16x16x32_bf16 v[52:55],v[96:99],a[80:83],v[52:55]
v_mfma_f32_16x16x32_bf16 v[20:23],a[84:87],v[16:19],v[20:23]
s_add_u32 s96,s96,196608
s_add_u32 m0,s97,126976
s_nop 0
buffer_load_dwordx4 v67,s[36:39],s96 offen offset:0 lds
ds_read_b64_tr_b16 a[114:115],v68 offset:13824
ds_read_b64_tr_b16 a[116:117],v72 offset:14336
v_mfma_f32_16x16x32_bf16 v[24:27],a[88:91],v[16:19],v[24:27]
v_mfma_f32_16x16x32_bf16 v[32:35],v[16:19],a[92:95],v[32:35]
buffer_load_dwordx4 v67,s[40:43],s96 offen offset:1024 lds
ds_read_b64_tr_b16 a[118:119],v72 offset:14848
ds_read_b128 v[76:79],v58 offset:16384
v_mfma_f32_16x16x32_bf16 v[36:39],a[88:91],v[100:103],v[36:39]
v_mfma_f32_16x16x32_bf16 v[40:43],v[100:103],a[92:95],v[40:43]
buffer_load_dwordx4 v67,s[44:47],s96 offen offset:2048 lds
ds_read_b128 v[80:83],v58 offset:18432
ds_read_b128 v[84:87],v59 offset:16384
ds_read_b128 v[88:91],v59 offset:18432
v_mfma_f32_16x16x32_bf16 v[44:47],a[84:87],v[104:107],v[44:47]
v_mfma_f32_16x16x32_bf16 v[48:51],a[88:91],v[104:107],v[48:51]
v_mfma_f32_16x16x32_bf16 v[52:55],v[104:107],a[92:95],v[52:55]
s_add_u32 s96,s96,196608
s_waitcnt lgkmcnt(0)
s_waitcnt vmcnt(14)
s_barrier
s_add_u32 m0,s57,56320
s_nop 0
buffer_load_dwordx4 v57,s[4:7],s56 offen offset:1024 lds
ds_read_b64_tr_b16 a[120:121],v69 offset:0
ds_read_b64_tr_b16 a[122:123],v69 offset:512
v_mfma_f32_16x16x32_bf16 v[20:23],a[96:99],v[16:19],v[20:23]
v_mfma_f32_16x16x32_bf16 v[24:27],a[100:103],v[16:19],v[24:27]
s_add_u32 m0,s97,0
s_nop 0
buffer_load_dwordx4 v67,s[36:39],s96 offen offset:0 lds
ds_read_b64_tr_b16 a[124:125],v69 offset:1024
ds_read_b64_tr_b16 a[126:127],v69 offset:1536
v_mfma_f32_16x16x32_bf16 v[32:35],v[16:19],a[104:107],v[32:35]
v_mfma_f32_16x16x32_bf16 v[36:39],a[100:103],v[76:79],v[36:39]
buffer_load_dwordx4 v67,s[40:43],s96 offen offset:1024 lds
ds_read_b64_tr_b16 a[128:129],v73 offset:2048
ds_read_b64_tr_b16 a[130:131],v73 offset:2560
v_mfma_f32_16x16x32_bf16 v[40:43],v[76:79],a[104:107],v[40:43]
v_mfma_f32_16x16x32_bf16 v[44:47],a[96:99],v[80:83],v[44:47]
buffer_load_dwordx4 v67,s[44:47],s96 offen offset:2048 lds
ds_read_b64_tr_b16 a[132:133],v69 offset:12288
ds_read_b64_tr_b16 a[134:135],v69 offset:12800
ds_read_b64_tr_b16 a[136:137],v69 offset:13312
v_mfma_f32_16x16x32_bf16 v[48:51],a[100:103],v[80:83],v[48:51]
v_mfma_f32_16x16x32_bf16 v[52:55],v[80:83],a[104:107],v[52:55]
v_mfma_f32_16x16x32_bf16 v[20:23],a[108:111],v[16:19],v[20:23]
s_add_u32 s96,s96,196608
s_add_u32 m0,s97,12288
s_nop 0
buffer_load_dwordx4 v67,s[36:39],s96 offen offset:0 lds
ds_read_b64_tr_b16 a[138:139],v69 offset:13824
ds_read_b64_tr_b16 a[140:141],v73 offset:14336
v_mfma_f32_16x16x32_bf16 v[24:27],a[112:115],v[16:19],v[24:27]
v_mfma_f32_16x16x32_bf16 v[32:35],v[16:19],a[116:119],v[32:35]
buffer_load_dwordx4 v67,s[40:43],s96 offen offset:1024 lds
ds_read_b64_tr_b16 a[142:143],v73 offset:14848
ds_read_b128 v[92:95],v58 offset:20480
v_mfma_f32_16x16x32_bf16 v[36:39],a[112:115],v[84:87],v[36:39]
v_mfma_f32_16x16x32_bf16 v[40:43],v[84:87],a[116:119],v[40:43]
buffer_load_dwordx4 v67,s[44:47],s96 offen offset:2048 lds
ds_read_b128 v[96:99],v58 offset:22528
ds_read_b128 v[100:103],v59 offset:20480
ds_read_b128 v[104:107],v59 offset:22528
v_mfma_f32_16x16x32_bf16 v[44:47],a[108:111],v[88:91],v[44:47]
v_mfma_f32_16x16x32_bf16 v[48:51],a[112:115],v[88:91],v[48:51]
v_mfma_f32_16x16x32_bf16 v[52:55],v[88:91],a[116:119],v[52:55]
s_add_u32 s96,s96,196608
s_waitcnt lgkmcnt(0)
s_waitcnt vmcnt(14)
s_barrier
s_add_u32 m0,s57,60288
s_nop 0
buffer_load_dwordx4 v57,s[4:7],s56 offen offset:1152 lds
ds_read_b64_tr_b16 a[144:145],v70 offset:0
ds_read_b64_tr_b16 a[146:147],v70 offset:512
v_mfma_f32_16x16x32_bf16 v[20:23],a[120:123],v[16:19],v[20:23]
v_mfma_f32_16x16x32_bf16 v[24:27],a[124:127],v[16:19],v[24:27]
s_add_u32 m0,s97,139264
s_nop 0
buffer_load_dwordx4 v67,s[36:39],s96 offen offset:0 lds
ds_read_b64_tr_b16 a[148:149],v70 offset:1024
ds_read_b64_tr_b16 a[150:151],v70 offset:1536
v_mfma_f32_16x16x32_bf16 v[32:35],v[16:19],a[128:131],v[32:35]
v_mfma_f32_16x16x32_bf16 v[36:39],a[124:127],v[92:95],v[36:39]
buffer_load_dwordx4 v67,s[40:43],s96 offen offset:1024 lds
ds_read_b64_tr_b16 a[152:153],v74 offset:2048
ds_read_b64_tr_b16 a[154:155],v74 offset:2560
v_mfma_f32_16x16x32_bf16 v[40:43],v[92:95],a[128:131],v[40:43]
v_mfma_f32_16x16x32_bf16 v[44:47],a[120:123],v[96:99],v[44:47]
buffer_load_dwordx4 v67,s[44:47],s96 offen offset:2048 lds
ds_read_b64_tr_b16 a[156:157],v70 offset:12288
ds_read_b64_tr_b16 a[158:159],v70 offset:12800
ds_read_b64_tr_b16 a[160:161],v70 offset:13312
v_mfma_f32_16x16x32_bf16 v[48:51],a[124:127],v[96:99],v[48:51]
v_mfma_f32_16x16x32_bf16 v[52:55],v[96:99],a[128:131],v[52:55]
v_mfma_f32_16x16x32_bf16 v[20:23],a[132:135],v[16:19],v[20:23]
s_add_u32 s96,s96,196608
s_add_u32 m0,s97,151552
s_nop 0
buffer_load_dwordx4 v67,s[36:39],s96 offen offset:0 lds
ds_read_b64_tr_b16 a[162:163],v70 offset:13824
ds_read_b64_tr_b16 a[164:165],v74 offset:14336
v_mfma_f32_16x16x32_bf16 v[24:27],a[136:139],v[16:19],v[24:27]
v_mfma_f32_16x16x32_bf16 v[32:35],v[16:19],a[140:143],v[32:35]
buffer_load_dwordx4 v67,s[40:43],s96 offen offset:1024 lds
ds_read_b64_tr_b16 a[166:167],v74 offset:14848
ds_read_b128 v[76:79],v58 offset:24576
v_mfma_f32_16x16x32_bf16 v[36:39],a[136:139],v[100:103],v[36:39]
v_mfma_f32_16x16x32_bf16 v[40:43],v[100:103],a[140:143],v[40:43]
buffer_load_dwordx4 v67,s[44:47],s96 offen offset:2048 lds
ds_read_b128 v[80:83],v58 offset:26624
ds_read_b128 v[84:87],v59 offset:24576
ds_read_b128 v[88:91],v59 offset:26624
v_mfma_f32_16x16x32_bf16 v[44:47],a[132:135],v[104:107],v[44:47]
v_mfma_f32_16x16x32_bf16 v[48:51],a[136:139],v[104:107],v[48:51]
v_mfma_f32_16x16x32_bf16 v[52:55],v[104:107],a[140:143],v[52:55]
s_add_u32 s96,s96,196608
s_waitcnt lgkmcnt(0)
s_waitcnt vmcnt(14)
s_barrier
s_add_u32 m0,s57,64256
s_nop 0
buffer_load_dwordx4 v57,s[4:7],s56 offen offset:1280 lds
ds_read_b64_tr_b16 a[168:169],v71 offset:0
ds_read_b64_tr_b16 a[170:171],v71 offset:512
v_mfma_f32_16x16x32_bf16 v[20:23],a[144:147],v[16:19],v[20:23]
v_mfma_f32_16x16x32_bf16 v[24:27],a[148:151],v[16:19],v[24:27]
s_add_u32 m0,s97,90112
s_nop 0
buffer_load_dwordx4 v67,s[36:39],s96 offen offset:0 lds
ds_read_b64_tr_b16 a[172:173],v71 offset:1024
ds_read_b64_tr_b16 a[174:175],v71 offset:1536
v_mfma_f32_16x16x32_bf16 v[32:35],v[16:19],a[152:155],v[32:35]
v_mfma_f32_16x16x32_bf16 v[36:39],a[148:151],v[76:79],v[36:39]
buffer_load_dwordx4 v67,s[40:43],s96 offen offset:1024 lds
ds_read_b64_tr_b16 a[176:177],v75 offset:2048
ds_read_b64_tr_b16 a[178:179],v75 offset:2560
v_mfma_f32_16x16x32_bf16 v[40:43],v[76:79],a[152:155],v[40:43]
v_mfma_f32_16x16x32_bf16 v[44:47],a[144:147],v[80:83],v[44:47]
buffer_load_dwordx4 v67,s[44:47],s96 offen offset:2048 lds
ds_read_b64_tr_b16 a[180:181],v71 offset:12288
ds_read_b64_tr_b16 a[182:183],v71 offset:12800
v_mfma_f32_16x16x32_bf16 v[48:51],a[148:151],v[80:83],v[48:51]
v_mfma_f32_16x16x32_bf16 v[52:55],v[80:83],a[152:155],v[52:55]
s_add_u32 s96,s96,196608
s_add_u32 m0,s97,102400
s_nop 0
buffer_load_dwordx4 v67,s[36:39],s96 offen offset:0 lds
ds_read_b64_tr_b16 a[184:185],v71 offset:13312
ds_read_b64_tr_b16 a[186:187],v71 offset:13824
v_mfma_f32_16x16x32_bf16 v[20:23],a[156:159],v[16:19],v[20:23]
v_mfma_f32_16x16x32_bf16 v[24:27],a[160:163],v[16:19],v[24:27]
buffer_load_dwordx4 v67,s[40:43],s96 offen offset:1024 lds
ds_read_b64_tr_b16 a[188:189],v75 offset:14336
ds_read_b64_tr_b16 a[190:191],v75 offset:14848
v_mfma_f32_16x16x32_bf16 v[32:35],v[16:19],a[164:167],v[32:35]
v_mfma_f32_16x16x32_bf16 v[36:39],a[160:163],v[84:87],v[36:39]
buffer_load_dwordx4 v67,s[44:47],s96 offen offset:2048 lds
ds_read_b128 v[92:95],v58 offset:28672
ds_read_b128 v[96:99],v58 offset:30720
v_mfma_f32_16x16x32_bf16 v[40:43],v[84:87],a[164:167],v[40:43]
v_mfma_f32_16x16x32_bf16 v[44:47],a[156:159],v[88:91],v[44:47]
s_add_u32 s96,s96,196608
buffer_load_dwordx2 v[60:61],v56,s[0:3],s66 offen sc1
ds_read_b128 v[100:103],v59 offset:28672
ds_read_b128 v[104:107],v59 offset:30720
v_mfma_f32_16x16x32_bf16 v[48:51],a[160:163],v[88:91],v[48:51]
v_mfma_f32_16x16x32_bf16 v[52:55],v[88:91],a[164:167],v[52:55]
s_waitcnt lgkmcnt(0)
s_waitcnt vmcnt(15)
s_barrier
s_add_u32 m0,s57,68224
s_nop 0
buffer_load_dwordx4 v57,s[4:7],s56 offen offset:1408 lds
ds_read_b64_tr_b16 a[192:193],v68 offset:0
ds_read_b64_tr_b16 a[194:195],v68 offset:512
v_mfma_f32_16x16x32_bf16 v[20:23],a[168:171],v[16:19],v[20:23]
v_mfma_f32_16x16x32_bf16 v[24:27],a[172:175],v[16:19],v[24:27]
s_add_u32 m0,s97,114688
s_nop 0
buffer_load_dwordx4 v67,s[36:39],s96 offen offset:0 lds
ds_read_b64_tr_b16 a[196:197],v68 offset:1024
ds_read_b64_tr_b16 a[198:199],v68 offset:1536
v_mfma_f32_16x16x32_bf16 v[32:35],v[16:19],a[176:179],v[32:35]
v_mfma_f32_16x16x32_bf16 v[36:39],a[172:175],v[92:95],v[36:39]
buffer_load_dwordx4 v67,s[40:43],s96 offen offset:1024 lds
ds_read_b64_tr_b16 a[200:201],v72 offset:2048
ds_read_b64_tr_b16 a[202:203],v72 offset:2560
v_mfma_f32_16x16x32_bf16 v[40:43],v[92:95],a[176:179],v[40:43]
v_mfma_f32_16x16x32_bf16 v[44:47],a[168:171],v[96:99],v[44:47]
buffer_load_dwordx4 v67,s[44:47],s96 offen offset:2048 lds
ds_read_b64_tr_b16 a[204:205],v68 offset:12288
ds_read_b64_tr_b16 a[206:207],v68 offset:12800
ds_read_b64_tr_b16 a[208:209],v68 offset:13312
v_mfma_f32_16x16x32_bf16 v[48:51],a[172:175],v[96:99],v[48:51]
v_mfma_f32_16x16x32_bf16 v[52:55],v[96:99],a[176:179],v[52:55]
v_mfma_f32_16x16x32_bf16 v[20:23],a[180:183],v[16:19],v[20:23]
s_add_u32 s96,s96,196608
s_add_u32 m0,s97,126976
s_nop 0
buffer_load_dwordx4 v67,s[36:39],s96 offen offset:0 lds
ds_read_b64_tr_b16 a[210:211],v68 offset:13824
ds_read_b64_tr_b16 a[212:213],v72 offset:14336
v_mfma_f32_16x16x32_bf16 v[24:27],a[184:187],v[16:19],v[24:27]
v_mfma_f32_16x16x32_bf16 v[32:35],v[16:19],a[188:191],v[32:35]
buffer_load_dwordx4 v67,s[40:43],s96 offen offset:1024 lds
ds_read_b64_tr_b16 a[214:215],v72 offset:14848
ds_read_b128 v[76:79],v58 offset:32768
v_mfma_f32_16x16x32_bf16 v[36:39],a[184:187],v[100:103],v[36:39]
v_mfma_f32_16x16x32_bf16 v[40:43],v[100:103],a[188:191],v[40:43]
buffer_load_dwordx4 v67,s[44:47],s96 offen offset:2048 lds
ds_read_b128 v[80:83],v58 offset:34816
ds_read_b128 v[84:87],v59 offset:32768
ds_read_b128 v[88:91],v59 offset:34816
v_mfma_f32_16x16x32_bf16 v[44:47],a[180:183],v[104:107],v[44:47]
v_mfma_f32_16x16x32_bf16 v[48:51],a[184:187],v[104:107],v[48:51]
v_mfma_f32_16x16x32_bf16 v[52:55],v[104:107],a[188:191],v[52:55]
s_add_u32 s96,s96,196608
s_waitcnt lgkmcnt(0)
s_waitcnt vmcnt(15)
s_barrier
s_add_u32 m0,s57,72192
s_nop 0
buffer_load_dwordx4 v57,s[4:7],s56 offen offset:1536 lds
ds_read_b64_tr_b16 a[216:217],v69 offset:0
ds_read_b64_tr_b16 a[218:219],v69 offset:512
v_mfma_f32_16x16x32_bf16 v[20:23],a[192:195],v[16:19],v[20:23]
v_mfma_f32_16x16x32_bf16 v[24:27],a[196:199],v[16:19],v[24:27]
s_add_u32 m0,s97,0
s_nop 0
buffer_load_dwordx4 v67,s[36:39],s96 offen offset:0 lds
ds_read_b64_tr_b16 a[220:221],v69 offset:1024
ds_read_b64_tr_b16 a[222:223],v69 offset:1536
v_mfma_f32_16x16x32_bf16 v[32:35],v[16:19],a[200:203],v[32:35]
v_mfma_f32_16x16x32_bf16 v[36:39],a[196:199],v[76:79],v[36:39]
buffer_load_dwordx4 v67,s[40:43],s96 offen offset:1024 lds
ds_read_b64_tr_b16 a[224:225],v73 offset:2048
ds_read_b64_tr_b16 a[226:227],v73 offset:2560
v_mfma_f32_16x16x32_bf16 v[40:43],v[76:79],a[200:203],v[40:43]
v_mfma_f32_16x16x32_bf16 v[44:47],a[192:195],v[80:83],v[44:47]
buffer_load_dwordx4 v67,s[44:47],s96 offen offset:2048 lds
ds_read_b64_tr_b16 a[228:229],v69 offset:12288
ds_read_b64_tr_b16 a[230:231],v69 offset:12800
ds_read_b64_tr_b16 a[232:233],v69 offset:13312
v_mfma_f32_16x16x32_bf16 v[48:51],a[196:199],v[80:83],v[48:51]
v_mfma_f32_16x16x32_bf16 v[52:55],v[80:83],a[200:203],v[52:55]
v_mfma_f32_16x16x32_bf16 v[20:23],a[204:207],v[16:19],v[20:23]
s_add_u32 s96,s96,196608
s_add_u32 m0,s97,12288
s_nop 0
buffer_load_dwordx4 v67,s[36:39],s96 offen offset:0 lds
ds_read_b64_tr_b16 a[234:235],v69 offset:13824
ds_read_b64_tr_b16 a[236:237],v73 offset:14336
v_mfma_f32_16x16x32_bf16 v[24:27],a[208:211],v[16:19],v[24:27]
v_mfma_f32_16x16x32_bf16 v[32:35],v[16:19],a[212:215],v[32:35]
buffer_load_dwordx4 v67,s[40:43],s96 offen offset:1024 lds
ds_read_b64_tr_b16 a[238:239],v73 offset:14848
ds_read_b128 v[92:95],v58 offset:36864
v_mfma_f32_16x16x32_bf16 v[36:39],a[208:211],v[84:87],v[36:39]
v_mfma_f32_16x16x32_bf16 v[40:43],v[84:87],a[212:215],v[40:43]
buffer_load_dwordx4 v67,s[44:47],s96 offen offset:2048 lds
ds_read_b128 v[96:99],v58 offset:38912
ds_read_b128 v[100:103],v59 offset:36864
ds_read_b128 v[104:107],v59 offset:38912
v_mfma_f32_16x16x32_bf16 v[44:47],a[204:207],v[88:91],v[44:47]
v_mfma_f32_16x16x32_bf16 v[48:51],a[208:211],v[88:91],v[48:51]
v_mfma_f32_16x16x32_bf16 v[52:55],v[88:91],a[212:215],v[52:55]
s_add_u32 s96,s96,196608
s_waitcnt lgkmcnt(0)
s_waitcnt vmcnt(15)
s_barrier
s_add_u32 m0,s57,76160
s_nop 0
buffer_load_dwordx4 v57,s[4:7],s56 offen offset:1664 lds
ds_read_b64_tr_b16 a[240:241],v70 offset:0
ds_read_b64_tr_b16 a[242:243],v70 offset:512
v_mfma_f32_16x16x32_bf16 v[20:23],a[216:219],v[16:19],v[20:23]
v_mfma_f32_16x16x32_bf16 v[24:27],a[220:223],v[16:19],v[24:27]
s_add_u32 m0,s97,139264
s_nop 0
buffer_load_dwordx4 v67,s[36:39],s96 offen offset:0 lds
ds_read_b64_tr_b16 a[244:245],v70 offset:1024
ds_read_b64_tr_b16 a[246:247],v70 offset:1536
v_mfma_f32_16x16x32_bf16 v[32:35],v[16:19],a[224:227],v[32:35]
v_mfma_f32_16x16x32_bf16 v[36:39],a[220:223],v[92:95],v[36:39]
buffer_load_dwordx4 v67,s[40:43],s96 offen offset:1024 lds
ds_read_b64_tr_b16 a[248:249],v74 offset:2048
ds_read_b64_tr_b16 a[250:251],v74 offset:2560
v_mfma_f32_16x16x32_bf16 v[40:43],v[92:95],a[224:227],v[40:43]
)#"
R"#(
v_mfma_f32_16x16x32_bf16 v[44:47],a[216:219],v[96:99],v[44:47]
buffer_load_dwordx4 v67,s[44:47],s96 offen offset:2048 lds
ds_read_b64_tr_b16 a[252:253],v70 offset:12288
ds_read_b64_tr_b16 a[254:255],v70 offset:12800
ds_read_b64_tr_b16 v[128:129],v70 offset:13312
v_mfma_f32_16x16x32_bf16 v[48:51],a[220:223],v[96:99],v[48:51]
v_mfma_f32_16x16x32_bf16 v[52:55],v[96:99],a[224:227],v[52:55]
v_mfma_f32_16x16x32_bf16 v[20:23],a[228:231],v[16:19],v[20:23]
s_add_u32 s96,s96,196608
s_add_u32 m0,s97,151552
s_nop 0
buffer_load_dwordx4 v67,s[36:39],s96 offen offset:0 lds
ds_read_b64_tr_b16 v[130:131],v70 offset:13824
ds_read_b64_tr_b16 v[132:133],v74 offset:14336
v_mfma_f32_16x16x32_bf16 v[24:27],a[232:235],v[16:19],v[24:27]
v_mfma_f32_16x16x32_bf16 v[32:35],v[16:19],a[236:239],v[32:35]
buffer_load_dwordx4 v67,s[40:43],s96 offen offset:1024 lds
ds_read_b64_tr_b16 v[134:135],v74 offset:14848
ds_read_b128 v[76:79],v58 offset:40960
v_mfma_f32_16x16x32_bf16 v[36:39],a[232:235],v[100:103],v[36:39]
v_mfma_f32_16x16x32_bf16 v[40:43],v[100:103],a[236:239],v[40:43]
buffer_load_dwordx4 v67,s[44:47],s96 offen offset:2048 lds
ds_read_b128 v[80:83],v58 offset:43008
ds_read_b128 v[84:87],v59 offset:40960
ds_read_b128 v[88:91],v59 offset:43008
v_mfma_f32_16x16x32_bf16 v[44:47],a[228:231],v[104:107],v[44:47]
v_mfma_f32_16x16x32_bf16 v[48:51],a[232:235],v[104:107],v[48:51]
v_mfma_f32_16x16x32_bf16 v[52:55],v[104:107],a[236:239],v[52:55]
s_add_u32 s96,s96,196608
s_waitcnt lgkmcnt(0)
s_waitcnt vmcnt(14)
s_barrier
s_add_u32 m0,s57,80128
s_nop 0
buffer_load_dwordx4 v57,s[4:7],s56 offen offset:1792 lds
ds_read_b64_tr_b16 v[136:137],v71 offset:0
ds_read_b64_tr_b16 v[138:139],v71 offset:512
v_mfma_f32_16x16x32_bf16 v[20:23],a[240:243],v[16:19],v[20:23]
v_mfma_f32_16x16x32_bf16 v[24:27],a[244:247],v[16:19],v[24:27]
s_add_u32 m0,s97,90112
s_nop 0
buffer_load_dwordx4 v67,s[36:39],s96 offen offset:0 lds
ds_read_b64_tr_b16 v[140:141],v71 offset:1024
ds_read_b64_tr_b16 v[142:143],v71 offset:1536
v_mfma_f32_16x16x32_bf16 v[32:35],v[16:19],a[248:251],v[32:35]
v_mfma_f32_16x16x32_bf16 v[36:39],a[244:247],v[76:79],v[36:39]
buffer_load_dwordx4 v67,s[40:43],s96 offen offset:1024 lds
ds_read_b64_tr_b16 v[144:145],v75 offset:2048
ds_read_b64_tr_b16 v[146:147],v75 offset:2560
v_mfma_f32_16x16x32_bf16 v[40:43],v[76:79],a[248:251],v[40:43]
v_mfma_f32_16x16x32_bf16 v[44:47],a[240:243],v[80:83],v[44:47]
buffer_load_dwordx4 v67,s[44:47],s96 offen offset:2048 lds
ds_read_b64_tr_b16 v[148:149],v71 offset:12288
ds_read_b64_tr_b16 v[150:151],v71 offset:12800
ds_read_b64_tr_b16 v[152:153],v71 offset:13312
v_mfma_f32_16x16x32_bf16 v[48:51],a[244:247],v[80:83],v[48:51]
v_mfma_f32_16x16x32_bf16 v[52:55],v[80:83],a[248:251],v[52:55]
v_mfma_f32_16x16x32_bf16 v[20:23],a[252:255],v[16:19],v[20:23]
s_add_u32 s96,s96,196608
s_add_u32 m0,s97,102400
s_nop 0
buffer_load_dwordx4 v67,s[36:39],s96 offen offset:0 lds
ds_read_b64_tr_b16 v[154:155],v71 offset:13824
ds_read_b64_tr_b16 v[156:157],v75 offset:14336
v_mfma_f32_16x16x32_bf16 v[24:27],v[128:131],v[16:19],v[24:27]
v_mfma_f32_16x16x32_bf16 v[32:35],v[16:19],v[132:135],v[32:35]
buffer_load_dwordx4 v67,s[40:43],s96 offen offset:1024 lds
ds_read_b64_tr_b16 v[158:159],v75 offset:14848
ds_read_b128 v[92:95],v58 offset:45056
v_mfma_f32_16x16x32_bf16 v[36:39],v[128:131],v[84:87],v[36:39]
v_mfma_f32_16x16x32_bf16 v[40:43],v[84:87],v[132:135],v[40:43]
buffer_load_dwordx4 v67,s[44:47],s96 offen offset:2048 lds
ds_read_b128 v[96:99],v58 offset:47104
ds_read_b128 v[100:103],v59 offset:45056
ds_read_b128 v[104:107],v59 offset:47104
v_mfma_f32_16x16x32_bf16 v[44:47],a[252:255],v[88:91],v[44:47]
v_mfma_f32_16x16x32_bf16 v[48:51],v[128:131],v[88:91],v[48:51]
v_mfma_f32_16x16x32_bf16 v[52:55],v[88:91],v[132:135],v[52:55]
s_add_u32 s96,s96,196608
s_waitcnt lgkmcnt(0)
s_waitcnt vmcnt(14)
s_barrier
s_add_u32 m0,s57,84096
s_nop 0
buffer_load_dwordx4 v57,s[4:7],s56 offen offset:1920 lds
ds_read_b64_tr_b16 v[160:161],v68 offset:0
v_mfma_f32_16x16x32_bf16 v[20:23],v[136:139],v[16:19],v[20:23]
s_add_u32 m0,s97,114688
s_nop 0
buffer_load_dwordx4 v67,s[36:39],s96 offen offset:0 lds
ds_read_b64_tr_b16 v[162:163],v68 offset:512
v_mfma_f32_16x16x32_bf16 v[24:27],v[140:143],v[16:19],v[24:27]
buffer_load_dwordx4 v67,s[40:43],s96 offen offset:1024 lds
ds_read_b64_tr_b16 v[164:165],v68 offset:1024
v_mfma_f32_16x16x32_bf16 v[32:35],v[16:19],v[144:147],v[32:35]
buffer_load_dwordx4 v67,s[44:47],s96 offen offset:2048 lds
ds_read_b64_tr_b16 v[166:167],v68 offset:1536
v_mfma_f32_16x16x32_bf16 v[36:39],v[140:143],v[92:95],v[36:39]
s_add_u32 s96,s96,196608
s_add_u32 m0,s97,126976
s_nop 0
buffer_load_dwordx4 v67,s[36:39],s96 offen offset:0 lds
ds_read_b64_tr_b16 v[168:169],v72 offset:2048
v_mfma_f32_16x16x32_bf16 v[40:43],v[92:95],v[144:147],v[40:43]
buffer_load_dwordx4 v67,s[40:43],s96 offen offset:1024 lds
ds_read_b64_tr_b16 v[170:171],v72 offset:2560
v_mfma_f32_16x16x32_bf16 v[44:47],v[136:139],v[96:99],v[44:47]
buffer_load_dwordx4 v67,s[44:47],s96 offen offset:2048 lds
ds_read_b64_tr_b16 v[172:173],v68 offset:12288
v_mfma_f32_16x16x32_bf16 v[48:51],v[140:143],v[96:99],v[48:51]
s_add_u32 s96,s96,196608
v_cmp_ne_u64 vcc,s[54:55],v[60:61]
s_cbranch_vccnz .L_rtqe
.L_bkqe:
buffer_load_dword v108,v6,s[32:35],s74 offen sc1
ds_read_b64_tr_b16 v[174:175],v68 offset:12800
v_mfma_f32_16x16x32_bf16 v[52:55],v[96:99],v[144:147],v[52:55]
s_add_u32 s74,s74,9216
buffer_load_dword v109,v6,s[32:35],s74 offen sc1
ds_read_b64_tr_b16 v[176:177],v68 offset:13312
v_mfma_f32_16x16x32_bf16 v[20:23],v[148:151],v[16:19],v[20:23]
s_add_u32 s74,s74,9216
buffer_load_dword v110,v6,s[32:35],s74 offen sc1
ds_read_b64_tr_b16 v[178:179],v68 offset:13824
v_mfma_f32_16x16x32_bf16 v[24:27],v[152:155],v[16:19],v[24:27]
s_add_u32 s74,s74,9216
buffer_load_dword v111,v6,s[32:35],s74 offen sc1
ds_read_b64_tr_b16 v[180:181],v72 offset:14336
v_mfma_f32_16x16x32_bf16 v[32:35],v[16:19],v[156:159],v[32:35]
s_sub_u32 s74,s66,286720
buffer_load_dword v112,v6,s[32:35],s74 offen sc1
ds_read_b64_tr_b16 v[182:183],v72 offset:14848
v_mfma_f32_16x16x32_bf16 v[36:39],v[152:155],v[100:103],v[36:39]
s_add_u32 s74,s74,9216
buffer_load_dword v113,v6,s[32:35],s74 offen sc1
ds_read_b128 v[76:79],v58 offset:49152
v_mfma_f32_16x16x32_bf16 v[40:43],v[100:103],v[156:159],v[40:43]
s_add_u32 s74,s74,9216
buffer_load_dword v114,v6,s[32:35],s74 offen sc1
ds_read_b128 v[80:83],v58 offset:51200
v_mfma_f32_16x16x32_bf16 v[44:47],v[148:151],v[104:107],v[44:47]
s_add_u32 s74,s74,9216
buffer_load_dword v115,v6,s[32:35],s74 offen sc1
ds_read_b128 v[84:87],v59 offset:49152
v_mfma_f32_16x16x32_bf16 v[48:51],v[152:155],v[104:107],v[48:51]
s_add_u32 s74,s74,9216
s_add_u32 s66,s66,1187840
s_add_u32 s63,s66,s71
s_add_u32 s66,s66,2375680
buffer_load_dwordx2 v[12:13],v0,s[0:3],s66 offen sc1
ds_read_b128 v[88:91],v59 offset:51200
v_mfma_f32_16x16x32_bf16 v[52:55],v[104:107],v[156:159],v[52:55]
s_waitcnt lgkmcnt(0)
s_waitcnt vmcnt(23)
s_barrier
s_add_u32 m0,s64,0
s_nop 0
buffer_load_dwordx4 v2,s[32:35],s63 offen offset:0 sc1 lds
ds_read_b64_tr_b16 v[184:185],v69 offset:0
v_mfma_f32_16x16x32_bf16 v[20:23],v[160:163],v[16:19],v[20:23]
buffer_load_dwordx4 v2,s[32:35],s63 offen offset:1024 sc1 lds
ds_read_b64_tr_b16 v[186:187],v69 offset:512
ds_read_b64_tr_b16 v[188:189],v69 offset:1024
v_mfma_f32_16x16x32_bf16 v[24:27],v[164:167],v[16:19],v[24:27]
v_mfma_f32_16x16x32_bf16 v[32:35],v[16:19],v[168:171],v[32:35]
buffer_load_dwordx4 v2,s[32:35],s63 offen offset:2048 sc1 lds
ds_read_b64_tr_b16 v[190:191],v69 offset:1536
ds_read_b64_tr_b16 v[192:193],v73 offset:2048
v_mfma_f32_16x16x32_bf16 v[36:39],v[164:167],v[76:79],v[36:39]
v_mfma_f32_16x16x32_bf16 v[40:43],v[76:79],v[168:171],v[40:43]
buffer_load_dwordx4 v2,s[32:35],s63 offen offset:3072 sc1 lds
ds_read_b64_tr_b16 v[194:195],v73 offset:2560
ds_read_b64_tr_b16 v[196:197],v69 offset:12288
v_mfma_f32_16x16x32_bf16 v[44:47],v[160:163],v[80:83],v[44:47]
v_mfma_f32_16x16x32_bf16 v[48:51],v[164:167],v[80:83],v[48:51]
s_add_u32 s63,s63,4096
s_add_u32 m0,s64,4096
s_nop 0
buffer_load_dwordx4 v2,s[32:35],s63 offen offset:0 sc1 lds
ds_read_b64_tr_b16 v[198:199],v69 offset:12800
v_mfma_f32_16x16x32_bf16 v[52:55],v[80:83],v[168:171],v[52:55]
buffer_load_dwordx4 v2,s[32:35],s63 offen offset:1024 sc1 lds
ds_read_b64_tr_b16 v[200:201],v69 offset:13312
ds_read_b64_tr_b16 v[202:203],v69 offset:13824
v_mfma_f32_16x16x32_bf16 v[20:23],v[172:175],v[16:19],v[20:23]
v_mfma_f32_16x16x32_bf16 v[24:27],v[176:179],v[16:19],v[24:27]
buffer_load_dwordx4 v2,s[32:35],s63 offen offset:2048 sc1 lds
ds_read_b64_tr_b16 v[204:205],v73 offset:14336
ds_read_b64_tr_b16 v[206:207],v73 offset:14848
v_mfma_f32_16x16x32_bf16 v[32:35],v[16:19],v[180:183],v[32:35]
v_mfma_f32_16x16x32_bf16 v[36:39],v[176:179],v[84:87],v[36:39]
buffer_load_dwordx4 v2,s[32:35],s63 offen offset:3072 sc1 lds
ds_read_b128 v[92:95],v58 offset:53248
ds_read_b128 v[96:99],v58 offset:55296
v_mfma_f32_16x16x32_bf16 v[40:43],v[84:87],v[180:183],v[40:43]
v_mfma_f32_16x16x32_bf16 v[44:47],v[172:175],v[88:91],v[44:47]
s_add_u32 s63,s63,4096
s_add_u32 m0,s64,8192
s_nop 0
buffer_load_dwordx4 v2,s[32:35],s63 offen offset:0 sc1 lds
ds_read_b128 v[100:103],v59 offset:53248
ds_read_b128 v[104:107],v59 offset:55296
v_mfma_f32_16x16x32_bf16 v[48:51],v[176:179],v[88:91],v[48:51]
v_mfma_f32_16x16x32_bf16 v[52:55],v[88:91],v[180:183],v[52:55]
s_waitcnt lgkmcnt(0)
s_waitcnt vmcnt(25)
s_barrier
s_add_u32 s63,s63,1179648
s_add_u32 m0,s64,36864
s_nop 0
buffer_load_dwordx4 v2,s[32:35],s63 offen offset:0 sc1 lds
ds_read_b64_tr_b16 v[208:209],v70 offset:0
ds_read_b64_tr_b16 v[210:211],v70 offset:512
ds_read_b64_tr_b16 v[212:213],v70 offset:1024
ds_read_b64_tr_b16 v[214:215],v70 offset:1536
v_mfma_f32_16x16x32_bf16 v[20:23],v[184:187],v[16:19],v[20:23]
v_mfma_f32_16x16x32_bf16 v[24:27],v[188:191],v[16:19],v[24:27]
v_mfma_f32_16x16x32_bf16 v[32:35],v[16:19],v[192:195],v[32:35]
v_mfma_f32_16x16x32_bf16 v[36:39],v[188:191],v[92:95],v[36:39]
buffer_load_dwordx4 v2,s[32:35],s63 offen offset:1024 sc1 lds
ds_read_b64_tr_b16 v[216:217],v74 offset:2048
ds_read_b64_tr_b16 v[218:219],v74 offset:2560
ds_read_b64_tr_b16 v[220:221],v70 offset:12288
ds_read_b64_tr_b16 v[222:223],v70 offset:12800
v_mfma_f32_16x16x32_bf16 v[40:43],v[92:95],v[192:195],v[40:43]
v_mfma_f32_16x16x32_bf16 v[44:47],v[184:187],v[96:99],v[44:47]
v_mfma_f32_16x16x32_bf16 v[48:51],v[188:191],v[96:99],v[48:51]
v_mfma_f32_16x16x32_bf16 v[52:55],v[96:99],v[192:195],v[52:55]
buffer_load_dwordx4 v2,s[32:35],s63 offen offset:2048 sc1 lds
ds_read_b64_tr_b16 v[224:225],v70 offset:13312
ds_read_b64_tr_b16 v[226:227],v70 offset:13824
ds_read_b64_tr_b16 v[228:229],v74 offset:14336
ds_read_b64_tr_b16 v[230:231],v74 offset:14848
v_mfma_f32_16x16x32_bf16 v[20:23],v[196:199],v[16:19],v[20:23]
v_mfma_f32_16x16x32_bf16 v[24:27],v[200:203],v[16:19],v[24:27]
v_mfma_f32_16x16x32_bf16 v[32:35],v[16:19],v[204:207],v[32:35]
v_mfma_f32_16x16x32_bf16 v[36:39],v[200:203],v[100:103],v[36:39]
buffer_load_dwordx4 v2,s[32:35],s63 offen offset:3072 sc1 lds
ds_read_b128 v[76:79],v58 offset:57344
ds_read_b128 v[80:83],v58 offset:59392
ds_read_b128 v[84:87],v59 offset:57344
ds_read_b128 v[88:91],v59 offset:59392
v_mfma_f32_16x16x32_bf16 v[40:43],v[100:103],v[204:207],v[40:43]
v_mfma_f32_16x16x32_bf16 v[44:47],v[196:199],v[104:107],v[44:47]
v_mfma_f32_16x16x32_bf16 v[48:51],v[200:203],v[104:107],v[48:51]
v_mfma_f32_16x16x32_bf16 v[52:55],v[104:107],v[204:207],v[52:55]
s_waitcnt lgkmcnt(0)
s_waitcnt vmcnt(22)
s_barrier
s_add_u32 s63,s63,4096
s_add_u32 m0,s64,40960
s_nop 0
buffer_load_dwordx4 v2,s[32:35],s63 offen offset:0 sc1 lds
ds_read_b64_tr_b16 v[232:233],v71 offset:0
ds_read_b64_tr_b16 v[234:235],v71 offset:512
ds_read_b64_tr_b16 v[236:237],v71 offset:1024
v_mfma_f32_16x16x32_bf16 v[20:23],v[208:211],v[16:19],v[20:23]
v_mfma_f32_16x16x32_bf16 v[24:27],v[212:215],v[16:19],v[24:27]
v_mfma_f32_16x16x32_bf16 v[32:35],v[16:19],v[216:219],v[32:35]
buffer_load_dwordx4 v2,s[32:35],s63 offen offset:1024 sc1 lds
ds_read_b64_tr_b16 v[238:239],v71 offset:1536
ds_read_b64_tr_b16 v[240:241],v75 offset:2048
ds_read_b64_tr_b16 v[242:243],v75 offset:2560
v_mfma_f32_16x16x32_bf16 v[36:39],v[212:215],v[76:79],v[36:39]
v_mfma_f32_16x16x32_bf16 v[40:43],v[76:79],v[216:219],v[40:43]
v_mfma_f32_16x16x32_bf16 v[44:47],v[208:211],v[80:83],v[44:47]
buffer_load_dwordx4 v2,s[32:35],s63 offen offset:2048 sc1 lds
ds_read_b64_tr_b16 v[244:245],v71 offset:12288
ds_read_b64_tr_b16 v[246:247],v71 offset:12800
ds_read_b64_tr_b16 v[248:249],v71 offset:13312
v_mfma_f32_16x16x32_bf16 v[48:51],v[212:215],v[80:83],v[48:51]
v_mfma_f32_16x16x32_bf16 v[52:55],v[80:83],v[216:219],v[52:55]
v_mfma_f32_16x16x32_bf16 v[20:23],v[220:223],v[16:19],v[20:23]
buffer_load_dwordx4 v2,s[32:35],s63 offen offset:3072 sc1 lds
ds_read_b64_tr_b16 v[250:251],v71 offset:13824
ds_read_b64_tr_b16 v[252:253],v75 offset:14336
ds_read_b64_tr_b16 v[254:255],v75 offset:14848
v_mfma_f32_16x16x32_bf16 v[24:27],v[224:227],v[16:19],v[24:27]
v_mfma_f32_16x16x32_bf16 v[32:35],v[16:19],v[228:231],v[32:35]
v_mfma_f32_16x16x32_bf16 v[36:39],v[224:227],v[84:87],v[36:39]
s_add_u32 s63,s63,4096
s_add_u32 m0,s64,45056
s_nop 0
buffer_load_dwordx4 v2,s[32:35],s63 offen offset:0 sc1 lds
ds_read_b128 v[92:95],v58 offset:61440
ds_read_b128 v[96:99],v58 offset:63488
ds_read_b128 v[100:103],v59 offset:61440
ds_read_b128 v[104:107],v59 offset:63488
v_mfma_f32_16x16x32_bf16 v[40:43],v[84:87],v[228:231],v[40:43]
v_mfma_f32_16x16x32_bf16 v[44:47],v[220:223],v[88:91],v[44:47]
v_mfma_f32_16x16x32_bf16 v[48:51],v[224:227],v[88:91],v[48:51]
v_mfma_f32_16x16x32_bf16 v[52:55],v[88:91],v[228:231],v[52:55]
s_xor_b32 s64,s64,s65
s_waitcnt lgkmcnt(0)
v_mfma_f32_16x16x32_bf16 v[20:23],v[232:235],v[16:19],v[20:23]
v_mfma_f32_16x16x32_bf16 v[24:27],v[236:239],v[16:19],v[24:27]
v_mfma_f32_16x16x32_bf16 v[32:35],v[16:19],v[240:243],v[32:35]
v_mfma_f32_16x16x32_bf16 v[36:39],v[236:239],v[92:95],v[36:39]
v_mfma_f32_16x16x32_bf16 v[40:43],v[92:95],v[240:243],v[40:43]
v_mfma_f32_16x16x32_bf16 v[44:47],v[232:235],v[96:99],v[44:47]
v_mfma_f32_16x16x32_bf16 v[48:51],v[236:239],v[96:99],v[48:51]
v_mfma_f32_16x16x32_bf16 v[52:55],v[96:99],v[240:243],v[52:55]
v_mfma_f32_16x16x32_bf16 v[20:23],v[244:247],v[16:19],v[20:23]
v_mfma_f32_16x16x32_bf16 v[24:27],v[248:251],v[16:19],v[24:27]
v_mfma_f32_16x16x32_bf16 v[32:35],v[16:19],v[252:255],v[32:35]
v_mfma_f32_16x16x32_bf16 v[36:39],v[248:251],v[100:103],v[36:39]
v_mfma_f32_16x16x32_bf16 v[40:43],v[100:103],v[252:255],v[40:43]
v_mfma_f32_16x16x32_bf16 v[44:47],v[244:247],v[104:107],v[44:47]
v_mfma_f32_16x16x32_bf16 v[48:51],v[248:251],v[104:107],v[48:51]
v_mfma_f32_16x16x32_bf16 v[52:55],v[104:107],v[252:255],v[52:55]
s_nop 2
v_mov_b32 v28,v32
s_waitcnt vmcnt(19)
v_add_f32 v108,v108,v109
v_add_f32 v110,v110,v111
v_add_f32 v108,v108,v110
s_nop 1
v_add_f32_dpp v108,v108,v108 row_ror:8 row_mask:0xf bank_mask:0xf
s_nop 1
v_add_f32_dpp v108,v108,v108 row_ror:4 row_mask:0xf bank_mask:0xf
s_nop 1
v_add_f32_dpp v108,v108,v108 row_ror:2 row_mask:0xf bank_mask:0xf
v_mul_f32 v108,0x3a800000,v108
v_mul_f32 v109,v108,v108
s_nop 1
v_sub_f32_dpp v109,v108,v109 quad_perm:[1,1,3,3] row_mask:0xf bank_mask:0xf
v_max_f32 v109,0,v109
v_cvt_pk_bf16_f32 v109,0,v109
v_add_f32 v109,0x3727c5ac,v109
v_cvt_pk_bf16_f32 v109,0,v109
v_sqrt_f32 v109,v109
v_cvt_pk_bf16_f32 v108,0,v108
v_cvt_pk_bf16_f32 v109,0,v109
v_rcp_f32 v109,v109
s_nop 0
ds_write2_b32 v8,v108,v109 offset0:0 offset1:16
v_add_f32 v112,v112,v113
v_add_f32 v114,v114,v115
v_add_f32 v112,v112,v114
s_nop 1
v_add_f32_dpp v112,v112,v112 row_ror:8 row_mask:0xf bank_mask:0xf
s_nop 1
v_add_f32_dpp v112,v112,v112 row_ror:4 row_mask:0xf bank_mask:0xf
s_nop 1
v_add_f32_dpp v112,v112,v112 row_ror:2 row_mask:0xf bank_mask:0xf
v_mul_f32 v112,0x3a800000,v112
v_mul_f32 v113,v112,v112
s_nop 1
v_sub_f32_dpp v113,v112,v113 quad_perm:[1,1,3,3] row_mask:0xf bank_mask:0xf
v_max_f32 v113,0,v113
v_cvt_pk_bf16_f32 v113,0,v113
v_add_f32 v113,0x3727c5ac,v113
v_cvt_pk_bf16_f32 v113,0,v113
v_sqrt_f32 v113,v113
v_cvt_pk_bf16_f32 v112,0,v112
v_cvt_pk_bf16_f32 v113,0,v113
v_rcp_f32 v113,v113
s_nop 0
ds_write2_b32 v8,v112,v113 offset0:32 offset1:48
s_waitcnt lgkmcnt(0)
s_barrier
ds_read2_b32 v[14:15],v9 offset0:0 offset1:16
ds_read_b128 v[92:95],v10 offset:0
ds_read_b128 v[96:99],v10 offset:64
s_add_u32 s68,s68,1187840
s_waitcnt lgkmcnt(0)
v_mul_f32 v29,0x3e8293ee,v15
v_fma_f32 v36,-v14,v24,v36
v_fma_f32 v37,-v14,v25,v37
v_fma_f32 v38,-v14,v26,v38
v_fma_f32 v39,-v14,v27,v39
v_fma_f32 v32,-v14,v20,v32
v_fma_f32 v33,-v14,v21,v33
v_fma_f32 v34,-v14,v22,v34
v_fma_f32 v35,-v14,v23,v35
v_mul_f32 v36,v36,v15
v_mul_f32 v37,v37,v15
v_mul_f32 v38,v38,v15
v_mul_f32 v39,v39,v15
v_mul_f32 v32,v32,v29
v_mul_f32 v33,v33,v29
v_mul_f32 v34,v34,v29
v_mul_f32 v35,v35,v29
v_cvt_pk_bf16_f32 v104,v36,v37
v_cvt_pk_bf16_f32 v105,v38,v39
v_cvt_pk_bf16_f32 v100,v32,v33
v_cvt_pk_bf16_f32 v101,v34,v35
ds_write2st64_b64 v11,v[100:101],v[104:105] offset0:0 offset1:1
v_fma_f32 v40,-v92,v28,v40
v_fma_f32 v41,-v93,v28,v41
v_fma_f32 v42,-v94,v28,v42
v_fma_f32 v43,-v95,v28,v43
v_mul_f32 v40,v40,v96
v_mul_f32 v41,v41,v97
v_mul_f32 v42,v42,v98
v_mul_f32 v43,v43,v99
s_waitcnt lgkmcnt(0)
s_barrier
ds_read_b64 v[102:103],v3
ds_read_b64 v[106:107],v3 offset:512
v_cvt_pk_bf16_f32 v112,v40,v41
v_cvt_pk_bf16_f32 v113,v42,v43
s_waitcnt lgkmcnt(0)
s_waitcnt vmcnt(9)
s_barrier
ds_read_b128 v[56:59],v4 offset:0
ds_read_b128 v[60:63],v4 offset:1152
ds_read_b128 v[64:67],v4 offset:2304
ds_read_b128 v[68:71],v4 offset:3456
ds_read_b128 v[72:75],v4 offset:4608
ds_read_b128 v[76:79],v4 offset:5760
.L_qgo:
.p2align 6
.L_ql:
s_bitcmp1_b64 s[58:59],s62
s_cbranch_scc1 .L_qs0
.L_qb0:
s_waitcnt lgkmcnt(2)
v_mfma_f32_16x16x32_bf16 v[32:35],a[0:3],v[56:59],0
ds_read_b128 v[80:83],v4 offset:6912
ds_read2_b32 v[14:15],v9 offset0:32 offset1:48
v_mfma_f32_16x16x32_bf16 v[32:35],a[12:15],v[60:63],v[32:35]
ds_read_b128 v[92:95],v10 offset:128
ds_read_b128 v[96:99],v10 offset:192
v_mfma_f32_16x16x32_bf16 v[36:39],a[16:19],v[60:63],0
s_add_u32 s68,s68,1187840
v_mfma_f32_16x16x32_bf16 v[36:39],a[4:7],v[56:59],v[36:39]
ds_read_b128 v[84:87],v4 offset:8064
v_mfma_f32_16x16x32_bf16 v[40:43],v[56:59],a[8:11],0
ds_read2st64_b32 v[88:89],v6 offset0:0 offset1:36
v_mfma_f32_16x16x32_bf16 v[40:43],v[60:63],a[20:23],v[40:43]
s_waitcnt vmcnt(4)
v_cmp_ne_u64 vcc,v[30:31],v[12:13]
v_mfma_f32_16x16x32_bf16 v[40:43],v[64:67],a[32:35],v[40:43]
ds_read_b128 v[56:59],v4 offset:9216
ds_read2st64_b32 v[90:91],v6 offset0:72 offset1:108
v_mfma_f32_16x16x32_bf16 v[40:43],v[68:71],a[44:47],v[40:43]
s_cbranch_vccnz .L_rtq0
.L_bkq0:
s_add_u32 s63,s66,s71
v_mfma_f32_16x16x32_bf16 v[36:39],a[40:43],v[68:71],v[36:39]
s_add_u32 s66,s66,s72
s_add_u32 m0,s64,0
v_mfma_f32_16x16x32_bf16 v[36:39],a[28:31],v[64:67],v[36:39]
ds_read_b128 v[60:63],v4 offset:10368
buffer_load_dwordx4 v2,s[32:35],s63 offen offset:0 sc1 lds
v_mfma_f32_16x16x32_bf16 v[32:35],a[24:27],v[64:67],v[32:35]
s_waitcnt lgkmcnt(5)
v_mul_f32 v29,0x3e8293ee,v15
v_mfma_f32_16x16x32_bf16 v[32:35],a[36:39],v[68:71],v[32:35]
v_fma_f32 v48,-v14,v24,v48
s_waitcnt lgkmcnt(4)
v_mfma_f32_16x16x32_bf16 v[32:35],a[48:51],v[72:75],v[32:35]
ds_read_b128 v[64:67],v4 offset:11520
v_fma_f32 v49,-v14,v25,v49
v_mfma_f32_16x16x32_bf16 v[32:35],a[60:63],v[76:79],v[32:35]
v_fma_f32 v50,-v14,v26,v50
v_fma_f32 v51,-v14,v27,v51
v_mfma_f32_16x16x32_bf16 v[36:39],a[64:67],v[76:79],v[36:39]
v_fma_f32 v44,-v14,v20,v44
v_fma_f32 v45,-v14,v21,v45
v_mfma_f32_16x16x32_bf16 v[36:39],a[52:55],v[72:75],v[36:39]
ds_read_b128 v[68:71],v4 offset:12672
v_fma_f32 v46,-v14,v22,v46
v_mfma_f32_16x16x32_bf16 v[40:43],v[72:75],a[56:59],v[40:43]
buffer_load_dwordx4 v2,s[32:35],s63 offen offset:1024 sc1 lds
v_fma_f32 v47,-v14,v23,v47
v_mfma_f32_16x16x32_bf16 v[40:43],v[76:79],a[68:71],v[40:43]
v_mul_f32 v48,v48,v15
v_mul_f32 v49,v49,v15
v_mfma_f32_16x16x32_bf16 v[40:43],v[80:83],a[80:83],v[40:43]
ds_read_b128 v[72:75],v4 offset:13824
v_mul_f32 v50,v50,v15
v_mfma_f32_16x16x32_bf16 v[40:43],v[84:87],a[92:95],v[40:43]
v_mul_f32 v51,v51,v15
v_mul_f32 v44,v44,v29
v_mfma_f32_16x16x32_bf16 v[36:39],a[88:91],v[84:87],v[36:39]
v_mul_f32 v45,v45,v29
v_mul_f32 v46,v46,v29
v_mfma_f32_16x16x32_bf16 v[36:39],a[76:79],v[80:83],v[36:39]
ds_read_b128 v[76:79],v4 offset:14976
v_mul_f32 v47,v47,v29
v_mfma_f32_16x16x32_bf16 v[32:35],a[72:75],v[80:83],v[32:35]
v_cvt_pk_bf16_f32 v108,v48,v49
v_cvt_pk_bf16_f32 v109,v50,v51
v_mfma_f32_16x16x32_bf16 v[32:35],a[84:87],v[84:87],v[32:35]
v_cvt_pk_bf16_f32 v100,v44,v45
s_waitcnt lgkmcnt(2)
v_mfma_f32_16x16x32_bf16 v[32:35],a[96:99],v[56:59],v[32:35]
ds_read_b128 v[80:83],v4 offset:16128
buffer_load_dwordx4 v2,s[32:35],s63 offen offset:2048 sc1 lds
v_mfma_f32_16x16x32_bf16 v[32:35],a[108:111],v[60:63],v[32:35]
v_cvt_pk_bf16_f32 v101,v46,v47
v_mfma_f32_16x16x32_bf16 v[36:39],a[112:115],v[60:63],v[36:39]
ds_write2st64_b64 v11,v[100:101],v[108:109] offset0:0 offset1:1
v_fma_f32 v52,-v92,v28,v52
v_mfma_f32_16x16x32_bf16 v[36:39],a[100:103],v[56:59],v[36:39]
ds_read_b128 v[84:87],v4 offset:17280
v_fma_f32 v53,-v93,v28,v53
v_mfma_f32_16x16x32_bf16 v[40:43],v[56:59],a[104:107],v[40:43]
)#"
R"#(
v_fma_f32 v54,-v94,v28,v54
v_fma_f32 v55,-v95,v28,v55
v_mfma_f32_16x16x32_bf16 v[40:43],v[60:63],a[116:119],v[40:43]
v_mul_f32 v52,v52,v96
v_mul_f32 v53,v53,v97
v_mfma_f32_16x16x32_bf16 v[40:43],v[64:67],a[128:131],v[40:43]
ds_read_b128 v[56:59],v4 offset:18432
v_mul_f32 v54,v54,v98
v_mfma_f32_16x16x32_bf16 v[40:43],v[68:71],a[140:143],v[40:43]
v_mul_f32 v55,v55,v99
s_waitcnt lgkmcnt(2)
v_mfma_f32_16x16x32_bf16 v[36:39],a[136:139],v[68:71],v[36:39]
buffer_load_dwordx4 v2,s[32:35],s63 offen offset:3072 sc1 lds
s_waitcnt vmcnt(4)
v_mfma_f32_16x16x32_bf16 v[36:39],a[124:127],v[64:67],v[36:39]
ds_read_b128 v[60:63],v4 offset:19584
s_barrier
v_mfma_f32_16x16x32_bf16 v[32:35],a[120:123],v[64:67],v[32:35]
ds_read_b64 v[102:103],v3
ds_read_b64 v[110:111],v3 offset:512
v_mfma_f32_16x16x32_bf16 v[32:35],a[132:135],v[68:71],v[32:35]
buffer_store_dwordx2 v[30:31],v2,s[92:95],s67 idxen sc1
s_waitcnt lgkmcnt(4)
v_mfma_f32_16x16x32_bf16 v[32:35],a[144:147],v[72:75],v[32:35]
ds_read_b128 v[64:67],v4 offset:20736
v_cvt_pk_bf16_f32 v114,v52,v53
v_mfma_f32_16x16x32_bf16 v[32:35],a[156:159],v[76:79],v[32:35]
v_cvt_pk_bf16_f32 v115,v54,v55
v_add_f32 v88,v88,v89
v_mfma_f32_16x16x32_bf16 v[36:39],a[160:163],v[76:79],v[36:39]
v_add_f32 v90,v90,v91
v_mfma_f32_16x16x32_bf16 v[36:39],a[148:151],v[72:75],v[36:39]
ds_read_b128 v[68:71],v4 offset:21888
s_add_u32 s63,s63,4096
v_mfma_f32_16x16x32_bf16 v[40:43],v[72:75],a[152:155],v[40:43]
s_add_u32 m0,s64,4096
v_add_f32 v88,v88,v90
v_mfma_f32_16x16x32_bf16 v[40:43],v[76:79],a[164:167],v[40:43]
buffer_load_dwordx4 v2,s[32:35],s63 offen offset:0 sc1 lds
v_add_f32_dpp v88,v88,v88 row_ror:8 row_mask:0xf bank_mask:0xf
v_mfma_f32_16x16x32_bf16 v[40:43],v[80:83],a[176:179],v[40:43]
ds_read_b128 v[72:75],v4 offset:23040
s_waitcnt lgkmcnt(3)
v_mfma_f32_16x16x32_bf16 v[48:51],v[104:107],v[100:103],v[120:123]
v_add_f32_dpp v88,v88,v88 row_ror:4 row_mask:0xf bank_mask:0xf
v_mfma_f32_16x16x32_bf16 v[44:47],v[108:111],v[100:103],v[116:119]
v_mfma_f32_16x16x32_bf16 v[40:43],v[84:87],a[188:191],v[40:43]
v_add_f32_dpp v88,v88,v88 row_ror:2 row_mask:0xf bank_mask:0xf
v_mfma_f32_16x16x32_bf16 v[36:39],a[184:187],v[84:87],v[36:39]
v_mul_f32 v88,0x3a800000,v88
v_mfma_f32_16x16x32_bf16 v[36:39],a[172:175],v[80:83],v[36:39]
ds_read_b128 v[76:79],v4 offset:24192
v_mul_f32 v89,v88,v88
v_mfma_f32_16x16x32_bf16 v[32:35],a[168:171],v[80:83],v[32:35]
v_max3_f32 v44,v44,v45,v48
v_max3_f32 v46,v46,v47,v50
v_mfma_f32_16x16x32_bf16 v[32:35],a[180:183],v[84:87],v[32:35]
v_max_f32 v44,v44,v49
s_waitcnt lgkmcnt(2)
v_mfma_f32_16x16x32_bf16 v[32:35],a[192:195],v[56:59],v[32:35]
ds_read_b128 v[80:83],v4 offset:25344
buffer_load_dwordx4 v2,s[32:35],s63 offen offset:1024 sc1 lds
v_mfma_f32_16x16x32_bf16 v[32:35],a[204:207],v[60:63],v[32:35]
v_max_f32 v46,v46,v51
v_sub_f32_dpp v89,v88,v89 quad_perm:[1,1,3,3] row_mask:0xf bank_mask:0xf
v_mfma_f32_16x16x32_bf16 v[36:39],a[208:211],v[60:63],v[36:39]
buffer_load_dwordx2 v[12:13],v0,s[0:3],s66 offen sc1
v_max_f32 v96,v44,v46
v_mfma_f32_16x16x32_bf16 v[36:39],a[196:199],v[56:59],v[36:39]
ds_read_b128 v[84:87],v4 offset:26496
v_max_f32 v97,v44,v46
v_mfma_f32_16x16x32_bf16 v[40:43],v[56:59],a[200:203],v[40:43]
v_max_f32 v89,0,v89
v_cvt_pk_bf16_f32 v88,0,v88
v_mfma_f32_16x16x32_bf16 v[40:43],v[60:63],a[212:215],v[40:43]
v_permlane16_swap_b32 v96,v97
v_mfma_f32_16x16x32_bf16 v[40:43],v[64:67],a[224:227],v[40:43]
ds_read_b128 v[56:59],v4 offset:27648
v_max_f32 v98,v96,v97
v_mfma_f32_16x16x32_bf16 v[40:43],v[68:71],a[236:239],v[40:43]
v_max_f32 v99,v96,v97
v_cvt_pk_bf16_f32 v89,0,v89
v_mfma_f32_16x16x32_bf16 v[36:39],a[232:235],v[68:71],v[36:39]
buffer_load_dwordx4 v2,s[32:35],s63 offen offset:2048 sc1 lds
v_add_f32 v89,0x3727c5ac,v89
v_mfma_f32_16x16x32_bf16 v[36:39],a[220:223],v[64:67],v[36:39]
ds_read_b128 v[60:63],v4 offset:28800
v_cvt_pk_bf16_f32 v89,0,v89
v_mfma_f32_16x16x32_bf16 v[32:35],a[216:219],v[64:67],v[32:35]
v_permlane32_swap_b32 v98,v99
v_mfma_f32_16x16x32_bf16 v[32:35],a[228:231],v[68:71],v[32:35]
v_max_f32 v96,v98,v99
s_waitcnt lgkmcnt(2)
v_mfma_f32_16x16x32_bf16 v[32:35],a[240:243],v[72:75],v[32:35]
ds_read_b128 v[64:67],v4 offset:29952
v_sub_f32 v44,v44,v96
v_mfma_f32_16x16x32_bf16 v[32:35],a[252:255],v[76:79],v[32:35]
v_exp_f32 v44,v44
v_mfma_f32_16x16x32_bf16 v[36:39],v[128:131],v[76:79],v[36:39]
v_sqrt_f32 v89,v89
v_mfma_f32_16x16x32_bf16 v[36:39],a[244:247],v[72:75],v[36:39]
ds_read_b128 v[68:71],v4 offset:31104
v_sub_f32 v46,v46,v96
v_mfma_f32_16x16x32_bf16 v[40:43],v[72:75],a[248:251],v[40:43]
buffer_load_dwordx4 v2,s[32:35],s63 offen offset:3072 sc1 lds
v_cvt_pk_bf16_f32 v44,v44,v44
v_mfma_f32_16x16x32_bf16 v[40:43],v[76:79],v[132:135],v[40:43]
v_exp_f32 v46,v46
v_mfma_f32_16x16x32_bf16 v[40:43],v[80:83],v[144:147],v[40:43]
ds_read_b128 v[72:75],v4 offset:32256
v_cvt_pk_bf16_f32 v46,v46,v46
v_mfma_f32_16x16x32_bf16 v[40:43],v[84:87],v[156:159],v[40:43]
v_and_b32 v94,v44,v124
v_and_b32 v92,v44,v126
v_mfma_f32_16x16x32_bf16 v[36:39],v[152:155],v[84:87],v[36:39]
v_and_b32 v95,v46,v125
v_and_b32 v93,v46,v127
v_mfma_f32_16x16x32_bf16 v[36:39],v[140:143],v[80:83],v[36:39]
ds_read_b128 v[76:79],v4 offset:33408
v_cvt_pk_bf16_f32 v89,0,v89
v_mfma_f32_16x16x32_bf16 v[48:51],v[16:19],v[92:95],0
v_rcp_f32 v89,v89
v_mfma_f32_16x16x32_bf16 v[52:55],v[112:115],v[92:95],0
ds_write2_b32 v8,v88,v89 offset0:0 offset1:16
v_mfma_f32_16x16x32_bf16 v[32:35],v[136:139],v[80:83],v[32:35]
v_mfma_f32_16x16x32_bf16 v[32:35],v[148:151],v[84:87],v[32:35]
s_add_u32 s63,s63,4096
s_waitcnt lgkmcnt(3)
v_mfma_f32_16x16x32_bf16 v[32:35],v[160:163],v[56:59],v[32:35]
ds_read_b128 v[80:83],v4 offset:34560
s_add_u32 m0,s64,8192
v_mfma_f32_16x16x32_bf16 v[32:35],v[172:175],v[60:63],v[32:35]
buffer_load_dwordx4 v2,s[32:35],s63 offen offset:0 sc1 lds
v_mfma_f32_16x16x32_bf16 v[36:39],v[176:179],v[60:63],v[36:39]
v_rcp_f32 v48,v48
v_mfma_f32_16x16x32_bf16 v[36:39],v[164:167],v[56:59],v[36:39]
ds_read_b128 v[84:87],v4 offset:35712
v_mul_f32 v52,v52,v48
v_mfma_f32_16x16x32_bf16 v[40:43],v[56:59],v[168:171],v[40:43]
v_xor_b32 v4,73728,v4
v_mul_f32 v53,v53,v48
v_mfma_f32_16x16x32_bf16 v[40:43],v[60:63],v[180:183],v[40:43]
s_waitcnt lgkmcnt(2)
s_barrier
v_mfma_f32_16x16x32_bf16 v[40:43],v[64:67],v[192:195],v[40:43]
ds_read_b128 v[56:59],v5 offset:0
v_mul_f32 v54,v54,v48
v_mfma_f32_16x16x32_bf16 v[40:43],v[68:71],v[204:207],v[40:43]
v_mul_f32 v55,v55,v48
v_cvt_pk_bf16_f32 v100,v52,v53
v_mfma_f32_16x16x32_bf16 v[36:39],v[200:203],v[68:71],v[36:39]
v_cvt_pk_bf16_f32 v101,v54,v55
v_mfma_f32_16x16x32_bf16 v[36:39],v[188:191],v[64:67],v[36:39]
ds_read_b128 v[60:63],v5 offset:1152
v_mfma_f32_16x16x32_bf16 v[32:35],v[184:187],v[64:67],v[32:35]
v_permlane32_swap_b32 v100,v102
v_mfma_f32_16x16x32_bf16 v[32:35],v[196:199],v[68:71],v[32:35]
s_waitcnt lgkmcnt(2)
v_mfma_f32_16x16x32_bf16 v[32:35],v[208:211],v[72:75],v[32:35]
ds_read_b128 v[64:67],v5 offset:2304
v_mfma_f32_16x16x32_bf16 v[32:35],v[220:223],v[76:79],v[32:35]
v_permlane32_swap_b32 v101,v103
v_mfma_f32_16x16x32_bf16 v[36:39],v[224:227],v[76:79],v[36:39]
buffer_store_dwordx4 v[100:103],v1,s[32:35],s68 offen sc1
s_mov_b32 s67,s68
v_mfma_f32_16x16x32_bf16 v[36:39],v[212:215],v[72:75],v[36:39]
ds_read_b128 v[68:71],v5 offset:3456
v_mfma_f32_16x16x32_bf16 v[40:43],v[72:75],v[216:219],v[40:43]
v_mfma_f32_16x16x32_bf16 v[40:43],v[76:79],v[228:231],v[40:43]
v_mfma_f32_16x16x32_bf16 v[40:43],v[80:83],v[240:243],v[40:43]
ds_read_b128 v[72:75],v5 offset:4608
v_mfma_f32_16x16x32_bf16 v[40:43],v[84:87],v[252:255],v[40:43]
v_mfma_f32_16x16x32_bf16 v[36:39],v[248:251],v[84:87],v[36:39]
v_mfma_f32_16x16x32_bf16 v[36:39],v[236:239],v[80:83],v[36:39]
ds_read_b128 v[76:79],v5 offset:5760
v_mfma_f32_16x16x32_bf16 v[32:35],v[232:235],v[80:83],v[32:35]
v_mfma_f32_16x16x32_bf16 v[32:35],v[244:247],v[84:87],v[32:35]
s_bitcmp1_b64 s[60:61],s62
s_cbranch_scc1 .L_qs1
.L_qb1:
s_waitcnt lgkmcnt(2)
v_mfma_f32_16x16x32_bf16 v[44:47],a[0:3],v[56:59],0
ds_read_b128 v[80:83],v5 offset:6912
ds_read2_b32 v[14:15],v9 offset0:0 offset1:16
v_mfma_f32_16x16x32_bf16 v[44:47],a[12:15],v[60:63],v[44:47]
ds_read_b128 v[92:95],v10 offset:0
ds_read_b128 v[96:99],v10 offset:64
v_mfma_f32_16x16x32_bf16 v[48:51],a[16:19],v[60:63],0
s_add_u32 s68,s68,1187840
v_mfma_f32_16x16x32_bf16 v[48:51],a[4:7],v[56:59],v[48:51]
ds_read_b128 v[84:87],v5 offset:8064
v_mfma_f32_16x16x32_bf16 v[52:55],v[56:59],a[8:11],0
ds_read2st64_b32 v[88:89],v6 offset0:144 offset1:180
v_mfma_f32_16x16x32_bf16 v[52:55],v[60:63],a[20:23],v[52:55]
s_waitcnt vmcnt(4)
v_cmp_ne_u64 vcc,v[30:31],v[12:13]
v_mfma_f32_16x16x32_bf16 v[52:55],v[64:67],a[32:35],v[52:55]
ds_read_b128 v[56:59],v5 offset:9216
ds_read2st64_b32 v[90:91],v6 offset0:216 offset1:252
v_mfma_f32_16x16x32_bf16 v[52:55],v[68:71],a[44:47],v[52:55]
s_cbranch_vccnz .L_rtq1
.L_bkq1:
s_add_u32 s63,s66,s71
v_mfma_f32_16x16x32_bf16 v[48:51],a[40:43],v[68:71],v[48:51]
s_add_u32 s66,s66,s72
s_add_u32 m0,s64,36864
v_mfma_f32_16x16x32_bf16 v[48:51],a[28:31],v[64:67],v[48:51]
ds_read_b128 v[60:63],v5 offset:10368
buffer_load_dwordx4 v2,s[32:35],s63 offen offset:0 sc1 lds
v_mfma_f32_16x16x32_bf16 v[44:47],a[24:27],v[64:67],v[44:47]
v_xor_b32 v6,v7,v6
s_waitcnt lgkmcnt(5)
v_mfma_f32_16x16x32_bf16 v[44:47],a[36:39],v[68:71],v[44:47]
v_mul_f32 v29,0x3e8293ee,v15
s_waitcnt lgkmcnt(4)
v_mfma_f32_16x16x32_bf16 v[44:47],a[48:51],v[72:75],v[44:47]
ds_read_b128 v[64:67],v5 offset:11520
v_fma_f32 v36,-v14,v24,v36
v_mfma_f32_16x16x32_bf16 v[44:47],a[60:63],v[76:79],v[44:47]
v_fma_f32 v37,-v14,v25,v37
v_fma_f32 v38,-v14,v26,v38
v_mfma_f32_16x16x32_bf16 v[48:51],a[64:67],v[76:79],v[48:51]
v_fma_f32 v39,-v14,v27,v39
v_fma_f32 v32,-v14,v20,v32
v_mfma_f32_16x16x32_bf16 v[48:51],a[52:55],v[72:75],v[48:51]
ds_read_b128 v[68:71],v5 offset:12672
v_fma_f32 v33,-v14,v21,v33
v_mfma_f32_16x16x32_bf16 v[52:55],v[72:75],a[56:59],v[52:55]
buffer_load_dwordx4 v2,s[32:35],s63 offen offset:1024 sc1 lds
v_fma_f32 v34,-v14,v22,v34
v_mfma_f32_16x16x32_bf16 v[52:55],v[76:79],a[68:71],v[52:55]
v_fma_f32 v35,-v14,v23,v35
v_mul_f32 v36,v36,v15
v_mfma_f32_16x16x32_bf16 v[52:55],v[80:83],a[80:83],v[52:55]
ds_read_b128 v[72:75],v5 offset:13824
v_mul_f32 v37,v37,v15
v_mfma_f32_16x16x32_bf16 v[52:55],v[84:87],a[92:95],v[52:55]
v_mul_f32 v38,v38,v15
v_mul_f32 v39,v39,v15
v_mfma_f32_16x16x32_bf16 v[48:51],a[88:91],v[84:87],v[48:51]
v_mul_f32 v32,v32,v29
v_mul_f32 v33,v33,v29
v_mfma_f32_16x16x32_bf16 v[48:51],a[76:79],v[80:83],v[48:51]
ds_read_b128 v[76:79],v5 offset:14976
v_mul_f32 v34,v34,v29
v_mfma_f32_16x16x32_bf16 v[44:47],a[72:75],v[80:83],v[44:47]
v_mul_f32 v35,v35,v29
v_cvt_pk_bf16_f32 v104,v36,v37
v_mfma_f32_16x16x32_bf16 v[44:47],a[84:87],v[84:87],v[44:47]
v_cvt_pk_bf16_f32 v105,v38,v39
s_waitcnt lgkmcnt(2)
v_mfma_f32_16x16x32_bf16 v[44:47],a[96:99],v[56:59],v[44:47]
ds_read_b128 v[80:83],v5 offset:16128
buffer_load_dwordx4 v2,s[32:35],s63 offen offset:2048 sc1 lds
v_mfma_f32_16x16x32_bf16 v[44:47],a[108:111],v[60:63],v[44:47]
v_cvt_pk_bf16_f32 v100,v32,v33
v_cvt_pk_bf16_f32 v101,v34,v35
v_mfma_f32_16x16x32_bf16 v[48:51],a[112:115],v[60:63],v[48:51]
ds_write2st64_b64 v11,v[100:101],v[104:105] offset0:0 offset1:1
v_fma_f32 v40,-v92,v28,v40
v_mfma_f32_16x16x32_bf16 v[48:51],a[100:103],v[56:59],v[48:51]
ds_read_b128 v[84:87],v5 offset:17280
v_fma_f32 v41,-v93,v28,v41
v_mfma_f32_16x16x32_bf16 v[52:55],v[56:59],a[104:107],v[52:55]
v_fma_f32 v42,-v94,v28,v42
v_fma_f32 v43,-v95,v28,v43
v_mfma_f32_16x16x32_bf16 v[52:55],v[60:63],a[116:119],v[52:55]
v_mul_f32 v40,v40,v96
v_mul_f32 v41,v41,v97
v_mfma_f32_16x16x32_bf16 v[52:55],v[64:67],a[128:131],v[52:55]
ds_read_b128 v[56:59],v5 offset:18432
v_mul_f32 v42,v42,v98
v_mfma_f32_16x16x32_bf16 v[52:55],v[68:71],a[140:143],v[52:55]
v_mul_f32 v43,v43,v99
s_waitcnt lgkmcnt(2)
v_mfma_f32_16x16x32_bf16 v[48:51],a[136:139],v[68:71],v[48:51]
buffer_load_dwordx4 v2,s[32:35],s63 offen offset:3072 sc1 lds
s_waitcnt vmcnt(4)
v_mfma_f32_16x16x32_bf16 v[48:51],a[124:127],v[64:67],v[48:51]
ds_read_b128 v[60:63],v5 offset:19584
s_barrier
v_mfma_f32_16x16x32_bf16 v[44:47],a[120:123],v[64:67],v[44:47]
ds_read_b64 v[102:103],v3
ds_read_b64 v[106:107],v3 offset:512
v_mfma_f32_16x16x32_bf16 v[44:47],a[132:135],v[68:71],v[44:47]
buffer_store_dwordx2 v[30:31],v2,s[92:95],s67 idxen sc1
s_waitcnt lgkmcnt(4)
v_mfma_f32_16x16x32_bf16 v[44:47],a[144:147],v[72:75],v[44:47]
ds_read_b128 v[64:67],v5 offset:20736
v_cvt_pk_bf16_f32 v112,v40,v41
v_mfma_f32_16x16x32_bf16 v[44:47],a[156:159],v[76:79],v[44:47]
v_cvt_pk_bf16_f32 v113,v42,v43
v_add_f32 v88,v88,v89
v_mfma_f32_16x16x32_bf16 v[48:51],a[160:163],v[76:79],v[48:51]
v_add_f32 v90,v90,v91
v_mfma_f32_16x16x32_bf16 v[48:51],a[148:151],v[72:75],v[48:51]
ds_read_b128 v[68:71],v5 offset:21888
s_add_u32 s63,s63,4096
v_mfma_f32_16x16x32_bf16 v[52:55],v[72:75],a[152:155],v[52:55]
s_add_u32 m0,s64,40960
v_add_f32 v88,v88,v90
v_mfma_f32_16x16x32_bf16 v[52:55],v[76:79],a[164:167],v[52:55]
buffer_load_dwordx4 v2,s[32:35],s63 offen offset:0 sc1 lds
v_add_f32_dpp v88,v88,v88 row_ror:8 row_mask:0xf bank_mask:0xf
v_mfma_f32_16x16x32_bf16 v[52:55],v[80:83],a[176:179],v[52:55]
ds_read_b128 v[72:75],v5 offset:23040
s_waitcnt lgkmcnt(3)
v_mfma_f32_16x16x32_bf16 v[36:39],v[108:111],v[100:103],v[120:123]
v_add_f32_dpp v88,v88,v88 row_ror:4 row_mask:0xf bank_mask:0xf
v_mfma_f32_16x16x32_bf16 v[32:35],v[104:107],v[100:103],v[116:119]
v_mfma_f32_16x16x32_bf16 v[52:55],v[84:87],a[188:191],v[52:55]
v_add_f32_dpp v88,v88,v88 row_ror:2 row_mask:0xf bank_mask:0xf
v_mfma_f32_16x16x32_bf16 v[48:51],a[184:187],v[84:87],v[48:51]
v_mul_f32 v88,0x3a800000,v88
v_mfma_f32_16x16x32_bf16 v[48:51],a[172:175],v[80:83],v[48:51]
ds_read_b128 v[76:79],v5 offset:24192
v_mul_f32 v89,v88,v88
v_mfma_f32_16x16x32_bf16 v[44:47],a[168:171],v[80:83],v[44:47]
v_max3_f32 v32,v32,v33,v36
v_max3_f32 v34,v34,v35,v38
v_mfma_f32_16x16x32_bf16 v[44:47],a[180:183],v[84:87],v[44:47]
v_max_f32 v32,v32,v37
s_waitcnt lgkmcnt(2)
v_mfma_f32_16x16x32_bf16 v[44:47],a[192:195],v[56:59],v[44:47]
ds_read_b128 v[80:83],v5 offset:25344
buffer_load_dwordx4 v2,s[32:35],s63 offen offset:1024 sc1 lds
v_mfma_f32_16x16x32_bf16 v[44:47],a[204:207],v[60:63],v[44:47]
v_max_f32 v34,v34,v39
v_sub_f32_dpp v89,v88,v89 quad_perm:[1,1,3,3] row_mask:0xf bank_mask:0xf
v_mfma_f32_16x16x32_bf16 v[48:51],a[208:211],v[60:63],v[48:51]
buffer_load_dwordx2 v[12:13],v0,s[0:3],s66 offen sc1
v_max_f32 v96,v32,v34
v_mfma_f32_16x16x32_bf16 v[48:51],a[196:199],v[56:59],v[48:51]
ds_read_b128 v[84:87],v5 offset:26496
v_max_f32 v97,v32,v34
v_mfma_f32_16x16x32_bf16 v[52:55],v[56:59],a[200:203],v[52:55]
v_max_f32 v89,0,v89
v_cvt_pk_bf16_f32 v88,0,v88
v_mfma_f32_16x16x32_bf16 v[52:55],v[60:63],a[212:215],v[52:55]
v_permlane16_swap_b32 v96,v97
v_mfma_f32_16x16x32_bf16 v[52:55],v[64:67],a[224:227],v[52:55]
ds_read_b128 v[56:59],v5 offset:27648
v_max_f32 v98,v96,v97
v_mfma_f32_16x16x32_bf16 v[52:55],v[68:71],a[236:239],v[52:55]
v_max_f32 v99,v96,v97
v_cvt_pk_bf16_f32 v89,0,v89
v_mfma_f32_16x16x32_bf16 v[48:51],a[232:235],v[68:71],v[48:51]
buffer_load_dwordx4 v2,s[32:35],s63 offen offset:2048 sc1 lds
v_add_f32 v89,0x3727c5ac,v89
v_mfma_f32_16x16x32_bf16 v[48:51],a[220:223],v[64:67],v[48:51]
ds_read_b128 v[60:63],v5 offset:28800
v_cvt_pk_bf16_f32 v89,0,v89
v_mfma_f32_16x16x32_bf16 v[44:47],a[216:219],v[64:67],v[44:47]
v_permlane32_swap_b32 v98,v99
v_mfma_f32_16x16x32_bf16 v[44:47],a[228:231],v[68:71],v[44:47]
v_max_f32 v96,v98,v99
s_waitcnt lgkmcnt(2)
v_mfma_f32_16x16x32_bf16 v[44:47],a[240:243],v[72:75],v[44:47]
ds_read_b128 v[64:67],v5 offset:29952
v_sub_f32 v32,v32,v96
v_mfma_f32_16x16x32_bf16 v[44:47],a[252:255],v[76:79],v[44:47]
v_exp_f32 v32,v32
v_mfma_f32_16x16x32_bf16 v[48:51],v[128:131],v[76:79],v[48:51]
v_sqrt_f32 v89,v89
v_mfma_f32_16x16x32_bf16 v[48:51],a[244:247],v[72:75],v[48:51]
ds_read_b128 v[68:71],v5 offset:31104
v_sub_f32 v34,v34,v96
v_mfma_f32_16x16x32_bf16 v[52:55],v[72:75],a[248:251],v[52:55]
buffer_load_dwordx4 v2,s[32:35],s63 offen offset:3072 sc1 lds
v_cvt_pk_bf16_f32 v32,v32,v32
v_mfma_f32_16x16x32_bf16 v[52:55],v[76:79],v[132:135],v[52:55]
v_exp_f32 v34,v34
v_mfma_f32_16x16x32_bf16 v[52:55],v[80:83],v[144:147],v[52:55]
ds_read_b128 v[72:75],v5 offset:32256
v_cvt_pk_bf16_f32 v34,v34,v34
v_mfma_f32_16x16x32_bf16 v[52:55],v[84:87],v[156:159],v[52:55]
v_and_b32 v92,v32,v124
v_and_b32 v94,v32,v126
v_mfma_f32_16x16x32_bf16 v[48:51],v[152:155],v[84:87],v[48:51]
v_and_b32 v93,v34,v125
v_and_b32 v95,v34,v127
v_mfma_f32_16x16x32_bf16 v[48:51],v[140:143],v[80:83],v[48:51]
ds_read_b128 v[76:79],v5 offset:33408
v_cvt_pk_bf16_f32 v89,0,v89
v_mfma_f32_16x16x32_bf16 v[36:39],v[16:19],v[92:95],0
v_rcp_f32 v89,v89
v_mfma_f32_16x16x32_bf16 v[40:43],v[112:115],v[92:95],0
ds_write2_b32 v8,v88,v89 offset0:32 offset1:48
v_mfma_f32_16x16x32_bf16 v[44:47],v[136:139],v[80:83],v[44:47]
v_mfma_f32_16x16x32_bf16 v[44:47],v[148:151],v[84:87],v[44:47]
s_add_u32 s63,s63,4096
s_waitcnt lgkmcnt(3)
v_mfma_f32_16x16x32_bf16 v[44:47],v[160:163],v[56:59],v[44:47]
ds_read_b128 v[80:83],v5 offset:34560
s_add_u32 m0,s64,45056
v_mfma_f32_16x16x32_bf16 v[44:47],v[172:175],v[60:63],v[44:47]
buffer_load_dwordx4 v2,s[32:35],s63 offen offset:0 sc1 lds
s_xor_b32 s64,s64,s65
v_mfma_f32_16x16x32_bf16 v[48:51],v[176:179],v[60:63],v[48:51]
v_rcp_f32 v36,v36
v_mfma_f32_16x16x32_bf16 v[48:51],v[164:167],v[56:59],v[48:51]
ds_read_b128 v[84:87],v5 offset:35712
v_mul_f32 v40,v40,v36
v_mfma_f32_16x16x32_bf16 v[52:55],v[56:59],v[168:171],v[52:55]
v_xor_b32 v5,73728,v5
v_mul_f32 v41,v41,v36
v_mfma_f32_16x16x32_bf16 v[52:55],v[60:63],v[180:183],v[52:55]
s_waitcnt lgkmcnt(2)
s_barrier
v_mfma_f32_16x16x32_bf16 v[52:55],v[64:67],v[192:195],v[52:55]
ds_read_b128 v[56:59],v4 offset:0
v_mul_f32 v42,v42,v36
v_mfma_f32_16x16x32_bf16 v[52:55],v[68:71],v[204:207],v[52:55]
v_mul_f32 v43,v43,v36
v_cvt_pk_bf16_f32 v100,v40,v41
v_mfma_f32_16x16x32_bf16 v[48:51],v[200:203],v[68:71],v[48:51]
v_cvt_pk_bf16_f32 v101,v42,v43
v_mfma_f32_16x16x32_bf16 v[48:51],v[188:191],v[64:67],v[48:51]
ds_read_b128 v[60:63],v4 offset:1152
v_mfma_f32_16x16x32_bf16 v[44:47],v[184:187],v[64:67],v[44:47]
v_permlane32_swap_b32 v100,v102
v_mfma_f32_16x16x32_bf16 v[44:47],v[196:199],v[68:71],v[44:47]
s_waitcnt lgkmcnt(2)
v_mfma_f32_16x16x32_bf16 v[44:47],v[208:211],v[72:75],v[44:47]
ds_read_b128 v[64:67],v4 offset:2304
v_mfma_f32_16x16x32_bf16 v[44:47],v[220:223],v[76:79],v[44:47]
v_permlane32_swap_b32 v101,v103
v_mfma_f32_16x16x32_bf16 v[48:51],v[224:227],v[76:79],v[48:51]
buffer_store_dwordx4 v[100:103],v1,s[32:35],s68 offen sc1
s_mov_b32 s67,s68
v_mfma_f32_16x16x32_bf16 v[48:51],v[212:215],v[72:75],v[48:51]
ds_read_b128 v[68:71],v4 offset:3456
v_mfma_f32_16x16x32_bf16 v[52:55],v[72:75],v[216:219],v[52:55]
v_mfma_f32_16x16x32_bf16 v[52:55],v[76:79],v[228:231],v[52:55]
v_mfma_f32_16x16x32_bf16 v[52:55],v[80:83],v[240:243],v[52:55]
ds_read_b128 v[72:75],v4 offset:4608
v_mfma_f32_16x16x32_bf16 v[52:55],v[84:87],v[252:255],v[52:55]
v_mfma_f32_16x16x32_bf16 v[48:51],v[248:251],v[84:87],v[48:51]
v_mfma_f32_16x16x32_bf16 v[48:51],v[236:239],v[80:83],v[48:51]
ds_read_b128 v[76:79],v4 offset:5760
v_mfma_f32_16x16x32_bf16 v[44:47],v[232:235],v[80:83],v[44:47]
s_sub_u32 s62,s62,1
v_mfma_f32_16x16x32_bf16 v[44:47],v[244:247],v[84:87],v[44:47]
s_cbranch_scc0 .L_ql
s_nop 1
ds_read2_b32 v[14:15],v9 offset0:32 offset1:48
ds_read_b128 v[92:95],v10 offset:128
ds_read_b128 v[96:99],v10 offset:192
s_add_u32 s68,s68,1187840
s_waitcnt lgkmcnt(0)
v_mul_f32 v29,0x3e8293ee,v15
v_fma_f32 v48,-v14,v24,v48
v_fma_f32 v49,-v14,v25,v49
v_fma_f32 v50,-v14,v26,v50
v_fma_f32 v51,-v14,v27,v51
v_fma_f32 v44,-v14,v20,v44
v_fma_f32 v45,-v14,v21,v45
v_fma_f32 v46,-v14,v22,v46
v_fma_f32 v47,-v14,v23,v47
v_mul_f32 v48,v48,v15
v_mul_f32 v49,v49,v15
v_mul_f32 v50,v50,v15
v_mul_f32 v51,v51,v15
v_mul_f32 v44,v44,v29
v_mul_f32 v45,v45,v29
v_mul_f32 v46,v46,v29
v_mul_f32 v47,v47,v29
v_cvt_pk_bf16_f32 v108,v48,v49
v_cvt_pk_bf16_f32 v109,v50,v51
v_cvt_pk_bf16_f32 v100,v44,v45
v_cvt_pk_bf16_f32 v101,v46,v47
ds_write2st64_b64 v11,v[100:101],v[108:109] offset0:0 offset1:1
v_fma_f32 v52,-v92,v28,v52
v_fma_f32 v53,-v93,v28,v53
v_fma_f32 v54,-v94,v28,v54
v_fma_f32 v55,-v95,v28,v55
v_mul_f32 v52,v52,v96
v_mul_f32 v53,v53,v97
v_mul_f32 v54,v54,v98
v_mul_f32 v55,v55,v99
s_waitcnt lgkmcnt(0)
s_waitcnt vmcnt(0)
s_barrier
ds_read_b64 v[102:103],v3
ds_read_b64 v[110:111],v3 offset:512
buffer_store_dwordx2 v[30:31],v2,s[92:95],s67 idxen sc1
v_cvt_pk_bf16_f32 v114,v52,v53
v_cvt_pk_bf16_f32 v115,v54,v55
s_waitcnt lgkmcnt(0)
v_mfma_f32_16x16x32_bf16 v[48:51],v[104:107],v[100:103],v[120:123]
v_mfma_f32_16x16x32_bf16 v[44:47],v[108:111],v[100:103],v[116:119]
s_nop 7
v_max3_f32 v44,v44,v45,v48
v_max3_f32 v46,v46,v47,v50
v_max_f32 v44,v44,v49
v_max_f32 v46,v46,v51
v_max_f32 v96,v44,v46
v_max_f32 v97,v44,v46
s_nop 1
v_permlane16_swap_b32 v96,v97
)#"
R"#(
v_max_f32 v98,v96,v97
v_max_f32 v99,v96,v97
s_nop 1
v_permlane32_swap_b32 v98,v99
v_max_f32 v96,v98,v99
v_sub_f32 v44,v44,v96
v_sub_f32 v46,v46,v96
v_exp_f32 v44,v44
v_exp_f32 v46,v46
v_cvt_pk_bf16_f32 v44,v44,v44
v_cvt_pk_bf16_f32 v46,v46,v46
v_and_b32 v94,v44,v124
v_and_b32 v92,v44,v126
v_and_b32 v95,v46,v125
v_and_b32 v93,v46,v127
s_nop 1
v_mfma_f32_16x16x32_bf16 v[48:51],v[16:19],v[92:95],0
v_mfma_f32_16x16x32_bf16 v[52:55],v[112:115],v[92:95],0
s_nop 6
v_rcp_f32 v48,v48
s_nop 0
v_mul_f32 v52,v52,v48
v_mul_f32 v53,v53,v48
v_mul_f32 v54,v54,v48
v_mul_f32 v55,v55,v48
v_cvt_pk_bf16_f32 v100,v52,v53
v_cvt_pk_bf16_f32 v101,v54,v55
s_nop 0
v_permlane32_swap_b32 v100,v102
v_permlane32_swap_b32 v101,v103
buffer_store_dwordx4 v[100:103],v1,s[32:35],s68 offen sc1
s_mov_b32 s67,s68
s_waitcnt vmcnt(0)
s_barrier
buffer_store_dwordx2 v[30:31],v2,s[92:95],s67 idxen sc1
s_branch .L_two
.L_w:
s_mov_b32 s96,0
s_add_u32 s36,s12,0x0
s_addc_u32 s37,s13,0x0
s_and_b32 s37,s37,0xffff
s_mov_b32 s38,0x200000
s_mov_b32 s39,0x20000
s_add_u32 s40,s12,0xfffffc80
s_addc_u32 s41,s13,0xffffffff
s_and_b32 s41,s41,0xffff
s_mov_b32 s42,0x200380
s_mov_b32 s43,0x20000
s_lshl_b32 s48,s27,8
s_add_u32 s96,s96,s48
v_and_b32 v8,3,v1
v_mul_u32_u24 v8,16,v8
s_and_b32 s48,s26,1
s_lshl_b32 s48,s48,6
s_lshr_b32 s49,s26,1
s_lshl_b32 s49,s49,10
s_add_u32 s48,s48,s49
v_add_u32 v8,s48,v8
v_lshrrev_b32 v4,3,v1
v_bfe_u32 v5,v1,4,1
s_and_b32 s48,s26,1
s_lshl_b32 s48,s48,1
v_or_b32 v5,s48,v5
v_lshlrev_b32 v5,1,v5
v_and_b32 v6,7,v1
v_xor_b32 v5,v5,v6
v_mul_u32_u24 v4,2048,v4
v_lshl_add_u32 v7,v5,4,v4
s_mul_i32 s48,s26,16384
s_add_u32 s96,s96,s48
s_mul_i32 s97,s26,2048
s_add_u32 m0,s97,0
s_nop 0
buffer_load_dwordx4 v7,s[36:39],s96 offen offset:0 lds
buffer_load_dwordx4 v7,s[40:43],s96 offen offset:1024 lds
s_add_u32 s96,s96,65536
s_add_u32 m0,s97,8192
s_nop 0
buffer_load_dwordx4 v7,s[36:39],s96 offen offset:0 lds
buffer_load_dwordx4 v7,s[40:43],s96 offen offset:1024 lds
s_add_u32 s96,s96,65536
s_add_u32 m0,s97,16384
s_nop 0
buffer_load_dwordx4 v7,s[36:39],s96 offen offset:0 lds
buffer_load_dwordx4 v7,s[40:43],s96 offen offset:1024 lds
s_add_u32 s96,s96,65536
s_add_u32 m0,s97,24576
s_nop 0
buffer_load_dwordx4 v7,s[36:39],s96 offen offset:0 lds
buffer_load_dwordx4 v7,s[40:43],s96 offen offset:1024 lds
s_add_u32 s96,s96,65536
s_add_u32 m0,s97,32768
s_nop 0
buffer_load_dwordx4 v7,s[36:39],s96 offen offset:0 lds
buffer_load_dwordx4 v7,s[40:43],s96 offen offset:1024 lds
s_add_u32 s96,s96,65536
s_add_u32 m0,s97,40960
s_nop 0
buffer_load_dwordx4 v7,s[36:39],s96 offen offset:0 lds
buffer_load_dwordx4 v7,s[40:43],s96 offen offset:1024 lds
s_add_u32 s96,s96,65536
s_add_u32 m0,s97,49152
s_nop 0
buffer_load_dwordx4 v7,s[36:39],s96 offen offset:0 lds
buffer_load_dwordx4 v7,s[40:43],s96 offen offset:1024 lds
s_add_u32 s96,s96,65536
s_add_u32 m0,s97,57344
s_nop 0
buffer_load_dwordx4 v7,s[36:39],s96 offen offset:0 lds
buffer_load_dwordx4 v7,s[40:43],s96 offen offset:1024 lds
s_add_u32 s96,s96,65536
s_add_u32 m0,s97,65536
s_nop 0
buffer_load_dwordx4 v7,s[36:39],s96 offen offset:0 lds
buffer_load_dwordx4 v7,s[40:43],s96 offen offset:1024 lds
s_add_u32 s96,s96,65536
s_add_u32 m0,s97,73728
s_nop 0
buffer_load_dwordx4 v7,s[36:39],s96 offen offset:0 lds
buffer_load_dwordx4 v7,s[40:43],s96 offen offset:1024 lds
s_add_u32 s96,s96,65536
s_add_u32 m0,s97,81920
s_nop 0
buffer_load_dwordx4 v7,s[36:39],s96 offen offset:0 lds
buffer_load_dwordx4 v7,s[40:43],s96 offen offset:1024 lds
s_add_u32 s96,s96,65536
s_add_u32 m0,s97,90112
s_nop 0
buffer_load_dwordx4 v7,s[36:39],s96 offen offset:0 lds
buffer_load_dwordx4 v7,s[40:43],s96 offen offset:1024 lds
s_add_u32 s96,s96,65536
s_add_u32 m0,s97,98304
s_nop 0
buffer_load_dwordx4 v7,s[36:39],s96 offen offset:0 lds
buffer_load_dwordx4 v7,s[40:43],s96 offen offset:1024 lds
s_add_u32 s96,s96,65536
s_add_u32 m0,s97,106496
s_nop 0
buffer_load_dwordx4 v7,s[36:39],s96 offen offset:0 lds
buffer_load_dwordx4 v7,s[40:43],s96 offen offset:1024 lds
s_add_u32 s96,s96,65536
v_lshrrev_b32 v5,4,v1
v_bfe_u32 v4,v1,3,1
v_and_b32 v6,1,v5
v_lshl_add_u32 v4,v6,1,v4
v_lshlrev_b32 v4,5,v4
v_mul_u32_u24 v5,2048,v5
v_bfe_u32 v6,v1,2,2
v_lshl_add_u32 v5,v6,7,v5
v_xor_b32 v8,v4,v8
v_add_u32 v8,v5,v8
v_add_u32 v9,65536,v8
s_waitcnt vmcnt(24)
s_barrier
s_add_u32 m0,s97,114688
s_nop 0
buffer_load_dwordx4 v7,s[36:39],s96 offen offset:0 lds
buffer_load_dwordx4 v7,s[40:43],s96 offen offset:1024 lds
s_add_u32 s96,s96,65536
s_add_u32 m0,s97,122880
s_nop 0
buffer_load_dwordx4 v7,s[36:39],s96 offen offset:0 lds
buffer_load_dwordx4 v7,s[40:43],s96 offen offset:1024 lds
s_add_u32 s96,s96,65536
ds_read_b64_tr_b16 a[0:1],v8 offset:0
ds_read_b64_tr_b16 a[2:3],v8 offset:512
ds_read_b64_tr_b16 a[4:5],v8 offset:8
ds_read_b64_tr_b16 a[6:7],v8 offset:520
ds_read_b64_tr_b16 a[8:9],v8 offset:8192
ds_read_b64_tr_b16 a[10:11],v8 offset:8704
ds_read_b64_tr_b16 a[12:13],v8 offset:8200
ds_read_b64_tr_b16 a[14:15],v8 offset:8712
s_waitcnt lgkmcnt(0)
s_waitcnt vmcnt(24)
s_barrier
s_add_u32 m0,s97,0
s_nop 0
buffer_load_dwordx4 v7,s[36:39],s96 offen offset:0 lds
buffer_load_dwordx4 v7,s[40:43],s96 offen offset:1024 lds
s_add_u32 s96,s96,65536
s_add_u32 m0,s97,8192
s_nop 0
buffer_load_dwordx4 v7,s[36:39],s96 offen offset:0 lds
buffer_load_dwordx4 v7,s[40:43],s96 offen offset:1024 lds
s_add_u32 s96,s96,65536
ds_read_b64_tr_b16 a[16:17],v8 offset:16384
ds_read_b64_tr_b16 a[18:19],v8 offset:16896
ds_read_b64_tr_b16 a[20:21],v8 offset:16392
ds_read_b64_tr_b16 a[22:23],v8 offset:16904
ds_read_b64_tr_b16 a[24:25],v8 offset:24576
ds_read_b64_tr_b16 a[26:27],v8 offset:25088
ds_read_b64_tr_b16 a[28:29],v8 offset:24584
ds_read_b64_tr_b16 a[30:31],v8 offset:25096
s_waitcnt lgkmcnt(0)
s_waitcnt vmcnt(24)
s_barrier
s_add_u32 m0,s97,16384
s_nop 0
buffer_load_dwordx4 v7,s[36:39],s96 offen offset:0 lds
buffer_load_dwordx4 v7,s[40:43],s96 offen offset:1024 lds
s_add_u32 s96,s96,65536
s_add_u32 m0,s97,24576
s_nop 0
buffer_load_dwordx4 v7,s[36:39],s96 offen offset:0 lds
buffer_load_dwordx4 v7,s[40:43],s96 offen offset:1024 lds
s_add_u32 s96,s96,65536
ds_read_b64_tr_b16 a[32:33],v8 offset:32768
ds_read_b64_tr_b16 a[34:35],v8 offset:33280
ds_read_b64_tr_b16 a[36:37],v8 offset:32776
ds_read_b64_tr_b16 a[38:39],v8 offset:33288
ds_read_b64_tr_b16 a[40:41],v8 offset:40960
ds_read_b64_tr_b16 a[42:43],v8 offset:41472
ds_read_b64_tr_b16 a[44:45],v8 offset:40968
ds_read_b64_tr_b16 a[46:47],v8 offset:41480
s_waitcnt lgkmcnt(0)
s_waitcnt vmcnt(24)
s_barrier
s_add_u32 m0,s97,32768
s_nop 0
buffer_load_dwordx4 v7,s[36:39],s96 offen offset:0 lds
buffer_load_dwordx4 v7,s[40:43],s96 offen offset:1024 lds
s_add_u32 s96,s96,65536
s_add_u32 m0,s97,40960
s_nop 0
buffer_load_dwordx4 v7,s[36:39],s96 offen offset:0 lds
buffer_load_dwordx4 v7,s[40:43],s96 offen offset:1024 lds
s_add_u32 s96,s96,65536
ds_read_b64_tr_b16 a[48:49],v8 offset:49152
ds_read_b64_tr_b16 a[50:51],v8 offset:49664
ds_read_b64_tr_b16 a[52:53],v8 offset:49160
ds_read_b64_tr_b16 a[54:55],v8 offset:49672
ds_read_b64_tr_b16 a[56:57],v8 offset:57344
ds_read_b64_tr_b16 a[58:59],v8 offset:57856
ds_read_b64_tr_b16 a[60:61],v8 offset:57352
ds_read_b64_tr_b16 a[62:63],v8 offset:57864
s_waitcnt lgkmcnt(0)
s_waitcnt vmcnt(24)
s_barrier
s_add_u32 m0,s97,49152
s_nop 0
buffer_load_dwordx4 v7,s[36:39],s96 offen offset:0 lds
buffer_load_dwordx4 v7,s[40:43],s96 offen offset:1024 lds
s_add_u32 s96,s96,65536
s_add_u32 m0,s97,57344
s_nop 0
buffer_load_dwordx4 v7,s[36:39],s96 offen offset:0 lds
buffer_load_dwordx4 v7,s[40:43],s96 offen offset:1024 lds
s_add_u32 s96,s96,65536
ds_read_b64_tr_b16 a[64:65],v9 offset:0
ds_read_b64_tr_b16 a[66:67],v9 offset:512
ds_read_b64_tr_b16 a[68:69],v9 offset:8
ds_read_b64_tr_b16 a[70:71],v9 offset:520
ds_read_b64_tr_b16 a[72:73],v9 offset:8192
ds_read_b64_tr_b16 a[74:75],v9 offset:8704
ds_read_b64_tr_b16 a[76:77],v9 offset:8200
ds_read_b64_tr_b16 a[78:79],v9 offset:8712
s_waitcnt lgkmcnt(0)
s_waitcnt vmcnt(24)
s_barrier
s_add_u32 m0,s97,65536
s_nop 0
buffer_load_dwordx4 v7,s[36:39],s96 offen offset:0 lds
buffer_load_dwordx4 v7,s[40:43],s96 offen offset:1024 lds
s_add_u32 s96,s96,65536
s_add_u32 m0,s97,73728
s_nop 0
buffer_load_dwordx4 v7,s[36:39],s96 offen offset:0 lds
buffer_load_dwordx4 v7,s[40:43],s96 offen offset:1024 lds
s_add_u32 s96,s96,65536
ds_read_b64_tr_b16 a[80:81],v9 offset:16384
ds_read_b64_tr_b16 a[82:83],v9 offset:16896
ds_read_b64_tr_b16 a[84:85],v9 offset:16392
ds_read_b64_tr_b16 a[86:87],v9 offset:16904
ds_read_b64_tr_b16 a[88:89],v9 offset:24576
ds_read_b64_tr_b16 a[90:91],v9 offset:25088
ds_read_b64_tr_b16 a[92:93],v9 offset:24584
ds_read_b64_tr_b16 a[94:95],v9 offset:25096
s_waitcnt lgkmcnt(0)
s_waitcnt vmcnt(24)
s_barrier
s_add_u32 m0,s97,81920
s_nop 0
buffer_load_dwordx4 v7,s[36:39],s96 offen offset:0 lds
buffer_load_dwordx4 v7,s[40:43],s96 offen offset:1024 lds
s_add_u32 s96,s96,65536
s_add_u32 m0,s97,90112
s_nop 0
buffer_load_dwordx4 v7,s[36:39],s96 offen offset:0 lds
buffer_load_dwordx4 v7,s[40:43],s96 offen offset:1024 lds
s_add_u32 s96,s96,65536
ds_read_b64_tr_b16 a[96:97],v9 offset:32768
ds_read_b64_tr_b16 a[98:99],v9 offset:33280
ds_read_b64_tr_b16 a[100:101],v9 offset:32776
ds_read_b64_tr_b16 a[102:103],v9 offset:33288
ds_read_b64_tr_b16 a[104:105],v9 offset:40960
ds_read_b64_tr_b16 a[106:107],v9 offset:41472
ds_read_b64_tr_b16 a[108:109],v9 offset:40968
ds_read_b64_tr_b16 a[110:111],v9 offset:41480
s_waitcnt lgkmcnt(0)
s_waitcnt vmcnt(24)
s_barrier
s_add_u32 m0,s97,98304
s_nop 0
buffer_load_dwordx4 v7,s[36:39],s96 offen offset:0 lds
buffer_load_dwordx4 v7,s[40:43],s96 offen offset:1024 lds
s_add_u32 s96,s96,65536
s_add_u32 m0,s97,106496
s_nop 0
buffer_load_dwordx4 v7,s[36:39],s96 offen offset:0 lds
buffer_load_dwordx4 v7,s[40:43],s96 offen offset:1024 lds
s_add_u32 s96,s96,65536
ds_read_b64_tr_b16 a[112:113],v9 offset:49152
ds_read_b64_tr_b16 a[114:115],v9 offset:49664
ds_read_b64_tr_b16 a[116:117],v9 offset:49160
ds_read_b64_tr_b16 a[118:119],v9 offset:49672
ds_read_b64_tr_b16 a[120:121],v9 offset:57344
ds_read_b64_tr_b16 a[122:123],v9 offset:57856
ds_read_b64_tr_b16 a[124:125],v9 offset:57352
ds_read_b64_tr_b16 a[126:127],v9 offset:57864
s_waitcnt lgkmcnt(0)
s_waitcnt vmcnt(24)
s_barrier
s_add_u32 m0,s97,114688
s_nop 0
buffer_load_dwordx4 v7,s[36:39],s96 offen offset:0 lds
buffer_load_dwordx4 v7,s[40:43],s96 offen offset:1024 lds
s_add_u32 s96,s96,65536
s_add_u32 m0,s97,122880
s_nop 0
buffer_load_dwordx4 v7,s[36:39],s96 offen offset:0 lds
buffer_load_dwordx4 v7,s[40:43],s96 offen offset:1024 lds
s_add_u32 s96,s96,65536
ds_read_b64_tr_b16 a[128:129],v8 offset:0
ds_read_b64_tr_b16 a[130:131],v8 offset:512
ds_read_b64_tr_b16 a[132:133],v8 offset:8
ds_read_b64_tr_b16 a[134:135],v8 offset:520
ds_read_b64_tr_b16 a[136:137],v8 offset:8192
ds_read_b64_tr_b16 a[138:139],v8 offset:8704
ds_read_b64_tr_b16 a[140:141],v8 offset:8200
ds_read_b64_tr_b16 a[142:143],v8 offset:8712
s_waitcnt lgkmcnt(0)
s_waitcnt vmcnt(24)
s_barrier
ds_read_b64_tr_b16 a[144:145],v8 offset:16384
ds_read_b64_tr_b16 a[146:147],v8 offset:16896
ds_read_b64_tr_b16 a[148:149],v8 offset:16392
ds_read_b64_tr_b16 a[150:151],v8 offset:16904
ds_read_b64_tr_b16 a[152:153],v8 offset:24576
ds_read_b64_tr_b16 a[154:155],v8 offset:25088
ds_read_b64_tr_b16 a[156:157],v8 offset:24584
ds_read_b64_tr_b16 a[158:159],v8 offset:25096
s_waitcnt lgkmcnt(0)
s_waitcnt vmcnt(20)
s_barrier
ds_read_b64_tr_b16 a[160:161],v8 offset:32768
ds_read_b64_tr_b16 a[162:163],v8 offset:33280
ds_read_b64_tr_b16 a[164:165],v8 offset:32776
ds_read_b64_tr_b16 a[166:167],v8 offset:33288
ds_read_b64_tr_b16 a[168:169],v8 offset:40960
ds_read_b64_tr_b16 a[170:171],v8 offset:41472
ds_read_b64_tr_b16 a[172:173],v8 offset:40968
ds_read_b64_tr_b16 a[174:175],v8 offset:41480
s_waitcnt lgkmcnt(0)
s_waitcnt vmcnt(16)
s_barrier
ds_read_b64_tr_b16 a[176:177],v8 offset:49152
ds_read_b64_tr_b16 a[178:179],v8 offset:49664
ds_read_b64_tr_b16 a[180:181],v8 offset:49160
ds_read_b64_tr_b16 a[182:183],v8 offset:49672
ds_read_b64_tr_b16 a[184:185],v8 offset:57344
ds_read_b64_tr_b16 a[186:187],v8 offset:57856
ds_read_b64_tr_b16 a[188:189],v8 offset:57352
ds_read_b64_tr_b16 a[190:191],v8 offset:57864
s_waitcnt lgkmcnt(0)
s_waitcnt vmcnt(12)
s_barrier
ds_read_b64_tr_b16 a[192:193],v9 offset:0
ds_read_b64_tr_b16 a[194:195],v9 offset:512
ds_read_b64_tr_b16 a[196:197],v9 offset:8
ds_read_b64_tr_b16 a[198:199],v9 offset:520
ds_read_b64_tr_b16 a[200:201],v9 offset:8192
ds_read_b64_tr_b16 a[202:203],v9 offset:8704
ds_read_b64_tr_b16 a[204:205],v9 offset:8200
ds_read_b64_tr_b16 a[206:207],v9 offset:8712
s_waitcnt lgkmcnt(0)
s_waitcnt vmcnt(8)
s_barrier
ds_read_b64_tr_b16 a[208:209],v9 offset:16384
ds_read_b64_tr_b16 a[210:211],v9 offset:16896
ds_read_b64_tr_b16 a[212:213],v9 offset:16392
ds_read_b64_tr_b16 a[214:215],v9 offset:16904
ds_read_b64_tr_b16 a[216:217],v9 offset:24576
ds_read_b64_tr_b16 a[218:219],v9 offset:25088
ds_read_b64_tr_b16 a[220:221],v9 offset:24584
ds_read_b64_tr_b16 a[222:223],v9 offset:25096
s_waitcnt lgkmcnt(0)
s_waitcnt vmcnt(4)
s_barrier
ds_read_b64_tr_b16 a[224:225],v9 offset:32768
ds_read_b64_tr_b16 a[226:227],v9 offset:33280
ds_read_b64_tr_b16 a[228:229],v9 offset:32776
ds_read_b64_tr_b16 a[230:231],v9 offset:33288
ds_read_b64_tr_b16 a[232:233],v9 offset:40960
ds_read_b64_tr_b16 a[234:235],v9 offset:41472
ds_read_b64_tr_b16 a[236:237],v9 offset:40968
ds_read_b64_tr_b16 a[238:239],v9 offset:41480
s_waitcnt lgkmcnt(0)
s_waitcnt vmcnt(0)
s_barrier
ds_read_b64_tr_b16 a[240:241],v9 offset:49152
ds_read_b64_tr_b16 a[242:243],v9 offset:49664
ds_read_b64_tr_b16 a[244:245],v9 offset:49160
ds_read_b64_tr_b16 a[246:247],v9 offset:49672
ds_read_b64_tr_b16 a[248:249],v9 offset:57344
ds_read_b64_tr_b16 a[250:251],v9 offset:57856
ds_read_b64_tr_b16 a[252:253],v9 offset:57352
ds_read_b64_tr_b16 a[254:255],v9 offset:57864
s_waitcnt lgkmcnt(0)
s_barrier
s_lshl2_add_u32 s50,s27,s26
s_and_b32 s51,s50,6
s_lshl_b32 s51,s51,1
v_add_u32 v3,s51,v1
v_bfi_b32 v3,15,v3,v1
v_lshlrev_b32 v3,2,v3
v_cmp_gt_u32 vcc,32,v1
v_mov_b32 v15,0x7fff0000
s_nop 0
v_cndmask_b32 v15,v15,v3,vcc
s_mul_i32 s48,s50,1152
s_add_u32 s68,s48,4681728
s_lshl2_add_u32 s49,s27,96
v_lshlrev_b32 v7,3,v1
s_mov_b32 s30,593920
s_mul_i32 s57,s28,296960
s_mov_b32 s56,76021760
s_cmp_lg_u32 s25,0
s_cmov_b32 s30,296960
s_cmov_b32 s57,81960960
s_cmov_b32 s56,81960960
s_lshl_b32 s31,s30,1
s_waitcnt lgkmcnt(0)
s_add_u32 s54,s52,0x7f4a7c15
s_addc_u32 s55,s53,0x9e3779b9
s_lshl_b32 s67,s49,5
s_add_u32 s67,s67,4898816
s_sub_u32 s51,s67,s68
s_add_u32 s92,s32,s51
s_addc_u32 s93,s33,0x10000
s_sub_u32 s92,s92,s30
s_subb_u32 s93,s93,0
s_cmp_eq_u32 s26,0
s_cselect_b32 s94,1,0
s_mov_b32 s95,0x20000
v_mov_b32 v16,s54
v_mov_b32 v17,s55
v_cmp_gt_u32 vcc,12,v1
v_mul_u32_u24 v3,296960,v1
v_mov_b32 v12,0x7fff0000
v_cndmask_b32 v3,v12,v3,vcc
s_add_u32 s51,s67,s56
buffer_store_dwordx2 v[16:17],v3,s[0:3],s51 offen sc1
s_sub_u32 s67,s68,296960
s_add_u32 s68,s68,s57
s_sub_u32 s68,s68,s31
s_sub_u32 s62,255,s28
s_lshr_b32 s62,s62,2
s_mov_b32 s49,s57
s_cmp_lg_u32 s25,0
s_cmov_b32 s62,-1
s_cmov_b32 s49,76021760
s_add_u32 s48,s48,s49
s_add_u32 s71,s48,4612096
s_add_u32 s63,s49,4648960
s_add_u32 s66,s49,4898816
s_mul_i32 s64,s26,8192
s_add_u32 s65,s64,65536
s_xor_b32 s65,s65,s64
s_add_u32 s63,s63,s64
v_mov_b32 v4,v2
v_add_u32 v6,4096,v2
v_and_b32 v14,15,v1
v_lshlrev_b32 v14,2,v14
s_mov_b32 s48,32
v_add_u32 v14,s48,v14
v_lshrrev_b32 v14,2,v14
v_lshlrev_b32 v14,7,v14
v_lshrrev_b32 v3,4,v1
v_mul_u32_u24 v3,s30,v3
v_add_u32 v14,v3,v14
v_cmp_gt_u32 vcc,32,v1
v_mov_b32 v3,0x7fff0000
s_nop 0
v_cndmask_b32 v14,v3,v14,vcc
v_cndmask_b32 v16,0,v16,vcc
v_cndmask_b32 v17,0,v17,vcc
s_add_u32 s44,s20,0x0
s_addc_u32 s45,s21,0x0
s_and_b32 s45,s45,0xffff
s_mov_b32 s46,0x400000
)#"
R"#(
s_mov_b32 s47,0x20000
v_mov_b32 v64,0
v_mov_b32 v65,0
v_mov_b32 v66,0
v_mov_b32 v67,0
s_lshl3_add_u32 s48,s28,s27
s_lshl2_add_u32 s48,s48,s26
s_lshl_b32 s48,s48,16
s_mov_b32 s49,15
.L_wz:
buffer_store_dwordx4 v[64:67],v2,s[44:47],s48 offen offset:0 sc1
buffer_store_dwordx4 v[64:67],v2,s[44:47],s48 offen offset:1024 sc1
buffer_store_dwordx4 v[64:67],v2,s[44:47],s48 offen offset:2048 sc1
buffer_store_dwordx4 v[64:67],v2,s[44:47],s48 offen offset:3072 sc1
s_add_u32 s48,s48,4096
s_sub_u32 s49,s49,1
s_cbranch_scc0 .L_wz
s_mov_b64 s[8:9],s[62:63]
s_mov_b64 s[10:11],s[64:65]
s_mov_b64 s[12:13],s[66:67]
s_mov_b64 s[14:15],s[68:69]
s_mov_b64 s[16:17],s[70:71]
s_mov_b64 s[18:19],s[72:73]
s_mov_b64 s[20:21],s[74:75]
s_mov_b32 s62,0
s_mov_b64 vcc,0
s_mov_b64 exec,0
s_branch .L_wgo
.L_wbk:
s_mov_b64 exec,-1
s_mov_b64 s[62:63],s[8:9]
s_mov_b64 s[64:65],s[10:11]
s_mov_b64 s[66:67],s[12:13]
s_mov_b64 s[68:69],s[14:15]
s_mov_b64 s[70:71],s[16:17]
s_mov_b64 s[72:73],s[18:19]
s_mov_b64 s[74:75],s[20:21]
s_waitcnt vmcnt(0) lgkmcnt(0)
.L_wnd:
s_cmp_lg_u32 s26,1
s_cbranch_scc1 .L_skwa
s_mov_b32 s48,20000
s_branch .L_rfwa
.L_rlwa:
s_sleep 2
.L_rfwa:
buffer_load_dwordx2 v[12:13],v14,s[0:3],s66 offen sc1
s_sub_u32 s48,s48,1
s_cbranch_scc1 .L_end
s_waitcnt vmcnt(0)
v_cmp_ne_u64 vcc,v[16:17],v[12:13]
s_cbranch_vccnz .L_rlwa
.L_skwa:
s_waitcnt vmcnt(0)
s_barrier
s_add_u32 s66,s66,s31
s_add_u32 m0,s64,0
s_nop 0
buffer_load_dwordx4 v2,s[32:35],s63 offen offset:0 sc1 lds
buffer_load_dwordx4 v2,s[32:35],s63 offen offset:1024 sc1 lds
buffer_load_dwordx4 v2,s[32:35],s63 offen offset:2048 sc1 lds
buffer_load_dwordx4 v2,s[32:35],s63 offen offset:3072 sc1 lds
s_add_u32 m0,s64,4096
s_nop 0
buffer_load_dwordx4 v6,s[32:35],s63 offen offset:0 sc1 lds
buffer_load_dwordx4 v6,s[32:35],s63 offen offset:1024 sc1 lds
buffer_load_dwordx4 v6,s[32:35],s63 offen offset:2048 sc1 lds
buffer_load_dwordx4 v6,s[32:35],s63 offen offset:3072 sc1 lds
s_add_u32 s63,s63,s30
buffer_load_dwordx4 v[48:51],v2,s[32:35],s71 offen sc1
s_add_u32 s71,s71,s30
buffer_load_dwordx4 v[52:55],v2,s[32:35],s71 offen sc1
s_add_u32 s71,s71,s30
s_add_u32 m0,s64,32768
s_nop 0
buffer_load_dwordx4 v2,s[32:35],s63 offen offset:0 sc1 lds
buffer_load_dwordx4 v2,s[32:35],s63 offen offset:1024 sc1 lds
buffer_load_dwordx4 v2,s[32:35],s63 offen offset:2048 sc1 lds
buffer_load_dwordx4 v2,s[32:35],s63 offen offset:3072 sc1 lds
s_add_u32 m0,s64,36864
s_nop 0
buffer_load_dwordx4 v6,s[32:35],s63 offen offset:0 sc1 lds
buffer_load_dwordx4 v6,s[32:35],s63 offen offset:1024 sc1 lds
buffer_load_dwordx4 v6,s[32:35],s63 offen offset:2048 sc1 lds
buffer_load_dwordx4 v6,s[32:35],s63 offen offset:3072 sc1 lds
s_add_u32 s63,s63,s30
s_xor_b32 s64,s64,s65
s_cmp_lg_u32 s26,1
s_cbranch_scc1 .L_skwc
s_mov_b32 s48,20000
s_branch .L_rfwc
.L_rlwc:
s_sleep 2
.L_rfwc:
buffer_load_dwordx2 v[12:13],v14,s[0:3],s66 offen sc1
s_sub_u32 s48,s48,1
s_cbranch_scc1 .L_end
s_waitcnt vmcnt(0)
v_cmp_ne_u64 vcc,v[16:17],v[12:13]
s_cbranch_vccnz .L_rlwc
.L_skwc:
s_waitcnt vmcnt(0)
s_barrier
s_add_u32 s66,s66,s31
s_add_u32 m0,s64,0
s_nop 0
buffer_load_dwordx4 v2,s[32:35],s63 offen offset:0 sc1 lds
buffer_load_dwordx4 v2,s[32:35],s63 offen offset:1024 sc1 lds
buffer_load_dwordx4 v2,s[32:35],s63 offen offset:2048 sc1 lds
buffer_load_dwordx4 v2,s[32:35],s63 offen offset:3072 sc1 lds
s_add_u32 m0,s64,4096
s_nop 0
buffer_load_dwordx4 v6,s[32:35],s63 offen offset:0 sc1 lds
s_barrier
ds_read_b128 v[128:131],v4 offset:0
ds_read_b128 v[132:135],v4 offset:1024
ds_read_b128 v[136:139],v4 offset:2048
ds_read_b128 v[140:143],v4 offset:3072
ds_read_b128 v[144:147],v4 offset:4096
ds_read_b128 v[148:151],v4 offset:5120
ds_read_b128 v[152:155],v4 offset:6144
ds_read_b128 v[156:159],v4 offset:7168
ds_read_b128 v[160:163],v4 offset:8192
ds_read_b128 v[164:167],v4 offset:9216
ds_read_b128 v[168:171],v4 offset:10240
ds_read_b128 v[172:175],v4 offset:11264
v_lshlrev_b32 v32,16,v48
v_and_b32 v33,0xffff0000,v48
v_lshlrev_b32 v34,16,v49
v_and_b32 v35,0xffff0000,v49
v_lshlrev_b32 v36,16,v50
v_and_b32 v37,0xffff0000,v50
v_lshlrev_b32 v38,16,v51
v_and_b32 v39,0xffff0000,v51
.L_wgo:
s_cmp_eq_u32 s26,1
s_cbranch_scc1 .L_wv1
s_cmp_eq_u32 s26,2
s_cbranch_scc1 .L_wv2
s_cmp_eq_u32 s26,3
s_cbranch_scc1 .L_wv3
.p2align 6
.L_wv0:
.L_wl_0:
s_waitcnt lgkmcnt(4)
v_mfma_f32_16x16x32_bf16 v[32:35],a[0:3],v[128:131],v[32:35]
ds_read_b128 v[176:179],v4 offset:12288
v_mfma_f32_16x16x32_bf16 v[32:35],a[8:11],v[132:135],v[32:35]
buffer_load_dwordx4 v6,s[32:35],s63 offen offset:1024 sc1 lds
v_mfma_f32_16x16x32_bf16 v[32:35],a[16:19],v[136:139],v[32:35]
ds_read_b128 v[180:183],v4 offset:13312
s_add_u32 s68,s68,s30
v_mfma_f32_16x16x32_bf16 v[32:35],a[24:27],v[140:143],v[32:35]
v_cvt_pk_bf16_f32 v56,v40,v41
v_cvt_pk_bf16_f32 v57,v42,v43
v_mfma_f32_16x16x32_bf16 v[36:39],a[28:31],v[140:143],v[36:39]
ds_read_b128 v[184:187],v4 offset:14336
v_cvt_pk_bf16_f32 v58,v44,v45
v_mfma_f32_16x16x32_bf16 v[36:39],a[20:23],v[136:139],v[36:39]
v_cvt_pk_bf16_f32 v59,v46,v47
v_add_f32 v60,v40,v41
v_mfma_f32_16x16x32_bf16 v[36:39],a[12:15],v[132:135],v[36:39]
ds_read_b128 v[188:191],v4 offset:15360
v_mul_f32 v61,v40,v40
v_mfma_f32_16x16x32_bf16 v[36:39],a[4:7],v[128:131],v[36:39]
v_fmac_f32 v61,v41,v41
v_add_f32 v60,v60,v42
v_mfma_f32_16x16x32_bf16 v[36:39],a[36:39],v[144:147],v[36:39]
ds_read_b128 v[128:131],v4 offset:16384
v_fmac_f32 v61,v42,v42
v_mfma_f32_16x16x32_bf16 v[36:39],a[44:47],v[148:151],v[36:39]
buffer_store_dwordx4 v[56:59],v2,s[32:35],s68 offen sc1
v_mfma_f32_16x16x32_bf16 v[36:39],a[52:55],v[152:155],v[36:39]
ds_read_b128 v[132:135],v4 offset:17408
v_add_f32 v60,v60,v43
v_mfma_f32_16x16x32_bf16 v[36:39],a[60:63],v[156:159],v[36:39]
v_fmac_f32 v61,v43,v43
v_add_f32 v60,v60,v44
v_mfma_f32_16x16x32_bf16 v[32:35],a[56:59],v[156:159],v[32:35]
ds_read_b128 v[136:139],v4 offset:18432
v_fmac_f32 v61,v44,v44
v_mfma_f32_16x16x32_bf16 v[32:35],a[48:51],v[152:155],v[32:35]
v_add_f32 v60,v60,v45
v_fmac_f32 v61,v45,v45
v_mfma_f32_16x16x32_bf16 v[32:35],a[40:43],v[148:151],v[32:35]
ds_read_b128 v[140:143],v4 offset:19456
v_add_f32 v60,v60,v46
v_mfma_f32_16x16x32_bf16 v[32:35],a[32:35],v[144:147],v[32:35]
v_fmac_f32 v61,v46,v46
s_waitcnt lgkmcnt(4)
v_mfma_f32_16x16x32_bf16 v[32:35],a[64:67],v[160:163],v[32:35]
ds_read_b128 v[144:147],v4 offset:20480
v_add_f32 v60,v60,v47
v_mfma_f32_16x16x32_bf16 v[32:35],a[72:75],v[164:167],v[32:35]
v_fmac_f32 v61,v47,v47
v_mfma_f32_16x16x32_bf16 v[32:35],a[80:83],v[168:171],v[32:35]
ds_read_b128 v[148:151],v4 offset:21504
v_mfma_f32_16x16x32_bf16 v[32:35],a[88:91],v[172:175],v[32:35]
v_permlane16_swap_b32 v60,v61
v_mfma_f32_16x16x32_bf16 v[36:39],a[92:95],v[172:175],v[36:39]
ds_read_b128 v[152:155],v4 offset:22528
v_add_f32 v60,v60,v61
v_mfma_f32_16x16x32_bf16 v[36:39],a[84:87],v[168:171],v[36:39]
buffer_load_dwordx4 v[48:51],v2,s[32:35],s71 offen sc1
v_mfma_f32_16x16x32_bf16 v[36:39],a[76:79],v[164:167],v[36:39]
ds_read_b128 v[156:159],v4 offset:23552
s_add_u32 s71,s71,s30
v_mfma_f32_16x16x32_bf16 v[36:39],a[68:71],v[160:163],v[36:39]
v_permlane32_swap_b32 v60,v61
v_mfma_f32_16x16x32_bf16 v[36:39],a[100:103],v[176:179],v[36:39]
ds_read_b128 v[160:163],v4 offset:24576
v_add_f32 v60,v60,v61
v_mfma_f32_16x16x32_bf16 v[36:39],a[108:111],v[180:183],v[36:39]
buffer_store_dword v60,v15,s[32:35],s68 offen offset:1024 sc1
v_mfma_f32_16x16x32_bf16 v[36:39],a[116:119],v[184:187],v[36:39]
ds_read_b128 v[164:167],v4 offset:25600
v_mfma_f32_16x16x32_bf16 v[36:39],a[124:127],v[188:191],v[36:39]
v_mfma_f32_16x16x32_bf16 v[32:35],a[120:123],v[188:191],v[32:35]
ds_read_b128 v[168:171],v4 offset:26624
v_mfma_f32_16x16x32_bf16 v[32:35],a[112:115],v[184:187],v[32:35]
buffer_load_dwordx4 v6,s[32:35],s63 offen offset:2048 sc1 lds
v_mfma_f32_16x16x32_bf16 v[32:35],a[104:107],v[180:183],v[32:35]
ds_read_b128 v[172:175],v4 offset:27648
v_mfma_f32_16x16x32_bf16 v[32:35],a[96:99],v[176:179],v[32:35]
s_waitcnt lgkmcnt(4)
v_mfma_f32_16x16x32_bf16 v[32:35],a[128:131],v[128:131],v[32:35]
ds_read_b128 v[176:179],v4 offset:28672
v_mfma_f32_16x16x32_bf16 v[32:35],a[136:139],v[132:135],v[32:35]
buffer_load_dwordx4 v6,s[32:35],s63 offen offset:3072 sc1 lds
v_mfma_f32_16x16x32_bf16 v[32:35],a[144:147],v[136:139],v[32:35]
ds_read_b128 v[180:183],v4 offset:29696
s_add_u32 s63,s63,s30
v_mfma_f32_16x16x32_bf16 v[32:35],a[152:155],v[140:143],v[32:35]
v_mfma_f32_16x16x32_bf16 v[36:39],a[156:159],v[140:143],v[36:39]
ds_read_b128 v[184:187],v4 offset:30720
v_mfma_f32_16x16x32_bf16 v[36:39],a[148:151],v[136:139],v[36:39]
v_mfma_f32_16x16x32_bf16 v[36:39],a[140:143],v[132:135],v[36:39]
ds_read_b128 v[188:191],v4 offset:31744
v_mfma_f32_16x16x32_bf16 v[36:39],a[132:135],v[128:131],v[36:39]
s_waitcnt vmcnt(12)
s_barrier
v_mfma_f32_16x16x32_bf16 v[36:39],a[164:167],v[144:147],v[36:39]
ds_read_b128 v[128:131],v4 offset:32768
v_lshlrev_b32 v40,16,v52
v_mfma_f32_16x16x32_bf16 v[36:39],a[172:175],v[148:151],v[36:39]
buffer_store_dwordx2 v[16:17],v2,s[92:95],s68 idxen sc1
v_mfma_f32_16x16x32_bf16 v[36:39],a[180:183],v[152:155],v[36:39]
ds_read_b128 v[132:135],v4 offset:33792
v_and_b32 v41,0xffff0000,v52
v_mfma_f32_16x16x32_bf16 v[36:39],a[188:191],v[156:159],v[36:39]
v_lshlrev_b32 v42,16,v53
v_and_b32 v43,0xffff0000,v53
v_mfma_f32_16x16x32_bf16 v[32:35],a[184:187],v[156:159],v[32:35]
ds_read_b128 v[136:139],v4 offset:34816
s_add_u32 m0,s64,32768
v_mfma_f32_16x16x32_bf16 v[32:35],a[176:179],v[152:155],v[32:35]
buffer_load_dwordx4 v2,s[32:35],s63 offen offset:0 sc1 lds
v_mfma_f32_16x16x32_bf16 v[32:35],a[168:171],v[148:151],v[32:35]
ds_read_b128 v[140:143],v4 offset:35840
v_lshlrev_b32 v44,16,v54
v_mfma_f32_16x16x32_bf16 v[32:35],a[160:163],v[144:147],v[32:35]
v_and_b32 v45,0xffff0000,v54
s_waitcnt lgkmcnt(4)
v_mfma_f32_16x16x32_bf16 v[32:35],a[192:195],v[160:163],v[32:35]
ds_read_b128 v[144:147],v4 offset:36864
v_lshlrev_b32 v46,16,v55
v_mfma_f32_16x16x32_bf16 v[32:35],a[200:203],v[164:167],v[32:35]
buffer_load_dwordx4 v2,s[32:35],s63 offen offset:1024 sc1 lds
v_mfma_f32_16x16x32_bf16 v[32:35],a[208:211],v[168:171],v[32:35]
ds_read_b128 v[148:151],v4 offset:37888
v_and_b32 v47,0xffff0000,v55
v_mfma_f32_16x16x32_bf16 v[32:35],a[216:219],v[172:175],v[32:35]
v_mfma_f32_16x16x32_bf16 v[36:39],a[220:223],v[172:175],v[36:39]
ds_read_b128 v[152:155],v4 offset:38912
v_mfma_f32_16x16x32_bf16 v[36:39],a[212:215],v[168:171],v[36:39]
buffer_load_dwordx4 v2,s[32:35],s63 offen offset:2048 sc1 lds
v_mfma_f32_16x16x32_bf16 v[36:39],a[204:207],v[164:167],v[36:39]
ds_read_b128 v[156:159],v4 offset:39936
v_mfma_f32_16x16x32_bf16 v[36:39],a[196:199],v[160:163],v[36:39]
v_mfma_f32_16x16x32_bf16 v[36:39],a[228:231],v[176:179],v[36:39]
ds_read_b128 v[160:163],v4 offset:40960
v_mfma_f32_16x16x32_bf16 v[36:39],a[236:239],v[180:183],v[36:39]
buffer_load_dwordx4 v2,s[32:35],s63 offen offset:3072 sc1 lds
v_mfma_f32_16x16x32_bf16 v[36:39],a[244:247],v[184:187],v[36:39]
ds_read_b128 v[164:167],v4 offset:41984
v_mfma_f32_16x16x32_bf16 v[36:39],a[252:255],v[188:191],v[36:39]
v_mfma_f32_16x16x32_bf16 v[32:35],a[248:251],v[188:191],v[32:35]
ds_read_b128 v[168:171],v4 offset:43008
s_add_u32 m0,s64,36864
v_mfma_f32_16x16x32_bf16 v[32:35],a[240:243],v[184:187],v[32:35]
buffer_load_dwordx4 v6,s[32:35],s63 offen offset:0 sc1 lds
v_mfma_f32_16x16x32_bf16 v[32:35],a[232:235],v[180:183],v[32:35]
ds_read_b128 v[172:175],v4 offset:44032
v_mfma_f32_16x16x32_bf16 v[32:35],a[224:227],v[176:179],v[32:35]
s_waitcnt lgkmcnt(4)
v_mfma_f32_16x16x32_bf16 v[40:43],a[0:3],v[128:131],v[40:43]
ds_read_b128 v[176:179],v4 offset:45056
v_mfma_f32_16x16x32_bf16 v[40:43],a[8:11],v[132:135],v[40:43]
buffer_load_dwordx4 v6,s[32:35],s63 offen offset:1024 sc1 lds
v_mfma_f32_16x16x32_bf16 v[40:43],a[16:19],v[136:139],v[40:43]
ds_read_b128 v[180:183],v4 offset:46080
s_add_u32 s68,s68,s30
v_mfma_f32_16x16x32_bf16 v[40:43],a[24:27],v[140:143],v[40:43]
v_cvt_pk_bf16_f32 v56,v32,v33
v_cvt_pk_bf16_f32 v57,v34,v35
v_mfma_f32_16x16x32_bf16 v[44:47],a[28:31],v[140:143],v[44:47]
ds_read_b128 v[184:187],v4 offset:47104
v_cvt_pk_bf16_f32 v58,v36,v37
v_mfma_f32_16x16x32_bf16 v[44:47],a[20:23],v[136:139],v[44:47]
v_cvt_pk_bf16_f32 v59,v38,v39
v_add_f32 v60,v32,v33
v_mfma_f32_16x16x32_bf16 v[44:47],a[12:15],v[132:135],v[44:47]
ds_read_b128 v[188:191],v4 offset:48128
v_mul_f32 v61,v32,v32
v_mfma_f32_16x16x32_bf16 v[44:47],a[4:7],v[128:131],v[44:47]
v_fmac_f32 v61,v33,v33
v_add_f32 v60,v60,v34
v_mfma_f32_16x16x32_bf16 v[44:47],a[36:39],v[144:147],v[44:47]
ds_read_b128 v[128:131],v4 offset:49152
v_fmac_f32 v61,v34,v34
v_mfma_f32_16x16x32_bf16 v[44:47],a[44:47],v[148:151],v[44:47]
buffer_store_dwordx4 v[56:59],v2,s[32:35],s68 offen sc1
v_mfma_f32_16x16x32_bf16 v[44:47],a[52:55],v[152:155],v[44:47]
ds_read_b128 v[132:135],v4 offset:50176
v_add_f32 v60,v60,v35
v_mfma_f32_16x16x32_bf16 v[44:47],a[60:63],v[156:159],v[44:47]
v_fmac_f32 v61,v35,v35
v_add_f32 v60,v60,v36
v_mfma_f32_16x16x32_bf16 v[40:43],a[56:59],v[156:159],v[40:43]
ds_read_b128 v[136:139],v4 offset:51200
v_fmac_f32 v61,v36,v36
v_mfma_f32_16x16x32_bf16 v[40:43],a[48:51],v[152:155],v[40:43]
v_add_f32 v60,v60,v37
v_fmac_f32 v61,v37,v37
v_mfma_f32_16x16x32_bf16 v[40:43],a[40:43],v[148:151],v[40:43]
ds_read_b128 v[140:143],v4 offset:52224
v_add_f32 v60,v60,v38
v_mfma_f32_16x16x32_bf16 v[40:43],a[32:35],v[144:147],v[40:43]
v_fmac_f32 v61,v38,v38
s_waitcnt lgkmcnt(4)
v_mfma_f32_16x16x32_bf16 v[40:43],a[64:67],v[160:163],v[40:43]
ds_read_b128 v[144:147],v4 offset:53248
v_add_f32 v60,v60,v39
v_mfma_f32_16x16x32_bf16 v[40:43],a[72:75],v[164:167],v[40:43]
v_fmac_f32 v61,v39,v39
v_mfma_f32_16x16x32_bf16 v[40:43],a[80:83],v[168:171],v[40:43]
ds_read_b128 v[148:151],v4 offset:54272
v_mfma_f32_16x16x32_bf16 v[40:43],a[88:91],v[172:175],v[40:43]
v_permlane16_swap_b32 v60,v61
v_mfma_f32_16x16x32_bf16 v[44:47],a[92:95],v[172:175],v[44:47]
ds_read_b128 v[152:155],v4 offset:55296
v_add_f32 v60,v60,v61
v_mfma_f32_16x16x32_bf16 v[44:47],a[84:87],v[168:171],v[44:47]
buffer_load_dwordx4 v[52:55],v2,s[32:35],s71 offen sc1
v_mfma_f32_16x16x32_bf16 v[44:47],a[76:79],v[164:167],v[44:47]
ds_read_b128 v[156:159],v4 offset:56320
s_add_u32 s71,s71,s30
v_mfma_f32_16x16x32_bf16 v[44:47],a[68:71],v[160:163],v[44:47]
v_permlane32_swap_b32 v60,v61
v_mfma_f32_16x16x32_bf16 v[44:47],a[100:103],v[176:179],v[44:47]
ds_read_b128 v[160:163],v4 offset:57344
v_add_f32 v60,v60,v61
v_mfma_f32_16x16x32_bf16 v[44:47],a[108:111],v[180:183],v[44:47]
buffer_store_dword v60,v15,s[32:35],s68 offen offset:1024 sc1
v_mfma_f32_16x16x32_bf16 v[44:47],a[116:119],v[184:187],v[44:47]
ds_read_b128 v[164:167],v4 offset:58368
v_mfma_f32_16x16x32_bf16 v[44:47],a[124:127],v[188:191],v[44:47]
v_mfma_f32_16x16x32_bf16 v[40:43],a[120:123],v[188:191],v[40:43]
ds_read_b128 v[168:171],v4 offset:59392
v_mfma_f32_16x16x32_bf16 v[40:43],a[112:115],v[184:187],v[40:43]
buffer_load_dwordx4 v6,s[32:35],s63 offen offset:2048 sc1 lds
v_mfma_f32_16x16x32_bf16 v[40:43],a[104:107],v[180:183],v[40:43]
ds_read_b128 v[172:175],v4 offset:60416
v_mfma_f32_16x16x32_bf16 v[40:43],a[96:99],v[176:179],v[40:43]
s_waitcnt lgkmcnt(4)
v_mfma_f32_16x16x32_bf16 v[40:43],a[128:131],v[128:131],v[40:43]
ds_read_b128 v[176:179],v4 offset:61440
v_mfma_f32_16x16x32_bf16 v[40:43],a[136:139],v[132:135],v[40:43]
buffer_load_dwordx4 v6,s[32:35],s63 offen offset:3072 sc1 lds
v_mfma_f32_16x16x32_bf16 v[40:43],a[144:147],v[136:139],v[40:43]
ds_read_b128 v[180:183],v4 offset:62464
s_add_u32 s63,s63,s30
v_mfma_f32_16x16x32_bf16 v[40:43],a[152:155],v[140:143],v[40:43]
s_xor_b32 s64,s64,s65
v_mfma_f32_16x16x32_bf16 v[44:47],a[156:159],v[140:143],v[44:47]
ds_read_b128 v[184:187],v4 offset:63488
v_mfma_f32_16x16x32_bf16 v[44:47],a[148:151],v[136:139],v[44:47]
v_mfma_f32_16x16x32_bf16 v[44:47],a[140:143],v[132:135],v[44:47]
ds_read_b128 v[188:191],v4 offset:64512
v_xor_b32 v4,65536,v4
v_mfma_f32_16x16x32_bf16 v[44:47],a[132:135],v[128:131],v[44:47]
s_waitcnt vmcnt(12)
s_barrier
v_mfma_f32_16x16x32_bf16 v[44:47],a[164:167],v[144:147],v[44:47]
ds_read_b128 v[128:131],v4 offset:0
v_lshlrev_b32 v32,16,v48
v_mfma_f32_16x16x32_bf16 v[44:47],a[172:175],v[148:151],v[44:47]
buffer_store_dwordx2 v[16:17],v2,s[92:95],s68 idxen sc1
v_mfma_f32_16x16x32_bf16 v[44:47],a[180:183],v[152:155],v[44:47]
ds_read_b128 v[132:135],v4 offset:1024
v_and_b32 v33,0xffff0000,v48
v_mfma_f32_16x16x32_bf16 v[44:47],a[188:191],v[156:159],v[44:47]
v_lshlrev_b32 v34,16,v49
v_and_b32 v35,0xffff0000,v49
v_mfma_f32_16x16x32_bf16 v[40:43],a[184:187],v[156:159],v[40:43]
ds_read_b128 v[136:139],v4 offset:2048
s_add_u32 m0,s64,0
v_mfma_f32_16x16x32_bf16 v[40:43],a[176:179],v[152:155],v[40:43]
buffer_load_dwordx4 v2,s[32:35],s63 offen offset:0 sc1 lds
v_mfma_f32_16x16x32_bf16 v[40:43],a[168:171],v[148:151],v[40:43]
ds_read_b128 v[140:143],v4 offset:3072
v_lshlrev_b32 v36,16,v50
v_mfma_f32_16x16x32_bf16 v[40:43],a[160:163],v[144:147],v[40:43]
v_and_b32 v37,0xffff0000,v50
s_waitcnt lgkmcnt(4)
v_mfma_f32_16x16x32_bf16 v[40:43],a[192:195],v[160:163],v[40:43]
ds_read_b128 v[144:147],v4 offset:4096
v_lshlrev_b32 v38,16,v51
v_mfma_f32_16x16x32_bf16 v[40:43],a[200:203],v[164:167],v[40:43]
buffer_load_dwordx4 v2,s[32:35],s63 offen offset:1024 sc1 lds
v_mfma_f32_16x16x32_bf16 v[40:43],a[208:211],v[168:171],v[40:43]
ds_read_b128 v[148:151],v4 offset:5120
v_and_b32 v39,0xffff0000,v51
v_mfma_f32_16x16x32_bf16 v[40:43],a[216:219],v[172:175],v[40:43]
v_mfma_f32_16x16x32_bf16 v[44:47],a[220:223],v[172:175],v[44:47]
ds_read_b128 v[152:155],v4 offset:6144
v_mfma_f32_16x16x32_bf16 v[44:47],a[212:215],v[168:171],v[44:47]
buffer_load_dwordx4 v2,s[32:35],s63 offen offset:2048 sc1 lds
v_mfma_f32_16x16x32_bf16 v[44:47],a[204:207],v[164:167],v[44:47]
ds_read_b128 v[156:159],v4 offset:7168
v_mfma_f32_16x16x32_bf16 v[44:47],a[196:199],v[160:163],v[44:47]
v_mfma_f32_16x16x32_bf16 v[44:47],a[228:231],v[176:179],v[44:47]
ds_read_b128 v[160:163],v4 offset:8192
v_mfma_f32_16x16x32_bf16 v[44:47],a[236:239],v[180:183],v[44:47]
buffer_load_dwordx4 v2,s[32:35],s63 offen offset:3072 sc1 lds
v_mfma_f32_16x16x32_bf16 v[44:47],a[244:247],v[184:187],v[44:47]
ds_read_b128 v[164:167],v4 offset:9216
v_mfma_f32_16x16x32_bf16 v[44:47],a[252:255],v[188:191],v[44:47]
v_mfma_f32_16x16x32_bf16 v[40:43],a[248:251],v[188:191],v[40:43]
ds_read_b128 v[168:171],v4 offset:10240
s_add_u32 m0,s64,4096
v_mfma_f32_16x16x32_bf16 v[40:43],a[240:243],v[184:187],v[40:43]
buffer_load_dwordx4 v6,s[32:35],s63 offen offset:0 sc1 lds
v_mfma_f32_16x16x32_bf16 v[40:43],a[232:235],v[180:183],v[40:43]
ds_read_b128 v[172:175],v4 offset:11264
s_sub_u32 s62,s62,1
v_mfma_f32_16x16x32_bf16 v[40:43],a[224:227],v[176:179],v[40:43]
s_cbranch_scc0 .L_wl_0
s_nop 1
s_add_u32 s68,s68,s30
s_nop 3
v_cvt_pk_bf16_f32 v56,v40,v41
v_cvt_pk_bf16_f32 v57,v42,v43
v_cvt_pk_bf16_f32 v58,v44,v45
v_cvt_pk_bf16_f32 v59,v46,v47
buffer_store_dwordx4 v[56:59],v2,s[32:35],s68 offen sc1
v_add_f32 v60,v40,v41
v_mul_f32 v61,v40,v40
v_fmac_f32 v61,v41,v41
v_add_f32 v60,v60,v42
v_fmac_f32 v61,v42,v42
v_add_f32 v60,v60,v43
v_fmac_f32 v61,v43,v43
v_add_f32 v60,v60,v44
v_fmac_f32 v61,v44,v44
v_add_f32 v60,v60,v45
v_fmac_f32 v61,v45,v45
v_add_f32 v60,v60,v46
v_fmac_f32 v61,v46,v46
v_add_f32 v60,v60,v47
v_fmac_f32 v61,v47,v47
s_nop 1
v_permlane16_swap_b32 v60,v61
v_add_f32 v60,v60,v61
s_nop 1
v_permlane32_swap_b32 v60,v61
v_add_f32 v60,v60,v61
buffer_store_dword v60,v15,s[32:35],s68 offen offset:1024 sc1
s_waitcnt vmcnt(10)
s_barrier
buffer_store_dwordx2 v[16:17],v2,s[92:95],s68 idxen sc1
s_add_u32 s68,s68,s30
s_waitcnt vmcnt(1)
s_barrier
buffer_store_dwordx2 v[16:17],v2,s[92:95],s68 idxen sc1
s_cbranch_execz .L_wbk
s_branch .L_end
.p2align 6
.L_wv1:
.L_wl_1:
s_waitcnt lgkmcnt(4)
v_mfma_f32_16x16x32_bf16 v[32:35],a[0:3],v[128:131],v[32:35]
ds_read_b128 v[176:179],v4 offset:12288
v_mfma_f32_16x16x32_bf16 v[32:35],a[8:11],v[132:135],v[32:35]
buffer_load_dwordx4 v6,s[32:35],s63 offen offset:1024 sc1 lds
v_mfma_f32_16x16x32_bf16 v[32:35],a[16:19],v[136:139],v[32:35]
ds_read_b128 v[180:183],v4 offset:13312
s_add_u32 s68,s68,s30
v_mfma_f32_16x16x32_bf16 v[32:35],a[24:27],v[140:143],v[32:35]
v_cvt_pk_bf16_f32 v56,v40,v41
)#"
R"#(
v_cvt_pk_bf16_f32 v57,v42,v43
v_mfma_f32_16x16x32_bf16 v[36:39],a[28:31],v[140:143],v[36:39]
ds_read_b128 v[184:187],v4 offset:14336
v_cvt_pk_bf16_f32 v58,v44,v45
v_mfma_f32_16x16x32_bf16 v[36:39],a[20:23],v[136:139],v[36:39]
v_cvt_pk_bf16_f32 v59,v46,v47
v_add_f32 v60,v40,v41
v_mfma_f32_16x16x32_bf16 v[36:39],a[12:15],v[132:135],v[36:39]
ds_read_b128 v[188:191],v4 offset:15360
v_mul_f32 v61,v40,v40
v_mfma_f32_16x16x32_bf16 v[36:39],a[4:7],v[128:131],v[36:39]
v_fmac_f32 v61,v41,v41
v_add_f32 v60,v60,v42
v_mfma_f32_16x16x32_bf16 v[36:39],a[36:39],v[144:147],v[36:39]
ds_read_b128 v[128:131],v4 offset:16384
v_fmac_f32 v61,v42,v42
v_mfma_f32_16x16x32_bf16 v[36:39],a[44:47],v[148:151],v[36:39]
buffer_store_dwordx4 v[56:59],v2,s[32:35],s68 offen sc1
v_mfma_f32_16x16x32_bf16 v[36:39],a[52:55],v[152:155],v[36:39]
ds_read_b128 v[132:135],v4 offset:17408
v_add_f32 v60,v60,v43
v_mfma_f32_16x16x32_bf16 v[36:39],a[60:63],v[156:159],v[36:39]
v_fmac_f32 v61,v43,v43
v_add_f32 v60,v60,v44
v_mfma_f32_16x16x32_bf16 v[32:35],a[56:59],v[156:159],v[32:35]
ds_read_b128 v[136:139],v4 offset:18432
v_fmac_f32 v61,v44,v44
v_mfma_f32_16x16x32_bf16 v[32:35],a[48:51],v[152:155],v[32:35]
v_add_f32 v60,v60,v45
v_fmac_f32 v61,v45,v45
v_mfma_f32_16x16x32_bf16 v[32:35],a[40:43],v[148:151],v[32:35]
ds_read_b128 v[140:143],v4 offset:19456
v_add_f32 v60,v60,v46
v_mfma_f32_16x16x32_bf16 v[32:35],a[32:35],v[144:147],v[32:35]
v_fmac_f32 v61,v46,v46
s_waitcnt lgkmcnt(4)
v_mfma_f32_16x16x32_bf16 v[32:35],a[64:67],v[160:163],v[32:35]
ds_read_b128 v[144:147],v4 offset:20480
v_add_f32 v60,v60,v47
v_mfma_f32_16x16x32_bf16 v[32:35],a[72:75],v[164:167],v[32:35]
v_fmac_f32 v61,v47,v47
v_mfma_f32_16x16x32_bf16 v[32:35],a[80:83],v[168:171],v[32:35]
ds_read_b128 v[148:151],v4 offset:21504
v_mfma_f32_16x16x32_bf16 v[32:35],a[88:91],v[172:175],v[32:35]
v_permlane16_swap_b32 v60,v61
v_mfma_f32_16x16x32_bf16 v[36:39],a[92:95],v[172:175],v[36:39]
ds_read_b128 v[152:155],v4 offset:22528
v_add_f32 v60,v60,v61
v_mfma_f32_16x16x32_bf16 v[36:39],a[84:87],v[168:171],v[36:39]
buffer_load_dwordx4 v[48:51],v2,s[32:35],s71 offen sc1
v_mfma_f32_16x16x32_bf16 v[36:39],a[76:79],v[164:167],v[36:39]
ds_read_b128 v[156:159],v4 offset:23552
s_add_u32 s71,s71,s30
v_mfma_f32_16x16x32_bf16 v[36:39],a[68:71],v[160:163],v[36:39]
v_permlane32_swap_b32 v60,v61
v_mfma_f32_16x16x32_bf16 v[36:39],a[100:103],v[176:179],v[36:39]
ds_read_b128 v[160:163],v4 offset:24576
v_add_f32 v60,v60,v61
v_mfma_f32_16x16x32_bf16 v[36:39],a[108:111],v[180:183],v[36:39]
buffer_store_dword v60,v15,s[32:35],s68 offen offset:1024 sc1
v_mfma_f32_16x16x32_bf16 v[36:39],a[116:119],v[184:187],v[36:39]
ds_read_b128 v[164:167],v4 offset:25600
v_mfma_f32_16x16x32_bf16 v[36:39],a[124:127],v[188:191],v[36:39]
v_mfma_f32_16x16x32_bf16 v[32:35],a[120:123],v[188:191],v[32:35]
ds_read_b128 v[168:171],v4 offset:26624
v_mfma_f32_16x16x32_bf16 v[32:35],a[112:115],v[184:187],v[32:35]
buffer_load_dwordx4 v6,s[32:35],s63 offen offset:2048 sc1 lds
v_mfma_f32_16x16x32_bf16 v[32:35],a[104:107],v[180:183],v[32:35]
ds_read_b128 v[172:175],v4 offset:27648
v_mfma_f32_16x16x32_bf16 v[32:35],a[96:99],v[176:179],v[32:35]
s_waitcnt lgkmcnt(4)
v_mfma_f32_16x16x32_bf16 v[32:35],a[128:131],v[128:131],v[32:35]
ds_read_b128 v[176:179],v4 offset:28672
v_mfma_f32_16x16x32_bf16 v[32:35],a[136:139],v[132:135],v[32:35]
buffer_load_dwordx4 v6,s[32:35],s63 offen offset:3072 sc1 lds
v_mfma_f32_16x16x32_bf16 v[32:35],a[144:147],v[136:139],v[32:35]
ds_read_b128 v[180:183],v4 offset:29696
s_add_u32 s63,s63,s30
v_mfma_f32_16x16x32_bf16 v[32:35],a[152:155],v[140:143],v[32:35]
v_mfma_f32_16x16x32_bf16 v[36:39],a[156:159],v[140:143],v[36:39]
ds_read_b128 v[184:187],v4 offset:30720
v_mfma_f32_16x16x32_bf16 v[36:39],a[148:151],v[136:139],v[36:39]
buffer_load_dwordx2 v[12:13],v14,s[0:3],s66 offen sc1
v_mfma_f32_16x16x32_bf16 v[36:39],a[140:143],v[132:135],v[36:39]
s_waitcnt vmcnt(12)
s_barrier
v_mfma_f32_16x16x32_bf16 v[36:39],a[132:135],v[128:131],v[36:39]
ds_read_b128 v[188:191],v4 offset:31744
v_mfma_f32_16x16x32_bf16 v[36:39],a[164:167],v[144:147],v[36:39]
ds_read_b128 v[128:131],v4 offset:32768
v_lshlrev_b32 v40,16,v52
v_mfma_f32_16x16x32_bf16 v[36:39],a[172:175],v[148:151],v[36:39]
v_and_b32 v41,0xffff0000,v52
v_lshlrev_b32 v42,16,v53
v_mfma_f32_16x16x32_bf16 v[36:39],a[180:183],v[152:155],v[36:39]
ds_read_b128 v[132:135],v4 offset:33792
v_and_b32 v43,0xffff0000,v53
v_mfma_f32_16x16x32_bf16 v[36:39],a[188:191],v[156:159],v[36:39]
v_lshlrev_b32 v44,16,v54
v_and_b32 v45,0xffff0000,v54
v_mfma_f32_16x16x32_bf16 v[32:35],a[184:187],v[156:159],v[32:35]
ds_read_b128 v[136:139],v4 offset:34816
s_add_u32 m0,s64,32768
v_mfma_f32_16x16x32_bf16 v[32:35],a[176:179],v[152:155],v[32:35]
buffer_load_dwordx4 v2,s[32:35],s63 offen offset:0 sc1 lds
v_mfma_f32_16x16x32_bf16 v[32:35],a[168:171],v[148:151],v[32:35]
ds_read_b128 v[140:143],v4 offset:35840
v_lshlrev_b32 v46,16,v55
v_mfma_f32_16x16x32_bf16 v[32:35],a[160:163],v[144:147],v[32:35]
v_and_b32 v47,0xffff0000,v55
s_waitcnt lgkmcnt(4)
v_mfma_f32_16x16x32_bf16 v[32:35],a[192:195],v[160:163],v[32:35]
ds_read_b128 v[144:147],v4 offset:36864
v_mfma_f32_16x16x32_bf16 v[32:35],a[200:203],v[164:167],v[32:35]
buffer_load_dwordx4 v2,s[32:35],s63 offen offset:1024 sc1 lds
v_mfma_f32_16x16x32_bf16 v[32:35],a[208:211],v[168:171],v[32:35]
ds_read_b128 v[148:151],v4 offset:37888
v_mfma_f32_16x16x32_bf16 v[32:35],a[216:219],v[172:175],v[32:35]
v_mfma_f32_16x16x32_bf16 v[36:39],a[220:223],v[172:175],v[36:39]
ds_read_b128 v[152:155],v4 offset:38912
v_mfma_f32_16x16x32_bf16 v[36:39],a[212:215],v[168:171],v[36:39]
buffer_load_dwordx4 v2,s[32:35],s63 offen offset:2048 sc1 lds
v_mfma_f32_16x16x32_bf16 v[36:39],a[204:207],v[164:167],v[36:39]
ds_read_b128 v[156:159],v4 offset:39936
v_mfma_f32_16x16x32_bf16 v[36:39],a[196:199],v[160:163],v[36:39]
v_mfma_f32_16x16x32_bf16 v[36:39],a[228:231],v[176:179],v[36:39]
ds_read_b128 v[160:163],v4 offset:40960
v_mfma_f32_16x16x32_bf16 v[36:39],a[236:239],v[180:183],v[36:39]
buffer_load_dwordx4 v2,s[32:35],s63 offen offset:3072 sc1 lds
v_mfma_f32_16x16x32_bf16 v[36:39],a[244:247],v[184:187],v[36:39]
ds_read_b128 v[164:167],v4 offset:41984
v_mfma_f32_16x16x32_bf16 v[36:39],a[252:255],v[188:191],v[36:39]
v_mfma_f32_16x16x32_bf16 v[32:35],a[248:251],v[188:191],v[32:35]
ds_read_b128 v[168:171],v4 offset:43008
s_add_u32 m0,s64,36864
v_mfma_f32_16x16x32_bf16 v[32:35],a[240:243],v[184:187],v[32:35]
buffer_load_dwordx4 v6,s[32:35],s63 offen offset:0 sc1 lds
v_mfma_f32_16x16x32_bf16 v[32:35],a[232:235],v[180:183],v[32:35]
ds_read_b128 v[172:175],v4 offset:44032
v_mfma_f32_16x16x32_bf16 v[32:35],a[224:227],v[176:179],v[32:35]
s_waitcnt lgkmcnt(4)
v_mfma_f32_16x16x32_bf16 v[40:43],a[0:3],v[128:131],v[40:43]
ds_read_b128 v[176:179],v4 offset:45056
v_mfma_f32_16x16x32_bf16 v[40:43],a[8:11],v[132:135],v[40:43]
buffer_load_dwordx4 v6,s[32:35],s63 offen offset:1024 sc1 lds
v_mfma_f32_16x16x32_bf16 v[40:43],a[16:19],v[136:139],v[40:43]
ds_read_b128 v[180:183],v4 offset:46080
s_add_u32 s68,s68,s30
v_mfma_f32_16x16x32_bf16 v[40:43],a[24:27],v[140:143],v[40:43]
v_cvt_pk_bf16_f32 v56,v32,v33
v_cvt_pk_bf16_f32 v57,v34,v35
v_mfma_f32_16x16x32_bf16 v[44:47],a[28:31],v[140:143],v[44:47]
ds_read_b128 v[184:187],v4 offset:47104
v_cvt_pk_bf16_f32 v58,v36,v37
v_mfma_f32_16x16x32_bf16 v[44:47],a[20:23],v[136:139],v[44:47]
v_cvt_pk_bf16_f32 v59,v38,v39
v_add_f32 v60,v32,v33
v_mfma_f32_16x16x32_bf16 v[44:47],a[12:15],v[132:135],v[44:47]
ds_read_b128 v[188:191],v4 offset:48128
v_mul_f32 v61,v32,v32
v_mfma_f32_16x16x32_bf16 v[44:47],a[4:7],v[128:131],v[44:47]
v_fmac_f32 v61,v33,v33
v_add_f32 v60,v60,v34
v_mfma_f32_16x16x32_bf16 v[44:47],a[36:39],v[144:147],v[44:47]
ds_read_b128 v[128:131],v4 offset:49152
v_fmac_f32 v61,v34,v34
v_mfma_f32_16x16x32_bf16 v[44:47],a[44:47],v[148:151],v[44:47]
buffer_store_dwordx4 v[56:59],v2,s[32:35],s68 offen sc1
v_mfma_f32_16x16x32_bf16 v[44:47],a[52:55],v[152:155],v[44:47]
ds_read_b128 v[132:135],v4 offset:50176
v_add_f32 v60,v60,v35
v_mfma_f32_16x16x32_bf16 v[44:47],a[60:63],v[156:159],v[44:47]
v_fmac_f32 v61,v35,v35
v_add_f32 v60,v60,v36
v_mfma_f32_16x16x32_bf16 v[40:43],a[56:59],v[156:159],v[40:43]
ds_read_b128 v[136:139],v4 offset:51200
v_fmac_f32 v61,v36,v36
v_mfma_f32_16x16x32_bf16 v[40:43],a[48:51],v[152:155],v[40:43]
v_add_f32 v60,v60,v37
v_fmac_f32 v61,v37,v37
v_mfma_f32_16x16x32_bf16 v[40:43],a[40:43],v[148:151],v[40:43]
ds_read_b128 v[140:143],v4 offset:52224
v_add_f32 v60,v60,v38
v_mfma_f32_16x16x32_bf16 v[40:43],a[32:35],v[144:147],v[40:43]
v_fmac_f32 v61,v38,v38
s_waitcnt lgkmcnt(4)
v_mfma_f32_16x16x32_bf16 v[40:43],a[64:67],v[160:163],v[40:43]
ds_read_b128 v[144:147],v4 offset:53248
v_add_f32 v60,v60,v39
v_mfma_f32_16x16x32_bf16 v[40:43],a[72:75],v[164:167],v[40:43]
v_fmac_f32 v61,v39,v39
v_mfma_f32_16x16x32_bf16 v[40:43],a[80:83],v[168:171],v[40:43]
ds_read_b128 v[148:151],v4 offset:54272
v_mfma_f32_16x16x32_bf16 v[40:43],a[88:91],v[172:175],v[40:43]
v_permlane16_swap_b32 v60,v61
v_mfma_f32_16x16x32_bf16 v[44:47],a[92:95],v[172:175],v[44:47]
ds_read_b128 v[152:155],v4 offset:55296
v_add_f32 v60,v60,v61
v_mfma_f32_16x16x32_bf16 v[44:47],a[84:87],v[168:171],v[44:47]
buffer_load_dwordx4 v[52:55],v2,s[32:35],s71 offen sc1
v_mfma_f32_16x16x32_bf16 v[44:47],a[76:79],v[164:167],v[44:47]
ds_read_b128 v[156:159],v4 offset:56320
s_add_u32 s71,s71,s30
v_mfma_f32_16x16x32_bf16 v[44:47],a[68:71],v[160:163],v[44:47]
v_permlane32_swap_b32 v60,v61
v_mfma_f32_16x16x32_bf16 v[44:47],a[100:103],v[176:179],v[44:47]
ds_read_b128 v[160:163],v4 offset:57344
v_add_f32 v60,v60,v61
v_mfma_f32_16x16x32_bf16 v[44:47],a[108:111],v[180:183],v[44:47]
buffer_store_dword v60,v15,s[32:35],s68 offen offset:1024 sc1
v_mfma_f32_16x16x32_bf16 v[44:47],a[116:119],v[184:187],v[44:47]
ds_read_b128 v[164:167],v4 offset:58368
v_mfma_f32_16x16x32_bf16 v[44:47],a[124:127],v[188:191],v[44:47]
v_mfma_f32_16x16x32_bf16 v[40:43],a[120:123],v[188:191],v[40:43]
ds_read_b128 v[168:171],v4 offset:59392
v_mfma_f32_16x16x32_bf16 v[40:43],a[112:115],v[184:187],v[40:43]
buffer_load_dwordx4 v6,s[32:35],s63 offen offset:2048 sc1 lds
v_mfma_f32_16x16x32_bf16 v[40:43],a[104:107],v[180:183],v[40:43]
ds_read_b128 v[172:175],v4 offset:60416
v_mfma_f32_16x16x32_bf16 v[40:43],a[96:99],v[176:179],v[40:43]
s_waitcnt lgkmcnt(4)
v_mfma_f32_16x16x32_bf16 v[40:43],a[128:131],v[128:131],v[40:43]
ds_read_b128 v[176:179],v4 offset:61440
v_mfma_f32_16x16x32_bf16 v[40:43],a[136:139],v[132:135],v[40:43]
buffer_load_dwordx4 v6,s[32:35],s63 offen offset:3072 sc1 lds
v_mfma_f32_16x16x32_bf16 v[40:43],a[144:147],v[136:139],v[40:43]
ds_read_b128 v[180:183],v4 offset:62464
s_waitcnt vmcnt(11)
v_mfma_f32_16x16x32_bf16 v[40:43],a[152:155],v[140:143],v[40:43]
v_cmp_ne_u64 vcc,v[16:17],v[12:13]
s_add_u32 s63,s63,s30
v_mfma_f32_16x16x32_bf16 v[44:47],a[156:159],v[140:143],v[44:47]
ds_read_b128 v[184:187],v4 offset:63488
s_cbranch_vccnz .L_rtw1_1
.L_bkw1_1:
v_mfma_f32_16x16x32_bf16 v[44:47],a[148:151],v[136:139],v[44:47]
s_add_u32 s66,s66,s31
s_xor_b32 s64,s64,s65
v_mfma_f32_16x16x32_bf16 v[44:47],a[140:143],v[132:135],v[44:47]
ds_read_b128 v[188:191],v4 offset:64512
s_barrier
v_mfma_f32_16x16x32_bf16 v[44:47],a[132:135],v[128:131],v[44:47]
v_xor_b32 v4,65536,v4
v_mfma_f32_16x16x32_bf16 v[44:47],a[164:167],v[144:147],v[44:47]
ds_read_b128 v[128:131],v4 offset:0
v_lshlrev_b32 v32,16,v48
v_mfma_f32_16x16x32_bf16 v[44:47],a[172:175],v[148:151],v[44:47]
v_and_b32 v33,0xffff0000,v48
v_lshlrev_b32 v34,16,v49
v_mfma_f32_16x16x32_bf16 v[44:47],a[180:183],v[152:155],v[44:47]
ds_read_b128 v[132:135],v4 offset:1024
v_and_b32 v35,0xffff0000,v49
v_mfma_f32_16x16x32_bf16 v[44:47],a[188:191],v[156:159],v[44:47]
v_lshlrev_b32 v36,16,v50
v_and_b32 v37,0xffff0000,v50
v_mfma_f32_16x16x32_bf16 v[40:43],a[184:187],v[156:159],v[40:43]
ds_read_b128 v[136:139],v4 offset:2048
s_add_u32 m0,s64,0
v_mfma_f32_16x16x32_bf16 v[40:43],a[176:179],v[152:155],v[40:43]
buffer_load_dwordx4 v2,s[32:35],s63 offen offset:0 sc1 lds
v_mfma_f32_16x16x32_bf16 v[40:43],a[168:171],v[148:151],v[40:43]
ds_read_b128 v[140:143],v4 offset:3072
v_lshlrev_b32 v38,16,v51
v_mfma_f32_16x16x32_bf16 v[40:43],a[160:163],v[144:147],v[40:43]
v_and_b32 v39,0xffff0000,v51
s_waitcnt lgkmcnt(4)
v_mfma_f32_16x16x32_bf16 v[40:43],a[192:195],v[160:163],v[40:43]
ds_read_b128 v[144:147],v4 offset:4096
v_mfma_f32_16x16x32_bf16 v[40:43],a[200:203],v[164:167],v[40:43]
buffer_load_dwordx4 v2,s[32:35],s63 offen offset:1024 sc1 lds
v_mfma_f32_16x16x32_bf16 v[40:43],a[208:211],v[168:171],v[40:43]
ds_read_b128 v[148:151],v4 offset:5120
v_mfma_f32_16x16x32_bf16 v[40:43],a[216:219],v[172:175],v[40:43]
v_mfma_f32_16x16x32_bf16 v[44:47],a[220:223],v[172:175],v[44:47]
ds_read_b128 v[152:155],v4 offset:6144
v_mfma_f32_16x16x32_bf16 v[44:47],a[212:215],v[168:171],v[44:47]
buffer_load_dwordx4 v2,s[32:35],s63 offen offset:2048 sc1 lds
v_mfma_f32_16x16x32_bf16 v[44:47],a[204:207],v[164:167],v[44:47]
ds_read_b128 v[156:159],v4 offset:7168
v_mfma_f32_16x16x32_bf16 v[44:47],a[196:199],v[160:163],v[44:47]
v_mfma_f32_16x16x32_bf16 v[44:47],a[228:231],v[176:179],v[44:47]
ds_read_b128 v[160:163],v4 offset:8192
v_mfma_f32_16x16x32_bf16 v[44:47],a[236:239],v[180:183],v[44:47]
buffer_load_dwordx4 v2,s[32:35],s63 offen offset:3072 sc1 lds
v_mfma_f32_16x16x32_bf16 v[44:47],a[244:247],v[184:187],v[44:47]
ds_read_b128 v[164:167],v4 offset:9216
v_mfma_f32_16x16x32_bf16 v[44:47],a[252:255],v[188:191],v[44:47]
v_mfma_f32_16x16x32_bf16 v[40:43],a[248:251],v[188:191],v[40:43]
ds_read_b128 v[168:171],v4 offset:10240
s_add_u32 m0,s64,4096
v_mfma_f32_16x16x32_bf16 v[40:43],a[240:243],v[184:187],v[40:43]
buffer_load_dwordx4 v6,s[32:35],s63 offen offset:0 sc1 lds
v_mfma_f32_16x16x32_bf16 v[40:43],a[232:235],v[180:183],v[40:43]
ds_read_b128 v[172:175],v4 offset:11264
s_sub_u32 s62,s62,1
v_mfma_f32_16x16x32_bf16 v[40:43],a[224:227],v[176:179],v[40:43]
s_cbranch_scc0 .L_wl_1
s_nop 1
s_add_u32 s68,s68,s30
s_nop 3
v_cvt_pk_bf16_f32 v56,v40,v41
v_cvt_pk_bf16_f32 v57,v42,v43
v_cvt_pk_bf16_f32 v58,v44,v45
v_cvt_pk_bf16_f32 v59,v46,v47
buffer_store_dwordx4 v[56:59],v2,s[32:35],s68 offen sc1
v_add_f32 v60,v40,v41
v_mul_f32 v61,v40,v40
v_fmac_f32 v61,v41,v41
v_add_f32 v60,v60,v42
v_fmac_f32 v61,v42,v42
v_add_f32 v60,v60,v43
v_fmac_f32 v61,v43,v43
v_add_f32 v60,v60,v44
v_fmac_f32 v61,v44,v44
v_add_f32 v60,v60,v45
v_fmac_f32 v61,v45,v45
v_add_f32 v60,v60,v46
v_fmac_f32 v61,v46,v46
v_add_f32 v60,v60,v47
v_fmac_f32 v61,v47,v47
s_nop 1
v_permlane16_swap_b32 v60,v61
v_add_f32 v60,v60,v61
s_nop 1
v_permlane32_swap_b32 v60,v61
v_add_f32 v60,v60,v61
buffer_store_dword v60,v15,s[32:35],s68 offen offset:1024 sc1
s_waitcnt vmcnt(9)
s_barrier
buffer_store_dwordx2 v[16:17],v2,s[92:95],s68 idxen sc1
s_add_u32 s68,s68,s30
s_waitcnt vmcnt(1)
s_barrier
buffer_store_dwordx2 v[16:17],v2,s[92:95],s68 idxen sc1
s_cbranch_execz .L_wbk
s_branch .L_end
.p2align 6
.L_wv2:
.L_wl_2:
s_waitcnt lgkmcnt(4)
v_mfma_f32_16x16x32_bf16 v[32:35],a[0:3],v[128:131],v[32:35]
ds_read_b128 v[176:179],v4 offset:12288
v_mfma_f32_16x16x32_bf16 v[32:35],a[8:11],v[132:135],v[32:35]
buffer_load_dwordx4 v6,s[32:35],s63 offen offset:1024 sc1 lds
v_mfma_f32_16x16x32_bf16 v[32:35],a[16:19],v[136:139],v[32:35]
ds_read_b128 v[180:183],v4 offset:13312
s_add_u32 s68,s68,s30
v_mfma_f32_16x16x32_bf16 v[32:35],a[24:27],v[140:143],v[32:35]
v_cvt_pk_bf16_f32 v56,v40,v41
v_cvt_pk_bf16_f32 v57,v42,v43
v_mfma_f32_16x16x32_bf16 v[36:39],a[28:31],v[140:143],v[36:39]
ds_read_b128 v[184:187],v4 offset:14336
v_cvt_pk_bf16_f32 v58,v44,v45
v_mfma_f32_16x16x32_bf16 v[36:39],a[20:23],v[136:139],v[36:39]
v_cvt_pk_bf16_f32 v59,v46,v47
v_add_f32 v60,v40,v41
v_mfma_f32_16x16x32_bf16 v[36:39],a[12:15],v[132:135],v[36:39]
ds_read_b128 v[188:191],v4 offset:15360
v_mul_f32 v61,v40,v40
v_mfma_f32_16x16x32_bf16 v[36:39],a[4:7],v[128:131],v[36:39]
v_fmac_f32 v61,v41,v41
v_add_f32 v60,v60,v42
v_mfma_f32_16x16x32_bf16 v[36:39],a[36:39],v[144:147],v[36:39]
ds_read_b128 v[128:131],v4 offset:16384
v_fmac_f32 v61,v42,v42
v_mfma_f32_16x16x32_bf16 v[36:39],a[44:47],v[148:151],v[36:39]
buffer_store_dwordx4 v[56:59],v2,s[32:35],s68 offen sc1
v_mfma_f32_16x16x32_bf16 v[36:39],a[52:55],v[152:155],v[36:39]
ds_read_b128 v[132:135],v4 offset:17408
v_add_f32 v60,v60,v43
v_mfma_f32_16x16x32_bf16 v[36:39],a[60:63],v[156:159],v[36:39]
v_fmac_f32 v61,v43,v43
v_add_f32 v60,v60,v44
v_mfma_f32_16x16x32_bf16 v[32:35],a[56:59],v[156:159],v[32:35]
ds_read_b128 v[136:139],v4 offset:18432
v_fmac_f32 v61,v44,v44
v_mfma_f32_16x16x32_bf16 v[32:35],a[48:51],v[152:155],v[32:35]
v_add_f32 v60,v60,v45
v_fmac_f32 v61,v45,v45
v_mfma_f32_16x16x32_bf16 v[32:35],a[40:43],v[148:151],v[32:35]
ds_read_b128 v[140:143],v4 offset:19456
v_add_f32 v60,v60,v46
v_mfma_f32_16x16x32_bf16 v[32:35],a[32:35],v[144:147],v[32:35]
v_fmac_f32 v61,v46,v46
s_waitcnt lgkmcnt(4)
v_mfma_f32_16x16x32_bf16 v[32:35],a[64:67],v[160:163],v[32:35]
ds_read_b128 v[144:147],v4 offset:20480
v_add_f32 v60,v60,v47
v_mfma_f32_16x16x32_bf16 v[32:35],a[72:75],v[164:167],v[32:35]
v_fmac_f32 v61,v47,v47
v_mfma_f32_16x16x32_bf16 v[32:35],a[80:83],v[168:171],v[32:35]
ds_read_b128 v[148:151],v4 offset:21504
v_mfma_f32_16x16x32_bf16 v[32:35],a[88:91],v[172:175],v[32:35]
v_permlane16_swap_b32 v60,v61
v_mfma_f32_16x16x32_bf16 v[36:39],a[92:95],v[172:175],v[36:39]
ds_read_b128 v[152:155],v4 offset:22528
v_add_f32 v60,v60,v61
v_mfma_f32_16x16x32_bf16 v[36:39],a[84:87],v[168:171],v[36:39]
buffer_load_dwordx4 v[48:51],v2,s[32:35],s71 offen sc1
v_mfma_f32_16x16x32_bf16 v[36:39],a[76:79],v[164:167],v[36:39]
ds_read_b128 v[156:159],v4 offset:23552
s_add_u32 s71,s71,s30
v_mfma_f32_16x16x32_bf16 v[36:39],a[68:71],v[160:163],v[36:39]
v_permlane32_swap_b32 v60,v61
v_mfma_f32_16x16x32_bf16 v[36:39],a[100:103],v[176:179],v[36:39]
ds_read_b128 v[160:163],v4 offset:24576
v_add_f32 v60,v60,v61
v_mfma_f32_16x16x32_bf16 v[36:39],a[108:111],v[180:183],v[36:39]
buffer_store_dword v60,v15,s[32:35],s68 offen offset:1024 sc1
v_mfma_f32_16x16x32_bf16 v[36:39],a[116:119],v[184:187],v[36:39]
ds_read_b128 v[164:167],v4 offset:25600
v_mfma_f32_16x16x32_bf16 v[36:39],a[124:127],v[188:191],v[36:39]
v_mfma_f32_16x16x32_bf16 v[32:35],a[120:123],v[188:191],v[32:35]
ds_read_b128 v[168:171],v4 offset:26624
v_mfma_f32_16x16x32_bf16 v[32:35],a[112:115],v[184:187],v[32:35]
buffer_load_dwordx4 v6,s[32:35],s63 offen offset:2048 sc1 lds
v_mfma_f32_16x16x32_bf16 v[32:35],a[104:107],v[180:183],v[32:35]
ds_read_b128 v[172:175],v4 offset:27648
v_mfma_f32_16x16x32_bf16 v[32:35],a[96:99],v[176:179],v[32:35]
s_waitcnt lgkmcnt(4)
v_mfma_f32_16x16x32_bf16 v[32:35],a[128:131],v[128:131],v[32:35]
ds_read_b128 v[176:179],v4 offset:28672
v_mfma_f32_16x16x32_bf16 v[32:35],a[136:139],v[132:135],v[32:35]
buffer_load_dwordx4 v6,s[32:35],s63 offen offset:3072 sc1 lds
v_mfma_f32_16x16x32_bf16 v[32:35],a[144:147],v[136:139],v[32:35]
ds_read_b128 v[180:183],v4 offset:29696
s_add_u32 s63,s63,s30
v_mfma_f32_16x16x32_bf16 v[32:35],a[152:155],v[140:143],v[32:35]
v_mfma_f32_16x16x32_bf16 v[36:39],a[156:159],v[140:143],v[36:39]
ds_read_b128 v[184:187],v4 offset:30720
v_mfma_f32_16x16x32_bf16 v[36:39],a[148:151],v[136:139],v[36:39]
s_waitcnt vmcnt(11)
s_barrier
v_mfma_f32_16x16x32_bf16 v[36:39],a[140:143],v[132:135],v[36:39]
ds_read_b128 v[188:191],v4 offset:31744
v_mfma_f32_16x16x32_bf16 v[36:39],a[132:135],v[128:131],v[36:39]
v_mfma_f32_16x16x32_bf16 v[36:39],a[164:167],v[144:147],v[36:39]
ds_read_b128 v[128:131],v4 offset:32768
v_lshlrev_b32 v40,16,v52
v_mfma_f32_16x16x32_bf16 v[36:39],a[172:175],v[148:151],v[36:39]
v_and_b32 v41,0xffff0000,v52
v_lshlrev_b32 v42,16,v53
v_mfma_f32_16x16x32_bf16 v[36:39],a[180:183],v[152:155],v[36:39]
ds_read_b128 v[132:135],v4 offset:33792
v_and_b32 v43,0xffff0000,v53
v_mfma_f32_16x16x32_bf16 v[36:39],a[188:191],v[156:159],v[36:39]
v_lshlrev_b32 v44,16,v54
v_and_b32 v45,0xffff0000,v54
v_mfma_f32_16x16x32_bf16 v[32:35],a[184:187],v[156:159],v[32:35]
ds_read_b128 v[136:139],v4 offset:34816
s_add_u32 m0,s64,32768
v_mfma_f32_16x16x32_bf16 v[32:35],a[176:179],v[152:155],v[32:35]
buffer_load_dwordx4 v2,s[32:35],s63 offen offset:0 sc1 lds
v_mfma_f32_16x16x32_bf16 v[32:35],a[168:171],v[148:151],v[32:35]
ds_read_b128 v[140:143],v4 offset:35840
v_lshlrev_b32 v46,16,v55
v_mfma_f32_16x16x32_bf16 v[32:35],a[160:163],v[144:147],v[32:35]
v_and_b32 v47,0xffff0000,v55
s_waitcnt lgkmcnt(4)
v_mfma_f32_16x16x32_bf16 v[32:35],a[192:195],v[160:163],v[32:35]
ds_read_b128 v[144:147],v4 offset:36864
v_mfma_f32_16x16x32_bf16 v[32:35],a[200:203],v[164:167],v[32:35]
buffer_load_dwordx4 v2,s[32:35],s63 offen offset:1024 sc1 lds
v_mfma_f32_16x16x32_bf16 v[32:35],a[208:211],v[168:171],v[32:35]
ds_read_b128 v[148:151],v4 offset:37888
v_mfma_f32_16x16x32_bf16 v[32:35],a[216:219],v[172:175],v[32:35]
v_mfma_f32_16x16x32_bf16 v[36:39],a[220:223],v[172:175],v[36:39]
ds_read_b128 v[152:155],v4 offset:38912
v_mfma_f32_16x16x32_bf16 v[36:39],a[212:215],v[168:171],v[36:39]
buffer_load_dwordx4 v2,s[32:35],s63 offen offset:2048 sc1 lds
v_mfma_f32_16x16x32_bf16 v[36:39],a[204:207],v[164:167],v[36:39]
ds_read_b128 v[156:159],v4 offset:39936
v_mfma_f32_16x16x32_bf16 v[36:39],a[196:199],v[160:163],v[36:39]
v_mfma_f32_16x16x32_bf16 v[36:39],a[228:231],v[176:179],v[36:39]
ds_read_b128 v[160:163],v4 offset:40960
v_mfma_f32_16x16x32_bf16 v[36:39],a[236:239],v[180:183],v[36:39]
buffer_load_dwordx4 v2,s[32:35],s63 offen offset:3072 sc1 lds
v_mfma_f32_16x16x32_bf16 v[36:39],a[244:247],v[184:187],v[36:39]
ds_read_b128 v[164:167],v4 offset:41984
v_mfma_f32_16x16x32_bf16 v[36:39],a[252:255],v[188:191],v[36:39]
v_mfma_f32_16x16x32_bf16 v[32:35],a[248:251],v[188:191],v[32:35]
ds_read_b128 v[168:171],v4 offset:43008
s_add_u32 m0,s64,36864
v_mfma_f32_16x16x32_bf16 v[32:35],a[240:243],v[184:187],v[32:35]
buffer_load_dwordx4 v6,s[32:35],s63 offen offset:0 sc1 lds
v_mfma_f32_16x16x32_bf16 v[32:35],a[232:235],v[180:183],v[32:35]
ds_read_b128 v[172:175],v4 offset:44032
v_mfma_f32_16x16x32_bf16 v[32:35],a[224:227],v[176:179],v[32:35]
s_waitcnt lgkmcnt(4)
v_mfma_f32_16x16x32_bf16 v[40:43],a[0:3],v[128:131],v[40:43]
ds_read_b128 v[176:179],v4 offset:45056
v_mfma_f32_16x16x32_bf16 v[40:43],a[8:11],v[132:135],v[40:43]
buffer_load_dwordx4 v6,s[32:35],s63 offen offset:1024 sc1 lds
v_mfma_f32_16x16x32_bf16 v[40:43],a[16:19],v[136:139],v[40:43]
ds_read_b128 v[180:183],v4 offset:46080
s_add_u32 s68,s68,s30
v_mfma_f32_16x16x32_bf16 v[40:43],a[24:27],v[140:143],v[40:43]
v_cvt_pk_bf16_f32 v56,v32,v33
v_cvt_pk_bf16_f32 v57,v34,v35
v_mfma_f32_16x16x32_bf16 v[44:47],a[28:31],v[140:143],v[44:47]
)#"
R"#(
ds_read_b128 v[184:187],v4 offset:47104
v_cvt_pk_bf16_f32 v58,v36,v37
v_mfma_f32_16x16x32_bf16 v[44:47],a[20:23],v[136:139],v[44:47]
v_cvt_pk_bf16_f32 v59,v38,v39
v_add_f32 v60,v32,v33
v_mfma_f32_16x16x32_bf16 v[44:47],a[12:15],v[132:135],v[44:47]
ds_read_b128 v[188:191],v4 offset:48128
v_mul_f32 v61,v32,v32
v_mfma_f32_16x16x32_bf16 v[44:47],a[4:7],v[128:131],v[44:47]
v_fmac_f32 v61,v33,v33
v_add_f32 v60,v60,v34
v_mfma_f32_16x16x32_bf16 v[44:47],a[36:39],v[144:147],v[44:47]
ds_read_b128 v[128:131],v4 offset:49152
v_fmac_f32 v61,v34,v34
v_mfma_f32_16x16x32_bf16 v[44:47],a[44:47],v[148:151],v[44:47]
buffer_store_dwordx4 v[56:59],v2,s[32:35],s68 offen sc1
v_mfma_f32_16x16x32_bf16 v[44:47],a[52:55],v[152:155],v[44:47]
ds_read_b128 v[132:135],v4 offset:50176
v_add_f32 v60,v60,v35
v_mfma_f32_16x16x32_bf16 v[44:47],a[60:63],v[156:159],v[44:47]
v_fmac_f32 v61,v35,v35
v_add_f32 v60,v60,v36
v_mfma_f32_16x16x32_bf16 v[40:43],a[56:59],v[156:159],v[40:43]
ds_read_b128 v[136:139],v4 offset:51200
v_fmac_f32 v61,v36,v36
v_mfma_f32_16x16x32_bf16 v[40:43],a[48:51],v[152:155],v[40:43]
v_add_f32 v60,v60,v37
v_fmac_f32 v61,v37,v37
v_mfma_f32_16x16x32_bf16 v[40:43],a[40:43],v[148:151],v[40:43]
ds_read_b128 v[140:143],v4 offset:52224
v_add_f32 v60,v60,v38
v_mfma_f32_16x16x32_bf16 v[40:43],a[32:35],v[144:147],v[40:43]
v_fmac_f32 v61,v38,v38
s_waitcnt lgkmcnt(4)
v_mfma_f32_16x16x32_bf16 v[40:43],a[64:67],v[160:163],v[40:43]
ds_read_b128 v[144:147],v4 offset:53248
v_add_f32 v60,v60,v39
v_mfma_f32_16x16x32_bf16 v[40:43],a[72:75],v[164:167],v[40:43]
v_fmac_f32 v61,v39,v39
v_mfma_f32_16x16x32_bf16 v[40:43],a[80:83],v[168:171],v[40:43]
ds_read_b128 v[148:151],v4 offset:54272
v_mfma_f32_16x16x32_bf16 v[40:43],a[88:91],v[172:175],v[40:43]
v_permlane16_swap_b32 v60,v61
v_mfma_f32_16x16x32_bf16 v[44:47],a[92:95],v[172:175],v[44:47]
ds_read_b128 v[152:155],v4 offset:55296
v_add_f32 v60,v60,v61
v_mfma_f32_16x16x32_bf16 v[44:47],a[84:87],v[168:171],v[44:47]
buffer_load_dwordx4 v[52:55],v2,s[32:35],s71 offen sc1
v_mfma_f32_16x16x32_bf16 v[44:47],a[76:79],v[164:167],v[44:47]
ds_read_b128 v[156:159],v4 offset:56320
s_add_u32 s71,s71,s30
v_mfma_f32_16x16x32_bf16 v[44:47],a[68:71],v[160:163],v[44:47]
v_permlane32_swap_b32 v60,v61
v_mfma_f32_16x16x32_bf16 v[44:47],a[100:103],v[176:179],v[44:47]
ds_read_b128 v[160:163],v4 offset:57344
v_add_f32 v60,v60,v61
v_mfma_f32_16x16x32_bf16 v[44:47],a[108:111],v[180:183],v[44:47]
buffer_store_dword v60,v15,s[32:35],s68 offen offset:1024 sc1
v_mfma_f32_16x16x32_bf16 v[44:47],a[116:119],v[184:187],v[44:47]
ds_read_b128 v[164:167],v4 offset:58368
v_mfma_f32_16x16x32_bf16 v[44:47],a[124:127],v[188:191],v[44:47]
v_mfma_f32_16x16x32_bf16 v[40:43],a[120:123],v[188:191],v[40:43]
ds_read_b128 v[168:171],v4 offset:59392
v_mfma_f32_16x16x32_bf16 v[40:43],a[112:115],v[184:187],v[40:43]
buffer_load_dwordx4 v6,s[32:35],s63 offen offset:2048 sc1 lds
v_mfma_f32_16x16x32_bf16 v[40:43],a[104:107],v[180:183],v[40:43]
ds_read_b128 v[172:175],v4 offset:60416
v_mfma_f32_16x16x32_bf16 v[40:43],a[96:99],v[176:179],v[40:43]
s_waitcnt lgkmcnt(4)
v_mfma_f32_16x16x32_bf16 v[40:43],a[128:131],v[128:131],v[40:43]
ds_read_b128 v[176:179],v4 offset:61440
v_mfma_f32_16x16x32_bf16 v[40:43],a[136:139],v[132:135],v[40:43]
buffer_load_dwordx4 v6,s[32:35],s63 offen offset:3072 sc1 lds
v_mfma_f32_16x16x32_bf16 v[40:43],a[144:147],v[136:139],v[40:43]
ds_read_b128 v[180:183],v4 offset:62464
s_add_u32 s63,s63,s30
v_mfma_f32_16x16x32_bf16 v[40:43],a[152:155],v[140:143],v[40:43]
s_xor_b32 s64,s64,s65
v_mfma_f32_16x16x32_bf16 v[44:47],a[156:159],v[140:143],v[44:47]
ds_read_b128 v[184:187],v4 offset:63488
v_mfma_f32_16x16x32_bf16 v[44:47],a[148:151],v[136:139],v[44:47]
s_waitcnt vmcnt(11)
s_barrier
v_mfma_f32_16x16x32_bf16 v[44:47],a[140:143],v[132:135],v[44:47]
ds_read_b128 v[188:191],v4 offset:64512
v_xor_b32 v4,65536,v4
v_mfma_f32_16x16x32_bf16 v[44:47],a[132:135],v[128:131],v[44:47]
v_mfma_f32_16x16x32_bf16 v[44:47],a[164:167],v[144:147],v[44:47]
ds_read_b128 v[128:131],v4 offset:0
v_lshlrev_b32 v32,16,v48
v_mfma_f32_16x16x32_bf16 v[44:47],a[172:175],v[148:151],v[44:47]
v_and_b32 v33,0xffff0000,v48
v_lshlrev_b32 v34,16,v49
v_mfma_f32_16x16x32_bf16 v[44:47],a[180:183],v[152:155],v[44:47]
ds_read_b128 v[132:135],v4 offset:1024
v_and_b32 v35,0xffff0000,v49
v_mfma_f32_16x16x32_bf16 v[44:47],a[188:191],v[156:159],v[44:47]
v_lshlrev_b32 v36,16,v50
v_and_b32 v37,0xffff0000,v50
v_mfma_f32_16x16x32_bf16 v[40:43],a[184:187],v[156:159],v[40:43]
ds_read_b128 v[136:139],v4 offset:2048
s_add_u32 m0,s64,0
v_mfma_f32_16x16x32_bf16 v[40:43],a[176:179],v[152:155],v[40:43]
buffer_load_dwordx4 v2,s[32:35],s63 offen offset:0 sc1 lds
v_mfma_f32_16x16x32_bf16 v[40:43],a[168:171],v[148:151],v[40:43]
ds_read_b128 v[140:143],v4 offset:3072
v_lshlrev_b32 v38,16,v51
v_mfma_f32_16x16x32_bf16 v[40:43],a[160:163],v[144:147],v[40:43]
v_and_b32 v39,0xffff0000,v51
s_waitcnt lgkmcnt(4)
v_mfma_f32_16x16x32_bf16 v[40:43],a[192:195],v[160:163],v[40:43]
ds_read_b128 v[144:147],v4 offset:4096
v_mfma_f32_16x16x32_bf16 v[40:43],a[200:203],v[164:167],v[40:43]
buffer_load_dwordx4 v2,s[32:35],s63 offen offset:1024 sc1 lds
v_mfma_f32_16x16x32_bf16 v[40:43],a[208:211],v[168:171],v[40:43]
ds_read_b128 v[148:151],v4 offset:5120
v_mfma_f32_16x16x32_bf16 v[40:43],a[216:219],v[172:175],v[40:43]
v_mfma_f32_16x16x32_bf16 v[44:47],a[220:223],v[172:175],v[44:47]
ds_read_b128 v[152:155],v4 offset:6144
v_mfma_f32_16x16x32_bf16 v[44:47],a[212:215],v[168:171],v[44:47]
buffer_load_dwordx4 v2,s[32:35],s63 offen offset:2048 sc1 lds
v_mfma_f32_16x16x32_bf16 v[44:47],a[204:207],v[164:167],v[44:47]
ds_read_b128 v[156:159],v4 offset:7168
v_mfma_f32_16x16x32_bf16 v[44:47],a[196:199],v[160:163],v[44:47]
v_mfma_f32_16x16x32_bf16 v[44:47],a[228:231],v[176:179],v[44:47]
ds_read_b128 v[160:163],v4 offset:8192
v_mfma_f32_16x16x32_bf16 v[44:47],a[236:239],v[180:183],v[44:47]
buffer_load_dwordx4 v2,s[32:35],s63 offen offset:3072 sc1 lds
v_mfma_f32_16x16x32_bf16 v[44:47],a[244:247],v[184:187],v[44:47]
ds_read_b128 v[164:167],v4 offset:9216
v_mfma_f32_16x16x32_bf16 v[44:47],a[252:255],v[188:191],v[44:47]
v_mfma_f32_16x16x32_bf16 v[40:43],a[248:251],v[188:191],v[40:43]
ds_read_b128 v[168:171],v4 offset:10240
s_add_u32 m0,s64,4096
v_mfma_f32_16x16x32_bf16 v[40:43],a[240:243],v[184:187],v[40:43]
buffer_load_dwordx4 v6,s[32:35],s63 offen offset:0 sc1 lds
v_mfma_f32_16x16x32_bf16 v[40:43],a[232:235],v[180:183],v[40:43]
ds_read_b128 v[172:175],v4 offset:11264
s_sub_u32 s62,s62,1
v_mfma_f32_16x16x32_bf16 v[40:43],a[224:227],v[176:179],v[40:43]
s_cbranch_scc0 .L_wl_2
s_nop 1
s_add_u32 s68,s68,s30
s_nop 3
v_cvt_pk_bf16_f32 v56,v40,v41
v_cvt_pk_bf16_f32 v57,v42,v43
v_cvt_pk_bf16_f32 v58,v44,v45
v_cvt_pk_bf16_f32 v59,v46,v47
buffer_store_dwordx4 v[56:59],v2,s[32:35],s68 offen sc1
v_add_f32 v60,v40,v41
v_mul_f32 v61,v40,v40
v_fmac_f32 v61,v41,v41
v_add_f32 v60,v60,v42
v_fmac_f32 v61,v42,v42
v_add_f32 v60,v60,v43
v_fmac_f32 v61,v43,v43
v_add_f32 v60,v60,v44
v_fmac_f32 v61,v44,v44
v_add_f32 v60,v60,v45
v_fmac_f32 v61,v45,v45
v_add_f32 v60,v60,v46
v_fmac_f32 v61,v46,v46
v_add_f32 v60,v60,v47
v_fmac_f32 v61,v47,v47
s_nop 1
v_permlane16_swap_b32 v60,v61
v_add_f32 v60,v60,v61
s_nop 1
v_permlane32_swap_b32 v60,v61
v_add_f32 v60,v60,v61
buffer_store_dword v60,v15,s[32:35],s68 offen offset:1024 sc1
s_waitcnt vmcnt(9)
s_barrier
buffer_store_dwordx2 v[16:17],v2,s[92:95],s68 idxen sc1
s_add_u32 s68,s68,s30
s_waitcnt vmcnt(1)
s_barrier
buffer_store_dwordx2 v[16:17],v2,s[92:95],s68 idxen sc1
s_cbranch_execz .L_wbk
s_branch .L_end
.p2align 6
.L_wv3:
.L_wl_3:
s_waitcnt lgkmcnt(4)
v_mfma_f32_16x16x32_bf16 v[32:35],a[0:3],v[128:131],v[32:35]
ds_read_b128 v[176:179],v4 offset:12288
v_mfma_f32_16x16x32_bf16 v[32:35],a[8:11],v[132:135],v[32:35]
buffer_load_dwordx4 v6,s[32:35],s63 offen offset:1024 sc1 lds
v_mfma_f32_16x16x32_bf16 v[32:35],a[16:19],v[136:139],v[32:35]
ds_read_b128 v[180:183],v4 offset:13312
s_add_u32 s68,s68,s30
v_mfma_f32_16x16x32_bf16 v[32:35],a[24:27],v[140:143],v[32:35]
v_cvt_pk_bf16_f32 v56,v40,v41
v_cvt_pk_bf16_f32 v57,v42,v43
v_mfma_f32_16x16x32_bf16 v[36:39],a[28:31],v[140:143],v[36:39]
ds_read_b128 v[184:187],v4 offset:14336
v_cvt_pk_bf16_f32 v58,v44,v45
v_mfma_f32_16x16x32_bf16 v[36:39],a[20:23],v[136:139],v[36:39]
v_cvt_pk_bf16_f32 v59,v46,v47
v_add_f32 v60,v40,v41
v_mfma_f32_16x16x32_bf16 v[36:39],a[12:15],v[132:135],v[36:39]
ds_read_b128 v[188:191],v4 offset:15360
v_mul_f32 v61,v40,v40
v_mfma_f32_16x16x32_bf16 v[36:39],a[4:7],v[128:131],v[36:39]
v_fmac_f32 v61,v41,v41
v_add_f32 v60,v60,v42
v_mfma_f32_16x16x32_bf16 v[36:39],a[36:39],v[144:147],v[36:39]
ds_read_b128 v[128:131],v4 offset:16384
v_fmac_f32 v61,v42,v42
v_mfma_f32_16x16x32_bf16 v[36:39],a[44:47],v[148:151],v[36:39]
buffer_store_dwordx4 v[56:59],v2,s[32:35],s68 offen sc1
v_mfma_f32_16x16x32_bf16 v[36:39],a[52:55],v[152:155],v[36:39]
ds_read_b128 v[132:135],v4 offset:17408
v_add_f32 v60,v60,v43
v_mfma_f32_16x16x32_bf16 v[36:39],a[60:63],v[156:159],v[36:39]
v_fmac_f32 v61,v43,v43
v_add_f32 v60,v60,v44
v_mfma_f32_16x16x32_bf16 v[32:35],a[56:59],v[156:159],v[32:35]
ds_read_b128 v[136:139],v4 offset:18432
v_fmac_f32 v61,v44,v44
v_mfma_f32_16x16x32_bf16 v[32:35],a[48:51],v[152:155],v[32:35]
v_add_f32 v60,v60,v45
v_fmac_f32 v61,v45,v45
v_mfma_f32_16x16x32_bf16 v[32:35],a[40:43],v[148:151],v[32:35]
ds_read_b128 v[140:143],v4 offset:19456
v_add_f32 v60,v60,v46
v_mfma_f32_16x16x32_bf16 v[32:35],a[32:35],v[144:147],v[32:35]
v_fmac_f32 v61,v46,v46
s_waitcnt lgkmcnt(4)
v_mfma_f32_16x16x32_bf16 v[32:35],a[64:67],v[160:163],v[32:35]
ds_read_b128 v[144:147],v4 offset:20480
v_add_f32 v60,v60,v47
v_mfma_f32_16x16x32_bf16 v[32:35],a[72:75],v[164:167],v[32:35]
v_fmac_f32 v61,v47,v47
v_mfma_f32_16x16x32_bf16 v[32:35],a[80:83],v[168:171],v[32:35]
ds_read_b128 v[148:151],v4 offset:21504
v_mfma_f32_16x16x32_bf16 v[32:35],a[88:91],v[172:175],v[32:35]
v_permlane16_swap_b32 v60,v61
v_mfma_f32_16x16x32_bf16 v[36:39],a[92:95],v[172:175],v[36:39]
ds_read_b128 v[152:155],v4 offset:22528
v_add_f32 v60,v60,v61
v_mfma_f32_16x16x32_bf16 v[36:39],a[84:87],v[168:171],v[36:39]
buffer_load_dwordx4 v[48:51],v2,s[32:35],s71 offen sc1
v_mfma_f32_16x16x32_bf16 v[36:39],a[76:79],v[164:167],v[36:39]
ds_read_b128 v[156:159],v4 offset:23552
s_add_u32 s71,s71,s30
v_mfma_f32_16x16x32_bf16 v[36:39],a[68:71],v[160:163],v[36:39]
v_permlane32_swap_b32 v60,v61
v_mfma_f32_16x16x32_bf16 v[36:39],a[100:103],v[176:179],v[36:39]
ds_read_b128 v[160:163],v4 offset:24576
v_add_f32 v60,v60,v61
v_mfma_f32_16x16x32_bf16 v[36:39],a[108:111],v[180:183],v[36:39]
buffer_store_dword v60,v15,s[32:35],s68 offen offset:1024 sc1
v_mfma_f32_16x16x32_bf16 v[36:39],a[116:119],v[184:187],v[36:39]
ds_read_b128 v[164:167],v4 offset:25600
v_mfma_f32_16x16x32_bf16 v[36:39],a[124:127],v[188:191],v[36:39]
v_mfma_f32_16x16x32_bf16 v[32:35],a[120:123],v[188:191],v[32:35]
ds_read_b128 v[168:171],v4 offset:26624
v_mfma_f32_16x16x32_bf16 v[32:35],a[112:115],v[184:187],v[32:35]
buffer_load_dwordx4 v6,s[32:35],s63 offen offset:2048 sc1 lds
v_mfma_f32_16x16x32_bf16 v[32:35],a[104:107],v[180:183],v[32:35]
ds_read_b128 v[172:175],v4 offset:27648
v_mfma_f32_16x16x32_bf16 v[32:35],a[96:99],v[176:179],v[32:35]
s_waitcnt lgkmcnt(4)
v_mfma_f32_16x16x32_bf16 v[32:35],a[128:131],v[128:131],v[32:35]
ds_read_b128 v[176:179],v4 offset:28672
v_mfma_f32_16x16x32_bf16 v[32:35],a[136:139],v[132:135],v[32:35]
buffer_load_dwordx4 v6,s[32:35],s63 offen offset:3072 sc1 lds
v_mfma_f32_16x16x32_bf16 v[32:35],a[144:147],v[136:139],v[32:35]
ds_read_b128 v[180:183],v4 offset:29696
s_add_u32 s63,s63,s30
v_mfma_f32_16x16x32_bf16 v[32:35],a[152:155],v[140:143],v[32:35]
v_mfma_f32_16x16x32_bf16 v[36:39],a[156:159],v[140:143],v[36:39]
s_waitcnt vmcnt(11)
s_barrier
v_mfma_f32_16x16x32_bf16 v[36:39],a[148:151],v[136:139],v[36:39]
ds_read_b128 v[184:187],v4 offset:30720
v_mfma_f32_16x16x32_bf16 v[36:39],a[140:143],v[132:135],v[36:39]
ds_read_b128 v[188:191],v4 offset:31744
v_mfma_f32_16x16x32_bf16 v[36:39],a[132:135],v[128:131],v[36:39]
v_mfma_f32_16x16x32_bf16 v[36:39],a[164:167],v[144:147],v[36:39]
ds_read_b128 v[128:131],v4 offset:32768
v_lshlrev_b32 v40,16,v52
v_mfma_f32_16x16x32_bf16 v[36:39],a[172:175],v[148:151],v[36:39]
v_and_b32 v41,0xffff0000,v52
v_lshlrev_b32 v42,16,v53
v_mfma_f32_16x16x32_bf16 v[36:39],a[180:183],v[152:155],v[36:39]
ds_read_b128 v[132:135],v4 offset:33792
v_and_b32 v43,0xffff0000,v53
v_mfma_f32_16x16x32_bf16 v[36:39],a[188:191],v[156:159],v[36:39]
v_lshlrev_b32 v44,16,v54
v_and_b32 v45,0xffff0000,v54
v_mfma_f32_16x16x32_bf16 v[32:35],a[184:187],v[156:159],v[32:35]
ds_read_b128 v[136:139],v4 offset:34816
s_add_u32 m0,s64,32768
v_mfma_f32_16x16x32_bf16 v[32:35],a[176:179],v[152:155],v[32:35]
buffer_load_dwordx4 v2,s[32:35],s63 offen offset:0 sc1 lds
v_mfma_f32_16x16x32_bf16 v[32:35],a[168:171],v[148:151],v[32:35]
ds_read_b128 v[140:143],v4 offset:35840
v_lshlrev_b32 v46,16,v55
v_mfma_f32_16x16x32_bf16 v[32:35],a[160:163],v[144:147],v[32:35]
v_and_b32 v47,0xffff0000,v55
s_waitcnt lgkmcnt(4)
v_mfma_f32_16x16x32_bf16 v[32:35],a[192:195],v[160:163],v[32:35]
ds_read_b128 v[144:147],v4 offset:36864
v_mfma_f32_16x16x32_bf16 v[32:35],a[200:203],v[164:167],v[32:35]
buffer_load_dwordx4 v2,s[32:35],s63 offen offset:1024 sc1 lds
v_mfma_f32_16x16x32_bf16 v[32:35],a[208:211],v[168:171],v[32:35]
ds_read_b128 v[148:151],v4 offset:37888
v_mfma_f32_16x16x32_bf16 v[32:35],a[216:219],v[172:175],v[32:35]
v_mfma_f32_16x16x32_bf16 v[36:39],a[220:223],v[172:175],v[36:39]
ds_read_b128 v[152:155],v4 offset:38912
v_mfma_f32_16x16x32_bf16 v[36:39],a[212:215],v[168:171],v[36:39]
buffer_load_dwordx4 v2,s[32:35],s63 offen offset:2048 sc1 lds
v_mfma_f32_16x16x32_bf16 v[36:39],a[204:207],v[164:167],v[36:39]
ds_read_b128 v[156:159],v4 offset:39936
v_mfma_f32_16x16x32_bf16 v[36:39],a[196:199],v[160:163],v[36:39]
v_mfma_f32_16x16x32_bf16 v[36:39],a[228:231],v[176:179],v[36:39]
ds_read_b128 v[160:163],v4 offset:40960
v_mfma_f32_16x16x32_bf16 v[36:39],a[236:239],v[180:183],v[36:39]
buffer_load_dwordx4 v2,s[32:35],s63 offen offset:3072 sc1 lds
v_mfma_f32_16x16x32_bf16 v[36:39],a[244:247],v[184:187],v[36:39]
ds_read_b128 v[164:167],v4 offset:41984
v_mfma_f32_16x16x32_bf16 v[36:39],a[252:255],v[188:191],v[36:39]
v_mfma_f32_16x16x32_bf16 v[32:35],a[248:251],v[188:191],v[32:35]
ds_read_b128 v[168:171],v4 offset:43008
s_add_u32 m0,s64,36864
v_mfma_f32_16x16x32_bf16 v[32:35],a[240:243],v[184:187],v[32:35]
buffer_load_dwordx4 v6,s[32:35],s63 offen offset:0 sc1 lds
v_mfma_f32_16x16x32_bf16 v[32:35],a[232:235],v[180:183],v[32:35]
ds_read_b128 v[172:175],v4 offset:44032
v_mfma_f32_16x16x32_bf16 v[32:35],a[224:227],v[176:179],v[32:35]
s_waitcnt lgkmcnt(4)
v_mfma_f32_16x16x32_bf16 v[40:43],a[0:3],v[128:131],v[40:43]
ds_read_b128 v[176:179],v4 offset:45056
v_mfma_f32_16x16x32_bf16 v[40:43],a[8:11],v[132:135],v[40:43]
buffer_load_dwordx4 v6,s[32:35],s63 offen offset:1024 sc1 lds
v_mfma_f32_16x16x32_bf16 v[40:43],a[16:19],v[136:139],v[40:43]
ds_read_b128 v[180:183],v4 offset:46080
s_add_u32 s68,s68,s30
v_mfma_f32_16x16x32_bf16 v[40:43],a[24:27],v[140:143],v[40:43]
v_cvt_pk_bf16_f32 v56,v32,v33
v_cvt_pk_bf16_f32 v57,v34,v35
v_mfma_f32_16x16x32_bf16 v[44:47],a[28:31],v[140:143],v[44:47]
ds_read_b128 v[184:187],v4 offset:47104
v_cvt_pk_bf16_f32 v58,v36,v37
v_mfma_f32_16x16x32_bf16 v[44:47],a[20:23],v[136:139],v[44:47]
v_cvt_pk_bf16_f32 v59,v38,v39
v_add_f32 v60,v32,v33
v_mfma_f32_16x16x32_bf16 v[44:47],a[12:15],v[132:135],v[44:47]
ds_read_b128 v[188:191],v4 offset:48128
v_mul_f32 v61,v32,v32
v_mfma_f32_16x16x32_bf16 v[44:47],a[4:7],v[128:131],v[44:47]
v_fmac_f32 v61,v33,v33
v_add_f32 v60,v60,v34
v_mfma_f32_16x16x32_bf16 v[44:47],a[36:39],v[144:147],v[44:47]
ds_read_b128 v[128:131],v4 offset:49152
v_fmac_f32 v61,v34,v34
v_mfma_f32_16x16x32_bf16 v[44:47],a[44:47],v[148:151],v[44:47]
buffer_store_dwordx4 v[56:59],v2,s[32:35],s68 offen sc1
v_mfma_f32_16x16x32_bf16 v[44:47],a[52:55],v[152:155],v[44:47]
ds_read_b128 v[132:135],v4 offset:50176
v_add_f32 v60,v60,v35
v_mfma_f32_16x16x32_bf16 v[44:47],a[60:63],v[156:159],v[44:47]
v_fmac_f32 v61,v35,v35
v_add_f32 v60,v60,v36
v_mfma_f32_16x16x32_bf16 v[40:43],a[56:59],v[156:159],v[40:43]
ds_read_b128 v[136:139],v4 offset:51200
v_fmac_f32 v61,v36,v36
v_mfma_f32_16x16x32_bf16 v[40:43],a[48:51],v[152:155],v[40:43]
v_add_f32 v60,v60,v37
v_fmac_f32 v61,v37,v37
v_mfma_f32_16x16x32_bf16 v[40:43],a[40:43],v[148:151],v[40:43]
ds_read_b128 v[140:143],v4 offset:52224
v_add_f32 v60,v60,v38
v_mfma_f32_16x16x32_bf16 v[40:43],a[32:35],v[144:147],v[40:43]
v_fmac_f32 v61,v38,v38
s_waitcnt lgkmcnt(4)
v_mfma_f32_16x16x32_bf16 v[40:43],a[64:67],v[160:163],v[40:43]
ds_read_b128 v[144:147],v4 offset:53248
v_add_f32 v60,v60,v39
v_mfma_f32_16x16x32_bf16 v[40:43],a[72:75],v[164:167],v[40:43]
v_fmac_f32 v61,v39,v39
v_mfma_f32_16x16x32_bf16 v[40:43],a[80:83],v[168:171],v[40:43]
ds_read_b128 v[148:151],v4 offset:54272
v_mfma_f32_16x16x32_bf16 v[40:43],a[88:91],v[172:175],v[40:43]
v_permlane16_swap_b32 v60,v61
v_mfma_f32_16x16x32_bf16 v[44:47],a[92:95],v[172:175],v[44:47]
ds_read_b128 v[152:155],v4 offset:55296
v_add_f32 v60,v60,v61
v_mfma_f32_16x16x32_bf16 v[44:47],a[84:87],v[168:171],v[44:47]
buffer_load_dwordx4 v[52:55],v2,s[32:35],s71 offen sc1
v_mfma_f32_16x16x32_bf16 v[44:47],a[76:79],v[164:167],v[44:47]
ds_read_b128 v[156:159],v4 offset:56320
s_add_u32 s71,s71,s30
v_mfma_f32_16x16x32_bf16 v[44:47],a[68:71],v[160:163],v[44:47]
v_permlane32_swap_b32 v60,v61
v_mfma_f32_16x16x32_bf16 v[44:47],a[100:103],v[176:179],v[44:47]
ds_read_b128 v[160:163],v4 offset:57344
v_add_f32 v60,v60,v61
v_mfma_f32_16x16x32_bf16 v[44:47],a[108:111],v[180:183],v[44:47]
buffer_store_dword v60,v15,s[32:35],s68 offen offset:1024 sc1
v_mfma_f32_16x16x32_bf16 v[44:47],a[116:119],v[184:187],v[44:47]
ds_read_b128 v[164:167],v4 offset:58368
v_mfma_f32_16x16x32_bf16 v[44:47],a[124:127],v[188:191],v[44:47]
v_mfma_f32_16x16x32_bf16 v[40:43],a[120:123],v[188:191],v[40:43]
ds_read_b128 v[168:171],v4 offset:59392
v_mfma_f32_16x16x32_bf16 v[40:43],a[112:115],v[184:187],v[40:43]
buffer_load_dwordx4 v6,s[32:35],s63 offen offset:2048 sc1 lds
v_mfma_f32_16x16x32_bf16 v[40:43],a[104:107],v[180:183],v[40:43]
ds_read_b128 v[172:175],v4 offset:60416
v_mfma_f32_16x16x32_bf16 v[40:43],a[96:99],v[176:179],v[40:43]
s_waitcnt lgkmcnt(4)
v_mfma_f32_16x16x32_bf16 v[40:43],a[128:131],v[128:131],v[40:43]
ds_read_b128 v[176:179],v4 offset:61440
v_mfma_f32_16x16x32_bf16 v[40:43],a[136:139],v[132:135],v[40:43]
buffer_load_dwordx4 v6,s[32:35],s63 offen offset:3072 sc1 lds
v_mfma_f32_16x16x32_bf16 v[40:43],a[144:147],v[136:139],v[40:43]
ds_read_b128 v[180:183],v4 offset:62464
s_add_u32 s63,s63,s30
v_mfma_f32_16x16x32_bf16 v[40:43],a[152:155],v[140:143],v[40:43]
s_xor_b32 s64,s64,s65
v_mfma_f32_16x16x32_bf16 v[44:47],a[156:159],v[140:143],v[44:47]
s_waitcnt vmcnt(11)
s_barrier
v_mfma_f32_16x16x32_bf16 v[44:47],a[148:151],v[136:139],v[44:47]
ds_read_b128 v[184:187],v4 offset:63488
v_mfma_f32_16x16x32_bf16 v[44:47],a[140:143],v[132:135],v[44:47]
ds_read_b128 v[188:191],v4 offset:64512
v_xor_b32 v4,65536,v4
v_mfma_f32_16x16x32_bf16 v[44:47],a[132:135],v[128:131],v[44:47]
v_mfma_f32_16x16x32_bf16 v[44:47],a[164:167],v[144:147],v[44:47]
ds_read_b128 v[128:131],v4 offset:0
v_lshlrev_b32 v32,16,v48
v_mfma_f32_16x16x32_bf16 v[44:47],a[172:175],v[148:151],v[44:47]
v_and_b32 v33,0xffff0000,v48
v_lshlrev_b32 v34,16,v49
v_mfma_f32_16x16x32_bf16 v[44:47],a[180:183],v[152:155],v[44:47]
ds_read_b128 v[132:135],v4 offset:1024
v_and_b32 v35,0xffff0000,v49
v_mfma_f32_16x16x32_bf16 v[44:47],a[188:191],v[156:159],v[44:47]
v_lshlrev_b32 v36,16,v50
v_and_b32 v37,0xffff0000,v50
v_mfma_f32_16x16x32_bf16 v[40:43],a[184:187],v[156:159],v[40:43]
ds_read_b128 v[136:139],v4 offset:2048
s_add_u32 m0,s64,0
v_mfma_f32_16x16x32_bf16 v[40:43],a[176:179],v[152:155],v[40:43]
buffer_load_dwordx4 v2,s[32:35],s63 offen offset:0 sc1 lds
v_mfma_f32_16x16x32_bf16 v[40:43],a[168:171],v[148:151],v[40:43]
ds_read_b128 v[140:143],v4 offset:3072
v_lshlrev_b32 v38,16,v51
v_mfma_f32_16x16x32_bf16 v[40:43],a[160:163],v[144:147],v[40:43]
v_and_b32 v39,0xffff0000,v51
s_waitcnt lgkmcnt(4)
v_mfma_f32_16x16x32_bf16 v[40:43],a[192:195],v[160:163],v[40:43]
ds_read_b128 v[144:147],v4 offset:4096
v_mfma_f32_16x16x32_bf16 v[40:43],a[200:203],v[164:167],v[40:43]
buffer_load_dwordx4 v2,s[32:35],s63 offen offset:1024 sc1 lds
v_mfma_f32_16x16x32_bf16 v[40:43],a[208:211],v[168:171],v[40:43]
ds_read_b128 v[148:151],v4 offset:5120
v_mfma_f32_16x16x32_bf16 v[40:43],a[216:219],v[172:175],v[40:43]
v_mfma_f32_16x16x32_bf16 v[44:47],a[220:223],v[172:175],v[44:47]
ds_read_b128 v[152:155],v4 offset:6144
v_mfma_f32_16x16x32_bf16 v[44:47],a[212:215],v[168:171],v[44:47]
buffer_load_dwordx4 v2,s[32:35],s63 offen offset:2048 sc1 lds
v_mfma_f32_16x16x32_bf16 v[44:47],a[204:207],v[164:167],v[44:47]
ds_read_b128 v[156:159],v4 offset:7168
v_mfma_f32_16x16x32_bf16 v[44:47],a[196:199],v[160:163],v[44:47]
v_mfma_f32_16x16x32_bf16 v[44:47],a[228:231],v[176:179],v[44:47]
ds_read_b128 v[160:163],v4 offset:8192
v_mfma_f32_16x16x32_bf16 v[44:47],a[236:239],v[180:183],v[44:47]
buffer_load_dwordx4 v2,s[32:35],s63 offen offset:3072 sc1 lds
v_mfma_f32_16x16x32_bf16 v[44:47],a[244:247],v[184:187],v[44:47]
ds_read_b128 v[164:167],v4 offset:9216
v_mfma_f32_16x16x32_bf16 v[44:47],a[252:255],v[188:191],v[44:47]
v_mfma_f32_16x16x32_bf16 v[40:43],a[248:251],v[188:191],v[40:43]
ds_read_b128 v[168:171],v4 offset:10240
s_add_u32 m0,s64,4096
v_mfma_f32_16x16x32_bf16 v[40:43],a[240:243],v[184:187],v[40:43]
buffer_load_dwordx4 v6,s[32:35],s63 offen offset:0 sc1 lds
v_mfma_f32_16x16x32_bf16 v[40:43],a[232:235],v[180:183],v[40:43]
ds_read_b128 v[172:175],v4 offset:11264
s_sub_u32 s62,s62,1
v_mfma_f32_16x16x32_bf16 v[40:43],a[224:227],v[176:179],v[40:43]
s_cbranch_scc0 .L_wl_3
s_nop 1
s_add_u32 s68,s68,s30
s_nop 3
v_cvt_pk_bf16_f32 v56,v40,v41
v_cvt_pk_bf16_f32 v57,v42,v43
v_cvt_pk_bf16_f32 v58,v44,v45
v_cvt_pk_bf16_f32 v59,v46,v47
buffer_store_dwordx4 v[56:59],v2,s[32:35],s68 offen sc1
v_add_f32 v60,v40,v41
v_mul_f32 v61,v40,v40
v_fmac_f32 v61,v41,v41
v_add_f32 v60,v60,v42
v_fmac_f32 v61,v42,v42
v_add_f32 v60,v60,v43
v_fmac_f32 v61,v43,v43
)#"
R"#(
v_add_f32 v60,v60,v44
v_fmac_f32 v61,v44,v44
v_add_f32 v60,v60,v45
v_fmac_f32 v61,v45,v45
v_add_f32 v60,v60,v46
v_fmac_f32 v61,v46,v46
v_add_f32 v60,v60,v47
v_fmac_f32 v61,v47,v47
s_nop 1
v_permlane16_swap_b32 v60,v61
v_add_f32 v60,v60,v61
s_nop 1
v_permlane32_swap_b32 v60,v61
v_add_f32 v60,v60,v61
buffer_store_dword v60,v15,s[32:35],s68 offen offset:1024 sc1
s_waitcnt vmcnt(9)
s_barrier
buffer_store_dwordx2 v[16:17],v2,s[92:95],s68 idxen sc1
s_add_u32 s68,s68,s30
s_waitcnt vmcnt(1)
s_barrier
buffer_store_dwordx2 v[16:17],v2,s[92:95],s68 idxen sc1
s_cbranch_execz .L_wbk
s_branch .L_end
.L_h:
v_mov_b32 v16,0x3f803f80
v_mov_b32 v17,0x3f803f80
v_mov_b32 v18,0x3f803f80
v_mov_b32 v19,0x3f803f80
s_add_u32 s36,s14,0x0
s_addc_u32 s37,s15,0x0
s_and_b32 s37,s37,0xffff
s_mov_b32 s38,0x700000
s_mov_b32 s39,0x20000
s_add_u32 s40,s16,0xfffffc00
s_addc_u32 s41,s17,0xffffffff
s_and_b32 s41,s41,0xffff
s_mov_b32 s42,0x700400
s_mov_b32 s43,0x20000
s_lshl_b32 s96,s27,7
s_add_u32 s96,s96,6912
v_and_b32 v8,1,v1
v_bfe_u32 v3,v1,1,1
v_lshl_add_u32 v8,v8,1,v3
v_mul_u32_u24 v8,8,v8
s_lshl_b32 s48,s26,5
v_add_u32 v8,s48,v8
v_bfe_u32 v3,v1,4,1
v_lshl_add_u32 v8,v3,10,v8
v_lshrrev_b32 v4,3,v1
v_bfe_u32 v5,v1,4,1
s_and_b32 s48,s26,1
s_lshl_b32 s48,s48,1
v_or_b32 v5,s48,v5
v_lshlrev_b32 v5,1,v5
v_and_b32 v6,7,v1
v_xor_b32 v5,v5,v6
v_mul_u32_u24 v4,7168,v4
v_lshl_add_u32 v7,v5,4,v4
s_mul_i32 s48,s26,57344
s_add_u32 s96,s96,s48
s_mul_i32 s97,s26,2048
s_add_u32 m0,s97,0
s_nop 0
buffer_load_dwordx4 v7,s[36:39],s96 offen offset:0 lds
buffer_load_dwordx4 v7,s[40:43],s96 offen offset:1024 lds
s_add_u32 s96,s96,229376
s_add_u32 m0,s97,8192
s_nop 0
buffer_load_dwordx4 v7,s[36:39],s96 offen offset:0 lds
buffer_load_dwordx4 v7,s[40:43],s96 offen offset:1024 lds
s_add_u32 s96,s96,229376
s_add_u32 m0,s97,16384
s_nop 0
buffer_load_dwordx4 v7,s[36:39],s96 offen offset:0 lds
buffer_load_dwordx4 v7,s[40:43],s96 offen offset:1024 lds
s_add_u32 s96,s96,229376
s_add_u32 m0,s97,24576
s_nop 0
buffer_load_dwordx4 v7,s[36:39],s96 offen offset:0 lds
buffer_load_dwordx4 v7,s[40:43],s96 offen offset:1024 lds
s_add_u32 s96,s96,229376
s_add_u32 m0,s97,32768
s_nop 0
buffer_load_dwordx4 v7,s[36:39],s96 offen offset:0 lds
buffer_load_dwordx4 v7,s[40:43],s96 offen offset:1024 lds
s_add_u32 s96,s96,229376
s_add_u32 m0,s97,40960
s_nop 0
buffer_load_dwordx4 v7,s[36:39],s96 offen offset:0 lds
buffer_load_dwordx4 v7,s[40:43],s96 offen offset:1024 lds
s_add_u32 s96,s96,229376
s_add_u32 m0,s97,49152
s_nop 0
buffer_load_dwordx4 v7,s[36:39],s96 offen offset:0 lds
buffer_load_dwordx4 v7,s[40:43],s96 offen offset:1024 lds
s_add_u32 s96,s96,229376
s_add_u32 m0,s97,57344
s_nop 0
buffer_load_dwordx4 v7,s[36:39],s96 offen offset:0 lds
buffer_load_dwordx4 v7,s[40:43],s96 offen offset:1024 lds
s_add_u32 s96,s96,229376
s_add_u32 m0,s97,65536
s_nop 0
buffer_load_dwordx4 v7,s[36:39],s96 offen offset:0 lds
buffer_load_dwordx4 v7,s[40:43],s96 offen offset:1024 lds
s_add_u32 s96,s96,229376
s_add_u32 m0,s97,73728
s_nop 0
buffer_load_dwordx4 v7,s[36:39],s96 offen offset:0 lds
buffer_load_dwordx4 v7,s[40:43],s96 offen offset:1024 lds
s_add_u32 s96,s96,229376
s_add_u32 m0,s97,81920
s_nop 0
buffer_load_dwordx4 v7,s[36:39],s96 offen offset:0 lds
buffer_load_dwordx4 v7,s[40:43],s96 offen offset:1024 lds
s_add_u32 s96,s96,229376
s_add_u32 m0,s97,90112
s_nop 0
buffer_load_dwordx4 v7,s[36:39],s96 offen offset:0 lds
buffer_load_dwordx4 v7,s[40:43],s96 offen offset:1024 lds
s_add_u32 s96,s96,229376
s_add_u32 m0,s97,98304
s_nop 0
buffer_load_dwordx4 v7,s[36:39],s96 offen offset:0 lds
buffer_load_dwordx4 v7,s[40:43],s96 offen offset:1024 lds
s_add_u32 s96,s96,229376
s_add_u32 m0,s97,106496
s_nop 0
buffer_load_dwordx4 v7,s[36:39],s96 offen offset:0 lds
buffer_load_dwordx4 v7,s[40:43],s96 offen offset:1024 lds
s_add_u32 s96,s96,229376
v_lshrrev_b32 v5,5,v1
v_bfe_u32 v4,v1,3,1
v_and_b32 v6,1,v5
v_lshl_add_u32 v4,v6,1,v4
v_lshlrev_b32 v4,5,v4
v_mul_u32_u24 v5,2048,v5
v_bfe_u32 v6,v1,2,2
v_lshl_add_u32 v5,v6,7,v5
v_xor_b32 v8,v4,v8
v_add_u32 v8,v5,v8
v_add_u32 v9,65536,v8
s_waitcnt vmcnt(24)
s_barrier
s_add_u32 m0,s97,114688
s_nop 0
buffer_load_dwordx4 v7,s[36:39],s96 offen offset:0 lds
buffer_load_dwordx4 v7,s[40:43],s96 offen offset:1024 lds
s_add_u32 s96,s96,229376
s_add_u32 m0,s97,122880
s_nop 0
buffer_load_dwordx4 v7,s[36:39],s96 offen offset:0 lds
buffer_load_dwordx4 v7,s[40:43],s96 offen offset:1024 lds
s_add_u32 s96,s96,229376
ds_read_b64_tr_b16 a[0:1],v8 offset:0
ds_read_b64_tr_b16 a[2:3],v8 offset:512
ds_read_b64_tr_b16 a[4:5],v8 offset:4096
ds_read_b64_tr_b16 a[6:7],v8 offset:4608
ds_read_b64_tr_b16 a[32:33],v8 offset:8192
ds_read_b64_tr_b16 a[34:35],v8 offset:8704
ds_read_b64_tr_b16 a[36:37],v8 offset:12288
ds_read_b64_tr_b16 a[38:39],v8 offset:12800
s_waitcnt lgkmcnt(0)
v_mfma_f32_32x32x16_bf16 v[112:127],a[0:3],v[16:19],0
v_mfma_f32_32x32x16_bf16 v[112:127],a[4:7],v[16:19],v[112:127]
v_mfma_f32_32x32x16_bf16 v[112:127],a[32:35],v[16:19],v[112:127]
v_mfma_f32_32x32x16_bf16 v[112:127],a[36:39],v[16:19],v[112:127]
s_waitcnt vmcnt(24)
s_barrier
s_add_u32 m0,s97,0
s_nop 0
buffer_load_dwordx4 v7,s[36:39],s96 offen offset:0 lds
buffer_load_dwordx4 v7,s[40:43],s96 offen offset:1024 lds
s_add_u32 s96,s96,229376
s_add_u32 m0,s97,8192
s_nop 0
buffer_load_dwordx4 v7,s[36:39],s96 offen offset:0 lds
buffer_load_dwordx4 v7,s[40:43],s96 offen offset:1024 lds
s_add_u32 s96,s96,229376
ds_read_b64_tr_b16 a[64:65],v8 offset:16384
ds_read_b64_tr_b16 a[66:67],v8 offset:16896
ds_read_b64_tr_b16 a[68:69],v8 offset:20480
ds_read_b64_tr_b16 a[70:71],v8 offset:20992
ds_read_b64_tr_b16 a[96:97],v8 offset:24576
ds_read_b64_tr_b16 a[98:99],v8 offset:25088
ds_read_b64_tr_b16 a[100:101],v8 offset:28672
ds_read_b64_tr_b16 a[102:103],v8 offset:29184
s_waitcnt lgkmcnt(0)
v_mfma_f32_32x32x16_bf16 v[112:127],a[64:67],v[16:19],v[112:127]
v_mfma_f32_32x32x16_bf16 v[112:127],a[68:71],v[16:19],v[112:127]
v_mfma_f32_32x32x16_bf16 v[112:127],a[96:99],v[16:19],v[112:127]
v_mfma_f32_32x32x16_bf16 v[112:127],a[100:103],v[16:19],v[112:127]
s_waitcnt vmcnt(24)
s_barrier
s_add_u32 m0,s97,16384
s_nop 0
buffer_load_dwordx4 v7,s[36:39],s96 offen offset:0 lds
buffer_load_dwordx4 v7,s[40:43],s96 offen offset:1024 lds
s_add_u32 s96,s96,229376
s_add_u32 m0,s97,24576
s_nop 0
buffer_load_dwordx4 v7,s[36:39],s96 offen offset:0 lds
buffer_load_dwordx4 v7,s[40:43],s96 offen offset:1024 lds
s_add_u32 s96,s96,229376
ds_read_b64_tr_b16 a[128:129],v8 offset:32768
ds_read_b64_tr_b16 a[130:131],v8 offset:33280
ds_read_b64_tr_b16 a[132:133],v8 offset:36864
ds_read_b64_tr_b16 a[134:135],v8 offset:37376
ds_read_b64_tr_b16 a[160:161],v8 offset:40960
ds_read_b64_tr_b16 a[162:163],v8 offset:41472
ds_read_b64_tr_b16 a[164:165],v8 offset:45056
ds_read_b64_tr_b16 a[166:167],v8 offset:45568
s_waitcnt lgkmcnt(0)
v_mfma_f32_32x32x16_bf16 v[112:127],a[128:131],v[16:19],v[112:127]
v_mfma_f32_32x32x16_bf16 v[112:127],a[132:135],v[16:19],v[112:127]
v_mfma_f32_32x32x16_bf16 v[112:127],a[160:163],v[16:19],v[112:127]
v_mfma_f32_32x32x16_bf16 v[112:127],a[164:167],v[16:19],v[112:127]
s_waitcnt vmcnt(24)
s_barrier
s_add_u32 m0,s97,32768
s_nop 0
buffer_load_dwordx4 v7,s[36:39],s96 offen offset:0 lds
buffer_load_dwordx4 v7,s[40:43],s96 offen offset:1024 lds
s_add_u32 s96,s96,229376
s_add_u32 m0,s97,40960
s_nop 0
buffer_load_dwordx4 v7,s[36:39],s96 offen offset:0 lds
buffer_load_dwordx4 v7,s[40:43],s96 offen offset:1024 lds
s_add_u32 s96,s96,229376
ds_read_b64_tr_b16 a[192:193],v8 offset:49152
ds_read_b64_tr_b16 a[194:195],v8 offset:49664
ds_read_b64_tr_b16 a[196:197],v8 offset:53248
ds_read_b64_tr_b16 a[198:199],v8 offset:53760
ds_read_b64_tr_b16 a[224:225],v8 offset:57344
ds_read_b64_tr_b16 a[226:227],v8 offset:57856
ds_read_b64_tr_b16 a[228:229],v8 offset:61440
ds_read_b64_tr_b16 a[230:231],v8 offset:61952
s_waitcnt lgkmcnt(0)
v_mfma_f32_32x32x16_bf16 v[112:127],a[192:195],v[16:19],v[112:127]
v_mfma_f32_32x32x16_bf16 v[112:127],a[196:199],v[16:19],v[112:127]
v_mfma_f32_32x32x16_bf16 v[112:127],a[224:227],v[16:19],v[112:127]
v_mfma_f32_32x32x16_bf16 v[112:127],a[228:231],v[16:19],v[112:127]
s_waitcnt vmcnt(24)
s_barrier
s_add_u32 m0,s97,49152
s_nop 0
buffer_load_dwordx4 v7,s[36:39],s96 offen offset:0 lds
buffer_load_dwordx4 v7,s[40:43],s96 offen offset:1024 lds
s_add_u32 s96,s96,229376
s_add_u32 m0,s97,57344
s_nop 0
buffer_load_dwordx4 v7,s[36:39],s96 offen offset:0 lds
buffer_load_dwordx4 v7,s[40:43],s96 offen offset:1024 lds
s_add_u32 s96,s96,229376
ds_read_b64_tr_b16 a[8:9],v9 offset:0
ds_read_b64_tr_b16 a[10:11],v9 offset:512
ds_read_b64_tr_b16 a[12:13],v9 offset:4096
ds_read_b64_tr_b16 a[14:15],v9 offset:4608
ds_read_b64_tr_b16 a[40:41],v9 offset:8192
ds_read_b64_tr_b16 a[42:43],v9 offset:8704
ds_read_b64_tr_b16 a[44:45],v9 offset:12288
ds_read_b64_tr_b16 a[46:47],v9 offset:12800
s_waitcnt lgkmcnt(0)
v_mfma_f32_32x32x16_bf16 v[112:127],a[8:11],v[16:19],v[112:127]
v_mfma_f32_32x32x16_bf16 v[112:127],a[12:15],v[16:19],v[112:127]
v_mfma_f32_32x32x16_bf16 v[112:127],a[40:43],v[16:19],v[112:127]
v_mfma_f32_32x32x16_bf16 v[112:127],a[44:47],v[16:19],v[112:127]
s_waitcnt vmcnt(24)
s_barrier
s_add_u32 m0,s97,65536
s_nop 0
buffer_load_dwordx4 v7,s[36:39],s96 offen offset:0 lds
buffer_load_dwordx4 v7,s[40:43],s96 offen offset:1024 lds
s_add_u32 s96,s96,229376
s_add_u32 m0,s97,73728
s_nop 0
buffer_load_dwordx4 v7,s[36:39],s96 offen offset:0 lds
buffer_load_dwordx4 v7,s[40:43],s96 offen offset:1024 lds
s_add_u32 s96,s96,229376
ds_read_b64_tr_b16 a[72:73],v9 offset:16384
ds_read_b64_tr_b16 a[74:75],v9 offset:16896
ds_read_b64_tr_b16 a[76:77],v9 offset:20480
ds_read_b64_tr_b16 a[78:79],v9 offset:20992
ds_read_b64_tr_b16 a[104:105],v9 offset:24576
ds_read_b64_tr_b16 a[106:107],v9 offset:25088
ds_read_b64_tr_b16 a[108:109],v9 offset:28672
ds_read_b64_tr_b16 a[110:111],v9 offset:29184
s_waitcnt lgkmcnt(0)
v_mfma_f32_32x32x16_bf16 v[112:127],a[72:75],v[16:19],v[112:127]
v_mfma_f32_32x32x16_bf16 v[112:127],a[76:79],v[16:19],v[112:127]
v_mfma_f32_32x32x16_bf16 v[112:127],a[104:107],v[16:19],v[112:127]
v_mfma_f32_32x32x16_bf16 v[112:127],a[108:111],v[16:19],v[112:127]
s_waitcnt vmcnt(24)
s_barrier
s_add_u32 m0,s97,81920
s_nop 0
buffer_load_dwordx4 v7,s[36:39],s96 offen offset:0 lds
buffer_load_dwordx4 v7,s[40:43],s96 offen offset:1024 lds
s_add_u32 s96,s96,229376
s_add_u32 m0,s97,90112
s_nop 0
buffer_load_dwordx4 v7,s[36:39],s96 offen offset:0 lds
buffer_load_dwordx4 v7,s[40:43],s96 offen offset:1024 lds
s_add_u32 s96,s96,229376
ds_read_b64_tr_b16 a[136:137],v9 offset:32768
ds_read_b64_tr_b16 a[138:139],v9 offset:33280
ds_read_b64_tr_b16 a[140:141],v9 offset:36864
ds_read_b64_tr_b16 a[142:143],v9 offset:37376
ds_read_b64_tr_b16 a[168:169],v9 offset:40960
ds_read_b64_tr_b16 a[170:171],v9 offset:41472
ds_read_b64_tr_b16 a[172:173],v9 offset:45056
ds_read_b64_tr_b16 a[174:175],v9 offset:45568
s_waitcnt lgkmcnt(0)
v_mfma_f32_32x32x16_bf16 v[112:127],a[136:139],v[16:19],v[112:127]
v_mfma_f32_32x32x16_bf16 v[112:127],a[140:143],v[16:19],v[112:127]
v_mfma_f32_32x32x16_bf16 v[112:127],a[168:171],v[16:19],v[112:127]
v_mfma_f32_32x32x16_bf16 v[112:127],a[172:175],v[16:19],v[112:127]
s_waitcnt vmcnt(24)
s_barrier
s_add_u32 m0,s97,98304
s_nop 0
buffer_load_dwordx4 v7,s[36:39],s96 offen offset:0 lds
buffer_load_dwordx4 v7,s[40:43],s96 offen offset:1024 lds
s_add_u32 s96,s96,229376
s_add_u32 m0,s97,106496
s_nop 0
buffer_load_dwordx4 v7,s[36:39],s96 offen offset:0 lds
buffer_load_dwordx4 v7,s[40:43],s96 offen offset:1024 lds
s_add_u32 s96,s96,229376
ds_read_b64_tr_b16 a[200:201],v9 offset:49152
ds_read_b64_tr_b16 a[202:203],v9 offset:49664
ds_read_b64_tr_b16 a[204:205],v9 offset:53248
ds_read_b64_tr_b16 a[206:207],v9 offset:53760
ds_read_b64_tr_b16 a[232:233],v9 offset:57344
ds_read_b64_tr_b16 a[234:235],v9 offset:57856
ds_read_b64_tr_b16 a[236:237],v9 offset:61440
ds_read_b64_tr_b16 a[238:239],v9 offset:61952
s_waitcnt lgkmcnt(0)
v_mfma_f32_32x32x16_bf16 v[112:127],a[200:203],v[16:19],v[112:127]
v_mfma_f32_32x32x16_bf16 v[112:127],a[204:207],v[16:19],v[112:127]
v_mfma_f32_32x32x16_bf16 v[112:127],a[232:235],v[16:19],v[112:127]
v_mfma_f32_32x32x16_bf16 v[112:127],a[236:239],v[16:19],v[112:127]
s_waitcnt vmcnt(24)
s_barrier
s_add_u32 m0,s97,114688
s_nop 0
buffer_load_dwordx4 v7,s[36:39],s96 offen offset:0 lds
buffer_load_dwordx4 v7,s[40:43],s96 offen offset:1024 lds
s_add_u32 s96,s96,229376
s_add_u32 m0,s97,122880
s_nop 0
buffer_load_dwordx4 v7,s[36:39],s96 offen offset:0 lds
buffer_load_dwordx4 v7,s[40:43],s96 offen offset:1024 lds
s_add_u32 s96,s96,229376
ds_read_b64_tr_b16 a[16:17],v8 offset:0
ds_read_b64_tr_b16 a[18:19],v8 offset:512
ds_read_b64_tr_b16 a[20:21],v8 offset:4096
ds_read_b64_tr_b16 a[22:23],v8 offset:4608
ds_read_b64_tr_b16 a[48:49],v8 offset:8192
ds_read_b64_tr_b16 a[50:51],v8 offset:8704
ds_read_b64_tr_b16 a[52:53],v8 offset:12288
ds_read_b64_tr_b16 a[54:55],v8 offset:12800
s_waitcnt lgkmcnt(0)
v_mfma_f32_32x32x16_bf16 v[112:127],a[16:19],v[16:19],v[112:127]
v_mfma_f32_32x32x16_bf16 v[112:127],a[20:23],v[16:19],v[112:127]
v_mfma_f32_32x32x16_bf16 v[112:127],a[48:51],v[16:19],v[112:127]
v_mfma_f32_32x32x16_bf16 v[112:127],a[52:55],v[16:19],v[112:127]
s_waitcnt vmcnt(24)
s_barrier
ds_read_b64_tr_b16 a[80:81],v8 offset:16384
ds_read_b64_tr_b16 a[82:83],v8 offset:16896
ds_read_b64_tr_b16 a[84:85],v8 offset:20480
ds_read_b64_tr_b16 a[86:87],v8 offset:20992
ds_read_b64_tr_b16 a[112:113],v8 offset:24576
ds_read_b64_tr_b16 a[114:115],v8 offset:25088
ds_read_b64_tr_b16 a[116:117],v8 offset:28672
ds_read_b64_tr_b16 a[118:119],v8 offset:29184
s_waitcnt lgkmcnt(0)
v_mfma_f32_32x32x16_bf16 v[112:127],a[80:83],v[16:19],v[112:127]
v_mfma_f32_32x32x16_bf16 v[112:127],a[84:87],v[16:19],v[112:127]
v_mfma_f32_32x32x16_bf16 v[112:127],a[112:115],v[16:19],v[112:127]
v_mfma_f32_32x32x16_bf16 v[112:127],a[116:119],v[16:19],v[112:127]
s_waitcnt vmcnt(20)
s_barrier
ds_read_b64_tr_b16 a[144:145],v8 offset:32768
ds_read_b64_tr_b16 a[146:147],v8 offset:33280
ds_read_b64_tr_b16 a[148:149],v8 offset:36864
ds_read_b64_tr_b16 a[150:151],v8 offset:37376
ds_read_b64_tr_b16 a[176:177],v8 offset:40960
ds_read_b64_tr_b16 a[178:179],v8 offset:41472
ds_read_b64_tr_b16 a[180:181],v8 offset:45056
ds_read_b64_tr_b16 a[182:183],v8 offset:45568
s_waitcnt lgkmcnt(0)
v_mfma_f32_32x32x16_bf16 v[112:127],a[144:147],v[16:19],v[112:127]
v_mfma_f32_32x32x16_bf16 v[112:127],a[148:151],v[16:19],v[112:127]
v_mfma_f32_32x32x16_bf16 v[112:127],a[176:179],v[16:19],v[112:127]
v_mfma_f32_32x32x16_bf16 v[112:127],a[180:183],v[16:19],v[112:127]
s_waitcnt vmcnt(16)
s_barrier
ds_read_b64_tr_b16 a[208:209],v8 offset:49152
ds_read_b64_tr_b16 a[210:211],v8 offset:49664
ds_read_b64_tr_b16 a[212:213],v8 offset:53248
ds_read_b64_tr_b16 a[214:215],v8 offset:53760
ds_read_b64_tr_b16 a[240:241],v8 offset:57344
ds_read_b64_tr_b16 a[242:243],v8 offset:57856
ds_read_b64_tr_b16 a[244:245],v8 offset:61440
ds_read_b64_tr_b16 a[246:247],v8 offset:61952
s_waitcnt lgkmcnt(0)
v_mfma_f32_32x32x16_bf16 v[112:127],a[208:211],v[16:19],v[112:127]
v_mfma_f32_32x32x16_bf16 v[112:127],a[212:215],v[16:19],v[112:127]
v_mfma_f32_32x32x16_bf16 v[112:127],a[240:243],v[16:19],v[112:127]
v_mfma_f32_32x32x16_bf16 v[112:127],a[244:247],v[16:19],v[112:127]
s_waitcnt vmcnt(12)
s_barrier
ds_read_b64_tr_b16 a[24:25],v9 offset:0
ds_read_b64_tr_b16 a[26:27],v9 offset:512
ds_read_b64_tr_b16 a[28:29],v9 offset:4096
ds_read_b64_tr_b16 a[30:31],v9 offset:4608
ds_read_b64_tr_b16 a[56:57],v9 offset:8192
ds_read_b64_tr_b16 a[58:59],v9 offset:8704
ds_read_b64_tr_b16 a[60:61],v9 offset:12288
ds_read_b64_tr_b16 a[62:63],v9 offset:12800
s_waitcnt lgkmcnt(0)
v_mfma_f32_32x32x16_bf16 v[112:127],a[24:27],v[16:19],v[112:127]
v_mfma_f32_32x32x16_bf16 v[112:127],a[28:31],v[16:19],v[112:127]
v_mfma_f32_32x32x16_bf16 v[112:127],a[56:59],v[16:19],v[112:127]
v_mfma_f32_32x32x16_bf16 v[112:127],a[60:63],v[16:19],v[112:127]
s_waitcnt vmcnt(8)
s_barrier
ds_read_b64_tr_b16 a[88:89],v9 offset:16384
ds_read_b64_tr_b16 a[90:91],v9 offset:16896
ds_read_b64_tr_b16 a[92:93],v9 offset:20480
ds_read_b64_tr_b16 a[94:95],v9 offset:20992
ds_read_b64_tr_b16 a[120:121],v9 offset:24576
ds_read_b64_tr_b16 a[122:123],v9 offset:25088
ds_read_b64_tr_b16 a[124:125],v9 offset:28672
ds_read_b64_tr_b16 a[126:127],v9 offset:29184
s_waitcnt lgkmcnt(0)
v_mfma_f32_32x32x16_bf16 v[112:127],a[88:91],v[16:19],v[112:127]
v_mfma_f32_32x32x16_bf16 v[112:127],a[92:95],v[16:19],v[112:127]
v_mfma_f32_32x32x16_bf16 v[112:127],a[120:123],v[16:19],v[112:127]
v_mfma_f32_32x32x16_bf16 v[112:127],a[124:127],v[16:19],v[112:127]
s_waitcnt vmcnt(4)
s_barrier
ds_read_b64_tr_b16 a[152:153],v9 offset:32768
ds_read_b64_tr_b16 a[154:155],v9 offset:33280
ds_read_b64_tr_b16 a[156:157],v9 offset:36864
ds_read_b64_tr_b16 a[158:159],v9 offset:37376
ds_read_b64_tr_b16 a[184:185],v9 offset:40960
ds_read_b64_tr_b16 a[186:187],v9 offset:41472
ds_read_b64_tr_b16 a[188:189],v9 offset:45056
ds_read_b64_tr_b16 a[190:191],v9 offset:45568
s_waitcnt lgkmcnt(0)
v_mfma_f32_32x32x16_bf16 v[112:127],a[152:155],v[16:19],v[112:127]
v_mfma_f32_32x32x16_bf16 v[112:127],a[156:159],v[16:19],v[112:127]
v_mfma_f32_32x32x16_bf16 v[112:127],a[184:187],v[16:19],v[112:127]
v_mfma_f32_32x32x16_bf16 v[112:127],a[188:191],v[16:19],v[112:127]
s_waitcnt vmcnt(0)
s_barrier
ds_read_b64_tr_b16 a[216:217],v9 offset:49152
ds_read_b64_tr_b16 a[218:219],v9 offset:49664
ds_read_b64_tr_b16 a[220:221],v9 offset:53248
ds_read_b64_tr_b16 a[222:223],v9 offset:53760
ds_read_b64_tr_b16 a[248:249],v9 offset:57344
ds_read_b64_tr_b16 a[250:251],v9 offset:57856
ds_read_b64_tr_b16 a[252:253],v9 offset:61440
ds_read_b64_tr_b16 a[254:255],v9 offset:61952
s_waitcnt lgkmcnt(0)
v_mfma_f32_32x32x16_bf16 v[112:127],a[216:219],v[16:19],v[112:127]
v_mfma_f32_32x32x16_bf16 v[112:127],a[220:223],v[16:19],v[112:127]
v_mfma_f32_32x32x16_bf16 v[112:127],a[248:251],v[16:19],v[112:127]
v_mfma_f32_32x32x16_bf16 v[112:127],a[252:255],v[16:19],v[112:127]
s_barrier
s_lshl2_add_u32 s49,s27,272
s_lshl2_add_u32 s68,s27,s26
s_lshl_b32 s68,s68,9
s_add_u32 s68,s68,4829184
s_mov_b32 s63,4681728
s_mov_b32 s30,1187840
s_mul_i32 s57,s28,593920
s_mov_b32 s56,76021760
s_cmp_lg_u32 s25,0
s_cmov_b32 s30,593920
s_cmov_b32 s57,81960960
s_cmov_b32 s56,81960960
s_lshl_b32 s31,s30,1
s_waitcnt lgkmcnt(0)
s_add_u32 s54,s52,0x7f4a7c15
s_addc_u32 s55,s53,0x9e3779b9
s_lshl_b32 s67,s49,5
s_add_u32 s67,s67,4898816
s_sub_u32 s51,s67,s68
s_add_u32 s92,s32,s51
s_addc_u32 s93,s33,0x10000
s_cmp_eq_u32 s26,0
s_cselect_b32 s94,1,0
s_mov_b32 s95,0x20000
v_mov_b32 v16,s54
v_mov_b32 v17,s55
)#"
R"#(
v_cmp_gt_u32 vcc,12,v1
v_mul_u32_u24 v3,296960,v1
v_mov_b32 v12,0x7fff0000
v_cndmask_b32 v3,v12,v3,vcc
s_add_u32 s51,s67,s56
buffer_store_dwordx2 v[16:17],v3,s[0:3],s51 offen sc1
s_sub_u32 s67,s68,296960
s_sub_u32 s51,s51,s56
s_sub_u32 s51,s51,s68
v_mul_u32_u24 v3,296960,v1
v_add_u32 v3,s51,v3
v_subrev_u32 v18,0,v0
v_cmp_gt_u32 vcc,2,v18
v_mov_b32 v18,0x7fff0000
s_nop 0
v_cndmask_b32 v18,v18,v3,vcc
s_add_u32 s68,s68,s57
s_sub_u32 s68,s68,s31
s_add_u32 s63,s63,s57
s_add_u32 s66,s57,4898816
s_mul_i32 s64,s26,9216
s_add_u32 s63,s63,s64
s_mov_b32 s62,31
s_cmp_lg_u32 s28,0
s_cmov_b32 s62,31
v_and_b32 v4,0xf0,v2
v_lshrrev_b32 v3,5,v1
v_lshl_add_u32 v4,v3,8,v4
v_bfe_u32 v3,v1,4,1
v_mul_u32_u24 v15,296960,v3
v_add_u32 v15,v15,v4
v_add_u32 v15,0,v15
v_mul_u32_u24 v3,36864,v3
v_add_u32 v4,v3,v4
v_add_u32 v5,73728,v4
v_add_u32 v20,0,v2
v_add_u32 v21,296960,v2
v_add_u32 v22,4096,v2
v_add_u32 v23,301056,v2
v_add_u32 v24,5120,v2
v_add_u32 v25,302080,v2
v_and_b32 v14,7,v1
s_mul_i32 s48,s26,8
s_add_u32 s48,s48,96
v_add_u32 v14,s48,v14
v_lshrrev_b32 v14,2,v14
v_lshlrev_b32 v14,7,v14
v_lshrrev_b32 v3,4,v1
v_mul_u32_u24 v3,296960,v3
v_add_u32 v14,v3,v14
v_cmp_gt_u32 vcc,32,v1
v_mov_b32 v3,0x7fff0000
s_nop 0
v_cndmask_b32 v14,v3,v14,vcc
v_cndmask_b32 v16,0,v16,vcc
v_cndmask_b32 v17,0,v17,vcc
v_bfe_u32 v3,v1,1,3
v_mul_u32_u24 v8,1152,v3
v_lshrrev_b32 v3,1,v3
v_add_u32 v3,s26,v3
v_lshrrev_b32 v9,4,v1
v_lshl_add_u32 v3,v3,2,v9
v_and_b32 v3,15,v3
v_lshl_add_u32 v8,v3,2,v8
v_and_b32 v3,1,v1
v_lshl_add_u32 v8,v3,6,v8
v_add_u32 v8,1024,v8
s_lshl_b32 s48,s26,2
v_add_u32 v10,s48,v9
v_and_b32 v11,15,v1
v_add_u32 v3,64,v1
v_cmp_eq_u32 vcc,0,v11
v_add_u32 v9,73728,v8
v_xor_b32 v9,v9,v8
v_cndmask_b32 v10,v3,v10,vcc
v_lshlrev_b32 v10,2,v10
v_add_u32 v10,147456,v10
v_lshlrev_b32 v11,2,v11
v_add_u32 v11,147456,v11
v_and_b32 v3,15,v1
v_cmp_eq_u32 vcc,8,v3
v_lshrrev_b32 v3,4,v1
s_lshl2_add_u32 s48,s26,36880
v_add_lshl_u32 v3,v3,s48,2
v_cndmask_b32 v10,v10,v3,vcc
v_and_b32 v3,16,v1
v_lshl_add_u32 v11,v3,2,v11
s_cmp_lg_u32 s25,0
s_cbranch_scc1 .L_hnd
s_mov_b64 s[8:9],s[62:63]
s_mov_b64 s[10:11],s[64:65]
s_mov_b64 s[12:13],s[66:67]
s_mov_b64 s[14:15],s[68:69]
s_mov_b64 s[16:17],s[70:71]
s_mov_b64 s[18:19],s[72:73]
s_mov_b64 s[20:21],s[74:75]
s_mov_b32 s62,0
s_mov_b32 s70,-1
s_mov_b64 vcc,0
s_mov_b64 exec,0
s_branch .L_hgo
.L_hbk:
s_mov_b64 exec,-1
s_mov_b64 s[62:63],s[8:9]
s_mov_b64 s[64:65],s[10:11]
s_mov_b64 s[66:67],s[12:13]
s_mov_b64 s[68:69],s[14:15]
s_mov_b64 s[70:71],s[16:17]
s_mov_b64 s[72:73],s[18:19]
s_mov_b64 s[74:75],s[20:21]
s_waitcnt vmcnt(0) lgkmcnt(0)
.L_hnd:
s_mov_b32 s48,20000
s_branch .L_rfha
.L_rlha:
s_sleep 2
.L_rfha:
buffer_load_dwordx2 v[12:13],v14,s[0:3],s66 offen sc1
s_sub_u32 s48,s48,1
s_cbranch_scc1 .L_end
s_waitcnt vmcnt(0)
v_cmp_ne_u64 vcc,v[16:17],v[12:13]
s_cbranch_vccnz .L_rlha
s_add_u32 s66,s66,s30
s_add_u32 m0,s64,0
s_nop 0
buffer_load_dwordx4 v20,s[32:35],s63 offen offset:0 sc1 lds
buffer_load_dwordx4 v20,s[32:35],s63 offen offset:1024 sc1 lds
buffer_load_dwordx4 v20,s[32:35],s63 offen offset:2048 sc1 lds
buffer_load_dwordx4 v20,s[32:35],s63 offen offset:3072 sc1 lds
s_add_u32 m0,s64,36864
s_nop 0
buffer_load_dwordx4 v21,s[32:35],s63 offen offset:0 sc1 lds
buffer_load_dwordx4 v21,s[32:35],s63 offen offset:1024 sc1 lds
buffer_load_dwordx4 v21,s[32:35],s63 offen offset:2048 sc1 lds
buffer_load_dwordx4 v21,s[32:35],s63 offen offset:3072 sc1 lds
s_add_u32 m0,s64,4096
s_nop 0
buffer_load_dwordx4 v22,s[32:35],s63 offen offset:0 sc1 lds
s_add_u32 m0,s64,40960
s_nop 0
buffer_load_dwordx4 v23,s[32:35],s63 offen offset:0 sc1 lds
s_add_u32 m0,s64,5120
s_nop 0
buffer_load_dwordx4 v24,s[32:35],s63 offen offset:0 sc1 lds
buffer_load_dwordx4 v24,s[32:35],s63 offen offset:1024 sc1 lds
buffer_load_dwordx4 v24,s[32:35],s63 offen offset:2048 sc1 lds
buffer_load_dwordx4 v24,s[32:35],s63 offen offset:3072 sc1 lds
s_add_u32 m0,s64,41984
s_nop 0
buffer_load_dwordx4 v25,s[32:35],s63 offen offset:0 sc1 lds
buffer_load_dwordx4 v25,s[32:35],s63 offen offset:1024 sc1 lds
buffer_load_dwordx4 v25,s[32:35],s63 offen offset:2048 sc1 lds
buffer_load_dwordx4 v25,s[32:35],s63 offen offset:3072 sc1 lds
s_add_u32 s63,s63,s30
s_waitcnt vmcnt(8)
s_barrier
ds_read_b128 v[128:131],v4 offset:0
ds_read_b128 v[132:135],v4 offset:512
ds_read_b128 v[136:139],v4 offset:9216
ds_read_b128 v[140:143],v4 offset:9728
ds_read_b128 v[144:147],v4 offset:18432
ds_read_b128 v[148:151],v4 offset:18944
ds_read_b128 v[152:155],v4 offset:27648
ds_read_b128 v[156:159],v4 offset:28160
s_mov_b32 s48,20000
s_branch .L_rfhb
.L_rlhb:
s_sleep 2
.L_rfhb:
buffer_load_dwordx2 v[12:13],v14,s[0:3],s66 offen sc1
s_sub_u32 s48,s48,1
s_cbranch_scc1 .L_end
s_waitcnt vmcnt(0)
v_cmp_ne_u64 vcc,v[16:17],v[12:13]
s_cbranch_vccnz .L_rlhb
.L_hgo:
s_cmp_eq_u32 s26,1
s_cbranch_scc1 .L_hv1
s_cmp_eq_u32 s26,2
s_cbranch_scc1 .L_hv2
s_cmp_eq_u32 s26,3
s_cbranch_scc1 .L_hv3
.p2align 6
.L_hv0:
.L_hl_0:
s_waitcnt lgkmcnt(4)
v_mfma_f32_32x32x16_bf16 v[32:47],a[0:3],v[128:131],0
ds_read_b128 v[160:163],v4 offset:1152
s_waitcnt vmcnt(6)
v_cmp_ne_u64 vcc,v[16:17],v[12:13]
s_add_u32 s68,s68,s30
v_mfma_f32_32x32x16_bf16 v[32:47],a[4:7],v[132:135],v[32:47]
ds_read_b128 v[164:167],v4 offset:1664
s_cbranch_vccnz .L_rth0_0
.L_bkh0_0:
s_add_u32 s66,s66,s30
v_mfma_f32_32x32x16_bf16 v[32:47],a[8:11],v[136:139],v[32:47]
ds_read_b128 v[168:171],v4 offset:10368
s_add_u32 m0,s64,73728
v_fma_f32 v48,-v72,v112,v48
buffer_load_dwordx4 v20,s[32:35],s63 offen offset:0 sc1 lds
v_mfma_f32_32x32x16_bf16 v[32:47],a[12:15],v[140:143],v[32:47]
ds_read_b128 v[172:175],v4 offset:10880
v_fma_f32 v49,-v72,v113,v49
v_fma_f32 v50,-v72,v114,v50
v_fma_f32 v51,-v72,v115,v51
v_fma_f32 v52,-v72,v116,v52
s_waitcnt lgkmcnt(4)
v_mfma_f32_32x32x16_bf16 v[32:47],a[16:19],v[144:147],v[32:47]
buffer_load_dwordx4 v20,s[32:35],s63 offen offset:1024 sc1 lds
ds_read_b128 v[176:179],v4 offset:19584
v_fma_f32 v53,-v72,v117,v53
v_fma_f32 v54,-v72,v118,v54
v_mfma_f32_32x32x16_bf16 v[32:47],a[20:23],v[148:151],v[32:47]
ds_read_b128 v[180:183],v4 offset:20096
v_fma_f32 v55,-v72,v119,v55
v_fma_f32 v56,-v72,v120,v56
v_fma_f32 v57,-v72,v121,v57
v_fma_f32 v58,-v72,v122,v58
v_fma_f32 v59,-v72,v123,v59
v_mfma_f32_32x32x16_bf16 v[32:47],a[24:27],v[152:155],v[32:47]
buffer_load_dwordx4 v20,s[32:35],s63 offen offset:2048 sc1 lds
ds_read_b128 v[184:187],v4 offset:28800
v_fma_f32 v60,-v72,v124,v60
v_fma_f32 v61,-v72,v125,v61
v_mfma_f32_32x32x16_bf16 v[32:47],a[28:31],v[156:159],v[32:47]
ds_read_b128 v[188:191],v4 offset:29312
v_fma_f32 v62,-v72,v126,v62
v_fma_f32 v63,-v72,v127,v63
v_mul_f32 v76,v48,v73
v_mul_f32 v77,v49,v73
s_waitcnt lgkmcnt(4)
v_mfma_f32_32x32x16_bf16 v[32:47],a[32:35],v[160:163],v[32:47]
buffer_load_dwordx4 v20,s[32:35],s63 offen offset:3072 sc1 lds
ds_read_b128 v[128:131],v4 offset:2304
v_exp_f32 v76,v76
v_mfma_f32_32x32x16_bf16 v[32:47],a[36:39],v[164:167],v[32:47]
ds_read_b128 v[132:135],v4 offset:2816
v_exp_f32 v77,v77
v_mul_f32 v78,v50,v73
v_mul_f32 v79,v51,v73
v_mul_f32 v80,v52,v73
v_mfma_f32_32x32x16_bf16 v[32:47],a[40:43],v[168:171],v[32:47]
ds_read_b128 v[136:139],v4 offset:11520
s_add_u32 m0,s64,110592
v_exp_f32 v78,v78
v_exp_f32 v79,v79
v_mfma_f32_32x32x16_bf16 v[32:47],a[44:47],v[172:175],v[32:47]
ds_read_b128 v[140:143],v4 offset:12032
v_exp_f32 v80,v80
v_mul_f32 v81,v53,v73
v_mul_f32 v82,v54,v73
s_waitcnt lgkmcnt(4)
v_mfma_f32_32x32x16_bf16 v[32:47],a[48:51],v[176:179],v[32:47]
buffer_load_dwordx4 v21,s[32:35],s63 offen offset:0 sc1 lds
ds_read_b128 v[144:147],v4 offset:20736
v_exp_f32 v81,v81
v_mfma_f32_32x32x16_bf16 v[32:47],a[52:55],v[180:183],v[32:47]
ds_read_b128 v[148:151],v4 offset:21248
v_exp_f32 v82,v82
v_mul_f32 v83,v55,v73
v_mul_f32 v48,v48,v56
v_mul_f32 v49,v49,v57
v_mfma_f32_32x32x16_bf16 v[32:47],a[56:59],v[184:187],v[32:47]
buffer_load_dwordx4 v21,s[32:35],s63 offen offset:1024 sc1 lds
ds_read_b128 v[152:155],v4 offset:29952
v_exp_f32 v83,v83
v_mfma_f32_32x32x16_bf16 v[32:47],a[60:63],v[188:191],v[32:47]
ds_read_b128 v[156:159],v4 offset:30464
v_mul_f32 v50,v50,v58
v_mul_f32 v51,v51,v59
v_mul_f32 v52,v52,v60
v_mul_f32 v53,v53,v61
s_waitcnt lgkmcnt(4)
v_mfma_f32_32x32x16_bf16 v[32:47],a[64:67],v[128:131],v[32:47]
buffer_load_dwordx4 v21,s[32:35],s63 offen offset:2048 sc1 lds
ds_read_b128 v[160:163],v4 offset:3456
v_mul_f32 v54,v54,v62
v_mul_f32 v55,v55,v63
v_mfma_f32_32x32x16_bf16 v[32:47],a[68:71],v[132:135],v[32:47]
ds_read_b128 v[164:167],v4 offset:3968
v_fma_f32 v76,v76,v74,v74
v_fma_f32 v77,v77,v74,v74
v_rcp_f32 v76,v76
v_fma_f32 v78,v78,v74,v74
v_mfma_f32_32x32x16_bf16 v[32:47],a[72:75],v[136:139],v[32:47]
buffer_load_dwordx4 v21,s[32:35],s63 offen offset:3072 sc1 lds
ds_read_b128 v[168:171],v4 offset:12672
s_add_u32 m0,s64,77824
v_fma_f32 v79,v79,v74,v74
v_mfma_f32_32x32x16_bf16 v[32:47],a[76:79],v[140:143],v[32:47]
ds_read_b128 v[172:175],v4 offset:13184
v_rcp_f32 v77,v77
v_rcp_f32 v78,v78
s_waitcnt lgkmcnt(4)
v_mfma_f32_32x32x16_bf16 v[32:47],a[80:83],v[144:147],v[32:47]
buffer_load_dwordx4 v22,s[32:35],s63 offen offset:0 sc1 lds
ds_read_b128 v[176:179],v4 offset:21888
s_add_u32 m0,s64,114688
v_fma_f32 v80,v80,v74,v74
v_mfma_f32_32x32x16_bf16 v[32:47],a[84:87],v[148:151],v[32:47]
ds_read_b128 v[180:183],v4 offset:22400
v_rcp_f32 v79,v79
v_rcp_f32 v80,v80
v_fma_f32 v81,v81,v74,v74
v_mfma_f32_32x32x16_bf16 v[32:47],a[88:91],v[152:155],v[32:47]
buffer_load_dwordx4 v23,s[32:35],s63 offen offset:0 sc1 lds
ds_read_b128 v[184:187],v4 offset:31104
v_rcp_f32 v81,v81
v_mfma_f32_32x32x16_bf16 v[32:47],a[92:95],v[156:159],v[32:47]
ds_read_b128 v[188:191],v4 offset:31616
v_fma_f32 v82,v82,v74,v74
v_fma_f32 v83,v83,v74,v74
v_rcp_f32 v82,v82
s_waitcnt lgkmcnt(4)
v_mfma_f32_32x32x16_bf16 v[32:47],a[96:99],v[160:163],v[32:47]
s_waitcnt vmcnt(11)
s_barrier
ds_read_b128 v[128:131],v4 offset:4608
v_rcp_f32 v83,v83
v_mul_f32 v48,v48,v76
v_mfma_f32_32x32x16_bf16 v[32:47],a[100:103],v[164:167],v[32:47]
ds_read_b128 v[132:135],v4 offset:5120
v_mul_f32 v49,v49,v77
v_mul_f32 v50,v50,v78
v_mul_f32 v51,v51,v79
v_mul_f32 v52,v52,v80
v_mul_f32 v53,v53,v81
v_mfma_f32_32x32x16_bf16 v[32:47],a[104:107],v[168:171],v[32:47]
ds_read_b128 v[136:139],v4 offset:13824
s_add_u32 m0,s64,78848
v_mul_f32 v54,v54,v82
buffer_load_dwordx4 v24,s[32:35],s63 offen offset:0 sc1 lds
v_mfma_f32_32x32x16_bf16 v[32:47],a[108:111],v[172:175],v[32:47]
ds_read_b128 v[140:143],v4 offset:14336
v_mul_f32 v55,v55,v83
v_cvt_pk_bf16_f32 v84,v48,v49
v_cvt_pk_bf16_f32 v85,v50,v51
v_cvt_pk_bf16_f32 v86,v52,v53
s_waitcnt lgkmcnt(4)
v_mfma_f32_32x32x16_bf16 v[32:47],a[112:115],v[176:179],v[32:47]
ds_read_b128 v[144:147],v4 offset:23040
v_cvt_pk_bf16_f32 v87,v54,v55
ds_read2st64_b32 v[64:65],v8 offset0:0 offset1:36
ds_read2st64_b32 v[66:67],v8 offset0:72 offset1:108
ds_read2st64_b32 v[68:69],v8 offset0:144 offset1:180
ds_read2st64_b32 v[70:71],v8 offset0:216 offset1:252
v_mfma_f32_32x32x16_bf16 v[32:47],a[116:119],v[180:183],v[32:47]
ds_read_b128 v[148:151],v4 offset:23552
v_xor_b32 v8,v9,v8
v_mfma_f32_32x32x16_bf16 v[32:47],a[120:123],v[184:187],v[32:47]
buffer_store_dwordx4 v[84:87],v15,s[32:35],s68 offen sc1
ds_read_b128 v[152:155],v4 offset:32256
v_mfma_f32_32x32x16_bf16 v[32:47],a[124:127],v[188:191],v[32:47]
ds_read_b128 v[156:159],v4 offset:32768
s_waitcnt lgkmcnt(8)
v_mfma_f32_32x32x16_bf16 v[32:47],a[128:131],v[128:131],v[32:47]
buffer_load_dwordx4 v24,s[32:35],s63 offen offset:1024 sc1 lds
ds_read_b128 v[160:163],v4 offset:5760
v_mfma_f32_32x32x16_bf16 v[32:47],a[132:135],v[132:135],v[32:47]
ds_read_b128 v[164:167],v4 offset:6272
s_waitcnt lgkmcnt(5)
v_add_f32 v64,v64,v65
v_add_f32 v66,v66,v67
v_add_f32 v68,v68,v69
v_add_f32 v70,v70,v71
v_mfma_f32_32x32x16_bf16 v[32:47],a[136:139],v[136:139],v[32:47]
buffer_load_dwordx4 v24,s[32:35],s63 offen offset:2048 sc1 lds
ds_read_b128 v[168:171],v4 offset:14976
v_add_f32 v66,v64,v66
v_add_f32 v68,v68,v70
v_mfma_f32_32x32x16_bf16 v[32:47],a[140:143],v[140:143],v[32:47]
ds_read_b128 v[172:175],v4 offset:15488
v_add_f32_dpp v64,v66,v66 row_ror:8 row_mask:0xf bank_mask:0x3
s_waitcnt lgkmcnt(4)
v_mfma_f32_32x32x16_bf16 v[32:47],a[144:147],v[144:147],v[32:47]
buffer_load_dwordx2 v[12:13],v14,s[0:3],s66 offen sc1
ds_read_b128 v[176:179],v4 offset:24192
v_add_f32_dpp v64,v68,v68 row_ror:8 row_mask:0xf bank_mask:0xc
v_mfma_f32_32x32x16_bf16 v[32:47],a[148:151],v[148:151],v[32:47]
ds_read_b128 v[180:183],v4 offset:24704
v_add_f32_dpp v64,v64,v64 row_ror:12 row_mask:0xf bank_mask:0xf
v_mfma_f32_32x32x16_bf16 v[32:47],a[152:155],v[152:155],v[32:47]
buffer_load_dwordx4 v24,s[32:35],s63 offen offset:3072 sc1 lds
ds_read_b128 v[184:187],v4 offset:33408
v_add_f32_dpp v64,v64,v64 quad_perm:[2,3,0,1] row_mask:0xf bank_mask:0xf
v_mfma_f32_32x32x16_bf16 v[32:47],a[156:159],v[156:159],v[32:47]
ds_read_b128 v[188:191],v4 offset:33920
s_add_u32 m0,s64,115712
v_mul_f32 v64,0x3a800000,v64
s_waitcnt lgkmcnt(4)
v_mfma_f32_32x32x16_bf16 v[32:47],a[160:163],v[160:163],v[32:47]
buffer_load_dwordx4 v25,s[32:35],s63 offen offset:0 sc1 lds
ds_read_b128 v[128:131],v4 offset:6912
v_mul_f32 v65,v64,v64
v_mfma_f32_32x32x16_bf16 v[32:47],a[164:167],v[164:167],v[32:47]
ds_read_b128 v[132:135],v4 offset:7424
v_sub_f32_dpp v65,v64,v65 quad_perm:[1,1,3,3] row_mask:0xf bank_mask:0xf
v_mfma_f32_32x32x16_bf16 v[32:47],a[168:171],v[168:171],v[32:47]
ds_read_b128 v[136:139],v4 offset:16128
v_max_f32 v65,0,v65
v_mfma_f32_32x32x16_bf16 v[32:47],a[172:175],v[172:175],v[32:47]
ds_read_b128 v[140:143],v4 offset:16640
v_cvt_pk_bf16_f32 v65,0,v65
s_waitcnt lgkmcnt(4)
v_mfma_f32_32x32x16_bf16 v[32:47],a[176:179],v[176:179],v[32:47]
buffer_load_dwordx4 v25,s[32:35],s63 offen offset:1024 sc1 lds
ds_read_b128 v[144:147],v4 offset:25344
v_add_f32 v65,0x3727c5ac,v65
v_mfma_f32_32x32x16_bf16 v[32:47],a[180:183],v[180:183],v[32:47]
ds_read_b128 v[148:151],v4 offset:25856
v_cvt_pk_bf16_f32 v65,0,v65
v_mfma_f32_32x32x16_bf16 v[32:47],a[184:187],v[184:187],v[32:47]
buffer_load_dwordx4 v25,s[32:35],s63 offen offset:2048 sc1 lds
ds_read_b128 v[152:155],v4 offset:34560
v_sqrt_f32 v65,v65
v_mfma_f32_32x32x16_bf16 v[32:47],a[188:191],v[188:191],v[32:47]
ds_read_b128 v[156:159],v4 offset:35072
v_cvt_pk_bf16_f32 v65,v64,v65
s_waitcnt lgkmcnt(4)
v_mfma_f32_32x32x16_bf16 v[32:47],a[192:195],v[128:131],v[32:47]
ds_read_b128 v[160:163],v4 offset:8064
ds_write_b32 v10,v65 offset:0
v_mfma_f32_32x32x16_bf16 v[32:47],a[196:199],v[132:135],v[32:47]
ds_read_b128 v[164:167],v4 offset:8576
v_mfma_f32_32x32x16_bf16 v[32:47],a[200:203],v[136:139],v[32:47]
buffer_load_dwordx4 v25,s[32:35],s63 offen offset:3072 sc1 lds
ds_read_b128 v[168:171],v4 offset:17280
s_add_u32 s63,s63,s30
v_mfma_f32_32x32x16_bf16 v[32:47],a[204:207],v[140:143],v[32:47]
ds_read_b128 v[172:175],v4 offset:17792
s_waitcnt lgkmcnt(5)
v_mfma_f32_32x32x16_bf16 v[32:47],a[208:211],v[144:147],v[32:47]
ds_read_b128 v[176:179],v4 offset:26496
v_mfma_f32_32x32x16_bf16 v[32:47],a[212:215],v[148:151],v[32:47]
ds_read_b128 v[180:183],v4 offset:27008
v_mfma_f32_32x32x16_bf16 v[32:47],a[216:219],v[152:155],v[32:47]
ds_read_b128 v[184:187],v4 offset:35712
v_mfma_f32_32x32x16_bf16 v[32:47],a[220:223],v[156:159],v[32:47]
ds_read_b128 v[188:191],v4 offset:36224
s_waitcnt lgkmcnt(4)
v_mfma_f32_32x32x16_bf16 v[32:47],a[224:227],v[160:163],v[32:47]
s_waitcnt vmcnt(8)
s_barrier
buffer_store_dwordx2 v[16:17],v18,s[32:35],s68 offen sc1
ds_read_b128 v[128:131],v5 offset:0
v_mfma_f32_32x32x16_bf16 v[32:47],a[228:231],v[164:167],v[32:47]
ds_read_b128 v[132:135],v5 offset:512
ds_read_b32 v74,v11 offset:0
v_mfma_f32_32x32x16_bf16 v[32:47],a[232:235],v[168:171],v[32:47]
ds_read_b128 v[136:139],v5 offset:9216
v_mfma_f32_32x32x16_bf16 v[32:47],a[236:239],v[172:175],v[32:47]
ds_read_b128 v[140:143],v5 offset:9728
s_waitcnt lgkmcnt(5)
v_mfma_f32_32x32x16_bf16 v[32:47],a[240:243],v[176:179],v[32:47]
ds_read_b128 v[144:147],v5 offset:18432
s_waitcnt lgkmcnt(3)
v_lshlrev_b32 v72,16,v74
v_and_b32 v74,0xffff0000,v74
v_mfma_f32_32x32x16_bf16 v[32:47],a[244:247],v[180:183],v[32:47]
ds_read_b128 v[148:151],v5 offset:18944
v_rcp_f32 v73,v74
v_mul_f32 v74,v74,v74
v_mfma_f32_32x32x16_bf16 v[32:47],a[248:251],v[184:187],v[32:47]
ds_read_b128 v[152:155],v5 offset:27648
v_mul_f32 v73,0xbfb8aa3b,v73
v_mfma_f32_32x32x16_bf16 v[32:47],a[252:255],v[188:191],v[32:47]
ds_read_b128 v[156:159],v5 offset:28160
s_waitcnt lgkmcnt(4)
v_mfma_f32_32x32x16_bf16 v[48:63],a[0:3],v[128:131],0
ds_read_b128 v[160:163],v5 offset:1152
s_waitcnt vmcnt(6)
v_cmp_ne_u64 vcc,v[16:17],v[12:13]
s_add_u32 s68,s68,s30
v_mfma_f32_32x32x16_bf16 v[48:63],a[4:7],v[132:135],v[48:63]
ds_read_b128 v[164:167],v5 offset:1664
s_cbranch_vccnz .L_rth1_0
.L_bkh1_0:
s_add_u32 s66,s66,s30
v_mfma_f32_32x32x16_bf16 v[48:63],a[8:11],v[136:139],v[48:63]
ds_read_b128 v[168:171],v5 offset:10368
s_add_u32 m0,s64,0
v_fma_f32 v32,-v72,v112,v32
buffer_load_dwordx4 v20,s[32:35],s63 offen offset:0 sc1 lds
v_mfma_f32_32x32x16_bf16 v[48:63],a[12:15],v[140:143],v[48:63]
ds_read_b128 v[172:175],v5 offset:10880
v_fma_f32 v33,-v72,v113,v33
v_fma_f32 v34,-v72,v114,v34
v_fma_f32 v35,-v72,v115,v35
v_fma_f32 v36,-v72,v116,v36
s_waitcnt lgkmcnt(4)
v_mfma_f32_32x32x16_bf16 v[48:63],a[16:19],v[144:147],v[48:63]
buffer_load_dwordx4 v20,s[32:35],s63 offen offset:1024 sc1 lds
ds_read_b128 v[176:179],v5 offset:19584
v_fma_f32 v37,-v72,v117,v37
v_fma_f32 v38,-v72,v118,v38
v_mfma_f32_32x32x16_bf16 v[48:63],a[20:23],v[148:151],v[48:63]
ds_read_b128 v[180:183],v5 offset:20096
v_fma_f32 v39,-v72,v119,v39
)#"
R"#(
v_fma_f32 v40,-v72,v120,v40
v_fma_f32 v41,-v72,v121,v41
v_fma_f32 v42,-v72,v122,v42
v_fma_f32 v43,-v72,v123,v43
v_mfma_f32_32x32x16_bf16 v[48:63],a[24:27],v[152:155],v[48:63]
buffer_load_dwordx4 v20,s[32:35],s63 offen offset:2048 sc1 lds
ds_read_b128 v[184:187],v5 offset:28800
v_fma_f32 v44,-v72,v124,v44
v_fma_f32 v45,-v72,v125,v45
v_mfma_f32_32x32x16_bf16 v[48:63],a[28:31],v[156:159],v[48:63]
ds_read_b128 v[188:191],v5 offset:29312
v_fma_f32 v46,-v72,v126,v46
v_fma_f32 v47,-v72,v127,v47
v_mul_f32 v76,v32,v73
v_mul_f32 v77,v33,v73
s_waitcnt lgkmcnt(4)
v_mfma_f32_32x32x16_bf16 v[48:63],a[32:35],v[160:163],v[48:63]
buffer_load_dwordx4 v20,s[32:35],s63 offen offset:3072 sc1 lds
ds_read_b128 v[128:131],v5 offset:2304
v_exp_f32 v76,v76
v_mfma_f32_32x32x16_bf16 v[48:63],a[36:39],v[164:167],v[48:63]
ds_read_b128 v[132:135],v5 offset:2816
v_exp_f32 v77,v77
v_mul_f32 v78,v34,v73
v_mul_f32 v79,v35,v73
v_mul_f32 v80,v36,v73
v_mfma_f32_32x32x16_bf16 v[48:63],a[40:43],v[168:171],v[48:63]
ds_read_b128 v[136:139],v5 offset:11520
s_add_u32 m0,s64,36864
v_exp_f32 v78,v78
v_exp_f32 v79,v79
v_mfma_f32_32x32x16_bf16 v[48:63],a[44:47],v[172:175],v[48:63]
ds_read_b128 v[140:143],v5 offset:12032
v_exp_f32 v80,v80
v_mul_f32 v81,v37,v73
v_mul_f32 v82,v38,v73
s_waitcnt lgkmcnt(4)
v_mfma_f32_32x32x16_bf16 v[48:63],a[48:51],v[176:179],v[48:63]
buffer_load_dwordx4 v21,s[32:35],s63 offen offset:0 sc1 lds
ds_read_b128 v[144:147],v5 offset:20736
v_exp_f32 v81,v81
v_mfma_f32_32x32x16_bf16 v[48:63],a[52:55],v[180:183],v[48:63]
ds_read_b128 v[148:151],v5 offset:21248
v_exp_f32 v82,v82
v_mul_f32 v83,v39,v73
v_mul_f32 v32,v32,v40
v_mul_f32 v33,v33,v41
v_mfma_f32_32x32x16_bf16 v[48:63],a[56:59],v[184:187],v[48:63]
buffer_load_dwordx4 v21,s[32:35],s63 offen offset:1024 sc1 lds
ds_read_b128 v[152:155],v5 offset:29952
v_exp_f32 v83,v83
v_mfma_f32_32x32x16_bf16 v[48:63],a[60:63],v[188:191],v[48:63]
ds_read_b128 v[156:159],v5 offset:30464
v_mul_f32 v34,v34,v42
v_mul_f32 v35,v35,v43
v_mul_f32 v36,v36,v44
v_mul_f32 v37,v37,v45
s_waitcnt lgkmcnt(4)
v_mfma_f32_32x32x16_bf16 v[48:63],a[64:67],v[128:131],v[48:63]
buffer_load_dwordx4 v21,s[32:35],s63 offen offset:2048 sc1 lds
ds_read_b128 v[160:163],v5 offset:3456
v_mul_f32 v38,v38,v46
v_mul_f32 v39,v39,v47
v_mfma_f32_32x32x16_bf16 v[48:63],a[68:71],v[132:135],v[48:63]
ds_read_b128 v[164:167],v5 offset:3968
v_fma_f32 v76,v76,v74,v74
v_fma_f32 v77,v77,v74,v74
v_rcp_f32 v76,v76
v_fma_f32 v78,v78,v74,v74
v_mfma_f32_32x32x16_bf16 v[48:63],a[72:75],v[136:139],v[48:63]
buffer_load_dwordx4 v21,s[32:35],s63 offen offset:3072 sc1 lds
ds_read_b128 v[168:171],v5 offset:12672
s_add_u32 m0,s64,4096
v_fma_f32 v79,v79,v74,v74
v_mfma_f32_32x32x16_bf16 v[48:63],a[76:79],v[140:143],v[48:63]
ds_read_b128 v[172:175],v5 offset:13184
v_rcp_f32 v77,v77
v_rcp_f32 v78,v78
s_waitcnt lgkmcnt(4)
v_mfma_f32_32x32x16_bf16 v[48:63],a[80:83],v[144:147],v[48:63]
buffer_load_dwordx4 v22,s[32:35],s63 offen offset:0 sc1 lds
ds_read_b128 v[176:179],v5 offset:21888
s_add_u32 m0,s64,40960
v_fma_f32 v80,v80,v74,v74
v_mfma_f32_32x32x16_bf16 v[48:63],a[84:87],v[148:151],v[48:63]
ds_read_b128 v[180:183],v5 offset:22400
v_rcp_f32 v79,v79
v_rcp_f32 v80,v80
v_fma_f32 v81,v81,v74,v74
v_mfma_f32_32x32x16_bf16 v[48:63],a[88:91],v[152:155],v[48:63]
buffer_load_dwordx4 v23,s[32:35],s63 offen offset:0 sc1 lds
ds_read_b128 v[184:187],v5 offset:31104
v_rcp_f32 v81,v81
v_mfma_f32_32x32x16_bf16 v[48:63],a[92:95],v[156:159],v[48:63]
ds_read_b128 v[188:191],v5 offset:31616
v_fma_f32 v82,v82,v74,v74
v_fma_f32 v83,v83,v74,v74
v_rcp_f32 v82,v82
s_waitcnt lgkmcnt(4)
v_mfma_f32_32x32x16_bf16 v[48:63],a[96:99],v[160:163],v[48:63]
s_waitcnt vmcnt(11)
s_barrier
ds_read_b128 v[128:131],v5 offset:4608
v_rcp_f32 v83,v83
v_mul_f32 v32,v32,v76
v_mfma_f32_32x32x16_bf16 v[48:63],a[100:103],v[164:167],v[48:63]
ds_read_b128 v[132:135],v5 offset:5120
v_mul_f32 v33,v33,v77
v_mul_f32 v34,v34,v78
v_mul_f32 v35,v35,v79
v_mul_f32 v36,v36,v80
v_mul_f32 v37,v37,v81
v_mfma_f32_32x32x16_bf16 v[48:63],a[104:107],v[168:171],v[48:63]
ds_read_b128 v[136:139],v5 offset:13824
s_add_u32 m0,s64,5120
v_mul_f32 v38,v38,v82
buffer_load_dwordx4 v24,s[32:35],s63 offen offset:0 sc1 lds
v_mfma_f32_32x32x16_bf16 v[48:63],a[108:111],v[172:175],v[48:63]
ds_read_b128 v[140:143],v5 offset:14336
v_mul_f32 v39,v39,v83
v_cvt_pk_bf16_f32 v84,v32,v33
v_cvt_pk_bf16_f32 v85,v34,v35
v_cvt_pk_bf16_f32 v86,v36,v37
s_waitcnt lgkmcnt(4)
v_mfma_f32_32x32x16_bf16 v[48:63],a[112:115],v[176:179],v[48:63]
ds_read_b128 v[144:147],v5 offset:23040
v_cvt_pk_bf16_f32 v87,v38,v39
ds_read2st64_b32 v[64:65],v8 offset0:0 offset1:36
ds_read2st64_b32 v[66:67],v8 offset0:72 offset1:108
ds_read2st64_b32 v[68:69],v8 offset0:144 offset1:180
ds_read2st64_b32 v[70:71],v8 offset0:216 offset1:252
v_mfma_f32_32x32x16_bf16 v[48:63],a[116:119],v[180:183],v[48:63]
ds_read_b128 v[148:151],v5 offset:23552
v_xor_b32 v8,v9,v8
v_mfma_f32_32x32x16_bf16 v[48:63],a[120:123],v[184:187],v[48:63]
buffer_store_dwordx4 v[84:87],v15,s[32:35],s68 offen sc1
ds_read_b128 v[152:155],v5 offset:32256
v_mfma_f32_32x32x16_bf16 v[48:63],a[124:127],v[188:191],v[48:63]
ds_read_b128 v[156:159],v5 offset:32768
s_waitcnt lgkmcnt(8)
v_mfma_f32_32x32x16_bf16 v[48:63],a[128:131],v[128:131],v[48:63]
buffer_load_dwordx4 v24,s[32:35],s63 offen offset:1024 sc1 lds
ds_read_b128 v[160:163],v5 offset:5760
v_mfma_f32_32x32x16_bf16 v[48:63],a[132:135],v[132:135],v[48:63]
ds_read_b128 v[164:167],v5 offset:6272
s_waitcnt lgkmcnt(5)
v_add_f32 v64,v64,v65
v_add_f32 v66,v66,v67
v_add_f32 v68,v68,v69
v_add_f32 v70,v70,v71
v_mfma_f32_32x32x16_bf16 v[48:63],a[136:139],v[136:139],v[48:63]
buffer_load_dwordx4 v24,s[32:35],s63 offen offset:2048 sc1 lds
ds_read_b128 v[168:171],v5 offset:14976
v_add_f32 v66,v64,v66
v_add_f32 v68,v68,v70
v_mfma_f32_32x32x16_bf16 v[48:63],a[140:143],v[140:143],v[48:63]
ds_read_b128 v[172:175],v5 offset:15488
v_add_f32_dpp v64,v66,v66 row_ror:8 row_mask:0xf bank_mask:0x3
s_waitcnt lgkmcnt(4)
v_mfma_f32_32x32x16_bf16 v[48:63],a[144:147],v[144:147],v[48:63]
buffer_load_dwordx2 v[12:13],v14,s[0:3],s66 offen sc1
ds_read_b128 v[176:179],v5 offset:24192
v_add_f32_dpp v64,v68,v68 row_ror:8 row_mask:0xf bank_mask:0xc
v_mfma_f32_32x32x16_bf16 v[48:63],a[148:151],v[148:151],v[48:63]
ds_read_b128 v[180:183],v5 offset:24704
v_add_f32_dpp v64,v64,v64 row_ror:12 row_mask:0xf bank_mask:0xf
v_mfma_f32_32x32x16_bf16 v[48:63],a[152:155],v[152:155],v[48:63]
buffer_load_dwordx4 v24,s[32:35],s63 offen offset:3072 sc1 lds
ds_read_b128 v[184:187],v5 offset:33408
v_add_f32_dpp v64,v64,v64 quad_perm:[2,3,0,1] row_mask:0xf bank_mask:0xf
v_mfma_f32_32x32x16_bf16 v[48:63],a[156:159],v[156:159],v[48:63]
ds_read_b128 v[188:191],v5 offset:33920
s_add_u32 m0,s64,41984
v_mul_f32 v64,0x3a800000,v64
s_waitcnt lgkmcnt(4)
v_mfma_f32_32x32x16_bf16 v[48:63],a[160:163],v[160:163],v[48:63]
buffer_load_dwordx4 v25,s[32:35],s63 offen offset:0 sc1 lds
ds_read_b128 v[128:131],v5 offset:6912
v_mul_f32 v65,v64,v64
v_mfma_f32_32x32x16_bf16 v[48:63],a[164:167],v[164:167],v[48:63]
ds_read_b128 v[132:135],v5 offset:7424
v_sub_f32_dpp v65,v64,v65 quad_perm:[1,1,3,3] row_mask:0xf bank_mask:0xf
v_mfma_f32_32x32x16_bf16 v[48:63],a[168:171],v[168:171],v[48:63]
ds_read_b128 v[136:139],v5 offset:16128
v_max_f32 v65,0,v65
v_mfma_f32_32x32x16_bf16 v[48:63],a[172:175],v[172:175],v[48:63]
ds_read_b128 v[140:143],v5 offset:16640
v_cvt_pk_bf16_f32 v65,0,v65
s_waitcnt lgkmcnt(4)
v_mfma_f32_32x32x16_bf16 v[48:63],a[176:179],v[176:179],v[48:63]
buffer_load_dwordx4 v25,s[32:35],s63 offen offset:1024 sc1 lds
ds_read_b128 v[144:147],v5 offset:25344
v_add_f32 v65,0x3727c5ac,v65
v_mfma_f32_32x32x16_bf16 v[48:63],a[180:183],v[180:183],v[48:63]
ds_read_b128 v[148:151],v5 offset:25856
v_cvt_pk_bf16_f32 v65,0,v65
v_mfma_f32_32x32x16_bf16 v[48:63],a[184:187],v[184:187],v[48:63]
buffer_load_dwordx4 v25,s[32:35],s63 offen offset:2048 sc1 lds
ds_read_b128 v[152:155],v5 offset:34560
v_sqrt_f32 v65,v65
v_mfma_f32_32x32x16_bf16 v[48:63],a[188:191],v[188:191],v[48:63]
ds_read_b128 v[156:159],v5 offset:35072
v_cvt_pk_bf16_f32 v65,v64,v65
s_waitcnt lgkmcnt(4)
v_mfma_f32_32x32x16_bf16 v[48:63],a[192:195],v[128:131],v[48:63]
ds_read_b128 v[160:163],v5 offset:8064
ds_write_b32 v10,v65 offset:128
v_mfma_f32_32x32x16_bf16 v[48:63],a[196:199],v[132:135],v[48:63]
ds_read_b128 v[164:167],v5 offset:8576
v_mfma_f32_32x32x16_bf16 v[48:63],a[200:203],v[136:139],v[48:63]
buffer_load_dwordx4 v25,s[32:35],s63 offen offset:3072 sc1 lds
ds_read_b128 v[168:171],v5 offset:17280
s_add_u32 s63,s63,s30
v_mfma_f32_32x32x16_bf16 v[48:63],a[204:207],v[140:143],v[48:63]
ds_read_b128 v[172:175],v5 offset:17792
s_waitcnt lgkmcnt(5)
v_mfma_f32_32x32x16_bf16 v[48:63],a[208:211],v[144:147],v[48:63]
ds_read_b128 v[176:179],v5 offset:26496
v_mfma_f32_32x32x16_bf16 v[48:63],a[212:215],v[148:151],v[48:63]
ds_read_b128 v[180:183],v5 offset:27008
v_mfma_f32_32x32x16_bf16 v[48:63],a[216:219],v[152:155],v[48:63]
ds_read_b128 v[184:187],v5 offset:35712
v_mfma_f32_32x32x16_bf16 v[48:63],a[220:223],v[156:159],v[48:63]
ds_read_b128 v[188:191],v5 offset:36224
s_waitcnt lgkmcnt(4)
v_mfma_f32_32x32x16_bf16 v[48:63],a[224:227],v[160:163],v[48:63]
s_waitcnt vmcnt(8)
s_barrier
buffer_store_dwordx2 v[16:17],v18,s[32:35],s68 offen sc1
ds_read_b128 v[128:131],v4 offset:0
v_mfma_f32_32x32x16_bf16 v[48:63],a[228:231],v[164:167],v[48:63]
ds_read_b128 v[132:135],v4 offset:512
ds_read_b32 v74,v11 offset:128
v_mfma_f32_32x32x16_bf16 v[48:63],a[232:235],v[168:171],v[48:63]
ds_read_b128 v[136:139],v4 offset:9216
v_mfma_f32_32x32x16_bf16 v[48:63],a[236:239],v[172:175],v[48:63]
ds_read_b128 v[140:143],v4 offset:9728
s_waitcnt lgkmcnt(5)
v_mfma_f32_32x32x16_bf16 v[48:63],a[240:243],v[176:179],v[48:63]
ds_read_b128 v[144:147],v4 offset:18432
s_waitcnt lgkmcnt(3)
v_lshlrev_b32 v72,16,v74
v_and_b32 v74,0xffff0000,v74
v_mfma_f32_32x32x16_bf16 v[48:63],a[244:247],v[180:183],v[48:63]
ds_read_b128 v[148:151],v4 offset:18944
v_rcp_f32 v73,v74
v_mul_f32 v74,v74,v74
v_mfma_f32_32x32x16_bf16 v[48:63],a[248:251],v[184:187],v[48:63]
ds_read_b128 v[152:155],v4 offset:27648
v_mul_f32 v73,0xbfb8aa3b,v73
s_sub_u32 s62,s62,1
v_mfma_f32_32x32x16_bf16 v[48:63],a[252:255],v[188:191],v[48:63]
ds_read_b128 v[156:159],v4 offset:28160
s_cbranch_scc0 .L_hl_0
s_nop 1
s_add_u32 s68,s68,s30
s_nop 6
v_fma_f32 v48,-v72,v112,v48
v_fma_f32 v49,-v72,v113,v49
v_fma_f32 v50,-v72,v114,v50
v_fma_f32 v51,-v72,v115,v51
v_fma_f32 v52,-v72,v116,v52
v_fma_f32 v53,-v72,v117,v53
v_fma_f32 v54,-v72,v118,v54
v_fma_f32 v55,-v72,v119,v55
v_fma_f32 v56,-v72,v120,v56
v_fma_f32 v57,-v72,v121,v57
v_fma_f32 v58,-v72,v122,v58
v_fma_f32 v59,-v72,v123,v59
v_fma_f32 v60,-v72,v124,v60
v_fma_f32 v61,-v72,v125,v61
v_fma_f32 v62,-v72,v126,v62
v_fma_f32 v63,-v72,v127,v63
v_mul_f32 v76,v48,v73
v_mul_f32 v77,v49,v73
v_mul_f32 v78,v50,v73
v_mul_f32 v79,v51,v73
v_mul_f32 v80,v52,v73
v_mul_f32 v81,v53,v73
v_mul_f32 v82,v54,v73
v_mul_f32 v83,v55,v73
v_exp_f32 v76,v76
v_exp_f32 v77,v77
v_mul_f32 v48,v48,v56
v_mul_f32 v49,v49,v57
v_exp_f32 v78,v78
v_exp_f32 v79,v79
v_mul_f32 v50,v50,v58
v_mul_f32 v51,v51,v59
v_exp_f32 v80,v80
v_exp_f32 v81,v81
v_mul_f32 v52,v52,v60
v_mul_f32 v53,v53,v61
v_exp_f32 v82,v82
v_exp_f32 v83,v83
v_mul_f32 v54,v54,v62
v_mul_f32 v55,v55,v63
v_fma_f32 v76,v76,v74,v74
v_fma_f32 v77,v77,v74,v74
v_rcp_f32 v76,v76
v_rcp_f32 v77,v77
v_fma_f32 v78,v78,v74,v74
v_fma_f32 v79,v79,v74,v74
v_rcp_f32 v78,v78
v_rcp_f32 v79,v79
v_fma_f32 v80,v80,v74,v74
v_fma_f32 v81,v81,v74,v74
v_rcp_f32 v80,v80
v_rcp_f32 v81,v81
v_fma_f32 v82,v82,v74,v74
v_fma_f32 v83,v83,v74,v74
v_rcp_f32 v82,v82
v_rcp_f32 v83,v83
v_mul_f32 v48,v48,v76
v_mul_f32 v49,v49,v77
v_mul_f32 v50,v50,v78
v_mul_f32 v51,v51,v79
v_mul_f32 v52,v52,v80
v_mul_f32 v53,v53,v81
v_mul_f32 v54,v54,v82
v_mul_f32 v55,v55,v83
v_cvt_pk_bf16_f32 v84,v48,v49
v_cvt_pk_bf16_f32 v85,v50,v51
v_cvt_pk_bf16_f32 v86,v52,v53
v_cvt_pk_bf16_f32 v87,v54,v55
buffer_store_dwordx4 v[84:87],v15,s[32:35],s68 offen sc1
s_waitcnt vmcnt(0)
s_barrier
buffer_store_dwordx2 v[16:17],v18,s[32:35],s68 offen sc1
s_cbranch_execz .L_hbk
s_branch .L_end
.p2align 6
.L_hv1:
.L_hl_1:
s_waitcnt lgkmcnt(4)
v_mfma_f32_32x32x16_bf16 v[32:47],a[0:3],v[128:131],0
ds_read_b128 v[160:163],v4 offset:1152
s_waitcnt vmcnt(5)
v_cmp_ne_u64 vcc,v[16:17],v[12:13]
s_add_u32 s68,s68,s30
v_mfma_f32_32x32x16_bf16 v[32:47],a[4:7],v[132:135],v[32:47]
ds_read_b128 v[164:167],v4 offset:1664
s_cbranch_vccnz .L_rth0_1
.L_bkh0_1:
s_add_u32 s66,s66,s30
v_mfma_f32_32x32x16_bf16 v[32:47],a[8:11],v[136:139],v[32:47]
ds_read_b128 v[168:171],v4 offset:10368
s_add_u32 m0,s64,73728
v_fma_f32 v48,-v72,v112,v48
buffer_load_dwordx4 v20,s[32:35],s63 offen offset:0 sc1 lds
v_mfma_f32_32x32x16_bf16 v[32:47],a[12:15],v[140:143],v[32:47]
ds_read_b128 v[172:175],v4 offset:10880
v_fma_f32 v49,-v72,v113,v49
v_fma_f32 v50,-v72,v114,v50
v_fma_f32 v51,-v72,v115,v51
v_fma_f32 v52,-v72,v116,v52
s_waitcnt lgkmcnt(4)
v_mfma_f32_32x32x16_bf16 v[32:47],a[16:19],v[144:147],v[32:47]
buffer_load_dwordx4 v20,s[32:35],s63 offen offset:1024 sc1 lds
ds_read_b128 v[176:179],v4 offset:19584
v_fma_f32 v53,-v72,v117,v53
v_fma_f32 v54,-v72,v118,v54
v_mfma_f32_32x32x16_bf16 v[32:47],a[20:23],v[148:151],v[32:47]
ds_read_b128 v[180:183],v4 offset:20096
v_fma_f32 v55,-v72,v119,v55
v_fma_f32 v56,-v72,v120,v56
v_fma_f32 v57,-v72,v121,v57
v_fma_f32 v58,-v72,v122,v58
v_fma_f32 v59,-v72,v123,v59
v_mfma_f32_32x32x16_bf16 v[32:47],a[24:27],v[152:155],v[32:47]
buffer_load_dwordx4 v20,s[32:35],s63 offen offset:2048 sc1 lds
ds_read_b128 v[184:187],v4 offset:28800
v_fma_f32 v60,-v72,v124,v60
v_fma_f32 v61,-v72,v125,v61
v_mfma_f32_32x32x16_bf16 v[32:47],a[28:31],v[156:159],v[32:47]
ds_read_b128 v[188:191],v4 offset:29312
v_fma_f32 v62,-v72,v126,v62
v_fma_f32 v63,-v72,v127,v63
v_mul_f32 v76,v48,v73
v_mul_f32 v77,v49,v73
s_waitcnt lgkmcnt(4)
v_mfma_f32_32x32x16_bf16 v[32:47],a[32:35],v[160:163],v[32:47]
buffer_load_dwordx4 v20,s[32:35],s63 offen offset:3072 sc1 lds
ds_read_b128 v[128:131],v4 offset:2304
v_exp_f32 v76,v76
v_mfma_f32_32x32x16_bf16 v[32:47],a[36:39],v[164:167],v[32:47]
ds_read_b128 v[132:135],v4 offset:2816
v_exp_f32 v77,v77
v_mul_f32 v78,v50,v73
v_mul_f32 v79,v51,v73
v_mul_f32 v80,v52,v73
v_mfma_f32_32x32x16_bf16 v[32:47],a[40:43],v[168:171],v[32:47]
ds_read_b128 v[136:139],v4 offset:11520
s_add_u32 m0,s64,110592
v_exp_f32 v78,v78
v_exp_f32 v79,v79
v_mfma_f32_32x32x16_bf16 v[32:47],a[44:47],v[172:175],v[32:47]
ds_read_b128 v[140:143],v4 offset:12032
v_exp_f32 v80,v80
v_mul_f32 v81,v53,v73
v_mul_f32 v82,v54,v73
s_waitcnt lgkmcnt(4)
v_mfma_f32_32x32x16_bf16 v[32:47],a[48:51],v[176:179],v[32:47]
buffer_load_dwordx4 v21,s[32:35],s63 offen offset:0 sc1 lds
ds_read_b128 v[144:147],v4 offset:20736
v_exp_f32 v81,v81
v_mfma_f32_32x32x16_bf16 v[32:47],a[52:55],v[180:183],v[32:47]
ds_read_b128 v[148:151],v4 offset:21248
v_exp_f32 v82,v82
v_mul_f32 v83,v55,v73
v_mul_f32 v48,v48,v56
v_mul_f32 v49,v49,v57
v_mfma_f32_32x32x16_bf16 v[32:47],a[56:59],v[184:187],v[32:47]
buffer_load_dwordx4 v21,s[32:35],s63 offen offset:1024 sc1 lds
ds_read_b128 v[152:155],v4 offset:29952
v_exp_f32 v83,v83
v_mfma_f32_32x32x16_bf16 v[32:47],a[60:63],v[188:191],v[32:47]
ds_read_b128 v[156:159],v4 offset:30464
v_mul_f32 v50,v50,v58
v_mul_f32 v51,v51,v59
v_mul_f32 v52,v52,v60
v_mul_f32 v53,v53,v61
s_waitcnt lgkmcnt(4)
v_mfma_f32_32x32x16_bf16 v[32:47],a[64:67],v[128:131],v[32:47]
buffer_load_dwordx4 v21,s[32:35],s63 offen offset:2048 sc1 lds
ds_read_b128 v[160:163],v4 offset:3456
v_mul_f32 v54,v54,v62
v_mul_f32 v55,v55,v63
v_mfma_f32_32x32x16_bf16 v[32:47],a[68:71],v[132:135],v[32:47]
ds_read_b128 v[164:167],v4 offset:3968
v_fma_f32 v76,v76,v74,v74
v_fma_f32 v77,v77,v74,v74
v_rcp_f32 v76,v76
v_fma_f32 v78,v78,v74,v74
v_mfma_f32_32x32x16_bf16 v[32:47],a[72:75],v[136:139],v[32:47]
buffer_load_dwordx4 v21,s[32:35],s63 offen offset:3072 sc1 lds
ds_read_b128 v[168:171],v4 offset:12672
s_add_u32 m0,s64,77824
v_fma_f32 v79,v79,v74,v74
v_mfma_f32_32x32x16_bf16 v[32:47],a[76:79],v[140:143],v[32:47]
ds_read_b128 v[172:175],v4 offset:13184
v_rcp_f32 v77,v77
v_rcp_f32 v78,v78
s_waitcnt lgkmcnt(4)
v_mfma_f32_32x32x16_bf16 v[32:47],a[80:83],v[144:147],v[32:47]
buffer_load_dwordx4 v22,s[32:35],s63 offen offset:0 sc1 lds
ds_read_b128 v[176:179],v4 offset:21888
s_add_u32 m0,s64,114688
v_fma_f32 v80,v80,v74,v74
v_mfma_f32_32x32x16_bf16 v[32:47],a[84:87],v[148:151],v[32:47]
ds_read_b128 v[180:183],v4 offset:22400
v_rcp_f32 v79,v79
v_rcp_f32 v80,v80
v_fma_f32 v81,v81,v74,v74
v_mfma_f32_32x32x16_bf16 v[32:47],a[88:91],v[152:155],v[32:47]
buffer_load_dwordx4 v23,s[32:35],s63 offen offset:0 sc1 lds
ds_read_b128 v[184:187],v4 offset:31104
v_rcp_f32 v81,v81
v_mfma_f32_32x32x16_bf16 v[32:47],a[92:95],v[156:159],v[32:47]
ds_read_b128 v[188:191],v4 offset:31616
v_fma_f32 v82,v82,v74,v74
v_fma_f32 v83,v83,v74,v74
v_rcp_f32 v82,v82
s_waitcnt lgkmcnt(4)
s_waitcnt vmcnt(10)
s_barrier
v_mfma_f32_32x32x16_bf16 v[32:47],a[96:99],v[160:163],v[32:47]
ds_read_b128 v[128:131],v4 offset:4608
v_rcp_f32 v83,v83
v_mul_f32 v48,v48,v76
v_mfma_f32_32x32x16_bf16 v[32:47],a[100:103],v[164:167],v[32:47]
ds_read_b128 v[132:135],v4 offset:5120
v_mul_f32 v49,v49,v77
v_mul_f32 v50,v50,v78
v_mul_f32 v51,v51,v79
v_mul_f32 v52,v52,v80
v_mul_f32 v53,v53,v81
v_mfma_f32_32x32x16_bf16 v[32:47],a[104:107],v[168:171],v[32:47]
ds_read_b128 v[136:139],v4 offset:13824
s_add_u32 m0,s64,78848
v_mul_f32 v54,v54,v82
buffer_load_dwordx4 v24,s[32:35],s63 offen offset:0 sc1 lds
v_mfma_f32_32x32x16_bf16 v[32:47],a[108:111],v[172:175],v[32:47]
ds_read_b128 v[140:143],v4 offset:14336
v_mul_f32 v55,v55,v83
v_cvt_pk_bf16_f32 v84,v48,v49
v_cvt_pk_bf16_f32 v85,v50,v51
v_cvt_pk_bf16_f32 v86,v52,v53
s_waitcnt lgkmcnt(4)
v_mfma_f32_32x32x16_bf16 v[32:47],a[112:115],v[176:179],v[32:47]
ds_read_b128 v[144:147],v4 offset:23040
v_cvt_pk_bf16_f32 v87,v54,v55
ds_read2st64_b32 v[64:65],v8 offset0:0 offset1:36
ds_read2st64_b32 v[66:67],v8 offset0:72 offset1:108
ds_read2st64_b32 v[68:69],v8 offset0:144 offset1:180
ds_read2st64_b32 v[70:71],v8 offset0:216 offset1:252
v_mfma_f32_32x32x16_bf16 v[32:47],a[116:119],v[180:183],v[32:47]
ds_read_b128 v[148:151],v4 offset:23552
v_xor_b32 v8,v9,v8
v_mfma_f32_32x32x16_bf16 v[32:47],a[120:123],v[184:187],v[32:47]
)#"
R"#(
buffer_store_dwordx4 v[84:87],v15,s[32:35],s68 offen sc1
ds_read_b128 v[152:155],v4 offset:32256
v_mfma_f32_32x32x16_bf16 v[32:47],a[124:127],v[188:191],v[32:47]
ds_read_b128 v[156:159],v4 offset:32768
s_waitcnt lgkmcnt(8)
v_mfma_f32_32x32x16_bf16 v[32:47],a[128:131],v[128:131],v[32:47]
buffer_load_dwordx4 v24,s[32:35],s63 offen offset:1024 sc1 lds
ds_read_b128 v[160:163],v4 offset:5760
v_mfma_f32_32x32x16_bf16 v[32:47],a[132:135],v[132:135],v[32:47]
ds_read_b128 v[164:167],v4 offset:6272
s_waitcnt lgkmcnt(5)
v_add_f32 v64,v64,v65
v_add_f32 v66,v66,v67
v_add_f32 v68,v68,v69
v_add_f32 v70,v70,v71
v_mfma_f32_32x32x16_bf16 v[32:47],a[136:139],v[136:139],v[32:47]
buffer_load_dwordx4 v24,s[32:35],s63 offen offset:2048 sc1 lds
ds_read_b128 v[168:171],v4 offset:14976
v_add_f32 v66,v64,v66
v_add_f32 v68,v68,v70
v_mfma_f32_32x32x16_bf16 v[32:47],a[140:143],v[140:143],v[32:47]
ds_read_b128 v[172:175],v4 offset:15488
v_add_f32_dpp v64,v66,v66 row_ror:8 row_mask:0xf bank_mask:0x3
s_waitcnt lgkmcnt(4)
v_mfma_f32_32x32x16_bf16 v[32:47],a[144:147],v[144:147],v[32:47]
buffer_load_dwordx2 v[12:13],v14,s[0:3],s66 offen sc1
ds_read_b128 v[176:179],v4 offset:24192
v_add_f32_dpp v64,v68,v68 row_ror:8 row_mask:0xf bank_mask:0xc
v_mfma_f32_32x32x16_bf16 v[32:47],a[148:151],v[148:151],v[32:47]
ds_read_b128 v[180:183],v4 offset:24704
v_add_f32_dpp v64,v64,v64 row_ror:12 row_mask:0xf bank_mask:0xf
v_mfma_f32_32x32x16_bf16 v[32:47],a[152:155],v[152:155],v[32:47]
buffer_load_dwordx4 v24,s[32:35],s63 offen offset:3072 sc1 lds
ds_read_b128 v[184:187],v4 offset:33408
v_add_f32_dpp v64,v64,v64 quad_perm:[2,3,0,1] row_mask:0xf bank_mask:0xf
v_mfma_f32_32x32x16_bf16 v[32:47],a[156:159],v[156:159],v[32:47]
ds_read_b128 v[188:191],v4 offset:33920
s_add_u32 m0,s64,115712
v_mul_f32 v64,0x3a800000,v64
s_waitcnt lgkmcnt(4)
v_mfma_f32_32x32x16_bf16 v[32:47],a[160:163],v[160:163],v[32:47]
buffer_load_dwordx4 v25,s[32:35],s63 offen offset:0 sc1 lds
ds_read_b128 v[128:131],v4 offset:6912
v_mul_f32 v65,v64,v64
v_mfma_f32_32x32x16_bf16 v[32:47],a[164:167],v[164:167],v[32:47]
ds_read_b128 v[132:135],v4 offset:7424
v_sub_f32_dpp v65,v64,v65 quad_perm:[1,1,3,3] row_mask:0xf bank_mask:0xf
v_mfma_f32_32x32x16_bf16 v[32:47],a[168:171],v[168:171],v[32:47]
ds_read_b128 v[136:139],v4 offset:16128
v_max_f32 v65,0,v65
v_mfma_f32_32x32x16_bf16 v[32:47],a[172:175],v[172:175],v[32:47]
ds_read_b128 v[140:143],v4 offset:16640
v_cvt_pk_bf16_f32 v65,0,v65
s_waitcnt lgkmcnt(4)
v_mfma_f32_32x32x16_bf16 v[32:47],a[176:179],v[176:179],v[32:47]
buffer_load_dwordx4 v25,s[32:35],s63 offen offset:1024 sc1 lds
ds_read_b128 v[144:147],v4 offset:25344
v_add_f32 v65,0x3727c5ac,v65
v_mfma_f32_32x32x16_bf16 v[32:47],a[180:183],v[180:183],v[32:47]
ds_read_b128 v[148:151],v4 offset:25856
v_cvt_pk_bf16_f32 v65,0,v65
v_mfma_f32_32x32x16_bf16 v[32:47],a[184:187],v[184:187],v[32:47]
buffer_load_dwordx4 v25,s[32:35],s63 offen offset:2048 sc1 lds
ds_read_b128 v[152:155],v4 offset:34560
v_sqrt_f32 v65,v65
v_mfma_f32_32x32x16_bf16 v[32:47],a[188:191],v[188:191],v[32:47]
ds_read_b128 v[156:159],v4 offset:35072
v_cvt_pk_bf16_f32 v65,v64,v65
s_waitcnt lgkmcnt(4)
v_mfma_f32_32x32x16_bf16 v[32:47],a[192:195],v[128:131],v[32:47]
ds_read_b128 v[160:163],v4 offset:8064
ds_write_b32 v10,v65 offset:0
v_mfma_f32_32x32x16_bf16 v[32:47],a[196:199],v[132:135],v[32:47]
ds_read_b128 v[164:167],v4 offset:8576
v_mfma_f32_32x32x16_bf16 v[32:47],a[200:203],v[136:139],v[32:47]
buffer_load_dwordx4 v25,s[32:35],s63 offen offset:3072 sc1 lds
ds_read_b128 v[168:171],v4 offset:17280
s_add_u32 s63,s63,s30
v_mfma_f32_32x32x16_bf16 v[32:47],a[204:207],v[140:143],v[32:47]
ds_read_b128 v[172:175],v4 offset:17792
s_waitcnt lgkmcnt(5)
v_mfma_f32_32x32x16_bf16 v[32:47],a[208:211],v[144:147],v[32:47]
ds_read_b128 v[176:179],v4 offset:26496
v_mfma_f32_32x32x16_bf16 v[32:47],a[212:215],v[148:151],v[32:47]
ds_read_b128 v[180:183],v4 offset:27008
v_mfma_f32_32x32x16_bf16 v[32:47],a[216:219],v[152:155],v[32:47]
ds_read_b128 v[184:187],v4 offset:35712
v_mfma_f32_32x32x16_bf16 v[32:47],a[220:223],v[156:159],v[32:47]
ds_read_b128 v[188:191],v4 offset:36224
s_waitcnt lgkmcnt(4)
s_waitcnt vmcnt(8)
s_barrier
v_mfma_f32_32x32x16_bf16 v[32:47],a[224:227],v[160:163],v[32:47]
ds_read_b128 v[128:131],v5 offset:0
v_mfma_f32_32x32x16_bf16 v[32:47],a[228:231],v[164:167],v[32:47]
ds_read_b128 v[132:135],v5 offset:512
ds_read_b32 v74,v11 offset:0
v_mfma_f32_32x32x16_bf16 v[32:47],a[232:235],v[168:171],v[32:47]
ds_read_b128 v[136:139],v5 offset:9216
v_mfma_f32_32x32x16_bf16 v[32:47],a[236:239],v[172:175],v[32:47]
ds_read_b128 v[140:143],v5 offset:9728
s_waitcnt lgkmcnt(5)
v_mfma_f32_32x32x16_bf16 v[32:47],a[240:243],v[176:179],v[32:47]
ds_read_b128 v[144:147],v5 offset:18432
s_waitcnt lgkmcnt(3)
v_lshlrev_b32 v72,16,v74
v_and_b32 v74,0xffff0000,v74
v_mfma_f32_32x32x16_bf16 v[32:47],a[244:247],v[180:183],v[32:47]
ds_read_b128 v[148:151],v5 offset:18944
v_rcp_f32 v73,v74
v_mul_f32 v74,v74,v74
v_mfma_f32_32x32x16_bf16 v[32:47],a[248:251],v[184:187],v[32:47]
ds_read_b128 v[152:155],v5 offset:27648
v_mul_f32 v73,0xbfb8aa3b,v73
v_mfma_f32_32x32x16_bf16 v[32:47],a[252:255],v[188:191],v[32:47]
ds_read_b128 v[156:159],v5 offset:28160
s_waitcnt lgkmcnt(4)
v_mfma_f32_32x32x16_bf16 v[48:63],a[0:3],v[128:131],0
ds_read_b128 v[160:163],v5 offset:1152
s_waitcnt vmcnt(5)
v_cmp_ne_u64 vcc,v[16:17],v[12:13]
s_add_u32 s68,s68,s30
v_mfma_f32_32x32x16_bf16 v[48:63],a[4:7],v[132:135],v[48:63]
ds_read_b128 v[164:167],v5 offset:1664
s_cbranch_vccnz .L_rth1_1
.L_bkh1_1:
s_add_u32 s66,s66,s30
v_mfma_f32_32x32x16_bf16 v[48:63],a[8:11],v[136:139],v[48:63]
ds_read_b128 v[168:171],v5 offset:10368
s_add_u32 m0,s64,0
v_fma_f32 v32,-v72,v112,v32
buffer_load_dwordx4 v20,s[32:35],s63 offen offset:0 sc1 lds
v_mfma_f32_32x32x16_bf16 v[48:63],a[12:15],v[140:143],v[48:63]
ds_read_b128 v[172:175],v5 offset:10880
v_fma_f32 v33,-v72,v113,v33
v_fma_f32 v34,-v72,v114,v34
v_fma_f32 v35,-v72,v115,v35
v_fma_f32 v36,-v72,v116,v36
s_waitcnt lgkmcnt(4)
v_mfma_f32_32x32x16_bf16 v[48:63],a[16:19],v[144:147],v[48:63]
buffer_load_dwordx4 v20,s[32:35],s63 offen offset:1024 sc1 lds
ds_read_b128 v[176:179],v5 offset:19584
v_fma_f32 v37,-v72,v117,v37
v_fma_f32 v38,-v72,v118,v38
v_mfma_f32_32x32x16_bf16 v[48:63],a[20:23],v[148:151],v[48:63]
ds_read_b128 v[180:183],v5 offset:20096
v_fma_f32 v39,-v72,v119,v39
v_fma_f32 v40,-v72,v120,v40
v_fma_f32 v41,-v72,v121,v41
v_fma_f32 v42,-v72,v122,v42
v_fma_f32 v43,-v72,v123,v43
v_mfma_f32_32x32x16_bf16 v[48:63],a[24:27],v[152:155],v[48:63]
buffer_load_dwordx4 v20,s[32:35],s63 offen offset:2048 sc1 lds
ds_read_b128 v[184:187],v5 offset:28800
v_fma_f32 v44,-v72,v124,v44
v_fma_f32 v45,-v72,v125,v45
v_mfma_f32_32x32x16_bf16 v[48:63],a[28:31],v[156:159],v[48:63]
ds_read_b128 v[188:191],v5 offset:29312
v_fma_f32 v46,-v72,v126,v46
v_fma_f32 v47,-v72,v127,v47
v_mul_f32 v76,v32,v73
v_mul_f32 v77,v33,v73
s_waitcnt lgkmcnt(4)
v_mfma_f32_32x32x16_bf16 v[48:63],a[32:35],v[160:163],v[48:63]
buffer_load_dwordx4 v20,s[32:35],s63 offen offset:3072 sc1 lds
ds_read_b128 v[128:131],v5 offset:2304
v_exp_f32 v76,v76
v_mfma_f32_32x32x16_bf16 v[48:63],a[36:39],v[164:167],v[48:63]
ds_read_b128 v[132:135],v5 offset:2816
v_exp_f32 v77,v77
v_mul_f32 v78,v34,v73
v_mul_f32 v79,v35,v73
v_mul_f32 v80,v36,v73
v_mfma_f32_32x32x16_bf16 v[48:63],a[40:43],v[168:171],v[48:63]
ds_read_b128 v[136:139],v5 offset:11520
s_add_u32 m0,s64,36864
v_exp_f32 v78,v78
v_exp_f32 v79,v79
v_mfma_f32_32x32x16_bf16 v[48:63],a[44:47],v[172:175],v[48:63]
ds_read_b128 v[140:143],v5 offset:12032
v_exp_f32 v80,v80
v_mul_f32 v81,v37,v73
v_mul_f32 v82,v38,v73
s_waitcnt lgkmcnt(4)
v_mfma_f32_32x32x16_bf16 v[48:63],a[48:51],v[176:179],v[48:63]
buffer_load_dwordx4 v21,s[32:35],s63 offen offset:0 sc1 lds
ds_read_b128 v[144:147],v5 offset:20736
v_exp_f32 v81,v81
v_mfma_f32_32x32x16_bf16 v[48:63],a[52:55],v[180:183],v[48:63]
ds_read_b128 v[148:151],v5 offset:21248
v_exp_f32 v82,v82
v_mul_f32 v83,v39,v73
v_mul_f32 v32,v32,v40
v_mul_f32 v33,v33,v41
v_mfma_f32_32x32x16_bf16 v[48:63],a[56:59],v[184:187],v[48:63]
buffer_load_dwordx4 v21,s[32:35],s63 offen offset:1024 sc1 lds
ds_read_b128 v[152:155],v5 offset:29952
v_exp_f32 v83,v83
v_mfma_f32_32x32x16_bf16 v[48:63],a[60:63],v[188:191],v[48:63]
ds_read_b128 v[156:159],v5 offset:30464
v_mul_f32 v34,v34,v42
v_mul_f32 v35,v35,v43
v_mul_f32 v36,v36,v44
v_mul_f32 v37,v37,v45
s_waitcnt lgkmcnt(4)
v_mfma_f32_32x32x16_bf16 v[48:63],a[64:67],v[128:131],v[48:63]
buffer_load_dwordx4 v21,s[32:35],s63 offen offset:2048 sc1 lds
ds_read_b128 v[160:163],v5 offset:3456
v_mul_f32 v38,v38,v46
v_mul_f32 v39,v39,v47
v_mfma_f32_32x32x16_bf16 v[48:63],a[68:71],v[132:135],v[48:63]
ds_read_b128 v[164:167],v5 offset:3968
v_fma_f32 v76,v76,v74,v74
v_fma_f32 v77,v77,v74,v74
v_rcp_f32 v76,v76
v_fma_f32 v78,v78,v74,v74
v_mfma_f32_32x32x16_bf16 v[48:63],a[72:75],v[136:139],v[48:63]
buffer_load_dwordx4 v21,s[32:35],s63 offen offset:3072 sc1 lds
ds_read_b128 v[168:171],v5 offset:12672
s_add_u32 m0,s64,4096
v_fma_f32 v79,v79,v74,v74
v_mfma_f32_32x32x16_bf16 v[48:63],a[76:79],v[140:143],v[48:63]
ds_read_b128 v[172:175],v5 offset:13184
v_rcp_f32 v77,v77
v_rcp_f32 v78,v78
s_waitcnt lgkmcnt(4)
v_mfma_f32_32x32x16_bf16 v[48:63],a[80:83],v[144:147],v[48:63]
buffer_load_dwordx4 v22,s[32:35],s63 offen offset:0 sc1 lds
ds_read_b128 v[176:179],v5 offset:21888
s_add_u32 m0,s64,40960
v_fma_f32 v80,v80,v74,v74
v_mfma_f32_32x32x16_bf16 v[48:63],a[84:87],v[148:151],v[48:63]
ds_read_b128 v[180:183],v5 offset:22400
v_rcp_f32 v79,v79
v_rcp_f32 v80,v80
v_fma_f32 v81,v81,v74,v74
v_mfma_f32_32x32x16_bf16 v[48:63],a[88:91],v[152:155],v[48:63]
buffer_load_dwordx4 v23,s[32:35],s63 offen offset:0 sc1 lds
ds_read_b128 v[184:187],v5 offset:31104
v_rcp_f32 v81,v81
v_mfma_f32_32x32x16_bf16 v[48:63],a[92:95],v[156:159],v[48:63]
ds_read_b128 v[188:191],v5 offset:31616
v_fma_f32 v82,v82,v74,v74
v_fma_f32 v83,v83,v74,v74
v_rcp_f32 v82,v82
s_waitcnt lgkmcnt(4)
s_waitcnt vmcnt(10)
s_barrier
v_mfma_f32_32x32x16_bf16 v[48:63],a[96:99],v[160:163],v[48:63]
ds_read_b128 v[128:131],v5 offset:4608
v_rcp_f32 v83,v83
v_mul_f32 v32,v32,v76
v_mfma_f32_32x32x16_bf16 v[48:63],a[100:103],v[164:167],v[48:63]
ds_read_b128 v[132:135],v5 offset:5120
v_mul_f32 v33,v33,v77
v_mul_f32 v34,v34,v78
v_mul_f32 v35,v35,v79
v_mul_f32 v36,v36,v80
v_mul_f32 v37,v37,v81
v_mfma_f32_32x32x16_bf16 v[48:63],a[104:107],v[168:171],v[48:63]
ds_read_b128 v[136:139],v5 offset:13824
s_add_u32 m0,s64,5120
v_mul_f32 v38,v38,v82
buffer_load_dwordx4 v24,s[32:35],s63 offen offset:0 sc1 lds
v_mfma_f32_32x32x16_bf16 v[48:63],a[108:111],v[172:175],v[48:63]
ds_read_b128 v[140:143],v5 offset:14336
v_mul_f32 v39,v39,v83
v_cvt_pk_bf16_f32 v84,v32,v33
v_cvt_pk_bf16_f32 v85,v34,v35
v_cvt_pk_bf16_f32 v86,v36,v37
s_waitcnt lgkmcnt(4)
v_mfma_f32_32x32x16_bf16 v[48:63],a[112:115],v[176:179],v[48:63]
ds_read_b128 v[144:147],v5 offset:23040
v_cvt_pk_bf16_f32 v87,v38,v39
ds_read2st64_b32 v[64:65],v8 offset0:0 offset1:36
ds_read2st64_b32 v[66:67],v8 offset0:72 offset1:108
ds_read2st64_b32 v[68:69],v8 offset0:144 offset1:180
ds_read2st64_b32 v[70:71],v8 offset0:216 offset1:252
v_mfma_f32_32x32x16_bf16 v[48:63],a[116:119],v[180:183],v[48:63]
ds_read_b128 v[148:151],v5 offset:23552
v_xor_b32 v8,v9,v8
v_mfma_f32_32x32x16_bf16 v[48:63],a[120:123],v[184:187],v[48:63]
buffer_store_dwordx4 v[84:87],v15,s[32:35],s68 offen sc1
ds_read_b128 v[152:155],v5 offset:32256
v_mfma_f32_32x32x16_bf16 v[48:63],a[124:127],v[188:191],v[48:63]
ds_read_b128 v[156:159],v5 offset:32768
s_waitcnt lgkmcnt(8)
v_mfma_f32_32x32x16_bf16 v[48:63],a[128:131],v[128:131],v[48:63]
buffer_load_dwordx4 v24,s[32:35],s63 offen offset:1024 sc1 lds
ds_read_b128 v[160:163],v5 offset:5760
v_mfma_f32_32x32x16_bf16 v[48:63],a[132:135],v[132:135],v[48:63]
ds_read_b128 v[164:167],v5 offset:6272
s_waitcnt lgkmcnt(5)
v_add_f32 v64,v64,v65
v_add_f32 v66,v66,v67
v_add_f32 v68,v68,v69
v_add_f32 v70,v70,v71
v_mfma_f32_32x32x16_bf16 v[48:63],a[136:139],v[136:139],v[48:63]
buffer_load_dwordx4 v24,s[32:35],s63 offen offset:2048 sc1 lds
ds_read_b128 v[168:171],v5 offset:14976
v_add_f32 v66,v64,v66
v_add_f32 v68,v68,v70
v_mfma_f32_32x32x16_bf16 v[48:63],a[140:143],v[140:143],v[48:63]
ds_read_b128 v[172:175],v5 offset:15488
v_add_f32_dpp v64,v66,v66 row_ror:8 row_mask:0xf bank_mask:0x3
s_waitcnt lgkmcnt(4)
v_mfma_f32_32x32x16_bf16 v[48:63],a[144:147],v[144:147],v[48:63]
buffer_load_dwordx2 v[12:13],v14,s[0:3],s66 offen sc1
ds_read_b128 v[176:179],v5 offset:24192
v_add_f32_dpp v64,v68,v68 row_ror:8 row_mask:0xf bank_mask:0xc
v_mfma_f32_32x32x16_bf16 v[48:63],a[148:151],v[148:151],v[48:63]
ds_read_b128 v[180:183],v5 offset:24704
v_add_f32_dpp v64,v64,v64 row_ror:12 row_mask:0xf bank_mask:0xf
v_mfma_f32_32x32x16_bf16 v[48:63],a[152:155],v[152:155],v[48:63]
buffer_load_dwordx4 v24,s[32:35],s63 offen offset:3072 sc1 lds
ds_read_b128 v[184:187],v5 offset:33408
v_add_f32_dpp v64,v64,v64 quad_perm:[2,3,0,1] row_mask:0xf bank_mask:0xf
v_mfma_f32_32x32x16_bf16 v[48:63],a[156:159],v[156:159],v[48:63]
ds_read_b128 v[188:191],v5 offset:33920
s_add_u32 m0,s64,41984
v_mul_f32 v64,0x3a800000,v64
s_waitcnt lgkmcnt(4)
v_mfma_f32_32x32x16_bf16 v[48:63],a[160:163],v[160:163],v[48:63]
buffer_load_dwordx4 v25,s[32:35],s63 offen offset:0 sc1 lds
ds_read_b128 v[128:131],v5 offset:6912
v_mul_f32 v65,v64,v64
v_mfma_f32_32x32x16_bf16 v[48:63],a[164:167],v[164:167],v[48:63]
ds_read_b128 v[132:135],v5 offset:7424
v_sub_f32_dpp v65,v64,v65 quad_perm:[1,1,3,3] row_mask:0xf bank_mask:0xf
v_mfma_f32_32x32x16_bf16 v[48:63],a[168:171],v[168:171],v[48:63]
ds_read_b128 v[136:139],v5 offset:16128
v_max_f32 v65,0,v65
v_mfma_f32_32x32x16_bf16 v[48:63],a[172:175],v[172:175],v[48:63]
ds_read_b128 v[140:143],v5 offset:16640
v_cvt_pk_bf16_f32 v65,0,v65
s_waitcnt lgkmcnt(4)
v_mfma_f32_32x32x16_bf16 v[48:63],a[176:179],v[176:179],v[48:63]
buffer_load_dwordx4 v25,s[32:35],s63 offen offset:1024 sc1 lds
ds_read_b128 v[144:147],v5 offset:25344
v_add_f32 v65,0x3727c5ac,v65
v_mfma_f32_32x32x16_bf16 v[48:63],a[180:183],v[180:183],v[48:63]
ds_read_b128 v[148:151],v5 offset:25856
v_cvt_pk_bf16_f32 v65,0,v65
v_mfma_f32_32x32x16_bf16 v[48:63],a[184:187],v[184:187],v[48:63]
buffer_load_dwordx4 v25,s[32:35],s63 offen offset:2048 sc1 lds
ds_read_b128 v[152:155],v5 offset:34560
v_sqrt_f32 v65,v65
v_mfma_f32_32x32x16_bf16 v[48:63],a[188:191],v[188:191],v[48:63]
ds_read_b128 v[156:159],v5 offset:35072
v_cvt_pk_bf16_f32 v65,v64,v65
s_waitcnt lgkmcnt(4)
v_mfma_f32_32x32x16_bf16 v[48:63],a[192:195],v[128:131],v[48:63]
ds_read_b128 v[160:163],v5 offset:8064
ds_write_b32 v10,v65 offset:128
v_mfma_f32_32x32x16_bf16 v[48:63],a[196:199],v[132:135],v[48:63]
ds_read_b128 v[164:167],v5 offset:8576
v_mfma_f32_32x32x16_bf16 v[48:63],a[200:203],v[136:139],v[48:63]
buffer_load_dwordx4 v25,s[32:35],s63 offen offset:3072 sc1 lds
ds_read_b128 v[168:171],v5 offset:17280
s_add_u32 s63,s63,s30
v_mfma_f32_32x32x16_bf16 v[48:63],a[204:207],v[140:143],v[48:63]
ds_read_b128 v[172:175],v5 offset:17792
s_waitcnt lgkmcnt(5)
v_mfma_f32_32x32x16_bf16 v[48:63],a[208:211],v[144:147],v[48:63]
ds_read_b128 v[176:179],v5 offset:26496
v_mfma_f32_32x32x16_bf16 v[48:63],a[212:215],v[148:151],v[48:63]
ds_read_b128 v[180:183],v5 offset:27008
v_mfma_f32_32x32x16_bf16 v[48:63],a[216:219],v[152:155],v[48:63]
ds_read_b128 v[184:187],v5 offset:35712
v_mfma_f32_32x32x16_bf16 v[48:63],a[220:223],v[156:159],v[48:63]
ds_read_b128 v[188:191],v5 offset:36224
s_waitcnt lgkmcnt(4)
s_waitcnt vmcnt(8)
s_barrier
v_mfma_f32_32x32x16_bf16 v[48:63],a[224:227],v[160:163],v[48:63]
ds_read_b128 v[128:131],v4 offset:0
v_mfma_f32_32x32x16_bf16 v[48:63],a[228:231],v[164:167],v[48:63]
ds_read_b128 v[132:135],v4 offset:512
ds_read_b32 v74,v11 offset:128
v_mfma_f32_32x32x16_bf16 v[48:63],a[232:235],v[168:171],v[48:63]
ds_read_b128 v[136:139],v4 offset:9216
v_mfma_f32_32x32x16_bf16 v[48:63],a[236:239],v[172:175],v[48:63]
ds_read_b128 v[140:143],v4 offset:9728
s_waitcnt lgkmcnt(5)
v_mfma_f32_32x32x16_bf16 v[48:63],a[240:243],v[176:179],v[48:63]
ds_read_b128 v[144:147],v4 offset:18432
s_waitcnt lgkmcnt(3)
v_lshlrev_b32 v72,16,v74
v_and_b32 v74,0xffff0000,v74
v_mfma_f32_32x32x16_bf16 v[48:63],a[244:247],v[180:183],v[48:63]
ds_read_b128 v[148:151],v4 offset:18944
v_rcp_f32 v73,v74
v_mul_f32 v74,v74,v74
v_mfma_f32_32x32x16_bf16 v[48:63],a[248:251],v[184:187],v[48:63]
ds_read_b128 v[152:155],v4 offset:27648
v_mul_f32 v73,0xbfb8aa3b,v73
s_sub_u32 s62,s62,1
v_mfma_f32_32x32x16_bf16 v[48:63],a[252:255],v[188:191],v[48:63]
ds_read_b128 v[156:159],v4 offset:28160
s_cbranch_scc0 .L_hl_1
s_nop 1
s_add_u32 s68,s68,s30
s_nop 6
v_fma_f32 v48,-v72,v112,v48
v_fma_f32 v49,-v72,v113,v49
v_fma_f32 v50,-v72,v114,v50
v_fma_f32 v51,-v72,v115,v51
v_fma_f32 v52,-v72,v116,v52
v_fma_f32 v53,-v72,v117,v53
v_fma_f32 v54,-v72,v118,v54
v_fma_f32 v55,-v72,v119,v55
v_fma_f32 v56,-v72,v120,v56
v_fma_f32 v57,-v72,v121,v57
v_fma_f32 v58,-v72,v122,v58
v_fma_f32 v59,-v72,v123,v59
v_fma_f32 v60,-v72,v124,v60
v_fma_f32 v61,-v72,v125,v61
v_fma_f32 v62,-v72,v126,v62
v_fma_f32 v63,-v72,v127,v63
v_mul_f32 v76,v48,v73
v_mul_f32 v77,v49,v73
v_mul_f32 v78,v50,v73
v_mul_f32 v79,v51,v73
v_mul_f32 v80,v52,v73
v_mul_f32 v81,v53,v73
v_mul_f32 v82,v54,v73
v_mul_f32 v83,v55,v73
v_exp_f32 v76,v76
v_exp_f32 v77,v77
v_mul_f32 v48,v48,v56
v_mul_f32 v49,v49,v57
v_exp_f32 v78,v78
v_exp_f32 v79,v79
v_mul_f32 v50,v50,v58
v_mul_f32 v51,v51,v59
v_exp_f32 v80,v80
v_exp_f32 v81,v81
v_mul_f32 v52,v52,v60
v_mul_f32 v53,v53,v61
v_exp_f32 v82,v82
v_exp_f32 v83,v83
v_mul_f32 v54,v54,v62
v_mul_f32 v55,v55,v63
v_fma_f32 v76,v76,v74,v74
v_fma_f32 v77,v77,v74,v74
v_rcp_f32 v76,v76
v_rcp_f32 v77,v77
v_fma_f32 v78,v78,v74,v74
v_fma_f32 v79,v79,v74,v74
v_rcp_f32 v78,v78
v_rcp_f32 v79,v79
v_fma_f32 v80,v80,v74,v74
v_fma_f32 v81,v81,v74,v74
v_rcp_f32 v80,v80
v_rcp_f32 v81,v81
v_fma_f32 v82,v82,v74,v74
v_fma_f32 v83,v83,v74,v74
v_rcp_f32 v82,v82
v_rcp_f32 v83,v83
v_mul_f32 v48,v48,v76
v_mul_f32 v49,v49,v77
v_mul_f32 v50,v50,v78
v_mul_f32 v51,v51,v79
v_mul_f32 v52,v52,v80
v_mul_f32 v53,v53,v81
v_mul_f32 v54,v54,v82
v_mul_f32 v55,v55,v83
v_cvt_pk_bf16_f32 v84,v48,v49
v_cvt_pk_bf16_f32 v85,v50,v51
v_cvt_pk_bf16_f32 v86,v52,v53
v_cvt_pk_bf16_f32 v87,v54,v55
buffer_store_dwordx4 v[84:87],v15,s[32:35],s68 offen sc1
s_waitcnt vmcnt(0)
s_barrier
buffer_store_dwordx2 v[16:17],v18,s[32:35],s68 offen sc1
s_cbranch_execz .L_hbk
s_branch .L_end
.p2align 6
.L_hv2:
.L_hl_2:
s_waitcnt lgkmcnt(4)
v_mfma_f32_32x32x16_bf16 v[32:47],a[0:3],v[128:131],0
ds_read_b128 v[160:163],v4 offset:1152
s_waitcnt vmcnt(5)
v_cmp_ne_u64 vcc,v[16:17],v[12:13]
s_add_u32 s68,s68,s30
v_mfma_f32_32x32x16_bf16 v[32:47],a[4:7],v[132:135],v[32:47]
ds_read_b128 v[164:167],v4 offset:1664
s_cbranch_vccnz .L_rth0_2
.L_bkh0_2:
s_add_u32 s66,s66,s30
v_mfma_f32_32x32x16_bf16 v[32:47],a[8:11],v[136:139],v[32:47]
ds_read_b128 v[168:171],v4 offset:10368
s_add_u32 m0,s64,73728
v_fma_f32 v48,-v72,v112,v48
buffer_load_dwordx4 v20,s[32:35],s63 offen offset:0 sc1 lds
v_mfma_f32_32x32x16_bf16 v[32:47],a[12:15],v[140:143],v[32:47]
ds_read_b128 v[172:175],v4 offset:10880
v_fma_f32 v49,-v72,v113,v49
v_fma_f32 v50,-v72,v114,v50
)#"
R"#(
v_fma_f32 v51,-v72,v115,v51
v_fma_f32 v52,-v72,v116,v52
s_waitcnt lgkmcnt(4)
v_mfma_f32_32x32x16_bf16 v[32:47],a[16:19],v[144:147],v[32:47]
buffer_load_dwordx4 v20,s[32:35],s63 offen offset:1024 sc1 lds
ds_read_b128 v[176:179],v4 offset:19584
v_fma_f32 v53,-v72,v117,v53
v_fma_f32 v54,-v72,v118,v54
v_mfma_f32_32x32x16_bf16 v[32:47],a[20:23],v[148:151],v[32:47]
ds_read_b128 v[180:183],v4 offset:20096
v_fma_f32 v55,-v72,v119,v55
v_fma_f32 v56,-v72,v120,v56
v_fma_f32 v57,-v72,v121,v57
v_fma_f32 v58,-v72,v122,v58
v_fma_f32 v59,-v72,v123,v59
v_mfma_f32_32x32x16_bf16 v[32:47],a[24:27],v[152:155],v[32:47]
buffer_load_dwordx4 v20,s[32:35],s63 offen offset:2048 sc1 lds
ds_read_b128 v[184:187],v4 offset:28800
v_fma_f32 v60,-v72,v124,v60
v_fma_f32 v61,-v72,v125,v61
v_mfma_f32_32x32x16_bf16 v[32:47],a[28:31],v[156:159],v[32:47]
ds_read_b128 v[188:191],v4 offset:29312
v_fma_f32 v62,-v72,v126,v62
v_fma_f32 v63,-v72,v127,v63
v_mul_f32 v76,v48,v73
v_mul_f32 v77,v49,v73
s_waitcnt lgkmcnt(4)
v_mfma_f32_32x32x16_bf16 v[32:47],a[32:35],v[160:163],v[32:47]
buffer_load_dwordx4 v20,s[32:35],s63 offen offset:3072 sc1 lds
ds_read_b128 v[128:131],v4 offset:2304
v_exp_f32 v76,v76
v_mfma_f32_32x32x16_bf16 v[32:47],a[36:39],v[164:167],v[32:47]
ds_read_b128 v[132:135],v4 offset:2816
v_exp_f32 v77,v77
v_mul_f32 v78,v50,v73
v_mul_f32 v79,v51,v73
v_mul_f32 v80,v52,v73
v_mfma_f32_32x32x16_bf16 v[32:47],a[40:43],v[168:171],v[32:47]
ds_read_b128 v[136:139],v4 offset:11520
s_add_u32 m0,s64,110592
v_exp_f32 v78,v78
v_exp_f32 v79,v79
v_mfma_f32_32x32x16_bf16 v[32:47],a[44:47],v[172:175],v[32:47]
ds_read_b128 v[140:143],v4 offset:12032
v_exp_f32 v80,v80
v_mul_f32 v81,v53,v73
v_mul_f32 v82,v54,v73
s_waitcnt lgkmcnt(4)
v_mfma_f32_32x32x16_bf16 v[32:47],a[48:51],v[176:179],v[32:47]
buffer_load_dwordx4 v21,s[32:35],s63 offen offset:0 sc1 lds
ds_read_b128 v[144:147],v4 offset:20736
v_exp_f32 v81,v81
v_mfma_f32_32x32x16_bf16 v[32:47],a[52:55],v[180:183],v[32:47]
ds_read_b128 v[148:151],v4 offset:21248
v_exp_f32 v82,v82
v_mul_f32 v83,v55,v73
v_mul_f32 v48,v48,v56
v_mul_f32 v49,v49,v57
v_mfma_f32_32x32x16_bf16 v[32:47],a[56:59],v[184:187],v[32:47]
buffer_load_dwordx4 v21,s[32:35],s63 offen offset:1024 sc1 lds
ds_read_b128 v[152:155],v4 offset:29952
v_exp_f32 v83,v83
v_mfma_f32_32x32x16_bf16 v[32:47],a[60:63],v[188:191],v[32:47]
ds_read_b128 v[156:159],v4 offset:30464
v_mul_f32 v50,v50,v58
v_mul_f32 v51,v51,v59
v_mul_f32 v52,v52,v60
v_mul_f32 v53,v53,v61
s_waitcnt lgkmcnt(4)
v_mfma_f32_32x32x16_bf16 v[32:47],a[64:67],v[128:131],v[32:47]
buffer_load_dwordx4 v21,s[32:35],s63 offen offset:2048 sc1 lds
ds_read_b128 v[160:163],v4 offset:3456
v_mul_f32 v54,v54,v62
v_mul_f32 v55,v55,v63
v_mfma_f32_32x32x16_bf16 v[32:47],a[68:71],v[132:135],v[32:47]
ds_read_b128 v[164:167],v4 offset:3968
v_fma_f32 v76,v76,v74,v74
v_fma_f32 v77,v77,v74,v74
v_rcp_f32 v76,v76
v_fma_f32 v78,v78,v74,v74
v_mfma_f32_32x32x16_bf16 v[32:47],a[72:75],v[136:139],v[32:47]
buffer_load_dwordx4 v21,s[32:35],s63 offen offset:3072 sc1 lds
ds_read_b128 v[168:171],v4 offset:12672
s_add_u32 m0,s64,77824
v_fma_f32 v79,v79,v74,v74
v_mfma_f32_32x32x16_bf16 v[32:47],a[76:79],v[140:143],v[32:47]
ds_read_b128 v[172:175],v4 offset:13184
v_rcp_f32 v77,v77
v_rcp_f32 v78,v78
s_waitcnt lgkmcnt(4)
v_mfma_f32_32x32x16_bf16 v[32:47],a[80:83],v[144:147],v[32:47]
buffer_load_dwordx4 v22,s[32:35],s63 offen offset:0 sc1 lds
ds_read_b128 v[176:179],v4 offset:21888
s_add_u32 m0,s64,114688
v_fma_f32 v80,v80,v74,v74
v_mfma_f32_32x32x16_bf16 v[32:47],a[84:87],v[148:151],v[32:47]
ds_read_b128 v[180:183],v4 offset:22400
v_rcp_f32 v79,v79
v_rcp_f32 v80,v80
v_fma_f32 v81,v81,v74,v74
v_mfma_f32_32x32x16_bf16 v[32:47],a[88:91],v[152:155],v[32:47]
buffer_load_dwordx4 v23,s[32:35],s63 offen offset:0 sc1 lds
ds_read_b128 v[184:187],v4 offset:31104
v_rcp_f32 v81,v81
s_waitcnt vmcnt(10)
s_barrier
v_mfma_f32_32x32x16_bf16 v[32:47],a[92:95],v[156:159],v[32:47]
ds_read_b128 v[188:191],v4 offset:31616
v_fma_f32 v82,v82,v74,v74
v_fma_f32 v83,v83,v74,v74
v_rcp_f32 v82,v82
s_waitcnt lgkmcnt(4)
v_mfma_f32_32x32x16_bf16 v[32:47],a[96:99],v[160:163],v[32:47]
ds_read_b128 v[128:131],v4 offset:4608
v_rcp_f32 v83,v83
v_mul_f32 v48,v48,v76
v_mfma_f32_32x32x16_bf16 v[32:47],a[100:103],v[164:167],v[32:47]
ds_read_b128 v[132:135],v4 offset:5120
v_mul_f32 v49,v49,v77
v_mul_f32 v50,v50,v78
v_mul_f32 v51,v51,v79
v_mul_f32 v52,v52,v80
v_mul_f32 v53,v53,v81
v_mfma_f32_32x32x16_bf16 v[32:47],a[104:107],v[168:171],v[32:47]
ds_read_b128 v[136:139],v4 offset:13824
s_add_u32 m0,s64,78848
v_mul_f32 v54,v54,v82
buffer_load_dwordx4 v24,s[32:35],s63 offen offset:0 sc1 lds
v_mfma_f32_32x32x16_bf16 v[32:47],a[108:111],v[172:175],v[32:47]
ds_read_b128 v[140:143],v4 offset:14336
v_mul_f32 v55,v55,v83
v_cvt_pk_bf16_f32 v84,v48,v49
v_cvt_pk_bf16_f32 v85,v50,v51
v_cvt_pk_bf16_f32 v86,v52,v53
s_waitcnt lgkmcnt(4)
v_mfma_f32_32x32x16_bf16 v[32:47],a[112:115],v[176:179],v[32:47]
ds_read_b128 v[144:147],v4 offset:23040
v_cvt_pk_bf16_f32 v87,v54,v55
ds_read2st64_b32 v[64:65],v8 offset0:0 offset1:36
ds_read2st64_b32 v[66:67],v8 offset0:72 offset1:108
ds_read2st64_b32 v[68:69],v8 offset0:144 offset1:180
ds_read2st64_b32 v[70:71],v8 offset0:216 offset1:252
v_mfma_f32_32x32x16_bf16 v[32:47],a[116:119],v[180:183],v[32:47]
ds_read_b128 v[148:151],v4 offset:23552
v_xor_b32 v8,v9,v8
v_mfma_f32_32x32x16_bf16 v[32:47],a[120:123],v[184:187],v[32:47]
buffer_store_dwordx4 v[84:87],v15,s[32:35],s68 offen sc1
ds_read_b128 v[152:155],v4 offset:32256
v_mfma_f32_32x32x16_bf16 v[32:47],a[124:127],v[188:191],v[32:47]
ds_read_b128 v[156:159],v4 offset:32768
s_waitcnt lgkmcnt(8)
v_mfma_f32_32x32x16_bf16 v[32:47],a[128:131],v[128:131],v[32:47]
buffer_load_dwordx4 v24,s[32:35],s63 offen offset:1024 sc1 lds
ds_read_b128 v[160:163],v4 offset:5760
v_mfma_f32_32x32x16_bf16 v[32:47],a[132:135],v[132:135],v[32:47]
ds_read_b128 v[164:167],v4 offset:6272
s_waitcnt lgkmcnt(5)
v_add_f32 v64,v64,v65
v_add_f32 v66,v66,v67
v_add_f32 v68,v68,v69
v_add_f32 v70,v70,v71
v_mfma_f32_32x32x16_bf16 v[32:47],a[136:139],v[136:139],v[32:47]
buffer_load_dwordx4 v24,s[32:35],s63 offen offset:2048 sc1 lds
ds_read_b128 v[168:171],v4 offset:14976
v_add_f32 v66,v64,v66
v_add_f32 v68,v68,v70
v_mfma_f32_32x32x16_bf16 v[32:47],a[140:143],v[140:143],v[32:47]
ds_read_b128 v[172:175],v4 offset:15488
v_add_f32_dpp v64,v66,v66 row_ror:8 row_mask:0xf bank_mask:0x3
s_waitcnt lgkmcnt(4)
v_mfma_f32_32x32x16_bf16 v[32:47],a[144:147],v[144:147],v[32:47]
buffer_load_dwordx2 v[12:13],v14,s[0:3],s66 offen sc1
ds_read_b128 v[176:179],v4 offset:24192
v_add_f32_dpp v64,v68,v68 row_ror:8 row_mask:0xf bank_mask:0xc
v_mfma_f32_32x32x16_bf16 v[32:47],a[148:151],v[148:151],v[32:47]
ds_read_b128 v[180:183],v4 offset:24704
v_add_f32_dpp v64,v64,v64 row_ror:12 row_mask:0xf bank_mask:0xf
v_mfma_f32_32x32x16_bf16 v[32:47],a[152:155],v[152:155],v[32:47]
buffer_load_dwordx4 v24,s[32:35],s63 offen offset:3072 sc1 lds
ds_read_b128 v[184:187],v4 offset:33408
v_add_f32_dpp v64,v64,v64 quad_perm:[2,3,0,1] row_mask:0xf bank_mask:0xf
v_mfma_f32_32x32x16_bf16 v[32:47],a[156:159],v[156:159],v[32:47]
ds_read_b128 v[188:191],v4 offset:33920
s_add_u32 m0,s64,115712
v_mul_f32 v64,0x3a800000,v64
s_waitcnt lgkmcnt(4)
v_mfma_f32_32x32x16_bf16 v[32:47],a[160:163],v[160:163],v[32:47]
buffer_load_dwordx4 v25,s[32:35],s63 offen offset:0 sc1 lds
ds_read_b128 v[128:131],v4 offset:6912
v_mul_f32 v65,v64,v64
v_mfma_f32_32x32x16_bf16 v[32:47],a[164:167],v[164:167],v[32:47]
ds_read_b128 v[132:135],v4 offset:7424
v_sub_f32_dpp v65,v64,v65 quad_perm:[1,1,3,3] row_mask:0xf bank_mask:0xf
v_mfma_f32_32x32x16_bf16 v[32:47],a[168:171],v[168:171],v[32:47]
ds_read_b128 v[136:139],v4 offset:16128
v_max_f32 v65,0,v65
v_mfma_f32_32x32x16_bf16 v[32:47],a[172:175],v[172:175],v[32:47]
ds_read_b128 v[140:143],v4 offset:16640
v_cvt_pk_bf16_f32 v65,0,v65
s_waitcnt lgkmcnt(4)
v_mfma_f32_32x32x16_bf16 v[32:47],a[176:179],v[176:179],v[32:47]
buffer_load_dwordx4 v25,s[32:35],s63 offen offset:1024 sc1 lds
ds_read_b128 v[144:147],v4 offset:25344
v_add_f32 v65,0x3727c5ac,v65
v_mfma_f32_32x32x16_bf16 v[32:47],a[180:183],v[180:183],v[32:47]
ds_read_b128 v[148:151],v4 offset:25856
v_cvt_pk_bf16_f32 v65,0,v65
v_mfma_f32_32x32x16_bf16 v[32:47],a[184:187],v[184:187],v[32:47]
buffer_load_dwordx4 v25,s[32:35],s63 offen offset:2048 sc1 lds
ds_read_b128 v[152:155],v4 offset:34560
v_sqrt_f32 v65,v65
v_mfma_f32_32x32x16_bf16 v[32:47],a[188:191],v[188:191],v[32:47]
ds_read_b128 v[156:159],v4 offset:35072
v_cvt_pk_bf16_f32 v65,v64,v65
s_waitcnt lgkmcnt(4)
v_mfma_f32_32x32x16_bf16 v[32:47],a[192:195],v[128:131],v[32:47]
ds_read_b128 v[160:163],v4 offset:8064
ds_write_b32 v10,v65 offset:0
v_mfma_f32_32x32x16_bf16 v[32:47],a[196:199],v[132:135],v[32:47]
ds_read_b128 v[164:167],v4 offset:8576
v_mfma_f32_32x32x16_bf16 v[32:47],a[200:203],v[136:139],v[32:47]
buffer_load_dwordx4 v25,s[32:35],s63 offen offset:3072 sc1 lds
ds_read_b128 v[168:171],v4 offset:17280
s_add_u32 s63,s63,s30
v_mfma_f32_32x32x16_bf16 v[32:47],a[204:207],v[140:143],v[32:47]
ds_read_b128 v[172:175],v4 offset:17792
s_waitcnt lgkmcnt(5)
v_mfma_f32_32x32x16_bf16 v[32:47],a[208:211],v[144:147],v[32:47]
ds_read_b128 v[176:179],v4 offset:26496
v_mfma_f32_32x32x16_bf16 v[32:47],a[212:215],v[148:151],v[32:47]
ds_read_b128 v[180:183],v4 offset:27008
v_mfma_f32_32x32x16_bf16 v[32:47],a[216:219],v[152:155],v[32:47]
ds_read_b128 v[184:187],v4 offset:35712
s_waitcnt vmcnt(8)
s_waitcnt lgkmcnt(6)
s_barrier
v_mfma_f32_32x32x16_bf16 v[32:47],a[220:223],v[156:159],v[32:47]
ds_read_b128 v[188:191],v4 offset:36224
s_waitcnt lgkmcnt(4)
v_mfma_f32_32x32x16_bf16 v[32:47],a[224:227],v[160:163],v[32:47]
ds_read_b128 v[128:131],v5 offset:0
v_mfma_f32_32x32x16_bf16 v[32:47],a[228:231],v[164:167],v[32:47]
ds_read_b128 v[132:135],v5 offset:512
ds_read_b32 v74,v11 offset:0
v_mfma_f32_32x32x16_bf16 v[32:47],a[232:235],v[168:171],v[32:47]
ds_read_b128 v[136:139],v5 offset:9216
v_mfma_f32_32x32x16_bf16 v[32:47],a[236:239],v[172:175],v[32:47]
ds_read_b128 v[140:143],v5 offset:9728
s_waitcnt lgkmcnt(5)
v_mfma_f32_32x32x16_bf16 v[32:47],a[240:243],v[176:179],v[32:47]
ds_read_b128 v[144:147],v5 offset:18432
s_waitcnt lgkmcnt(3)
v_lshlrev_b32 v72,16,v74
v_and_b32 v74,0xffff0000,v74
v_mfma_f32_32x32x16_bf16 v[32:47],a[244:247],v[180:183],v[32:47]
ds_read_b128 v[148:151],v5 offset:18944
v_rcp_f32 v73,v74
v_mul_f32 v74,v74,v74
v_mfma_f32_32x32x16_bf16 v[32:47],a[248:251],v[184:187],v[32:47]
ds_read_b128 v[152:155],v5 offset:27648
v_mul_f32 v73,0xbfb8aa3b,v73
v_mfma_f32_32x32x16_bf16 v[32:47],a[252:255],v[188:191],v[32:47]
ds_read_b128 v[156:159],v5 offset:28160
s_waitcnt lgkmcnt(4)
v_mfma_f32_32x32x16_bf16 v[48:63],a[0:3],v[128:131],0
ds_read_b128 v[160:163],v5 offset:1152
s_waitcnt vmcnt(5)
v_cmp_ne_u64 vcc,v[16:17],v[12:13]
s_add_u32 s68,s68,s30
v_mfma_f32_32x32x16_bf16 v[48:63],a[4:7],v[132:135],v[48:63]
ds_read_b128 v[164:167],v5 offset:1664
s_cbranch_vccnz .L_rth1_2
.L_bkh1_2:
s_add_u32 s66,s66,s30
v_mfma_f32_32x32x16_bf16 v[48:63],a[8:11],v[136:139],v[48:63]
ds_read_b128 v[168:171],v5 offset:10368
s_add_u32 m0,s64,0
v_fma_f32 v32,-v72,v112,v32
buffer_load_dwordx4 v20,s[32:35],s63 offen offset:0 sc1 lds
v_mfma_f32_32x32x16_bf16 v[48:63],a[12:15],v[140:143],v[48:63]
ds_read_b128 v[172:175],v5 offset:10880
v_fma_f32 v33,-v72,v113,v33
v_fma_f32 v34,-v72,v114,v34
v_fma_f32 v35,-v72,v115,v35
v_fma_f32 v36,-v72,v116,v36
s_waitcnt lgkmcnt(4)
v_mfma_f32_32x32x16_bf16 v[48:63],a[16:19],v[144:147],v[48:63]
buffer_load_dwordx4 v20,s[32:35],s63 offen offset:1024 sc1 lds
ds_read_b128 v[176:179],v5 offset:19584
v_fma_f32 v37,-v72,v117,v37
v_fma_f32 v38,-v72,v118,v38
v_mfma_f32_32x32x16_bf16 v[48:63],a[20:23],v[148:151],v[48:63]
ds_read_b128 v[180:183],v5 offset:20096
v_fma_f32 v39,-v72,v119,v39
v_fma_f32 v40,-v72,v120,v40
v_fma_f32 v41,-v72,v121,v41
v_fma_f32 v42,-v72,v122,v42
v_fma_f32 v43,-v72,v123,v43
v_mfma_f32_32x32x16_bf16 v[48:63],a[24:27],v[152:155],v[48:63]
buffer_load_dwordx4 v20,s[32:35],s63 offen offset:2048 sc1 lds
ds_read_b128 v[184:187],v5 offset:28800
v_fma_f32 v44,-v72,v124,v44
v_fma_f32 v45,-v72,v125,v45
v_mfma_f32_32x32x16_bf16 v[48:63],a[28:31],v[156:159],v[48:63]
ds_read_b128 v[188:191],v5 offset:29312
v_fma_f32 v46,-v72,v126,v46
v_fma_f32 v47,-v72,v127,v47
v_mul_f32 v76,v32,v73
v_mul_f32 v77,v33,v73
s_waitcnt lgkmcnt(4)
v_mfma_f32_32x32x16_bf16 v[48:63],a[32:35],v[160:163],v[48:63]
buffer_load_dwordx4 v20,s[32:35],s63 offen offset:3072 sc1 lds
ds_read_b128 v[128:131],v5 offset:2304
v_exp_f32 v76,v76
v_mfma_f32_32x32x16_bf16 v[48:63],a[36:39],v[164:167],v[48:63]
ds_read_b128 v[132:135],v5 offset:2816
v_exp_f32 v77,v77
v_mul_f32 v78,v34,v73
v_mul_f32 v79,v35,v73
v_mul_f32 v80,v36,v73
v_mfma_f32_32x32x16_bf16 v[48:63],a[40:43],v[168:171],v[48:63]
ds_read_b128 v[136:139],v5 offset:11520
s_add_u32 m0,s64,36864
v_exp_f32 v78,v78
v_exp_f32 v79,v79
v_mfma_f32_32x32x16_bf16 v[48:63],a[44:47],v[172:175],v[48:63]
ds_read_b128 v[140:143],v5 offset:12032
v_exp_f32 v80,v80
v_mul_f32 v81,v37,v73
v_mul_f32 v82,v38,v73
s_waitcnt lgkmcnt(4)
v_mfma_f32_32x32x16_bf16 v[48:63],a[48:51],v[176:179],v[48:63]
buffer_load_dwordx4 v21,s[32:35],s63 offen offset:0 sc1 lds
ds_read_b128 v[144:147],v5 offset:20736
v_exp_f32 v81,v81
v_mfma_f32_32x32x16_bf16 v[48:63],a[52:55],v[180:183],v[48:63]
ds_read_b128 v[148:151],v5 offset:21248
v_exp_f32 v82,v82
v_mul_f32 v83,v39,v73
v_mul_f32 v32,v32,v40
v_mul_f32 v33,v33,v41
v_mfma_f32_32x32x16_bf16 v[48:63],a[56:59],v[184:187],v[48:63]
buffer_load_dwordx4 v21,s[32:35],s63 offen offset:1024 sc1 lds
ds_read_b128 v[152:155],v5 offset:29952
v_exp_f32 v83,v83
v_mfma_f32_32x32x16_bf16 v[48:63],a[60:63],v[188:191],v[48:63]
ds_read_b128 v[156:159],v5 offset:30464
v_mul_f32 v34,v34,v42
v_mul_f32 v35,v35,v43
v_mul_f32 v36,v36,v44
v_mul_f32 v37,v37,v45
s_waitcnt lgkmcnt(4)
v_mfma_f32_32x32x16_bf16 v[48:63],a[64:67],v[128:131],v[48:63]
buffer_load_dwordx4 v21,s[32:35],s63 offen offset:2048 sc1 lds
ds_read_b128 v[160:163],v5 offset:3456
v_mul_f32 v38,v38,v46
v_mul_f32 v39,v39,v47
v_mfma_f32_32x32x16_bf16 v[48:63],a[68:71],v[132:135],v[48:63]
ds_read_b128 v[164:167],v5 offset:3968
v_fma_f32 v76,v76,v74,v74
v_fma_f32 v77,v77,v74,v74
v_rcp_f32 v76,v76
v_fma_f32 v78,v78,v74,v74
v_mfma_f32_32x32x16_bf16 v[48:63],a[72:75],v[136:139],v[48:63]
buffer_load_dwordx4 v21,s[32:35],s63 offen offset:3072 sc1 lds
ds_read_b128 v[168:171],v5 offset:12672
s_add_u32 m0,s64,4096
v_fma_f32 v79,v79,v74,v74
v_mfma_f32_32x32x16_bf16 v[48:63],a[76:79],v[140:143],v[48:63]
ds_read_b128 v[172:175],v5 offset:13184
v_rcp_f32 v77,v77
v_rcp_f32 v78,v78
s_waitcnt lgkmcnt(4)
v_mfma_f32_32x32x16_bf16 v[48:63],a[80:83],v[144:147],v[48:63]
buffer_load_dwordx4 v22,s[32:35],s63 offen offset:0 sc1 lds
ds_read_b128 v[176:179],v5 offset:21888
s_add_u32 m0,s64,40960
v_fma_f32 v80,v80,v74,v74
v_mfma_f32_32x32x16_bf16 v[48:63],a[84:87],v[148:151],v[48:63]
ds_read_b128 v[180:183],v5 offset:22400
v_rcp_f32 v79,v79
v_rcp_f32 v80,v80
v_fma_f32 v81,v81,v74,v74
v_mfma_f32_32x32x16_bf16 v[48:63],a[88:91],v[152:155],v[48:63]
buffer_load_dwordx4 v23,s[32:35],s63 offen offset:0 sc1 lds
ds_read_b128 v[184:187],v5 offset:31104
v_rcp_f32 v81,v81
s_waitcnt vmcnt(10)
s_barrier
v_mfma_f32_32x32x16_bf16 v[48:63],a[92:95],v[156:159],v[48:63]
ds_read_b128 v[188:191],v5 offset:31616
v_fma_f32 v82,v82,v74,v74
v_fma_f32 v83,v83,v74,v74
v_rcp_f32 v82,v82
s_waitcnt lgkmcnt(4)
v_mfma_f32_32x32x16_bf16 v[48:63],a[96:99],v[160:163],v[48:63]
ds_read_b128 v[128:131],v5 offset:4608
v_rcp_f32 v83,v83
v_mul_f32 v32,v32,v76
v_mfma_f32_32x32x16_bf16 v[48:63],a[100:103],v[164:167],v[48:63]
ds_read_b128 v[132:135],v5 offset:5120
v_mul_f32 v33,v33,v77
v_mul_f32 v34,v34,v78
v_mul_f32 v35,v35,v79
v_mul_f32 v36,v36,v80
v_mul_f32 v37,v37,v81
v_mfma_f32_32x32x16_bf16 v[48:63],a[104:107],v[168:171],v[48:63]
ds_read_b128 v[136:139],v5 offset:13824
s_add_u32 m0,s64,5120
v_mul_f32 v38,v38,v82
buffer_load_dwordx4 v24,s[32:35],s63 offen offset:0 sc1 lds
v_mfma_f32_32x32x16_bf16 v[48:63],a[108:111],v[172:175],v[48:63]
ds_read_b128 v[140:143],v5 offset:14336
v_mul_f32 v39,v39,v83
v_cvt_pk_bf16_f32 v84,v32,v33
v_cvt_pk_bf16_f32 v85,v34,v35
v_cvt_pk_bf16_f32 v86,v36,v37
s_waitcnt lgkmcnt(4)
v_mfma_f32_32x32x16_bf16 v[48:63],a[112:115],v[176:179],v[48:63]
ds_read_b128 v[144:147],v5 offset:23040
v_cvt_pk_bf16_f32 v87,v38,v39
ds_read2st64_b32 v[64:65],v8 offset0:0 offset1:36
ds_read2st64_b32 v[66:67],v8 offset0:72 offset1:108
ds_read2st64_b32 v[68:69],v8 offset0:144 offset1:180
ds_read2st64_b32 v[70:71],v8 offset0:216 offset1:252
v_mfma_f32_32x32x16_bf16 v[48:63],a[116:119],v[180:183],v[48:63]
ds_read_b128 v[148:151],v5 offset:23552
v_xor_b32 v8,v9,v8
v_mfma_f32_32x32x16_bf16 v[48:63],a[120:123],v[184:187],v[48:63]
buffer_store_dwordx4 v[84:87],v15,s[32:35],s68 offen sc1
ds_read_b128 v[152:155],v5 offset:32256
v_mfma_f32_32x32x16_bf16 v[48:63],a[124:127],v[188:191],v[48:63]
ds_read_b128 v[156:159],v5 offset:32768
s_waitcnt lgkmcnt(8)
v_mfma_f32_32x32x16_bf16 v[48:63],a[128:131],v[128:131],v[48:63]
buffer_load_dwordx4 v24,s[32:35],s63 offen offset:1024 sc1 lds
ds_read_b128 v[160:163],v5 offset:5760
v_mfma_f32_32x32x16_bf16 v[48:63],a[132:135],v[132:135],v[48:63]
ds_read_b128 v[164:167],v5 offset:6272
s_waitcnt lgkmcnt(5)
v_add_f32 v64,v64,v65
v_add_f32 v66,v66,v67
v_add_f32 v68,v68,v69
v_add_f32 v70,v70,v71
v_mfma_f32_32x32x16_bf16 v[48:63],a[136:139],v[136:139],v[48:63]
buffer_load_dwordx4 v24,s[32:35],s63 offen offset:2048 sc1 lds
ds_read_b128 v[168:171],v5 offset:14976
v_add_f32 v66,v64,v66
v_add_f32 v68,v68,v70
v_mfma_f32_32x32x16_bf16 v[48:63],a[140:143],v[140:143],v[48:63]
ds_read_b128 v[172:175],v5 offset:15488
v_add_f32_dpp v64,v66,v66 row_ror:8 row_mask:0xf bank_mask:0x3
s_waitcnt lgkmcnt(4)
v_mfma_f32_32x32x16_bf16 v[48:63],a[144:147],v[144:147],v[48:63]
buffer_load_dwordx2 v[12:13],v14,s[0:3],s66 offen sc1
ds_read_b128 v[176:179],v5 offset:24192
v_add_f32_dpp v64,v68,v68 row_ror:8 row_mask:0xf bank_mask:0xc
v_mfma_f32_32x32x16_bf16 v[48:63],a[148:151],v[148:151],v[48:63]
ds_read_b128 v[180:183],v5 offset:24704
v_add_f32_dpp v64,v64,v64 row_ror:12 row_mask:0xf bank_mask:0xf
v_mfma_f32_32x32x16_bf16 v[48:63],a[152:155],v[152:155],v[48:63]
buffer_load_dwordx4 v24,s[32:35],s63 offen offset:3072 sc1 lds
ds_read_b128 v[184:187],v5 offset:33408
v_add_f32_dpp v64,v64,v64 quad_perm:[2,3,0,1] row_mask:0xf bank_mask:0xf
v_mfma_f32_32x32x16_bf16 v[48:63],a[156:159],v[156:159],v[48:63]
ds_read_b128 v[188:191],v5 offset:33920
s_add_u32 m0,s64,41984
v_mul_f32 v64,0x3a800000,v64
s_waitcnt lgkmcnt(4)
v_mfma_f32_32x32x16_bf16 v[48:63],a[160:163],v[160:163],v[48:63]
buffer_load_dwordx4 v25,s[32:35],s63 offen offset:0 sc1 lds
ds_read_b128 v[128:131],v5 offset:6912
v_mul_f32 v65,v64,v64
v_mfma_f32_32x32x16_bf16 v[48:63],a[164:167],v[164:167],v[48:63]
ds_read_b128 v[132:135],v5 offset:7424
v_sub_f32_dpp v65,v64,v65 quad_perm:[1,1,3,3] row_mask:0xf bank_mask:0xf
v_mfma_f32_32x32x16_bf16 v[48:63],a[168:171],v[168:171],v[48:63]
ds_read_b128 v[136:139],v5 offset:16128
v_max_f32 v65,0,v65
v_mfma_f32_32x32x16_bf16 v[48:63],a[172:175],v[172:175],v[48:63]
ds_read_b128 v[140:143],v5 offset:16640
v_cvt_pk_bf16_f32 v65,0,v65
s_waitcnt lgkmcnt(4)
v_mfma_f32_32x32x16_bf16 v[48:63],a[176:179],v[176:179],v[48:63]
buffer_load_dwordx4 v25,s[32:35],s63 offen offset:1024 sc1 lds
ds_read_b128 v[144:147],v5 offset:25344
v_add_f32 v65,0x3727c5ac,v65
v_mfma_f32_32x32x16_bf16 v[48:63],a[180:183],v[180:183],v[48:63]
ds_read_b128 v[148:151],v5 offset:25856
v_cvt_pk_bf16_f32 v65,0,v65
v_mfma_f32_32x32x16_bf16 v[48:63],a[184:187],v[184:187],v[48:63]
buffer_load_dwordx4 v25,s[32:35],s63 offen offset:2048 sc1 lds
ds_read_b128 v[152:155],v5 offset:34560
v_sqrt_f32 v65,v65
v_mfma_f32_32x32x16_bf16 v[48:63],a[188:191],v[188:191],v[48:63]
ds_read_b128 v[156:159],v5 offset:35072
v_cvt_pk_bf16_f32 v65,v64,v65
s_waitcnt lgkmcnt(4)
v_mfma_f32_32x32x16_bf16 v[48:63],a[192:195],v[128:131],v[48:63]
ds_read_b128 v[160:163],v5 offset:8064
)#"
R"#(
ds_write_b32 v10,v65 offset:128
v_mfma_f32_32x32x16_bf16 v[48:63],a[196:199],v[132:135],v[48:63]
ds_read_b128 v[164:167],v5 offset:8576
v_mfma_f32_32x32x16_bf16 v[48:63],a[200:203],v[136:139],v[48:63]
buffer_load_dwordx4 v25,s[32:35],s63 offen offset:3072 sc1 lds
ds_read_b128 v[168:171],v5 offset:17280
s_add_u32 s63,s63,s30
v_mfma_f32_32x32x16_bf16 v[48:63],a[204:207],v[140:143],v[48:63]
ds_read_b128 v[172:175],v5 offset:17792
s_waitcnt lgkmcnt(5)
v_mfma_f32_32x32x16_bf16 v[48:63],a[208:211],v[144:147],v[48:63]
ds_read_b128 v[176:179],v5 offset:26496
v_mfma_f32_32x32x16_bf16 v[48:63],a[212:215],v[148:151],v[48:63]
ds_read_b128 v[180:183],v5 offset:27008
v_mfma_f32_32x32x16_bf16 v[48:63],a[216:219],v[152:155],v[48:63]
ds_read_b128 v[184:187],v5 offset:35712
s_waitcnt vmcnt(8)
s_waitcnt lgkmcnt(6)
s_barrier
v_mfma_f32_32x32x16_bf16 v[48:63],a[220:223],v[156:159],v[48:63]
ds_read_b128 v[188:191],v5 offset:36224
s_waitcnt lgkmcnt(4)
v_mfma_f32_32x32x16_bf16 v[48:63],a[224:227],v[160:163],v[48:63]
ds_read_b128 v[128:131],v4 offset:0
v_mfma_f32_32x32x16_bf16 v[48:63],a[228:231],v[164:167],v[48:63]
ds_read_b128 v[132:135],v4 offset:512
ds_read_b32 v74,v11 offset:128
v_mfma_f32_32x32x16_bf16 v[48:63],a[232:235],v[168:171],v[48:63]
ds_read_b128 v[136:139],v4 offset:9216
v_mfma_f32_32x32x16_bf16 v[48:63],a[236:239],v[172:175],v[48:63]
ds_read_b128 v[140:143],v4 offset:9728
s_waitcnt lgkmcnt(5)
v_mfma_f32_32x32x16_bf16 v[48:63],a[240:243],v[176:179],v[48:63]
ds_read_b128 v[144:147],v4 offset:18432
s_waitcnt lgkmcnt(3)
v_lshlrev_b32 v72,16,v74
v_and_b32 v74,0xffff0000,v74
v_mfma_f32_32x32x16_bf16 v[48:63],a[244:247],v[180:183],v[48:63]
ds_read_b128 v[148:151],v4 offset:18944
v_rcp_f32 v73,v74
v_mul_f32 v74,v74,v74
v_mfma_f32_32x32x16_bf16 v[48:63],a[248:251],v[184:187],v[48:63]
ds_read_b128 v[152:155],v4 offset:27648
v_mul_f32 v73,0xbfb8aa3b,v73
s_sub_u32 s62,s62,1
v_mfma_f32_32x32x16_bf16 v[48:63],a[252:255],v[188:191],v[48:63]
ds_read_b128 v[156:159],v4 offset:28160
s_cbranch_scc0 .L_hl_2
s_nop 1
s_add_u32 s68,s68,s30
s_nop 6
v_fma_f32 v48,-v72,v112,v48
v_fma_f32 v49,-v72,v113,v49
v_fma_f32 v50,-v72,v114,v50
v_fma_f32 v51,-v72,v115,v51
v_fma_f32 v52,-v72,v116,v52
v_fma_f32 v53,-v72,v117,v53
v_fma_f32 v54,-v72,v118,v54
v_fma_f32 v55,-v72,v119,v55
v_fma_f32 v56,-v72,v120,v56
v_fma_f32 v57,-v72,v121,v57
v_fma_f32 v58,-v72,v122,v58
v_fma_f32 v59,-v72,v123,v59
v_fma_f32 v60,-v72,v124,v60
v_fma_f32 v61,-v72,v125,v61
v_fma_f32 v62,-v72,v126,v62
v_fma_f32 v63,-v72,v127,v63
v_mul_f32 v76,v48,v73
v_mul_f32 v77,v49,v73
v_mul_f32 v78,v50,v73
v_mul_f32 v79,v51,v73
v_mul_f32 v80,v52,v73
v_mul_f32 v81,v53,v73
v_mul_f32 v82,v54,v73
v_mul_f32 v83,v55,v73
v_exp_f32 v76,v76
v_exp_f32 v77,v77
v_mul_f32 v48,v48,v56
v_mul_f32 v49,v49,v57
v_exp_f32 v78,v78
v_exp_f32 v79,v79
v_mul_f32 v50,v50,v58
v_mul_f32 v51,v51,v59
v_exp_f32 v80,v80
v_exp_f32 v81,v81
v_mul_f32 v52,v52,v60
v_mul_f32 v53,v53,v61
v_exp_f32 v82,v82
v_exp_f32 v83,v83
v_mul_f32 v54,v54,v62
v_mul_f32 v55,v55,v63
v_fma_f32 v76,v76,v74,v74
v_fma_f32 v77,v77,v74,v74
v_rcp_f32 v76,v76
v_rcp_f32 v77,v77
v_fma_f32 v78,v78,v74,v74
v_fma_f32 v79,v79,v74,v74
v_rcp_f32 v78,v78
v_rcp_f32 v79,v79
v_fma_f32 v80,v80,v74,v74
v_fma_f32 v81,v81,v74,v74
v_rcp_f32 v80,v80
v_rcp_f32 v81,v81
v_fma_f32 v82,v82,v74,v74
v_fma_f32 v83,v83,v74,v74
v_rcp_f32 v82,v82
v_rcp_f32 v83,v83
v_mul_f32 v48,v48,v76
v_mul_f32 v49,v49,v77
v_mul_f32 v50,v50,v78
v_mul_f32 v51,v51,v79
v_mul_f32 v52,v52,v80
v_mul_f32 v53,v53,v81
v_mul_f32 v54,v54,v82
v_mul_f32 v55,v55,v83
v_cvt_pk_bf16_f32 v84,v48,v49
v_cvt_pk_bf16_f32 v85,v50,v51
v_cvt_pk_bf16_f32 v86,v52,v53
v_cvt_pk_bf16_f32 v87,v54,v55
buffer_store_dwordx4 v[84:87],v15,s[32:35],s68 offen sc1
s_waitcnt vmcnt(0)
s_barrier
buffer_store_dwordx2 v[16:17],v18,s[32:35],s68 offen sc1
s_cbranch_execz .L_hbk
s_branch .L_end
.p2align 6
.L_hv3:
.L_hl_3:
s_waitcnt lgkmcnt(4)
v_mfma_f32_32x32x16_bf16 v[32:47],a[0:3],v[128:131],0
ds_read_b128 v[160:163],v4 offset:1152
s_waitcnt vmcnt(5)
v_cmp_ne_u64 vcc,v[16:17],v[12:13]
s_add_u32 s68,s68,s30
v_mfma_f32_32x32x16_bf16 v[32:47],a[4:7],v[132:135],v[32:47]
ds_read_b128 v[164:167],v4 offset:1664
s_cbranch_vccnz .L_rth0_3
.L_bkh0_3:
s_add_u32 s66,s66,s30
v_mfma_f32_32x32x16_bf16 v[32:47],a[8:11],v[136:139],v[32:47]
ds_read_b128 v[168:171],v4 offset:10368
s_add_u32 m0,s64,73728
v_fma_f32 v48,-v72,v112,v48
buffer_load_dwordx4 v20,s[32:35],s63 offen offset:0 sc1 lds
v_mfma_f32_32x32x16_bf16 v[32:47],a[12:15],v[140:143],v[32:47]
ds_read_b128 v[172:175],v4 offset:10880
v_fma_f32 v49,-v72,v113,v49
v_fma_f32 v50,-v72,v114,v50
v_fma_f32 v51,-v72,v115,v51
v_fma_f32 v52,-v72,v116,v52
s_waitcnt lgkmcnt(4)
v_mfma_f32_32x32x16_bf16 v[32:47],a[16:19],v[144:147],v[32:47]
buffer_load_dwordx4 v20,s[32:35],s63 offen offset:1024 sc1 lds
ds_read_b128 v[176:179],v4 offset:19584
v_fma_f32 v53,-v72,v117,v53
v_fma_f32 v54,-v72,v118,v54
v_mfma_f32_32x32x16_bf16 v[32:47],a[20:23],v[148:151],v[32:47]
ds_read_b128 v[180:183],v4 offset:20096
v_fma_f32 v55,-v72,v119,v55
v_fma_f32 v56,-v72,v120,v56
v_fma_f32 v57,-v72,v121,v57
v_fma_f32 v58,-v72,v122,v58
v_fma_f32 v59,-v72,v123,v59
v_mfma_f32_32x32x16_bf16 v[32:47],a[24:27],v[152:155],v[32:47]
buffer_load_dwordx4 v20,s[32:35],s63 offen offset:2048 sc1 lds
ds_read_b128 v[184:187],v4 offset:28800
v_fma_f32 v60,-v72,v124,v60
v_fma_f32 v61,-v72,v125,v61
v_mfma_f32_32x32x16_bf16 v[32:47],a[28:31],v[156:159],v[32:47]
ds_read_b128 v[188:191],v4 offset:29312
v_fma_f32 v62,-v72,v126,v62
v_fma_f32 v63,-v72,v127,v63
v_mul_f32 v76,v48,v73
v_mul_f32 v77,v49,v73
s_waitcnt lgkmcnt(4)
v_mfma_f32_32x32x16_bf16 v[32:47],a[32:35],v[160:163],v[32:47]
buffer_load_dwordx4 v20,s[32:35],s63 offen offset:3072 sc1 lds
ds_read_b128 v[128:131],v4 offset:2304
v_exp_f32 v76,v76
v_mfma_f32_32x32x16_bf16 v[32:47],a[36:39],v[164:167],v[32:47]
ds_read_b128 v[132:135],v4 offset:2816
v_exp_f32 v77,v77
v_mul_f32 v78,v50,v73
v_mul_f32 v79,v51,v73
v_mul_f32 v80,v52,v73
v_mfma_f32_32x32x16_bf16 v[32:47],a[40:43],v[168:171],v[32:47]
ds_read_b128 v[136:139],v4 offset:11520
s_add_u32 m0,s64,110592
v_exp_f32 v78,v78
v_exp_f32 v79,v79
v_mfma_f32_32x32x16_bf16 v[32:47],a[44:47],v[172:175],v[32:47]
ds_read_b128 v[140:143],v4 offset:12032
v_exp_f32 v80,v80
v_mul_f32 v81,v53,v73
v_mul_f32 v82,v54,v73
s_waitcnt lgkmcnt(4)
v_mfma_f32_32x32x16_bf16 v[32:47],a[48:51],v[176:179],v[32:47]
buffer_load_dwordx4 v21,s[32:35],s63 offen offset:0 sc1 lds
ds_read_b128 v[144:147],v4 offset:20736
v_exp_f32 v81,v81
v_mfma_f32_32x32x16_bf16 v[32:47],a[52:55],v[180:183],v[32:47]
ds_read_b128 v[148:151],v4 offset:21248
v_exp_f32 v82,v82
v_mul_f32 v83,v55,v73
v_mul_f32 v48,v48,v56
v_mul_f32 v49,v49,v57
v_mfma_f32_32x32x16_bf16 v[32:47],a[56:59],v[184:187],v[32:47]
buffer_load_dwordx4 v21,s[32:35],s63 offen offset:1024 sc1 lds
ds_read_b128 v[152:155],v4 offset:29952
v_exp_f32 v83,v83
v_mfma_f32_32x32x16_bf16 v[32:47],a[60:63],v[188:191],v[32:47]
ds_read_b128 v[156:159],v4 offset:30464
v_mul_f32 v50,v50,v58
v_mul_f32 v51,v51,v59
v_mul_f32 v52,v52,v60
v_mul_f32 v53,v53,v61
s_waitcnt lgkmcnt(4)
v_mfma_f32_32x32x16_bf16 v[32:47],a[64:67],v[128:131],v[32:47]
buffer_load_dwordx4 v21,s[32:35],s63 offen offset:2048 sc1 lds
ds_read_b128 v[160:163],v4 offset:3456
v_mul_f32 v54,v54,v62
v_mul_f32 v55,v55,v63
v_mfma_f32_32x32x16_bf16 v[32:47],a[68:71],v[132:135],v[32:47]
ds_read_b128 v[164:167],v4 offset:3968
v_fma_f32 v76,v76,v74,v74
v_fma_f32 v77,v77,v74,v74
v_rcp_f32 v76,v76
v_fma_f32 v78,v78,v74,v74
v_mfma_f32_32x32x16_bf16 v[32:47],a[72:75],v[136:139],v[32:47]
buffer_load_dwordx4 v21,s[32:35],s63 offen offset:3072 sc1 lds
ds_read_b128 v[168:171],v4 offset:12672
s_add_u32 m0,s64,77824
v_fma_f32 v79,v79,v74,v74
v_mfma_f32_32x32x16_bf16 v[32:47],a[76:79],v[140:143],v[32:47]
ds_read_b128 v[172:175],v4 offset:13184
v_rcp_f32 v77,v77
v_rcp_f32 v78,v78
s_waitcnt lgkmcnt(4)
v_mfma_f32_32x32x16_bf16 v[32:47],a[80:83],v[144:147],v[32:47]
buffer_load_dwordx4 v22,s[32:35],s63 offen offset:0 sc1 lds
ds_read_b128 v[176:179],v4 offset:21888
s_add_u32 m0,s64,114688
v_fma_f32 v80,v80,v74,v74
v_mfma_f32_32x32x16_bf16 v[32:47],a[84:87],v[148:151],v[32:47]
ds_read_b128 v[180:183],v4 offset:22400
v_rcp_f32 v79,v79
v_rcp_f32 v80,v80
v_fma_f32 v81,v81,v74,v74
s_waitcnt vmcnt(9)
s_barrier
v_mfma_f32_32x32x16_bf16 v[32:47],a[88:91],v[152:155],v[32:47]
buffer_load_dwordx4 v23,s[32:35],s63 offen offset:0 sc1 lds
ds_read_b128 v[184:187],v4 offset:31104
v_rcp_f32 v81,v81
v_mfma_f32_32x32x16_bf16 v[32:47],a[92:95],v[156:159],v[32:47]
ds_read_b128 v[188:191],v4 offset:31616
v_fma_f32 v82,v82,v74,v74
v_fma_f32 v83,v83,v74,v74
v_rcp_f32 v82,v82
s_waitcnt lgkmcnt(4)
v_mfma_f32_32x32x16_bf16 v[32:47],a[96:99],v[160:163],v[32:47]
ds_read_b128 v[128:131],v4 offset:4608
v_rcp_f32 v83,v83
v_mul_f32 v48,v48,v76
v_mfma_f32_32x32x16_bf16 v[32:47],a[100:103],v[164:167],v[32:47]
ds_read_b128 v[132:135],v4 offset:5120
v_mul_f32 v49,v49,v77
v_mul_f32 v50,v50,v78
v_mul_f32 v51,v51,v79
v_mul_f32 v52,v52,v80
v_mul_f32 v53,v53,v81
v_mfma_f32_32x32x16_bf16 v[32:47],a[104:107],v[168:171],v[32:47]
ds_read_b128 v[136:139],v4 offset:13824
s_add_u32 m0,s64,78848
v_mul_f32 v54,v54,v82
buffer_load_dwordx4 v24,s[32:35],s63 offen offset:0 sc1 lds
v_mfma_f32_32x32x16_bf16 v[32:47],a[108:111],v[172:175],v[32:47]
ds_read_b128 v[140:143],v4 offset:14336
v_mul_f32 v55,v55,v83
v_cvt_pk_bf16_f32 v84,v48,v49
v_cvt_pk_bf16_f32 v85,v50,v51
v_cvt_pk_bf16_f32 v86,v52,v53
s_waitcnt lgkmcnt(4)
v_mfma_f32_32x32x16_bf16 v[32:47],a[112:115],v[176:179],v[32:47]
ds_read_b128 v[144:147],v4 offset:23040
v_cvt_pk_bf16_f32 v87,v54,v55
ds_read2st64_b32 v[64:65],v8 offset0:0 offset1:36
ds_read2st64_b32 v[66:67],v8 offset0:72 offset1:108
ds_read2st64_b32 v[68:69],v8 offset0:144 offset1:180
ds_read2st64_b32 v[70:71],v8 offset0:216 offset1:252
v_mfma_f32_32x32x16_bf16 v[32:47],a[116:119],v[180:183],v[32:47]
ds_read_b128 v[148:151],v4 offset:23552
v_xor_b32 v8,v9,v8
v_mfma_f32_32x32x16_bf16 v[32:47],a[120:123],v[184:187],v[32:47]
buffer_store_dwordx4 v[84:87],v15,s[32:35],s68 offen sc1
ds_read_b128 v[152:155],v4 offset:32256
v_mfma_f32_32x32x16_bf16 v[32:47],a[124:127],v[188:191],v[32:47]
ds_read_b128 v[156:159],v4 offset:32768
s_waitcnt lgkmcnt(8)
v_mfma_f32_32x32x16_bf16 v[32:47],a[128:131],v[128:131],v[32:47]
buffer_load_dwordx4 v24,s[32:35],s63 offen offset:1024 sc1 lds
ds_read_b128 v[160:163],v4 offset:5760
v_mfma_f32_32x32x16_bf16 v[32:47],a[132:135],v[132:135],v[32:47]
ds_read_b128 v[164:167],v4 offset:6272
s_waitcnt lgkmcnt(5)
v_add_f32 v64,v64,v65
v_add_f32 v66,v66,v67
v_add_f32 v68,v68,v69
v_add_f32 v70,v70,v71
v_mfma_f32_32x32x16_bf16 v[32:47],a[136:139],v[136:139],v[32:47]
buffer_load_dwordx4 v24,s[32:35],s63 offen offset:2048 sc1 lds
ds_read_b128 v[168:171],v4 offset:14976
v_add_f32 v66,v64,v66
v_add_f32 v68,v68,v70
v_mfma_f32_32x32x16_bf16 v[32:47],a[140:143],v[140:143],v[32:47]
ds_read_b128 v[172:175],v4 offset:15488
v_add_f32_dpp v64,v66,v66 row_ror:8 row_mask:0xf bank_mask:0x3
s_waitcnt lgkmcnt(4)
v_mfma_f32_32x32x16_bf16 v[32:47],a[144:147],v[144:147],v[32:47]
buffer_load_dwordx2 v[12:13],v14,s[0:3],s66 offen sc1
ds_read_b128 v[176:179],v4 offset:24192
v_add_f32_dpp v64,v68,v68 row_ror:8 row_mask:0xf bank_mask:0xc
v_mfma_f32_32x32x16_bf16 v[32:47],a[148:151],v[148:151],v[32:47]
ds_read_b128 v[180:183],v4 offset:24704
v_add_f32_dpp v64,v64,v64 row_ror:12 row_mask:0xf bank_mask:0xf
v_mfma_f32_32x32x16_bf16 v[32:47],a[152:155],v[152:155],v[32:47]
buffer_load_dwordx4 v24,s[32:35],s63 offen offset:3072 sc1 lds
ds_read_b128 v[184:187],v4 offset:33408
v_add_f32_dpp v64,v64,v64 quad_perm:[2,3,0,1] row_mask:0xf bank_mask:0xf
v_mfma_f32_32x32x16_bf16 v[32:47],a[156:159],v[156:159],v[32:47]
ds_read_b128 v[188:191],v4 offset:33920
s_add_u32 m0,s64,115712
v_mul_f32 v64,0x3a800000,v64
s_waitcnt lgkmcnt(4)
v_mfma_f32_32x32x16_bf16 v[32:47],a[160:163],v[160:163],v[32:47]
buffer_load_dwordx4 v25,s[32:35],s63 offen offset:0 sc1 lds
ds_read_b128 v[128:131],v4 offset:6912
v_mul_f32 v65,v64,v64
v_mfma_f32_32x32x16_bf16 v[32:47],a[164:167],v[164:167],v[32:47]
ds_read_b128 v[132:135],v4 offset:7424
v_sub_f32_dpp v65,v64,v65 quad_perm:[1,1,3,3] row_mask:0xf bank_mask:0xf
v_mfma_f32_32x32x16_bf16 v[32:47],a[168:171],v[168:171],v[32:47]
ds_read_b128 v[136:139],v4 offset:16128
v_max_f32 v65,0,v65
v_mfma_f32_32x32x16_bf16 v[32:47],a[172:175],v[172:175],v[32:47]
ds_read_b128 v[140:143],v4 offset:16640
v_cvt_pk_bf16_f32 v65,0,v65
s_waitcnt lgkmcnt(4)
v_mfma_f32_32x32x16_bf16 v[32:47],a[176:179],v[176:179],v[32:47]
buffer_load_dwordx4 v25,s[32:35],s63 offen offset:1024 sc1 lds
ds_read_b128 v[144:147],v4 offset:25344
v_add_f32 v65,0x3727c5ac,v65
v_mfma_f32_32x32x16_bf16 v[32:47],a[180:183],v[180:183],v[32:47]
ds_read_b128 v[148:151],v4 offset:25856
v_cvt_pk_bf16_f32 v65,0,v65
v_mfma_f32_32x32x16_bf16 v[32:47],a[184:187],v[184:187],v[32:47]
buffer_load_dwordx4 v25,s[32:35],s63 offen offset:2048 sc1 lds
ds_read_b128 v[152:155],v4 offset:34560
v_sqrt_f32 v65,v65
v_mfma_f32_32x32x16_bf16 v[32:47],a[188:191],v[188:191],v[32:47]
ds_read_b128 v[156:159],v4 offset:35072
v_cvt_pk_bf16_f32 v65,v64,v65
s_waitcnt lgkmcnt(4)
v_mfma_f32_32x32x16_bf16 v[32:47],a[192:195],v[128:131],v[32:47]
ds_read_b128 v[160:163],v4 offset:8064
ds_write_b32 v10,v65 offset:0
v_mfma_f32_32x32x16_bf16 v[32:47],a[196:199],v[132:135],v[32:47]
ds_read_b128 v[164:167],v4 offset:8576
v_mfma_f32_32x32x16_bf16 v[32:47],a[200:203],v[136:139],v[32:47]
buffer_load_dwordx4 v25,s[32:35],s63 offen offset:3072 sc1 lds
ds_read_b128 v[168:171],v4 offset:17280
s_add_u32 s63,s63,s30
v_mfma_f32_32x32x16_bf16 v[32:47],a[204:207],v[140:143],v[32:47]
ds_read_b128 v[172:175],v4 offset:17792
s_waitcnt lgkmcnt(5)
v_mfma_f32_32x32x16_bf16 v[32:47],a[208:211],v[144:147],v[32:47]
ds_read_b128 v[176:179],v4 offset:26496
v_mfma_f32_32x32x16_bf16 v[32:47],a[212:215],v[148:151],v[32:47]
ds_read_b128 v[180:183],v4 offset:27008
s_waitcnt vmcnt(8)
s_waitcnt lgkmcnt(5)
s_barrier
v_mfma_f32_32x32x16_bf16 v[32:47],a[216:219],v[152:155],v[32:47]
ds_read_b128 v[184:187],v4 offset:35712
v_mfma_f32_32x32x16_bf16 v[32:47],a[220:223],v[156:159],v[32:47]
ds_read_b128 v[188:191],v4 offset:36224
s_waitcnt lgkmcnt(4)
v_mfma_f32_32x32x16_bf16 v[32:47],a[224:227],v[160:163],v[32:47]
ds_read_b128 v[128:131],v5 offset:0
v_mfma_f32_32x32x16_bf16 v[32:47],a[228:231],v[164:167],v[32:47]
ds_read_b128 v[132:135],v5 offset:512
ds_read_b32 v74,v11 offset:0
v_mfma_f32_32x32x16_bf16 v[32:47],a[232:235],v[168:171],v[32:47]
ds_read_b128 v[136:139],v5 offset:9216
v_mfma_f32_32x32x16_bf16 v[32:47],a[236:239],v[172:175],v[32:47]
ds_read_b128 v[140:143],v5 offset:9728
s_waitcnt lgkmcnt(5)
v_mfma_f32_32x32x16_bf16 v[32:47],a[240:243],v[176:179],v[32:47]
ds_read_b128 v[144:147],v5 offset:18432
s_waitcnt lgkmcnt(3)
v_lshlrev_b32 v72,16,v74
v_and_b32 v74,0xffff0000,v74
v_mfma_f32_32x32x16_bf16 v[32:47],a[244:247],v[180:183],v[32:47]
ds_read_b128 v[148:151],v5 offset:18944
v_rcp_f32 v73,v74
v_mul_f32 v74,v74,v74
v_mfma_f32_32x32x16_bf16 v[32:47],a[248:251],v[184:187],v[32:47]
ds_read_b128 v[152:155],v5 offset:27648
v_mul_f32 v73,0xbfb8aa3b,v73
v_mfma_f32_32x32x16_bf16 v[32:47],a[252:255],v[188:191],v[32:47]
ds_read_b128 v[156:159],v5 offset:28160
s_waitcnt lgkmcnt(4)
v_mfma_f32_32x32x16_bf16 v[48:63],a[0:3],v[128:131],0
ds_read_b128 v[160:163],v5 offset:1152
s_waitcnt vmcnt(5)
v_cmp_ne_u64 vcc,v[16:17],v[12:13]
s_add_u32 s68,s68,s30
v_mfma_f32_32x32x16_bf16 v[48:63],a[4:7],v[132:135],v[48:63]
ds_read_b128 v[164:167],v5 offset:1664
s_cbranch_vccnz .L_rth1_3
.L_bkh1_3:
s_add_u32 s66,s66,s30
v_mfma_f32_32x32x16_bf16 v[48:63],a[8:11],v[136:139],v[48:63]
ds_read_b128 v[168:171],v5 offset:10368
s_add_u32 m0,s64,0
v_fma_f32 v32,-v72,v112,v32
buffer_load_dwordx4 v20,s[32:35],s63 offen offset:0 sc1 lds
v_mfma_f32_32x32x16_bf16 v[48:63],a[12:15],v[140:143],v[48:63]
ds_read_b128 v[172:175],v5 offset:10880
v_fma_f32 v33,-v72,v113,v33
v_fma_f32 v34,-v72,v114,v34
v_fma_f32 v35,-v72,v115,v35
v_fma_f32 v36,-v72,v116,v36
s_waitcnt lgkmcnt(4)
v_mfma_f32_32x32x16_bf16 v[48:63],a[16:19],v[144:147],v[48:63]
buffer_load_dwordx4 v20,s[32:35],s63 offen offset:1024 sc1 lds
ds_read_b128 v[176:179],v5 offset:19584
v_fma_f32 v37,-v72,v117,v37
v_fma_f32 v38,-v72,v118,v38
v_mfma_f32_32x32x16_bf16 v[48:63],a[20:23],v[148:151],v[48:63]
ds_read_b128 v[180:183],v5 offset:20096
v_fma_f32 v39,-v72,v119,v39
v_fma_f32 v40,-v72,v120,v40
v_fma_f32 v41,-v72,v121,v41
v_fma_f32 v42,-v72,v122,v42
v_fma_f32 v43,-v72,v123,v43
v_mfma_f32_32x32x16_bf16 v[48:63],a[24:27],v[152:155],v[48:63]
buffer_load_dwordx4 v20,s[32:35],s63 offen offset:2048 sc1 lds
ds_read_b128 v[184:187],v5 offset:28800
v_fma_f32 v44,-v72,v124,v44
v_fma_f32 v45,-v72,v125,v45
v_mfma_f32_32x32x16_bf16 v[48:63],a[28:31],v[156:159],v[48:63]
ds_read_b128 v[188:191],v5 offset:29312
v_fma_f32 v46,-v72,v126,v46
v_fma_f32 v47,-v72,v127,v47
v_mul_f32 v76,v32,v73
v_mul_f32 v77,v33,v73
s_waitcnt lgkmcnt(4)
v_mfma_f32_32x32x16_bf16 v[48:63],a[32:35],v[160:163],v[48:63]
buffer_load_dwordx4 v20,s[32:35],s63 offen offset:3072 sc1 lds
ds_read_b128 v[128:131],v5 offset:2304
v_exp_f32 v76,v76
v_mfma_f32_32x32x16_bf16 v[48:63],a[36:39],v[164:167],v[48:63]
ds_read_b128 v[132:135],v5 offset:2816
v_exp_f32 v77,v77
v_mul_f32 v78,v34,v73
v_mul_f32 v79,v35,v73
v_mul_f32 v80,v36,v73
v_mfma_f32_32x32x16_bf16 v[48:63],a[40:43],v[168:171],v[48:63]
ds_read_b128 v[136:139],v5 offset:11520
s_add_u32 m0,s64,36864
v_exp_f32 v78,v78
v_exp_f32 v79,v79
v_mfma_f32_32x32x16_bf16 v[48:63],a[44:47],v[172:175],v[48:63]
ds_read_b128 v[140:143],v5 offset:12032
v_exp_f32 v80,v80
v_mul_f32 v81,v37,v73
v_mul_f32 v82,v38,v73
s_waitcnt lgkmcnt(4)
v_mfma_f32_32x32x16_bf16 v[48:63],a[48:51],v[176:179],v[48:63]
buffer_load_dwordx4 v21,s[32:35],s63 offen offset:0 sc1 lds
ds_read_b128 v[144:147],v5 offset:20736
v_exp_f32 v81,v81
v_mfma_f32_32x32x16_bf16 v[48:63],a[52:55],v[180:183],v[48:63]
ds_read_b128 v[148:151],v5 offset:21248
v_exp_f32 v82,v82
v_mul_f32 v83,v39,v73
v_mul_f32 v32,v32,v40
v_mul_f32 v33,v33,v41
v_mfma_f32_32x32x16_bf16 v[48:63],a[56:59],v[184:187],v[48:63]
buffer_load_dwordx4 v21,s[32:35],s63 offen offset:1024 sc1 lds
ds_read_b128 v[152:155],v5 offset:29952
v_exp_f32 v83,v83
v_mfma_f32_32x32x16_bf16 v[48:63],a[60:63],v[188:191],v[48:63]
ds_read_b128 v[156:159],v5 offset:30464
v_mul_f32 v34,v34,v42
v_mul_f32 v35,v35,v43
v_mul_f32 v36,v36,v44
v_mul_f32 v37,v37,v45
s_waitcnt lgkmcnt(4)
)#"
R"#(
v_mfma_f32_32x32x16_bf16 v[48:63],a[64:67],v[128:131],v[48:63]
buffer_load_dwordx4 v21,s[32:35],s63 offen offset:2048 sc1 lds
ds_read_b128 v[160:163],v5 offset:3456
v_mul_f32 v38,v38,v46
v_mul_f32 v39,v39,v47
v_mfma_f32_32x32x16_bf16 v[48:63],a[68:71],v[132:135],v[48:63]
ds_read_b128 v[164:167],v5 offset:3968
v_fma_f32 v76,v76,v74,v74
v_fma_f32 v77,v77,v74,v74
v_rcp_f32 v76,v76
v_fma_f32 v78,v78,v74,v74
v_mfma_f32_32x32x16_bf16 v[48:63],a[72:75],v[136:139],v[48:63]
buffer_load_dwordx4 v21,s[32:35],s63 offen offset:3072 sc1 lds
ds_read_b128 v[168:171],v5 offset:12672
s_add_u32 m0,s64,4096
v_fma_f32 v79,v79,v74,v74
v_mfma_f32_32x32x16_bf16 v[48:63],a[76:79],v[140:143],v[48:63]
ds_read_b128 v[172:175],v5 offset:13184
v_rcp_f32 v77,v77
v_rcp_f32 v78,v78
s_waitcnt lgkmcnt(4)
v_mfma_f32_32x32x16_bf16 v[48:63],a[80:83],v[144:147],v[48:63]
buffer_load_dwordx4 v22,s[32:35],s63 offen offset:0 sc1 lds
ds_read_b128 v[176:179],v5 offset:21888
s_add_u32 m0,s64,40960
v_fma_f32 v80,v80,v74,v74
v_mfma_f32_32x32x16_bf16 v[48:63],a[84:87],v[148:151],v[48:63]
ds_read_b128 v[180:183],v5 offset:22400
v_rcp_f32 v79,v79
v_rcp_f32 v80,v80
v_fma_f32 v81,v81,v74,v74
s_waitcnt vmcnt(9)
s_barrier
v_mfma_f32_32x32x16_bf16 v[48:63],a[88:91],v[152:155],v[48:63]
buffer_load_dwordx4 v23,s[32:35],s63 offen offset:0 sc1 lds
ds_read_b128 v[184:187],v5 offset:31104
v_rcp_f32 v81,v81
v_mfma_f32_32x32x16_bf16 v[48:63],a[92:95],v[156:159],v[48:63]
ds_read_b128 v[188:191],v5 offset:31616
v_fma_f32 v82,v82,v74,v74
v_fma_f32 v83,v83,v74,v74
v_rcp_f32 v82,v82
s_waitcnt lgkmcnt(4)
v_mfma_f32_32x32x16_bf16 v[48:63],a[96:99],v[160:163],v[48:63]
ds_read_b128 v[128:131],v5 offset:4608
v_rcp_f32 v83,v83
v_mul_f32 v32,v32,v76
v_mfma_f32_32x32x16_bf16 v[48:63],a[100:103],v[164:167],v[48:63]
ds_read_b128 v[132:135],v5 offset:5120
v_mul_f32 v33,v33,v77
v_mul_f32 v34,v34,v78
v_mul_f32 v35,v35,v79
v_mul_f32 v36,v36,v80
v_mul_f32 v37,v37,v81
v_mfma_f32_32x32x16_bf16 v[48:63],a[104:107],v[168:171],v[48:63]
ds_read_b128 v[136:139],v5 offset:13824
s_add_u32 m0,s64,5120
v_mul_f32 v38,v38,v82
buffer_load_dwordx4 v24,s[32:35],s63 offen offset:0 sc1 lds
v_mfma_f32_32x32x16_bf16 v[48:63],a[108:111],v[172:175],v[48:63]
ds_read_b128 v[140:143],v5 offset:14336
v_mul_f32 v39,v39,v83
v_cvt_pk_bf16_f32 v84,v32,v33
v_cvt_pk_bf16_f32 v85,v34,v35
v_cvt_pk_bf16_f32 v86,v36,v37
s_waitcnt lgkmcnt(4)
v_mfma_f32_32x32x16_bf16 v[48:63],a[112:115],v[176:179],v[48:63]
ds_read_b128 v[144:147],v5 offset:23040
v_cvt_pk_bf16_f32 v87,v38,v39
ds_read2st64_b32 v[64:65],v8 offset0:0 offset1:36
ds_read2st64_b32 v[66:67],v8 offset0:72 offset1:108
ds_read2st64_b32 v[68:69],v8 offset0:144 offset1:180
ds_read2st64_b32 v[70:71],v8 offset0:216 offset1:252
v_mfma_f32_32x32x16_bf16 v[48:63],a[116:119],v[180:183],v[48:63]
ds_read_b128 v[148:151],v5 offset:23552
v_xor_b32 v8,v9,v8
v_mfma_f32_32x32x16_bf16 v[48:63],a[120:123],v[184:187],v[48:63]
buffer_store_dwordx4 v[84:87],v15,s[32:35],s68 offen sc1
ds_read_b128 v[152:155],v5 offset:32256
v_mfma_f32_32x32x16_bf16 v[48:63],a[124:127],v[188:191],v[48:63]
ds_read_b128 v[156:159],v5 offset:32768
s_waitcnt lgkmcnt(8)
v_mfma_f32_32x32x16_bf16 v[48:63],a[128:131],v[128:131],v[48:63]
buffer_load_dwordx4 v24,s[32:35],s63 offen offset:1024 sc1 lds
ds_read_b128 v[160:163],v5 offset:5760
v_mfma_f32_32x32x16_bf16 v[48:63],a[132:135],v[132:135],v[48:63]
ds_read_b128 v[164:167],v5 offset:6272
s_waitcnt lgkmcnt(5)
v_add_f32 v64,v64,v65
v_add_f32 v66,v66,v67
v_add_f32 v68,v68,v69
v_add_f32 v70,v70,v71
v_mfma_f32_32x32x16_bf16 v[48:63],a[136:139],v[136:139],v[48:63]
buffer_load_dwordx4 v24,s[32:35],s63 offen offset:2048 sc1 lds
ds_read_b128 v[168:171],v5 offset:14976
v_add_f32 v66,v64,v66
v_add_f32 v68,v68,v70
v_mfma_f32_32x32x16_bf16 v[48:63],a[140:143],v[140:143],v[48:63]
ds_read_b128 v[172:175],v5 offset:15488
v_add_f32_dpp v64,v66,v66 row_ror:8 row_mask:0xf bank_mask:0x3
s_waitcnt lgkmcnt(4)
v_mfma_f32_32x32x16_bf16 v[48:63],a[144:147],v[144:147],v[48:63]
buffer_load_dwordx2 v[12:13],v14,s[0:3],s66 offen sc1
ds_read_b128 v[176:179],v5 offset:24192
v_add_f32_dpp v64,v68,v68 row_ror:8 row_mask:0xf bank_mask:0xc
v_mfma_f32_32x32x16_bf16 v[48:63],a[148:151],v[148:151],v[48:63]
ds_read_b128 v[180:183],v5 offset:24704
v_add_f32_dpp v64,v64,v64 row_ror:12 row_mask:0xf bank_mask:0xf
v_mfma_f32_32x32x16_bf16 v[48:63],a[152:155],v[152:155],v[48:63]
buffer_load_dwordx4 v24,s[32:35],s63 offen offset:3072 sc1 lds
ds_read_b128 v[184:187],v5 offset:33408
v_add_f32_dpp v64,v64,v64 quad_perm:[2,3,0,1] row_mask:0xf bank_mask:0xf
v_mfma_f32_32x32x16_bf16 v[48:63],a[156:159],v[156:159],v[48:63]
ds_read_b128 v[188:191],v5 offset:33920
s_add_u32 m0,s64,41984
v_mul_f32 v64,0x3a800000,v64
s_waitcnt lgkmcnt(4)
v_mfma_f32_32x32x16_bf16 v[48:63],a[160:163],v[160:163],v[48:63]
buffer_load_dwordx4 v25,s[32:35],s63 offen offset:0 sc1 lds
ds_read_b128 v[128:131],v5 offset:6912
v_mul_f32 v65,v64,v64
v_mfma_f32_32x32x16_bf16 v[48:63],a[164:167],v[164:167],v[48:63]
ds_read_b128 v[132:135],v5 offset:7424
v_sub_f32_dpp v65,v64,v65 quad_perm:[1,1,3,3] row_mask:0xf bank_mask:0xf
v_mfma_f32_32x32x16_bf16 v[48:63],a[168:171],v[168:171],v[48:63]
ds_read_b128 v[136:139],v5 offset:16128
v_max_f32 v65,0,v65
v_mfma_f32_32x32x16_bf16 v[48:63],a[172:175],v[172:175],v[48:63]
ds_read_b128 v[140:143],v5 offset:16640
v_cvt_pk_bf16_f32 v65,0,v65
s_waitcnt lgkmcnt(4)
v_mfma_f32_32x32x16_bf16 v[48:63],a[176:179],v[176:179],v[48:63]
buffer_load_dwordx4 v25,s[32:35],s63 offen offset:1024 sc1 lds
ds_read_b128 v[144:147],v5 offset:25344
v_add_f32 v65,0x3727c5ac,v65
v_mfma_f32_32x32x16_bf16 v[48:63],a[180:183],v[180:183],v[48:63]
ds_read_b128 v[148:151],v5 offset:25856
v_cvt_pk_bf16_f32 v65,0,v65
v_mfma_f32_32x32x16_bf16 v[48:63],a[184:187],v[184:187],v[48:63]
buffer_load_dwordx4 v25,s[32:35],s63 offen offset:2048 sc1 lds
ds_read_b128 v[152:155],v5 offset:34560
v_sqrt_f32 v65,v65
v_mfma_f32_32x32x16_bf16 v[48:63],a[188:191],v[188:191],v[48:63]
ds_read_b128 v[156:159],v5 offset:35072
v_cvt_pk_bf16_f32 v65,v64,v65
s_waitcnt lgkmcnt(4)
v_mfma_f32_32x32x16_bf16 v[48:63],a[192:195],v[128:131],v[48:63]
ds_read_b128 v[160:163],v5 offset:8064
ds_write_b32 v10,v65 offset:128
v_mfma_f32_32x32x16_bf16 v[48:63],a[196:199],v[132:135],v[48:63]
ds_read_b128 v[164:167],v5 offset:8576
v_mfma_f32_32x32x16_bf16 v[48:63],a[200:203],v[136:139],v[48:63]
buffer_load_dwordx4 v25,s[32:35],s63 offen offset:3072 sc1 lds
ds_read_b128 v[168:171],v5 offset:17280
s_add_u32 s63,s63,s30
v_mfma_f32_32x32x16_bf16 v[48:63],a[204:207],v[140:143],v[48:63]
ds_read_b128 v[172:175],v5 offset:17792
s_waitcnt lgkmcnt(5)
v_mfma_f32_32x32x16_bf16 v[48:63],a[208:211],v[144:147],v[48:63]
ds_read_b128 v[176:179],v5 offset:26496
v_mfma_f32_32x32x16_bf16 v[48:63],a[212:215],v[148:151],v[48:63]
ds_read_b128 v[180:183],v5 offset:27008
s_waitcnt vmcnt(8)
s_waitcnt lgkmcnt(5)
s_barrier
v_mfma_f32_32x32x16_bf16 v[48:63],a[216:219],v[152:155],v[48:63]
ds_read_b128 v[184:187],v5 offset:35712
v_mfma_f32_32x32x16_bf16 v[48:63],a[220:223],v[156:159],v[48:63]
ds_read_b128 v[188:191],v5 offset:36224
s_waitcnt lgkmcnt(4)
v_mfma_f32_32x32x16_bf16 v[48:63],a[224:227],v[160:163],v[48:63]
ds_read_b128 v[128:131],v4 offset:0
v_mfma_f32_32x32x16_bf16 v[48:63],a[228:231],v[164:167],v[48:63]
ds_read_b128 v[132:135],v4 offset:512
ds_read_b32 v74,v11 offset:128
v_mfma_f32_32x32x16_bf16 v[48:63],a[232:235],v[168:171],v[48:63]
ds_read_b128 v[136:139],v4 offset:9216
v_mfma_f32_32x32x16_bf16 v[48:63],a[236:239],v[172:175],v[48:63]
ds_read_b128 v[140:143],v4 offset:9728
s_waitcnt lgkmcnt(5)
v_mfma_f32_32x32x16_bf16 v[48:63],a[240:243],v[176:179],v[48:63]
ds_read_b128 v[144:147],v4 offset:18432
s_waitcnt lgkmcnt(3)
v_lshlrev_b32 v72,16,v74
v_and_b32 v74,0xffff0000,v74
v_mfma_f32_32x32x16_bf16 v[48:63],a[244:247],v[180:183],v[48:63]
ds_read_b128 v[148:151],v4 offset:18944
v_rcp_f32 v73,v74
v_mul_f32 v74,v74,v74
v_mfma_f32_32x32x16_bf16 v[48:63],a[248:251],v[184:187],v[48:63]
ds_read_b128 v[152:155],v4 offset:27648
v_mul_f32 v73,0xbfb8aa3b,v73
s_sub_u32 s62,s62,1
v_mfma_f32_32x32x16_bf16 v[48:63],a[252:255],v[188:191],v[48:63]
ds_read_b128 v[156:159],v4 offset:28160
s_cbranch_scc0 .L_hl_3
s_nop 1
s_add_u32 s68,s68,s30
s_nop 6
v_fma_f32 v48,-v72,v112,v48
v_fma_f32 v49,-v72,v113,v49
v_fma_f32 v50,-v72,v114,v50
v_fma_f32 v51,-v72,v115,v51
v_fma_f32 v52,-v72,v116,v52
v_fma_f32 v53,-v72,v117,v53
v_fma_f32 v54,-v72,v118,v54
v_fma_f32 v55,-v72,v119,v55
v_fma_f32 v56,-v72,v120,v56
v_fma_f32 v57,-v72,v121,v57
v_fma_f32 v58,-v72,v122,v58
v_fma_f32 v59,-v72,v123,v59
v_fma_f32 v60,-v72,v124,v60
v_fma_f32 v61,-v72,v125,v61
v_fma_f32 v62,-v72,v126,v62
v_fma_f32 v63,-v72,v127,v63
v_mul_f32 v76,v48,v73
v_mul_f32 v77,v49,v73
v_mul_f32 v78,v50,v73
v_mul_f32 v79,v51,v73
v_mul_f32 v80,v52,v73
v_mul_f32 v81,v53,v73
v_mul_f32 v82,v54,v73
v_mul_f32 v83,v55,v73
v_exp_f32 v76,v76
v_exp_f32 v77,v77
v_mul_f32 v48,v48,v56
v_mul_f32 v49,v49,v57
v_exp_f32 v78,v78
v_exp_f32 v79,v79
v_mul_f32 v50,v50,v58
v_mul_f32 v51,v51,v59
v_exp_f32 v80,v80
v_exp_f32 v81,v81
v_mul_f32 v52,v52,v60
v_mul_f32 v53,v53,v61
v_exp_f32 v82,v82
v_exp_f32 v83,v83
v_mul_f32 v54,v54,v62
v_mul_f32 v55,v55,v63
v_fma_f32 v76,v76,v74,v74
v_fma_f32 v77,v77,v74,v74
v_rcp_f32 v76,v76
v_rcp_f32 v77,v77
v_fma_f32 v78,v78,v74,v74
v_fma_f32 v79,v79,v74,v74
v_rcp_f32 v78,v78
v_rcp_f32 v79,v79
v_fma_f32 v80,v80,v74,v74
v_fma_f32 v81,v81,v74,v74
v_rcp_f32 v80,v80
v_rcp_f32 v81,v81
v_fma_f32 v82,v82,v74,v74
v_fma_f32 v83,v83,v74,v74
v_rcp_f32 v82,v82
v_rcp_f32 v83,v83
v_mul_f32 v48,v48,v76
v_mul_f32 v49,v49,v77
v_mul_f32 v50,v50,v78
v_mul_f32 v51,v51,v79
v_mul_f32 v52,v52,v80
v_mul_f32 v53,v53,v81
v_mul_f32 v54,v54,v82
v_mul_f32 v55,v55,v83
v_cvt_pk_bf16_f32 v84,v48,v49
v_cvt_pk_bf16_f32 v85,v50,v51
v_cvt_pk_bf16_f32 v86,v52,v53
v_cvt_pk_bf16_f32 v87,v54,v55
buffer_store_dwordx4 v[84:87],v15,s[32:35],s68 offen sc1
s_waitcnt vmcnt(0)
s_barrier
buffer_store_dwordx2 v[16:17],v18,s[32:35],s68 offen sc1
s_cbranch_execz .L_hbk
s_branch .L_end
.L_rtg0_1:
s_mov_b32 s48,20000
.L_rlg0_1:
s_sleep 8
.L_rfg0_1:
buffer_load_dwordx2 v[12:13],v14,s[0:3],s66 offen sc1
s_sub_u32 s48,s48,1
s_cbranch_scc1 .L_end
s_waitcnt vmcnt(0)
v_cmp_ne_u64 vcc,v[16:17],v[12:13]
s_cbranch_vccnz .L_rlg0_1
s_branch .L_bkg0_1
.L_rtg1_2:
s_mov_b32 s48,20000
.L_rlg1_2:
s_sleep 8
.L_rfg1_2:
buffer_load_dwordx2 v[12:13],v14,s[0:3],s66 offen sc1
s_sub_u32 s48,s48,1
s_cbranch_scc1 .L_end
s_waitcnt vmcnt(0)
v_cmp_ne_u64 vcc,v[16:17],v[12:13]
s_cbranch_vccnz .L_rlg1_2
s_branch .L_bkg1_2
.L_rtqe:
s_mov_b32 s48,20000
s_branch .L_rfqe
.L_rlqe:
s_sleep 2
.L_rfqe:
buffer_load_dwordx2 v[60:61],v56,s[0:3],s66 offen sc1
s_sub_u32 s48,s48,1
s_cbranch_scc1 .L_end
s_waitcnt vmcnt(0)
v_cmp_ne_u64 vcc,s[54:55],v[60:61]
s_cbranch_vccnz .L_rlqe
s_branch .L_bkqe
.L_rtq0:
s_mov_b32 s48,20000
.L_rlq0:
s_sleep 8
.L_rfq0:
buffer_load_dwordx2 v[12:13],v0,s[0:3],s66 offen sc1
s_sub_u32 s48,s48,1
s_cbranch_scc1 .L_end
s_waitcnt vmcnt(0)
v_cmp_ne_u64 vcc,v[30:31],v[12:13]
s_cbranch_vccnz .L_rlq0
s_branch .L_bkq0
.L_rtq1:
s_mov_b32 s48,20000
.L_rlq1:
s_sleep 8
.L_rfq1:
buffer_load_dwordx2 v[12:13],v0,s[0:3],s66 offen sc1
s_sub_u32 s48,s48,1
s_cbranch_scc1 .L_end
s_waitcnt vmcnt(0)
v_cmp_ne_u64 vcc,v[30:31],v[12:13]
s_cbranch_vccnz .L_rlq1
s_branch .L_bkq1
.L_qs0:
s_cmp_lg_u32 s62,30
s_cbranch_scc1 .L_qn4
v_mov_b32 v120,0xff61b1e6
v_mov_b32 v121,0xff61b1e6
v_mov_b32 v122,0xff61b1e6
v_mov_b32 v123,0xff61b1e6
v_cmp_ne_u32 vcc,0xffff,v126
s_nop 1
v_cndmask_b32 v120,0,v120,vcc
v_cmp_ne_u32 vcc,0xffff0000,v126
s_nop 1
v_cndmask_b32 v121,0,v121,vcc
v_cmp_ne_u32 vcc,0xffff,v127
s_nop 1
v_cndmask_b32 v122,0,v122,vcc
v_cmp_ne_u32 vcc,0xffff0000,v127
s_nop 1
v_cndmask_b32 v123,0,v123,vcc
s_nop 1
s_branch .L_qb0
.L_qn4:
s_cmp_lg_u32 s62,17
s_cbranch_scc1 .L_qn30
s_mov_b32 s72,s69
s_nop 1
s_branch .L_qb0
.L_qn30:
s_cmp_lg_u32 s62,16
s_cbranch_scc1 .L_qn32
s_mov_b32 s72,1187840
s_nop 1
s_branch .L_qb0
.L_qn32:
s_cmp_lg_u32 s62,15
s_cbranch_scc1 .L_qn34
s_sub_u32 s68,s68,39198720
s_nop 1
s_branch .L_qb0
.L_qn34:
s_cmp_lg_u32 s62,14
s_cbranch_scc1 .L_qn36
v_mov_b32 v120,0xff61b1e6
v_mov_b32 v121,0xff61b1e6
v_mov_b32 v122,0xff61b1e6
v_mov_b32 v123,0xff61b1e6
v_cmp_ne_u32 vcc,0xffff,v126
s_nop 1
v_cndmask_b32 v120,0,v120,vcc
v_cmp_ne_u32 vcc,0xffff0000,v126
s_nop 1
v_cndmask_b32 v121,0,v121,vcc
v_cmp_ne_u32 vcc,0xffff,v127
s_nop 1
v_cndmask_b32 v122,0,v122,vcc
v_cmp_ne_u32 vcc,0xffff0000,v127
s_nop 1
v_cndmask_b32 v123,0,v123,vcc
s_nop 1
s_branch .L_qb0
.L_qn36:
s_branch .L_qb0
.L_qs1:
s_cmp_lg_u32 s62,31
s_cbranch_scc1 .L_qn3
s_cmp_lg_u32 s28,0
s_cbranch_scc1 .L_qb1
v_mov_b32 v120,0xff61b1e6
v_mov_b32 v121,0xff61b1e6
v_mov_b32 v122,0xff61b1e6
v_mov_b32 v123,0xff61b1e6
s_nop 1
s_branch .L_qb1
.L_qn3:
s_cmp_lg_u32 s62,17
s_cbranch_scc1 .L_qn31
s_mov_b32 s72,s70
s_nop 1
s_branch .L_qb1
.L_qn31:
s_cmp_lg_u32 s62,15
s_cbranch_scc1 .L_qn35
s_add_u32 s68,s68,38010880
s_cmp_lg_u32 s28,0
s_cbranch_scc1 .L_qb1
v_mov_b32 v120,0xff61b1e6
v_mov_b32 v121,0xff61b1e6
v_mov_b32 v122,0xff61b1e6
v_mov_b32 v123,0xff61b1e6
s_nop 1
s_branch .L_qb1
.L_qn35:
s_branch .L_qb1
.L_rtw1_1:
s_mov_b32 s48,20000
.L_rlw1_1:
s_sleep 8
.L_rfw1_1:
buffer_load_dwordx2 v[12:13],v14,s[0:3],s66 offen sc1
s_sub_u32 s48,s48,1
s_cbranch_scc1 .L_end
s_waitcnt vmcnt(0)
v_cmp_ne_u64 vcc,v[16:17],v[12:13]
s_cbranch_vccnz .L_rlw1_1
s_branch .L_bkw1_1
.L_rth0_0:
s_mov_b32 s48,20000
.L_rlh0_0:
s_sleep 8
.L_rfh0_0:
buffer_load_dwordx2 v[12:13],v14,s[0:3],s66 offen sc1
s_sub_u32 s48,s48,1
s_cbranch_scc1 .L_end
s_waitcnt vmcnt(0)
v_cmp_ne_u64 vcc,v[16:17],v[12:13]
s_cbranch_vccnz .L_rlh0_0
s_branch .L_bkh0_0
.L_rth1_0:
s_mov_b32 s48,20000
.L_rlh1_0:
s_sleep 8
.L_rfh1_0:
buffer_load_dwordx2 v[12:13],v14,s[0:3],s66 offen sc1
s_sub_u32 s48,s48,1
s_cbranch_scc1 .L_end
s_waitcnt vmcnt(0)
v_cmp_ne_u64 vcc,v[16:17],v[12:13]
s_cbranch_vccnz .L_rlh1_0
s_branch .L_bkh1_0
.L_rth0_1:
s_mov_b32 s48,20000
.L_rlh0_1:
s_sleep 8
.L_rfh0_1:
buffer_load_dwordx2 v[12:13],v14,s[0:3],s66 offen sc1
s_sub_u32 s48,s48,1
s_cbranch_scc1 .L_end
s_waitcnt vmcnt(0)
v_cmp_ne_u64 vcc,v[16:17],v[12:13]
s_cbranch_vccnz .L_rlh0_1
s_branch .L_bkh0_1
.L_rth1_1:
s_mov_b32 s48,20000
.L_rlh1_1:
s_sleep 8
.L_rfh1_1:
buffer_load_dwordx2 v[12:13],v14,s[0:3],s66 offen sc1
s_sub_u32 s48,s48,1
s_cbranch_scc1 .L_end
s_waitcnt vmcnt(0)
v_cmp_ne_u64 vcc,v[16:17],v[12:13]
s_cbranch_vccnz .L_rlh1_1
s_branch .L_bkh1_1
.L_rth0_2:
s_mov_b32 s48,20000
.L_rlh0_2:
s_sleep 8
.L_rfh0_2:
buffer_load_dwordx2 v[12:13],v14,s[0:3],s66 offen sc1
s_sub_u32 s48,s48,1
s_cbranch_scc1 .L_end
s_waitcnt vmcnt(0)
v_cmp_ne_u64 vcc,v[16:17],v[12:13]
s_cbranch_vccnz .L_rlh0_2
s_branch .L_bkh0_2
.L_rth1_2:
)#"
R"#(
s_mov_b32 s48,20000
.L_rlh1_2:
s_sleep 8
.L_rfh1_2:
buffer_load_dwordx2 v[12:13],v14,s[0:3],s66 offen sc1
s_sub_u32 s48,s48,1
s_cbranch_scc1 .L_end
s_waitcnt vmcnt(0)
v_cmp_ne_u64 vcc,v[16:17],v[12:13]
s_cbranch_vccnz .L_rlh1_2
s_branch .L_bkh1_2
.L_rth0_3:
s_mov_b32 s48,20000
.L_rlh0_3:
s_sleep 8
.L_rfh0_3:
buffer_load_dwordx2 v[12:13],v14,s[0:3],s66 offen sc1
s_sub_u32 s48,s48,1
s_cbranch_scc1 .L_end
s_waitcnt vmcnt(0)
v_cmp_ne_u64 vcc,v[16:17],v[12:13]
s_cbranch_vccnz .L_rlh0_3
s_branch .L_bkh0_3
.L_rth1_3:
s_mov_b32 s48,20000
.L_rlh1_3:
s_sleep 8
.L_rfh1_3:
buffer_load_dwordx2 v[12:13],v14,s[0:3],s66 offen sc1
s_sub_u32 s48,s48,1
s_cbranch_scc1 .L_end
s_waitcnt vmcnt(0)
v_cmp_ne_u64 vcc,v[16:17],v[12:13]
s_cbranch_vccnz .L_rlh1_3
s_branch .L_bkh1_3
.L_rtl0_1:
s_mov_b32 s48,20000
.L_rll0_1:
s_sleep 8
.L_rfl0_1:
buffer_load_dwordx2 v[6:7],v0,s[0:3],s66 offen sc1
s_sub_u32 s48,s48,1
s_cbranch_scc1 .L_end
s_waitcnt vmcnt(0)
v_cmp_ne_u64 vcc,v[8:9],v[6:7]
s_cbranch_vccnz .L_rll0_1
s_branch .L_bkl0_1
.L_rtl1_1:
s_mov_b32 s48,20000
.L_rll1_1:
s_sleep 8
.L_rfl1_1:
buffer_load_dwordx2 v[6:7],v0,s[0:3],s66 offen sc1
s_sub_u32 s48,s48,1
s_cbranch_scc1 .L_end
s_waitcnt vmcnt(0)
v_cmp_ne_u64 vcc,v[8:9],v[6:7]
s_cbranch_vccnz .L_rll1_1
s_branch .L_bkl1_1
.L_rtm1_1:
s_cmp_eq_u32 s62,0
s_cbranch_scc1 .L_bkm1_1
s_mov_b32 s48,20000
.L_rlm1_1:
s_sleep 8
.L_rfm1_1:
buffer_load_dwordx2 v[6:7],v0,s[0:3],s66 offen sc1
s_sub_u32 s48,s48,1
s_cbranch_scc1 .L_end
s_waitcnt vmcnt(0)
v_cmp_ne_u64 vcc,v[8:9],v[6:7]
s_cbranch_vccnz .L_rlm1_1
s_branch .L_bkm1_1
.L_rtm0_1:
s_cmp_eq_u32 s62,0
s_cbranch_scc1 .L_bkm0_1
s_mov_b32 s48,20000
.L_rlm0_1:
s_sleep 8
.L_rfm0_1:
buffer_load_dwordx2 v[6:7],v0,s[0:3],s66 offen sc1
s_sub_u32 s48,s48,1
s_cbranch_scc1 .L_end
s_waitcnt vmcnt(0)
v_cmp_ne_u64 vcc,v[8:9],v[6:7]
s_cbranch_vccnz .L_rlm0_1
s_branch .L_bkm0_1
.L_rta0_1:
s_mov_b32 s48,20000
.L_rla0_1:
s_sleep 8
.L_rfa0_1:
buffer_load_dwordx2 v[6:7],v0,s[0:3],s66 offen sc1
s_sub_u32 s48,s48,1
s_cbranch_scc1 .L_end
s_waitcnt vmcnt(0)
v_cmp_ne_u64 vcc,v[8:9],v[6:7]
s_cbranch_vccnz .L_rla0_1
s_branch .L_bka0_1
.L_rta1_1:
s_mov_b32 s48,20000
.L_rla1_1:
s_sleep 8
.L_rfa1_1:
buffer_load_dwordx2 v[6:7],v0,s[0:3],s66 offen sc1
s_sub_u32 s48,s48,1
s_cbranch_scc1 .L_end
s_waitcnt vmcnt(0)
v_cmp_ne_u64 vcc,v[8:9],v[6:7]
s_cbranch_vccnz .L_rla1_1
s_branch .L_bka1_1
.L_rtb1_1:
s_cmp_eq_u32 s62,0
s_cbranch_scc1 .L_bkb1_1
s_mov_b32 s48,20000
.L_rlb1_1:
s_sleep 8
.L_rfb1_1:
buffer_load_dwordx2 v[6:7],v0,s[0:3],s66 offen sc1
s_sub_u32 s48,s48,1
s_cbranch_scc1 .L_end
s_waitcnt vmcnt(0)
v_cmp_ne_u64 vcc,v[8:9],v[6:7]
s_cbranch_vccnz .L_rlb1_1
s_branch .L_bkb1_1
.L_rtb0_1:
s_cmp_eq_u32 s62,0
s_cbranch_scc1 .L_bkb0_1
s_mov_b32 s48,20000
.L_rlb0_1:
s_sleep 8
.L_rfb0_1:
buffer_load_dwordx2 v[6:7],v0,s[0:3],s66 offen sc1
s_sub_u32 s48,s48,1
s_cbranch_scc1 .L_end
s_waitcnt vmcnt(0)
v_cmp_ne_u64 vcc,v[8:9],v[6:7]
s_cbranch_vccnz .L_rlb0_1
s_branch .L_bkb0_1
.L_two:
v_lshrrev_b32 v1,4,v2
s_mov_b32 s25,1
s_mov_b32 s73,0
s_bfe_u32 s28,s24,0x20001
s_bfe_u32 s27,s24,0x30003
s_bfe_u32 s29,s24,0x10006
s_lshl2_add_u32 s50,s27,s26
s_add_u32 s48,s28,136
s_mul_i32 s48,s48,296960
s_cmp_eq_u32 s29,0
s_cselect_b32 s48,s48,78397440
s_mul_i32 s70,s50,1152
s_add_u32 s70,s70,s48
v_mov_b32 v21,0x7fff0000
s_branch .L_d
.L_end:
s_waitcnt vmcnt(0) lgkmcnt(0)
s_endpgm
.L_d:
s_lshl_b32 s65,s50,11
s_mul_i32 s48,s50,1152
s_add_u32 s48,s48,4612096
s_add_u32 s65,s65,4833280
s_cmp_eq_u32 s29,0
s_cselect_b32 s48,s65,s48
s_cselect_b32 s49,280,0
s_lshl2_add_u32 s49,s27,s49
s_waitcnt lgkmcnt(0)
s_add_u32 s54,s52,0x7f4a7c15
s_addc_u32 s55,s53,0x9e3779b9
s_lshl_b32 s67,s49,5
s_add_u32 s67,s67,4898816
s_sub_u32 s51,s67,s48
s_add_u32 s92,s32,s51
s_addc_u32 s93,s33,0x10000
s_cmp_eq_u32 s26,0
s_cselect_b32 s94,1,0
s_mov_b32 s95,0x20000
v_mov_b32 v8,s54
v_mov_b32 v9,s55
v_cmp_gt_u32 vcc,12,v1
v_mul_u32_u24 v3,296960,v1
v_mov_b32 v6,0x7fff0000
v_cndmask_b32 v3,v6,v3,vcc
s_add_u32 s51,s67,76021760
buffer_store_dwordx2 v[8:9],v3,s[0:3],s51 offen sc1
s_sub_u32 s67,s48,296960
s_mov_b32 s30,1187840
s_mov_b32 s31,1175552
s_mul_i32 s57,s25,136
s_add_u32 s57,s57,s28
s_mul_i32 s57,s57,296960
s_mul_i32 s48,s25,2375680
s_add_u32 s65,s65,s48
s_add_u32 s65,s65,s57
s_mul_i32 s96,s29,3670016
s_add_u32 s36,s18,0x0
s_addc_u32 s37,s19,0x0
s_and_b32 s37,s37,0xffff
s_mov_b32 s38,0x700000
s_mov_b32 s39,0x20000
s_add_u32 s40,s18,0xfffffc80
s_addc_u32 s41,s19,0xffffffff
s_and_b32 s41,s41,0xffff
s_mov_b32 s42,0x700380
s_mov_b32 s43,0x20000
s_lshl_b32 s48,s27,8
s_add_u32 s96,s96,s48
v_and_b32 v8,3,v1
v_mul_u32_u24 v8,16,v8
s_and_b32 s48,s26,1
s_lshl_b32 s48,s48,6
s_lshr_b32 s49,s26,1
s_lshl_b32 s49,s49,10
s_add_u32 s48,s48,s49
v_add_u32 v8,s48,v8
v_lshrrev_b32 v4,3,v1
v_bfe_u32 v5,v1,4,1
s_and_b32 s48,s26,1
s_lshl_b32 s48,s48,1
v_or_b32 v5,s48,v5
v_lshlrev_b32 v5,1,v5
v_and_b32 v6,7,v1
v_xor_b32 v5,v5,v6
v_mul_u32_u24 v4,2048,v4
v_lshl_add_u32 v7,v5,4,v4
s_mul_i32 s48,s26,16384
s_add_u32 s96,s96,s48
s_mul_i32 s97,s26,2048
s_add_u32 m0,s97,0
s_nop 0
buffer_load_dwordx4 v7,s[36:39],s96 offen offset:0 lds
buffer_load_dwordx4 v7,s[40:43],s96 offen offset:1024 lds
s_add_u32 s96,s96,65536
s_add_u32 m0,s97,8192
s_nop 0
buffer_load_dwordx4 v7,s[36:39],s96 offen offset:0 lds
buffer_load_dwordx4 v7,s[40:43],s96 offen offset:1024 lds
s_add_u32 s96,s96,65536
s_add_u32 m0,s97,16384
s_nop 0
buffer_load_dwordx4 v7,s[36:39],s96 offen offset:0 lds
buffer_load_dwordx4 v7,s[40:43],s96 offen offset:1024 lds
s_add_u32 s96,s96,65536
s_add_u32 m0,s97,24576
s_nop 0
buffer_load_dwordx4 v7,s[36:39],s96 offen offset:0 lds
buffer_load_dwordx4 v7,s[40:43],s96 offen offset:1024 lds
s_add_u32 s96,s96,65536
s_add_u32 m0,s97,32768
s_nop 0
buffer_load_dwordx4 v7,s[36:39],s96 offen offset:0 lds
buffer_load_dwordx4 v7,s[40:43],s96 offen offset:1024 lds
s_add_u32 s96,s96,65536
s_add_u32 m0,s97,40960
s_nop 0
buffer_load_dwordx4 v7,s[36:39],s96 offen offset:0 lds
buffer_load_dwordx4 v7,s[40:43],s96 offen offset:1024 lds
s_add_u32 s96,s96,65536
s_add_u32 m0,s97,49152
s_nop 0
buffer_load_dwordx4 v7,s[36:39],s96 offen offset:0 lds
buffer_load_dwordx4 v7,s[40:43],s96 offen offset:1024 lds
s_add_u32 s96,s96,65536
s_add_u32 m0,s97,57344
s_nop 0
buffer_load_dwordx4 v7,s[36:39],s96 offen offset:0 lds
buffer_load_dwordx4 v7,s[40:43],s96 offen offset:1024 lds
s_add_u32 s96,s96,65536
s_add_u32 m0,s97,65536
s_nop 0
buffer_load_dwordx4 v7,s[36:39],s96 offen offset:0 lds
buffer_load_dwordx4 v7,s[40:43],s96 offen offset:1024 lds
s_add_u32 s96,s96,65536
s_add_u32 m0,s97,73728
s_nop 0
buffer_load_dwordx4 v7,s[36:39],s96 offen offset:0 lds
buffer_load_dwordx4 v7,s[40:43],s96 offen offset:1024 lds
s_add_u32 s96,s96,65536
s_add_u32 m0,s97,81920
s_nop 0
buffer_load_dwordx4 v7,s[36:39],s96 offen offset:0 lds
buffer_load_dwordx4 v7,s[40:43],s96 offen offset:1024 lds
s_add_u32 s96,s96,65536
s_add_u32 m0,s97,90112
s_nop 0
buffer_load_dwordx4 v7,s[36:39],s96 offen offset:0 lds
buffer_load_dwordx4 v7,s[40:43],s96 offen offset:1024 lds
s_add_u32 s96,s96,65536
s_add_u32 m0,s97,98304
s_nop 0
buffer_load_dwordx4 v7,s[36:39],s96 offen offset:0 lds
buffer_load_dwordx4 v7,s[40:43],s96 offen offset:1024 lds
s_add_u32 s96,s96,65536
s_add_u32 m0,s97,106496
s_nop 0
buffer_load_dwordx4 v7,s[36:39],s96 offen offset:0 lds
buffer_load_dwordx4 v7,s[40:43],s96 offen offset:1024 lds
s_add_u32 s96,s96,65536
v_lshrrev_b32 v5,4,v1
v_bfe_u32 v4,v1,3,1
v_and_b32 v6,1,v5
v_lshl_add_u32 v4,v6,1,v4
v_lshlrev_b32 v4,5,v4
v_mul_u32_u24 v5,2048,v5
v_bfe_u32 v6,v1,2,2
v_lshl_add_u32 v5,v6,7,v5
v_xor_b32 v8,v4,v8
v_add_u32 v8,v5,v8
v_add_u32 v9,65536,v8
s_waitcnt vmcnt(24)
s_barrier
s_add_u32 m0,s97,114688
s_nop 0
buffer_load_dwordx4 v7,s[36:39],s96 offen offset:0 lds
buffer_load_dwordx4 v7,s[40:43],s96 offen offset:1024 lds
s_add_u32 s96,s96,65536
s_add_u32 m0,s97,122880
s_nop 0
buffer_load_dwordx4 v7,s[36:39],s96 offen offset:0 lds
buffer_load_dwordx4 v7,s[40:43],s96 offen offset:1024 lds
s_add_u32 s96,s96,65536
ds_read_b64_tr_b16 a[0:1],v8 offset:0
ds_read_b64_tr_b16 a[2:3],v8 offset:512
ds_read_b64_tr_b16 a[4:5],v8 offset:8
ds_read_b64_tr_b16 a[6:7],v8 offset:520
ds_read_b64_tr_b16 a[8:9],v8 offset:8192
ds_read_b64_tr_b16 a[10:11],v8 offset:8704
ds_read_b64_tr_b16 a[12:13],v8 offset:8200
ds_read_b64_tr_b16 a[14:15],v8 offset:8712
s_waitcnt lgkmcnt(0)
s_waitcnt vmcnt(24)
s_barrier
s_add_u32 m0,s97,0
s_nop 0
buffer_load_dwordx4 v7,s[36:39],s96 offen offset:0 lds
buffer_load_dwordx4 v7,s[40:43],s96 offen offset:1024 lds
s_add_u32 s96,s96,65536
s_add_u32 m0,s97,8192
s_nop 0
buffer_load_dwordx4 v7,s[36:39],s96 offen offset:0 lds
buffer_load_dwordx4 v7,s[40:43],s96 offen offset:1024 lds
s_add_u32 s96,s96,65536
ds_read_b64_tr_b16 a[16:17],v8 offset:16384
ds_read_b64_tr_b16 a[18:19],v8 offset:16896
ds_read_b64_tr_b16 a[20:21],v8 offset:16392
ds_read_b64_tr_b16 a[22:23],v8 offset:16904
ds_read_b64_tr_b16 a[24:25],v8 offset:24576
ds_read_b64_tr_b16 a[26:27],v8 offset:25088
ds_read_b64_tr_b16 a[28:29],v8 offset:24584
ds_read_b64_tr_b16 a[30:31],v8 offset:25096
s_waitcnt lgkmcnt(0)
s_waitcnt vmcnt(24)
s_barrier
s_add_u32 m0,s97,16384
s_nop 0
buffer_load_dwordx4 v7,s[36:39],s96 offen offset:0 lds
buffer_load_dwordx4 v7,s[40:43],s96 offen offset:1024 lds
s_add_u32 s96,s96,65536
s_add_u32 m0,s97,24576
s_nop 0
buffer_load_dwordx4 v7,s[36:39],s96 offen offset:0 lds
buffer_load_dwordx4 v7,s[40:43],s96 offen offset:1024 lds
s_add_u32 s96,s96,65536
ds_read_b64_tr_b16 a[32:33],v8 offset:32768
ds_read_b64_tr_b16 a[34:35],v8 offset:33280
ds_read_b64_tr_b16 a[36:37],v8 offset:32776
ds_read_b64_tr_b16 a[38:39],v8 offset:33288
ds_read_b64_tr_b16 a[40:41],v8 offset:40960
ds_read_b64_tr_b16 a[42:43],v8 offset:41472
ds_read_b64_tr_b16 a[44:45],v8 offset:40968
ds_read_b64_tr_b16 a[46:47],v8 offset:41480
s_waitcnt lgkmcnt(0)
s_waitcnt vmcnt(24)
s_barrier
s_add_u32 m0,s97,32768
s_nop 0
buffer_load_dwordx4 v7,s[36:39],s96 offen offset:0 lds
buffer_load_dwordx4 v7,s[40:43],s96 offen offset:1024 lds
s_add_u32 s96,s96,65536
s_add_u32 m0,s97,40960
s_nop 0
buffer_load_dwordx4 v7,s[36:39],s96 offen offset:0 lds
buffer_load_dwordx4 v7,s[40:43],s96 offen offset:1024 lds
s_add_u32 s96,s96,65536
ds_read_b64_tr_b16 a[48:49],v8 offset:49152
ds_read_b64_tr_b16 a[50:51],v8 offset:49664
ds_read_b64_tr_b16 a[52:53],v8 offset:49160
ds_read_b64_tr_b16 a[54:55],v8 offset:49672
ds_read_b64_tr_b16 a[56:57],v8 offset:57344
ds_read_b64_tr_b16 a[58:59],v8 offset:57856
ds_read_b64_tr_b16 a[60:61],v8 offset:57352
ds_read_b64_tr_b16 a[62:63],v8 offset:57864
s_waitcnt lgkmcnt(0)
s_waitcnt vmcnt(24)
s_barrier
s_add_u32 m0,s97,49152
s_nop 0
buffer_load_dwordx4 v7,s[36:39],s96 offen offset:0 lds
buffer_load_dwordx4 v7,s[40:43],s96 offen offset:1024 lds
s_add_u32 s96,s96,65536
s_add_u32 m0,s97,57344
s_nop 0
buffer_load_dwordx4 v7,s[36:39],s96 offen offset:0 lds
buffer_load_dwordx4 v7,s[40:43],s96 offen offset:1024 lds
s_add_u32 s96,s96,65536
ds_read_b64_tr_b16 a[64:65],v9 offset:0
ds_read_b64_tr_b16 a[66:67],v9 offset:512
ds_read_b64_tr_b16 a[68:69],v9 offset:8
ds_read_b64_tr_b16 a[70:71],v9 offset:520
ds_read_b64_tr_b16 a[72:73],v9 offset:8192
ds_read_b64_tr_b16 a[74:75],v9 offset:8704
ds_read_b64_tr_b16 a[76:77],v9 offset:8200
ds_read_b64_tr_b16 a[78:79],v9 offset:8712
s_waitcnt lgkmcnt(0)
s_waitcnt vmcnt(24)
s_barrier
s_add_u32 m0,s97,65536
s_nop 0
buffer_load_dwordx4 v7,s[36:39],s96 offen offset:0 lds
buffer_load_dwordx4 v7,s[40:43],s96 offen offset:1024 lds
s_add_u32 s96,s96,65536
s_add_u32 m0,s97,73728
s_nop 0
buffer_load_dwordx4 v7,s[36:39],s96 offen offset:0 lds
buffer_load_dwordx4 v7,s[40:43],s96 offen offset:1024 lds
s_add_u32 s96,s96,65536
ds_read_b64_tr_b16 a[80:81],v9 offset:16384
ds_read_b64_tr_b16 a[82:83],v9 offset:16896
ds_read_b64_tr_b16 a[84:85],v9 offset:16392
ds_read_b64_tr_b16 a[86:87],v9 offset:16904
ds_read_b64_tr_b16 a[88:89],v9 offset:24576
ds_read_b64_tr_b16 a[90:91],v9 offset:25088
ds_read_b64_tr_b16 a[92:93],v9 offset:24584
ds_read_b64_tr_b16 a[94:95],v9 offset:25096
s_waitcnt lgkmcnt(0)
s_waitcnt vmcnt(24)
s_barrier
s_add_u32 m0,s97,81920
s_nop 0
buffer_load_dwordx4 v7,s[36:39],s96 offen offset:0 lds
buffer_load_dwordx4 v7,s[40:43],s96 offen offset:1024 lds
s_add_u32 s96,s96,65536
s_add_u32 m0,s97,90112
s_nop 0
buffer_load_dwordx4 v7,s[36:39],s96 offen offset:0 lds
buffer_load_dwordx4 v7,s[40:43],s96 offen offset:1024 lds
s_add_u32 s96,s96,65536
ds_read_b64_tr_b16 a[96:97],v9 offset:32768
ds_read_b64_tr_b16 a[98:99],v9 offset:33280
ds_read_b64_tr_b16 a[100:101],v9 offset:32776
ds_read_b64_tr_b16 a[102:103],v9 offset:33288
ds_read_b64_tr_b16 a[104:105],v9 offset:40960
ds_read_b64_tr_b16 a[106:107],v9 offset:41472
ds_read_b64_tr_b16 a[108:109],v9 offset:40968
ds_read_b64_tr_b16 a[110:111],v9 offset:41480
s_waitcnt lgkmcnt(0)
s_waitcnt vmcnt(24)
s_barrier
s_add_u32 m0,s97,98304
s_nop 0
buffer_load_dwordx4 v7,s[36:39],s96 offen offset:0 lds
buffer_load_dwordx4 v7,s[40:43],s96 offen offset:1024 lds
s_add_u32 s96,s96,65536
s_add_u32 m0,s97,106496
s_nop 0
buffer_load_dwordx4 v7,s[36:39],s96 offen offset:0 lds
buffer_load_dwordx4 v7,s[40:43],s96 offen offset:1024 lds
s_add_u32 s96,s96,65536
ds_read_b64_tr_b16 a[112:113],v9 offset:49152
ds_read_b64_tr_b16 a[114:115],v9 offset:49664
ds_read_b64_tr_b16 a[116:117],v9 offset:49160
ds_read_b64_tr_b16 a[118:119],v9 offset:49672
ds_read_b64_tr_b16 a[120:121],v9 offset:57344
ds_read_b64_tr_b16 a[122:123],v9 offset:57856
ds_read_b64_tr_b16 a[124:125],v9 offset:57352
ds_read_b64_tr_b16 a[126:127],v9 offset:57864
s_waitcnt lgkmcnt(0)
s_waitcnt vmcnt(24)
s_barrier
s_add_u32 m0,s97,114688
s_nop 0
buffer_load_dwordx4 v7,s[36:39],s96 offen offset:0 lds
buffer_load_dwordx4 v7,s[40:43],s96 offen offset:1024 lds
s_add_u32 s96,s96,65536
s_add_u32 m0,s97,122880
s_nop 0
buffer_load_dwordx4 v7,s[36:39],s96 offen offset:0 lds
buffer_load_dwordx4 v7,s[40:43],s96 offen offset:1024 lds
s_add_u32 s96,s96,65536
ds_read_b64_tr_b16 a[128:129],v8 offset:0
ds_read_b64_tr_b16 a[130:131],v8 offset:512
ds_read_b64_tr_b16 a[132:133],v8 offset:8
ds_read_b64_tr_b16 a[134:135],v8 offset:520
ds_read_b64_tr_b16 a[136:137],v8 offset:8192
ds_read_b64_tr_b16 a[138:139],v8 offset:8704
ds_read_b64_tr_b16 a[140:141],v8 offset:8200
ds_read_b64_tr_b16 a[142:143],v8 offset:8712
s_waitcnt lgkmcnt(0)
s_waitcnt vmcnt(24)
)#"
R"#(
s_barrier
s_add_u32 m0,s97,0
s_nop 0
buffer_load_dwordx4 v7,s[36:39],s96 offen offset:0 lds
buffer_load_dwordx4 v7,s[40:43],s96 offen offset:1024 lds
s_add_u32 s96,s96,65536
s_add_u32 m0,s97,8192
s_nop 0
buffer_load_dwordx4 v7,s[36:39],s96 offen offset:0 lds
buffer_load_dwordx4 v7,s[40:43],s96 offen offset:1024 lds
s_add_u32 s96,s96,65536
ds_read_b64_tr_b16 a[144:145],v8 offset:16384
ds_read_b64_tr_b16 a[146:147],v8 offset:16896
ds_read_b64_tr_b16 a[148:149],v8 offset:16392
ds_read_b64_tr_b16 a[150:151],v8 offset:16904
ds_read_b64_tr_b16 a[152:153],v8 offset:24576
ds_read_b64_tr_b16 a[154:155],v8 offset:25088
ds_read_b64_tr_b16 a[156:157],v8 offset:24584
ds_read_b64_tr_b16 a[158:159],v8 offset:25096
s_waitcnt lgkmcnt(0)
s_waitcnt vmcnt(24)
s_barrier
s_add_u32 m0,s97,16384
s_nop 0
buffer_load_dwordx4 v7,s[36:39],s96 offen offset:0 lds
buffer_load_dwordx4 v7,s[40:43],s96 offen offset:1024 lds
s_add_u32 s96,s96,65536
s_add_u32 m0,s97,24576
s_nop 0
buffer_load_dwordx4 v7,s[36:39],s96 offen offset:0 lds
buffer_load_dwordx4 v7,s[40:43],s96 offen offset:1024 lds
s_add_u32 s96,s96,65536
ds_read_b64_tr_b16 a[160:161],v8 offset:32768
ds_read_b64_tr_b16 a[162:163],v8 offset:33280
ds_read_b64_tr_b16 a[164:165],v8 offset:32776
ds_read_b64_tr_b16 a[166:167],v8 offset:33288
ds_read_b64_tr_b16 a[168:169],v8 offset:40960
ds_read_b64_tr_b16 a[170:171],v8 offset:41472
ds_read_b64_tr_b16 a[172:173],v8 offset:40968
ds_read_b64_tr_b16 a[174:175],v8 offset:41480
s_waitcnt lgkmcnt(0)
s_waitcnt vmcnt(24)
s_barrier
s_add_u32 m0,s97,32768
s_nop 0
buffer_load_dwordx4 v7,s[36:39],s96 offen offset:0 lds
buffer_load_dwordx4 v7,s[40:43],s96 offen offset:1024 lds
s_add_u32 s96,s96,65536
s_add_u32 m0,s97,40960
s_nop 0
buffer_load_dwordx4 v7,s[36:39],s96 offen offset:0 lds
buffer_load_dwordx4 v7,s[40:43],s96 offen offset:1024 lds
s_add_u32 s96,s96,65536
ds_read_b64_tr_b16 a[176:177],v8 offset:49152
ds_read_b64_tr_b16 a[178:179],v8 offset:49664
ds_read_b64_tr_b16 a[180:181],v8 offset:49160
ds_read_b64_tr_b16 a[182:183],v8 offset:49672
ds_read_b64_tr_b16 a[184:185],v8 offset:57344
ds_read_b64_tr_b16 a[186:187],v8 offset:57856
ds_read_b64_tr_b16 a[188:189],v8 offset:57352
ds_read_b64_tr_b16 a[190:191],v8 offset:57864
s_waitcnt lgkmcnt(0)
s_waitcnt vmcnt(24)
s_barrier
s_add_u32 m0,s97,49152
s_nop 0
buffer_load_dwordx4 v7,s[36:39],s96 offen offset:0 lds
buffer_load_dwordx4 v7,s[40:43],s96 offen offset:1024 lds
s_add_u32 s96,s96,65536
s_add_u32 m0,s97,57344
s_nop 0
buffer_load_dwordx4 v7,s[36:39],s96 offen offset:0 lds
buffer_load_dwordx4 v7,s[40:43],s96 offen offset:1024 lds
s_add_u32 s96,s96,65536
ds_read_b64_tr_b16 a[192:193],v9 offset:0
ds_read_b64_tr_b16 a[194:195],v9 offset:512
ds_read_b64_tr_b16 a[196:197],v9 offset:8
ds_read_b64_tr_b16 a[198:199],v9 offset:520
ds_read_b64_tr_b16 a[200:201],v9 offset:8192
ds_read_b64_tr_b16 a[202:203],v9 offset:8704
ds_read_b64_tr_b16 a[204:205],v9 offset:8200
ds_read_b64_tr_b16 a[206:207],v9 offset:8712
s_waitcnt lgkmcnt(0)
s_waitcnt vmcnt(24)
s_barrier
s_add_u32 m0,s97,65536
s_nop 0
buffer_load_dwordx4 v7,s[36:39],s96 offen offset:0 lds
buffer_load_dwordx4 v7,s[40:43],s96 offen offset:1024 lds
s_add_u32 s96,s96,65536
s_add_u32 m0,s97,73728
s_nop 0
buffer_load_dwordx4 v7,s[36:39],s96 offen offset:0 lds
buffer_load_dwordx4 v7,s[40:43],s96 offen offset:1024 lds
s_add_u32 s96,s96,65536
ds_read_b64_tr_b16 a[208:209],v9 offset:16384
ds_read_b64_tr_b16 a[210:211],v9 offset:16896
ds_read_b64_tr_b16 a[212:213],v9 offset:16392
ds_read_b64_tr_b16 a[214:215],v9 offset:16904
ds_read_b64_tr_b16 a[216:217],v9 offset:24576
ds_read_b64_tr_b16 a[218:219],v9 offset:25088
ds_read_b64_tr_b16 a[220:221],v9 offset:24584
ds_read_b64_tr_b16 a[222:223],v9 offset:25096
s_waitcnt lgkmcnt(0)
s_waitcnt vmcnt(24)
s_barrier
s_add_u32 m0,s97,81920
s_nop 0
buffer_load_dwordx4 v7,s[36:39],s96 offen offset:0 lds
buffer_load_dwordx4 v7,s[40:43],s96 offen offset:1024 lds
s_add_u32 s96,s96,65536
s_add_u32 m0,s97,90112
s_nop 0
buffer_load_dwordx4 v7,s[36:39],s96 offen offset:0 lds
buffer_load_dwordx4 v7,s[40:43],s96 offen offset:1024 lds
s_add_u32 s96,s96,65536
ds_read_b64_tr_b16 a[224:225],v9 offset:32768
ds_read_b64_tr_b16 a[226:227],v9 offset:33280
ds_read_b64_tr_b16 a[228:229],v9 offset:32776
ds_read_b64_tr_b16 a[230:231],v9 offset:33288
ds_read_b64_tr_b16 a[232:233],v9 offset:40960
ds_read_b64_tr_b16 a[234:235],v9 offset:41472
ds_read_b64_tr_b16 a[236:237],v9 offset:40968
ds_read_b64_tr_b16 a[238:239],v9 offset:41480
s_waitcnt lgkmcnt(0)
s_waitcnt vmcnt(24)
s_barrier
s_add_u32 m0,s97,98304
s_nop 0
buffer_load_dwordx4 v7,s[36:39],s96 offen offset:0 lds
buffer_load_dwordx4 v7,s[40:43],s96 offen offset:1024 lds
s_add_u32 s96,s96,65536
s_add_u32 m0,s97,106496
s_nop 0
buffer_load_dwordx4 v7,s[36:39],s96 offen offset:0 lds
buffer_load_dwordx4 v7,s[40:43],s96 offen offset:1024 lds
s_add_u32 s96,s96,65536
ds_read_b64_tr_b16 a[240:241],v9 offset:49152
ds_read_b64_tr_b16 a[242:243],v9 offset:49664
ds_read_b64_tr_b16 a[244:245],v9 offset:49160
ds_read_b64_tr_b16 a[246:247],v9 offset:49672
ds_read_b64_tr_b16 a[248:249],v9 offset:57344
ds_read_b64_tr_b16 a[250:251],v9 offset:57856
ds_read_b64_tr_b16 a[252:253],v9 offset:57352
ds_read_b64_tr_b16 a[254:255],v9 offset:57864
s_waitcnt lgkmcnt(0)
s_waitcnt vmcnt(24)
s_barrier
s_add_u32 m0,s97,114688
s_nop 0
buffer_load_dwordx4 v7,s[36:39],s96 offen offset:0 lds
buffer_load_dwordx4 v7,s[40:43],s96 offen offset:1024 lds
s_add_u32 s96,s96,65536
s_add_u32 m0,s97,122880
s_nop 0
buffer_load_dwordx4 v7,s[36:39],s96 offen offset:0 lds
buffer_load_dwordx4 v7,s[40:43],s96 offen offset:1024 lds
s_add_u32 s96,s96,65536
ds_read_b64_tr_b16 v[64:65],v8 offset:0
ds_read_b64_tr_b16 v[66:67],v8 offset:512
ds_read_b64_tr_b16 v[68:69],v8 offset:8
ds_read_b64_tr_b16 v[70:71],v8 offset:520
ds_read_b64_tr_b16 v[72:73],v8 offset:8192
ds_read_b64_tr_b16 v[74:75],v8 offset:8704
ds_read_b64_tr_b16 v[76:77],v8 offset:8200
ds_read_b64_tr_b16 v[78:79],v8 offset:8712
s_waitcnt lgkmcnt(0)
s_waitcnt vmcnt(24)
s_barrier
s_add_u32 m0,s97,0
s_nop 0
buffer_load_dwordx4 v7,s[36:39],s96 offen offset:0 lds
buffer_load_dwordx4 v7,s[40:43],s96 offen offset:1024 lds
s_add_u32 s96,s96,65536
s_add_u32 m0,s97,8192
s_nop 0
buffer_load_dwordx4 v7,s[36:39],s96 offen offset:0 lds
buffer_load_dwordx4 v7,s[40:43],s96 offen offset:1024 lds
s_add_u32 s96,s96,65536
ds_read_b64_tr_b16 v[80:81],v8 offset:16384
ds_read_b64_tr_b16 v[82:83],v8 offset:16896
ds_read_b64_tr_b16 v[84:85],v8 offset:16392
ds_read_b64_tr_b16 v[86:87],v8 offset:16904
ds_read_b64_tr_b16 v[88:89],v8 offset:24576
ds_read_b64_tr_b16 v[90:91],v8 offset:25088
ds_read_b64_tr_b16 v[92:93],v8 offset:24584
ds_read_b64_tr_b16 v[94:95],v8 offset:25096
s_waitcnt lgkmcnt(0)
s_waitcnt vmcnt(24)
s_barrier
s_add_u32 m0,s97,16384
s_nop 0
buffer_load_dwordx4 v7,s[36:39],s96 offen offset:0 lds
buffer_load_dwordx4 v7,s[40:43],s96 offen offset:1024 lds
s_add_u32 s96,s96,65536
s_add_u32 m0,s97,24576
s_nop 0
buffer_load_dwordx4 v7,s[36:39],s96 offen offset:0 lds
buffer_load_dwordx4 v7,s[40:43],s96 offen offset:1024 lds
s_add_u32 s96,s96,65536
ds_read_b64_tr_b16 v[96:97],v8 offset:32768
ds_read_b64_tr_b16 v[98:99],v8 offset:33280
ds_read_b64_tr_b16 v[100:101],v8 offset:32776
ds_read_b64_tr_b16 v[102:103],v8 offset:33288
ds_read_b64_tr_b16 v[104:105],v8 offset:40960
ds_read_b64_tr_b16 v[106:107],v8 offset:41472
ds_read_b64_tr_b16 v[108:109],v8 offset:40968
ds_read_b64_tr_b16 v[110:111],v8 offset:41480
s_waitcnt lgkmcnt(0)
s_waitcnt vmcnt(24)
s_barrier
s_add_u32 m0,s97,32768
s_nop 0
buffer_load_dwordx4 v7,s[36:39],s96 offen offset:0 lds
buffer_load_dwordx4 v7,s[40:43],s96 offen offset:1024 lds
s_add_u32 s96,s96,65536
s_add_u32 m0,s97,40960
s_nop 0
buffer_load_dwordx4 v7,s[36:39],s96 offen offset:0 lds
buffer_load_dwordx4 v7,s[40:43],s96 offen offset:1024 lds
s_add_u32 s96,s96,65536
ds_read_b64_tr_b16 v[112:113],v8 offset:49152
ds_read_b64_tr_b16 v[114:115],v8 offset:49664
ds_read_b64_tr_b16 v[116:117],v8 offset:49160
ds_read_b64_tr_b16 v[118:119],v8 offset:49672
ds_read_b64_tr_b16 v[120:121],v8 offset:57344
ds_read_b64_tr_b16 v[122:123],v8 offset:57856
ds_read_b64_tr_b16 v[124:125],v8 offset:57352
ds_read_b64_tr_b16 v[126:127],v8 offset:57864
s_waitcnt lgkmcnt(0)
s_waitcnt vmcnt(24)
s_barrier
s_add_u32 m0,s97,49152
s_nop 0
buffer_load_dwordx4 v7,s[36:39],s96 offen offset:0 lds
buffer_load_dwordx4 v7,s[40:43],s96 offen offset:1024 lds
s_add_u32 s96,s96,65536
s_add_u32 m0,s97,57344
s_nop 0
buffer_load_dwordx4 v7,s[36:39],s96 offen offset:0 lds
buffer_load_dwordx4 v7,s[40:43],s96 offen offset:1024 lds
s_add_u32 s96,s96,65536
ds_read_b64_tr_b16 v[128:129],v9 offset:0
ds_read_b64_tr_b16 v[130:131],v9 offset:512
ds_read_b64_tr_b16 v[132:133],v9 offset:8
ds_read_b64_tr_b16 v[134:135],v9 offset:520
ds_read_b64_tr_b16 v[136:137],v9 offset:8192
ds_read_b64_tr_b16 v[138:139],v9 offset:8704
ds_read_b64_tr_b16 v[140:141],v9 offset:8200
ds_read_b64_tr_b16 v[142:143],v9 offset:8712
s_waitcnt lgkmcnt(0)
s_waitcnt vmcnt(24)
s_barrier
ds_read_b64_tr_b16 v[144:145],v9 offset:16384
ds_read_b64_tr_b16 v[146:147],v9 offset:16896
ds_read_b64_tr_b16 v[148:149],v9 offset:16392
ds_read_b64_tr_b16 v[150:151],v9 offset:16904
ds_read_b64_tr_b16 v[152:153],v9 offset:24576
ds_read_b64_tr_b16 v[154:155],v9 offset:25088
ds_read_b64_tr_b16 v[156:157],v9 offset:24584
ds_read_b64_tr_b16 v[158:159],v9 offset:25096
s_waitcnt lgkmcnt(0)
s_waitcnt vmcnt(20)
s_barrier
ds_read_b64_tr_b16 v[160:161],v9 offset:32768
ds_read_b64_tr_b16 v[162:163],v9 offset:33280
ds_read_b64_tr_b16 v[164:165],v9 offset:32776
ds_read_b64_tr_b16 v[166:167],v9 offset:33288
ds_read_b64_tr_b16 v[168:169],v9 offset:40960
ds_read_b64_tr_b16 v[170:171],v9 offset:41472
ds_read_b64_tr_b16 v[172:173],v9 offset:40968
ds_read_b64_tr_b16 v[174:175],v9 offset:41480
s_waitcnt lgkmcnt(0)
s_waitcnt vmcnt(16)
s_barrier
ds_read_b64_tr_b16 v[176:177],v9 offset:49152
ds_read_b64_tr_b16 v[178:179],v9 offset:49664
ds_read_b64_tr_b16 v[180:181],v9 offset:49160
ds_read_b64_tr_b16 v[182:183],v9 offset:49672
ds_read_b64_tr_b16 v[184:185],v9 offset:57344
ds_read_b64_tr_b16 v[186:187],v9 offset:57856
ds_read_b64_tr_b16 v[188:189],v9 offset:57352
ds_read_b64_tr_b16 v[190:191],v9 offset:57864
s_waitcnt lgkmcnt(0)
s_waitcnt vmcnt(12)
s_barrier
ds_read_b64_tr_b16 v[192:193],v8 offset:0
ds_read_b64_tr_b16 v[194:195],v8 offset:512
ds_read_b64_tr_b16 v[196:197],v8 offset:8
ds_read_b64_tr_b16 v[198:199],v8 offset:520
ds_read_b64_tr_b16 v[200:201],v8 offset:8192
ds_read_b64_tr_b16 v[202:203],v8 offset:8704
ds_read_b64_tr_b16 v[204:205],v8 offset:8200
ds_read_b64_tr_b16 v[206:207],v8 offset:8712
s_waitcnt lgkmcnt(0)
s_waitcnt vmcnt(8)
s_barrier
ds_read_b64_tr_b16 v[208:209],v8 offset:16384
ds_read_b64_tr_b16 v[210:211],v8 offset:16896
ds_read_b64_tr_b16 v[212:213],v8 offset:16392
ds_read_b64_tr_b16 v[214:215],v8 offset:16904
ds_read_b64_tr_b16 v[216:217],v8 offset:24576
ds_read_b64_tr_b16 v[218:219],v8 offset:25088
ds_read_b64_tr_b16 v[220:221],v8 offset:24584
ds_read_b64_tr_b16 v[222:223],v8 offset:25096
s_waitcnt lgkmcnt(0)
s_waitcnt vmcnt(4)
s_barrier
ds_read_b64_tr_b16 v[224:225],v8 offset:32768
ds_read_b64_tr_b16 v[226:227],v8 offset:33280
ds_read_b64_tr_b16 v[228:229],v8 offset:32776
ds_read_b64_tr_b16 v[230:231],v8 offset:33288
ds_read_b64_tr_b16 v[232:233],v8 offset:40960
ds_read_b64_tr_b16 v[234:235],v8 offset:41472
ds_read_b64_tr_b16 v[236:237],v8 offset:40968
ds_read_b64_tr_b16 v[238:239],v8 offset:41480
s_waitcnt lgkmcnt(0)
s_waitcnt vmcnt(0)
s_barrier
ds_read_b64_tr_b16 v[240:241],v8 offset:49152
ds_read_b64_tr_b16 v[242:243],v8 offset:49664
ds_read_b64_tr_b16 v[244:245],v8 offset:49160
ds_read_b64_tr_b16 v[246:247],v8 offset:49672
ds_read_b64_tr_b16 v[248:249],v8 offset:57344
ds_read_b64_tr_b16 v[250:251],v8 offset:57856
ds_read_b64_tr_b16 v[252:253],v8 offset:57352
ds_read_b64_tr_b16 v[254:255],v8 offset:57864
s_waitcnt lgkmcnt(0)
s_barrier
v_mov_b32 v8,s54
v_mov_b32 v9,s55
s_lshl2_add_u32 s48,s29,s26
s_mul_i32 s63,s48,14336
s_add_u32 s48,s29,18
s_mul_i32 s49,s29,72
s_add_u32 s49,s49,128
v_lshl_add_u32 v0,v1,2,s49
s_add_u32 s49,s50,280
v_writelane_b32 v0,s49,63
v_lshrrev_b32 v0,2,v0
v_lshlrev_b32 v0,7,v0
v_mov_b32 v3,2375680
v_writelane_b32 v3,0,63
v_add_u32 v0,v3,v0
v_cmp_ge_u32 vcc,s48,v1
s_andn2_b32 s49,s29,s25
s_lshl_b32 s49,s49,31
s_nop 4
s_or_b32 vcc_hi,vcc_hi,s49
s_nop 4
v_mov_b32 v3,0x7fff0000
v_cndmask_b32 v0,v3,v0,vcc
v_cndmask_b32 v8,0,v8,vcc
v_cndmask_b32 v9,0,v9,vcc
v_mov_b32 v3,v21
s_add_u32 s63,s63,s57
s_add_u32 s63,s63,4718592
s_add_u32 s66,s57,2523136
s_mul_i32 s64,s26,14336
s_lshl_b32 s74,s26,11
s_add_u32 s74,s74,114688
v_add_u32 v5,s74,v2
v_add_u32 v4,57344,v2
s_add_u32 s44,s20,0x0
s_addc_u32 s45,s21,0x0
s_and_b32 s45,s45,0xffff
s_mov_b32 s46,0x400000
s_mov_b32 s47,0x20000
s_mov_b32 s62,16
s_sub_u32 s58,111,s28
s_lshr_b32 s58,s58,2
s_lshl_b32 s59,s28,20
s_mov_b32 s75,32768
s_mul_i32 s48,s25,65536
s_add_u32 s59,s59,s48
s_cmp_lg_u32 s25,0
s_cmov_b32 s62,0
s_cmov_b32 s58,-1
s_cbranch_scc1 .L_x
s_cmp_lg_u32 s29,0
s_cbranch_scc1 .L_dh
s_cmp_lg_u32 s25,0
s_cbranch_scc1 .L_dn
s_lshl_b32 s48,s27,7
s_add_u32 s48,s48,s66
s_add_u32 s48,s48,8960
v_add_u32 v10,34,v1
v_mul_u32_u24 v10,1187840,v10
v_writelane_b32 v10,0,32
s_cmp_eq_u32 s26,0
s_cselect_b32 s49,33,0
v_cmp_gt_u32 vcc,s49,v1
v_mov_b32 v11,0x7fff0000
v_mov_b32 v12,s54
v_mov_b32 v13,s55
v_cndmask_b32 v10,v11,v10,vcc
buffer_store_dwordx2 v[12:13],v10,s[32:35],s48 offen sc1
.L_dn:
v_lshrrev_b32 v10,4,v1
v_and_b32 v1,15,v1
s_lshl_b32 s48,s50,6
v_lshl_add_u32 v1,v1,2,s48
v_lshl_add_u32 v1,v10,12,v1
s_add_u32 s71,s70,4681728
s_sub_u32 s68,s65,2375680
s_and_b32 s65,s59,0x3f8000
s_cmp_lg_u32 s26,1
s_cbranch_scc1 .L_skdl
s_mov_b32 s48,20000
s_branch .L_rfdl
.L_rldl:
s_sleep 2
.L_rfdl:
buffer_load_dwordx2 v[6:7],v0,s[0:3],s66 offen sc1
s_sub_u32 s48,s48,1
s_cbranch_scc1 .L_end
s_waitcnt vmcnt(0)
v_cmp_ne_u64 vcc,v[8:9],v[6:7]
s_cbranch_vccnz .L_rldl
.L_skdl:
s_waitcnt vmcnt(0)
s_barrier
s_add_u32 s66,s66,s30
s_add_u32 m0,s64,0
s_nop 0
buffer_load_dwordx4 v2,s[32:35],s63 offen offset:0 sc1 lds
buffer_load_dwordx4 v2,s[32:35],s63 offen offset:1024 sc1 lds
buffer_load_dwordx4 v2,s[32:35],s63 offen offset:2048 sc1 lds
buffer_load_dwordx4 v2,s[32:35],s63 offen offset:3072 sc1 lds
s_add_u32 s63,s63,4096
s_add_u32 m0,s64,4096
s_nop 0
buffer_load_dwordx4 v2,s[32:35],s63 offen offset:0 sc1 lds
buffer_load_dwordx4 v2,s[32:35],s63 offen offset:1024 sc1 lds
buffer_load_dwordx4 v2,s[32:35],s63 offen offset:2048 sc1 lds
buffer_load_dwordx4 v2,s[32:35],s63 offen offset:3072 sc1 lds
s_add_u32 s63,s63,4096
s_add_u32 m0,s64,8192
s_nop 0
buffer_load_dwordx4 v2,s[32:35],s63 offen offset:0 sc1 lds
buffer_load_dwordx4 v2,s[32:35],s63 offen offset:1024 sc1 lds
buffer_load_dwordx4 v2,s[32:35],s63 offen offset:2048 sc1 lds
buffer_load_dwordx4 v2,s[32:35],s63 offen offset:3072 sc1 lds
s_add_u32 s63,s63,4096
s_add_u32 m0,s64,12288
s_nop 0
buffer_load_dwordx4 v2,s[32:35],s63 offen offset:0 sc1 lds
buffer_load_dwordx4 v2,s[32:35],s63 offen offset:1024 sc1 lds
s_add_u32 s63,s63,s31
buffer_load_dwordx4 v[12:15],v2,s[32:35],s71 offen sc1
s_add_u32 s71,s71,s30
buffer_load_dwordx2 v[6:7],v0,s[0:3],s66 offen sc1
s_waitcnt vmcnt(0)
s_barrier
v_lshlrev_b32 v16,16,v12
v_and_b32 v17,0xffff0000,v12
v_lshlrev_b32 v18,16,v13
v_and_b32 v19,0xffff0000,v13
v_lshlrev_b32 v20,16,v14
v_and_b32 v21,0xffff0000,v14
v_lshlrev_b32 v22,16,v15
v_and_b32 v23,0xffff0000,v15
ds_read_b128 v[32:35],v2 offset:0
ds_read_b128 v[36:39],v2 offset:14336
ds_read_b128 v[40:43],v2 offset:28672
ds_read_b128 v[44:47],v2 offset:43008
ds_read_b128 v[48:51],v2 offset:1024
ds_read_b128 v[52:55],v2 offset:15360
.L_dlgo:
s_cmp_eq_u32 s26,1
s_cbranch_scc1 .L_dlv1
s_cmp_eq_u32 s26,2
s_cbranch_scc1 .L_dlv2
s_cmp_eq_u32 s26,3
s_cbranch_scc1 .L_dlv3
.p2align 6
.L_dlv0:
.L_dll_0:
s_waitcnt lgkmcnt(4)
v_mfma_f32_16x16x32_bf16 v[16:19],a[0:3],v[32:35],v[16:19]
ds_read_b128 v[56:59],v2 offset:29696
v_mfma_f32_16x16x32_bf16 v[16:19],a[112:115],v[36:39],v[16:19]
v_mfma_f32_16x16x32_bf16 v[20:23],a[116:119],v[36:39],v[20:23]
ds_read_b128 v[60:63],v2 offset:44032
v_mfma_f32_16x16x32_bf16 v[20:23],a[4:7],v[32:35],v[20:23]
s_add_u32 s68,s68,1187840
s_waitcnt lgkmcnt(4)
v_mfma_f32_16x16x32_bf16 v[20:23],a[228:231],v[40:43],v[20:23]
ds_read_b128 v[32:35],v2 offset:2048
v_mfma_f32_16x16x32_bf16 v[20:23],v[148:151],v[44:47],v[20:23]
buffer_store_dwordx4 v[28:31],v2,s[32:35],s68 offen offset:1024 sc1
v_mfma_f32_16x16x32_bf16 v[16:19],v[144:147],v[44:47],v[16:19]
ds_read_b128 v[36:39],v2 offset:16384
v_mfma_f32_16x16x32_bf16 v[16:19],a[224:227],v[40:43],v[16:19]
s_waitcnt lgkmcnt(4)
v_mfma_f32_16x16x32_bf16 v[16:19],a[8:11],v[48:51],v[16:19]
ds_read_b128 v[40:43],v2 offset:30720
v_mfma_f32_16x16x32_bf16 v[16:19],a[120:123],v[52:55],v[16:19]
buffer_store_dwordx4 v[24:27],v2,s[32:35],s68 offen sc1
s_mov_b32 s67,s68
)#"
R"#(
v_mfma_f32_16x16x32_bf16 v[20:23],a[124:127],v[52:55],v[20:23]
ds_read_b128 v[44:47],v2 offset:45056
v_mfma_f32_16x16x32_bf16 v[20:23],a[12:15],v[48:51],v[20:23]
s_waitcnt lgkmcnt(4)
v_mfma_f32_16x16x32_bf16 v[20:23],a[236:239],v[56:59],v[20:23]
ds_read_b128 v[48:51],v2 offset:3072
v_mfma_f32_16x16x32_bf16 v[20:23],v[156:159],v[60:63],v[20:23]
v_mfma_f32_16x16x32_bf16 v[16:19],v[152:155],v[60:63],v[16:19]
ds_read_b128 v[52:55],v2 offset:17408
v_mfma_f32_16x16x32_bf16 v[16:19],a[232:235],v[56:59],v[16:19]
s_waitcnt lgkmcnt(4)
v_mfma_f32_16x16x32_bf16 v[16:19],a[16:19],v[32:35],v[16:19]
ds_read_b128 v[56:59],v2 offset:31744
v_mfma_f32_16x16x32_bf16 v[16:19],a[128:131],v[36:39],v[16:19]
v_mfma_f32_16x16x32_bf16 v[20:23],a[132:135],v[36:39],v[20:23]
ds_read_b128 v[60:63],v2 offset:46080
v_mfma_f32_16x16x32_bf16 v[20:23],a[20:23],v[32:35],v[20:23]
s_waitcnt lgkmcnt(4)
s_waitcnt vmcnt(3)
s_barrier
v_mfma_f32_16x16x32_bf16 v[20:23],a[244:247],v[40:43],v[20:23]
ds_read_b128 v[32:35],v2 offset:4096
s_add_u32 m0,s64,57344
v_mfma_f32_16x16x32_bf16 v[20:23],v[164:167],v[44:47],v[20:23]
buffer_load_dwordx4 v2,s[32:35],s63 offen offset:0 sc1 lds
v_mfma_f32_16x16x32_bf16 v[16:19],v[160:163],v[44:47],v[16:19]
ds_read_b128 v[36:39],v2 offset:18432
v_mfma_f32_16x16x32_bf16 v[16:19],a[240:243],v[40:43],v[16:19]
s_waitcnt lgkmcnt(4)
v_mfma_f32_16x16x32_bf16 v[16:19],a[24:27],v[48:51],v[16:19]
ds_read_b128 v[40:43],v2 offset:32768
v_mfma_f32_16x16x32_bf16 v[16:19],a[136:139],v[52:55],v[16:19]
v_mfma_f32_16x16x32_bf16 v[20:23],a[140:143],v[52:55],v[20:23]
ds_read_b128 v[44:47],v2 offset:47104
v_mfma_f32_16x16x32_bf16 v[20:23],a[28:31],v[48:51],v[20:23]
s_waitcnt lgkmcnt(4)
v_mfma_f32_16x16x32_bf16 v[20:23],a[252:255],v[56:59],v[20:23]
ds_read_b128 v[48:51],v2 offset:5120
v_mfma_f32_16x16x32_bf16 v[20:23],v[172:175],v[60:63],v[20:23]
buffer_load_dwordx4 v2,s[32:35],s63 offen offset:1024 sc1 lds
v_mfma_f32_16x16x32_bf16 v[16:19],v[168:171],v[60:63],v[16:19]
ds_read_b128 v[52:55],v2 offset:19456
v_mfma_f32_16x16x32_bf16 v[16:19],a[248:251],v[56:59],v[16:19]
s_waitcnt lgkmcnt(4)
v_mfma_f32_16x16x32_bf16 v[16:19],a[32:35],v[32:35],v[16:19]
ds_read_b128 v[56:59],v2 offset:33792
v_mfma_f32_16x16x32_bf16 v[16:19],a[144:147],v[36:39],v[16:19]
buffer_load_dwordx4 v2,s[32:35],s63 offen offset:2048 sc1 lds
v_mfma_f32_16x16x32_bf16 v[20:23],a[148:151],v[36:39],v[20:23]
ds_read_b128 v[60:63],v2 offset:48128
v_mfma_f32_16x16x32_bf16 v[20:23],a[36:39],v[32:35],v[20:23]
s_waitcnt lgkmcnt(4)
v_mfma_f32_16x16x32_bf16 v[20:23],v[68:71],v[40:43],v[20:23]
ds_read_b128 v[32:35],v2 offset:6144
v_mfma_f32_16x16x32_bf16 v[20:23],v[180:183],v[44:47],v[20:23]
buffer_load_dwordx4 v2,s[32:35],s63 offen offset:3072 sc1 lds
v_mfma_f32_16x16x32_bf16 v[16:19],v[176:179],v[44:47],v[16:19]
ds_read_b128 v[36:39],v2 offset:20480
v_mfma_f32_16x16x32_bf16 v[16:19],v[64:67],v[40:43],v[16:19]
s_waitcnt lgkmcnt(4)
v_mfma_f32_16x16x32_bf16 v[16:19],a[40:43],v[48:51],v[16:19]
ds_read_b128 v[40:43],v2 offset:34816
v_mfma_f32_16x16x32_bf16 v[16:19],a[152:155],v[52:55],v[16:19]
buffer_load_dwordx4 v[12:15],v2,s[32:35],s71 offen sc1
s_add_u32 s71,s71,s30
v_mfma_f32_16x16x32_bf16 v[20:23],a[156:159],v[52:55],v[20:23]
ds_read_b128 v[44:47],v2 offset:49152
s_add_u32 s63,s63,4096
v_mfma_f32_16x16x32_bf16 v[20:23],a[44:47],v[48:51],v[20:23]
s_add_u32 m0,s64,61440
s_waitcnt lgkmcnt(4)
v_mfma_f32_16x16x32_bf16 v[20:23],v[76:79],v[56:59],v[20:23]
ds_read_b128 v[48:51],v2 offset:7168
v_mfma_f32_16x16x32_bf16 v[20:23],v[188:191],v[60:63],v[20:23]
buffer_load_dwordx4 v2,s[32:35],s63 offen offset:0 sc1 lds
v_mfma_f32_16x16x32_bf16 v[16:19],v[184:187],v[60:63],v[16:19]
ds_read_b128 v[52:55],v2 offset:21504
v_mfma_f32_16x16x32_bf16 v[16:19],v[72:75],v[56:59],v[16:19]
s_waitcnt lgkmcnt(4)
v_mfma_f32_16x16x32_bf16 v[16:19],a[48:51],v[32:35],v[16:19]
ds_read_b128 v[56:59],v2 offset:35840
v_mfma_f32_16x16x32_bf16 v[16:19],a[160:163],v[36:39],v[16:19]
buffer_load_dwordx4 v2,s[32:35],s63 offen offset:1024 sc1 lds
v_mfma_f32_16x16x32_bf16 v[20:23],a[164:167],v[36:39],v[20:23]
ds_read_b128 v[60:63],v2 offset:50176
v_mfma_f32_16x16x32_bf16 v[20:23],a[52:55],v[32:35],v[20:23]
s_waitcnt lgkmcnt(4)
v_mfma_f32_16x16x32_bf16 v[20:23],v[84:87],v[40:43],v[20:23]
ds_read_b128 v[32:35],v2 offset:8192
v_mfma_f32_16x16x32_bf16 v[20:23],v[196:199],v[44:47],v[20:23]
buffer_load_dwordx4 v2,s[32:35],s63 offen offset:2048 sc1 lds
v_mfma_f32_16x16x32_bf16 v[16:19],v[192:195],v[44:47],v[16:19]
ds_read_b128 v[36:39],v2 offset:22528
v_mfma_f32_16x16x32_bf16 v[16:19],v[80:83],v[40:43],v[16:19]
s_waitcnt lgkmcnt(4)
v_mfma_f32_16x16x32_bf16 v[16:19],a[56:59],v[48:51],v[16:19]
ds_read_b128 v[40:43],v2 offset:36864
v_mfma_f32_16x16x32_bf16 v[16:19],a[168:171],v[52:55],v[16:19]
buffer_load_dwordx4 v2,s[32:35],s63 offen offset:3072 sc1 lds
v_mfma_f32_16x16x32_bf16 v[20:23],a[172:175],v[52:55],v[20:23]
ds_read_b128 v[44:47],v2 offset:51200
v_mfma_f32_16x16x32_bf16 v[20:23],a[60:63],v[48:51],v[20:23]
s_waitcnt lgkmcnt(4)
v_mfma_f32_16x16x32_bf16 v[20:23],v[92:95],v[56:59],v[20:23]
ds_read_b128 v[48:51],v2 offset:9216
v_mfma_f32_16x16x32_bf16 v[20:23],v[204:207],v[60:63],v[20:23]
s_add_u32 s63,s63,4096
s_add_u32 m0,s64,65536
v_mfma_f32_16x16x32_bf16 v[16:19],v[200:203],v[60:63],v[16:19]
ds_read_b128 v[52:55],v2 offset:23552
v_mfma_f32_16x16x32_bf16 v[16:19],v[88:91],v[56:59],v[16:19]
s_waitcnt lgkmcnt(4)
v_mfma_f32_16x16x32_bf16 v[16:19],a[64:67],v[32:35],v[16:19]
ds_read_b128 v[56:59],v2 offset:37888
v_mfma_f32_16x16x32_bf16 v[16:19],a[176:179],v[36:39],v[16:19]
buffer_load_dwordx4 v2,s[32:35],s63 offen offset:0 sc1 lds
v_mfma_f32_16x16x32_bf16 v[20:23],a[180:183],v[36:39],v[20:23]
ds_read_b128 v[60:63],v2 offset:52224
v_mfma_f32_16x16x32_bf16 v[20:23],a[68:71],v[32:35],v[20:23]
s_waitcnt lgkmcnt(4)
v_mfma_f32_16x16x32_bf16 v[20:23],v[100:103],v[40:43],v[20:23]
ds_read_b128 v[32:35],v2 offset:10240
v_mfma_f32_16x16x32_bf16 v[20:23],v[212:215],v[44:47],v[20:23]
buffer_load_dwordx4 v2,s[32:35],s63 offen offset:1024 sc1 lds
v_mfma_f32_16x16x32_bf16 v[16:19],v[208:211],v[44:47],v[16:19]
ds_read_b128 v[36:39],v2 offset:24576
v_mfma_f32_16x16x32_bf16 v[16:19],v[96:99],v[40:43],v[16:19]
s_waitcnt lgkmcnt(4)
v_mfma_f32_16x16x32_bf16 v[16:19],a[72:75],v[48:51],v[16:19]
ds_read_b128 v[40:43],v2 offset:38912
v_mfma_f32_16x16x32_bf16 v[16:19],a[184:187],v[52:55],v[16:19]
buffer_load_dwordx4 v2,s[32:35],s63 offen offset:2048 sc1 lds
v_mfma_f32_16x16x32_bf16 v[20:23],a[188:191],v[52:55],v[20:23]
ds_read_b128 v[44:47],v2 offset:53248
v_mfma_f32_16x16x32_bf16 v[20:23],a[76:79],v[48:51],v[20:23]
s_waitcnt lgkmcnt(4)
v_mfma_f32_16x16x32_bf16 v[20:23],v[108:111],v[56:59],v[20:23]
ds_read_b128 v[48:51],v2 offset:11264
v_mfma_f32_16x16x32_bf16 v[20:23],v[220:223],v[60:63],v[20:23]
v_mfma_f32_16x16x32_bf16 v[16:19],v[216:219],v[60:63],v[16:19]
ds_read_b128 v[52:55],v2 offset:25600
v_mfma_f32_16x16x32_bf16 v[16:19],v[104:107],v[56:59],v[16:19]
s_waitcnt lgkmcnt(4)
v_mfma_f32_16x16x32_bf16 v[16:19],a[80:83],v[32:35],v[16:19]
ds_read_b128 v[56:59],v2 offset:39936
v_mfma_f32_16x16x32_bf16 v[16:19],a[192:195],v[36:39],v[16:19]
buffer_load_dwordx4 v2,s[32:35],s63 offen offset:3072 sc1 lds
s_add_u32 s63,s63,4096
v_mfma_f32_16x16x32_bf16 v[20:23],a[196:199],v[36:39],v[20:23]
ds_read_b128 v[60:63],v2 offset:54272
s_add_u32 m0,s64,69632
v_mfma_f32_16x16x32_bf16 v[20:23],a[84:87],v[32:35],v[20:23]
s_waitcnt lgkmcnt(4)
v_mfma_f32_16x16x32_bf16 v[20:23],v[116:119],v[40:43],v[20:23]
ds_read_b128 v[32:35],v2 offset:12288
v_mfma_f32_16x16x32_bf16 v[20:23],v[228:231],v[44:47],v[20:23]
buffer_load_dwordx4 v2,s[32:35],s63 offen offset:0 sc1 lds
v_mfma_f32_16x16x32_bf16 v[16:19],v[224:227],v[44:47],v[16:19]
ds_read_b128 v[36:39],v2 offset:26624
v_mfma_f32_16x16x32_bf16 v[16:19],v[112:115],v[40:43],v[16:19]
s_waitcnt lgkmcnt(4)
v_mfma_f32_16x16x32_bf16 v[16:19],a[88:91],v[48:51],v[16:19]
ds_read_b128 v[40:43],v2 offset:40960
v_mfma_f32_16x16x32_bf16 v[16:19],a[200:203],v[52:55],v[16:19]
buffer_load_dwordx4 v2,s[32:35],s63 offen offset:1024 sc1 lds
s_add_u32 s63,s63,s31
v_mfma_f32_16x16x32_bf16 v[20:23],a[204:207],v[52:55],v[20:23]
ds_read_b128 v[44:47],v2 offset:55296
v_mfma_f32_16x16x32_bf16 v[20:23],a[92:95],v[48:51],v[20:23]
s_waitcnt lgkmcnt(4)
v_mfma_f32_16x16x32_bf16 v[20:23],v[124:127],v[56:59],v[20:23]
ds_read_b128 v[48:51],v2 offset:13312
s_waitcnt vmcnt(10)
v_mfma_f32_16x16x32_bf16 v[20:23],v[236:239],v[60:63],v[20:23]
v_lshlrev_b32 v24,16,v12
v_and_b32 v25,0xffff0000,v12
v_mfma_f32_16x16x32_bf16 v[16:19],v[232:235],v[60:63],v[16:19]
ds_read_b128 v[52:55],v2 offset:27648
v_lshlrev_b32 v26,16,v13
v_mfma_f32_16x16x32_bf16 v[16:19],v[120:123],v[56:59],v[16:19]
v_and_b32 v27,0xffff0000,v13
s_waitcnt lgkmcnt(4)
v_mfma_f32_16x16x32_bf16 v[16:19],a[96:99],v[32:35],v[16:19]
ds_read_b128 v[56:59],v2 offset:41984
v_lshlrev_b32 v28,16,v14
v_mfma_f32_16x16x32_bf16 v[16:19],a[208:211],v[36:39],v[16:19]
v_and_b32 v29,0xffff0000,v14
v_lshlrev_b32 v30,16,v15
v_mfma_f32_16x16x32_bf16 v[20:23],a[212:215],v[36:39],v[20:23]
ds_read_b128 v[60:63],v2 offset:56320
v_and_b32 v31,0xffff0000,v15
v_mfma_f32_16x16x32_bf16 v[20:23],a[100:103],v[32:35],v[20:23]
s_waitcnt lgkmcnt(4)
s_barrier
v_mfma_f32_16x16x32_bf16 v[20:23],v[132:135],v[40:43],v[20:23]
ds_read_b128 v[32:35],v4 offset:0
v_mfma_f32_16x16x32_bf16 v[20:23],v[244:247],v[44:47],v[20:23]
v_mfma_f32_16x16x32_bf16 v[16:19],v[240:243],v[44:47],v[16:19]
ds_read_b128 v[36:39],v4 offset:14336
v_mfma_f32_16x16x32_bf16 v[16:19],v[128:131],v[40:43],v[16:19]
s_waitcnt lgkmcnt(4)
v_mfma_f32_16x16x32_bf16 v[16:19],a[104:107],v[48:51],v[16:19]
ds_read_b128 v[40:43],v4 offset:28672
v_mfma_f32_16x16x32_bf16 v[16:19],a[216:219],v[52:55],v[16:19]
buffer_store_dwordx2 v[8:9],v2,s[92:95],s67 idxen sc1
v_mfma_f32_16x16x32_bf16 v[20:23],a[220:223],v[52:55],v[20:23]
ds_read_b128 v[44:47],v4 offset:43008
v_mfma_f32_16x16x32_bf16 v[20:23],a[108:111],v[48:51],v[20:23]
s_waitcnt lgkmcnt(4)
v_mfma_f32_16x16x32_bf16 v[20:23],v[140:143],v[56:59],v[20:23]
ds_read_b128 v[48:51],v4 offset:1024
v_mfma_f32_16x16x32_bf16 v[20:23],v[252:255],v[60:63],v[20:23]
v_mfma_f32_16x16x32_bf16 v[16:19],v[248:251],v[60:63],v[16:19]
ds_read_b128 v[52:55],v4 offset:15360
s_sub_u32 s62,s62,1
v_mfma_f32_16x16x32_bf16 v[16:19],v[136:139],v[56:59],v[16:19]
s_cbranch_scc1 .L_dlb_0
s_waitcnt lgkmcnt(4)
v_mfma_f32_16x16x32_bf16 v[24:27],a[0:3],v[32:35],v[24:27]
ds_read_b128 v[56:59],v4 offset:29696
v_mfma_f32_16x16x32_bf16 v[24:27],a[112:115],v[36:39],v[24:27]
v_mfma_f32_16x16x32_bf16 v[28:31],a[116:119],v[36:39],v[28:31]
ds_read_b128 v[60:63],v4 offset:44032
v_mfma_f32_16x16x32_bf16 v[28:31],a[4:7],v[32:35],v[28:31]
s_add_u32 s68,s68,1187840
s_waitcnt lgkmcnt(4)
v_mfma_f32_16x16x32_bf16 v[28:31],a[228:231],v[40:43],v[28:31]
ds_read_b128 v[32:35],v4 offset:2048
v_mfma_f32_16x16x32_bf16 v[28:31],v[148:151],v[44:47],v[28:31]
buffer_store_dwordx4 v[20:23],v2,s[32:35],s68 offen offset:1024 sc1
v_mfma_f32_16x16x32_bf16 v[24:27],v[144:147],v[44:47],v[24:27]
ds_read_b128 v[36:39],v4 offset:16384
v_mfma_f32_16x16x32_bf16 v[24:27],a[224:227],v[40:43],v[24:27]
s_waitcnt lgkmcnt(4)
v_mfma_f32_16x16x32_bf16 v[24:27],a[8:11],v[48:51],v[24:27]
ds_read_b128 v[40:43],v4 offset:30720
v_mfma_f32_16x16x32_bf16 v[24:27],a[120:123],v[52:55],v[24:27]
buffer_store_dwordx4 v[16:19],v2,s[32:35],s68 offen sc1
s_mov_b32 s67,s68
v_mfma_f32_16x16x32_bf16 v[28:31],a[124:127],v[52:55],v[28:31]
ds_read_b128 v[44:47],v4 offset:45056
v_mfma_f32_16x16x32_bf16 v[28:31],a[12:15],v[48:51],v[28:31]
s_waitcnt lgkmcnt(4)
v_mfma_f32_16x16x32_bf16 v[28:31],a[236:239],v[56:59],v[28:31]
ds_read_b128 v[48:51],v4 offset:3072
v_mfma_f32_16x16x32_bf16 v[28:31],v[156:159],v[60:63],v[28:31]
v_mfma_f32_16x16x32_bf16 v[24:27],v[152:155],v[60:63],v[24:27]
ds_read_b128 v[52:55],v4 offset:17408
v_mfma_f32_16x16x32_bf16 v[24:27],a[232:235],v[56:59],v[24:27]
s_waitcnt lgkmcnt(4)
v_mfma_f32_16x16x32_bf16 v[24:27],a[16:19],v[32:35],v[24:27]
ds_read_b128 v[56:59],v4 offset:31744
v_mfma_f32_16x16x32_bf16 v[24:27],a[128:131],v[36:39],v[24:27]
v_mfma_f32_16x16x32_bf16 v[28:31],a[132:135],v[36:39],v[28:31]
ds_read_b128 v[60:63],v4 offset:46080
v_mfma_f32_16x16x32_bf16 v[28:31],a[20:23],v[32:35],v[28:31]
s_waitcnt lgkmcnt(4)
s_waitcnt vmcnt(3)
s_barrier
v_mfma_f32_16x16x32_bf16 v[28:31],a[244:247],v[40:43],v[28:31]
ds_read_b128 v[32:35],v4 offset:4096
s_add_u32 m0,s64,0
v_mfma_f32_16x16x32_bf16 v[28:31],v[164:167],v[44:47],v[28:31]
buffer_load_dwordx4 v2,s[32:35],s63 offen offset:0 sc1 lds
v_mfma_f32_16x16x32_bf16 v[24:27],v[160:163],v[44:47],v[24:27]
ds_read_b128 v[36:39],v4 offset:18432
v_mfma_f32_16x16x32_bf16 v[24:27],a[240:243],v[40:43],v[24:27]
s_waitcnt lgkmcnt(4)
v_mfma_f32_16x16x32_bf16 v[24:27],a[24:27],v[48:51],v[24:27]
ds_read_b128 v[40:43],v4 offset:32768
v_mfma_f32_16x16x32_bf16 v[24:27],a[136:139],v[52:55],v[24:27]
v_mfma_f32_16x16x32_bf16 v[28:31],a[140:143],v[52:55],v[28:31]
ds_read_b128 v[44:47],v4 offset:47104
v_mfma_f32_16x16x32_bf16 v[28:31],a[28:31],v[48:51],v[28:31]
s_waitcnt lgkmcnt(4)
v_mfma_f32_16x16x32_bf16 v[28:31],a[252:255],v[56:59],v[28:31]
ds_read_b128 v[48:51],v4 offset:5120
v_mfma_f32_16x16x32_bf16 v[28:31],v[172:175],v[60:63],v[28:31]
buffer_load_dwordx4 v2,s[32:35],s63 offen offset:1024 sc1 lds
v_mfma_f32_16x16x32_bf16 v[24:27],v[168:171],v[60:63],v[24:27]
ds_read_b128 v[52:55],v4 offset:19456
v_mfma_f32_16x16x32_bf16 v[24:27],a[248:251],v[56:59],v[24:27]
s_waitcnt lgkmcnt(4)
v_mfma_f32_16x16x32_bf16 v[24:27],a[32:35],v[32:35],v[24:27]
ds_read_b128 v[56:59],v4 offset:33792
v_mfma_f32_16x16x32_bf16 v[24:27],a[144:147],v[36:39],v[24:27]
buffer_load_dwordx4 v2,s[32:35],s63 offen offset:2048 sc1 lds
v_mfma_f32_16x16x32_bf16 v[28:31],a[148:151],v[36:39],v[28:31]
ds_read_b128 v[60:63],v4 offset:48128
v_mfma_f32_16x16x32_bf16 v[28:31],a[36:39],v[32:35],v[28:31]
s_waitcnt lgkmcnt(4)
v_mfma_f32_16x16x32_bf16 v[28:31],v[68:71],v[40:43],v[28:31]
ds_read_b128 v[32:35],v4 offset:6144
v_mfma_f32_16x16x32_bf16 v[28:31],v[180:183],v[44:47],v[28:31]
buffer_load_dwordx4 v2,s[32:35],s63 offen offset:3072 sc1 lds
v_mfma_f32_16x16x32_bf16 v[24:27],v[176:179],v[44:47],v[24:27]
ds_read_b128 v[36:39],v4 offset:20480
v_mfma_f32_16x16x32_bf16 v[24:27],v[64:67],v[40:43],v[24:27]
s_waitcnt lgkmcnt(4)
v_mfma_f32_16x16x32_bf16 v[24:27],a[40:43],v[48:51],v[24:27]
ds_read_b128 v[40:43],v4 offset:34816
v_mfma_f32_16x16x32_bf16 v[24:27],a[152:155],v[52:55],v[24:27]
buffer_load_dwordx4 v[12:15],v2,s[32:35],s71 offen sc1
s_add_u32 s71,s71,s30
v_mfma_f32_16x16x32_bf16 v[28:31],a[156:159],v[52:55],v[28:31]
ds_read_b128 v[44:47],v4 offset:49152
s_add_u32 s63,s63,4096
v_mfma_f32_16x16x32_bf16 v[28:31],a[44:47],v[48:51],v[28:31]
s_add_u32 m0,s64,4096
s_waitcnt lgkmcnt(4)
v_mfma_f32_16x16x32_bf16 v[28:31],v[76:79],v[56:59],v[28:31]
ds_read_b128 v[48:51],v4 offset:7168
v_mfma_f32_16x16x32_bf16 v[28:31],v[188:191],v[60:63],v[28:31]
buffer_load_dwordx4 v2,s[32:35],s63 offen offset:0 sc1 lds
v_mfma_f32_16x16x32_bf16 v[24:27],v[184:187],v[60:63],v[24:27]
ds_read_b128 v[52:55],v4 offset:21504
v_mfma_f32_16x16x32_bf16 v[24:27],v[72:75],v[56:59],v[24:27]
s_waitcnt lgkmcnt(4)
v_mfma_f32_16x16x32_bf16 v[24:27],a[48:51],v[32:35],v[24:27]
ds_read_b128 v[56:59],v4 offset:35840
v_mfma_f32_16x16x32_bf16 v[24:27],a[160:163],v[36:39],v[24:27]
buffer_load_dwordx4 v2,s[32:35],s63 offen offset:1024 sc1 lds
v_mfma_f32_16x16x32_bf16 v[28:31],a[164:167],v[36:39],v[28:31]
ds_read_b128 v[60:63],v4 offset:50176
v_mfma_f32_16x16x32_bf16 v[28:31],a[52:55],v[32:35],v[28:31]
s_waitcnt lgkmcnt(4)
v_mfma_f32_16x16x32_bf16 v[28:31],v[84:87],v[40:43],v[28:31]
ds_read_b128 v[32:35],v4 offset:8192
v_mfma_f32_16x16x32_bf16 v[28:31],v[196:199],v[44:47],v[28:31]
buffer_load_dwordx4 v2,s[32:35],s63 offen offset:2048 sc1 lds
v_mfma_f32_16x16x32_bf16 v[24:27],v[192:195],v[44:47],v[24:27]
ds_read_b128 v[36:39],v4 offset:22528
v_mfma_f32_16x16x32_bf16 v[24:27],v[80:83],v[40:43],v[24:27]
s_waitcnt lgkmcnt(4)
v_mfma_f32_16x16x32_bf16 v[24:27],a[56:59],v[48:51],v[24:27]
ds_read_b128 v[40:43],v4 offset:36864
v_mfma_f32_16x16x32_bf16 v[24:27],a[168:171],v[52:55],v[24:27]
buffer_load_dwordx4 v2,s[32:35],s63 offen offset:3072 sc1 lds
v_mfma_f32_16x16x32_bf16 v[28:31],a[172:175],v[52:55],v[28:31]
ds_read_b128 v[44:47],v4 offset:51200
v_mfma_f32_16x16x32_bf16 v[28:31],a[60:63],v[48:51],v[28:31]
s_waitcnt lgkmcnt(4)
v_mfma_f32_16x16x32_bf16 v[28:31],v[92:95],v[56:59],v[28:31]
ds_read_b128 v[48:51],v4 offset:9216
v_mfma_f32_16x16x32_bf16 v[28:31],v[204:207],v[60:63],v[28:31]
s_add_u32 s63,s63,4096
s_add_u32 m0,s64,8192
v_mfma_f32_16x16x32_bf16 v[24:27],v[200:203],v[60:63],v[24:27]
ds_read_b128 v[52:55],v4 offset:23552
v_mfma_f32_16x16x32_bf16 v[24:27],v[88:91],v[56:59],v[24:27]
s_waitcnt lgkmcnt(4)
v_mfma_f32_16x16x32_bf16 v[24:27],a[64:67],v[32:35],v[24:27]
ds_read_b128 v[56:59],v4 offset:37888
v_mfma_f32_16x16x32_bf16 v[24:27],a[176:179],v[36:39],v[24:27]
buffer_load_dwordx4 v2,s[32:35],s63 offen offset:0 sc1 lds
v_mfma_f32_16x16x32_bf16 v[28:31],a[180:183],v[36:39],v[28:31]
ds_read_b128 v[60:63],v4 offset:52224
v_mfma_f32_16x16x32_bf16 v[28:31],a[68:71],v[32:35],v[28:31]
s_waitcnt lgkmcnt(4)
v_mfma_f32_16x16x32_bf16 v[28:31],v[100:103],v[40:43],v[28:31]
ds_read_b128 v[32:35],v4 offset:10240
v_mfma_f32_16x16x32_bf16 v[28:31],v[212:215],v[44:47],v[28:31]
buffer_load_dwordx4 v2,s[32:35],s63 offen offset:1024 sc1 lds
v_mfma_f32_16x16x32_bf16 v[24:27],v[208:211],v[44:47],v[24:27]
ds_read_b128 v[36:39],v4 offset:24576
v_mfma_f32_16x16x32_bf16 v[24:27],v[96:99],v[40:43],v[24:27]
s_waitcnt lgkmcnt(4)
v_mfma_f32_16x16x32_bf16 v[24:27],a[72:75],v[48:51],v[24:27]
ds_read_b128 v[40:43],v4 offset:38912
v_mfma_f32_16x16x32_bf16 v[24:27],a[184:187],v[52:55],v[24:27]
buffer_load_dwordx4 v2,s[32:35],s63 offen offset:2048 sc1 lds
v_mfma_f32_16x16x32_bf16 v[28:31],a[188:191],v[52:55],v[28:31]
ds_read_b128 v[44:47],v4 offset:53248
v_mfma_f32_16x16x32_bf16 v[28:31],a[76:79],v[48:51],v[28:31]
s_waitcnt lgkmcnt(4)
v_mfma_f32_16x16x32_bf16 v[28:31],v[108:111],v[56:59],v[28:31]
ds_read_b128 v[48:51],v4 offset:11264
v_mfma_f32_16x16x32_bf16 v[28:31],v[220:223],v[60:63],v[28:31]
v_mfma_f32_16x16x32_bf16 v[24:27],v[216:219],v[60:63],v[24:27]
ds_read_b128 v[52:55],v4 offset:25600
v_mfma_f32_16x16x32_bf16 v[24:27],v[104:107],v[56:59],v[24:27]
s_waitcnt lgkmcnt(4)
v_mfma_f32_16x16x32_bf16 v[24:27],a[80:83],v[32:35],v[24:27]
ds_read_b128 v[56:59],v4 offset:39936
v_mfma_f32_16x16x32_bf16 v[24:27],a[192:195],v[36:39],v[24:27]
buffer_load_dwordx4 v2,s[32:35],s63 offen offset:3072 sc1 lds
s_add_u32 s63,s63,4096
v_mfma_f32_16x16x32_bf16 v[28:31],a[196:199],v[36:39],v[28:31]
ds_read_b128 v[60:63],v4 offset:54272
s_add_u32 m0,s64,12288
v_mfma_f32_16x16x32_bf16 v[28:31],a[84:87],v[32:35],v[28:31]
s_waitcnt lgkmcnt(4)
v_mfma_f32_16x16x32_bf16 v[28:31],v[116:119],v[40:43],v[28:31]
ds_read_b128 v[32:35],v4 offset:12288
v_mfma_f32_16x16x32_bf16 v[28:31],v[228:231],v[44:47],v[28:31]
buffer_load_dwordx4 v2,s[32:35],s63 offen offset:0 sc1 lds
v_mfma_f32_16x16x32_bf16 v[24:27],v[224:227],v[44:47],v[24:27]
ds_read_b128 v[36:39],v4 offset:26624
v_mfma_f32_16x16x32_bf16 v[24:27],v[112:115],v[40:43],v[24:27]
s_waitcnt lgkmcnt(4)
v_mfma_f32_16x16x32_bf16 v[24:27],a[88:91],v[48:51],v[24:27]
ds_read_b128 v[40:43],v4 offset:40960
v_mfma_f32_16x16x32_bf16 v[24:27],a[200:203],v[52:55],v[24:27]
buffer_load_dwordx4 v2,s[32:35],s63 offen offset:1024 sc1 lds
s_add_u32 s63,s63,s31
v_mfma_f32_16x16x32_bf16 v[28:31],a[204:207],v[52:55],v[28:31]
ds_read_b128 v[44:47],v4 offset:55296
v_mfma_f32_16x16x32_bf16 v[28:31],a[92:95],v[48:51],v[28:31]
s_waitcnt lgkmcnt(4)
v_mfma_f32_16x16x32_bf16 v[28:31],v[124:127],v[56:59],v[28:31]
ds_read_b128 v[48:51],v4 offset:13312
s_waitcnt vmcnt(10)
v_mfma_f32_16x16x32_bf16 v[28:31],v[236:239],v[60:63],v[28:31]
v_lshlrev_b32 v16,16,v12
v_and_b32 v17,0xffff0000,v12
v_mfma_f32_16x16x32_bf16 v[24:27],v[232:235],v[60:63],v[24:27]
ds_read_b128 v[52:55],v4 offset:27648
v_lshlrev_b32 v18,16,v13
v_mfma_f32_16x16x32_bf16 v[24:27],v[120:123],v[56:59],v[24:27]
v_and_b32 v19,0xffff0000,v13
s_waitcnt lgkmcnt(4)
v_mfma_f32_16x16x32_bf16 v[24:27],a[96:99],v[32:35],v[24:27]
ds_read_b128 v[56:59],v4 offset:41984
v_lshlrev_b32 v20,16,v14
v_mfma_f32_16x16x32_bf16 v[24:27],a[208:211],v[36:39],v[24:27]
v_and_b32 v21,0xffff0000,v14
v_lshlrev_b32 v22,16,v15
v_mfma_f32_16x16x32_bf16 v[28:31],a[212:215],v[36:39],v[28:31]
ds_read_b128 v[60:63],v4 offset:56320
v_and_b32 v23,0xffff0000,v15
v_mfma_f32_16x16x32_bf16 v[28:31],a[100:103],v[32:35],v[28:31]
s_waitcnt lgkmcnt(4)
s_barrier
v_mfma_f32_16x16x32_bf16 v[28:31],v[132:135],v[40:43],v[28:31]
ds_read_b128 v[32:35],v2 offset:0
v_mfma_f32_16x16x32_bf16 v[28:31],v[244:247],v[44:47],v[28:31]
v_mfma_f32_16x16x32_bf16 v[24:27],v[240:243],v[44:47],v[24:27]
ds_read_b128 v[36:39],v2 offset:14336
v_mfma_f32_16x16x32_bf16 v[24:27],v[128:131],v[40:43],v[24:27]
s_waitcnt lgkmcnt(4)
v_mfma_f32_16x16x32_bf16 v[24:27],a[104:107],v[48:51],v[24:27]
ds_read_b128 v[40:43],v2 offset:28672
v_mfma_f32_16x16x32_bf16 v[24:27],a[216:219],v[52:55],v[24:27]
buffer_store_dwordx2 v[8:9],v2,s[92:95],s67 idxen sc1
v_mfma_f32_16x16x32_bf16 v[28:31],a[220:223],v[52:55],v[28:31]
ds_read_b128 v[44:47],v2 offset:43008
v_mfma_f32_16x16x32_bf16 v[28:31],a[108:111],v[48:51],v[28:31]
s_waitcnt lgkmcnt(4)
v_mfma_f32_16x16x32_bf16 v[28:31],v[140:143],v[56:59],v[28:31]
ds_read_b128 v[48:51],v2 offset:1024
v_mfma_f32_16x16x32_bf16 v[28:31],v[252:255],v[60:63],v[28:31]
v_mfma_f32_16x16x32_bf16 v[24:27],v[248:251],v[60:63],v[24:27]
ds_read_b128 v[52:55],v2 offset:15360
v_mfma_f32_16x16x32_bf16 v[24:27],v[136:139],v[56:59],v[24:27]
s_branch .L_dll_0
.L_dlb_0:
s_mov_b32 s62,s58
s_mov_b32 s70,s59
s_add_u32 s74,s74,8192
v_lshrrev_b32 v5,8,v2
v_lshl_add_u32 v5,v5,4,s74
v_and_b32 v3,128,v2
v_lshl_add_u32 v5,v3,2,v5
v_and_b32 v3,16,v2
v_lshl_add_u32 v5,v3,4,v5
v_and_b32 v3,96,v2
v_lshl_add_u32 v5,v3,1,v5
v_lshrrev_b32 v3,2,v2
v_add_u32 v3,s74,v3
s_add_u32 s66,s66,2375680
s_add_u32 s63,s63,2375680
s_add_u32 s71,s71,2375680
s_mov_b32 s68,98304
s_mov_b32 s34,79446016
.p2align 6
.L_dml_0:
s_waitcnt lgkmcnt(4)
v_mfma_f32_16x16x32_bf16 v[24:27],a[0:3],v[32:35],v[24:27]
ds_read_b128 v[56:59],v4 offset:29696
v_mfma_f32_16x16x32_bf16 v[24:27],a[112:115],v[36:39],v[24:27]
v_mfma_f32_16x16x32_bf16 v[28:31],a[116:119],v[36:39],v[28:31]
ds_read_b128 v[60:63],v4 offset:44032
v_mfma_f32_16x16x32_bf16 v[28:31],a[4:7],v[32:35],v[28:31]
v_cvt_pk_bf16_f32 v15,v22,v23
s_waitcnt lgkmcnt(4)
v_mfma_f32_16x16x32_bf16 v[28:31],a[228:231],v[40:43],v[28:31]
ds_read_b128 v[32:35],v4 offset:2048
v_cvt_pk_bf16_f32 v14,v20,v21
v_mfma_f32_16x16x32_bf16 v[28:31],v[148:151],v[44:47],v[28:31]
v_cvt_pk_bf16_f32 v13,v18,v19
v_cvt_pk_bf16_f32 v12,v16,v17
v_mfma_f32_16x16x32_bf16 v[24:27],v[144:147],v[44:47],v[24:27]
ds_read_b128 v[36:39],v4 offset:16384
ds_write_b128 v5,v[12:15]
v_mfma_f32_16x16x32_bf16 v[24:27],a[224:227],v[40:43],v[24:27]
ds_read2st64_b32 v[12:13],v3 offset0:0 offset1:1
s_waitcnt lgkmcnt(6)
v_mfma_f32_16x16x32_bf16 v[24:27],a[8:11],v[48:51],v[24:27]
ds_read_b128 v[40:43],v4 offset:30720
ds_read2st64_b32 v[14:15],v3 offset0:2 offset1:3
)#"
R"#(
v_mfma_f32_16x16x32_bf16 v[24:27],a[120:123],v[52:55],v[24:27]
v_mfma_f32_16x16x32_bf16 v[28:31],a[124:127],v[52:55],v[28:31]
ds_read_b128 v[44:47],v4 offset:45056
v_mfma_f32_16x16x32_bf16 v[28:31],a[12:15],v[48:51],v[28:31]
s_waitcnt lgkmcnt(7)
v_mfma_f32_16x16x32_bf16 v[28:31],a[236:239],v[56:59],v[28:31]
ds_read_b128 v[48:51],v4 offset:3072
v_mfma_f32_16x16x32_bf16 v[28:31],v[156:159],v[60:63],v[28:31]
s_waitcnt lgkmcnt(2)
buffer_atomic_pk_add_bf16 v12,v1,s[44:47],s65 offen offset:0 sc1
v_mfma_f32_16x16x32_bf16 v[24:27],v[152:155],v[60:63],v[24:27]
ds_read_b128 v[52:55],v4 offset:17408
s_add_u32 s70,s70,s75
v_mfma_f32_16x16x32_bf16 v[24:27],a[232:235],v[56:59],v[24:27]
buffer_atomic_pk_add_bf16 v13,v1,s[44:47],s65 offen offset:2048 sc1
v_mfma_f32_16x16x32_bf16 v[24:27],a[16:19],v[32:35],v[24:27]
ds_read_b128 v[56:59],v4 offset:31744
s_add_u32 s65,s65,16384
v_mfma_f32_16x16x32_bf16 v[24:27],a[128:131],v[36:39],v[24:27]
buffer_atomic_pk_add_bf16 v14,v1,s[44:47],s65 offen offset:0 sc1
v_mfma_f32_16x16x32_bf16 v[28:31],a[132:135],v[36:39],v[28:31]
ds_read_b128 v[60:63],v4 offset:46080
v_mfma_f32_16x16x32_bf16 v[28:31],a[20:23],v[32:35],v[28:31]
s_waitcnt lgkmcnt(4)
s_waitcnt vmcnt(3)
s_barrier
v_mfma_f32_16x16x32_bf16 v[28:31],a[244:247],v[40:43],v[28:31]
ds_read_b128 v[32:35],v4 offset:4096
s_add_u32 m0,s64,0
v_mfma_f32_16x16x32_bf16 v[28:31],v[164:167],v[44:47],v[28:31]
buffer_atomic_pk_add_bf16 v15,v1,s[44:47],s65 offen offset:2048 sc1
s_and_b32 s65,s70,0x3f8000
v_mfma_f32_16x16x32_bf16 v[24:27],v[160:163],v[44:47],v[24:27]
ds_read_b128 v[36:39],v4 offset:18432
v_mfma_f32_16x16x32_bf16 v[24:27],a[240:243],v[40:43],v[24:27]
s_waitcnt lgkmcnt(4)
v_mfma_f32_16x16x32_bf16 v[24:27],a[24:27],v[48:51],v[24:27]
ds_read_b128 v[40:43],v4 offset:32768
v_mfma_f32_16x16x32_bf16 v[24:27],a[136:139],v[52:55],v[24:27]
buffer_load_dwordx4 v2,s[32:35],s63 offen offset:0 sc1 lds
v_mfma_f32_16x16x32_bf16 v[28:31],a[140:143],v[52:55],v[28:31]
ds_read_b128 v[44:47],v4 offset:47104
v_mfma_f32_16x16x32_bf16 v[28:31],a[28:31],v[48:51],v[28:31]
s_waitcnt lgkmcnt(4)
v_mfma_f32_16x16x32_bf16 v[28:31],a[252:255],v[56:59],v[28:31]
ds_read_b128 v[48:51],v4 offset:5120
v_mfma_f32_16x16x32_bf16 v[28:31],v[172:175],v[60:63],v[28:31]
buffer_load_dwordx4 v2,s[32:35],s63 offen offset:1024 sc1 lds
v_mfma_f32_16x16x32_bf16 v[24:27],v[168:171],v[60:63],v[24:27]
ds_read_b128 v[52:55],v4 offset:19456
v_mfma_f32_16x16x32_bf16 v[24:27],a[248:251],v[56:59],v[24:27]
s_waitcnt lgkmcnt(4)
v_mfma_f32_16x16x32_bf16 v[24:27],a[32:35],v[32:35],v[24:27]
ds_read_b128 v[56:59],v4 offset:33792
v_mfma_f32_16x16x32_bf16 v[24:27],a[144:147],v[36:39],v[24:27]
buffer_load_dwordx4 v2,s[32:35],s63 offen offset:2048 sc1 lds
v_mfma_f32_16x16x32_bf16 v[28:31],a[148:151],v[36:39],v[28:31]
ds_read_b128 v[60:63],v4 offset:48128
v_mfma_f32_16x16x32_bf16 v[28:31],a[36:39],v[32:35],v[28:31]
s_waitcnt lgkmcnt(4)
v_mfma_f32_16x16x32_bf16 v[28:31],v[68:71],v[40:43],v[28:31]
ds_read_b128 v[32:35],v4 offset:6144
v_mfma_f32_16x16x32_bf16 v[28:31],v[180:183],v[44:47],v[28:31]
buffer_load_dwordx4 v2,s[32:35],s63 offen offset:3072 sc1 lds
v_mfma_f32_16x16x32_bf16 v[24:27],v[176:179],v[44:47],v[24:27]
ds_read_b128 v[36:39],v4 offset:20480
v_mfma_f32_16x16x32_bf16 v[24:27],v[64:67],v[40:43],v[24:27]
s_waitcnt lgkmcnt(4)
v_mfma_f32_16x16x32_bf16 v[24:27],a[40:43],v[48:51],v[24:27]
ds_read_b128 v[40:43],v4 offset:34816
v_mfma_f32_16x16x32_bf16 v[24:27],a[152:155],v[52:55],v[24:27]
buffer_load_dwordx4 v[12:15],v2,s[32:35],s71 offen sc1
s_add_u32 s71,s71,s30
v_mfma_f32_16x16x32_bf16 v[28:31],a[156:159],v[52:55],v[28:31]
ds_read_b128 v[44:47],v4 offset:49152
s_add_u32 s63,s63,4096
v_mfma_f32_16x16x32_bf16 v[28:31],a[44:47],v[48:51],v[28:31]
s_add_u32 m0,s64,4096
s_waitcnt lgkmcnt(4)
v_mfma_f32_16x16x32_bf16 v[28:31],v[76:79],v[56:59],v[28:31]
ds_read_b128 v[48:51],v4 offset:7168
v_mfma_f32_16x16x32_bf16 v[28:31],v[188:191],v[60:63],v[28:31]
buffer_load_dwordx4 v2,s[32:35],s63 offen offset:0 sc1 lds
v_mfma_f32_16x16x32_bf16 v[24:27],v[184:187],v[60:63],v[24:27]
ds_read_b128 v[52:55],v4 offset:21504
v_mfma_f32_16x16x32_bf16 v[24:27],v[72:75],v[56:59],v[24:27]
s_waitcnt lgkmcnt(4)
v_mfma_f32_16x16x32_bf16 v[24:27],a[48:51],v[32:35],v[24:27]
ds_read_b128 v[56:59],v4 offset:35840
v_mfma_f32_16x16x32_bf16 v[24:27],a[160:163],v[36:39],v[24:27]
buffer_load_dwordx4 v2,s[32:35],s63 offen offset:1024 sc1 lds
v_mfma_f32_16x16x32_bf16 v[28:31],a[164:167],v[36:39],v[28:31]
ds_read_b128 v[60:63],v4 offset:50176
v_mfma_f32_16x16x32_bf16 v[28:31],a[52:55],v[32:35],v[28:31]
s_waitcnt lgkmcnt(4)
v_mfma_f32_16x16x32_bf16 v[28:31],v[84:87],v[40:43],v[28:31]
ds_read_b128 v[32:35],v4 offset:8192
v_mfma_f32_16x16x32_bf16 v[28:31],v[196:199],v[44:47],v[28:31]
buffer_load_dwordx4 v2,s[32:35],s63 offen offset:2048 sc1 lds
v_mfma_f32_16x16x32_bf16 v[24:27],v[192:195],v[44:47],v[24:27]
ds_read_b128 v[36:39],v4 offset:22528
v_mfma_f32_16x16x32_bf16 v[24:27],v[80:83],v[40:43],v[24:27]
s_waitcnt lgkmcnt(4)
v_mfma_f32_16x16x32_bf16 v[24:27],a[56:59],v[48:51],v[24:27]
ds_read_b128 v[40:43],v4 offset:36864
v_mfma_f32_16x16x32_bf16 v[24:27],a[168:171],v[52:55],v[24:27]
buffer_load_dwordx4 v2,s[32:35],s63 offen offset:3072 sc1 lds
v_mfma_f32_16x16x32_bf16 v[28:31],a[172:175],v[52:55],v[28:31]
ds_read_b128 v[44:47],v4 offset:51200
v_mfma_f32_16x16x32_bf16 v[28:31],a[60:63],v[48:51],v[28:31]
s_waitcnt lgkmcnt(4)
v_mfma_f32_16x16x32_bf16 v[28:31],v[92:95],v[56:59],v[28:31]
ds_read_b128 v[48:51],v4 offset:9216
v_mfma_f32_16x16x32_bf16 v[28:31],v[204:207],v[60:63],v[28:31]
s_add_u32 s63,s63,4096
s_add_u32 m0,s64,8192
v_mfma_f32_16x16x32_bf16 v[24:27],v[200:203],v[60:63],v[24:27]
ds_read_b128 v[52:55],v4 offset:23552
v_mfma_f32_16x16x32_bf16 v[24:27],v[88:91],v[56:59],v[24:27]
s_waitcnt lgkmcnt(4)
v_mfma_f32_16x16x32_bf16 v[24:27],a[64:67],v[32:35],v[24:27]
ds_read_b128 v[56:59],v4 offset:37888
v_mfma_f32_16x16x32_bf16 v[24:27],a[176:179],v[36:39],v[24:27]
buffer_load_dwordx4 v2,s[32:35],s63 offen offset:0 sc1 lds
v_mfma_f32_16x16x32_bf16 v[28:31],a[180:183],v[36:39],v[28:31]
ds_read_b128 v[60:63],v4 offset:52224
v_mfma_f32_16x16x32_bf16 v[28:31],a[68:71],v[32:35],v[28:31]
s_waitcnt lgkmcnt(4)
v_mfma_f32_16x16x32_bf16 v[28:31],v[100:103],v[40:43],v[28:31]
ds_read_b128 v[32:35],v4 offset:10240
v_mfma_f32_16x16x32_bf16 v[28:31],v[212:215],v[44:47],v[28:31]
buffer_load_dwordx4 v2,s[32:35],s63 offen offset:1024 sc1 lds
v_mfma_f32_16x16x32_bf16 v[24:27],v[208:211],v[44:47],v[24:27]
ds_read_b128 v[36:39],v4 offset:24576
v_mfma_f32_16x16x32_bf16 v[24:27],v[96:99],v[40:43],v[24:27]
s_waitcnt lgkmcnt(4)
v_mfma_f32_16x16x32_bf16 v[24:27],a[72:75],v[48:51],v[24:27]
ds_read_b128 v[40:43],v4 offset:38912
v_mfma_f32_16x16x32_bf16 v[24:27],a[184:187],v[52:55],v[24:27]
buffer_load_dwordx4 v2,s[32:35],s63 offen offset:2048 sc1 lds
v_mfma_f32_16x16x32_bf16 v[28:31],a[188:191],v[52:55],v[28:31]
ds_read_b128 v[44:47],v4 offset:53248
v_mfma_f32_16x16x32_bf16 v[28:31],a[76:79],v[48:51],v[28:31]
s_waitcnt lgkmcnt(4)
v_mfma_f32_16x16x32_bf16 v[28:31],v[108:111],v[56:59],v[28:31]
ds_read_b128 v[48:51],v4 offset:11264
v_mfma_f32_16x16x32_bf16 v[28:31],v[220:223],v[60:63],v[28:31]
v_mfma_f32_16x16x32_bf16 v[24:27],v[216:219],v[60:63],v[24:27]
ds_read_b128 v[52:55],v4 offset:25600
v_mfma_f32_16x16x32_bf16 v[24:27],v[104:107],v[56:59],v[24:27]
s_waitcnt lgkmcnt(4)
v_mfma_f32_16x16x32_bf16 v[24:27],a[80:83],v[32:35],v[24:27]
ds_read_b128 v[56:59],v4 offset:39936
v_mfma_f32_16x16x32_bf16 v[24:27],a[192:195],v[36:39],v[24:27]
buffer_load_dwordx4 v2,s[32:35],s63 offen offset:3072 sc1 lds
s_add_u32 s63,s63,4096
v_mfma_f32_16x16x32_bf16 v[28:31],a[196:199],v[36:39],v[28:31]
ds_read_b128 v[60:63],v4 offset:54272
s_add_u32 m0,s64,12288
v_mfma_f32_16x16x32_bf16 v[28:31],a[84:87],v[32:35],v[28:31]
s_waitcnt lgkmcnt(4)
v_mfma_f32_16x16x32_bf16 v[28:31],v[116:119],v[40:43],v[28:31]
ds_read_b128 v[32:35],v4 offset:12288
v_mfma_f32_16x16x32_bf16 v[28:31],v[228:231],v[44:47],v[28:31]
buffer_load_dwordx4 v2,s[32:35],s63 offen offset:0 sc1 lds
v_mfma_f32_16x16x32_bf16 v[24:27],v[224:227],v[44:47],v[24:27]
ds_read_b128 v[36:39],v4 offset:26624
v_mfma_f32_16x16x32_bf16 v[24:27],v[112:115],v[40:43],v[24:27]
s_waitcnt lgkmcnt(4)
v_mfma_f32_16x16x32_bf16 v[24:27],a[88:91],v[48:51],v[24:27]
ds_read_b128 v[40:43],v4 offset:40960
v_mfma_f32_16x16x32_bf16 v[24:27],a[200:203],v[52:55],v[24:27]
buffer_load_dwordx4 v2,s[32:35],s63 offen offset:1024 sc1 lds
s_add_u32 s63,s63,s31
v_mfma_f32_16x16x32_bf16 v[28:31],a[204:207],v[52:55],v[28:31]
ds_read_b128 v[44:47],v4 offset:55296
v_mfma_f32_16x16x32_bf16 v[28:31],a[92:95],v[48:51],v[28:31]
s_waitcnt lgkmcnt(4)
v_mfma_f32_16x16x32_bf16 v[28:31],v[124:127],v[56:59],v[28:31]
ds_read_b128 v[48:51],v4 offset:13312
s_waitcnt vmcnt(10)
v_mfma_f32_16x16x32_bf16 v[28:31],v[236:239],v[60:63],v[28:31]
v_lshlrev_b32 v16,16,v12
v_and_b32 v17,0xffff0000,v12
v_mfma_f32_16x16x32_bf16 v[24:27],v[232:235],v[60:63],v[24:27]
ds_read_b128 v[52:55],v4 offset:27648
v_lshlrev_b32 v18,16,v13
v_mfma_f32_16x16x32_bf16 v[24:27],v[120:123],v[56:59],v[24:27]
v_and_b32 v19,0xffff0000,v13
s_waitcnt lgkmcnt(4)
v_mfma_f32_16x16x32_bf16 v[24:27],a[96:99],v[32:35],v[24:27]
ds_read_b128 v[56:59],v4 offset:41984
v_lshlrev_b32 v20,16,v14
v_mfma_f32_16x16x32_bf16 v[24:27],a[208:211],v[36:39],v[24:27]
v_and_b32 v21,0xffff0000,v14
v_lshlrev_b32 v22,16,v15
v_mfma_f32_16x16x32_bf16 v[28:31],a[212:215],v[36:39],v[28:31]
ds_read_b128 v[60:63],v4 offset:56320
v_and_b32 v23,0xffff0000,v15
v_mfma_f32_16x16x32_bf16 v[28:31],a[100:103],v[32:35],v[28:31]
s_waitcnt lgkmcnt(4)
s_barrier
v_mfma_f32_16x16x32_bf16 v[28:31],v[132:135],v[40:43],v[28:31]
ds_read_b128 v[32:35],v2 offset:0
v_mfma_f32_16x16x32_bf16 v[28:31],v[244:247],v[44:47],v[28:31]
v_mfma_f32_16x16x32_bf16 v[24:27],v[240:243],v[44:47],v[24:27]
ds_read_b128 v[36:39],v2 offset:14336
v_mfma_f32_16x16x32_bf16 v[24:27],v[128:131],v[40:43],v[24:27]
s_waitcnt lgkmcnt(4)
v_mfma_f32_16x16x32_bf16 v[24:27],a[104:107],v[48:51],v[24:27]
ds_read_b128 v[40:43],v2 offset:28672
v_mfma_f32_16x16x32_bf16 v[24:27],a[216:219],v[52:55],v[24:27]
v_mfma_f32_16x16x32_bf16 v[28:31],a[220:223],v[52:55],v[28:31]
ds_read_b128 v[44:47],v2 offset:43008
v_mfma_f32_16x16x32_bf16 v[28:31],a[108:111],v[48:51],v[28:31]
s_waitcnt lgkmcnt(4)
v_mfma_f32_16x16x32_bf16 v[28:31],v[140:143],v[56:59],v[28:31]
ds_read_b128 v[48:51],v2 offset:1024
v_mfma_f32_16x16x32_bf16 v[28:31],v[252:255],v[60:63],v[28:31]
v_mfma_f32_16x16x32_bf16 v[24:27],v[248:251],v[60:63],v[24:27]
ds_read_b128 v[52:55],v2 offset:15360
s_sub_u32 s62,s62,1
v_mfma_f32_16x16x32_bf16 v[24:27],v[136:139],v[56:59],v[24:27]
s_cbranch_scc1 .L_dmt_0
s_waitcnt lgkmcnt(4)
v_mfma_f32_16x16x32_bf16 v[16:19],a[0:3],v[32:35],v[16:19]
ds_read_b128 v[56:59],v2 offset:29696
v_mfma_f32_16x16x32_bf16 v[16:19],a[112:115],v[36:39],v[16:19]
v_mfma_f32_16x16x32_bf16 v[20:23],a[116:119],v[36:39],v[20:23]
ds_read_b128 v[60:63],v2 offset:44032
v_mfma_f32_16x16x32_bf16 v[20:23],a[4:7],v[32:35],v[20:23]
v_cvt_pk_bf16_f32 v15,v30,v31
s_waitcnt lgkmcnt(4)
v_mfma_f32_16x16x32_bf16 v[20:23],a[228:231],v[40:43],v[20:23]
ds_read_b128 v[32:35],v2 offset:2048
v_cvt_pk_bf16_f32 v14,v28,v29
v_mfma_f32_16x16x32_bf16 v[20:23],v[148:151],v[44:47],v[20:23]
v_cvt_pk_bf16_f32 v13,v26,v27
v_cvt_pk_bf16_f32 v12,v24,v25
v_mfma_f32_16x16x32_bf16 v[16:19],v[144:147],v[44:47],v[16:19]
ds_read_b128 v[36:39],v2 offset:16384
ds_write_b128 v5,v[12:15]
v_mfma_f32_16x16x32_bf16 v[16:19],a[224:227],v[40:43],v[16:19]
ds_read2st64_b32 v[12:13],v3 offset0:0 offset1:1
s_waitcnt lgkmcnt(6)
v_mfma_f32_16x16x32_bf16 v[16:19],a[8:11],v[48:51],v[16:19]
ds_read_b128 v[40:43],v2 offset:30720
ds_read2st64_b32 v[14:15],v3 offset0:2 offset1:3
v_mfma_f32_16x16x32_bf16 v[16:19],a[120:123],v[52:55],v[16:19]
v_mfma_f32_16x16x32_bf16 v[20:23],a[124:127],v[52:55],v[20:23]
ds_read_b128 v[44:47],v2 offset:45056
v_mfma_f32_16x16x32_bf16 v[20:23],a[12:15],v[48:51],v[20:23]
s_waitcnt lgkmcnt(7)
v_mfma_f32_16x16x32_bf16 v[20:23],a[236:239],v[56:59],v[20:23]
ds_read_b128 v[48:51],v2 offset:3072
v_mfma_f32_16x16x32_bf16 v[20:23],v[156:159],v[60:63],v[20:23]
s_waitcnt lgkmcnt(2)
buffer_atomic_pk_add_bf16 v12,v1,s[44:47],s65 offen offset:0 sc1
v_mfma_f32_16x16x32_bf16 v[16:19],v[152:155],v[60:63],v[16:19]
ds_read_b128 v[52:55],v2 offset:17408
s_add_u32 s70,s70,s68
v_mfma_f32_16x16x32_bf16 v[16:19],a[232:235],v[56:59],v[16:19]
buffer_atomic_pk_add_bf16 v13,v1,s[44:47],s65 offen offset:2048 sc1
v_mfma_f32_16x16x32_bf16 v[16:19],a[16:19],v[32:35],v[16:19]
ds_read_b128 v[56:59],v2 offset:31744
s_add_u32 s65,s65,16384
v_mfma_f32_16x16x32_bf16 v[16:19],a[128:131],v[36:39],v[16:19]
buffer_atomic_pk_add_bf16 v14,v1,s[44:47],s65 offen offset:0 sc1
s_mov_b32 s68,s75
v_mfma_f32_16x16x32_bf16 v[20:23],a[132:135],v[36:39],v[20:23]
ds_read_b128 v[60:63],v2 offset:46080
v_mfma_f32_16x16x32_bf16 v[20:23],a[20:23],v[32:35],v[20:23]
s_waitcnt lgkmcnt(4)
s_waitcnt vmcnt(3)
s_barrier
v_mfma_f32_16x16x32_bf16 v[20:23],a[244:247],v[40:43],v[20:23]
ds_read_b128 v[32:35],v2 offset:4096
s_add_u32 m0,s64,57344
v_mfma_f32_16x16x32_bf16 v[20:23],v[164:167],v[44:47],v[20:23]
buffer_atomic_pk_add_bf16 v15,v1,s[44:47],s65 offen offset:2048 sc1
s_and_b32 s65,s70,0x3f8000
v_mfma_f32_16x16x32_bf16 v[16:19],v[160:163],v[44:47],v[16:19]
ds_read_b128 v[36:39],v2 offset:18432
v_mfma_f32_16x16x32_bf16 v[16:19],a[240:243],v[40:43],v[16:19]
s_waitcnt lgkmcnt(4)
v_mfma_f32_16x16x32_bf16 v[16:19],a[24:27],v[48:51],v[16:19]
ds_read_b128 v[40:43],v2 offset:32768
v_mfma_f32_16x16x32_bf16 v[16:19],a[136:139],v[52:55],v[16:19]
buffer_load_dwordx4 v2,s[32:35],s63 offen offset:0 sc1 lds
v_mfma_f32_16x16x32_bf16 v[20:23],a[140:143],v[52:55],v[20:23]
ds_read_b128 v[44:47],v2 offset:47104
v_mfma_f32_16x16x32_bf16 v[20:23],a[28:31],v[48:51],v[20:23]
s_waitcnt lgkmcnt(4)
v_mfma_f32_16x16x32_bf16 v[20:23],a[252:255],v[56:59],v[20:23]
ds_read_b128 v[48:51],v2 offset:5120
v_mfma_f32_16x16x32_bf16 v[20:23],v[172:175],v[60:63],v[20:23]
buffer_load_dwordx4 v2,s[32:35],s63 offen offset:1024 sc1 lds
v_mfma_f32_16x16x32_bf16 v[16:19],v[168:171],v[60:63],v[16:19]
ds_read_b128 v[52:55],v2 offset:19456
v_mfma_f32_16x16x32_bf16 v[16:19],a[248:251],v[56:59],v[16:19]
s_waitcnt lgkmcnt(4)
v_mfma_f32_16x16x32_bf16 v[16:19],a[32:35],v[32:35],v[16:19]
ds_read_b128 v[56:59],v2 offset:33792
v_mfma_f32_16x16x32_bf16 v[16:19],a[144:147],v[36:39],v[16:19]
buffer_load_dwordx4 v2,s[32:35],s63 offen offset:2048 sc1 lds
v_mfma_f32_16x16x32_bf16 v[20:23],a[148:151],v[36:39],v[20:23]
ds_read_b128 v[60:63],v2 offset:48128
v_mfma_f32_16x16x32_bf16 v[20:23],a[36:39],v[32:35],v[20:23]
s_waitcnt lgkmcnt(4)
v_mfma_f32_16x16x32_bf16 v[20:23],v[68:71],v[40:43],v[20:23]
ds_read_b128 v[32:35],v2 offset:6144
v_mfma_f32_16x16x32_bf16 v[20:23],v[180:183],v[44:47],v[20:23]
buffer_load_dwordx4 v2,s[32:35],s63 offen offset:3072 sc1 lds
v_mfma_f32_16x16x32_bf16 v[16:19],v[176:179],v[44:47],v[16:19]
ds_read_b128 v[36:39],v2 offset:20480
v_mfma_f32_16x16x32_bf16 v[16:19],v[64:67],v[40:43],v[16:19]
s_waitcnt lgkmcnt(4)
v_mfma_f32_16x16x32_bf16 v[16:19],a[40:43],v[48:51],v[16:19]
ds_read_b128 v[40:43],v2 offset:34816
v_mfma_f32_16x16x32_bf16 v[16:19],a[152:155],v[52:55],v[16:19]
buffer_load_dwordx4 v[12:15],v2,s[32:35],s71 offen sc1
s_add_u32 s71,s71,s30
v_mfma_f32_16x16x32_bf16 v[20:23],a[156:159],v[52:55],v[20:23]
ds_read_b128 v[44:47],v2 offset:49152
s_add_u32 s63,s63,4096
v_mfma_f32_16x16x32_bf16 v[20:23],a[44:47],v[48:51],v[20:23]
s_add_u32 m0,s64,61440
s_waitcnt lgkmcnt(4)
v_mfma_f32_16x16x32_bf16 v[20:23],v[76:79],v[56:59],v[20:23]
ds_read_b128 v[48:51],v2 offset:7168
v_mfma_f32_16x16x32_bf16 v[20:23],v[188:191],v[60:63],v[20:23]
buffer_load_dwordx4 v2,s[32:35],s63 offen offset:0 sc1 lds
v_mfma_f32_16x16x32_bf16 v[16:19],v[184:187],v[60:63],v[16:19]
ds_read_b128 v[52:55],v2 offset:21504
v_mfma_f32_16x16x32_bf16 v[16:19],v[72:75],v[56:59],v[16:19]
s_waitcnt lgkmcnt(4)
v_mfma_f32_16x16x32_bf16 v[16:19],a[48:51],v[32:35],v[16:19]
ds_read_b128 v[56:59],v2 offset:35840
v_mfma_f32_16x16x32_bf16 v[16:19],a[160:163],v[36:39],v[16:19]
buffer_load_dwordx4 v2,s[32:35],s63 offen offset:1024 sc1 lds
v_mfma_f32_16x16x32_bf16 v[20:23],a[164:167],v[36:39],v[20:23]
ds_read_b128 v[60:63],v2 offset:50176
v_mfma_f32_16x16x32_bf16 v[20:23],a[52:55],v[32:35],v[20:23]
s_waitcnt lgkmcnt(4)
v_mfma_f32_16x16x32_bf16 v[20:23],v[84:87],v[40:43],v[20:23]
ds_read_b128 v[32:35],v2 offset:8192
v_mfma_f32_16x16x32_bf16 v[20:23],v[196:199],v[44:47],v[20:23]
buffer_load_dwordx4 v2,s[32:35],s63 offen offset:2048 sc1 lds
v_mfma_f32_16x16x32_bf16 v[16:19],v[192:195],v[44:47],v[16:19]
ds_read_b128 v[36:39],v2 offset:22528
v_mfma_f32_16x16x32_bf16 v[16:19],v[80:83],v[40:43],v[16:19]
s_waitcnt lgkmcnt(4)
v_mfma_f32_16x16x32_bf16 v[16:19],a[56:59],v[48:51],v[16:19]
ds_read_b128 v[40:43],v2 offset:36864
v_mfma_f32_16x16x32_bf16 v[16:19],a[168:171],v[52:55],v[16:19]
buffer_load_dwordx4 v2,s[32:35],s63 offen offset:3072 sc1 lds
v_mfma_f32_16x16x32_bf16 v[20:23],a[172:175],v[52:55],v[20:23]
ds_read_b128 v[44:47],v2 offset:51200
v_mfma_f32_16x16x32_bf16 v[20:23],a[60:63],v[48:51],v[20:23]
s_waitcnt lgkmcnt(4)
v_mfma_f32_16x16x32_bf16 v[20:23],v[92:95],v[56:59],v[20:23]
ds_read_b128 v[48:51],v2 offset:9216
v_mfma_f32_16x16x32_bf16 v[20:23],v[204:207],v[60:63],v[20:23]
s_add_u32 s63,s63,4096
s_add_u32 m0,s64,65536
v_mfma_f32_16x16x32_bf16 v[16:19],v[200:203],v[60:63],v[16:19]
ds_read_b128 v[52:55],v2 offset:23552
v_mfma_f32_16x16x32_bf16 v[16:19],v[88:91],v[56:59],v[16:19]
s_waitcnt lgkmcnt(4)
v_mfma_f32_16x16x32_bf16 v[16:19],a[64:67],v[32:35],v[16:19]
ds_read_b128 v[56:59],v2 offset:37888
v_mfma_f32_16x16x32_bf16 v[16:19],a[176:179],v[36:39],v[16:19]
buffer_load_dwordx4 v2,s[32:35],s63 offen offset:0 sc1 lds
v_mfma_f32_16x16x32_bf16 v[20:23],a[180:183],v[36:39],v[20:23]
ds_read_b128 v[60:63],v2 offset:52224
v_mfma_f32_16x16x32_bf16 v[20:23],a[68:71],v[32:35],v[20:23]
s_waitcnt lgkmcnt(4)
v_mfma_f32_16x16x32_bf16 v[20:23],v[100:103],v[40:43],v[20:23]
ds_read_b128 v[32:35],v2 offset:10240
v_mfma_f32_16x16x32_bf16 v[20:23],v[212:215],v[44:47],v[20:23]
buffer_load_dwordx4 v2,s[32:35],s63 offen offset:1024 sc1 lds
v_mfma_f32_16x16x32_bf16 v[16:19],v[208:211],v[44:47],v[16:19]
ds_read_b128 v[36:39],v2 offset:24576
v_mfma_f32_16x16x32_bf16 v[16:19],v[96:99],v[40:43],v[16:19]
s_waitcnt lgkmcnt(4)
v_mfma_f32_16x16x32_bf16 v[16:19],a[72:75],v[48:51],v[16:19]
ds_read_b128 v[40:43],v2 offset:38912
v_mfma_f32_16x16x32_bf16 v[16:19],a[184:187],v[52:55],v[16:19]
buffer_load_dwordx4 v2,s[32:35],s63 offen offset:2048 sc1 lds
v_mfma_f32_16x16x32_bf16 v[20:23],a[188:191],v[52:55],v[20:23]
ds_read_b128 v[44:47],v2 offset:53248
v_mfma_f32_16x16x32_bf16 v[20:23],a[76:79],v[48:51],v[20:23]
s_waitcnt lgkmcnt(4)
v_mfma_f32_16x16x32_bf16 v[20:23],v[108:111],v[56:59],v[20:23]
ds_read_b128 v[48:51],v2 offset:11264
v_mfma_f32_16x16x32_bf16 v[20:23],v[220:223],v[60:63],v[20:23]
v_mfma_f32_16x16x32_bf16 v[16:19],v[216:219],v[60:63],v[16:19]
ds_read_b128 v[52:55],v2 offset:25600
v_mfma_f32_16x16x32_bf16 v[16:19],v[104:107],v[56:59],v[16:19]
s_waitcnt lgkmcnt(4)
v_mfma_f32_16x16x32_bf16 v[16:19],a[80:83],v[32:35],v[16:19]
ds_read_b128 v[56:59],v2 offset:39936
v_mfma_f32_16x16x32_bf16 v[16:19],a[192:195],v[36:39],v[16:19]
buffer_load_dwordx4 v2,s[32:35],s63 offen offset:3072 sc1 lds
s_add_u32 s63,s63,4096
v_mfma_f32_16x16x32_bf16 v[20:23],a[196:199],v[36:39],v[20:23]
ds_read_b128 v[60:63],v2 offset:54272
s_add_u32 m0,s64,69632
v_mfma_f32_16x16x32_bf16 v[20:23],a[84:87],v[32:35],v[20:23]
s_waitcnt lgkmcnt(4)
v_mfma_f32_16x16x32_bf16 v[20:23],v[116:119],v[40:43],v[20:23]
ds_read_b128 v[32:35],v2 offset:12288
v_mfma_f32_16x16x32_bf16 v[20:23],v[228:231],v[44:47],v[20:23]
buffer_load_dwordx4 v2,s[32:35],s63 offen offset:0 sc1 lds
v_mfma_f32_16x16x32_bf16 v[16:19],v[224:227],v[44:47],v[16:19]
ds_read_b128 v[36:39],v2 offset:26624
v_mfma_f32_16x16x32_bf16 v[16:19],v[112:115],v[40:43],v[16:19]
s_waitcnt lgkmcnt(4)
v_mfma_f32_16x16x32_bf16 v[16:19],a[88:91],v[48:51],v[16:19]
ds_read_b128 v[40:43],v2 offset:40960
v_mfma_f32_16x16x32_bf16 v[16:19],a[200:203],v[52:55],v[16:19]
buffer_load_dwordx4 v2,s[32:35],s63 offen offset:1024 sc1 lds
s_add_u32 s63,s63,s31
v_mfma_f32_16x16x32_bf16 v[20:23],a[204:207],v[52:55],v[20:23]
ds_read_b128 v[44:47],v2 offset:55296
v_mfma_f32_16x16x32_bf16 v[20:23],a[92:95],v[48:51],v[20:23]
s_waitcnt lgkmcnt(4)
v_mfma_f32_16x16x32_bf16 v[20:23],v[124:127],v[56:59],v[20:23]
ds_read_b128 v[48:51],v2 offset:13312
s_waitcnt vmcnt(10)
v_mfma_f32_16x16x32_bf16 v[20:23],v[236:239],v[60:63],v[20:23]
v_lshlrev_b32 v24,16,v12
v_and_b32 v25,0xffff0000,v12
v_mfma_f32_16x16x32_bf16 v[16:19],v[232:235],v[60:63],v[16:19]
ds_read_b128 v[52:55],v2 offset:27648
v_lshlrev_b32 v26,16,v13
v_mfma_f32_16x16x32_bf16 v[16:19],v[120:123],v[56:59],v[16:19]
v_and_b32 v27,0xffff0000,v13
s_waitcnt lgkmcnt(4)
v_mfma_f32_16x16x32_bf16 v[16:19],a[96:99],v[32:35],v[16:19]
ds_read_b128 v[56:59],v2 offset:41984
v_lshlrev_b32 v28,16,v14
v_mfma_f32_16x16x32_bf16 v[16:19],a[208:211],v[36:39],v[16:19]
v_and_b32 v29,0xffff0000,v14
v_lshlrev_b32 v30,16,v15
v_mfma_f32_16x16x32_bf16 v[20:23],a[212:215],v[36:39],v[20:23]
ds_read_b128 v[60:63],v2 offset:56320
v_and_b32 v31,0xffff0000,v15
v_mfma_f32_16x16x32_bf16 v[20:23],a[100:103],v[32:35],v[20:23]
s_waitcnt lgkmcnt(4)
s_barrier
v_mfma_f32_16x16x32_bf16 v[20:23],v[132:135],v[40:43],v[20:23]
ds_read_b128 v[32:35],v4 offset:0
v_mfma_f32_16x16x32_bf16 v[20:23],v[244:247],v[44:47],v[20:23]
v_mfma_f32_16x16x32_bf16 v[16:19],v[240:243],v[44:47],v[16:19]
ds_read_b128 v[36:39],v4 offset:14336
v_mfma_f32_16x16x32_bf16 v[16:19],v[128:131],v[40:43],v[16:19]
s_waitcnt lgkmcnt(4)
v_mfma_f32_16x16x32_bf16 v[16:19],a[104:107],v[48:51],v[16:19]
ds_read_b128 v[40:43],v4 offset:28672
v_mfma_f32_16x16x32_bf16 v[16:19],a[216:219],v[52:55],v[16:19]
v_mfma_f32_16x16x32_bf16 v[20:23],a[220:223],v[52:55],v[20:23]
ds_read_b128 v[44:47],v4 offset:43008
v_mfma_f32_16x16x32_bf16 v[20:23],a[108:111],v[48:51],v[20:23]
s_waitcnt lgkmcnt(4)
v_mfma_f32_16x16x32_bf16 v[20:23],v[140:143],v[56:59],v[20:23]
ds_read_b128 v[48:51],v4 offset:1024
v_mfma_f32_16x16x32_bf16 v[20:23],v[252:255],v[60:63],v[20:23]
v_mfma_f32_16x16x32_bf16 v[16:19],v[248:251],v[60:63],v[16:19]
ds_read_b128 v[52:55],v4 offset:15360
s_sub_u32 s62,s62,1
v_mfma_f32_16x16x32_bf16 v[16:19],v[136:139],v[56:59],v[16:19]
s_cbranch_scc0 .L_dml_0
.L_dmu_0:
s_nop 1
s_nop 0
v_cvt_pk_bf16_f32 v15,v22,v23
v_cvt_pk_bf16_f32 v14,v20,v21
s_nop 1
v_cvt_pk_bf16_f32 v13,v18,v19
v_cvt_pk_bf16_f32 v12,v16,v17
ds_write_b128 v5,v[12:15]
ds_read2st64_b32 v[12:13],v3 offset0:0 offset1:1
ds_read2st64_b32 v[14:15],v3 offset0:2 offset1:3
s_waitcnt lgkmcnt(0)
buffer_atomic_pk_add_bf16 v12,v1,s[44:47],s65 offen offset:0 sc1
s_add_u32 s70,s70,s75
buffer_atomic_pk_add_bf16 v13,v1,s[44:47],s65 offen offset:2048 sc1
s_add_u32 s65,s65,16384
buffer_atomic_pk_add_bf16 v14,v1,s[44:47],s65 offen offset:0 sc1
buffer_atomic_pk_add_bf16 v15,v1,s[44:47],s65 offen offset:2048 sc1
s_and_b32 s65,s70,0x3f8000
s_branch .L_end
.L_dmt_0:
s_nop 1
s_nop 0
v_cvt_pk_bf16_f32 v15,v30,v31
v_cvt_pk_bf16_f32 v14,v28,v29
s_nop 1
v_cvt_pk_bf16_f32 v13,v26,v27
)#"
R"#(
v_cvt_pk_bf16_f32 v12,v24,v25
ds_write_b128 v5,v[12:15]
ds_read2st64_b32 v[12:13],v3 offset0:0 offset1:1
ds_read2st64_b32 v[14:15],v3 offset0:2 offset1:3
s_waitcnt lgkmcnt(0)
buffer_atomic_pk_add_bf16 v12,v1,s[44:47],s65 offen offset:0 sc1
s_add_u32 s70,s70,s68
buffer_atomic_pk_add_bf16 v13,v1,s[44:47],s65 offen offset:2048 sc1
s_add_u32 s65,s65,16384
buffer_atomic_pk_add_bf16 v14,v1,s[44:47],s65 offen offset:0 sc1
s_mov_b32 s68,s75
buffer_atomic_pk_add_bf16 v15,v1,s[44:47],s65 offen offset:2048 sc1
s_and_b32 s65,s70,0x3f8000
s_branch .L_end
.p2align 6
.L_dlv1:
.L_dll_1:
s_waitcnt lgkmcnt(4)
v_mfma_f32_16x16x32_bf16 v[16:19],a[0:3],v[32:35],v[16:19]
ds_read_b128 v[56:59],v2 offset:29696
v_mfma_f32_16x16x32_bf16 v[16:19],a[112:115],v[36:39],v[16:19]
v_mfma_f32_16x16x32_bf16 v[20:23],a[116:119],v[36:39],v[20:23]
ds_read_b128 v[60:63],v2 offset:44032
v_mfma_f32_16x16x32_bf16 v[20:23],a[4:7],v[32:35],v[20:23]
s_add_u32 s68,s68,1187840
s_waitcnt lgkmcnt(4)
v_mfma_f32_16x16x32_bf16 v[20:23],a[228:231],v[40:43],v[20:23]
ds_read_b128 v[32:35],v2 offset:2048
v_mfma_f32_16x16x32_bf16 v[20:23],v[148:151],v[44:47],v[20:23]
buffer_store_dwordx4 v[28:31],v2,s[32:35],s68 offen offset:1024 sc1
v_mfma_f32_16x16x32_bf16 v[16:19],v[144:147],v[44:47],v[16:19]
ds_read_b128 v[36:39],v2 offset:16384
v_mfma_f32_16x16x32_bf16 v[16:19],a[224:227],v[40:43],v[16:19]
s_waitcnt lgkmcnt(4)
v_mfma_f32_16x16x32_bf16 v[16:19],a[8:11],v[48:51],v[16:19]
ds_read_b128 v[40:43],v2 offset:30720
v_mfma_f32_16x16x32_bf16 v[16:19],a[120:123],v[52:55],v[16:19]
buffer_store_dwordx4 v[24:27],v2,s[32:35],s68 offen sc1
s_mov_b32 s67,s68
v_mfma_f32_16x16x32_bf16 v[20:23],a[124:127],v[52:55],v[20:23]
ds_read_b128 v[44:47],v2 offset:45056
v_mfma_f32_16x16x32_bf16 v[20:23],a[12:15],v[48:51],v[20:23]
s_waitcnt lgkmcnt(4)
v_mfma_f32_16x16x32_bf16 v[20:23],a[236:239],v[56:59],v[20:23]
ds_read_b128 v[48:51],v2 offset:3072
v_mfma_f32_16x16x32_bf16 v[20:23],v[156:159],v[60:63],v[20:23]
v_mfma_f32_16x16x32_bf16 v[16:19],v[152:155],v[60:63],v[16:19]
ds_read_b128 v[52:55],v2 offset:17408
s_waitcnt vmcnt(5)
v_mfma_f32_16x16x32_bf16 v[16:19],a[232:235],v[56:59],v[16:19]
v_cmp_ne_u64 vcc,v[8:9],v[6:7]
s_waitcnt lgkmcnt(4)
v_mfma_f32_16x16x32_bf16 v[16:19],a[16:19],v[32:35],v[16:19]
ds_read_b128 v[56:59],v2 offset:31744
s_cbranch_vccnz .L_rtl0_1
.L_bkl0_1:
v_mfma_f32_16x16x32_bf16 v[16:19],a[128:131],v[36:39],v[16:19]
s_add_u32 s66,s66,s30
v_mfma_f32_16x16x32_bf16 v[20:23],a[132:135],v[36:39],v[20:23]
s_waitcnt vmcnt(2)
s_barrier
v_mfma_f32_16x16x32_bf16 v[20:23],a[20:23],v[32:35],v[20:23]
ds_read_b128 v[60:63],v2 offset:46080
s_waitcnt lgkmcnt(4)
v_mfma_f32_16x16x32_bf16 v[20:23],a[244:247],v[40:43],v[20:23]
ds_read_b128 v[32:35],v2 offset:4096
s_add_u32 m0,s64,57344
v_mfma_f32_16x16x32_bf16 v[20:23],v[164:167],v[44:47],v[20:23]
buffer_load_dwordx4 v2,s[32:35],s63 offen offset:0 sc1 lds
v_mfma_f32_16x16x32_bf16 v[16:19],v[160:163],v[44:47],v[16:19]
ds_read_b128 v[36:39],v2 offset:18432
v_mfma_f32_16x16x32_bf16 v[16:19],a[240:243],v[40:43],v[16:19]
s_waitcnt lgkmcnt(4)
v_mfma_f32_16x16x32_bf16 v[16:19],a[24:27],v[48:51],v[16:19]
ds_read_b128 v[40:43],v2 offset:32768
v_mfma_f32_16x16x32_bf16 v[16:19],a[136:139],v[52:55],v[16:19]
v_mfma_f32_16x16x32_bf16 v[20:23],a[140:143],v[52:55],v[20:23]
ds_read_b128 v[44:47],v2 offset:47104
v_mfma_f32_16x16x32_bf16 v[20:23],a[28:31],v[48:51],v[20:23]
s_waitcnt lgkmcnt(4)
v_mfma_f32_16x16x32_bf16 v[20:23],a[252:255],v[56:59],v[20:23]
ds_read_b128 v[48:51],v2 offset:5120
v_mfma_f32_16x16x32_bf16 v[20:23],v[172:175],v[60:63],v[20:23]
buffer_load_dwordx4 v2,s[32:35],s63 offen offset:1024 sc1 lds
v_mfma_f32_16x16x32_bf16 v[16:19],v[168:171],v[60:63],v[16:19]
ds_read_b128 v[52:55],v2 offset:19456
v_mfma_f32_16x16x32_bf16 v[16:19],a[248:251],v[56:59],v[16:19]
s_waitcnt lgkmcnt(4)
v_mfma_f32_16x16x32_bf16 v[16:19],a[32:35],v[32:35],v[16:19]
ds_read_b128 v[56:59],v2 offset:33792
v_mfma_f32_16x16x32_bf16 v[16:19],a[144:147],v[36:39],v[16:19]
buffer_load_dwordx4 v2,s[32:35],s63 offen offset:2048 sc1 lds
v_mfma_f32_16x16x32_bf16 v[20:23],a[148:151],v[36:39],v[20:23]
ds_read_b128 v[60:63],v2 offset:48128
v_mfma_f32_16x16x32_bf16 v[20:23],a[36:39],v[32:35],v[20:23]
s_waitcnt lgkmcnt(4)
v_mfma_f32_16x16x32_bf16 v[20:23],v[68:71],v[40:43],v[20:23]
ds_read_b128 v[32:35],v2 offset:6144
v_mfma_f32_16x16x32_bf16 v[20:23],v[180:183],v[44:47],v[20:23]
buffer_load_dwordx4 v2,s[32:35],s63 offen offset:3072 sc1 lds
v_mfma_f32_16x16x32_bf16 v[16:19],v[176:179],v[44:47],v[16:19]
ds_read_b128 v[36:39],v2 offset:20480
v_mfma_f32_16x16x32_bf16 v[16:19],v[64:67],v[40:43],v[16:19]
s_waitcnt lgkmcnt(4)
v_mfma_f32_16x16x32_bf16 v[16:19],a[40:43],v[48:51],v[16:19]
ds_read_b128 v[40:43],v2 offset:34816
v_mfma_f32_16x16x32_bf16 v[16:19],a[152:155],v[52:55],v[16:19]
buffer_load_dwordx4 v[12:15],v2,s[32:35],s71 offen sc1
s_add_u32 s71,s71,s30
v_mfma_f32_16x16x32_bf16 v[20:23],a[156:159],v[52:55],v[20:23]
ds_read_b128 v[44:47],v2 offset:49152
s_add_u32 s63,s63,4096
v_mfma_f32_16x16x32_bf16 v[20:23],a[44:47],v[48:51],v[20:23]
s_add_u32 m0,s64,61440
s_waitcnt lgkmcnt(4)
v_mfma_f32_16x16x32_bf16 v[20:23],v[76:79],v[56:59],v[20:23]
ds_read_b128 v[48:51],v2 offset:7168
v_mfma_f32_16x16x32_bf16 v[20:23],v[188:191],v[60:63],v[20:23]
buffer_load_dwordx4 v2,s[32:35],s63 offen offset:0 sc1 lds
v_mfma_f32_16x16x32_bf16 v[16:19],v[184:187],v[60:63],v[16:19]
ds_read_b128 v[52:55],v2 offset:21504
v_mfma_f32_16x16x32_bf16 v[16:19],v[72:75],v[56:59],v[16:19]
s_waitcnt lgkmcnt(4)
v_mfma_f32_16x16x32_bf16 v[16:19],a[48:51],v[32:35],v[16:19]
ds_read_b128 v[56:59],v2 offset:35840
v_mfma_f32_16x16x32_bf16 v[16:19],a[160:163],v[36:39],v[16:19]
buffer_load_dwordx4 v2,s[32:35],s63 offen offset:1024 sc1 lds
v_mfma_f32_16x16x32_bf16 v[20:23],a[164:167],v[36:39],v[20:23]
ds_read_b128 v[60:63],v2 offset:50176
v_mfma_f32_16x16x32_bf16 v[20:23],a[52:55],v[32:35],v[20:23]
s_waitcnt lgkmcnt(4)
v_mfma_f32_16x16x32_bf16 v[20:23],v[84:87],v[40:43],v[20:23]
ds_read_b128 v[32:35],v2 offset:8192
v_mfma_f32_16x16x32_bf16 v[20:23],v[196:199],v[44:47],v[20:23]
buffer_load_dwordx4 v2,s[32:35],s63 offen offset:2048 sc1 lds
v_mfma_f32_16x16x32_bf16 v[16:19],v[192:195],v[44:47],v[16:19]
ds_read_b128 v[36:39],v2 offset:22528
v_mfma_f32_16x16x32_bf16 v[16:19],v[80:83],v[40:43],v[16:19]
s_waitcnt lgkmcnt(4)
v_mfma_f32_16x16x32_bf16 v[16:19],a[56:59],v[48:51],v[16:19]
ds_read_b128 v[40:43],v2 offset:36864
v_mfma_f32_16x16x32_bf16 v[16:19],a[168:171],v[52:55],v[16:19]
buffer_load_dwordx4 v2,s[32:35],s63 offen offset:3072 sc1 lds
v_mfma_f32_16x16x32_bf16 v[20:23],a[172:175],v[52:55],v[20:23]
ds_read_b128 v[44:47],v2 offset:51200
v_mfma_f32_16x16x32_bf16 v[20:23],a[60:63],v[48:51],v[20:23]
s_waitcnt lgkmcnt(4)
v_mfma_f32_16x16x32_bf16 v[20:23],v[92:95],v[56:59],v[20:23]
ds_read_b128 v[48:51],v2 offset:9216
v_mfma_f32_16x16x32_bf16 v[20:23],v[204:207],v[60:63],v[20:23]
s_add_u32 s63,s63,4096
s_add_u32 m0,s64,65536
v_mfma_f32_16x16x32_bf16 v[16:19],v[200:203],v[60:63],v[16:19]
ds_read_b128 v[52:55],v2 offset:23552
v_mfma_f32_16x16x32_bf16 v[16:19],v[88:91],v[56:59],v[16:19]
s_waitcnt lgkmcnt(4)
v_mfma_f32_16x16x32_bf16 v[16:19],a[64:67],v[32:35],v[16:19]
ds_read_b128 v[56:59],v2 offset:37888
v_mfma_f32_16x16x32_bf16 v[16:19],a[176:179],v[36:39],v[16:19]
buffer_load_dwordx4 v2,s[32:35],s63 offen offset:0 sc1 lds
v_mfma_f32_16x16x32_bf16 v[20:23],a[180:183],v[36:39],v[20:23]
ds_read_b128 v[60:63],v2 offset:52224
v_mfma_f32_16x16x32_bf16 v[20:23],a[68:71],v[32:35],v[20:23]
s_waitcnt lgkmcnt(4)
v_mfma_f32_16x16x32_bf16 v[20:23],v[100:103],v[40:43],v[20:23]
ds_read_b128 v[32:35],v2 offset:10240
v_mfma_f32_16x16x32_bf16 v[20:23],v[212:215],v[44:47],v[20:23]
buffer_load_dwordx4 v2,s[32:35],s63 offen offset:1024 sc1 lds
v_mfma_f32_16x16x32_bf16 v[16:19],v[208:211],v[44:47],v[16:19]
ds_read_b128 v[36:39],v2 offset:24576
v_mfma_f32_16x16x32_bf16 v[16:19],v[96:99],v[40:43],v[16:19]
s_waitcnt lgkmcnt(4)
v_mfma_f32_16x16x32_bf16 v[16:19],a[72:75],v[48:51],v[16:19]
ds_read_b128 v[40:43],v2 offset:38912
v_mfma_f32_16x16x32_bf16 v[16:19],a[184:187],v[52:55],v[16:19]
buffer_load_dwordx4 v2,s[32:35],s63 offen offset:2048 sc1 lds
v_mfma_f32_16x16x32_bf16 v[20:23],a[188:191],v[52:55],v[20:23]
ds_read_b128 v[44:47],v2 offset:53248
v_mfma_f32_16x16x32_bf16 v[20:23],a[76:79],v[48:51],v[20:23]
s_waitcnt lgkmcnt(4)
v_mfma_f32_16x16x32_bf16 v[20:23],v[108:111],v[56:59],v[20:23]
ds_read_b128 v[48:51],v2 offset:11264
v_mfma_f32_16x16x32_bf16 v[20:23],v[220:223],v[60:63],v[20:23]
buffer_load_dwordx2 v[6:7],v0,s[0:3],s66 offen sc1
v_mfma_f32_16x16x32_bf16 v[16:19],v[216:219],v[60:63],v[16:19]
ds_read_b128 v[52:55],v2 offset:25600
v_mfma_f32_16x16x32_bf16 v[16:19],v[104:107],v[56:59],v[16:19]
s_waitcnt lgkmcnt(4)
v_mfma_f32_16x16x32_bf16 v[16:19],a[80:83],v[32:35],v[16:19]
ds_read_b128 v[56:59],v2 offset:39936
v_mfma_f32_16x16x32_bf16 v[16:19],a[192:195],v[36:39],v[16:19]
buffer_load_dwordx4 v2,s[32:35],s63 offen offset:3072 sc1 lds
s_add_u32 s63,s63,4096
v_mfma_f32_16x16x32_bf16 v[20:23],a[196:199],v[36:39],v[20:23]
ds_read_b128 v[60:63],v2 offset:54272
s_add_u32 m0,s64,69632
v_mfma_f32_16x16x32_bf16 v[20:23],a[84:87],v[32:35],v[20:23]
s_waitcnt lgkmcnt(4)
v_mfma_f32_16x16x32_bf16 v[20:23],v[116:119],v[40:43],v[20:23]
ds_read_b128 v[32:35],v2 offset:12288
v_mfma_f32_16x16x32_bf16 v[20:23],v[228:231],v[44:47],v[20:23]
buffer_load_dwordx4 v2,s[32:35],s63 offen offset:0 sc1 lds
v_mfma_f32_16x16x32_bf16 v[16:19],v[224:227],v[44:47],v[16:19]
ds_read_b128 v[36:39],v2 offset:26624
v_mfma_f32_16x16x32_bf16 v[16:19],v[112:115],v[40:43],v[16:19]
s_waitcnt lgkmcnt(4)
v_mfma_f32_16x16x32_bf16 v[16:19],a[88:91],v[48:51],v[16:19]
ds_read_b128 v[40:43],v2 offset:40960
v_mfma_f32_16x16x32_bf16 v[16:19],a[200:203],v[52:55],v[16:19]
buffer_load_dwordx4 v2,s[32:35],s63 offen offset:1024 sc1 lds
s_add_u32 s63,s63,s31
v_mfma_f32_16x16x32_bf16 v[20:23],a[204:207],v[52:55],v[20:23]
ds_read_b128 v[44:47],v2 offset:55296
v_mfma_f32_16x16x32_bf16 v[20:23],a[92:95],v[48:51],v[20:23]
s_waitcnt lgkmcnt(4)
v_mfma_f32_16x16x32_bf16 v[20:23],v[124:127],v[56:59],v[20:23]
ds_read_b128 v[48:51],v2 offset:13312
s_waitcnt vmcnt(11)
v_mfma_f32_16x16x32_bf16 v[20:23],v[236:239],v[60:63],v[20:23]
v_lshlrev_b32 v24,16,v12
v_and_b32 v25,0xffff0000,v12
v_mfma_f32_16x16x32_bf16 v[16:19],v[232:235],v[60:63],v[16:19]
ds_read_b128 v[52:55],v2 offset:27648
v_lshlrev_b32 v26,16,v13
v_mfma_f32_16x16x32_bf16 v[16:19],v[120:123],v[56:59],v[16:19]
v_and_b32 v27,0xffff0000,v13
s_waitcnt lgkmcnt(4)
v_mfma_f32_16x16x32_bf16 v[16:19],a[96:99],v[32:35],v[16:19]
ds_read_b128 v[56:59],v2 offset:41984
v_lshlrev_b32 v28,16,v14
v_mfma_f32_16x16x32_bf16 v[16:19],a[208:211],v[36:39],v[16:19]
v_and_b32 v29,0xffff0000,v14
v_lshlrev_b32 v30,16,v15
v_mfma_f32_16x16x32_bf16 v[20:23],a[212:215],v[36:39],v[20:23]
s_barrier
v_mfma_f32_16x16x32_bf16 v[20:23],a[100:103],v[32:35],v[20:23]
ds_read_b128 v[60:63],v2 offset:56320
s_waitcnt lgkmcnt(4)
v_mfma_f32_16x16x32_bf16 v[20:23],v[132:135],v[40:43],v[20:23]
ds_read_b128 v[32:35],v4 offset:0
v_and_b32 v31,0xffff0000,v15
v_mfma_f32_16x16x32_bf16 v[20:23],v[244:247],v[44:47],v[20:23]
v_mfma_f32_16x16x32_bf16 v[16:19],v[240:243],v[44:47],v[16:19]
ds_read_b128 v[36:39],v4 offset:14336
v_mfma_f32_16x16x32_bf16 v[16:19],v[128:131],v[40:43],v[16:19]
s_waitcnt lgkmcnt(4)
v_mfma_f32_16x16x32_bf16 v[16:19],a[104:107],v[48:51],v[16:19]
ds_read_b128 v[40:43],v4 offset:28672
v_mfma_f32_16x16x32_bf16 v[16:19],a[216:219],v[52:55],v[16:19]
v_mfma_f32_16x16x32_bf16 v[20:23],a[220:223],v[52:55],v[20:23]
ds_read_b128 v[44:47],v4 offset:43008
v_mfma_f32_16x16x32_bf16 v[20:23],a[108:111],v[48:51],v[20:23]
s_waitcnt lgkmcnt(4)
v_mfma_f32_16x16x32_bf16 v[20:23],v[140:143],v[56:59],v[20:23]
ds_read_b128 v[48:51],v4 offset:1024
v_mfma_f32_16x16x32_bf16 v[20:23],v[252:255],v[60:63],v[20:23]
v_mfma_f32_16x16x32_bf16 v[16:19],v[248:251],v[60:63],v[16:19]
ds_read_b128 v[52:55],v4 offset:15360
s_sub_u32 s62,s62,1
v_mfma_f32_16x16x32_bf16 v[16:19],v[136:139],v[56:59],v[16:19]
s_cbranch_scc1 .L_dlb_1
s_waitcnt lgkmcnt(4)
v_mfma_f32_16x16x32_bf16 v[24:27],a[0:3],v[32:35],v[24:27]
ds_read_b128 v[56:59],v4 offset:29696
v_mfma_f32_16x16x32_bf16 v[24:27],a[112:115],v[36:39],v[24:27]
v_mfma_f32_16x16x32_bf16 v[28:31],a[116:119],v[36:39],v[28:31]
ds_read_b128 v[60:63],v4 offset:44032
v_mfma_f32_16x16x32_bf16 v[28:31],a[4:7],v[32:35],v[28:31]
s_add_u32 s68,s68,1187840
s_waitcnt lgkmcnt(4)
v_mfma_f32_16x16x32_bf16 v[28:31],a[228:231],v[40:43],v[28:31]
ds_read_b128 v[32:35],v4 offset:2048
v_mfma_f32_16x16x32_bf16 v[28:31],v[148:151],v[44:47],v[28:31]
buffer_store_dwordx4 v[20:23],v2,s[32:35],s68 offen offset:1024 sc1
v_mfma_f32_16x16x32_bf16 v[24:27],v[144:147],v[44:47],v[24:27]
ds_read_b128 v[36:39],v4 offset:16384
v_mfma_f32_16x16x32_bf16 v[24:27],a[224:227],v[40:43],v[24:27]
s_waitcnt lgkmcnt(4)
v_mfma_f32_16x16x32_bf16 v[24:27],a[8:11],v[48:51],v[24:27]
ds_read_b128 v[40:43],v4 offset:30720
v_mfma_f32_16x16x32_bf16 v[24:27],a[120:123],v[52:55],v[24:27]
buffer_store_dwordx4 v[16:19],v2,s[32:35],s68 offen sc1
s_mov_b32 s67,s68
v_mfma_f32_16x16x32_bf16 v[28:31],a[124:127],v[52:55],v[28:31]
ds_read_b128 v[44:47],v4 offset:45056
v_mfma_f32_16x16x32_bf16 v[28:31],a[12:15],v[48:51],v[28:31]
s_waitcnt lgkmcnt(4)
v_mfma_f32_16x16x32_bf16 v[28:31],a[236:239],v[56:59],v[28:31]
ds_read_b128 v[48:51],v4 offset:3072
v_mfma_f32_16x16x32_bf16 v[28:31],v[156:159],v[60:63],v[28:31]
v_mfma_f32_16x16x32_bf16 v[24:27],v[152:155],v[60:63],v[24:27]
ds_read_b128 v[52:55],v4 offset:17408
s_waitcnt vmcnt(5)
v_mfma_f32_16x16x32_bf16 v[24:27],a[232:235],v[56:59],v[24:27]
v_cmp_ne_u64 vcc,v[8:9],v[6:7]
s_waitcnt lgkmcnt(4)
v_mfma_f32_16x16x32_bf16 v[24:27],a[16:19],v[32:35],v[24:27]
ds_read_b128 v[56:59],v4 offset:31744
s_cbranch_vccnz .L_rtl1_1
.L_bkl1_1:
v_mfma_f32_16x16x32_bf16 v[24:27],a[128:131],v[36:39],v[24:27]
s_add_u32 s66,s66,s30
v_mfma_f32_16x16x32_bf16 v[28:31],a[132:135],v[36:39],v[28:31]
s_waitcnt vmcnt(2)
s_barrier
v_mfma_f32_16x16x32_bf16 v[28:31],a[20:23],v[32:35],v[28:31]
ds_read_b128 v[60:63],v4 offset:46080
s_waitcnt lgkmcnt(4)
v_mfma_f32_16x16x32_bf16 v[28:31],a[244:247],v[40:43],v[28:31]
ds_read_b128 v[32:35],v4 offset:4096
s_add_u32 m0,s64,0
v_mfma_f32_16x16x32_bf16 v[28:31],v[164:167],v[44:47],v[28:31]
buffer_load_dwordx4 v2,s[32:35],s63 offen offset:0 sc1 lds
v_mfma_f32_16x16x32_bf16 v[24:27],v[160:163],v[44:47],v[24:27]
ds_read_b128 v[36:39],v4 offset:18432
v_mfma_f32_16x16x32_bf16 v[24:27],a[240:243],v[40:43],v[24:27]
s_waitcnt lgkmcnt(4)
v_mfma_f32_16x16x32_bf16 v[24:27],a[24:27],v[48:51],v[24:27]
ds_read_b128 v[40:43],v4 offset:32768
v_mfma_f32_16x16x32_bf16 v[24:27],a[136:139],v[52:55],v[24:27]
v_mfma_f32_16x16x32_bf16 v[28:31],a[140:143],v[52:55],v[28:31]
ds_read_b128 v[44:47],v4 offset:47104
v_mfma_f32_16x16x32_bf16 v[28:31],a[28:31],v[48:51],v[28:31]
s_waitcnt lgkmcnt(4)
v_mfma_f32_16x16x32_bf16 v[28:31],a[252:255],v[56:59],v[28:31]
ds_read_b128 v[48:51],v4 offset:5120
v_mfma_f32_16x16x32_bf16 v[28:31],v[172:175],v[60:63],v[28:31]
buffer_load_dwordx4 v2,s[32:35],s63 offen offset:1024 sc1 lds
v_mfma_f32_16x16x32_bf16 v[24:27],v[168:171],v[60:63],v[24:27]
ds_read_b128 v[52:55],v4 offset:19456
v_mfma_f32_16x16x32_bf16 v[24:27],a[248:251],v[56:59],v[24:27]
s_waitcnt lgkmcnt(4)
v_mfma_f32_16x16x32_bf16 v[24:27],a[32:35],v[32:35],v[24:27]
ds_read_b128 v[56:59],v4 offset:33792
v_mfma_f32_16x16x32_bf16 v[24:27],a[144:147],v[36:39],v[24:27]
buffer_load_dwordx4 v2,s[32:35],s63 offen offset:2048 sc1 lds
v_mfma_f32_16x16x32_bf16 v[28:31],a[148:151],v[36:39],v[28:31]
ds_read_b128 v[60:63],v4 offset:48128
v_mfma_f32_16x16x32_bf16 v[28:31],a[36:39],v[32:35],v[28:31]
s_waitcnt lgkmcnt(4)
v_mfma_f32_16x16x32_bf16 v[28:31],v[68:71],v[40:43],v[28:31]
ds_read_b128 v[32:35],v4 offset:6144
v_mfma_f32_16x16x32_bf16 v[28:31],v[180:183],v[44:47],v[28:31]
buffer_load_dwordx4 v2,s[32:35],s63 offen offset:3072 sc1 lds
v_mfma_f32_16x16x32_bf16 v[24:27],v[176:179],v[44:47],v[24:27]
ds_read_b128 v[36:39],v4 offset:20480
v_mfma_f32_16x16x32_bf16 v[24:27],v[64:67],v[40:43],v[24:27]
s_waitcnt lgkmcnt(4)
v_mfma_f32_16x16x32_bf16 v[24:27],a[40:43],v[48:51],v[24:27]
ds_read_b128 v[40:43],v4 offset:34816
v_mfma_f32_16x16x32_bf16 v[24:27],a[152:155],v[52:55],v[24:27]
buffer_load_dwordx4 v[12:15],v2,s[32:35],s71 offen sc1
s_add_u32 s71,s71,s30
v_mfma_f32_16x16x32_bf16 v[28:31],a[156:159],v[52:55],v[28:31]
ds_read_b128 v[44:47],v4 offset:49152
s_add_u32 s63,s63,4096
v_mfma_f32_16x16x32_bf16 v[28:31],a[44:47],v[48:51],v[28:31]
s_add_u32 m0,s64,4096
s_waitcnt lgkmcnt(4)
v_mfma_f32_16x16x32_bf16 v[28:31],v[76:79],v[56:59],v[28:31]
ds_read_b128 v[48:51],v4 offset:7168
v_mfma_f32_16x16x32_bf16 v[28:31],v[188:191],v[60:63],v[28:31]
buffer_load_dwordx4 v2,s[32:35],s63 offen offset:0 sc1 lds
v_mfma_f32_16x16x32_bf16 v[24:27],v[184:187],v[60:63],v[24:27]
ds_read_b128 v[52:55],v4 offset:21504
v_mfma_f32_16x16x32_bf16 v[24:27],v[72:75],v[56:59],v[24:27]
s_waitcnt lgkmcnt(4)
v_mfma_f32_16x16x32_bf16 v[24:27],a[48:51],v[32:35],v[24:27]
ds_read_b128 v[56:59],v4 offset:35840
v_mfma_f32_16x16x32_bf16 v[24:27],a[160:163],v[36:39],v[24:27]
buffer_load_dwordx4 v2,s[32:35],s63 offen offset:1024 sc1 lds
v_mfma_f32_16x16x32_bf16 v[28:31],a[164:167],v[36:39],v[28:31]
ds_read_b128 v[60:63],v4 offset:50176
v_mfma_f32_16x16x32_bf16 v[28:31],a[52:55],v[32:35],v[28:31]
s_waitcnt lgkmcnt(4)
v_mfma_f32_16x16x32_bf16 v[28:31],v[84:87],v[40:43],v[28:31]
ds_read_b128 v[32:35],v4 offset:8192
v_mfma_f32_16x16x32_bf16 v[28:31],v[196:199],v[44:47],v[28:31]
buffer_load_dwordx4 v2,s[32:35],s63 offen offset:2048 sc1 lds
v_mfma_f32_16x16x32_bf16 v[24:27],v[192:195],v[44:47],v[24:27]
ds_read_b128 v[36:39],v4 offset:22528
v_mfma_f32_16x16x32_bf16 v[24:27],v[80:83],v[40:43],v[24:27]
s_waitcnt lgkmcnt(4)
v_mfma_f32_16x16x32_bf16 v[24:27],a[56:59],v[48:51],v[24:27]
ds_read_b128 v[40:43],v4 offset:36864
v_mfma_f32_16x16x32_bf16 v[24:27],a[168:171],v[52:55],v[24:27]
buffer_load_dwordx4 v2,s[32:35],s63 offen offset:3072 sc1 lds
v_mfma_f32_16x16x32_bf16 v[28:31],a[172:175],v[52:55],v[28:31]
ds_read_b128 v[44:47],v4 offset:51200
v_mfma_f32_16x16x32_bf16 v[28:31],a[60:63],v[48:51],v[28:31]
s_waitcnt lgkmcnt(4)
v_mfma_f32_16x16x32_bf16 v[28:31],v[92:95],v[56:59],v[28:31]
ds_read_b128 v[48:51],v4 offset:9216
v_mfma_f32_16x16x32_bf16 v[28:31],v[204:207],v[60:63],v[28:31]
s_add_u32 s63,s63,4096
s_add_u32 m0,s64,8192
v_mfma_f32_16x16x32_bf16 v[24:27],v[200:203],v[60:63],v[24:27]
ds_read_b128 v[52:55],v4 offset:23552
v_mfma_f32_16x16x32_bf16 v[24:27],v[88:91],v[56:59],v[24:27]
s_waitcnt lgkmcnt(4)
v_mfma_f32_16x16x32_bf16 v[24:27],a[64:67],v[32:35],v[24:27]
ds_read_b128 v[56:59],v4 offset:37888
v_mfma_f32_16x16x32_bf16 v[24:27],a[176:179],v[36:39],v[24:27]
buffer_load_dwordx4 v2,s[32:35],s63 offen offset:0 sc1 lds
v_mfma_f32_16x16x32_bf16 v[28:31],a[180:183],v[36:39],v[28:31]
ds_read_b128 v[60:63],v4 offset:52224
v_mfma_f32_16x16x32_bf16 v[28:31],a[68:71],v[32:35],v[28:31]
s_waitcnt lgkmcnt(4)
v_mfma_f32_16x16x32_bf16 v[28:31],v[100:103],v[40:43],v[28:31]
ds_read_b128 v[32:35],v4 offset:10240
v_mfma_f32_16x16x32_bf16 v[28:31],v[212:215],v[44:47],v[28:31]
buffer_load_dwordx4 v2,s[32:35],s63 offen offset:1024 sc1 lds
v_mfma_f32_16x16x32_bf16 v[24:27],v[208:211],v[44:47],v[24:27]
ds_read_b128 v[36:39],v4 offset:24576
v_mfma_f32_16x16x32_bf16 v[24:27],v[96:99],v[40:43],v[24:27]
s_waitcnt lgkmcnt(4)
v_mfma_f32_16x16x32_bf16 v[24:27],a[72:75],v[48:51],v[24:27]
ds_read_b128 v[40:43],v4 offset:38912
v_mfma_f32_16x16x32_bf16 v[24:27],a[184:187],v[52:55],v[24:27]
buffer_load_dwordx4 v2,s[32:35],s63 offen offset:2048 sc1 lds
v_mfma_f32_16x16x32_bf16 v[28:31],a[188:191],v[52:55],v[28:31]
ds_read_b128 v[44:47],v4 offset:53248
v_mfma_f32_16x16x32_bf16 v[28:31],a[76:79],v[48:51],v[28:31]
s_waitcnt lgkmcnt(4)
v_mfma_f32_16x16x32_bf16 v[28:31],v[108:111],v[56:59],v[28:31]
ds_read_b128 v[48:51],v4 offset:11264
v_mfma_f32_16x16x32_bf16 v[28:31],v[220:223],v[60:63],v[28:31]
buffer_load_dwordx2 v[6:7],v0,s[0:3],s66 offen sc1
v_mfma_f32_16x16x32_bf16 v[24:27],v[216:219],v[60:63],v[24:27]
ds_read_b128 v[52:55],v4 offset:25600
v_mfma_f32_16x16x32_bf16 v[24:27],v[104:107],v[56:59],v[24:27]
s_waitcnt lgkmcnt(4)
v_mfma_f32_16x16x32_bf16 v[24:27],a[80:83],v[32:35],v[24:27]
ds_read_b128 v[56:59],v4 offset:39936
v_mfma_f32_16x16x32_bf16 v[24:27],a[192:195],v[36:39],v[24:27]
buffer_load_dwordx4 v2,s[32:35],s63 offen offset:3072 sc1 lds
s_add_u32 s63,s63,4096
v_mfma_f32_16x16x32_bf16 v[28:31],a[196:199],v[36:39],v[28:31]
ds_read_b128 v[60:63],v4 offset:54272
s_add_u32 m0,s64,12288
v_mfma_f32_16x16x32_bf16 v[28:31],a[84:87],v[32:35],v[28:31]
s_waitcnt lgkmcnt(4)
v_mfma_f32_16x16x32_bf16 v[28:31],v[116:119],v[40:43],v[28:31]
ds_read_b128 v[32:35],v4 offset:12288
v_mfma_f32_16x16x32_bf16 v[28:31],v[228:231],v[44:47],v[28:31]
buffer_load_dwordx4 v2,s[32:35],s63 offen offset:0 sc1 lds
v_mfma_f32_16x16x32_bf16 v[24:27],v[224:227],v[44:47],v[24:27]
ds_read_b128 v[36:39],v4 offset:26624
v_mfma_f32_16x16x32_bf16 v[24:27],v[112:115],v[40:43],v[24:27]
s_waitcnt lgkmcnt(4)
v_mfma_f32_16x16x32_bf16 v[24:27],a[88:91],v[48:51],v[24:27]
ds_read_b128 v[40:43],v4 offset:40960
v_mfma_f32_16x16x32_bf16 v[24:27],a[200:203],v[52:55],v[24:27]
buffer_load_dwordx4 v2,s[32:35],s63 offen offset:1024 sc1 lds
s_add_u32 s63,s63,s31
v_mfma_f32_16x16x32_bf16 v[28:31],a[204:207],v[52:55],v[28:31]
ds_read_b128 v[44:47],v4 offset:55296
v_mfma_f32_16x16x32_bf16 v[28:31],a[92:95],v[48:51],v[28:31]
s_waitcnt lgkmcnt(4)
v_mfma_f32_16x16x32_bf16 v[28:31],v[124:127],v[56:59],v[28:31]
ds_read_b128 v[48:51],v4 offset:13312
s_waitcnt vmcnt(11)
v_mfma_f32_16x16x32_bf16 v[28:31],v[236:239],v[60:63],v[28:31]
v_lshlrev_b32 v16,16,v12
v_and_b32 v17,0xffff0000,v12
v_mfma_f32_16x16x32_bf16 v[24:27],v[232:235],v[60:63],v[24:27]
ds_read_b128 v[52:55],v4 offset:27648
v_lshlrev_b32 v18,16,v13
v_mfma_f32_16x16x32_bf16 v[24:27],v[120:123],v[56:59],v[24:27]
v_and_b32 v19,0xffff0000,v13
s_waitcnt lgkmcnt(4)
v_mfma_f32_16x16x32_bf16 v[24:27],a[96:99],v[32:35],v[24:27]
ds_read_b128 v[56:59],v4 offset:41984
v_lshlrev_b32 v20,16,v14
v_mfma_f32_16x16x32_bf16 v[24:27],a[208:211],v[36:39],v[24:27]
v_and_b32 v21,0xffff0000,v14
v_lshlrev_b32 v22,16,v15
v_mfma_f32_16x16x32_bf16 v[28:31],a[212:215],v[36:39],v[28:31]
s_barrier
v_mfma_f32_16x16x32_bf16 v[28:31],a[100:103],v[32:35],v[28:31]
ds_read_b128 v[60:63],v4 offset:56320
s_waitcnt lgkmcnt(4)
v_mfma_f32_16x16x32_bf16 v[28:31],v[132:135],v[40:43],v[28:31]
ds_read_b128 v[32:35],v2 offset:0
v_and_b32 v23,0xffff0000,v15
v_mfma_f32_16x16x32_bf16 v[28:31],v[244:247],v[44:47],v[28:31]
v_mfma_f32_16x16x32_bf16 v[24:27],v[240:243],v[44:47],v[24:27]
ds_read_b128 v[36:39],v2 offset:14336
v_mfma_f32_16x16x32_bf16 v[24:27],v[128:131],v[40:43],v[24:27]
s_waitcnt lgkmcnt(4)
v_mfma_f32_16x16x32_bf16 v[24:27],a[104:107],v[48:51],v[24:27]
ds_read_b128 v[40:43],v2 offset:28672
v_mfma_f32_16x16x32_bf16 v[24:27],a[216:219],v[52:55],v[24:27]
v_mfma_f32_16x16x32_bf16 v[28:31],a[220:223],v[52:55],v[28:31]
ds_read_b128 v[44:47],v2 offset:43008
v_mfma_f32_16x16x32_bf16 v[28:31],a[108:111],v[48:51],v[28:31]
s_waitcnt lgkmcnt(4)
v_mfma_f32_16x16x32_bf16 v[28:31],v[140:143],v[56:59],v[28:31]
ds_read_b128 v[48:51],v2 offset:1024
v_mfma_f32_16x16x32_bf16 v[28:31],v[252:255],v[60:63],v[28:31]
)#"
R"#(
v_mfma_f32_16x16x32_bf16 v[24:27],v[248:251],v[60:63],v[24:27]
ds_read_b128 v[52:55],v2 offset:15360
v_mfma_f32_16x16x32_bf16 v[24:27],v[136:139],v[56:59],v[24:27]
s_branch .L_dll_1
.L_dlb_1:
s_mov_b32 s62,s58
s_mov_b32 s70,s59
s_add_u32 s74,s74,8192
v_lshrrev_b32 v5,8,v2
v_lshl_add_u32 v5,v5,4,s74
v_and_b32 v3,128,v2
v_lshl_add_u32 v5,v3,2,v5
v_and_b32 v3,16,v2
v_lshl_add_u32 v5,v3,4,v5
v_and_b32 v3,96,v2
v_lshl_add_u32 v5,v3,1,v5
v_lshrrev_b32 v3,2,v2
v_add_u32 v3,s74,v3
s_add_u32 s66,s66,2375680
s_add_u32 s63,s63,2375680
s_add_u32 s71,s71,2375680
s_mov_b32 s68,98304
s_waitcnt vmcnt(3)
v_not_b32 v6,v8
s_mov_b32 s34,79446016
.p2align 6
.L_dml_1:
s_waitcnt lgkmcnt(4)
v_mfma_f32_16x16x32_bf16 v[24:27],a[0:3],v[32:35],v[24:27]
ds_read_b128 v[56:59],v4 offset:29696
v_mfma_f32_16x16x32_bf16 v[24:27],a[112:115],v[36:39],v[24:27]
v_mfma_f32_16x16x32_bf16 v[28:31],a[116:119],v[36:39],v[28:31]
ds_read_b128 v[60:63],v4 offset:44032
v_mfma_f32_16x16x32_bf16 v[28:31],a[4:7],v[32:35],v[28:31]
v_cvt_pk_bf16_f32 v15,v22,v23
s_waitcnt lgkmcnt(4)
v_mfma_f32_16x16x32_bf16 v[28:31],a[228:231],v[40:43],v[28:31]
ds_read_b128 v[32:35],v4 offset:2048
v_cvt_pk_bf16_f32 v14,v20,v21
v_mfma_f32_16x16x32_bf16 v[28:31],v[148:151],v[44:47],v[28:31]
v_cvt_pk_bf16_f32 v13,v18,v19
v_cvt_pk_bf16_f32 v12,v16,v17
v_mfma_f32_16x16x32_bf16 v[24:27],v[144:147],v[44:47],v[24:27]
ds_read_b128 v[36:39],v4 offset:16384
ds_write_b128 v5,v[12:15]
v_mfma_f32_16x16x32_bf16 v[24:27],a[224:227],v[40:43],v[24:27]
ds_read2st64_b32 v[12:13],v3 offset0:0 offset1:1
s_waitcnt lgkmcnt(6)
v_mfma_f32_16x16x32_bf16 v[24:27],a[8:11],v[48:51],v[24:27]
ds_read_b128 v[40:43],v4 offset:30720
ds_read2st64_b32 v[14:15],v3 offset0:2 offset1:3
v_mfma_f32_16x16x32_bf16 v[24:27],a[120:123],v[52:55],v[24:27]
v_mfma_f32_16x16x32_bf16 v[28:31],a[124:127],v[52:55],v[28:31]
ds_read_b128 v[44:47],v4 offset:45056
v_mfma_f32_16x16x32_bf16 v[28:31],a[12:15],v[48:51],v[28:31]
s_waitcnt lgkmcnt(7)
v_mfma_f32_16x16x32_bf16 v[28:31],a[236:239],v[56:59],v[28:31]
ds_read_b128 v[48:51],v4 offset:3072
v_mfma_f32_16x16x32_bf16 v[28:31],v[156:159],v[60:63],v[28:31]
s_waitcnt lgkmcnt(2)
buffer_atomic_pk_add_bf16 v12,v1,s[44:47],s65 offen offset:0 sc1
v_mfma_f32_16x16x32_bf16 v[24:27],v[152:155],v[60:63],v[24:27]
ds_read_b128 v[52:55],v4 offset:17408
s_waitcnt vmcnt(4)
v_mfma_f32_16x16x32_bf16 v[24:27],a[232:235],v[56:59],v[24:27]
v_cmp_ne_u64 vcc,v[8:9],v[6:7]
v_mfma_f32_16x16x32_bf16 v[24:27],a[16:19],v[32:35],v[24:27]
ds_read_b128 v[56:59],v4 offset:31744
s_cbranch_vccnz .L_rtm1_1
.L_bkm1_1:
v_mfma_f32_16x16x32_bf16 v[24:27],a[128:131],v[36:39],v[24:27]
s_add_u32 s66,s66,s30
s_add_u32 s70,s70,s75
v_mfma_f32_16x16x32_bf16 v[28:31],a[132:135],v[36:39],v[28:31]
s_waitcnt vmcnt(1)
s_barrier
v_mfma_f32_16x16x32_bf16 v[28:31],a[20:23],v[32:35],v[28:31]
buffer_atomic_pk_add_bf16 v13,v1,s[44:47],s65 offen offset:2048 sc1
s_waitcnt lgkmcnt(3)
v_mfma_f32_16x16x32_bf16 v[28:31],a[244:247],v[40:43],v[28:31]
ds_read_b128 v[60:63],v4 offset:46080
ds_read_b128 v[32:35],v4 offset:4096
v_mfma_f32_16x16x32_bf16 v[28:31],v[164:167],v[44:47],v[28:31]
s_add_u32 m0,s64,0
s_add_u32 s65,s65,16384
v_mfma_f32_16x16x32_bf16 v[24:27],v[160:163],v[44:47],v[24:27]
ds_read_b128 v[36:39],v4 offset:18432
v_mfma_f32_16x16x32_bf16 v[24:27],a[240:243],v[40:43],v[24:27]
buffer_atomic_pk_add_bf16 v14,v1,s[44:47],s65 offen offset:0 sc1
s_waitcnt lgkmcnt(4)
v_mfma_f32_16x16x32_bf16 v[24:27],a[24:27],v[48:51],v[24:27]
ds_read_b128 v[40:43],v4 offset:32768
v_mfma_f32_16x16x32_bf16 v[24:27],a[136:139],v[52:55],v[24:27]
buffer_atomic_pk_add_bf16 v15,v1,s[44:47],s65 offen offset:2048 sc1
s_and_b32 s65,s70,0x3f8000
v_mfma_f32_16x16x32_bf16 v[28:31],a[140:143],v[52:55],v[28:31]
ds_read_b128 v[44:47],v4 offset:47104
v_mfma_f32_16x16x32_bf16 v[28:31],a[28:31],v[48:51],v[28:31]
s_waitcnt lgkmcnt(4)
v_mfma_f32_16x16x32_bf16 v[28:31],a[252:255],v[56:59],v[28:31]
ds_read_b128 v[48:51],v4 offset:5120
v_mfma_f32_16x16x32_bf16 v[28:31],v[172:175],v[60:63],v[28:31]
buffer_load_dwordx4 v2,s[32:35],s63 offen offset:0 sc1 lds
v_mfma_f32_16x16x32_bf16 v[24:27],v[168:171],v[60:63],v[24:27]
ds_read_b128 v[52:55],v4 offset:19456
v_mfma_f32_16x16x32_bf16 v[24:27],a[248:251],v[56:59],v[24:27]
s_waitcnt lgkmcnt(4)
v_mfma_f32_16x16x32_bf16 v[24:27],a[32:35],v[32:35],v[24:27]
ds_read_b128 v[56:59],v4 offset:33792
v_mfma_f32_16x16x32_bf16 v[24:27],a[144:147],v[36:39],v[24:27]
buffer_load_dwordx4 v2,s[32:35],s63 offen offset:1024 sc1 lds
v_mfma_f32_16x16x32_bf16 v[28:31],a[148:151],v[36:39],v[28:31]
ds_read_b128 v[60:63],v4 offset:48128
v_mfma_f32_16x16x32_bf16 v[28:31],a[36:39],v[32:35],v[28:31]
s_waitcnt lgkmcnt(4)
v_mfma_f32_16x16x32_bf16 v[28:31],v[68:71],v[40:43],v[28:31]
ds_read_b128 v[32:35],v4 offset:6144
v_mfma_f32_16x16x32_bf16 v[28:31],v[180:183],v[44:47],v[28:31]
buffer_load_dwordx4 v2,s[32:35],s63 offen offset:2048 sc1 lds
v_mfma_f32_16x16x32_bf16 v[24:27],v[176:179],v[44:47],v[24:27]
ds_read_b128 v[36:39],v4 offset:20480
v_mfma_f32_16x16x32_bf16 v[24:27],v[64:67],v[40:43],v[24:27]
s_waitcnt lgkmcnt(4)
v_mfma_f32_16x16x32_bf16 v[24:27],a[40:43],v[48:51],v[24:27]
ds_read_b128 v[40:43],v4 offset:34816
v_mfma_f32_16x16x32_bf16 v[24:27],a[152:155],v[52:55],v[24:27]
buffer_load_dwordx4 v2,s[32:35],s63 offen offset:3072 sc1 lds
v_mfma_f32_16x16x32_bf16 v[28:31],a[156:159],v[52:55],v[28:31]
ds_read_b128 v[44:47],v4 offset:49152
v_mfma_f32_16x16x32_bf16 v[28:31],a[44:47],v[48:51],v[28:31]
s_waitcnt lgkmcnt(4)
v_mfma_f32_16x16x32_bf16 v[28:31],v[76:79],v[56:59],v[28:31]
ds_read_b128 v[48:51],v4 offset:7168
v_mfma_f32_16x16x32_bf16 v[28:31],v[188:191],v[60:63],v[28:31]
buffer_load_dwordx4 v[12:15],v2,s[32:35],s71 offen sc1
s_add_u32 s71,s71,s30
v_mfma_f32_16x16x32_bf16 v[24:27],v[184:187],v[60:63],v[24:27]
ds_read_b128 v[52:55],v4 offset:21504
s_add_u32 s63,s63,4096
v_mfma_f32_16x16x32_bf16 v[24:27],v[72:75],v[56:59],v[24:27]
s_add_u32 m0,s64,4096
s_waitcnt lgkmcnt(4)
v_mfma_f32_16x16x32_bf16 v[24:27],a[48:51],v[32:35],v[24:27]
ds_read_b128 v[56:59],v4 offset:35840
v_mfma_f32_16x16x32_bf16 v[24:27],a[160:163],v[36:39],v[24:27]
buffer_load_dwordx4 v2,s[32:35],s63 offen offset:0 sc1 lds
v_mfma_f32_16x16x32_bf16 v[28:31],a[164:167],v[36:39],v[28:31]
ds_read_b128 v[60:63],v4 offset:50176
v_mfma_f32_16x16x32_bf16 v[28:31],a[52:55],v[32:35],v[28:31]
s_waitcnt lgkmcnt(4)
v_mfma_f32_16x16x32_bf16 v[28:31],v[84:87],v[40:43],v[28:31]
ds_read_b128 v[32:35],v4 offset:8192
v_mfma_f32_16x16x32_bf16 v[28:31],v[196:199],v[44:47],v[28:31]
buffer_load_dwordx4 v2,s[32:35],s63 offen offset:1024 sc1 lds
v_mfma_f32_16x16x32_bf16 v[24:27],v[192:195],v[44:47],v[24:27]
ds_read_b128 v[36:39],v4 offset:22528
v_mfma_f32_16x16x32_bf16 v[24:27],v[80:83],v[40:43],v[24:27]
s_waitcnt lgkmcnt(4)
v_mfma_f32_16x16x32_bf16 v[24:27],a[56:59],v[48:51],v[24:27]
ds_read_b128 v[40:43],v4 offset:36864
v_mfma_f32_16x16x32_bf16 v[24:27],a[168:171],v[52:55],v[24:27]
buffer_load_dwordx4 v2,s[32:35],s63 offen offset:2048 sc1 lds
v_mfma_f32_16x16x32_bf16 v[28:31],a[172:175],v[52:55],v[28:31]
ds_read_b128 v[44:47],v4 offset:51200
v_mfma_f32_16x16x32_bf16 v[28:31],a[60:63],v[48:51],v[28:31]
s_waitcnt lgkmcnt(4)
v_mfma_f32_16x16x32_bf16 v[28:31],v[92:95],v[56:59],v[28:31]
ds_read_b128 v[48:51],v4 offset:9216
v_mfma_f32_16x16x32_bf16 v[28:31],v[204:207],v[60:63],v[28:31]
buffer_load_dwordx4 v2,s[32:35],s63 offen offset:3072 sc1 lds
s_add_u32 s63,s63,4096
v_mfma_f32_16x16x32_bf16 v[24:27],v[200:203],v[60:63],v[24:27]
ds_read_b128 v[52:55],v4 offset:23552
s_add_u32 m0,s64,8192
v_mfma_f32_16x16x32_bf16 v[24:27],v[88:91],v[56:59],v[24:27]
s_waitcnt lgkmcnt(4)
v_mfma_f32_16x16x32_bf16 v[24:27],a[64:67],v[32:35],v[24:27]
ds_read_b128 v[56:59],v4 offset:37888
v_mfma_f32_16x16x32_bf16 v[24:27],a[176:179],v[36:39],v[24:27]
buffer_load_dwordx4 v2,s[32:35],s63 offen offset:0 sc1 lds
v_mfma_f32_16x16x32_bf16 v[28:31],a[180:183],v[36:39],v[28:31]
ds_read_b128 v[60:63],v4 offset:52224
v_mfma_f32_16x16x32_bf16 v[28:31],a[68:71],v[32:35],v[28:31]
s_waitcnt lgkmcnt(4)
v_mfma_f32_16x16x32_bf16 v[28:31],v[100:103],v[40:43],v[28:31]
ds_read_b128 v[32:35],v4 offset:10240
v_mfma_f32_16x16x32_bf16 v[28:31],v[212:215],v[44:47],v[28:31]
buffer_load_dwordx4 v2,s[32:35],s63 offen offset:1024 sc1 lds
v_mfma_f32_16x16x32_bf16 v[24:27],v[208:211],v[44:47],v[24:27]
ds_read_b128 v[36:39],v4 offset:24576
v_mfma_f32_16x16x32_bf16 v[24:27],v[96:99],v[40:43],v[24:27]
s_waitcnt lgkmcnt(4)
v_mfma_f32_16x16x32_bf16 v[24:27],a[72:75],v[48:51],v[24:27]
ds_read_b128 v[40:43],v4 offset:38912
v_mfma_f32_16x16x32_bf16 v[24:27],a[184:187],v[52:55],v[24:27]
buffer_load_dwordx4 v2,s[32:35],s63 offen offset:2048 sc1 lds
v_mfma_f32_16x16x32_bf16 v[28:31],a[188:191],v[52:55],v[28:31]
ds_read_b128 v[44:47],v4 offset:53248
v_mfma_f32_16x16x32_bf16 v[28:31],a[76:79],v[48:51],v[28:31]
s_waitcnt lgkmcnt(4)
v_mfma_f32_16x16x32_bf16 v[28:31],v[108:111],v[56:59],v[28:31]
ds_read_b128 v[48:51],v4 offset:11264
v_mfma_f32_16x16x32_bf16 v[28:31],v[220:223],v[60:63],v[28:31]
buffer_load_dwordx2 v[6:7],v0,s[0:3],s66 offen sc1
v_mfma_f32_16x16x32_bf16 v[24:27],v[216:219],v[60:63],v[24:27]
ds_read_b128 v[52:55],v4 offset:25600
v_mfma_f32_16x16x32_bf16 v[24:27],v[104:107],v[56:59],v[24:27]
s_waitcnt lgkmcnt(4)
v_mfma_f32_16x16x32_bf16 v[24:27],a[80:83],v[32:35],v[24:27]
ds_read_b128 v[56:59],v4 offset:39936
v_mfma_f32_16x16x32_bf16 v[24:27],a[192:195],v[36:39],v[24:27]
buffer_load_dwordx4 v2,s[32:35],s63 offen offset:3072 sc1 lds
s_add_u32 s63,s63,4096
v_mfma_f32_16x16x32_bf16 v[28:31],a[196:199],v[36:39],v[28:31]
ds_read_b128 v[60:63],v4 offset:54272
s_add_u32 m0,s64,12288
v_mfma_f32_16x16x32_bf16 v[28:31],a[84:87],v[32:35],v[28:31]
s_waitcnt lgkmcnt(4)
v_mfma_f32_16x16x32_bf16 v[28:31],v[116:119],v[40:43],v[28:31]
ds_read_b128 v[32:35],v4 offset:12288
v_mfma_f32_16x16x32_bf16 v[28:31],v[228:231],v[44:47],v[28:31]
buffer_load_dwordx4 v2,s[32:35],s63 offen offset:0 sc1 lds
v_mfma_f32_16x16x32_bf16 v[24:27],v[224:227],v[44:47],v[24:27]
ds_read_b128 v[36:39],v4 offset:26624
v_mfma_f32_16x16x32_bf16 v[24:27],v[112:115],v[40:43],v[24:27]
s_waitcnt lgkmcnt(4)
v_mfma_f32_16x16x32_bf16 v[24:27],a[88:91],v[48:51],v[24:27]
ds_read_b128 v[40:43],v4 offset:40960
v_mfma_f32_16x16x32_bf16 v[24:27],a[200:203],v[52:55],v[24:27]
buffer_load_dwordx4 v2,s[32:35],s63 offen offset:1024 sc1 lds
s_add_u32 s63,s63,s31
v_mfma_f32_16x16x32_bf16 v[28:31],a[204:207],v[52:55],v[28:31]
ds_read_b128 v[44:47],v4 offset:55296
v_mfma_f32_16x16x32_bf16 v[28:31],a[92:95],v[48:51],v[28:31]
s_waitcnt lgkmcnt(4)
v_mfma_f32_16x16x32_bf16 v[28:31],v[124:127],v[56:59],v[28:31]
ds_read_b128 v[48:51],v4 offset:13312
s_waitcnt vmcnt(11)
v_mfma_f32_16x16x32_bf16 v[28:31],v[236:239],v[60:63],v[28:31]
v_lshlrev_b32 v16,16,v12
v_and_b32 v17,0xffff0000,v12
v_mfma_f32_16x16x32_bf16 v[24:27],v[232:235],v[60:63],v[24:27]
ds_read_b128 v[52:55],v4 offset:27648
v_lshlrev_b32 v18,16,v13
v_mfma_f32_16x16x32_bf16 v[24:27],v[120:123],v[56:59],v[24:27]
v_and_b32 v19,0xffff0000,v13
s_waitcnt lgkmcnt(4)
v_mfma_f32_16x16x32_bf16 v[24:27],a[96:99],v[32:35],v[24:27]
ds_read_b128 v[56:59],v4 offset:41984
v_lshlrev_b32 v20,16,v14
v_mfma_f32_16x16x32_bf16 v[24:27],a[208:211],v[36:39],v[24:27]
v_and_b32 v21,0xffff0000,v14
v_lshlrev_b32 v22,16,v15
v_mfma_f32_16x16x32_bf16 v[28:31],a[212:215],v[36:39],v[28:31]
s_barrier
v_mfma_f32_16x16x32_bf16 v[28:31],a[100:103],v[32:35],v[28:31]
ds_read_b128 v[60:63],v4 offset:56320
s_waitcnt lgkmcnt(4)
v_mfma_f32_16x16x32_bf16 v[28:31],v[132:135],v[40:43],v[28:31]
ds_read_b128 v[32:35],v2 offset:0
v_and_b32 v23,0xffff0000,v15
v_mfma_f32_16x16x32_bf16 v[28:31],v[244:247],v[44:47],v[28:31]
v_mfma_f32_16x16x32_bf16 v[24:27],v[240:243],v[44:47],v[24:27]
ds_read_b128 v[36:39],v2 offset:14336
v_mfma_f32_16x16x32_bf16 v[24:27],v[128:131],v[40:43],v[24:27]
s_waitcnt lgkmcnt(4)
v_mfma_f32_16x16x32_bf16 v[24:27],a[104:107],v[48:51],v[24:27]
ds_read_b128 v[40:43],v2 offset:28672
v_mfma_f32_16x16x32_bf16 v[24:27],a[216:219],v[52:55],v[24:27]
v_mfma_f32_16x16x32_bf16 v[28:31],a[220:223],v[52:55],v[28:31]
ds_read_b128 v[44:47],v2 offset:43008
v_mfma_f32_16x16x32_bf16 v[28:31],a[108:111],v[48:51],v[28:31]
s_waitcnt lgkmcnt(4)
v_mfma_f32_16x16x32_bf16 v[28:31],v[140:143],v[56:59],v[28:31]
ds_read_b128 v[48:51],v2 offset:1024
v_mfma_f32_16x16x32_bf16 v[28:31],v[252:255],v[60:63],v[28:31]
v_mfma_f32_16x16x32_bf16 v[24:27],v[248:251],v[60:63],v[24:27]
ds_read_b128 v[52:55],v2 offset:15360
s_sub_u32 s62,s62,1
v_mfma_f32_16x16x32_bf16 v[24:27],v[136:139],v[56:59],v[24:27]
s_cbranch_scc1 .L_dmt_1
s_waitcnt lgkmcnt(4)
v_mfma_f32_16x16x32_bf16 v[16:19],a[0:3],v[32:35],v[16:19]
ds_read_b128 v[56:59],v2 offset:29696
v_mfma_f32_16x16x32_bf16 v[16:19],a[112:115],v[36:39],v[16:19]
v_mfma_f32_16x16x32_bf16 v[20:23],a[116:119],v[36:39],v[20:23]
ds_read_b128 v[60:63],v2 offset:44032
v_mfma_f32_16x16x32_bf16 v[20:23],a[4:7],v[32:35],v[20:23]
v_cvt_pk_bf16_f32 v15,v30,v31
s_waitcnt lgkmcnt(4)
v_mfma_f32_16x16x32_bf16 v[20:23],a[228:231],v[40:43],v[20:23]
ds_read_b128 v[32:35],v2 offset:2048
v_cvt_pk_bf16_f32 v14,v28,v29
v_mfma_f32_16x16x32_bf16 v[20:23],v[148:151],v[44:47],v[20:23]
v_cvt_pk_bf16_f32 v13,v26,v27
v_cvt_pk_bf16_f32 v12,v24,v25
v_mfma_f32_16x16x32_bf16 v[16:19],v[144:147],v[44:47],v[16:19]
ds_read_b128 v[36:39],v2 offset:16384
ds_write_b128 v5,v[12:15]
v_mfma_f32_16x16x32_bf16 v[16:19],a[224:227],v[40:43],v[16:19]
ds_read2st64_b32 v[12:13],v3 offset0:0 offset1:1
s_waitcnt lgkmcnt(6)
v_mfma_f32_16x16x32_bf16 v[16:19],a[8:11],v[48:51],v[16:19]
ds_read_b128 v[40:43],v2 offset:30720
ds_read2st64_b32 v[14:15],v3 offset0:2 offset1:3
v_mfma_f32_16x16x32_bf16 v[16:19],a[120:123],v[52:55],v[16:19]
v_mfma_f32_16x16x32_bf16 v[20:23],a[124:127],v[52:55],v[20:23]
ds_read_b128 v[44:47],v2 offset:45056
v_mfma_f32_16x16x32_bf16 v[20:23],a[12:15],v[48:51],v[20:23]
s_waitcnt lgkmcnt(7)
v_mfma_f32_16x16x32_bf16 v[20:23],a[236:239],v[56:59],v[20:23]
ds_read_b128 v[48:51],v2 offset:3072
v_mfma_f32_16x16x32_bf16 v[20:23],v[156:159],v[60:63],v[20:23]
s_waitcnt lgkmcnt(2)
buffer_atomic_pk_add_bf16 v12,v1,s[44:47],s65 offen offset:0 sc1
v_mfma_f32_16x16x32_bf16 v[16:19],v[152:155],v[60:63],v[16:19]
ds_read_b128 v[52:55],v2 offset:17408
s_waitcnt vmcnt(4)
v_mfma_f32_16x16x32_bf16 v[16:19],a[232:235],v[56:59],v[16:19]
v_cmp_ne_u64 vcc,v[8:9],v[6:7]
v_mfma_f32_16x16x32_bf16 v[16:19],a[16:19],v[32:35],v[16:19]
ds_read_b128 v[56:59],v2 offset:31744
s_cbranch_vccnz .L_rtm0_1
.L_bkm0_1:
v_mfma_f32_16x16x32_bf16 v[16:19],a[128:131],v[36:39],v[16:19]
s_add_u32 s66,s66,s30
s_add_u32 s70,s70,s68
v_mfma_f32_16x16x32_bf16 v[20:23],a[132:135],v[36:39],v[20:23]
s_waitcnt vmcnt(1)
s_barrier
v_mfma_f32_16x16x32_bf16 v[20:23],a[20:23],v[32:35],v[20:23]
buffer_atomic_pk_add_bf16 v13,v1,s[44:47],s65 offen offset:2048 sc1
s_waitcnt lgkmcnt(3)
v_mfma_f32_16x16x32_bf16 v[20:23],a[244:247],v[40:43],v[20:23]
ds_read_b128 v[60:63],v2 offset:46080
ds_read_b128 v[32:35],v2 offset:4096
v_mfma_f32_16x16x32_bf16 v[20:23],v[164:167],v[44:47],v[20:23]
s_add_u32 m0,s64,57344
s_add_u32 s65,s65,16384
v_mfma_f32_16x16x32_bf16 v[16:19],v[160:163],v[44:47],v[16:19]
ds_read_b128 v[36:39],v2 offset:18432
v_mfma_f32_16x16x32_bf16 v[16:19],a[240:243],v[40:43],v[16:19]
buffer_atomic_pk_add_bf16 v14,v1,s[44:47],s65 offen offset:0 sc1
s_waitcnt lgkmcnt(4)
v_mfma_f32_16x16x32_bf16 v[16:19],a[24:27],v[48:51],v[16:19]
ds_read_b128 v[40:43],v2 offset:32768
s_mov_b32 s68,s75
v_mfma_f32_16x16x32_bf16 v[16:19],a[136:139],v[52:55],v[16:19]
buffer_atomic_pk_add_bf16 v15,v1,s[44:47],s65 offen offset:2048 sc1
s_and_b32 s65,s70,0x3f8000
v_mfma_f32_16x16x32_bf16 v[20:23],a[140:143],v[52:55],v[20:23]
ds_read_b128 v[44:47],v2 offset:47104
v_mfma_f32_16x16x32_bf16 v[20:23],a[28:31],v[48:51],v[20:23]
s_waitcnt lgkmcnt(4)
v_mfma_f32_16x16x32_bf16 v[20:23],a[252:255],v[56:59],v[20:23]
ds_read_b128 v[48:51],v2 offset:5120
v_mfma_f32_16x16x32_bf16 v[20:23],v[172:175],v[60:63],v[20:23]
buffer_load_dwordx4 v2,s[32:35],s63 offen offset:0 sc1 lds
v_mfma_f32_16x16x32_bf16 v[16:19],v[168:171],v[60:63],v[16:19]
ds_read_b128 v[52:55],v2 offset:19456
v_mfma_f32_16x16x32_bf16 v[16:19],a[248:251],v[56:59],v[16:19]
s_waitcnt lgkmcnt(4)
v_mfma_f32_16x16x32_bf16 v[16:19],a[32:35],v[32:35],v[16:19]
ds_read_b128 v[56:59],v2 offset:33792
v_mfma_f32_16x16x32_bf16 v[16:19],a[144:147],v[36:39],v[16:19]
buffer_load_dwordx4 v2,s[32:35],s63 offen offset:1024 sc1 lds
v_mfma_f32_16x16x32_bf16 v[20:23],a[148:151],v[36:39],v[20:23]
ds_read_b128 v[60:63],v2 offset:48128
v_mfma_f32_16x16x32_bf16 v[20:23],a[36:39],v[32:35],v[20:23]
s_waitcnt lgkmcnt(4)
v_mfma_f32_16x16x32_bf16 v[20:23],v[68:71],v[40:43],v[20:23]
ds_read_b128 v[32:35],v2 offset:6144
v_mfma_f32_16x16x32_bf16 v[20:23],v[180:183],v[44:47],v[20:23]
buffer_load_dwordx4 v2,s[32:35],s63 offen offset:2048 sc1 lds
v_mfma_f32_16x16x32_bf16 v[16:19],v[176:179],v[44:47],v[16:19]
ds_read_b128 v[36:39],v2 offset:20480
v_mfma_f32_16x16x32_bf16 v[16:19],v[64:67],v[40:43],v[16:19]
s_waitcnt lgkmcnt(4)
v_mfma_f32_16x16x32_bf16 v[16:19],a[40:43],v[48:51],v[16:19]
ds_read_b128 v[40:43],v2 offset:34816
v_mfma_f32_16x16x32_bf16 v[16:19],a[152:155],v[52:55],v[16:19]
buffer_load_dwordx4 v2,s[32:35],s63 offen offset:3072 sc1 lds
v_mfma_f32_16x16x32_bf16 v[20:23],a[156:159],v[52:55],v[20:23]
ds_read_b128 v[44:47],v2 offset:49152
v_mfma_f32_16x16x32_bf16 v[20:23],a[44:47],v[48:51],v[20:23]
s_waitcnt lgkmcnt(4)
v_mfma_f32_16x16x32_bf16 v[20:23],v[76:79],v[56:59],v[20:23]
ds_read_b128 v[48:51],v2 offset:7168
v_mfma_f32_16x16x32_bf16 v[20:23],v[188:191],v[60:63],v[20:23]
buffer_load_dwordx4 v[12:15],v2,s[32:35],s71 offen sc1
s_add_u32 s71,s71,s30
v_mfma_f32_16x16x32_bf16 v[16:19],v[184:187],v[60:63],v[16:19]
ds_read_b128 v[52:55],v2 offset:21504
s_add_u32 s63,s63,4096
v_mfma_f32_16x16x32_bf16 v[16:19],v[72:75],v[56:59],v[16:19]
s_add_u32 m0,s64,61440
s_waitcnt lgkmcnt(4)
v_mfma_f32_16x16x32_bf16 v[16:19],a[48:51],v[32:35],v[16:19]
ds_read_b128 v[56:59],v2 offset:35840
v_mfma_f32_16x16x32_bf16 v[16:19],a[160:163],v[36:39],v[16:19]
buffer_load_dwordx4 v2,s[32:35],s63 offen offset:0 sc1 lds
v_mfma_f32_16x16x32_bf16 v[20:23],a[164:167],v[36:39],v[20:23]
ds_read_b128 v[60:63],v2 offset:50176
v_mfma_f32_16x16x32_bf16 v[20:23],a[52:55],v[32:35],v[20:23]
s_waitcnt lgkmcnt(4)
v_mfma_f32_16x16x32_bf16 v[20:23],v[84:87],v[40:43],v[20:23]
ds_read_b128 v[32:35],v2 offset:8192
v_mfma_f32_16x16x32_bf16 v[20:23],v[196:199],v[44:47],v[20:23]
buffer_load_dwordx4 v2,s[32:35],s63 offen offset:1024 sc1 lds
v_mfma_f32_16x16x32_bf16 v[16:19],v[192:195],v[44:47],v[16:19]
ds_read_b128 v[36:39],v2 offset:22528
v_mfma_f32_16x16x32_bf16 v[16:19],v[80:83],v[40:43],v[16:19]
s_waitcnt lgkmcnt(4)
v_mfma_f32_16x16x32_bf16 v[16:19],a[56:59],v[48:51],v[16:19]
ds_read_b128 v[40:43],v2 offset:36864
v_mfma_f32_16x16x32_bf16 v[16:19],a[168:171],v[52:55],v[16:19]
buffer_load_dwordx4 v2,s[32:35],s63 offen offset:2048 sc1 lds
v_mfma_f32_16x16x32_bf16 v[20:23],a[172:175],v[52:55],v[20:23]
ds_read_b128 v[44:47],v2 offset:51200
v_mfma_f32_16x16x32_bf16 v[20:23],a[60:63],v[48:51],v[20:23]
s_waitcnt lgkmcnt(4)
v_mfma_f32_16x16x32_bf16 v[20:23],v[92:95],v[56:59],v[20:23]
ds_read_b128 v[48:51],v2 offset:9216
v_mfma_f32_16x16x32_bf16 v[20:23],v[204:207],v[60:63],v[20:23]
buffer_load_dwordx4 v2,s[32:35],s63 offen offset:3072 sc1 lds
s_add_u32 s63,s63,4096
v_mfma_f32_16x16x32_bf16 v[16:19],v[200:203],v[60:63],v[16:19]
ds_read_b128 v[52:55],v2 offset:23552
s_add_u32 m0,s64,65536
v_mfma_f32_16x16x32_bf16 v[16:19],v[88:91],v[56:59],v[16:19]
s_waitcnt lgkmcnt(4)
v_mfma_f32_16x16x32_bf16 v[16:19],a[64:67],v[32:35],v[16:19]
ds_read_b128 v[56:59],v2 offset:37888
v_mfma_f32_16x16x32_bf16 v[16:19],a[176:179],v[36:39],v[16:19]
buffer_load_dwordx4 v2,s[32:35],s63 offen offset:0 sc1 lds
v_mfma_f32_16x16x32_bf16 v[20:23],a[180:183],v[36:39],v[20:23]
ds_read_b128 v[60:63],v2 offset:52224
v_mfma_f32_16x16x32_bf16 v[20:23],a[68:71],v[32:35],v[20:23]
s_waitcnt lgkmcnt(4)
v_mfma_f32_16x16x32_bf16 v[20:23],v[100:103],v[40:43],v[20:23]
ds_read_b128 v[32:35],v2 offset:10240
v_mfma_f32_16x16x32_bf16 v[20:23],v[212:215],v[44:47],v[20:23]
buffer_load_dwordx4 v2,s[32:35],s63 offen offset:1024 sc1 lds
v_mfma_f32_16x16x32_bf16 v[16:19],v[208:211],v[44:47],v[16:19]
ds_read_b128 v[36:39],v2 offset:24576
v_mfma_f32_16x16x32_bf16 v[16:19],v[96:99],v[40:43],v[16:19]
s_waitcnt lgkmcnt(4)
v_mfma_f32_16x16x32_bf16 v[16:19],a[72:75],v[48:51],v[16:19]
ds_read_b128 v[40:43],v2 offset:38912
v_mfma_f32_16x16x32_bf16 v[16:19],a[184:187],v[52:55],v[16:19]
buffer_load_dwordx4 v2,s[32:35],s63 offen offset:2048 sc1 lds
v_mfma_f32_16x16x32_bf16 v[20:23],a[188:191],v[52:55],v[20:23]
ds_read_b128 v[44:47],v2 offset:53248
v_mfma_f32_16x16x32_bf16 v[20:23],a[76:79],v[48:51],v[20:23]
s_waitcnt lgkmcnt(4)
v_mfma_f32_16x16x32_bf16 v[20:23],v[108:111],v[56:59],v[20:23]
ds_read_b128 v[48:51],v2 offset:11264
v_mfma_f32_16x16x32_bf16 v[20:23],v[220:223],v[60:63],v[20:23]
buffer_load_dwordx2 v[6:7],v0,s[0:3],s66 offen sc1
v_mfma_f32_16x16x32_bf16 v[16:19],v[216:219],v[60:63],v[16:19]
ds_read_b128 v[52:55],v2 offset:25600
v_mfma_f32_16x16x32_bf16 v[16:19],v[104:107],v[56:59],v[16:19]
s_waitcnt lgkmcnt(4)
v_mfma_f32_16x16x32_bf16 v[16:19],a[80:83],v[32:35],v[16:19]
ds_read_b128 v[56:59],v2 offset:39936
v_mfma_f32_16x16x32_bf16 v[16:19],a[192:195],v[36:39],v[16:19]
buffer_load_dwordx4 v2,s[32:35],s63 offen offset:3072 sc1 lds
s_add_u32 s63,s63,4096
v_mfma_f32_16x16x32_bf16 v[20:23],a[196:199],v[36:39],v[20:23]
ds_read_b128 v[60:63],v2 offset:54272
s_add_u32 m0,s64,69632
v_mfma_f32_16x16x32_bf16 v[20:23],a[84:87],v[32:35],v[20:23]
s_waitcnt lgkmcnt(4)
v_mfma_f32_16x16x32_bf16 v[20:23],v[116:119],v[40:43],v[20:23]
ds_read_b128 v[32:35],v2 offset:12288
v_mfma_f32_16x16x32_bf16 v[20:23],v[228:231],v[44:47],v[20:23]
buffer_load_dwordx4 v2,s[32:35],s63 offen offset:0 sc1 lds
v_mfma_f32_16x16x32_bf16 v[16:19],v[224:227],v[44:47],v[16:19]
ds_read_b128 v[36:39],v2 offset:26624
v_mfma_f32_16x16x32_bf16 v[16:19],v[112:115],v[40:43],v[16:19]
s_waitcnt lgkmcnt(4)
v_mfma_f32_16x16x32_bf16 v[16:19],a[88:91],v[48:51],v[16:19]
ds_read_b128 v[40:43],v2 offset:40960
v_mfma_f32_16x16x32_bf16 v[16:19],a[200:203],v[52:55],v[16:19]
buffer_load_dwordx4 v2,s[32:35],s63 offen offset:1024 sc1 lds
s_add_u32 s63,s63,s31
v_mfma_f32_16x16x32_bf16 v[20:23],a[204:207],v[52:55],v[20:23]
ds_read_b128 v[44:47],v2 offset:55296
v_mfma_f32_16x16x32_bf16 v[20:23],a[92:95],v[48:51],v[20:23]
s_waitcnt lgkmcnt(4)
v_mfma_f32_16x16x32_bf16 v[20:23],v[124:127],v[56:59],v[20:23]
ds_read_b128 v[48:51],v2 offset:13312
s_waitcnt vmcnt(11)
v_mfma_f32_16x16x32_bf16 v[20:23],v[236:239],v[60:63],v[20:23]
v_lshlrev_b32 v24,16,v12
v_and_b32 v25,0xffff0000,v12
v_mfma_f32_16x16x32_bf16 v[16:19],v[232:235],v[60:63],v[16:19]
ds_read_b128 v[52:55],v2 offset:27648
v_lshlrev_b32 v26,16,v13
v_mfma_f32_16x16x32_bf16 v[16:19],v[120:123],v[56:59],v[16:19]
)#"
R"#(
v_and_b32 v27,0xffff0000,v13
s_waitcnt lgkmcnt(4)
v_mfma_f32_16x16x32_bf16 v[16:19],a[96:99],v[32:35],v[16:19]
ds_read_b128 v[56:59],v2 offset:41984
v_lshlrev_b32 v28,16,v14
v_mfma_f32_16x16x32_bf16 v[16:19],a[208:211],v[36:39],v[16:19]
v_and_b32 v29,0xffff0000,v14
v_lshlrev_b32 v30,16,v15
v_mfma_f32_16x16x32_bf16 v[20:23],a[212:215],v[36:39],v[20:23]
s_barrier
v_mfma_f32_16x16x32_bf16 v[20:23],a[100:103],v[32:35],v[20:23]
ds_read_b128 v[60:63],v2 offset:56320
s_waitcnt lgkmcnt(4)
v_mfma_f32_16x16x32_bf16 v[20:23],v[132:135],v[40:43],v[20:23]
ds_read_b128 v[32:35],v4 offset:0
v_and_b32 v31,0xffff0000,v15
v_mfma_f32_16x16x32_bf16 v[20:23],v[244:247],v[44:47],v[20:23]
v_mfma_f32_16x16x32_bf16 v[16:19],v[240:243],v[44:47],v[16:19]
ds_read_b128 v[36:39],v4 offset:14336
v_mfma_f32_16x16x32_bf16 v[16:19],v[128:131],v[40:43],v[16:19]
s_waitcnt lgkmcnt(4)
v_mfma_f32_16x16x32_bf16 v[16:19],a[104:107],v[48:51],v[16:19]
ds_read_b128 v[40:43],v4 offset:28672
v_mfma_f32_16x16x32_bf16 v[16:19],a[216:219],v[52:55],v[16:19]
v_mfma_f32_16x16x32_bf16 v[20:23],a[220:223],v[52:55],v[20:23]
ds_read_b128 v[44:47],v4 offset:43008
v_mfma_f32_16x16x32_bf16 v[20:23],a[108:111],v[48:51],v[20:23]
s_waitcnt lgkmcnt(4)
v_mfma_f32_16x16x32_bf16 v[20:23],v[140:143],v[56:59],v[20:23]
ds_read_b128 v[48:51],v4 offset:1024
v_mfma_f32_16x16x32_bf16 v[20:23],v[252:255],v[60:63],v[20:23]
v_mfma_f32_16x16x32_bf16 v[16:19],v[248:251],v[60:63],v[16:19]
ds_read_b128 v[52:55],v4 offset:15360
s_sub_u32 s62,s62,1
v_mfma_f32_16x16x32_bf16 v[16:19],v[136:139],v[56:59],v[16:19]
s_cbranch_scc0 .L_dml_1
.L_dmu_1:
s_nop 1
s_nop 0
v_cvt_pk_bf16_f32 v15,v22,v23
v_cvt_pk_bf16_f32 v14,v20,v21
s_nop 1
v_cvt_pk_bf16_f32 v13,v18,v19
v_cvt_pk_bf16_f32 v12,v16,v17
ds_write_b128 v5,v[12:15]
ds_read2st64_b32 v[12:13],v3 offset0:0 offset1:1
ds_read2st64_b32 v[14:15],v3 offset0:2 offset1:3
s_waitcnt lgkmcnt(0)
buffer_atomic_pk_add_bf16 v12,v1,s[44:47],s65 offen offset:0 sc1
s_add_u32 s70,s70,s75
buffer_atomic_pk_add_bf16 v13,v1,s[44:47],s65 offen offset:2048 sc1
s_add_u32 s65,s65,16384
buffer_atomic_pk_add_bf16 v14,v1,s[44:47],s65 offen offset:0 sc1
buffer_atomic_pk_add_bf16 v15,v1,s[44:47],s65 offen offset:2048 sc1
s_and_b32 s65,s70,0x3f8000
s_branch .L_end
.L_dmt_1:
s_nop 1
s_nop 0
v_cvt_pk_bf16_f32 v15,v30,v31
v_cvt_pk_bf16_f32 v14,v28,v29
s_nop 1
v_cvt_pk_bf16_f32 v13,v26,v27
v_cvt_pk_bf16_f32 v12,v24,v25
ds_write_b128 v5,v[12:15]
ds_read2st64_b32 v[12:13],v3 offset0:0 offset1:1
ds_read2st64_b32 v[14:15],v3 offset0:2 offset1:3
s_waitcnt lgkmcnt(0)
buffer_atomic_pk_add_bf16 v12,v1,s[44:47],s65 offen offset:0 sc1
s_add_u32 s70,s70,s68
buffer_atomic_pk_add_bf16 v13,v1,s[44:47],s65 offen offset:2048 sc1
s_add_u32 s65,s65,16384
buffer_atomic_pk_add_bf16 v14,v1,s[44:47],s65 offen offset:0 sc1
s_mov_b32 s68,s75
buffer_atomic_pk_add_bf16 v15,v1,s[44:47],s65 offen offset:2048 sc1
s_and_b32 s65,s70,0x3f8000
s_branch .L_end
.p2align 6
.L_dlv2:
.L_dll_2:
s_waitcnt lgkmcnt(4)
v_mfma_f32_16x16x32_bf16 v[16:19],a[0:3],v[32:35],v[16:19]
ds_read_b128 v[56:59],v2 offset:29696
v_mfma_f32_16x16x32_bf16 v[16:19],a[112:115],v[36:39],v[16:19]
v_mfma_f32_16x16x32_bf16 v[20:23],a[116:119],v[36:39],v[20:23]
ds_read_b128 v[60:63],v2 offset:44032
v_mfma_f32_16x16x32_bf16 v[20:23],a[4:7],v[32:35],v[20:23]
s_add_u32 s68,s68,1187840
s_waitcnt lgkmcnt(4)
v_mfma_f32_16x16x32_bf16 v[20:23],a[228:231],v[40:43],v[20:23]
ds_read_b128 v[32:35],v2 offset:2048
v_mfma_f32_16x16x32_bf16 v[20:23],v[148:151],v[44:47],v[20:23]
buffer_store_dwordx4 v[28:31],v2,s[32:35],s68 offen offset:1024 sc1
v_mfma_f32_16x16x32_bf16 v[16:19],v[144:147],v[44:47],v[16:19]
ds_read_b128 v[36:39],v2 offset:16384
v_mfma_f32_16x16x32_bf16 v[16:19],a[224:227],v[40:43],v[16:19]
s_waitcnt lgkmcnt(4)
v_mfma_f32_16x16x32_bf16 v[16:19],a[8:11],v[48:51],v[16:19]
ds_read_b128 v[40:43],v2 offset:30720
v_mfma_f32_16x16x32_bf16 v[16:19],a[120:123],v[52:55],v[16:19]
buffer_store_dwordx4 v[24:27],v2,s[32:35],s68 offen sc1
s_mov_b32 s67,s68
v_mfma_f32_16x16x32_bf16 v[20:23],a[124:127],v[52:55],v[20:23]
ds_read_b128 v[44:47],v2 offset:45056
v_mfma_f32_16x16x32_bf16 v[20:23],a[12:15],v[48:51],v[20:23]
s_waitcnt lgkmcnt(4)
v_mfma_f32_16x16x32_bf16 v[20:23],a[236:239],v[56:59],v[20:23]
ds_read_b128 v[48:51],v2 offset:3072
v_mfma_f32_16x16x32_bf16 v[20:23],v[156:159],v[60:63],v[20:23]
v_mfma_f32_16x16x32_bf16 v[16:19],v[152:155],v[60:63],v[16:19]
ds_read_b128 v[52:55],v2 offset:17408
v_mfma_f32_16x16x32_bf16 v[16:19],a[232:235],v[56:59],v[16:19]
s_waitcnt lgkmcnt(4)
v_mfma_f32_16x16x32_bf16 v[16:19],a[16:19],v[32:35],v[16:19]
ds_read_b128 v[56:59],v2 offset:31744
v_mfma_f32_16x16x32_bf16 v[16:19],a[128:131],v[36:39],v[16:19]
s_waitcnt vmcnt(2)
s_barrier
v_mfma_f32_16x16x32_bf16 v[20:23],a[132:135],v[36:39],v[20:23]
ds_read_b128 v[60:63],v2 offset:46080
v_mfma_f32_16x16x32_bf16 v[20:23],a[20:23],v[32:35],v[20:23]
s_waitcnt lgkmcnt(4)
v_mfma_f32_16x16x32_bf16 v[20:23],a[244:247],v[40:43],v[20:23]
ds_read_b128 v[32:35],v2 offset:4096
s_add_u32 m0,s64,57344
v_mfma_f32_16x16x32_bf16 v[20:23],v[164:167],v[44:47],v[20:23]
buffer_load_dwordx4 v2,s[32:35],s63 offen offset:0 sc1 lds
v_mfma_f32_16x16x32_bf16 v[16:19],v[160:163],v[44:47],v[16:19]
ds_read_b128 v[36:39],v2 offset:18432
v_mfma_f32_16x16x32_bf16 v[16:19],a[240:243],v[40:43],v[16:19]
s_waitcnt lgkmcnt(4)
v_mfma_f32_16x16x32_bf16 v[16:19],a[24:27],v[48:51],v[16:19]
ds_read_b128 v[40:43],v2 offset:32768
v_mfma_f32_16x16x32_bf16 v[16:19],a[136:139],v[52:55],v[16:19]
v_mfma_f32_16x16x32_bf16 v[20:23],a[140:143],v[52:55],v[20:23]
ds_read_b128 v[44:47],v2 offset:47104
v_mfma_f32_16x16x32_bf16 v[20:23],a[28:31],v[48:51],v[20:23]
s_waitcnt lgkmcnt(4)
v_mfma_f32_16x16x32_bf16 v[20:23],a[252:255],v[56:59],v[20:23]
ds_read_b128 v[48:51],v2 offset:5120
v_mfma_f32_16x16x32_bf16 v[20:23],v[172:175],v[60:63],v[20:23]
buffer_load_dwordx4 v2,s[32:35],s63 offen offset:1024 sc1 lds
v_mfma_f32_16x16x32_bf16 v[16:19],v[168:171],v[60:63],v[16:19]
ds_read_b128 v[52:55],v2 offset:19456
v_mfma_f32_16x16x32_bf16 v[16:19],a[248:251],v[56:59],v[16:19]
s_waitcnt lgkmcnt(4)
v_mfma_f32_16x16x32_bf16 v[16:19],a[32:35],v[32:35],v[16:19]
ds_read_b128 v[56:59],v2 offset:33792
v_mfma_f32_16x16x32_bf16 v[16:19],a[144:147],v[36:39],v[16:19]
buffer_load_dwordx4 v2,s[32:35],s63 offen offset:2048 sc1 lds
v_mfma_f32_16x16x32_bf16 v[20:23],a[148:151],v[36:39],v[20:23]
ds_read_b128 v[60:63],v2 offset:48128
v_mfma_f32_16x16x32_bf16 v[20:23],a[36:39],v[32:35],v[20:23]
s_waitcnt lgkmcnt(4)
v_mfma_f32_16x16x32_bf16 v[20:23],v[68:71],v[40:43],v[20:23]
ds_read_b128 v[32:35],v2 offset:6144
v_mfma_f32_16x16x32_bf16 v[20:23],v[180:183],v[44:47],v[20:23]
buffer_load_dwordx4 v2,s[32:35],s63 offen offset:3072 sc1 lds
v_mfma_f32_16x16x32_bf16 v[16:19],v[176:179],v[44:47],v[16:19]
ds_read_b128 v[36:39],v2 offset:20480
v_mfma_f32_16x16x32_bf16 v[16:19],v[64:67],v[40:43],v[16:19]
s_waitcnt lgkmcnt(4)
v_mfma_f32_16x16x32_bf16 v[16:19],a[40:43],v[48:51],v[16:19]
ds_read_b128 v[40:43],v2 offset:34816
v_mfma_f32_16x16x32_bf16 v[16:19],a[152:155],v[52:55],v[16:19]
buffer_load_dwordx4 v[12:15],v2,s[32:35],s71 offen sc1
s_add_u32 s71,s71,s30
v_mfma_f32_16x16x32_bf16 v[20:23],a[156:159],v[52:55],v[20:23]
ds_read_b128 v[44:47],v2 offset:49152
s_add_u32 s63,s63,4096
v_mfma_f32_16x16x32_bf16 v[20:23],a[44:47],v[48:51],v[20:23]
s_add_u32 m0,s64,61440
s_waitcnt lgkmcnt(4)
v_mfma_f32_16x16x32_bf16 v[20:23],v[76:79],v[56:59],v[20:23]
ds_read_b128 v[48:51],v2 offset:7168
v_mfma_f32_16x16x32_bf16 v[20:23],v[188:191],v[60:63],v[20:23]
buffer_load_dwordx4 v2,s[32:35],s63 offen offset:0 sc1 lds
v_mfma_f32_16x16x32_bf16 v[16:19],v[184:187],v[60:63],v[16:19]
ds_read_b128 v[52:55],v2 offset:21504
v_mfma_f32_16x16x32_bf16 v[16:19],v[72:75],v[56:59],v[16:19]
s_waitcnt lgkmcnt(4)
v_mfma_f32_16x16x32_bf16 v[16:19],a[48:51],v[32:35],v[16:19]
ds_read_b128 v[56:59],v2 offset:35840
v_mfma_f32_16x16x32_bf16 v[16:19],a[160:163],v[36:39],v[16:19]
buffer_load_dwordx4 v2,s[32:35],s63 offen offset:1024 sc1 lds
v_mfma_f32_16x16x32_bf16 v[20:23],a[164:167],v[36:39],v[20:23]
ds_read_b128 v[60:63],v2 offset:50176
v_mfma_f32_16x16x32_bf16 v[20:23],a[52:55],v[32:35],v[20:23]
s_waitcnt lgkmcnt(4)
v_mfma_f32_16x16x32_bf16 v[20:23],v[84:87],v[40:43],v[20:23]
ds_read_b128 v[32:35],v2 offset:8192
v_mfma_f32_16x16x32_bf16 v[20:23],v[196:199],v[44:47],v[20:23]
buffer_load_dwordx4 v2,s[32:35],s63 offen offset:2048 sc1 lds
v_mfma_f32_16x16x32_bf16 v[16:19],v[192:195],v[44:47],v[16:19]
ds_read_b128 v[36:39],v2 offset:22528
v_mfma_f32_16x16x32_bf16 v[16:19],v[80:83],v[40:43],v[16:19]
s_waitcnt lgkmcnt(4)
v_mfma_f32_16x16x32_bf16 v[16:19],a[56:59],v[48:51],v[16:19]
ds_read_b128 v[40:43],v2 offset:36864
v_mfma_f32_16x16x32_bf16 v[16:19],a[168:171],v[52:55],v[16:19]
buffer_load_dwordx4 v2,s[32:35],s63 offen offset:3072 sc1 lds
v_mfma_f32_16x16x32_bf16 v[20:23],a[172:175],v[52:55],v[20:23]
ds_read_b128 v[44:47],v2 offset:51200
v_mfma_f32_16x16x32_bf16 v[20:23],a[60:63],v[48:51],v[20:23]
s_waitcnt lgkmcnt(4)
v_mfma_f32_16x16x32_bf16 v[20:23],v[92:95],v[56:59],v[20:23]
ds_read_b128 v[48:51],v2 offset:9216
v_mfma_f32_16x16x32_bf16 v[20:23],v[204:207],v[60:63],v[20:23]
s_add_u32 s63,s63,4096
s_add_u32 m0,s64,65536
v_mfma_f32_16x16x32_bf16 v[16:19],v[200:203],v[60:63],v[16:19]
ds_read_b128 v[52:55],v2 offset:23552
v_mfma_f32_16x16x32_bf16 v[16:19],v[88:91],v[56:59],v[16:19]
s_waitcnt lgkmcnt(4)
v_mfma_f32_16x16x32_bf16 v[16:19],a[64:67],v[32:35],v[16:19]
ds_read_b128 v[56:59],v2 offset:37888
v_mfma_f32_16x16x32_bf16 v[16:19],a[176:179],v[36:39],v[16:19]
buffer_load_dwordx4 v2,s[32:35],s63 offen offset:0 sc1 lds
v_mfma_f32_16x16x32_bf16 v[20:23],a[180:183],v[36:39],v[20:23]
ds_read_b128 v[60:63],v2 offset:52224
v_mfma_f32_16x16x32_bf16 v[20:23],a[68:71],v[32:35],v[20:23]
s_waitcnt lgkmcnt(4)
v_mfma_f32_16x16x32_bf16 v[20:23],v[100:103],v[40:43],v[20:23]
ds_read_b128 v[32:35],v2 offset:10240
v_mfma_f32_16x16x32_bf16 v[20:23],v[212:215],v[44:47],v[20:23]
buffer_load_dwordx4 v2,s[32:35],s63 offen offset:1024 sc1 lds
v_mfma_f32_16x16x32_bf16 v[16:19],v[208:211],v[44:47],v[16:19]
ds_read_b128 v[36:39],v2 offset:24576
v_mfma_f32_16x16x32_bf16 v[16:19],v[96:99],v[40:43],v[16:19]
s_waitcnt lgkmcnt(4)
v_mfma_f32_16x16x32_bf16 v[16:19],a[72:75],v[48:51],v[16:19]
ds_read_b128 v[40:43],v2 offset:38912
v_mfma_f32_16x16x32_bf16 v[16:19],a[184:187],v[52:55],v[16:19]
buffer_load_dwordx4 v2,s[32:35],s63 offen offset:2048 sc1 lds
v_mfma_f32_16x16x32_bf16 v[20:23],a[188:191],v[52:55],v[20:23]
ds_read_b128 v[44:47],v2 offset:53248
v_mfma_f32_16x16x32_bf16 v[20:23],a[76:79],v[48:51],v[20:23]
s_waitcnt lgkmcnt(4)
v_mfma_f32_16x16x32_bf16 v[20:23],v[108:111],v[56:59],v[20:23]
ds_read_b128 v[48:51],v2 offset:11264
v_mfma_f32_16x16x32_bf16 v[20:23],v[220:223],v[60:63],v[20:23]
v_mfma_f32_16x16x32_bf16 v[16:19],v[216:219],v[60:63],v[16:19]
ds_read_b128 v[52:55],v2 offset:25600
v_mfma_f32_16x16x32_bf16 v[16:19],v[104:107],v[56:59],v[16:19]
s_waitcnt lgkmcnt(4)
v_mfma_f32_16x16x32_bf16 v[16:19],a[80:83],v[32:35],v[16:19]
ds_read_b128 v[56:59],v2 offset:39936
v_mfma_f32_16x16x32_bf16 v[16:19],a[192:195],v[36:39],v[16:19]
buffer_load_dwordx4 v2,s[32:35],s63 offen offset:3072 sc1 lds
s_add_u32 s63,s63,4096
v_mfma_f32_16x16x32_bf16 v[20:23],a[196:199],v[36:39],v[20:23]
ds_read_b128 v[60:63],v2 offset:54272
s_add_u32 m0,s64,69632
v_mfma_f32_16x16x32_bf16 v[20:23],a[84:87],v[32:35],v[20:23]
s_waitcnt lgkmcnt(4)
v_mfma_f32_16x16x32_bf16 v[20:23],v[116:119],v[40:43],v[20:23]
ds_read_b128 v[32:35],v2 offset:12288
v_mfma_f32_16x16x32_bf16 v[20:23],v[228:231],v[44:47],v[20:23]
buffer_load_dwordx4 v2,s[32:35],s63 offen offset:0 sc1 lds
v_mfma_f32_16x16x32_bf16 v[16:19],v[224:227],v[44:47],v[16:19]
ds_read_b128 v[36:39],v2 offset:26624
v_mfma_f32_16x16x32_bf16 v[16:19],v[112:115],v[40:43],v[16:19]
s_waitcnt lgkmcnt(4)
v_mfma_f32_16x16x32_bf16 v[16:19],a[88:91],v[48:51],v[16:19]
ds_read_b128 v[40:43],v2 offset:40960
v_mfma_f32_16x16x32_bf16 v[16:19],a[200:203],v[52:55],v[16:19]
buffer_load_dwordx4 v2,s[32:35],s63 offen offset:1024 sc1 lds
s_add_u32 s63,s63,s31
v_mfma_f32_16x16x32_bf16 v[20:23],a[204:207],v[52:55],v[20:23]
ds_read_b128 v[44:47],v2 offset:55296
v_mfma_f32_16x16x32_bf16 v[20:23],a[92:95],v[48:51],v[20:23]
s_waitcnt lgkmcnt(4)
v_mfma_f32_16x16x32_bf16 v[20:23],v[124:127],v[56:59],v[20:23]
ds_read_b128 v[48:51],v2 offset:13312
s_waitcnt vmcnt(10)
v_mfma_f32_16x16x32_bf16 v[20:23],v[236:239],v[60:63],v[20:23]
v_lshlrev_b32 v24,16,v12
v_and_b32 v25,0xffff0000,v12
v_mfma_f32_16x16x32_bf16 v[16:19],v[232:235],v[60:63],v[16:19]
ds_read_b128 v[52:55],v2 offset:27648
v_lshlrev_b32 v26,16,v13
v_mfma_f32_16x16x32_bf16 v[16:19],v[120:123],v[56:59],v[16:19]
v_and_b32 v27,0xffff0000,v13
s_waitcnt lgkmcnt(4)
v_mfma_f32_16x16x32_bf16 v[16:19],a[96:99],v[32:35],v[16:19]
ds_read_b128 v[56:59],v2 offset:41984
v_lshlrev_b32 v28,16,v14
v_mfma_f32_16x16x32_bf16 v[16:19],a[208:211],v[36:39],v[16:19]
s_barrier
v_mfma_f32_16x16x32_bf16 v[20:23],a[212:215],v[36:39],v[20:23]
ds_read_b128 v[60:63],v2 offset:56320
v_and_b32 v29,0xffff0000,v14
v_mfma_f32_16x16x32_bf16 v[20:23],a[100:103],v[32:35],v[20:23]
v_lshlrev_b32 v30,16,v15
s_waitcnt lgkmcnt(4)
v_mfma_f32_16x16x32_bf16 v[20:23],v[132:135],v[40:43],v[20:23]
ds_read_b128 v[32:35],v4 offset:0
v_and_b32 v31,0xffff0000,v15
v_mfma_f32_16x16x32_bf16 v[20:23],v[244:247],v[44:47],v[20:23]
v_mfma_f32_16x16x32_bf16 v[16:19],v[240:243],v[44:47],v[16:19]
ds_read_b128 v[36:39],v4 offset:14336
v_mfma_f32_16x16x32_bf16 v[16:19],v[128:131],v[40:43],v[16:19]
s_waitcnt lgkmcnt(4)
v_mfma_f32_16x16x32_bf16 v[16:19],a[104:107],v[48:51],v[16:19]
ds_read_b128 v[40:43],v4 offset:28672
v_mfma_f32_16x16x32_bf16 v[16:19],a[216:219],v[52:55],v[16:19]
v_mfma_f32_16x16x32_bf16 v[20:23],a[220:223],v[52:55],v[20:23]
ds_read_b128 v[44:47],v4 offset:43008
v_mfma_f32_16x16x32_bf16 v[20:23],a[108:111],v[48:51],v[20:23]
s_waitcnt lgkmcnt(4)
v_mfma_f32_16x16x32_bf16 v[20:23],v[140:143],v[56:59],v[20:23]
ds_read_b128 v[48:51],v4 offset:1024
v_mfma_f32_16x16x32_bf16 v[20:23],v[252:255],v[60:63],v[20:23]
v_mfma_f32_16x16x32_bf16 v[16:19],v[248:251],v[60:63],v[16:19]
ds_read_b128 v[52:55],v4 offset:15360
s_sub_u32 s62,s62,1
v_mfma_f32_16x16x32_bf16 v[16:19],v[136:139],v[56:59],v[16:19]
s_cbranch_scc1 .L_dlb_2
s_waitcnt lgkmcnt(4)
v_mfma_f32_16x16x32_bf16 v[24:27],a[0:3],v[32:35],v[24:27]
ds_read_b128 v[56:59],v4 offset:29696
v_mfma_f32_16x16x32_bf16 v[24:27],a[112:115],v[36:39],v[24:27]
v_mfma_f32_16x16x32_bf16 v[28:31],a[116:119],v[36:39],v[28:31]
ds_read_b128 v[60:63],v4 offset:44032
v_mfma_f32_16x16x32_bf16 v[28:31],a[4:7],v[32:35],v[28:31]
s_add_u32 s68,s68,1187840
s_waitcnt lgkmcnt(4)
v_mfma_f32_16x16x32_bf16 v[28:31],a[228:231],v[40:43],v[28:31]
ds_read_b128 v[32:35],v4 offset:2048
v_mfma_f32_16x16x32_bf16 v[28:31],v[148:151],v[44:47],v[28:31]
buffer_store_dwordx4 v[20:23],v2,s[32:35],s68 offen offset:1024 sc1
v_mfma_f32_16x16x32_bf16 v[24:27],v[144:147],v[44:47],v[24:27]
ds_read_b128 v[36:39],v4 offset:16384
v_mfma_f32_16x16x32_bf16 v[24:27],a[224:227],v[40:43],v[24:27]
s_waitcnt lgkmcnt(4)
v_mfma_f32_16x16x32_bf16 v[24:27],a[8:11],v[48:51],v[24:27]
ds_read_b128 v[40:43],v4 offset:30720
v_mfma_f32_16x16x32_bf16 v[24:27],a[120:123],v[52:55],v[24:27]
buffer_store_dwordx4 v[16:19],v2,s[32:35],s68 offen sc1
s_mov_b32 s67,s68
v_mfma_f32_16x16x32_bf16 v[28:31],a[124:127],v[52:55],v[28:31]
ds_read_b128 v[44:47],v4 offset:45056
v_mfma_f32_16x16x32_bf16 v[28:31],a[12:15],v[48:51],v[28:31]
s_waitcnt lgkmcnt(4)
v_mfma_f32_16x16x32_bf16 v[28:31],a[236:239],v[56:59],v[28:31]
ds_read_b128 v[48:51],v4 offset:3072
v_mfma_f32_16x16x32_bf16 v[28:31],v[156:159],v[60:63],v[28:31]
v_mfma_f32_16x16x32_bf16 v[24:27],v[152:155],v[60:63],v[24:27]
ds_read_b128 v[52:55],v4 offset:17408
v_mfma_f32_16x16x32_bf16 v[24:27],a[232:235],v[56:59],v[24:27]
s_waitcnt lgkmcnt(4)
v_mfma_f32_16x16x32_bf16 v[24:27],a[16:19],v[32:35],v[24:27]
ds_read_b128 v[56:59],v4 offset:31744
v_mfma_f32_16x16x32_bf16 v[24:27],a[128:131],v[36:39],v[24:27]
s_waitcnt vmcnt(2)
s_barrier
v_mfma_f32_16x16x32_bf16 v[28:31],a[132:135],v[36:39],v[28:31]
ds_read_b128 v[60:63],v4 offset:46080
v_mfma_f32_16x16x32_bf16 v[28:31],a[20:23],v[32:35],v[28:31]
s_waitcnt lgkmcnt(4)
v_mfma_f32_16x16x32_bf16 v[28:31],a[244:247],v[40:43],v[28:31]
ds_read_b128 v[32:35],v4 offset:4096
s_add_u32 m0,s64,0
v_mfma_f32_16x16x32_bf16 v[28:31],v[164:167],v[44:47],v[28:31]
buffer_load_dwordx4 v2,s[32:35],s63 offen offset:0 sc1 lds
v_mfma_f32_16x16x32_bf16 v[24:27],v[160:163],v[44:47],v[24:27]
ds_read_b128 v[36:39],v4 offset:18432
v_mfma_f32_16x16x32_bf16 v[24:27],a[240:243],v[40:43],v[24:27]
s_waitcnt lgkmcnt(4)
v_mfma_f32_16x16x32_bf16 v[24:27],a[24:27],v[48:51],v[24:27]
ds_read_b128 v[40:43],v4 offset:32768
v_mfma_f32_16x16x32_bf16 v[24:27],a[136:139],v[52:55],v[24:27]
v_mfma_f32_16x16x32_bf16 v[28:31],a[140:143],v[52:55],v[28:31]
ds_read_b128 v[44:47],v4 offset:47104
v_mfma_f32_16x16x32_bf16 v[28:31],a[28:31],v[48:51],v[28:31]
s_waitcnt lgkmcnt(4)
v_mfma_f32_16x16x32_bf16 v[28:31],a[252:255],v[56:59],v[28:31]
ds_read_b128 v[48:51],v4 offset:5120
v_mfma_f32_16x16x32_bf16 v[28:31],v[172:175],v[60:63],v[28:31]
buffer_load_dwordx4 v2,s[32:35],s63 offen offset:1024 sc1 lds
v_mfma_f32_16x16x32_bf16 v[24:27],v[168:171],v[60:63],v[24:27]
ds_read_b128 v[52:55],v4 offset:19456
v_mfma_f32_16x16x32_bf16 v[24:27],a[248:251],v[56:59],v[24:27]
s_waitcnt lgkmcnt(4)
v_mfma_f32_16x16x32_bf16 v[24:27],a[32:35],v[32:35],v[24:27]
ds_read_b128 v[56:59],v4 offset:33792
v_mfma_f32_16x16x32_bf16 v[24:27],a[144:147],v[36:39],v[24:27]
buffer_load_dwordx4 v2,s[32:35],s63 offen offset:2048 sc1 lds
v_mfma_f32_16x16x32_bf16 v[28:31],a[148:151],v[36:39],v[28:31]
ds_read_b128 v[60:63],v4 offset:48128
v_mfma_f32_16x16x32_bf16 v[28:31],a[36:39],v[32:35],v[28:31]
s_waitcnt lgkmcnt(4)
v_mfma_f32_16x16x32_bf16 v[28:31],v[68:71],v[40:43],v[28:31]
ds_read_b128 v[32:35],v4 offset:6144
v_mfma_f32_16x16x32_bf16 v[28:31],v[180:183],v[44:47],v[28:31]
buffer_load_dwordx4 v2,s[32:35],s63 offen offset:3072 sc1 lds
v_mfma_f32_16x16x32_bf16 v[24:27],v[176:179],v[44:47],v[24:27]
ds_read_b128 v[36:39],v4 offset:20480
v_mfma_f32_16x16x32_bf16 v[24:27],v[64:67],v[40:43],v[24:27]
s_waitcnt lgkmcnt(4)
v_mfma_f32_16x16x32_bf16 v[24:27],a[40:43],v[48:51],v[24:27]
ds_read_b128 v[40:43],v4 offset:34816
v_mfma_f32_16x16x32_bf16 v[24:27],a[152:155],v[52:55],v[24:27]
buffer_load_dwordx4 v[12:15],v2,s[32:35],s71 offen sc1
s_add_u32 s71,s71,s30
v_mfma_f32_16x16x32_bf16 v[28:31],a[156:159],v[52:55],v[28:31]
ds_read_b128 v[44:47],v4 offset:49152
s_add_u32 s63,s63,4096
v_mfma_f32_16x16x32_bf16 v[28:31],a[44:47],v[48:51],v[28:31]
s_add_u32 m0,s64,4096
s_waitcnt lgkmcnt(4)
v_mfma_f32_16x16x32_bf16 v[28:31],v[76:79],v[56:59],v[28:31]
ds_read_b128 v[48:51],v4 offset:7168
v_mfma_f32_16x16x32_bf16 v[28:31],v[188:191],v[60:63],v[28:31]
buffer_load_dwordx4 v2,s[32:35],s63 offen offset:0 sc1 lds
v_mfma_f32_16x16x32_bf16 v[24:27],v[184:187],v[60:63],v[24:27]
ds_read_b128 v[52:55],v4 offset:21504
v_mfma_f32_16x16x32_bf16 v[24:27],v[72:75],v[56:59],v[24:27]
s_waitcnt lgkmcnt(4)
v_mfma_f32_16x16x32_bf16 v[24:27],a[48:51],v[32:35],v[24:27]
ds_read_b128 v[56:59],v4 offset:35840
v_mfma_f32_16x16x32_bf16 v[24:27],a[160:163],v[36:39],v[24:27]
buffer_load_dwordx4 v2,s[32:35],s63 offen offset:1024 sc1 lds
v_mfma_f32_16x16x32_bf16 v[28:31],a[164:167],v[36:39],v[28:31]
ds_read_b128 v[60:63],v4 offset:50176
v_mfma_f32_16x16x32_bf16 v[28:31],a[52:55],v[32:35],v[28:31]
s_waitcnt lgkmcnt(4)
v_mfma_f32_16x16x32_bf16 v[28:31],v[84:87],v[40:43],v[28:31]
ds_read_b128 v[32:35],v4 offset:8192
v_mfma_f32_16x16x32_bf16 v[28:31],v[196:199],v[44:47],v[28:31]
buffer_load_dwordx4 v2,s[32:35],s63 offen offset:2048 sc1 lds
v_mfma_f32_16x16x32_bf16 v[24:27],v[192:195],v[44:47],v[24:27]
ds_read_b128 v[36:39],v4 offset:22528
v_mfma_f32_16x16x32_bf16 v[24:27],v[80:83],v[40:43],v[24:27]
s_waitcnt lgkmcnt(4)
v_mfma_f32_16x16x32_bf16 v[24:27],a[56:59],v[48:51],v[24:27]
ds_read_b128 v[40:43],v4 offset:36864
v_mfma_f32_16x16x32_bf16 v[24:27],a[168:171],v[52:55],v[24:27]
buffer_load_dwordx4 v2,s[32:35],s63 offen offset:3072 sc1 lds
v_mfma_f32_16x16x32_bf16 v[28:31],a[172:175],v[52:55],v[28:31]
ds_read_b128 v[44:47],v4 offset:51200
v_mfma_f32_16x16x32_bf16 v[28:31],a[60:63],v[48:51],v[28:31]
s_waitcnt lgkmcnt(4)
v_mfma_f32_16x16x32_bf16 v[28:31],v[92:95],v[56:59],v[28:31]
ds_read_b128 v[48:51],v4 offset:9216
v_mfma_f32_16x16x32_bf16 v[28:31],v[204:207],v[60:63],v[28:31]
s_add_u32 s63,s63,4096
s_add_u32 m0,s64,8192
v_mfma_f32_16x16x32_bf16 v[24:27],v[200:203],v[60:63],v[24:27]
ds_read_b128 v[52:55],v4 offset:23552
v_mfma_f32_16x16x32_bf16 v[24:27],v[88:91],v[56:59],v[24:27]
s_waitcnt lgkmcnt(4)
v_mfma_f32_16x16x32_bf16 v[24:27],a[64:67],v[32:35],v[24:27]
ds_read_b128 v[56:59],v4 offset:37888
v_mfma_f32_16x16x32_bf16 v[24:27],a[176:179],v[36:39],v[24:27]
buffer_load_dwordx4 v2,s[32:35],s63 offen offset:0 sc1 lds
v_mfma_f32_16x16x32_bf16 v[28:31],a[180:183],v[36:39],v[28:31]
ds_read_b128 v[60:63],v4 offset:52224
v_mfma_f32_16x16x32_bf16 v[28:31],a[68:71],v[32:35],v[28:31]
s_waitcnt lgkmcnt(4)
v_mfma_f32_16x16x32_bf16 v[28:31],v[100:103],v[40:43],v[28:31]
ds_read_b128 v[32:35],v4 offset:10240
v_mfma_f32_16x16x32_bf16 v[28:31],v[212:215],v[44:47],v[28:31]
buffer_load_dwordx4 v2,s[32:35],s63 offen offset:1024 sc1 lds
v_mfma_f32_16x16x32_bf16 v[24:27],v[208:211],v[44:47],v[24:27]
ds_read_b128 v[36:39],v4 offset:24576
v_mfma_f32_16x16x32_bf16 v[24:27],v[96:99],v[40:43],v[24:27]
s_waitcnt lgkmcnt(4)
v_mfma_f32_16x16x32_bf16 v[24:27],a[72:75],v[48:51],v[24:27]
ds_read_b128 v[40:43],v4 offset:38912
v_mfma_f32_16x16x32_bf16 v[24:27],a[184:187],v[52:55],v[24:27]
buffer_load_dwordx4 v2,s[32:35],s63 offen offset:2048 sc1 lds
v_mfma_f32_16x16x32_bf16 v[28:31],a[188:191],v[52:55],v[28:31]
ds_read_b128 v[44:47],v4 offset:53248
v_mfma_f32_16x16x32_bf16 v[28:31],a[76:79],v[48:51],v[28:31]
s_waitcnt lgkmcnt(4)
v_mfma_f32_16x16x32_bf16 v[28:31],v[108:111],v[56:59],v[28:31]
ds_read_b128 v[48:51],v4 offset:11264
v_mfma_f32_16x16x32_bf16 v[28:31],v[220:223],v[60:63],v[28:31]
v_mfma_f32_16x16x32_bf16 v[24:27],v[216:219],v[60:63],v[24:27]
ds_read_b128 v[52:55],v4 offset:25600
v_mfma_f32_16x16x32_bf16 v[24:27],v[104:107],v[56:59],v[24:27]
s_waitcnt lgkmcnt(4)
v_mfma_f32_16x16x32_bf16 v[24:27],a[80:83],v[32:35],v[24:27]
ds_read_b128 v[56:59],v4 offset:39936
v_mfma_f32_16x16x32_bf16 v[24:27],a[192:195],v[36:39],v[24:27]
buffer_load_dwordx4 v2,s[32:35],s63 offen offset:3072 sc1 lds
s_add_u32 s63,s63,4096
v_mfma_f32_16x16x32_bf16 v[28:31],a[196:199],v[36:39],v[28:31]
ds_read_b128 v[60:63],v4 offset:54272
s_add_u32 m0,s64,12288
v_mfma_f32_16x16x32_bf16 v[28:31],a[84:87],v[32:35],v[28:31]
s_waitcnt lgkmcnt(4)
v_mfma_f32_16x16x32_bf16 v[28:31],v[116:119],v[40:43],v[28:31]
ds_read_b128 v[32:35],v4 offset:12288
v_mfma_f32_16x16x32_bf16 v[28:31],v[228:231],v[44:47],v[28:31]
buffer_load_dwordx4 v2,s[32:35],s63 offen offset:0 sc1 lds
v_mfma_f32_16x16x32_bf16 v[24:27],v[224:227],v[44:47],v[24:27]
ds_read_b128 v[36:39],v4 offset:26624
v_mfma_f32_16x16x32_bf16 v[24:27],v[112:115],v[40:43],v[24:27]
)#"
R"#(
s_waitcnt lgkmcnt(4)
v_mfma_f32_16x16x32_bf16 v[24:27],a[88:91],v[48:51],v[24:27]
ds_read_b128 v[40:43],v4 offset:40960
v_mfma_f32_16x16x32_bf16 v[24:27],a[200:203],v[52:55],v[24:27]
buffer_load_dwordx4 v2,s[32:35],s63 offen offset:1024 sc1 lds
s_add_u32 s63,s63,s31
v_mfma_f32_16x16x32_bf16 v[28:31],a[204:207],v[52:55],v[28:31]
ds_read_b128 v[44:47],v4 offset:55296
v_mfma_f32_16x16x32_bf16 v[28:31],a[92:95],v[48:51],v[28:31]
s_waitcnt lgkmcnt(4)
v_mfma_f32_16x16x32_bf16 v[28:31],v[124:127],v[56:59],v[28:31]
ds_read_b128 v[48:51],v4 offset:13312
s_waitcnt vmcnt(10)
v_mfma_f32_16x16x32_bf16 v[28:31],v[236:239],v[60:63],v[28:31]
v_lshlrev_b32 v16,16,v12
v_and_b32 v17,0xffff0000,v12
v_mfma_f32_16x16x32_bf16 v[24:27],v[232:235],v[60:63],v[24:27]
ds_read_b128 v[52:55],v4 offset:27648
v_lshlrev_b32 v18,16,v13
v_mfma_f32_16x16x32_bf16 v[24:27],v[120:123],v[56:59],v[24:27]
v_and_b32 v19,0xffff0000,v13
s_waitcnt lgkmcnt(4)
v_mfma_f32_16x16x32_bf16 v[24:27],a[96:99],v[32:35],v[24:27]
ds_read_b128 v[56:59],v4 offset:41984
v_lshlrev_b32 v20,16,v14
v_mfma_f32_16x16x32_bf16 v[24:27],a[208:211],v[36:39],v[24:27]
s_barrier
v_mfma_f32_16x16x32_bf16 v[28:31],a[212:215],v[36:39],v[28:31]
ds_read_b128 v[60:63],v4 offset:56320
v_and_b32 v21,0xffff0000,v14
v_mfma_f32_16x16x32_bf16 v[28:31],a[100:103],v[32:35],v[28:31]
v_lshlrev_b32 v22,16,v15
s_waitcnt lgkmcnt(4)
v_mfma_f32_16x16x32_bf16 v[28:31],v[132:135],v[40:43],v[28:31]
ds_read_b128 v[32:35],v2 offset:0
v_and_b32 v23,0xffff0000,v15
v_mfma_f32_16x16x32_bf16 v[28:31],v[244:247],v[44:47],v[28:31]
v_mfma_f32_16x16x32_bf16 v[24:27],v[240:243],v[44:47],v[24:27]
ds_read_b128 v[36:39],v2 offset:14336
v_mfma_f32_16x16x32_bf16 v[24:27],v[128:131],v[40:43],v[24:27]
s_waitcnt lgkmcnt(4)
v_mfma_f32_16x16x32_bf16 v[24:27],a[104:107],v[48:51],v[24:27]
ds_read_b128 v[40:43],v2 offset:28672
v_mfma_f32_16x16x32_bf16 v[24:27],a[216:219],v[52:55],v[24:27]
v_mfma_f32_16x16x32_bf16 v[28:31],a[220:223],v[52:55],v[28:31]
ds_read_b128 v[44:47],v2 offset:43008
v_mfma_f32_16x16x32_bf16 v[28:31],a[108:111],v[48:51],v[28:31]
s_waitcnt lgkmcnt(4)
v_mfma_f32_16x16x32_bf16 v[28:31],v[140:143],v[56:59],v[28:31]
ds_read_b128 v[48:51],v2 offset:1024
v_mfma_f32_16x16x32_bf16 v[28:31],v[252:255],v[60:63],v[28:31]
v_mfma_f32_16x16x32_bf16 v[24:27],v[248:251],v[60:63],v[24:27]
ds_read_b128 v[52:55],v2 offset:15360
v_mfma_f32_16x16x32_bf16 v[24:27],v[136:139],v[56:59],v[24:27]
s_branch .L_dll_2
.L_dlb_2:
s_mov_b32 s62,s58
s_mov_b32 s70,s59
s_add_u32 s74,s74,8192
v_lshrrev_b32 v5,8,v2
v_lshl_add_u32 v5,v5,4,s74
v_and_b32 v3,128,v2
v_lshl_add_u32 v5,v3,2,v5
v_and_b32 v3,16,v2
v_lshl_add_u32 v5,v3,4,v5
v_and_b32 v3,96,v2
v_lshl_add_u32 v5,v3,1,v5
v_lshrrev_b32 v3,2,v2
v_add_u32 v3,s74,v3
s_add_u32 s66,s66,2375680
s_add_u32 s63,s63,2375680
s_add_u32 s71,s71,2375680
s_mov_b32 s68,98304
s_mov_b32 s34,79446016
.p2align 6
.L_dml_2:
s_waitcnt lgkmcnt(4)
v_mfma_f32_16x16x32_bf16 v[24:27],a[0:3],v[32:35],v[24:27]
ds_read_b128 v[56:59],v4 offset:29696
v_mfma_f32_16x16x32_bf16 v[24:27],a[112:115],v[36:39],v[24:27]
v_mfma_f32_16x16x32_bf16 v[28:31],a[116:119],v[36:39],v[28:31]
ds_read_b128 v[60:63],v4 offset:44032
v_mfma_f32_16x16x32_bf16 v[28:31],a[4:7],v[32:35],v[28:31]
v_cvt_pk_bf16_f32 v15,v22,v23
s_waitcnt lgkmcnt(4)
v_mfma_f32_16x16x32_bf16 v[28:31],a[228:231],v[40:43],v[28:31]
ds_read_b128 v[32:35],v4 offset:2048
v_cvt_pk_bf16_f32 v14,v20,v21
v_mfma_f32_16x16x32_bf16 v[28:31],v[148:151],v[44:47],v[28:31]
v_cvt_pk_bf16_f32 v13,v18,v19
v_cvt_pk_bf16_f32 v12,v16,v17
v_mfma_f32_16x16x32_bf16 v[24:27],v[144:147],v[44:47],v[24:27]
ds_read_b128 v[36:39],v4 offset:16384
ds_write_b128 v5,v[12:15]
v_mfma_f32_16x16x32_bf16 v[24:27],a[224:227],v[40:43],v[24:27]
ds_read2st64_b32 v[12:13],v3 offset0:0 offset1:1
s_waitcnt lgkmcnt(6)
v_mfma_f32_16x16x32_bf16 v[24:27],a[8:11],v[48:51],v[24:27]
ds_read_b128 v[40:43],v4 offset:30720
ds_read2st64_b32 v[14:15],v3 offset0:2 offset1:3
v_mfma_f32_16x16x32_bf16 v[24:27],a[120:123],v[52:55],v[24:27]
v_mfma_f32_16x16x32_bf16 v[28:31],a[124:127],v[52:55],v[28:31]
ds_read_b128 v[44:47],v4 offset:45056
v_mfma_f32_16x16x32_bf16 v[28:31],a[12:15],v[48:51],v[28:31]
s_waitcnt lgkmcnt(7)
v_mfma_f32_16x16x32_bf16 v[28:31],a[236:239],v[56:59],v[28:31]
ds_read_b128 v[48:51],v4 offset:3072
v_mfma_f32_16x16x32_bf16 v[28:31],v[156:159],v[60:63],v[28:31]
s_waitcnt lgkmcnt(2)
buffer_atomic_pk_add_bf16 v12,v1,s[44:47],s65 offen offset:0 sc1
v_mfma_f32_16x16x32_bf16 v[24:27],v[152:155],v[60:63],v[24:27]
ds_read_b128 v[52:55],v4 offset:17408
s_add_u32 s70,s70,s75
v_mfma_f32_16x16x32_bf16 v[24:27],a[232:235],v[56:59],v[24:27]
buffer_atomic_pk_add_bf16 v13,v1,s[44:47],s65 offen offset:2048 sc1
v_mfma_f32_16x16x32_bf16 v[24:27],a[16:19],v[32:35],v[24:27]
ds_read_b128 v[56:59],v4 offset:31744
s_add_u32 s65,s65,16384
v_mfma_f32_16x16x32_bf16 v[24:27],a[128:131],v[36:39],v[24:27]
s_waitcnt vmcnt(2)
s_barrier
v_mfma_f32_16x16x32_bf16 v[28:31],a[132:135],v[36:39],v[28:31]
ds_read_b128 v[60:63],v4 offset:46080
v_mfma_f32_16x16x32_bf16 v[28:31],a[20:23],v[32:35],v[28:31]
buffer_atomic_pk_add_bf16 v14,v1,s[44:47],s65 offen offset:0 sc1
s_waitcnt lgkmcnt(4)
v_mfma_f32_16x16x32_bf16 v[28:31],a[244:247],v[40:43],v[28:31]
ds_read_b128 v[32:35],v4 offset:4096
s_add_u32 m0,s64,0
v_mfma_f32_16x16x32_bf16 v[28:31],v[164:167],v[44:47],v[28:31]
buffer_atomic_pk_add_bf16 v15,v1,s[44:47],s65 offen offset:2048 sc1
s_and_b32 s65,s70,0x3f8000
v_mfma_f32_16x16x32_bf16 v[24:27],v[160:163],v[44:47],v[24:27]
ds_read_b128 v[36:39],v4 offset:18432
v_mfma_f32_16x16x32_bf16 v[24:27],a[240:243],v[40:43],v[24:27]
s_waitcnt lgkmcnt(4)
v_mfma_f32_16x16x32_bf16 v[24:27],a[24:27],v[48:51],v[24:27]
ds_read_b128 v[40:43],v4 offset:32768
v_mfma_f32_16x16x32_bf16 v[24:27],a[136:139],v[52:55],v[24:27]
buffer_load_dwordx4 v2,s[32:35],s63 offen offset:0 sc1 lds
v_mfma_f32_16x16x32_bf16 v[28:31],a[140:143],v[52:55],v[28:31]
ds_read_b128 v[44:47],v4 offset:47104
v_mfma_f32_16x16x32_bf16 v[28:31],a[28:31],v[48:51],v[28:31]
s_waitcnt lgkmcnt(4)
v_mfma_f32_16x16x32_bf16 v[28:31],a[252:255],v[56:59],v[28:31]
ds_read_b128 v[48:51],v4 offset:5120
v_mfma_f32_16x16x32_bf16 v[28:31],v[172:175],v[60:63],v[28:31]
buffer_load_dwordx4 v2,s[32:35],s63 offen offset:1024 sc1 lds
v_mfma_f32_16x16x32_bf16 v[24:27],v[168:171],v[60:63],v[24:27]
ds_read_b128 v[52:55],v4 offset:19456
v_mfma_f32_16x16x32_bf16 v[24:27],a[248:251],v[56:59],v[24:27]
s_waitcnt lgkmcnt(4)
v_mfma_f32_16x16x32_bf16 v[24:27],a[32:35],v[32:35],v[24:27]
ds_read_b128 v[56:59],v4 offset:33792
v_mfma_f32_16x16x32_bf16 v[24:27],a[144:147],v[36:39],v[24:27]
buffer_load_dwordx4 v2,s[32:35],s63 offen offset:2048 sc1 lds
v_mfma_f32_16x16x32_bf16 v[28:31],a[148:151],v[36:39],v[28:31]
ds_read_b128 v[60:63],v4 offset:48128
v_mfma_f32_16x16x32_bf16 v[28:31],a[36:39],v[32:35],v[28:31]
s_waitcnt lgkmcnt(4)
v_mfma_f32_16x16x32_bf16 v[28:31],v[68:71],v[40:43],v[28:31]
ds_read_b128 v[32:35],v4 offset:6144
v_mfma_f32_16x16x32_bf16 v[28:31],v[180:183],v[44:47],v[28:31]
buffer_load_dwordx4 v2,s[32:35],s63 offen offset:3072 sc1 lds
v_mfma_f32_16x16x32_bf16 v[24:27],v[176:179],v[44:47],v[24:27]
ds_read_b128 v[36:39],v4 offset:20480
v_mfma_f32_16x16x32_bf16 v[24:27],v[64:67],v[40:43],v[24:27]
s_waitcnt lgkmcnt(4)
v_mfma_f32_16x16x32_bf16 v[24:27],a[40:43],v[48:51],v[24:27]
ds_read_b128 v[40:43],v4 offset:34816
v_mfma_f32_16x16x32_bf16 v[24:27],a[152:155],v[52:55],v[24:27]
buffer_load_dwordx4 v[12:15],v2,s[32:35],s71 offen sc1
s_add_u32 s71,s71,s30
v_mfma_f32_16x16x32_bf16 v[28:31],a[156:159],v[52:55],v[28:31]
ds_read_b128 v[44:47],v4 offset:49152
s_add_u32 s63,s63,4096
v_mfma_f32_16x16x32_bf16 v[28:31],a[44:47],v[48:51],v[28:31]
s_add_u32 m0,s64,4096
s_waitcnt lgkmcnt(4)
v_mfma_f32_16x16x32_bf16 v[28:31],v[76:79],v[56:59],v[28:31]
ds_read_b128 v[48:51],v4 offset:7168
v_mfma_f32_16x16x32_bf16 v[28:31],v[188:191],v[60:63],v[28:31]
buffer_load_dwordx4 v2,s[32:35],s63 offen offset:0 sc1 lds
v_mfma_f32_16x16x32_bf16 v[24:27],v[184:187],v[60:63],v[24:27]
ds_read_b128 v[52:55],v4 offset:21504
v_mfma_f32_16x16x32_bf16 v[24:27],v[72:75],v[56:59],v[24:27]
s_waitcnt lgkmcnt(4)
v_mfma_f32_16x16x32_bf16 v[24:27],a[48:51],v[32:35],v[24:27]
ds_read_b128 v[56:59],v4 offset:35840
v_mfma_f32_16x16x32_bf16 v[24:27],a[160:163],v[36:39],v[24:27]
buffer_load_dwordx4 v2,s[32:35],s63 offen offset:1024 sc1 lds
v_mfma_f32_16x16x32_bf16 v[28:31],a[164:167],v[36:39],v[28:31]
ds_read_b128 v[60:63],v4 offset:50176
v_mfma_f32_16x16x32_bf16 v[28:31],a[52:55],v[32:35],v[28:31]
s_waitcnt lgkmcnt(4)
v_mfma_f32_16x16x32_bf16 v[28:31],v[84:87],v[40:43],v[28:31]
ds_read_b128 v[32:35],v4 offset:8192
v_mfma_f32_16x16x32_bf16 v[28:31],v[196:199],v[44:47],v[28:31]
buffer_load_dwordx4 v2,s[32:35],s63 offen offset:2048 sc1 lds
v_mfma_f32_16x16x32_bf16 v[24:27],v[192:195],v[44:47],v[24:27]
ds_read_b128 v[36:39],v4 offset:22528
v_mfma_f32_16x16x32_bf16 v[24:27],v[80:83],v[40:43],v[24:27]
s_waitcnt lgkmcnt(4)
v_mfma_f32_16x16x32_bf16 v[24:27],a[56:59],v[48:51],v[24:27]
ds_read_b128 v[40:43],v4 offset:36864
v_mfma_f32_16x16x32_bf16 v[24:27],a[168:171],v[52:55],v[24:27]
buffer_load_dwordx4 v2,s[32:35],s63 offen offset:3072 sc1 lds
v_mfma_f32_16x16x32_bf16 v[28:31],a[172:175],v[52:55],v[28:31]
ds_read_b128 v[44:47],v4 offset:51200
v_mfma_f32_16x16x32_bf16 v[28:31],a[60:63],v[48:51],v[28:31]
s_waitcnt lgkmcnt(4)
v_mfma_f32_16x16x32_bf16 v[28:31],v[92:95],v[56:59],v[28:31]
ds_read_b128 v[48:51],v4 offset:9216
v_mfma_f32_16x16x32_bf16 v[28:31],v[204:207],v[60:63],v[28:31]
s_add_u32 s63,s63,4096
s_add_u32 m0,s64,8192
v_mfma_f32_16x16x32_bf16 v[24:27],v[200:203],v[60:63],v[24:27]
ds_read_b128 v[52:55],v4 offset:23552
v_mfma_f32_16x16x32_bf16 v[24:27],v[88:91],v[56:59],v[24:27]
s_waitcnt lgkmcnt(4)
v_mfma_f32_16x16x32_bf16 v[24:27],a[64:67],v[32:35],v[24:27]
ds_read_b128 v[56:59],v4 offset:37888
v_mfma_f32_16x16x32_bf16 v[24:27],a[176:179],v[36:39],v[24:27]
buffer_load_dwordx4 v2,s[32:35],s63 offen offset:0 sc1 lds
v_mfma_f32_16x16x32_bf16 v[28:31],a[180:183],v[36:39],v[28:31]
ds_read_b128 v[60:63],v4 offset:52224
v_mfma_f32_16x16x32_bf16 v[28:31],a[68:71],v[32:35],v[28:31]
s_waitcnt lgkmcnt(4)
v_mfma_f32_16x16x32_bf16 v[28:31],v[100:103],v[40:43],v[28:31]
ds_read_b128 v[32:35],v4 offset:10240
v_mfma_f32_16x16x32_bf16 v[28:31],v[212:215],v[44:47],v[28:31]
buffer_load_dwordx4 v2,s[32:35],s63 offen offset:1024 sc1 lds
v_mfma_f32_16x16x32_bf16 v[24:27],v[208:211],v[44:47],v[24:27]
ds_read_b128 v[36:39],v4 offset:24576
v_mfma_f32_16x16x32_bf16 v[24:27],v[96:99],v[40:43],v[24:27]
s_waitcnt lgkmcnt(4)
v_mfma_f32_16x16x32_bf16 v[24:27],a[72:75],v[48:51],v[24:27]
ds_read_b128 v[40:43],v4 offset:38912
v_mfma_f32_16x16x32_bf16 v[24:27],a[184:187],v[52:55],v[24:27]
buffer_load_dwordx4 v2,s[32:35],s63 offen offset:2048 sc1 lds
v_mfma_f32_16x16x32_bf16 v[28:31],a[188:191],v[52:55],v[28:31]
ds_read_b128 v[44:47],v4 offset:53248
v_mfma_f32_16x16x32_bf16 v[28:31],a[76:79],v[48:51],v[28:31]
s_waitcnt lgkmcnt(4)
v_mfma_f32_16x16x32_bf16 v[28:31],v[108:111],v[56:59],v[28:31]
ds_read_b128 v[48:51],v4 offset:11264
v_mfma_f32_16x16x32_bf16 v[28:31],v[220:223],v[60:63],v[28:31]
v_mfma_f32_16x16x32_bf16 v[24:27],v[216:219],v[60:63],v[24:27]
ds_read_b128 v[52:55],v4 offset:25600
v_mfma_f32_16x16x32_bf16 v[24:27],v[104:107],v[56:59],v[24:27]
s_waitcnt lgkmcnt(4)
v_mfma_f32_16x16x32_bf16 v[24:27],a[80:83],v[32:35],v[24:27]
ds_read_b128 v[56:59],v4 offset:39936
v_mfma_f32_16x16x32_bf16 v[24:27],a[192:195],v[36:39],v[24:27]
buffer_load_dwordx4 v2,s[32:35],s63 offen offset:3072 sc1 lds
s_add_u32 s63,s63,4096
v_mfma_f32_16x16x32_bf16 v[28:31],a[196:199],v[36:39],v[28:31]
ds_read_b128 v[60:63],v4 offset:54272
s_add_u32 m0,s64,12288
v_mfma_f32_16x16x32_bf16 v[28:31],a[84:87],v[32:35],v[28:31]
s_waitcnt lgkmcnt(4)
v_mfma_f32_16x16x32_bf16 v[28:31],v[116:119],v[40:43],v[28:31]
ds_read_b128 v[32:35],v4 offset:12288
v_mfma_f32_16x16x32_bf16 v[28:31],v[228:231],v[44:47],v[28:31]
buffer_load_dwordx4 v2,s[32:35],s63 offen offset:0 sc1 lds
v_mfma_f32_16x16x32_bf16 v[24:27],v[224:227],v[44:47],v[24:27]
ds_read_b128 v[36:39],v4 offset:26624
v_mfma_f32_16x16x32_bf16 v[24:27],v[112:115],v[40:43],v[24:27]
s_waitcnt lgkmcnt(4)
v_mfma_f32_16x16x32_bf16 v[24:27],a[88:91],v[48:51],v[24:27]
ds_read_b128 v[40:43],v4 offset:40960
v_mfma_f32_16x16x32_bf16 v[24:27],a[200:203],v[52:55],v[24:27]
buffer_load_dwordx4 v2,s[32:35],s63 offen offset:1024 sc1 lds
s_add_u32 s63,s63,s31
v_mfma_f32_16x16x32_bf16 v[28:31],a[204:207],v[52:55],v[28:31]
ds_read_b128 v[44:47],v4 offset:55296
v_mfma_f32_16x16x32_bf16 v[28:31],a[92:95],v[48:51],v[28:31]
s_waitcnt lgkmcnt(4)
v_mfma_f32_16x16x32_bf16 v[28:31],v[124:127],v[56:59],v[28:31]
ds_read_b128 v[48:51],v4 offset:13312
s_waitcnt vmcnt(10)
v_mfma_f32_16x16x32_bf16 v[28:31],v[236:239],v[60:63],v[28:31]
v_lshlrev_b32 v16,16,v12
v_and_b32 v17,0xffff0000,v12
v_mfma_f32_16x16x32_bf16 v[24:27],v[232:235],v[60:63],v[24:27]
ds_read_b128 v[52:55],v4 offset:27648
v_lshlrev_b32 v18,16,v13
v_mfma_f32_16x16x32_bf16 v[24:27],v[120:123],v[56:59],v[24:27]
v_and_b32 v19,0xffff0000,v13
s_waitcnt lgkmcnt(4)
v_mfma_f32_16x16x32_bf16 v[24:27],a[96:99],v[32:35],v[24:27]
ds_read_b128 v[56:59],v4 offset:41984
v_lshlrev_b32 v20,16,v14
v_mfma_f32_16x16x32_bf16 v[24:27],a[208:211],v[36:39],v[24:27]
s_barrier
v_mfma_f32_16x16x32_bf16 v[28:31],a[212:215],v[36:39],v[28:31]
ds_read_b128 v[60:63],v4 offset:56320
v_and_b32 v21,0xffff0000,v14
v_mfma_f32_16x16x32_bf16 v[28:31],a[100:103],v[32:35],v[28:31]
v_lshlrev_b32 v22,16,v15
s_waitcnt lgkmcnt(4)
v_mfma_f32_16x16x32_bf16 v[28:31],v[132:135],v[40:43],v[28:31]
ds_read_b128 v[32:35],v2 offset:0
v_and_b32 v23,0xffff0000,v15
v_mfma_f32_16x16x32_bf16 v[28:31],v[244:247],v[44:47],v[28:31]
v_mfma_f32_16x16x32_bf16 v[24:27],v[240:243],v[44:47],v[24:27]
ds_read_b128 v[36:39],v2 offset:14336
v_mfma_f32_16x16x32_bf16 v[24:27],v[128:131],v[40:43],v[24:27]
s_waitcnt lgkmcnt(4)
v_mfma_f32_16x16x32_bf16 v[24:27],a[104:107],v[48:51],v[24:27]
ds_read_b128 v[40:43],v2 offset:28672
v_mfma_f32_16x16x32_bf16 v[24:27],a[216:219],v[52:55],v[24:27]
v_mfma_f32_16x16x32_bf16 v[28:31],a[220:223],v[52:55],v[28:31]
ds_read_b128 v[44:47],v2 offset:43008
v_mfma_f32_16x16x32_bf16 v[28:31],a[108:111],v[48:51],v[28:31]
s_waitcnt lgkmcnt(4)
v_mfma_f32_16x16x32_bf16 v[28:31],v[140:143],v[56:59],v[28:31]
ds_read_b128 v[48:51],v2 offset:1024
v_mfma_f32_16x16x32_bf16 v[28:31],v[252:255],v[60:63],v[28:31]
v_mfma_f32_16x16x32_bf16 v[24:27],v[248:251],v[60:63],v[24:27]
ds_read_b128 v[52:55],v2 offset:15360
s_sub_u32 s62,s62,1
v_mfma_f32_16x16x32_bf16 v[24:27],v[136:139],v[56:59],v[24:27]
s_cbranch_scc1 .L_dmt_2
s_waitcnt lgkmcnt(4)
v_mfma_f32_16x16x32_bf16 v[16:19],a[0:3],v[32:35],v[16:19]
ds_read_b128 v[56:59],v2 offset:29696
v_mfma_f32_16x16x32_bf16 v[16:19],a[112:115],v[36:39],v[16:19]
v_mfma_f32_16x16x32_bf16 v[20:23],a[116:119],v[36:39],v[20:23]
ds_read_b128 v[60:63],v2 offset:44032
v_mfma_f32_16x16x32_bf16 v[20:23],a[4:7],v[32:35],v[20:23]
v_cvt_pk_bf16_f32 v15,v30,v31
s_waitcnt lgkmcnt(4)
v_mfma_f32_16x16x32_bf16 v[20:23],a[228:231],v[40:43],v[20:23]
ds_read_b128 v[32:35],v2 offset:2048
v_cvt_pk_bf16_f32 v14,v28,v29
v_mfma_f32_16x16x32_bf16 v[20:23],v[148:151],v[44:47],v[20:23]
v_cvt_pk_bf16_f32 v13,v26,v27
v_cvt_pk_bf16_f32 v12,v24,v25
v_mfma_f32_16x16x32_bf16 v[16:19],v[144:147],v[44:47],v[16:19]
ds_read_b128 v[36:39],v2 offset:16384
ds_write_b128 v5,v[12:15]
v_mfma_f32_16x16x32_bf16 v[16:19],a[224:227],v[40:43],v[16:19]
ds_read2st64_b32 v[12:13],v3 offset0:0 offset1:1
s_waitcnt lgkmcnt(6)
v_mfma_f32_16x16x32_bf16 v[16:19],a[8:11],v[48:51],v[16:19]
ds_read_b128 v[40:43],v2 offset:30720
ds_read2st64_b32 v[14:15],v3 offset0:2 offset1:3
v_mfma_f32_16x16x32_bf16 v[16:19],a[120:123],v[52:55],v[16:19]
v_mfma_f32_16x16x32_bf16 v[20:23],a[124:127],v[52:55],v[20:23]
ds_read_b128 v[44:47],v2 offset:45056
v_mfma_f32_16x16x32_bf16 v[20:23],a[12:15],v[48:51],v[20:23]
s_waitcnt lgkmcnt(7)
v_mfma_f32_16x16x32_bf16 v[20:23],a[236:239],v[56:59],v[20:23]
ds_read_b128 v[48:51],v2 offset:3072
v_mfma_f32_16x16x32_bf16 v[20:23],v[156:159],v[60:63],v[20:23]
s_waitcnt lgkmcnt(2)
buffer_atomic_pk_add_bf16 v12,v1,s[44:47],s65 offen offset:0 sc1
v_mfma_f32_16x16x32_bf16 v[16:19],v[152:155],v[60:63],v[16:19]
ds_read_b128 v[52:55],v2 offset:17408
s_add_u32 s70,s70,s68
v_mfma_f32_16x16x32_bf16 v[16:19],a[232:235],v[56:59],v[16:19]
buffer_atomic_pk_add_bf16 v13,v1,s[44:47],s65 offen offset:2048 sc1
v_mfma_f32_16x16x32_bf16 v[16:19],a[16:19],v[32:35],v[16:19]
ds_read_b128 v[56:59],v2 offset:31744
s_add_u32 s65,s65,16384
v_mfma_f32_16x16x32_bf16 v[16:19],a[128:131],v[36:39],v[16:19]
s_waitcnt vmcnt(2)
s_barrier
v_mfma_f32_16x16x32_bf16 v[20:23],a[132:135],v[36:39],v[20:23]
ds_read_b128 v[60:63],v2 offset:46080
v_mfma_f32_16x16x32_bf16 v[20:23],a[20:23],v[32:35],v[20:23]
buffer_atomic_pk_add_bf16 v14,v1,s[44:47],s65 offen offset:0 sc1
s_waitcnt lgkmcnt(4)
v_mfma_f32_16x16x32_bf16 v[20:23],a[244:247],v[40:43],v[20:23]
ds_read_b128 v[32:35],v2 offset:4096
s_add_u32 m0,s64,57344
v_mfma_f32_16x16x32_bf16 v[20:23],v[164:167],v[44:47],v[20:23]
buffer_load_dwordx4 v2,s[32:35],s63 offen offset:0 sc1 lds
s_mov_b32 s68,s75
v_mfma_f32_16x16x32_bf16 v[16:19],v[160:163],v[44:47],v[16:19]
ds_read_b128 v[36:39],v2 offset:18432
v_mfma_f32_16x16x32_bf16 v[16:19],a[240:243],v[40:43],v[16:19]
buffer_atomic_pk_add_bf16 v15,v1,s[44:47],s65 offen offset:2048 sc1
s_waitcnt lgkmcnt(4)
v_mfma_f32_16x16x32_bf16 v[16:19],a[24:27],v[48:51],v[16:19]
ds_read_b128 v[40:43],v2 offset:32768
s_and_b32 s65,s70,0x3f8000
v_mfma_f32_16x16x32_bf16 v[16:19],a[136:139],v[52:55],v[16:19]
v_mfma_f32_16x16x32_bf16 v[20:23],a[140:143],v[52:55],v[20:23]
ds_read_b128 v[44:47],v2 offset:47104
v_mfma_f32_16x16x32_bf16 v[20:23],a[28:31],v[48:51],v[20:23]
s_waitcnt lgkmcnt(4)
v_mfma_f32_16x16x32_bf16 v[20:23],a[252:255],v[56:59],v[20:23]
ds_read_b128 v[48:51],v2 offset:5120
v_mfma_f32_16x16x32_bf16 v[20:23],v[172:175],v[60:63],v[20:23]
buffer_load_dwordx4 v2,s[32:35],s63 offen offset:1024 sc1 lds
v_mfma_f32_16x16x32_bf16 v[16:19],v[168:171],v[60:63],v[16:19]
ds_read_b128 v[52:55],v2 offset:19456
v_mfma_f32_16x16x32_bf16 v[16:19],a[248:251],v[56:59],v[16:19]
s_waitcnt lgkmcnt(4)
v_mfma_f32_16x16x32_bf16 v[16:19],a[32:35],v[32:35],v[16:19]
ds_read_b128 v[56:59],v2 offset:33792
v_mfma_f32_16x16x32_bf16 v[16:19],a[144:147],v[36:39],v[16:19]
buffer_load_dwordx4 v2,s[32:35],s63 offen offset:2048 sc1 lds
v_mfma_f32_16x16x32_bf16 v[20:23],a[148:151],v[36:39],v[20:23]
ds_read_b128 v[60:63],v2 offset:48128
v_mfma_f32_16x16x32_bf16 v[20:23],a[36:39],v[32:35],v[20:23]
s_waitcnt lgkmcnt(4)
v_mfma_f32_16x16x32_bf16 v[20:23],v[68:71],v[40:43],v[20:23]
ds_read_b128 v[32:35],v2 offset:6144
v_mfma_f32_16x16x32_bf16 v[20:23],v[180:183],v[44:47],v[20:23]
buffer_load_dwordx4 v2,s[32:35],s63 offen offset:3072 sc1 lds
v_mfma_f32_16x16x32_bf16 v[16:19],v[176:179],v[44:47],v[16:19]
ds_read_b128 v[36:39],v2 offset:20480
v_mfma_f32_16x16x32_bf16 v[16:19],v[64:67],v[40:43],v[16:19]
s_waitcnt lgkmcnt(4)
v_mfma_f32_16x16x32_bf16 v[16:19],a[40:43],v[48:51],v[16:19]
ds_read_b128 v[40:43],v2 offset:34816
v_mfma_f32_16x16x32_bf16 v[16:19],a[152:155],v[52:55],v[16:19]
buffer_load_dwordx4 v[12:15],v2,s[32:35],s71 offen sc1
s_add_u32 s71,s71,s30
v_mfma_f32_16x16x32_bf16 v[20:23],a[156:159],v[52:55],v[20:23]
ds_read_b128 v[44:47],v2 offset:49152
s_add_u32 s63,s63,4096
v_mfma_f32_16x16x32_bf16 v[20:23],a[44:47],v[48:51],v[20:23]
s_add_u32 m0,s64,61440
s_waitcnt lgkmcnt(4)
v_mfma_f32_16x16x32_bf16 v[20:23],v[76:79],v[56:59],v[20:23]
ds_read_b128 v[48:51],v2 offset:7168
v_mfma_f32_16x16x32_bf16 v[20:23],v[188:191],v[60:63],v[20:23]
buffer_load_dwordx4 v2,s[32:35],s63 offen offset:0 sc1 lds
v_mfma_f32_16x16x32_bf16 v[16:19],v[184:187],v[60:63],v[16:19]
ds_read_b128 v[52:55],v2 offset:21504
v_mfma_f32_16x16x32_bf16 v[16:19],v[72:75],v[56:59],v[16:19]
s_waitcnt lgkmcnt(4)
v_mfma_f32_16x16x32_bf16 v[16:19],a[48:51],v[32:35],v[16:19]
ds_read_b128 v[56:59],v2 offset:35840
v_mfma_f32_16x16x32_bf16 v[16:19],a[160:163],v[36:39],v[16:19]
buffer_load_dwordx4 v2,s[32:35],s63 offen offset:1024 sc1 lds
v_mfma_f32_16x16x32_bf16 v[20:23],a[164:167],v[36:39],v[20:23]
ds_read_b128 v[60:63],v2 offset:50176
v_mfma_f32_16x16x32_bf16 v[20:23],a[52:55],v[32:35],v[20:23]
s_waitcnt lgkmcnt(4)
v_mfma_f32_16x16x32_bf16 v[20:23],v[84:87],v[40:43],v[20:23]
ds_read_b128 v[32:35],v2 offset:8192
v_mfma_f32_16x16x32_bf16 v[20:23],v[196:199],v[44:47],v[20:23]
buffer_load_dwordx4 v2,s[32:35],s63 offen offset:2048 sc1 lds
v_mfma_f32_16x16x32_bf16 v[16:19],v[192:195],v[44:47],v[16:19]
ds_read_b128 v[36:39],v2 offset:22528
v_mfma_f32_16x16x32_bf16 v[16:19],v[80:83],v[40:43],v[16:19]
s_waitcnt lgkmcnt(4)
v_mfma_f32_16x16x32_bf16 v[16:19],a[56:59],v[48:51],v[16:19]
ds_read_b128 v[40:43],v2 offset:36864
v_mfma_f32_16x16x32_bf16 v[16:19],a[168:171],v[52:55],v[16:19]
buffer_load_dwordx4 v2,s[32:35],s63 offen offset:3072 sc1 lds
v_mfma_f32_16x16x32_bf16 v[20:23],a[172:175],v[52:55],v[20:23]
ds_read_b128 v[44:47],v2 offset:51200
v_mfma_f32_16x16x32_bf16 v[20:23],a[60:63],v[48:51],v[20:23]
s_waitcnt lgkmcnt(4)
v_mfma_f32_16x16x32_bf16 v[20:23],v[92:95],v[56:59],v[20:23]
ds_read_b128 v[48:51],v2 offset:9216
v_mfma_f32_16x16x32_bf16 v[20:23],v[204:207],v[60:63],v[20:23]
s_add_u32 s63,s63,4096
s_add_u32 m0,s64,65536
v_mfma_f32_16x16x32_bf16 v[16:19],v[200:203],v[60:63],v[16:19]
ds_read_b128 v[52:55],v2 offset:23552
v_mfma_f32_16x16x32_bf16 v[16:19],v[88:91],v[56:59],v[16:19]
s_waitcnt lgkmcnt(4)
v_mfma_f32_16x16x32_bf16 v[16:19],a[64:67],v[32:35],v[16:19]
ds_read_b128 v[56:59],v2 offset:37888
v_mfma_f32_16x16x32_bf16 v[16:19],a[176:179],v[36:39],v[16:19]
buffer_load_dwordx4 v2,s[32:35],s63 offen offset:0 sc1 lds
v_mfma_f32_16x16x32_bf16 v[20:23],a[180:183],v[36:39],v[20:23]
ds_read_b128 v[60:63],v2 offset:52224
v_mfma_f32_16x16x32_bf16 v[20:23],a[68:71],v[32:35],v[20:23]
s_waitcnt lgkmcnt(4)
v_mfma_f32_16x16x32_bf16 v[20:23],v[100:103],v[40:43],v[20:23]
ds_read_b128 v[32:35],v2 offset:10240
v_mfma_f32_16x16x32_bf16 v[20:23],v[212:215],v[44:47],v[20:23]
buffer_load_dwordx4 v2,s[32:35],s63 offen offset:1024 sc1 lds
v_mfma_f32_16x16x32_bf16 v[16:19],v[208:211],v[44:47],v[16:19]
ds_read_b128 v[36:39],v2 offset:24576
v_mfma_f32_16x16x32_bf16 v[16:19],v[96:99],v[40:43],v[16:19]
s_waitcnt lgkmcnt(4)
v_mfma_f32_16x16x32_bf16 v[16:19],a[72:75],v[48:51],v[16:19]
ds_read_b128 v[40:43],v2 offset:38912
v_mfma_f32_16x16x32_bf16 v[16:19],a[184:187],v[52:55],v[16:19]
buffer_load_dwordx4 v2,s[32:35],s63 offen offset:2048 sc1 lds
v_mfma_f32_16x16x32_bf16 v[20:23],a[188:191],v[52:55],v[20:23]
ds_read_b128 v[44:47],v2 offset:53248
v_mfma_f32_16x16x32_bf16 v[20:23],a[76:79],v[48:51],v[20:23]
s_waitcnt lgkmcnt(4)
v_mfma_f32_16x16x32_bf16 v[20:23],v[108:111],v[56:59],v[20:23]
ds_read_b128 v[48:51],v2 offset:11264
v_mfma_f32_16x16x32_bf16 v[20:23],v[220:223],v[60:63],v[20:23]
v_mfma_f32_16x16x32_bf16 v[16:19],v[216:219],v[60:63],v[16:19]
ds_read_b128 v[52:55],v2 offset:25600
v_mfma_f32_16x16x32_bf16 v[16:19],v[104:107],v[56:59],v[16:19]
s_waitcnt lgkmcnt(4)
)#"
R"#(
v_mfma_f32_16x16x32_bf16 v[16:19],a[80:83],v[32:35],v[16:19]
ds_read_b128 v[56:59],v2 offset:39936
v_mfma_f32_16x16x32_bf16 v[16:19],a[192:195],v[36:39],v[16:19]
buffer_load_dwordx4 v2,s[32:35],s63 offen offset:3072 sc1 lds
s_add_u32 s63,s63,4096
v_mfma_f32_16x16x32_bf16 v[20:23],a[196:199],v[36:39],v[20:23]
ds_read_b128 v[60:63],v2 offset:54272
s_add_u32 m0,s64,69632
v_mfma_f32_16x16x32_bf16 v[20:23],a[84:87],v[32:35],v[20:23]
s_waitcnt lgkmcnt(4)
v_mfma_f32_16x16x32_bf16 v[20:23],v[116:119],v[40:43],v[20:23]
ds_read_b128 v[32:35],v2 offset:12288
v_mfma_f32_16x16x32_bf16 v[20:23],v[228:231],v[44:47],v[20:23]
buffer_load_dwordx4 v2,s[32:35],s63 offen offset:0 sc1 lds
v_mfma_f32_16x16x32_bf16 v[16:19],v[224:227],v[44:47],v[16:19]
ds_read_b128 v[36:39],v2 offset:26624
v_mfma_f32_16x16x32_bf16 v[16:19],v[112:115],v[40:43],v[16:19]
s_waitcnt lgkmcnt(4)
v_mfma_f32_16x16x32_bf16 v[16:19],a[88:91],v[48:51],v[16:19]
ds_read_b128 v[40:43],v2 offset:40960
v_mfma_f32_16x16x32_bf16 v[16:19],a[200:203],v[52:55],v[16:19]
buffer_load_dwordx4 v2,s[32:35],s63 offen offset:1024 sc1 lds
s_add_u32 s63,s63,s31
v_mfma_f32_16x16x32_bf16 v[20:23],a[204:207],v[52:55],v[20:23]
ds_read_b128 v[44:47],v2 offset:55296
v_mfma_f32_16x16x32_bf16 v[20:23],a[92:95],v[48:51],v[20:23]
s_waitcnt lgkmcnt(4)
v_mfma_f32_16x16x32_bf16 v[20:23],v[124:127],v[56:59],v[20:23]
ds_read_b128 v[48:51],v2 offset:13312
s_waitcnt vmcnt(10)
v_mfma_f32_16x16x32_bf16 v[20:23],v[236:239],v[60:63],v[20:23]
v_lshlrev_b32 v24,16,v12
v_and_b32 v25,0xffff0000,v12
v_mfma_f32_16x16x32_bf16 v[16:19],v[232:235],v[60:63],v[16:19]
ds_read_b128 v[52:55],v2 offset:27648
v_lshlrev_b32 v26,16,v13
v_mfma_f32_16x16x32_bf16 v[16:19],v[120:123],v[56:59],v[16:19]
v_and_b32 v27,0xffff0000,v13
s_waitcnt lgkmcnt(4)
v_mfma_f32_16x16x32_bf16 v[16:19],a[96:99],v[32:35],v[16:19]
ds_read_b128 v[56:59],v2 offset:41984
v_lshlrev_b32 v28,16,v14
v_mfma_f32_16x16x32_bf16 v[16:19],a[208:211],v[36:39],v[16:19]
s_barrier
v_mfma_f32_16x16x32_bf16 v[20:23],a[212:215],v[36:39],v[20:23]
ds_read_b128 v[60:63],v2 offset:56320
v_and_b32 v29,0xffff0000,v14
v_mfma_f32_16x16x32_bf16 v[20:23],a[100:103],v[32:35],v[20:23]
v_lshlrev_b32 v30,16,v15
s_waitcnt lgkmcnt(4)
v_mfma_f32_16x16x32_bf16 v[20:23],v[132:135],v[40:43],v[20:23]
ds_read_b128 v[32:35],v4 offset:0
v_and_b32 v31,0xffff0000,v15
v_mfma_f32_16x16x32_bf16 v[20:23],v[244:247],v[44:47],v[20:23]
v_mfma_f32_16x16x32_bf16 v[16:19],v[240:243],v[44:47],v[16:19]
ds_read_b128 v[36:39],v4 offset:14336
v_mfma_f32_16x16x32_bf16 v[16:19],v[128:131],v[40:43],v[16:19]
s_waitcnt lgkmcnt(4)
v_mfma_f32_16x16x32_bf16 v[16:19],a[104:107],v[48:51],v[16:19]
ds_read_b128 v[40:43],v4 offset:28672
v_mfma_f32_16x16x32_bf16 v[16:19],a[216:219],v[52:55],v[16:19]
v_mfma_f32_16x16x32_bf16 v[20:23],a[220:223],v[52:55],v[20:23]
ds_read_b128 v[44:47],v4 offset:43008
v_mfma_f32_16x16x32_bf16 v[20:23],a[108:111],v[48:51],v[20:23]
s_waitcnt lgkmcnt(4)
v_mfma_f32_16x16x32_bf16 v[20:23],v[140:143],v[56:59],v[20:23]
ds_read_b128 v[48:51],v4 offset:1024
v_mfma_f32_16x16x32_bf16 v[20:23],v[252:255],v[60:63],v[20:23]
v_mfma_f32_16x16x32_bf16 v[16:19],v[248:251],v[60:63],v[16:19]
ds_read_b128 v[52:55],v4 offset:15360
s_sub_u32 s62,s62,1
v_mfma_f32_16x16x32_bf16 v[16:19],v[136:139],v[56:59],v[16:19]
s_cbranch_scc0 .L_dml_2
.L_dmu_2:
s_nop 1
s_nop 0
v_cvt_pk_bf16_f32 v15,v22,v23
v_cvt_pk_bf16_f32 v14,v20,v21
s_nop 1
v_cvt_pk_bf16_f32 v13,v18,v19
v_cvt_pk_bf16_f32 v12,v16,v17
ds_write_b128 v5,v[12:15]
ds_read2st64_b32 v[12:13],v3 offset0:0 offset1:1
ds_read2st64_b32 v[14:15],v3 offset0:2 offset1:3
s_waitcnt lgkmcnt(0)
buffer_atomic_pk_add_bf16 v12,v1,s[44:47],s65 offen offset:0 sc1
s_add_u32 s70,s70,s75
buffer_atomic_pk_add_bf16 v13,v1,s[44:47],s65 offen offset:2048 sc1
s_add_u32 s65,s65,16384
buffer_atomic_pk_add_bf16 v14,v1,s[44:47],s65 offen offset:0 sc1
buffer_atomic_pk_add_bf16 v15,v1,s[44:47],s65 offen offset:2048 sc1
s_and_b32 s65,s70,0x3f8000
s_branch .L_end
.L_dmt_2:
s_nop 1
s_nop 0
v_cvt_pk_bf16_f32 v15,v30,v31
v_cvt_pk_bf16_f32 v14,v28,v29
s_nop 1
v_cvt_pk_bf16_f32 v13,v26,v27
v_cvt_pk_bf16_f32 v12,v24,v25
ds_write_b128 v5,v[12:15]
ds_read2st64_b32 v[12:13],v3 offset0:0 offset1:1
ds_read2st64_b32 v[14:15],v3 offset0:2 offset1:3
s_waitcnt lgkmcnt(0)
buffer_atomic_pk_add_bf16 v12,v1,s[44:47],s65 offen offset:0 sc1
s_add_u32 s70,s70,s68
buffer_atomic_pk_add_bf16 v13,v1,s[44:47],s65 offen offset:2048 sc1
s_add_u32 s65,s65,16384
buffer_atomic_pk_add_bf16 v14,v1,s[44:47],s65 offen offset:0 sc1
s_mov_b32 s68,s75
buffer_atomic_pk_add_bf16 v15,v1,s[44:47],s65 offen offset:2048 sc1
s_and_b32 s65,s70,0x3f8000
s_branch .L_end
.p2align 6
.L_dlv3:
.L_dll_3:
s_waitcnt lgkmcnt(4)
v_mfma_f32_16x16x32_bf16 v[16:19],a[0:3],v[32:35],v[16:19]
ds_read_b128 v[56:59],v2 offset:29696
v_mfma_f32_16x16x32_bf16 v[16:19],a[112:115],v[36:39],v[16:19]
v_mfma_f32_16x16x32_bf16 v[20:23],a[116:119],v[36:39],v[20:23]
ds_read_b128 v[60:63],v2 offset:44032
v_mfma_f32_16x16x32_bf16 v[20:23],a[4:7],v[32:35],v[20:23]
s_add_u32 s68,s68,1187840
s_waitcnt lgkmcnt(4)
v_mfma_f32_16x16x32_bf16 v[20:23],a[228:231],v[40:43],v[20:23]
ds_read_b128 v[32:35],v2 offset:2048
v_mfma_f32_16x16x32_bf16 v[20:23],v[148:151],v[44:47],v[20:23]
buffer_store_dwordx4 v[28:31],v2,s[32:35],s68 offen offset:1024 sc1
v_mfma_f32_16x16x32_bf16 v[16:19],v[144:147],v[44:47],v[16:19]
ds_read_b128 v[36:39],v2 offset:16384
v_mfma_f32_16x16x32_bf16 v[16:19],a[224:227],v[40:43],v[16:19]
s_waitcnt lgkmcnt(4)
v_mfma_f32_16x16x32_bf16 v[16:19],a[8:11],v[48:51],v[16:19]
ds_read_b128 v[40:43],v2 offset:30720
v_mfma_f32_16x16x32_bf16 v[16:19],a[120:123],v[52:55],v[16:19]
buffer_store_dwordx4 v[24:27],v2,s[32:35],s68 offen sc1
s_mov_b32 s67,s68
v_mfma_f32_16x16x32_bf16 v[20:23],a[124:127],v[52:55],v[20:23]
ds_read_b128 v[44:47],v2 offset:45056
v_mfma_f32_16x16x32_bf16 v[20:23],a[12:15],v[48:51],v[20:23]
s_waitcnt lgkmcnt(4)
v_mfma_f32_16x16x32_bf16 v[20:23],a[236:239],v[56:59],v[20:23]
ds_read_b128 v[48:51],v2 offset:3072
v_mfma_f32_16x16x32_bf16 v[20:23],v[156:159],v[60:63],v[20:23]
v_mfma_f32_16x16x32_bf16 v[16:19],v[152:155],v[60:63],v[16:19]
ds_read_b128 v[52:55],v2 offset:17408
v_mfma_f32_16x16x32_bf16 v[16:19],a[232:235],v[56:59],v[16:19]
s_waitcnt lgkmcnt(4)
v_mfma_f32_16x16x32_bf16 v[16:19],a[16:19],v[32:35],v[16:19]
s_waitcnt vmcnt(2)
s_barrier
v_mfma_f32_16x16x32_bf16 v[16:19],a[128:131],v[36:39],v[16:19]
ds_read_b128 v[56:59],v2 offset:31744
v_mfma_f32_16x16x32_bf16 v[20:23],a[132:135],v[36:39],v[20:23]
ds_read_b128 v[60:63],v2 offset:46080
v_mfma_f32_16x16x32_bf16 v[20:23],a[20:23],v[32:35],v[20:23]
s_waitcnt lgkmcnt(4)
v_mfma_f32_16x16x32_bf16 v[20:23],a[244:247],v[40:43],v[20:23]
ds_read_b128 v[32:35],v2 offset:4096
s_add_u32 m0,s64,57344
v_mfma_f32_16x16x32_bf16 v[20:23],v[164:167],v[44:47],v[20:23]
buffer_load_dwordx4 v2,s[32:35],s63 offen offset:0 sc1 lds
v_mfma_f32_16x16x32_bf16 v[16:19],v[160:163],v[44:47],v[16:19]
ds_read_b128 v[36:39],v2 offset:18432
v_mfma_f32_16x16x32_bf16 v[16:19],a[240:243],v[40:43],v[16:19]
s_waitcnt lgkmcnt(4)
v_mfma_f32_16x16x32_bf16 v[16:19],a[24:27],v[48:51],v[16:19]
ds_read_b128 v[40:43],v2 offset:32768
v_mfma_f32_16x16x32_bf16 v[16:19],a[136:139],v[52:55],v[16:19]
v_mfma_f32_16x16x32_bf16 v[20:23],a[140:143],v[52:55],v[20:23]
ds_read_b128 v[44:47],v2 offset:47104
v_mfma_f32_16x16x32_bf16 v[20:23],a[28:31],v[48:51],v[20:23]
s_waitcnt lgkmcnt(4)
v_mfma_f32_16x16x32_bf16 v[20:23],a[252:255],v[56:59],v[20:23]
ds_read_b128 v[48:51],v2 offset:5120
v_mfma_f32_16x16x32_bf16 v[20:23],v[172:175],v[60:63],v[20:23]
buffer_load_dwordx4 v2,s[32:35],s63 offen offset:1024 sc1 lds
v_mfma_f32_16x16x32_bf16 v[16:19],v[168:171],v[60:63],v[16:19]
ds_read_b128 v[52:55],v2 offset:19456
v_mfma_f32_16x16x32_bf16 v[16:19],a[248:251],v[56:59],v[16:19]
s_waitcnt lgkmcnt(4)
v_mfma_f32_16x16x32_bf16 v[16:19],a[32:35],v[32:35],v[16:19]
ds_read_b128 v[56:59],v2 offset:33792
v_mfma_f32_16x16x32_bf16 v[16:19],a[144:147],v[36:39],v[16:19]
buffer_load_dwordx4 v2,s[32:35],s63 offen offset:2048 sc1 lds
v_mfma_f32_16x16x32_bf16 v[20:23],a[148:151],v[36:39],v[20:23]
ds_read_b128 v[60:63],v2 offset:48128
v_mfma_f32_16x16x32_bf16 v[20:23],a[36:39],v[32:35],v[20:23]
s_waitcnt lgkmcnt(4)
v_mfma_f32_16x16x32_bf16 v[20:23],v[68:71],v[40:43],v[20:23]
ds_read_b128 v[32:35],v2 offset:6144
v_mfma_f32_16x16x32_bf16 v[20:23],v[180:183],v[44:47],v[20:23]
buffer_load_dwordx4 v2,s[32:35],s63 offen offset:3072 sc1 lds
v_mfma_f32_16x16x32_bf16 v[16:19],v[176:179],v[44:47],v[16:19]
ds_read_b128 v[36:39],v2 offset:20480
v_mfma_f32_16x16x32_bf16 v[16:19],v[64:67],v[40:43],v[16:19]
s_waitcnt lgkmcnt(4)
v_mfma_f32_16x16x32_bf16 v[16:19],a[40:43],v[48:51],v[16:19]
ds_read_b128 v[40:43],v2 offset:34816
v_mfma_f32_16x16x32_bf16 v[16:19],a[152:155],v[52:55],v[16:19]
buffer_load_dwordx4 v[12:15],v2,s[32:35],s71 offen sc1
s_add_u32 s71,s71,s30
v_mfma_f32_16x16x32_bf16 v[20:23],a[156:159],v[52:55],v[20:23]
ds_read_b128 v[44:47],v2 offset:49152
s_add_u32 s63,s63,4096
v_mfma_f32_16x16x32_bf16 v[20:23],a[44:47],v[48:51],v[20:23]
s_add_u32 m0,s64,61440
s_waitcnt lgkmcnt(4)
v_mfma_f32_16x16x32_bf16 v[20:23],v[76:79],v[56:59],v[20:23]
ds_read_b128 v[48:51],v2 offset:7168
v_mfma_f32_16x16x32_bf16 v[20:23],v[188:191],v[60:63],v[20:23]
buffer_load_dwordx4 v2,s[32:35],s63 offen offset:0 sc1 lds
v_mfma_f32_16x16x32_bf16 v[16:19],v[184:187],v[60:63],v[16:19]
ds_read_b128 v[52:55],v2 offset:21504
v_mfma_f32_16x16x32_bf16 v[16:19],v[72:75],v[56:59],v[16:19]
s_waitcnt lgkmcnt(4)
v_mfma_f32_16x16x32_bf16 v[16:19],a[48:51],v[32:35],v[16:19]
ds_read_b128 v[56:59],v2 offset:35840
v_mfma_f32_16x16x32_bf16 v[16:19],a[160:163],v[36:39],v[16:19]
buffer_load_dwordx4 v2,s[32:35],s63 offen offset:1024 sc1 lds
v_mfma_f32_16x16x32_bf16 v[20:23],a[164:167],v[36:39],v[20:23]
ds_read_b128 v[60:63],v2 offset:50176
v_mfma_f32_16x16x32_bf16 v[20:23],a[52:55],v[32:35],v[20:23]
s_waitcnt lgkmcnt(4)
v_mfma_f32_16x16x32_bf16 v[20:23],v[84:87],v[40:43],v[20:23]
ds_read_b128 v[32:35],v2 offset:8192
v_mfma_f32_16x16x32_bf16 v[20:23],v[196:199],v[44:47],v[20:23]
buffer_load_dwordx4 v2,s[32:35],s63 offen offset:2048 sc1 lds
v_mfma_f32_16x16x32_bf16 v[16:19],v[192:195],v[44:47],v[16:19]
ds_read_b128 v[36:39],v2 offset:22528
v_mfma_f32_16x16x32_bf16 v[16:19],v[80:83],v[40:43],v[16:19]
s_waitcnt lgkmcnt(4)
v_mfma_f32_16x16x32_bf16 v[16:19],a[56:59],v[48:51],v[16:19]
ds_read_b128 v[40:43],v2 offset:36864
v_mfma_f32_16x16x32_bf16 v[16:19],a[168:171],v[52:55],v[16:19]
buffer_load_dwordx4 v2,s[32:35],s63 offen offset:3072 sc1 lds
v_mfma_f32_16x16x32_bf16 v[20:23],a[172:175],v[52:55],v[20:23]
ds_read_b128 v[44:47],v2 offset:51200
v_mfma_f32_16x16x32_bf16 v[20:23],a[60:63],v[48:51],v[20:23]
s_waitcnt lgkmcnt(4)
v_mfma_f32_16x16x32_bf16 v[20:23],v[92:95],v[56:59],v[20:23]
ds_read_b128 v[48:51],v2 offset:9216
v_mfma_f32_16x16x32_bf16 v[20:23],v[204:207],v[60:63],v[20:23]
s_add_u32 s63,s63,4096
s_add_u32 m0,s64,65536
v_mfma_f32_16x16x32_bf16 v[16:19],v[200:203],v[60:63],v[16:19]
ds_read_b128 v[52:55],v2 offset:23552
v_mfma_f32_16x16x32_bf16 v[16:19],v[88:91],v[56:59],v[16:19]
s_waitcnt lgkmcnt(4)
v_mfma_f32_16x16x32_bf16 v[16:19],a[64:67],v[32:35],v[16:19]
ds_read_b128 v[56:59],v2 offset:37888
v_mfma_f32_16x16x32_bf16 v[16:19],a[176:179],v[36:39],v[16:19]
buffer_load_dwordx4 v2,s[32:35],s63 offen offset:0 sc1 lds
v_mfma_f32_16x16x32_bf16 v[20:23],a[180:183],v[36:39],v[20:23]
ds_read_b128 v[60:63],v2 offset:52224
v_mfma_f32_16x16x32_bf16 v[20:23],a[68:71],v[32:35],v[20:23]
s_waitcnt lgkmcnt(4)
v_mfma_f32_16x16x32_bf16 v[20:23],v[100:103],v[40:43],v[20:23]
ds_read_b128 v[32:35],v2 offset:10240
v_mfma_f32_16x16x32_bf16 v[20:23],v[212:215],v[44:47],v[20:23]
buffer_load_dwordx4 v2,s[32:35],s63 offen offset:1024 sc1 lds
v_mfma_f32_16x16x32_bf16 v[16:19],v[208:211],v[44:47],v[16:19]
ds_read_b128 v[36:39],v2 offset:24576
v_mfma_f32_16x16x32_bf16 v[16:19],v[96:99],v[40:43],v[16:19]
s_waitcnt lgkmcnt(4)
v_mfma_f32_16x16x32_bf16 v[16:19],a[72:75],v[48:51],v[16:19]
ds_read_b128 v[40:43],v2 offset:38912
v_mfma_f32_16x16x32_bf16 v[16:19],a[184:187],v[52:55],v[16:19]
buffer_load_dwordx4 v2,s[32:35],s63 offen offset:2048 sc1 lds
v_mfma_f32_16x16x32_bf16 v[20:23],a[188:191],v[52:55],v[20:23]
ds_read_b128 v[44:47],v2 offset:53248
v_mfma_f32_16x16x32_bf16 v[20:23],a[76:79],v[48:51],v[20:23]
s_waitcnt lgkmcnt(4)
v_mfma_f32_16x16x32_bf16 v[20:23],v[108:111],v[56:59],v[20:23]
ds_read_b128 v[48:51],v2 offset:11264
v_mfma_f32_16x16x32_bf16 v[20:23],v[220:223],v[60:63],v[20:23]
v_mfma_f32_16x16x32_bf16 v[16:19],v[216:219],v[60:63],v[16:19]
ds_read_b128 v[52:55],v2 offset:25600
v_mfma_f32_16x16x32_bf16 v[16:19],v[104:107],v[56:59],v[16:19]
s_waitcnt lgkmcnt(4)
v_mfma_f32_16x16x32_bf16 v[16:19],a[80:83],v[32:35],v[16:19]
ds_read_b128 v[56:59],v2 offset:39936
v_mfma_f32_16x16x32_bf16 v[16:19],a[192:195],v[36:39],v[16:19]
buffer_load_dwordx4 v2,s[32:35],s63 offen offset:3072 sc1 lds
s_add_u32 s63,s63,4096
v_mfma_f32_16x16x32_bf16 v[20:23],a[196:199],v[36:39],v[20:23]
ds_read_b128 v[60:63],v2 offset:54272
s_add_u32 m0,s64,69632
v_mfma_f32_16x16x32_bf16 v[20:23],a[84:87],v[32:35],v[20:23]
s_waitcnt lgkmcnt(4)
v_mfma_f32_16x16x32_bf16 v[20:23],v[116:119],v[40:43],v[20:23]
ds_read_b128 v[32:35],v2 offset:12288
v_mfma_f32_16x16x32_bf16 v[20:23],v[228:231],v[44:47],v[20:23]
buffer_load_dwordx4 v2,s[32:35],s63 offen offset:0 sc1 lds
v_mfma_f32_16x16x32_bf16 v[16:19],v[224:227],v[44:47],v[16:19]
ds_read_b128 v[36:39],v2 offset:26624
v_mfma_f32_16x16x32_bf16 v[16:19],v[112:115],v[40:43],v[16:19]
s_waitcnt lgkmcnt(4)
v_mfma_f32_16x16x32_bf16 v[16:19],a[88:91],v[48:51],v[16:19]
ds_read_b128 v[40:43],v2 offset:40960
v_mfma_f32_16x16x32_bf16 v[16:19],a[200:203],v[52:55],v[16:19]
buffer_load_dwordx4 v2,s[32:35],s63 offen offset:1024 sc1 lds
s_add_u32 s63,s63,s31
v_mfma_f32_16x16x32_bf16 v[20:23],a[204:207],v[52:55],v[20:23]
ds_read_b128 v[44:47],v2 offset:55296
v_mfma_f32_16x16x32_bf16 v[20:23],a[92:95],v[48:51],v[20:23]
s_waitcnt lgkmcnt(4)
v_mfma_f32_16x16x32_bf16 v[20:23],v[124:127],v[56:59],v[20:23]
ds_read_b128 v[48:51],v2 offset:13312
s_waitcnt vmcnt(10)
v_mfma_f32_16x16x32_bf16 v[20:23],v[236:239],v[60:63],v[20:23]
v_lshlrev_b32 v24,16,v12
v_and_b32 v25,0xffff0000,v12
v_mfma_f32_16x16x32_bf16 v[16:19],v[232:235],v[60:63],v[16:19]
ds_read_b128 v[52:55],v2 offset:27648
v_lshlrev_b32 v26,16,v13
v_mfma_f32_16x16x32_bf16 v[16:19],v[120:123],v[56:59],v[16:19]
v_and_b32 v27,0xffff0000,v13
s_waitcnt lgkmcnt(4)
v_mfma_f32_16x16x32_bf16 v[16:19],a[96:99],v[32:35],v[16:19]
s_barrier
v_mfma_f32_16x16x32_bf16 v[16:19],a[208:211],v[36:39],v[16:19]
ds_read_b128 v[56:59],v2 offset:41984
v_lshlrev_b32 v28,16,v14
v_mfma_f32_16x16x32_bf16 v[20:23],a[212:215],v[36:39],v[20:23]
ds_read_b128 v[60:63],v2 offset:56320
v_and_b32 v29,0xffff0000,v14
v_mfma_f32_16x16x32_bf16 v[20:23],a[100:103],v[32:35],v[20:23]
v_lshlrev_b32 v30,16,v15
s_waitcnt lgkmcnt(4)
v_mfma_f32_16x16x32_bf16 v[20:23],v[132:135],v[40:43],v[20:23]
ds_read_b128 v[32:35],v4 offset:0
v_and_b32 v31,0xffff0000,v15
v_mfma_f32_16x16x32_bf16 v[20:23],v[244:247],v[44:47],v[20:23]
v_mfma_f32_16x16x32_bf16 v[16:19],v[240:243],v[44:47],v[16:19]
ds_read_b128 v[36:39],v4 offset:14336
v_mfma_f32_16x16x32_bf16 v[16:19],v[128:131],v[40:43],v[16:19]
s_waitcnt lgkmcnt(4)
v_mfma_f32_16x16x32_bf16 v[16:19],a[104:107],v[48:51],v[16:19]
ds_read_b128 v[40:43],v4 offset:28672
v_mfma_f32_16x16x32_bf16 v[16:19],a[216:219],v[52:55],v[16:19]
v_mfma_f32_16x16x32_bf16 v[20:23],a[220:223],v[52:55],v[20:23]
ds_read_b128 v[44:47],v4 offset:43008
v_mfma_f32_16x16x32_bf16 v[20:23],a[108:111],v[48:51],v[20:23]
s_waitcnt lgkmcnt(4)
v_mfma_f32_16x16x32_bf16 v[20:23],v[140:143],v[56:59],v[20:23]
ds_read_b128 v[48:51],v4 offset:1024
v_mfma_f32_16x16x32_bf16 v[20:23],v[252:255],v[60:63],v[20:23]
v_mfma_f32_16x16x32_bf16 v[16:19],v[248:251],v[60:63],v[16:19]
ds_read_b128 v[52:55],v4 offset:15360
s_sub_u32 s62,s62,1
v_mfma_f32_16x16x32_bf16 v[16:19],v[136:139],v[56:59],v[16:19]
s_cbranch_scc1 .L_dlb_3
s_waitcnt lgkmcnt(4)
v_mfma_f32_16x16x32_bf16 v[24:27],a[0:3],v[32:35],v[24:27]
ds_read_b128 v[56:59],v4 offset:29696
v_mfma_f32_16x16x32_bf16 v[24:27],a[112:115],v[36:39],v[24:27]
v_mfma_f32_16x16x32_bf16 v[28:31],a[116:119],v[36:39],v[28:31]
ds_read_b128 v[60:63],v4 offset:44032
v_mfma_f32_16x16x32_bf16 v[28:31],a[4:7],v[32:35],v[28:31]
s_add_u32 s68,s68,1187840
s_waitcnt lgkmcnt(4)
v_mfma_f32_16x16x32_bf16 v[28:31],a[228:231],v[40:43],v[28:31]
ds_read_b128 v[32:35],v4 offset:2048
v_mfma_f32_16x16x32_bf16 v[28:31],v[148:151],v[44:47],v[28:31]
buffer_store_dwordx4 v[20:23],v2,s[32:35],s68 offen offset:1024 sc1
v_mfma_f32_16x16x32_bf16 v[24:27],v[144:147],v[44:47],v[24:27]
ds_read_b128 v[36:39],v4 offset:16384
v_mfma_f32_16x16x32_bf16 v[24:27],a[224:227],v[40:43],v[24:27]
s_waitcnt lgkmcnt(4)
v_mfma_f32_16x16x32_bf16 v[24:27],a[8:11],v[48:51],v[24:27]
ds_read_b128 v[40:43],v4 offset:30720
v_mfma_f32_16x16x32_bf16 v[24:27],a[120:123],v[52:55],v[24:27]
buffer_store_dwordx4 v[16:19],v2,s[32:35],s68 offen sc1
s_mov_b32 s67,s68
v_mfma_f32_16x16x32_bf16 v[28:31],a[124:127],v[52:55],v[28:31]
ds_read_b128 v[44:47],v4 offset:45056
v_mfma_f32_16x16x32_bf16 v[28:31],a[12:15],v[48:51],v[28:31]
s_waitcnt lgkmcnt(4)
v_mfma_f32_16x16x32_bf16 v[28:31],a[236:239],v[56:59],v[28:31]
ds_read_b128 v[48:51],v4 offset:3072
v_mfma_f32_16x16x32_bf16 v[28:31],v[156:159],v[60:63],v[28:31]
v_mfma_f32_16x16x32_bf16 v[24:27],v[152:155],v[60:63],v[24:27]
ds_read_b128 v[52:55],v4 offset:17408
v_mfma_f32_16x16x32_bf16 v[24:27],a[232:235],v[56:59],v[24:27]
s_waitcnt lgkmcnt(4)
v_mfma_f32_16x16x32_bf16 v[24:27],a[16:19],v[32:35],v[24:27]
s_waitcnt vmcnt(2)
s_barrier
v_mfma_f32_16x16x32_bf16 v[24:27],a[128:131],v[36:39],v[24:27]
ds_read_b128 v[56:59],v4 offset:31744
v_mfma_f32_16x16x32_bf16 v[28:31],a[132:135],v[36:39],v[28:31]
ds_read_b128 v[60:63],v4 offset:46080
v_mfma_f32_16x16x32_bf16 v[28:31],a[20:23],v[32:35],v[28:31]
s_waitcnt lgkmcnt(4)
v_mfma_f32_16x16x32_bf16 v[28:31],a[244:247],v[40:43],v[28:31]
ds_read_b128 v[32:35],v4 offset:4096
s_add_u32 m0,s64,0
v_mfma_f32_16x16x32_bf16 v[28:31],v[164:167],v[44:47],v[28:31]
buffer_load_dwordx4 v2,s[32:35],s63 offen offset:0 sc1 lds
v_mfma_f32_16x16x32_bf16 v[24:27],v[160:163],v[44:47],v[24:27]
ds_read_b128 v[36:39],v4 offset:18432
v_mfma_f32_16x16x32_bf16 v[24:27],a[240:243],v[40:43],v[24:27]
s_waitcnt lgkmcnt(4)
v_mfma_f32_16x16x32_bf16 v[24:27],a[24:27],v[48:51],v[24:27]
ds_read_b128 v[40:43],v4 offset:32768
v_mfma_f32_16x16x32_bf16 v[24:27],a[136:139],v[52:55],v[24:27]
v_mfma_f32_16x16x32_bf16 v[28:31],a[140:143],v[52:55],v[28:31]
ds_read_b128 v[44:47],v4 offset:47104
v_mfma_f32_16x16x32_bf16 v[28:31],a[28:31],v[48:51],v[28:31]
s_waitcnt lgkmcnt(4)
v_mfma_f32_16x16x32_bf16 v[28:31],a[252:255],v[56:59],v[28:31]
ds_read_b128 v[48:51],v4 offset:5120
v_mfma_f32_16x16x32_bf16 v[28:31],v[172:175],v[60:63],v[28:31]
buffer_load_dwordx4 v2,s[32:35],s63 offen offset:1024 sc1 lds
v_mfma_f32_16x16x32_bf16 v[24:27],v[168:171],v[60:63],v[24:27]
ds_read_b128 v[52:55],v4 offset:19456
v_mfma_f32_16x16x32_bf16 v[24:27],a[248:251],v[56:59],v[24:27]
s_waitcnt lgkmcnt(4)
v_mfma_f32_16x16x32_bf16 v[24:27],a[32:35],v[32:35],v[24:27]
ds_read_b128 v[56:59],v4 offset:33792
v_mfma_f32_16x16x32_bf16 v[24:27],a[144:147],v[36:39],v[24:27]
buffer_load_dwordx4 v2,s[32:35],s63 offen offset:2048 sc1 lds
v_mfma_f32_16x16x32_bf16 v[28:31],a[148:151],v[36:39],v[28:31]
ds_read_b128 v[60:63],v4 offset:48128
v_mfma_f32_16x16x32_bf16 v[28:31],a[36:39],v[32:35],v[28:31]
s_waitcnt lgkmcnt(4)
v_mfma_f32_16x16x32_bf16 v[28:31],v[68:71],v[40:43],v[28:31]
ds_read_b128 v[32:35],v4 offset:6144
v_mfma_f32_16x16x32_bf16 v[28:31],v[180:183],v[44:47],v[28:31]
buffer_load_dwordx4 v2,s[32:35],s63 offen offset:3072 sc1 lds
v_mfma_f32_16x16x32_bf16 v[24:27],v[176:179],v[44:47],v[24:27]
ds_read_b128 v[36:39],v4 offset:20480
v_mfma_f32_16x16x32_bf16 v[24:27],v[64:67],v[40:43],v[24:27]
s_waitcnt lgkmcnt(4)
v_mfma_f32_16x16x32_bf16 v[24:27],a[40:43],v[48:51],v[24:27]
ds_read_b128 v[40:43],v4 offset:34816
v_mfma_f32_16x16x32_bf16 v[24:27],a[152:155],v[52:55],v[24:27]
buffer_load_dwordx4 v[12:15],v2,s[32:35],s71 offen sc1
s_add_u32 s71,s71,s30
v_mfma_f32_16x16x32_bf16 v[28:31],a[156:159],v[52:55],v[28:31]
ds_read_b128 v[44:47],v4 offset:49152
s_add_u32 s63,s63,4096
v_mfma_f32_16x16x32_bf16 v[28:31],a[44:47],v[48:51],v[28:31]
s_add_u32 m0,s64,4096
s_waitcnt lgkmcnt(4)
v_mfma_f32_16x16x32_bf16 v[28:31],v[76:79],v[56:59],v[28:31]
ds_read_b128 v[48:51],v4 offset:7168
v_mfma_f32_16x16x32_bf16 v[28:31],v[188:191],v[60:63],v[28:31]
buffer_load_dwordx4 v2,s[32:35],s63 offen offset:0 sc1 lds
v_mfma_f32_16x16x32_bf16 v[24:27],v[184:187],v[60:63],v[24:27]
ds_read_b128 v[52:55],v4 offset:21504
v_mfma_f32_16x16x32_bf16 v[24:27],v[72:75],v[56:59],v[24:27]
s_waitcnt lgkmcnt(4)
v_mfma_f32_16x16x32_bf16 v[24:27],a[48:51],v[32:35],v[24:27]
ds_read_b128 v[56:59],v4 offset:35840
v_mfma_f32_16x16x32_bf16 v[24:27],a[160:163],v[36:39],v[24:27]
buffer_load_dwordx4 v2,s[32:35],s63 offen offset:1024 sc1 lds
v_mfma_f32_16x16x32_bf16 v[28:31],a[164:167],v[36:39],v[28:31]
ds_read_b128 v[60:63],v4 offset:50176
v_mfma_f32_16x16x32_bf16 v[28:31],a[52:55],v[32:35],v[28:31]
s_waitcnt lgkmcnt(4)
v_mfma_f32_16x16x32_bf16 v[28:31],v[84:87],v[40:43],v[28:31]
ds_read_b128 v[32:35],v4 offset:8192
v_mfma_f32_16x16x32_bf16 v[28:31],v[196:199],v[44:47],v[28:31]
buffer_load_dwordx4 v2,s[32:35],s63 offen offset:2048 sc1 lds
v_mfma_f32_16x16x32_bf16 v[24:27],v[192:195],v[44:47],v[24:27]
ds_read_b128 v[36:39],v4 offset:22528
v_mfma_f32_16x16x32_bf16 v[24:27],v[80:83],v[40:43],v[24:27]
s_waitcnt lgkmcnt(4)
v_mfma_f32_16x16x32_bf16 v[24:27],a[56:59],v[48:51],v[24:27]
ds_read_b128 v[40:43],v4 offset:36864
v_mfma_f32_16x16x32_bf16 v[24:27],a[168:171],v[52:55],v[24:27]
buffer_load_dwordx4 v2,s[32:35],s63 offen offset:3072 sc1 lds
v_mfma_f32_16x16x32_bf16 v[28:31],a[172:175],v[52:55],v[28:31]
ds_read_b128 v[44:47],v4 offset:51200
v_mfma_f32_16x16x32_bf16 v[28:31],a[60:63],v[48:51],v[28:31]
s_waitcnt lgkmcnt(4)
v_mfma_f32_16x16x32_bf16 v[28:31],v[92:95],v[56:59],v[28:31]
ds_read_b128 v[48:51],v4 offset:9216
v_mfma_f32_16x16x32_bf16 v[28:31],v[204:207],v[60:63],v[28:31]
s_add_u32 s63,s63,4096
s_add_u32 m0,s64,8192
v_mfma_f32_16x16x32_bf16 v[24:27],v[200:203],v[60:63],v[24:27]
ds_read_b128 v[52:55],v4 offset:23552
v_mfma_f32_16x16x32_bf16 v[24:27],v[88:91],v[56:59],v[24:27]
s_waitcnt lgkmcnt(4)
v_mfma_f32_16x16x32_bf16 v[24:27],a[64:67],v[32:35],v[24:27]
ds_read_b128 v[56:59],v4 offset:37888
v_mfma_f32_16x16x32_bf16 v[24:27],a[176:179],v[36:39],v[24:27]
buffer_load_dwordx4 v2,s[32:35],s63 offen offset:0 sc1 lds
v_mfma_f32_16x16x32_bf16 v[28:31],a[180:183],v[36:39],v[28:31]
ds_read_b128 v[60:63],v4 offset:52224
v_mfma_f32_16x16x32_bf16 v[28:31],a[68:71],v[32:35],v[28:31]
s_waitcnt lgkmcnt(4)
v_mfma_f32_16x16x32_bf16 v[28:31],v[100:103],v[40:43],v[28:31]
ds_read_b128 v[32:35],v4 offset:10240
v_mfma_f32_16x16x32_bf16 v[28:31],v[212:215],v[44:47],v[28:31]
)#"
R"#(
buffer_load_dwordx4 v2,s[32:35],s63 offen offset:1024 sc1 lds
v_mfma_f32_16x16x32_bf16 v[24:27],v[208:211],v[44:47],v[24:27]
ds_read_b128 v[36:39],v4 offset:24576
v_mfma_f32_16x16x32_bf16 v[24:27],v[96:99],v[40:43],v[24:27]
s_waitcnt lgkmcnt(4)
v_mfma_f32_16x16x32_bf16 v[24:27],a[72:75],v[48:51],v[24:27]
ds_read_b128 v[40:43],v4 offset:38912
v_mfma_f32_16x16x32_bf16 v[24:27],a[184:187],v[52:55],v[24:27]
buffer_load_dwordx4 v2,s[32:35],s63 offen offset:2048 sc1 lds
v_mfma_f32_16x16x32_bf16 v[28:31],a[188:191],v[52:55],v[28:31]
ds_read_b128 v[44:47],v4 offset:53248
v_mfma_f32_16x16x32_bf16 v[28:31],a[76:79],v[48:51],v[28:31]
s_waitcnt lgkmcnt(4)
v_mfma_f32_16x16x32_bf16 v[28:31],v[108:111],v[56:59],v[28:31]
ds_read_b128 v[48:51],v4 offset:11264
v_mfma_f32_16x16x32_bf16 v[28:31],v[220:223],v[60:63],v[28:31]
v_mfma_f32_16x16x32_bf16 v[24:27],v[216:219],v[60:63],v[24:27]
ds_read_b128 v[52:55],v4 offset:25600
v_mfma_f32_16x16x32_bf16 v[24:27],v[104:107],v[56:59],v[24:27]
s_waitcnt lgkmcnt(4)
v_mfma_f32_16x16x32_bf16 v[24:27],a[80:83],v[32:35],v[24:27]
ds_read_b128 v[56:59],v4 offset:39936
v_mfma_f32_16x16x32_bf16 v[24:27],a[192:195],v[36:39],v[24:27]
buffer_load_dwordx4 v2,s[32:35],s63 offen offset:3072 sc1 lds
s_add_u32 s63,s63,4096
v_mfma_f32_16x16x32_bf16 v[28:31],a[196:199],v[36:39],v[28:31]
ds_read_b128 v[60:63],v4 offset:54272
s_add_u32 m0,s64,12288
v_mfma_f32_16x16x32_bf16 v[28:31],a[84:87],v[32:35],v[28:31]
s_waitcnt lgkmcnt(4)
v_mfma_f32_16x16x32_bf16 v[28:31],v[116:119],v[40:43],v[28:31]
ds_read_b128 v[32:35],v4 offset:12288
v_mfma_f32_16x16x32_bf16 v[28:31],v[228:231],v[44:47],v[28:31]
buffer_load_dwordx4 v2,s[32:35],s63 offen offset:0 sc1 lds
v_mfma_f32_16x16x32_bf16 v[24:27],v[224:227],v[44:47],v[24:27]
ds_read_b128 v[36:39],v4 offset:26624
v_mfma_f32_16x16x32_bf16 v[24:27],v[112:115],v[40:43],v[24:27]
s_waitcnt lgkmcnt(4)
v_mfma_f32_16x16x32_bf16 v[24:27],a[88:91],v[48:51],v[24:27]
ds_read_b128 v[40:43],v4 offset:40960
v_mfma_f32_16x16x32_bf16 v[24:27],a[200:203],v[52:55],v[24:27]
buffer_load_dwordx4 v2,s[32:35],s63 offen offset:1024 sc1 lds
s_add_u32 s63,s63,s31
v_mfma_f32_16x16x32_bf16 v[28:31],a[204:207],v[52:55],v[28:31]
ds_read_b128 v[44:47],v4 offset:55296
v_mfma_f32_16x16x32_bf16 v[28:31],a[92:95],v[48:51],v[28:31]
s_waitcnt lgkmcnt(4)
v_mfma_f32_16x16x32_bf16 v[28:31],v[124:127],v[56:59],v[28:31]
ds_read_b128 v[48:51],v4 offset:13312
s_waitcnt vmcnt(10)
v_mfma_f32_16x16x32_bf16 v[28:31],v[236:239],v[60:63],v[28:31]
v_lshlrev_b32 v16,16,v12
v_and_b32 v17,0xffff0000,v12
v_mfma_f32_16x16x32_bf16 v[24:27],v[232:235],v[60:63],v[24:27]
ds_read_b128 v[52:55],v4 offset:27648
v_lshlrev_b32 v18,16,v13
v_mfma_f32_16x16x32_bf16 v[24:27],v[120:123],v[56:59],v[24:27]
v_and_b32 v19,0xffff0000,v13
s_waitcnt lgkmcnt(4)
v_mfma_f32_16x16x32_bf16 v[24:27],a[96:99],v[32:35],v[24:27]
s_barrier
v_mfma_f32_16x16x32_bf16 v[24:27],a[208:211],v[36:39],v[24:27]
ds_read_b128 v[56:59],v4 offset:41984
v_lshlrev_b32 v20,16,v14
v_mfma_f32_16x16x32_bf16 v[28:31],a[212:215],v[36:39],v[28:31]
ds_read_b128 v[60:63],v4 offset:56320
v_and_b32 v21,0xffff0000,v14
v_mfma_f32_16x16x32_bf16 v[28:31],a[100:103],v[32:35],v[28:31]
v_lshlrev_b32 v22,16,v15
s_waitcnt lgkmcnt(4)
v_mfma_f32_16x16x32_bf16 v[28:31],v[132:135],v[40:43],v[28:31]
ds_read_b128 v[32:35],v2 offset:0
v_and_b32 v23,0xffff0000,v15
v_mfma_f32_16x16x32_bf16 v[28:31],v[244:247],v[44:47],v[28:31]
v_mfma_f32_16x16x32_bf16 v[24:27],v[240:243],v[44:47],v[24:27]
ds_read_b128 v[36:39],v2 offset:14336
v_mfma_f32_16x16x32_bf16 v[24:27],v[128:131],v[40:43],v[24:27]
s_waitcnt lgkmcnt(4)
v_mfma_f32_16x16x32_bf16 v[24:27],a[104:107],v[48:51],v[24:27]
ds_read_b128 v[40:43],v2 offset:28672
v_mfma_f32_16x16x32_bf16 v[24:27],a[216:219],v[52:55],v[24:27]
v_mfma_f32_16x16x32_bf16 v[28:31],a[220:223],v[52:55],v[28:31]
ds_read_b128 v[44:47],v2 offset:43008
v_mfma_f32_16x16x32_bf16 v[28:31],a[108:111],v[48:51],v[28:31]
s_waitcnt lgkmcnt(4)
v_mfma_f32_16x16x32_bf16 v[28:31],v[140:143],v[56:59],v[28:31]
ds_read_b128 v[48:51],v2 offset:1024
v_mfma_f32_16x16x32_bf16 v[28:31],v[252:255],v[60:63],v[28:31]
v_mfma_f32_16x16x32_bf16 v[24:27],v[248:251],v[60:63],v[24:27]
ds_read_b128 v[52:55],v2 offset:15360
v_mfma_f32_16x16x32_bf16 v[24:27],v[136:139],v[56:59],v[24:27]
s_branch .L_dll_3
.L_dlb_3:
s_mov_b32 s62,s58
s_mov_b32 s70,s59
s_add_u32 s74,s74,8192
v_lshrrev_b32 v5,8,v2
v_lshl_add_u32 v5,v5,4,s74
v_and_b32 v3,128,v2
v_lshl_add_u32 v5,v3,2,v5
v_and_b32 v3,16,v2
v_lshl_add_u32 v5,v3,4,v5
v_and_b32 v3,96,v2
v_lshl_add_u32 v5,v3,1,v5
v_lshrrev_b32 v3,2,v2
v_add_u32 v3,s74,v3
s_add_u32 s66,s66,2375680
s_add_u32 s63,s63,2375680
s_add_u32 s71,s71,2375680
s_mov_b32 s68,98304
s_mov_b32 s34,79446016
.p2align 6
.L_dml_3:
s_waitcnt lgkmcnt(4)
v_mfma_f32_16x16x32_bf16 v[24:27],a[0:3],v[32:35],v[24:27]
ds_read_b128 v[56:59],v4 offset:29696
v_mfma_f32_16x16x32_bf16 v[24:27],a[112:115],v[36:39],v[24:27]
v_mfma_f32_16x16x32_bf16 v[28:31],a[116:119],v[36:39],v[28:31]
ds_read_b128 v[60:63],v4 offset:44032
v_mfma_f32_16x16x32_bf16 v[28:31],a[4:7],v[32:35],v[28:31]
v_cvt_pk_bf16_f32 v15,v22,v23
s_waitcnt lgkmcnt(4)
v_mfma_f32_16x16x32_bf16 v[28:31],a[228:231],v[40:43],v[28:31]
ds_read_b128 v[32:35],v4 offset:2048
v_cvt_pk_bf16_f32 v14,v20,v21
v_mfma_f32_16x16x32_bf16 v[28:31],v[148:151],v[44:47],v[28:31]
v_cvt_pk_bf16_f32 v13,v18,v19
v_cvt_pk_bf16_f32 v12,v16,v17
v_mfma_f32_16x16x32_bf16 v[24:27],v[144:147],v[44:47],v[24:27]
ds_read_b128 v[36:39],v4 offset:16384
ds_write_b128 v5,v[12:15]
v_mfma_f32_16x16x32_bf16 v[24:27],a[224:227],v[40:43],v[24:27]
ds_read2st64_b32 v[12:13],v3 offset0:0 offset1:1
s_waitcnt lgkmcnt(6)
v_mfma_f32_16x16x32_bf16 v[24:27],a[8:11],v[48:51],v[24:27]
ds_read_b128 v[40:43],v4 offset:30720
ds_read2st64_b32 v[14:15],v3 offset0:2 offset1:3
v_mfma_f32_16x16x32_bf16 v[24:27],a[120:123],v[52:55],v[24:27]
v_mfma_f32_16x16x32_bf16 v[28:31],a[124:127],v[52:55],v[28:31]
ds_read_b128 v[44:47],v4 offset:45056
v_mfma_f32_16x16x32_bf16 v[28:31],a[12:15],v[48:51],v[28:31]
s_waitcnt lgkmcnt(7)
v_mfma_f32_16x16x32_bf16 v[28:31],a[236:239],v[56:59],v[28:31]
ds_read_b128 v[48:51],v4 offset:3072
v_mfma_f32_16x16x32_bf16 v[28:31],v[156:159],v[60:63],v[28:31]
s_waitcnt lgkmcnt(2)
buffer_atomic_pk_add_bf16 v12,v1,s[44:47],s65 offen offset:0 sc1
v_mfma_f32_16x16x32_bf16 v[24:27],v[152:155],v[60:63],v[24:27]
ds_read_b128 v[52:55],v4 offset:17408
s_add_u32 s70,s70,s75
v_mfma_f32_16x16x32_bf16 v[24:27],a[232:235],v[56:59],v[24:27]
buffer_atomic_pk_add_bf16 v13,v1,s[44:47],s65 offen offset:2048 sc1
v_mfma_f32_16x16x32_bf16 v[24:27],a[16:19],v[32:35],v[24:27]
s_waitcnt vmcnt(2)
s_barrier
v_mfma_f32_16x16x32_bf16 v[24:27],a[128:131],v[36:39],v[24:27]
ds_read_b128 v[56:59],v4 offset:31744
s_add_u32 s65,s65,16384
v_mfma_f32_16x16x32_bf16 v[28:31],a[132:135],v[36:39],v[28:31]
ds_read_b128 v[60:63],v4 offset:46080
v_mfma_f32_16x16x32_bf16 v[28:31],a[20:23],v[32:35],v[28:31]
buffer_atomic_pk_add_bf16 v14,v1,s[44:47],s65 offen offset:0 sc1
s_waitcnt lgkmcnt(4)
v_mfma_f32_16x16x32_bf16 v[28:31],a[244:247],v[40:43],v[28:31]
ds_read_b128 v[32:35],v4 offset:4096
s_add_u32 m0,s64,0
v_mfma_f32_16x16x32_bf16 v[28:31],v[164:167],v[44:47],v[28:31]
buffer_atomic_pk_add_bf16 v15,v1,s[44:47],s65 offen offset:2048 sc1
s_and_b32 s65,s70,0x3f8000
v_mfma_f32_16x16x32_bf16 v[24:27],v[160:163],v[44:47],v[24:27]
ds_read_b128 v[36:39],v4 offset:18432
v_mfma_f32_16x16x32_bf16 v[24:27],a[240:243],v[40:43],v[24:27]
s_waitcnt lgkmcnt(4)
v_mfma_f32_16x16x32_bf16 v[24:27],a[24:27],v[48:51],v[24:27]
ds_read_b128 v[40:43],v4 offset:32768
v_mfma_f32_16x16x32_bf16 v[24:27],a[136:139],v[52:55],v[24:27]
buffer_load_dwordx4 v2,s[32:35],s63 offen offset:0 sc1 lds
v_mfma_f32_16x16x32_bf16 v[28:31],a[140:143],v[52:55],v[28:31]
ds_read_b128 v[44:47],v4 offset:47104
v_mfma_f32_16x16x32_bf16 v[28:31],a[28:31],v[48:51],v[28:31]
s_waitcnt lgkmcnt(4)
v_mfma_f32_16x16x32_bf16 v[28:31],a[252:255],v[56:59],v[28:31]
ds_read_b128 v[48:51],v4 offset:5120
v_mfma_f32_16x16x32_bf16 v[28:31],v[172:175],v[60:63],v[28:31]
buffer_load_dwordx4 v2,s[32:35],s63 offen offset:1024 sc1 lds
v_mfma_f32_16x16x32_bf16 v[24:27],v[168:171],v[60:63],v[24:27]
ds_read_b128 v[52:55],v4 offset:19456
v_mfma_f32_16x16x32_bf16 v[24:27],a[248:251],v[56:59],v[24:27]
s_waitcnt lgkmcnt(4)
v_mfma_f32_16x16x32_bf16 v[24:27],a[32:35],v[32:35],v[24:27]
ds_read_b128 v[56:59],v4 offset:33792
v_mfma_f32_16x16x32_bf16 v[24:27],a[144:147],v[36:39],v[24:27]
buffer_load_dwordx4 v2,s[32:35],s63 offen offset:2048 sc1 lds
v_mfma_f32_16x16x32_bf16 v[28:31],a[148:151],v[36:39],v[28:31]
ds_read_b128 v[60:63],v4 offset:48128
v_mfma_f32_16x16x32_bf16 v[28:31],a[36:39],v[32:35],v[28:31]
s_waitcnt lgkmcnt(4)
v_mfma_f32_16x16x32_bf16 v[28:31],v[68:71],v[40:43],v[28:31]
ds_read_b128 v[32:35],v4 offset:6144
v_mfma_f32_16x16x32_bf16 v[28:31],v[180:183],v[44:47],v[28:31]
buffer_load_dwordx4 v2,s[32:35],s63 offen offset:3072 sc1 lds
v_mfma_f32_16x16x32_bf16 v[24:27],v[176:179],v[44:47],v[24:27]
ds_read_b128 v[36:39],v4 offset:20480
v_mfma_f32_16x16x32_bf16 v[24:27],v[64:67],v[40:43],v[24:27]
s_waitcnt lgkmcnt(4)
v_mfma_f32_16x16x32_bf16 v[24:27],a[40:43],v[48:51],v[24:27]
ds_read_b128 v[40:43],v4 offset:34816
v_mfma_f32_16x16x32_bf16 v[24:27],a[152:155],v[52:55],v[24:27]
buffer_load_dwordx4 v[12:15],v2,s[32:35],s71 offen sc1
s_add_u32 s71,s71,s30
v_mfma_f32_16x16x32_bf16 v[28:31],a[156:159],v[52:55],v[28:31]
ds_read_b128 v[44:47],v4 offset:49152
s_add_u32 s63,s63,4096
v_mfma_f32_16x16x32_bf16 v[28:31],a[44:47],v[48:51],v[28:31]
s_add_u32 m0,s64,4096
s_waitcnt lgkmcnt(4)
v_mfma_f32_16x16x32_bf16 v[28:31],v[76:79],v[56:59],v[28:31]
ds_read_b128 v[48:51],v4 offset:7168
v_mfma_f32_16x16x32_bf16 v[28:31],v[188:191],v[60:63],v[28:31]
buffer_load_dwordx4 v2,s[32:35],s63 offen offset:0 sc1 lds
v_mfma_f32_16x16x32_bf16 v[24:27],v[184:187],v[60:63],v[24:27]
ds_read_b128 v[52:55],v4 offset:21504
v_mfma_f32_16x16x32_bf16 v[24:27],v[72:75],v[56:59],v[24:27]
s_waitcnt lgkmcnt(4)
v_mfma_f32_16x16x32_bf16 v[24:27],a[48:51],v[32:35],v[24:27]
ds_read_b128 v[56:59],v4 offset:35840
v_mfma_f32_16x16x32_bf16 v[24:27],a[160:163],v[36:39],v[24:27]
buffer_load_dwordx4 v2,s[32:35],s63 offen offset:1024 sc1 lds
v_mfma_f32_16x16x32_bf16 v[28:31],a[164:167],v[36:39],v[28:31]
ds_read_b128 v[60:63],v4 offset:50176
v_mfma_f32_16x16x32_bf16 v[28:31],a[52:55],v[32:35],v[28:31]
s_waitcnt lgkmcnt(4)
v_mfma_f32_16x16x32_bf16 v[28:31],v[84:87],v[40:43],v[28:31]
ds_read_b128 v[32:35],v4 offset:8192
v_mfma_f32_16x16x32_bf16 v[28:31],v[196:199],v[44:47],v[28:31]
buffer_load_dwordx4 v2,s[32:35],s63 offen offset:2048 sc1 lds
v_mfma_f32_16x16x32_bf16 v[24:27],v[192:195],v[44:47],v[24:27]
ds_read_b128 v[36:39],v4 offset:22528
v_mfma_f32_16x16x32_bf16 v[24:27],v[80:83],v[40:43],v[24:27]
s_waitcnt lgkmcnt(4)
v_mfma_f32_16x16x32_bf16 v[24:27],a[56:59],v[48:51],v[24:27]
ds_read_b128 v[40:43],v4 offset:36864
v_mfma_f32_16x16x32_bf16 v[24:27],a[168:171],v[52:55],v[24:27]
buffer_load_dwordx4 v2,s[32:35],s63 offen offset:3072 sc1 lds
v_mfma_f32_16x16x32_bf16 v[28:31],a[172:175],v[52:55],v[28:31]
ds_read_b128 v[44:47],v4 offset:51200
v_mfma_f32_16x16x32_bf16 v[28:31],a[60:63],v[48:51],v[28:31]
s_waitcnt lgkmcnt(4)
v_mfma_f32_16x16x32_bf16 v[28:31],v[92:95],v[56:59],v[28:31]
ds_read_b128 v[48:51],v4 offset:9216
v_mfma_f32_16x16x32_bf16 v[28:31],v[204:207],v[60:63],v[28:31]
s_add_u32 s63,s63,4096
s_add_u32 m0,s64,8192
v_mfma_f32_16x16x32_bf16 v[24:27],v[200:203],v[60:63],v[24:27]
ds_read_b128 v[52:55],v4 offset:23552
v_mfma_f32_16x16x32_bf16 v[24:27],v[88:91],v[56:59],v[24:27]
s_waitcnt lgkmcnt(4)
v_mfma_f32_16x16x32_bf16 v[24:27],a[64:67],v[32:35],v[24:27]
ds_read_b128 v[56:59],v4 offset:37888
v_mfma_f32_16x16x32_bf16 v[24:27],a[176:179],v[36:39],v[24:27]
buffer_load_dwordx4 v2,s[32:35],s63 offen offset:0 sc1 lds
v_mfma_f32_16x16x32_bf16 v[28:31],a[180:183],v[36:39],v[28:31]
ds_read_b128 v[60:63],v4 offset:52224
v_mfma_f32_16x16x32_bf16 v[28:31],a[68:71],v[32:35],v[28:31]
s_waitcnt lgkmcnt(4)
v_mfma_f32_16x16x32_bf16 v[28:31],v[100:103],v[40:43],v[28:31]
ds_read_b128 v[32:35],v4 offset:10240
v_mfma_f32_16x16x32_bf16 v[28:31],v[212:215],v[44:47],v[28:31]
buffer_load_dwordx4 v2,s[32:35],s63 offen offset:1024 sc1 lds
v_mfma_f32_16x16x32_bf16 v[24:27],v[208:211],v[44:47],v[24:27]
ds_read_b128 v[36:39],v4 offset:24576
v_mfma_f32_16x16x32_bf16 v[24:27],v[96:99],v[40:43],v[24:27]
s_waitcnt lgkmcnt(4)
v_mfma_f32_16x16x32_bf16 v[24:27],a[72:75],v[48:51],v[24:27]
ds_read_b128 v[40:43],v4 offset:38912
v_mfma_f32_16x16x32_bf16 v[24:27],a[184:187],v[52:55],v[24:27]
buffer_load_dwordx4 v2,s[32:35],s63 offen offset:2048 sc1 lds
v_mfma_f32_16x16x32_bf16 v[28:31],a[188:191],v[52:55],v[28:31]
ds_read_b128 v[44:47],v4 offset:53248
v_mfma_f32_16x16x32_bf16 v[28:31],a[76:79],v[48:51],v[28:31]
s_waitcnt lgkmcnt(4)
v_mfma_f32_16x16x32_bf16 v[28:31],v[108:111],v[56:59],v[28:31]
ds_read_b128 v[48:51],v4 offset:11264
v_mfma_f32_16x16x32_bf16 v[28:31],v[220:223],v[60:63],v[28:31]
v_mfma_f32_16x16x32_bf16 v[24:27],v[216:219],v[60:63],v[24:27]
ds_read_b128 v[52:55],v4 offset:25600
v_mfma_f32_16x16x32_bf16 v[24:27],v[104:107],v[56:59],v[24:27]
s_waitcnt lgkmcnt(4)
v_mfma_f32_16x16x32_bf16 v[24:27],a[80:83],v[32:35],v[24:27]
ds_read_b128 v[56:59],v4 offset:39936
v_mfma_f32_16x16x32_bf16 v[24:27],a[192:195],v[36:39],v[24:27]
buffer_load_dwordx4 v2,s[32:35],s63 offen offset:3072 sc1 lds
s_add_u32 s63,s63,4096
v_mfma_f32_16x16x32_bf16 v[28:31],a[196:199],v[36:39],v[28:31]
ds_read_b128 v[60:63],v4 offset:54272
s_add_u32 m0,s64,12288
v_mfma_f32_16x16x32_bf16 v[28:31],a[84:87],v[32:35],v[28:31]
s_waitcnt lgkmcnt(4)
v_mfma_f32_16x16x32_bf16 v[28:31],v[116:119],v[40:43],v[28:31]
ds_read_b128 v[32:35],v4 offset:12288
v_mfma_f32_16x16x32_bf16 v[28:31],v[228:231],v[44:47],v[28:31]
buffer_load_dwordx4 v2,s[32:35],s63 offen offset:0 sc1 lds
v_mfma_f32_16x16x32_bf16 v[24:27],v[224:227],v[44:47],v[24:27]
ds_read_b128 v[36:39],v4 offset:26624
v_mfma_f32_16x16x32_bf16 v[24:27],v[112:115],v[40:43],v[24:27]
s_waitcnt lgkmcnt(4)
v_mfma_f32_16x16x32_bf16 v[24:27],a[88:91],v[48:51],v[24:27]
ds_read_b128 v[40:43],v4 offset:40960
v_mfma_f32_16x16x32_bf16 v[24:27],a[200:203],v[52:55],v[24:27]
buffer_load_dwordx4 v2,s[32:35],s63 offen offset:1024 sc1 lds
s_add_u32 s63,s63,s31
v_mfma_f32_16x16x32_bf16 v[28:31],a[204:207],v[52:55],v[28:31]
ds_read_b128 v[44:47],v4 offset:55296
v_mfma_f32_16x16x32_bf16 v[28:31],a[92:95],v[48:51],v[28:31]
s_waitcnt lgkmcnt(4)
v_mfma_f32_16x16x32_bf16 v[28:31],v[124:127],v[56:59],v[28:31]
ds_read_b128 v[48:51],v4 offset:13312
s_waitcnt vmcnt(10)
v_mfma_f32_16x16x32_bf16 v[28:31],v[236:239],v[60:63],v[28:31]
v_lshlrev_b32 v16,16,v12
v_and_b32 v17,0xffff0000,v12
v_mfma_f32_16x16x32_bf16 v[24:27],v[232:235],v[60:63],v[24:27]
ds_read_b128 v[52:55],v4 offset:27648
v_lshlrev_b32 v18,16,v13
v_mfma_f32_16x16x32_bf16 v[24:27],v[120:123],v[56:59],v[24:27]
v_and_b32 v19,0xffff0000,v13
s_waitcnt lgkmcnt(4)
v_mfma_f32_16x16x32_bf16 v[24:27],a[96:99],v[32:35],v[24:27]
s_barrier
v_mfma_f32_16x16x32_bf16 v[24:27],a[208:211],v[36:39],v[24:27]
ds_read_b128 v[56:59],v4 offset:41984
v_lshlrev_b32 v20,16,v14
v_mfma_f32_16x16x32_bf16 v[28:31],a[212:215],v[36:39],v[28:31]
ds_read_b128 v[60:63],v4 offset:56320
v_and_b32 v21,0xffff0000,v14
v_mfma_f32_16x16x32_bf16 v[28:31],a[100:103],v[32:35],v[28:31]
v_lshlrev_b32 v22,16,v15
s_waitcnt lgkmcnt(4)
v_mfma_f32_16x16x32_bf16 v[28:31],v[132:135],v[40:43],v[28:31]
ds_read_b128 v[32:35],v2 offset:0
v_and_b32 v23,0xffff0000,v15
v_mfma_f32_16x16x32_bf16 v[28:31],v[244:247],v[44:47],v[28:31]
v_mfma_f32_16x16x32_bf16 v[24:27],v[240:243],v[44:47],v[24:27]
ds_read_b128 v[36:39],v2 offset:14336
v_mfma_f32_16x16x32_bf16 v[24:27],v[128:131],v[40:43],v[24:27]
s_waitcnt lgkmcnt(4)
v_mfma_f32_16x16x32_bf16 v[24:27],a[104:107],v[48:51],v[24:27]
ds_read_b128 v[40:43],v2 offset:28672
v_mfma_f32_16x16x32_bf16 v[24:27],a[216:219],v[52:55],v[24:27]
v_mfma_f32_16x16x32_bf16 v[28:31],a[220:223],v[52:55],v[28:31]
ds_read_b128 v[44:47],v2 offset:43008
v_mfma_f32_16x16x32_bf16 v[28:31],a[108:111],v[48:51],v[28:31]
s_waitcnt lgkmcnt(4)
v_mfma_f32_16x16x32_bf16 v[28:31],v[140:143],v[56:59],v[28:31]
ds_read_b128 v[48:51],v2 offset:1024
v_mfma_f32_16x16x32_bf16 v[28:31],v[252:255],v[60:63],v[28:31]
v_mfma_f32_16x16x32_bf16 v[24:27],v[248:251],v[60:63],v[24:27]
ds_read_b128 v[52:55],v2 offset:15360
s_sub_u32 s62,s62,1
v_mfma_f32_16x16x32_bf16 v[24:27],v[136:139],v[56:59],v[24:27]
s_cbranch_scc1 .L_dmt_3
s_waitcnt lgkmcnt(4)
v_mfma_f32_16x16x32_bf16 v[16:19],a[0:3],v[32:35],v[16:19]
ds_read_b128 v[56:59],v2 offset:29696
v_mfma_f32_16x16x32_bf16 v[16:19],a[112:115],v[36:39],v[16:19]
v_mfma_f32_16x16x32_bf16 v[20:23],a[116:119],v[36:39],v[20:23]
ds_read_b128 v[60:63],v2 offset:44032
v_mfma_f32_16x16x32_bf16 v[20:23],a[4:7],v[32:35],v[20:23]
v_cvt_pk_bf16_f32 v15,v30,v31
s_waitcnt lgkmcnt(4)
v_mfma_f32_16x16x32_bf16 v[20:23],a[228:231],v[40:43],v[20:23]
ds_read_b128 v[32:35],v2 offset:2048
v_cvt_pk_bf16_f32 v14,v28,v29
v_mfma_f32_16x16x32_bf16 v[20:23],v[148:151],v[44:47],v[20:23]
v_cvt_pk_bf16_f32 v13,v26,v27
v_cvt_pk_bf16_f32 v12,v24,v25
v_mfma_f32_16x16x32_bf16 v[16:19],v[144:147],v[44:47],v[16:19]
ds_read_b128 v[36:39],v2 offset:16384
ds_write_b128 v5,v[12:15]
v_mfma_f32_16x16x32_bf16 v[16:19],a[224:227],v[40:43],v[16:19]
ds_read2st64_b32 v[12:13],v3 offset0:0 offset1:1
s_waitcnt lgkmcnt(6)
v_mfma_f32_16x16x32_bf16 v[16:19],a[8:11],v[48:51],v[16:19]
ds_read_b128 v[40:43],v2 offset:30720
ds_read2st64_b32 v[14:15],v3 offset0:2 offset1:3
v_mfma_f32_16x16x32_bf16 v[16:19],a[120:123],v[52:55],v[16:19]
v_mfma_f32_16x16x32_bf16 v[20:23],a[124:127],v[52:55],v[20:23]
ds_read_b128 v[44:47],v2 offset:45056
v_mfma_f32_16x16x32_bf16 v[20:23],a[12:15],v[48:51],v[20:23]
s_waitcnt lgkmcnt(7)
v_mfma_f32_16x16x32_bf16 v[20:23],a[236:239],v[56:59],v[20:23]
ds_read_b128 v[48:51],v2 offset:3072
v_mfma_f32_16x16x32_bf16 v[20:23],v[156:159],v[60:63],v[20:23]
s_waitcnt lgkmcnt(2)
buffer_atomic_pk_add_bf16 v12,v1,s[44:47],s65 offen offset:0 sc1
v_mfma_f32_16x16x32_bf16 v[16:19],v[152:155],v[60:63],v[16:19]
ds_read_b128 v[52:55],v2 offset:17408
s_add_u32 s70,s70,s68
v_mfma_f32_16x16x32_bf16 v[16:19],a[232:235],v[56:59],v[16:19]
buffer_atomic_pk_add_bf16 v13,v1,s[44:47],s65 offen offset:2048 sc1
v_mfma_f32_16x16x32_bf16 v[16:19],a[16:19],v[32:35],v[16:19]
s_waitcnt vmcnt(2)
s_barrier
v_mfma_f32_16x16x32_bf16 v[16:19],a[128:131],v[36:39],v[16:19]
ds_read_b128 v[56:59],v2 offset:31744
s_add_u32 s65,s65,16384
v_mfma_f32_16x16x32_bf16 v[20:23],a[132:135],v[36:39],v[20:23]
ds_read_b128 v[60:63],v2 offset:46080
v_mfma_f32_16x16x32_bf16 v[20:23],a[20:23],v[32:35],v[20:23]
buffer_atomic_pk_add_bf16 v14,v1,s[44:47],s65 offen offset:0 sc1
s_waitcnt lgkmcnt(4)
v_mfma_f32_16x16x32_bf16 v[20:23],a[244:247],v[40:43],v[20:23]
ds_read_b128 v[32:35],v2 offset:4096
s_add_u32 m0,s64,57344
v_mfma_f32_16x16x32_bf16 v[20:23],v[164:167],v[44:47],v[20:23]
buffer_load_dwordx4 v2,s[32:35],s63 offen offset:0 sc1 lds
s_mov_b32 s68,s75
v_mfma_f32_16x16x32_bf16 v[16:19],v[160:163],v[44:47],v[16:19]
ds_read_b128 v[36:39],v2 offset:18432
v_mfma_f32_16x16x32_bf16 v[16:19],a[240:243],v[40:43],v[16:19]
buffer_atomic_pk_add_bf16 v15,v1,s[44:47],s65 offen offset:2048 sc1
s_waitcnt lgkmcnt(4)
v_mfma_f32_16x16x32_bf16 v[16:19],a[24:27],v[48:51],v[16:19]
ds_read_b128 v[40:43],v2 offset:32768
s_and_b32 s65,s70,0x3f8000
v_mfma_f32_16x16x32_bf16 v[16:19],a[136:139],v[52:55],v[16:19]
v_mfma_f32_16x16x32_bf16 v[20:23],a[140:143],v[52:55],v[20:23]
ds_read_b128 v[44:47],v2 offset:47104
v_mfma_f32_16x16x32_bf16 v[20:23],a[28:31],v[48:51],v[20:23]
s_waitcnt lgkmcnt(4)
v_mfma_f32_16x16x32_bf16 v[20:23],a[252:255],v[56:59],v[20:23]
ds_read_b128 v[48:51],v2 offset:5120
v_mfma_f32_16x16x32_bf16 v[20:23],v[172:175],v[60:63],v[20:23]
buffer_load_dwordx4 v2,s[32:35],s63 offen offset:1024 sc1 lds
v_mfma_f32_16x16x32_bf16 v[16:19],v[168:171],v[60:63],v[16:19]
ds_read_b128 v[52:55],v2 offset:19456
v_mfma_f32_16x16x32_bf16 v[16:19],a[248:251],v[56:59],v[16:19]
s_waitcnt lgkmcnt(4)
v_mfma_f32_16x16x32_bf16 v[16:19],a[32:35],v[32:35],v[16:19]
ds_read_b128 v[56:59],v2 offset:33792
v_mfma_f32_16x16x32_bf16 v[16:19],a[144:147],v[36:39],v[16:19]
buffer_load_dwordx4 v2,s[32:35],s63 offen offset:2048 sc1 lds
v_mfma_f32_16x16x32_bf16 v[20:23],a[148:151],v[36:39],v[20:23]
ds_read_b128 v[60:63],v2 offset:48128
v_mfma_f32_16x16x32_bf16 v[20:23],a[36:39],v[32:35],v[20:23]
s_waitcnt lgkmcnt(4)
v_mfma_f32_16x16x32_bf16 v[20:23],v[68:71],v[40:43],v[20:23]
ds_read_b128 v[32:35],v2 offset:6144
v_mfma_f32_16x16x32_bf16 v[20:23],v[180:183],v[44:47],v[20:23]
buffer_load_dwordx4 v2,s[32:35],s63 offen offset:3072 sc1 lds
v_mfma_f32_16x16x32_bf16 v[16:19],v[176:179],v[44:47],v[16:19]
ds_read_b128 v[36:39],v2 offset:20480
v_mfma_f32_16x16x32_bf16 v[16:19],v[64:67],v[40:43],v[16:19]
s_waitcnt lgkmcnt(4)
v_mfma_f32_16x16x32_bf16 v[16:19],a[40:43],v[48:51],v[16:19]
ds_read_b128 v[40:43],v2 offset:34816
v_mfma_f32_16x16x32_bf16 v[16:19],a[152:155],v[52:55],v[16:19]
buffer_load_dwordx4 v[12:15],v2,s[32:35],s71 offen sc1
s_add_u32 s71,s71,s30
v_mfma_f32_16x16x32_bf16 v[20:23],a[156:159],v[52:55],v[20:23]
ds_read_b128 v[44:47],v2 offset:49152
s_add_u32 s63,s63,4096
v_mfma_f32_16x16x32_bf16 v[20:23],a[44:47],v[48:51],v[20:23]
s_add_u32 m0,s64,61440
s_waitcnt lgkmcnt(4)
v_mfma_f32_16x16x32_bf16 v[20:23],v[76:79],v[56:59],v[20:23]
ds_read_b128 v[48:51],v2 offset:7168
v_mfma_f32_16x16x32_bf16 v[20:23],v[188:191],v[60:63],v[20:23]
buffer_load_dwordx4 v2,s[32:35],s63 offen offset:0 sc1 lds
v_mfma_f32_16x16x32_bf16 v[16:19],v[184:187],v[60:63],v[16:19]
ds_read_b128 v[52:55],v2 offset:21504
v_mfma_f32_16x16x32_bf16 v[16:19],v[72:75],v[56:59],v[16:19]
s_waitcnt lgkmcnt(4)
v_mfma_f32_16x16x32_bf16 v[16:19],a[48:51],v[32:35],v[16:19]
ds_read_b128 v[56:59],v2 offset:35840
v_mfma_f32_16x16x32_bf16 v[16:19],a[160:163],v[36:39],v[16:19]
buffer_load_dwordx4 v2,s[32:35],s63 offen offset:1024 sc1 lds
v_mfma_f32_16x16x32_bf16 v[20:23],a[164:167],v[36:39],v[20:23]
ds_read_b128 v[60:63],v2 offset:50176
v_mfma_f32_16x16x32_bf16 v[20:23],a[52:55],v[32:35],v[20:23]
s_waitcnt lgkmcnt(4)
v_mfma_f32_16x16x32_bf16 v[20:23],v[84:87],v[40:43],v[20:23]
ds_read_b128 v[32:35],v2 offset:8192
v_mfma_f32_16x16x32_bf16 v[20:23],v[196:199],v[44:47],v[20:23]
buffer_load_dwordx4 v2,s[32:35],s63 offen offset:2048 sc1 lds
v_mfma_f32_16x16x32_bf16 v[16:19],v[192:195],v[44:47],v[16:19]
ds_read_b128 v[36:39],v2 offset:22528
v_mfma_f32_16x16x32_bf16 v[16:19],v[80:83],v[40:43],v[16:19]
s_waitcnt lgkmcnt(4)
v_mfma_f32_16x16x32_bf16 v[16:19],a[56:59],v[48:51],v[16:19]
ds_read_b128 v[40:43],v2 offset:36864
v_mfma_f32_16x16x32_bf16 v[16:19],a[168:171],v[52:55],v[16:19]
buffer_load_dwordx4 v2,s[32:35],s63 offen offset:3072 sc1 lds
v_mfma_f32_16x16x32_bf16 v[20:23],a[172:175],v[52:55],v[20:23]
ds_read_b128 v[44:47],v2 offset:51200
v_mfma_f32_16x16x32_bf16 v[20:23],a[60:63],v[48:51],v[20:23]
s_waitcnt lgkmcnt(4)
v_mfma_f32_16x16x32_bf16 v[20:23],v[92:95],v[56:59],v[20:23]
ds_read_b128 v[48:51],v2 offset:9216
v_mfma_f32_16x16x32_bf16 v[20:23],v[204:207],v[60:63],v[20:23]
)#"
R"#(
s_add_u32 s63,s63,4096
s_add_u32 m0,s64,65536
v_mfma_f32_16x16x32_bf16 v[16:19],v[200:203],v[60:63],v[16:19]
ds_read_b128 v[52:55],v2 offset:23552
v_mfma_f32_16x16x32_bf16 v[16:19],v[88:91],v[56:59],v[16:19]
s_waitcnt lgkmcnt(4)
v_mfma_f32_16x16x32_bf16 v[16:19],a[64:67],v[32:35],v[16:19]
ds_read_b128 v[56:59],v2 offset:37888
v_mfma_f32_16x16x32_bf16 v[16:19],a[176:179],v[36:39],v[16:19]
buffer_load_dwordx4 v2,s[32:35],s63 offen offset:0 sc1 lds
v_mfma_f32_16x16x32_bf16 v[20:23],a[180:183],v[36:39],v[20:23]
ds_read_b128 v[60:63],v2 offset:52224
v_mfma_f32_16x16x32_bf16 v[20:23],a[68:71],v[32:35],v[20:23]
s_waitcnt lgkmcnt(4)
v_mfma_f32_16x16x32_bf16 v[20:23],v[100:103],v[40:43],v[20:23]
ds_read_b128 v[32:35],v2 offset:10240
v_mfma_f32_16x16x32_bf16 v[20:23],v[212:215],v[44:47],v[20:23]
buffer_load_dwordx4 v2,s[32:35],s63 offen offset:1024 sc1 lds
v_mfma_f32_16x16x32_bf16 v[16:19],v[208:211],v[44:47],v[16:19]
ds_read_b128 v[36:39],v2 offset:24576
v_mfma_f32_16x16x32_bf16 v[16:19],v[96:99],v[40:43],v[16:19]
s_waitcnt lgkmcnt(4)
v_mfma_f32_16x16x32_bf16 v[16:19],a[72:75],v[48:51],v[16:19]
ds_read_b128 v[40:43],v2 offset:38912
v_mfma_f32_16x16x32_bf16 v[16:19],a[184:187],v[52:55],v[16:19]
buffer_load_dwordx4 v2,s[32:35],s63 offen offset:2048 sc1 lds
v_mfma_f32_16x16x32_bf16 v[20:23],a[188:191],v[52:55],v[20:23]
ds_read_b128 v[44:47],v2 offset:53248
v_mfma_f32_16x16x32_bf16 v[20:23],a[76:79],v[48:51],v[20:23]
s_waitcnt lgkmcnt(4)
v_mfma_f32_16x16x32_bf16 v[20:23],v[108:111],v[56:59],v[20:23]
ds_read_b128 v[48:51],v2 offset:11264
v_mfma_f32_16x16x32_bf16 v[20:23],v[220:223],v[60:63],v[20:23]
v_mfma_f32_16x16x32_bf16 v[16:19],v[216:219],v[60:63],v[16:19]
ds_read_b128 v[52:55],v2 offset:25600
v_mfma_f32_16x16x32_bf16 v[16:19],v[104:107],v[56:59],v[16:19]
s_waitcnt lgkmcnt(4)
v_mfma_f32_16x16x32_bf16 v[16:19],a[80:83],v[32:35],v[16:19]
ds_read_b128 v[56:59],v2 offset:39936
v_mfma_f32_16x16x32_bf16 v[16:19],a[192:195],v[36:39],v[16:19]
buffer_load_dwordx4 v2,s[32:35],s63 offen offset:3072 sc1 lds
s_add_u32 s63,s63,4096
v_mfma_f32_16x16x32_bf16 v[20:23],a[196:199],v[36:39],v[20:23]
ds_read_b128 v[60:63],v2 offset:54272
s_add_u32 m0,s64,69632
v_mfma_f32_16x16x32_bf16 v[20:23],a[84:87],v[32:35],v[20:23]
s_waitcnt lgkmcnt(4)
v_mfma_f32_16x16x32_bf16 v[20:23],v[116:119],v[40:43],v[20:23]
ds_read_b128 v[32:35],v2 offset:12288
v_mfma_f32_16x16x32_bf16 v[20:23],v[228:231],v[44:47],v[20:23]
buffer_load_dwordx4 v2,s[32:35],s63 offen offset:0 sc1 lds
v_mfma_f32_16x16x32_bf16 v[16:19],v[224:227],v[44:47],v[16:19]
ds_read_b128 v[36:39],v2 offset:26624
v_mfma_f32_16x16x32_bf16 v[16:19],v[112:115],v[40:43],v[16:19]
s_waitcnt lgkmcnt(4)
v_mfma_f32_16x16x32_bf16 v[16:19],a[88:91],v[48:51],v[16:19]
ds_read_b128 v[40:43],v2 offset:40960
v_mfma_f32_16x16x32_bf16 v[16:19],a[200:203],v[52:55],v[16:19]
buffer_load_dwordx4 v2,s[32:35],s63 offen offset:1024 sc1 lds
s_add_u32 s63,s63,s31
v_mfma_f32_16x16x32_bf16 v[20:23],a[204:207],v[52:55],v[20:23]
ds_read_b128 v[44:47],v2 offset:55296
v_mfma_f32_16x16x32_bf16 v[20:23],a[92:95],v[48:51],v[20:23]
s_waitcnt lgkmcnt(4)
v_mfma_f32_16x16x32_bf16 v[20:23],v[124:127],v[56:59],v[20:23]
ds_read_b128 v[48:51],v2 offset:13312
s_waitcnt vmcnt(10)
v_mfma_f32_16x16x32_bf16 v[20:23],v[236:239],v[60:63],v[20:23]
v_lshlrev_b32 v24,16,v12
v_and_b32 v25,0xffff0000,v12
v_mfma_f32_16x16x32_bf16 v[16:19],v[232:235],v[60:63],v[16:19]
ds_read_b128 v[52:55],v2 offset:27648
v_lshlrev_b32 v26,16,v13
v_mfma_f32_16x16x32_bf16 v[16:19],v[120:123],v[56:59],v[16:19]
v_and_b32 v27,0xffff0000,v13
s_waitcnt lgkmcnt(4)
v_mfma_f32_16x16x32_bf16 v[16:19],a[96:99],v[32:35],v[16:19]
s_barrier
v_mfma_f32_16x16x32_bf16 v[16:19],a[208:211],v[36:39],v[16:19]
ds_read_b128 v[56:59],v2 offset:41984
v_lshlrev_b32 v28,16,v14
v_mfma_f32_16x16x32_bf16 v[20:23],a[212:215],v[36:39],v[20:23]
ds_read_b128 v[60:63],v2 offset:56320
v_and_b32 v29,0xffff0000,v14
v_mfma_f32_16x16x32_bf16 v[20:23],a[100:103],v[32:35],v[20:23]
v_lshlrev_b32 v30,16,v15
s_waitcnt lgkmcnt(4)
v_mfma_f32_16x16x32_bf16 v[20:23],v[132:135],v[40:43],v[20:23]
ds_read_b128 v[32:35],v4 offset:0
v_and_b32 v31,0xffff0000,v15
v_mfma_f32_16x16x32_bf16 v[20:23],v[244:247],v[44:47],v[20:23]
v_mfma_f32_16x16x32_bf16 v[16:19],v[240:243],v[44:47],v[16:19]
ds_read_b128 v[36:39],v4 offset:14336
v_mfma_f32_16x16x32_bf16 v[16:19],v[128:131],v[40:43],v[16:19]
s_waitcnt lgkmcnt(4)
v_mfma_f32_16x16x32_bf16 v[16:19],a[104:107],v[48:51],v[16:19]
ds_read_b128 v[40:43],v4 offset:28672
v_mfma_f32_16x16x32_bf16 v[16:19],a[216:219],v[52:55],v[16:19]
v_mfma_f32_16x16x32_bf16 v[20:23],a[220:223],v[52:55],v[20:23]
ds_read_b128 v[44:47],v4 offset:43008
v_mfma_f32_16x16x32_bf16 v[20:23],a[108:111],v[48:51],v[20:23]
s_waitcnt lgkmcnt(4)
v_mfma_f32_16x16x32_bf16 v[20:23],v[140:143],v[56:59],v[20:23]
ds_read_b128 v[48:51],v4 offset:1024
v_mfma_f32_16x16x32_bf16 v[20:23],v[252:255],v[60:63],v[20:23]
v_mfma_f32_16x16x32_bf16 v[16:19],v[248:251],v[60:63],v[16:19]
ds_read_b128 v[52:55],v4 offset:15360
s_sub_u32 s62,s62,1
v_mfma_f32_16x16x32_bf16 v[16:19],v[136:139],v[56:59],v[16:19]
s_cbranch_scc0 .L_dml_3
.L_dmu_3:
s_nop 1
s_nop 0
v_cvt_pk_bf16_f32 v15,v22,v23
v_cvt_pk_bf16_f32 v14,v20,v21
s_nop 1
v_cvt_pk_bf16_f32 v13,v18,v19
v_cvt_pk_bf16_f32 v12,v16,v17
ds_write_b128 v5,v[12:15]
ds_read2st64_b32 v[12:13],v3 offset0:0 offset1:1
ds_read2st64_b32 v[14:15],v3 offset0:2 offset1:3
s_waitcnt lgkmcnt(0)
buffer_atomic_pk_add_bf16 v12,v1,s[44:47],s65 offen offset:0 sc1
s_add_u32 s70,s70,s75
buffer_atomic_pk_add_bf16 v13,v1,s[44:47],s65 offen offset:2048 sc1
s_add_u32 s65,s65,16384
buffer_atomic_pk_add_bf16 v14,v1,s[44:47],s65 offen offset:0 sc1
buffer_atomic_pk_add_bf16 v15,v1,s[44:47],s65 offen offset:2048 sc1
s_and_b32 s65,s70,0x3f8000
s_branch .L_end
.L_dmt_3:
s_nop 1
s_nop 0
v_cvt_pk_bf16_f32 v15,v30,v31
v_cvt_pk_bf16_f32 v14,v28,v29
s_nop 1
v_cvt_pk_bf16_f32 v13,v26,v27
v_cvt_pk_bf16_f32 v12,v24,v25
ds_write_b128 v5,v[12:15]
ds_read2st64_b32 v[12:13],v3 offset0:0 offset1:1
ds_read2st64_b32 v[14:15],v3 offset0:2 offset1:3
s_waitcnt lgkmcnt(0)
buffer_atomic_pk_add_bf16 v12,v1,s[44:47],s65 offen offset:0 sc1
s_add_u32 s70,s70,s68
buffer_atomic_pk_add_bf16 v13,v1,s[44:47],s65 offen offset:2048 sc1
s_add_u32 s65,s65,16384
buffer_atomic_pk_add_bf16 v14,v1,s[44:47],s65 offen offset:0 sc1
s_mov_b32 s68,s75
buffer_atomic_pk_add_bf16 v15,v1,s[44:47],s65 offen offset:2048 sc1
s_and_b32 s65,s70,0x3f8000
s_branch .L_end
.L_dh:
v_lshrrev_b32 v10,4,v1
v_and_b32 v1,15,v1
s_lshl_b32 s48,s50,6
v_lshl_add_u32 v1,v1,2,s48
v_lshl_add_u32 v1,v10,12,v1
s_sub_u32 s71,s65,1187840
s_add_u32 s69,s69,38010880
s_add_u32 s68,s70,2236416
s_mov_b32 s67,s68
s_mov_b32 s72,0x3f803f80
s_add_u32 s70,s70,42622976
s_and_b32 s65,s59,0x3f8000
s_cmp_lg_u32 s26,1
s_cbranch_scc1 .L_skda
s_mov_b32 s48,20000
s_branch .L_rfda
.L_rlda:
s_sleep 2
.L_rfda:
buffer_load_dwordx2 v[6:7],v0,s[0:3],s66 offen sc1
s_sub_u32 s48,s48,1
s_cbranch_scc1 .L_end
s_waitcnt vmcnt(0)
v_cmp_ne_u64 vcc,v[8:9],v[6:7]
s_cbranch_vccnz .L_rlda
.L_skda:
s_waitcnt vmcnt(0)
s_barrier
s_add_u32 s66,s66,s30
s_add_u32 m0,s64,0
s_nop 0
buffer_load_dwordx4 v2,s[32:35],s63 offen offset:0 sc1 lds
buffer_load_dwordx4 v2,s[32:35],s63 offen offset:1024 sc1 lds
buffer_load_dwordx4 v2,s[32:35],s63 offen offset:2048 sc1 lds
buffer_load_dwordx4 v2,s[32:35],s63 offen offset:3072 sc1 lds
s_add_u32 s63,s63,4096
s_add_u32 m0,s64,4096
s_nop 0
buffer_load_dwordx4 v2,s[32:35],s63 offen offset:0 sc1 lds
buffer_load_dwordx4 v2,s[32:35],s63 offen offset:1024 sc1 lds
buffer_load_dwordx4 v2,s[32:35],s63 offen offset:2048 sc1 lds
buffer_load_dwordx4 v2,s[32:35],s63 offen offset:3072 sc1 lds
s_add_u32 s63,s63,4096
s_add_u32 m0,s64,8192
s_nop 0
buffer_load_dwordx4 v2,s[32:35],s63 offen offset:0 sc1 lds
buffer_load_dwordx4 v2,s[32:35],s63 offen offset:1024 sc1 lds
buffer_load_dwordx4 v2,s[32:35],s63 offen offset:2048 sc1 lds
buffer_load_dwordx4 v2,s[32:35],s63 offen offset:3072 sc1 lds
s_add_u32 s63,s63,4096
s_add_u32 m0,s64,12288
s_nop 0
buffer_load_dwordx4 v2,s[32:35],s63 offen offset:0 sc1 lds
buffer_load_dwordx4 v2,s[32:35],s63 offen offset:1024 sc1 lds
s_add_u32 s63,s63,s31
buffer_load_dwordx2 v[6:7],v0,s[0:3],s66 offen sc1
s_waitcnt vmcnt(0)
s_barrier
ds_read_b128 v[32:35],v2 offset:0
ds_read_b128 v[36:39],v2 offset:14336
ds_read_b128 v[40:43],v2 offset:28672
ds_read_b128 v[44:47],v2 offset:43008
ds_read_b128 v[48:51],v2 offset:1024
ds_read_b128 v[52:55],v2 offset:15360
.L_dhgo:
s_cmp_eq_u32 s26,1
s_cbranch_scc1 .L_dhv1
s_cmp_eq_u32 s26,2
s_cbranch_scc1 .L_dhv2
s_cmp_eq_u32 s26,3
s_cbranch_scc1 .L_dhv3
.p2align 6
.L_dhv0:
.L_dal_0:
s_waitcnt lgkmcnt(4)
v_mfma_f32_16x16x32_bf16 v[16:19],a[0:3],v[32:35],0
ds_read_b128 v[56:59],v2 offset:29696
v_dot2_f32_bf16 v10,v12,s72,0
v_mfma_f32_16x16x32_bf16 v[16:19],a[112:115],v[36:39],v[16:19]
v_dot2_f32_bf16 v11,v12,v12,0
v_dot2_f32_bf16 v10,v13,s72,v10
v_mfma_f32_16x16x32_bf16 v[20:23],a[116:119],v[36:39],0
ds_read_b128 v[60:63],v2 offset:44032
v_dot2_f32_bf16 v11,v13,v13,v11
v_mfma_f32_16x16x32_bf16 v[20:23],a[4:7],v[32:35],v[20:23]
v_dot2_f32_bf16 v10,v14,s72,v10
s_waitcnt lgkmcnt(4)
v_mfma_f32_16x16x32_bf16 v[20:23],a[228:231],v[40:43],v[20:23]
ds_read_b128 v[32:35],v2 offset:2048
v_dot2_f32_bf16 v11,v14,v14,v11
v_mfma_f32_16x16x32_bf16 v[20:23],v[148:151],v[44:47],v[20:23]
v_dot2_f32_bf16 v10,v15,s72,v10
v_dot2_f32_bf16 v11,v15,v15,v11
v_mfma_f32_16x16x32_bf16 v[16:19],v[144:147],v[44:47],v[16:19]
ds_read_b128 v[36:39],v2 offset:16384
v_mfma_f32_16x16x32_bf16 v[16:19],a[224:227],v[40:43],v[16:19]
v_permlane16_swap_b32 v10,v11
s_waitcnt lgkmcnt(4)
v_mfma_f32_16x16x32_bf16 v[16:19],a[8:11],v[48:51],v[16:19]
ds_read_b128 v[40:43],v2 offset:30720
v_add_f32 v10,v10,v11
v_mfma_f32_16x16x32_bf16 v[16:19],a[120:123],v[52:55],v[16:19]
v_mfma_f32_16x16x32_bf16 v[20:23],a[124:127],v[52:55],v[20:23]
ds_read_b128 v[44:47],v2 offset:45056
v_permlane32_swap_b32 v10,v11
v_mfma_f32_16x16x32_bf16 v[20:23],a[12:15],v[48:51],v[20:23]
v_add_f32 v10,v10,v11
s_waitcnt lgkmcnt(4)
v_mfma_f32_16x16x32_bf16 v[20:23],a[236:239],v[56:59],v[20:23]
ds_read_b128 v[48:51],v2 offset:3072
v_mfma_f32_16x16x32_bf16 v[20:23],v[156:159],v[60:63],v[20:23]
buffer_store_dword v10,v3,s[32:35],s67 offen offset:1024 sc1
v_mfma_f32_16x16x32_bf16 v[16:19],v[152:155],v[60:63],v[16:19]
ds_read_b128 v[52:55],v2 offset:17408
v_mfma_f32_16x16x32_bf16 v[16:19],a[232:235],v[56:59],v[16:19]
s_waitcnt lgkmcnt(4)
v_mfma_f32_16x16x32_bf16 v[16:19],a[16:19],v[32:35],v[16:19]
ds_read_b128 v[56:59],v2 offset:31744
v_mfma_f32_16x16x32_bf16 v[16:19],a[128:131],v[36:39],v[16:19]
v_mfma_f32_16x16x32_bf16 v[20:23],a[132:135],v[36:39],v[20:23]
ds_read_b128 v[60:63],v2 offset:46080
v_mfma_f32_16x16x32_bf16 v[20:23],a[20:23],v[32:35],v[20:23]
s_waitcnt lgkmcnt(4)
s_waitcnt vmcnt(3)
s_barrier
v_mfma_f32_16x16x32_bf16 v[20:23],a[244:247],v[40:43],v[20:23]
ds_read_b128 v[32:35],v2 offset:4096
s_mov_b32 m0,s74
v_mfma_f32_16x16x32_bf16 v[20:23],v[164:167],v[44:47],v[20:23]
buffer_load_dwordx4 v2,s[32:35],s71 offen offset:0 sc1 lds
v_mfma_f32_16x16x32_bf16 v[16:19],v[160:163],v[44:47],v[16:19]
ds_read_b128 v[36:39],v2 offset:18432
v_mfma_f32_16x16x32_bf16 v[16:19],a[240:243],v[40:43],v[16:19]
s_waitcnt lgkmcnt(4)
v_mfma_f32_16x16x32_bf16 v[16:19],a[24:27],v[48:51],v[16:19]
ds_read_b128 v[40:43],v2 offset:32768
v_mfma_f32_16x16x32_bf16 v[16:19],a[136:139],v[52:55],v[16:19]
buffer_load_dwordx4 v2,s[32:35],s71 offen offset:1024 sc1 lds
s_add_u32 s71,s71,s30
v_mfma_f32_16x16x32_bf16 v[20:23],a[140:143],v[52:55],v[20:23]
ds_read_b128 v[44:47],v2 offset:47104
s_add_u32 m0,s64,57344
v_mfma_f32_16x16x32_bf16 v[20:23],a[28:31],v[48:51],v[20:23]
s_waitcnt lgkmcnt(4)
v_mfma_f32_16x16x32_bf16 v[20:23],a[252:255],v[56:59],v[20:23]
ds_read_b128 v[48:51],v2 offset:5120
v_mfma_f32_16x16x32_bf16 v[20:23],v[172:175],v[60:63],v[20:23]
buffer_load_dwordx4 v2,s[32:35],s63 offen offset:0 sc1 lds
v_mfma_f32_16x16x32_bf16 v[16:19],v[168:171],v[60:63],v[16:19]
ds_read_b128 v[52:55],v2 offset:19456
v_mfma_f32_16x16x32_bf16 v[16:19],a[248:251],v[56:59],v[16:19]
s_waitcnt lgkmcnt(4)
v_mfma_f32_16x16x32_bf16 v[16:19],a[32:35],v[32:35],v[16:19]
ds_read_b128 v[56:59],v2 offset:33792
v_mfma_f32_16x16x32_bf16 v[16:19],a[144:147],v[36:39],v[16:19]
buffer_load_dwordx4 v2,s[32:35],s63 offen offset:1024 sc1 lds
v_mfma_f32_16x16x32_bf16 v[20:23],a[148:151],v[36:39],v[20:23]
ds_read_b128 v[60:63],v2 offset:48128
v_mfma_f32_16x16x32_bf16 v[20:23],a[36:39],v[32:35],v[20:23]
s_waitcnt lgkmcnt(4)
v_mfma_f32_16x16x32_bf16 v[20:23],v[68:71],v[40:43],v[20:23]
ds_read_b128 v[32:35],v2 offset:6144
v_mfma_f32_16x16x32_bf16 v[20:23],v[180:183],v[44:47],v[20:23]
buffer_load_dwordx4 v2,s[32:35],s63 offen offset:2048 sc1 lds
v_mfma_f32_16x16x32_bf16 v[16:19],v[176:179],v[44:47],v[16:19]
ds_read_b128 v[36:39],v2 offset:20480
v_mfma_f32_16x16x32_bf16 v[16:19],v[64:67],v[40:43],v[16:19]
s_waitcnt lgkmcnt(4)
v_mfma_f32_16x16x32_bf16 v[16:19],a[40:43],v[48:51],v[16:19]
ds_read_b128 v[40:43],v2 offset:34816
v_mfma_f32_16x16x32_bf16 v[16:19],a[152:155],v[52:55],v[16:19]
buffer_load_dwordx4 v2,s[32:35],s63 offen offset:3072 sc1 lds
s_add_u32 s63,s63,4096
v_mfma_f32_16x16x32_bf16 v[20:23],a[156:159],v[52:55],v[20:23]
ds_read_b128 v[44:47],v2 offset:49152
s_add_u32 m0,s64,61440
v_mfma_f32_16x16x32_bf16 v[20:23],a[44:47],v[48:51],v[20:23]
s_waitcnt lgkmcnt(4)
v_mfma_f32_16x16x32_bf16 v[20:23],v[76:79],v[56:59],v[20:23]
ds_read_b128 v[48:51],v2 offset:7168
v_mfma_f32_16x16x32_bf16 v[20:23],v[188:191],v[60:63],v[20:23]
buffer_load_dwordx4 v2,s[32:35],s63 offen offset:0 sc1 lds
v_mfma_f32_16x16x32_bf16 v[16:19],v[184:187],v[60:63],v[16:19]
ds_read_b128 v[52:55],v2 offset:21504
v_mfma_f32_16x16x32_bf16 v[16:19],v[72:75],v[56:59],v[16:19]
s_waitcnt lgkmcnt(4)
v_mfma_f32_16x16x32_bf16 v[16:19],a[48:51],v[32:35],v[16:19]
ds_read_b128 v[56:59],v2 offset:35840
v_mfma_f32_16x16x32_bf16 v[16:19],a[160:163],v[36:39],v[16:19]
buffer_load_dwordx4 v2,s[32:35],s63 offen offset:1024 sc1 lds
v_mfma_f32_16x16x32_bf16 v[20:23],a[164:167],v[36:39],v[20:23]
ds_read_b128 v[60:63],v2 offset:50176
v_mfma_f32_16x16x32_bf16 v[20:23],a[52:55],v[32:35],v[20:23]
s_waitcnt lgkmcnt(4)
v_mfma_f32_16x16x32_bf16 v[20:23],v[84:87],v[40:43],v[20:23]
ds_read_b128 v[32:35],v2 offset:8192
v_mfma_f32_16x16x32_bf16 v[20:23],v[196:199],v[44:47],v[20:23]
buffer_load_dwordx4 v2,s[32:35],s63 offen offset:2048 sc1 lds
v_mfma_f32_16x16x32_bf16 v[16:19],v[192:195],v[44:47],v[16:19]
ds_read_b128 v[36:39],v2 offset:22528
v_mfma_f32_16x16x32_bf16 v[16:19],v[80:83],v[40:43],v[16:19]
s_waitcnt lgkmcnt(4)
v_mfma_f32_16x16x32_bf16 v[16:19],a[56:59],v[48:51],v[16:19]
ds_read_b128 v[40:43],v2 offset:36864
v_mfma_f32_16x16x32_bf16 v[16:19],a[168:171],v[52:55],v[16:19]
buffer_load_dwordx4 v2,s[32:35],s63 offen offset:3072 sc1 lds
v_mfma_f32_16x16x32_bf16 v[20:23],a[172:175],v[52:55],v[20:23]
ds_read_b128 v[44:47],v2 offset:51200
v_mfma_f32_16x16x32_bf16 v[20:23],a[60:63],v[48:51],v[20:23]
s_waitcnt lgkmcnt(4)
v_mfma_f32_16x16x32_bf16 v[20:23],v[92:95],v[56:59],v[20:23]
ds_read_b128 v[48:51],v2 offset:9216
v_mfma_f32_16x16x32_bf16 v[20:23],v[204:207],v[60:63],v[20:23]
s_add_u32 s63,s63,4096
s_add_u32 m0,s64,65536
v_mfma_f32_16x16x32_bf16 v[16:19],v[200:203],v[60:63],v[16:19]
ds_read_b128 v[52:55],v2 offset:23552
v_mfma_f32_16x16x32_bf16 v[16:19],v[88:91],v[56:59],v[16:19]
s_waitcnt lgkmcnt(4)
v_mfma_f32_16x16x32_bf16 v[16:19],a[64:67],v[32:35],v[16:19]
ds_read_b128 v[56:59],v2 offset:37888
v_mfma_f32_16x16x32_bf16 v[16:19],a[176:179],v[36:39],v[16:19]
buffer_load_dwordx4 v2,s[32:35],s63 offen offset:0 sc1 lds
v_mfma_f32_16x16x32_bf16 v[20:23],a[180:183],v[36:39],v[20:23]
ds_read_b128 v[60:63],v2 offset:52224
v_mfma_f32_16x16x32_bf16 v[20:23],a[68:71],v[32:35],v[20:23]
s_waitcnt lgkmcnt(4)
v_mfma_f32_16x16x32_bf16 v[20:23],v[100:103],v[40:43],v[20:23]
ds_read_b128 v[32:35],v2 offset:10240
v_mfma_f32_16x16x32_bf16 v[20:23],v[212:215],v[44:47],v[20:23]
buffer_load_dwordx4 v2,s[32:35],s63 offen offset:1024 sc1 lds
v_mfma_f32_16x16x32_bf16 v[16:19],v[208:211],v[44:47],v[16:19]
ds_read_b128 v[36:39],v2 offset:24576
v_mfma_f32_16x16x32_bf16 v[16:19],v[96:99],v[40:43],v[16:19]
s_waitcnt lgkmcnt(4)
v_mfma_f32_16x16x32_bf16 v[16:19],a[72:75],v[48:51],v[16:19]
ds_read_b128 v[40:43],v2 offset:38912
s_waitcnt vmcnt(10)
v_mfma_f32_16x16x32_bf16 v[16:19],a[184:187],v[52:55],v[16:19]
buffer_load_dwordx4 v2,s[32:35],s63 offen offset:2048 sc1 lds
ds_read_b128 v[12:15],v5 offset:0
v_mfma_f32_16x16x32_bf16 v[20:23],a[188:191],v[52:55],v[20:23]
ds_read_b128 v[44:47],v2 offset:53248
v_mfma_f32_16x16x32_bf16 v[20:23],a[76:79],v[48:51],v[20:23]
s_waitcnt lgkmcnt(5)
v_mfma_f32_16x16x32_bf16 v[20:23],v[108:111],v[56:59],v[20:23]
ds_read_b128 v[48:51],v2 offset:11264
v_mfma_f32_16x16x32_bf16 v[20:23],v[220:223],v[60:63],v[20:23]
v_mfma_f32_16x16x32_bf16 v[16:19],v[216:219],v[60:63],v[16:19]
ds_read_b128 v[52:55],v2 offset:25600
s_waitcnt lgkmcnt(3)
v_mfma_f32_16x16x32_bf16 v[16:19],v[104:107],v[56:59],v[16:19]
v_add_f32 v24,v24,v12
v_mfma_f32_16x16x32_bf16 v[16:19],a[80:83],v[32:35],v[16:19]
ds_read_b128 v[56:59],v2 offset:39936
v_add_f32 v25,v25,v13
v_mfma_f32_16x16x32_bf16 v[16:19],a[192:195],v[36:39],v[16:19]
buffer_load_dwordx4 v2,s[32:35],s63 offen offset:3072 sc1 lds
s_add_u32 s63,s63,4096
v_mfma_f32_16x16x32_bf16 v[20:23],a[196:199],v[36:39],v[20:23]
ds_read_b128 v[60:63],v2 offset:54272
s_add_u32 m0,s64,69632
v_mfma_f32_16x16x32_bf16 v[20:23],a[84:87],v[32:35],v[20:23]
v_add_f32 v26,v26,v14
s_waitcnt lgkmcnt(4)
v_mfma_f32_16x16x32_bf16 v[20:23],v[116:119],v[40:43],v[20:23]
ds_read_b128 v[32:35],v2 offset:12288
v_add_f32 v27,v27,v15
v_mfma_f32_16x16x32_bf16 v[20:23],v[228:231],v[44:47],v[20:23]
buffer_load_dwordx4 v2,s[32:35],s63 offen offset:0 sc1 lds
ds_read_b128 v[12:15],v5 offset:1024
v_mfma_f32_16x16x32_bf16 v[16:19],v[224:227],v[44:47],v[16:19]
ds_read_b128 v[36:39],v2 offset:26624
v_mfma_f32_16x16x32_bf16 v[16:19],v[112:115],v[40:43],v[16:19]
s_waitcnt lgkmcnt(5)
v_mfma_f32_16x16x32_bf16 v[16:19],a[88:91],v[48:51],v[16:19]
ds_read_b128 v[40:43],v2 offset:40960
v_mfma_f32_16x16x32_bf16 v[16:19],a[200:203],v[52:55],v[16:19]
buffer_load_dwordx4 v2,s[32:35],s63 offen offset:1024 sc1 lds
s_add_u32 s63,s63,s31
v_mfma_f32_16x16x32_bf16 v[20:23],a[204:207],v[52:55],v[20:23]
ds_read_b128 v[44:47],v2 offset:55296
s_waitcnt lgkmcnt(3)
v_mfma_f32_16x16x32_bf16 v[20:23],a[92:95],v[48:51],v[20:23]
v_add_f32 v28,v28,v12
v_mfma_f32_16x16x32_bf16 v[20:23],v[124:127],v[56:59],v[20:23]
ds_read_b128 v[48:51],v2 offset:13312
v_add_f32 v29,v29,v13
v_mfma_f32_16x16x32_bf16 v[20:23],v[236:239],v[60:63],v[20:23]
v_add_f32 v30,v30,v14
v_add_f32 v31,v31,v15
v_mfma_f32_16x16x32_bf16 v[16:19],v[232:235],v[60:63],v[16:19]
ds_read_b128 v[52:55],v2 offset:27648
v_cvt_pk_bf16_f32 v12,v24,v25
v_mfma_f32_16x16x32_bf16 v[16:19],v[120:123],v[56:59],v[16:19]
v_cvt_pk_bf16_f32 v13,v26,v27
s_waitcnt lgkmcnt(4)
v_mfma_f32_16x16x32_bf16 v[16:19],a[96:99],v[32:35],v[16:19]
ds_read_b128 v[56:59],v2 offset:41984
v_cvt_pk_bf16_f32 v14,v28,v29
v_mfma_f32_16x16x32_bf16 v[16:19],a[208:211],v[36:39],v[16:19]
v_cvt_pk_bf16_f32 v15,v30,v31
v_mfma_f32_16x16x32_bf16 v[20:23],a[212:215],v[36:39],v[20:23]
ds_read_b128 v[60:63],v2 offset:56320
v_mfma_f32_16x16x32_bf16 v[20:23],a[100:103],v[32:35],v[20:23]
s_waitcnt lgkmcnt(4)
s_waitcnt vmcnt(10)
s_barrier
v_mfma_f32_16x16x32_bf16 v[20:23],v[132:135],v[40:43],v[20:23]
ds_read_b128 v[32:35],v4 offset:0
v_mfma_f32_16x16x32_bf16 v[20:23],v[244:247],v[44:47],v[20:23]
buffer_store_dwordx4 v[12:15],v2,s[32:35],s68 offen sc1
v_mfma_f32_16x16x32_bf16 v[16:19],v[240:243],v[44:47],v[16:19]
ds_read_b128 v[36:39],v4 offset:14336
v_mfma_f32_16x16x32_bf16 v[16:19],v[128:131],v[40:43],v[16:19]
s_waitcnt lgkmcnt(4)
v_mfma_f32_16x16x32_bf16 v[16:19],a[104:107],v[48:51],v[16:19]
ds_read_b128 v[40:43],v4 offset:28672
v_mfma_f32_16x16x32_bf16 v[16:19],a[216:219],v[52:55],v[16:19]
buffer_store_dwordx2 v[8:9],v2,s[92:95],s67 idxen sc1
v_mfma_f32_16x16x32_bf16 v[20:23],a[220:223],v[52:55],v[20:23]
ds_read_b128 v[44:47],v4 offset:43008
s_mov_b32 s67,s68
v_mfma_f32_16x16x32_bf16 v[20:23],a[108:111],v[48:51],v[20:23]
s_mov_b32 s68,s70
s_waitcnt lgkmcnt(4)
v_mfma_f32_16x16x32_bf16 v[20:23],v[140:143],v[56:59],v[20:23]
ds_read_b128 v[48:51],v4 offset:1024
s_add_u32 s70,s70,1187840
v_mfma_f32_16x16x32_bf16 v[20:23],v[252:255],v[60:63],v[20:23]
v_mfma_f32_16x16x32_bf16 v[16:19],v[248:251],v[60:63],v[16:19]
ds_read_b128 v[52:55],v4 offset:15360
s_sub_u32 s62,s62,1
v_mfma_f32_16x16x32_bf16 v[16:19],v[136:139],v[56:59],v[16:19]
s_cbranch_scc1 .L_dab_0
s_waitcnt lgkmcnt(4)
v_mfma_f32_16x16x32_bf16 v[24:27],a[0:3],v[32:35],0
ds_read_b128 v[56:59],v4 offset:29696
v_dot2_f32_bf16 v10,v12,s72,0
v_mfma_f32_16x16x32_bf16 v[24:27],a[112:115],v[36:39],v[24:27]
v_dot2_f32_bf16 v11,v12,v12,0
v_dot2_f32_bf16 v10,v13,s72,v10
v_mfma_f32_16x16x32_bf16 v[28:31],a[116:119],v[36:39],0
ds_read_b128 v[60:63],v4 offset:44032
v_dot2_f32_bf16 v11,v13,v13,v11
v_mfma_f32_16x16x32_bf16 v[28:31],a[4:7],v[32:35],v[28:31]
v_dot2_f32_bf16 v10,v14,s72,v10
s_waitcnt lgkmcnt(4)
)#"
R"#(
v_mfma_f32_16x16x32_bf16 v[28:31],a[228:231],v[40:43],v[28:31]
ds_read_b128 v[32:35],v4 offset:2048
v_dot2_f32_bf16 v11,v14,v14,v11
v_mfma_f32_16x16x32_bf16 v[28:31],v[148:151],v[44:47],v[28:31]
v_dot2_f32_bf16 v10,v15,s72,v10
v_dot2_f32_bf16 v11,v15,v15,v11
v_mfma_f32_16x16x32_bf16 v[24:27],v[144:147],v[44:47],v[24:27]
ds_read_b128 v[36:39],v4 offset:16384
v_mfma_f32_16x16x32_bf16 v[24:27],a[224:227],v[40:43],v[24:27]
v_permlane16_swap_b32 v10,v11
s_waitcnt lgkmcnt(4)
v_mfma_f32_16x16x32_bf16 v[24:27],a[8:11],v[48:51],v[24:27]
ds_read_b128 v[40:43],v4 offset:30720
v_add_f32 v10,v10,v11
v_mfma_f32_16x16x32_bf16 v[24:27],a[120:123],v[52:55],v[24:27]
v_mfma_f32_16x16x32_bf16 v[28:31],a[124:127],v[52:55],v[28:31]
ds_read_b128 v[44:47],v4 offset:45056
v_permlane32_swap_b32 v10,v11
v_mfma_f32_16x16x32_bf16 v[28:31],a[12:15],v[48:51],v[28:31]
v_add_f32 v10,v10,v11
s_waitcnt lgkmcnt(4)
v_mfma_f32_16x16x32_bf16 v[28:31],a[236:239],v[56:59],v[28:31]
ds_read_b128 v[48:51],v4 offset:3072
v_mfma_f32_16x16x32_bf16 v[28:31],v[156:159],v[60:63],v[28:31]
buffer_store_dword v10,v3,s[32:35],s67 offen offset:1024 sc1
v_mfma_f32_16x16x32_bf16 v[24:27],v[152:155],v[60:63],v[24:27]
ds_read_b128 v[52:55],v4 offset:17408
v_mfma_f32_16x16x32_bf16 v[24:27],a[232:235],v[56:59],v[24:27]
s_waitcnt lgkmcnt(4)
v_mfma_f32_16x16x32_bf16 v[24:27],a[16:19],v[32:35],v[24:27]
ds_read_b128 v[56:59],v4 offset:31744
v_mfma_f32_16x16x32_bf16 v[24:27],a[128:131],v[36:39],v[24:27]
v_mfma_f32_16x16x32_bf16 v[28:31],a[132:135],v[36:39],v[28:31]
ds_read_b128 v[60:63],v4 offset:46080
v_mfma_f32_16x16x32_bf16 v[28:31],a[20:23],v[32:35],v[28:31]
s_waitcnt lgkmcnt(4)
s_waitcnt vmcnt(3)
s_barrier
v_mfma_f32_16x16x32_bf16 v[28:31],a[244:247],v[40:43],v[28:31]
ds_read_b128 v[32:35],v4 offset:4096
s_mov_b32 m0,s74
v_mfma_f32_16x16x32_bf16 v[28:31],v[164:167],v[44:47],v[28:31]
buffer_load_dwordx4 v2,s[32:35],s71 offen offset:0 sc1 lds
v_mfma_f32_16x16x32_bf16 v[24:27],v[160:163],v[44:47],v[24:27]
ds_read_b128 v[36:39],v4 offset:18432
v_mfma_f32_16x16x32_bf16 v[24:27],a[240:243],v[40:43],v[24:27]
s_waitcnt lgkmcnt(4)
v_mfma_f32_16x16x32_bf16 v[24:27],a[24:27],v[48:51],v[24:27]
ds_read_b128 v[40:43],v4 offset:32768
v_mfma_f32_16x16x32_bf16 v[24:27],a[136:139],v[52:55],v[24:27]
buffer_load_dwordx4 v2,s[32:35],s71 offen offset:1024 sc1 lds
s_add_u32 s71,s71,s30
v_mfma_f32_16x16x32_bf16 v[28:31],a[140:143],v[52:55],v[28:31]
ds_read_b128 v[44:47],v4 offset:47104
s_add_u32 m0,s64,0
v_mfma_f32_16x16x32_bf16 v[28:31],a[28:31],v[48:51],v[28:31]
s_waitcnt lgkmcnt(4)
v_mfma_f32_16x16x32_bf16 v[28:31],a[252:255],v[56:59],v[28:31]
ds_read_b128 v[48:51],v4 offset:5120
v_mfma_f32_16x16x32_bf16 v[28:31],v[172:175],v[60:63],v[28:31]
buffer_load_dwordx4 v2,s[32:35],s63 offen offset:0 sc1 lds
v_mfma_f32_16x16x32_bf16 v[24:27],v[168:171],v[60:63],v[24:27]
ds_read_b128 v[52:55],v4 offset:19456
v_mfma_f32_16x16x32_bf16 v[24:27],a[248:251],v[56:59],v[24:27]
s_waitcnt lgkmcnt(4)
v_mfma_f32_16x16x32_bf16 v[24:27],a[32:35],v[32:35],v[24:27]
ds_read_b128 v[56:59],v4 offset:33792
v_mfma_f32_16x16x32_bf16 v[24:27],a[144:147],v[36:39],v[24:27]
buffer_load_dwordx4 v2,s[32:35],s63 offen offset:1024 sc1 lds
v_mfma_f32_16x16x32_bf16 v[28:31],a[148:151],v[36:39],v[28:31]
ds_read_b128 v[60:63],v4 offset:48128
v_mfma_f32_16x16x32_bf16 v[28:31],a[36:39],v[32:35],v[28:31]
s_waitcnt lgkmcnt(4)
v_mfma_f32_16x16x32_bf16 v[28:31],v[68:71],v[40:43],v[28:31]
ds_read_b128 v[32:35],v4 offset:6144
v_mfma_f32_16x16x32_bf16 v[28:31],v[180:183],v[44:47],v[28:31]
buffer_load_dwordx4 v2,s[32:35],s63 offen offset:2048 sc1 lds
v_mfma_f32_16x16x32_bf16 v[24:27],v[176:179],v[44:47],v[24:27]
ds_read_b128 v[36:39],v4 offset:20480
v_mfma_f32_16x16x32_bf16 v[24:27],v[64:67],v[40:43],v[24:27]
s_waitcnt lgkmcnt(4)
v_mfma_f32_16x16x32_bf16 v[24:27],a[40:43],v[48:51],v[24:27]
ds_read_b128 v[40:43],v4 offset:34816
v_mfma_f32_16x16x32_bf16 v[24:27],a[152:155],v[52:55],v[24:27]
buffer_load_dwordx4 v2,s[32:35],s63 offen offset:3072 sc1 lds
s_add_u32 s63,s63,4096
v_mfma_f32_16x16x32_bf16 v[28:31],a[156:159],v[52:55],v[28:31]
ds_read_b128 v[44:47],v4 offset:49152
s_add_u32 m0,s64,4096
v_mfma_f32_16x16x32_bf16 v[28:31],a[44:47],v[48:51],v[28:31]
s_waitcnt lgkmcnt(4)
v_mfma_f32_16x16x32_bf16 v[28:31],v[76:79],v[56:59],v[28:31]
ds_read_b128 v[48:51],v4 offset:7168
v_mfma_f32_16x16x32_bf16 v[28:31],v[188:191],v[60:63],v[28:31]
buffer_load_dwordx4 v2,s[32:35],s63 offen offset:0 sc1 lds
v_mfma_f32_16x16x32_bf16 v[24:27],v[184:187],v[60:63],v[24:27]
ds_read_b128 v[52:55],v4 offset:21504
v_mfma_f32_16x16x32_bf16 v[24:27],v[72:75],v[56:59],v[24:27]
s_waitcnt lgkmcnt(4)
v_mfma_f32_16x16x32_bf16 v[24:27],a[48:51],v[32:35],v[24:27]
ds_read_b128 v[56:59],v4 offset:35840
v_mfma_f32_16x16x32_bf16 v[24:27],a[160:163],v[36:39],v[24:27]
buffer_load_dwordx4 v2,s[32:35],s63 offen offset:1024 sc1 lds
v_mfma_f32_16x16x32_bf16 v[28:31],a[164:167],v[36:39],v[28:31]
ds_read_b128 v[60:63],v4 offset:50176
v_mfma_f32_16x16x32_bf16 v[28:31],a[52:55],v[32:35],v[28:31]
s_waitcnt lgkmcnt(4)
v_mfma_f32_16x16x32_bf16 v[28:31],v[84:87],v[40:43],v[28:31]
ds_read_b128 v[32:35],v4 offset:8192
v_mfma_f32_16x16x32_bf16 v[28:31],v[196:199],v[44:47],v[28:31]
buffer_load_dwordx4 v2,s[32:35],s63 offen offset:2048 sc1 lds
v_mfma_f32_16x16x32_bf16 v[24:27],v[192:195],v[44:47],v[24:27]
ds_read_b128 v[36:39],v4 offset:22528
v_mfma_f32_16x16x32_bf16 v[24:27],v[80:83],v[40:43],v[24:27]
s_waitcnt lgkmcnt(4)
v_mfma_f32_16x16x32_bf16 v[24:27],a[56:59],v[48:51],v[24:27]
ds_read_b128 v[40:43],v4 offset:36864
v_mfma_f32_16x16x32_bf16 v[24:27],a[168:171],v[52:55],v[24:27]
buffer_load_dwordx4 v2,s[32:35],s63 offen offset:3072 sc1 lds
v_mfma_f32_16x16x32_bf16 v[28:31],a[172:175],v[52:55],v[28:31]
ds_read_b128 v[44:47],v4 offset:51200
v_mfma_f32_16x16x32_bf16 v[28:31],a[60:63],v[48:51],v[28:31]
s_waitcnt lgkmcnt(4)
v_mfma_f32_16x16x32_bf16 v[28:31],v[92:95],v[56:59],v[28:31]
ds_read_b128 v[48:51],v4 offset:9216
v_mfma_f32_16x16x32_bf16 v[28:31],v[204:207],v[60:63],v[28:31]
s_add_u32 s63,s63,4096
s_add_u32 m0,s64,8192
v_mfma_f32_16x16x32_bf16 v[24:27],v[200:203],v[60:63],v[24:27]
ds_read_b128 v[52:55],v4 offset:23552
v_mfma_f32_16x16x32_bf16 v[24:27],v[88:91],v[56:59],v[24:27]
s_waitcnt lgkmcnt(4)
v_mfma_f32_16x16x32_bf16 v[24:27],a[64:67],v[32:35],v[24:27]
ds_read_b128 v[56:59],v4 offset:37888
v_mfma_f32_16x16x32_bf16 v[24:27],a[176:179],v[36:39],v[24:27]
buffer_load_dwordx4 v2,s[32:35],s63 offen offset:0 sc1 lds
v_mfma_f32_16x16x32_bf16 v[28:31],a[180:183],v[36:39],v[28:31]
ds_read_b128 v[60:63],v4 offset:52224
v_mfma_f32_16x16x32_bf16 v[28:31],a[68:71],v[32:35],v[28:31]
s_waitcnt lgkmcnt(4)
v_mfma_f32_16x16x32_bf16 v[28:31],v[100:103],v[40:43],v[28:31]
ds_read_b128 v[32:35],v4 offset:10240
v_mfma_f32_16x16x32_bf16 v[28:31],v[212:215],v[44:47],v[28:31]
buffer_load_dwordx4 v2,s[32:35],s63 offen offset:1024 sc1 lds
v_mfma_f32_16x16x32_bf16 v[24:27],v[208:211],v[44:47],v[24:27]
ds_read_b128 v[36:39],v4 offset:24576
v_mfma_f32_16x16x32_bf16 v[24:27],v[96:99],v[40:43],v[24:27]
s_waitcnt lgkmcnt(4)
v_mfma_f32_16x16x32_bf16 v[24:27],a[72:75],v[48:51],v[24:27]
ds_read_b128 v[40:43],v4 offset:38912
s_waitcnt vmcnt(10)
v_mfma_f32_16x16x32_bf16 v[24:27],a[184:187],v[52:55],v[24:27]
buffer_load_dwordx4 v2,s[32:35],s63 offen offset:2048 sc1 lds
ds_read_b128 v[12:15],v5 offset:0
v_mfma_f32_16x16x32_bf16 v[28:31],a[188:191],v[52:55],v[28:31]
ds_read_b128 v[44:47],v4 offset:53248
v_mfma_f32_16x16x32_bf16 v[28:31],a[76:79],v[48:51],v[28:31]
s_waitcnt lgkmcnt(5)
v_mfma_f32_16x16x32_bf16 v[28:31],v[108:111],v[56:59],v[28:31]
ds_read_b128 v[48:51],v4 offset:11264
v_mfma_f32_16x16x32_bf16 v[28:31],v[220:223],v[60:63],v[28:31]
v_mfma_f32_16x16x32_bf16 v[24:27],v[216:219],v[60:63],v[24:27]
ds_read_b128 v[52:55],v4 offset:25600
s_waitcnt lgkmcnt(3)
v_mfma_f32_16x16x32_bf16 v[24:27],v[104:107],v[56:59],v[24:27]
v_add_f32 v16,v16,v12
v_mfma_f32_16x16x32_bf16 v[24:27],a[80:83],v[32:35],v[24:27]
ds_read_b128 v[56:59],v4 offset:39936
v_add_f32 v17,v17,v13
v_mfma_f32_16x16x32_bf16 v[24:27],a[192:195],v[36:39],v[24:27]
buffer_load_dwordx4 v2,s[32:35],s63 offen offset:3072 sc1 lds
s_add_u32 s63,s63,4096
v_mfma_f32_16x16x32_bf16 v[28:31],a[196:199],v[36:39],v[28:31]
ds_read_b128 v[60:63],v4 offset:54272
s_add_u32 m0,s64,12288
v_mfma_f32_16x16x32_bf16 v[28:31],a[84:87],v[32:35],v[28:31]
v_add_f32 v18,v18,v14
s_waitcnt lgkmcnt(4)
v_mfma_f32_16x16x32_bf16 v[28:31],v[116:119],v[40:43],v[28:31]
ds_read_b128 v[32:35],v4 offset:12288
v_add_f32 v19,v19,v15
v_mfma_f32_16x16x32_bf16 v[28:31],v[228:231],v[44:47],v[28:31]
buffer_load_dwordx4 v2,s[32:35],s63 offen offset:0 sc1 lds
ds_read_b128 v[12:15],v5 offset:1024
v_mfma_f32_16x16x32_bf16 v[24:27],v[224:227],v[44:47],v[24:27]
ds_read_b128 v[36:39],v4 offset:26624
v_mfma_f32_16x16x32_bf16 v[24:27],v[112:115],v[40:43],v[24:27]
s_waitcnt lgkmcnt(5)
v_mfma_f32_16x16x32_bf16 v[24:27],a[88:91],v[48:51],v[24:27]
ds_read_b128 v[40:43],v4 offset:40960
v_mfma_f32_16x16x32_bf16 v[24:27],a[200:203],v[52:55],v[24:27]
buffer_load_dwordx4 v2,s[32:35],s63 offen offset:1024 sc1 lds
s_add_u32 s63,s63,s31
v_mfma_f32_16x16x32_bf16 v[28:31],a[204:207],v[52:55],v[28:31]
ds_read_b128 v[44:47],v4 offset:55296
s_waitcnt lgkmcnt(3)
v_mfma_f32_16x16x32_bf16 v[28:31],a[92:95],v[48:51],v[28:31]
v_add_f32 v20,v20,v12
v_mfma_f32_16x16x32_bf16 v[28:31],v[124:127],v[56:59],v[28:31]
ds_read_b128 v[48:51],v4 offset:13312
v_add_f32 v21,v21,v13
v_mfma_f32_16x16x32_bf16 v[28:31],v[236:239],v[60:63],v[28:31]
v_add_f32 v22,v22,v14
v_add_f32 v23,v23,v15
v_mfma_f32_16x16x32_bf16 v[24:27],v[232:235],v[60:63],v[24:27]
ds_read_b128 v[52:55],v4 offset:27648
v_cvt_pk_bf16_f32 v12,v16,v17
v_mfma_f32_16x16x32_bf16 v[24:27],v[120:123],v[56:59],v[24:27]
v_cvt_pk_bf16_f32 v13,v18,v19
s_waitcnt lgkmcnt(4)
v_mfma_f32_16x16x32_bf16 v[24:27],a[96:99],v[32:35],v[24:27]
ds_read_b128 v[56:59],v4 offset:41984
v_cvt_pk_bf16_f32 v14,v20,v21
v_mfma_f32_16x16x32_bf16 v[24:27],a[208:211],v[36:39],v[24:27]
v_cvt_pk_bf16_f32 v15,v22,v23
v_mfma_f32_16x16x32_bf16 v[28:31],a[212:215],v[36:39],v[28:31]
ds_read_b128 v[60:63],v4 offset:56320
v_mfma_f32_16x16x32_bf16 v[28:31],a[100:103],v[32:35],v[28:31]
s_waitcnt lgkmcnt(4)
s_waitcnt vmcnt(10)
s_barrier
v_mfma_f32_16x16x32_bf16 v[28:31],v[132:135],v[40:43],v[28:31]
ds_read_b128 v[32:35],v2 offset:0
v_mfma_f32_16x16x32_bf16 v[28:31],v[244:247],v[44:47],v[28:31]
buffer_store_dwordx4 v[12:15],v2,s[32:35],s69 offen sc1
v_mfma_f32_16x16x32_bf16 v[24:27],v[240:243],v[44:47],v[24:27]
ds_read_b128 v[36:39],v2 offset:14336
v_mfma_f32_16x16x32_bf16 v[24:27],v[128:131],v[40:43],v[24:27]
s_waitcnt lgkmcnt(4)
v_mfma_f32_16x16x32_bf16 v[24:27],a[104:107],v[48:51],v[24:27]
ds_read_b128 v[40:43],v2 offset:28672
v_mfma_f32_16x16x32_bf16 v[24:27],a[216:219],v[52:55],v[24:27]
buffer_store_dwordx2 v[8:9],v2,s[92:95],s67 idxen sc1
v_mfma_f32_16x16x32_bf16 v[28:31],a[220:223],v[52:55],v[28:31]
ds_read_b128 v[44:47],v2 offset:43008
s_mov_b32 s67,s69
v_mfma_f32_16x16x32_bf16 v[28:31],a[108:111],v[48:51],v[28:31]
s_mov_b32 s69,s70
s_waitcnt lgkmcnt(4)
v_mfma_f32_16x16x32_bf16 v[28:31],v[140:143],v[56:59],v[28:31]
ds_read_b128 v[48:51],v2 offset:1024
s_add_u32 s70,s70,1187840
v_mfma_f32_16x16x32_bf16 v[28:31],v[252:255],v[60:63],v[28:31]
v_mfma_f32_16x16x32_bf16 v[24:27],v[248:251],v[60:63],v[24:27]
ds_read_b128 v[52:55],v2 offset:15360
v_mfma_f32_16x16x32_bf16 v[24:27],v[136:139],v[56:59],v[24:27]
s_branch .L_dal_0
.L_dab_0:
v_dot2_f32_bf16 v10,v12,s72,0
v_dot2_f32_bf16 v11,v12,v12,0
v_dot2_f32_bf16 v10,v13,s72,v10
v_dot2_f32_bf16 v11,v13,v13,v11
v_dot2_f32_bf16 v10,v14,s72,v10
v_dot2_f32_bf16 v11,v14,v14,v11
v_dot2_f32_bf16 v10,v15,s72,v10
v_dot2_f32_bf16 v11,v15,v15,v11
s_nop 2
v_permlane16_swap_b32 v10,v11
v_add_f32 v10,v10,v11
s_nop 1
v_permlane32_swap_b32 v10,v11
v_add_f32 v10,v10,v11
buffer_store_dword v10,v3,s[32:35],s67 offen offset:1024 sc1
s_mov_b32 s62,s58
s_mov_b32 s70,s59
s_add_u32 s74,s74,8192
v_lshrrev_b32 v5,8,v2
v_lshl_add_u32 v5,v5,4,s74
v_and_b32 v3,128,v2
v_lshl_add_u32 v5,v3,2,v5
v_and_b32 v3,16,v2
v_lshl_add_u32 v5,v3,4,v5
v_and_b32 v3,96,v2
v_lshl_add_u32 v5,v3,1,v5
v_lshrrev_b32 v3,2,v2
v_add_u32 v3,s74,v3
s_add_u32 s66,s66,2375680
s_add_u32 s63,s63,2375680
s_add_u32 s71,s71,2375680
s_mov_b32 s68,98304
s_mov_b32 s34,79446016
.p2align 6
.L_dbl_0:
s_waitcnt lgkmcnt(4)
v_mfma_f32_16x16x32_bf16 v[24:27],a[0:3],v[32:35],0
ds_read_b128 v[56:59],v4 offset:29696
v_mfma_f32_16x16x32_bf16 v[24:27],a[112:115],v[36:39],v[24:27]
v_mfma_f32_16x16x32_bf16 v[28:31],a[116:119],v[36:39],0
ds_read_b128 v[60:63],v4 offset:44032
v_mfma_f32_16x16x32_bf16 v[28:31],a[4:7],v[32:35],v[28:31]
v_cvt_pk_bf16_f32 v15,v22,v23
s_waitcnt lgkmcnt(4)
v_mfma_f32_16x16x32_bf16 v[28:31],a[228:231],v[40:43],v[28:31]
ds_read_b128 v[32:35],v4 offset:2048
v_cvt_pk_bf16_f32 v14,v20,v21
v_mfma_f32_16x16x32_bf16 v[28:31],v[148:151],v[44:47],v[28:31]
v_cvt_pk_bf16_f32 v13,v18,v19
v_cvt_pk_bf16_f32 v12,v16,v17
v_mfma_f32_16x16x32_bf16 v[24:27],v[144:147],v[44:47],v[24:27]
ds_read_b128 v[36:39],v4 offset:16384
ds_write_b128 v5,v[12:15]
v_mfma_f32_16x16x32_bf16 v[24:27],a[224:227],v[40:43],v[24:27]
ds_read2st64_b32 v[12:13],v3 offset0:0 offset1:1
s_waitcnt lgkmcnt(6)
v_mfma_f32_16x16x32_bf16 v[24:27],a[8:11],v[48:51],v[24:27]
ds_read_b128 v[40:43],v4 offset:30720
ds_read2st64_b32 v[14:15],v3 offset0:2 offset1:3
v_mfma_f32_16x16x32_bf16 v[24:27],a[120:123],v[52:55],v[24:27]
v_mfma_f32_16x16x32_bf16 v[28:31],a[124:127],v[52:55],v[28:31]
ds_read_b128 v[44:47],v4 offset:45056
v_mfma_f32_16x16x32_bf16 v[28:31],a[12:15],v[48:51],v[28:31]
s_waitcnt lgkmcnt(7)
v_mfma_f32_16x16x32_bf16 v[28:31],a[236:239],v[56:59],v[28:31]
ds_read_b128 v[48:51],v4 offset:3072
v_mfma_f32_16x16x32_bf16 v[28:31],v[156:159],v[60:63],v[28:31]
s_waitcnt lgkmcnt(2)
buffer_atomic_pk_add_bf16 v12,v1,s[44:47],s65 offen offset:0 sc1
v_mfma_f32_16x16x32_bf16 v[24:27],v[152:155],v[60:63],v[24:27]
ds_read_b128 v[52:55],v4 offset:17408
s_add_u32 s70,s70,s75
v_mfma_f32_16x16x32_bf16 v[24:27],a[232:235],v[56:59],v[24:27]
buffer_atomic_pk_add_bf16 v13,v1,s[44:47],s65 offen offset:2048 sc1
v_mfma_f32_16x16x32_bf16 v[24:27],a[16:19],v[32:35],v[24:27]
ds_read_b128 v[56:59],v4 offset:31744
s_add_u32 s65,s65,16384
v_mfma_f32_16x16x32_bf16 v[24:27],a[128:131],v[36:39],v[24:27]
buffer_atomic_pk_add_bf16 v14,v1,s[44:47],s65 offen offset:0 sc1
v_mfma_f32_16x16x32_bf16 v[28:31],a[132:135],v[36:39],v[28:31]
ds_read_b128 v[60:63],v4 offset:46080
v_mfma_f32_16x16x32_bf16 v[28:31],a[20:23],v[32:35],v[28:31]
s_waitcnt lgkmcnt(4)
s_waitcnt vmcnt(3)
s_barrier
v_mfma_f32_16x16x32_bf16 v[28:31],a[244:247],v[40:43],v[28:31]
ds_read_b128 v[32:35],v4 offset:4096
s_add_u32 m0,s64,0
v_mfma_f32_16x16x32_bf16 v[28:31],v[164:167],v[44:47],v[28:31]
buffer_atomic_pk_add_bf16 v15,v1,s[44:47],s65 offen offset:2048 sc1
s_and_b32 s65,s70,0x3f8000
v_mfma_f32_16x16x32_bf16 v[24:27],v[160:163],v[44:47],v[24:27]
ds_read_b128 v[36:39],v4 offset:18432
v_mfma_f32_16x16x32_bf16 v[24:27],a[240:243],v[40:43],v[24:27]
s_waitcnt lgkmcnt(4)
v_mfma_f32_16x16x32_bf16 v[24:27],a[24:27],v[48:51],v[24:27]
ds_read_b128 v[40:43],v4 offset:32768
v_mfma_f32_16x16x32_bf16 v[24:27],a[136:139],v[52:55],v[24:27]
buffer_load_dwordx4 v2,s[32:35],s63 offen offset:0 sc1 lds
v_mfma_f32_16x16x32_bf16 v[28:31],a[140:143],v[52:55],v[28:31]
ds_read_b128 v[44:47],v4 offset:47104
v_mfma_f32_16x16x32_bf16 v[28:31],a[28:31],v[48:51],v[28:31]
s_waitcnt lgkmcnt(4)
v_mfma_f32_16x16x32_bf16 v[28:31],a[252:255],v[56:59],v[28:31]
ds_read_b128 v[48:51],v4 offset:5120
v_mfma_f32_16x16x32_bf16 v[28:31],v[172:175],v[60:63],v[28:31]
buffer_load_dwordx4 v2,s[32:35],s63 offen offset:1024 sc1 lds
v_mfma_f32_16x16x32_bf16 v[24:27],v[168:171],v[60:63],v[24:27]
ds_read_b128 v[52:55],v4 offset:19456
v_mfma_f32_16x16x32_bf16 v[24:27],a[248:251],v[56:59],v[24:27]
s_waitcnt lgkmcnt(4)
v_mfma_f32_16x16x32_bf16 v[24:27],a[32:35],v[32:35],v[24:27]
ds_read_b128 v[56:59],v4 offset:33792
v_mfma_f32_16x16x32_bf16 v[24:27],a[144:147],v[36:39],v[24:27]
buffer_load_dwordx4 v2,s[32:35],s63 offen offset:2048 sc1 lds
v_mfma_f32_16x16x32_bf16 v[28:31],a[148:151],v[36:39],v[28:31]
ds_read_b128 v[60:63],v4 offset:48128
v_mfma_f32_16x16x32_bf16 v[28:31],a[36:39],v[32:35],v[28:31]
s_waitcnt lgkmcnt(4)
v_mfma_f32_16x16x32_bf16 v[28:31],v[68:71],v[40:43],v[28:31]
ds_read_b128 v[32:35],v4 offset:6144
v_mfma_f32_16x16x32_bf16 v[28:31],v[180:183],v[44:47],v[28:31]
buffer_load_dwordx4 v2,s[32:35],s63 offen offset:3072 sc1 lds
v_mfma_f32_16x16x32_bf16 v[24:27],v[176:179],v[44:47],v[24:27]
ds_read_b128 v[36:39],v4 offset:20480
v_mfma_f32_16x16x32_bf16 v[24:27],v[64:67],v[40:43],v[24:27]
s_waitcnt lgkmcnt(4)
v_mfma_f32_16x16x32_bf16 v[24:27],a[40:43],v[48:51],v[24:27]
ds_read_b128 v[40:43],v4 offset:34816
s_add_u32 s63,s63,4096
v_mfma_f32_16x16x32_bf16 v[24:27],a[152:155],v[52:55],v[24:27]
s_add_u32 m0,s64,4096
v_mfma_f32_16x16x32_bf16 v[28:31],a[156:159],v[52:55],v[28:31]
ds_read_b128 v[44:47],v4 offset:49152
v_mfma_f32_16x16x32_bf16 v[28:31],a[44:47],v[48:51],v[28:31]
s_waitcnt lgkmcnt(4)
v_mfma_f32_16x16x32_bf16 v[28:31],v[76:79],v[56:59],v[28:31]
ds_read_b128 v[48:51],v4 offset:7168
v_mfma_f32_16x16x32_bf16 v[28:31],v[188:191],v[60:63],v[28:31]
buffer_load_dwordx4 v2,s[32:35],s63 offen offset:0 sc1 lds
v_mfma_f32_16x16x32_bf16 v[24:27],v[184:187],v[60:63],v[24:27]
ds_read_b128 v[52:55],v4 offset:21504
v_mfma_f32_16x16x32_bf16 v[24:27],v[72:75],v[56:59],v[24:27]
s_waitcnt lgkmcnt(4)
v_mfma_f32_16x16x32_bf16 v[24:27],a[48:51],v[32:35],v[24:27]
ds_read_b128 v[56:59],v4 offset:35840
v_mfma_f32_16x16x32_bf16 v[24:27],a[160:163],v[36:39],v[24:27]
buffer_load_dwordx4 v2,s[32:35],s63 offen offset:1024 sc1 lds
v_mfma_f32_16x16x32_bf16 v[28:31],a[164:167],v[36:39],v[28:31]
ds_read_b128 v[60:63],v4 offset:50176
v_mfma_f32_16x16x32_bf16 v[28:31],a[52:55],v[32:35],v[28:31]
s_waitcnt lgkmcnt(4)
v_mfma_f32_16x16x32_bf16 v[28:31],v[84:87],v[40:43],v[28:31]
ds_read_b128 v[32:35],v4 offset:8192
v_mfma_f32_16x16x32_bf16 v[28:31],v[196:199],v[44:47],v[28:31]
buffer_load_dwordx4 v2,s[32:35],s63 offen offset:2048 sc1 lds
v_mfma_f32_16x16x32_bf16 v[24:27],v[192:195],v[44:47],v[24:27]
ds_read_b128 v[36:39],v4 offset:22528
v_mfma_f32_16x16x32_bf16 v[24:27],v[80:83],v[40:43],v[24:27]
s_waitcnt lgkmcnt(4)
v_mfma_f32_16x16x32_bf16 v[24:27],a[56:59],v[48:51],v[24:27]
ds_read_b128 v[40:43],v4 offset:36864
v_mfma_f32_16x16x32_bf16 v[24:27],a[168:171],v[52:55],v[24:27]
buffer_load_dwordx4 v2,s[32:35],s63 offen offset:3072 sc1 lds
v_mfma_f32_16x16x32_bf16 v[28:31],a[172:175],v[52:55],v[28:31]
ds_read_b128 v[44:47],v4 offset:51200
v_mfma_f32_16x16x32_bf16 v[28:31],a[60:63],v[48:51],v[28:31]
s_waitcnt lgkmcnt(4)
v_mfma_f32_16x16x32_bf16 v[28:31],v[92:95],v[56:59],v[28:31]
ds_read_b128 v[48:51],v4 offset:9216
v_mfma_f32_16x16x32_bf16 v[28:31],v[204:207],v[60:63],v[28:31]
s_add_u32 s63,s63,4096
s_add_u32 m0,s64,8192
v_mfma_f32_16x16x32_bf16 v[24:27],v[200:203],v[60:63],v[24:27]
ds_read_b128 v[52:55],v4 offset:23552
v_mfma_f32_16x16x32_bf16 v[24:27],v[88:91],v[56:59],v[24:27]
s_waitcnt lgkmcnt(4)
v_mfma_f32_16x16x32_bf16 v[24:27],a[64:67],v[32:35],v[24:27]
ds_read_b128 v[56:59],v4 offset:37888
v_mfma_f32_16x16x32_bf16 v[24:27],a[176:179],v[36:39],v[24:27]
buffer_load_dwordx4 v2,s[32:35],s63 offen offset:0 sc1 lds
v_mfma_f32_16x16x32_bf16 v[28:31],a[180:183],v[36:39],v[28:31]
ds_read_b128 v[60:63],v4 offset:52224
v_mfma_f32_16x16x32_bf16 v[28:31],a[68:71],v[32:35],v[28:31]
s_waitcnt lgkmcnt(4)
v_mfma_f32_16x16x32_bf16 v[28:31],v[100:103],v[40:43],v[28:31]
ds_read_b128 v[32:35],v4 offset:10240
v_mfma_f32_16x16x32_bf16 v[28:31],v[212:215],v[44:47],v[28:31]
buffer_load_dwordx4 v2,s[32:35],s63 offen offset:1024 sc1 lds
v_mfma_f32_16x16x32_bf16 v[24:27],v[208:211],v[44:47],v[24:27]
ds_read_b128 v[36:39],v4 offset:24576
v_mfma_f32_16x16x32_bf16 v[24:27],v[96:99],v[40:43],v[24:27]
s_waitcnt lgkmcnt(4)
v_mfma_f32_16x16x32_bf16 v[24:27],a[72:75],v[48:51],v[24:27]
ds_read_b128 v[40:43],v4 offset:38912
v_mfma_f32_16x16x32_bf16 v[24:27],a[184:187],v[52:55],v[24:27]
buffer_load_dwordx4 v2,s[32:35],s63 offen offset:2048 sc1 lds
v_mfma_f32_16x16x32_bf16 v[28:31],a[188:191],v[52:55],v[28:31]
ds_read_b128 v[44:47],v4 offset:53248
v_mfma_f32_16x16x32_bf16 v[28:31],a[76:79],v[48:51],v[28:31]
s_waitcnt lgkmcnt(4)
v_mfma_f32_16x16x32_bf16 v[28:31],v[108:111],v[56:59],v[28:31]
ds_read_b128 v[48:51],v4 offset:11264
v_mfma_f32_16x16x32_bf16 v[28:31],v[220:223],v[60:63],v[28:31]
v_mfma_f32_16x16x32_bf16 v[24:27],v[216:219],v[60:63],v[24:27]
ds_read_b128 v[52:55],v4 offset:25600
v_mfma_f32_16x16x32_bf16 v[24:27],v[104:107],v[56:59],v[24:27]
s_waitcnt lgkmcnt(4)
v_mfma_f32_16x16x32_bf16 v[24:27],a[80:83],v[32:35],v[24:27]
ds_read_b128 v[56:59],v4 offset:39936
v_mfma_f32_16x16x32_bf16 v[24:27],a[192:195],v[36:39],v[24:27]
buffer_load_dwordx4 v2,s[32:35],s63 offen offset:3072 sc1 lds
s_add_u32 s63,s63,4096
v_mfma_f32_16x16x32_bf16 v[28:31],a[196:199],v[36:39],v[28:31]
ds_read_b128 v[60:63],v4 offset:54272
s_add_u32 m0,s64,12288
v_mfma_f32_16x16x32_bf16 v[28:31],a[84:87],v[32:35],v[28:31]
s_waitcnt lgkmcnt(4)
v_mfma_f32_16x16x32_bf16 v[28:31],v[116:119],v[40:43],v[28:31]
ds_read_b128 v[32:35],v4 offset:12288
v_mfma_f32_16x16x32_bf16 v[28:31],v[228:231],v[44:47],v[28:31]
buffer_load_dwordx4 v2,s[32:35],s63 offen offset:0 sc1 lds
v_mfma_f32_16x16x32_bf16 v[24:27],v[224:227],v[44:47],v[24:27]
ds_read_b128 v[36:39],v4 offset:26624
v_mfma_f32_16x16x32_bf16 v[24:27],v[112:115],v[40:43],v[24:27]
s_waitcnt lgkmcnt(4)
v_mfma_f32_16x16x32_bf16 v[24:27],a[88:91],v[48:51],v[24:27]
ds_read_b128 v[40:43],v4 offset:40960
v_mfma_f32_16x16x32_bf16 v[24:27],a[200:203],v[52:55],v[24:27]
buffer_load_dwordx4 v2,s[32:35],s63 offen offset:1024 sc1 lds
s_add_u32 s63,s63,s31
v_mfma_f32_16x16x32_bf16 v[28:31],a[204:207],v[52:55],v[28:31]
ds_read_b128 v[44:47],v4 offset:55296
v_mfma_f32_16x16x32_bf16 v[28:31],a[92:95],v[48:51],v[28:31]
s_waitcnt lgkmcnt(4)
v_mfma_f32_16x16x32_bf16 v[28:31],v[124:127],v[56:59],v[28:31]
ds_read_b128 v[48:51],v4 offset:13312
v_mfma_f32_16x16x32_bf16 v[28:31],v[236:239],v[60:63],v[28:31]
v_mfma_f32_16x16x32_bf16 v[24:27],v[232:235],v[60:63],v[24:27]
ds_read_b128 v[52:55],v4 offset:27648
v_mfma_f32_16x16x32_bf16 v[24:27],v[120:123],v[56:59],v[24:27]
s_waitcnt lgkmcnt(4)
v_mfma_f32_16x16x32_bf16 v[24:27],a[96:99],v[32:35],v[24:27]
ds_read_b128 v[56:59],v4 offset:41984
v_mfma_f32_16x16x32_bf16 v[24:27],a[208:211],v[36:39],v[24:27]
v_mfma_f32_16x16x32_bf16 v[28:31],a[212:215],v[36:39],v[28:31]
ds_read_b128 v[60:63],v4 offset:56320
v_mfma_f32_16x16x32_bf16 v[28:31],a[100:103],v[32:35],v[28:31]
s_waitcnt lgkmcnt(4)
s_waitcnt vmcnt(10)
s_barrier
v_mfma_f32_16x16x32_bf16 v[28:31],v[132:135],v[40:43],v[28:31]
)#"
R"#(
ds_read_b128 v[32:35],v2 offset:0
v_mfma_f32_16x16x32_bf16 v[28:31],v[244:247],v[44:47],v[28:31]
v_mfma_f32_16x16x32_bf16 v[24:27],v[240:243],v[44:47],v[24:27]
ds_read_b128 v[36:39],v2 offset:14336
v_mfma_f32_16x16x32_bf16 v[24:27],v[128:131],v[40:43],v[24:27]
s_waitcnt lgkmcnt(4)
v_mfma_f32_16x16x32_bf16 v[24:27],a[104:107],v[48:51],v[24:27]
ds_read_b128 v[40:43],v2 offset:28672
v_mfma_f32_16x16x32_bf16 v[24:27],a[216:219],v[52:55],v[24:27]
buffer_store_dwordx2 v[8:9],v2,s[92:95],s67 idxen sc1
v_mfma_f32_16x16x32_bf16 v[28:31],a[220:223],v[52:55],v[28:31]
ds_read_b128 v[44:47],v2 offset:43008
v_mfma_f32_16x16x32_bf16 v[28:31],a[108:111],v[48:51],v[28:31]
s_waitcnt lgkmcnt(4)
v_mfma_f32_16x16x32_bf16 v[28:31],v[140:143],v[56:59],v[28:31]
ds_read_b128 v[48:51],v2 offset:1024
v_mfma_f32_16x16x32_bf16 v[28:31],v[252:255],v[60:63],v[28:31]
v_mfma_f32_16x16x32_bf16 v[24:27],v[248:251],v[60:63],v[24:27]
ds_read_b128 v[52:55],v2 offset:15360
s_sub_u32 s62,s62,1
v_mfma_f32_16x16x32_bf16 v[24:27],v[136:139],v[56:59],v[24:27]
s_cbranch_scc1 .L_dbt_0
s_waitcnt lgkmcnt(4)
v_mfma_f32_16x16x32_bf16 v[16:19],a[0:3],v[32:35],0
ds_read_b128 v[56:59],v2 offset:29696
v_mfma_f32_16x16x32_bf16 v[16:19],a[112:115],v[36:39],v[16:19]
v_mfma_f32_16x16x32_bf16 v[20:23],a[116:119],v[36:39],0
ds_read_b128 v[60:63],v2 offset:44032
v_mfma_f32_16x16x32_bf16 v[20:23],a[4:7],v[32:35],v[20:23]
v_cvt_pk_bf16_f32 v15,v30,v31
s_waitcnt lgkmcnt(4)
v_mfma_f32_16x16x32_bf16 v[20:23],a[228:231],v[40:43],v[20:23]
ds_read_b128 v[32:35],v2 offset:2048
v_cvt_pk_bf16_f32 v14,v28,v29
v_mfma_f32_16x16x32_bf16 v[20:23],v[148:151],v[44:47],v[20:23]
v_cvt_pk_bf16_f32 v13,v26,v27
v_cvt_pk_bf16_f32 v12,v24,v25
v_mfma_f32_16x16x32_bf16 v[16:19],v[144:147],v[44:47],v[16:19]
ds_read_b128 v[36:39],v2 offset:16384
ds_write_b128 v5,v[12:15]
v_mfma_f32_16x16x32_bf16 v[16:19],a[224:227],v[40:43],v[16:19]
ds_read2st64_b32 v[12:13],v3 offset0:0 offset1:1
s_waitcnt lgkmcnt(6)
v_mfma_f32_16x16x32_bf16 v[16:19],a[8:11],v[48:51],v[16:19]
ds_read_b128 v[40:43],v2 offset:30720
ds_read2st64_b32 v[14:15],v3 offset0:2 offset1:3
v_mfma_f32_16x16x32_bf16 v[16:19],a[120:123],v[52:55],v[16:19]
v_mfma_f32_16x16x32_bf16 v[20:23],a[124:127],v[52:55],v[20:23]
ds_read_b128 v[44:47],v2 offset:45056
v_mfma_f32_16x16x32_bf16 v[20:23],a[12:15],v[48:51],v[20:23]
s_waitcnt lgkmcnt(7)
v_mfma_f32_16x16x32_bf16 v[20:23],a[236:239],v[56:59],v[20:23]
ds_read_b128 v[48:51],v2 offset:3072
v_mfma_f32_16x16x32_bf16 v[20:23],v[156:159],v[60:63],v[20:23]
s_waitcnt lgkmcnt(2)
buffer_atomic_pk_add_bf16 v12,v1,s[44:47],s65 offen offset:0 sc1
v_mfma_f32_16x16x32_bf16 v[16:19],v[152:155],v[60:63],v[16:19]
ds_read_b128 v[52:55],v2 offset:17408
s_add_u32 s70,s70,s68
v_mfma_f32_16x16x32_bf16 v[16:19],a[232:235],v[56:59],v[16:19]
buffer_atomic_pk_add_bf16 v13,v1,s[44:47],s65 offen offset:2048 sc1
v_mfma_f32_16x16x32_bf16 v[16:19],a[16:19],v[32:35],v[16:19]
ds_read_b128 v[56:59],v2 offset:31744
s_add_u32 s65,s65,16384
v_mfma_f32_16x16x32_bf16 v[16:19],a[128:131],v[36:39],v[16:19]
buffer_atomic_pk_add_bf16 v14,v1,s[44:47],s65 offen offset:0 sc1
s_mov_b32 s68,s75
v_mfma_f32_16x16x32_bf16 v[20:23],a[132:135],v[36:39],v[20:23]
ds_read_b128 v[60:63],v2 offset:46080
v_mfma_f32_16x16x32_bf16 v[20:23],a[20:23],v[32:35],v[20:23]
s_waitcnt lgkmcnt(4)
s_waitcnt vmcnt(4)
s_barrier
v_mfma_f32_16x16x32_bf16 v[20:23],a[244:247],v[40:43],v[20:23]
ds_read_b128 v[32:35],v2 offset:4096
s_add_u32 m0,s64,57344
v_mfma_f32_16x16x32_bf16 v[20:23],v[164:167],v[44:47],v[20:23]
buffer_atomic_pk_add_bf16 v15,v1,s[44:47],s65 offen offset:2048 sc1
s_and_b32 s65,s70,0x3f8000
v_mfma_f32_16x16x32_bf16 v[16:19],v[160:163],v[44:47],v[16:19]
ds_read_b128 v[36:39],v2 offset:18432
v_mfma_f32_16x16x32_bf16 v[16:19],a[240:243],v[40:43],v[16:19]
s_waitcnt lgkmcnt(4)
v_mfma_f32_16x16x32_bf16 v[16:19],a[24:27],v[48:51],v[16:19]
ds_read_b128 v[40:43],v2 offset:32768
v_mfma_f32_16x16x32_bf16 v[16:19],a[136:139],v[52:55],v[16:19]
buffer_load_dwordx4 v2,s[32:35],s63 offen offset:0 sc1 lds
v_mfma_f32_16x16x32_bf16 v[20:23],a[140:143],v[52:55],v[20:23]
ds_read_b128 v[44:47],v2 offset:47104
v_mfma_f32_16x16x32_bf16 v[20:23],a[28:31],v[48:51],v[20:23]
s_waitcnt lgkmcnt(4)
v_mfma_f32_16x16x32_bf16 v[20:23],a[252:255],v[56:59],v[20:23]
ds_read_b128 v[48:51],v2 offset:5120
v_mfma_f32_16x16x32_bf16 v[20:23],v[172:175],v[60:63],v[20:23]
buffer_load_dwordx4 v2,s[32:35],s63 offen offset:1024 sc1 lds
v_mfma_f32_16x16x32_bf16 v[16:19],v[168:171],v[60:63],v[16:19]
ds_read_b128 v[52:55],v2 offset:19456
v_mfma_f32_16x16x32_bf16 v[16:19],a[248:251],v[56:59],v[16:19]
s_waitcnt lgkmcnt(4)
v_mfma_f32_16x16x32_bf16 v[16:19],a[32:35],v[32:35],v[16:19]
ds_read_b128 v[56:59],v2 offset:33792
v_mfma_f32_16x16x32_bf16 v[16:19],a[144:147],v[36:39],v[16:19]
buffer_load_dwordx4 v2,s[32:35],s63 offen offset:2048 sc1 lds
v_mfma_f32_16x16x32_bf16 v[20:23],a[148:151],v[36:39],v[20:23]
ds_read_b128 v[60:63],v2 offset:48128
v_mfma_f32_16x16x32_bf16 v[20:23],a[36:39],v[32:35],v[20:23]
s_waitcnt lgkmcnt(4)
v_mfma_f32_16x16x32_bf16 v[20:23],v[68:71],v[40:43],v[20:23]
ds_read_b128 v[32:35],v2 offset:6144
v_mfma_f32_16x16x32_bf16 v[20:23],v[180:183],v[44:47],v[20:23]
buffer_load_dwordx4 v2,s[32:35],s63 offen offset:3072 sc1 lds
v_mfma_f32_16x16x32_bf16 v[16:19],v[176:179],v[44:47],v[16:19]
ds_read_b128 v[36:39],v2 offset:20480
v_mfma_f32_16x16x32_bf16 v[16:19],v[64:67],v[40:43],v[16:19]
s_waitcnt lgkmcnt(4)
v_mfma_f32_16x16x32_bf16 v[16:19],a[40:43],v[48:51],v[16:19]
ds_read_b128 v[40:43],v2 offset:34816
s_add_u32 s63,s63,4096
v_mfma_f32_16x16x32_bf16 v[16:19],a[152:155],v[52:55],v[16:19]
s_add_u32 m0,s64,61440
v_mfma_f32_16x16x32_bf16 v[20:23],a[156:159],v[52:55],v[20:23]
ds_read_b128 v[44:47],v2 offset:49152
v_mfma_f32_16x16x32_bf16 v[20:23],a[44:47],v[48:51],v[20:23]
s_waitcnt lgkmcnt(4)
v_mfma_f32_16x16x32_bf16 v[20:23],v[76:79],v[56:59],v[20:23]
ds_read_b128 v[48:51],v2 offset:7168
v_mfma_f32_16x16x32_bf16 v[20:23],v[188:191],v[60:63],v[20:23]
buffer_load_dwordx4 v2,s[32:35],s63 offen offset:0 sc1 lds
v_mfma_f32_16x16x32_bf16 v[16:19],v[184:187],v[60:63],v[16:19]
ds_read_b128 v[52:55],v2 offset:21504
v_mfma_f32_16x16x32_bf16 v[16:19],v[72:75],v[56:59],v[16:19]
s_waitcnt lgkmcnt(4)
v_mfma_f32_16x16x32_bf16 v[16:19],a[48:51],v[32:35],v[16:19]
ds_read_b128 v[56:59],v2 offset:35840
v_mfma_f32_16x16x32_bf16 v[16:19],a[160:163],v[36:39],v[16:19]
buffer_load_dwordx4 v2,s[32:35],s63 offen offset:1024 sc1 lds
v_mfma_f32_16x16x32_bf16 v[20:23],a[164:167],v[36:39],v[20:23]
ds_read_b128 v[60:63],v2 offset:50176
v_mfma_f32_16x16x32_bf16 v[20:23],a[52:55],v[32:35],v[20:23]
s_waitcnt lgkmcnt(4)
v_mfma_f32_16x16x32_bf16 v[20:23],v[84:87],v[40:43],v[20:23]
ds_read_b128 v[32:35],v2 offset:8192
v_mfma_f32_16x16x32_bf16 v[20:23],v[196:199],v[44:47],v[20:23]
buffer_load_dwordx4 v2,s[32:35],s63 offen offset:2048 sc1 lds
v_mfma_f32_16x16x32_bf16 v[16:19],v[192:195],v[44:47],v[16:19]
ds_read_b128 v[36:39],v2 offset:22528
v_mfma_f32_16x16x32_bf16 v[16:19],v[80:83],v[40:43],v[16:19]
s_waitcnt lgkmcnt(4)
v_mfma_f32_16x16x32_bf16 v[16:19],a[56:59],v[48:51],v[16:19]
ds_read_b128 v[40:43],v2 offset:36864
v_mfma_f32_16x16x32_bf16 v[16:19],a[168:171],v[52:55],v[16:19]
buffer_load_dwordx4 v2,s[32:35],s63 offen offset:3072 sc1 lds
v_mfma_f32_16x16x32_bf16 v[20:23],a[172:175],v[52:55],v[20:23]
ds_read_b128 v[44:47],v2 offset:51200
v_mfma_f32_16x16x32_bf16 v[20:23],a[60:63],v[48:51],v[20:23]
s_waitcnt lgkmcnt(4)
v_mfma_f32_16x16x32_bf16 v[20:23],v[92:95],v[56:59],v[20:23]
ds_read_b128 v[48:51],v2 offset:9216
v_mfma_f32_16x16x32_bf16 v[20:23],v[204:207],v[60:63],v[20:23]
s_add_u32 s63,s63,4096
s_add_u32 m0,s64,65536
v_mfma_f32_16x16x32_bf16 v[16:19],v[200:203],v[60:63],v[16:19]
ds_read_b128 v[52:55],v2 offset:23552
v_mfma_f32_16x16x32_bf16 v[16:19],v[88:91],v[56:59],v[16:19]
s_waitcnt lgkmcnt(4)
v_mfma_f32_16x16x32_bf16 v[16:19],a[64:67],v[32:35],v[16:19]
ds_read_b128 v[56:59],v2 offset:37888
v_mfma_f32_16x16x32_bf16 v[16:19],a[176:179],v[36:39],v[16:19]
buffer_load_dwordx4 v2,s[32:35],s63 offen offset:0 sc1 lds
v_mfma_f32_16x16x32_bf16 v[20:23],a[180:183],v[36:39],v[20:23]
ds_read_b128 v[60:63],v2 offset:52224
v_mfma_f32_16x16x32_bf16 v[20:23],a[68:71],v[32:35],v[20:23]
s_waitcnt lgkmcnt(4)
v_mfma_f32_16x16x32_bf16 v[20:23],v[100:103],v[40:43],v[20:23]
ds_read_b128 v[32:35],v2 offset:10240
v_mfma_f32_16x16x32_bf16 v[20:23],v[212:215],v[44:47],v[20:23]
buffer_load_dwordx4 v2,s[32:35],s63 offen offset:1024 sc1 lds
v_mfma_f32_16x16x32_bf16 v[16:19],v[208:211],v[44:47],v[16:19]
ds_read_b128 v[36:39],v2 offset:24576
v_mfma_f32_16x16x32_bf16 v[16:19],v[96:99],v[40:43],v[16:19]
s_waitcnt lgkmcnt(4)
v_mfma_f32_16x16x32_bf16 v[16:19],a[72:75],v[48:51],v[16:19]
ds_read_b128 v[40:43],v2 offset:38912
v_mfma_f32_16x16x32_bf16 v[16:19],a[184:187],v[52:55],v[16:19]
buffer_load_dwordx4 v2,s[32:35],s63 offen offset:2048 sc1 lds
v_mfma_f32_16x16x32_bf16 v[20:23],a[188:191],v[52:55],v[20:23]
ds_read_b128 v[44:47],v2 offset:53248
v_mfma_f32_16x16x32_bf16 v[20:23],a[76:79],v[48:51],v[20:23]
s_waitcnt lgkmcnt(4)
v_mfma_f32_16x16x32_bf16 v[20:23],v[108:111],v[56:59],v[20:23]
ds_read_b128 v[48:51],v2 offset:11264
v_mfma_f32_16x16x32_bf16 v[20:23],v[220:223],v[60:63],v[20:23]
v_mfma_f32_16x16x32_bf16 v[16:19],v[216:219],v[60:63],v[16:19]
ds_read_b128 v[52:55],v2 offset:25600
v_mfma_f32_16x16x32_bf16 v[16:19],v[104:107],v[56:59],v[16:19]
s_waitcnt lgkmcnt(4)
v_mfma_f32_16x16x32_bf16 v[16:19],a[80:83],v[32:35],v[16:19]
ds_read_b128 v[56:59],v2 offset:39936
v_mfma_f32_16x16x32_bf16 v[16:19],a[192:195],v[36:39],v[16:19]
buffer_load_dwordx4 v2,s[32:35],s63 offen offset:3072 sc1 lds
s_add_u32 s63,s63,4096
v_mfma_f32_16x16x32_bf16 v[20:23],a[196:199],v[36:39],v[20:23]
ds_read_b128 v[60:63],v2 offset:54272
s_add_u32 m0,s64,69632
v_mfma_f32_16x16x32_bf16 v[20:23],a[84:87],v[32:35],v[20:23]
s_waitcnt lgkmcnt(4)
v_mfma_f32_16x16x32_bf16 v[20:23],v[116:119],v[40:43],v[20:23]
ds_read_b128 v[32:35],v2 offset:12288
v_mfma_f32_16x16x32_bf16 v[20:23],v[228:231],v[44:47],v[20:23]
buffer_load_dwordx4 v2,s[32:35],s63 offen offset:0 sc1 lds
v_mfma_f32_16x16x32_bf16 v[16:19],v[224:227],v[44:47],v[16:19]
ds_read_b128 v[36:39],v2 offset:26624
v_mfma_f32_16x16x32_bf16 v[16:19],v[112:115],v[40:43],v[16:19]
s_waitcnt lgkmcnt(4)
v_mfma_f32_16x16x32_bf16 v[16:19],a[88:91],v[48:51],v[16:19]
ds_read_b128 v[40:43],v2 offset:40960
v_mfma_f32_16x16x32_bf16 v[16:19],a[200:203],v[52:55],v[16:19]
buffer_load_dwordx4 v2,s[32:35],s63 offen offset:1024 sc1 lds
s_add_u32 s63,s63,s31
v_mfma_f32_16x16x32_bf16 v[20:23],a[204:207],v[52:55],v[20:23]
ds_read_b128 v[44:47],v2 offset:55296
v_mfma_f32_16x16x32_bf16 v[20:23],a[92:95],v[48:51],v[20:23]
s_waitcnt lgkmcnt(4)
v_mfma_f32_16x16x32_bf16 v[20:23],v[124:127],v[56:59],v[20:23]
ds_read_b128 v[48:51],v2 offset:13312
v_mfma_f32_16x16x32_bf16 v[20:23],v[236:239],v[60:63],v[20:23]
v_mfma_f32_16x16x32_bf16 v[16:19],v[232:235],v[60:63],v[16:19]
ds_read_b128 v[52:55],v2 offset:27648
v_mfma_f32_16x16x32_bf16 v[16:19],v[120:123],v[56:59],v[16:19]
s_waitcnt lgkmcnt(4)
v_mfma_f32_16x16x32_bf16 v[16:19],a[96:99],v[32:35],v[16:19]
ds_read_b128 v[56:59],v2 offset:41984
v_mfma_f32_16x16x32_bf16 v[16:19],a[208:211],v[36:39],v[16:19]
v_mfma_f32_16x16x32_bf16 v[20:23],a[212:215],v[36:39],v[20:23]
ds_read_b128 v[60:63],v2 offset:56320
v_mfma_f32_16x16x32_bf16 v[20:23],a[100:103],v[32:35],v[20:23]
s_waitcnt lgkmcnt(4)
s_waitcnt vmcnt(10)
s_barrier
v_mfma_f32_16x16x32_bf16 v[20:23],v[132:135],v[40:43],v[20:23]
ds_read_b128 v[32:35],v4 offset:0
v_mfma_f32_16x16x32_bf16 v[20:23],v[244:247],v[44:47],v[20:23]
v_mfma_f32_16x16x32_bf16 v[16:19],v[240:243],v[44:47],v[16:19]
ds_read_b128 v[36:39],v4 offset:14336
v_mfma_f32_16x16x32_bf16 v[16:19],v[128:131],v[40:43],v[16:19]
s_waitcnt lgkmcnt(4)
v_mfma_f32_16x16x32_bf16 v[16:19],a[104:107],v[48:51],v[16:19]
ds_read_b128 v[40:43],v4 offset:28672
v_mfma_f32_16x16x32_bf16 v[16:19],a[216:219],v[52:55],v[16:19]
v_mfma_f32_16x16x32_bf16 v[20:23],a[220:223],v[52:55],v[20:23]
ds_read_b128 v[44:47],v4 offset:43008
v_mfma_f32_16x16x32_bf16 v[20:23],a[108:111],v[48:51],v[20:23]
s_waitcnt lgkmcnt(4)
v_mfma_f32_16x16x32_bf16 v[20:23],v[140:143],v[56:59],v[20:23]
ds_read_b128 v[48:51],v4 offset:1024
v_mfma_f32_16x16x32_bf16 v[20:23],v[252:255],v[60:63],v[20:23]
v_mfma_f32_16x16x32_bf16 v[16:19],v[248:251],v[60:63],v[16:19]
ds_read_b128 v[52:55],v4 offset:15360
s_sub_u32 s62,s62,1
v_mfma_f32_16x16x32_bf16 v[16:19],v[136:139],v[56:59],v[16:19]
s_cbranch_scc0 .L_dbl_0
.L_dbu_0:
s_nop 1
s_nop 0
v_cvt_pk_bf16_f32 v15,v22,v23
v_cvt_pk_bf16_f32 v14,v20,v21
s_nop 1
v_cvt_pk_bf16_f32 v13,v18,v19
v_cvt_pk_bf16_f32 v12,v16,v17
ds_write_b128 v5,v[12:15]
ds_read2st64_b32 v[12:13],v3 offset0:0 offset1:1
ds_read2st64_b32 v[14:15],v3 offset0:2 offset1:3
s_waitcnt lgkmcnt(0)
buffer_atomic_pk_add_bf16 v12,v1,s[44:47],s65 offen offset:0 sc1
s_add_u32 s70,s70,s75
buffer_atomic_pk_add_bf16 v13,v1,s[44:47],s65 offen offset:2048 sc1
s_add_u32 s65,s65,16384
buffer_atomic_pk_add_bf16 v14,v1,s[44:47],s65 offen offset:0 sc1
buffer_atomic_pk_add_bf16 v15,v1,s[44:47],s65 offen offset:2048 sc1
s_and_b32 s65,s70,0x3f8000
s_or_b32 s48,s28,s27
s_or_b32 s48,s48,s26
s_cbranch_scc1 .L_end
v_mov_b32 v8,s54
v_mov_b32 v9,s55
buffer_store_dwordx2 v[8:9],off,s[32:35],0 sc1
s_branch .L_end
.L_dbt_0:
s_nop 1
s_nop 0
v_cvt_pk_bf16_f32 v15,v30,v31
v_cvt_pk_bf16_f32 v14,v28,v29
s_nop 1
v_cvt_pk_bf16_f32 v13,v26,v27
v_cvt_pk_bf16_f32 v12,v24,v25
ds_write_b128 v5,v[12:15]
ds_read2st64_b32 v[12:13],v3 offset0:0 offset1:1
ds_read2st64_b32 v[14:15],v3 offset0:2 offset1:3
s_waitcnt lgkmcnt(0)
buffer_atomic_pk_add_bf16 v12,v1,s[44:47],s65 offen offset:0 sc1
s_add_u32 s70,s70,s68
buffer_atomic_pk_add_bf16 v13,v1,s[44:47],s65 offen offset:2048 sc1
s_add_u32 s65,s65,16384
buffer_atomic_pk_add_bf16 v14,v1,s[44:47],s65 offen offset:0 sc1
s_mov_b32 s68,s75
buffer_atomic_pk_add_bf16 v15,v1,s[44:47],s65 offen offset:2048 sc1
s_and_b32 s65,s70,0x3f8000
s_or_b32 s48,s28,s27
s_or_b32 s48,s48,s26
s_cbranch_scc1 .L_end
v_mov_b32 v8,s54
v_mov_b32 v9,s55
buffer_store_dwordx2 v[8:9],off,s[32:35],0 sc1
s_branch .L_end
.p2align 6
.L_dhv1:
.L_dal_1:
s_waitcnt lgkmcnt(4)
v_mfma_f32_16x16x32_bf16 v[16:19],a[0:3],v[32:35],0
ds_read_b128 v[56:59],v2 offset:29696
v_dot2_f32_bf16 v10,v12,s72,0
v_mfma_f32_16x16x32_bf16 v[16:19],a[112:115],v[36:39],v[16:19]
v_dot2_f32_bf16 v11,v12,v12,0
v_dot2_f32_bf16 v10,v13,s72,v10
v_mfma_f32_16x16x32_bf16 v[20:23],a[116:119],v[36:39],0
ds_read_b128 v[60:63],v2 offset:44032
v_dot2_f32_bf16 v11,v13,v13,v11
v_mfma_f32_16x16x32_bf16 v[20:23],a[4:7],v[32:35],v[20:23]
v_dot2_f32_bf16 v10,v14,s72,v10
s_waitcnt lgkmcnt(4)
v_mfma_f32_16x16x32_bf16 v[20:23],a[228:231],v[40:43],v[20:23]
ds_read_b128 v[32:35],v2 offset:2048
v_dot2_f32_bf16 v11,v14,v14,v11
v_mfma_f32_16x16x32_bf16 v[20:23],v[148:151],v[44:47],v[20:23]
v_dot2_f32_bf16 v10,v15,s72,v10
v_dot2_f32_bf16 v11,v15,v15,v11
v_mfma_f32_16x16x32_bf16 v[16:19],v[144:147],v[44:47],v[16:19]
ds_read_b128 v[36:39],v2 offset:16384
v_mfma_f32_16x16x32_bf16 v[16:19],a[224:227],v[40:43],v[16:19]
v_permlane16_swap_b32 v10,v11
s_waitcnt lgkmcnt(4)
v_mfma_f32_16x16x32_bf16 v[16:19],a[8:11],v[48:51],v[16:19]
ds_read_b128 v[40:43],v2 offset:30720
v_add_f32 v10,v10,v11
v_mfma_f32_16x16x32_bf16 v[16:19],a[120:123],v[52:55],v[16:19]
v_mfma_f32_16x16x32_bf16 v[20:23],a[124:127],v[52:55],v[20:23]
ds_read_b128 v[44:47],v2 offset:45056
v_permlane32_swap_b32 v10,v11
v_mfma_f32_16x16x32_bf16 v[20:23],a[12:15],v[48:51],v[20:23]
v_add_f32 v10,v10,v11
s_waitcnt lgkmcnt(4)
v_mfma_f32_16x16x32_bf16 v[20:23],a[236:239],v[56:59],v[20:23]
ds_read_b128 v[48:51],v2 offset:3072
v_mfma_f32_16x16x32_bf16 v[20:23],v[156:159],v[60:63],v[20:23]
buffer_store_dword v10,v3,s[32:35],s67 offen offset:1024 sc1
v_mfma_f32_16x16x32_bf16 v[16:19],v[152:155],v[60:63],v[16:19]
ds_read_b128 v[52:55],v2 offset:17408
s_waitcnt vmcnt(5)
v_mfma_f32_16x16x32_bf16 v[16:19],a[232:235],v[56:59],v[16:19]
v_cmp_ne_u64 vcc,v[8:9],v[6:7]
s_waitcnt lgkmcnt(4)
v_mfma_f32_16x16x32_bf16 v[16:19],a[16:19],v[32:35],v[16:19]
ds_read_b128 v[56:59],v2 offset:31744
s_cbranch_vccnz .L_rta0_1
.L_bka0_1:
v_mfma_f32_16x16x32_bf16 v[16:19],a[128:131],v[36:39],v[16:19]
s_add_u32 s66,s66,s30
v_mfma_f32_16x16x32_bf16 v[20:23],a[132:135],v[36:39],v[20:23]
s_waitcnt vmcnt(2)
s_barrier
v_mfma_f32_16x16x32_bf16 v[20:23],a[20:23],v[32:35],v[20:23]
ds_read_b128 v[60:63],v2 offset:46080
s_waitcnt lgkmcnt(4)
v_mfma_f32_16x16x32_bf16 v[20:23],a[244:247],v[40:43],v[20:23]
ds_read_b128 v[32:35],v2 offset:4096
s_mov_b32 m0,s74
v_mfma_f32_16x16x32_bf16 v[20:23],v[164:167],v[44:47],v[20:23]
buffer_load_dwordx4 v2,s[32:35],s71 offen offset:0 sc1 lds
v_mfma_f32_16x16x32_bf16 v[16:19],v[160:163],v[44:47],v[16:19]
ds_read_b128 v[36:39],v2 offset:18432
v_mfma_f32_16x16x32_bf16 v[16:19],a[240:243],v[40:43],v[16:19]
s_waitcnt lgkmcnt(4)
v_mfma_f32_16x16x32_bf16 v[16:19],a[24:27],v[48:51],v[16:19]
ds_read_b128 v[40:43],v2 offset:32768
v_mfma_f32_16x16x32_bf16 v[16:19],a[136:139],v[52:55],v[16:19]
buffer_load_dwordx4 v2,s[32:35],s71 offen offset:1024 sc1 lds
s_add_u32 s71,s71,s30
v_mfma_f32_16x16x32_bf16 v[20:23],a[140:143],v[52:55],v[20:23]
ds_read_b128 v[44:47],v2 offset:47104
s_add_u32 m0,s64,57344
v_mfma_f32_16x16x32_bf16 v[20:23],a[28:31],v[48:51],v[20:23]
s_waitcnt lgkmcnt(4)
v_mfma_f32_16x16x32_bf16 v[20:23],a[252:255],v[56:59],v[20:23]
ds_read_b128 v[48:51],v2 offset:5120
v_mfma_f32_16x16x32_bf16 v[20:23],v[172:175],v[60:63],v[20:23]
buffer_load_dwordx4 v2,s[32:35],s63 offen offset:0 sc1 lds
v_mfma_f32_16x16x32_bf16 v[16:19],v[168:171],v[60:63],v[16:19]
ds_read_b128 v[52:55],v2 offset:19456
v_mfma_f32_16x16x32_bf16 v[16:19],a[248:251],v[56:59],v[16:19]
s_waitcnt lgkmcnt(4)
v_mfma_f32_16x16x32_bf16 v[16:19],a[32:35],v[32:35],v[16:19]
ds_read_b128 v[56:59],v2 offset:33792
v_mfma_f32_16x16x32_bf16 v[16:19],a[144:147],v[36:39],v[16:19]
buffer_load_dwordx4 v2,s[32:35],s63 offen offset:1024 sc1 lds
v_mfma_f32_16x16x32_bf16 v[20:23],a[148:151],v[36:39],v[20:23]
ds_read_b128 v[60:63],v2 offset:48128
v_mfma_f32_16x16x32_bf16 v[20:23],a[36:39],v[32:35],v[20:23]
s_waitcnt lgkmcnt(4)
v_mfma_f32_16x16x32_bf16 v[20:23],v[68:71],v[40:43],v[20:23]
ds_read_b128 v[32:35],v2 offset:6144
v_mfma_f32_16x16x32_bf16 v[20:23],v[180:183],v[44:47],v[20:23]
buffer_load_dwordx4 v2,s[32:35],s63 offen offset:2048 sc1 lds
v_mfma_f32_16x16x32_bf16 v[16:19],v[176:179],v[44:47],v[16:19]
ds_read_b128 v[36:39],v2 offset:20480
v_mfma_f32_16x16x32_bf16 v[16:19],v[64:67],v[40:43],v[16:19]
s_waitcnt lgkmcnt(4)
v_mfma_f32_16x16x32_bf16 v[16:19],a[40:43],v[48:51],v[16:19]
ds_read_b128 v[40:43],v2 offset:34816
v_mfma_f32_16x16x32_bf16 v[16:19],a[152:155],v[52:55],v[16:19]
buffer_load_dwordx4 v2,s[32:35],s63 offen offset:3072 sc1 lds
s_add_u32 s63,s63,4096
v_mfma_f32_16x16x32_bf16 v[20:23],a[156:159],v[52:55],v[20:23]
ds_read_b128 v[44:47],v2 offset:49152
s_add_u32 m0,s64,61440
v_mfma_f32_16x16x32_bf16 v[20:23],a[44:47],v[48:51],v[20:23]
s_waitcnt lgkmcnt(4)
v_mfma_f32_16x16x32_bf16 v[20:23],v[76:79],v[56:59],v[20:23]
ds_read_b128 v[48:51],v2 offset:7168
v_mfma_f32_16x16x32_bf16 v[20:23],v[188:191],v[60:63],v[20:23]
buffer_load_dwordx4 v2,s[32:35],s63 offen offset:0 sc1 lds
v_mfma_f32_16x16x32_bf16 v[16:19],v[184:187],v[60:63],v[16:19]
ds_read_b128 v[52:55],v2 offset:21504
v_mfma_f32_16x16x32_bf16 v[16:19],v[72:75],v[56:59],v[16:19]
s_waitcnt lgkmcnt(4)
v_mfma_f32_16x16x32_bf16 v[16:19],a[48:51],v[32:35],v[16:19]
ds_read_b128 v[56:59],v2 offset:35840
v_mfma_f32_16x16x32_bf16 v[16:19],a[160:163],v[36:39],v[16:19]
buffer_load_dwordx4 v2,s[32:35],s63 offen offset:1024 sc1 lds
v_mfma_f32_16x16x32_bf16 v[20:23],a[164:167],v[36:39],v[20:23]
ds_read_b128 v[60:63],v2 offset:50176
v_mfma_f32_16x16x32_bf16 v[20:23],a[52:55],v[32:35],v[20:23]
s_waitcnt lgkmcnt(4)
v_mfma_f32_16x16x32_bf16 v[20:23],v[84:87],v[40:43],v[20:23]
ds_read_b128 v[32:35],v2 offset:8192
v_mfma_f32_16x16x32_bf16 v[20:23],v[196:199],v[44:47],v[20:23]
buffer_load_dwordx4 v2,s[32:35],s63 offen offset:2048 sc1 lds
v_mfma_f32_16x16x32_bf16 v[16:19],v[192:195],v[44:47],v[16:19]
ds_read_b128 v[36:39],v2 offset:22528
v_mfma_f32_16x16x32_bf16 v[16:19],v[80:83],v[40:43],v[16:19]
s_waitcnt lgkmcnt(4)
v_mfma_f32_16x16x32_bf16 v[16:19],a[56:59],v[48:51],v[16:19]
ds_read_b128 v[40:43],v2 offset:36864
v_mfma_f32_16x16x32_bf16 v[16:19],a[168:171],v[52:55],v[16:19]
buffer_load_dwordx4 v2,s[32:35],s63 offen offset:3072 sc1 lds
v_mfma_f32_16x16x32_bf16 v[20:23],a[172:175],v[52:55],v[20:23]
ds_read_b128 v[44:47],v2 offset:51200
v_mfma_f32_16x16x32_bf16 v[20:23],a[60:63],v[48:51],v[20:23]
s_waitcnt lgkmcnt(4)
v_mfma_f32_16x16x32_bf16 v[20:23],v[92:95],v[56:59],v[20:23]
ds_read_b128 v[48:51],v2 offset:9216
v_mfma_f32_16x16x32_bf16 v[20:23],v[204:207],v[60:63],v[20:23]
s_add_u32 s63,s63,4096
s_add_u32 m0,s64,65536
v_mfma_f32_16x16x32_bf16 v[16:19],v[200:203],v[60:63],v[16:19]
ds_read_b128 v[52:55],v2 offset:23552
v_mfma_f32_16x16x32_bf16 v[16:19],v[88:91],v[56:59],v[16:19]
s_waitcnt lgkmcnt(4)
v_mfma_f32_16x16x32_bf16 v[16:19],a[64:67],v[32:35],v[16:19]
ds_read_b128 v[56:59],v2 offset:37888
v_mfma_f32_16x16x32_bf16 v[16:19],a[176:179],v[36:39],v[16:19]
buffer_load_dwordx4 v2,s[32:35],s63 offen offset:0 sc1 lds
v_mfma_f32_16x16x32_bf16 v[20:23],a[180:183],v[36:39],v[20:23]
ds_read_b128 v[60:63],v2 offset:52224
v_mfma_f32_16x16x32_bf16 v[20:23],a[68:71],v[32:35],v[20:23]
s_waitcnt lgkmcnt(4)
v_mfma_f32_16x16x32_bf16 v[20:23],v[100:103],v[40:43],v[20:23]
ds_read_b128 v[32:35],v2 offset:10240
v_mfma_f32_16x16x32_bf16 v[20:23],v[212:215],v[44:47],v[20:23]
buffer_load_dwordx4 v2,s[32:35],s63 offen offset:1024 sc1 lds
v_mfma_f32_16x16x32_bf16 v[16:19],v[208:211],v[44:47],v[16:19]
ds_read_b128 v[36:39],v2 offset:24576
v_mfma_f32_16x16x32_bf16 v[16:19],v[96:99],v[40:43],v[16:19]
s_waitcnt lgkmcnt(4)
v_mfma_f32_16x16x32_bf16 v[16:19],a[72:75],v[48:51],v[16:19]
ds_read_b128 v[40:43],v2 offset:38912
s_waitcnt vmcnt(10)
v_mfma_f32_16x16x32_bf16 v[16:19],a[184:187],v[52:55],v[16:19]
buffer_load_dwordx4 v2,s[32:35],s63 offen offset:2048 sc1 lds
ds_read_b128 v[12:15],v5 offset:0
v_mfma_f32_16x16x32_bf16 v[20:23],a[188:191],v[52:55],v[20:23]
ds_read_b128 v[44:47],v2 offset:53248
v_mfma_f32_16x16x32_bf16 v[20:23],a[76:79],v[48:51],v[20:23]
s_waitcnt lgkmcnt(5)
v_mfma_f32_16x16x32_bf16 v[20:23],v[108:111],v[56:59],v[20:23]
ds_read_b128 v[48:51],v2 offset:11264
v_mfma_f32_16x16x32_bf16 v[20:23],v[220:223],v[60:63],v[20:23]
buffer_load_dwordx2 v[6:7],v0,s[0:3],s66 offen sc1
v_mfma_f32_16x16x32_bf16 v[16:19],v[216:219],v[60:63],v[16:19]
ds_read_b128 v[52:55],v2 offset:25600
s_waitcnt lgkmcnt(3)
)#"
R"#(
v_mfma_f32_16x16x32_bf16 v[16:19],v[104:107],v[56:59],v[16:19]
v_add_f32 v24,v24,v12
v_mfma_f32_16x16x32_bf16 v[16:19],a[80:83],v[32:35],v[16:19]
ds_read_b128 v[56:59],v2 offset:39936
v_add_f32 v25,v25,v13
v_mfma_f32_16x16x32_bf16 v[16:19],a[192:195],v[36:39],v[16:19]
buffer_load_dwordx4 v2,s[32:35],s63 offen offset:3072 sc1 lds
s_add_u32 s63,s63,4096
v_mfma_f32_16x16x32_bf16 v[20:23],a[196:199],v[36:39],v[20:23]
ds_read_b128 v[60:63],v2 offset:54272
s_add_u32 m0,s64,69632
v_mfma_f32_16x16x32_bf16 v[20:23],a[84:87],v[32:35],v[20:23]
v_add_f32 v26,v26,v14
s_waitcnt lgkmcnt(4)
v_mfma_f32_16x16x32_bf16 v[20:23],v[116:119],v[40:43],v[20:23]
ds_read_b128 v[32:35],v2 offset:12288
v_add_f32 v27,v27,v15
v_mfma_f32_16x16x32_bf16 v[20:23],v[228:231],v[44:47],v[20:23]
buffer_load_dwordx4 v2,s[32:35],s63 offen offset:0 sc1 lds
ds_read_b128 v[12:15],v5 offset:1024
v_mfma_f32_16x16x32_bf16 v[16:19],v[224:227],v[44:47],v[16:19]
ds_read_b128 v[36:39],v2 offset:26624
v_mfma_f32_16x16x32_bf16 v[16:19],v[112:115],v[40:43],v[16:19]
s_waitcnt lgkmcnt(5)
v_mfma_f32_16x16x32_bf16 v[16:19],a[88:91],v[48:51],v[16:19]
ds_read_b128 v[40:43],v2 offset:40960
v_mfma_f32_16x16x32_bf16 v[16:19],a[200:203],v[52:55],v[16:19]
buffer_load_dwordx4 v2,s[32:35],s63 offen offset:1024 sc1 lds
s_add_u32 s63,s63,s31
v_mfma_f32_16x16x32_bf16 v[20:23],a[204:207],v[52:55],v[20:23]
ds_read_b128 v[44:47],v2 offset:55296
s_waitcnt lgkmcnt(3)
v_mfma_f32_16x16x32_bf16 v[20:23],a[92:95],v[48:51],v[20:23]
v_add_f32 v28,v28,v12
v_mfma_f32_16x16x32_bf16 v[20:23],v[124:127],v[56:59],v[20:23]
ds_read_b128 v[48:51],v2 offset:13312
v_add_f32 v29,v29,v13
v_mfma_f32_16x16x32_bf16 v[20:23],v[236:239],v[60:63],v[20:23]
v_add_f32 v30,v30,v14
v_add_f32 v31,v31,v15
v_mfma_f32_16x16x32_bf16 v[16:19],v[232:235],v[60:63],v[16:19]
ds_read_b128 v[52:55],v2 offset:27648
v_cvt_pk_bf16_f32 v12,v24,v25
v_mfma_f32_16x16x32_bf16 v[16:19],v[120:123],v[56:59],v[16:19]
v_cvt_pk_bf16_f32 v13,v26,v27
s_waitcnt lgkmcnt(4)
v_mfma_f32_16x16x32_bf16 v[16:19],a[96:99],v[32:35],v[16:19]
ds_read_b128 v[56:59],v2 offset:41984
v_cvt_pk_bf16_f32 v14,v28,v29
v_mfma_f32_16x16x32_bf16 v[16:19],a[208:211],v[36:39],v[16:19]
v_cvt_pk_bf16_f32 v15,v30,v31
v_mfma_f32_16x16x32_bf16 v[20:23],a[212:215],v[36:39],v[20:23]
s_waitcnt vmcnt(11)
s_barrier
v_mfma_f32_16x16x32_bf16 v[20:23],a[100:103],v[32:35],v[20:23]
ds_read_b128 v[60:63],v2 offset:56320
s_waitcnt lgkmcnt(4)
v_mfma_f32_16x16x32_bf16 v[20:23],v[132:135],v[40:43],v[20:23]
ds_read_b128 v[32:35],v4 offset:0
v_mfma_f32_16x16x32_bf16 v[20:23],v[244:247],v[44:47],v[20:23]
buffer_store_dwordx4 v[12:15],v2,s[32:35],s68 offen sc1
v_mfma_f32_16x16x32_bf16 v[16:19],v[240:243],v[44:47],v[16:19]
ds_read_b128 v[36:39],v4 offset:14336
v_mfma_f32_16x16x32_bf16 v[16:19],v[128:131],v[40:43],v[16:19]
s_waitcnt lgkmcnt(4)
v_mfma_f32_16x16x32_bf16 v[16:19],a[104:107],v[48:51],v[16:19]
ds_read_b128 v[40:43],v4 offset:28672
v_mfma_f32_16x16x32_bf16 v[16:19],a[216:219],v[52:55],v[16:19]
v_mfma_f32_16x16x32_bf16 v[20:23],a[220:223],v[52:55],v[20:23]
ds_read_b128 v[44:47],v4 offset:43008
s_mov_b32 s67,s68
v_mfma_f32_16x16x32_bf16 v[20:23],a[108:111],v[48:51],v[20:23]
s_mov_b32 s68,s70
s_waitcnt lgkmcnt(4)
v_mfma_f32_16x16x32_bf16 v[20:23],v[140:143],v[56:59],v[20:23]
ds_read_b128 v[48:51],v4 offset:1024
s_add_u32 s70,s70,1187840
v_mfma_f32_16x16x32_bf16 v[20:23],v[252:255],v[60:63],v[20:23]
v_mfma_f32_16x16x32_bf16 v[16:19],v[248:251],v[60:63],v[16:19]
ds_read_b128 v[52:55],v4 offset:15360
s_sub_u32 s62,s62,1
v_mfma_f32_16x16x32_bf16 v[16:19],v[136:139],v[56:59],v[16:19]
s_cbranch_scc1 .L_dab_1
s_waitcnt lgkmcnt(4)
v_mfma_f32_16x16x32_bf16 v[24:27],a[0:3],v[32:35],0
ds_read_b128 v[56:59],v4 offset:29696
v_dot2_f32_bf16 v10,v12,s72,0
v_mfma_f32_16x16x32_bf16 v[24:27],a[112:115],v[36:39],v[24:27]
v_dot2_f32_bf16 v11,v12,v12,0
v_dot2_f32_bf16 v10,v13,s72,v10
v_mfma_f32_16x16x32_bf16 v[28:31],a[116:119],v[36:39],0
ds_read_b128 v[60:63],v4 offset:44032
v_dot2_f32_bf16 v11,v13,v13,v11
v_mfma_f32_16x16x32_bf16 v[28:31],a[4:7],v[32:35],v[28:31]
v_dot2_f32_bf16 v10,v14,s72,v10
s_waitcnt lgkmcnt(4)
v_mfma_f32_16x16x32_bf16 v[28:31],a[228:231],v[40:43],v[28:31]
ds_read_b128 v[32:35],v4 offset:2048
v_dot2_f32_bf16 v11,v14,v14,v11
v_mfma_f32_16x16x32_bf16 v[28:31],v[148:151],v[44:47],v[28:31]
v_dot2_f32_bf16 v10,v15,s72,v10
v_dot2_f32_bf16 v11,v15,v15,v11
v_mfma_f32_16x16x32_bf16 v[24:27],v[144:147],v[44:47],v[24:27]
ds_read_b128 v[36:39],v4 offset:16384
v_mfma_f32_16x16x32_bf16 v[24:27],a[224:227],v[40:43],v[24:27]
v_permlane16_swap_b32 v10,v11
s_waitcnt lgkmcnt(4)
v_mfma_f32_16x16x32_bf16 v[24:27],a[8:11],v[48:51],v[24:27]
ds_read_b128 v[40:43],v4 offset:30720
v_add_f32 v10,v10,v11
v_mfma_f32_16x16x32_bf16 v[24:27],a[120:123],v[52:55],v[24:27]
v_mfma_f32_16x16x32_bf16 v[28:31],a[124:127],v[52:55],v[28:31]
ds_read_b128 v[44:47],v4 offset:45056
v_permlane32_swap_b32 v10,v11
v_mfma_f32_16x16x32_bf16 v[28:31],a[12:15],v[48:51],v[28:31]
v_add_f32 v10,v10,v11
s_waitcnt lgkmcnt(4)
v_mfma_f32_16x16x32_bf16 v[28:31],a[236:239],v[56:59],v[28:31]
ds_read_b128 v[48:51],v4 offset:3072
v_mfma_f32_16x16x32_bf16 v[28:31],v[156:159],v[60:63],v[28:31]
buffer_store_dword v10,v3,s[32:35],s67 offen offset:1024 sc1
v_mfma_f32_16x16x32_bf16 v[24:27],v[152:155],v[60:63],v[24:27]
ds_read_b128 v[52:55],v4 offset:17408
s_waitcnt vmcnt(5)
v_mfma_f32_16x16x32_bf16 v[24:27],a[232:235],v[56:59],v[24:27]
v_cmp_ne_u64 vcc,v[8:9],v[6:7]
s_waitcnt lgkmcnt(4)
v_mfma_f32_16x16x32_bf16 v[24:27],a[16:19],v[32:35],v[24:27]
ds_read_b128 v[56:59],v4 offset:31744
s_cbranch_vccnz .L_rta1_1
.L_bka1_1:
v_mfma_f32_16x16x32_bf16 v[24:27],a[128:131],v[36:39],v[24:27]
s_add_u32 s66,s66,s30
v_mfma_f32_16x16x32_bf16 v[28:31],a[132:135],v[36:39],v[28:31]
s_waitcnt vmcnt(2)
s_barrier
v_mfma_f32_16x16x32_bf16 v[28:31],a[20:23],v[32:35],v[28:31]
ds_read_b128 v[60:63],v4 offset:46080
s_waitcnt lgkmcnt(4)
v_mfma_f32_16x16x32_bf16 v[28:31],a[244:247],v[40:43],v[28:31]
ds_read_b128 v[32:35],v4 offset:4096
s_mov_b32 m0,s74
v_mfma_f32_16x16x32_bf16 v[28:31],v[164:167],v[44:47],v[28:31]
buffer_load_dwordx4 v2,s[32:35],s71 offen offset:0 sc1 lds
v_mfma_f32_16x16x32_bf16 v[24:27],v[160:163],v[44:47],v[24:27]
ds_read_b128 v[36:39],v4 offset:18432
v_mfma_f32_16x16x32_bf16 v[24:27],a[240:243],v[40:43],v[24:27]
s_waitcnt lgkmcnt(4)
v_mfma_f32_16x16x32_bf16 v[24:27],a[24:27],v[48:51],v[24:27]
ds_read_b128 v[40:43],v4 offset:32768
v_mfma_f32_16x16x32_bf16 v[24:27],a[136:139],v[52:55],v[24:27]
buffer_load_dwordx4 v2,s[32:35],s71 offen offset:1024 sc1 lds
s_add_u32 s71,s71,s30
v_mfma_f32_16x16x32_bf16 v[28:31],a[140:143],v[52:55],v[28:31]
ds_read_b128 v[44:47],v4 offset:47104
s_add_u32 m0,s64,0
v_mfma_f32_16x16x32_bf16 v[28:31],a[28:31],v[48:51],v[28:31]
s_waitcnt lgkmcnt(4)
v_mfma_f32_16x16x32_bf16 v[28:31],a[252:255],v[56:59],v[28:31]
ds_read_b128 v[48:51],v4 offset:5120
v_mfma_f32_16x16x32_bf16 v[28:31],v[172:175],v[60:63],v[28:31]
buffer_load_dwordx4 v2,s[32:35],s63 offen offset:0 sc1 lds
v_mfma_f32_16x16x32_bf16 v[24:27],v[168:171],v[60:63],v[24:27]
ds_read_b128 v[52:55],v4 offset:19456
v_mfma_f32_16x16x32_bf16 v[24:27],a[248:251],v[56:59],v[24:27]
s_waitcnt lgkmcnt(4)
v_mfma_f32_16x16x32_bf16 v[24:27],a[32:35],v[32:35],v[24:27]
ds_read_b128 v[56:59],v4 offset:33792
v_mfma_f32_16x16x32_bf16 v[24:27],a[144:147],v[36:39],v[24:27]
buffer_load_dwordx4 v2,s[32:35],s63 offen offset:1024 sc1 lds
v_mfma_f32_16x16x32_bf16 v[28:31],a[148:151],v[36:39],v[28:31]
ds_read_b128 v[60:63],v4 offset:48128
v_mfma_f32_16x16x32_bf16 v[28:31],a[36:39],v[32:35],v[28:31]
s_waitcnt lgkmcnt(4)
v_mfma_f32_16x16x32_bf16 v[28:31],v[68:71],v[40:43],v[28:31]
ds_read_b128 v[32:35],v4 offset:6144
v_mfma_f32_16x16x32_bf16 v[28:31],v[180:183],v[44:47],v[28:31]
buffer_load_dwordx4 v2,s[32:35],s63 offen offset:2048 sc1 lds
v_mfma_f32_16x16x32_bf16 v[24:27],v[176:179],v[44:47],v[24:27]
ds_read_b128 v[36:39],v4 offset:20480
v_mfma_f32_16x16x32_bf16 v[24:27],v[64:67],v[40:43],v[24:27]
s_waitcnt lgkmcnt(4)
v_mfma_f32_16x16x32_bf16 v[24:27],a[40:43],v[48:51],v[24:27]
ds_read_b128 v[40:43],v4 offset:34816
v_mfma_f32_16x16x32_bf16 v[24:27],a[152:155],v[52:55],v[24:27]
buffer_load_dwordx4 v2,s[32:35],s63 offen offset:3072 sc1 lds
s_add_u32 s63,s63,4096
v_mfma_f32_16x16x32_bf16 v[28:31],a[156:159],v[52:55],v[28:31]
ds_read_b128 v[44:47],v4 offset:49152
s_add_u32 m0,s64,4096
v_mfma_f32_16x16x32_bf16 v[28:31],a[44:47],v[48:51],v[28:31]
s_waitcnt lgkmcnt(4)
v_mfma_f32_16x16x32_bf16 v[28:31],v[76:79],v[56:59],v[28:31]
ds_read_b128 v[48:51],v4 offset:7168
v_mfma_f32_16x16x32_bf16 v[28:31],v[188:191],v[60:63],v[28:31]
buffer_load_dwordx4 v2,s[32:35],s63 offen offset:0 sc1 lds
v_mfma_f32_16x16x32_bf16 v[24:27],v[184:187],v[60:63],v[24:27]
ds_read_b128 v[52:55],v4 offset:21504
v_mfma_f32_16x16x32_bf16 v[24:27],v[72:75],v[56:59],v[24:27]
s_waitcnt lgkmcnt(4)
v_mfma_f32_16x16x32_bf16 v[24:27],a[48:51],v[32:35],v[24:27]
ds_read_b128 v[56:59],v4 offset:35840
v_mfma_f32_16x16x32_bf16 v[24:27],a[160:163],v[36:39],v[24:27]
buffer_load_dwordx4 v2,s[32:35],s63 offen offset:1024 sc1 lds
v_mfma_f32_16x16x32_bf16 v[28:31],a[164:167],v[36:39],v[28:31]
ds_read_b128 v[60:63],v4 offset:50176
v_mfma_f32_16x16x32_bf16 v[28:31],a[52:55],v[32:35],v[28:31]
s_waitcnt lgkmcnt(4)
v_mfma_f32_16x16x32_bf16 v[28:31],v[84:87],v[40:43],v[28:31]
ds_read_b128 v[32:35],v4 offset:8192
v_mfma_f32_16x16x32_bf16 v[28:31],v[196:199],v[44:47],v[28:31]
buffer_load_dwordx4 v2,s[32:35],s63 offen offset:2048 sc1 lds
v_mfma_f32_16x16x32_bf16 v[24:27],v[192:195],v[44:47],v[24:27]
ds_read_b128 v[36:39],v4 offset:22528
v_mfma_f32_16x16x32_bf16 v[24:27],v[80:83],v[40:43],v[24:27]
s_waitcnt lgkmcnt(4)
v_mfma_f32_16x16x32_bf16 v[24:27],a[56:59],v[48:51],v[24:27]
ds_read_b128 v[40:43],v4 offset:36864
v_mfma_f32_16x16x32_bf16 v[24:27],a[168:171],v[52:55],v[24:27]
buffer_load_dwordx4 v2,s[32:35],s63 offen offset:3072 sc1 lds
v_mfma_f32_16x16x32_bf16 v[28:31],a[172:175],v[52:55],v[28:31]
ds_read_b128 v[44:47],v4 offset:51200
v_mfma_f32_16x16x32_bf16 v[28:31],a[60:63],v[48:51],v[28:31]
s_waitcnt lgkmcnt(4)
v_mfma_f32_16x16x32_bf16 v[28:31],v[92:95],v[56:59],v[28:31]
ds_read_b128 v[48:51],v4 offset:9216
v_mfma_f32_16x16x32_bf16 v[28:31],v[204:207],v[60:63],v[28:31]
s_add_u32 s63,s63,4096
s_add_u32 m0,s64,8192
v_mfma_f32_16x16x32_bf16 v[24:27],v[200:203],v[60:63],v[24:27]
ds_read_b128 v[52:55],v4 offset:23552
v_mfma_f32_16x16x32_bf16 v[24:27],v[88:91],v[56:59],v[24:27]
s_waitcnt lgkmcnt(4)
v_mfma_f32_16x16x32_bf16 v[24:27],a[64:67],v[32:35],v[24:27]
ds_read_b128 v[56:59],v4 offset:37888
v_mfma_f32_16x16x32_bf16 v[24:27],a[176:179],v[36:39],v[24:27]
buffer_load_dwordx4 v2,s[32:35],s63 offen offset:0 sc1 lds
v_mfma_f32_16x16x32_bf16 v[28:31],a[180:183],v[36:39],v[28:31]
ds_read_b128 v[60:63],v4 offset:52224
v_mfma_f32_16x16x32_bf16 v[28:31],a[68:71],v[32:35],v[28:31]
s_waitcnt lgkmcnt(4)
v_mfma_f32_16x16x32_bf16 v[28:31],v[100:103],v[40:43],v[28:31]
ds_read_b128 v[32:35],v4 offset:10240
v_mfma_f32_16x16x32_bf16 v[28:31],v[212:215],v[44:47],v[28:31]
buffer_load_dwordx4 v2,s[32:35],s63 offen offset:1024 sc1 lds
v_mfma_f32_16x16x32_bf16 v[24:27],v[208:211],v[44:47],v[24:27]
ds_read_b128 v[36:39],v4 offset:24576
v_mfma_f32_16x16x32_bf16 v[24:27],v[96:99],v[40:43],v[24:27]
s_waitcnt lgkmcnt(4)
v_mfma_f32_16x16x32_bf16 v[24:27],a[72:75],v[48:51],v[24:27]
ds_read_b128 v[40:43],v4 offset:38912
s_waitcnt vmcnt(10)
v_mfma_f32_16x16x32_bf16 v[24:27],a[184:187],v[52:55],v[24:27]
buffer_load_dwordx4 v2,s[32:35],s63 offen offset:2048 sc1 lds
ds_read_b128 v[12:15],v5 offset:0
v_mfma_f32_16x16x32_bf16 v[28:31],a[188:191],v[52:55],v[28:31]
ds_read_b128 v[44:47],v4 offset:53248
v_mfma_f32_16x16x32_bf16 v[28:31],a[76:79],v[48:51],v[28:31]
s_waitcnt lgkmcnt(5)
v_mfma_f32_16x16x32_bf16 v[28:31],v[108:111],v[56:59],v[28:31]
ds_read_b128 v[48:51],v4 offset:11264
v_mfma_f32_16x16x32_bf16 v[28:31],v[220:223],v[60:63],v[28:31]
buffer_load_dwordx2 v[6:7],v0,s[0:3],s66 offen sc1
v_mfma_f32_16x16x32_bf16 v[24:27],v[216:219],v[60:63],v[24:27]
ds_read_b128 v[52:55],v4 offset:25600
s_waitcnt lgkmcnt(3)
v_mfma_f32_16x16x32_bf16 v[24:27],v[104:107],v[56:59],v[24:27]
v_add_f32 v16,v16,v12
v_mfma_f32_16x16x32_bf16 v[24:27],a[80:83],v[32:35],v[24:27]
ds_read_b128 v[56:59],v4 offset:39936
v_add_f32 v17,v17,v13
v_mfma_f32_16x16x32_bf16 v[24:27],a[192:195],v[36:39],v[24:27]
buffer_load_dwordx4 v2,s[32:35],s63 offen offset:3072 sc1 lds
s_add_u32 s63,s63,4096
v_mfma_f32_16x16x32_bf16 v[28:31],a[196:199],v[36:39],v[28:31]
ds_read_b128 v[60:63],v4 offset:54272
s_add_u32 m0,s64,12288
v_mfma_f32_16x16x32_bf16 v[28:31],a[84:87],v[32:35],v[28:31]
v_add_f32 v18,v18,v14
s_waitcnt lgkmcnt(4)
v_mfma_f32_16x16x32_bf16 v[28:31],v[116:119],v[40:43],v[28:31]
ds_read_b128 v[32:35],v4 offset:12288
v_add_f32 v19,v19,v15
v_mfma_f32_16x16x32_bf16 v[28:31],v[228:231],v[44:47],v[28:31]
buffer_load_dwordx4 v2,s[32:35],s63 offen offset:0 sc1 lds
ds_read_b128 v[12:15],v5 offset:1024
v_mfma_f32_16x16x32_bf16 v[24:27],v[224:227],v[44:47],v[24:27]
ds_read_b128 v[36:39],v4 offset:26624
v_mfma_f32_16x16x32_bf16 v[24:27],v[112:115],v[40:43],v[24:27]
s_waitcnt lgkmcnt(5)
v_mfma_f32_16x16x32_bf16 v[24:27],a[88:91],v[48:51],v[24:27]
ds_read_b128 v[40:43],v4 offset:40960
v_mfma_f32_16x16x32_bf16 v[24:27],a[200:203],v[52:55],v[24:27]
buffer_load_dwordx4 v2,s[32:35],s63 offen offset:1024 sc1 lds
s_add_u32 s63,s63,s31
v_mfma_f32_16x16x32_bf16 v[28:31],a[204:207],v[52:55],v[28:31]
ds_read_b128 v[44:47],v4 offset:55296
s_waitcnt lgkmcnt(3)
v_mfma_f32_16x16x32_bf16 v[28:31],a[92:95],v[48:51],v[28:31]
v_add_f32 v20,v20,v12
v_mfma_f32_16x16x32_bf16 v[28:31],v[124:127],v[56:59],v[28:31]
ds_read_b128 v[48:51],v4 offset:13312
v_add_f32 v21,v21,v13
v_mfma_f32_16x16x32_bf16 v[28:31],v[236:239],v[60:63],v[28:31]
v_add_f32 v22,v22,v14
v_add_f32 v23,v23,v15
v_mfma_f32_16x16x32_bf16 v[24:27],v[232:235],v[60:63],v[24:27]
ds_read_b128 v[52:55],v4 offset:27648
v_cvt_pk_bf16_f32 v12,v16,v17
v_mfma_f32_16x16x32_bf16 v[24:27],v[120:123],v[56:59],v[24:27]
v_cvt_pk_bf16_f32 v13,v18,v19
s_waitcnt lgkmcnt(4)
v_mfma_f32_16x16x32_bf16 v[24:27],a[96:99],v[32:35],v[24:27]
ds_read_b128 v[56:59],v4 offset:41984
v_cvt_pk_bf16_f32 v14,v20,v21
v_mfma_f32_16x16x32_bf16 v[24:27],a[208:211],v[36:39],v[24:27]
v_cvt_pk_bf16_f32 v15,v22,v23
v_mfma_f32_16x16x32_bf16 v[28:31],a[212:215],v[36:39],v[28:31]
s_waitcnt vmcnt(11)
s_barrier
v_mfma_f32_16x16x32_bf16 v[28:31],a[100:103],v[32:35],v[28:31]
ds_read_b128 v[60:63],v4 offset:56320
s_waitcnt lgkmcnt(4)
v_mfma_f32_16x16x32_bf16 v[28:31],v[132:135],v[40:43],v[28:31]
ds_read_b128 v[32:35],v2 offset:0
v_mfma_f32_16x16x32_bf16 v[28:31],v[244:247],v[44:47],v[28:31]
buffer_store_dwordx4 v[12:15],v2,s[32:35],s69 offen sc1
v_mfma_f32_16x16x32_bf16 v[24:27],v[240:243],v[44:47],v[24:27]
ds_read_b128 v[36:39],v2 offset:14336
v_mfma_f32_16x16x32_bf16 v[24:27],v[128:131],v[40:43],v[24:27]
s_waitcnt lgkmcnt(4)
v_mfma_f32_16x16x32_bf16 v[24:27],a[104:107],v[48:51],v[24:27]
ds_read_b128 v[40:43],v2 offset:28672
v_mfma_f32_16x16x32_bf16 v[24:27],a[216:219],v[52:55],v[24:27]
v_mfma_f32_16x16x32_bf16 v[28:31],a[220:223],v[52:55],v[28:31]
ds_read_b128 v[44:47],v2 offset:43008
s_mov_b32 s67,s69
v_mfma_f32_16x16x32_bf16 v[28:31],a[108:111],v[48:51],v[28:31]
s_mov_b32 s69,s70
s_waitcnt lgkmcnt(4)
v_mfma_f32_16x16x32_bf16 v[28:31],v[140:143],v[56:59],v[28:31]
ds_read_b128 v[48:51],v2 offset:1024
s_add_u32 s70,s70,1187840
v_mfma_f32_16x16x32_bf16 v[28:31],v[252:255],v[60:63],v[28:31]
v_mfma_f32_16x16x32_bf16 v[24:27],v[248:251],v[60:63],v[24:27]
ds_read_b128 v[52:55],v2 offset:15360
v_mfma_f32_16x16x32_bf16 v[24:27],v[136:139],v[56:59],v[24:27]
s_branch .L_dal_1
.L_dab_1:
v_dot2_f32_bf16 v10,v12,s72,0
v_dot2_f32_bf16 v11,v12,v12,0
v_dot2_f32_bf16 v10,v13,s72,v10
v_dot2_f32_bf16 v11,v13,v13,v11
v_dot2_f32_bf16 v10,v14,s72,v10
v_dot2_f32_bf16 v11,v14,v14,v11
v_dot2_f32_bf16 v10,v15,s72,v10
v_dot2_f32_bf16 v11,v15,v15,v11
s_nop 2
v_permlane16_swap_b32 v10,v11
v_add_f32 v10,v10,v11
s_nop 1
v_permlane32_swap_b32 v10,v11
v_add_f32 v10,v10,v11
buffer_store_dword v10,v3,s[32:35],s67 offen offset:1024 sc1
s_mov_b32 s62,s58
s_mov_b32 s70,s59
s_add_u32 s74,s74,8192
v_lshrrev_b32 v5,8,v2
v_lshl_add_u32 v5,v5,4,s74
v_and_b32 v3,128,v2
v_lshl_add_u32 v5,v3,2,v5
v_and_b32 v3,16,v2
v_lshl_add_u32 v5,v3,4,v5
v_and_b32 v3,96,v2
v_lshl_add_u32 v5,v3,1,v5
v_lshrrev_b32 v3,2,v2
v_add_u32 v3,s74,v3
s_add_u32 s66,s66,2375680
s_add_u32 s63,s63,2375680
s_add_u32 s71,s71,2375680
s_mov_b32 s68,98304
s_waitcnt vmcnt(5)
v_not_b32 v6,v8
s_mov_b32 s34,79446016
.p2align 6
.L_dbl_1:
s_waitcnt lgkmcnt(4)
v_mfma_f32_16x16x32_bf16 v[24:27],a[0:3],v[32:35],0
ds_read_b128 v[56:59],v4 offset:29696
v_mfma_f32_16x16x32_bf16 v[24:27],a[112:115],v[36:39],v[24:27]
v_mfma_f32_16x16x32_bf16 v[28:31],a[116:119],v[36:39],0
ds_read_b128 v[60:63],v4 offset:44032
v_mfma_f32_16x16x32_bf16 v[28:31],a[4:7],v[32:35],v[28:31]
v_cvt_pk_bf16_f32 v15,v22,v23
s_waitcnt lgkmcnt(4)
v_mfma_f32_16x16x32_bf16 v[28:31],a[228:231],v[40:43],v[28:31]
ds_read_b128 v[32:35],v4 offset:2048
v_cvt_pk_bf16_f32 v14,v20,v21
v_mfma_f32_16x16x32_bf16 v[28:31],v[148:151],v[44:47],v[28:31]
v_cvt_pk_bf16_f32 v13,v18,v19
v_cvt_pk_bf16_f32 v12,v16,v17
v_mfma_f32_16x16x32_bf16 v[24:27],v[144:147],v[44:47],v[24:27]
ds_read_b128 v[36:39],v4 offset:16384
ds_write_b128 v5,v[12:15]
v_mfma_f32_16x16x32_bf16 v[24:27],a[224:227],v[40:43],v[24:27]
ds_read2st64_b32 v[12:13],v3 offset0:0 offset1:1
s_waitcnt lgkmcnt(6)
v_mfma_f32_16x16x32_bf16 v[24:27],a[8:11],v[48:51],v[24:27]
ds_read_b128 v[40:43],v4 offset:30720
ds_read2st64_b32 v[14:15],v3 offset0:2 offset1:3
v_mfma_f32_16x16x32_bf16 v[24:27],a[120:123],v[52:55],v[24:27]
v_mfma_f32_16x16x32_bf16 v[28:31],a[124:127],v[52:55],v[28:31]
ds_read_b128 v[44:47],v4 offset:45056
v_mfma_f32_16x16x32_bf16 v[28:31],a[12:15],v[48:51],v[28:31]
s_waitcnt lgkmcnt(7)
v_mfma_f32_16x16x32_bf16 v[28:31],a[236:239],v[56:59],v[28:31]
ds_read_b128 v[48:51],v4 offset:3072
v_mfma_f32_16x16x32_bf16 v[28:31],v[156:159],v[60:63],v[28:31]
s_waitcnt lgkmcnt(2)
buffer_atomic_pk_add_bf16 v12,v1,s[44:47],s65 offen offset:0 sc1
v_mfma_f32_16x16x32_bf16 v[24:27],v[152:155],v[60:63],v[24:27]
ds_read_b128 v[52:55],v4 offset:17408
s_waitcnt vmcnt(4)
v_mfma_f32_16x16x32_bf16 v[24:27],a[232:235],v[56:59],v[24:27]
v_cmp_ne_u64 vcc,v[8:9],v[6:7]
v_mfma_f32_16x16x32_bf16 v[24:27],a[16:19],v[32:35],v[24:27]
ds_read_b128 v[56:59],v4 offset:31744
s_cbranch_vccnz .L_rtb1_1
.L_bkb1_1:
v_mfma_f32_16x16x32_bf16 v[24:27],a[128:131],v[36:39],v[24:27]
s_add_u32 s66,s66,s30
s_add_u32 s70,s70,s75
v_mfma_f32_16x16x32_bf16 v[28:31],a[132:135],v[36:39],v[28:31]
s_waitcnt vmcnt(1)
s_barrier
v_mfma_f32_16x16x32_bf16 v[28:31],a[20:23],v[32:35],v[28:31]
buffer_atomic_pk_add_bf16 v13,v1,s[44:47],s65 offen offset:2048 sc1
s_waitcnt lgkmcnt(3)
v_mfma_f32_16x16x32_bf16 v[28:31],a[244:247],v[40:43],v[28:31]
ds_read_b128 v[60:63],v4 offset:46080
ds_read_b128 v[32:35],v4 offset:4096
v_mfma_f32_16x16x32_bf16 v[28:31],v[164:167],v[44:47],v[28:31]
s_add_u32 m0,s64,0
s_add_u32 s65,s65,16384
v_mfma_f32_16x16x32_bf16 v[24:27],v[160:163],v[44:47],v[24:27]
ds_read_b128 v[36:39],v4 offset:18432
v_mfma_f32_16x16x32_bf16 v[24:27],a[240:243],v[40:43],v[24:27]
buffer_atomic_pk_add_bf16 v14,v1,s[44:47],s65 offen offset:0 sc1
s_waitcnt lgkmcnt(4)
v_mfma_f32_16x16x32_bf16 v[24:27],a[24:27],v[48:51],v[24:27]
ds_read_b128 v[40:43],v4 offset:32768
v_mfma_f32_16x16x32_bf16 v[24:27],a[136:139],v[52:55],v[24:27]
buffer_atomic_pk_add_bf16 v15,v1,s[44:47],s65 offen offset:2048 sc1
s_and_b32 s65,s70,0x3f8000
v_mfma_f32_16x16x32_bf16 v[28:31],a[140:143],v[52:55],v[28:31]
ds_read_b128 v[44:47],v4 offset:47104
v_mfma_f32_16x16x32_bf16 v[28:31],a[28:31],v[48:51],v[28:31]
s_waitcnt lgkmcnt(4)
v_mfma_f32_16x16x32_bf16 v[28:31],a[252:255],v[56:59],v[28:31]
ds_read_b128 v[48:51],v4 offset:5120
v_mfma_f32_16x16x32_bf16 v[28:31],v[172:175],v[60:63],v[28:31]
buffer_load_dwordx4 v2,s[32:35],s63 offen offset:0 sc1 lds
v_mfma_f32_16x16x32_bf16 v[24:27],v[168:171],v[60:63],v[24:27]
ds_read_b128 v[52:55],v4 offset:19456
v_mfma_f32_16x16x32_bf16 v[24:27],a[248:251],v[56:59],v[24:27]
s_waitcnt lgkmcnt(4)
v_mfma_f32_16x16x32_bf16 v[24:27],a[32:35],v[32:35],v[24:27]
ds_read_b128 v[56:59],v4 offset:33792
v_mfma_f32_16x16x32_bf16 v[24:27],a[144:147],v[36:39],v[24:27]
buffer_load_dwordx4 v2,s[32:35],s63 offen offset:1024 sc1 lds
v_mfma_f32_16x16x32_bf16 v[28:31],a[148:151],v[36:39],v[28:31]
ds_read_b128 v[60:63],v4 offset:48128
v_mfma_f32_16x16x32_bf16 v[28:31],a[36:39],v[32:35],v[28:31]
s_waitcnt lgkmcnt(4)
v_mfma_f32_16x16x32_bf16 v[28:31],v[68:71],v[40:43],v[28:31]
ds_read_b128 v[32:35],v4 offset:6144
v_mfma_f32_16x16x32_bf16 v[28:31],v[180:183],v[44:47],v[28:31]
buffer_load_dwordx4 v2,s[32:35],s63 offen offset:2048 sc1 lds
v_mfma_f32_16x16x32_bf16 v[24:27],v[176:179],v[44:47],v[24:27]
ds_read_b128 v[36:39],v4 offset:20480
v_mfma_f32_16x16x32_bf16 v[24:27],v[64:67],v[40:43],v[24:27]
s_waitcnt lgkmcnt(4)
v_mfma_f32_16x16x32_bf16 v[24:27],a[40:43],v[48:51],v[24:27]
ds_read_b128 v[40:43],v4 offset:34816
v_mfma_f32_16x16x32_bf16 v[24:27],a[152:155],v[52:55],v[24:27]
buffer_load_dwordx4 v2,s[32:35],s63 offen offset:3072 sc1 lds
s_add_u32 s63,s63,4096
v_mfma_f32_16x16x32_bf16 v[28:31],a[156:159],v[52:55],v[28:31]
ds_read_b128 v[44:47],v4 offset:49152
s_add_u32 m0,s64,4096
v_mfma_f32_16x16x32_bf16 v[28:31],a[44:47],v[48:51],v[28:31]
s_waitcnt lgkmcnt(4)
v_mfma_f32_16x16x32_bf16 v[28:31],v[76:79],v[56:59],v[28:31]
ds_read_b128 v[48:51],v4 offset:7168
v_mfma_f32_16x16x32_bf16 v[28:31],v[188:191],v[60:63],v[28:31]
buffer_load_dwordx4 v2,s[32:35],s63 offen offset:0 sc1 lds
v_mfma_f32_16x16x32_bf16 v[24:27],v[184:187],v[60:63],v[24:27]
ds_read_b128 v[52:55],v4 offset:21504
v_mfma_f32_16x16x32_bf16 v[24:27],v[72:75],v[56:59],v[24:27]
s_waitcnt lgkmcnt(4)
)#"
R"#(
v_mfma_f32_16x16x32_bf16 v[24:27],a[48:51],v[32:35],v[24:27]
ds_read_b128 v[56:59],v4 offset:35840
v_mfma_f32_16x16x32_bf16 v[24:27],a[160:163],v[36:39],v[24:27]
buffer_load_dwordx4 v2,s[32:35],s63 offen offset:1024 sc1 lds
v_mfma_f32_16x16x32_bf16 v[28:31],a[164:167],v[36:39],v[28:31]
ds_read_b128 v[60:63],v4 offset:50176
v_mfma_f32_16x16x32_bf16 v[28:31],a[52:55],v[32:35],v[28:31]
s_waitcnt lgkmcnt(4)
v_mfma_f32_16x16x32_bf16 v[28:31],v[84:87],v[40:43],v[28:31]
ds_read_b128 v[32:35],v4 offset:8192
v_mfma_f32_16x16x32_bf16 v[28:31],v[196:199],v[44:47],v[28:31]
buffer_load_dwordx4 v2,s[32:35],s63 offen offset:2048 sc1 lds
v_mfma_f32_16x16x32_bf16 v[24:27],v[192:195],v[44:47],v[24:27]
ds_read_b128 v[36:39],v4 offset:22528
v_mfma_f32_16x16x32_bf16 v[24:27],v[80:83],v[40:43],v[24:27]
s_waitcnt lgkmcnt(4)
v_mfma_f32_16x16x32_bf16 v[24:27],a[56:59],v[48:51],v[24:27]
ds_read_b128 v[40:43],v4 offset:36864
v_mfma_f32_16x16x32_bf16 v[24:27],a[168:171],v[52:55],v[24:27]
buffer_load_dwordx4 v2,s[32:35],s63 offen offset:3072 sc1 lds
v_mfma_f32_16x16x32_bf16 v[28:31],a[172:175],v[52:55],v[28:31]
ds_read_b128 v[44:47],v4 offset:51200
v_mfma_f32_16x16x32_bf16 v[28:31],a[60:63],v[48:51],v[28:31]
s_waitcnt lgkmcnt(4)
v_mfma_f32_16x16x32_bf16 v[28:31],v[92:95],v[56:59],v[28:31]
ds_read_b128 v[48:51],v4 offset:9216
v_mfma_f32_16x16x32_bf16 v[28:31],v[204:207],v[60:63],v[28:31]
s_add_u32 s63,s63,4096
s_add_u32 m0,s64,8192
v_mfma_f32_16x16x32_bf16 v[24:27],v[200:203],v[60:63],v[24:27]
ds_read_b128 v[52:55],v4 offset:23552
v_mfma_f32_16x16x32_bf16 v[24:27],v[88:91],v[56:59],v[24:27]
s_waitcnt lgkmcnt(4)
v_mfma_f32_16x16x32_bf16 v[24:27],a[64:67],v[32:35],v[24:27]
ds_read_b128 v[56:59],v4 offset:37888
v_mfma_f32_16x16x32_bf16 v[24:27],a[176:179],v[36:39],v[24:27]
buffer_load_dwordx4 v2,s[32:35],s63 offen offset:0 sc1 lds
v_mfma_f32_16x16x32_bf16 v[28:31],a[180:183],v[36:39],v[28:31]
ds_read_b128 v[60:63],v4 offset:52224
v_mfma_f32_16x16x32_bf16 v[28:31],a[68:71],v[32:35],v[28:31]
s_waitcnt lgkmcnt(4)
v_mfma_f32_16x16x32_bf16 v[28:31],v[100:103],v[40:43],v[28:31]
ds_read_b128 v[32:35],v4 offset:10240
v_mfma_f32_16x16x32_bf16 v[28:31],v[212:215],v[44:47],v[28:31]
buffer_load_dwordx4 v2,s[32:35],s63 offen offset:1024 sc1 lds
v_mfma_f32_16x16x32_bf16 v[24:27],v[208:211],v[44:47],v[24:27]
ds_read_b128 v[36:39],v4 offset:24576
v_mfma_f32_16x16x32_bf16 v[24:27],v[96:99],v[40:43],v[24:27]
s_waitcnt lgkmcnt(4)
v_mfma_f32_16x16x32_bf16 v[24:27],a[72:75],v[48:51],v[24:27]
ds_read_b128 v[40:43],v4 offset:38912
v_mfma_f32_16x16x32_bf16 v[24:27],a[184:187],v[52:55],v[24:27]
buffer_load_dwordx4 v2,s[32:35],s63 offen offset:2048 sc1 lds
v_mfma_f32_16x16x32_bf16 v[28:31],a[188:191],v[52:55],v[28:31]
ds_read_b128 v[44:47],v4 offset:53248
v_mfma_f32_16x16x32_bf16 v[28:31],a[76:79],v[48:51],v[28:31]
s_waitcnt lgkmcnt(4)
v_mfma_f32_16x16x32_bf16 v[28:31],v[108:111],v[56:59],v[28:31]
ds_read_b128 v[48:51],v4 offset:11264
v_mfma_f32_16x16x32_bf16 v[28:31],v[220:223],v[60:63],v[28:31]
buffer_load_dwordx2 v[6:7],v0,s[0:3],s66 offen sc1
v_mfma_f32_16x16x32_bf16 v[24:27],v[216:219],v[60:63],v[24:27]
ds_read_b128 v[52:55],v4 offset:25600
v_mfma_f32_16x16x32_bf16 v[24:27],v[104:107],v[56:59],v[24:27]
s_waitcnt lgkmcnt(4)
v_mfma_f32_16x16x32_bf16 v[24:27],a[80:83],v[32:35],v[24:27]
ds_read_b128 v[56:59],v4 offset:39936
v_mfma_f32_16x16x32_bf16 v[24:27],a[192:195],v[36:39],v[24:27]
buffer_load_dwordx4 v2,s[32:35],s63 offen offset:3072 sc1 lds
s_add_u32 s63,s63,4096
v_mfma_f32_16x16x32_bf16 v[28:31],a[196:199],v[36:39],v[28:31]
ds_read_b128 v[60:63],v4 offset:54272
s_add_u32 m0,s64,12288
v_mfma_f32_16x16x32_bf16 v[28:31],a[84:87],v[32:35],v[28:31]
s_waitcnt lgkmcnt(4)
v_mfma_f32_16x16x32_bf16 v[28:31],v[116:119],v[40:43],v[28:31]
ds_read_b128 v[32:35],v4 offset:12288
v_mfma_f32_16x16x32_bf16 v[28:31],v[228:231],v[44:47],v[28:31]
buffer_load_dwordx4 v2,s[32:35],s63 offen offset:0 sc1 lds
v_mfma_f32_16x16x32_bf16 v[24:27],v[224:227],v[44:47],v[24:27]
ds_read_b128 v[36:39],v4 offset:26624
v_mfma_f32_16x16x32_bf16 v[24:27],v[112:115],v[40:43],v[24:27]
s_waitcnt lgkmcnt(4)
v_mfma_f32_16x16x32_bf16 v[24:27],a[88:91],v[48:51],v[24:27]
ds_read_b128 v[40:43],v4 offset:40960
v_mfma_f32_16x16x32_bf16 v[24:27],a[200:203],v[52:55],v[24:27]
buffer_load_dwordx4 v2,s[32:35],s63 offen offset:1024 sc1 lds
s_add_u32 s63,s63,s31
v_mfma_f32_16x16x32_bf16 v[28:31],a[204:207],v[52:55],v[28:31]
ds_read_b128 v[44:47],v4 offset:55296
v_mfma_f32_16x16x32_bf16 v[28:31],a[92:95],v[48:51],v[28:31]
s_waitcnt lgkmcnt(4)
v_mfma_f32_16x16x32_bf16 v[28:31],v[124:127],v[56:59],v[28:31]
ds_read_b128 v[48:51],v4 offset:13312
v_mfma_f32_16x16x32_bf16 v[28:31],v[236:239],v[60:63],v[28:31]
v_mfma_f32_16x16x32_bf16 v[24:27],v[232:235],v[60:63],v[24:27]
ds_read_b128 v[52:55],v4 offset:27648
v_mfma_f32_16x16x32_bf16 v[24:27],v[120:123],v[56:59],v[24:27]
s_waitcnt lgkmcnt(4)
v_mfma_f32_16x16x32_bf16 v[24:27],a[96:99],v[32:35],v[24:27]
ds_read_b128 v[56:59],v4 offset:41984
v_mfma_f32_16x16x32_bf16 v[24:27],a[208:211],v[36:39],v[24:27]
v_mfma_f32_16x16x32_bf16 v[28:31],a[212:215],v[36:39],v[28:31]
s_waitcnt vmcnt(11)
s_barrier
v_mfma_f32_16x16x32_bf16 v[28:31],a[100:103],v[32:35],v[28:31]
ds_read_b128 v[60:63],v4 offset:56320
s_waitcnt lgkmcnt(4)
v_mfma_f32_16x16x32_bf16 v[28:31],v[132:135],v[40:43],v[28:31]
ds_read_b128 v[32:35],v2 offset:0
v_mfma_f32_16x16x32_bf16 v[28:31],v[244:247],v[44:47],v[28:31]
v_mfma_f32_16x16x32_bf16 v[24:27],v[240:243],v[44:47],v[24:27]
ds_read_b128 v[36:39],v2 offset:14336
v_mfma_f32_16x16x32_bf16 v[24:27],v[128:131],v[40:43],v[24:27]
s_waitcnt lgkmcnt(4)
v_mfma_f32_16x16x32_bf16 v[24:27],a[104:107],v[48:51],v[24:27]
ds_read_b128 v[40:43],v2 offset:28672
v_mfma_f32_16x16x32_bf16 v[24:27],a[216:219],v[52:55],v[24:27]
v_mfma_f32_16x16x32_bf16 v[28:31],a[220:223],v[52:55],v[28:31]
ds_read_b128 v[44:47],v2 offset:43008
v_mfma_f32_16x16x32_bf16 v[28:31],a[108:111],v[48:51],v[28:31]
s_waitcnt lgkmcnt(4)
v_mfma_f32_16x16x32_bf16 v[28:31],v[140:143],v[56:59],v[28:31]
ds_read_b128 v[48:51],v2 offset:1024
v_mfma_f32_16x16x32_bf16 v[28:31],v[252:255],v[60:63],v[28:31]
v_mfma_f32_16x16x32_bf16 v[24:27],v[248:251],v[60:63],v[24:27]
ds_read_b128 v[52:55],v2 offset:15360
s_sub_u32 s62,s62,1
v_mfma_f32_16x16x32_bf16 v[24:27],v[136:139],v[56:59],v[24:27]
s_cbranch_scc1 .L_dbt_1
s_waitcnt lgkmcnt(4)
v_mfma_f32_16x16x32_bf16 v[16:19],a[0:3],v[32:35],0
ds_read_b128 v[56:59],v2 offset:29696
v_mfma_f32_16x16x32_bf16 v[16:19],a[112:115],v[36:39],v[16:19]
v_mfma_f32_16x16x32_bf16 v[20:23],a[116:119],v[36:39],0
ds_read_b128 v[60:63],v2 offset:44032
v_mfma_f32_16x16x32_bf16 v[20:23],a[4:7],v[32:35],v[20:23]
v_cvt_pk_bf16_f32 v15,v30,v31
s_waitcnt lgkmcnt(4)
v_mfma_f32_16x16x32_bf16 v[20:23],a[228:231],v[40:43],v[20:23]
ds_read_b128 v[32:35],v2 offset:2048
v_cvt_pk_bf16_f32 v14,v28,v29
v_mfma_f32_16x16x32_bf16 v[20:23],v[148:151],v[44:47],v[20:23]
v_cvt_pk_bf16_f32 v13,v26,v27
v_cvt_pk_bf16_f32 v12,v24,v25
v_mfma_f32_16x16x32_bf16 v[16:19],v[144:147],v[44:47],v[16:19]
ds_read_b128 v[36:39],v2 offset:16384
ds_write_b128 v5,v[12:15]
v_mfma_f32_16x16x32_bf16 v[16:19],a[224:227],v[40:43],v[16:19]
ds_read2st64_b32 v[12:13],v3 offset0:0 offset1:1
s_waitcnt lgkmcnt(6)
v_mfma_f32_16x16x32_bf16 v[16:19],a[8:11],v[48:51],v[16:19]
ds_read_b128 v[40:43],v2 offset:30720
ds_read2st64_b32 v[14:15],v3 offset0:2 offset1:3
v_mfma_f32_16x16x32_bf16 v[16:19],a[120:123],v[52:55],v[16:19]
v_mfma_f32_16x16x32_bf16 v[20:23],a[124:127],v[52:55],v[20:23]
ds_read_b128 v[44:47],v2 offset:45056
v_mfma_f32_16x16x32_bf16 v[20:23],a[12:15],v[48:51],v[20:23]
s_waitcnt lgkmcnt(7)
v_mfma_f32_16x16x32_bf16 v[20:23],a[236:239],v[56:59],v[20:23]
ds_read_b128 v[48:51],v2 offset:3072
v_mfma_f32_16x16x32_bf16 v[20:23],v[156:159],v[60:63],v[20:23]
s_waitcnt lgkmcnt(2)
buffer_atomic_pk_add_bf16 v12,v1,s[44:47],s65 offen offset:0 sc1
v_mfma_f32_16x16x32_bf16 v[16:19],v[152:155],v[60:63],v[16:19]
ds_read_b128 v[52:55],v2 offset:17408
s_waitcnt vmcnt(4)
v_mfma_f32_16x16x32_bf16 v[16:19],a[232:235],v[56:59],v[16:19]
v_cmp_ne_u64 vcc,v[8:9],v[6:7]
v_mfma_f32_16x16x32_bf16 v[16:19],a[16:19],v[32:35],v[16:19]
ds_read_b128 v[56:59],v2 offset:31744
s_cbranch_vccnz .L_rtb0_1
.L_bkb0_1:
v_mfma_f32_16x16x32_bf16 v[16:19],a[128:131],v[36:39],v[16:19]
s_add_u32 s66,s66,s30
s_add_u32 s70,s70,s68
v_mfma_f32_16x16x32_bf16 v[20:23],a[132:135],v[36:39],v[20:23]
s_waitcnt vmcnt(1)
s_barrier
v_mfma_f32_16x16x32_bf16 v[20:23],a[20:23],v[32:35],v[20:23]
buffer_atomic_pk_add_bf16 v13,v1,s[44:47],s65 offen offset:2048 sc1
s_waitcnt lgkmcnt(3)
v_mfma_f32_16x16x32_bf16 v[20:23],a[244:247],v[40:43],v[20:23]
ds_read_b128 v[60:63],v2 offset:46080
ds_read_b128 v[32:35],v2 offset:4096
v_mfma_f32_16x16x32_bf16 v[20:23],v[164:167],v[44:47],v[20:23]
s_add_u32 m0,s64,57344
s_add_u32 s65,s65,16384
v_mfma_f32_16x16x32_bf16 v[16:19],v[160:163],v[44:47],v[16:19]
ds_read_b128 v[36:39],v2 offset:18432
v_mfma_f32_16x16x32_bf16 v[16:19],a[240:243],v[40:43],v[16:19]
buffer_atomic_pk_add_bf16 v14,v1,s[44:47],s65 offen offset:0 sc1
s_waitcnt lgkmcnt(4)
v_mfma_f32_16x16x32_bf16 v[16:19],a[24:27],v[48:51],v[16:19]
ds_read_b128 v[40:43],v2 offset:32768
s_mov_b32 s68,s75
v_mfma_f32_16x16x32_bf16 v[16:19],a[136:139],v[52:55],v[16:19]
buffer_atomic_pk_add_bf16 v15,v1,s[44:47],s65 offen offset:2048 sc1
s_and_b32 s65,s70,0x3f8000
v_mfma_f32_16x16x32_bf16 v[20:23],a[140:143],v[52:55],v[20:23]
ds_read_b128 v[44:47],v2 offset:47104
v_mfma_f32_16x16x32_bf16 v[20:23],a[28:31],v[48:51],v[20:23]
s_waitcnt lgkmcnt(4)
v_mfma_f32_16x16x32_bf16 v[20:23],a[252:255],v[56:59],v[20:23]
ds_read_b128 v[48:51],v2 offset:5120
v_mfma_f32_16x16x32_bf16 v[20:23],v[172:175],v[60:63],v[20:23]
buffer_load_dwordx4 v2,s[32:35],s63 offen offset:0 sc1 lds
v_mfma_f32_16x16x32_bf16 v[16:19],v[168:171],v[60:63],v[16:19]
ds_read_b128 v[52:55],v2 offset:19456
v_mfma_f32_16x16x32_bf16 v[16:19],a[248:251],v[56:59],v[16:19]
s_waitcnt lgkmcnt(4)
v_mfma_f32_16x16x32_bf16 v[16:19],a[32:35],v[32:35],v[16:19]
ds_read_b128 v[56:59],v2 offset:33792
v_mfma_f32_16x16x32_bf16 v[16:19],a[144:147],v[36:39],v[16:19]
buffer_load_dwordx4 v2,s[32:35],s63 offen offset:1024 sc1 lds
v_mfma_f32_16x16x32_bf16 v[20:23],a[148:151],v[36:39],v[20:23]
ds_read_b128 v[60:63],v2 offset:48128
v_mfma_f32_16x16x32_bf16 v[20:23],a[36:39],v[32:35],v[20:23]
s_waitcnt lgkmcnt(4)
v_mfma_f32_16x16x32_bf16 v[20:23],v[68:71],v[40:43],v[20:23]
ds_read_b128 v[32:35],v2 offset:6144
v_mfma_f32_16x16x32_bf16 v[20:23],v[180:183],v[44:47],v[20:23]
buffer_load_dwordx4 v2,s[32:35],s63 offen offset:2048 sc1 lds
v_mfma_f32_16x16x32_bf16 v[16:19],v[176:179],v[44:47],v[16:19]
ds_read_b128 v[36:39],v2 offset:20480
v_mfma_f32_16x16x32_bf16 v[16:19],v[64:67],v[40:43],v[16:19]
s_waitcnt lgkmcnt(4)
v_mfma_f32_16x16x32_bf16 v[16:19],a[40:43],v[48:51],v[16:19]
ds_read_b128 v[40:43],v2 offset:34816
v_mfma_f32_16x16x32_bf16 v[16:19],a[152:155],v[52:55],v[16:19]
buffer_load_dwordx4 v2,s[32:35],s63 offen offset:3072 sc1 lds
s_add_u32 s63,s63,4096
v_mfma_f32_16x16x32_bf16 v[20:23],a[156:159],v[52:55],v[20:23]
ds_read_b128 v[44:47],v2 offset:49152
s_add_u32 m0,s64,61440
v_mfma_f32_16x16x32_bf16 v[20:23],a[44:47],v[48:51],v[20:23]
s_waitcnt lgkmcnt(4)
v_mfma_f32_16x16x32_bf16 v[20:23],v[76:79],v[56:59],v[20:23]
ds_read_b128 v[48:51],v2 offset:7168
v_mfma_f32_16x16x32_bf16 v[20:23],v[188:191],v[60:63],v[20:23]
buffer_load_dwordx4 v2,s[32:35],s63 offen offset:0 sc1 lds
v_mfma_f32_16x16x32_bf16 v[16:19],v[184:187],v[60:63],v[16:19]
ds_read_b128 v[52:55],v2 offset:21504
v_mfma_f32_16x16x32_bf16 v[16:19],v[72:75],v[56:59],v[16:19]
s_waitcnt lgkmcnt(4)
v_mfma_f32_16x16x32_bf16 v[16:19],a[48:51],v[32:35],v[16:19]
ds_read_b128 v[56:59],v2 offset:35840
v_mfma_f32_16x16x32_bf16 v[16:19],a[160:163],v[36:39],v[16:19]
buffer_load_dwordx4 v2,s[32:35],s63 offen offset:1024 sc1 lds
v_mfma_f32_16x16x32_bf16 v[20:23],a[164:167],v[36:39],v[20:23]
ds_read_b128 v[60:63],v2 offset:50176
v_mfma_f32_16x16x32_bf16 v[20:23],a[52:55],v[32:35],v[20:23]
s_waitcnt lgkmcnt(4)
v_mfma_f32_16x16x32_bf16 v[20:23],v[84:87],v[40:43],v[20:23]
ds_read_b128 v[32:35],v2 offset:8192
v_mfma_f32_16x16x32_bf16 v[20:23],v[196:199],v[44:47],v[20:23]
buffer_load_dwordx4 v2,s[32:35],s63 offen offset:2048 sc1 lds
v_mfma_f32_16x16x32_bf16 v[16:19],v[192:195],v[44:47],v[16:19]
ds_read_b128 v[36:39],v2 offset:22528
v_mfma_f32_16x16x32_bf16 v[16:19],v[80:83],v[40:43],v[16:19]
s_waitcnt lgkmcnt(4)
v_mfma_f32_16x16x32_bf16 v[16:19],a[56:59],v[48:51],v[16:19]
ds_read_b128 v[40:43],v2 offset:36864
v_mfma_f32_16x16x32_bf16 v[16:19],a[168:171],v[52:55],v[16:19]
buffer_load_dwordx4 v2,s[32:35],s63 offen offset:3072 sc1 lds
v_mfma_f32_16x16x32_bf16 v[20:23],a[172:175],v[52:55],v[20:23]
ds_read_b128 v[44:47],v2 offset:51200
v_mfma_f32_16x16x32_bf16 v[20:23],a[60:63],v[48:51],v[20:23]
s_waitcnt lgkmcnt(4)
v_mfma_f32_16x16x32_bf16 v[20:23],v[92:95],v[56:59],v[20:23]
ds_read_b128 v[48:51],v2 offset:9216
v_mfma_f32_16x16x32_bf16 v[20:23],v[204:207],v[60:63],v[20:23]
s_add_u32 s63,s63,4096
s_add_u32 m0,s64,65536
v_mfma_f32_16x16x32_bf16 v[16:19],v[200:203],v[60:63],v[16:19]
ds_read_b128 v[52:55],v2 offset:23552
v_mfma_f32_16x16x32_bf16 v[16:19],v[88:91],v[56:59],v[16:19]
s_waitcnt lgkmcnt(4)
v_mfma_f32_16x16x32_bf16 v[16:19],a[64:67],v[32:35],v[16:19]
ds_read_b128 v[56:59],v2 offset:37888
v_mfma_f32_16x16x32_bf16 v[16:19],a[176:179],v[36:39],v[16:19]
buffer_load_dwordx4 v2,s[32:35],s63 offen offset:0 sc1 lds
v_mfma_f32_16x16x32_bf16 v[20:23],a[180:183],v[36:39],v[20:23]
ds_read_b128 v[60:63],v2 offset:52224
v_mfma_f32_16x16x32_bf16 v[20:23],a[68:71],v[32:35],v[20:23]
s_waitcnt lgkmcnt(4)
v_mfma_f32_16x16x32_bf16 v[20:23],v[100:103],v[40:43],v[20:23]
ds_read_b128 v[32:35],v2 offset:10240
v_mfma_f32_16x16x32_bf16 v[20:23],v[212:215],v[44:47],v[20:23]
buffer_load_dwordx4 v2,s[32:35],s63 offen offset:1024 sc1 lds
v_mfma_f32_16x16x32_bf16 v[16:19],v[208:211],v[44:47],v[16:19]
ds_read_b128 v[36:39],v2 offset:24576
v_mfma_f32_16x16x32_bf16 v[16:19],v[96:99],v[40:43],v[16:19]
s_waitcnt lgkmcnt(4)
v_mfma_f32_16x16x32_bf16 v[16:19],a[72:75],v[48:51],v[16:19]
ds_read_b128 v[40:43],v2 offset:38912
v_mfma_f32_16x16x32_bf16 v[16:19],a[184:187],v[52:55],v[16:19]
buffer_load_dwordx4 v2,s[32:35],s63 offen offset:2048 sc1 lds
v_mfma_f32_16x16x32_bf16 v[20:23],a[188:191],v[52:55],v[20:23]
ds_read_b128 v[44:47],v2 offset:53248
v_mfma_f32_16x16x32_bf16 v[20:23],a[76:79],v[48:51],v[20:23]
s_waitcnt lgkmcnt(4)
v_mfma_f32_16x16x32_bf16 v[20:23],v[108:111],v[56:59],v[20:23]
ds_read_b128 v[48:51],v2 offset:11264
v_mfma_f32_16x16x32_bf16 v[20:23],v[220:223],v[60:63],v[20:23]
buffer_load_dwordx2 v[6:7],v0,s[0:3],s66 offen sc1
v_mfma_f32_16x16x32_bf16 v[16:19],v[216:219],v[60:63],v[16:19]
ds_read_b128 v[52:55],v2 offset:25600
v_mfma_f32_16x16x32_bf16 v[16:19],v[104:107],v[56:59],v[16:19]
s_waitcnt lgkmcnt(4)
v_mfma_f32_16x16x32_bf16 v[16:19],a[80:83],v[32:35],v[16:19]
ds_read_b128 v[56:59],v2 offset:39936
v_mfma_f32_16x16x32_bf16 v[16:19],a[192:195],v[36:39],v[16:19]
buffer_load_dwordx4 v2,s[32:35],s63 offen offset:3072 sc1 lds
s_add_u32 s63,s63,4096
v_mfma_f32_16x16x32_bf16 v[20:23],a[196:199],v[36:39],v[20:23]
ds_read_b128 v[60:63],v2 offset:54272
s_add_u32 m0,s64,69632
v_mfma_f32_16x16x32_bf16 v[20:23],a[84:87],v[32:35],v[20:23]
s_waitcnt lgkmcnt(4)
v_mfma_f32_16x16x32_bf16 v[20:23],v[116:119],v[40:43],v[20:23]
ds_read_b128 v[32:35],v2 offset:12288
v_mfma_f32_16x16x32_bf16 v[20:23],v[228:231],v[44:47],v[20:23]
buffer_load_dwordx4 v2,s[32:35],s63 offen offset:0 sc1 lds
v_mfma_f32_16x16x32_bf16 v[16:19],v[224:227],v[44:47],v[16:19]
ds_read_b128 v[36:39],v2 offset:26624
v_mfma_f32_16x16x32_bf16 v[16:19],v[112:115],v[40:43],v[16:19]
s_waitcnt lgkmcnt(4)
v_mfma_f32_16x16x32_bf16 v[16:19],a[88:91],v[48:51],v[16:19]
ds_read_b128 v[40:43],v2 offset:40960
v_mfma_f32_16x16x32_bf16 v[16:19],a[200:203],v[52:55],v[16:19]
buffer_load_dwordx4 v2,s[32:35],s63 offen offset:1024 sc1 lds
s_add_u32 s63,s63,s31
v_mfma_f32_16x16x32_bf16 v[20:23],a[204:207],v[52:55],v[20:23]
ds_read_b128 v[44:47],v2 offset:55296
v_mfma_f32_16x16x32_bf16 v[20:23],a[92:95],v[48:51],v[20:23]
s_waitcnt lgkmcnt(4)
v_mfma_f32_16x16x32_bf16 v[20:23],v[124:127],v[56:59],v[20:23]
ds_read_b128 v[48:51],v2 offset:13312
v_mfma_f32_16x16x32_bf16 v[20:23],v[236:239],v[60:63],v[20:23]
v_mfma_f32_16x16x32_bf16 v[16:19],v[232:235],v[60:63],v[16:19]
ds_read_b128 v[52:55],v2 offset:27648
v_mfma_f32_16x16x32_bf16 v[16:19],v[120:123],v[56:59],v[16:19]
s_waitcnt lgkmcnt(4)
v_mfma_f32_16x16x32_bf16 v[16:19],a[96:99],v[32:35],v[16:19]
ds_read_b128 v[56:59],v2 offset:41984
v_mfma_f32_16x16x32_bf16 v[16:19],a[208:211],v[36:39],v[16:19]
v_mfma_f32_16x16x32_bf16 v[20:23],a[212:215],v[36:39],v[20:23]
s_waitcnt vmcnt(11)
s_barrier
v_mfma_f32_16x16x32_bf16 v[20:23],a[100:103],v[32:35],v[20:23]
ds_read_b128 v[60:63],v2 offset:56320
s_waitcnt lgkmcnt(4)
v_mfma_f32_16x16x32_bf16 v[20:23],v[132:135],v[40:43],v[20:23]
ds_read_b128 v[32:35],v4 offset:0
v_mfma_f32_16x16x32_bf16 v[20:23],v[244:247],v[44:47],v[20:23]
v_mfma_f32_16x16x32_bf16 v[16:19],v[240:243],v[44:47],v[16:19]
ds_read_b128 v[36:39],v4 offset:14336
v_mfma_f32_16x16x32_bf16 v[16:19],v[128:131],v[40:43],v[16:19]
s_waitcnt lgkmcnt(4)
v_mfma_f32_16x16x32_bf16 v[16:19],a[104:107],v[48:51],v[16:19]
ds_read_b128 v[40:43],v4 offset:28672
v_mfma_f32_16x16x32_bf16 v[16:19],a[216:219],v[52:55],v[16:19]
v_mfma_f32_16x16x32_bf16 v[20:23],a[220:223],v[52:55],v[20:23]
ds_read_b128 v[44:47],v4 offset:43008
v_mfma_f32_16x16x32_bf16 v[20:23],a[108:111],v[48:51],v[20:23]
s_waitcnt lgkmcnt(4)
v_mfma_f32_16x16x32_bf16 v[20:23],v[140:143],v[56:59],v[20:23]
ds_read_b128 v[48:51],v4 offset:1024
v_mfma_f32_16x16x32_bf16 v[20:23],v[252:255],v[60:63],v[20:23]
v_mfma_f32_16x16x32_bf16 v[16:19],v[248:251],v[60:63],v[16:19]
ds_read_b128 v[52:55],v4 offset:15360
s_sub_u32 s62,s62,1
v_mfma_f32_16x16x32_bf16 v[16:19],v[136:139],v[56:59],v[16:19]
s_cbranch_scc0 .L_dbl_1
.L_dbu_1:
s_nop 1
s_nop 0
v_cvt_pk_bf16_f32 v15,v22,v23
v_cvt_pk_bf16_f32 v14,v20,v21
s_nop 1
v_cvt_pk_bf16_f32 v13,v18,v19
v_cvt_pk_bf16_f32 v12,v16,v17
ds_write_b128 v5,v[12:15]
ds_read2st64_b32 v[12:13],v3 offset0:0 offset1:1
ds_read2st64_b32 v[14:15],v3 offset0:2 offset1:3
s_waitcnt lgkmcnt(0)
buffer_atomic_pk_add_bf16 v12,v1,s[44:47],s65 offen offset:0 sc1
s_add_u32 s70,s70,s75
buffer_atomic_pk_add_bf16 v13,v1,s[44:47],s65 offen offset:2048 sc1
s_add_u32 s65,s65,16384
buffer_atomic_pk_add_bf16 v14,v1,s[44:47],s65 offen offset:0 sc1
buffer_atomic_pk_add_bf16 v15,v1,s[44:47],s65 offen offset:2048 sc1
s_and_b32 s65,s70,0x3f8000
s_or_b32 s48,s28,s27
s_or_b32 s48,s48,s26
s_cbranch_scc1 .L_end
v_mov_b32 v8,s54
v_mov_b32 v9,s55
buffer_store_dwordx2 v[8:9],off,s[32:35],0 sc1
s_branch .L_end
.L_dbt_1:
s_nop 1
s_nop 0
v_cvt_pk_bf16_f32 v15,v30,v31
v_cvt_pk_bf16_f32 v14,v28,v29
s_nop 1
v_cvt_pk_bf16_f32 v13,v26,v27
v_cvt_pk_bf16_f32 v12,v24,v25
ds_write_b128 v5,v[12:15]
ds_read2st64_b32 v[12:13],v3 offset0:0 offset1:1
ds_read2st64_b32 v[14:15],v3 offset0:2 offset1:3
s_waitcnt lgkmcnt(0)
buffer_atomic_pk_add_bf16 v12,v1,s[44:47],s65 offen offset:0 sc1
s_add_u32 s70,s70,s68
buffer_atomic_pk_add_bf16 v13,v1,s[44:47],s65 offen offset:2048 sc1
s_add_u32 s65,s65,16384
buffer_atomic_pk_add_bf16 v14,v1,s[44:47],s65 offen offset:0 sc1
s_mov_b32 s68,s75
buffer_atomic_pk_add_bf16 v15,v1,s[44:47],s65 offen offset:2048 sc1
s_and_b32 s65,s70,0x3f8000
s_or_b32 s48,s28,s27
s_or_b32 s48,s48,s26
s_cbranch_scc1 .L_end
v_mov_b32 v8,s54
v_mov_b32 v9,s55
buffer_store_dwordx2 v[8:9],off,s[32:35],0 sc1
s_branch .L_end
.p2align 6
.L_dhv2:
.L_dal_2:
s_waitcnt lgkmcnt(4)
v_mfma_f32_16x16x32_bf16 v[16:19],a[0:3],v[32:35],0
ds_read_b128 v[56:59],v2 offset:29696
v_dot2_f32_bf16 v10,v12,s72,0
v_mfma_f32_16x16x32_bf16 v[16:19],a[112:115],v[36:39],v[16:19]
v_dot2_f32_bf16 v11,v12,v12,0
v_dot2_f32_bf16 v10,v13,s72,v10
v_mfma_f32_16x16x32_bf16 v[20:23],a[116:119],v[36:39],0
ds_read_b128 v[60:63],v2 offset:44032
v_dot2_f32_bf16 v11,v13,v13,v11
v_mfma_f32_16x16x32_bf16 v[20:23],a[4:7],v[32:35],v[20:23]
v_dot2_f32_bf16 v10,v14,s72,v10
s_waitcnt lgkmcnt(4)
v_mfma_f32_16x16x32_bf16 v[20:23],a[228:231],v[40:43],v[20:23]
ds_read_b128 v[32:35],v2 offset:2048
v_dot2_f32_bf16 v11,v14,v14,v11
v_mfma_f32_16x16x32_bf16 v[20:23],v[148:151],v[44:47],v[20:23]
v_dot2_f32_bf16 v10,v15,s72,v10
v_dot2_f32_bf16 v11,v15,v15,v11
v_mfma_f32_16x16x32_bf16 v[16:19],v[144:147],v[44:47],v[16:19]
ds_read_b128 v[36:39],v2 offset:16384
v_mfma_f32_16x16x32_bf16 v[16:19],a[224:227],v[40:43],v[16:19]
v_permlane16_swap_b32 v10,v11
s_waitcnt lgkmcnt(4)
v_mfma_f32_16x16x32_bf16 v[16:19],a[8:11],v[48:51],v[16:19]
ds_read_b128 v[40:43],v2 offset:30720
v_add_f32 v10,v10,v11
v_mfma_f32_16x16x32_bf16 v[16:19],a[120:123],v[52:55],v[16:19]
v_mfma_f32_16x16x32_bf16 v[20:23],a[124:127],v[52:55],v[20:23]
ds_read_b128 v[44:47],v2 offset:45056
v_permlane32_swap_b32 v10,v11
v_mfma_f32_16x16x32_bf16 v[20:23],a[12:15],v[48:51],v[20:23]
v_add_f32 v10,v10,v11
s_waitcnt lgkmcnt(4)
v_mfma_f32_16x16x32_bf16 v[20:23],a[236:239],v[56:59],v[20:23]
ds_read_b128 v[48:51],v2 offset:3072
v_mfma_f32_16x16x32_bf16 v[20:23],v[156:159],v[60:63],v[20:23]
buffer_store_dword v10,v3,s[32:35],s67 offen offset:1024 sc1
v_mfma_f32_16x16x32_bf16 v[16:19],v[152:155],v[60:63],v[16:19]
ds_read_b128 v[52:55],v2 offset:17408
v_mfma_f32_16x16x32_bf16 v[16:19],a[232:235],v[56:59],v[16:19]
s_waitcnt lgkmcnt(4)
v_mfma_f32_16x16x32_bf16 v[16:19],a[16:19],v[32:35],v[16:19]
ds_read_b128 v[56:59],v2 offset:31744
v_mfma_f32_16x16x32_bf16 v[16:19],a[128:131],v[36:39],v[16:19]
s_waitcnt vmcnt(2)
s_barrier
v_mfma_f32_16x16x32_bf16 v[20:23],a[132:135],v[36:39],v[20:23]
ds_read_b128 v[60:63],v2 offset:46080
v_mfma_f32_16x16x32_bf16 v[20:23],a[20:23],v[32:35],v[20:23]
s_waitcnt lgkmcnt(4)
v_mfma_f32_16x16x32_bf16 v[20:23],a[244:247],v[40:43],v[20:23]
ds_read_b128 v[32:35],v2 offset:4096
s_mov_b32 m0,s74
v_mfma_f32_16x16x32_bf16 v[20:23],v[164:167],v[44:47],v[20:23]
buffer_load_dwordx4 v2,s[32:35],s71 offen offset:0 sc1 lds
v_mfma_f32_16x16x32_bf16 v[16:19],v[160:163],v[44:47],v[16:19]
ds_read_b128 v[36:39],v2 offset:18432
v_mfma_f32_16x16x32_bf16 v[16:19],a[240:243],v[40:43],v[16:19]
s_waitcnt lgkmcnt(4)
v_mfma_f32_16x16x32_bf16 v[16:19],a[24:27],v[48:51],v[16:19]
ds_read_b128 v[40:43],v2 offset:32768
v_mfma_f32_16x16x32_bf16 v[16:19],a[136:139],v[52:55],v[16:19]
buffer_load_dwordx4 v2,s[32:35],s71 offen offset:1024 sc1 lds
s_add_u32 s71,s71,s30
v_mfma_f32_16x16x32_bf16 v[20:23],a[140:143],v[52:55],v[20:23]
ds_read_b128 v[44:47],v2 offset:47104
s_add_u32 m0,s64,57344
v_mfma_f32_16x16x32_bf16 v[20:23],a[28:31],v[48:51],v[20:23]
)#"
R"#(
s_waitcnt lgkmcnt(4)
v_mfma_f32_16x16x32_bf16 v[20:23],a[252:255],v[56:59],v[20:23]
ds_read_b128 v[48:51],v2 offset:5120
v_mfma_f32_16x16x32_bf16 v[20:23],v[172:175],v[60:63],v[20:23]
buffer_load_dwordx4 v2,s[32:35],s63 offen offset:0 sc1 lds
v_mfma_f32_16x16x32_bf16 v[16:19],v[168:171],v[60:63],v[16:19]
ds_read_b128 v[52:55],v2 offset:19456
v_mfma_f32_16x16x32_bf16 v[16:19],a[248:251],v[56:59],v[16:19]
s_waitcnt lgkmcnt(4)
v_mfma_f32_16x16x32_bf16 v[16:19],a[32:35],v[32:35],v[16:19]
ds_read_b128 v[56:59],v2 offset:33792
v_mfma_f32_16x16x32_bf16 v[16:19],a[144:147],v[36:39],v[16:19]
buffer_load_dwordx4 v2,s[32:35],s63 offen offset:1024 sc1 lds
v_mfma_f32_16x16x32_bf16 v[20:23],a[148:151],v[36:39],v[20:23]
ds_read_b128 v[60:63],v2 offset:48128
v_mfma_f32_16x16x32_bf16 v[20:23],a[36:39],v[32:35],v[20:23]
s_waitcnt lgkmcnt(4)
v_mfma_f32_16x16x32_bf16 v[20:23],v[68:71],v[40:43],v[20:23]
ds_read_b128 v[32:35],v2 offset:6144
v_mfma_f32_16x16x32_bf16 v[20:23],v[180:183],v[44:47],v[20:23]
buffer_load_dwordx4 v2,s[32:35],s63 offen offset:2048 sc1 lds
v_mfma_f32_16x16x32_bf16 v[16:19],v[176:179],v[44:47],v[16:19]
ds_read_b128 v[36:39],v2 offset:20480
v_mfma_f32_16x16x32_bf16 v[16:19],v[64:67],v[40:43],v[16:19]
s_waitcnt lgkmcnt(4)
v_mfma_f32_16x16x32_bf16 v[16:19],a[40:43],v[48:51],v[16:19]
ds_read_b128 v[40:43],v2 offset:34816
v_mfma_f32_16x16x32_bf16 v[16:19],a[152:155],v[52:55],v[16:19]
buffer_load_dwordx4 v2,s[32:35],s63 offen offset:3072 sc1 lds
s_add_u32 s63,s63,4096
v_mfma_f32_16x16x32_bf16 v[20:23],a[156:159],v[52:55],v[20:23]
ds_read_b128 v[44:47],v2 offset:49152
s_add_u32 m0,s64,61440
v_mfma_f32_16x16x32_bf16 v[20:23],a[44:47],v[48:51],v[20:23]
s_waitcnt lgkmcnt(4)
v_mfma_f32_16x16x32_bf16 v[20:23],v[76:79],v[56:59],v[20:23]
ds_read_b128 v[48:51],v2 offset:7168
v_mfma_f32_16x16x32_bf16 v[20:23],v[188:191],v[60:63],v[20:23]
buffer_load_dwordx4 v2,s[32:35],s63 offen offset:0 sc1 lds
v_mfma_f32_16x16x32_bf16 v[16:19],v[184:187],v[60:63],v[16:19]
ds_read_b128 v[52:55],v2 offset:21504
v_mfma_f32_16x16x32_bf16 v[16:19],v[72:75],v[56:59],v[16:19]
s_waitcnt lgkmcnt(4)
v_mfma_f32_16x16x32_bf16 v[16:19],a[48:51],v[32:35],v[16:19]
ds_read_b128 v[56:59],v2 offset:35840
v_mfma_f32_16x16x32_bf16 v[16:19],a[160:163],v[36:39],v[16:19]
buffer_load_dwordx4 v2,s[32:35],s63 offen offset:1024 sc1 lds
v_mfma_f32_16x16x32_bf16 v[20:23],a[164:167],v[36:39],v[20:23]
ds_read_b128 v[60:63],v2 offset:50176
v_mfma_f32_16x16x32_bf16 v[20:23],a[52:55],v[32:35],v[20:23]
s_waitcnt lgkmcnt(4)
v_mfma_f32_16x16x32_bf16 v[20:23],v[84:87],v[40:43],v[20:23]
ds_read_b128 v[32:35],v2 offset:8192
v_mfma_f32_16x16x32_bf16 v[20:23],v[196:199],v[44:47],v[20:23]
buffer_load_dwordx4 v2,s[32:35],s63 offen offset:2048 sc1 lds
v_mfma_f32_16x16x32_bf16 v[16:19],v[192:195],v[44:47],v[16:19]
ds_read_b128 v[36:39],v2 offset:22528
v_mfma_f32_16x16x32_bf16 v[16:19],v[80:83],v[40:43],v[16:19]
s_waitcnt lgkmcnt(4)
v_mfma_f32_16x16x32_bf16 v[16:19],a[56:59],v[48:51],v[16:19]
ds_read_b128 v[40:43],v2 offset:36864
v_mfma_f32_16x16x32_bf16 v[16:19],a[168:171],v[52:55],v[16:19]
buffer_load_dwordx4 v2,s[32:35],s63 offen offset:3072 sc1 lds
v_mfma_f32_16x16x32_bf16 v[20:23],a[172:175],v[52:55],v[20:23]
ds_read_b128 v[44:47],v2 offset:51200
v_mfma_f32_16x16x32_bf16 v[20:23],a[60:63],v[48:51],v[20:23]
s_waitcnt lgkmcnt(4)
v_mfma_f32_16x16x32_bf16 v[20:23],v[92:95],v[56:59],v[20:23]
ds_read_b128 v[48:51],v2 offset:9216
v_mfma_f32_16x16x32_bf16 v[20:23],v[204:207],v[60:63],v[20:23]
s_add_u32 s63,s63,4096
s_add_u32 m0,s64,65536
v_mfma_f32_16x16x32_bf16 v[16:19],v[200:203],v[60:63],v[16:19]
ds_read_b128 v[52:55],v2 offset:23552
v_mfma_f32_16x16x32_bf16 v[16:19],v[88:91],v[56:59],v[16:19]
s_waitcnt lgkmcnt(4)
v_mfma_f32_16x16x32_bf16 v[16:19],a[64:67],v[32:35],v[16:19]
ds_read_b128 v[56:59],v2 offset:37888
v_mfma_f32_16x16x32_bf16 v[16:19],a[176:179],v[36:39],v[16:19]
buffer_load_dwordx4 v2,s[32:35],s63 offen offset:0 sc1 lds
v_mfma_f32_16x16x32_bf16 v[20:23],a[180:183],v[36:39],v[20:23]
ds_read_b128 v[60:63],v2 offset:52224
v_mfma_f32_16x16x32_bf16 v[20:23],a[68:71],v[32:35],v[20:23]
s_waitcnt lgkmcnt(4)
v_mfma_f32_16x16x32_bf16 v[20:23],v[100:103],v[40:43],v[20:23]
ds_read_b128 v[32:35],v2 offset:10240
v_mfma_f32_16x16x32_bf16 v[20:23],v[212:215],v[44:47],v[20:23]
buffer_load_dwordx4 v2,s[32:35],s63 offen offset:1024 sc1 lds
v_mfma_f32_16x16x32_bf16 v[16:19],v[208:211],v[44:47],v[16:19]
ds_read_b128 v[36:39],v2 offset:24576
v_mfma_f32_16x16x32_bf16 v[16:19],v[96:99],v[40:43],v[16:19]
s_waitcnt lgkmcnt(4)
v_mfma_f32_16x16x32_bf16 v[16:19],a[72:75],v[48:51],v[16:19]
ds_read_b128 v[40:43],v2 offset:38912
s_waitcnt vmcnt(10)
v_mfma_f32_16x16x32_bf16 v[16:19],a[184:187],v[52:55],v[16:19]
buffer_load_dwordx4 v2,s[32:35],s63 offen offset:2048 sc1 lds
ds_read_b128 v[12:15],v5 offset:0
v_mfma_f32_16x16x32_bf16 v[20:23],a[188:191],v[52:55],v[20:23]
ds_read_b128 v[44:47],v2 offset:53248
v_mfma_f32_16x16x32_bf16 v[20:23],a[76:79],v[48:51],v[20:23]
s_waitcnt lgkmcnt(5)
v_mfma_f32_16x16x32_bf16 v[20:23],v[108:111],v[56:59],v[20:23]
ds_read_b128 v[48:51],v2 offset:11264
v_mfma_f32_16x16x32_bf16 v[20:23],v[220:223],v[60:63],v[20:23]
v_mfma_f32_16x16x32_bf16 v[16:19],v[216:219],v[60:63],v[16:19]
ds_read_b128 v[52:55],v2 offset:25600
s_waitcnt lgkmcnt(3)
v_mfma_f32_16x16x32_bf16 v[16:19],v[104:107],v[56:59],v[16:19]
v_add_f32 v24,v24,v12
v_mfma_f32_16x16x32_bf16 v[16:19],a[80:83],v[32:35],v[16:19]
ds_read_b128 v[56:59],v2 offset:39936
v_add_f32 v25,v25,v13
v_mfma_f32_16x16x32_bf16 v[16:19],a[192:195],v[36:39],v[16:19]
buffer_load_dwordx4 v2,s[32:35],s63 offen offset:3072 sc1 lds
s_add_u32 s63,s63,4096
v_mfma_f32_16x16x32_bf16 v[20:23],a[196:199],v[36:39],v[20:23]
ds_read_b128 v[60:63],v2 offset:54272
s_add_u32 m0,s64,69632
v_mfma_f32_16x16x32_bf16 v[20:23],a[84:87],v[32:35],v[20:23]
v_add_f32 v26,v26,v14
s_waitcnt lgkmcnt(4)
v_mfma_f32_16x16x32_bf16 v[20:23],v[116:119],v[40:43],v[20:23]
ds_read_b128 v[32:35],v2 offset:12288
v_add_f32 v27,v27,v15
v_mfma_f32_16x16x32_bf16 v[20:23],v[228:231],v[44:47],v[20:23]
buffer_load_dwordx4 v2,s[32:35],s63 offen offset:0 sc1 lds
ds_read_b128 v[12:15],v5 offset:1024
v_mfma_f32_16x16x32_bf16 v[16:19],v[224:227],v[44:47],v[16:19]
ds_read_b128 v[36:39],v2 offset:26624
v_mfma_f32_16x16x32_bf16 v[16:19],v[112:115],v[40:43],v[16:19]
s_waitcnt lgkmcnt(5)
v_mfma_f32_16x16x32_bf16 v[16:19],a[88:91],v[48:51],v[16:19]
ds_read_b128 v[40:43],v2 offset:40960
v_mfma_f32_16x16x32_bf16 v[16:19],a[200:203],v[52:55],v[16:19]
buffer_load_dwordx4 v2,s[32:35],s63 offen offset:1024 sc1 lds
s_add_u32 s63,s63,s31
v_mfma_f32_16x16x32_bf16 v[20:23],a[204:207],v[52:55],v[20:23]
ds_read_b128 v[44:47],v2 offset:55296
s_waitcnt lgkmcnt(3)
v_mfma_f32_16x16x32_bf16 v[20:23],a[92:95],v[48:51],v[20:23]
v_add_f32 v28,v28,v12
v_mfma_f32_16x16x32_bf16 v[20:23],v[124:127],v[56:59],v[20:23]
ds_read_b128 v[48:51],v2 offset:13312
v_add_f32 v29,v29,v13
v_mfma_f32_16x16x32_bf16 v[20:23],v[236:239],v[60:63],v[20:23]
v_add_f32 v30,v30,v14
v_add_f32 v31,v31,v15
v_mfma_f32_16x16x32_bf16 v[16:19],v[232:235],v[60:63],v[16:19]
ds_read_b128 v[52:55],v2 offset:27648
v_cvt_pk_bf16_f32 v12,v24,v25
v_mfma_f32_16x16x32_bf16 v[16:19],v[120:123],v[56:59],v[16:19]
v_cvt_pk_bf16_f32 v13,v26,v27
s_waitcnt lgkmcnt(4)
v_mfma_f32_16x16x32_bf16 v[16:19],a[96:99],v[32:35],v[16:19]
ds_read_b128 v[56:59],v2 offset:41984
v_cvt_pk_bf16_f32 v14,v28,v29
v_mfma_f32_16x16x32_bf16 v[16:19],a[208:211],v[36:39],v[16:19]
s_waitcnt vmcnt(10)
s_barrier
v_mfma_f32_16x16x32_bf16 v[20:23],a[212:215],v[36:39],v[20:23]
ds_read_b128 v[60:63],v2 offset:56320
v_cvt_pk_bf16_f32 v15,v30,v31
v_mfma_f32_16x16x32_bf16 v[20:23],a[100:103],v[32:35],v[20:23]
s_waitcnt lgkmcnt(4)
v_mfma_f32_16x16x32_bf16 v[20:23],v[132:135],v[40:43],v[20:23]
ds_read_b128 v[32:35],v4 offset:0
v_mfma_f32_16x16x32_bf16 v[20:23],v[244:247],v[44:47],v[20:23]
buffer_store_dwordx4 v[12:15],v2,s[32:35],s68 offen sc1
v_mfma_f32_16x16x32_bf16 v[16:19],v[240:243],v[44:47],v[16:19]
ds_read_b128 v[36:39],v4 offset:14336
v_mfma_f32_16x16x32_bf16 v[16:19],v[128:131],v[40:43],v[16:19]
s_waitcnt lgkmcnt(4)
v_mfma_f32_16x16x32_bf16 v[16:19],a[104:107],v[48:51],v[16:19]
ds_read_b128 v[40:43],v4 offset:28672
v_mfma_f32_16x16x32_bf16 v[16:19],a[216:219],v[52:55],v[16:19]
v_mfma_f32_16x16x32_bf16 v[20:23],a[220:223],v[52:55],v[20:23]
ds_read_b128 v[44:47],v4 offset:43008
s_mov_b32 s67,s68
v_mfma_f32_16x16x32_bf16 v[20:23],a[108:111],v[48:51],v[20:23]
s_mov_b32 s68,s70
s_waitcnt lgkmcnt(4)
v_mfma_f32_16x16x32_bf16 v[20:23],v[140:143],v[56:59],v[20:23]
ds_read_b128 v[48:51],v4 offset:1024
s_add_u32 s70,s70,1187840
v_mfma_f32_16x16x32_bf16 v[20:23],v[252:255],v[60:63],v[20:23]
v_mfma_f32_16x16x32_bf16 v[16:19],v[248:251],v[60:63],v[16:19]
ds_read_b128 v[52:55],v4 offset:15360
s_sub_u32 s62,s62,1
v_mfma_f32_16x16x32_bf16 v[16:19],v[136:139],v[56:59],v[16:19]
s_cbranch_scc1 .L_dab_2
s_waitcnt lgkmcnt(4)
v_mfma_f32_16x16x32_bf16 v[24:27],a[0:3],v[32:35],0
ds_read_b128 v[56:59],v4 offset:29696
v_dot2_f32_bf16 v10,v12,s72,0
v_mfma_f32_16x16x32_bf16 v[24:27],a[112:115],v[36:39],v[24:27]
v_dot2_f32_bf16 v11,v12,v12,0
v_dot2_f32_bf16 v10,v13,s72,v10
v_mfma_f32_16x16x32_bf16 v[28:31],a[116:119],v[36:39],0
ds_read_b128 v[60:63],v4 offset:44032
v_dot2_f32_bf16 v11,v13,v13,v11
v_mfma_f32_16x16x32_bf16 v[28:31],a[4:7],v[32:35],v[28:31]
v_dot2_f32_bf16 v10,v14,s72,v10
s_waitcnt lgkmcnt(4)
v_mfma_f32_16x16x32_bf16 v[28:31],a[228:231],v[40:43],v[28:31]
ds_read_b128 v[32:35],v4 offset:2048
v_dot2_f32_bf16 v11,v14,v14,v11
v_mfma_f32_16x16x32_bf16 v[28:31],v[148:151],v[44:47],v[28:31]
v_dot2_f32_bf16 v10,v15,s72,v10
v_dot2_f32_bf16 v11,v15,v15,v11
v_mfma_f32_16x16x32_bf16 v[24:27],v[144:147],v[44:47],v[24:27]
ds_read_b128 v[36:39],v4 offset:16384
v_mfma_f32_16x16x32_bf16 v[24:27],a[224:227],v[40:43],v[24:27]
v_permlane16_swap_b32 v10,v11
s_waitcnt lgkmcnt(4)
v_mfma_f32_16x16x32_bf16 v[24:27],a[8:11],v[48:51],v[24:27]
ds_read_b128 v[40:43],v4 offset:30720
v_add_f32 v10,v10,v11
v_mfma_f32_16x16x32_bf16 v[24:27],a[120:123],v[52:55],v[24:27]
v_mfma_f32_16x16x32_bf16 v[28:31],a[124:127],v[52:55],v[28:31]
ds_read_b128 v[44:47],v4 offset:45056
v_permlane32_swap_b32 v10,v11
v_mfma_f32_16x16x32_bf16 v[28:31],a[12:15],v[48:51],v[28:31]
v_add_f32 v10,v10,v11
s_waitcnt lgkmcnt(4)
v_mfma_f32_16x16x32_bf16 v[28:31],a[236:239],v[56:59],v[28:31]
ds_read_b128 v[48:51],v4 offset:3072
v_mfma_f32_16x16x32_bf16 v[28:31],v[156:159],v[60:63],v[28:31]
buffer_store_dword v10,v3,s[32:35],s67 offen offset:1024 sc1
v_mfma_f32_16x16x32_bf16 v[24:27],v[152:155],v[60:63],v[24:27]
ds_read_b128 v[52:55],v4 offset:17408
v_mfma_f32_16x16x32_bf16 v[24:27],a[232:235],v[56:59],v[24:27]
s_waitcnt lgkmcnt(4)
v_mfma_f32_16x16x32_bf16 v[24:27],a[16:19],v[32:35],v[24:27]
ds_read_b128 v[56:59],v4 offset:31744
v_mfma_f32_16x16x32_bf16 v[24:27],a[128:131],v[36:39],v[24:27]
s_waitcnt vmcnt(2)
s_barrier
v_mfma_f32_16x16x32_bf16 v[28:31],a[132:135],v[36:39],v[28:31]
ds_read_b128 v[60:63],v4 offset:46080
v_mfma_f32_16x16x32_bf16 v[28:31],a[20:23],v[32:35],v[28:31]
s_waitcnt lgkmcnt(4)
v_mfma_f32_16x16x32_bf16 v[28:31],a[244:247],v[40:43],v[28:31]
ds_read_b128 v[32:35],v4 offset:4096
s_mov_b32 m0,s74
v_mfma_f32_16x16x32_bf16 v[28:31],v[164:167],v[44:47],v[28:31]
buffer_load_dwordx4 v2,s[32:35],s71 offen offset:0 sc1 lds
v_mfma_f32_16x16x32_bf16 v[24:27],v[160:163],v[44:47],v[24:27]
ds_read_b128 v[36:39],v4 offset:18432
v_mfma_f32_16x16x32_bf16 v[24:27],a[240:243],v[40:43],v[24:27]
s_waitcnt lgkmcnt(4)
v_mfma_f32_16x16x32_bf16 v[24:27],a[24:27],v[48:51],v[24:27]
ds_read_b128 v[40:43],v4 offset:32768
v_mfma_f32_16x16x32_bf16 v[24:27],a[136:139],v[52:55],v[24:27]
buffer_load_dwordx4 v2,s[32:35],s71 offen offset:1024 sc1 lds
s_add_u32 s71,s71,s30
v_mfma_f32_16x16x32_bf16 v[28:31],a[140:143],v[52:55],v[28:31]
ds_read_b128 v[44:47],v4 offset:47104
s_add_u32 m0,s64,0
v_mfma_f32_16x16x32_bf16 v[28:31],a[28:31],v[48:51],v[28:31]
s_waitcnt lgkmcnt(4)
v_mfma_f32_16x16x32_bf16 v[28:31],a[252:255],v[56:59],v[28:31]
ds_read_b128 v[48:51],v4 offset:5120
v_mfma_f32_16x16x32_bf16 v[28:31],v[172:175],v[60:63],v[28:31]
buffer_load_dwordx4 v2,s[32:35],s63 offen offset:0 sc1 lds
v_mfma_f32_16x16x32_bf16 v[24:27],v[168:171],v[60:63],v[24:27]
ds_read_b128 v[52:55],v4 offset:19456
v_mfma_f32_16x16x32_bf16 v[24:27],a[248:251],v[56:59],v[24:27]
s_waitcnt lgkmcnt(4)
v_mfma_f32_16x16x32_bf16 v[24:27],a[32:35],v[32:35],v[24:27]
ds_read_b128 v[56:59],v4 offset:33792
v_mfma_f32_16x16x32_bf16 v[24:27],a[144:147],v[36:39],v[24:27]
buffer_load_dwordx4 v2,s[32:35],s63 offen offset:1024 sc1 lds
v_mfma_f32_16x16x32_bf16 v[28:31],a[148:151],v[36:39],v[28:31]
ds_read_b128 v[60:63],v4 offset:48128
v_mfma_f32_16x16x32_bf16 v[28:31],a[36:39],v[32:35],v[28:31]
s_waitcnt lgkmcnt(4)
v_mfma_f32_16x16x32_bf16 v[28:31],v[68:71],v[40:43],v[28:31]
ds_read_b128 v[32:35],v4 offset:6144
v_mfma_f32_16x16x32_bf16 v[28:31],v[180:183],v[44:47],v[28:31]
buffer_load_dwordx4 v2,s[32:35],s63 offen offset:2048 sc1 lds
v_mfma_f32_16x16x32_bf16 v[24:27],v[176:179],v[44:47],v[24:27]
ds_read_b128 v[36:39],v4 offset:20480
v_mfma_f32_16x16x32_bf16 v[24:27],v[64:67],v[40:43],v[24:27]
s_waitcnt lgkmcnt(4)
v_mfma_f32_16x16x32_bf16 v[24:27],a[40:43],v[48:51],v[24:27]
ds_read_b128 v[40:43],v4 offset:34816
v_mfma_f32_16x16x32_bf16 v[24:27],a[152:155],v[52:55],v[24:27]
buffer_load_dwordx4 v2,s[32:35],s63 offen offset:3072 sc1 lds
s_add_u32 s63,s63,4096
v_mfma_f32_16x16x32_bf16 v[28:31],a[156:159],v[52:55],v[28:31]
ds_read_b128 v[44:47],v4 offset:49152
s_add_u32 m0,s64,4096
v_mfma_f32_16x16x32_bf16 v[28:31],a[44:47],v[48:51],v[28:31]
s_waitcnt lgkmcnt(4)
v_mfma_f32_16x16x32_bf16 v[28:31],v[76:79],v[56:59],v[28:31]
ds_read_b128 v[48:51],v4 offset:7168
v_mfma_f32_16x16x32_bf16 v[28:31],v[188:191],v[60:63],v[28:31]
buffer_load_dwordx4 v2,s[32:35],s63 offen offset:0 sc1 lds
v_mfma_f32_16x16x32_bf16 v[24:27],v[184:187],v[60:63],v[24:27]
ds_read_b128 v[52:55],v4 offset:21504
v_mfma_f32_16x16x32_bf16 v[24:27],v[72:75],v[56:59],v[24:27]
s_waitcnt lgkmcnt(4)
v_mfma_f32_16x16x32_bf16 v[24:27],a[48:51],v[32:35],v[24:27]
ds_read_b128 v[56:59],v4 offset:35840
v_mfma_f32_16x16x32_bf16 v[24:27],a[160:163],v[36:39],v[24:27]
buffer_load_dwordx4 v2,s[32:35],s63 offen offset:1024 sc1 lds
v_mfma_f32_16x16x32_bf16 v[28:31],a[164:167],v[36:39],v[28:31]
ds_read_b128 v[60:63],v4 offset:50176
v_mfma_f32_16x16x32_bf16 v[28:31],a[52:55],v[32:35],v[28:31]
s_waitcnt lgkmcnt(4)
v_mfma_f32_16x16x32_bf16 v[28:31],v[84:87],v[40:43],v[28:31]
ds_read_b128 v[32:35],v4 offset:8192
v_mfma_f32_16x16x32_bf16 v[28:31],v[196:199],v[44:47],v[28:31]
buffer_load_dwordx4 v2,s[32:35],s63 offen offset:2048 sc1 lds
v_mfma_f32_16x16x32_bf16 v[24:27],v[192:195],v[44:47],v[24:27]
ds_read_b128 v[36:39],v4 offset:22528
v_mfma_f32_16x16x32_bf16 v[24:27],v[80:83],v[40:43],v[24:27]
s_waitcnt lgkmcnt(4)
v_mfma_f32_16x16x32_bf16 v[24:27],a[56:59],v[48:51],v[24:27]
ds_read_b128 v[40:43],v4 offset:36864
v_mfma_f32_16x16x32_bf16 v[24:27],a[168:171],v[52:55],v[24:27]
buffer_load_dwordx4 v2,s[32:35],s63 offen offset:3072 sc1 lds
v_mfma_f32_16x16x32_bf16 v[28:31],a[172:175],v[52:55],v[28:31]
ds_read_b128 v[44:47],v4 offset:51200
v_mfma_f32_16x16x32_bf16 v[28:31],a[60:63],v[48:51],v[28:31]
s_waitcnt lgkmcnt(4)
v_mfma_f32_16x16x32_bf16 v[28:31],v[92:95],v[56:59],v[28:31]
ds_read_b128 v[48:51],v4 offset:9216
v_mfma_f32_16x16x32_bf16 v[28:31],v[204:207],v[60:63],v[28:31]
s_add_u32 s63,s63,4096
s_add_u32 m0,s64,8192
v_mfma_f32_16x16x32_bf16 v[24:27],v[200:203],v[60:63],v[24:27]
ds_read_b128 v[52:55],v4 offset:23552
v_mfma_f32_16x16x32_bf16 v[24:27],v[88:91],v[56:59],v[24:27]
s_waitcnt lgkmcnt(4)
v_mfma_f32_16x16x32_bf16 v[24:27],a[64:67],v[32:35],v[24:27]
ds_read_b128 v[56:59],v4 offset:37888
v_mfma_f32_16x16x32_bf16 v[24:27],a[176:179],v[36:39],v[24:27]
buffer_load_dwordx4 v2,s[32:35],s63 offen offset:0 sc1 lds
v_mfma_f32_16x16x32_bf16 v[28:31],a[180:183],v[36:39],v[28:31]
ds_read_b128 v[60:63],v4 offset:52224
v_mfma_f32_16x16x32_bf16 v[28:31],a[68:71],v[32:35],v[28:31]
s_waitcnt lgkmcnt(4)
v_mfma_f32_16x16x32_bf16 v[28:31],v[100:103],v[40:43],v[28:31]
ds_read_b128 v[32:35],v4 offset:10240
v_mfma_f32_16x16x32_bf16 v[28:31],v[212:215],v[44:47],v[28:31]
buffer_load_dwordx4 v2,s[32:35],s63 offen offset:1024 sc1 lds
v_mfma_f32_16x16x32_bf16 v[24:27],v[208:211],v[44:47],v[24:27]
ds_read_b128 v[36:39],v4 offset:24576
v_mfma_f32_16x16x32_bf16 v[24:27],v[96:99],v[40:43],v[24:27]
s_waitcnt lgkmcnt(4)
v_mfma_f32_16x16x32_bf16 v[24:27],a[72:75],v[48:51],v[24:27]
ds_read_b128 v[40:43],v4 offset:38912
s_waitcnt vmcnt(10)
v_mfma_f32_16x16x32_bf16 v[24:27],a[184:187],v[52:55],v[24:27]
buffer_load_dwordx4 v2,s[32:35],s63 offen offset:2048 sc1 lds
ds_read_b128 v[12:15],v5 offset:0
v_mfma_f32_16x16x32_bf16 v[28:31],a[188:191],v[52:55],v[28:31]
ds_read_b128 v[44:47],v4 offset:53248
v_mfma_f32_16x16x32_bf16 v[28:31],a[76:79],v[48:51],v[28:31]
s_waitcnt lgkmcnt(5)
v_mfma_f32_16x16x32_bf16 v[28:31],v[108:111],v[56:59],v[28:31]
ds_read_b128 v[48:51],v4 offset:11264
v_mfma_f32_16x16x32_bf16 v[28:31],v[220:223],v[60:63],v[28:31]
v_mfma_f32_16x16x32_bf16 v[24:27],v[216:219],v[60:63],v[24:27]
ds_read_b128 v[52:55],v4 offset:25600
s_waitcnt lgkmcnt(3)
v_mfma_f32_16x16x32_bf16 v[24:27],v[104:107],v[56:59],v[24:27]
v_add_f32 v16,v16,v12
v_mfma_f32_16x16x32_bf16 v[24:27],a[80:83],v[32:35],v[24:27]
ds_read_b128 v[56:59],v4 offset:39936
v_add_f32 v17,v17,v13
v_mfma_f32_16x16x32_bf16 v[24:27],a[192:195],v[36:39],v[24:27]
buffer_load_dwordx4 v2,s[32:35],s63 offen offset:3072 sc1 lds
s_add_u32 s63,s63,4096
v_mfma_f32_16x16x32_bf16 v[28:31],a[196:199],v[36:39],v[28:31]
ds_read_b128 v[60:63],v4 offset:54272
s_add_u32 m0,s64,12288
v_mfma_f32_16x16x32_bf16 v[28:31],a[84:87],v[32:35],v[28:31]
v_add_f32 v18,v18,v14
s_waitcnt lgkmcnt(4)
v_mfma_f32_16x16x32_bf16 v[28:31],v[116:119],v[40:43],v[28:31]
ds_read_b128 v[32:35],v4 offset:12288
v_add_f32 v19,v19,v15
v_mfma_f32_16x16x32_bf16 v[28:31],v[228:231],v[44:47],v[28:31]
buffer_load_dwordx4 v2,s[32:35],s63 offen offset:0 sc1 lds
ds_read_b128 v[12:15],v5 offset:1024
v_mfma_f32_16x16x32_bf16 v[24:27],v[224:227],v[44:47],v[24:27]
ds_read_b128 v[36:39],v4 offset:26624
v_mfma_f32_16x16x32_bf16 v[24:27],v[112:115],v[40:43],v[24:27]
s_waitcnt lgkmcnt(5)
v_mfma_f32_16x16x32_bf16 v[24:27],a[88:91],v[48:51],v[24:27]
ds_read_b128 v[40:43],v4 offset:40960
v_mfma_f32_16x16x32_bf16 v[24:27],a[200:203],v[52:55],v[24:27]
buffer_load_dwordx4 v2,s[32:35],s63 offen offset:1024 sc1 lds
s_add_u32 s63,s63,s31
v_mfma_f32_16x16x32_bf16 v[28:31],a[204:207],v[52:55],v[28:31]
ds_read_b128 v[44:47],v4 offset:55296
s_waitcnt lgkmcnt(3)
v_mfma_f32_16x16x32_bf16 v[28:31],a[92:95],v[48:51],v[28:31]
v_add_f32 v20,v20,v12
v_mfma_f32_16x16x32_bf16 v[28:31],v[124:127],v[56:59],v[28:31]
ds_read_b128 v[48:51],v4 offset:13312
v_add_f32 v21,v21,v13
v_mfma_f32_16x16x32_bf16 v[28:31],v[236:239],v[60:63],v[28:31]
v_add_f32 v22,v22,v14
v_add_f32 v23,v23,v15
v_mfma_f32_16x16x32_bf16 v[24:27],v[232:235],v[60:63],v[24:27]
ds_read_b128 v[52:55],v4 offset:27648
v_cvt_pk_bf16_f32 v12,v16,v17
v_mfma_f32_16x16x32_bf16 v[24:27],v[120:123],v[56:59],v[24:27]
v_cvt_pk_bf16_f32 v13,v18,v19
s_waitcnt lgkmcnt(4)
v_mfma_f32_16x16x32_bf16 v[24:27],a[96:99],v[32:35],v[24:27]
ds_read_b128 v[56:59],v4 offset:41984
v_cvt_pk_bf16_f32 v14,v20,v21
v_mfma_f32_16x16x32_bf16 v[24:27],a[208:211],v[36:39],v[24:27]
s_waitcnt vmcnt(10)
s_barrier
v_mfma_f32_16x16x32_bf16 v[28:31],a[212:215],v[36:39],v[28:31]
ds_read_b128 v[60:63],v4 offset:56320
v_cvt_pk_bf16_f32 v15,v22,v23
v_mfma_f32_16x16x32_bf16 v[28:31],a[100:103],v[32:35],v[28:31]
s_waitcnt lgkmcnt(4)
v_mfma_f32_16x16x32_bf16 v[28:31],v[132:135],v[40:43],v[28:31]
ds_read_b128 v[32:35],v2 offset:0
v_mfma_f32_16x16x32_bf16 v[28:31],v[244:247],v[44:47],v[28:31]
buffer_store_dwordx4 v[12:15],v2,s[32:35],s69 offen sc1
v_mfma_f32_16x16x32_bf16 v[24:27],v[240:243],v[44:47],v[24:27]
ds_read_b128 v[36:39],v2 offset:14336
v_mfma_f32_16x16x32_bf16 v[24:27],v[128:131],v[40:43],v[24:27]
s_waitcnt lgkmcnt(4)
v_mfma_f32_16x16x32_bf16 v[24:27],a[104:107],v[48:51],v[24:27]
ds_read_b128 v[40:43],v2 offset:28672
v_mfma_f32_16x16x32_bf16 v[24:27],a[216:219],v[52:55],v[24:27]
v_mfma_f32_16x16x32_bf16 v[28:31],a[220:223],v[52:55],v[28:31]
ds_read_b128 v[44:47],v2 offset:43008
s_mov_b32 s67,s69
v_mfma_f32_16x16x32_bf16 v[28:31],a[108:111],v[48:51],v[28:31]
s_mov_b32 s69,s70
s_waitcnt lgkmcnt(4)
v_mfma_f32_16x16x32_bf16 v[28:31],v[140:143],v[56:59],v[28:31]
ds_read_b128 v[48:51],v2 offset:1024
s_add_u32 s70,s70,1187840
v_mfma_f32_16x16x32_bf16 v[28:31],v[252:255],v[60:63],v[28:31]
v_mfma_f32_16x16x32_bf16 v[24:27],v[248:251],v[60:63],v[24:27]
ds_read_b128 v[52:55],v2 offset:15360
v_mfma_f32_16x16x32_bf16 v[24:27],v[136:139],v[56:59],v[24:27]
s_branch .L_dal_2
.L_dab_2:
v_dot2_f32_bf16 v10,v12,s72,0
v_dot2_f32_bf16 v11,v12,v12,0
v_dot2_f32_bf16 v10,v13,s72,v10
v_dot2_f32_bf16 v11,v13,v13,v11
v_dot2_f32_bf16 v10,v14,s72,v10
v_dot2_f32_bf16 v11,v14,v14,v11
v_dot2_f32_bf16 v10,v15,s72,v10
v_dot2_f32_bf16 v11,v15,v15,v11
s_nop 2
v_permlane16_swap_b32 v10,v11
v_add_f32 v10,v10,v11
s_nop 1
v_permlane32_swap_b32 v10,v11
v_add_f32 v10,v10,v11
buffer_store_dword v10,v3,s[32:35],s67 offen offset:1024 sc1
s_mov_b32 s62,s58
s_mov_b32 s70,s59
s_add_u32 s74,s74,8192
v_lshrrev_b32 v5,8,v2
v_lshl_add_u32 v5,v5,4,s74
v_and_b32 v3,128,v2
v_lshl_add_u32 v5,v3,2,v5
v_and_b32 v3,16,v2
v_lshl_add_u32 v5,v3,4,v5
v_and_b32 v3,96,v2
v_lshl_add_u32 v5,v3,1,v5
v_lshrrev_b32 v3,2,v2
v_add_u32 v3,s74,v3
s_add_u32 s66,s66,2375680
s_add_u32 s63,s63,2375680
s_add_u32 s71,s71,2375680
s_mov_b32 s68,98304
s_mov_b32 s34,79446016
.p2align 6
.L_dbl_2:
s_waitcnt lgkmcnt(4)
v_mfma_f32_16x16x32_bf16 v[24:27],a[0:3],v[32:35],0
ds_read_b128 v[56:59],v4 offset:29696
v_mfma_f32_16x16x32_bf16 v[24:27],a[112:115],v[36:39],v[24:27]
v_mfma_f32_16x16x32_bf16 v[28:31],a[116:119],v[36:39],0
ds_read_b128 v[60:63],v4 offset:44032
v_mfma_f32_16x16x32_bf16 v[28:31],a[4:7],v[32:35],v[28:31]
v_cvt_pk_bf16_f32 v15,v22,v23
s_waitcnt lgkmcnt(4)
v_mfma_f32_16x16x32_bf16 v[28:31],a[228:231],v[40:43],v[28:31]
ds_read_b128 v[32:35],v4 offset:2048
v_cvt_pk_bf16_f32 v14,v20,v21
v_mfma_f32_16x16x32_bf16 v[28:31],v[148:151],v[44:47],v[28:31]
v_cvt_pk_bf16_f32 v13,v18,v19
)#"
R"#(
v_cvt_pk_bf16_f32 v12,v16,v17
v_mfma_f32_16x16x32_bf16 v[24:27],v[144:147],v[44:47],v[24:27]
ds_read_b128 v[36:39],v4 offset:16384
ds_write_b128 v5,v[12:15]
v_mfma_f32_16x16x32_bf16 v[24:27],a[224:227],v[40:43],v[24:27]
ds_read2st64_b32 v[12:13],v3 offset0:0 offset1:1
s_waitcnt lgkmcnt(6)
v_mfma_f32_16x16x32_bf16 v[24:27],a[8:11],v[48:51],v[24:27]
ds_read_b128 v[40:43],v4 offset:30720
ds_read2st64_b32 v[14:15],v3 offset0:2 offset1:3
v_mfma_f32_16x16x32_bf16 v[24:27],a[120:123],v[52:55],v[24:27]
v_mfma_f32_16x16x32_bf16 v[28:31],a[124:127],v[52:55],v[28:31]
ds_read_b128 v[44:47],v4 offset:45056
v_mfma_f32_16x16x32_bf16 v[28:31],a[12:15],v[48:51],v[28:31]
s_waitcnt lgkmcnt(7)
v_mfma_f32_16x16x32_bf16 v[28:31],a[236:239],v[56:59],v[28:31]
ds_read_b128 v[48:51],v4 offset:3072
v_mfma_f32_16x16x32_bf16 v[28:31],v[156:159],v[60:63],v[28:31]
s_waitcnt lgkmcnt(2)
buffer_atomic_pk_add_bf16 v12,v1,s[44:47],s65 offen offset:0 sc1
v_mfma_f32_16x16x32_bf16 v[24:27],v[152:155],v[60:63],v[24:27]
ds_read_b128 v[52:55],v4 offset:17408
s_add_u32 s70,s70,s75
v_mfma_f32_16x16x32_bf16 v[24:27],a[232:235],v[56:59],v[24:27]
buffer_atomic_pk_add_bf16 v13,v1,s[44:47],s65 offen offset:2048 sc1
v_mfma_f32_16x16x32_bf16 v[24:27],a[16:19],v[32:35],v[24:27]
ds_read_b128 v[56:59],v4 offset:31744
s_add_u32 s65,s65,16384
v_mfma_f32_16x16x32_bf16 v[24:27],a[128:131],v[36:39],v[24:27]
s_waitcnt vmcnt(2)
s_barrier
v_mfma_f32_16x16x32_bf16 v[28:31],a[132:135],v[36:39],v[28:31]
ds_read_b128 v[60:63],v4 offset:46080
v_mfma_f32_16x16x32_bf16 v[28:31],a[20:23],v[32:35],v[28:31]
buffer_atomic_pk_add_bf16 v14,v1,s[44:47],s65 offen offset:0 sc1
s_waitcnt lgkmcnt(4)
v_mfma_f32_16x16x32_bf16 v[28:31],a[244:247],v[40:43],v[28:31]
ds_read_b128 v[32:35],v4 offset:4096
s_add_u32 m0,s64,0
v_mfma_f32_16x16x32_bf16 v[28:31],v[164:167],v[44:47],v[28:31]
buffer_atomic_pk_add_bf16 v15,v1,s[44:47],s65 offen offset:2048 sc1
s_and_b32 s65,s70,0x3f8000
v_mfma_f32_16x16x32_bf16 v[24:27],v[160:163],v[44:47],v[24:27]
ds_read_b128 v[36:39],v4 offset:18432
v_mfma_f32_16x16x32_bf16 v[24:27],a[240:243],v[40:43],v[24:27]
s_waitcnt lgkmcnt(4)
v_mfma_f32_16x16x32_bf16 v[24:27],a[24:27],v[48:51],v[24:27]
ds_read_b128 v[40:43],v4 offset:32768
v_mfma_f32_16x16x32_bf16 v[24:27],a[136:139],v[52:55],v[24:27]
buffer_load_dwordx4 v2,s[32:35],s63 offen offset:0 sc1 lds
v_mfma_f32_16x16x32_bf16 v[28:31],a[140:143],v[52:55],v[28:31]
ds_read_b128 v[44:47],v4 offset:47104
v_mfma_f32_16x16x32_bf16 v[28:31],a[28:31],v[48:51],v[28:31]
s_waitcnt lgkmcnt(4)
v_mfma_f32_16x16x32_bf16 v[28:31],a[252:255],v[56:59],v[28:31]
ds_read_b128 v[48:51],v4 offset:5120
v_mfma_f32_16x16x32_bf16 v[28:31],v[172:175],v[60:63],v[28:31]
buffer_load_dwordx4 v2,s[32:35],s63 offen offset:1024 sc1 lds
v_mfma_f32_16x16x32_bf16 v[24:27],v[168:171],v[60:63],v[24:27]
ds_read_b128 v[52:55],v4 offset:19456
v_mfma_f32_16x16x32_bf16 v[24:27],a[248:251],v[56:59],v[24:27]
s_waitcnt lgkmcnt(4)
v_mfma_f32_16x16x32_bf16 v[24:27],a[32:35],v[32:35],v[24:27]
ds_read_b128 v[56:59],v4 offset:33792
v_mfma_f32_16x16x32_bf16 v[24:27],a[144:147],v[36:39],v[24:27]
buffer_load_dwordx4 v2,s[32:35],s63 offen offset:2048 sc1 lds
v_mfma_f32_16x16x32_bf16 v[28:31],a[148:151],v[36:39],v[28:31]
ds_read_b128 v[60:63],v4 offset:48128
v_mfma_f32_16x16x32_bf16 v[28:31],a[36:39],v[32:35],v[28:31]
s_waitcnt lgkmcnt(4)
v_mfma_f32_16x16x32_bf16 v[28:31],v[68:71],v[40:43],v[28:31]
ds_read_b128 v[32:35],v4 offset:6144
v_mfma_f32_16x16x32_bf16 v[28:31],v[180:183],v[44:47],v[28:31]
buffer_load_dwordx4 v2,s[32:35],s63 offen offset:3072 sc1 lds
v_mfma_f32_16x16x32_bf16 v[24:27],v[176:179],v[44:47],v[24:27]
ds_read_b128 v[36:39],v4 offset:20480
v_mfma_f32_16x16x32_bf16 v[24:27],v[64:67],v[40:43],v[24:27]
s_waitcnt lgkmcnt(4)
v_mfma_f32_16x16x32_bf16 v[24:27],a[40:43],v[48:51],v[24:27]
ds_read_b128 v[40:43],v4 offset:34816
s_add_u32 s63,s63,4096
v_mfma_f32_16x16x32_bf16 v[24:27],a[152:155],v[52:55],v[24:27]
s_add_u32 m0,s64,4096
v_mfma_f32_16x16x32_bf16 v[28:31],a[156:159],v[52:55],v[28:31]
ds_read_b128 v[44:47],v4 offset:49152
v_mfma_f32_16x16x32_bf16 v[28:31],a[44:47],v[48:51],v[28:31]
s_waitcnt lgkmcnt(4)
v_mfma_f32_16x16x32_bf16 v[28:31],v[76:79],v[56:59],v[28:31]
ds_read_b128 v[48:51],v4 offset:7168
v_mfma_f32_16x16x32_bf16 v[28:31],v[188:191],v[60:63],v[28:31]
buffer_load_dwordx4 v2,s[32:35],s63 offen offset:0 sc1 lds
v_mfma_f32_16x16x32_bf16 v[24:27],v[184:187],v[60:63],v[24:27]
ds_read_b128 v[52:55],v4 offset:21504
v_mfma_f32_16x16x32_bf16 v[24:27],v[72:75],v[56:59],v[24:27]
s_waitcnt lgkmcnt(4)
v_mfma_f32_16x16x32_bf16 v[24:27],a[48:51],v[32:35],v[24:27]
ds_read_b128 v[56:59],v4 offset:35840
v_mfma_f32_16x16x32_bf16 v[24:27],a[160:163],v[36:39],v[24:27]
buffer_load_dwordx4 v2,s[32:35],s63 offen offset:1024 sc1 lds
v_mfma_f32_16x16x32_bf16 v[28:31],a[164:167],v[36:39],v[28:31]
ds_read_b128 v[60:63],v4 offset:50176
v_mfma_f32_16x16x32_bf16 v[28:31],a[52:55],v[32:35],v[28:31]
s_waitcnt lgkmcnt(4)
v_mfma_f32_16x16x32_bf16 v[28:31],v[84:87],v[40:43],v[28:31]
ds_read_b128 v[32:35],v4 offset:8192
v_mfma_f32_16x16x32_bf16 v[28:31],v[196:199],v[44:47],v[28:31]
buffer_load_dwordx4 v2,s[32:35],s63 offen offset:2048 sc1 lds
v_mfma_f32_16x16x32_bf16 v[24:27],v[192:195],v[44:47],v[24:27]
ds_read_b128 v[36:39],v4 offset:22528
v_mfma_f32_16x16x32_bf16 v[24:27],v[80:83],v[40:43],v[24:27]
s_waitcnt lgkmcnt(4)
v_mfma_f32_16x16x32_bf16 v[24:27],a[56:59],v[48:51],v[24:27]
ds_read_b128 v[40:43],v4 offset:36864
v_mfma_f32_16x16x32_bf16 v[24:27],a[168:171],v[52:55],v[24:27]
buffer_load_dwordx4 v2,s[32:35],s63 offen offset:3072 sc1 lds
v_mfma_f32_16x16x32_bf16 v[28:31],a[172:175],v[52:55],v[28:31]
ds_read_b128 v[44:47],v4 offset:51200
v_mfma_f32_16x16x32_bf16 v[28:31],a[60:63],v[48:51],v[28:31]
s_waitcnt lgkmcnt(4)
v_mfma_f32_16x16x32_bf16 v[28:31],v[92:95],v[56:59],v[28:31]
ds_read_b128 v[48:51],v4 offset:9216
v_mfma_f32_16x16x32_bf16 v[28:31],v[204:207],v[60:63],v[28:31]
s_add_u32 s63,s63,4096
s_add_u32 m0,s64,8192
v_mfma_f32_16x16x32_bf16 v[24:27],v[200:203],v[60:63],v[24:27]
ds_read_b128 v[52:55],v4 offset:23552
v_mfma_f32_16x16x32_bf16 v[24:27],v[88:91],v[56:59],v[24:27]
s_waitcnt lgkmcnt(4)
v_mfma_f32_16x16x32_bf16 v[24:27],a[64:67],v[32:35],v[24:27]
ds_read_b128 v[56:59],v4 offset:37888
v_mfma_f32_16x16x32_bf16 v[24:27],a[176:179],v[36:39],v[24:27]
buffer_load_dwordx4 v2,s[32:35],s63 offen offset:0 sc1 lds
v_mfma_f32_16x16x32_bf16 v[28:31],a[180:183],v[36:39],v[28:31]
ds_read_b128 v[60:63],v4 offset:52224
v_mfma_f32_16x16x32_bf16 v[28:31],a[68:71],v[32:35],v[28:31]
s_waitcnt lgkmcnt(4)
v_mfma_f32_16x16x32_bf16 v[28:31],v[100:103],v[40:43],v[28:31]
ds_read_b128 v[32:35],v4 offset:10240
v_mfma_f32_16x16x32_bf16 v[28:31],v[212:215],v[44:47],v[28:31]
buffer_load_dwordx4 v2,s[32:35],s63 offen offset:1024 sc1 lds
v_mfma_f32_16x16x32_bf16 v[24:27],v[208:211],v[44:47],v[24:27]
ds_read_b128 v[36:39],v4 offset:24576
v_mfma_f32_16x16x32_bf16 v[24:27],v[96:99],v[40:43],v[24:27]
s_waitcnt lgkmcnt(4)
v_mfma_f32_16x16x32_bf16 v[24:27],a[72:75],v[48:51],v[24:27]
ds_read_b128 v[40:43],v4 offset:38912
v_mfma_f32_16x16x32_bf16 v[24:27],a[184:187],v[52:55],v[24:27]
buffer_load_dwordx4 v2,s[32:35],s63 offen offset:2048 sc1 lds
v_mfma_f32_16x16x32_bf16 v[28:31],a[188:191],v[52:55],v[28:31]
ds_read_b128 v[44:47],v4 offset:53248
v_mfma_f32_16x16x32_bf16 v[28:31],a[76:79],v[48:51],v[28:31]
s_waitcnt lgkmcnt(4)
v_mfma_f32_16x16x32_bf16 v[28:31],v[108:111],v[56:59],v[28:31]
ds_read_b128 v[48:51],v4 offset:11264
v_mfma_f32_16x16x32_bf16 v[28:31],v[220:223],v[60:63],v[28:31]
v_mfma_f32_16x16x32_bf16 v[24:27],v[216:219],v[60:63],v[24:27]
ds_read_b128 v[52:55],v4 offset:25600
v_mfma_f32_16x16x32_bf16 v[24:27],v[104:107],v[56:59],v[24:27]
s_waitcnt lgkmcnt(4)
v_mfma_f32_16x16x32_bf16 v[24:27],a[80:83],v[32:35],v[24:27]
ds_read_b128 v[56:59],v4 offset:39936
v_mfma_f32_16x16x32_bf16 v[24:27],a[192:195],v[36:39],v[24:27]
buffer_load_dwordx4 v2,s[32:35],s63 offen offset:3072 sc1 lds
s_add_u32 s63,s63,4096
v_mfma_f32_16x16x32_bf16 v[28:31],a[196:199],v[36:39],v[28:31]
ds_read_b128 v[60:63],v4 offset:54272
s_add_u32 m0,s64,12288
v_mfma_f32_16x16x32_bf16 v[28:31],a[84:87],v[32:35],v[28:31]
s_waitcnt lgkmcnt(4)
v_mfma_f32_16x16x32_bf16 v[28:31],v[116:119],v[40:43],v[28:31]
ds_read_b128 v[32:35],v4 offset:12288
v_mfma_f32_16x16x32_bf16 v[28:31],v[228:231],v[44:47],v[28:31]
buffer_load_dwordx4 v2,s[32:35],s63 offen offset:0 sc1 lds
v_mfma_f32_16x16x32_bf16 v[24:27],v[224:227],v[44:47],v[24:27]
ds_read_b128 v[36:39],v4 offset:26624
v_mfma_f32_16x16x32_bf16 v[24:27],v[112:115],v[40:43],v[24:27]
s_waitcnt lgkmcnt(4)
v_mfma_f32_16x16x32_bf16 v[24:27],a[88:91],v[48:51],v[24:27]
ds_read_b128 v[40:43],v4 offset:40960
v_mfma_f32_16x16x32_bf16 v[24:27],a[200:203],v[52:55],v[24:27]
buffer_load_dwordx4 v2,s[32:35],s63 offen offset:1024 sc1 lds
s_add_u32 s63,s63,s31
v_mfma_f32_16x16x32_bf16 v[28:31],a[204:207],v[52:55],v[28:31]
ds_read_b128 v[44:47],v4 offset:55296
v_mfma_f32_16x16x32_bf16 v[28:31],a[92:95],v[48:51],v[28:31]
s_waitcnt lgkmcnt(4)
v_mfma_f32_16x16x32_bf16 v[28:31],v[124:127],v[56:59],v[28:31]
ds_read_b128 v[48:51],v4 offset:13312
v_mfma_f32_16x16x32_bf16 v[28:31],v[236:239],v[60:63],v[28:31]
v_mfma_f32_16x16x32_bf16 v[24:27],v[232:235],v[60:63],v[24:27]
ds_read_b128 v[52:55],v4 offset:27648
v_mfma_f32_16x16x32_bf16 v[24:27],v[120:123],v[56:59],v[24:27]
s_waitcnt lgkmcnt(4)
v_mfma_f32_16x16x32_bf16 v[24:27],a[96:99],v[32:35],v[24:27]
ds_read_b128 v[56:59],v4 offset:41984
v_mfma_f32_16x16x32_bf16 v[24:27],a[208:211],v[36:39],v[24:27]
s_waitcnt vmcnt(10)
s_barrier
v_mfma_f32_16x16x32_bf16 v[28:31],a[212:215],v[36:39],v[28:31]
ds_read_b128 v[60:63],v4 offset:56320
v_mfma_f32_16x16x32_bf16 v[28:31],a[100:103],v[32:35],v[28:31]
s_waitcnt lgkmcnt(4)
v_mfma_f32_16x16x32_bf16 v[28:31],v[132:135],v[40:43],v[28:31]
ds_read_b128 v[32:35],v2 offset:0
v_mfma_f32_16x16x32_bf16 v[28:31],v[244:247],v[44:47],v[28:31]
v_mfma_f32_16x16x32_bf16 v[24:27],v[240:243],v[44:47],v[24:27]
ds_read_b128 v[36:39],v2 offset:14336
v_mfma_f32_16x16x32_bf16 v[24:27],v[128:131],v[40:43],v[24:27]
s_waitcnt lgkmcnt(4)
v_mfma_f32_16x16x32_bf16 v[24:27],a[104:107],v[48:51],v[24:27]
ds_read_b128 v[40:43],v2 offset:28672
v_mfma_f32_16x16x32_bf16 v[24:27],a[216:219],v[52:55],v[24:27]
v_mfma_f32_16x16x32_bf16 v[28:31],a[220:223],v[52:55],v[28:31]
ds_read_b128 v[44:47],v2 offset:43008
v_mfma_f32_16x16x32_bf16 v[28:31],a[108:111],v[48:51],v[28:31]
s_waitcnt lgkmcnt(4)
v_mfma_f32_16x16x32_bf16 v[28:31],v[140:143],v[56:59],v[28:31]
ds_read_b128 v[48:51],v2 offset:1024
v_mfma_f32_16x16x32_bf16 v[28:31],v[252:255],v[60:63],v[28:31]
v_mfma_f32_16x16x32_bf16 v[24:27],v[248:251],v[60:63],v[24:27]
ds_read_b128 v[52:55],v2 offset:15360
s_sub_u32 s62,s62,1
v_mfma_f32_16x16x32_bf16 v[24:27],v[136:139],v[56:59],v[24:27]
s_cbranch_scc1 .L_dbt_2
s_waitcnt lgkmcnt(4)
v_mfma_f32_16x16x32_bf16 v[16:19],a[0:3],v[32:35],0
ds_read_b128 v[56:59],v2 offset:29696
v_mfma_f32_16x16x32_bf16 v[16:19],a[112:115],v[36:39],v[16:19]
v_mfma_f32_16x16x32_bf16 v[20:23],a[116:119],v[36:39],0
ds_read_b128 v[60:63],v2 offset:44032
v_mfma_f32_16x16x32_bf16 v[20:23],a[4:7],v[32:35],v[20:23]
v_cvt_pk_bf16_f32 v15,v30,v31
s_waitcnt lgkmcnt(4)
v_mfma_f32_16x16x32_bf16 v[20:23],a[228:231],v[40:43],v[20:23]
ds_read_b128 v[32:35],v2 offset:2048
v_cvt_pk_bf16_f32 v14,v28,v29
v_mfma_f32_16x16x32_bf16 v[20:23],v[148:151],v[44:47],v[20:23]
v_cvt_pk_bf16_f32 v13,v26,v27
v_cvt_pk_bf16_f32 v12,v24,v25
v_mfma_f32_16x16x32_bf16 v[16:19],v[144:147],v[44:47],v[16:19]
ds_read_b128 v[36:39],v2 offset:16384
ds_write_b128 v5,v[12:15]
v_mfma_f32_16x16x32_bf16 v[16:19],a[224:227],v[40:43],v[16:19]
ds_read2st64_b32 v[12:13],v3 offset0:0 offset1:1
s_waitcnt lgkmcnt(6)
v_mfma_f32_16x16x32_bf16 v[16:19],a[8:11],v[48:51],v[16:19]
ds_read_b128 v[40:43],v2 offset:30720
ds_read2st64_b32 v[14:15],v3 offset0:2 offset1:3
v_mfma_f32_16x16x32_bf16 v[16:19],a[120:123],v[52:55],v[16:19]
v_mfma_f32_16x16x32_bf16 v[20:23],a[124:127],v[52:55],v[20:23]
ds_read_b128 v[44:47],v2 offset:45056
v_mfma_f32_16x16x32_bf16 v[20:23],a[12:15],v[48:51],v[20:23]
s_waitcnt lgkmcnt(7)
v_mfma_f32_16x16x32_bf16 v[20:23],a[236:239],v[56:59],v[20:23]
ds_read_b128 v[48:51],v2 offset:3072
v_mfma_f32_16x16x32_bf16 v[20:23],v[156:159],v[60:63],v[20:23]
s_waitcnt lgkmcnt(2)
buffer_atomic_pk_add_bf16 v12,v1,s[44:47],s65 offen offset:0 sc1
v_mfma_f32_16x16x32_bf16 v[16:19],v[152:155],v[60:63],v[16:19]
ds_read_b128 v[52:55],v2 offset:17408
s_add_u32 s70,s70,s68
v_mfma_f32_16x16x32_bf16 v[16:19],a[232:235],v[56:59],v[16:19]
buffer_atomic_pk_add_bf16 v13,v1,s[44:47],s65 offen offset:2048 sc1
v_mfma_f32_16x16x32_bf16 v[16:19],a[16:19],v[32:35],v[16:19]
ds_read_b128 v[56:59],v2 offset:31744
s_add_u32 s65,s65,16384
v_mfma_f32_16x16x32_bf16 v[16:19],a[128:131],v[36:39],v[16:19]
s_waitcnt vmcnt(2)
s_barrier
v_mfma_f32_16x16x32_bf16 v[20:23],a[132:135],v[36:39],v[20:23]
ds_read_b128 v[60:63],v2 offset:46080
v_mfma_f32_16x16x32_bf16 v[20:23],a[20:23],v[32:35],v[20:23]
buffer_atomic_pk_add_bf16 v14,v1,s[44:47],s65 offen offset:0 sc1
s_waitcnt lgkmcnt(4)
v_mfma_f32_16x16x32_bf16 v[20:23],a[244:247],v[40:43],v[20:23]
ds_read_b128 v[32:35],v2 offset:4096
s_add_u32 m0,s64,57344
v_mfma_f32_16x16x32_bf16 v[20:23],v[164:167],v[44:47],v[20:23]
buffer_load_dwordx4 v2,s[32:35],s63 offen offset:0 sc1 lds
s_mov_b32 s68,s75
v_mfma_f32_16x16x32_bf16 v[16:19],v[160:163],v[44:47],v[16:19]
ds_read_b128 v[36:39],v2 offset:18432
v_mfma_f32_16x16x32_bf16 v[16:19],a[240:243],v[40:43],v[16:19]
buffer_atomic_pk_add_bf16 v15,v1,s[44:47],s65 offen offset:2048 sc1
s_waitcnt lgkmcnt(4)
v_mfma_f32_16x16x32_bf16 v[16:19],a[24:27],v[48:51],v[16:19]
ds_read_b128 v[40:43],v2 offset:32768
s_and_b32 s65,s70,0x3f8000
v_mfma_f32_16x16x32_bf16 v[16:19],a[136:139],v[52:55],v[16:19]
v_mfma_f32_16x16x32_bf16 v[20:23],a[140:143],v[52:55],v[20:23]
ds_read_b128 v[44:47],v2 offset:47104
v_mfma_f32_16x16x32_bf16 v[20:23],a[28:31],v[48:51],v[20:23]
s_waitcnt lgkmcnt(4)
v_mfma_f32_16x16x32_bf16 v[20:23],a[252:255],v[56:59],v[20:23]
ds_read_b128 v[48:51],v2 offset:5120
v_mfma_f32_16x16x32_bf16 v[20:23],v[172:175],v[60:63],v[20:23]
buffer_load_dwordx4 v2,s[32:35],s63 offen offset:1024 sc1 lds
v_mfma_f32_16x16x32_bf16 v[16:19],v[168:171],v[60:63],v[16:19]
ds_read_b128 v[52:55],v2 offset:19456
v_mfma_f32_16x16x32_bf16 v[16:19],a[248:251],v[56:59],v[16:19]
s_waitcnt lgkmcnt(4)
v_mfma_f32_16x16x32_bf16 v[16:19],a[32:35],v[32:35],v[16:19]
ds_read_b128 v[56:59],v2 offset:33792
v_mfma_f32_16x16x32_bf16 v[16:19],a[144:147],v[36:39],v[16:19]
buffer_load_dwordx4 v2,s[32:35],s63 offen offset:2048 sc1 lds
v_mfma_f32_16x16x32_bf16 v[20:23],a[148:151],v[36:39],v[20:23]
ds_read_b128 v[60:63],v2 offset:48128
v_mfma_f32_16x16x32_bf16 v[20:23],a[36:39],v[32:35],v[20:23]
s_waitcnt lgkmcnt(4)
v_mfma_f32_16x16x32_bf16 v[20:23],v[68:71],v[40:43],v[20:23]
ds_read_b128 v[32:35],v2 offset:6144
v_mfma_f32_16x16x32_bf16 v[20:23],v[180:183],v[44:47],v[20:23]
buffer_load_dwordx4 v2,s[32:35],s63 offen offset:3072 sc1 lds
v_mfma_f32_16x16x32_bf16 v[16:19],v[176:179],v[44:47],v[16:19]
ds_read_b128 v[36:39],v2 offset:20480
v_mfma_f32_16x16x32_bf16 v[16:19],v[64:67],v[40:43],v[16:19]
s_waitcnt lgkmcnt(4)
v_mfma_f32_16x16x32_bf16 v[16:19],a[40:43],v[48:51],v[16:19]
ds_read_b128 v[40:43],v2 offset:34816
s_add_u32 s63,s63,4096
v_mfma_f32_16x16x32_bf16 v[16:19],a[152:155],v[52:55],v[16:19]
s_add_u32 m0,s64,61440
v_mfma_f32_16x16x32_bf16 v[20:23],a[156:159],v[52:55],v[20:23]
ds_read_b128 v[44:47],v2 offset:49152
v_mfma_f32_16x16x32_bf16 v[20:23],a[44:47],v[48:51],v[20:23]
s_waitcnt lgkmcnt(4)
v_mfma_f32_16x16x32_bf16 v[20:23],v[76:79],v[56:59],v[20:23]
ds_read_b128 v[48:51],v2 offset:7168
v_mfma_f32_16x16x32_bf16 v[20:23],v[188:191],v[60:63],v[20:23]
buffer_load_dwordx4 v2,s[32:35],s63 offen offset:0 sc1 lds
v_mfma_f32_16x16x32_bf16 v[16:19],v[184:187],v[60:63],v[16:19]
ds_read_b128 v[52:55],v2 offset:21504
v_mfma_f32_16x16x32_bf16 v[16:19],v[72:75],v[56:59],v[16:19]
s_waitcnt lgkmcnt(4)
v_mfma_f32_16x16x32_bf16 v[16:19],a[48:51],v[32:35],v[16:19]
ds_read_b128 v[56:59],v2 offset:35840
v_mfma_f32_16x16x32_bf16 v[16:19],a[160:163],v[36:39],v[16:19]
buffer_load_dwordx4 v2,s[32:35],s63 offen offset:1024 sc1 lds
v_mfma_f32_16x16x32_bf16 v[20:23],a[164:167],v[36:39],v[20:23]
ds_read_b128 v[60:63],v2 offset:50176
v_mfma_f32_16x16x32_bf16 v[20:23],a[52:55],v[32:35],v[20:23]
s_waitcnt lgkmcnt(4)
v_mfma_f32_16x16x32_bf16 v[20:23],v[84:87],v[40:43],v[20:23]
ds_read_b128 v[32:35],v2 offset:8192
v_mfma_f32_16x16x32_bf16 v[20:23],v[196:199],v[44:47],v[20:23]
buffer_load_dwordx4 v2,s[32:35],s63 offen offset:2048 sc1 lds
v_mfma_f32_16x16x32_bf16 v[16:19],v[192:195],v[44:47],v[16:19]
ds_read_b128 v[36:39],v2 offset:22528
v_mfma_f32_16x16x32_bf16 v[16:19],v[80:83],v[40:43],v[16:19]
s_waitcnt lgkmcnt(4)
v_mfma_f32_16x16x32_bf16 v[16:19],a[56:59],v[48:51],v[16:19]
ds_read_b128 v[40:43],v2 offset:36864
v_mfma_f32_16x16x32_bf16 v[16:19],a[168:171],v[52:55],v[16:19]
buffer_load_dwordx4 v2,s[32:35],s63 offen offset:3072 sc1 lds
v_mfma_f32_16x16x32_bf16 v[20:23],a[172:175],v[52:55],v[20:23]
ds_read_b128 v[44:47],v2 offset:51200
v_mfma_f32_16x16x32_bf16 v[20:23],a[60:63],v[48:51],v[20:23]
s_waitcnt lgkmcnt(4)
v_mfma_f32_16x16x32_bf16 v[20:23],v[92:95],v[56:59],v[20:23]
ds_read_b128 v[48:51],v2 offset:9216
v_mfma_f32_16x16x32_bf16 v[20:23],v[204:207],v[60:63],v[20:23]
s_add_u32 s63,s63,4096
s_add_u32 m0,s64,65536
v_mfma_f32_16x16x32_bf16 v[16:19],v[200:203],v[60:63],v[16:19]
ds_read_b128 v[52:55],v2 offset:23552
v_mfma_f32_16x16x32_bf16 v[16:19],v[88:91],v[56:59],v[16:19]
s_waitcnt lgkmcnt(4)
v_mfma_f32_16x16x32_bf16 v[16:19],a[64:67],v[32:35],v[16:19]
ds_read_b128 v[56:59],v2 offset:37888
v_mfma_f32_16x16x32_bf16 v[16:19],a[176:179],v[36:39],v[16:19]
buffer_load_dwordx4 v2,s[32:35],s63 offen offset:0 sc1 lds
v_mfma_f32_16x16x32_bf16 v[20:23],a[180:183],v[36:39],v[20:23]
ds_read_b128 v[60:63],v2 offset:52224
v_mfma_f32_16x16x32_bf16 v[20:23],a[68:71],v[32:35],v[20:23]
s_waitcnt lgkmcnt(4)
v_mfma_f32_16x16x32_bf16 v[20:23],v[100:103],v[40:43],v[20:23]
ds_read_b128 v[32:35],v2 offset:10240
v_mfma_f32_16x16x32_bf16 v[20:23],v[212:215],v[44:47],v[20:23]
buffer_load_dwordx4 v2,s[32:35],s63 offen offset:1024 sc1 lds
v_mfma_f32_16x16x32_bf16 v[16:19],v[208:211],v[44:47],v[16:19]
ds_read_b128 v[36:39],v2 offset:24576
v_mfma_f32_16x16x32_bf16 v[16:19],v[96:99],v[40:43],v[16:19]
s_waitcnt lgkmcnt(4)
v_mfma_f32_16x16x32_bf16 v[16:19],a[72:75],v[48:51],v[16:19]
ds_read_b128 v[40:43],v2 offset:38912
v_mfma_f32_16x16x32_bf16 v[16:19],a[184:187],v[52:55],v[16:19]
buffer_load_dwordx4 v2,s[32:35],s63 offen offset:2048 sc1 lds
v_mfma_f32_16x16x32_bf16 v[20:23],a[188:191],v[52:55],v[20:23]
ds_read_b128 v[44:47],v2 offset:53248
v_mfma_f32_16x16x32_bf16 v[20:23],a[76:79],v[48:51],v[20:23]
s_waitcnt lgkmcnt(4)
v_mfma_f32_16x16x32_bf16 v[20:23],v[108:111],v[56:59],v[20:23]
ds_read_b128 v[48:51],v2 offset:11264
v_mfma_f32_16x16x32_bf16 v[20:23],v[220:223],v[60:63],v[20:23]
v_mfma_f32_16x16x32_bf16 v[16:19],v[216:219],v[60:63],v[16:19]
ds_read_b128 v[52:55],v2 offset:25600
v_mfma_f32_16x16x32_bf16 v[16:19],v[104:107],v[56:59],v[16:19]
s_waitcnt lgkmcnt(4)
v_mfma_f32_16x16x32_bf16 v[16:19],a[80:83],v[32:35],v[16:19]
ds_read_b128 v[56:59],v2 offset:39936
v_mfma_f32_16x16x32_bf16 v[16:19],a[192:195],v[36:39],v[16:19]
buffer_load_dwordx4 v2,s[32:35],s63 offen offset:3072 sc1 lds
s_add_u32 s63,s63,4096
v_mfma_f32_16x16x32_bf16 v[20:23],a[196:199],v[36:39],v[20:23]
ds_read_b128 v[60:63],v2 offset:54272
s_add_u32 m0,s64,69632
v_mfma_f32_16x16x32_bf16 v[20:23],a[84:87],v[32:35],v[20:23]
s_waitcnt lgkmcnt(4)
v_mfma_f32_16x16x32_bf16 v[20:23],v[116:119],v[40:43],v[20:23]
ds_read_b128 v[32:35],v2 offset:12288
v_mfma_f32_16x16x32_bf16 v[20:23],v[228:231],v[44:47],v[20:23]
buffer_load_dwordx4 v2,s[32:35],s63 offen offset:0 sc1 lds
v_mfma_f32_16x16x32_bf16 v[16:19],v[224:227],v[44:47],v[16:19]
ds_read_b128 v[36:39],v2 offset:26624
v_mfma_f32_16x16x32_bf16 v[16:19],v[112:115],v[40:43],v[16:19]
s_waitcnt lgkmcnt(4)
v_mfma_f32_16x16x32_bf16 v[16:19],a[88:91],v[48:51],v[16:19]
ds_read_b128 v[40:43],v2 offset:40960
v_mfma_f32_16x16x32_bf16 v[16:19],a[200:203],v[52:55],v[16:19]
buffer_load_dwordx4 v2,s[32:35],s63 offen offset:1024 sc1 lds
s_add_u32 s63,s63,s31
v_mfma_f32_16x16x32_bf16 v[20:23],a[204:207],v[52:55],v[20:23]
ds_read_b128 v[44:47],v2 offset:55296
v_mfma_f32_16x16x32_bf16 v[20:23],a[92:95],v[48:51],v[20:23]
s_waitcnt lgkmcnt(4)
v_mfma_f32_16x16x32_bf16 v[20:23],v[124:127],v[56:59],v[20:23]
ds_read_b128 v[48:51],v2 offset:13312
v_mfma_f32_16x16x32_bf16 v[20:23],v[236:239],v[60:63],v[20:23]
v_mfma_f32_16x16x32_bf16 v[16:19],v[232:235],v[60:63],v[16:19]
ds_read_b128 v[52:55],v2 offset:27648
v_mfma_f32_16x16x32_bf16 v[16:19],v[120:123],v[56:59],v[16:19]
s_waitcnt lgkmcnt(4)
v_mfma_f32_16x16x32_bf16 v[16:19],a[96:99],v[32:35],v[16:19]
ds_read_b128 v[56:59],v2 offset:41984
v_mfma_f32_16x16x32_bf16 v[16:19],a[208:211],v[36:39],v[16:19]
s_waitcnt vmcnt(10)
s_barrier
v_mfma_f32_16x16x32_bf16 v[20:23],a[212:215],v[36:39],v[20:23]
ds_read_b128 v[60:63],v2 offset:56320
v_mfma_f32_16x16x32_bf16 v[20:23],a[100:103],v[32:35],v[20:23]
s_waitcnt lgkmcnt(4)
v_mfma_f32_16x16x32_bf16 v[20:23],v[132:135],v[40:43],v[20:23]
ds_read_b128 v[32:35],v4 offset:0
v_mfma_f32_16x16x32_bf16 v[20:23],v[244:247],v[44:47],v[20:23]
v_mfma_f32_16x16x32_bf16 v[16:19],v[240:243],v[44:47],v[16:19]
ds_read_b128 v[36:39],v4 offset:14336
v_mfma_f32_16x16x32_bf16 v[16:19],v[128:131],v[40:43],v[16:19]
s_waitcnt lgkmcnt(4)
v_mfma_f32_16x16x32_bf16 v[16:19],a[104:107],v[48:51],v[16:19]
ds_read_b128 v[40:43],v4 offset:28672
v_mfma_f32_16x16x32_bf16 v[16:19],a[216:219],v[52:55],v[16:19]
v_mfma_f32_16x16x32_bf16 v[20:23],a[220:223],v[52:55],v[20:23]
ds_read_b128 v[44:47],v4 offset:43008
v_mfma_f32_16x16x32_bf16 v[20:23],a[108:111],v[48:51],v[20:23]
s_waitcnt lgkmcnt(4)
v_mfma_f32_16x16x32_bf16 v[20:23],v[140:143],v[56:59],v[20:23]
ds_read_b128 v[48:51],v4 offset:1024
v_mfma_f32_16x16x32_bf16 v[20:23],v[252:255],v[60:63],v[20:23]
v_mfma_f32_16x16x32_bf16 v[16:19],v[248:251],v[60:63],v[16:19]
ds_read_b128 v[52:55],v4 offset:15360
s_sub_u32 s62,s62,1
v_mfma_f32_16x16x32_bf16 v[16:19],v[136:139],v[56:59],v[16:19]
s_cbranch_scc0 .L_dbl_2
.L_dbu_2:
s_nop 1
s_nop 0
v_cvt_pk_bf16_f32 v15,v22,v23
v_cvt_pk_bf16_f32 v14,v20,v21
s_nop 1
v_cvt_pk_bf16_f32 v13,v18,v19
v_cvt_pk_bf16_f32 v12,v16,v17
ds_write_b128 v5,v[12:15]
ds_read2st64_b32 v[12:13],v3 offset0:0 offset1:1
ds_read2st64_b32 v[14:15],v3 offset0:2 offset1:3
s_waitcnt lgkmcnt(0)
buffer_atomic_pk_add_bf16 v12,v1,s[44:47],s65 offen offset:0 sc1
s_add_u32 s70,s70,s75
buffer_atomic_pk_add_bf16 v13,v1,s[44:47],s65 offen offset:2048 sc1
s_add_u32 s65,s65,16384
buffer_atomic_pk_add_bf16 v14,v1,s[44:47],s65 offen offset:0 sc1
buffer_atomic_pk_add_bf16 v15,v1,s[44:47],s65 offen offset:2048 sc1
s_and_b32 s65,s70,0x3f8000
s_or_b32 s48,s28,s27
s_or_b32 s48,s48,s26
s_cbranch_scc1 .L_end
v_mov_b32 v8,s54
v_mov_b32 v9,s55
buffer_store_dwordx2 v[8:9],off,s[32:35],0 sc1
s_branch .L_end
.L_dbt_2:
s_nop 1
s_nop 0
v_cvt_pk_bf16_f32 v15,v30,v31
v_cvt_pk_bf16_f32 v14,v28,v29
s_nop 1
v_cvt_pk_bf16_f32 v13,v26,v27
v_cvt_pk_bf16_f32 v12,v24,v25
ds_write_b128 v5,v[12:15]
ds_read2st64_b32 v[12:13],v3 offset0:0 offset1:1
ds_read2st64_b32 v[14:15],v3 offset0:2 offset1:3
)#"
R"#(
s_waitcnt lgkmcnt(0)
buffer_atomic_pk_add_bf16 v12,v1,s[44:47],s65 offen offset:0 sc1
s_add_u32 s70,s70,s68
buffer_atomic_pk_add_bf16 v13,v1,s[44:47],s65 offen offset:2048 sc1
s_add_u32 s65,s65,16384
buffer_atomic_pk_add_bf16 v14,v1,s[44:47],s65 offen offset:0 sc1
s_mov_b32 s68,s75
buffer_atomic_pk_add_bf16 v15,v1,s[44:47],s65 offen offset:2048 sc1
s_and_b32 s65,s70,0x3f8000
s_or_b32 s48,s28,s27
s_or_b32 s48,s48,s26
s_cbranch_scc1 .L_end
v_mov_b32 v8,s54
v_mov_b32 v9,s55
buffer_store_dwordx2 v[8:9],off,s[32:35],0 sc1
s_branch .L_end
.p2align 6
.L_dhv3:
.L_dal_3:
s_waitcnt lgkmcnt(4)
v_mfma_f32_16x16x32_bf16 v[16:19],a[0:3],v[32:35],0
ds_read_b128 v[56:59],v2 offset:29696
v_dot2_f32_bf16 v10,v12,s72,0
v_mfma_f32_16x16x32_bf16 v[16:19],a[112:115],v[36:39],v[16:19]
v_dot2_f32_bf16 v11,v12,v12,0
v_dot2_f32_bf16 v10,v13,s72,v10
v_mfma_f32_16x16x32_bf16 v[20:23],a[116:119],v[36:39],0
ds_read_b128 v[60:63],v2 offset:44032
v_dot2_f32_bf16 v11,v13,v13,v11
v_mfma_f32_16x16x32_bf16 v[20:23],a[4:7],v[32:35],v[20:23]
v_dot2_f32_bf16 v10,v14,s72,v10
s_waitcnt lgkmcnt(4)
v_mfma_f32_16x16x32_bf16 v[20:23],a[228:231],v[40:43],v[20:23]
ds_read_b128 v[32:35],v2 offset:2048
v_dot2_f32_bf16 v11,v14,v14,v11
v_mfma_f32_16x16x32_bf16 v[20:23],v[148:151],v[44:47],v[20:23]
v_dot2_f32_bf16 v10,v15,s72,v10
v_dot2_f32_bf16 v11,v15,v15,v11
v_mfma_f32_16x16x32_bf16 v[16:19],v[144:147],v[44:47],v[16:19]
ds_read_b128 v[36:39],v2 offset:16384
v_mfma_f32_16x16x32_bf16 v[16:19],a[224:227],v[40:43],v[16:19]
v_permlane16_swap_b32 v10,v11
s_waitcnt lgkmcnt(4)
v_mfma_f32_16x16x32_bf16 v[16:19],a[8:11],v[48:51],v[16:19]
ds_read_b128 v[40:43],v2 offset:30720
v_add_f32 v10,v10,v11
v_mfma_f32_16x16x32_bf16 v[16:19],a[120:123],v[52:55],v[16:19]
v_mfma_f32_16x16x32_bf16 v[20:23],a[124:127],v[52:55],v[20:23]
ds_read_b128 v[44:47],v2 offset:45056
v_permlane32_swap_b32 v10,v11
v_mfma_f32_16x16x32_bf16 v[20:23],a[12:15],v[48:51],v[20:23]
v_add_f32 v10,v10,v11
s_waitcnt lgkmcnt(4)
v_mfma_f32_16x16x32_bf16 v[20:23],a[236:239],v[56:59],v[20:23]
ds_read_b128 v[48:51],v2 offset:3072
v_mfma_f32_16x16x32_bf16 v[20:23],v[156:159],v[60:63],v[20:23]
buffer_store_dword v10,v3,s[32:35],s67 offen offset:1024 sc1
v_mfma_f32_16x16x32_bf16 v[16:19],v[152:155],v[60:63],v[16:19]
ds_read_b128 v[52:55],v2 offset:17408
v_mfma_f32_16x16x32_bf16 v[16:19],a[232:235],v[56:59],v[16:19]
s_waitcnt lgkmcnt(4)
v_mfma_f32_16x16x32_bf16 v[16:19],a[16:19],v[32:35],v[16:19]
s_waitcnt vmcnt(2)
s_barrier
v_mfma_f32_16x16x32_bf16 v[16:19],a[128:131],v[36:39],v[16:19]
ds_read_b128 v[56:59],v2 offset:31744
v_mfma_f32_16x16x32_bf16 v[20:23],a[132:135],v[36:39],v[20:23]
ds_read_b128 v[60:63],v2 offset:46080
v_mfma_f32_16x16x32_bf16 v[20:23],a[20:23],v[32:35],v[20:23]
s_waitcnt lgkmcnt(4)
v_mfma_f32_16x16x32_bf16 v[20:23],a[244:247],v[40:43],v[20:23]
ds_read_b128 v[32:35],v2 offset:4096
s_mov_b32 m0,s74
v_mfma_f32_16x16x32_bf16 v[20:23],v[164:167],v[44:47],v[20:23]
buffer_load_dwordx4 v2,s[32:35],s71 offen offset:0 sc1 lds
v_mfma_f32_16x16x32_bf16 v[16:19],v[160:163],v[44:47],v[16:19]
ds_read_b128 v[36:39],v2 offset:18432
v_mfma_f32_16x16x32_bf16 v[16:19],a[240:243],v[40:43],v[16:19]
s_waitcnt lgkmcnt(4)
v_mfma_f32_16x16x32_bf16 v[16:19],a[24:27],v[48:51],v[16:19]
ds_read_b128 v[40:43],v2 offset:32768
v_mfma_f32_16x16x32_bf16 v[16:19],a[136:139],v[52:55],v[16:19]
buffer_load_dwordx4 v2,s[32:35],s71 offen offset:1024 sc1 lds
s_add_u32 s71,s71,s30
v_mfma_f32_16x16x32_bf16 v[20:23],a[140:143],v[52:55],v[20:23]
ds_read_b128 v[44:47],v2 offset:47104
s_add_u32 m0,s64,57344
v_mfma_f32_16x16x32_bf16 v[20:23],a[28:31],v[48:51],v[20:23]
s_waitcnt lgkmcnt(4)
v_mfma_f32_16x16x32_bf16 v[20:23],a[252:255],v[56:59],v[20:23]
ds_read_b128 v[48:51],v2 offset:5120
v_mfma_f32_16x16x32_bf16 v[20:23],v[172:175],v[60:63],v[20:23]
buffer_load_dwordx4 v2,s[32:35],s63 offen offset:0 sc1 lds
v_mfma_f32_16x16x32_bf16 v[16:19],v[168:171],v[60:63],v[16:19]
ds_read_b128 v[52:55],v2 offset:19456
v_mfma_f32_16x16x32_bf16 v[16:19],a[248:251],v[56:59],v[16:19]
s_waitcnt lgkmcnt(4)
v_mfma_f32_16x16x32_bf16 v[16:19],a[32:35],v[32:35],v[16:19]
ds_read_b128 v[56:59],v2 offset:33792
v_mfma_f32_16x16x32_bf16 v[16:19],a[144:147],v[36:39],v[16:19]
buffer_load_dwordx4 v2,s[32:35],s63 offen offset:1024 sc1 lds
v_mfma_f32_16x16x32_bf16 v[20:23],a[148:151],v[36:39],v[20:23]
ds_read_b128 v[60:63],v2 offset:48128
v_mfma_f32_16x16x32_bf16 v[20:23],a[36:39],v[32:35],v[20:23]
s_waitcnt lgkmcnt(4)
v_mfma_f32_16x16x32_bf16 v[20:23],v[68:71],v[40:43],v[20:23]
ds_read_b128 v[32:35],v2 offset:6144
v_mfma_f32_16x16x32_bf16 v[20:23],v[180:183],v[44:47],v[20:23]
buffer_load_dwordx4 v2,s[32:35],s63 offen offset:2048 sc1 lds
v_mfma_f32_16x16x32_bf16 v[16:19],v[176:179],v[44:47],v[16:19]
ds_read_b128 v[36:39],v2 offset:20480
v_mfma_f32_16x16x32_bf16 v[16:19],v[64:67],v[40:43],v[16:19]
s_waitcnt lgkmcnt(4)
v_mfma_f32_16x16x32_bf16 v[16:19],a[40:43],v[48:51],v[16:19]
ds_read_b128 v[40:43],v2 offset:34816
v_mfma_f32_16x16x32_bf16 v[16:19],a[152:155],v[52:55],v[16:19]
buffer_load_dwordx4 v2,s[32:35],s63 offen offset:3072 sc1 lds
s_add_u32 s63,s63,4096
v_mfma_f32_16x16x32_bf16 v[20:23],a[156:159],v[52:55],v[20:23]
ds_read_b128 v[44:47],v2 offset:49152
s_add_u32 m0,s64,61440
v_mfma_f32_16x16x32_bf16 v[20:23],a[44:47],v[48:51],v[20:23]
s_waitcnt lgkmcnt(4)
v_mfma_f32_16x16x32_bf16 v[20:23],v[76:79],v[56:59],v[20:23]
ds_read_b128 v[48:51],v2 offset:7168
v_mfma_f32_16x16x32_bf16 v[20:23],v[188:191],v[60:63],v[20:23]
buffer_load_dwordx4 v2,s[32:35],s63 offen offset:0 sc1 lds
v_mfma_f32_16x16x32_bf16 v[16:19],v[184:187],v[60:63],v[16:19]
ds_read_b128 v[52:55],v2 offset:21504
v_mfma_f32_16x16x32_bf16 v[16:19],v[72:75],v[56:59],v[16:19]
s_waitcnt lgkmcnt(4)
v_mfma_f32_16x16x32_bf16 v[16:19],a[48:51],v[32:35],v[16:19]
ds_read_b128 v[56:59],v2 offset:35840
v_mfma_f32_16x16x32_bf16 v[16:19],a[160:163],v[36:39],v[16:19]
buffer_load_dwordx4 v2,s[32:35],s63 offen offset:1024 sc1 lds
v_mfma_f32_16x16x32_bf16 v[20:23],a[164:167],v[36:39],v[20:23]
ds_read_b128 v[60:63],v2 offset:50176
v_mfma_f32_16x16x32_bf16 v[20:23],a[52:55],v[32:35],v[20:23]
s_waitcnt lgkmcnt(4)
v_mfma_f32_16x16x32_bf16 v[20:23],v[84:87],v[40:43],v[20:23]
ds_read_b128 v[32:35],v2 offset:8192
v_mfma_f32_16x16x32_bf16 v[20:23],v[196:199],v[44:47],v[20:23]
buffer_load_dwordx4 v2,s[32:35],s63 offen offset:2048 sc1 lds
v_mfma_f32_16x16x32_bf16 v[16:19],v[192:195],v[44:47],v[16:19]
ds_read_b128 v[36:39],v2 offset:22528
v_mfma_f32_16x16x32_bf16 v[16:19],v[80:83],v[40:43],v[16:19]
s_waitcnt lgkmcnt(4)
v_mfma_f32_16x16x32_bf16 v[16:19],a[56:59],v[48:51],v[16:19]
ds_read_b128 v[40:43],v2 offset:36864
v_mfma_f32_16x16x32_bf16 v[16:19],a[168:171],v[52:55],v[16:19]
buffer_load_dwordx4 v2,s[32:35],s63 offen offset:3072 sc1 lds
v_mfma_f32_16x16x32_bf16 v[20:23],a[172:175],v[52:55],v[20:23]
ds_read_b128 v[44:47],v2 offset:51200
v_mfma_f32_16x16x32_bf16 v[20:23],a[60:63],v[48:51],v[20:23]
s_waitcnt lgkmcnt(4)
v_mfma_f32_16x16x32_bf16 v[20:23],v[92:95],v[56:59],v[20:23]
ds_read_b128 v[48:51],v2 offset:9216
v_mfma_f32_16x16x32_bf16 v[20:23],v[204:207],v[60:63],v[20:23]
s_add_u32 s63,s63,4096
s_add_u32 m0,s64,65536
v_mfma_f32_16x16x32_bf16 v[16:19],v[200:203],v[60:63],v[16:19]
ds_read_b128 v[52:55],v2 offset:23552
v_mfma_f32_16x16x32_bf16 v[16:19],v[88:91],v[56:59],v[16:19]
s_waitcnt lgkmcnt(4)
v_mfma_f32_16x16x32_bf16 v[16:19],a[64:67],v[32:35],v[16:19]
ds_read_b128 v[56:59],v2 offset:37888
v_mfma_f32_16x16x32_bf16 v[16:19],a[176:179],v[36:39],v[16:19]
buffer_load_dwordx4 v2,s[32:35],s63 offen offset:0 sc1 lds
v_mfma_f32_16x16x32_bf16 v[20:23],a[180:183],v[36:39],v[20:23]
ds_read_b128 v[60:63],v2 offset:52224
v_mfma_f32_16x16x32_bf16 v[20:23],a[68:71],v[32:35],v[20:23]
s_waitcnt lgkmcnt(4)
v_mfma_f32_16x16x32_bf16 v[20:23],v[100:103],v[40:43],v[20:23]
ds_read_b128 v[32:35],v2 offset:10240
v_mfma_f32_16x16x32_bf16 v[20:23],v[212:215],v[44:47],v[20:23]
buffer_load_dwordx4 v2,s[32:35],s63 offen offset:1024 sc1 lds
v_mfma_f32_16x16x32_bf16 v[16:19],v[208:211],v[44:47],v[16:19]
ds_read_b128 v[36:39],v2 offset:24576
v_mfma_f32_16x16x32_bf16 v[16:19],v[96:99],v[40:43],v[16:19]
s_waitcnt lgkmcnt(4)
v_mfma_f32_16x16x32_bf16 v[16:19],a[72:75],v[48:51],v[16:19]
ds_read_b128 v[40:43],v2 offset:38912
s_waitcnt vmcnt(10)
v_mfma_f32_16x16x32_bf16 v[16:19],a[184:187],v[52:55],v[16:19]
buffer_load_dwordx4 v2,s[32:35],s63 offen offset:2048 sc1 lds
ds_read_b128 v[12:15],v5 offset:0
v_mfma_f32_16x16x32_bf16 v[20:23],a[188:191],v[52:55],v[20:23]
ds_read_b128 v[44:47],v2 offset:53248
v_mfma_f32_16x16x32_bf16 v[20:23],a[76:79],v[48:51],v[20:23]
s_waitcnt lgkmcnt(5)
v_mfma_f32_16x16x32_bf16 v[20:23],v[108:111],v[56:59],v[20:23]
ds_read_b128 v[48:51],v2 offset:11264
v_mfma_f32_16x16x32_bf16 v[20:23],v[220:223],v[60:63],v[20:23]
v_mfma_f32_16x16x32_bf16 v[16:19],v[216:219],v[60:63],v[16:19]
ds_read_b128 v[52:55],v2 offset:25600
s_waitcnt lgkmcnt(3)
v_mfma_f32_16x16x32_bf16 v[16:19],v[104:107],v[56:59],v[16:19]
v_add_f32 v24,v24,v12
v_mfma_f32_16x16x32_bf16 v[16:19],a[80:83],v[32:35],v[16:19]
ds_read_b128 v[56:59],v2 offset:39936
v_add_f32 v25,v25,v13
v_mfma_f32_16x16x32_bf16 v[16:19],a[192:195],v[36:39],v[16:19]
buffer_load_dwordx4 v2,s[32:35],s63 offen offset:3072 sc1 lds
s_add_u32 s63,s63,4096
v_mfma_f32_16x16x32_bf16 v[20:23],a[196:199],v[36:39],v[20:23]
ds_read_b128 v[60:63],v2 offset:54272
s_add_u32 m0,s64,69632
v_mfma_f32_16x16x32_bf16 v[20:23],a[84:87],v[32:35],v[20:23]
v_add_f32 v26,v26,v14
s_waitcnt lgkmcnt(4)
v_mfma_f32_16x16x32_bf16 v[20:23],v[116:119],v[40:43],v[20:23]
ds_read_b128 v[32:35],v2 offset:12288
v_add_f32 v27,v27,v15
v_mfma_f32_16x16x32_bf16 v[20:23],v[228:231],v[44:47],v[20:23]
buffer_load_dwordx4 v2,s[32:35],s63 offen offset:0 sc1 lds
ds_read_b128 v[12:15],v5 offset:1024
v_mfma_f32_16x16x32_bf16 v[16:19],v[224:227],v[44:47],v[16:19]
ds_read_b128 v[36:39],v2 offset:26624
v_mfma_f32_16x16x32_bf16 v[16:19],v[112:115],v[40:43],v[16:19]
s_waitcnt lgkmcnt(5)
v_mfma_f32_16x16x32_bf16 v[16:19],a[88:91],v[48:51],v[16:19]
ds_read_b128 v[40:43],v2 offset:40960
v_mfma_f32_16x16x32_bf16 v[16:19],a[200:203],v[52:55],v[16:19]
buffer_load_dwordx4 v2,s[32:35],s63 offen offset:1024 sc1 lds
s_add_u32 s63,s63,s31
v_mfma_f32_16x16x32_bf16 v[20:23],a[204:207],v[52:55],v[20:23]
ds_read_b128 v[44:47],v2 offset:55296
s_waitcnt lgkmcnt(3)
v_mfma_f32_16x16x32_bf16 v[20:23],a[92:95],v[48:51],v[20:23]
v_add_f32 v28,v28,v12
v_mfma_f32_16x16x32_bf16 v[20:23],v[124:127],v[56:59],v[20:23]
ds_read_b128 v[48:51],v2 offset:13312
v_add_f32 v29,v29,v13
v_mfma_f32_16x16x32_bf16 v[20:23],v[236:239],v[60:63],v[20:23]
v_add_f32 v30,v30,v14
v_add_f32 v31,v31,v15
v_mfma_f32_16x16x32_bf16 v[16:19],v[232:235],v[60:63],v[16:19]
ds_read_b128 v[52:55],v2 offset:27648
v_cvt_pk_bf16_f32 v12,v24,v25
v_mfma_f32_16x16x32_bf16 v[16:19],v[120:123],v[56:59],v[16:19]
v_cvt_pk_bf16_f32 v13,v26,v27
s_waitcnt lgkmcnt(4)
v_mfma_f32_16x16x32_bf16 v[16:19],a[96:99],v[32:35],v[16:19]
s_waitcnt vmcnt(10)
s_barrier
v_mfma_f32_16x16x32_bf16 v[16:19],a[208:211],v[36:39],v[16:19]
ds_read_b128 v[56:59],v2 offset:41984
v_cvt_pk_bf16_f32 v14,v28,v29
v_mfma_f32_16x16x32_bf16 v[20:23],a[212:215],v[36:39],v[20:23]
ds_read_b128 v[60:63],v2 offset:56320
v_cvt_pk_bf16_f32 v15,v30,v31
v_mfma_f32_16x16x32_bf16 v[20:23],a[100:103],v[32:35],v[20:23]
s_waitcnt lgkmcnt(4)
v_mfma_f32_16x16x32_bf16 v[20:23],v[132:135],v[40:43],v[20:23]
ds_read_b128 v[32:35],v4 offset:0
v_mfma_f32_16x16x32_bf16 v[20:23],v[244:247],v[44:47],v[20:23]
buffer_store_dwordx4 v[12:15],v2,s[32:35],s68 offen sc1
v_mfma_f32_16x16x32_bf16 v[16:19],v[240:243],v[44:47],v[16:19]
ds_read_b128 v[36:39],v4 offset:14336
v_mfma_f32_16x16x32_bf16 v[16:19],v[128:131],v[40:43],v[16:19]
s_waitcnt lgkmcnt(4)
v_mfma_f32_16x16x32_bf16 v[16:19],a[104:107],v[48:51],v[16:19]
ds_read_b128 v[40:43],v4 offset:28672
v_mfma_f32_16x16x32_bf16 v[16:19],a[216:219],v[52:55],v[16:19]
v_mfma_f32_16x16x32_bf16 v[20:23],a[220:223],v[52:55],v[20:23]
ds_read_b128 v[44:47],v4 offset:43008
s_mov_b32 s67,s68
v_mfma_f32_16x16x32_bf16 v[20:23],a[108:111],v[48:51],v[20:23]
s_mov_b32 s68,s70
s_waitcnt lgkmcnt(4)
v_mfma_f32_16x16x32_bf16 v[20:23],v[140:143],v[56:59],v[20:23]
ds_read_b128 v[48:51],v4 offset:1024
s_add_u32 s70,s70,1187840
v_mfma_f32_16x16x32_bf16 v[20:23],v[252:255],v[60:63],v[20:23]
v_mfma_f32_16x16x32_bf16 v[16:19],v[248:251],v[60:63],v[16:19]
ds_read_b128 v[52:55],v4 offset:15360
s_sub_u32 s62,s62,1
v_mfma_f32_16x16x32_bf16 v[16:19],v[136:139],v[56:59],v[16:19]
s_cbranch_scc1 .L_dab_3
s_waitcnt lgkmcnt(4)
v_mfma_f32_16x16x32_bf16 v[24:27],a[0:3],v[32:35],0
ds_read_b128 v[56:59],v4 offset:29696
v_dot2_f32_bf16 v10,v12,s72,0
v_mfma_f32_16x16x32_bf16 v[24:27],a[112:115],v[36:39],v[24:27]
v_dot2_f32_bf16 v11,v12,v12,0
v_dot2_f32_bf16 v10,v13,s72,v10
v_mfma_f32_16x16x32_bf16 v[28:31],a[116:119],v[36:39],0
ds_read_b128 v[60:63],v4 offset:44032
v_dot2_f32_bf16 v11,v13,v13,v11
v_mfma_f32_16x16x32_bf16 v[28:31],a[4:7],v[32:35],v[28:31]
v_dot2_f32_bf16 v10,v14,s72,v10
s_waitcnt lgkmcnt(4)
v_mfma_f32_16x16x32_bf16 v[28:31],a[228:231],v[40:43],v[28:31]
ds_read_b128 v[32:35],v4 offset:2048
v_dot2_f32_bf16 v11,v14,v14,v11
v_mfma_f32_16x16x32_bf16 v[28:31],v[148:151],v[44:47],v[28:31]
v_dot2_f32_bf16 v10,v15,s72,v10
v_dot2_f32_bf16 v11,v15,v15,v11
v_mfma_f32_16x16x32_bf16 v[24:27],v[144:147],v[44:47],v[24:27]
ds_read_b128 v[36:39],v4 offset:16384
v_mfma_f32_16x16x32_bf16 v[24:27],a[224:227],v[40:43],v[24:27]
v_permlane16_swap_b32 v10,v11
s_waitcnt lgkmcnt(4)
v_mfma_f32_16x16x32_bf16 v[24:27],a[8:11],v[48:51],v[24:27]
ds_read_b128 v[40:43],v4 offset:30720
v_add_f32 v10,v10,v11
v_mfma_f32_16x16x32_bf16 v[24:27],a[120:123],v[52:55],v[24:27]
v_mfma_f32_16x16x32_bf16 v[28:31],a[124:127],v[52:55],v[28:31]
ds_read_b128 v[44:47],v4 offset:45056
v_permlane32_swap_b32 v10,v11
v_mfma_f32_16x16x32_bf16 v[28:31],a[12:15],v[48:51],v[28:31]
v_add_f32 v10,v10,v11
s_waitcnt lgkmcnt(4)
v_mfma_f32_16x16x32_bf16 v[28:31],a[236:239],v[56:59],v[28:31]
ds_read_b128 v[48:51],v4 offset:3072
v_mfma_f32_16x16x32_bf16 v[28:31],v[156:159],v[60:63],v[28:31]
buffer_store_dword v10,v3,s[32:35],s67 offen offset:1024 sc1
v_mfma_f32_16x16x32_bf16 v[24:27],v[152:155],v[60:63],v[24:27]
ds_read_b128 v[52:55],v4 offset:17408
v_mfma_f32_16x16x32_bf16 v[24:27],a[232:235],v[56:59],v[24:27]
s_waitcnt lgkmcnt(4)
v_mfma_f32_16x16x32_bf16 v[24:27],a[16:19],v[32:35],v[24:27]
s_waitcnt vmcnt(2)
s_barrier
v_mfma_f32_16x16x32_bf16 v[24:27],a[128:131],v[36:39],v[24:27]
ds_read_b128 v[56:59],v4 offset:31744
v_mfma_f32_16x16x32_bf16 v[28:31],a[132:135],v[36:39],v[28:31]
ds_read_b128 v[60:63],v4 offset:46080
v_mfma_f32_16x16x32_bf16 v[28:31],a[20:23],v[32:35],v[28:31]
s_waitcnt lgkmcnt(4)
v_mfma_f32_16x16x32_bf16 v[28:31],a[244:247],v[40:43],v[28:31]
ds_read_b128 v[32:35],v4 offset:4096
s_mov_b32 m0,s74
v_mfma_f32_16x16x32_bf16 v[28:31],v[164:167],v[44:47],v[28:31]
buffer_load_dwordx4 v2,s[32:35],s71 offen offset:0 sc1 lds
v_mfma_f32_16x16x32_bf16 v[24:27],v[160:163],v[44:47],v[24:27]
ds_read_b128 v[36:39],v4 offset:18432
v_mfma_f32_16x16x32_bf16 v[24:27],a[240:243],v[40:43],v[24:27]
s_waitcnt lgkmcnt(4)
v_mfma_f32_16x16x32_bf16 v[24:27],a[24:27],v[48:51],v[24:27]
ds_read_b128 v[40:43],v4 offset:32768
v_mfma_f32_16x16x32_bf16 v[24:27],a[136:139],v[52:55],v[24:27]
buffer_load_dwordx4 v2,s[32:35],s71 offen offset:1024 sc1 lds
s_add_u32 s71,s71,s30
v_mfma_f32_16x16x32_bf16 v[28:31],a[140:143],v[52:55],v[28:31]
ds_read_b128 v[44:47],v4 offset:47104
s_add_u32 m0,s64,0
v_mfma_f32_16x16x32_bf16 v[28:31],a[28:31],v[48:51],v[28:31]
s_waitcnt lgkmcnt(4)
v_mfma_f32_16x16x32_bf16 v[28:31],a[252:255],v[56:59],v[28:31]
ds_read_b128 v[48:51],v4 offset:5120
v_mfma_f32_16x16x32_bf16 v[28:31],v[172:175],v[60:63],v[28:31]
buffer_load_dwordx4 v2,s[32:35],s63 offen offset:0 sc1 lds
v_mfma_f32_16x16x32_bf16 v[24:27],v[168:171],v[60:63],v[24:27]
ds_read_b128 v[52:55],v4 offset:19456
v_mfma_f32_16x16x32_bf16 v[24:27],a[248:251],v[56:59],v[24:27]
s_waitcnt lgkmcnt(4)
v_mfma_f32_16x16x32_bf16 v[24:27],a[32:35],v[32:35],v[24:27]
ds_read_b128 v[56:59],v4 offset:33792
v_mfma_f32_16x16x32_bf16 v[24:27],a[144:147],v[36:39],v[24:27]
buffer_load_dwordx4 v2,s[32:35],s63 offen offset:1024 sc1 lds
v_mfma_f32_16x16x32_bf16 v[28:31],a[148:151],v[36:39],v[28:31]
ds_read_b128 v[60:63],v4 offset:48128
v_mfma_f32_16x16x32_bf16 v[28:31],a[36:39],v[32:35],v[28:31]
s_waitcnt lgkmcnt(4)
v_mfma_f32_16x16x32_bf16 v[28:31],v[68:71],v[40:43],v[28:31]
ds_read_b128 v[32:35],v4 offset:6144
v_mfma_f32_16x16x32_bf16 v[28:31],v[180:183],v[44:47],v[28:31]
buffer_load_dwordx4 v2,s[32:35],s63 offen offset:2048 sc1 lds
v_mfma_f32_16x16x32_bf16 v[24:27],v[176:179],v[44:47],v[24:27]
ds_read_b128 v[36:39],v4 offset:20480
v_mfma_f32_16x16x32_bf16 v[24:27],v[64:67],v[40:43],v[24:27]
s_waitcnt lgkmcnt(4)
v_mfma_f32_16x16x32_bf16 v[24:27],a[40:43],v[48:51],v[24:27]
ds_read_b128 v[40:43],v4 offset:34816
v_mfma_f32_16x16x32_bf16 v[24:27],a[152:155],v[52:55],v[24:27]
buffer_load_dwordx4 v2,s[32:35],s63 offen offset:3072 sc1 lds
s_add_u32 s63,s63,4096
v_mfma_f32_16x16x32_bf16 v[28:31],a[156:159],v[52:55],v[28:31]
ds_read_b128 v[44:47],v4 offset:49152
s_add_u32 m0,s64,4096
v_mfma_f32_16x16x32_bf16 v[28:31],a[44:47],v[48:51],v[28:31]
s_waitcnt lgkmcnt(4)
v_mfma_f32_16x16x32_bf16 v[28:31],v[76:79],v[56:59],v[28:31]
ds_read_b128 v[48:51],v4 offset:7168
v_mfma_f32_16x16x32_bf16 v[28:31],v[188:191],v[60:63],v[28:31]
buffer_load_dwordx4 v2,s[32:35],s63 offen offset:0 sc1 lds
v_mfma_f32_16x16x32_bf16 v[24:27],v[184:187],v[60:63],v[24:27]
ds_read_b128 v[52:55],v4 offset:21504
v_mfma_f32_16x16x32_bf16 v[24:27],v[72:75],v[56:59],v[24:27]
s_waitcnt lgkmcnt(4)
v_mfma_f32_16x16x32_bf16 v[24:27],a[48:51],v[32:35],v[24:27]
ds_read_b128 v[56:59],v4 offset:35840
v_mfma_f32_16x16x32_bf16 v[24:27],a[160:163],v[36:39],v[24:27]
buffer_load_dwordx4 v2,s[32:35],s63 offen offset:1024 sc1 lds
v_mfma_f32_16x16x32_bf16 v[28:31],a[164:167],v[36:39],v[28:31]
ds_read_b128 v[60:63],v4 offset:50176
v_mfma_f32_16x16x32_bf16 v[28:31],a[52:55],v[32:35],v[28:31]
s_waitcnt lgkmcnt(4)
v_mfma_f32_16x16x32_bf16 v[28:31],v[84:87],v[40:43],v[28:31]
ds_read_b128 v[32:35],v4 offset:8192
v_mfma_f32_16x16x32_bf16 v[28:31],v[196:199],v[44:47],v[28:31]
buffer_load_dwordx4 v2,s[32:35],s63 offen offset:2048 sc1 lds
v_mfma_f32_16x16x32_bf16 v[24:27],v[192:195],v[44:47],v[24:27]
ds_read_b128 v[36:39],v4 offset:22528
v_mfma_f32_16x16x32_bf16 v[24:27],v[80:83],v[40:43],v[24:27]
s_waitcnt lgkmcnt(4)
v_mfma_f32_16x16x32_bf16 v[24:27],a[56:59],v[48:51],v[24:27]
ds_read_b128 v[40:43],v4 offset:36864
v_mfma_f32_16x16x32_bf16 v[24:27],a[168:171],v[52:55],v[24:27]
buffer_load_dwordx4 v2,s[32:35],s63 offen offset:3072 sc1 lds
v_mfma_f32_16x16x32_bf16 v[28:31],a[172:175],v[52:55],v[28:31]
ds_read_b128 v[44:47],v4 offset:51200
v_mfma_f32_16x16x32_bf16 v[28:31],a[60:63],v[48:51],v[28:31]
s_waitcnt lgkmcnt(4)
v_mfma_f32_16x16x32_bf16 v[28:31],v[92:95],v[56:59],v[28:31]
ds_read_b128 v[48:51],v4 offset:9216
v_mfma_f32_16x16x32_bf16 v[28:31],v[204:207],v[60:63],v[28:31]
s_add_u32 s63,s63,4096
s_add_u32 m0,s64,8192
v_mfma_f32_16x16x32_bf16 v[24:27],v[200:203],v[60:63],v[24:27]
ds_read_b128 v[52:55],v4 offset:23552
v_mfma_f32_16x16x32_bf16 v[24:27],v[88:91],v[56:59],v[24:27]
s_waitcnt lgkmcnt(4)
v_mfma_f32_16x16x32_bf16 v[24:27],a[64:67],v[32:35],v[24:27]
ds_read_b128 v[56:59],v4 offset:37888
v_mfma_f32_16x16x32_bf16 v[24:27],a[176:179],v[36:39],v[24:27]
buffer_load_dwordx4 v2,s[32:35],s63 offen offset:0 sc1 lds
v_mfma_f32_16x16x32_bf16 v[28:31],a[180:183],v[36:39],v[28:31]
ds_read_b128 v[60:63],v4 offset:52224
v_mfma_f32_16x16x32_bf16 v[28:31],a[68:71],v[32:35],v[28:31]
s_waitcnt lgkmcnt(4)
v_mfma_f32_16x16x32_bf16 v[28:31],v[100:103],v[40:43],v[28:31]
ds_read_b128 v[32:35],v4 offset:10240
v_mfma_f32_16x16x32_bf16 v[28:31],v[212:215],v[44:47],v[28:31]
buffer_load_dwordx4 v2,s[32:35],s63 offen offset:1024 sc1 lds
v_mfma_f32_16x16x32_bf16 v[24:27],v[208:211],v[44:47],v[24:27]
ds_read_b128 v[36:39],v4 offset:24576
v_mfma_f32_16x16x32_bf16 v[24:27],v[96:99],v[40:43],v[24:27]
s_waitcnt lgkmcnt(4)
v_mfma_f32_16x16x32_bf16 v[24:27],a[72:75],v[48:51],v[24:27]
ds_read_b128 v[40:43],v4 offset:38912
s_waitcnt vmcnt(10)
v_mfma_f32_16x16x32_bf16 v[24:27],a[184:187],v[52:55],v[24:27]
buffer_load_dwordx4 v2,s[32:35],s63 offen offset:2048 sc1 lds
ds_read_b128 v[12:15],v5 offset:0
v_mfma_f32_16x16x32_bf16 v[28:31],a[188:191],v[52:55],v[28:31]
ds_read_b128 v[44:47],v4 offset:53248
v_mfma_f32_16x16x32_bf16 v[28:31],a[76:79],v[48:51],v[28:31]
s_waitcnt lgkmcnt(5)
v_mfma_f32_16x16x32_bf16 v[28:31],v[108:111],v[56:59],v[28:31]
ds_read_b128 v[48:51],v4 offset:11264
v_mfma_f32_16x16x32_bf16 v[28:31],v[220:223],v[60:63],v[28:31]
v_mfma_f32_16x16x32_bf16 v[24:27],v[216:219],v[60:63],v[24:27]
ds_read_b128 v[52:55],v4 offset:25600
s_waitcnt lgkmcnt(3)
v_mfma_f32_16x16x32_bf16 v[24:27],v[104:107],v[56:59],v[24:27]
v_add_f32 v16,v16,v12
v_mfma_f32_16x16x32_bf16 v[24:27],a[80:83],v[32:35],v[24:27]
ds_read_b128 v[56:59],v4 offset:39936
v_add_f32 v17,v17,v13
v_mfma_f32_16x16x32_bf16 v[24:27],a[192:195],v[36:39],v[24:27]
buffer_load_dwordx4 v2,s[32:35],s63 offen offset:3072 sc1 lds
s_add_u32 s63,s63,4096
v_mfma_f32_16x16x32_bf16 v[28:31],a[196:199],v[36:39],v[28:31]
ds_read_b128 v[60:63],v4 offset:54272
s_add_u32 m0,s64,12288
v_mfma_f32_16x16x32_bf16 v[28:31],a[84:87],v[32:35],v[28:31]
v_add_f32 v18,v18,v14
s_waitcnt lgkmcnt(4)
v_mfma_f32_16x16x32_bf16 v[28:31],v[116:119],v[40:43],v[28:31]
ds_read_b128 v[32:35],v4 offset:12288
v_add_f32 v19,v19,v15
v_mfma_f32_16x16x32_bf16 v[28:31],v[228:231],v[44:47],v[28:31]
buffer_load_dwordx4 v2,s[32:35],s63 offen offset:0 sc1 lds
ds_read_b128 v[12:15],v5 offset:1024
v_mfma_f32_16x16x32_bf16 v[24:27],v[224:227],v[44:47],v[24:27]
ds_read_b128 v[36:39],v4 offset:26624
v_mfma_f32_16x16x32_bf16 v[24:27],v[112:115],v[40:43],v[24:27]
s_waitcnt lgkmcnt(5)
v_mfma_f32_16x16x32_bf16 v[24:27],a[88:91],v[48:51],v[24:27]
ds_read_b128 v[40:43],v4 offset:40960
v_mfma_f32_16x16x32_bf16 v[24:27],a[200:203],v[52:55],v[24:27]
buffer_load_dwordx4 v2,s[32:35],s63 offen offset:1024 sc1 lds
s_add_u32 s63,s63,s31
v_mfma_f32_16x16x32_bf16 v[28:31],a[204:207],v[52:55],v[28:31]
ds_read_b128 v[44:47],v4 offset:55296
s_waitcnt lgkmcnt(3)
v_mfma_f32_16x16x32_bf16 v[28:31],a[92:95],v[48:51],v[28:31]
v_add_f32 v20,v20,v12
v_mfma_f32_16x16x32_bf16 v[28:31],v[124:127],v[56:59],v[28:31]
ds_read_b128 v[48:51],v4 offset:13312
v_add_f32 v21,v21,v13
v_mfma_f32_16x16x32_bf16 v[28:31],v[236:239],v[60:63],v[28:31]
v_add_f32 v22,v22,v14
v_add_f32 v23,v23,v15
v_mfma_f32_16x16x32_bf16 v[24:27],v[232:235],v[60:63],v[24:27]
ds_read_b128 v[52:55],v4 offset:27648
v_cvt_pk_bf16_f32 v12,v16,v17
v_mfma_f32_16x16x32_bf16 v[24:27],v[120:123],v[56:59],v[24:27]
)#"
R"#(
v_cvt_pk_bf16_f32 v13,v18,v19
s_waitcnt lgkmcnt(4)
v_mfma_f32_16x16x32_bf16 v[24:27],a[96:99],v[32:35],v[24:27]
s_waitcnt vmcnt(10)
s_barrier
v_mfma_f32_16x16x32_bf16 v[24:27],a[208:211],v[36:39],v[24:27]
ds_read_b128 v[56:59],v4 offset:41984
v_cvt_pk_bf16_f32 v14,v20,v21
v_mfma_f32_16x16x32_bf16 v[28:31],a[212:215],v[36:39],v[28:31]
ds_read_b128 v[60:63],v4 offset:56320
v_cvt_pk_bf16_f32 v15,v22,v23
v_mfma_f32_16x16x32_bf16 v[28:31],a[100:103],v[32:35],v[28:31]
s_waitcnt lgkmcnt(4)
v_mfma_f32_16x16x32_bf16 v[28:31],v[132:135],v[40:43],v[28:31]
ds_read_b128 v[32:35],v2 offset:0
v_mfma_f32_16x16x32_bf16 v[28:31],v[244:247],v[44:47],v[28:31]
buffer_store_dwordx4 v[12:15],v2,s[32:35],s69 offen sc1
v_mfma_f32_16x16x32_bf16 v[24:27],v[240:243],v[44:47],v[24:27]
ds_read_b128 v[36:39],v2 offset:14336
v_mfma_f32_16x16x32_bf16 v[24:27],v[128:131],v[40:43],v[24:27]
s_waitcnt lgkmcnt(4)
v_mfma_f32_16x16x32_bf16 v[24:27],a[104:107],v[48:51],v[24:27]
ds_read_b128 v[40:43],v2 offset:28672
v_mfma_f32_16x16x32_bf16 v[24:27],a[216:219],v[52:55],v[24:27]
v_mfma_f32_16x16x32_bf16 v[28:31],a[220:223],v[52:55],v[28:31]
ds_read_b128 v[44:47],v2 offset:43008
s_mov_b32 s67,s69
v_mfma_f32_16x16x32_bf16 v[28:31],a[108:111],v[48:51],v[28:31]
s_mov_b32 s69,s70
s_waitcnt lgkmcnt(4)
v_mfma_f32_16x16x32_bf16 v[28:31],v[140:143],v[56:59],v[28:31]
ds_read_b128 v[48:51],v2 offset:1024
s_add_u32 s70,s70,1187840
v_mfma_f32_16x16x32_bf16 v[28:31],v[252:255],v[60:63],v[28:31]
v_mfma_f32_16x16x32_bf16 v[24:27],v[248:251],v[60:63],v[24:27]
ds_read_b128 v[52:55],v2 offset:15360
v_mfma_f32_16x16x32_bf16 v[24:27],v[136:139],v[56:59],v[24:27]
s_branch .L_dal_3
.L_dab_3:
v_dot2_f32_bf16 v10,v12,s72,0
v_dot2_f32_bf16 v11,v12,v12,0
v_dot2_f32_bf16 v10,v13,s72,v10
v_dot2_f32_bf16 v11,v13,v13,v11
v_dot2_f32_bf16 v10,v14,s72,v10
v_dot2_f32_bf16 v11,v14,v14,v11
v_dot2_f32_bf16 v10,v15,s72,v10
v_dot2_f32_bf16 v11,v15,v15,v11
s_nop 2
v_permlane16_swap_b32 v10,v11
v_add_f32 v10,v10,v11
s_nop 1
v_permlane32_swap_b32 v10,v11
v_add_f32 v10,v10,v11
buffer_store_dword v10,v3,s[32:35],s67 offen offset:1024 sc1
s_mov_b32 s62,s58
s_mov_b32 s70,s59
s_add_u32 s74,s74,8192
v_lshrrev_b32 v5,8,v2
v_lshl_add_u32 v5,v5,4,s74
v_and_b32 v3,128,v2
v_lshl_add_u32 v5,v3,2,v5
v_and_b32 v3,16,v2
v_lshl_add_u32 v5,v3,4,v5
v_and_b32 v3,96,v2
v_lshl_add_u32 v5,v3,1,v5
v_lshrrev_b32 v3,2,v2
v_add_u32 v3,s74,v3
s_add_u32 s66,s66,2375680
s_add_u32 s63,s63,2375680
s_add_u32 s71,s71,2375680
s_mov_b32 s68,98304
s_mov_b32 s34,79446016
.p2align 6
.L_dbl_3:
s_waitcnt lgkmcnt(4)
v_mfma_f32_16x16x32_bf16 v[24:27],a[0:3],v[32:35],0
ds_read_b128 v[56:59],v4 offset:29696
v_mfma_f32_16x16x32_bf16 v[24:27],a[112:115],v[36:39],v[24:27]
v_mfma_f32_16x16x32_bf16 v[28:31],a[116:119],v[36:39],0
ds_read_b128 v[60:63],v4 offset:44032
v_mfma_f32_16x16x32_bf16 v[28:31],a[4:7],v[32:35],v[28:31]
v_cvt_pk_bf16_f32 v15,v22,v23
s_waitcnt lgkmcnt(4)
v_mfma_f32_16x16x32_bf16 v[28:31],a[228:231],v[40:43],v[28:31]
ds_read_b128 v[32:35],v4 offset:2048
v_cvt_pk_bf16_f32 v14,v20,v21
v_mfma_f32_16x16x32_bf16 v[28:31],v[148:151],v[44:47],v[28:31]
v_cvt_pk_bf16_f32 v13,v18,v19
v_cvt_pk_bf16_f32 v12,v16,v17
v_mfma_f32_16x16x32_bf16 v[24:27],v[144:147],v[44:47],v[24:27]
ds_read_b128 v[36:39],v4 offset:16384
ds_write_b128 v5,v[12:15]
v_mfma_f32_16x16x32_bf16 v[24:27],a[224:227],v[40:43],v[24:27]
ds_read2st64_b32 v[12:13],v3 offset0:0 offset1:1
s_waitcnt lgkmcnt(6)
v_mfma_f32_16x16x32_bf16 v[24:27],a[8:11],v[48:51],v[24:27]
ds_read_b128 v[40:43],v4 offset:30720
ds_read2st64_b32 v[14:15],v3 offset0:2 offset1:3
v_mfma_f32_16x16x32_bf16 v[24:27],a[120:123],v[52:55],v[24:27]
v_mfma_f32_16x16x32_bf16 v[28:31],a[124:127],v[52:55],v[28:31]
ds_read_b128 v[44:47],v4 offset:45056
v_mfma_f32_16x16x32_bf16 v[28:31],a[12:15],v[48:51],v[28:31]
s_waitcnt lgkmcnt(7)
v_mfma_f32_16x16x32_bf16 v[28:31],a[236:239],v[56:59],v[28:31]
ds_read_b128 v[48:51],v4 offset:3072
v_mfma_f32_16x16x32_bf16 v[28:31],v[156:159],v[60:63],v[28:31]
s_waitcnt lgkmcnt(2)
buffer_atomic_pk_add_bf16 v12,v1,s[44:47],s65 offen offset:0 sc1
v_mfma_f32_16x16x32_bf16 v[24:27],v[152:155],v[60:63],v[24:27]
ds_read_b128 v[52:55],v4 offset:17408
s_add_u32 s70,s70,s75
v_mfma_f32_16x16x32_bf16 v[24:27],a[232:235],v[56:59],v[24:27]
buffer_atomic_pk_add_bf16 v13,v1,s[44:47],s65 offen offset:2048 sc1
v_mfma_f32_16x16x32_bf16 v[24:27],a[16:19],v[32:35],v[24:27]
s_waitcnt vmcnt(2)
s_barrier
v_mfma_f32_16x16x32_bf16 v[24:27],a[128:131],v[36:39],v[24:27]
ds_read_b128 v[56:59],v4 offset:31744
s_add_u32 s65,s65,16384
v_mfma_f32_16x16x32_bf16 v[28:31],a[132:135],v[36:39],v[28:31]
ds_read_b128 v[60:63],v4 offset:46080
v_mfma_f32_16x16x32_bf16 v[28:31],a[20:23],v[32:35],v[28:31]
buffer_atomic_pk_add_bf16 v14,v1,s[44:47],s65 offen offset:0 sc1
s_waitcnt lgkmcnt(4)
v_mfma_f32_16x16x32_bf16 v[28:31],a[244:247],v[40:43],v[28:31]
ds_read_b128 v[32:35],v4 offset:4096
s_add_u32 m0,s64,0
v_mfma_f32_16x16x32_bf16 v[28:31],v[164:167],v[44:47],v[28:31]
buffer_atomic_pk_add_bf16 v15,v1,s[44:47],s65 offen offset:2048 sc1
s_and_b32 s65,s70,0x3f8000
v_mfma_f32_16x16x32_bf16 v[24:27],v[160:163],v[44:47],v[24:27]
ds_read_b128 v[36:39],v4 offset:18432
v_mfma_f32_16x16x32_bf16 v[24:27],a[240:243],v[40:43],v[24:27]
s_waitcnt lgkmcnt(4)
v_mfma_f32_16x16x32_bf16 v[24:27],a[24:27],v[48:51],v[24:27]
ds_read_b128 v[40:43],v4 offset:32768
v_mfma_f32_16x16x32_bf16 v[24:27],a[136:139],v[52:55],v[24:27]
buffer_load_dwordx4 v2,s[32:35],s63 offen offset:0 sc1 lds
v_mfma_f32_16x16x32_bf16 v[28:31],a[140:143],v[52:55],v[28:31]
ds_read_b128 v[44:47],v4 offset:47104
v_mfma_f32_16x16x32_bf16 v[28:31],a[28:31],v[48:51],v[28:31]
s_waitcnt lgkmcnt(4)
v_mfma_f32_16x16x32_bf16 v[28:31],a[252:255],v[56:59],v[28:31]
ds_read_b128 v[48:51],v4 offset:5120
v_mfma_f32_16x16x32_bf16 v[28:31],v[172:175],v[60:63],v[28:31]
buffer_load_dwordx4 v2,s[32:35],s63 offen offset:1024 sc1 lds
v_mfma_f32_16x16x32_bf16 v[24:27],v[168:171],v[60:63],v[24:27]
ds_read_b128 v[52:55],v4 offset:19456
v_mfma_f32_16x16x32_bf16 v[24:27],a[248:251],v[56:59],v[24:27]
s_waitcnt lgkmcnt(4)
v_mfma_f32_16x16x32_bf16 v[24:27],a[32:35],v[32:35],v[24:27]
ds_read_b128 v[56:59],v4 offset:33792
v_mfma_f32_16x16x32_bf16 v[24:27],a[144:147],v[36:39],v[24:27]
buffer_load_dwordx4 v2,s[32:35],s63 offen offset:2048 sc1 lds
v_mfma_f32_16x16x32_bf16 v[28:31],a[148:151],v[36:39],v[28:31]
ds_read_b128 v[60:63],v4 offset:48128
v_mfma_f32_16x16x32_bf16 v[28:31],a[36:39],v[32:35],v[28:31]
s_waitcnt lgkmcnt(4)
v_mfma_f32_16x16x32_bf16 v[28:31],v[68:71],v[40:43],v[28:31]
ds_read_b128 v[32:35],v4 offset:6144
v_mfma_f32_16x16x32_bf16 v[28:31],v[180:183],v[44:47],v[28:31]
buffer_load_dwordx4 v2,s[32:35],s63 offen offset:3072 sc1 lds
v_mfma_f32_16x16x32_bf16 v[24:27],v[176:179],v[44:47],v[24:27]
ds_read_b128 v[36:39],v4 offset:20480
v_mfma_f32_16x16x32_bf16 v[24:27],v[64:67],v[40:43],v[24:27]
s_waitcnt lgkmcnt(4)
v_mfma_f32_16x16x32_bf16 v[24:27],a[40:43],v[48:51],v[24:27]
ds_read_b128 v[40:43],v4 offset:34816
s_add_u32 s63,s63,4096
v_mfma_f32_16x16x32_bf16 v[24:27],a[152:155],v[52:55],v[24:27]
s_add_u32 m0,s64,4096
v_mfma_f32_16x16x32_bf16 v[28:31],a[156:159],v[52:55],v[28:31]
ds_read_b128 v[44:47],v4 offset:49152
v_mfma_f32_16x16x32_bf16 v[28:31],a[44:47],v[48:51],v[28:31]
s_waitcnt lgkmcnt(4)
v_mfma_f32_16x16x32_bf16 v[28:31],v[76:79],v[56:59],v[28:31]
ds_read_b128 v[48:51],v4 offset:7168
v_mfma_f32_16x16x32_bf16 v[28:31],v[188:191],v[60:63],v[28:31]
buffer_load_dwordx4 v2,s[32:35],s63 offen offset:0 sc1 lds
v_mfma_f32_16x16x32_bf16 v[24:27],v[184:187],v[60:63],v[24:27]
ds_read_b128 v[52:55],v4 offset:21504
v_mfma_f32_16x16x32_bf16 v[24:27],v[72:75],v[56:59],v[24:27]
s_waitcnt lgkmcnt(4)
v_mfma_f32_16x16x32_bf16 v[24:27],a[48:51],v[32:35],v[24:27]
ds_read_b128 v[56:59],v4 offset:35840
v_mfma_f32_16x16x32_bf16 v[24:27],a[160:163],v[36:39],v[24:27]
buffer_load_dwordx4 v2,s[32:35],s63 offen offset:1024 sc1 lds
v_mfma_f32_16x16x32_bf16 v[28:31],a[164:167],v[36:39],v[28:31]
ds_read_b128 v[60:63],v4 offset:50176
v_mfma_f32_16x16x32_bf16 v[28:31],a[52:55],v[32:35],v[28:31]
s_waitcnt lgkmcnt(4)
v_mfma_f32_16x16x32_bf16 v[28:31],v[84:87],v[40:43],v[28:31]
ds_read_b128 v[32:35],v4 offset:8192
v_mfma_f32_16x16x32_bf16 v[28:31],v[196:199],v[44:47],v[28:31]
buffer_load_dwordx4 v2,s[32:35],s63 offen offset:2048 sc1 lds
v_mfma_f32_16x16x32_bf16 v[24:27],v[192:195],v[44:47],v[24:27]
ds_read_b128 v[36:39],v4 offset:22528
v_mfma_f32_16x16x32_bf16 v[24:27],v[80:83],v[40:43],v[24:27]
s_waitcnt lgkmcnt(4)
v_mfma_f32_16x16x32_bf16 v[24:27],a[56:59],v[48:51],v[24:27]
ds_read_b128 v[40:43],v4 offset:36864
v_mfma_f32_16x16x32_bf16 v[24:27],a[168:171],v[52:55],v[24:27]
buffer_load_dwordx4 v2,s[32:35],s63 offen offset:3072 sc1 lds
v_mfma_f32_16x16x32_bf16 v[28:31],a[172:175],v[52:55],v[28:31]
ds_read_b128 v[44:47],v4 offset:51200
v_mfma_f32_16x16x32_bf16 v[28:31],a[60:63],v[48:51],v[28:31]
s_waitcnt lgkmcnt(4)
v_mfma_f32_16x16x32_bf16 v[28:31],v[92:95],v[56:59],v[28:31]
ds_read_b128 v[48:51],v4 offset:9216
v_mfma_f32_16x16x32_bf16 v[28:31],v[204:207],v[60:63],v[28:31]
s_add_u32 s63,s63,4096
s_add_u32 m0,s64,8192
v_mfma_f32_16x16x32_bf16 v[24:27],v[200:203],v[60:63],v[24:27]
ds_read_b128 v[52:55],v4 offset:23552
v_mfma_f32_16x16x32_bf16 v[24:27],v[88:91],v[56:59],v[24:27]
s_waitcnt lgkmcnt(4)
v_mfma_f32_16x16x32_bf16 v[24:27],a[64:67],v[32:35],v[24:27]
ds_read_b128 v[56:59],v4 offset:37888
v_mfma_f32_16x16x32_bf16 v[24:27],a[176:179],v[36:39],v[24:27]
buffer_load_dwordx4 v2,s[32:35],s63 offen offset:0 sc1 lds
v_mfma_f32_16x16x32_bf16 v[28:31],a[180:183],v[36:39],v[28:31]
ds_read_b128 v[60:63],v4 offset:52224
v_mfma_f32_16x16x32_bf16 v[28:31],a[68:71],v[32:35],v[28:31]
s_waitcnt lgkmcnt(4)
v_mfma_f32_16x16x32_bf16 v[28:31],v[100:103],v[40:43],v[28:31]
ds_read_b128 v[32:35],v4 offset:10240
v_mfma_f32_16x16x32_bf16 v[28:31],v[212:215],v[44:47],v[28:31]
buffer_load_dwordx4 v2,s[32:35],s63 offen offset:1024 sc1 lds
v_mfma_f32_16x16x32_bf16 v[24:27],v[208:211],v[44:47],v[24:27]
ds_read_b128 v[36:39],v4 offset:24576
v_mfma_f32_16x16x32_bf16 v[24:27],v[96:99],v[40:43],v[24:27]
s_waitcnt lgkmcnt(4)
v_mfma_f32_16x16x32_bf16 v[24:27],a[72:75],v[48:51],v[24:27]
ds_read_b128 v[40:43],v4 offset:38912
v_mfma_f32_16x16x32_bf16 v[24:27],a[184:187],v[52:55],v[24:27]
buffer_load_dwordx4 v2,s[32:35],s63 offen offset:2048 sc1 lds
v_mfma_f32_16x16x32_bf16 v[28:31],a[188:191],v[52:55],v[28:31]
ds_read_b128 v[44:47],v4 offset:53248
v_mfma_f32_16x16x32_bf16 v[28:31],a[76:79],v[48:51],v[28:31]
s_waitcnt lgkmcnt(4)
v_mfma_f32_16x16x32_bf16 v[28:31],v[108:111],v[56:59],v[28:31]
ds_read_b128 v[48:51],v4 offset:11264
v_mfma_f32_16x16x32_bf16 v[28:31],v[220:223],v[60:63],v[28:31]
v_mfma_f32_16x16x32_bf16 v[24:27],v[216:219],v[60:63],v[24:27]
ds_read_b128 v[52:55],v4 offset:25600
v_mfma_f32_16x16x32_bf16 v[24:27],v[104:107],v[56:59],v[24:27]
s_waitcnt lgkmcnt(4)
v_mfma_f32_16x16x32_bf16 v[24:27],a[80:83],v[32:35],v[24:27]
ds_read_b128 v[56:59],v4 offset:39936
v_mfma_f32_16x16x32_bf16 v[24:27],a[192:195],v[36:39],v[24:27]
buffer_load_dwordx4 v2,s[32:35],s63 offen offset:3072 sc1 lds
s_add_u32 s63,s63,4096
v_mfma_f32_16x16x32_bf16 v[28:31],a[196:199],v[36:39],v[28:31]
ds_read_b128 v[60:63],v4 offset:54272
s_add_u32 m0,s64,12288
v_mfma_f32_16x16x32_bf16 v[28:31],a[84:87],v[32:35],v[28:31]
s_waitcnt lgkmcnt(4)
v_mfma_f32_16x16x32_bf16 v[28:31],v[116:119],v[40:43],v[28:31]
ds_read_b128 v[32:35],v4 offset:12288
v_mfma_f32_16x16x32_bf16 v[28:31],v[228:231],v[44:47],v[28:31]
buffer_load_dwordx4 v2,s[32:35],s63 offen offset:0 sc1 lds
v_mfma_f32_16x16x32_bf16 v[24:27],v[224:227],v[44:47],v[24:27]
ds_read_b128 v[36:39],v4 offset:26624
v_mfma_f32_16x16x32_bf16 v[24:27],v[112:115],v[40:43],v[24:27]
s_waitcnt lgkmcnt(4)
v_mfma_f32_16x16x32_bf16 v[24:27],a[88:91],v[48:51],v[24:27]
ds_read_b128 v[40:43],v4 offset:40960
v_mfma_f32_16x16x32_bf16 v[24:27],a[200:203],v[52:55],v[24:27]
buffer_load_dwordx4 v2,s[32:35],s63 offen offset:1024 sc1 lds
s_add_u32 s63,s63,s31
v_mfma_f32_16x16x32_bf16 v[28:31],a[204:207],v[52:55],v[28:31]
ds_read_b128 v[44:47],v4 offset:55296
v_mfma_f32_16x16x32_bf16 v[28:31],a[92:95],v[48:51],v[28:31]
s_waitcnt lgkmcnt(4)
v_mfma_f32_16x16x32_bf16 v[28:31],v[124:127],v[56:59],v[28:31]
ds_read_b128 v[48:51],v4 offset:13312
v_mfma_f32_16x16x32_bf16 v[28:31],v[236:239],v[60:63],v[28:31]
v_mfma_f32_16x16x32_bf16 v[24:27],v[232:235],v[60:63],v[24:27]
ds_read_b128 v[52:55],v4 offset:27648
v_mfma_f32_16x16x32_bf16 v[24:27],v[120:123],v[56:59],v[24:27]
s_waitcnt lgkmcnt(4)
v_mfma_f32_16x16x32_bf16 v[24:27],a[96:99],v[32:35],v[24:27]
s_waitcnt vmcnt(10)
s_barrier
v_mfma_f32_16x16x32_bf16 v[24:27],a[208:211],v[36:39],v[24:27]
ds_read_b128 v[56:59],v4 offset:41984
v_mfma_f32_16x16x32_bf16 v[28:31],a[212:215],v[36:39],v[28:31]
ds_read_b128 v[60:63],v4 offset:56320
v_mfma_f32_16x16x32_bf16 v[28:31],a[100:103],v[32:35],v[28:31]
s_waitcnt lgkmcnt(4)
v_mfma_f32_16x16x32_bf16 v[28:31],v[132:135],v[40:43],v[28:31]
ds_read_b128 v[32:35],v2 offset:0
v_mfma_f32_16x16x32_bf16 v[28:31],v[244:247],v[44:47],v[28:31]
v_mfma_f32_16x16x32_bf16 v[24:27],v[240:243],v[44:47],v[24:27]
ds_read_b128 v[36:39],v2 offset:14336
v_mfma_f32_16x16x32_bf16 v[24:27],v[128:131],v[40:43],v[24:27]
s_waitcnt lgkmcnt(4)
v_mfma_f32_16x16x32_bf16 v[24:27],a[104:107],v[48:51],v[24:27]
ds_read_b128 v[40:43],v2 offset:28672
v_mfma_f32_16x16x32_bf16 v[24:27],a[216:219],v[52:55],v[24:27]
v_mfma_f32_16x16x32_bf16 v[28:31],a[220:223],v[52:55],v[28:31]
ds_read_b128 v[44:47],v2 offset:43008
v_mfma_f32_16x16x32_bf16 v[28:31],a[108:111],v[48:51],v[28:31]
s_waitcnt lgkmcnt(4)
v_mfma_f32_16x16x32_bf16 v[28:31],v[140:143],v[56:59],v[28:31]
ds_read_b128 v[48:51],v2 offset:1024
v_mfma_f32_16x16x32_bf16 v[28:31],v[252:255],v[60:63],v[28:31]
v_mfma_f32_16x16x32_bf16 v[24:27],v[248:251],v[60:63],v[24:27]
ds_read_b128 v[52:55],v2 offset:15360
s_sub_u32 s62,s62,1
v_mfma_f32_16x16x32_bf16 v[24:27],v[136:139],v[56:59],v[24:27]
s_cbranch_scc1 .L_dbt_3
s_waitcnt lgkmcnt(4)
v_mfma_f32_16x16x32_bf16 v[16:19],a[0:3],v[32:35],0
ds_read_b128 v[56:59],v2 offset:29696
v_mfma_f32_16x16x32_bf16 v[16:19],a[112:115],v[36:39],v[16:19]
v_mfma_f32_16x16x32_bf16 v[20:23],a[116:119],v[36:39],0
ds_read_b128 v[60:63],v2 offset:44032
v_mfma_f32_16x16x32_bf16 v[20:23],a[4:7],v[32:35],v[20:23]
v_cvt_pk_bf16_f32 v15,v30,v31
s_waitcnt lgkmcnt(4)
v_mfma_f32_16x16x32_bf16 v[20:23],a[228:231],v[40:43],v[20:23]
ds_read_b128 v[32:35],v2 offset:2048
v_cvt_pk_bf16_f32 v14,v28,v29
v_mfma_f32_16x16x32_bf16 v[20:23],v[148:151],v[44:47],v[20:23]
v_cvt_pk_bf16_f32 v13,v26,v27
v_cvt_pk_bf16_f32 v12,v24,v25
v_mfma_f32_16x16x32_bf16 v[16:19],v[144:147],v[44:47],v[16:19]
ds_read_b128 v[36:39],v2 offset:16384
ds_write_b128 v5,v[12:15]
v_mfma_f32_16x16x32_bf16 v[16:19],a[224:227],v[40:43],v[16:19]
ds_read2st64_b32 v[12:13],v3 offset0:0 offset1:1
s_waitcnt lgkmcnt(6)
v_mfma_f32_16x16x32_bf16 v[16:19],a[8:11],v[48:51],v[16:19]
ds_read_b128 v[40:43],v2 offset:30720
ds_read2st64_b32 v[14:15],v3 offset0:2 offset1:3
v_mfma_f32_16x16x32_bf16 v[16:19],a[120:123],v[52:55],v[16:19]
v_mfma_f32_16x16x32_bf16 v[20:23],a[124:127],v[52:55],v[20:23]
ds_read_b128 v[44:47],v2 offset:45056
v_mfma_f32_16x16x32_bf16 v[20:23],a[12:15],v[48:51],v[20:23]
s_waitcnt lgkmcnt(7)
v_mfma_f32_16x16x32_bf16 v[20:23],a[236:239],v[56:59],v[20:23]
ds_read_b128 v[48:51],v2 offset:3072
v_mfma_f32_16x16x32_bf16 v[20:23],v[156:159],v[60:63],v[20:23]
s_waitcnt lgkmcnt(2)
buffer_atomic_pk_add_bf16 v12,v1,s[44:47],s65 offen offset:0 sc1
v_mfma_f32_16x16x32_bf16 v[16:19],v[152:155],v[60:63],v[16:19]
ds_read_b128 v[52:55],v2 offset:17408
s_add_u32 s70,s70,s68
v_mfma_f32_16x16x32_bf16 v[16:19],a[232:235],v[56:59],v[16:19]
buffer_atomic_pk_add_bf16 v13,v1,s[44:47],s65 offen offset:2048 sc1
v_mfma_f32_16x16x32_bf16 v[16:19],a[16:19],v[32:35],v[16:19]
s_waitcnt vmcnt(2)
s_barrier
v_mfma_f32_16x16x32_bf16 v[16:19],a[128:131],v[36:39],v[16:19]
ds_read_b128 v[56:59],v2 offset:31744
s_add_u32 s65,s65,16384
v_mfma_f32_16x16x32_bf16 v[20:23],a[132:135],v[36:39],v[20:23]
ds_read_b128 v[60:63],v2 offset:46080
v_mfma_f32_16x16x32_bf16 v[20:23],a[20:23],v[32:35],v[20:23]
buffer_atomic_pk_add_bf16 v14,v1,s[44:47],s65 offen offset:0 sc1
s_waitcnt lgkmcnt(4)
v_mfma_f32_16x16x32_bf16 v[20:23],a[244:247],v[40:43],v[20:23]
ds_read_b128 v[32:35],v2 offset:4096
s_add_u32 m0,s64,57344
v_mfma_f32_16x16x32_bf16 v[20:23],v[164:167],v[44:47],v[20:23]
buffer_load_dwordx4 v2,s[32:35],s63 offen offset:0 sc1 lds
s_mov_b32 s68,s75
v_mfma_f32_16x16x32_bf16 v[16:19],v[160:163],v[44:47],v[16:19]
ds_read_b128 v[36:39],v2 offset:18432
v_mfma_f32_16x16x32_bf16 v[16:19],a[240:243],v[40:43],v[16:19]
buffer_atomic_pk_add_bf16 v15,v1,s[44:47],s65 offen offset:2048 sc1
s_waitcnt lgkmcnt(4)
v_mfma_f32_16x16x32_bf16 v[16:19],a[24:27],v[48:51],v[16:19]
ds_read_b128 v[40:43],v2 offset:32768
s_and_b32 s65,s70,0x3f8000
v_mfma_f32_16x16x32_bf16 v[16:19],a[136:139],v[52:55],v[16:19]
v_mfma_f32_16x16x32_bf16 v[20:23],a[140:143],v[52:55],v[20:23]
ds_read_b128 v[44:47],v2 offset:47104
v_mfma_f32_16x16x32_bf16 v[20:23],a[28:31],v[48:51],v[20:23]
s_waitcnt lgkmcnt(4)
v_mfma_f32_16x16x32_bf16 v[20:23],a[252:255],v[56:59],v[20:23]
ds_read_b128 v[48:51],v2 offset:5120
v_mfma_f32_16x16x32_bf16 v[20:23],v[172:175],v[60:63],v[20:23]
buffer_load_dwordx4 v2,s[32:35],s63 offen offset:1024 sc1 lds
v_mfma_f32_16x16x32_bf16 v[16:19],v[168:171],v[60:63],v[16:19]
ds_read_b128 v[52:55],v2 offset:19456
v_mfma_f32_16x16x32_bf16 v[16:19],a[248:251],v[56:59],v[16:19]
s_waitcnt lgkmcnt(4)
v_mfma_f32_16x16x32_bf16 v[16:19],a[32:35],v[32:35],v[16:19]
ds_read_b128 v[56:59],v2 offset:33792
v_mfma_f32_16x16x32_bf16 v[16:19],a[144:147],v[36:39],v[16:19]
buffer_load_dwordx4 v2,s[32:35],s63 offen offset:2048 sc1 lds
v_mfma_f32_16x16x32_bf16 v[20:23],a[148:151],v[36:39],v[20:23]
ds_read_b128 v[60:63],v2 offset:48128
v_mfma_f32_16x16x32_bf16 v[20:23],a[36:39],v[32:35],v[20:23]
s_waitcnt lgkmcnt(4)
v_mfma_f32_16x16x32_bf16 v[20:23],v[68:71],v[40:43],v[20:23]
ds_read_b128 v[32:35],v2 offset:6144
v_mfma_f32_16x16x32_bf16 v[20:23],v[180:183],v[44:47],v[20:23]
buffer_load_dwordx4 v2,s[32:35],s63 offen offset:3072 sc1 lds
v_mfma_f32_16x16x32_bf16 v[16:19],v[176:179],v[44:47],v[16:19]
ds_read_b128 v[36:39],v2 offset:20480
v_mfma_f32_16x16x32_bf16 v[16:19],v[64:67],v[40:43],v[16:19]
s_waitcnt lgkmcnt(4)
v_mfma_f32_16x16x32_bf16 v[16:19],a[40:43],v[48:51],v[16:19]
ds_read_b128 v[40:43],v2 offset:34816
s_add_u32 s63,s63,4096
v_mfma_f32_16x16x32_bf16 v[16:19],a[152:155],v[52:55],v[16:19]
s_add_u32 m0,s64,61440
v_mfma_f32_16x16x32_bf16 v[20:23],a[156:159],v[52:55],v[20:23]
ds_read_b128 v[44:47],v2 offset:49152
v_mfma_f32_16x16x32_bf16 v[20:23],a[44:47],v[48:51],v[20:23]
s_waitcnt lgkmcnt(4)
v_mfma_f32_16x16x32_bf16 v[20:23],v[76:79],v[56:59],v[20:23]
ds_read_b128 v[48:51],v2 offset:7168
v_mfma_f32_16x16x32_bf16 v[20:23],v[188:191],v[60:63],v[20:23]
buffer_load_dwordx4 v2,s[32:35],s63 offen offset:0 sc1 lds
v_mfma_f32_16x16x32_bf16 v[16:19],v[184:187],v[60:63],v[16:19]
ds_read_b128 v[52:55],v2 offset:21504
v_mfma_f32_16x16x32_bf16 v[16:19],v[72:75],v[56:59],v[16:19]
s_waitcnt lgkmcnt(4)
v_mfma_f32_16x16x32_bf16 v[16:19],a[48:51],v[32:35],v[16:19]
ds_read_b128 v[56:59],v2 offset:35840
v_mfma_f32_16x16x32_bf16 v[16:19],a[160:163],v[36:39],v[16:19]
buffer_load_dwordx4 v2,s[32:35],s63 offen offset:1024 sc1 lds
v_mfma_f32_16x16x32_bf16 v[20:23],a[164:167],v[36:39],v[20:23]
ds_read_b128 v[60:63],v2 offset:50176
v_mfma_f32_16x16x32_bf16 v[20:23],a[52:55],v[32:35],v[20:23]
s_waitcnt lgkmcnt(4)
v_mfma_f32_16x16x32_bf16 v[20:23],v[84:87],v[40:43],v[20:23]
ds_read_b128 v[32:35],v2 offset:8192
v_mfma_f32_16x16x32_bf16 v[20:23],v[196:199],v[44:47],v[20:23]
buffer_load_dwordx4 v2,s[32:35],s63 offen offset:2048 sc1 lds
v_mfma_f32_16x16x32_bf16 v[16:19],v[192:195],v[44:47],v[16:19]
ds_read_b128 v[36:39],v2 offset:22528
v_mfma_f32_16x16x32_bf16 v[16:19],v[80:83],v[40:43],v[16:19]
s_waitcnt lgkmcnt(4)
v_mfma_f32_16x16x32_bf16 v[16:19],a[56:59],v[48:51],v[16:19]
ds_read_b128 v[40:43],v2 offset:36864
v_mfma_f32_16x16x32_bf16 v[16:19],a[168:171],v[52:55],v[16:19]
buffer_load_dwordx4 v2,s[32:35],s63 offen offset:3072 sc1 lds
v_mfma_f32_16x16x32_bf16 v[20:23],a[172:175],v[52:55],v[20:23]
ds_read_b128 v[44:47],v2 offset:51200
v_mfma_f32_16x16x32_bf16 v[20:23],a[60:63],v[48:51],v[20:23]
s_waitcnt lgkmcnt(4)
v_mfma_f32_16x16x32_bf16 v[20:23],v[92:95],v[56:59],v[20:23]
ds_read_b128 v[48:51],v2 offset:9216
v_mfma_f32_16x16x32_bf16 v[20:23],v[204:207],v[60:63],v[20:23]
s_add_u32 s63,s63,4096
s_add_u32 m0,s64,65536
v_mfma_f32_16x16x32_bf16 v[16:19],v[200:203],v[60:63],v[16:19]
ds_read_b128 v[52:55],v2 offset:23552
v_mfma_f32_16x16x32_bf16 v[16:19],v[88:91],v[56:59],v[16:19]
s_waitcnt lgkmcnt(4)
v_mfma_f32_16x16x32_bf16 v[16:19],a[64:67],v[32:35],v[16:19]
ds_read_b128 v[56:59],v2 offset:37888
v_mfma_f32_16x16x32_bf16 v[16:19],a[176:179],v[36:39],v[16:19]
buffer_load_dwordx4 v2,s[32:35],s63 offen offset:0 sc1 lds
v_mfma_f32_16x16x32_bf16 v[20:23],a[180:183],v[36:39],v[20:23]
ds_read_b128 v[60:63],v2 offset:52224
v_mfma_f32_16x16x32_bf16 v[20:23],a[68:71],v[32:35],v[20:23]
s_waitcnt lgkmcnt(4)
v_mfma_f32_16x16x32_bf16 v[20:23],v[100:103],v[40:43],v[20:23]
ds_read_b128 v[32:35],v2 offset:10240
v_mfma_f32_16x16x32_bf16 v[20:23],v[212:215],v[44:47],v[20:23]
buffer_load_dwordx4 v2,s[32:35],s63 offen offset:1024 sc1 lds
v_mfma_f32_16x16x32_bf16 v[16:19],v[208:211],v[44:47],v[16:19]
ds_read_b128 v[36:39],v2 offset:24576
v_mfma_f32_16x16x32_bf16 v[16:19],v[96:99],v[40:43],v[16:19]
s_waitcnt lgkmcnt(4)
v_mfma_f32_16x16x32_bf16 v[16:19],a[72:75],v[48:51],v[16:19]
ds_read_b128 v[40:43],v2 offset:38912
v_mfma_f32_16x16x32_bf16 v[16:19],a[184:187],v[52:55],v[16:19]
buffer_load_dwordx4 v2,s[32:35],s63 offen offset:2048 sc1 lds
v_mfma_f32_16x16x32_bf16 v[20:23],a[188:191],v[52:55],v[20:23]
ds_read_b128 v[44:47],v2 offset:53248
v_mfma_f32_16x16x32_bf16 v[20:23],a[76:79],v[48:51],v[20:23]
s_waitcnt lgkmcnt(4)
v_mfma_f32_16x16x32_bf16 v[20:23],v[108:111],v[56:59],v[20:23]
ds_read_b128 v[48:51],v2 offset:11264
v_mfma_f32_16x16x32_bf16 v[20:23],v[220:223],v[60:63],v[20:23]
v_mfma_f32_16x16x32_bf16 v[16:19],v[216:219],v[60:63],v[16:19]
ds_read_b128 v[52:55],v2 offset:25600
v_mfma_f32_16x16x32_bf16 v[16:19],v[104:107],v[56:59],v[16:19]
s_waitcnt lgkmcnt(4)
v_mfma_f32_16x16x32_bf16 v[16:19],a[80:83],v[32:35],v[16:19]
ds_read_b128 v[56:59],v2 offset:39936
v_mfma_f32_16x16x32_bf16 v[16:19],a[192:195],v[36:39],v[16:19]
buffer_load_dwordx4 v2,s[32:35],s63 offen offset:3072 sc1 lds
s_add_u32 s63,s63,4096
v_mfma_f32_16x16x32_bf16 v[20:23],a[196:199],v[36:39],v[20:23]
ds_read_b128 v[60:63],v2 offset:54272
s_add_u32 m0,s64,69632
v_mfma_f32_16x16x32_bf16 v[20:23],a[84:87],v[32:35],v[20:23]
s_waitcnt lgkmcnt(4)
v_mfma_f32_16x16x32_bf16 v[20:23],v[116:119],v[40:43],v[20:23]
ds_read_b128 v[32:35],v2 offset:12288
v_mfma_f32_16x16x32_bf16 v[20:23],v[228:231],v[44:47],v[20:23]
buffer_load_dwordx4 v2,s[32:35],s63 offen offset:0 sc1 lds
)#"
R"#(
v_mfma_f32_16x16x32_bf16 v[16:19],v[224:227],v[44:47],v[16:19]
ds_read_b128 v[36:39],v2 offset:26624
v_mfma_f32_16x16x32_bf16 v[16:19],v[112:115],v[40:43],v[16:19]
s_waitcnt lgkmcnt(4)
v_mfma_f32_16x16x32_bf16 v[16:19],a[88:91],v[48:51],v[16:19]
ds_read_b128 v[40:43],v2 offset:40960
v_mfma_f32_16x16x32_bf16 v[16:19],a[200:203],v[52:55],v[16:19]
buffer_load_dwordx4 v2,s[32:35],s63 offen offset:1024 sc1 lds
s_add_u32 s63,s63,s31
v_mfma_f32_16x16x32_bf16 v[20:23],a[204:207],v[52:55],v[20:23]
ds_read_b128 v[44:47],v2 offset:55296
v_mfma_f32_16x16x32_bf16 v[20:23],a[92:95],v[48:51],v[20:23]
s_waitcnt lgkmcnt(4)
v_mfma_f32_16x16x32_bf16 v[20:23],v[124:127],v[56:59],v[20:23]
ds_read_b128 v[48:51],v2 offset:13312
v_mfma_f32_16x16x32_bf16 v[20:23],v[236:239],v[60:63],v[20:23]
v_mfma_f32_16x16x32_bf16 v[16:19],v[232:235],v[60:63],v[16:19]
ds_read_b128 v[52:55],v2 offset:27648
v_mfma_f32_16x16x32_bf16 v[16:19],v[120:123],v[56:59],v[16:19]
s_waitcnt lgkmcnt(4)
v_mfma_f32_16x16x32_bf16 v[16:19],a[96:99],v[32:35],v[16:19]
s_waitcnt vmcnt(10)
s_barrier
v_mfma_f32_16x16x32_bf16 v[16:19],a[208:211],v[36:39],v[16:19]
ds_read_b128 v[56:59],v2 offset:41984
v_mfma_f32_16x16x32_bf16 v[20:23],a[212:215],v[36:39],v[20:23]
ds_read_b128 v[60:63],v2 offset:56320
v_mfma_f32_16x16x32_bf16 v[20:23],a[100:103],v[32:35],v[20:23]
s_waitcnt lgkmcnt(4)
v_mfma_f32_16x16x32_bf16 v[20:23],v[132:135],v[40:43],v[20:23]
ds_read_b128 v[32:35],v4 offset:0
v_mfma_f32_16x16x32_bf16 v[20:23],v[244:247],v[44:47],v[20:23]
v_mfma_f32_16x16x32_bf16 v[16:19],v[240:243],v[44:47],v[16:19]
ds_read_b128 v[36:39],v4 offset:14336
v_mfma_f32_16x16x32_bf16 v[16:19],v[128:131],v[40:43],v[16:19]
s_waitcnt lgkmcnt(4)
v_mfma_f32_16x16x32_bf16 v[16:19],a[104:107],v[48:51],v[16:19]
ds_read_b128 v[40:43],v4 offset:28672
v_mfma_f32_16x16x32_bf16 v[16:19],a[216:219],v[52:55],v[16:19]
v_mfma_f32_16x16x32_bf16 v[20:23],a[220:223],v[52:55],v[20:23]
ds_read_b128 v[44:47],v4 offset:43008
v_mfma_f32_16x16x32_bf16 v[20:23],a[108:111],v[48:51],v[20:23]
s_waitcnt lgkmcnt(4)
v_mfma_f32_16x16x32_bf16 v[20:23],v[140:143],v[56:59],v[20:23]
ds_read_b128 v[48:51],v4 offset:1024
v_mfma_f32_16x16x32_bf16 v[20:23],v[252:255],v[60:63],v[20:23]
v_mfma_f32_16x16x32_bf16 v[16:19],v[248:251],v[60:63],v[16:19]
ds_read_b128 v[52:55],v4 offset:15360
s_sub_u32 s62,s62,1
v_mfma_f32_16x16x32_bf16 v[16:19],v[136:139],v[56:59],v[16:19]
s_cbranch_scc0 .L_dbl_3
.L_dbu_3:
s_nop 1
s_nop 0
v_cvt_pk_bf16_f32 v15,v22,v23
v_cvt_pk_bf16_f32 v14,v20,v21
s_nop 1
v_cvt_pk_bf16_f32 v13,v18,v19
v_cvt_pk_bf16_f32 v12,v16,v17
ds_write_b128 v5,v[12:15]
ds_read2st64_b32 v[12:13],v3 offset0:0 offset1:1
ds_read2st64_b32 v[14:15],v3 offset0:2 offset1:3
s_waitcnt lgkmcnt(0)
buffer_atomic_pk_add_bf16 v12,v1,s[44:47],s65 offen offset:0 sc1
s_add_u32 s70,s70,s75
buffer_atomic_pk_add_bf16 v13,v1,s[44:47],s65 offen offset:2048 sc1
s_add_u32 s65,s65,16384
buffer_atomic_pk_add_bf16 v14,v1,s[44:47],s65 offen offset:0 sc1
buffer_atomic_pk_add_bf16 v15,v1,s[44:47],s65 offen offset:2048 sc1
s_and_b32 s65,s70,0x3f8000
s_or_b32 s48,s28,s27
s_or_b32 s48,s48,s26
s_cbranch_scc1 .L_end
v_mov_b32 v8,s54
v_mov_b32 v9,s55
buffer_store_dwordx2 v[8:9],off,s[32:35],0 sc1
s_branch .L_end
.L_dbt_3:
s_nop 1
s_nop 0
v_cvt_pk_bf16_f32 v15,v30,v31
v_cvt_pk_bf16_f32 v14,v28,v29
s_nop 1
v_cvt_pk_bf16_f32 v13,v26,v27
v_cvt_pk_bf16_f32 v12,v24,v25
ds_write_b128 v5,v[12:15]
ds_read2st64_b32 v[12:13],v3 offset0:0 offset1:1
ds_read2st64_b32 v[14:15],v3 offset0:2 offset1:3
s_waitcnt lgkmcnt(0)
buffer_atomic_pk_add_bf16 v12,v1,s[44:47],s65 offen offset:0 sc1
s_add_u32 s70,s70,s68
buffer_atomic_pk_add_bf16 v13,v1,s[44:47],s65 offen offset:2048 sc1
s_add_u32 s65,s65,16384
buffer_atomic_pk_add_bf16 v14,v1,s[44:47],s65 offen offset:0 sc1
s_mov_b32 s68,s75
buffer_atomic_pk_add_bf16 v15,v1,s[44:47],s65 offen offset:2048 sc1
s_and_b32 s65,s70,0x3f8000
s_or_b32 s48,s28,s27
s_or_b32 s48,s48,s26
s_cbranch_scc1 .L_end
v_mov_b32 v8,s54
v_mov_b32 v9,s55
buffer_store_dwordx2 v[8:9],off,s[32:35],0 sc1
s_branch .L_end
.L_x:
v_lshrrev_b32 v10,4,v1
v_and_b32 v1,15,v1
s_lshl_b32 s48,s50,6
v_lshl_add_u32 v1,v1,2,s48
v_lshl_add_u32 v1,v10,12,v1
s_add_u32 s71,s70,4681728
s_mov_b32 s65,s59
s_mov_b32 s70,s59
s_mov_b32 s62,2
s_add_u32 s74,s74,8192
v_lshrrev_b32 v5,8,v2
v_lshl_add_u32 v5,v5,4,s74
v_and_b32 v3,128,v2
v_lshl_add_u32 v5,v3,2,v5
v_and_b32 v3,16,v2
v_lshl_add_u32 v5,v3,4,v5
v_and_b32 v3,96,v2
v_lshl_add_u32 v5,v3,1,v5
v_lshrrev_b32 v3,2,v2
v_add_u32 v3,s74,v3
.L_xa:
s_cmp_lg_u32 s26,1
s_cbranch_scc1 .L_skx
s_mov_b32 s48,20000
s_branch .L_rfx
.L_rlx:
s_sleep 2
.L_rfx:
buffer_load_dwordx2 v[6:7],v0,s[0:3],s66 offen sc1
s_sub_u32 s48,s48,1
s_cbranch_scc1 .L_end
s_waitcnt vmcnt(0)
v_cmp_ne_u64 vcc,v[8:9],v[6:7]
s_cbranch_vccnz .L_rlx
.L_skx:
s_waitcnt vmcnt(0)
s_barrier
s_add_u32 s66,s66,s30
.L_xgo:
buffer_load_dwordx4 v[12:15],v2,s[32:35],s71 offen sc1
s_add_u32 s71,s71,s30
s_add_u32 m0,s64,0
s_nop 0
buffer_load_dwordx4 v2,s[32:35],s63 offen offset:0 sc1 lds
buffer_load_dwordx4 v2,s[32:35],s63 offen offset:1024 sc1 lds
buffer_load_dwordx4 v2,s[32:35],s63 offen offset:2048 sc1 lds
buffer_load_dwordx4 v2,s[32:35],s63 offen offset:3072 sc1 lds
s_add_u32 s63,s63,4096
s_add_u32 m0,s64,4096
s_nop 0
buffer_load_dwordx4 v2,s[32:35],s63 offen offset:0 sc1 lds
buffer_load_dwordx4 v2,s[32:35],s63 offen offset:1024 sc1 lds
buffer_load_dwordx4 v2,s[32:35],s63 offen offset:2048 sc1 lds
buffer_load_dwordx4 v2,s[32:35],s63 offen offset:3072 sc1 lds
s_add_u32 s63,s63,4096
s_add_u32 m0,s64,8192
s_nop 0
buffer_load_dwordx4 v2,s[32:35],s63 offen offset:0 sc1 lds
buffer_load_dwordx4 v2,s[32:35],s63 offen offset:1024 sc1 lds
buffer_load_dwordx4 v2,s[32:35],s63 offen offset:2048 sc1 lds
buffer_load_dwordx4 v2,s[32:35],s63 offen offset:3072 sc1 lds
s_add_u32 s63,s63,4096
s_add_u32 m0,s64,12288
s_nop 0
buffer_load_dwordx4 v2,s[32:35],s63 offen offset:0 sc1 lds
buffer_load_dwordx4 v2,s[32:35],s63 offen offset:1024 sc1 lds
s_add_u32 s63,s63,s31
s_waitcnt vmcnt(14)
v_lshlrev_b32 v16,16,v12
v_and_b32 v17,0xffff0000,v12
v_lshlrev_b32 v18,16,v13
v_and_b32 v19,0xffff0000,v13
v_lshlrev_b32 v20,16,v14
v_and_b32 v21,0xffff0000,v14
v_lshlrev_b32 v22,16,v15
v_and_b32 v23,0xffff0000,v15
s_waitcnt vmcnt(13)
s_barrier
ds_read_b128 v[32:35],v2 offset:0
ds_read_b128 v[36:39],v2 offset:14336
ds_read_b128 v[40:43],v2 offset:28672
ds_read_b128 v[44:47],v2 offset:43008
s_waitcnt vmcnt(11)
s_barrier
ds_read_b128 v[48:51],v2 offset:1024
ds_read_b128 v[52:55],v2 offset:15360
s_waitcnt lgkmcnt(4)
v_mfma_f32_16x16x32_bf16 v[16:19],a[0:3],v[32:35],v[16:19]
ds_read_b128 v[56:59],v2 offset:29696
v_mfma_f32_16x16x32_bf16 v[16:19],a[112:115],v[36:39],v[16:19]
v_mfma_f32_16x16x32_bf16 v[20:23],a[116:119],v[36:39],v[20:23]
ds_read_b128 v[60:63],v2 offset:44032
v_mfma_f32_16x16x32_bf16 v[20:23],a[4:7],v[32:35],v[20:23]
s_waitcnt lgkmcnt(4)
v_mfma_f32_16x16x32_bf16 v[20:23],a[228:231],v[40:43],v[20:23]
ds_read_b128 v[32:35],v2 offset:2048
v_mfma_f32_16x16x32_bf16 v[20:23],v[148:151],v[44:47],v[20:23]
v_mfma_f32_16x16x32_bf16 v[16:19],v[144:147],v[44:47],v[16:19]
ds_read_b128 v[36:39],v2 offset:16384
v_mfma_f32_16x16x32_bf16 v[16:19],a[224:227],v[40:43],v[16:19]
s_waitcnt lgkmcnt(4)
v_mfma_f32_16x16x32_bf16 v[16:19],a[8:11],v[48:51],v[16:19]
ds_read_b128 v[40:43],v2 offset:30720
v_mfma_f32_16x16x32_bf16 v[16:19],a[120:123],v[52:55],v[16:19]
v_mfma_f32_16x16x32_bf16 v[20:23],a[124:127],v[52:55],v[20:23]
ds_read_b128 v[44:47],v2 offset:45056
v_mfma_f32_16x16x32_bf16 v[20:23],a[12:15],v[48:51],v[20:23]
s_waitcnt vmcnt(9)
s_waitcnt lgkmcnt(4)
v_mfma_f32_16x16x32_bf16 v[20:23],a[236:239],v[56:59],v[20:23]
s_barrier
ds_read_b128 v[48:51],v2 offset:3072
v_mfma_f32_16x16x32_bf16 v[20:23],v[156:159],v[60:63],v[20:23]
v_mfma_f32_16x16x32_bf16 v[16:19],v[152:155],v[60:63],v[16:19]
ds_read_b128 v[52:55],v2 offset:17408
v_mfma_f32_16x16x32_bf16 v[16:19],a[232:235],v[56:59],v[16:19]
s_waitcnt lgkmcnt(4)
v_mfma_f32_16x16x32_bf16 v[16:19],a[16:19],v[32:35],v[16:19]
ds_read_b128 v[56:59],v2 offset:31744
v_mfma_f32_16x16x32_bf16 v[16:19],a[128:131],v[36:39],v[16:19]
v_mfma_f32_16x16x32_bf16 v[20:23],a[132:135],v[36:39],v[20:23]
ds_read_b128 v[60:63],v2 offset:46080
v_mfma_f32_16x16x32_bf16 v[20:23],a[20:23],v[32:35],v[20:23]
s_waitcnt lgkmcnt(4)
v_mfma_f32_16x16x32_bf16 v[20:23],a[244:247],v[40:43],v[20:23]
ds_read_b128 v[32:35],v2 offset:4096
v_mfma_f32_16x16x32_bf16 v[20:23],v[164:167],v[44:47],v[20:23]
v_mfma_f32_16x16x32_bf16 v[16:19],v[160:163],v[44:47],v[16:19]
ds_read_b128 v[36:39],v2 offset:18432
v_mfma_f32_16x16x32_bf16 v[16:19],a[240:243],v[40:43],v[16:19]
s_waitcnt lgkmcnt(4)
v_mfma_f32_16x16x32_bf16 v[16:19],a[24:27],v[48:51],v[16:19]
ds_read_b128 v[40:43],v2 offset:32768
v_mfma_f32_16x16x32_bf16 v[16:19],a[136:139],v[52:55],v[16:19]
v_mfma_f32_16x16x32_bf16 v[20:23],a[140:143],v[52:55],v[20:23]
ds_read_b128 v[44:47],v2 offset:47104
v_mfma_f32_16x16x32_bf16 v[20:23],a[28:31],v[48:51],v[20:23]
s_waitcnt vmcnt(7)
s_waitcnt lgkmcnt(4)
v_mfma_f32_16x16x32_bf16 v[20:23],a[252:255],v[56:59],v[20:23]
s_barrier
ds_read_b128 v[48:51],v2 offset:5120
v_mfma_f32_16x16x32_bf16 v[20:23],v[172:175],v[60:63],v[20:23]
v_mfma_f32_16x16x32_bf16 v[16:19],v[168:171],v[60:63],v[16:19]
ds_read_b128 v[52:55],v2 offset:19456
v_mfma_f32_16x16x32_bf16 v[16:19],a[248:251],v[56:59],v[16:19]
s_waitcnt lgkmcnt(4)
v_mfma_f32_16x16x32_bf16 v[16:19],a[32:35],v[32:35],v[16:19]
ds_read_b128 v[56:59],v2 offset:33792
v_mfma_f32_16x16x32_bf16 v[16:19],a[144:147],v[36:39],v[16:19]
v_mfma_f32_16x16x32_bf16 v[20:23],a[148:151],v[36:39],v[20:23]
ds_read_b128 v[60:63],v2 offset:48128
v_mfma_f32_16x16x32_bf16 v[20:23],a[36:39],v[32:35],v[20:23]
s_waitcnt lgkmcnt(4)
v_mfma_f32_16x16x32_bf16 v[20:23],v[68:71],v[40:43],v[20:23]
ds_read_b128 v[32:35],v2 offset:6144
v_mfma_f32_16x16x32_bf16 v[20:23],v[180:183],v[44:47],v[20:23]
v_mfma_f32_16x16x32_bf16 v[16:19],v[176:179],v[44:47],v[16:19]
ds_read_b128 v[36:39],v2 offset:20480
v_mfma_f32_16x16x32_bf16 v[16:19],v[64:67],v[40:43],v[16:19]
s_waitcnt lgkmcnt(4)
v_mfma_f32_16x16x32_bf16 v[16:19],a[40:43],v[48:51],v[16:19]
ds_read_b128 v[40:43],v2 offset:34816
v_mfma_f32_16x16x32_bf16 v[16:19],a[152:155],v[52:55],v[16:19]
v_mfma_f32_16x16x32_bf16 v[20:23],a[156:159],v[52:55],v[20:23]
ds_read_b128 v[44:47],v2 offset:49152
v_mfma_f32_16x16x32_bf16 v[20:23],a[44:47],v[48:51],v[20:23]
s_waitcnt vmcnt(5)
s_waitcnt lgkmcnt(4)
v_mfma_f32_16x16x32_bf16 v[20:23],v[76:79],v[56:59],v[20:23]
s_barrier
ds_read_b128 v[48:51],v2 offset:7168
v_mfma_f32_16x16x32_bf16 v[20:23],v[188:191],v[60:63],v[20:23]
v_mfma_f32_16x16x32_bf16 v[16:19],v[184:187],v[60:63],v[16:19]
ds_read_b128 v[52:55],v2 offset:21504
v_mfma_f32_16x16x32_bf16 v[16:19],v[72:75],v[56:59],v[16:19]
s_waitcnt lgkmcnt(4)
v_mfma_f32_16x16x32_bf16 v[16:19],a[48:51],v[32:35],v[16:19]
ds_read_b128 v[56:59],v2 offset:35840
v_mfma_f32_16x16x32_bf16 v[16:19],a[160:163],v[36:39],v[16:19]
v_mfma_f32_16x16x32_bf16 v[20:23],a[164:167],v[36:39],v[20:23]
ds_read_b128 v[60:63],v2 offset:50176
v_mfma_f32_16x16x32_bf16 v[20:23],a[52:55],v[32:35],v[20:23]
s_waitcnt lgkmcnt(4)
v_mfma_f32_16x16x32_bf16 v[20:23],v[84:87],v[40:43],v[20:23]
ds_read_b128 v[32:35],v2 offset:8192
v_mfma_f32_16x16x32_bf16 v[20:23],v[196:199],v[44:47],v[20:23]
v_mfma_f32_16x16x32_bf16 v[16:19],v[192:195],v[44:47],v[16:19]
ds_read_b128 v[36:39],v2 offset:22528
v_mfma_f32_16x16x32_bf16 v[16:19],v[80:83],v[40:43],v[16:19]
s_waitcnt lgkmcnt(4)
v_mfma_f32_16x16x32_bf16 v[16:19],a[56:59],v[48:51],v[16:19]
ds_read_b128 v[40:43],v2 offset:36864
v_mfma_f32_16x16x32_bf16 v[16:19],a[168:171],v[52:55],v[16:19]
v_mfma_f32_16x16x32_bf16 v[20:23],a[172:175],v[52:55],v[20:23]
ds_read_b128 v[44:47],v2 offset:51200
v_mfma_f32_16x16x32_bf16 v[20:23],a[60:63],v[48:51],v[20:23]
s_waitcnt vmcnt(3)
s_waitcnt lgkmcnt(4)
v_mfma_f32_16x16x32_bf16 v[20:23],v[92:95],v[56:59],v[20:23]
s_barrier
ds_read_b128 v[48:51],v2 offset:9216
v_mfma_f32_16x16x32_bf16 v[20:23],v[204:207],v[60:63],v[20:23]
v_mfma_f32_16x16x32_bf16 v[16:19],v[200:203],v[60:63],v[16:19]
ds_read_b128 v[52:55],v2 offset:23552
v_mfma_f32_16x16x32_bf16 v[16:19],v[88:91],v[56:59],v[16:19]
s_waitcnt lgkmcnt(4)
v_mfma_f32_16x16x32_bf16 v[16:19],a[64:67],v[32:35],v[16:19]
ds_read_b128 v[56:59],v2 offset:37888
v_mfma_f32_16x16x32_bf16 v[16:19],a[176:179],v[36:39],v[16:19]
v_mfma_f32_16x16x32_bf16 v[20:23],a[180:183],v[36:39],v[20:23]
ds_read_b128 v[60:63],v2 offset:52224
v_mfma_f32_16x16x32_bf16 v[20:23],a[68:71],v[32:35],v[20:23]
s_waitcnt lgkmcnt(4)
v_mfma_f32_16x16x32_bf16 v[20:23],v[100:103],v[40:43],v[20:23]
ds_read_b128 v[32:35],v2 offset:10240
v_mfma_f32_16x16x32_bf16 v[20:23],v[212:215],v[44:47],v[20:23]
v_mfma_f32_16x16x32_bf16 v[16:19],v[208:211],v[44:47],v[16:19]
ds_read_b128 v[36:39],v2 offset:24576
v_mfma_f32_16x16x32_bf16 v[16:19],v[96:99],v[40:43],v[16:19]
s_waitcnt lgkmcnt(4)
v_mfma_f32_16x16x32_bf16 v[16:19],a[72:75],v[48:51],v[16:19]
ds_read_b128 v[40:43],v2 offset:38912
v_mfma_f32_16x16x32_bf16 v[16:19],a[184:187],v[52:55],v[16:19]
v_mfma_f32_16x16x32_bf16 v[20:23],a[188:191],v[52:55],v[20:23]
ds_read_b128 v[44:47],v2 offset:53248
v_mfma_f32_16x16x32_bf16 v[20:23],a[76:79],v[48:51],v[20:23]
s_waitcnt vmcnt(1)
s_waitcnt lgkmcnt(4)
v_mfma_f32_16x16x32_bf16 v[20:23],v[108:111],v[56:59],v[20:23]
s_barrier
ds_read_b128 v[48:51],v2 offset:11264
v_mfma_f32_16x16x32_bf16 v[20:23],v[220:223],v[60:63],v[20:23]
v_mfma_f32_16x16x32_bf16 v[16:19],v[216:219],v[60:63],v[16:19]
ds_read_b128 v[52:55],v2 offset:25600
v_mfma_f32_16x16x32_bf16 v[16:19],v[104:107],v[56:59],v[16:19]
s_waitcnt lgkmcnt(4)
v_mfma_f32_16x16x32_bf16 v[16:19],a[80:83],v[32:35],v[16:19]
ds_read_b128 v[56:59],v2 offset:39936
v_mfma_f32_16x16x32_bf16 v[16:19],a[192:195],v[36:39],v[16:19]
v_mfma_f32_16x16x32_bf16 v[20:23],a[196:199],v[36:39],v[20:23]
ds_read_b128 v[60:63],v2 offset:54272
v_mfma_f32_16x16x32_bf16 v[20:23],a[84:87],v[32:35],v[20:23]
s_waitcnt lgkmcnt(4)
v_mfma_f32_16x16x32_bf16 v[20:23],v[116:119],v[40:43],v[20:23]
ds_read_b128 v[32:35],v2 offset:12288
v_mfma_f32_16x16x32_bf16 v[20:23],v[228:231],v[44:47],v[20:23]
v_mfma_f32_16x16x32_bf16 v[16:19],v[224:227],v[44:47],v[16:19]
ds_read_b128 v[36:39],v2 offset:26624
v_mfma_f32_16x16x32_bf16 v[16:19],v[112:115],v[40:43],v[16:19]
s_waitcnt lgkmcnt(4)
v_mfma_f32_16x16x32_bf16 v[16:19],a[88:91],v[48:51],v[16:19]
ds_read_b128 v[40:43],v2 offset:40960
v_mfma_f32_16x16x32_bf16 v[16:19],a[200:203],v[52:55],v[16:19]
v_mfma_f32_16x16x32_bf16 v[20:23],a[204:207],v[52:55],v[20:23]
ds_read_b128 v[44:47],v2 offset:55296
v_mfma_f32_16x16x32_bf16 v[20:23],a[92:95],v[48:51],v[20:23]
s_waitcnt vmcnt(0)
s_waitcnt lgkmcnt(4)
v_mfma_f32_16x16x32_bf16 v[20:23],v[124:127],v[56:59],v[20:23]
s_barrier
ds_read_b128 v[48:51],v2 offset:13312
v_mfma_f32_16x16x32_bf16 v[20:23],v[236:239],v[60:63],v[20:23]
v_mfma_f32_16x16x32_bf16 v[16:19],v[232:235],v[60:63],v[16:19]
ds_read_b128 v[52:55],v2 offset:27648
v_mfma_f32_16x16x32_bf16 v[16:19],v[120:123],v[56:59],v[16:19]
s_waitcnt lgkmcnt(4)
v_mfma_f32_16x16x32_bf16 v[16:19],a[96:99],v[32:35],v[16:19]
ds_read_b128 v[56:59],v2 offset:41984
v_mfma_f32_16x16x32_bf16 v[16:19],a[208:211],v[36:39],v[16:19]
v_mfma_f32_16x16x32_bf16 v[20:23],a[212:215],v[36:39],v[20:23]
ds_read_b128 v[60:63],v2 offset:56320
v_mfma_f32_16x16x32_bf16 v[20:23],a[100:103],v[32:35],v[20:23]
s_waitcnt lgkmcnt(4)
v_mfma_f32_16x16x32_bf16 v[20:23],v[132:135],v[40:43],v[20:23]
v_mfma_f32_16x16x32_bf16 v[20:23],v[244:247],v[44:47],v[20:23]
v_mfma_f32_16x16x32_bf16 v[16:19],v[240:243],v[44:47],v[16:19]
v_mfma_f32_16x16x32_bf16 v[16:19],v[128:131],v[40:43],v[16:19]
s_waitcnt lgkmcnt(2)
v_mfma_f32_16x16x32_bf16 v[16:19],a[104:107],v[48:51],v[16:19]
v_mfma_f32_16x16x32_bf16 v[16:19],a[216:219],v[52:55],v[16:19]
v_mfma_f32_16x16x32_bf16 v[20:23],a[220:223],v[52:55],v[20:23]
v_mfma_f32_16x16x32_bf16 v[20:23],a[108:111],v[48:51],v[20:23]
s_waitcnt lgkmcnt(0)
v_mfma_f32_16x16x32_bf16 v[20:23],v[140:143],v[56:59],v[20:23]
v_mfma_f32_16x16x32_bf16 v[20:23],v[252:255],v[60:63],v[20:23]
v_mfma_f32_16x16x32_bf16 v[16:19],v[248:251],v[60:63],v[16:19]
v_mfma_f32_16x16x32_bf16 v[16:19],v[136:139],v[56:59],v[16:19]
s_nop 1
s_nop 3
v_cvt_pk_bf16_f32 v15,v22,v23
v_cvt_pk_bf16_f32 v14,v20,v21
v_cvt_pk_bf16_f32 v13,v18,v19
v_cvt_pk_bf16_f32 v12,v16,v17
ds_write_b128 v5,v[12:15]
ds_read2st64_b32 v[12:13],v3 offset0:0 offset1:1
ds_read2st64_b32 v[14:15],v3 offset0:2 offset1:3
s_waitcnt lgkmcnt(0)
buffer_atomic_pk_add_bf16 v12,v1,s[44:47],s65 offen offset:0 sc1
s_add_u32 s70,s70,s75
buffer_atomic_pk_add_bf16 v13,v1,s[44:47],s65 offen offset:2048 sc1
s_add_u32 s65,s65,16384
buffer_atomic_pk_add_bf16 v14,v1,s[44:47],s65 offen offset:0 sc1
buffer_atomic_pk_add_bf16 v15,v1,s[44:47],s65 offen offset:2048 sc1
s_and_b32 s65,s70,0x3f8000
s_sub_u32 s62,s62,1
s_cbranch_scc1 .L_end
s_cmp_lg_u32 s62,0
s_cbranch_scc1 .L_xa
s_add_u32 s66,s66,32071680
s_add_u32 s63,s63,32071680
s_add_u32 s71,s71,32071680
s_add_u32 s70,s70,884736
s_mov_b32 s65,s70
s_branch .L_xa
.L_last:
)#"
    : "+{v0}"(tid)
    : "{s[8:9]}"(x), "{s[10:11]}"(wq), "{s[12:13]}"(wo), "{s[14:15]}"(wg), "{s[16:17]}"(wu), "{s[18:19]}"(wd),
      "{s[20:21]}"(out), "{s[22:23]}"(ws), "{s24}"(blockIdx.x)
    : "v1", "v2", "v3", "v4", "v5", "v6", "v7", "v8", "v9", "v10", "v11", "v12", "v13", "v14", "v15", "v16", "v17", "v18", "v19", "v20", "v21", "v22", "v23", "v24", "v25", "v26", "v27", "v28", "v29", "v30", "v31", "v32", "v33", "v34", "v35", "v36", "v37", "v38", "v39", "v40", "v41", "v42", "v43", "v44", "v45", "v46", "v47", "v48", "v49", "v50", "v51", "v52", "v53", "v54", "v55", "v56", "v57", "v58", "v59", "v60", "v61", "v62", "v63", "v64", "v65", "v66", "v67", "v68", "v69", "v70", "v71", "v72", "v73", "v74", "v75", "v76", "v77", "v78", "v79", "v80", "v81", "v82", "v83", "v84", "v85", "v86", "v87", "v88", "v89", "v90", "v91", "v92", "v93", "v94", "v95", "v96", "v97", "v98", "v99", "v100", "v101", "v102", "v103", "v104", "v105", "v106", "v107", "v108", "v109", "v110", "v111", "v112", "v113", "v114", "v115", "v116", "v117", "v118", "v119", "v120", "v121", "v122", "v123", "v124", "v125", "v126", "v127", "v128", "v129", "v130", "v131", "v132", "v133", "v134", "v135", "v136", "v137", "v138", "v139", "v140", "v141", "v142", "v143", "v144", "v145", "v146", "v147", "v148", "v149", "v150", "v151", "v152", "v153", "v154", "v155", "v156", "v157", "v158", "v159", "v160", "v161", "v162", "v163", "v164", "v165", "v166", "v167", "v168", "v169", "v170", "v171", "v172", "v173", "v174", "v175", "v176", "v177", "v178", "v179", "v180", "v181", "v182", "v183", "v184", "v185", "v186", "v187", "v188", "v189", "v190", "v191", "v192", "v193", "v194", "v195", "v196", "v197", "v198", "v199", "v200", "v201", "v202", "v203", "v204", "v205", "v206", "v207", "v208", "v209", "v210", "v211", "v212", "v213", "v214", "v215", "v216", "v217", "v218", "v219", "v220", "v221", "v222", "v223", "v224", "v225", "v226", "v227", "v228", "v229", "v230", "v231", "v232", "v233", "v234", "v235", "v236", "v237", "v238", "v239", "v240", "v241", "v242", "v243", "v244", "v245", "v246", "v247", "v248", "v249", "v250", "v251", "v252", "v253", "v254", "v255", "a0", "a1", "a2", "a3", "a4", "a5", "a6", "a7", "a8", "a9", "a10", "a11", "a12", "a13", "a14", "a15", "a16", "a17", "a18", "a19", "a20", "a21", "a22", "a23", "a24", "a25", "a26", "a27", "a28", "a29", "a30", "a31", "a32", "a33", "a34", "a35", "a36", "a37", "a38", "a39", "a40", "a41", "a42", "a43", "a44", "a45", "a46", "a47", "a48", "a49", "a50", "a51", "a52", "a53", "a54", "a55", "a56", "a57", "a58", "a59", "a60", "a61", "a62", "a63", "a64", "a65", "a66", "a67", "a68", "a69", "a70", "a71", "a72", "a73", "a74", "a75", "a76", "a77", "a78", "a79", "a80", "a81", "a82", "a83", "a84", "a85", "a86", "a87", "a88", "a89", "a90", "a91", "a92", "a93", "a94", "a95", "a96", "a97", "a98", "a99", "a100", "a101", "a102", "a103", "a104", "a105", "a106", "a107", "a108", "a109", "a110", "a111", "a112", "a113", "a114", "a115", "a116", "a117", "a118", "a119", "a120", "a121", "a122", "a123", "a124", "a125", "a126", "a127", "a128", "a129", "a130", "a131", "a132", "a133", "a134", "a135", "a136", "a137", "a138", "a139", "a140", "a141", "a142", "a143", "a144", "a145", "a146", "a147", "a148", "a149", "a150", "a151", "a152", "a153", "a154", "a155", "a156", "a157", "a158", "a159", "a160", "a161", "a162", "a163", "a164", "a165", "a166", "a167", "a168", "a169", "a170", "a171", "a172", "a173", "a174", "a175", "a176", "a177", "a178", "a179", "a180", "a181", "a182", "a183", "a184", "a185", "a186", "a187", "a188", "a189", "a190", "a191", "a192", "a193", "a194", "a195", "a196", "a197", "a198", "a199", "a200", "a201", "a202", "a203", "a204", "a205", "a206", "a207", "a208", "a209", "a210", "a211", "a212", "a213", "a214", "a215", "a216", "a217", "a218", "a219", "a220", "a221", "a222", "a223", "a224", "a225", "a226", "a227", "a228", "a229", "a230", "a231", "a232", "a233", "a234", "a235", "a236", "a237", "a238", "a239", "a240", "a241", "a242", "a243", "a244", "a245", "a246", "a247", "a248", "a249", "a250", "a251", "a252", "a253", "a254", "a255", "s25", "s26", "s27", "s28", "s29", "s30", "s31", "s32", "s33", "s34", "s35", "s36", "s37", "s38", "s39", "s40", "s41", "s42", "s43", "s44", "s45", "s46", "s47", "s48", "s49", "s50", "s51", "s52", "s53", "s54", "s55", "s56", "s57", "s58", "s59", "s60", "s61", "s62", "s63", "s64", "s65", "s66", "s67", "s68", "s69", "s70", "s71", "s72", "s73", "s74", "s75", "s76", "s77", "s78", "s79", "s80", "s81", "s82", "s83", "s84", "s85", "s86", "s87", "s88", "s89", "s90", "s91", "s92", "s93", "s94", "s95", "s96", "s97", "s98", "s99", "vcc", "scc", "m0", "memory");
}

extern "C" void kernel_launch(void* const* d_in, const int* in_sizes, int n_in, void* d_out, int out_size,
                              void* d_ws, size_t ws_size, hipStream_t stream) {
    constexpr int smem = 163840;
    (void)hipFuncSetAttribute((const void*)body_k, hipFuncAttributeMaxDynamicSharedMemorySize, smem);
    body_k<<<256, 256, smem, stream>>>(d_in[0], d_in[1], d_in[2], d_in[4], d_in[5], d_in[6], d_out, d_ws);
}
```
